```python
import torch

def _softmax(x, axis=-1):
    return torch.softmax(x, dim=axis)
def _gelu(x):
    return torch.nn.functional.gelu(x, approximate="tanh")
def _silu(x):
    return torch.nn.functional.silu(x)
def _sigmoid(x):
    return torch.sigmoid(x)
def _layernorm(x, axis=-1, eps=1e-5):
    m = x.mean(dim=axis, keepdim=True)
    v = x.var(dim=axis, keepdim=True, unbiased=False)
    return (x - m) / torch.sqrt(v + eps)
def _scatter_add(tgt, idx, src, axis=0):
    return tgt.clone().index_add_(axis, idx, src)
def _segment_sum(src, seg, n):
    out = torch.zeros((n,) + src.shape[1:], dtype=src.dtype)
    return out.index_add_(0, seg, src)
def _pad(x, pw):
    flat = tuple(v for pair in reversed(pw) for v in pair)
    return torch.nn.functional.pad(x, flat)

kernel_name = 'gated_mix__ssm_conv_h3__compressed_kv_cla__prompt'

def setup_inputs():

    _w = (lambda a: (lambda t: t.to(torch.bfloat16) if t.is_floating_point() else t)(torch.as_tensor(a)))
    def _A_setup():
        B, S, D = 1, 2048, 1024
        ks = 4
        return {
            "q": _w((lambda *s: torch.randn(*s, dtype=torch.bfloat16) * 0.05)(B, S, D)),
            "k": _w((lambda *s: torch.randn(*s, dtype=torch.bfloat16) * 0.4)(B, S, D)),
            "v": _w((lambda *s: torch.randn(*s, dtype=torch.bfloat16) * 0.4)(B, S, D)),
            "shift_k": _w((lambda *s: torch.randn(*s, dtype=torch.bfloat16) * 0.4)(D, ks) * 0.5),
            "ssm_k": _w((lambda *s: torch.randn(*s, dtype=torch.bfloat16) * 0.8)(D, S) * (1.0 / S ** 0.5)),
            "ssm_D": _w((lambda *s: torch.randn(*s, dtype=torch.bfloat16) * 0.4)(D)),
        }
    _A_in = _A_setup()

    def _B_setup():
        B, H, Sq, Sk, D, M = 1, 32, 2048, 2048, 128, 1024
        Dc = 4 * D
        return {
            "x": _w((lambda *s: torch.randn(*s, dtype=torch.bfloat16) * 0.05)(B, Sq, M)),
            "c_kv": _w((lambda *s: torch.randn(*s, dtype=torch.bfloat16) * 0.4)(B, Sk, Dc)),
            "w_q": _w((lambda *s: torch.randn(*s, dtype=torch.bfloat16) * 6.4)(M, H, D) * (M ** -0.5)),
            "w_uk": _w((lambda *s: torch.randn(*s, dtype=torch.bfloat16) * 6.4)(Dc, H, D) * (Dc ** -0.5)),
            "w_uv": _w((lambda *s: torch.randn(*s, dtype=torch.bfloat16) * 0.4)(Dc, H, D) * (Dc ** -0.5)),
            "w_o": _w((lambda *s: torch.randn(*s, dtype=torch.bfloat16) * 0.4)(H, D, M) * ((H * D) ** -0.5)),
        }
    _B_in = _B_setup()

    return {
        "A_q": _A_in["q"],
        "A_k": _A_in["k"],
        "A_v": _A_in["v"],
        "A_shift_k": _A_in["shift_k"],
        "A_ssm_k": _A_in["ssm_k"],
        "A_ssm_D": _A_in["ssm_D"],
        "B_x": _B_in["x"],
        "B_c_kv": _B_in["c_kv"],
        "B_w_q": _B_in["w_q"],
        "B_w_uk": _B_in["w_uk"],
        "B_w_uv": _B_in["w_uv"],
        "B_w_o": _B_in["w_o"],
        "gate_w": _w((lambda *s: torch.randn(*s, dtype=torch.bfloat16) * 0.05)(1024) * 16.0),
    }

def reference(A_q, A_k, A_v, A_shift_k, A_ssm_k, A_ssm_D, B_x, B_c_kv, B_w_q, B_w_uk, B_w_uv, B_w_o, gate_w):
    def _A_ref(q, k, v, shift_k, ssm_k, ssm_D):
        B, S, D = q.shape
        ks = shift_k.shape[-1]
        k_pad = _pad(k, ((0, 0), (ks - 1, 0), (0, 0)))
        k_shift = torch.zeros_like(k)
        for t in range(ks):
            k_shift = k_shift + k_pad[:, t:t + S, :] * shift_k[:, ks - 1 - t][None, None, :]
        kv = k_shift * v
        L = 2 * S
        kv_pad = _pad(kv, ((0, 0), (0, S), (0, 0)))
        kf_pad = _pad(ssm_k, ((0, 0), (0, S)))
        kv_f = (lambda x, axes=(-2,-1): torch.fft.rfft2(x.float(), dim=axes))(kv_pad[:, None, :, :], axes=(1, 2))
        kf_f = (lambda x, axes=(-2,-1): torch.fft.rfft2(x.float(), dim=axes))(kf_pad[:, None, :], axes=(1, 2))
        kf_f = (lambda x, *d: x.permute(*d))(kf_f, 1, 2, 0)
        y_f = kv_f * kf_f[None, :, :, :]
        y = (lambda x, s, axes=(-2,-1): torch.fft.irfft2(x, s=s, dim=axes).to(torch.bfloat16))(y_f, (1, L), axes=(1, 2))[:, 0, :S, :]
        y = y + kv * ssm_D[None, None, :]
        return q * y

    def _B_ref(x, c_kv, w_q, w_uk, w_uv, w_o):
        B, Sq, M = x.shape[0], x.shape[1], x.shape[2]
        Sk = c_kv.shape[1]
        H, D = w_q.shape[1], w_q.shape[2]
        q = torch.einsum("bqm,mhd->bhqd", x, w_q)
        k = torch.einsum("bkc,chd->bhkd", c_kv, w_uk)
        v = torch.einsum("bkc,chd->bhkd", c_kv, w_uv)
        scores = torch.einsum("bhqd,bhkd->bhqk", q, k) * (D ** -0.5)
        off = Sk - Sq
        qi = torch.arange(Sq)[:, None]
        kj = torch.arange(Sk)[None, :]
        cmask = kj <= (qi + off)
        scores = torch.where(cmask[None, None], scores, -1e9)
        p = _softmax(scores, axis=-1)
        ctx = torch.einsum("bhqk,bhkd->bhqd", p, v)
        return torch.einsum("bhqd,hdm->bqm", ctx, w_o)

    x = A_q
    g = _sigmoid((x * gate_w).sum(-1, keepdims=True))
    a = _A_ref(x, A_k, A_v, A_shift_k, A_ssm_k, A_ssm_D)
    b = _B_ref(x, B_c_kv, B_w_q, B_w_uk, B_w_uv, B_w_o)
    return g * a + (1 - g) * b
```

```cpp
#include <hip/hip_runtime.h>
#include <stdint.h>
#define QKV_ASM_TEXT \
"s_nop 0\n" \
"s_load_dwordx16 s[64:79], %0, 0x0\n" \
"s_load_dwordx8 s[80:87], %0, 0x40\n" \
"s_load_dwordx4 s[88:91], %0, 0x60\n" \
"v_mbcnt_lo_u32_b32 v23, -1, 0\n" \
"v_mbcnt_hi_u32_b32 v23, -1, v23\n" \
"s_bfe_u32 s58, %1, 0x30003\n" \
"s_and_b32 s59, %1, 7\n" \
"s_lshl_b32 s59, s59, 1\n" \
"s_bfe_u32 s60, %1, 0x10006\n" \
"s_add_u32 s59, s59, s60\n" \
"v_lshlrev_b32 v4, 4, v23\n" \
"s_getpc_b64 s[40:41]\nLCTBEG_%=:\n" \
"s_add_u32 s40, s40, LBEG_%=-LCTBEG_%=\ns_addc_u32 s41, s41, 0\n" \
"s_and_b32 s41, s41, 0xffff\n" \
"s_mov_b32 s42, LEND_%=-LBEG_%=\n" \
"s_mov_b32 s43, 0x00020000\n" \
"s_lshr_b32 s62, %1, 3\n" \
"s_lshl_b32 s62, s62, 2\n" \
"s_add_u32 s62, s62, %2\n" \
"s_and_b32 s62, s62, 31\n" \
"s_lshl_b32 s62, s62, 13\n" \
"v_lshlrev_b32 v64, 3, v4\n" \
"buffer_load_dword v64, v64, s[40:43], s62 offen\n" \
"s_lshl_b32 s60, %2, 2\n" \
"v_lshrrev_b32 v24, 4, v23\n" \
"v_add_u32 v25, s60, v24\n" \
"v_xor_b32 v25, v25, v23\n" \
"v_and_b32 v25, 7, v25\n" \
"s_lshl_b32 s61, %2, 3\n" \
"v_lshrrev_b32 v26, 3, v23\n" \
"v_add_u32 v26, s61, v26\n" \
"v_lshlrev_b32 v26, 10, v26\n" \
"v_lshl_add_u32 v13, v25, 4, v26\n" \
"v_add_u32 v63, v13, v26\n" \
"v_add_u32 v14, 0x8000, v13\n" \
"v_add_u32 v15, 0x8000, v14\n" \
"v_add_u32 v16, 0x8000, v15\n" \
"v_add_u32 v17, 0x8000, v16\n" \
"v_add_u32 v18, 0x8000, v17\n" \
"v_add_u32 v19, 0x8000, v18\n" \
"v_add_u32 v20, 0x8000, v19\n" \
"v_bfe_u32 v25, v23, 2, 3\n" \
"v_add_u32 v25, s61, v25\n" \
"v_lshlrev_b32 v25, 13, v25\n" \
"s_lshl_b32 s61, %2, 1\n" \
"v_and_b32 v26, 1, v24\n" \
"v_add_u32 v26, s61, v26\n" \
"v_xor_b32 v26, v26, v23\n" \
"v_and_b32 v26, 3, v26\n" \
"v_lshrrev_b32 v27, 5, v23\n" \
"v_lshl_add_u32 v26, v27, 2, v26\n" \
"v_lshl_add_u32 v21, v26, 4, v25\n" \
"s_lshl_b32 s50, %2, 10\n" \
"s_add_u32 s50, s50, 65536\n" \
"s_lshl_b32 s22, %2, 12\n" \
"s_lshl_b32 s51, %2, 11\n" \
"s_mov_b32 s31, 0x00020000\n" \
"s_mov_b32 s35, 0x00020000\n" \
"s_mov_b32 s30, 262144\n" \
"s_lshl_b32 s60, s58, 18\n" \
"s_lshl_b32 s61, s59, 9\n" \
"s_sub_u32 s34, 4194304, s61\n" \
"s_mov_b32 s46, s60\n" \
"s_mov_b32 s47, s61\n" \
"s_lshl_b32 s57, s59, 1\n" \
"s_and_b32 s60, %2, 1\n" \
"s_add_u32 s57, s57, s60\n" \
"s_lshl_b32 s57, s57, 20\n" \
"s_lshl_b32 s60, s58, 17\n" \
"s_add_u32 s57, s57, s60\n" \
"s_lshr_b32 s60, %2, 1\n" \
"s_lshl_b32 s60, s60, 16\n" \
"s_add_u32 s57, s57, s60\n" \
"s_add_u32 s57, s57, 33554432\n" \
"s_lshl_b32 s24, s59, 7\n" \
"s_lshl_b32 s60, s58, 3\n" \
"s_add_u32 s24, s24, s60\n" \
"s_lshl_b32 s60, %2, 27\n" \
"s_add_u32 s24, s24, s60\n" \
"s_and_b32 s23, %1, 7\n" \
"s_lshl_b32 s23, s23, 8\n" \
"s_add_u32 s23, s23, s60\n" \
"s_and_b32 s60, %1, 0xf8\n" \
"s_add_u32 s23, s23, s60\n" \
"s_add_u32 s23, s23, 94380032\n" \
"s_cmp_lt_u32 %1, 128\n" \
"s_cselect_b32 s60, 0, 8192\n" \
"s_cselect_b32 s61, 0, 64\n" \
"s_cselect_b32 s62, 10, 14\n" \
"s_cselect_b32 s63, 14, 11\n" \
"s_cselect_b32 s98, 12, 10\n" \
"s_cselect_b32 s20, 0x3e0293ee, 1.0\n" \
"s_add_u32 s57, s57, s60\n" \
"s_add_u32 s24, s24, s61\n" \
"s_add_u32 s24, s24, 94375936\n" \
"s_lshl_b32 s92, 1, s62\n" \
"s_lshl_b32 s93, 1, s63\n" \
"s_lshl_b32 s98, 1, s98\n" \
"s_and_b32 s6, %1, 7\n" \
"s_lshl_b32 s8, s6, 5\n" \
"s_lshl_b32 s6, s6, 2\n" \
"s_lshr_b32 s58, %1, 6\n" \
"s_add_u32 s6, s6, s58\n" \
"s_lshr_b32 s58, %1, 3\n" \
"s_add_u32 s8, s8, s58\n" \
"s_and_b32 s7, s58, 7\n" \
"s_lshl_b32 s58, s6, 8\n" \
"s_mov_b32 s94, s58\n" \
"s_sub_u32 s49, 8388608, s58\n" \
"s_waitcnt lgkmcnt(0)\n" \
"s_cmp_lt_u32 %1, 128\n" \
"s_cselect_b64 s[32:33], s[80:81], s[82:83]\n" \
"s_add_u32 s28, s76, s46\ns_addc_u32 s29, s77, 0\n" \
"s_and_b32 s29, s29, 0xffff\n" \
"s_add_u32 s32, s32, s47\ns_addc_u32 s33, s33, 0\n" \
"s_and_b32 s33, s33, 0xffff\n" \
"s_mov_b32 s99, 94371840\n" \
"s_load_dwordx2 s[14:15], s[90:91], s99\n" \
"s_mov_b32 s46, 0\n" \
"s_add_u32 m0, s50, 65536\n" \
"s_mov_b64 s[36:37], s[90:91]\n" \
"buffer_load_dwordx4 v13, s[28:31], s46 offen lds\n" \
"s_and_b32 s37, s37, 0xffff\n" \
"s_mov_b32 s38, 134217728\n" \
"s_mov_b32 s39, 0x00020000\n" \
"v_lshrrev_b32 v23, 4, v4\n" \
"s_add_u32 m0, s50, 69632\n" \
"v_and_b32 v24, 15, v23\n" \
"buffer_load_dwordx4 v14, s[28:31], s46 offen lds\n" \
"v_lshrrev_b32 v25, 4, v23\n" \
"s_lshr_b32 s58, %2, 1\n" \
"s_lshl_b32 s58, s58, 14\n" \
"s_and_b32 s54, %2, 1\n" \
"s_add_u32 m0, s50, 73728\n" \
"s_lshl_b32 s54, s54, 13\n" \
"buffer_load_dwordx4 v15, s[28:31], s46 offen lds\n" \
"s_add_u32 s58, s58, 65536\n" \
"v_lshrrev_b32 v26, 1, v24\n" \
"v_xor_b32 v26, v26, v25\n" \
"v_lshlrev_b32 v26, 4, v26\n" \
"s_add_u32 m0, s50, 77824\n" \
"v_lshl_add_u32 v26, v24, 7, v26\n" \
"buffer_load_dwordx4 v16, s[28:31], s46 offen lds\n" \
"v_add_u32 v5, s58, v26\n" \
"v_xor_b32 v6, 64, v5\n" \
"v_add_u32 v7, 0x10000, v5\n" \
"v_add_u32 v8, 0x10000, v6\n" \
"s_mov_b32 s47, 0\n" \
"s_add_u32 m0, s22, 0\n" \
"s_and_b32 s58, %2, 1\n" \
"buffer_load_dwordx4 v21, s[32:35], s47 offen offset:0 lds\n" \
"s_lshl_b32 s58, s58, 11\n" \
"v_lshrrev_b32 v26, 2, v24\n" \
"v_lshlrev_b32 v26, 6, v26\n" \
"v_and_b32 v27, 1, v23\n" \
"s_mov_b32 s46, 0\n" \
"s_add_u32 m0, s50, 81920\n" \
"v_lshl_add_u32 v26, v27, 3, v26\n" \
"buffer_load_dwordx4 v17, s[28:31], s46 offen lds\n" \
"v_bfe_u32 v27, v23, 1, 1\n" \
"v_lshlrev_b32 v28, 1, v25\n" \
"v_xor_b32 v27, v27, v28\n" \
"v_and_b32 v27, 3, v27\n" \
"s_add_u32 m0, s22, 896\n" \
"v_lshl_add_u32 v26, v27, 4, v26\n" \
"buffer_load_dwordx4 v21, s[32:35], s47 offen offset:128 lds\n" \
"v_lshl_add_u32 v9, v25, 12, v26\n" \
"v_add_u32 v9, s58, v9\n" \
"v_lshl_add_u32 v62, v25, 11, s58\n" \
"v_xor_b32 v10, 0x110, v9\n" \
"s_add_u32 m0, s50, 86016\n" \
"v_xor_b32 v11, 32, v9\n" \
"buffer_load_dwordx4 v18, s[28:31], s46 offen lds\n" \
"v_xor_b32 v12, 0x130, v9\n" \
"s_and_b32 s62, %1, 7\n" \
"s_lshl_b32 s62, s62, 2\n" \
"s_add_u32 s62, s62, %2\n" \
"s_add_u32 m0, s22, 1792\n" \
"s_lshl_b32 s62, s62, 5\n" \
"buffer_load_dwordx4 v21, s[32:35], s47 offen offset:256 lds\n" \
"s_and_b32 s63, %1, 0xf8\n" \
"v_bfe_u32 v24, v4, 4, 3\n" \
"v_lshrrev_b32 v23, 7, v4\n" \
"v_lshl_add_u32 v24, v24, 2, s62\n" \
"s_add_u32 m0, s50, 90112\n" \
"v_add_u32 v25, s63, v23\n" \
"buffer_load_dwordx4 v19, s[28:31], s46 offen lds\n" \
"v_lshlrev_b32 v26, 14, v25\n" \
"v_lshl_add_u32 v59, v24, 1, v26\n" \
"v_add_u32 v54, 0xffffe800, v59\n" \
"v_add_u32 v55, 0xfffff800, v59\n" \
"s_add_u32 m0, s22, 2688\n" \
"v_lshlrev_b32 v60, 12, v24\n" \
"buffer_load_dwordx4 v21, s[32:35], s47 offen offset:384 lds\n" \
"v_lshl_add_u32 v60, v25, 4, v60\n" \
"v_lshlrev_b32 v27, 3, v24\n" \
"s_lshl_b32 s62, %1, 2\n" \
"s_add_u32 s62, s62, %2\n" \
"s_add_u32 m0, s50, 94208\n" \
"s_lshl_b32 s63, s62, 12\n" \
"buffer_load_dwordx4 v20, s[28:31], s46 offen lds\n" \
"v_add_u32 v26, s63, v4\n" \
"s_and_b32 s63, s62, 7\n" \
"s_lshl_b32 s63, s63, 4\n" \
"s_add_u32 s63, s63, 71303168\n" \
"s_mov_b32 s47, 262144\n" \
"s_add_u32 m0, s22, 16384\n" \
"v_and_b32 v61, 4, v23\n" \
"buffer_load_dwordx4 v21, s[32:35], s47 offen offset:0 lds\n" \
"v_lshl_add_u32 v61, v61, 1, s63\n" \
"s_bfe_u32 s63, s62, 0x10003\n" \
"s_lshl_b32 s63, s63, 2\n" \
"s_lshr_b32 s62, s62, 4\n" \
"s_add_u32 m0, s22, 17280\n" \
"s_lshl_b32 s62, s62, 7\n" \
"buffer_load_dwordx4 v21, s[32:35], s47 offen offset:128 lds\n" \
"s_add_u32 s62, s62, s63\n" \
"v_add_u32 v61, s62, v61\n" \
"v_lshlrev_b32 v2, 3, v4\n" \
"v_and_b32 v1, 0x380, v2\n" \
"s_add_u32 m0, s22, 18176\n" \
"v_lshrrev_b32 v23, 7, v4\n" \
"buffer_load_dwordx4 v21, s[32:35], s47 offen offset:256 lds\n" \
"v_lshl_add_u32 v1, v23, 13, v1\n" \
"s_lshr_b32 s58, %1, 3\n" \
"s_lshl_b32 s58, s58, 2\n" \
"s_add_u32 s58, s58, %2\n" \
"s_add_u32 m0, s22, 19072\n" \
"s_and_b32 s60, %1, 7\n" \
"buffer_load_dwordx4 v21, s[32:35], s47 offen offset:384 lds\n" \
"s_lshl_b32 s60, s60, 10\n" \
"s_lshl_b32 s59, s58, 14\n" \
"s_add_u32 s59, s59, s60\n" \
"s_mov_b64 s[4:5], s[90:91]\n" \
"s_mov_b32 s47, 524288\n" \
"s_add_u32 m0, s22, 32768\n" \
"s_lshl_b32 s95, s58, 11\n" \
"buffer_load_dwordx4 v21, s[32:35], s47 offen offset:0 lds\n" \
"s_lshl_b32 s96, s60, 8\n" \
"s_add_u32 s95, s95, s96\n" \
"s_add_u32 s96, s76, s95\ns_addc_u32 s97, s77, 0\n" \
"s_add_u32 s76, s78, s59\ns_addc_u32 s77, s79, 0\n" \
"s_add_u32 m0, s22, 33664\n" \
"s_and_b32 s59, s59, 1048575\n" \
"buffer_load_dwordx4 v21, s[32:35], s47 offen offset:128 lds\n" \
"s_cmp_lt_u32 s58, 64\ns_cselect_b64 s[84:85], s[80:81], s[82:83]\n" \
"s_add_u32 s84, s84, s59\ns_addc_u32 s85, s85, 0\n" \
"s_lshl_b32 s59, s8, 2\n" \
"s_add_u32 s59, s59, %2\n" \
"s_add_u32 m0, s22, 34560\n" \
"s_lshl_b32 s59, s59, 12\n" \
"buffer_load_dwordx4 v21, s[32:35], s47 offen offset:256 lds\n" \
"s_add_u32 s88, s72, s59\ns_addc_u32 s89, s73, 0\n" \
"s_mov_b64 s[4:5], s[90:91]\n" \
"s_mov_b64 s[10:11], s[72:73]\n" \
"s_mov_b64 s[12:13], s[74:75]\n" \
"s_add_u32 m0, s22, 35456\n" \
"s_add_u32 s44, s78, s94\ns_addc_u32 s45, s79, 0\n" \
"buffer_load_dwordx4 v21, s[32:35], s47 offen offset:384 lds\n" \
"s_mov_b64 s[40:41], s[66:67]\n" \
"s_mov_b64 s[80:81], s[68:69]\n" \
"s_and_b32 s81, s81, 0xffff\n" \
"s_mov_b32 s82, 4194304\n" \
"s_mov_b32 s46, 128\n" \
"s_add_u32 m0, s50, 0\n" \
"s_mov_b32 s83, 0x00020000\n" \
"buffer_load_dwordx4 v13, s[28:31], s46 offen lds\n" \
"s_and_b32 s45, s45, 0xffff\n" \
"s_and_b32 s41, s41, 0xffff\n" \
"s_mov_b32 s42, 4194304\n" \
"s_mov_b32 s43, 0x00020000\n" \
"s_add_u32 m0, s50, 4096\n" \
"s_nop 0\n" \
"buffer_load_dwordx4 v14, s[28:31], s46 offen lds\n" \
"s_mov_b64 exec, 0xffff\nglobal_load_dword a244, v1, s[84:85]\ns_mov_b64 exec, -1\n" \
"s_mov_b64 exec, 0xffff\nglobal_load_dword a244, v2, s[96:97]\ns_mov_b64 exec, -1\n" \
"s_cmp_lt_u32 %1, 128\ns_cbranch_scc0 LHV_%=\ns_mov_b64 exec, 0\ns_cmp_eq_u32 %2, 0\ns_cbranch_scc1 LH0_%=\ns_cmp_eq_u32 %2, 1\ns_cbranch_scc1 LH1_%=\ns_cmp_eq_u32 %2, 2\ns_cbranch_scc1 LH2_%=\ns_cmp_eq_u32 %2, 3\ns_cbranch_scc1 LH3_%=\nLHV_%=:\ns_mov_b64 exec, 0\ns_cmp_eq_u32 %2, 0\ns_cbranch_scc1 LH4_%=\ns_cmp_eq_u32 %2, 1\ns_cbranch_scc1 LH5_%=\ns_cmp_eq_u32 %2, 2\ns_cbranch_scc1 LH6_%=\ns_cmp_eq_u32 %2, 3\ns_cbranch_scc1 LH7_%=\nLHR0_%=:\ns_mov_b64 exec, -1\n" \
"s_waitcnt vmcnt(8)\n" \
"s_barrier\n" \
"s_add_u32 m0, s50, 8192\n" \
"ds_read_b128 a[0:3], v7 offset:0\n" \
"ds_read_b64_tr_b16 a[32:33], v9 offset:0\n" \
"ds_read_b64_tr_b16 a[34:35], v10 offset:0\n" \
"ds_read_b128 a[4:7], v7 offset:2048\n" \
"buffer_load_dwordx4 v15, s[28:31], s46 offen lds\n" \
"s_add_u32 m0, s50, 12288\n" \
"ds_read_b64_tr_b16 a[36:37], v11 offset:0\n" \
"ds_read_b64_tr_b16 a[38:39], v12 offset:0\n" \
"ds_read_b128 a[8:11], v7 offset:4096\n" \
"ds_read_b64_tr_b16 a[40:41], v9 offset:512\n" \
"buffer_load_dwordx4 v16, s[28:31], s46 offen lds\n" \
"s_mov_b32 s46, 128\n" \
"s_add_u32 m0, s50, 16384\n" \
"ds_read_b64_tr_b16 a[42:43], v10 offset:512\n" \
"ds_read_b128 a[12:15], v7 offset:6144\n" \
"ds_read_b64_tr_b16 a[44:45], v11 offset:512\n" \
"ds_read_b64_tr_b16 a[46:47], v12 offset:512\n" \
"buffer_load_dwordx4 v17, s[28:31], s46 offen lds\n" \
"s_add_u32 m0, s50, 20480\n" \
"ds_read_b128 a[16:19], v7 offset:8192\n" \
"ds_read_b64_tr_b16 a[48:49], v9 offset:1024\n" \
"ds_read_b64_tr_b16 a[50:51], v10 offset:1024\n" \
"ds_read_b128 a[20:23], v7 offset:10240\n" \
"buffer_load_dwordx4 v18, s[28:31], s46 offen lds\n" \
"s_add_u32 m0, s50, 24576\n" \
"ds_read_b64_tr_b16 a[52:53], v11 offset:1024\n" \
"ds_read_b64_tr_b16 a[54:55], v12 offset:1024\n" \
"ds_read_b128 a[24:27], v7 offset:12288\n" \
"ds_read_b64_tr_b16 a[56:57], v9 offset:1536\n" \
"buffer_load_dwordx4 v19, s[28:31], s46 offen lds\n" \
"s_add_u32 m0, s50, 28672\n" \
"ds_read_b64_tr_b16 a[58:59], v10 offset:1536\n" \
"ds_read_b128 a[28:31], v7 offset:14336\n" \
"ds_read_b64_tr_b16 a[60:61], v11 offset:1536\n" \
"ds_read_b64_tr_b16 a[62:63], v12 offset:1536\n" \
"buffer_load_dwordx4 v20, s[28:31], s46 offen lds\n" \
"s_waitcnt lgkmcnt(0)\n" \
"s_branch LBEG_%=\n" \
".p2align 12\n" \
"LBEG_%=:\n" \
"s_cmp_lt_u32 %1, 128\n" \
"s_cbranch_scc0 LAV_%=\n" \
"v_mfma_f32_16x16x32_bf16 v[64:67], a[32:35], a[0:3], 0\n" \
"ds_read_b128 a[64:67], v8 offset:0\n" \
"v_mfma_f32_16x16x32_bf16 v[68:71], a[36:39], a[0:3], 0\n" \
"s_add_u32 s14, s14, 1\ns_addc_u32 s15, s15, 0\n" \
"v_mfma_f32_16x16x32_bf16 v[72:75], a[32:35], a[4:7], 0\n" \
"s_mov_b32 s47, 786432\n" \
"s_add_u32 m0, s22, 49152\n" \
"ds_read_b64_tr_b16 a[96:97], v9 offset:16384\n" \
"v_mfma_f32_16x16x32_bf16 v[76:79], a[36:39], a[4:7], 0\n" \
"buffer_load_dwordx4 v21, s[32:35], s47 offen offset:0 lds\n" \
"v_mfma_f32_16x16x32_bf16 v[80:83], a[40:43], a[0:3], 0\n" \
"ds_read_b64_tr_b16 a[98:99], v10 offset:16384\n" \
"v_mfma_f32_16x16x32_bf16 v[84:87], a[44:47], a[0:3], 0\n" \
"v_mfma_f32_16x16x32_bf16 v[88:91], a[40:43], a[4:7], 0\n" \
"s_add_u32 m0, s22, 50048\n" \
"ds_read_b128 a[68:71], v8 offset:2048\n" \
"v_mfma_f32_16x16x32_bf16 v[92:95], a[44:47], a[4:7], 0\n" \
"buffer_load_dwordx4 v21, s[32:35], s47 offen offset:128 lds\n" \
"v_mfma_f32_16x16x32_bf16 v[96:99], a[48:51], a[0:3], 0\n" \
"ds_read_b64_tr_b16 a[100:101], v11 offset:16384\n" \
"v_mfma_f32_16x16x32_bf16 v[100:103], a[52:55], a[0:3], 0\n" \
"v_mfma_f32_16x16x32_bf16 v[104:107], a[48:51], a[4:7], 0\n" \
"s_add_u32 m0, s22, 50944\n" \
"ds_read_b64_tr_b16 a[102:103], v12 offset:16384\n" \
"v_mfma_f32_16x16x32_bf16 v[108:111], a[52:55], a[4:7], 0\n" \
"buffer_load_dwordx4 v21, s[32:35], s47 offen offset:256 lds\n" \
"v_mfma_f32_16x16x32_bf16 v[112:115], a[56:59], a[0:3], 0\n" \
"ds_read_b128 a[72:75], v8 offset:4096\n" \
"v_mfma_f32_16x16x32_bf16 v[116:119], a[60:63], a[0:3], 0\n" \
"v_mfma_f32_16x16x32_bf16 v[120:123], a[56:59], a[4:7], 0\n" \
"s_add_u32 m0, s22, 51840\n" \
"ds_read_b64_tr_b16 a[104:105], v9 offset:16896\n" \
"v_mfma_f32_16x16x32_bf16 v[124:127], a[60:63], a[4:7], 0\n" \
"buffer_load_dwordx4 v21, s[32:35], s47 offen offset:384 lds\n" \
"v_mfma_f32_16x16x32_bf16 v[128:131], a[32:35], a[8:11], 0\n" \
"ds_read_b64_tr_b16 a[106:107], v10 offset:16896\n" \
"v_mfma_f32_16x16x32_bf16 v[132:135], a[36:39], a[8:11], 0\n" \
"v_mfma_f32_16x16x32_bf16 v[136:139], a[32:35], a[12:15], 0\n" \
"s_mov_b32 s46, 256\n" \
"s_add_u32 m0, s50, 32768\n" \
"ds_read_b128 a[76:79], v8 offset:6144\n" \
"v_mfma_f32_16x16x32_bf16 v[140:143], a[36:39], a[12:15], 0\n" \
"buffer_load_dwordx4 v13, s[28:31], s46 offen lds\n" \
"v_mfma_f32_16x16x32_bf16 v[144:147], a[40:43], a[8:11], 0\n" \
"ds_read_b64_tr_b16 a[108:109], v11 offset:16896\n" \
"v_mfma_f32_16x16x32_bf16 v[148:151], a[44:47], a[8:11], 0\n" \
"v_mfma_f32_16x16x32_bf16 v[152:155], a[40:43], a[12:15], 0\n" \
"s_add_u32 m0, s50, 36864\n" \
"ds_read_b64_tr_b16 a[110:111], v12 offset:16896\n" \
"v_mfma_f32_16x16x32_bf16 v[156:159], a[44:47], a[12:15], 0\n" \
"buffer_load_dwordx4 v14, s[28:31], s46 offen lds\n" \
"v_mfma_f32_16x16x32_bf16 v[160:163], a[48:51], a[8:11], 0\n" \
"ds_read_b128 a[80:83], v8 offset:8192\n" \
"v_mfma_f32_16x16x32_bf16 v[164:167], a[52:55], a[8:11], 0\n" \
"v_mfma_f32_16x16x32_bf16 v[168:171], a[48:51], a[12:15], 0\n" \
"s_add_u32 m0, s50, 40960\n" \
"ds_read_b64_tr_b16 a[112:113], v9 offset:17408\n" \
"v_mfma_f32_16x16x32_bf16 v[172:175], a[52:55], a[12:15], 0\n" \
"buffer_load_dwordx4 v15, s[28:31], s46 offen lds\n" \
"v_mfma_f32_16x16x32_bf16 v[176:179], a[56:59], a[8:11], 0\n" \
"ds_read_b64_tr_b16 a[114:115], v10 offset:17408\n" \
"v_mfma_f32_16x16x32_bf16 v[180:183], a[60:63], a[8:11], 0\n" \
"v_mfma_f32_16x16x32_bf16 v[184:187], a[56:59], a[12:15], 0\n" \
"s_add_u32 m0, s50, 45056\n" \
"ds_read_b128 a[84:87], v8 offset:10240\n" \
"v_mfma_f32_16x16x32_bf16 v[188:191], a[60:63], a[12:15], 0\n" \
"buffer_load_dwordx4 v16, s[28:31], s46 offen lds\n" \
"v_mfma_f32_16x16x32_bf16 v[192:195], a[32:35], a[16:19], 0\n" \
"ds_read_b64_tr_b16 a[116:117], v11 offset:17408\n" \
"v_mfma_f32_16x16x32_bf16 v[196:199], a[36:39], a[16:19], 0\n" \
"v_mfma_f32_16x16x32_bf16 v[200:203], a[32:35], a[20:23], 0\n" \
"s_mov_b32 s46, 256\n" \
"s_add_u32 m0, s50, 49152\n" \
"ds_read_b64_tr_b16 a[118:119], v12 offset:17408\n" \
"v_mfma_f32_16x16x32_bf16 v[204:207], a[36:39], a[20:23], 0\n" \
"buffer_load_dwordx4 v17, s[28:31], s46 offen lds\n" \
"v_mfma_f32_16x16x32_bf16 v[208:211], a[40:43], a[16:19], 0\n" \
"ds_read_b128 a[88:91], v8 offset:12288\n" \
"v_mfma_f32_16x16x32_bf16 v[212:215], a[44:47], a[16:19], 0\n" \
"v_mfma_f32_16x16x32_bf16 v[216:219], a[40:43], a[20:23], 0\n" \
"s_add_u32 m0, s50, 53248\n" \
"ds_read_b64_tr_b16 a[120:121], v9 offset:17920\n" \
"v_mfma_f32_16x16x32_bf16 v[220:223], a[44:47], a[20:23], 0\n" \
"buffer_load_dwordx4 v18, s[28:31], s46 offen lds\n" \
"v_mfma_f32_16x16x32_bf16 v[224:227], a[48:51], a[16:19], 0\n" \
"ds_read_b64_tr_b16 a[122:123], v10 offset:17920\n" \
"v_mfma_f32_16x16x32_bf16 v[228:231], a[52:55], a[16:19], 0\n" \
"v_mfma_f32_16x16x32_bf16 v[232:235], a[48:51], a[20:23], 0\n" \
"s_add_u32 m0, s50, 57344\n" \
"ds_read_b128 a[92:95], v8 offset:14336\n" \
"v_mfma_f32_16x16x32_bf16 v[236:239], a[52:55], a[20:23], 0\n" \
"buffer_load_dwordx4 v19, s[28:31], s46 offen lds\n" \
"v_mfma_f32_16x16x32_bf16 v[240:243], a[56:59], a[16:19], 0\n" \
"ds_read_b64_tr_b16 a[124:125], v11 offset:17920\n" \
"v_mfma_f32_16x16x32_bf16 v[244:247], a[60:63], a[16:19], 0\n" \
"v_mfma_f32_16x16x32_bf16 v[248:251], a[56:59], a[20:23], 0\n" \
"s_add_u32 m0, s50, 61440\n" \
"ds_read_b64_tr_b16 a[126:127], v12 offset:17920\n" \
"v_mfma_f32_16x16x32_bf16 v[252:255], a[60:63], a[20:23], 0\n" \
"buffer_load_dwordx4 v20, s[28:31], s46 offen lds\n" \
"v_mfma_f32_16x16x32_bf16 a[128:131], a[32:35], a[24:27], 0\n" \
"v_mfma_f32_16x16x32_bf16 a[132:135], a[36:39], a[24:27], 0\n" \
"v_mfma_f32_16x16x32_bf16 a[136:139], a[32:35], a[28:31], 0\n" \
"v_mfma_f32_16x16x32_bf16 a[140:143], a[36:39], a[28:31], 0\n" \
"global_load_dwordx4 v[46:49], v4, s[86:87] offset:0\n" \
"v_mfma_f32_16x16x32_bf16 a[144:147], a[40:43], a[24:27], 0\n" \
"v_mfma_f32_16x16x32_bf16 a[148:151], a[44:47], a[24:27], 0\n" \
"v_mfma_f32_16x16x32_bf16 a[152:155], a[40:43], a[28:31], 0\n" \
"v_mfma_f32_16x16x32_bf16 a[156:159], a[44:47], a[28:31], 0\n" \
"s_waitcnt vmcnt(13) lgkmcnt(0)\n" \
"v_mfma_f32_16x16x32_bf16 a[160:163], a[48:51], a[24:27], 0\n" \
"s_barrier\n" \
"v_mfma_f32_16x16x32_bf16 a[164:167], a[52:55], a[24:27], 0\n" \
"v_mfma_f32_16x16x32_bf16 a[168:171], a[48:51], a[28:31], 0\n" \
"v_mfma_f32_16x16x32_bf16 a[172:175], a[52:55], a[28:31], 0\n" \
"global_load_dwordx4 v[50:53], v4, s[86:87] offset:1024\n" \
"v_mfma_f32_16x16x32_bf16 a[176:179], a[56:59], a[24:27], 0\n" \
"v_mfma_f32_16x16x32_bf16 a[180:183], a[60:63], a[24:27], 0\n" \
"v_mfma_f32_16x16x32_bf16 a[184:187], a[56:59], a[28:31], 0\n" \
"v_mfma_f32_16x16x32_bf16 a[188:191], a[60:63], a[28:31], 0\n" \
"global_load_dwordx4 v[30:33], v26, s[64:65] offset:0\n" \
"v_mfma_f32_16x16x32_bf16 v[64:67], a[96:99], a[64:67], v[64:67]\n" \
"ds_read_b128 a[0:3], v5 offset:0\n" \
"v_mfma_f32_16x16x32_bf16 v[68:71], a[100:103], a[64:67], v[68:71]\n" \
"v_mfma_f32_16x16x32_bf16 v[72:75], a[96:99], a[68:71], v[72:75]\n" \
"ds_read_b64_tr_b16 a[32:33], v9 offset:32768\n" \
"v_mfma_f32_16x16x32_bf16 v[76:79], a[100:103], a[68:71], v[76:79]\n" \
"global_load_dwordx4 v[34:37], v26, s[64:65] offset:1024\n" \
"v_mfma_f32_16x16x32_bf16 v[80:83], a[104:107], a[64:67], v[80:83]\n" \
"ds_read_b64_tr_b16 a[34:35], v10 offset:32768\n" \
"v_mfma_f32_16x16x32_bf16 v[84:87], a[108:111], a[64:67], v[84:87]\n" \
"v_mfma_f32_16x16x32_bf16 v[88:91], a[104:107], a[68:71], v[88:91]\n" \
"ds_read_b128 a[4:7], v5 offset:2048\n" \
"v_mfma_f32_16x16x32_bf16 v[92:95], a[108:111], a[68:71], v[92:95]\n" \
"global_load_dwordx4 v[38:41], v26, s[64:65] offset:2048\n" \
"v_mfma_f32_16x16x32_bf16 v[96:99], a[112:115], a[64:67], v[96:99]\n" \
"ds_read_b64_tr_b16 a[36:37], v11 offset:32768\n" \
"v_mfma_f32_16x16x32_bf16 v[100:103], a[116:119], a[64:67], v[100:103]\n" \
"v_mfma_f32_16x16x32_bf16 v[104:107], a[112:115], a[68:71], v[104:107]\n" \
"ds_read_b64_tr_b16 a[38:39], v12 offset:32768\n" \
"v_mfma_f32_16x16x32_bf16 v[108:111], a[116:119], a[68:71], v[108:111]\n" \
"global_load_dwordx4 v[42:45], v26, s[64:65] offset:3072\n" \
"v_mfma_f32_16x16x32_bf16 v[112:115], a[120:123], a[64:67], v[112:115]\n" \
"ds_read_b128 a[8:11], v5 offset:4096\n" \
"v_mfma_f32_16x16x32_bf16 v[116:119], a[124:127], a[64:67], v[116:119]\n" \
"v_mfma_f32_16x16x32_bf16 v[120:123], a[120:123], a[68:71], v[120:123]\n" \
"s_mov_b32 s47, 1048576\n" \
"s_add_u32 m0, s22, 0\n" \
"ds_read_b64_tr_b16 a[40:41], v9 offset:33280\n" \
"v_mfma_f32_16x16x32_bf16 v[124:127], a[124:127], a[68:71], v[124:127]\n" \
"buffer_load_dwordx4 v21, s[32:35], s47 offen offset:0 lds\n" \
"v_mfma_f32_16x16x32_bf16 v[128:131], a[96:99], a[72:75], v[128:131]\n" \
"ds_read_b64_tr_b16 a[42:43], v10 offset:33280\n" \
"v_mfma_f32_16x16x32_bf16 v[132:135], a[100:103], a[72:75], v[132:135]\n" \
"v_mfma_f32_16x16x32_bf16 v[136:139], a[96:99], a[76:79], v[136:139]\n" \
"s_add_u32 m0, s22, 896\n" \
"ds_read_b128 a[12:15], v5 offset:6144\n" \
"v_mfma_f32_16x16x32_bf16 v[140:143], a[100:103], a[76:79], v[140:143]\n" \
"buffer_load_dwordx4 v21, s[32:35], s47 offen offset:128 lds\n" \
"v_mfma_f32_16x16x32_bf16 v[144:147], a[104:107], a[72:75], v[144:147]\n" \
"ds_read_b64_tr_b16 a[44:45], v11 offset:33280\n" \
"v_mfma_f32_16x16x32_bf16 v[148:151], a[108:111], a[72:75], v[148:151]\n" \
"v_mfma_f32_16x16x32_bf16 v[152:155], a[104:107], a[76:79], v[152:155]\n" \
"s_add_u32 m0, s22, 1792\n" \
"ds_read_b64_tr_b16 a[46:47], v12 offset:33280\n" \
"v_mfma_f32_16x16x32_bf16 v[156:159], a[108:111], a[76:79], v[156:159]\n" \
"buffer_load_dwordx4 v21, s[32:35], s47 offen offset:256 lds\n" \
"v_mfma_f32_16x16x32_bf16 v[160:163], a[112:115], a[72:75], v[160:163]\n" \
"ds_read_b128 a[16:19], v5 offset:8192\n" \
"v_mfma_f32_16x16x32_bf16 v[164:167], a[116:119], a[72:75], v[164:167]\n" \
"v_mfma_f32_16x16x32_bf16 v[168:171], a[112:115], a[76:79], v[168:171]\n" \
"s_add_u32 m0, s22, 2688\n" \
"ds_read_b64_tr_b16 a[48:49], v9 offset:33792\n" \
"v_mfma_f32_16x16x32_bf16 v[172:175], a[116:119], a[76:79], v[172:175]\n" \
"buffer_load_dwordx4 v21, s[32:35], s47 offen offset:384 lds\n" \
"v_mfma_f32_16x16x32_bf16 v[176:179], a[120:123], a[72:75], v[176:179]\n" \
"ds_read_b64_tr_b16 a[50:51], v10 offset:33792\n" \
"v_mfma_f32_16x16x32_bf16 v[180:183], a[124:127], a[72:75], v[180:183]\n" \
"v_mfma_f32_16x16x32_bf16 v[184:187], a[120:123], a[76:79], v[184:187]\n" \
"ds_read_b128 a[20:23], v5 offset:10240\n" \
"v_mfma_f32_16x16x32_bf16 v[188:191], a[124:127], a[76:79], v[188:191]\n" \
"global_load_dwordx4 a[230:233], v27, s[70:71] offset:0\n" \
"v_mfma_f32_16x16x32_bf16 v[192:195], a[96:99], a[80:83], v[192:195]\n" \
"ds_read_b64_tr_b16 a[52:53], v11 offset:33792\n" \
"v_mfma_f32_16x16x32_bf16 v[196:199], a[100:103], a[80:83], v[196:199]\n" \
"v_mfma_f32_16x16x32_bf16 v[200:203], a[96:99], a[84:87], v[200:203]\n" \
"ds_read_b64_tr_b16 a[54:55], v12 offset:33792\n" \
"v_mfma_f32_16x16x32_bf16 v[204:207], a[100:103], a[84:87], v[204:207]\n" \
"global_load_dwordx4 a[234:237], v27, s[70:71] offset:16\n" \
"v_mfma_f32_16x16x32_bf16 v[208:211], a[104:107], a[80:83], v[208:211]\n" \
"ds_read_b128 a[24:27], v5 offset:12288\n" \
"v_mfma_f32_16x16x32_bf16 v[212:215], a[108:111], a[80:83], v[212:215]\n" \
"v_mfma_f32_16x16x32_bf16 v[216:219], a[104:107], a[84:87], v[216:219]\n" \
"ds_read_b64_tr_b16 a[56:57], v9 offset:34304\n" \
"v_mfma_f32_16x16x32_bf16 v[220:223], a[108:111], a[84:87], v[220:223]\n" \
"buffer_load_dwordx2 a[192:193], v54, s[40:43], 0 offen offset:0\n" \
"v_mfma_f32_16x16x32_bf16 v[224:227], a[112:115], a[80:83], v[224:227]\n" \
"ds_read_b64_tr_b16 a[58:59], v10 offset:34304\n" \
"v_mfma_f32_16x16x32_bf16 v[228:231], a[116:119], a[80:83], v[228:231]\n" \
"v_mfma_f32_16x16x32_bf16 v[232:235], a[112:115], a[84:87], v[232:235]\n" \
"ds_read_b128 a[28:31], v5 offset:14336\n" \
"v_mfma_f32_16x16x32_bf16 v[236:239], a[116:119], a[84:87], v[236:239]\n" \
"buffer_load_dwordx2 a[194:195], v54, s[40:43], 0 offen offset:2048\n" \
"v_mfma_f32_16x16x32_bf16 v[240:243], a[120:123], a[80:83], v[240:243]\n" \
"ds_read_b64_tr_b16 a[60:61], v11 offset:34304\n" \
"v_mfma_f32_16x16x32_bf16 v[244:247], a[124:127], a[80:83], v[244:247]\n" \
"v_mfma_f32_16x16x32_bf16 v[248:251], a[120:123], a[84:87], v[248:251]\n" \
"ds_read_b64_tr_b16 a[62:63], v12 offset:34304\n" \
"v_mfma_f32_16x16x32_bf16 v[252:255], a[124:127], a[84:87], v[252:255]\n" \
"buffer_load_dwordx2 a[196:197], v55, s[40:43], 0 offen offset:0\n" \
"v_mfma_f32_16x16x32_bf16 a[128:131], a[96:99], a[88:91], a[128:131]\n" \
"v_mfma_f32_16x16x32_bf16 a[132:135], a[100:103], a[88:91], a[132:135]\n" \
"v_mfma_f32_16x16x32_bf16 a[136:139], a[96:99], a[92:95], a[136:139]\n" \
"s_mov_b32 s52, 0\n" \
"v_mfma_f32_16x16x32_bf16 a[140:143], a[100:103], a[92:95], a[140:143]\n" \
"buffer_load_dwordx2 a[198:199], v59, s[40:43], s52 offen offset:0\n" \
"v_mfma_f32_16x16x32_bf16 a[144:147], a[104:107], a[88:91], a[144:147]\n" \
"v_mfma_f32_16x16x32_bf16 a[148:151], a[108:111], a[88:91], a[148:151]\n" \
"v_mfma_f32_16x16x32_bf16 a[152:155], a[104:107], a[92:95], a[152:155]\n" \
"v_mfma_f32_16x16x32_bf16 a[156:159], a[108:111], a[92:95], a[156:159]\n" \
"s_waitcnt vmcnt(24) lgkmcnt(0)\n" \
"v_mfma_f32_16x16x32_bf16 a[160:163], a[112:115], a[88:91], a[160:163]\n" \
"s_barrier\n" \
"v_mfma_f32_16x16x32_bf16 a[164:167], a[116:119], a[88:91], a[164:167]\n" \
"v_mfma_f32_16x16x32_bf16 a[168:171], a[112:115], a[92:95], a[168:171]\n" \
"v_mfma_f32_16x16x32_bf16 a[172:175], a[116:119], a[92:95], a[172:175]\n" \
"buffer_load_dwordx2 a[214:215], v59, s[80:83], s52 offen offset:0\n" \
"v_mfma_f32_16x16x32_bf16 a[176:179], a[120:123], a[88:91], a[176:179]\n" \
"v_mfma_f32_16x16x32_bf16 a[180:183], a[124:127], a[88:91], a[180:183]\n" \
"s_waitcnt vmcnt(11)\n" \
"v_lshlrev_b32 v25, 16, v46\n" \
"v_mfma_f32_16x16x32_bf16 a[184:187], a[120:123], a[92:95], a[184:187]\n" \
"v_and_b32 v46, 0xffff0000, v46\n" \
"v_lshlrev_b32 v26, 16, v30\n" \
"v_mfma_f32_16x16x32_bf16 a[188:191], a[124:127], a[92:95], a[188:191]\n" \
"buffer_load_dwordx2 a[200:201], v59, s[40:43], s52 offen offset:2048\n" \
"v_mfma_f32_16x16x32_bf16 v[64:67], a[32:35], a[0:3], v[64:67]\n" \
"ds_read_b128 a[64:67], v6 offset:0\n" \
"v_and_b32 v30, 0xffff0000, v30\n" \
"v_mfma_f32_16x16x32_bf16 v[68:71], a[36:39], a[0:3], v[68:71]\n" \
"v_mul_f32 v23, v26, v25\n" \
"v_fmac_f32 v23, v30, v46\n" \
"v_mfma_f32_16x16x32_bf16 v[72:75], a[32:35], a[4:7], v[72:75]\n" \
"s_mov_b32 s47, 1310720\n" \
"s_add_u32 m0, s22, 16384\n" \
"ds_read_b64_tr_b16 a[96:97], v9 offset:49152\n" \
"v_mfma_f32_16x16x32_bf16 v[76:79], a[36:39], a[4:7], v[76:79]\n" \
"buffer_load_dwordx4 v21, s[32:35], s47 offen offset:0 lds\n" \
"v_mfma_f32_16x16x32_bf16 v[80:83], a[40:43], a[0:3], v[80:83]\n" \
"ds_read_b64_tr_b16 a[98:99], v10 offset:49152\n" \
"v_lshlrev_b32 v26, 16, v38\n" \
"v_mfma_f32_16x16x32_bf16 v[84:87], a[44:47], a[0:3], v[84:87]\n" \
"v_and_b32 v38, 0xffff0000, v38\n" \
"v_mul_f32 v24, v26, v25\n" \
"v_mfma_f32_16x16x32_bf16 v[88:91], a[40:43], a[4:7], v[88:91]\n" \
"s_add_u32 m0, s22, 17280\n" \
"ds_read_b128 a[68:71], v6 offset:2048\n" \
"v_fmac_f32 v24, v38, v46\n" \
"v_mfma_f32_16x16x32_bf16 v[92:95], a[44:47], a[4:7], v[92:95]\n" \
"buffer_load_dwordx4 v21, s[32:35], s47 offen offset:128 lds\n" \
"v_mfma_f32_16x16x32_bf16 v[96:99], a[48:51], a[0:3], v[96:99]\n" \
"ds_read_b64_tr_b16 a[100:101], v11 offset:49152\n" \
"v_lshlrev_b32 v25, 16, v47\n" \
"v_mfma_f32_16x16x32_bf16 v[100:103], a[52:55], a[0:3], v[100:103]\n" \
"v_and_b32 v47, 0xffff0000, v47\n" \
"v_lshlrev_b32 v26, 16, v31\n" \
"v_mfma_f32_16x16x32_bf16 v[104:107], a[48:51], a[4:7], v[104:107]\n" \
"s_add_u32 m0, s22, 18176\n" \
"ds_read_b64_tr_b16 a[102:103], v12 offset:49152\n" \
"v_and_b32 v31, 0xffff0000, v31\n" \
"v_mfma_f32_16x16x32_bf16 v[108:111], a[52:55], a[4:7], v[108:111]\n" \
"buffer_load_dwordx4 v21, s[32:35], s47 offen offset:256 lds\n" \
"v_mfma_f32_16x16x32_bf16 v[112:115], a[56:59], a[0:3], v[112:115]\n" \
"ds_read_b128 a[72:75], v6 offset:4096\n" \
"v_fmac_f32 v23, v26, v25\n" \
"v_mfma_f32_16x16x32_bf16 v[116:119], a[60:63], a[0:3], v[116:119]\n" \
"v_fmac_f32 v23, v31, v47\n" \
"v_lshlrev_b32 v26, 16, v39\n" \
"v_mfma_f32_16x16x32_bf16 v[120:123], a[56:59], a[4:7], v[120:123]\n" \
"s_add_u32 m0, s22, 19072\n" \
"ds_read_b64_tr_b16 a[104:105], v9 offset:49664\n" \
"v_and_b32 v39, 0xffff0000, v39\n" \
"v_mfma_f32_16x16x32_bf16 v[124:127], a[60:63], a[4:7], v[124:127]\n" \
"buffer_load_dwordx4 v21, s[32:35], s47 offen offset:384 lds\n" \
"v_mfma_f32_16x16x32_bf16 v[128:131], a[32:35], a[8:11], v[128:131]\n" \
"ds_read_b64_tr_b16 a[106:107], v10 offset:49664\n" \
"v_fmac_f32 v24, v26, v25\n" \
"v_mfma_f32_16x16x32_bf16 v[132:135], a[36:39], a[8:11], v[132:135]\n" \
"v_fmac_f32 v24, v39, v47\n" \
"v_lshlrev_b32 v25, 16, v48\n" \
"v_mfma_f32_16x16x32_bf16 v[136:139], a[32:35], a[12:15], v[136:139]\n" \
"ds_read_b128 a[76:79], v6 offset:6144\n" \
"v_and_b32 v48, 0xffff0000, v48\n" \
"v_mfma_f32_16x16x32_bf16 v[140:143], a[36:39], a[12:15], v[140:143]\n" \
"buffer_load_dwordx2 a[216:217], v59, s[80:83], s52 offen offset:2048\n" \
"v_mfma_f32_16x16x32_bf16 v[144:147], a[40:43], a[8:11], v[144:147]\n" \
"ds_read_b64_tr_b16 a[108:109], v11 offset:49664\n" \
"v_lshlrev_b32 v26, 16, v32\n" \
"v_mfma_f32_16x16x32_bf16 v[148:151], a[44:47], a[8:11], v[148:151]\n" \
"v_and_b32 v32, 0xffff0000, v32\n" \
"v_fmac_f32 v23, v26, v25\n" \
"v_mfma_f32_16x16x32_bf16 v[152:155], a[40:43], a[12:15], v[152:155]\n" \
"s_mov_b32 s52, 4096\n" \
"ds_read_b64_tr_b16 a[110:111], v12 offset:49664\n" \
"v_fmac_f32 v23, v32, v48\n" \
"v_mfma_f32_16x16x32_bf16 v[156:159], a[44:47], a[12:15], v[156:159]\n" \
"buffer_load_dwordx2 a[202:203], v59, s[40:43], s52 offen offset:0\n" \
"v_mfma_f32_16x16x32_bf16 v[160:163], a[48:51], a[8:11], v[160:163]\n" \
"ds_read_b128 a[80:83], v6 offset:8192\n" \
"v_lshlrev_b32 v26, 16, v40\n" \
"v_mfma_f32_16x16x32_bf16 v[164:167], a[52:55], a[8:11], v[164:167]\n" \
"v_and_b32 v40, 0xffff0000, v40\n" \
"v_fmac_f32 v24, v26, v25\n" \
"v_mfma_f32_16x16x32_bf16 v[168:171], a[48:51], a[12:15], v[168:171]\n" \
"ds_read_b64_tr_b16 a[112:113], v9 offset:50176\n" \
"v_fmac_f32 v24, v40, v48\n" \
"v_mfma_f32_16x16x32_bf16 v[172:175], a[52:55], a[12:15], v[172:175]\n" \
"buffer_load_dwordx2 a[218:219], v59, s[80:83], s52 offen offset:0\n" \
"v_mfma_f32_16x16x32_bf16 v[176:179], a[56:59], a[8:11], v[176:179]\n" \
"ds_read_b64_tr_b16 a[114:115], v10 offset:50176\n" \
"v_lshlrev_b32 v25, 16, v49\n" \
"v_mfma_f32_16x16x32_bf16 v[180:183], a[60:63], a[8:11], v[180:183]\n" \
"v_and_b32 v49, 0xffff0000, v49\n" \
"v_lshlrev_b32 v26, 16, v33\n" \
"v_mfma_f32_16x16x32_bf16 v[184:187], a[56:59], a[12:15], v[184:187]\n" \
"ds_read_b128 a[84:87], v6 offset:10240\n" \
"v_and_b32 v33, 0xffff0000, v33\n" \
"v_mfma_f32_16x16x32_bf16 v[188:191], a[60:63], a[12:15], v[188:191]\n" \
"buffer_load_dwordx2 a[204:205], v59, s[40:43], s52 offen offset:2048\n" \
"v_mfma_f32_16x16x32_bf16 v[192:195], a[32:35], a[16:19], v[192:195]\n" \
"ds_read_b64_tr_b16 a[116:117], v11 offset:50176\n" \
"v_fmac_f32 v23, v26, v25\n" \
"v_mfma_f32_16x16x32_bf16 v[196:199], a[36:39], a[16:19], v[196:199]\n" \
"v_fmac_f32 v23, v33, v49\n" \
"v_lshlrev_b32 v26, 16, v41\n" \
"v_mfma_f32_16x16x32_bf16 v[200:203], a[32:35], a[20:23], v[200:203]\n" \
"ds_read_b64_tr_b16 a[118:119], v12 offset:50176\n" \
"v_and_b32 v41, 0xffff0000, v41\n" \
"v_mfma_f32_16x16x32_bf16 v[204:207], a[36:39], a[20:23], v[204:207]\n" \
"buffer_load_dwordx2 a[220:221], v59, s[80:83], s52 offen offset:2048\n" \
"v_mfma_f32_16x16x32_bf16 v[208:211], a[40:43], a[16:19], v[208:211]\n" \
"ds_read_b128 a[88:91], v6 offset:12288\n" \
"v_fmac_f32 v24, v26, v25\n" \
"v_mfma_f32_16x16x32_bf16 v[212:215], a[44:47], a[16:19], v[212:215]\n" \
"v_fmac_f32 v24, v41, v49\n" \
"v_lshlrev_b32 v25, 16, v50\n" \
"v_mfma_f32_16x16x32_bf16 v[216:219], a[40:43], a[20:23], v[216:219]\n" \
"s_mov_b32 s52, 8192\n" \
"ds_read_b64_tr_b16 a[120:121], v9 offset:50688\n" \
"v_and_b32 v50, 0xffff0000, v50\n" \
"v_mfma_f32_16x16x32_bf16 v[220:223], a[44:47], a[20:23], v[220:223]\n" \
"buffer_load_dwordx2 a[206:207], v59, s[40:43], s52 offen offset:0\n" \
"v_mfma_f32_16x16x32_bf16 v[224:227], a[48:51], a[16:19], v[224:227]\n" \
"ds_read_b64_tr_b16 a[122:123], v10 offset:50688\n" \
"v_lshlrev_b32 v26, 16, v34\n" \
"v_mfma_f32_16x16x32_bf16 v[228:231], a[52:55], a[16:19], v[228:231]\n" \
"v_and_b32 v34, 0xffff0000, v34\n" \
"v_fmac_f32 v23, v26, v25\n" \
"v_mfma_f32_16x16x32_bf16 v[232:235], a[48:51], a[20:23], v[232:235]\n" \
"ds_read_b128 a[92:95], v6 offset:14336\n" \
"v_fmac_f32 v23, v34, v50\n" \
"v_mfma_f32_16x16x32_bf16 v[236:239], a[52:55], a[20:23], v[236:239]\n" \
"buffer_load_dwordx2 a[222:223], v59, s[80:83], s52 offen offset:0\n" \
"v_mfma_f32_16x16x32_bf16 v[240:243], a[56:59], a[16:19], v[240:243]\n" \
"ds_read_b64_tr_b16 a[124:125], v11 offset:50688\n" \
"v_lshlrev_b32 v26, 16, v42\n" \
"v_mfma_f32_16x16x32_bf16 v[244:247], a[60:63], a[16:19], v[244:247]\n" \
"v_and_b32 v42, 0xffff0000, v42\n" \
"v_fmac_f32 v24, v26, v25\n" \
"v_mfma_f32_16x16x32_bf16 v[248:251], a[56:59], a[20:23], v[248:251]\n" \
"s_mov_b32 s47, 1572864\n" \
"s_add_u32 m0, s22, 32768\n" \
"ds_read_b64_tr_b16 a[126:127], v12 offset:50688\n" \
"v_mfma_f32_16x16x32_bf16 v[252:255], a[60:63], a[20:23], v[252:255]\n" \
"buffer_load_dwordx4 v21, s[32:35], s47 offen offset:0 lds\n" \
"v_mfma_f32_16x16x32_bf16 a[128:131], a[32:35], a[24:27], a[128:131]\n" \
"v_fmac_f32 v24, v42, v50\n" \
"v_lshlrev_b32 v25, 16, v51\n" \
"v_mfma_f32_16x16x32_bf16 a[132:135], a[36:39], a[24:27], a[132:135]\n" \
"v_and_b32 v51, 0xffff0000, v51\n" \
"v_lshlrev_b32 v26, 16, v35\n" \
"v_mfma_f32_16x16x32_bf16 a[136:139], a[32:35], a[28:31], a[136:139]\n" \
"s_add_u32 m0, s22, 33664\n" \
"v_and_b32 v35, 0xffff0000, v35\n" \
"v_mfma_f32_16x16x32_bf16 a[140:143], a[36:39], a[28:31], a[140:143]\n" \
"buffer_load_dwordx4 v21, s[32:35], s47 offen offset:128 lds\n" \
"v_mfma_f32_16x16x32_bf16 a[144:147], a[40:43], a[24:27], a[144:147]\n" \
"v_fmac_f32 v23, v26, v25\n" \
"v_fmac_f32 v23, v35, v51\n" \
"v_mfma_f32_16x16x32_bf16 a[148:151], a[44:47], a[24:27], a[148:151]\n" \
"v_lshlrev_b32 v26, 16, v43\n" \
"v_and_b32 v43, 0xffff0000, v43\n" \
"v_mfma_f32_16x16x32_bf16 a[152:155], a[40:43], a[28:31], a[152:155]\n" \
"v_fmac_f32 v24, v26, v25\n" \
"v_fmac_f32 v24, v43, v51\n" \
"v_mfma_f32_16x16x32_bf16 a[156:159], a[44:47], a[28:31], a[156:159]\n" \
"s_waitcnt vmcnt(21) lgkmcnt(0)\n" \
"v_lshlrev_b32 v25, 16, v52\n" \
"v_mfma_f32_16x16x32_bf16 a[160:163], a[48:51], a[24:27], a[160:163]\n" \
"s_barrier\n" \
"v_mfma_f32_16x16x32_bf16 a[164:167], a[52:55], a[24:27], a[164:167]\n" \
"v_and_b32 v52, 0xffff0000, v52\n" \
"v_lshlrev_b32 v26, 16, v36\n" \
"v_mfma_f32_16x16x32_bf16 a[168:171], a[48:51], a[28:31], a[168:171]\n" \
"s_add_u32 m0, s22, 34560\n" \
"v_and_b32 v36, 0xffff0000, v36\n" \
"v_mfma_f32_16x16x32_bf16 a[172:175], a[52:55], a[28:31], a[172:175]\n" \
"buffer_load_dwordx4 v21, s[32:35], s47 offen offset:256 lds\n" \
"v_mfma_f32_16x16x32_bf16 a[176:179], a[56:59], a[24:27], a[176:179]\n" \
"s_add_u32 m0, s22, 35456\n" \
"v_fmac_f32 v23, v26, v25\n" \
"v_mfma_f32_16x16x32_bf16 a[180:183], a[60:63], a[24:27], a[180:183]\n" \
"buffer_load_dwordx4 v21, s[32:35], s47 offen offset:384 lds\n" \
"v_mfma_f32_16x16x32_bf16 a[184:187], a[56:59], a[28:31], a[184:187]\n" \
"s_add_u32 s84, s84, 1048576\ns_addc_u32 s85, s85, 0\n" \
"v_fmac_f32 v23, v36, v52\n" \
"v_mfma_f32_16x16x32_bf16 a[188:191], a[60:63], a[28:31], a[188:191]\n" \
"s_mov_b64 exec, 0xffff\nglobal_load_dword a244, v1, s[84:85]\ns_mov_b64 exec, -1\n" \
"v_mfma_f32_16x16x32_bf16 v[64:67], a[96:99], a[64:67], v[64:67]\n" \
"ds_read_b128 a[0:3], v5 offset:32768\n" \
"v_lshlrev_b32 v26, 16, v44\n" \
"v_and_b32 v44, 0xffff0000, v44\n" \
"v_mfma_f32_16x16x32_bf16 v[68:71], a[100:103], a[64:67], v[68:71]\n" \
"v_fmac_f32 v24, v26, v25\n" \
"v_fmac_f32 v24, v44, v52\n" \
"v_mfma_f32_16x16x32_bf16 v[72:75], a[96:99], a[68:71], v[72:75]\n" \
"s_mov_b32 s46, 384\n" \
"s_add_u32 m0, s50, 65536\n" \
"ds_read_b64_tr_b16 a[32:33], v9 offset:0\n" \
"v_mfma_f32_16x16x32_bf16 v[76:79], a[100:103], a[68:71], v[76:79]\n" \
"buffer_load_dwordx4 v13, s[28:31], s46 offen lds\n" \
"v_mfma_f32_16x16x32_bf16 v[80:83], a[104:107], a[64:67], v[80:83]\n" \
"ds_read_b64_tr_b16 a[34:35], v10 offset:0\n" \
"v_lshlrev_b32 v25, 16, v53\n" \
"v_and_b32 v53, 0xffff0000, v53\n" \
"v_mfma_f32_16x16x32_bf16 v[84:87], a[108:111], a[64:67], v[84:87]\n" \
"v_lshlrev_b32 v26, 16, v37\n" \
"v_and_b32 v37, 0xffff0000, v37\n" \
"v_mfma_f32_16x16x32_bf16 v[88:91], a[104:107], a[68:71], v[88:91]\n" \
"s_add_u32 m0, s50, 69632\n" \
"ds_read_b128 a[4:7], v5 offset:34816\n" \
"v_fmac_f32 v23, v26, v25\n" \
"v_mfma_f32_16x16x32_bf16 v[92:95], a[108:111], a[68:71], v[92:95]\n" \
"buffer_load_dwordx4 v14, s[28:31], s46 offen lds\n" \
"v_mfma_f32_16x16x32_bf16 v[96:99], a[112:115], a[64:67], v[96:99]\n" \
"ds_read_b64_tr_b16 a[36:37], v11 offset:0\n" \
"v_fmac_f32 v23, v37, v53\n" \
"v_lshlrev_b32 v26, 16, v45\n" \
"v_mfma_f32_16x16x32_bf16 v[100:103], a[116:119], a[64:67], v[100:103]\n" \
"v_and_b32 v45, 0xffff0000, v45\n" \
"v_fmac_f32 v24, v26, v25\n" \
"v_mfma_f32_16x16x32_bf16 v[104:107], a[112:115], a[68:71], v[104:107]\n" \
"s_add_u32 m0, s50, 73728\n" \
"ds_read_b64_tr_b16 a[38:39], v12 offset:0\n" \
"v_fmac_f32 v24, v45, v53\n" \
"v_mfma_f32_16x16x32_bf16 v[108:111], a[116:119], a[68:71], v[108:111]\n" \
"buffer_load_dwordx4 v15, s[28:31], s46 offen lds\n" \
"s_waitcnt vmcnt(10)\n" \
"v_mfma_f32_16x16x32_bf16 v[112:115], a[120:123], a[64:67], v[112:115]\n" \
"ds_read_b128 a[8:11], v5 offset:36864\n" \
"v_accvgpr_read_b32 v43, a230\n" \
"v_permlane32_swap_b32 v23, v24\n" \
"v_mfma_f32_16x16x32_bf16 v[116:119], a[124:127], a[64:67], v[116:119]\n" \
"v_add_f32 v23, v23, v24\n" \
"v_mov_b32 v24, v23\n" \
"v_mfma_f32_16x16x32_bf16 v[120:123], a[120:123], a[68:71], v[120:123]\n" \
"s_add_u32 m0, s50, 77824\n" \
"ds_read_b64_tr_b16 a[40:41], v9 offset:512\n" \
"v_lshlrev_b32 v42, 16, v43\n" \
"v_mfma_f32_16x16x32_bf16 v[124:127], a[124:127], a[68:71], v[124:127]\n" \
"buffer_load_dwordx4 v16, s[28:31], s46 offen lds\n" \
"v_mfma_f32_16x16x32_bf16 v[128:131], a[96:99], a[72:75], v[128:131]\n" \
"ds_read_b64_tr_b16 a[42:43], v10 offset:512\n" \
"v_and_b32 v43, 0xffff0000, v43\n" \
"v_permlane16_swap_b32 v23, v24\n" \
"v_mfma_f32_16x16x32_bf16 v[132:135], a[100:103], a[72:75], v[132:135]\n" \
"v_add_f32 v23, v23, v24\n" \
"v_accvgpr_read_b32 v45, a231\n" \
"v_mfma_f32_16x16x32_bf16 v[136:139], a[96:99], a[76:79], v[136:139]\n" \
"s_mov_b32 s46, 384\n" \
"s_add_u32 m0, s50, 81920\n" \
"ds_read_b128 a[12:15], v5 offset:38912\n" \
"v_mfma_f32_16x16x32_bf16 v[140:143], a[100:103], a[76:79], v[140:143]\n" \
"buffer_load_dwordx4 v17, s[28:31], s46 offen lds\n" \
"v_mfma_f32_16x16x32_bf16 v[144:147], a[104:107], a[72:75], v[144:147]\n" \
"ds_read_b64_tr_b16 a[44:45], v11 offset:512\n" \
"v_lshlrev_b32 v44, 16, v45\n" \
"v_add_f32 v23, v23, v23 row_ror:8 row_mask:0xf bank_mask:0xf\n" \
"v_mfma_f32_16x16x32_bf16 v[148:151], a[108:111], a[72:75], v[148:151]\n" \
"v_and_b32 v45, 0xffff0000, v45\n" \
"v_accvgpr_read_b32 v47, a232\n" \
"v_mfma_f32_16x16x32_bf16 v[152:155], a[104:107], a[76:79], v[152:155]\n" \
"s_add_u32 m0, s50, 86016\n" \
"ds_read_b64_tr_b16 a[46:47], v12 offset:512\n" \
"v_add_f32 v23, v23, v23 row_ror:4 row_mask:0xf bank_mask:0xf\n" \
"v_mfma_f32_16x16x32_bf16 v[156:159], a[108:111], a[76:79], v[156:159]\n" \
"buffer_load_dwordx4 v18, s[28:31], s46 offen lds\n" \
"v_mfma_f32_16x16x32_bf16 v[160:163], a[112:115], a[72:75], v[160:163]\n" \
"ds_read_b128 a[16:19], v5 offset:40960\n" \
"v_lshlrev_b32 v46, 16, v47\n" \
"v_and_b32 v47, 0xffff0000, v47\n" \
"v_mfma_f32_16x16x32_bf16 v[164:167], a[116:119], a[72:75], v[164:167]\n" \
"v_add_f32 v23, v23, v23 row_ror:2 row_mask:0xf bank_mask:0xf\n" \
"v_accvgpr_read_b32 v49, a233\n" \
"v_mfma_f32_16x16x32_bf16 v[168:171], a[112:115], a[76:79], v[168:171]\n" \
"s_add_u32 m0, s50, 90112\n" \
"ds_read_b64_tr_b16 a[48:49], v9 offset:1024\n" \
"v_lshlrev_b32 v48, 16, v49\n" \
"v_mfma_f32_16x16x32_bf16 v[172:175], a[116:119], a[76:79], v[172:175]\n" \
"buffer_load_dwordx4 v19, s[28:31], s46 offen lds\n" \
"v_mfma_f32_16x16x32_bf16 v[176:179], a[120:123], a[72:75], v[176:179]\n" \
"ds_read_b64_tr_b16 a[50:51], v10 offset:1024\n" \
"v_add_f32 v23, v23, v23 row_ror:1 row_mask:0xf bank_mask:0xf\n" \
"v_cvt_pk_bf16_f32 v23, v23, v23\n" \
"v_mfma_f32_16x16x32_bf16 v[180:183], a[124:127], a[72:75], v[180:183]\n" \
"v_and_b32 v23, 0xffff0000, v23\n" \
"v_mul_f32 v23, 0xbfb8aa3b, v23\n" \
"v_mfma_f32_16x16x32_bf16 v[184:187], a[120:123], a[76:79], v[184:187]\n" \
"s_add_u32 m0, s50, 94208\n" \
"ds_read_b128 a[20:23], v5 offset:43008\n" \
"v_exp_f32 v23, v23\n" \
"v_mfma_f32_16x16x32_bf16 v[188:191], a[124:127], a[76:79], v[188:191]\n" \
"buffer_load_dwordx4 v20, s[28:31], s46 offen lds\n" \
"v_mfma_f32_16x16x32_bf16 v[192:195], a[96:99], a[80:83], v[192:195]\n" \
"ds_read_b64_tr_b16 a[52:53], v11 offset:1024\n" \
"v_and_b32 v49, 0xffff0000, v49\n" \
"v_accvgpr_read_b32 v36, a192\n" \
"v_mfma_f32_16x16x32_bf16 v[196:199], a[100:103], a[80:83], v[196:199]\n" \
"v_add_f32 v23, 1.0, v23\n" \
"v_rcp_f32 v23, v23\n" \
"v_mfma_f32_16x16x32_bf16 v[200:203], a[96:99], a[84:87], v[200:203]\n" \
"s_mov_b32 s47, 1835008\n" \
"s_add_u32 m0, s22, 49152\n" \
"ds_read_b64_tr_b16 a[54:55], v12 offset:1024\n" \
"v_mfma_f32_16x16x32_bf16 v[204:207], a[100:103], a[84:87], v[204:207]\n" \
"buffer_load_dwordx4 v21, s[32:35], s47 offen offset:0 lds\n" \
"v_mfma_f32_16x16x32_bf16 v[208:211], a[104:107], a[80:83], v[208:211]\n" \
"ds_read_b128 a[24:27], v5 offset:45056\n" \
"v_lshlrev_b32 v30, 16, v36\n" \
"v_and_b32 v36, 0xffff0000, v36\n" \
"v_mfma_f32_16x16x32_bf16 v[212:215], a[108:111], a[80:83], v[212:215]\n" \
"v_cvt_pk_bf16_f32 v23, v23, v23\n" \
"v_and_b32 v23, 0xffff0000, v23\n" \
"v_mfma_f32_16x16x32_bf16 v[216:219], a[104:107], a[84:87], v[216:219]\n" \
"s_add_u32 m0, s22, 50048\n" \
"ds_read_b64_tr_b16 a[56:57], v9 offset:1536\n" \
"v_accvgpr_read_b32 v37, a194\n" \
"v_mfma_f32_16x16x32_bf16 v[220:223], a[108:111], a[84:87], v[220:223]\n" \
"LH0_%=:\ns_cbranch_execz LH8_%=\n" \
"buffer_load_dwordx4 v21, s[32:35], s47 offen offset:128 lds\n" \
"v_mfma_f32_16x16x32_bf16 v[224:227], a[112:115], a[80:83], v[224:227]\n" \
"ds_read_b64_tr_b16 a[58:59], v10 offset:1536\n" \
"v_lshlrev_b32 v31, 16, v37\n" \
"v_and_b32 v37, 0xffff0000, v37\n" \
"v_mfma_f32_16x16x32_bf16 v[228:231], a[116:119], a[80:83], v[228:231]\n" \
"v_accvgpr_read_b32 v38, a196\n" \
"v_lshlrev_b32 v32, 16, v38\n" \
"v_mfma_f32_16x16x32_bf16 v[232:235], a[112:115], a[84:87], v[232:235]\n" \
"s_add_u32 m0, s22, 50944\n" \
"ds_read_b128 a[28:31], v5 offset:47104\n" \
"v_and_b32 v38, 0xffff0000, v38\n" \
"v_mfma_f32_16x16x32_bf16 v[236:239], a[116:119], a[84:87], v[236:239]\n" \
"buffer_load_dwordx4 v21, s[32:35], s47 offen offset:256 lds\n" \
"v_mfma_f32_16x16x32_bf16 v[240:243], a[120:123], a[80:83], v[240:243]\n" \
"ds_read_b64_tr_b16 a[60:61], v11 offset:1536\n" \
"v_accvgpr_read_b32 v39, a198\n" \
"v_lshlrev_b32 v33, 16, v39\n" \
"v_mfma_f32_16x16x32_bf16 v[244:247], a[124:127], a[80:83], v[244:247]\n" \
"v_and_b32 v39, 0xffff0000, v39\n" \
"v_mul_f32 v25, v30, v45\n" \
"v_mfma_f32_16x16x32_bf16 v[248:251], a[120:123], a[84:87], v[248:251]\n" \
"s_add_u32 m0, s22, 51840\n" \
"ds_read_b64_tr_b16 a[62:63], v12 offset:1536\n" \
"v_mul_f32 v26, v36, v49\n" \
"v_mfma_f32_16x16x32_bf16 v[252:255], a[124:127], a[84:87], v[252:255]\n" \
"buffer_load_dwordx4 v21, s[32:35], s47 offen offset:384 lds\n" \
"v_mfma_f32_16x16x32_bf16 a[128:131], a[96:99], a[88:91], a[128:131]\n" \
"v_fmac_f32 v25, v31, v44\n" \
"v_fmac_f32 v26, v37, v48\n" \
"v_mfma_f32_16x16x32_bf16 a[132:135], a[100:103], a[88:91], a[132:135]\n" \
"v_fmac_f32 v25, v32, v43\n" \
"v_fmac_f32 v26, v38, v47\n" \
"v_mfma_f32_16x16x32_bf16 a[136:139], a[96:99], a[92:95], a[136:139]\n" \
"v_fmac_f32 v25, v33, v42\n" \
"v_fmac_f32 v26, v39, v46\n" \
"v_mfma_f32_16x16x32_bf16 a[140:143], a[100:103], a[92:95], a[140:143]\n" \
"buffer_load_dwordx2 a[208:209], v59, s[40:43], s52 offen offset:2048\n" \
"v_mfma_f32_16x16x32_bf16 a[144:147], a[104:107], a[88:91], a[144:147]\n" \
"v_accvgpr_read_b32 v28, a214\n" \
"v_cvt_pk_bf16_f32 v26, v25, v26\n" \
"v_mfma_f32_16x16x32_bf16 a[148:151], a[108:111], a[88:91], a[148:151]\n" \
"v_lshlrev_b32 v27, 16, v28\n" \
"v_lshlrev_b32 v25, 16, v26\n" \
"v_mfma_f32_16x16x32_bf16 a[152:155], a[104:107], a[92:95], a[152:155]\n" \
"v_and_b32 v28, 0xffff0000, v28\n" \
"v_and_b32 v26, 0xffff0000, v26\n" \
"v_mfma_f32_16x16x32_bf16 a[156:159], a[108:111], a[92:95], a[156:159]\n" \
"s_waitcnt vmcnt(25) lgkmcnt(0)\n" \
"v_mul_f32 v29, v25, v27\n" \
"v_mul_f32 v58, v26, v28\n" \
"v_mfma_f32_16x16x32_bf16 a[160:163], a[112:115], a[88:91], a[160:163]\n" \
"s_barrier\n" \
"v_mfma_f32_16x16x32_bf16 a[164:167], a[116:119], a[88:91], a[164:167]\n" \
"v_accvgpr_read_b32 v40, a200\n" \
"v_lshlrev_b32 v34, 16, v40\n" \
"v_mfma_f32_16x16x32_bf16 a[168:171], a[112:115], a[92:95], a[168:171]\n" \
"v_and_b32 v40, 0xffff0000, v40\n" \
"v_mul_f32 v25, v31, v45\n" \
"v_mfma_f32_16x16x32_bf16 a[172:175], a[116:119], a[92:95], a[172:175]\n" \
"buffer_load_dwordx2 a[224:225], v59, s[80:83], s52 offen offset:2048\n" \
"v_mfma_f32_16x16x32_bf16 a[176:179], a[120:123], a[88:91], a[176:179]\n" \
"v_mul_f32 v26, v37, v49\n" \
"v_fmac_f32 v25, v32, v44\n" \
"v_mfma_f32_16x16x32_bf16 a[180:183], a[124:127], a[88:91], a[180:183]\n" \
"v_fmac_f32 v26, v38, v48\n" \
"v_fmac_f32 v25, v33, v43\n" \
"v_mfma_f32_16x16x32_bf16 a[184:187], a[120:123], a[92:95], a[184:187]\n" \
"s_mov_b32 s52, 12288\n" \
"v_fmac_f32 v26, v39, v47\n" \
"v_fmac_f32 v25, v34, v42\n" \
"v_mfma_f32_16x16x32_bf16 a[188:191], a[124:127], a[92:95], a[188:191]\n" \
"buffer_load_dwordx2 a[210:211], v59, s[40:43], s52 offen offset:0\n" \
"v_mfma_f32_16x16x32_bf16 v[64:67], a[32:35], a[0:3], v[64:67]\n" \
"ds_read_b128 a[64:67], v6 offset:32768\n" \
"v_fmac_f32 v26, v40, v46\n" \
"v_accvgpr_read_b32 v28, a216\n" \
"v_mfma_f32_16x16x32_bf16 v[68:71], a[36:39], a[0:3], v[68:71]\n" \
"v_cvt_pk_bf16_f32 v26, v25, v26\n" \
"v_lshlrev_b32 v27, 16, v28\n" \
"v_mfma_f32_16x16x32_bf16 v[72:75], a[32:35], a[4:7], v[72:75]\n" \
"ds_read_b64_tr_b16 a[96:97], v9 offset:16384\n" \
"v_lshlrev_b32 v25, 16, v26\n" \
"v_and_b32 v28, 0xffff0000, v28\n" \
"v_mfma_f32_16x16x32_bf16 v[76:79], a[36:39], a[4:7], v[76:79]\n" \
"buffer_load_dwordx2 a[226:227], v59, s[80:83], s52 offen offset:0\n" \
"v_mfma_f32_16x16x32_bf16 v[80:83], a[40:43], a[0:3], v[80:83]\n" \
"ds_read_b64_tr_b16 a[98:99], v10 offset:16384\n" \
"v_and_b32 v26, 0xffff0000, v26\n" \
"v_mul_f32 v25, v25, v27\n" \
"v_mfma_f32_16x16x32_bf16 v[84:87], a[44:47], a[0:3], v[84:87]\n" \
"v_mul_f32 v26, v26, v28\n" \
"v_cvt_pk_bf16_f32 v53, v25, v29\n" \
"v_mfma_f32_16x16x32_bf16 v[88:91], a[40:43], a[4:7], v[88:91]\n" \
"ds_read_b128 a[68:71], v6 offset:34816\n" \
"v_cvt_pk_bf16_f32 v57, v26, v58\n" \
"v_accvgpr_read_b32 v41, a202\n" \
"v_mfma_f32_16x16x32_bf16 v[92:95], a[44:47], a[4:7], v[92:95]\n" \
"buffer_load_dwordx2 a[212:213], v59, s[40:43], s52 offen offset:2048\n" \
"v_mfma_f32_16x16x32_bf16 v[96:99], a[48:51], a[0:3], v[96:99]\n" \
"ds_read_b64_tr_b16 a[100:101], v11 offset:16384\n" \
"v_lshlrev_b32 v35, 16, v41\n" \
"v_and_b32 v41, 0xffff0000, v41\n" \
"v_mfma_f32_16x16x32_bf16 v[100:103], a[52:55], a[0:3], v[100:103]\n" \
"v_mul_f32 v25, v32, v45\n" \
"v_mul_f32 v26, v38, v49\n" \
"v_mfma_f32_16x16x32_bf16 v[104:107], a[48:51], a[4:7], v[104:107]\n" \
"ds_read_b64_tr_b16 a[102:103], v12 offset:16384\n" \
"v_fmac_f32 v25, v33, v44\n" \
"v_fmac_f32 v26, v39, v48\n" \
"v_mfma_f32_16x16x32_bf16 v[108:111], a[52:55], a[4:7], v[108:111]\n" \
"buffer_load_dwordx2 a[228:229], v59, s[80:83], s52 offen offset:2048\n" \
"v_mfma_f32_16x16x32_bf16 v[112:115], a[56:59], a[0:3], v[112:115]\n" \
"ds_read_b128 a[72:75], v6 offset:36864\n" \
"v_fmac_f32 v25, v34, v43\n" \
"v_fmac_f32 v26, v40, v47\n" \
"v_mfma_f32_16x16x32_bf16 v[116:119], a[60:63], a[0:3], v[116:119]\n" \
"v_fmac_f32 v25, v35, v42\n" \
"v_fmac_f32 v26, v41, v46\n" \
"v_mfma_f32_16x16x32_bf16 v[120:123], a[56:59], a[4:7], v[120:123]\n" \
"s_mov_b32 s46, 512\n" \
"s_add_u32 m0, s50, 0\n" \
"ds_read_b64_tr_b16 a[104:105], v9 offset:16896\n" \
"v_mfma_f32_16x16x32_bf16 v[124:127], a[60:63], a[4:7], v[124:127]\n" \
"buffer_load_dwordx4 v13, s[28:31], s46 offen lds\n" \
"v_mfma_f32_16x16x32_bf16 v[128:131], a[32:35], a[8:11], v[128:131]\n" \
"ds_read_b64_tr_b16 a[106:107], v10 offset:16896\n" \
"v_accvgpr_read_b32 v28, a218\n" \
"v_cvt_pk_bf16_f32 v26, v25, v26\n" \
"v_mfma_f32_16x16x32_bf16 v[132:135], a[36:39], a[8:11], v[132:135]\n" \
"v_lshlrev_b32 v27, 16, v28\n" \
"v_lshlrev_b32 v25, 16, v26\n" \
"v_mfma_f32_16x16x32_bf16 v[136:139], a[32:35], a[12:15], v[136:139]\n" \
"s_add_u32 m0, s50, 4096\n" \
"ds_read_b128 a[76:79], v6 offset:38912\n" \
"v_and_b32 v28, 0xffff0000, v28\n" \
"v_mfma_f32_16x16x32_bf16 v[140:143], a[36:39], a[12:15], v[140:143]\n" \
"buffer_load_dwordx4 v14, s[28:31], s46 offen lds\n" \
"v_mfma_f32_16x16x32_bf16 v[144:147], a[40:43], a[8:11], v[144:147]\n" \
"ds_read_b64_tr_b16 a[108:109], v11 offset:16896\n" \
"v_and_b32 v26, 0xffff0000, v26\n" \
"v_mul_f32 v29, v25, v27\n" \
"v_mfma_f32_16x16x32_bf16 v[148:151], a[44:47], a[8:11], v[148:151]\n" \
"v_mul_f32 v58, v26, v28\n" \
"v_accvgpr_read_b32 v36, a204\n" \
"v_mfma_f32_16x16x32_bf16 v[152:155], a[40:43], a[12:15], v[152:155]\n" \
"s_add_u32 m0, s50, 8192\n" \
"ds_read_b64_tr_b16 a[110:111], v12 offset:16896\n" \
"v_lshlrev_b32 v30, 16, v36\n" \
"v_mfma_f32_16x16x32_bf16 v[156:159], a[44:47], a[12:15], v[156:159]\n" \
"buffer_load_dwordx4 v15, s[28:31], s46 offen lds\n" \
"v_mfma_f32_16x16x32_bf16 v[160:163], a[48:51], a[8:11], v[160:163]\n" \
"ds_read_b128 a[80:83], v6 offset:40960\n" \
"v_and_b32 v36, 0xffff0000, v36\n" \
"v_mul_f32 v25, v33, v45\n" \
"v_mfma_f32_16x16x32_bf16 v[164:167], a[52:55], a[8:11], v[164:167]\n" \
"v_mul_f32 v26, v39, v49\n" \
"v_fmac_f32 v25, v34, v44\n" \
"v_mfma_f32_16x16x32_bf16 v[168:171], a[48:51], a[12:15], v[168:171]\n" \
"s_add_u32 m0, s50, 12288\n" \
"ds_read_b64_tr_b16 a[112:113], v9 offset:17408\n" \
"v_fmac_f32 v26, v40, v48\n" \
"v_mfma_f32_16x16x32_bf16 v[172:175], a[52:55], a[12:15], v[172:175]\n" \
"buffer_load_dwordx4 v16, s[28:31], s46 offen lds\n" \
"v_mfma_f32_16x16x32_bf16 v[176:179], a[56:59], a[8:11], v[176:179]\n" \
"ds_read_b64_tr_b16 a[114:115], v10 offset:17408\n" \
"v_fmac_f32 v25, v35, v43\n" \
"v_fmac_f32 v26, v41, v47\n" \
"v_mfma_f32_16x16x32_bf16 v[180:183], a[60:63], a[8:11], v[180:183]\n" \
"v_fmac_f32 v25, v30, v42\n" \
"v_fmac_f32 v26, v36, v46\n" \
"v_mfma_f32_16x16x32_bf16 v[184:187], a[56:59], a[12:15], v[184:187]\n" \
"s_mov_b32 s46, 512\n" \
"s_add_u32 m0, s50, 16384\n" \
"ds_read_b128 a[84:87], v6 offset:43008\n" \
"v_mfma_f32_16x16x32_bf16 v[188:191], a[60:63], a[12:15], v[188:191]\n" \
"buffer_load_dwordx4 v17, s[28:31], s46 offen lds\n" \
"v_mfma_f32_16x16x32_bf16 v[192:195], a[32:35], a[16:19], v[192:195]\n" \
"ds_read_b64_tr_b16 a[116:117], v11 offset:17408\n" \
"v_accvgpr_read_b32 v28, a220\n" \
"v_cvt_pk_bf16_f32 v26, v25, v26\n" \
"v_mfma_f32_16x16x32_bf16 v[196:199], a[36:39], a[16:19], v[196:199]\n" \
"v_lshlrev_b32 v27, 16, v28\n" \
"v_lshlrev_b32 v25, 16, v26\n" \
"v_mfma_f32_16x16x32_bf16 v[200:203], a[32:35], a[20:23], v[200:203]\n" \
"s_add_u32 m0, s50, 20480\n" \
"ds_read_b64_tr_b16 a[118:119], v12 offset:17408\n" \
"v_and_b32 v28, 0xffff0000, v28\n" \
"v_mfma_f32_16x16x32_bf16 v[204:207], a[36:39], a[20:23], v[204:207]\n" \
"buffer_load_dwordx4 v18, s[28:31], s46 offen lds\n" \
"v_mfma_f32_16x16x32_bf16 v[208:211], a[40:43], a[16:19], v[208:211]\n" \
"ds_read_b128 a[88:91], v6 offset:45056\n" \
"v_and_b32 v26, 0xffff0000, v26\n" \
"v_mul_f32 v25, v25, v27\n" \
"v_mfma_f32_16x16x32_bf16 v[212:215], a[44:47], a[16:19], v[212:215]\n" \
"v_mul_f32 v26, v26, v28\n" \
"v_cvt_pk_bf16_f32 v52, v25, v29\n" \
"v_mfma_f32_16x16x32_bf16 v[216:219], a[40:43], a[20:23], v[216:219]\n" \
"s_add_u32 m0, s50, 24576\n" \
"ds_read_b64_tr_b16 a[120:121], v9 offset:17920\n" \
"v_cvt_pk_bf16_f32 v56, v26, v58\n" \
"v_mfma_f32_16x16x32_bf16 v[220:223], a[44:47], a[20:23], v[220:223]\n" \
"buffer_load_dwordx4 v19, s[28:31], s46 offen lds\n" \
"v_mfma_f32_16x16x32_bf16 v[224:227], a[48:51], a[16:19], v[224:227]\n" \
"ds_read_b64_tr_b16 a[122:123], v10 offset:17920\n" \
"v_mfma_f32_16x16x32_bf16 v[228:231], a[52:55], a[16:19], v[228:231]\n" \
"v_mfma_f32_16x16x32_bf16 v[232:235], a[48:51], a[20:23], v[232:235]\n" \
"s_add_u32 m0, s50, 28672\n" \
"ds_read_b128 a[92:95], v6 offset:47104\n" \
"v_mfma_f32_16x16x32_bf16 v[236:239], a[52:55], a[20:23], v[236:239]\n" \
"buffer_load_dwordx4 v20, s[28:31], s46 offen lds\n" \
"v_mfma_f32_16x16x32_bf16 v[240:243], a[56:59], a[16:19], v[240:243]\n" \
"ds_read_b64_tr_b16 a[124:125], v11 offset:17920\n" \
"v_mfma_f32_16x16x32_bf16 v[244:247], a[60:63], a[16:19], v[244:247]\n" \
"v_mfma_f32_16x16x32_bf16 v[248:251], a[56:59], a[20:23], v[248:251]\n" \
"s_mov_b32 s47, 2097152\n" \
"s_add_u32 m0, s22, 0\n" \
"ds_read_b64_tr_b16 a[126:127], v12 offset:17920\n" \
"v_mfma_f32_16x16x32_bf16 v[252:255], a[60:63], a[20:23], v[252:255]\n" \
"buffer_load_dwordx4 v21, s[32:35], s47 offen offset:0 lds\n" \
"v_mfma_f32_16x16x32_bf16 a[128:131], a[32:35], a[24:27], a[128:131]\n" \
"v_mfma_f32_16x16x32_bf16 a[132:135], a[36:39], a[24:27], a[132:135]\n" \
"v_mfma_f32_16x16x32_bf16 a[136:139], a[32:35], a[28:31], a[136:139]\n" \
"s_add_u32 m0, s22, 896\n" \
"v_mfma_f32_16x16x32_bf16 a[140:143], a[36:39], a[28:31], a[140:143]\n" \
"buffer_load_dwordx4 v21, s[32:35], s47 offen offset:128 lds\n" \
"v_mfma_f32_16x16x32_bf16 a[144:147], a[40:43], a[24:27], a[144:147]\n" \
"v_mfma_f32_16x16x32_bf16 a[148:151], a[44:47], a[24:27], a[148:151]\n" \
"v_mfma_f32_16x16x32_bf16 a[152:155], a[40:43], a[28:31], a[152:155]\n" \
"s_waitcnt vmcnt(10)\n" \
"v_accvgpr_read_b32 v37, a206\n" \
"v_lshlrev_b32 v31, 16, v37\n" \
"v_mfma_f32_16x16x32_bf16 a[156:159], a[44:47], a[28:31], a[156:159]\n" \
"s_waitcnt vmcnt(20) lgkmcnt(0)\n" \
"v_and_b32 v37, 0xffff0000, v37\n" \
"v_mul_f32 v25, v34, v45\n" \
"v_mfma_f32_16x16x32_bf16 a[160:163], a[48:51], a[24:27], a[160:163]\n" \
"s_barrier\n" \
"v_mfma_f32_16x16x32_bf16 a[164:167], a[52:55], a[24:27], a[164:167]\n" \
"v_mul_f32 v26, v40, v49\n" \
"v_fmac_f32 v25, v35, v44\n" \
"v_mfma_f32_16x16x32_bf16 a[168:171], a[48:51], a[28:31], a[168:171]\n" \
"s_add_u32 m0, s22, 1792\n" \
"v_fmac_f32 v26, v41, v48\n" \
"v_fmac_f32 v25, v30, v43\n" \
"v_mfma_f32_16x16x32_bf16 a[172:175], a[52:55], a[28:31], a[172:175]\n" \
"buffer_load_dwordx4 v21, s[32:35], s47 offen offset:256 lds\n" \
"v_mfma_f32_16x16x32_bf16 a[176:179], a[56:59], a[24:27], a[176:179]\n" \
"s_add_u32 m0, s22, 2688\n" \
"v_fmac_f32 v26, v36, v47\n" \
"v_fmac_f32 v25, v31, v42\n" \
"v_mfma_f32_16x16x32_bf16 a[180:183], a[60:63], a[24:27], a[180:183]\n" \
"buffer_load_dwordx4 v21, s[32:35], s47 offen offset:384 lds\n" \
"v_mfma_f32_16x16x32_bf16 a[184:187], a[56:59], a[28:31], a[184:187]\n" \
"s_add_u32 s84, s84, 1048576\ns_addc_u32 s85, s85, 0\n" \
"v_fmac_f32 v26, v37, v46\n" \
"v_accvgpr_read_b32 v28, a222\n" \
"v_mfma_f32_16x16x32_bf16 a[188:191], a[60:63], a[28:31], a[188:191]\n" \
"s_mov_b64 exec, 0xffff\nglobal_load_dword a244, v1, s[84:85]\ns_mov_b64 exec, -1\n" \
"v_mfma_f32_16x16x32_bf16 v[64:67], a[96:99], a[64:67], v[64:67]\n" \
"ds_read_b128 a[0:3], v7 offset:0\n" \
"v_cvt_pk_bf16_f32 v26, v25, v26\n" \
"v_lshlrev_b32 v27, 16, v28\n" \
"v_mfma_f32_16x16x32_bf16 v[68:71], a[100:103], a[64:67], v[68:71]\n" \
"v_lshlrev_b32 v25, 16, v26\n" \
"v_and_b32 v28, 0xffff0000, v28\n" \
"v_mfma_f32_16x16x32_bf16 v[72:75], a[96:99], a[68:71], v[72:75]\n" \
"s_mov_b32 s47, 2359296\n" \
"s_add_u32 m0, s22, 16384\n" \
"ds_read_b64_tr_b16 a[32:33], v9 offset:32768\n" \
"v_mfma_f32_16x16x32_bf16 v[76:79], a[100:103], a[68:71], v[76:79]\n" \
"buffer_load_dwordx4 v21, s[32:35], s47 offen offset:0 lds\n" \
"v_mfma_f32_16x16x32_bf16 v[80:83], a[104:107], a[64:67], v[80:83]\n" \
"ds_read_b64_tr_b16 a[34:35], v10 offset:32768\n" \
"v_and_b32 v26, 0xffff0000, v26\n" \
"v_mul_f32 v29, v25, v27\n" \
"v_mfma_f32_16x16x32_bf16 v[84:87], a[108:111], a[64:67], v[84:87]\n" \
"v_mul_f32 v58, v26, v28\n" \
"v_accvgpr_read_b32 v38, a208\n" \
"v_mfma_f32_16x16x32_bf16 v[88:91], a[104:107], a[68:71], v[88:91]\n" \
"ds_read_b128 a[4:7], v7 offset:2048\n" \
"v_lshlrev_b32 v32, 16, v38\n" \
"v_and_b32 v38, 0xffff0000, v38\n" \
"v_mfma_f32_16x16x32_bf16 v[92:95], a[108:111], a[68:71], v[92:95]\n" \
"v_mul_f32 v25, v35, v45\n" \
"v_mul_f32 v26, v41, v49\n" \
"v_mfma_f32_16x16x32_bf16 v[96:99], a[112:115], a[64:67], v[96:99]\n" \
"s_mov_b32 s46, 640\n" \
"s_add_u32 m0, s50, 32768\n" \
"ds_read_b64_tr_b16 a[36:37], v11 offset:32768\n" \
"v_mfma_f32_16x16x32_bf16 v[100:103], a[116:119], a[64:67], v[100:103]\n" \
"buffer_load_dwordx4 v13, s[28:31], s46 offen lds\n" \
"v_mfma_f32_16x16x32_bf16 v[104:107], a[112:115], a[68:71], v[104:107]\n" \
"ds_read_b64_tr_b16 a[38:39], v12 offset:32768\n" \
"v_fmac_f32 v25, v30, v44\n" \
"v_fmac_f32 v26, v36, v48\n" \
"v_mfma_f32_16x16x32_bf16 v[108:111], a[116:119], a[68:71], v[108:111]\n" \
"v_fmac_f32 v25, v31, v43\n" \
"v_fmac_f32 v26, v37, v47\n" \
"v_mfma_f32_16x16x32_bf16 v[112:115], a[120:123], a[64:67], v[112:115]\n" \
"ds_read_b128 a[8:11], v7 offset:4096\n" \
"v_fmac_f32 v25, v32, v42\n" \
"v_fmac_f32 v26, v38, v46\n" \
"v_mfma_f32_16x16x32_bf16 v[116:119], a[124:127], a[64:67], v[116:119]\n" \
"v_accvgpr_read_b32 v28, a224\n" \
"v_cvt_pk_bf16_f32 v26, v25, v26\n" \
"v_mfma_f32_16x16x32_bf16 v[120:123], a[120:123], a[68:71], v[120:123]\n" \
"s_add_u32 m0, s22, 17280\n" \
"ds_read_b64_tr_b16 a[40:41], v9 offset:33280\n" \
"v_lshlrev_b32 v27, 16, v28\n" \
"v_mfma_f32_16x16x32_bf16 v[124:127], a[124:127], a[68:71], v[124:127]\n" \
"buffer_load_dwordx4 v21, s[32:35], s47 offen offset:128 lds\n" \
"v_mfma_f32_16x16x32_bf16 v[128:131], a[96:99], a[72:75], v[128:131]\n" \
"ds_read_b64_tr_b16 a[42:43], v10 offset:33280\n" \
"v_lshlrev_b32 v25, 16, v26\n" \
"v_and_b32 v28, 0xffff0000, v28\n" \
"v_mfma_f32_16x16x32_bf16 v[132:135], a[100:103], a[72:75], v[132:135]\n" \
"v_and_b32 v26, 0xffff0000, v26\n" \
"v_mul_f32 v25, v25, v27\n" \
"v_mfma_f32_16x16x32_bf16 v[136:139], a[96:99], a[76:79], v[136:139]\n" \
"ds_read_b128 a[12:15], v7 offset:6144\n" \
"v_mul_f32 v26, v26, v28\n" \
"v_cvt_pk_bf16_f32 v51, v25, v29\n" \
"v_mfma_f32_16x16x32_bf16 v[140:143], a[100:103], a[76:79], v[140:143]\n" \
"v_cvt_pk_bf16_f32 v55, v26, v58\n" \
"v_accvgpr_read_b32 v39, a210\n" \
"v_mfma_f32_16x16x32_bf16 v[144:147], a[104:107], a[72:75], v[144:147]\n" \
"s_add_u32 m0, s50, 36864\n" \
"ds_read_b64_tr_b16 a[44:45], v11 offset:33280\n" \
"v_lshlrev_b32 v33, 16, v39\n" \
"v_mfma_f32_16x16x32_bf16 v[148:151], a[108:111], a[72:75], v[148:151]\n" \
"buffer_load_dwordx4 v14, s[28:31], s46 offen lds\n" \
"v_mfma_f32_16x16x32_bf16 v[152:155], a[104:107], a[76:79], v[152:155]\n" \
"ds_read_b64_tr_b16 a[46:47], v12 offset:33280\n" \
"v_and_b32 v39, 0xffff0000, v39\n" \
"v_mul_f32 v25, v30, v45\n" \
"v_mfma_f32_16x16x32_bf16 v[156:159], a[108:111], a[76:79], v[156:159]\n" \
"v_mul_f32 v26, v36, v49\n" \
"v_fmac_f32 v25, v31, v44\n" \
"v_mfma_f32_16x16x32_bf16 v[160:163], a[112:115], a[72:75], v[160:163]\n" \
"ds_read_b128 a[16:19], v7 offset:8192\n" \
"v_fmac_f32 v26, v37, v48\n" \
"v_fmac_f32 v25, v32, v43\n" \
"v_mfma_f32_16x16x32_bf16 v[164:167], a[116:119], a[72:75], v[164:167]\n" \
"v_fmac_f32 v26, v38, v47\n" \
"v_fmac_f32 v25, v33, v42\n" \
"v_mfma_f32_16x16x32_bf16 v[168:171], a[112:115], a[76:79], v[168:171]\n" \
"s_add_u32 m0, s22, 18176\n" \
"ds_read_b64_tr_b16 a[48:49], v9 offset:33792\n" \
"v_fmac_f32 v26, v39, v46\n" \
"v_mfma_f32_16x16x32_bf16 v[172:175], a[116:119], a[76:79], v[172:175]\n" \
"buffer_load_dwordx4 v21, s[32:35], s47 offen offset:256 lds\n" \
"v_mfma_f32_16x16x32_bf16 v[176:179], a[120:123], a[72:75], v[176:179]\n" \
"ds_read_b64_tr_b16 a[50:51], v10 offset:33792\n" \
"v_accvgpr_read_b32 v28, a226\n" \
"v_cvt_pk_bf16_f32 v26, v25, v26\n" \
"v_mfma_f32_16x16x32_bf16 v[180:183], a[124:127], a[72:75], v[180:183]\n" \
"v_lshlrev_b32 v27, 16, v28\n" \
"v_lshlrev_b32 v25, 16, v26\n" \
"v_mfma_f32_16x16x32_bf16 v[184:187], a[120:123], a[76:79], v[184:187]\n" \
"ds_read_b128 a[20:23], v7 offset:10240\n" \
"v_and_b32 v28, 0xffff0000, v28\n" \
"v_and_b32 v26, 0xffff0000, v26\n" \
"v_mfma_f32_16x16x32_bf16 v[188:191], a[124:127], a[76:79], v[188:191]\n" \
"v_mul_f32 v29, v25, v27\n" \
"v_mul_f32 v58, v26, v28\n" \
"v_mfma_f32_16x16x32_bf16 v[192:195], a[96:99], a[80:83], v[192:195]\n" \
"s_add_u32 m0, s50, 40960\n" \
"ds_read_b64_tr_b16 a[52:53], v11 offset:33792\n" \
"v_accvgpr_read_b32 v40, a212\n" \
"v_mfma_f32_16x16x32_bf16 v[196:199], a[100:103], a[80:83], v[196:199]\n" \
"buffer_load_dwordx4 v15, s[28:31], s46 offen lds\n" \
"v_mfma_f32_16x16x32_bf16 v[200:203], a[96:99], a[84:87], v[200:203]\n" \
"ds_read_b64_tr_b16 a[54:55], v12 offset:33792\n" \
"v_lshlrev_b32 v34, 16, v40\n" \
"v_and_b32 v40, 0xffff0000, v40\n" \
"v_mfma_f32_16x16x32_bf16 v[204:207], a[100:103], a[84:87], v[204:207]\n" \
"v_mul_f32 v25, v31, v45\n" \
"v_mul_f32 v26, v37, v49\n" \
"v_mfma_f32_16x16x32_bf16 v[208:211], a[104:107], a[80:83], v[208:211]\n" \
"ds_read_b128 a[24:27], v7 offset:12288\n" \
"v_fmac_f32 v25, v32, v44\n" \
"v_fmac_f32 v26, v38, v48\n" \
"v_mfma_f32_16x16x32_bf16 v[212:215], a[108:111], a[80:83], v[212:215]\n" \
"v_fmac_f32 v25, v33, v43\n" \
"v_fmac_f32 v26, v39, v47\n" \
"v_mfma_f32_16x16x32_bf16 v[216:219], a[104:107], a[84:87], v[216:219]\n" \
"s_add_u32 m0, s22, 19072\n" \
"ds_read_b64_tr_b16 a[56:57], v9 offset:34304\n" \
"v_fmac_f32 v25, v34, v42\n" \
"v_mfma_f32_16x16x32_bf16 v[220:223], a[108:111], a[84:87], v[220:223]\n" \
"buffer_load_dwordx4 v21, s[32:35], s47 offen offset:384 lds\n" \
"v_mfma_f32_16x16x32_bf16 v[224:227], a[112:115], a[80:83], v[224:227]\n" \
"ds_read_b64_tr_b16 a[58:59], v10 offset:34304\n" \
"v_fmac_f32 v26, v40, v46\n" \
"v_accvgpr_read_b32 v28, a228\n" \
"v_mfma_f32_16x16x32_bf16 v[228:231], a[116:119], a[80:83], v[228:231]\n" \
"v_cvt_pk_bf16_f32 v26, v25, v26\n" \
"v_lshlrev_b32 v27, 16, v28\n" \
"v_mfma_f32_16x16x32_bf16 v[232:235], a[112:115], a[84:87], v[232:235]\n" \
"ds_read_b128 a[28:31], v7 offset:14336\n" \
"v_lshlrev_b32 v25, 16, v26\n" \
"v_and_b32 v28, 0xffff0000, v28\n" \
"v_mfma_f32_16x16x32_bf16 v[236:239], a[116:119], a[84:87], v[236:239]\n" \
"v_and_b32 v26, 0xffff0000, v26\n" \
"v_mul_f32 v25, v25, v27\n" \
"v_mfma_f32_16x16x32_bf16 v[240:243], a[120:123], a[80:83], v[240:243]\n" \
"s_add_u32 m0, s50, 45056\n" \
"ds_read_b64_tr_b16 a[60:61], v11 offset:34304\n" \
"v_mul_f32 v26, v26, v28\n" \
"v_mfma_f32_16x16x32_bf16 v[244:247], a[124:127], a[80:83], v[244:247]\n" \
"buffer_load_dwordx4 v16, s[28:31], s46 offen lds\n" \
"v_mfma_f32_16x16x32_bf16 v[248:251], a[120:123], a[84:87], v[248:251]\n" \
"ds_read_b64_tr_b16 a[62:63], v12 offset:34304\n" \
"v_cvt_pk_bf16_f32 v50, v25, v29\n" \
"v_cvt_pk_bf16_f32 v54, v26, v58\n" \
"v_mfma_f32_16x16x32_bf16 v[252:255], a[124:127], a[84:87], v[252:255]\n" \
"v_accvgpr_read_b32 v43, a234\n" \
"v_lshlrev_b32 v42, 16, v43\n" \
"v_mfma_f32_16x16x32_bf16 a[128:131], a[96:99], a[88:91], a[128:131]\n" \
"v_and_b32 v43, 0xffff0000, v43\n" \
"v_accvgpr_read_b32 v45, a235\n" \
"v_mfma_f32_16x16x32_bf16 a[132:135], a[100:103], a[88:91], a[132:135]\n" \
"buffer_store_dword v23, v61, s[36:39], 0 offen sc1\n" \
"s_mov_b32 s94, 79691776\n" \
"v_mfma_f32_16x16x32_bf16 a[136:139], a[96:99], a[92:95], a[136:139]\n" \
"v_lshlrev_b32 v44, 16, v45\n" \
"v_and_b32 v45, 0xffff0000, v45\n" \
"v_mfma_f32_16x16x32_bf16 a[140:143], a[100:103], a[92:95], a[140:143]\n" \
"v_accvgpr_read_b32 v47, a236\n" \
"v_lshlrev_b32 v46, 16, v47\n" \
"v_mfma_f32_16x16x32_bf16 a[144:147], a[104:107], a[88:91], a[144:147]\n" \
"v_and_b32 v47, 0xffff0000, v47\n" \
"v_accvgpr_read_b32 v49, a237\n" \
"v_mfma_f32_16x16x32_bf16 a[148:151], a[108:111], a[88:91], a[148:151]\n" \
"buffer_store_dwordx4 v[50:53], v60, s[36:39], s94 offen offset:0 sc1\n" \
"s_mov_b32 s95, 79695872\n" \
"v_mfma_f32_16x16x32_bf16 a[152:155], a[104:107], a[92:95], a[152:155]\n" \
"v_lshlrev_b32 v48, 16, v49\n" \
"v_and_b32 v49, 0xffff0000, v49\n" \
"v_mfma_f32_16x16x32_bf16 a[156:159], a[108:111], a[92:95], a[156:159]\n" \
"s_waitcnt vmcnt(29) lgkmcnt(0)\n" \
"v_accvgpr_read_b32 v36, a193\n" \
"v_lshlrev_b32 v30, 16, v36\n" \
"v_mfma_f32_16x16x32_bf16 a[160:163], a[112:115], a[88:91], a[160:163]\n" \
"s_barrier\n" \
"v_mfma_f32_16x16x32_bf16 a[164:167], a[116:119], a[88:91], a[164:167]\n" \
"v_and_b32 v36, 0xffff0000, v36\n" \
"v_accvgpr_read_b32 v37, a195\n" \
"v_mfma_f32_16x16x32_bf16 a[168:171], a[112:115], a[92:95], a[168:171]\n" \
"v_lshlrev_b32 v31, 16, v37\n" \
"v_and_b32 v37, 0xffff0000, v37\n" \
"v_mfma_f32_16x16x32_bf16 a[172:175], a[116:119], a[92:95], a[172:175]\n" \
"buffer_store_dwordx4 v[54:57], v60, s[36:39], s95 offen offset:0 sc1\n" \
"v_mfma_f32_16x16x32_bf16 a[176:179], a[120:123], a[88:91], a[176:179]\n" \
"v_accvgpr_read_b32 v38, a197\n" \
"v_lshlrev_b32 v32, 16, v38\n" \
"v_mfma_f32_16x16x32_bf16 a[180:183], a[124:127], a[88:91], a[180:183]\n" \
"v_and_b32 v38, 0xffff0000, v38\n" \
"v_accvgpr_read_b32 v39, a199\n" \
"v_mfma_f32_16x16x32_bf16 a[184:187], a[120:123], a[92:95], a[184:187]\n" \
"v_lshlrev_b32 v33, 16, v39\n" \
"v_and_b32 v39, 0xffff0000, v39\n" \
"v_mfma_f32_16x16x32_bf16 a[188:191], a[124:127], a[92:95], a[188:191]\n" \
"v_mul_f32 v25, v30, v45\n" \
"v_mul_f32 v26, v36, v49\n" \
"v_mfma_f32_16x16x32_bf16 v[64:67], a[32:35], a[0:3], v[64:67]\n" \
"ds_read_b128 a[64:67], v8 offset:0\n" \
"v_fmac_f32 v25, v31, v44\n" \
"v_fmac_f32 v26, v37, v48\n" \
"v_mfma_f32_16x16x32_bf16 v[68:71], a[36:39], a[0:3], v[68:71]\n" \
"v_fmac_f32 v25, v32, v43\n" \
"v_fmac_f32 v26, v38, v47\n" \
"v_mfma_f32_16x16x32_bf16 v[72:75], a[32:35], a[4:7], v[72:75]\n" \
"s_mov_b32 s47, 2621440\n" \
"s_add_u32 m0, s22, 32768\n" \
"ds_read_b64_tr_b16 a[96:97], v9 offset:49152\n" \
"v_mfma_f32_16x16x32_bf16 v[76:79], a[36:39], a[4:7], v[76:79]\n" \
"buffer_load_dwordx4 v21, s[32:35], s47 offen offset:0 lds\n" \
"v_mfma_f32_16x16x32_bf16 v[80:83], a[40:43], a[0:3], v[80:83]\n" \
"ds_read_b64_tr_b16 a[98:99], v10 offset:49152\n" \
"v_fmac_f32 v25, v33, v42\n" \
"v_fmac_f32 v26, v39, v46\n" \
"v_mfma_f32_16x16x32_bf16 v[84:87], a[44:47], a[0:3], v[84:87]\n" \
"v_accvgpr_read_b32 v28, a215\n" \
"v_cvt_pk_bf16_f32 v26, v25, v26\n" \
"v_mfma_f32_16x16x32_bf16 v[88:91], a[40:43], a[4:7], v[88:91]\n" \
"ds_read_b128 a[68:71], v8 offset:2048\n" \
"v_lshlrev_b32 v27, 16, v28\n" \
"v_lshlrev_b32 v25, 16, v26\n" \
"v_mfma_f32_16x16x32_bf16 v[92:95], a[44:47], a[4:7], v[92:95]\n" \
"v_and_b32 v28, 0xffff0000, v28\n" \
"v_and_b32 v26, 0xffff0000, v26\n" \
"v_mfma_f32_16x16x32_bf16 v[96:99], a[48:51], a[0:3], v[96:99]\n" \
"s_mov_b32 s46, 640\n" \
"s_add_u32 m0, s50, 49152\n" \
"ds_read_b64_tr_b16 a[100:101], v11 offset:49152\n" \
"v_mfma_f32_16x16x32_bf16 v[100:103], a[52:55], a[0:3], v[100:103]\n" \
"buffer_load_dwordx4 v17, s[28:31], s46 offen lds\n" \
"v_mfma_f32_16x16x32_bf16 v[104:107], a[48:51], a[4:7], v[104:107]\n" \
"ds_read_b64_tr_b16 a[102:103], v12 offset:49152\n" \
"v_mul_f32 v29, v25, v27\n" \
"v_mul_f32 v58, v26, v28\n" \
"v_mfma_f32_16x16x32_bf16 v[108:111], a[52:55], a[4:7], v[108:111]\n" \
"v_accvgpr_read_b32 v40, a201\n" \
"v_lshlrev_b32 v34, 16, v40\n" \
"v_mfma_f32_16x16x32_bf16 v[112:115], a[56:59], a[0:3], v[112:115]\n" \
"ds_read_b128 a[72:75], v8 offset:4096\n" \
"v_and_b32 v40, 0xffff0000, v40\n" \
"v_mul_f32 v25, v31, v45\n" \
"v_mfma_f32_16x16x32_bf16 v[116:119], a[60:63], a[0:3], v[116:119]\n" \
"v_mul_f32 v26, v37, v49\n" \
"v_fmac_f32 v25, v32, v44\n" \
"v_mfma_f32_16x16x32_bf16 v[120:123], a[56:59], a[4:7], v[120:123]\n" \
"s_add_u32 m0, s22, 33664\n" \
"ds_read_b64_tr_b16 a[104:105], v9 offset:49664\n" \
"v_fmac_f32 v26, v38, v48\n" \
"v_mfma_f32_16x16x32_bf16 v[124:127], a[60:63], a[4:7], v[124:127]\n" \
"buffer_load_dwordx4 v21, s[32:35], s47 offen offset:128 lds\n" \
"v_mfma_f32_16x16x32_bf16 v[128:131], a[32:35], a[8:11], v[128:131]\n" \
"ds_read_b64_tr_b16 a[106:107], v10 offset:49664\n" \
"v_fmac_f32 v25, v33, v43\n" \
"v_fmac_f32 v26, v39, v47\n" \
"v_mfma_f32_16x16x32_bf16 v[132:135], a[36:39], a[8:11], v[132:135]\n" \
"v_fmac_f32 v25, v34, v42\n" \
"v_fmac_f32 v26, v40, v46\n" \
"v_mfma_f32_16x16x32_bf16 v[136:139], a[32:35], a[12:15], v[136:139]\n" \
"ds_read_b128 a[76:79], v8 offset:6144\n" \
"v_accvgpr_read_b32 v28, a217\n" \
"v_cvt_pk_bf16_f32 v26, v25, v26\n" \
"v_mfma_f32_16x16x32_bf16 v[140:143], a[36:39], a[12:15], v[140:143]\n" \
"v_lshlrev_b32 v27, 16, v28\n" \
"v_lshlrev_b32 v25, 16, v26\n" \
"v_mfma_f32_16x16x32_bf16 v[144:147], a[40:43], a[8:11], v[144:147]\n" \
"s_add_u32 m0, s50, 53248\n" \
"ds_read_b64_tr_b16 a[108:109], v11 offset:49664\n" \
"v_and_b32 v28, 0xffff0000, v28\n" \
"v_mfma_f32_16x16x32_bf16 v[148:151], a[44:47], a[8:11], v[148:151]\n" \
"buffer_load_dwordx4 v18, s[28:31], s46 offen lds\n" \
"v_mfma_f32_16x16x32_bf16 v[152:155], a[40:43], a[12:15], v[152:155]\n" \
"ds_read_b64_tr_b16 a[110:111], v12 offset:49664\n" \
"v_and_b32 v26, 0xffff0000, v26\n" \
"v_mul_f32 v25, v25, v27\n" \
"v_mfma_f32_16x16x32_bf16 v[156:159], a[44:47], a[12:15], v[156:159]\n" \
"v_mul_f32 v26, v26, v28\n" \
"v_cvt_pk_bf16_f32 v53, v25, v29\n" \
"v_mfma_f32_16x16x32_bf16 v[160:163], a[48:51], a[8:11], v[160:163]\n" \
"ds_read_b128 a[80:83], v8 offset:8192\n" \
"v_cvt_pk_bf16_f32 v57, v26, v58\n" \
"v_accvgpr_read_b32 v41, a203\n" \
"v_mfma_f32_16x16x32_bf16 v[164:167], a[52:55], a[8:11], v[164:167]\n" \
"v_lshlrev_b32 v35, 16, v41\n" \
"v_and_b32 v41, 0xffff0000, v41\n" \
"v_mfma_f32_16x16x32_bf16 v[168:171], a[48:51], a[12:15], v[168:171]\n" \
"s_add_u32 m0, s22, 34560\n" \
"ds_read_b64_tr_b16 a[112:113], v9 offset:50176\n" \
"v_mul_f32 v25, v32, v45\n" \
"v_mfma_f32_16x16x32_bf16 v[172:175], a[52:55], a[12:15], v[172:175]\n" \
"buffer_load_dwordx4 v21, s[32:35], s47 offen offset:256 lds\n" \
"v_mfma_f32_16x16x32_bf16 v[176:179], a[56:59], a[8:11], v[176:179]\n" \
"ds_read_b64_tr_b16 a[114:115], v10 offset:50176\n" \
"v_mul_f32 v26, v38, v49\n" \
"v_fmac_f32 v25, v33, v44\n" \
"v_mfma_f32_16x16x32_bf16 v[180:183], a[60:63], a[8:11], v[180:183]\n" \
"v_fmac_f32 v26, v39, v48\n" \
"v_fmac_f32 v25, v34, v43\n" \
"v_mfma_f32_16x16x32_bf16 v[184:187], a[56:59], a[12:15], v[184:187]\n" \
"ds_read_b128 a[84:87], v8 offset:10240\n" \
"v_fmac_f32 v26, v40, v47\n" \
"v_fmac_f32 v25, v35, v42\n" \
"v_mfma_f32_16x16x32_bf16 v[188:191], a[60:63], a[12:15], v[188:191]\n" \
"v_fmac_f32 v26, v41, v46\n" \
"v_accvgpr_read_b32 v28, a219\n" \
"v_mfma_f32_16x16x32_bf16 v[192:195], a[32:35], a[16:19], v[192:195]\n" \
"s_add_u32 m0, s50, 57344\n" \
"ds_read_b64_tr_b16 a[116:117], v11 offset:50176\n" \
"v_cvt_pk_bf16_f32 v26, v25, v26\n" \
"v_mfma_f32_16x16x32_bf16 v[196:199], a[36:39], a[16:19], v[196:199]\n" \
"buffer_load_dwordx4 v19, s[28:31], s46 offen lds\n" \
"v_mfma_f32_16x16x32_bf16 v[200:203], a[32:35], a[20:23], v[200:203]\n" \
"ds_read_b64_tr_b16 a[118:119], v12 offset:50176\n" \
"v_lshlrev_b32 v27, 16, v28\n" \
"v_lshlrev_b32 v25, 16, v26\n" \
"v_mfma_f32_16x16x32_bf16 v[204:207], a[36:39], a[20:23], v[204:207]\n" \
"v_and_b32 v28, 0xffff0000, v28\n" \
"v_and_b32 v26, 0xffff0000, v26\n" \
"v_mfma_f32_16x16x32_bf16 v[208:211], a[40:43], a[16:19], v[208:211]\n" \
"ds_read_b128 a[88:91], v8 offset:12288\n" \
"v_mul_f32 v29, v25, v27\n" \
"v_mul_f32 v58, v26, v28\n" \
"v_mfma_f32_16x16x32_bf16 v[212:215], a[44:47], a[16:19], v[212:215]\n" \
"LH1_%=:\ns_cbranch_execz LH9_%=\n" \
"v_accvgpr_read_b32 v36, a205\n" \
"v_lshlrev_b32 v30, 16, v36\n" \
"v_mfma_f32_16x16x32_bf16 v[216:219], a[40:43], a[20:23], v[216:219]\n" \
"s_add_u32 m0, s22, 35456\n" \
"ds_read_b64_tr_b16 a[120:121], v9 offset:50688\n" \
"v_and_b32 v36, 0xffff0000, v36\n" \
"v_mfma_f32_16x16x32_bf16 v[220:223], a[44:47], a[20:23], v[220:223]\n" \
"buffer_load_dwordx4 v21, s[32:35], s47 offen offset:384 lds\n" \
"v_mfma_f32_16x16x32_bf16 v[224:227], a[48:51], a[16:19], v[224:227]\n" \
"ds_read_b64_tr_b16 a[122:123], v10 offset:50688\n" \
"v_mul_f32 v25, v33, v45\n" \
"v_mul_f32 v26, v39, v49\n" \
"v_mfma_f32_16x16x32_bf16 v[228:231], a[52:55], a[16:19], v[228:231]\n" \
"v_fmac_f32 v25, v34, v44\n" \
"v_fmac_f32 v26, v40, v48\n" \
"v_mfma_f32_16x16x32_bf16 v[232:235], a[48:51], a[20:23], v[232:235]\n" \
"ds_read_b128 a[92:95], v8 offset:14336\n" \
"v_fmac_f32 v25, v35, v43\n" \
"v_fmac_f32 v26, v41, v47\n" \
"v_mfma_f32_16x16x32_bf16 v[236:239], a[52:55], a[20:23], v[236:239]\n" \
"v_fmac_f32 v25, v30, v42\n" \
"v_fmac_f32 v26, v36, v46\n" \
"v_mfma_f32_16x16x32_bf16 v[240:243], a[56:59], a[16:19], v[240:243]\n" \
"s_add_u32 m0, s50, 61440\n" \
"ds_read_b64_tr_b16 a[124:125], v11 offset:50688\n" \
"v_accvgpr_read_b32 v28, a221\n" \
"v_mfma_f32_16x16x32_bf16 v[244:247], a[60:63], a[16:19], v[244:247]\n" \
"buffer_load_dwordx4 v20, s[28:31], s46 offen lds\n" \
"v_mfma_f32_16x16x32_bf16 v[248:251], a[56:59], a[20:23], v[248:251]\n" \
"ds_read_b64_tr_b16 a[126:127], v12 offset:50688\n" \
"v_cvt_pk_bf16_f32 v26, v25, v26\n" \
"v_lshlrev_b32 v27, 16, v28\n" \
"v_mfma_f32_16x16x32_bf16 v[252:255], a[60:63], a[20:23], v[252:255]\n" \
"v_lshlrev_b32 v25, 16, v26\n" \
"v_and_b32 v28, 0xffff0000, v28\n" \
"v_mfma_f32_16x16x32_bf16 a[128:131], a[32:35], a[24:27], a[128:131]\n" \
"s_add_u32 s84, s84, 1048576\ns_addc_u32 s85, s85, 0\n" \
"v_and_b32 v26, 0xffff0000, v26\n" \
"v_mul_f32 v25, v25, v27\n" \
"v_mfma_f32_16x16x32_bf16 a[132:135], a[36:39], a[24:27], a[132:135]\n" \
"s_mov_b64 exec, 0xffff\nglobal_load_dword a244, v1, s[84:85]\ns_mov_b64 exec, -1\n" \
"v_mfma_f32_16x16x32_bf16 a[136:139], a[32:35], a[28:31], a[136:139]\n" \
"v_mul_f32 v26, v26, v28\n" \
"v_cvt_pk_bf16_f32 v52, v25, v29\n" \
"v_mfma_f32_16x16x32_bf16 a[140:143], a[36:39], a[28:31], a[140:143]\n" \
"v_cvt_pk_bf16_f32 v56, v26, v58\n" \
"v_accvgpr_read_b32 v37, a207\n" \
"v_mfma_f32_16x16x32_bf16 a[144:147], a[40:43], a[24:27], a[144:147]\n" \
"v_lshlrev_b32 v31, 16, v37\n" \
"v_and_b32 v37, 0xffff0000, v37\n" \
"v_mfma_f32_16x16x32_bf16 a[148:151], a[44:47], a[24:27], a[148:151]\n" \
"v_mul_f32 v25, v34, v45\n" \
"v_mul_f32 v26, v40, v49\n" \
"v_mfma_f32_16x16x32_bf16 a[152:155], a[40:43], a[28:31], a[152:155]\n" \
"v_fmac_f32 v25, v35, v44\n" \
"v_fmac_f32 v26, v41, v48\n" \
"v_mfma_f32_16x16x32_bf16 a[156:159], a[44:47], a[28:31], a[156:159]\n" \
"s_waitcnt vmcnt(21) lgkmcnt(0)\n" \
"v_fmac_f32 v25, v30, v43\n" \
"v_fmac_f32 v26, v36, v47\n" \
"v_mfma_f32_16x16x32_bf16 a[160:163], a[48:51], a[24:27], a[160:163]\n" \
"s_barrier\n" \
"v_mfma_f32_16x16x32_bf16 a[164:167], a[52:55], a[24:27], a[164:167]\n" \
"v_fmac_f32 v25, v31, v42\n" \
"v_fmac_f32 v26, v37, v46\n" \
"v_mfma_f32_16x16x32_bf16 a[168:171], a[48:51], a[28:31], a[168:171]\n" \
"v_accvgpr_read_b32 v28, a223\n" \
"v_cvt_pk_bf16_f32 v26, v25, v26\n" \
"v_mfma_f32_16x16x32_bf16 a[172:175], a[52:55], a[28:31], a[172:175]\n" \
"v_lshlrev_b32 v27, 16, v28\n" \
"v_lshlrev_b32 v25, 16, v26\n" \
"v_mfma_f32_16x16x32_bf16 a[176:179], a[56:59], a[24:27], a[176:179]\n" \
"v_and_b32 v28, 0xffff0000, v28\n" \
"v_and_b32 v26, 0xffff0000, v26\n" \
"v_mfma_f32_16x16x32_bf16 a[180:183], a[60:63], a[24:27], a[180:183]\n" \
"v_mul_f32 v29, v25, v27\n" \
"v_mul_f32 v58, v26, v28\n" \
"v_mfma_f32_16x16x32_bf16 a[184:187], a[56:59], a[28:31], a[184:187]\n" \
"v_accvgpr_read_b32 v38, a209\n" \
"v_lshlrev_b32 v32, 16, v38\n" \
"v_mfma_f32_16x16x32_bf16 a[188:191], a[60:63], a[28:31], a[188:191]\n" \
"v_and_b32 v38, 0xffff0000, v38\n" \
"v_mul_f32 v25, v35, v45\n" \
"v_mfma_f32_16x16x32_bf16 v[64:67], a[96:99], a[64:67], v[64:67]\n" \
"ds_read_b128 a[0:3], v5 offset:0\n" \
"v_mul_f32 v26, v41, v49\n" \
"v_fmac_f32 v25, v30, v44\n" \
"v_mfma_f32_16x16x32_bf16 v[68:71], a[100:103], a[64:67], v[68:71]\n" \
"v_fmac_f32 v26, v36, v48\n" \
"v_fmac_f32 v25, v31, v43\n" \
"v_mfma_f32_16x16x32_bf16 v[72:75], a[96:99], a[68:71], v[72:75]\n" \
"s_mov_b32 s47, 2883584\n" \
"s_add_u32 m0, s22, 49152\n" \
"ds_read_b64_tr_b16 a[32:33], v9 offset:0\n" \
"v_mfma_f32_16x16x32_bf16 v[76:79], a[100:103], a[68:71], v[76:79]\n" \
"buffer_load_dwordx4 v21, s[32:35], s47 offen offset:0 lds\n" \
"v_mfma_f32_16x16x32_bf16 v[80:83], a[104:107], a[64:67], v[80:83]\n" \
"ds_read_b64_tr_b16 a[34:35], v10 offset:0\n" \
"v_fmac_f32 v26, v37, v47\n" \
"v_fmac_f32 v25, v32, v42\n" \
"v_mfma_f32_16x16x32_bf16 v[84:87], a[108:111], a[64:67], v[84:87]\n" \
"v_fmac_f32 v26, v38, v46\n" \
"v_accvgpr_read_b32 v28, a225\n" \
"v_mfma_f32_16x16x32_bf16 v[88:91], a[104:107], a[68:71], v[88:91]\n" \
"ds_read_b128 a[4:7], v5 offset:2048\n" \
"v_cvt_pk_bf16_f32 v26, v25, v26\n" \
"v_lshlrev_b32 v27, 16, v28\n" \
"v_mfma_f32_16x16x32_bf16 v[92:95], a[108:111], a[68:71], v[92:95]\n" \
"v_lshlrev_b32 v25, 16, v26\n" \
"v_and_b32 v28, 0xffff0000, v28\n" \
"v_mfma_f32_16x16x32_bf16 v[96:99], a[112:115], a[64:67], v[96:99]\n" \
"s_mov_b32 s46, 768\n" \
"s_add_u32 m0, s50, 65536\n" \
"ds_read_b64_tr_b16 a[36:37], v11 offset:0\n" \
"v_mfma_f32_16x16x32_bf16 v[100:103], a[116:119], a[64:67], v[100:103]\n" \
"buffer_load_dwordx4 v13, s[28:31], s46 offen lds\n" \
"v_mfma_f32_16x16x32_bf16 v[104:107], a[112:115], a[68:71], v[104:107]\n" \
"ds_read_b64_tr_b16 a[38:39], v12 offset:0\n" \
"v_and_b32 v26, 0xffff0000, v26\n" \
"v_mul_f32 v25, v25, v27\n" \
"v_mfma_f32_16x16x32_bf16 v[108:111], a[116:119], a[68:71], v[108:111]\n" \
"v_mul_f32 v26, v26, v28\n" \
"v_cvt_pk_bf16_f32 v51, v25, v29\n" \
"v_mfma_f32_16x16x32_bf16 v[112:115], a[120:123], a[64:67], v[112:115]\n" \
"ds_read_b128 a[8:11], v5 offset:4096\n" \
"v_cvt_pk_bf16_f32 v55, v26, v58\n" \
"v_accvgpr_read_b32 v39, a211\n" \
"v_mfma_f32_16x16x32_bf16 v[116:119], a[124:127], a[64:67], v[116:119]\n" \
"v_lshlrev_b32 v33, 16, v39\n" \
"v_and_b32 v39, 0xffff0000, v39\n" \
"v_mfma_f32_16x16x32_bf16 v[120:123], a[120:123], a[68:71], v[120:123]\n" \
"s_add_u32 m0, s22, 50048\n" \
"ds_read_b64_tr_b16 a[40:41], v9 offset:512\n" \
"v_mul_f32 v25, v30, v45\n" \
"v_mfma_f32_16x16x32_bf16 v[124:127], a[124:127], a[68:71], v[124:127]\n" \
"buffer_load_dwordx4 v21, s[32:35], s47 offen offset:128 lds\n" \
"v_mfma_f32_16x16x32_bf16 v[128:131], a[96:99], a[72:75], v[128:131]\n" \
"ds_read_b64_tr_b16 a[42:43], v10 offset:512\n" \
"v_mul_f32 v26, v36, v49\n" \
"v_fmac_f32 v25, v31, v44\n" \
"v_mfma_f32_16x16x32_bf16 v[132:135], a[100:103], a[72:75], v[132:135]\n" \
"v_fmac_f32 v26, v37, v48\n" \
"v_fmac_f32 v25, v32, v43\n" \
"v_mfma_f32_16x16x32_bf16 v[136:139], a[96:99], a[76:79], v[136:139]\n" \
"ds_read_b128 a[12:15], v5 offset:6144\n" \
"v_fmac_f32 v26, v38, v47\n" \
"v_fmac_f32 v25, v33, v42\n" \
"v_mfma_f32_16x16x32_bf16 v[140:143], a[100:103], a[76:79], v[140:143]\n" \
"v_fmac_f32 v26, v39, v46\n" \
"v_accvgpr_read_b32 v28, a227\n" \
"v_mfma_f32_16x16x32_bf16 v[144:147], a[104:107], a[72:75], v[144:147]\n" \
"s_add_u32 m0, s50, 69632\n" \
"ds_read_b64_tr_b16 a[44:45], v11 offset:512\n" \
"v_cvt_pk_bf16_f32 v26, v25, v26\n" \
"v_mfma_f32_16x16x32_bf16 v[148:151], a[108:111], a[72:75], v[148:151]\n" \
"buffer_load_dwordx4 v14, s[28:31], s46 offen lds\n" \
"v_mfma_f32_16x16x32_bf16 v[152:155], a[104:107], a[76:79], v[152:155]\n" \
"ds_read_b64_tr_b16 a[46:47], v12 offset:512\n" \
"v_lshlrev_b32 v27, 16, v28\n" \
"v_lshlrev_b32 v25, 16, v26\n" \
"v_mfma_f32_16x16x32_bf16 v[156:159], a[108:111], a[76:79], v[156:159]\n" \
"v_and_b32 v28, 0xffff0000, v28\n" \
"v_and_b32 v26, 0xffff0000, v26\n" \
"v_mfma_f32_16x16x32_bf16 v[160:163], a[112:115], a[72:75], v[160:163]\n" \
"ds_read_b128 a[16:19], v5 offset:8192\n" \
"v_mul_f32 v29, v25, v27\n" \
"v_mul_f32 v58, v26, v28\n" \
"v_mfma_f32_16x16x32_bf16 v[164:167], a[116:119], a[72:75], v[164:167]\n" \
"v_accvgpr_read_b32 v40, a213\n" \
"v_lshlrev_b32 v34, 16, v40\n" \
"v_mfma_f32_16x16x32_bf16 v[168:171], a[112:115], a[76:79], v[168:171]\n" \
"s_add_u32 m0, s22, 50944\n" \
"ds_read_b64_tr_b16 a[48:49], v9 offset:1024\n" \
"v_and_b32 v40, 0xffff0000, v40\n" \
"v_mfma_f32_16x16x32_bf16 v[172:175], a[116:119], a[76:79], v[172:175]\n" \
"buffer_load_dwordx4 v21, s[32:35], s47 offen offset:256 lds\n" \
"v_mfma_f32_16x16x32_bf16 v[176:179], a[120:123], a[72:75], v[176:179]\n" \
"ds_read_b64_tr_b16 a[50:51], v10 offset:1024\n" \
"v_mul_f32 v25, v31, v45\n" \
"v_mul_f32 v26, v37, v49\n" \
"v_mfma_f32_16x16x32_bf16 v[180:183], a[124:127], a[72:75], v[180:183]\n" \
"v_fmac_f32 v25, v32, v44\n" \
"v_fmac_f32 v26, v38, v48\n" \
"v_mfma_f32_16x16x32_bf16 v[184:187], a[120:123], a[76:79], v[184:187]\n" \
"ds_read_b128 a[20:23], v5 offset:10240\n" \
"v_fmac_f32 v25, v33, v43\n" \
"v_fmac_f32 v26, v39, v47\n" \
"v_mfma_f32_16x16x32_bf16 v[188:191], a[124:127], a[76:79], v[188:191]\n" \
"v_fmac_f32 v25, v34, v42\n" \
"v_fmac_f32 v26, v40, v46\n" \
"v_mfma_f32_16x16x32_bf16 v[192:195], a[96:99], a[80:83], v[192:195]\n" \
"s_add_u32 m0, s50, 73728\n" \
"ds_read_b64_tr_b16 a[52:53], v11 offset:1024\n" \
"v_accvgpr_read_b32 v28, a229\n" \
"v_mfma_f32_16x16x32_bf16 v[196:199], a[100:103], a[80:83], v[196:199]\n" \
"buffer_load_dwordx4 v15, s[28:31], s46 offen lds\n" \
"v_mfma_f32_16x16x32_bf16 v[200:203], a[96:99], a[84:87], v[200:203]\n" \
"ds_read_b64_tr_b16 a[54:55], v12 offset:1024\n" \
"v_cvt_pk_bf16_f32 v26, v25, v26\n" \
"v_lshlrev_b32 v27, 16, v28\n" \
"v_mfma_f32_16x16x32_bf16 v[204:207], a[100:103], a[84:87], v[204:207]\n" \
"v_lshlrev_b32 v25, 16, v26\n" \
"v_and_b32 v28, 0xffff0000, v28\n" \
"v_mfma_f32_16x16x32_bf16 v[208:211], a[104:107], a[80:83], v[208:211]\n" \
"ds_read_b128 a[24:27], v5 offset:12288\n" \
"v_and_b32 v26, 0xffff0000, v26\n" \
"v_mul_f32 v25, v25, v27\n" \
"v_mfma_f32_16x16x32_bf16 v[212:215], a[108:111], a[80:83], v[212:215]\n" \
"v_mul_f32 v26, v26, v28\n" \
"v_cvt_pk_bf16_f32 v50, v25, v29\n" \
"v_mfma_f32_16x16x32_bf16 v[216:219], a[104:107], a[84:87], v[216:219]\n" \
"s_add_u32 m0, s22, 51840\n" \
"ds_read_b64_tr_b16 a[56:57], v9 offset:1536\n" \
"v_cvt_pk_bf16_f32 v54, v26, v58\n" \
"v_mfma_f32_16x16x32_bf16 v[220:223], a[108:111], a[84:87], v[220:223]\n" \
"buffer_load_dwordx4 v21, s[32:35], s47 offen offset:384 lds\n" \
"s_mov_b32 s94, 79699968\n" \
"v_mfma_f32_16x16x32_bf16 v[224:227], a[112:115], a[80:83], v[224:227]\n" \
"ds_read_b64_tr_b16 a[58:59], v10 offset:1536\n" \
"v_mfma_f32_16x16x32_bf16 v[228:231], a[116:119], a[80:83], v[228:231]\n" \
"v_mfma_f32_16x16x32_bf16 v[232:235], a[112:115], a[84:87], v[232:235]\n" \
"ds_read_b128 a[28:31], v5 offset:14336\n" \
"v_mfma_f32_16x16x32_bf16 v[236:239], a[116:119], a[84:87], v[236:239]\n" \
"v_mfma_f32_16x16x32_bf16 v[240:243], a[120:123], a[80:83], v[240:243]\n" \
"s_add_u32 m0, s50, 77824\n" \
"ds_read_b64_tr_b16 a[60:61], v11 offset:1536\n" \
"v_mfma_f32_16x16x32_bf16 v[244:247], a[124:127], a[80:83], v[244:247]\n" \
"buffer_load_dwordx4 v16, s[28:31], s46 offen lds\n" \
"v_mfma_f32_16x16x32_bf16 v[248:251], a[120:123], a[84:87], v[248:251]\n" \
"ds_read_b64_tr_b16 a[62:63], v12 offset:1536\n" \
"v_mfma_f32_16x16x32_bf16 v[252:255], a[124:127], a[84:87], v[252:255]\n" \
"v_mfma_f32_16x16x32_bf16 a[128:131], a[96:99], a[88:91], a[128:131]\n" \
"v_mfma_f32_16x16x32_bf16 a[132:135], a[100:103], a[88:91], a[132:135]\n" \
"s_mov_b64 exec, 0xffff\nglobal_load_dword a244, v2, s[88:89]\ns_mov_b64 exec, -1\n" \
"v_mfma_f32_16x16x32_bf16 a[136:139], a[96:99], a[92:95], a[136:139]\n" \
"v_mfma_f32_16x16x32_bf16 a[140:143], a[100:103], a[92:95], a[140:143]\n" \
"v_mfma_f32_16x16x32_bf16 a[144:147], a[104:107], a[88:91], a[144:147]\n" \
"v_mfma_f32_16x16x32_bf16 a[148:151], a[108:111], a[88:91], a[148:151]\n" \
"buffer_store_dwordx4 v[50:53], v60, s[36:39], s94 offen offset:0 sc1\n" \
"s_mov_b32 s95, 79704064\n" \
"v_mfma_f32_16x16x32_bf16 a[152:155], a[104:107], a[92:95], a[152:155]\n" \
"v_mfma_f32_16x16x32_bf16 a[156:159], a[108:111], a[92:95], a[156:159]\n" \
"s_waitcnt vmcnt(23) lgkmcnt(0)\n" \
"v_mfma_f32_16x16x32_bf16 a[160:163], a[112:115], a[88:91], a[160:163]\n" \
"s_barrier\n" \
"v_mfma_f32_16x16x32_bf16 a[164:167], a[116:119], a[88:91], a[164:167]\n" \
"v_mfma_f32_16x16x32_bf16 a[168:171], a[112:115], a[92:95], a[168:171]\n" \
"v_mfma_f32_16x16x32_bf16 a[172:175], a[116:119], a[92:95], a[172:175]\n" \
"buffer_store_dwordx4 v[54:57], v60, s[36:39], s95 offen offset:0 sc1\n" \
"v_mfma_f32_16x16x32_bf16 a[176:179], a[120:123], a[88:91], a[176:179]\n" \
"s_lshl_b32 s62, s8, 2\n" \
"s_add_u32 s62, s62, %2\n" \
"s_lshl_b32 s63, s62, 12\n" \
"v_mfma_f32_16x16x32_bf16 a[180:183], a[124:127], a[88:91], a[180:183]\n" \
"s_add_u32 s68, s10, s63\ns_addc_u32 s69, s11, 0\n" \
"s_and_b32 s69, s69, 0xffff\n" \
"s_mov_b32 s70, 4096\n" \
"v_mfma_f32_16x16x32_bf16 a[184:187], a[120:123], a[92:95], a[184:187]\n" \
"s_mov_b32 s71, 0x00020000\n" \
"s_add_u32 s63, s63, 79691776\n" \
"s_add_u32 s72, s4, s63\ns_addc_u32 s73, s5, 0\n" \
"v_mfma_f32_16x16x32_bf16 a[188:191], a[124:127], a[92:95], a[188:191]\n" \
"s_and_b32 s73, s73, 0xffff\n" \
"s_mov_b32 s74, 4096\n" \
"s_mov_b32 s75, 0x00020000\n" \
"v_mfma_f32_16x16x32_bf16 v[64:67], a[32:35], a[0:3], v[64:67]\n" \
"ds_read_b128 a[64:67], v6 offset:0\n" \
"v_add_u32 v23, -32, v4\n" \
"v_add_u32 v24, 992, v4\n" \
"v_mfma_f32_16x16x32_bf16 v[68:71], a[36:39], a[0:3], v[68:71]\n" \
"v_add_u32 v25, -16, v4\n" \
"v_add_u32 v26, -512, v4\n" \
"v_mfma_f32_16x16x32_bf16 v[72:75], a[32:35], a[4:7], v[72:75]\n" \
"s_mov_b32 s47, 3145728\n" \
"s_add_u32 m0, s22, 0\n" \
"ds_read_b64_tr_b16 a[96:97], v9 offset:16384\n" \
"v_mfma_f32_16x16x32_bf16 v[76:79], a[36:39], a[4:7], v[76:79]\n" \
"buffer_load_dwordx4 v21, s[32:35], s47 offen offset:0 lds\n" \
"v_mfma_f32_16x16x32_bf16 v[80:83], a[40:43], a[0:3], v[80:83]\n" \
"ds_read_b64_tr_b16 a[98:99], v10 offset:16384\n" \
"v_add_u32 v27, 512, v4\n" \
"s_lshr_b32 s63, %2, 1\n" \
"v_mfma_f32_16x16x32_bf16 v[84:87], a[44:47], a[0:3], v[84:87]\n" \
"s_lshl_b32 s63, s63, 16\n" \
"s_lshl_b32 s56, %2, 14\n" \
"s_add_u32 s56, s56, s63\n" \
"v_mfma_f32_16x16x32_bf16 v[88:91], a[40:43], a[4:7], v[88:91]\n" \
"ds_read_b128 a[68:71], v6 offset:2048\n" \
"v_add_u32 v55, s56, v4\n" \
"v_mov_b32 v28, 0\n" \
"v_mfma_f32_16x16x32_bf16 v[92:95], a[44:47], a[4:7], v[92:95]\n" \
"s_lshl_b32 s62, s62, 1\n" \
"v_mov_b32 v29, s62\n" \
"v_lshlrev_b32 v56, 23, v4\n" \
"v_mfma_f32_16x16x32_bf16 v[96:99], a[48:51], a[0:3], v[96:99]\n" \
"s_mov_b32 s46, 768\n" \
"s_add_u32 m0, s50, 81920\n" \
"ds_read_b64_tr_b16 a[100:101], v11 offset:16384\n" \
"v_mfma_f32_16x16x32_bf16 v[100:103], a[52:55], a[0:3], v[100:103]\n" \
"buffer_load_dwordx4 v17, s[28:31], s46 offen lds\n" \
"s_and_b32 s62, %1, 7\n" \
"s_lshl_b32 s62, s62, 8\n" \
"v_mfma_f32_16x16x32_bf16 v[104:107], a[48:51], a[4:7], v[104:107]\n" \
"ds_read_b64_tr_b16 a[102:103], v12 offset:16384\n" \
"s_add_u32 s62, s62, 94380032\n" \
"v_bfe_u32 v57, v4, 4, 5\n" \
"v_mfma_f32_16x16x32_bf16 v[108:111], a[52:55], a[4:7], v[108:111]\n" \
"v_lshl_add_u32 v57, v57, 3, s62\n" \
"v_mfma_f32_16x16x32_bf16 v[112:115], a[56:59], a[0:3], v[112:115]\n" \
"ds_read_b128 a[72:75], v6 offset:4096\n" \
"v_mfma_f32_16x16x32_bf16 v[116:119], a[60:63], a[0:3], v[116:119]\n" \
"v_mfma_f32_16x16x32_bf16 v[120:123], a[56:59], a[4:7], v[120:123]\n" \
"s_add_u32 m0, s22, 896\n" \
"ds_read_b64_tr_b16 a[104:105], v9 offset:16896\n" \
"v_mfma_f32_16x16x32_bf16 v[124:127], a[60:63], a[4:7], v[124:127]\n" \
"buffer_load_dwordx4 v21, s[32:35], s47 offen offset:128 lds\n" \
"v_mfma_f32_16x16x32_bf16 v[128:131], a[32:35], a[8:11], v[128:131]\n" \
"ds_read_b64_tr_b16 a[106:107], v10 offset:16896\n" \
"v_mfma_f32_16x16x32_bf16 v[132:135], a[36:39], a[8:11], v[132:135]\n" \
"v_mfma_f32_16x16x32_bf16 v[136:139], a[32:35], a[12:15], v[136:139]\n" \
"ds_read_b128 a[76:79], v6 offset:6144\n" \
"v_mfma_f32_16x16x32_bf16 v[140:143], a[36:39], a[12:15], v[140:143]\n" \
"v_mfma_f32_16x16x32_bf16 v[144:147], a[40:43], a[8:11], v[144:147]\n" \
"s_add_u32 m0, s50, 86016\n" \
"ds_read_b64_tr_b16 a[108:109], v11 offset:16896\n" \
"v_mfma_f32_16x16x32_bf16 v[148:151], a[44:47], a[8:11], v[148:151]\n" \
"buffer_load_dwordx4 v18, s[28:31], s46 offen lds\n" \
"v_mfma_f32_16x16x32_bf16 v[152:155], a[40:43], a[12:15], v[152:155]\n" \
"ds_read_b64_tr_b16 a[110:111], v12 offset:16896\n" \
"v_mfma_f32_16x16x32_bf16 v[156:159], a[44:47], a[12:15], v[156:159]\n" \
"v_mfma_f32_16x16x32_bf16 v[160:163], a[48:51], a[8:11], v[160:163]\n" \
"ds_read_b128 a[80:83], v6 offset:8192\n" \
"v_mfma_f32_16x16x32_bf16 v[164:167], a[52:55], a[8:11], v[164:167]\n" \
"v_mfma_f32_16x16x32_bf16 v[168:171], a[48:51], a[12:15], v[168:171]\n" \
"s_add_u32 m0, s22, 1792\n" \
"ds_read_b64_tr_b16 a[112:113], v9 offset:17408\n" \
"v_mfma_f32_16x16x32_bf16 v[172:175], a[52:55], a[12:15], v[172:175]\n" \
"buffer_load_dwordx4 v21, s[32:35], s47 offen offset:256 lds\n" \
"v_mfma_f32_16x16x32_bf16 v[176:179], a[56:59], a[8:11], v[176:179]\n" \
"ds_read_b64_tr_b16 a[114:115], v10 offset:17408\n" \
"v_mfma_f32_16x16x32_bf16 v[180:183], a[60:63], a[8:11], v[180:183]\n" \
"v_mfma_f32_16x16x32_bf16 v[184:187], a[56:59], a[12:15], v[184:187]\n" \
"ds_read_b128 a[84:87], v6 offset:10240\n" \
"v_mfma_f32_16x16x32_bf16 v[188:191], a[60:63], a[12:15], v[188:191]\n" \
"v_mfma_f32_16x16x32_bf16 v[192:195], a[32:35], a[16:19], v[192:195]\n" \
"s_add_u32 m0, s50, 90112\n" \
"ds_read_b64_tr_b16 a[116:117], v11 offset:17408\n" \
"v_mfma_f32_16x16x32_bf16 v[196:199], a[36:39], a[16:19], v[196:199]\n" \
"buffer_load_dwordx4 v19, s[28:31], s46 offen lds\n" \
"v_mfma_f32_16x16x32_bf16 v[200:203], a[32:35], a[20:23], v[200:203]\n" \
"ds_read_b64_tr_b16 a[118:119], v12 offset:17408\n" \
"v_mfma_f32_16x16x32_bf16 v[204:207], a[36:39], a[20:23], v[204:207]\n" \
"v_mfma_f32_16x16x32_bf16 v[208:211], a[40:43], a[16:19], v[208:211]\n" \
"ds_read_b128 a[88:91], v6 offset:12288\n" \
"v_mfma_f32_16x16x32_bf16 v[212:215], a[44:47], a[16:19], v[212:215]\n" \
"v_mfma_f32_16x16x32_bf16 v[216:219], a[40:43], a[20:23], v[216:219]\n" \
"s_add_u32 m0, s22, 2688\n" \
"ds_read_b64_tr_b16 a[120:121], v9 offset:17920\n" \
"v_mfma_f32_16x16x32_bf16 v[220:223], a[44:47], a[20:23], v[220:223]\n" \
"buffer_load_dwordx4 v21, s[32:35], s47 offen offset:384 lds\n" \
"v_mfma_f32_16x16x32_bf16 v[224:227], a[48:51], a[16:19], v[224:227]\n" \
"ds_read_b64_tr_b16 a[122:123], v10 offset:17920\n" \
"v_mfma_f32_16x16x32_bf16 v[228:231], a[52:55], a[16:19], v[228:231]\n" \
"v_mfma_f32_16x16x32_bf16 v[232:235], a[48:51], a[20:23], v[232:235]\n" \
"ds_read_b128 a[92:95], v6 offset:14336\n" \
"v_mfma_f32_16x16x32_bf16 v[236:239], a[52:55], a[20:23], v[236:239]\n" \
"v_mfma_f32_16x16x32_bf16 v[240:243], a[56:59], a[16:19], v[240:243]\n" \
"s_add_u32 m0, s50, 94208\n" \
"ds_read_b64_tr_b16 a[124:125], v11 offset:17920\n" \
"v_mfma_f32_16x16x32_bf16 v[244:247], a[60:63], a[16:19], v[244:247]\n" \
"buffer_load_dwordx4 v20, s[28:31], s46 offen lds\n" \
"v_mfma_f32_16x16x32_bf16 v[248:251], a[56:59], a[20:23], v[248:251]\n" \
"ds_read_b64_tr_b16 a[126:127], v12 offset:17920\n" \
"v_mfma_f32_16x16x32_bf16 v[252:255], a[60:63], a[20:23], v[252:255]\n" \
"v_mfma_f32_16x16x32_bf16 a[128:131], a[32:35], a[24:27], a[128:131]\n" \
"s_add_u32 s88, s88, 2048\ns_addc_u32 s89, s89, 0\n" \
"v_mfma_f32_16x16x32_bf16 a[132:135], a[36:39], a[24:27], a[132:135]\n" \
"s_mov_b64 exec, 0xffff\nglobal_load_dword a244, v2, s[88:89]\ns_mov_b64 exec, -1\n" \
"v_mfma_f32_16x16x32_bf16 a[136:139], a[32:35], a[28:31], a[136:139]\n" \
"v_mfma_f32_16x16x32_bf16 a[140:143], a[36:39], a[28:31], a[140:143]\n" \
"v_mfma_f32_16x16x32_bf16 a[144:147], a[40:43], a[24:27], a[144:147]\n" \
"v_mfma_f32_16x16x32_bf16 a[148:151], a[44:47], a[24:27], a[148:151]\n" \
"v_mfma_f32_16x16x32_bf16 a[152:155], a[40:43], a[28:31], a[152:155]\n" \
"v_mfma_f32_16x16x32_bf16 a[156:159], a[44:47], a[28:31], a[156:159]\n" \
"s_waitcnt vmcnt(9) lgkmcnt(0)\n" \
"v_mfma_f32_16x16x32_bf16 a[160:163], a[48:51], a[24:27], a[160:163]\n" \
"s_barrier\n" \
"v_mfma_f32_16x16x32_bf16 a[164:167], a[52:55], a[24:27], a[164:167]\n" \
"v_mov_b32 v58, s14\n" \
"v_mfma_f32_16x16x32_bf16 a[168:171], a[48:51], a[28:31], a[168:171]\n" \
"v_mov_b32 v59, s15\n" \
"v_mfma_f32_16x16x32_bf16 a[172:175], a[52:55], a[28:31], a[172:175]\n" \
"v_mfma_f32_16x16x32_bf16 a[176:179], a[56:59], a[24:27], a[176:179]\n" \
"v_mfma_f32_16x16x32_bf16 a[180:183], a[60:63], a[24:27], a[180:183]\n" \
"v_mfma_f32_16x16x32_bf16 a[184:187], a[56:59], a[28:31], a[184:187]\n" \
"v_mfma_f32_16x16x32_bf16 a[188:191], a[60:63], a[28:31], a[188:191]\n" \
"v_mfma_f32_16x16x32_bf16 v[64:67], a[96:99], a[64:67], v[64:67]\n" \
"ds_read_b128 a[0:3], v5 offset:32768\n" \
"v_mfma_f32_16x16x32_bf16 v[68:71], a[100:103], a[64:67], v[68:71]\n" \
"v_mfma_f32_16x16x32_bf16 v[72:75], a[96:99], a[68:71], v[72:75]\n" \
"ds_read_b64_tr_b16 a[32:33], v9 offset:32768\n" \
"v_mfma_f32_16x16x32_bf16 v[76:79], a[100:103], a[68:71], v[76:79]\n" \
"buffer_store_dwordx2 v[58:59], v56, s[36:39], s23 offen sc1\n" \
"v_mfma_f32_16x16x32_bf16 v[80:83], a[104:107], a[64:67], v[80:83]\n" \
"ds_read_b64_tr_b16 a[34:35], v10 offset:32768\n" \
"v_mfma_f32_16x16x32_bf16 v[84:87], a[108:111], a[64:67], v[84:87]\n" \
"v_mfma_f32_16x16x32_bf16 v[88:91], a[104:107], a[68:71], v[88:91]\n" \
"s_mov_b32 s47, 3407872\n" \
"s_add_u32 m0, s22, 16384\n" \
"ds_read_b128 a[4:7], v5 offset:34816\n" \
"v_mfma_f32_16x16x32_bf16 v[92:95], a[108:111], a[68:71], v[92:95]\n" \
"buffer_load_dwordx4 v21, s[32:35], s47 offen offset:0 lds\n" \
"v_mfma_f32_16x16x32_bf16 v[96:99], a[112:115], a[64:67], v[96:99]\n" \
"ds_read_b64_tr_b16 a[36:37], v11 offset:32768\n" \
"v_mfma_f32_16x16x32_bf16 v[100:103], a[116:119], a[64:67], v[100:103]\n" \
"v_mfma_f32_16x16x32_bf16 v[104:107], a[112:115], a[68:71], v[104:107]\n" \
"s_mov_b32 s46, 896\n" \
"s_add_u32 m0, s50, 0\n" \
"ds_read_b64_tr_b16 a[38:39], v12 offset:32768\n" \
"v_mfma_f32_16x16x32_bf16 v[108:111], a[116:119], a[68:71], v[108:111]\n" \
"buffer_load_dwordx4 v13, s[28:31], s46 offen lds\n" \
"v_mfma_f32_16x16x32_bf16 v[112:115], a[120:123], a[64:67], v[112:115]\n" \
"ds_read_b128 a[8:11], v5 offset:36864\n" \
"v_mfma_f32_16x16x32_bf16 v[116:119], a[124:127], a[64:67], v[116:119]\n" \
"v_mfma_f32_16x16x32_bf16 v[120:123], a[120:123], a[68:71], v[120:123]\n" \
"s_add_u32 m0, s22, 17280\n" \
"ds_read_b64_tr_b16 a[40:41], v9 offset:33280\n" \
"v_mfma_f32_16x16x32_bf16 v[124:127], a[124:127], a[68:71], v[124:127]\n" \
"buffer_load_dwordx4 v21, s[32:35], s47 offen offset:128 lds\n" \
"v_mfma_f32_16x16x32_bf16 v[128:131], a[96:99], a[72:75], v[128:131]\n" \
"ds_read_b64_tr_b16 a[42:43], v10 offset:33280\n" \
"v_mfma_f32_16x16x32_bf16 v[132:135], a[100:103], a[72:75], v[132:135]\n" \
"v_mfma_f32_16x16x32_bf16 v[136:139], a[96:99], a[76:79], v[136:139]\n" \
"s_add_u32 m0, s50, 4096\n" \
"ds_read_b128 a[12:15], v5 offset:38912\n" \
"v_mfma_f32_16x16x32_bf16 v[140:143], a[100:103], a[76:79], v[140:143]\n" \
"buffer_load_dwordx4 v14, s[28:31], s46 offen lds\n" \
"v_mfma_f32_16x16x32_bf16 v[144:147], a[104:107], a[72:75], v[144:147]\n" \
"ds_read_b64_tr_b16 a[44:45], v11 offset:33280\n" \
"v_mfma_f32_16x16x32_bf16 v[148:151], a[108:111], a[72:75], v[148:151]\n" \
"v_mfma_f32_16x16x32_bf16 v[152:155], a[104:107], a[76:79], v[152:155]\n" \
"s_add_u32 m0, s22, 18176\n" \
"ds_read_b64_tr_b16 a[46:47], v12 offset:33280\n" \
"v_mfma_f32_16x16x32_bf16 v[156:159], a[108:111], a[76:79], v[156:159]\n" \
"buffer_load_dwordx4 v21, s[32:35], s47 offen offset:256 lds\n" \
"v_mfma_f32_16x16x32_bf16 v[160:163], a[112:115], a[72:75], v[160:163]\n" \
"ds_read_b128 a[16:19], v5 offset:40960\n" \
"v_mfma_f32_16x16x32_bf16 v[164:167], a[116:119], a[72:75], v[164:167]\n" \
"v_mfma_f32_16x16x32_bf16 v[168:171], a[112:115], a[76:79], v[168:171]\n" \
"s_add_u32 m0, s50, 8192\n" \
"ds_read_b64_tr_b16 a[48:49], v9 offset:33792\n" \
"v_mfma_f32_16x16x32_bf16 v[172:175], a[116:119], a[76:79], v[172:175]\n" \
"buffer_load_dwordx4 v15, s[28:31], s46 offen lds\n" \
"v_mfma_f32_16x16x32_bf16 v[176:179], a[120:123], a[72:75], v[176:179]\n" \
"ds_read_b64_tr_b16 a[50:51], v10 offset:33792\n" \
"v_mfma_f32_16x16x32_bf16 v[180:183], a[124:127], a[72:75], v[180:183]\n" \
"v_mfma_f32_16x16x32_bf16 v[184:187], a[120:123], a[76:79], v[184:187]\n" \
"s_add_u32 m0, s22, 19072\n" \
"ds_read_b128 a[20:23], v5 offset:43008\n" \
"v_mfma_f32_16x16x32_bf16 v[188:191], a[124:127], a[76:79], v[188:191]\n" \
"buffer_load_dwordx4 v21, s[32:35], s47 offen offset:384 lds\n" \
"v_mfma_f32_16x16x32_bf16 v[192:195], a[96:99], a[80:83], v[192:195]\n" \
"ds_read_b64_tr_b16 a[52:53], v11 offset:33792\n" \
"v_mfma_f32_16x16x32_bf16 v[196:199], a[100:103], a[80:83], v[196:199]\n" \
"v_mfma_f32_16x16x32_bf16 v[200:203], a[96:99], a[84:87], v[200:203]\n" \
"s_add_u32 m0, s50, 12288\n" \
"ds_read_b64_tr_b16 a[54:55], v12 offset:33792\n" \
"v_mfma_f32_16x16x32_bf16 v[204:207], a[100:103], a[84:87], v[204:207]\n" \
"buffer_load_dwordx4 v16, s[28:31], s46 offen lds\n" \
"v_mfma_f32_16x16x32_bf16 v[208:211], a[104:107], a[80:83], v[208:211]\n" \
"ds_read_b128 a[24:27], v5 offset:45056\n" \
"v_mfma_f32_16x16x32_bf16 v[212:215], a[108:111], a[80:83], v[212:215]\n" \
"v_mfma_f32_16x16x32_bf16 v[216:219], a[104:107], a[84:87], v[216:219]\n" \
"ds_read_b64_tr_b16 a[56:57], v9 offset:34304\n" \
"v_mfma_f32_16x16x32_bf16 v[220:223], a[108:111], a[84:87], v[220:223]\n" \
"buffer_load_dwordx4 v[30:33], v23, s[68:71], 0 offen offset:0\n" \
"v_mfma_f32_16x16x32_bf16 v[224:227], a[112:115], a[80:83], v[224:227]\n" \
"ds_read_b64_tr_b16 a[58:59], v10 offset:34304\n" \
"v_mfma_f32_16x16x32_bf16 v[228:231], a[116:119], a[80:83], v[228:231]\n" \
"v_mfma_f32_16x16x32_bf16 v[232:235], a[112:115], a[84:87], v[232:235]\n" \
"ds_read_b128 a[28:31], v5 offset:47104\n" \
"v_mfma_f32_16x16x32_bf16 v[236:239], a[116:119], a[84:87], v[236:239]\n" \
"buffer_load_dword v50, v25, s[68:71], 0 offen offset:0\n" \
"v_mfma_f32_16x16x32_bf16 v[240:243], a[120:123], a[80:83], v[240:243]\n" \
"ds_read_b64_tr_b16 a[60:61], v11 offset:34304\n" \
"v_mfma_f32_16x16x32_bf16 v[244:247], a[124:127], a[80:83], v[244:247]\n" \
"v_mfma_f32_16x16x32_bf16 v[248:251], a[120:123], a[84:87], v[248:251]\n" \
"ds_read_b64_tr_b16 a[62:63], v12 offset:34304\n" \
"v_mfma_f32_16x16x32_bf16 v[252:255], a[124:127], a[84:87], v[252:255]\n" \
"buffer_load_dwordx4 v[34:37], v24, s[68:71], 0 offen offset:0\n" \
"v_mfma_f32_16x16x32_bf16 a[128:131], a[96:99], a[88:91], a[128:131]\n" \
"v_mfma_f32_16x16x32_bf16 a[132:135], a[100:103], a[88:91], a[132:135]\n" \
"v_mfma_f32_16x16x32_bf16 a[136:139], a[96:99], a[92:95], a[136:139]\n" \
"v_mfma_f32_16x16x32_bf16 a[140:143], a[100:103], a[92:95], a[140:143]\n" \
"buffer_load_dword v51, v24, s[68:71], 0 offen offset:16\n" \
"v_mfma_f32_16x16x32_bf16 a[144:147], a[104:107], a[88:91], a[144:147]\n" \
"v_mfma_f32_16x16x32_bf16 a[148:151], a[108:111], a[88:91], a[148:151]\n" \
"v_mfma_f32_16x16x32_bf16 a[152:155], a[104:107], a[92:95], a[152:155]\n" \
"v_mfma_f32_16x16x32_bf16 a[156:159], a[108:111], a[92:95], a[156:159]\n" \
"s_waitcnt vmcnt(26) lgkmcnt(0)\n" \
"v_mfma_f32_16x16x32_bf16 a[160:163], a[112:115], a[88:91], a[160:163]\n" \
"s_barrier\n" \
"v_mfma_f32_16x16x32_bf16 a[164:167], a[116:119], a[88:91], a[164:167]\n" \
"v_mfma_f32_16x16x32_bf16 a[168:171], a[112:115], a[92:95], a[168:171]\n" \
"v_mfma_f32_16x16x32_bf16 a[172:175], a[116:119], a[92:95], a[172:175]\n" \
"buffer_load_dwordx4 v[38:41], v24, s[68:71], 0 offen offset:1024\n" \
"v_mfma_f32_16x16x32_bf16 a[176:179], a[120:123], a[88:91], a[176:179]\n" \
"v_mfma_f32_16x16x32_bf16 a[180:183], a[124:127], a[88:91], a[180:183]\n" \
"v_mfma_f32_16x16x32_bf16 a[184:187], a[120:123], a[92:95], a[184:187]\n" \
"v_mfma_f32_16x16x32_bf16 a[188:191], a[124:127], a[92:95], a[188:191]\n" \
"buffer_load_dword v52, v24, s[68:71], 0 offen offset:1040\n" \
"v_mfma_f32_16x16x32_bf16 v[64:67], a[32:35], a[0:3], v[64:67]\n" \
"ds_read_b128 a[64:67], v6 offset:32768\n" \
"v_mfma_f32_16x16x32_bf16 v[68:71], a[36:39], a[0:3], v[68:71]\n" \
"v_mfma_f32_16x16x32_bf16 v[72:75], a[32:35], a[4:7], v[72:75]\n" \
"ds_read_b64_tr_b16 a[96:97], v9 offset:49152\n" \
"v_mfma_f32_16x16x32_bf16 v[76:79], a[36:39], a[4:7], v[76:79]\n" \
"buffer_load_dwordx4 v[42:45], v24, s[68:71], 0 offen offset:2048\n" \
"v_mfma_f32_16x16x32_bf16 v[80:83], a[40:43], a[0:3], v[80:83]\n" \
"ds_read_b64_tr_b16 a[98:99], v10 offset:49152\n" \
"v_mfma_f32_16x16x32_bf16 v[84:87], a[44:47], a[0:3], v[84:87]\n" \
"v_mfma_f32_16x16x32_bf16 v[88:91], a[40:43], a[4:7], v[88:91]\n" \
"ds_read_b128 a[68:71], v6 offset:34816\n" \
"v_mfma_f32_16x16x32_bf16 v[92:95], a[44:47], a[4:7], v[92:95]\n" \
"buffer_load_dword v53, v24, s[68:71], 0 offen offset:2064\n" \
"v_mfma_f32_16x16x32_bf16 v[96:99], a[48:51], a[0:3], v[96:99]\n" \
"ds_read_b64_tr_b16 a[100:101], v11 offset:49152\n" \
"v_mfma_f32_16x16x32_bf16 v[100:103], a[52:55], a[0:3], v[100:103]\n" \
"v_mfma_f32_16x16x32_bf16 v[104:107], a[48:51], a[4:7], v[104:107]\n" \
"ds_read_b64_tr_b16 a[102:103], v12 offset:49152\n" \
"v_mfma_f32_16x16x32_bf16 v[108:111], a[52:55], a[4:7], v[108:111]\n" \
"buffer_load_dwordx4 v[46:49], v24, s[68:71], 0 offen offset:3072\n" \
"v_mfma_f32_16x16x32_bf16 v[112:115], a[56:59], a[0:3], v[112:115]\n" \
"ds_read_b128 a[72:75], v6 offset:36864\n" \
"v_mfma_f32_16x16x32_bf16 v[116:119], a[60:63], a[0:3], v[116:119]\n" \
"v_mfma_f32_16x16x32_bf16 v[120:123], a[56:59], a[4:7], v[120:123]\n" \
"ds_read_b64_tr_b16 a[104:105], v9 offset:49664\n" \
"v_mfma_f32_16x16x32_bf16 v[124:127], a[60:63], a[4:7], v[124:127]\n" \
"buffer_load_dword v54, v24, s[68:71], 0 offen offset:3088\n" \
"v_mfma_f32_16x16x32_bf16 v[128:131], a[32:35], a[8:11], v[128:131]\n" \
"ds_read_b64_tr_b16 a[106:107], v10 offset:49664\n" \
"v_mfma_f32_16x16x32_bf16 v[132:135], a[36:39], a[8:11], v[132:135]\n" \
"v_mfma_f32_16x16x32_bf16 v[136:139], a[32:35], a[12:15], v[136:139]\n" \
"ds_read_b128 a[76:79], v6 offset:38912\n" \
"v_mfma_f32_16x16x32_bf16 v[140:143], a[36:39], a[12:15], v[140:143]\n" \
"global_load_short_d16_hi v28, v29, s[12:13]\n" \
"v_mfma_f32_16x16x32_bf16 v[144:147], a[40:43], a[8:11], v[144:147]\n" \
"ds_read_b64_tr_b16 a[108:109], v11 offset:49664\n" \
"v_mfma_f32_16x16x32_bf16 v[148:151], a[44:47], a[8:11], v[148:151]\n" \
"v_mfma_f32_16x16x32_bf16 v[152:155], a[40:43], a[12:15], v[152:155]\n" \
"s_mov_b32 s47, 3670016\n" \
"s_add_u32 m0, s22, 32768\n" \
"ds_read_b64_tr_b16 a[110:111], v12 offset:49664\n" \
"v_mfma_f32_16x16x32_bf16 v[156:159], a[44:47], a[12:15], v[156:159]\n" \
"LH2_%=:\ns_cbranch_execz LH10_%=\n" \
"buffer_load_dwordx4 v21, s[32:35], s47 offen offset:0 lds\n" \
"v_mfma_f32_16x16x32_bf16 v[160:163], a[48:51], a[8:11], v[160:163]\n" \
"ds_read_b128 a[80:83], v6 offset:40960\n" \
"v_mfma_f32_16x16x32_bf16 v[164:167], a[52:55], a[8:11], v[164:167]\n" \
"v_mfma_f32_16x16x32_bf16 v[168:171], a[48:51], a[12:15], v[168:171]\n" \
"s_mov_b32 s46, 896\n" \
"s_add_u32 m0, s50, 16384\n" \
"ds_read_b64_tr_b16 a[112:113], v9 offset:50176\n" \
"v_mfma_f32_16x16x32_bf16 v[172:175], a[52:55], a[12:15], v[172:175]\n" \
"buffer_load_dwordx4 v17, s[28:31], s46 offen lds\n" \
"v_mfma_f32_16x16x32_bf16 v[176:179], a[56:59], a[8:11], v[176:179]\n" \
"ds_read_b64_tr_b16 a[114:115], v10 offset:50176\n" \
"v_mfma_f32_16x16x32_bf16 v[180:183], a[60:63], a[8:11], v[180:183]\n" \
"v_mfma_f32_16x16x32_bf16 v[184:187], a[56:59], a[12:15], v[184:187]\n" \
"s_add_u32 m0, s22, 33664\n" \
"ds_read_b128 a[84:87], v6 offset:43008\n" \
"v_mfma_f32_16x16x32_bf16 v[188:191], a[60:63], a[12:15], v[188:191]\n" \
"buffer_load_dwordx4 v21, s[32:35], s47 offen offset:128 lds\n" \
"v_mfma_f32_16x16x32_bf16 v[192:195], a[32:35], a[16:19], v[192:195]\n" \
"ds_read_b64_tr_b16 a[116:117], v11 offset:50176\n" \
"v_mfma_f32_16x16x32_bf16 v[196:199], a[36:39], a[16:19], v[196:199]\n" \
"v_mfma_f32_16x16x32_bf16 v[200:203], a[32:35], a[20:23], v[200:203]\n" \
"s_add_u32 m0, s50, 20480\n" \
"ds_read_b64_tr_b16 a[118:119], v12 offset:50176\n" \
"v_mfma_f32_16x16x32_bf16 v[204:207], a[36:39], a[20:23], v[204:207]\n" \
"buffer_load_dwordx4 v18, s[28:31], s46 offen lds\n" \
"v_mfma_f32_16x16x32_bf16 v[208:211], a[40:43], a[16:19], v[208:211]\n" \
"ds_read_b128 a[88:91], v6 offset:45056\n" \
"v_mfma_f32_16x16x32_bf16 v[212:215], a[44:47], a[16:19], v[212:215]\n" \
"v_mfma_f32_16x16x32_bf16 v[216:219], a[40:43], a[20:23], v[216:219]\n" \
"s_add_u32 m0, s22, 34560\n" \
"ds_read_b64_tr_b16 a[120:121], v9 offset:50688\n" \
"v_mfma_f32_16x16x32_bf16 v[220:223], a[44:47], a[20:23], v[220:223]\n" \
"buffer_load_dwordx4 v21, s[32:35], s47 offen offset:256 lds\n" \
"v_mfma_f32_16x16x32_bf16 v[224:227], a[48:51], a[16:19], v[224:227]\n" \
"ds_read_b64_tr_b16 a[122:123], v10 offset:50688\n" \
"v_mfma_f32_16x16x32_bf16 v[228:231], a[52:55], a[16:19], v[228:231]\n" \
"v_mfma_f32_16x16x32_bf16 v[232:235], a[48:51], a[20:23], v[232:235]\n" \
"s_add_u32 m0, s50, 24576\n" \
"ds_read_b128 a[92:95], v6 offset:47104\n" \
"v_mfma_f32_16x16x32_bf16 v[236:239], a[52:55], a[20:23], v[236:239]\n" \
"buffer_load_dwordx4 v19, s[28:31], s46 offen lds\n" \
"v_mfma_f32_16x16x32_bf16 v[240:243], a[56:59], a[16:19], v[240:243]\n" \
"ds_read_b64_tr_b16 a[124:125], v11 offset:50688\n" \
"v_mfma_f32_16x16x32_bf16 v[244:247], a[60:63], a[16:19], v[244:247]\n" \
"v_mfma_f32_16x16x32_bf16 v[248:251], a[56:59], a[20:23], v[248:251]\n" \
"s_add_u32 m0, s22, 35456\n" \
"ds_read_b64_tr_b16 a[126:127], v12 offset:50688\n" \
"v_mfma_f32_16x16x32_bf16 v[252:255], a[60:63], a[20:23], v[252:255]\n" \
"buffer_load_dwordx4 v21, s[32:35], s47 offen offset:384 lds\n" \
"v_mfma_f32_16x16x32_bf16 a[128:131], a[32:35], a[24:27], a[128:131]\n" \
"v_mfma_f32_16x16x32_bf16 a[132:135], a[36:39], a[24:27], a[132:135]\n" \
"v_mfma_f32_16x16x32_bf16 a[136:139], a[32:35], a[28:31], a[136:139]\n" \
"s_add_u32 m0, s50, 28672\n" \
"v_mfma_f32_16x16x32_bf16 a[140:143], a[36:39], a[28:31], a[140:143]\n" \
"buffer_load_dwordx4 v20, s[28:31], s46 offen lds\n" \
"v_mfma_f32_16x16x32_bf16 a[144:147], a[40:43], a[24:27], a[144:147]\n" \
"v_mfma_f32_16x16x32_bf16 a[148:151], a[44:47], a[24:27], a[148:151]\n" \
"v_mfma_f32_16x16x32_bf16 a[152:155], a[40:43], a[28:31], a[152:155]\n" \
"v_mfma_f32_16x16x32_bf16 a[156:159], a[44:47], a[28:31], a[156:159]\n" \
"s_waitcnt vmcnt(29) lgkmcnt(0)\n" \
"v_mfma_f32_16x16x32_bf16 a[160:163], a[48:51], a[24:27], a[160:163]\n" \
"s_barrier\n" \
"v_mfma_f32_16x16x32_bf16 a[164:167], a[52:55], a[24:27], a[164:167]\n" \
"v_mfma_f32_16x16x32_bf16 a[168:171], a[48:51], a[28:31], a[168:171]\n" \
"v_mfma_f32_16x16x32_bf16 a[172:175], a[52:55], a[28:31], a[172:175]\n" \
"v_mfma_f32_16x16x32_bf16 a[176:179], a[56:59], a[24:27], a[176:179]\n" \
"v_mfma_f32_16x16x32_bf16 a[180:183], a[60:63], a[24:27], a[180:183]\n" \
"v_mfma_f32_16x16x32_bf16 a[184:187], a[56:59], a[28:31], a[184:187]\n" \
"v_mfma_f32_16x16x32_bf16 a[188:191], a[60:63], a[28:31], a[188:191]\n" \
"v_mfma_f32_16x16x32_bf16 v[64:67], a[96:99], a[64:67], v[64:67]\n" \
"ds_read_b128 a[0:3], v7 offset:0\n" \
"v_mfma_f32_16x16x32_bf16 v[68:71], a[100:103], a[64:67], v[68:71]\n" \
"s_mov_b64 s[28:29], s[64:65]\n" \
"v_mfma_f32_16x16x32_bf16 v[72:75], a[96:99], a[68:71], v[72:75]\n" \
"s_mov_b32 s47, 3932160\n" \
"s_add_u32 m0, s22, 49152\n" \
"ds_read_b64_tr_b16 a[32:33], v9 offset:0\n" \
"v_mfma_f32_16x16x32_bf16 v[76:79], a[100:103], a[68:71], v[76:79]\n" \
"buffer_load_dwordx4 v21, s[32:35], s47 offen offset:0 lds\n" \
"v_mfma_f32_16x16x32_bf16 v[80:83], a[104:107], a[64:67], v[80:83]\n" \
"ds_read_b64_tr_b16 a[34:35], v10 offset:0\n" \
"v_mfma_f32_16x16x32_bf16 v[84:87], a[108:111], a[64:67], v[84:87]\n" \
"s_and_b32 s29, s29, 0xffff\n" \
"v_mfma_f32_16x16x32_bf16 v[88:91], a[104:107], a[68:71], v[88:91]\n" \
"ds_read_b128 a[4:7], v7 offset:2048\n" \
"v_mfma_f32_16x16x32_bf16 v[92:95], a[108:111], a[68:71], v[92:95]\n" \
"s_mov_b32 s30, 4194304\n" \
"v_mfma_f32_16x16x32_bf16 v[96:99], a[112:115], a[64:67], v[96:99]\n" \
"s_add_u32 m0, s22, 50048\n" \
"ds_read_b64_tr_b16 a[36:37], v11 offset:0\n" \
"v_mfma_f32_16x16x32_bf16 v[100:103], a[116:119], a[64:67], v[100:103]\n" \
"buffer_load_dwordx4 v21, s[32:35], s47 offen offset:128 lds\n" \
"v_mfma_f32_16x16x32_bf16 v[104:107], a[112:115], a[68:71], v[104:107]\n" \
"ds_read_b64_tr_b16 a[38:39], v12 offset:0\n" \
"v_mfma_f32_16x16x32_bf16 v[108:111], a[116:119], a[68:71], v[108:111]\n" \
"s_lshl_b32 s60, s7, 18\n" \
"v_mfma_f32_16x16x32_bf16 v[112:115], a[120:123], a[64:67], v[112:115]\n" \
"ds_read_b128 a[8:11], v7 offset:4096\n" \
"v_mfma_f32_16x16x32_bf16 v[116:119], a[124:127], a[64:67], v[116:119]\n" \
"s_sub_u32 s61, 3932160, s60\n" \
"v_mfma_f32_16x16x32_bf16 v[120:123], a[120:123], a[68:71], v[120:123]\n" \
"s_add_u32 m0, s22, 50944\n" \
"ds_read_b64_tr_b16 a[40:41], v9 offset:512\n" \
"v_mfma_f32_16x16x32_bf16 v[124:127], a[124:127], a[68:71], v[124:127]\n" \
"buffer_load_dwordx4 v21, s[32:35], s47 offen offset:256 lds\n" \
"v_mfma_f32_16x16x32_bf16 v[128:131], a[96:99], a[72:75], v[128:131]\n" \
"ds_read_b64_tr_b16 a[42:43], v10 offset:512\n" \
"v_mfma_f32_16x16x32_bf16 v[132:135], a[100:103], a[72:75], v[132:135]\n" \
"v_add_u32 v13, s60, v63\n" \
"v_mfma_f32_16x16x32_bf16 v[136:139], a[96:99], a[76:79], v[136:139]\n" \
"ds_read_b128 a[12:15], v7 offset:6144\n" \
"v_mfma_f32_16x16x32_bf16 v[140:143], a[100:103], a[76:79], v[140:143]\n" \
"v_add_u32 v17, s61, v63\n" \
"v_mfma_f32_16x16x32_bf16 v[144:147], a[104:107], a[72:75], v[144:147]\n" \
"s_add_u32 m0, s22, 51840\n" \
"ds_read_b64_tr_b16 a[44:45], v11 offset:512\n" \
"v_mfma_f32_16x16x32_bf16 v[148:151], a[108:111], a[72:75], v[148:151]\n" \
"buffer_load_dwordx4 v21, s[32:35], s47 offen offset:384 lds\n" \
"v_mfma_f32_16x16x32_bf16 v[152:155], a[104:107], a[76:79], v[152:155]\n" \
"ds_read_b64_tr_b16 a[46:47], v12 offset:512\n" \
"v_mfma_f32_16x16x32_bf16 v[156:159], a[108:111], a[76:79], v[156:159]\n" \
"v_add_u32 v14, 0x10000, v13\n" \
"v_mfma_f32_16x16x32_bf16 v[160:163], a[112:115], a[72:75], v[160:163]\n" \
"ds_read_b128 a[16:19], v7 offset:8192\n" \
"v_mfma_f32_16x16x32_bf16 v[164:167], a[116:119], a[72:75], v[164:167]\n" \
"v_add_u32 v18, 0x10000, v17\n" \
"v_mfma_f32_16x16x32_bf16 v[168:171], a[112:115], a[76:79], v[168:171]\n" \
"ds_read_b64_tr_b16 a[48:49], v9 offset:1024\n" \
"v_mfma_f32_16x16x32_bf16 v[172:175], a[116:119], a[76:79], v[172:175]\n" \
"v_mfma_f32_16x16x32_bf16 v[176:179], a[120:123], a[72:75], v[176:179]\n" \
"ds_read_b64_tr_b16 a[50:51], v10 offset:1024\n" \
"v_mfma_f32_16x16x32_bf16 v[180:183], a[124:127], a[72:75], v[180:183]\n" \
"v_add_u32 v15, 0x10000, v14\n" \
"v_mfma_f32_16x16x32_bf16 v[184:187], a[120:123], a[76:79], v[184:187]\n" \
"ds_read_b128 a[20:23], v7 offset:10240\n" \
"v_mfma_f32_16x16x32_bf16 v[188:191], a[124:127], a[76:79], v[188:191]\n" \
"v_add_u32 v19, 0x10000, v18\n" \
"v_mfma_f32_16x16x32_bf16 v[192:195], a[96:99], a[80:83], v[192:195]\n" \
"ds_read_b64_tr_b16 a[52:53], v11 offset:1024\n" \
"v_mfma_f32_16x16x32_bf16 v[196:199], a[100:103], a[80:83], v[196:199]\n" \
"v_mfma_f32_16x16x32_bf16 v[200:203], a[96:99], a[84:87], v[200:203]\n" \
"ds_read_b64_tr_b16 a[54:55], v12 offset:1024\n" \
"v_mfma_f32_16x16x32_bf16 v[204:207], a[100:103], a[84:87], v[204:207]\n" \
"v_add_u32 v16, 0x10000, v15\n" \
"v_mfma_f32_16x16x32_bf16 v[208:211], a[104:107], a[80:83], v[208:211]\n" \
"ds_read_b128 a[24:27], v7 offset:12288\n" \
"v_mfma_f32_16x16x32_bf16 v[212:215], a[108:111], a[80:83], v[212:215]\n" \
"v_add_u32 v20, 0x10000, v19\n" \
"v_mfma_f32_16x16x32_bf16 v[216:219], a[104:107], a[84:87], v[216:219]\n" \
"ds_read_b64_tr_b16 a[56:57], v9 offset:1536\n" \
"v_mfma_f32_16x16x32_bf16 v[220:223], a[108:111], a[84:87], v[220:223]\n" \
"v_mfma_f32_16x16x32_bf16 v[224:227], a[112:115], a[80:83], v[224:227]\n" \
"ds_read_b64_tr_b16 a[58:59], v10 offset:1536\n" \
"v_mfma_f32_16x16x32_bf16 v[228:231], a[116:119], a[80:83], v[228:231]\n" \
"v_mfma_f32_16x16x32_bf16 v[232:235], a[112:115], a[84:87], v[232:235]\n" \
"ds_read_b128 a[28:31], v7 offset:14336\n" \
"v_mfma_f32_16x16x32_bf16 v[236:239], a[116:119], a[84:87], v[236:239]\n" \
"v_mfma_f32_16x16x32_bf16 v[240:243], a[120:123], a[80:83], v[240:243]\n" \
"ds_read_b64_tr_b16 a[60:61], v11 offset:1536\n" \
"v_mfma_f32_16x16x32_bf16 v[244:247], a[124:127], a[80:83], v[244:247]\n" \
"v_mfma_f32_16x16x32_bf16 v[248:251], a[120:123], a[84:87], v[248:251]\n" \
"ds_read_b64_tr_b16 a[62:63], v12 offset:1536\n" \
"v_mfma_f32_16x16x32_bf16 v[252:255], a[124:127], a[84:87], v[252:255]\n" \
"v_mfma_f32_16x16x32_bf16 a[128:131], a[96:99], a[88:91], a[128:131]\n" \
"v_mfma_f32_16x16x32_bf16 a[132:135], a[100:103], a[88:91], a[132:135]\n" \
"v_mfma_f32_16x16x32_bf16 a[136:139], a[96:99], a[92:95], a[136:139]\n" \
"v_mfma_f32_16x16x32_bf16 a[140:143], a[100:103], a[92:95], a[140:143]\n" \
"v_mfma_f32_16x16x32_bf16 a[144:147], a[104:107], a[88:91], a[144:147]\n" \
"v_mfma_f32_16x16x32_bf16 a[148:151], a[108:111], a[88:91], a[148:151]\n" \
"v_mfma_f32_16x16x32_bf16 a[152:155], a[104:107], a[92:95], a[152:155]\n" \
"v_mfma_f32_16x16x32_bf16 a[156:159], a[108:111], a[92:95], a[156:159]\n" \
"s_waitcnt vmcnt(24) lgkmcnt(0)\n" \
"v_mfma_f32_16x16x32_bf16 a[160:163], a[112:115], a[88:91], a[160:163]\n" \
"s_barrier\n" \
"v_mfma_f32_16x16x32_bf16 a[164:167], a[116:119], a[88:91], a[164:167]\n" \
"v_mfma_f32_16x16x32_bf16 a[168:171], a[112:115], a[92:95], a[168:171]\n" \
"v_mfma_f32_16x16x32_bf16 a[172:175], a[116:119], a[92:95], a[172:175]\n" \
"v_mfma_f32_16x16x32_bf16 a[176:179], a[120:123], a[88:91], a[176:179]\n" \
"v_mfma_f32_16x16x32_bf16 a[180:183], a[124:127], a[88:91], a[180:183]\n" \
"v_mfma_f32_16x16x32_bf16 a[184:187], a[120:123], a[92:95], a[184:187]\n" \
"v_mfma_f32_16x16x32_bf16 a[188:191], a[124:127], a[92:95], a[188:191]\n" \
"v_mfma_f32_16x16x32_bf16 v[64:67], a[32:35], a[0:3], v[64:67]\n" \
"ds_read_b128 a[64:67], v8 offset:0\n" \
"v_mfma_f32_16x16x32_bf16 v[68:71], a[36:39], a[0:3], v[68:71]\n" \
"s_mov_b64 s[32:33], s[44:45]\n" \
"v_mfma_f32_16x16x32_bf16 v[72:75], a[32:35], a[4:7], v[72:75]\n" \
"ds_read_b64_tr_b16 a[96:97], v9 offset:16384\n" \
"v_mfma_f32_16x16x32_bf16 v[76:79], a[36:39], a[4:7], v[76:79]\n" \
"buffer_load_dwordx2 v[58:59], v57, s[36:39], 0 offen sc1\n" \
"v_mfma_f32_16x16x32_bf16 v[80:83], a[40:43], a[0:3], v[80:83]\n" \
"ds_read_b64_tr_b16 a[98:99], v10 offset:16384\n" \
"v_mfma_f32_16x16x32_bf16 v[84:87], a[44:47], a[0:3], v[84:87]\n" \
"s_mov_b32 s34, s49\n" \
"v_mfma_f32_16x16x32_bf16 v[88:91], a[40:43], a[4:7], v[88:91]\n" \
"ds_read_b128 a[68:71], v8 offset:2048\n" \
"v_mfma_f32_16x16x32_bf16 v[92:95], a[44:47], a[4:7], v[92:95]\n" \
"v_mfma_f32_16x16x32_bf16 v[96:99], a[48:51], a[0:3], v[96:99]\n" \
"ds_read_b64_tr_b16 a[100:101], v11 offset:16384\n" \
"v_mfma_f32_16x16x32_bf16 v[100:103], a[52:55], a[0:3], v[100:103]\n" \
"s_mov_b64 exec, 0xffff\nglobal_load_dword a244, v1, s[76:77]\ns_mov_b64 exec, -1\n" \
"v_mfma_f32_16x16x32_bf16 v[104:107], a[48:51], a[4:7], v[104:107]\n" \
"ds_read_b64_tr_b16 a[102:103], v12 offset:16384\n" \
"v_mfma_f32_16x16x32_bf16 v[108:111], a[52:55], a[4:7], v[108:111]\n" \
"v_mfma_f32_16x16x32_bf16 v[112:115], a[56:59], a[0:3], v[112:115]\n" \
"ds_read_b128 a[72:75], v8 offset:4096\n" \
"v_mfma_f32_16x16x32_bf16 v[116:119], a[60:63], a[0:3], v[116:119]\n" \
"v_mfma_f32_16x16x32_bf16 v[120:123], a[56:59], a[4:7], v[120:123]\n" \
"ds_read_b64_tr_b16 a[104:105], v9 offset:16896\n" \
"v_mfma_f32_16x16x32_bf16 v[124:127], a[60:63], a[4:7], v[124:127]\n" \
"v_mfma_f32_16x16x32_bf16 v[128:131], a[32:35], a[8:11], v[128:131]\n" \
"ds_read_b64_tr_b16 a[106:107], v10 offset:16896\n" \
"v_mfma_f32_16x16x32_bf16 v[132:135], a[36:39], a[8:11], v[132:135]\n" \
"v_mfma_f32_16x16x32_bf16 v[136:139], a[32:35], a[12:15], v[136:139]\n" \
"ds_read_b128 a[76:79], v8 offset:6144\n" \
"v_mfma_f32_16x16x32_bf16 v[140:143], a[36:39], a[12:15], v[140:143]\n" \
"v_mfma_f32_16x16x32_bf16 v[144:147], a[40:43], a[8:11], v[144:147]\n" \
"ds_read_b64_tr_b16 a[108:109], v11 offset:16896\n" \
"v_mfma_f32_16x16x32_bf16 v[148:151], a[44:47], a[8:11], v[148:151]\n" \
"v_mfma_f32_16x16x32_bf16 v[152:155], a[40:43], a[12:15], v[152:155]\n" \
"ds_read_b64_tr_b16 a[110:111], v12 offset:16896\n" \
"v_mfma_f32_16x16x32_bf16 v[156:159], a[44:47], a[12:15], v[156:159]\n" \
"v_mfma_f32_16x16x32_bf16 v[160:163], a[48:51], a[8:11], v[160:163]\n" \
"ds_read_b128 a[80:83], v8 offset:8192\n" \
"v_mfma_f32_16x16x32_bf16 v[164:167], a[52:55], a[8:11], v[164:167]\n" \
"v_mfma_f32_16x16x32_bf16 v[168:171], a[48:51], a[12:15], v[168:171]\n" \
"ds_read_b64_tr_b16 a[112:113], v9 offset:17408\n" \
"v_mfma_f32_16x16x32_bf16 v[172:175], a[52:55], a[12:15], v[172:175]\n" \
"v_mfma_f32_16x16x32_bf16 v[176:179], a[56:59], a[8:11], v[176:179]\n" \
"ds_read_b64_tr_b16 a[114:115], v10 offset:17408\n" \
"v_mfma_f32_16x16x32_bf16 v[180:183], a[60:63], a[8:11], v[180:183]\n" \
"v_mfma_f32_16x16x32_bf16 v[184:187], a[56:59], a[12:15], v[184:187]\n" \
"ds_read_b128 a[84:87], v8 offset:10240\n" \
"v_mfma_f32_16x16x32_bf16 v[188:191], a[60:63], a[12:15], v[188:191]\n" \
"v_mfma_f32_16x16x32_bf16 v[192:195], a[32:35], a[16:19], v[192:195]\n" \
"ds_read_b64_tr_b16 a[116:117], v11 offset:17408\n" \
"v_mfma_f32_16x16x32_bf16 v[196:199], a[36:39], a[16:19], v[196:199]\n" \
"v_mfma_f32_16x16x32_bf16 v[200:203], a[32:35], a[20:23], v[200:203]\n" \
"ds_read_b64_tr_b16 a[118:119], v12 offset:17408\n" \
"v_mfma_f32_16x16x32_bf16 v[204:207], a[36:39], a[20:23], v[204:207]\n" \
"v_mfma_f32_16x16x32_bf16 v[208:211], a[40:43], a[16:19], v[208:211]\n" \
"ds_read_b128 a[88:91], v8 offset:12288\n" \
"v_mfma_f32_16x16x32_bf16 v[212:215], a[44:47], a[16:19], v[212:215]\n" \
"v_mfma_f32_16x16x32_bf16 v[216:219], a[40:43], a[20:23], v[216:219]\n" \
"ds_read_b64_tr_b16 a[120:121], v9 offset:17920\n" \
"v_mfma_f32_16x16x32_bf16 v[220:223], a[44:47], a[20:23], v[220:223]\n" \
"v_mfma_f32_16x16x32_bf16 v[224:227], a[48:51], a[16:19], v[224:227]\n" \
"ds_read_b64_tr_b16 a[122:123], v10 offset:17920\n" \
"v_mfma_f32_16x16x32_bf16 v[228:231], a[52:55], a[16:19], v[228:231]\n" \
"v_mfma_f32_16x16x32_bf16 v[232:235], a[48:51], a[20:23], v[232:235]\n" \
"ds_read_b128 a[92:95], v8 offset:14336\n" \
"v_mfma_f32_16x16x32_bf16 v[236:239], a[52:55], a[20:23], v[236:239]\n" \
"v_mfma_f32_16x16x32_bf16 v[240:243], a[56:59], a[16:19], v[240:243]\n" \
"ds_read_b64_tr_b16 a[124:125], v11 offset:17920\n" \
"v_mfma_f32_16x16x32_bf16 v[244:247], a[60:63], a[16:19], v[244:247]\n" \
"v_mfma_f32_16x16x32_bf16 v[248:251], a[56:59], a[20:23], v[248:251]\n" \
"ds_read_b64_tr_b16 a[126:127], v12 offset:17920\n" \
"v_mfma_f32_16x16x32_bf16 v[252:255], a[60:63], a[20:23], v[252:255]\n" \
"v_mfma_f32_16x16x32_bf16 a[128:131], a[32:35], a[24:27], a[128:131]\n" \
"v_mfma_f32_16x16x32_bf16 a[132:135], a[36:39], a[24:27], a[132:135]\n" \
"v_mfma_f32_16x16x32_bf16 a[136:139], a[32:35], a[28:31], a[136:139]\n" \
"v_mfma_f32_16x16x32_bf16 a[140:143], a[36:39], a[28:31], a[140:143]\n" \
"v_mfma_f32_16x16x32_bf16 a[144:147], a[40:43], a[24:27], a[144:147]\n" \
"v_mfma_f32_16x16x32_bf16 a[148:151], a[44:47], a[24:27], a[148:151]\n" \
"v_mfma_f32_16x16x32_bf16 a[152:155], a[40:43], a[28:31], a[152:155]\n" \
"v_mfma_f32_16x16x32_bf16 a[156:159], a[44:47], a[28:31], a[156:159]\n" \
"s_waitcnt vmcnt(1) lgkmcnt(0)\n" \
"v_mfma_f32_16x16x32_bf16 a[160:163], a[48:51], a[24:27], a[160:163]\n" \
"s_barrier\n" \
"v_mfma_f32_16x16x32_bf16 a[164:167], a[52:55], a[24:27], a[164:167]\n" \
"v_cmp_ne_u64 vcc, s[14:15], v[58:59]\n" \
"v_mfma_f32_16x16x32_bf16 a[168:171], a[48:51], a[28:31], a[168:171]\n" \
"v_mfma_f32_16x16x32_bf16 a[172:175], a[52:55], a[28:31], a[172:175]\n" \
"s_cbranch_vccnz LPUK_%=\nLPUKR_%=:\n" \
"v_mfma_f32_16x16x32_bf16 a[176:179], a[56:59], a[24:27], a[176:179]\n" \
"v_mfma_f32_16x16x32_bf16 a[180:183], a[60:63], a[24:27], a[180:183]\n" \
"v_mfma_f32_16x16x32_bf16 a[184:187], a[56:59], a[28:31], a[184:187]\n" \
"v_mfma_f32_16x16x32_bf16 a[188:191], a[60:63], a[28:31], a[188:191]\n" \
"v_mfma_f32_16x16x32_bf16 v[64:67], a[96:99], a[64:67], v[64:67]\n" \
"ds_read_b128 a[0:3], v5 offset:0\n" \
"v_mfma_f32_16x16x32_bf16 v[68:71], a[100:103], a[64:67], v[68:71]\n" \
"v_mfma_f32_16x16x32_bf16 v[72:75], a[96:99], a[68:71], v[72:75]\n" \
"s_add_u32 m0, s56, 10304\n" \
"ds_read_b64_tr_b16 a[32:33], v9 offset:32768\n" \
"v_mfma_f32_16x16x32_bf16 v[76:79], a[100:103], a[68:71], v[76:79]\n" \
"buffer_load_dwordx4 v26, s[72:75], 0 offen offset:0 sc1 lds\n" \
"v_mfma_f32_16x16x32_bf16 v[80:83], a[104:107], a[64:67], v[80:83]\n" \
"ds_read_b64_tr_b16 a[34:35], v10 offset:32768\n" \
"v_mfma_f32_16x16x32_bf16 v[84:87], a[108:111], a[64:67], v[84:87]\n" \
"v_mfma_f32_16x16x32_bf16 v[88:91], a[104:107], a[68:71], v[88:91]\n" \
"s_add_u32 m0, s56, 11328\n" \
"ds_read_b128 a[4:7], v5 offset:2048\n" \
"v_mfma_f32_16x16x32_bf16 v[92:95], a[108:111], a[68:71], v[92:95]\n" \
"buffer_load_dwordx4 v27, s[72:75], 0 offen offset:0 sc1 lds\n" \
"v_mfma_f32_16x16x32_bf16 v[96:99], a[112:115], a[64:67], v[96:99]\n" \
"ds_read_b64_tr_b16 a[36:37], v11 offset:32768\n" \
"v_mfma_f32_16x16x32_bf16 v[100:103], a[116:119], a[64:67], v[100:103]\n" \
"v_mfma_f32_16x16x32_bf16 v[104:107], a[112:115], a[68:71], v[104:107]\n" \
"s_add_u32 m0, s56, 11328\n" \
"ds_read_b64_tr_b16 a[38:39], v12 offset:32768\n" \
"v_mfma_f32_16x16x32_bf16 v[108:111], a[116:119], a[68:71], v[108:111]\n" \
"buffer_load_dwordx4 v27, s[72:75], 0 offen offset:1024 sc1 lds\n" \
"v_mfma_f32_16x16x32_bf16 v[112:115], a[120:123], a[64:67], v[112:115]\n" \
"ds_read_b128 a[8:11], v5 offset:4096\n" \
"v_mfma_f32_16x16x32_bf16 v[116:119], a[124:127], a[64:67], v[116:119]\n" \
"v_mfma_f32_16x16x32_bf16 v[120:123], a[120:123], a[68:71], v[120:123]\n" \
"s_add_u32 m0, s56, 11328\n" \
"ds_read_b64_tr_b16 a[40:41], v9 offset:33280\n" \
"v_mfma_f32_16x16x32_bf16 v[124:127], a[124:127], a[68:71], v[124:127]\n" \
"buffer_load_dwordx4 v27, s[72:75], 0 offen offset:2048 sc1 lds\n" \
"v_mfma_f32_16x16x32_bf16 v[128:131], a[96:99], a[72:75], v[128:131]\n" \
"ds_read_b64_tr_b16 a[42:43], v10 offset:33280\n" \
"v_mfma_f32_16x16x32_bf16 v[132:135], a[100:103], a[72:75], v[132:135]\n" \
"v_mfma_f32_16x16x32_bf16 v[136:139], a[96:99], a[76:79], v[136:139]\n" \
"s_add_u32 m0, s56, 11328\n" \
"ds_read_b128 a[12:15], v5 offset:6144\n" \
"v_mfma_f32_16x16x32_bf16 v[140:143], a[100:103], a[76:79], v[140:143]\n" \
"buffer_load_dwordx4 v27, s[72:75], 0 offen offset:3072 sc1 lds\n" \
"v_mfma_f32_16x16x32_bf16 v[144:147], a[104:107], a[72:75], v[144:147]\n" \
"ds_read_b64_tr_b16 a[44:45], v11 offset:33280\n" \
"s_waitcnt vmcnt(19)\n" \
"v_alignbyte_b32 v30, v31, v30, 2\n" \
"v_mfma_f32_16x16x32_bf16 v[148:151], a[108:111], a[72:75], v[148:151]\n" \
"v_alignbyte_b32 v31, v32, v31, 2\n" \
"v_alignbyte_b32 v32, v33, v32, 2\n" \
"v_mfma_f32_16x16x32_bf16 v[152:155], a[104:107], a[76:79], v[152:155]\n" \
"s_add_u32 m0, s56, 0\n" \
"ds_read_b64_tr_b16 a[46:47], v12 offset:33280\n" \
"v_alignbyte_b32 v33, v50, v33, 2\n" \
"v_mfma_f32_16x16x32_bf16 v[156:159], a[108:111], a[76:79], v[156:159]\n" \
"buffer_load_dwordx4 v23, s[68:71], 0 offen offset:0 lds\n" \
"v_mfma_f32_16x16x32_bf16 v[160:163], a[112:115], a[72:75], v[160:163]\n" \
"ds_read_b128 a[16:19], v5 offset:8192\n" \
"v_mfma_f32_16x16x32_bf16 v[164:167], a[116:119], a[72:75], v[164:167]\n" \
"ds_write_b128 v55, v[30:33] offset:5184\n" \
"v_alignbyte_b32 v34, v35, v34, 2\n" \
"v_mfma_f32_16x16x32_bf16 v[168:171], a[112:115], a[76:79], v[168:171]\n" \
"s_add_u32 m0, s56, 1024\n" \
"ds_read_b64_tr_b16 a[48:49], v9 offset:33792\n" \
"v_alignbyte_b32 v35, v36, v35, 2\n" \
"v_mfma_f32_16x16x32_bf16 v[172:175], a[116:119], a[76:79], v[172:175]\n" \
"buffer_load_dwordx4 v24, s[68:71], 0 offen offset:0 lds\n" \
"v_mfma_f32_16x16x32_bf16 v[176:179], a[120:123], a[72:75], v[176:179]\n" \
"ds_read_b64_tr_b16 a[50:51], v10 offset:33792\n" \
"v_alignbyte_b32 v36, v37, v36, 2\n" \
"v_mfma_f32_16x16x32_bf16 v[180:183], a[124:127], a[72:75], v[180:183]\n" \
"v_alignbyte_b32 v37, v51, v37, 2\n" \
"ds_write_b128 v55, v[34:37] offset:6208\n" \
"v_mfma_f32_16x16x32_bf16 v[184:187], a[120:123], a[76:79], v[184:187]\n" \
"s_add_u32 m0, s56, 1024\n" \
"ds_read_b128 a[20:23], v5 offset:10240\n" \
"v_alignbyte_b32 v38, v39, v38, 2\n" \
"v_mfma_f32_16x16x32_bf16 v[188:191], a[124:127], a[76:79], v[188:191]\n" \
"buffer_load_dwordx4 v24, s[68:71], 0 offen offset:1024 lds\n" \
"v_mfma_f32_16x16x32_bf16 v[192:195], a[96:99], a[80:83], v[192:195]\n" \
"ds_read_b64_tr_b16 a[52:53], v11 offset:33792\n" \
"v_alignbyte_b32 v39, v40, v39, 2\n" \
"v_mfma_f32_16x16x32_bf16 v[196:199], a[100:103], a[80:83], v[196:199]\n" \
"v_alignbyte_b32 v40, v41, v40, 2\n" \
"v_alignbyte_b32 v41, v52, v41, 2\n" \
"ds_write_b128 v55, v[38:41] offset:7232\n" \
"v_mfma_f32_16x16x32_bf16 v[200:203], a[96:99], a[84:87], v[200:203]\n" \
"s_add_u32 m0, s56, 1024\n" \
"ds_read_b64_tr_b16 a[54:55], v12 offset:33792\n" \
"v_alignbyte_b32 v42, v43, v42, 2\n" \
"v_mfma_f32_16x16x32_bf16 v[204:207], a[100:103], a[84:87], v[204:207]\n" \
"buffer_load_dwordx4 v24, s[68:71], 0 offen offset:2048 lds\n" \
"v_mfma_f32_16x16x32_bf16 v[208:211], a[104:107], a[80:83], v[208:211]\n" \
"ds_read_b128 a[24:27], v5 offset:12288\n" \
"v_alignbyte_b32 v43, v44, v43, 2\n" \
"v_mfma_f32_16x16x32_bf16 v[212:215], a[108:111], a[80:83], v[212:215]\n" \
"v_alignbyte_b32 v44, v45, v44, 2\n" \
"v_alignbyte_b32 v45, v53, v45, 2\n" \
"ds_write_b128 v55, v[42:45] offset:8256\n" \
"v_mfma_f32_16x16x32_bf16 v[216:219], a[104:107], a[84:87], v[216:219]\n" \
"s_add_u32 m0, s56, 1024\n" \
"ds_read_b64_tr_b16 a[56:57], v9 offset:34304\n" \
"v_alignbyte_b32 v46, v47, v46, 2\n" \
"v_mfma_f32_16x16x32_bf16 v[220:223], a[108:111], a[84:87], v[220:223]\n" \
"buffer_load_dwordx4 v24, s[68:71], 0 offen offset:3072 lds\n" \
"v_mfma_f32_16x16x32_bf16 v[224:227], a[112:115], a[80:83], v[224:227]\n" \
"ds_read_b64_tr_b16 a[58:59], v10 offset:34304\n" \
"v_alignbyte_b32 v47, v48, v47, 2\n" \
"v_mfma_f32_16x16x32_bf16 v[228:231], a[116:119], a[80:83], v[228:231]\n" \
"v_alignbyte_b32 v48, v49, v48, 2\n" \
"v_alignbyte_b32 v49, v54, v49, 2\n" \
"ds_write_b128 v55, v[46:49] offset:9280\n" \
"v_mfma_f32_16x16x32_bf16 v[232:235], a[112:115], a[84:87], v[232:235]\n" \
"s_mov_b32 s46, 0\n" \
"s_add_u32 m0, s50, 65536\n" \
"ds_read_b128 a[28:31], v5 offset:14336\n" \
"v_mfma_f32_16x16x32_bf16 v[236:239], a[116:119], a[84:87], v[236:239]\n" \
"buffer_load_dwordx4 v13, s[28:31], s46 offen lds\n" \
"v_mfma_f32_16x16x32_bf16 v[240:243], a[120:123], a[80:83], v[240:243]\n" \
"ds_read_b64_tr_b16 a[60:61], v11 offset:34304\n" \
"v_lshrrev_b32 v30, 4, v4\n" \
"v_mfma_f32_16x16x32_bf16 v[244:247], a[124:127], a[80:83], v[244:247]\n" \
"v_and_b32 v31, 15, v30\n" \
"v_lshrrev_b32 v32, 4, v30\n" \
"v_mfma_f32_16x16x32_bf16 v[248:251], a[120:123], a[84:87], v[248:251]\n" \
"s_add_u32 m0, s50, 69632\n" \
"ds_read_b64_tr_b16 a[62:63], v12 offset:34304\n" \
"v_add_u32 v33, 1, v31\n" \
"v_mfma_f32_16x16x32_bf16 v[252:255], a[124:127], a[84:87], v[252:255]\n" \
"buffer_load_dwordx4 v14, s[28:31], s46 offen lds\n" \
"v_mfma_f32_16x16x32_bf16 a[128:131], a[96:99], a[88:91], a[128:131]\n" \
"v_and_b32 v34, 1, v33\n" \
"v_and_b32 v33, 30, v33\n" \
"v_mfma_f32_16x16x32_bf16 a[132:135], a[100:103], a[88:91], a[132:135]\n" \
"v_mul_u32_u24 v34, 5184, v34\n" \
"v_lshl_add_u32 v33, v33, 1, v34\n" \
"v_mfma_f32_16x16x32_bf16 a[136:139], a[96:99], a[92:95], a[136:139]\n" \
"s_add_u32 m0, s50, 73728\n" \
"v_lshl_add_u32 v33, v32, 4, v33\n" \
"v_mfma_f32_16x16x32_bf16 a[140:143], a[100:103], a[92:95], a[140:143]\n" \
"buffer_load_dwordx4 v15, s[28:31], s46 offen lds\n" \
"v_mfma_f32_16x16x32_bf16 a[144:147], a[104:107], a[88:91], a[144:147]\n" \
"v_add_u32 v46, s56, v33\n" \
"v_add_u32 v47, 1024, v46\n" \
"v_mfma_f32_16x16x32_bf16 a[148:151], a[108:111], a[88:91], a[148:151]\n" \
"v_add_u32 v48, 2048, v46\n" \
"v_add_u32 v49, 3072, v46\n" \
"v_mfma_f32_16x16x32_bf16 a[152:155], a[104:107], a[92:95], a[152:155]\n" \
"v_lshlrev_b32 v33, 1, v31\n" \
"v_sub_u32 v33, v33, v32\n" \
"v_mfma_f32_16x16x32_bf16 a[156:159], a[108:111], a[92:95], a[156:159]\n" \
"s_waitcnt vmcnt(15) lgkmcnt(0)\n" \
"v_lshlrev_b32 v33, 4, v33\n" \
"v_mfma_f32_16x16x32_bf16 a[160:163], a[112:115], a[88:91], a[160:163]\n" \
"s_barrier\n" \
"v_mfma_f32_16x16x32_bf16 a[164:167], a[116:119], a[88:91], a[164:167]\n" \
"v_add_u32 v33, 10320, v33\n" \
"v_add_u32 v50, s56, v33\n" \
"v_mfma_f32_16x16x32_bf16 a[168:171], a[112:115], a[92:95], a[168:171]\n" \
"s_add_u32 m0, s50, 77824\n" \
"v_lshlrev_b32 v33, 5, v31\n" \
"v_mfma_f32_16x16x32_bf16 a[172:175], a[116:119], a[92:95], a[172:175]\n" \
"buffer_load_dwordx4 v16, s[28:31], s46 offen lds\n" \
"v_mfma_f32_16x16x32_bf16 a[176:179], a[120:123], a[88:91], a[176:179]\n" \
"v_add_u32 v33, s56, v33\n" \
"v_lshl_add_u32 v52, v32, 3, v33\n" \
"v_mfma_f32_16x16x32_bf16 a[180:183], a[124:127], a[88:91], a[180:183]\n" \
"v_xor_b32 v32, 1, v32\n" \
"v_lshl_add_u32 v51, v32, 3, v33\n" \
"v_mfma_f32_16x16x32_bf16 a[184:187], a[120:123], a[92:95], a[184:187]\n" \
"v_mfma_f32_16x16x32_bf16 a[188:191], a[124:127], a[92:95], a[188:191]\n" \
"v_mfma_f32_16x16x32_bf16 v[64:67], a[32:35], a[0:3], v[64:67]\n" \
"ds_read_b128 a[64:67], v6 offset:0\n" \
"v_mfma_f32_16x16x32_bf16 v[68:71], a[36:39], a[0:3], v[68:71]\n" \
"v_mfma_f32_16x16x32_bf16 v[72:75], a[32:35], a[4:7], v[72:75]\n" \
"s_mov_b32 s46, 0\n" \
"s_add_u32 m0, s50, 81920\n" \
"ds_read_b64_tr_b16 a[96:97], v9 offset:49152\n" \
"v_mfma_f32_16x16x32_bf16 v[76:79], a[36:39], a[4:7], v[76:79]\n" \
"buffer_load_dwordx4 v17, s[28:31], s46 offen lds\n" \
"v_mfma_f32_16x16x32_bf16 v[80:83], a[40:43], a[0:3], v[80:83]\n" \
"ds_read_b64_tr_b16 a[98:99], v10 offset:49152\n" \
"v_mfma_f32_16x16x32_bf16 v[84:87], a[44:47], a[0:3], v[84:87]\n" \
"v_mfma_f32_16x16x32_bf16 v[88:91], a[40:43], a[4:7], v[88:91]\n" \
"ds_read_b128 a[68:71], v6 offset:2048\n" \
"v_mfma_f32_16x16x32_bf16 v[92:95], a[44:47], a[4:7], v[92:95]\n" \
"v_mfma_f32_16x16x32_bf16 v[96:99], a[48:51], a[0:3], v[96:99]\n" \
"s_add_u32 m0, s50, 86016\n" \
"ds_read_b64_tr_b16 a[100:101], v11 offset:49152\n" \
"v_mfma_f32_16x16x32_bf16 v[100:103], a[52:55], a[0:3], v[100:103]\n" \
"buffer_load_dwordx4 v18, s[28:31], s46 offen lds\n" \
"v_mfma_f32_16x16x32_bf16 v[104:107], a[48:51], a[4:7], v[104:107]\n" \
"ds_read_b64_tr_b16 a[102:103], v12 offset:49152\n" \
"v_mfma_f32_16x16x32_bf16 v[108:111], a[52:55], a[4:7], v[108:111]\n" \
"v_mfma_f32_16x16x32_bf16 v[112:115], a[56:59], a[0:3], v[112:115]\n" \
"ds_read_b128 a[72:75], v6 offset:4096\n" \
"v_mfma_f32_16x16x32_bf16 v[116:119], a[60:63], a[0:3], v[116:119]\n" \
"v_mfma_f32_16x16x32_bf16 v[120:123], a[56:59], a[4:7], v[120:123]\n" \
"s_add_u32 m0, s50, 90112\n" \
"ds_read_b64_tr_b16 a[104:105], v9 offset:49664\n" \
"v_mfma_f32_16x16x32_bf16 v[124:127], a[60:63], a[4:7], v[124:127]\n" \
"buffer_load_dwordx4 v19, s[28:31], s46 offen lds\n" \
"v_mfma_f32_16x16x32_bf16 v[128:131], a[32:35], a[8:11], v[128:131]\n" \
"ds_read_b64_tr_b16 a[106:107], v10 offset:49664\n" \
"v_mfma_f32_16x16x32_bf16 v[132:135], a[36:39], a[8:11], v[132:135]\n" \
"v_mfma_f32_16x16x32_bf16 v[136:139], a[32:35], a[12:15], v[136:139]\n" \
"ds_read_b128 a[76:79], v6 offset:6144\n" \
"v_mfma_f32_16x16x32_bf16 v[140:143], a[36:39], a[12:15], v[140:143]\n" \
"v_mfma_f32_16x16x32_bf16 v[144:147], a[40:43], a[8:11], v[144:147]\n" \
"s_add_u32 m0, s50, 94208\n" \
"ds_read_b64_tr_b16 a[108:109], v11 offset:49664\n" \
"v_mfma_f32_16x16x32_bf16 v[148:151], a[44:47], a[8:11], v[148:151]\n" \
"buffer_load_dwordx4 v20, s[28:31], s46 offen lds\n" \
"v_mfma_f32_16x16x32_bf16 v[152:155], a[40:43], a[12:15], v[152:155]\n" \
"ds_read_b64_tr_b16 a[110:111], v12 offset:49664\n" \
"v_mfma_f32_16x16x32_bf16 v[156:159], a[44:47], a[12:15], v[156:159]\n" \
"v_mfma_f32_16x16x32_bf16 v[160:163], a[48:51], a[8:11], v[160:163]\n" \
"ds_read_b128 a[80:83], v6 offset:8192\n" \
"v_mfma_f32_16x16x32_bf16 v[164:167], a[52:55], a[8:11], v[164:167]\n" \
"v_mfma_f32_16x16x32_bf16 v[168:171], a[48:51], a[12:15], v[168:171]\n" \
"s_mov_b32 s47, 0\n" \
"s_add_u32 m0, s51, 32768\n" \
"ds_read_b64_tr_b16 a[112:113], v9 offset:50176\n" \
"v_mfma_f32_16x16x32_bf16 v[172:175], a[52:55], a[12:15], v[172:175]\n" \
"buffer_load_dwordx4 v21, s[32:35], s47 offen offset:0 lds\n" \
"v_mfma_f32_16x16x32_bf16 v[176:179], a[56:59], a[8:11], v[176:179]\n" \
"ds_read_b64_tr_b16 a[114:115], v10 offset:50176\n" \
"v_mfma_f32_16x16x32_bf16 v[180:183], a[60:63], a[8:11], v[180:183]\n" \
"v_mfma_f32_16x16x32_bf16 v[184:187], a[56:59], a[12:15], v[184:187]\n" \
"ds_read_b128 a[84:87], v6 offset:10240\n" \
"v_mfma_f32_16x16x32_bf16 v[188:191], a[60:63], a[12:15], v[188:191]\n" \
"v_mfma_f32_16x16x32_bf16 v[192:195], a[32:35], a[16:19], v[192:195]\n" \
"s_add_u32 m0, s51, 33664\n" \
"ds_read_b64_tr_b16 a[116:117], v11 offset:50176\n" \
"v_mfma_f32_16x16x32_bf16 v[196:199], a[36:39], a[16:19], v[196:199]\n" \
"buffer_load_dwordx4 v21, s[32:35], s47 offen offset:128 lds\n" \
"v_mfma_f32_16x16x32_bf16 v[200:203], a[32:35], a[20:23], v[200:203]\n" \
"ds_read_b64_tr_b16 a[118:119], v12 offset:50176\n" \
"v_mfma_f32_16x16x32_bf16 v[204:207], a[36:39], a[20:23], v[204:207]\n" \
"v_mfma_f32_16x16x32_bf16 v[208:211], a[40:43], a[16:19], v[208:211]\n" \
"ds_read_b128 a[88:91], v6 offset:12288\n" \
"v_mfma_f32_16x16x32_bf16 v[212:215], a[44:47], a[16:19], v[212:215]\n" \
"v_mfma_f32_16x16x32_bf16 v[216:219], a[40:43], a[20:23], v[216:219]\n" \
"s_mov_b32 s47, 262144\n" \
"s_add_u32 m0, s51, 40960\n" \
"ds_read_b64_tr_b16 a[120:121], v9 offset:50688\n" \
"v_mfma_f32_16x16x32_bf16 v[220:223], a[44:47], a[20:23], v[220:223]\n" \
"buffer_load_dwordx4 v21, s[32:35], s47 offen offset:0 lds\n" \
"v_mfma_f32_16x16x32_bf16 v[224:227], a[48:51], a[16:19], v[224:227]\n" \
"ds_read_b64_tr_b16 a[122:123], v10 offset:50688\n" \
"v_mfma_f32_16x16x32_bf16 v[228:231], a[52:55], a[16:19], v[228:231]\n" \
"LH3_%=:\ns_cbranch_execz LH11_%=\n" \
"v_mfma_f32_16x16x32_bf16 v[232:235], a[48:51], a[20:23], v[232:235]\n" \
"ds_read_b128 a[92:95], v6 offset:14336\n" \
"v_mfma_f32_16x16x32_bf16 v[236:239], a[52:55], a[20:23], v[236:239]\n" \
"v_mfma_f32_16x16x32_bf16 v[240:243], a[56:59], a[16:19], v[240:243]\n" \
"s_add_u32 m0, s51, 41856\n" \
"ds_read_b64_tr_b16 a[124:125], v11 offset:50688\n" \
"v_mfma_f32_16x16x32_bf16 v[244:247], a[60:63], a[16:19], v[244:247]\n" \
"buffer_load_dwordx4 v21, s[32:35], s47 offen offset:128 lds\n" \
"v_mfma_f32_16x16x32_bf16 v[248:251], a[56:59], a[20:23], v[248:251]\n" \
"ds_read_b64_tr_b16 a[126:127], v12 offset:50688\n" \
"v_mfma_f32_16x16x32_bf16 v[252:255], a[60:63], a[20:23], v[252:255]\n" \
"v_add_u32 v5, s54, v5\n" \
"v_mfma_f32_16x16x32_bf16 a[128:131], a[32:35], a[24:27], a[128:131]\n" \
"s_add_u32 s76, s76, 2097152\ns_addc_u32 s77, s77, 0\n" \
"v_add_u32 v6, s54, v6\n" \
"v_mfma_f32_16x16x32_bf16 a[132:135], a[36:39], a[24:27], a[132:135]\n" \
"s_mov_b64 exec, 0xffff\nglobal_load_dword a244, v1, s[76:77]\ns_mov_b64 exec, -1\n" \
"v_mfma_f32_16x16x32_bf16 a[136:139], a[32:35], a[28:31], a[136:139]\n" \
"v_add_u32 v7, s54, v7\n" \
"v_mfma_f32_16x16x32_bf16 a[140:143], a[36:39], a[28:31], a[140:143]\n" \
"v_add_u32 v8, s54, v8\n" \
"v_mfma_f32_16x16x32_bf16 a[144:147], a[40:43], a[24:27], a[144:147]\n" \
"v_sub_u32 v9, v9, v62\n" \
"v_mfma_f32_16x16x32_bf16 a[148:151], a[44:47], a[24:27], a[148:151]\n" \
"v_mfma_f32_16x16x32_bf16 a[152:155], a[40:43], a[28:31], a[152:155]\n" \
"v_sub_u32 v10, v10, v62\n" \
"v_mfma_f32_16x16x32_bf16 a[156:159], a[44:47], a[28:31], a[156:159]\n" \
"s_waitcnt lgkmcnt(0)\n" \
"v_mfma_f32_16x16x32_bf16 a[160:163], a[48:51], a[24:27], a[160:163]\n" \
"s_barrier\n" \
"v_mfma_f32_16x16x32_bf16 a[164:167], a[52:55], a[24:27], a[164:167]\n" \
"v_sub_u32 v11, v11, v62\n" \
"v_mfma_f32_16x16x32_bf16 a[168:171], a[48:51], a[28:31], a[168:171]\n" \
"v_sub_u32 v12, v12, v62\n" \
"v_mfma_f32_16x16x32_bf16 a[172:175], a[52:55], a[28:31], a[172:175]\n" \
"v_mfma_f32_16x16x32_bf16 a[176:179], a[56:59], a[24:27], a[176:179]\n" \
"v_mfma_f32_16x16x32_bf16 a[180:183], a[60:63], a[24:27], a[180:183]\n" \
"v_mfma_f32_16x16x32_bf16 a[184:187], a[56:59], a[28:31], a[184:187]\n" \
"v_mfma_f32_16x16x32_bf16 a[188:191], a[60:63], a[28:31], a[188:191]\n" \
"v_mfma_f32_16x16x32_bf16 v[64:67], a[96:99], a[64:67], v[64:67]\n" \
"s_waitcnt vmcnt(13) lgkmcnt(0)\n" \
"v_mfma_f32_16x16x32_bf16 v[68:71], a[100:103], a[64:67], v[68:71]\n" \
"v_mfma_f32_16x16x32_bf16 v[72:75], a[96:99], a[68:71], v[72:75]\n" \
"s_mov_b32 s46, 128\n" \
"s_add_u32 m0, s50, 0\n" \
"ds_read_b128 a[32:35], v50 offset:512\n" \
"v_mfma_f32_16x16x32_bf16 v[76:79], a[100:103], a[68:71], v[76:79]\n" \
"buffer_load_dwordx4 v13, s[28:31], s46 offen lds\n" \
"s_mov_b32 s96, s57\n" \
"v_mfma_f32_16x16x32_bf16 v[80:83], a[104:107], a[64:67], v[80:83]\n" \
"ds_read2_b32 a[0:1], v46 offset0:0 offset1:1\n" \
"v_mul_f32 v64, s20, v64\n" \
"v_mfma_f32_16x16x32_bf16 v[84:87], a[108:111], a[64:67], v[84:87]\n" \
"v_mul_f32 v65, s20, v65\n" \
"v_mul_f32 v66, s20, v66\n" \
"v_mfma_f32_16x16x32_bf16 v[88:91], a[104:107], a[68:71], v[88:91]\n" \
"s_add_u32 m0, s50, 4096\n" \
"ds_read2_b32 a[2:3], v46 offset0:2 offset1:3\n" \
"v_mul_f32 v67, s20, v67\n" \
"v_mfma_f32_16x16x32_bf16 v[92:95], a[108:111], a[68:71], v[92:95]\n" \
"buffer_load_dwordx4 v14, s[28:31], s46 offen lds\n" \
"v_mfma_f32_16x16x32_bf16 v[96:99], a[112:115], a[64:67], v[96:99]\n" \
"ds_read_b128 a[36:39], v50 offset:1024\n" \
"v_cvt_pk_bf16_f32 v30, v64, v65\n" \
"v_mfma_f32_16x16x32_bf16 v[100:103], a[116:119], a[64:67], v[100:103]\n" \
"v_cvt_pk_bf16_f32 v31, v66, v67\n" \
"v_mul_f32 v68, s20, v68\n" \
"v_mfma_f32_16x16x32_bf16 v[104:107], a[112:115], a[68:71], v[104:107]\n" \
"s_add_u32 m0, s50, 8192\n" \
"ds_read2_b32 a[4:5], v46 offset0:128 offset1:129\n" \
"v_mul_f32 v69, s20, v69\n" \
"v_mfma_f32_16x16x32_bf16 v[108:111], a[116:119], a[68:71], v[108:111]\n" \
"buffer_load_dwordx4 v15, s[28:31], s46 offen lds\n" \
"v_mfma_f32_16x16x32_bf16 v[112:115], a[120:123], a[64:67], v[112:115]\n" \
"ds_read2_b32 a[6:7], v46 offset0:130 offset1:131\n" \
"v_mul_f32 v70, s20, v70\n" \
"v_mfma_f32_16x16x32_bf16 v[116:119], a[124:127], a[64:67], v[116:119]\n" \
"v_mul_f32 v71, s20, v71\n" \
"v_cvt_pk_bf16_f32 v32, v68, v69\n" \
"v_mfma_f32_16x16x32_bf16 v[120:123], a[120:123], a[68:71], v[120:123]\n" \
"s_add_u32 m0, s50, 12288\n" \
"ds_read_b128 a[40:43], v50 offset:1536\n" \
"v_cvt_pk_bf16_f32 v33, v70, v71\n" \
"v_mfma_f32_16x16x32_bf16 v[124:127], a[124:127], a[68:71], v[124:127]\n" \
"buffer_load_dwordx4 v16, s[28:31], s46 offen lds\n" \
"v_mfma_f32_16x16x32_bf16 v[128:131], a[96:99], a[72:75], v[128:131]\n" \
"ds_read2_b32 a[8:9], v47 offset0:0 offset1:1\n" \
"v_mul_f32 v72, s20, v72\n" \
"v_mfma_f32_16x16x32_bf16 v[132:135], a[100:103], a[72:75], v[132:135]\n" \
"v_mul_f32 v73, s20, v73\n" \
"v_mul_f32 v74, s20, v74\n" \
"v_mfma_f32_16x16x32_bf16 v[136:139], a[96:99], a[76:79], v[136:139]\n" \
"s_mov_b32 s46, 128\n" \
"s_add_u32 m0, s50, 16384\n" \
"ds_read2_b32 a[10:11], v47 offset0:2 offset1:3\n" \
"v_mfma_f32_16x16x32_bf16 v[140:143], a[100:103], a[76:79], v[140:143]\n" \
"buffer_load_dwordx4 v17, s[28:31], s46 offen lds\n" \
"v_mfma_f32_16x16x32_bf16 v[144:147], a[104:107], a[72:75], v[144:147]\n" \
"ds_read_b128 a[44:47], v50 offset:2048\n" \
"v_mul_f32 v75, s20, v75\n" \
"v_mfma_f32_16x16x32_bf16 v[148:151], a[108:111], a[72:75], v[148:151]\n" \
"v_cvt_pk_bf16_f32 v34, v72, v73\n" \
"v_cvt_pk_bf16_f32 v35, v74, v75\n" \
"v_mfma_f32_16x16x32_bf16 v[152:155], a[104:107], a[76:79], v[152:155]\n" \
"s_add_u32 m0, s50, 20480\n" \
"ds_read2_b32 a[12:13], v47 offset0:128 offset1:129\n" \
"v_mul_f32 v76, s20, v76\n" \
"v_mfma_f32_16x16x32_bf16 v[156:159], a[108:111], a[76:79], v[156:159]\n" \
"buffer_load_dwordx4 v18, s[28:31], s46 offen lds\n" \
"v_mfma_f32_16x16x32_bf16 v[160:163], a[112:115], a[72:75], v[160:163]\n" \
"ds_read2_b32 a[14:15], v47 offset0:130 offset1:131\n" \
"v_mul_f32 v77, s20, v77\n" \
"v_mfma_f32_16x16x32_bf16 v[164:167], a[116:119], a[72:75], v[164:167]\n" \
"v_mul_f32 v78, s20, v78\n" \
"v_mul_f32 v79, s20, v79\n" \
"v_mfma_f32_16x16x32_bf16 v[168:171], a[112:115], a[76:79], v[168:171]\n" \
"s_add_u32 m0, s50, 24576\n" \
"ds_read_b128 a[48:51], v50 offset:2560\n" \
"v_cvt_pk_bf16_f32 v36, v76, v77\n" \
"v_mfma_f32_16x16x32_bf16 v[172:175], a[116:119], a[76:79], v[172:175]\n" \
"buffer_load_dwordx4 v19, s[28:31], s46 offen lds\n" \
"v_mfma_f32_16x16x32_bf16 v[176:179], a[120:123], a[72:75], v[176:179]\n" \
"ds_read2_b32 a[16:17], v48 offset0:0 offset1:1\n" \
"v_cvt_pk_bf16_f32 v37, v78, v79\n" \
"v_mfma_f32_16x16x32_bf16 v[180:183], a[124:127], a[72:75], v[180:183]\n" \
"v_mul_f32 v80, s20, v80\n" \
"v_mul_f32 v81, s20, v81\n" \
"v_mfma_f32_16x16x32_bf16 v[184:187], a[120:123], a[76:79], v[184:187]\n" \
"s_add_u32 m0, s50, 28672\n" \
"ds_read2_b32 a[18:19], v48 offset0:2 offset1:3\n" \
"v_mul_f32 v82, s20, v82\n" \
"v_mfma_f32_16x16x32_bf16 v[188:191], a[124:127], a[76:79], v[188:191]\n" \
"buffer_load_dwordx4 v20, s[28:31], s46 offen lds\n" \
"v_mfma_f32_16x16x32_bf16 v[192:195], a[96:99], a[80:83], v[192:195]\n" \
"ds_read_b128 a[52:55], v50 offset:3072\n" \
"v_mul_f32 v83, s20, v83\n" \
"v_mfma_f32_16x16x32_bf16 v[196:199], a[100:103], a[80:83], v[196:199]\n" \
"v_cvt_pk_bf16_f32 v38, v80, v81\n" \
"v_cvt_pk_bf16_f32 v39, v82, v83\n" \
"v_mfma_f32_16x16x32_bf16 v[200:203], a[96:99], a[84:87], v[200:203]\n" \
"s_mov_b32 s47, 524288\n" \
"s_add_u32 m0, s51, 49152\n" \
"ds_read2_b32 a[20:21], v48 offset0:128 offset1:129\n" \
"v_mfma_f32_16x16x32_bf16 v[204:207], a[100:103], a[84:87], v[204:207]\n" \
"buffer_load_dwordx4 v21, s[32:35], s47 offen offset:0 lds\n" \
"v_mfma_f32_16x16x32_bf16 v[208:211], a[104:107], a[80:83], v[208:211]\n" \
"ds_read2_b32 a[22:23], v48 offset0:130 offset1:131\n" \
"v_mul_f32 v84, s20, v84\n" \
"v_mfma_f32_16x16x32_bf16 v[212:215], a[108:111], a[80:83], v[212:215]\n" \
"v_mul_f32 v85, s20, v85\n" \
"v_mul_f32 v86, s20, v86\n" \
"v_mfma_f32_16x16x32_bf16 v[216:219], a[104:107], a[84:87], v[216:219]\n" \
"s_add_u32 m0, s51, 50048\n" \
"ds_read_b128 a[56:59], v50 offset:3584\n" \
"v_mul_f32 v87, s20, v87\n" \
"v_mfma_f32_16x16x32_bf16 v[220:223], a[108:111], a[84:87], v[220:223]\n" \
"buffer_load_dwordx4 v21, s[32:35], s47 offen offset:128 lds\n" \
"v_mfma_f32_16x16x32_bf16 v[224:227], a[112:115], a[80:83], v[224:227]\n" \
"ds_read2_b32 a[24:25], v49 offset0:0 offset1:1\n" \
"v_cvt_pk_bf16_f32 v40, v84, v85\n" \
"v_mfma_f32_16x16x32_bf16 v[228:231], a[116:119], a[80:83], v[228:231]\n" \
"v_cvt_pk_bf16_f32 v41, v86, v87\n" \
"v_mul_f32 v88, s20, v88\n" \
"v_mfma_f32_16x16x32_bf16 v[232:235], a[112:115], a[84:87], v[232:235]\n" \
"s_mov_b32 s47, 786432\n" \
"s_add_u32 m0, s51, 57344\n" \
"ds_read2_b32 a[26:27], v49 offset0:2 offset1:3\n" \
"v_mfma_f32_16x16x32_bf16 v[236:239], a[116:119], a[84:87], v[236:239]\n" \
"buffer_load_dwordx4 v21, s[32:35], s47 offen offset:0 lds\n" \
"v_mfma_f32_16x16x32_bf16 v[240:243], a[120:123], a[80:83], v[240:243]\n" \
"ds_read_b128 a[60:63], v50 offset:4096\n" \
"v_mul_f32 v89, s20, v89\n" \
"v_mfma_f32_16x16x32_bf16 v[244:247], a[124:127], a[80:83], v[244:247]\n" \
"v_mul_f32 v90, s20, v90\n" \
"v_mul_f32 v91, s20, v91\n" \
"v_mfma_f32_16x16x32_bf16 v[248:251], a[120:123], a[84:87], v[248:251]\n" \
"s_add_u32 m0, s51, 58240\n" \
"ds_read2_b32 a[28:29], v49 offset0:128 offset1:129\n" \
"v_cvt_pk_bf16_f32 v42, v88, v89\n" \
"v_mfma_f32_16x16x32_bf16 v[252:255], a[124:127], a[84:87], v[252:255]\n" \
"buffer_load_dwordx4 v21, s[32:35], s47 offen offset:128 lds\n" \
"v_mfma_f32_16x16x32_bf16 a[128:131], a[96:99], a[88:91], a[128:131]\n" \
"ds_read2_b32 a[30:31], v49 offset0:130 offset1:131\n" \
"v_cvt_pk_bf16_f32 v43, v90, v91\n" \
"v_mfma_f32_16x16x32_bf16 a[132:135], a[100:103], a[88:91], a[132:135]\n" \
"v_mul_f32 v92, s20, v92\n" \
"v_mul_f32 v93, s20, v93\n" \
"v_mfma_f32_16x16x32_bf16 a[136:139], a[96:99], a[92:95], a[136:139]\n" \
"s_add_u32 s76, s76, 2097152\ns_addc_u32 s77, s77, 0\n" \
"v_mul_f32 v94, s20, v94\n" \
"v_mfma_f32_16x16x32_bf16 a[140:143], a[100:103], a[92:95], a[140:143]\n" \
"s_mov_b64 exec, 0xffff\nglobal_load_dword a244, v1, s[76:77]\ns_mov_b64 exec, -1\n" \
"v_mfma_f32_16x16x32_bf16 a[144:147], a[104:107], a[88:91], a[144:147]\n" \
"v_mul_f32 v95, s20, v95\n" \
"v_cvt_pk_bf16_f32 v44, v92, v93\n" \
"v_mfma_f32_16x16x32_bf16 a[148:151], a[108:111], a[88:91], a[148:151]\n" \
"v_cvt_pk_bf16_f32 v45, v94, v95\n" \
"v_mul_f32 v96, s20, v96\n" \
"v_mfma_f32_16x16x32_bf16 a[152:155], a[104:107], a[92:95], a[152:155]\n" \
"v_mul_f32 v97, s20, v97\n" \
"v_mul_f32 v98, s20, v98\n" \
"v_mfma_f32_16x16x32_bf16 a[156:159], a[108:111], a[92:95], a[156:159]\n" \
"v_mul_f32 v99, s20, v99\n" \
"v_mfma_f32_16x16x32_bf16 a[160:163], a[112:115], a[88:91], a[160:163]\n" \
"v_mfma_f32_16x16x32_bf16 a[164:167], a[116:119], a[88:91], a[164:167]\n" \
"v_mfma_f32_16x16x32_bf16 a[168:171], a[112:115], a[92:95], a[168:171]\n" \
"s_add_u32 s76, s76, 2097152\ns_addc_u32 s77, s77, 0\n" \
"v_mfma_f32_16x16x32_bf16 a[172:175], a[116:119], a[92:95], a[172:175]\n" \
"s_mov_b64 exec, 0xffff\nglobal_load_dword a244, v1, s[76:77]\ns_mov_b64 exec, -1\n" \
"v_mfma_f32_16x16x32_bf16 a[176:179], a[120:123], a[88:91], a[176:179]\n" \
"v_mfma_f32_16x16x32_bf16 a[180:183], a[124:127], a[88:91], a[180:183]\n" \
"v_mfma_f32_16x16x32_bf16 a[184:187], a[120:123], a[92:95], a[184:187]\n" \
"s_waitcnt lgkmcnt(0)\n" \
"v_mfma_f32_16x16x32_bf16 a[188:191], a[124:127], a[92:95], a[188:191]\n" \
"buffer_store_dwordx4 v[30:33], v4, s[36:39], s96 offen offset:0 sc1\n" \
"s_add_u32 s97, s96, s98\n" \
"s_branch LC_%=\n" \
"LAV_%=:\n" \
"v_mfma_f32_16x16x32_bf16 v[64:67], a[0:3], a[32:35], 0\n" \
"ds_read_b128 a[64:67], v8 offset:0\n" \
"v_mfma_f32_16x16x32_bf16 v[68:71], a[4:7], a[32:35], 0\n" \
"s_add_u32 s14, s14, 1\ns_addc_u32 s15, s15, 0\n" \
"v_mfma_f32_16x16x32_bf16 v[72:75], a[0:3], a[36:39], 0\n" \
"s_mov_b32 s47, 786432\n" \
"s_add_u32 m0, s22, 49152\n" \
"ds_read_b64_tr_b16 a[96:97], v9 offset:16384\n" \
"v_mfma_f32_16x16x32_bf16 v[76:79], a[4:7], a[36:39], 0\n" \
"buffer_load_dwordx4 v21, s[32:35], s47 offen offset:0 lds\n" \
"v_mfma_f32_16x16x32_bf16 v[80:83], a[8:11], a[32:35], 0\n" \
"ds_read_b64_tr_b16 a[98:99], v10 offset:16384\n" \
"v_mfma_f32_16x16x32_bf16 v[84:87], a[12:15], a[32:35], 0\n" \
"v_mfma_f32_16x16x32_bf16 v[88:91], a[8:11], a[36:39], 0\n" \
"s_add_u32 m0, s22, 50048\n" \
"ds_read_b128 a[68:71], v8 offset:2048\n" \
"v_mfma_f32_16x16x32_bf16 v[92:95], a[12:15], a[36:39], 0\n" \
"buffer_load_dwordx4 v21, s[32:35], s47 offen offset:128 lds\n" \
"v_mfma_f32_16x16x32_bf16 v[96:99], a[16:19], a[32:35], 0\n" \
"ds_read_b64_tr_b16 a[100:101], v11 offset:16384\n" \
"v_mfma_f32_16x16x32_bf16 v[100:103], a[20:23], a[32:35], 0\n" \
"v_mfma_f32_16x16x32_bf16 v[104:107], a[16:19], a[36:39], 0\n" \
"s_add_u32 m0, s22, 50944\n" \
"ds_read_b64_tr_b16 a[102:103], v12 offset:16384\n" \
"v_mfma_f32_16x16x32_bf16 v[108:111], a[20:23], a[36:39], 0\n" \
"buffer_load_dwordx4 v21, s[32:35], s47 offen offset:256 lds\n" \
"v_mfma_f32_16x16x32_bf16 v[112:115], a[24:27], a[32:35], 0\n" \
"ds_read_b128 a[72:75], v8 offset:4096\n" \
"v_mfma_f32_16x16x32_bf16 v[116:119], a[28:31], a[32:35], 0\n" \
"v_mfma_f32_16x16x32_bf16 v[120:123], a[24:27], a[36:39], 0\n" \
"s_add_u32 m0, s22, 51840\n" \
"ds_read_b64_tr_b16 a[104:105], v9 offset:16896\n" \
"v_mfma_f32_16x16x32_bf16 v[124:127], a[28:31], a[36:39], 0\n" \
"buffer_load_dwordx4 v21, s[32:35], s47 offen offset:384 lds\n" \
"v_mfma_f32_16x16x32_bf16 v[128:131], a[0:3], a[40:43], 0\n" \
"ds_read_b64_tr_b16 a[106:107], v10 offset:16896\n" \
"v_mfma_f32_16x16x32_bf16 v[132:135], a[4:7], a[40:43], 0\n" \
"v_mfma_f32_16x16x32_bf16 v[136:139], a[0:3], a[44:47], 0\n" \
"s_mov_b32 s46, 256\n" \
"s_add_u32 m0, s50, 32768\n" \
"ds_read_b128 a[76:79], v8 offset:6144\n" \
"v_mfma_f32_16x16x32_bf16 v[140:143], a[4:7], a[44:47], 0\n" \
"buffer_load_dwordx4 v13, s[28:31], s46 offen lds\n" \
"v_mfma_f32_16x16x32_bf16 v[144:147], a[8:11], a[40:43], 0\n" \
"ds_read_b64_tr_b16 a[108:109], v11 offset:16896\n" \
"v_mfma_f32_16x16x32_bf16 v[148:151], a[12:15], a[40:43], 0\n" \
"v_mfma_f32_16x16x32_bf16 v[152:155], a[8:11], a[44:47], 0\n" \
"s_add_u32 m0, s50, 36864\n" \
"ds_read_b64_tr_b16 a[110:111], v12 offset:16896\n" \
"v_mfma_f32_16x16x32_bf16 v[156:159], a[12:15], a[44:47], 0\n" \
"buffer_load_dwordx4 v14, s[28:31], s46 offen lds\n" \
"v_mfma_f32_16x16x32_bf16 v[160:163], a[16:19], a[40:43], 0\n" \
"ds_read_b128 a[80:83], v8 offset:8192\n" \
"v_mfma_f32_16x16x32_bf16 v[164:167], a[20:23], a[40:43], 0\n" \
"v_mfma_f32_16x16x32_bf16 v[168:171], a[16:19], a[44:47], 0\n" \
"s_add_u32 m0, s50, 40960\n" \
"ds_read_b64_tr_b16 a[112:113], v9 offset:17408\n" \
"v_mfma_f32_16x16x32_bf16 v[172:175], a[20:23], a[44:47], 0\n" \
"buffer_load_dwordx4 v15, s[28:31], s46 offen lds\n" \
"v_mfma_f32_16x16x32_bf16 v[176:179], a[24:27], a[40:43], 0\n" \
"ds_read_b64_tr_b16 a[114:115], v10 offset:17408\n" \
"v_mfma_f32_16x16x32_bf16 v[180:183], a[28:31], a[40:43], 0\n" \
"v_mfma_f32_16x16x32_bf16 v[184:187], a[24:27], a[44:47], 0\n" \
"s_add_u32 m0, s50, 45056\n" \
"ds_read_b128 a[84:87], v8 offset:10240\n" \
"v_mfma_f32_16x16x32_bf16 v[188:191], a[28:31], a[44:47], 0\n" \
"buffer_load_dwordx4 v16, s[28:31], s46 offen lds\n" \
"v_mfma_f32_16x16x32_bf16 v[192:195], a[0:3], a[48:51], 0\n" \
"ds_read_b64_tr_b16 a[116:117], v11 offset:17408\n" \
"v_mfma_f32_16x16x32_bf16 v[196:199], a[4:7], a[48:51], 0\n" \
"v_mfma_f32_16x16x32_bf16 v[200:203], a[0:3], a[52:55], 0\n" \
"s_mov_b32 s46, 256\n" \
"s_add_u32 m0, s50, 49152\n" \
"ds_read_b64_tr_b16 a[118:119], v12 offset:17408\n" \
"v_mfma_f32_16x16x32_bf16 v[204:207], a[4:7], a[52:55], 0\n" \
"buffer_load_dwordx4 v17, s[28:31], s46 offen lds\n" \
"v_mfma_f32_16x16x32_bf16 v[208:211], a[8:11], a[48:51], 0\n" \
"ds_read_b128 a[88:91], v8 offset:12288\n" \
"v_mfma_f32_16x16x32_bf16 v[212:215], a[12:15], a[48:51], 0\n" \
"v_mfma_f32_16x16x32_bf16 v[216:219], a[8:11], a[52:55], 0\n" \
"s_add_u32 m0, s50, 53248\n" \
"ds_read_b64_tr_b16 a[120:121], v9 offset:17920\n" \
"v_mfma_f32_16x16x32_bf16 v[220:223], a[12:15], a[52:55], 0\n" \
"buffer_load_dwordx4 v18, s[28:31], s46 offen lds\n" \
"v_mfma_f32_16x16x32_bf16 v[224:227], a[16:19], a[48:51], 0\n" \
"ds_read_b64_tr_b16 a[122:123], v10 offset:17920\n" \
"v_mfma_f32_16x16x32_bf16 v[228:231], a[20:23], a[48:51], 0\n" \
"v_mfma_f32_16x16x32_bf16 v[232:235], a[16:19], a[52:55], 0\n" \
"s_add_u32 m0, s50, 57344\n" \
"ds_read_b128 a[92:95], v8 offset:14336\n" \
"v_mfma_f32_16x16x32_bf16 v[236:239], a[20:23], a[52:55], 0\n" \
"buffer_load_dwordx4 v19, s[28:31], s46 offen lds\n" \
"v_mfma_f32_16x16x32_bf16 v[240:243], a[24:27], a[48:51], 0\n" \
"ds_read_b64_tr_b16 a[124:125], v11 offset:17920\n" \
"v_mfma_f32_16x16x32_bf16 v[244:247], a[28:31], a[48:51], 0\n" \
"v_mfma_f32_16x16x32_bf16 v[248:251], a[24:27], a[52:55], 0\n" \
"s_add_u32 m0, s50, 61440\n" \
"ds_read_b64_tr_b16 a[126:127], v12 offset:17920\n" \
"v_mfma_f32_16x16x32_bf16 v[252:255], a[28:31], a[52:55], 0\n" \
"buffer_load_dwordx4 v20, s[28:31], s46 offen lds\n" \
"v_mfma_f32_16x16x32_bf16 a[128:131], a[0:3], a[56:59], 0\n" \
"v_mfma_f32_16x16x32_bf16 a[132:135], a[4:7], a[56:59], 0\n" \
"v_mfma_f32_16x16x32_bf16 a[136:139], a[0:3], a[60:63], 0\n" \
"v_mfma_f32_16x16x32_bf16 a[140:143], a[4:7], a[60:63], 0\n" \
"global_load_dwordx4 v[46:49], v4, s[86:87] offset:0\n" \
"v_mfma_f32_16x16x32_bf16 a[144:147], a[8:11], a[56:59], 0\n" \
"v_mfma_f32_16x16x32_bf16 a[148:151], a[12:15], a[56:59], 0\n" \
"v_mfma_f32_16x16x32_bf16 a[152:155], a[8:11], a[60:63], 0\n" \
"v_mfma_f32_16x16x32_bf16 a[156:159], a[12:15], a[60:63], 0\n" \
"s_waitcnt vmcnt(13) lgkmcnt(0)\n" \
"v_mfma_f32_16x16x32_bf16 a[160:163], a[16:19], a[56:59], 0\n" \
"s_barrier\n" \
"v_mfma_f32_16x16x32_bf16 a[164:167], a[20:23], a[56:59], 0\n" \
"v_mfma_f32_16x16x32_bf16 a[168:171], a[16:19], a[60:63], 0\n" \
"v_mfma_f32_16x16x32_bf16 a[172:175], a[20:23], a[60:63], 0\n" \
"global_load_dwordx4 v[50:53], v4, s[86:87] offset:1024\n" \
"v_mfma_f32_16x16x32_bf16 a[176:179], a[24:27], a[56:59], 0\n" \
"v_mfma_f32_16x16x32_bf16 a[180:183], a[28:31], a[56:59], 0\n" \
"v_mfma_f32_16x16x32_bf16 a[184:187], a[24:27], a[60:63], 0\n" \
"v_mfma_f32_16x16x32_bf16 a[188:191], a[28:31], a[60:63], 0\n" \
"global_load_dwordx4 v[30:33], v26, s[64:65] offset:0\n" \
"v_mfma_f32_16x16x32_bf16 v[64:67], a[64:67], a[96:99], v[64:67]\n" \
"ds_read_b128 a[0:3], v5 offset:0\n" \
"v_mfma_f32_16x16x32_bf16 v[68:71], a[68:71], a[96:99], v[68:71]\n" \
"v_mfma_f32_16x16x32_bf16 v[72:75], a[64:67], a[100:103], v[72:75]\n" \
"ds_read_b64_tr_b16 a[32:33], v9 offset:32768\n" \
"v_mfma_f32_16x16x32_bf16 v[76:79], a[68:71], a[100:103], v[76:79]\n" \
"global_load_dwordx4 v[34:37], v26, s[64:65] offset:1024\n" \
"v_mfma_f32_16x16x32_bf16 v[80:83], a[72:75], a[96:99], v[80:83]\n" \
"ds_read_b64_tr_b16 a[34:35], v10 offset:32768\n" \
"v_mfma_f32_16x16x32_bf16 v[84:87], a[76:79], a[96:99], v[84:87]\n" \
"v_mfma_f32_16x16x32_bf16 v[88:91], a[72:75], a[100:103], v[88:91]\n" \
"ds_read_b128 a[4:7], v5 offset:2048\n" \
"v_mfma_f32_16x16x32_bf16 v[92:95], a[76:79], a[100:103], v[92:95]\n" \
"global_load_dwordx4 v[38:41], v26, s[64:65] offset:2048\n" \
"v_mfma_f32_16x16x32_bf16 v[96:99], a[80:83], a[96:99], v[96:99]\n" \
"ds_read_b64_tr_b16 a[36:37], v11 offset:32768\n" \
"v_mfma_f32_16x16x32_bf16 v[100:103], a[84:87], a[96:99], v[100:103]\n" \
"v_mfma_f32_16x16x32_bf16 v[104:107], a[80:83], a[100:103], v[104:107]\n" \
"ds_read_b64_tr_b16 a[38:39], v12 offset:32768\n" \
"v_mfma_f32_16x16x32_bf16 v[108:111], a[84:87], a[100:103], v[108:111]\n" \
"global_load_dwordx4 v[42:45], v26, s[64:65] offset:3072\n" \
"v_mfma_f32_16x16x32_bf16 v[112:115], a[88:91], a[96:99], v[112:115]\n" \
"ds_read_b128 a[8:11], v5 offset:4096\n" \
"v_mfma_f32_16x16x32_bf16 v[116:119], a[92:95], a[96:99], v[116:119]\n" \
"v_mfma_f32_16x16x32_bf16 v[120:123], a[88:91], a[100:103], v[120:123]\n" \
"s_mov_b32 s47, 1048576\n" \
"s_add_u32 m0, s22, 0\n" \
"ds_read_b64_tr_b16 a[40:41], v9 offset:33280\n" \
"v_mfma_f32_16x16x32_bf16 v[124:127], a[92:95], a[100:103], v[124:127]\n" \
"buffer_load_dwordx4 v21, s[32:35], s47 offen offset:0 lds\n" \
"v_mfma_f32_16x16x32_bf16 v[128:131], a[64:67], a[104:107], v[128:131]\n" \
"ds_read_b64_tr_b16 a[42:43], v10 offset:33280\n" \
"v_mfma_f32_16x16x32_bf16 v[132:135], a[68:71], a[104:107], v[132:135]\n" \
"v_mfma_f32_16x16x32_bf16 v[136:139], a[64:67], a[108:111], v[136:139]\n" \
"s_add_u32 m0, s22, 896\n" \
"ds_read_b128 a[12:15], v5 offset:6144\n" \
"v_mfma_f32_16x16x32_bf16 v[140:143], a[68:71], a[108:111], v[140:143]\n" \
"buffer_load_dwordx4 v21, s[32:35], s47 offen offset:128 lds\n" \
"v_mfma_f32_16x16x32_bf16 v[144:147], a[72:75], a[104:107], v[144:147]\n" \
"ds_read_b64_tr_b16 a[44:45], v11 offset:33280\n" \
"v_mfma_f32_16x16x32_bf16 v[148:151], a[76:79], a[104:107], v[148:151]\n" \
"v_mfma_f32_16x16x32_bf16 v[152:155], a[72:75], a[108:111], v[152:155]\n" \
"s_add_u32 m0, s22, 1792\n" \
"ds_read_b64_tr_b16 a[46:47], v12 offset:33280\n" \
"v_mfma_f32_16x16x32_bf16 v[156:159], a[76:79], a[108:111], v[156:159]\n" \
"buffer_load_dwordx4 v21, s[32:35], s47 offen offset:256 lds\n" \
"v_mfma_f32_16x16x32_bf16 v[160:163], a[80:83], a[104:107], v[160:163]\n" \
"ds_read_b128 a[16:19], v5 offset:8192\n" \
"v_mfma_f32_16x16x32_bf16 v[164:167], a[84:87], a[104:107], v[164:167]\n" \
"v_mfma_f32_16x16x32_bf16 v[168:171], a[80:83], a[108:111], v[168:171]\n" \
"s_add_u32 m0, s22, 2688\n" \
"ds_read_b64_tr_b16 a[48:49], v9 offset:33792\n" \
"v_mfma_f32_16x16x32_bf16 v[172:175], a[84:87], a[108:111], v[172:175]\n" \
"buffer_load_dwordx4 v21, s[32:35], s47 offen offset:384 lds\n" \
"v_mfma_f32_16x16x32_bf16 v[176:179], a[88:91], a[104:107], v[176:179]\n" \
"ds_read_b64_tr_b16 a[50:51], v10 offset:33792\n" \
"v_mfma_f32_16x16x32_bf16 v[180:183], a[92:95], a[104:107], v[180:183]\n" \
"v_mfma_f32_16x16x32_bf16 v[184:187], a[88:91], a[108:111], v[184:187]\n" \
"ds_read_b128 a[20:23], v5 offset:10240\n" \
"v_mfma_f32_16x16x32_bf16 v[188:191], a[92:95], a[108:111], v[188:191]\n" \
"global_load_dwordx4 a[230:233], v27, s[70:71] offset:0\n" \
"v_mfma_f32_16x16x32_bf16 v[192:195], a[64:67], a[112:115], v[192:195]\n" \
"ds_read_b64_tr_b16 a[52:53], v11 offset:33792\n" \
"v_mfma_f32_16x16x32_bf16 v[196:199], a[68:71], a[112:115], v[196:199]\n" \
"v_mfma_f32_16x16x32_bf16 v[200:203], a[64:67], a[116:119], v[200:203]\n" \
"ds_read_b64_tr_b16 a[54:55], v12 offset:33792\n" \
"v_mfma_f32_16x16x32_bf16 v[204:207], a[68:71], a[116:119], v[204:207]\n" \
"global_load_dwordx4 a[234:237], v27, s[70:71] offset:16\n" \
"v_mfma_f32_16x16x32_bf16 v[208:211], a[72:75], a[112:115], v[208:211]\n" \
"ds_read_b128 a[24:27], v5 offset:12288\n" \
"v_mfma_f32_16x16x32_bf16 v[212:215], a[76:79], a[112:115], v[212:215]\n" \
"v_mfma_f32_16x16x32_bf16 v[216:219], a[72:75], a[116:119], v[216:219]\n" \
"ds_read_b64_tr_b16 a[56:57], v9 offset:34304\n" \
"v_mfma_f32_16x16x32_bf16 v[220:223], a[76:79], a[116:119], v[220:223]\n" \
"buffer_load_dwordx2 a[192:193], v54, s[40:43], 0 offen offset:0\n" \
"v_mfma_f32_16x16x32_bf16 v[224:227], a[80:83], a[112:115], v[224:227]\n" \
"ds_read_b64_tr_b16 a[58:59], v10 offset:34304\n" \
"v_mfma_f32_16x16x32_bf16 v[228:231], a[84:87], a[112:115], v[228:231]\n" \
"v_mfma_f32_16x16x32_bf16 v[232:235], a[80:83], a[116:119], v[232:235]\n" \
"ds_read_b128 a[28:31], v5 offset:14336\n" \
"v_mfma_f32_16x16x32_bf16 v[236:239], a[84:87], a[116:119], v[236:239]\n" \
"buffer_load_dwordx2 a[194:195], v54, s[40:43], 0 offen offset:2048\n" \
"v_mfma_f32_16x16x32_bf16 v[240:243], a[88:91], a[112:115], v[240:243]\n" \
"ds_read_b64_tr_b16 a[60:61], v11 offset:34304\n" \
"v_mfma_f32_16x16x32_bf16 v[244:247], a[92:95], a[112:115], v[244:247]\n" \
"v_mfma_f32_16x16x32_bf16 v[248:251], a[88:91], a[116:119], v[248:251]\n" \
"ds_read_b64_tr_b16 a[62:63], v12 offset:34304\n" \
"v_mfma_f32_16x16x32_bf16 v[252:255], a[92:95], a[116:119], v[252:255]\n" \
"buffer_load_dwordx2 a[196:197], v55, s[40:43], 0 offen offset:0\n" \
"v_mfma_f32_16x16x32_bf16 a[128:131], a[64:67], a[120:123], a[128:131]\n" \
"v_mfma_f32_16x16x32_bf16 a[132:135], a[68:71], a[120:123], a[132:135]\n" \
"v_mfma_f32_16x16x32_bf16 a[136:139], a[64:67], a[124:127], a[136:139]\n" \
"s_mov_b32 s52, 0\n" \
"v_mfma_f32_16x16x32_bf16 a[140:143], a[68:71], a[124:127], a[140:143]\n" \
"buffer_load_dwordx2 a[198:199], v59, s[40:43], s52 offen offset:0\n" \
"v_mfma_f32_16x16x32_bf16 a[144:147], a[72:75], a[120:123], a[144:147]\n" \
"v_mfma_f32_16x16x32_bf16 a[148:151], a[76:79], a[120:123], a[148:151]\n" \
"v_mfma_f32_16x16x32_bf16 a[152:155], a[72:75], a[124:127], a[152:155]\n" \
"v_mfma_f32_16x16x32_bf16 a[156:159], a[76:79], a[124:127], a[156:159]\n" \
"s_waitcnt vmcnt(24) lgkmcnt(0)\n" \
"v_mfma_f32_16x16x32_bf16 a[160:163], a[80:83], a[120:123], a[160:163]\n" \
"s_barrier\n" \
"v_mfma_f32_16x16x32_bf16 a[164:167], a[84:87], a[120:123], a[164:167]\n" \
"v_mfma_f32_16x16x32_bf16 a[168:171], a[80:83], a[124:127], a[168:171]\n" \
"v_mfma_f32_16x16x32_bf16 a[172:175], a[84:87], a[124:127], a[172:175]\n" \
"buffer_load_dwordx2 a[214:215], v59, s[80:83], s52 offen offset:0\n" \
"v_mfma_f32_16x16x32_bf16 a[176:179], a[88:91], a[120:123], a[176:179]\n" \
"v_mfma_f32_16x16x32_bf16 a[180:183], a[92:95], a[120:123], a[180:183]\n" \
"s_waitcnt vmcnt(11)\n" \
"v_lshlrev_b32 v25, 16, v46\n" \
"v_mfma_f32_16x16x32_bf16 a[184:187], a[88:91], a[124:127], a[184:187]\n" \
"v_and_b32 v46, 0xffff0000, v46\n" \
"v_lshlrev_b32 v26, 16, v30\n" \
"v_mfma_f32_16x16x32_bf16 a[188:191], a[92:95], a[124:127], a[188:191]\n" \
"buffer_load_dwordx2 a[200:201], v59, s[40:43], s52 offen offset:2048\n" \
"v_mfma_f32_16x16x32_bf16 v[64:67], a[0:3], a[32:35], v[64:67]\n" \
"ds_read_b128 a[64:67], v6 offset:0\n" \
"v_and_b32 v30, 0xffff0000, v30\n" \
"v_mfma_f32_16x16x32_bf16 v[68:71], a[4:7], a[32:35], v[68:71]\n" \
"v_mul_f32 v23, v26, v25\n" \
"v_fmac_f32 v23, v30, v46\n" \
"v_mfma_f32_16x16x32_bf16 v[72:75], a[0:3], a[36:39], v[72:75]\n" \
"s_mov_b32 s47, 1310720\n" \
"s_add_u32 m0, s22, 16384\n" \
"ds_read_b64_tr_b16 a[96:97], v9 offset:49152\n" \
"v_mfma_f32_16x16x32_bf16 v[76:79], a[4:7], a[36:39], v[76:79]\n" \
"buffer_load_dwordx4 v21, s[32:35], s47 offen offset:0 lds\n" \
"v_mfma_f32_16x16x32_bf16 v[80:83], a[8:11], a[32:35], v[80:83]\n" \
"ds_read_b64_tr_b16 a[98:99], v10 offset:49152\n" \
"v_lshlrev_b32 v26, 16, v38\n" \
"v_mfma_f32_16x16x32_bf16 v[84:87], a[12:15], a[32:35], v[84:87]\n" \
"v_and_b32 v38, 0xffff0000, v38\n" \
"v_mul_f32 v24, v26, v25\n" \
"v_mfma_f32_16x16x32_bf16 v[88:91], a[8:11], a[36:39], v[88:91]\n" \
"s_add_u32 m0, s22, 17280\n" \
"ds_read_b128 a[68:71], v6 offset:2048\n" \
"v_fmac_f32 v24, v38, v46\n" \
"v_mfma_f32_16x16x32_bf16 v[92:95], a[12:15], a[36:39], v[92:95]\n" \
"buffer_load_dwordx4 v21, s[32:35], s47 offen offset:128 lds\n" \
"v_mfma_f32_16x16x32_bf16 v[96:99], a[16:19], a[32:35], v[96:99]\n" \
"ds_read_b64_tr_b16 a[100:101], v11 offset:49152\n" \
"v_lshlrev_b32 v25, 16, v47\n" \
"v_mfma_f32_16x16x32_bf16 v[100:103], a[20:23], a[32:35], v[100:103]\n" \
"v_and_b32 v47, 0xffff0000, v47\n" \
"v_lshlrev_b32 v26, 16, v31\n" \
"v_mfma_f32_16x16x32_bf16 v[104:107], a[16:19], a[36:39], v[104:107]\n" \
"s_add_u32 m0, s22, 18176\n" \
"ds_read_b64_tr_b16 a[102:103], v12 offset:49152\n" \
"v_and_b32 v31, 0xffff0000, v31\n" \
"v_mfma_f32_16x16x32_bf16 v[108:111], a[20:23], a[36:39], v[108:111]\n" \
"buffer_load_dwordx4 v21, s[32:35], s47 offen offset:256 lds\n" \
"v_mfma_f32_16x16x32_bf16 v[112:115], a[24:27], a[32:35], v[112:115]\n" \
"ds_read_b128 a[72:75], v6 offset:4096\n" \
"v_fmac_f32 v23, v26, v25\n" \
"v_mfma_f32_16x16x32_bf16 v[116:119], a[28:31], a[32:35], v[116:119]\n" \
"v_fmac_f32 v23, v31, v47\n" \
"v_lshlrev_b32 v26, 16, v39\n" \
"v_mfma_f32_16x16x32_bf16 v[120:123], a[24:27], a[36:39], v[120:123]\n" \
"s_add_u32 m0, s22, 19072\n" \
"ds_read_b64_tr_b16 a[104:105], v9 offset:49664\n" \
"v_and_b32 v39, 0xffff0000, v39\n" \
"v_mfma_f32_16x16x32_bf16 v[124:127], a[28:31], a[36:39], v[124:127]\n" \
"buffer_load_dwordx4 v21, s[32:35], s47 offen offset:384 lds\n" \
"v_mfma_f32_16x16x32_bf16 v[128:131], a[0:3], a[40:43], v[128:131]\n" \
"ds_read_b64_tr_b16 a[106:107], v10 offset:49664\n" \
"v_fmac_f32 v24, v26, v25\n" \
"v_mfma_f32_16x16x32_bf16 v[132:135], a[4:7], a[40:43], v[132:135]\n" \
"v_fmac_f32 v24, v39, v47\n" \
"v_lshlrev_b32 v25, 16, v48\n" \
"v_mfma_f32_16x16x32_bf16 v[136:139], a[0:3], a[44:47], v[136:139]\n" \
"ds_read_b128 a[76:79], v6 offset:6144\n" \
"v_and_b32 v48, 0xffff0000, v48\n" \
"v_mfma_f32_16x16x32_bf16 v[140:143], a[4:7], a[44:47], v[140:143]\n" \
"buffer_load_dwordx2 a[216:217], v59, s[80:83], s52 offen offset:2048\n" \
"v_mfma_f32_16x16x32_bf16 v[144:147], a[8:11], a[40:43], v[144:147]\n" \
"ds_read_b64_tr_b16 a[108:109], v11 offset:49664\n" \
"v_lshlrev_b32 v26, 16, v32\n" \
"v_mfma_f32_16x16x32_bf16 v[148:151], a[12:15], a[40:43], v[148:151]\n" \
"v_and_b32 v32, 0xffff0000, v32\n" \
"v_fmac_f32 v23, v26, v25\n" \
"v_mfma_f32_16x16x32_bf16 v[152:155], a[8:11], a[44:47], v[152:155]\n" \
"s_mov_b32 s52, 4096\n" \
"ds_read_b64_tr_b16 a[110:111], v12 offset:49664\n" \
"v_fmac_f32 v23, v32, v48\n" \
"v_mfma_f32_16x16x32_bf16 v[156:159], a[12:15], a[44:47], v[156:159]\n" \
"buffer_load_dwordx2 a[202:203], v59, s[40:43], s52 offen offset:0\n" \
"v_mfma_f32_16x16x32_bf16 v[160:163], a[16:19], a[40:43], v[160:163]\n" \
"ds_read_b128 a[80:83], v6 offset:8192\n" \
"v_lshlrev_b32 v26, 16, v40\n" \
"v_mfma_f32_16x16x32_bf16 v[164:167], a[20:23], a[40:43], v[164:167]\n" \
"v_and_b32 v40, 0xffff0000, v40\n" \
"v_fmac_f32 v24, v26, v25\n" \
"v_mfma_f32_16x16x32_bf16 v[168:171], a[16:19], a[44:47], v[168:171]\n" \
"ds_read_b64_tr_b16 a[112:113], v9 offset:50176\n" \
"v_fmac_f32 v24, v40, v48\n" \
"v_mfma_f32_16x16x32_bf16 v[172:175], a[20:23], a[44:47], v[172:175]\n" \
"buffer_load_dwordx2 a[218:219], v59, s[80:83], s52 offen offset:0\n" \
"v_mfma_f32_16x16x32_bf16 v[176:179], a[24:27], a[40:43], v[176:179]\n" \
"ds_read_b64_tr_b16 a[114:115], v10 offset:50176\n" \
"v_lshlrev_b32 v25, 16, v49\n" \
"v_mfma_f32_16x16x32_bf16 v[180:183], a[28:31], a[40:43], v[180:183]\n" \
"v_and_b32 v49, 0xffff0000, v49\n" \
"v_lshlrev_b32 v26, 16, v33\n" \
"v_mfma_f32_16x16x32_bf16 v[184:187], a[24:27], a[44:47], v[184:187]\n" \
"ds_read_b128 a[84:87], v6 offset:10240\n" \
"v_and_b32 v33, 0xffff0000, v33\n" \
"v_mfma_f32_16x16x32_bf16 v[188:191], a[28:31], a[44:47], v[188:191]\n" \
"LH4_%=:\ns_cbranch_execz LH8_%=\n" \
"buffer_load_dwordx2 a[204:205], v59, s[40:43], s52 offen offset:2048\n" \
"v_mfma_f32_16x16x32_bf16 v[192:195], a[0:3], a[48:51], v[192:195]\n" \
"ds_read_b64_tr_b16 a[116:117], v11 offset:50176\n" \
"v_fmac_f32 v23, v26, v25\n" \
"v_mfma_f32_16x16x32_bf16 v[196:199], a[4:7], a[48:51], v[196:199]\n" \
"v_fmac_f32 v23, v33, v49\n" \
"v_lshlrev_b32 v26, 16, v41\n" \
"v_mfma_f32_16x16x32_bf16 v[200:203], a[0:3], a[52:55], v[200:203]\n" \
"ds_read_b64_tr_b16 a[118:119], v12 offset:50176\n" \
"v_and_b32 v41, 0xffff0000, v41\n" \
"v_mfma_f32_16x16x32_bf16 v[204:207], a[4:7], a[52:55], v[204:207]\n" \
"buffer_load_dwordx2 a[220:221], v59, s[80:83], s52 offen offset:2048\n" \
"v_mfma_f32_16x16x32_bf16 v[208:211], a[8:11], a[48:51], v[208:211]\n" \
"ds_read_b128 a[88:91], v6 offset:12288\n" \
"v_fmac_f32 v24, v26, v25\n" \
"v_mfma_f32_16x16x32_bf16 v[212:215], a[12:15], a[48:51], v[212:215]\n" \
"v_fmac_f32 v24, v41, v49\n" \
"v_lshlrev_b32 v25, 16, v50\n" \
"v_mfma_f32_16x16x32_bf16 v[216:219], a[8:11], a[52:55], v[216:219]\n" \
"s_mov_b32 s52, 8192\n" \
"ds_read_b64_tr_b16 a[120:121], v9 offset:50688\n" \
"v_and_b32 v50, 0xffff0000, v50\n" \
"v_mfma_f32_16x16x32_bf16 v[220:223], a[12:15], a[52:55], v[220:223]\n" \
"buffer_load_dwordx2 a[206:207], v59, s[40:43], s52 offen offset:0\n" \
"v_mfma_f32_16x16x32_bf16 v[224:227], a[16:19], a[48:51], v[224:227]\n" \
"ds_read_b64_tr_b16 a[122:123], v10 offset:50688\n" \
"v_lshlrev_b32 v26, 16, v34\n" \
"v_mfma_f32_16x16x32_bf16 v[228:231], a[20:23], a[48:51], v[228:231]\n" \
"v_and_b32 v34, 0xffff0000, v34\n" \
"v_fmac_f32 v23, v26, v25\n" \
"v_mfma_f32_16x16x32_bf16 v[232:235], a[16:19], a[52:55], v[232:235]\n" \
"ds_read_b128 a[92:95], v6 offset:14336\n" \
"v_fmac_f32 v23, v34, v50\n" \
"v_mfma_f32_16x16x32_bf16 v[236:239], a[20:23], a[52:55], v[236:239]\n" \
"buffer_load_dwordx2 a[222:223], v59, s[80:83], s52 offen offset:0\n" \
"v_mfma_f32_16x16x32_bf16 v[240:243], a[24:27], a[48:51], v[240:243]\n" \
"ds_read_b64_tr_b16 a[124:125], v11 offset:50688\n" \
"v_lshlrev_b32 v26, 16, v42\n" \
"v_mfma_f32_16x16x32_bf16 v[244:247], a[28:31], a[48:51], v[244:247]\n" \
"v_and_b32 v42, 0xffff0000, v42\n" \
"v_fmac_f32 v24, v26, v25\n" \
"v_mfma_f32_16x16x32_bf16 v[248:251], a[24:27], a[52:55], v[248:251]\n" \
"s_mov_b32 s47, 1572864\n" \
"s_add_u32 m0, s22, 32768\n" \
"ds_read_b64_tr_b16 a[126:127], v12 offset:50688\n" \
"v_mfma_f32_16x16x32_bf16 v[252:255], a[28:31], a[52:55], v[252:255]\n" \
"buffer_load_dwordx4 v21, s[32:35], s47 offen offset:0 lds\n" \
"v_mfma_f32_16x16x32_bf16 a[128:131], a[0:3], a[56:59], a[128:131]\n" \
"v_fmac_f32 v24, v42, v50\n" \
"v_lshlrev_b32 v25, 16, v51\n" \
"v_mfma_f32_16x16x32_bf16 a[132:135], a[4:7], a[56:59], a[132:135]\n" \
"v_and_b32 v51, 0xffff0000, v51\n" \
"v_lshlrev_b32 v26, 16, v35\n" \
"v_mfma_f32_16x16x32_bf16 a[136:139], a[0:3], a[60:63], a[136:139]\n" \
"s_add_u32 m0, s22, 33664\n" \
"v_and_b32 v35, 0xffff0000, v35\n" \
"v_mfma_f32_16x16x32_bf16 a[140:143], a[4:7], a[60:63], a[140:143]\n" \
"buffer_load_dwordx4 v21, s[32:35], s47 offen offset:128 lds\n" \
"v_mfma_f32_16x16x32_bf16 a[144:147], a[8:11], a[56:59], a[144:147]\n" \
"v_fmac_f32 v23, v26, v25\n" \
"v_fmac_f32 v23, v35, v51\n" \
"v_mfma_f32_16x16x32_bf16 a[148:151], a[12:15], a[56:59], a[148:151]\n" \
"v_lshlrev_b32 v26, 16, v43\n" \
"v_and_b32 v43, 0xffff0000, v43\n" \
"v_mfma_f32_16x16x32_bf16 a[152:155], a[8:11], a[60:63], a[152:155]\n" \
"v_fmac_f32 v24, v26, v25\n" \
"v_fmac_f32 v24, v43, v51\n" \
"v_mfma_f32_16x16x32_bf16 a[156:159], a[12:15], a[60:63], a[156:159]\n" \
"s_waitcnt vmcnt(21) lgkmcnt(0)\n" \
"v_lshlrev_b32 v25, 16, v52\n" \
"v_mfma_f32_16x16x32_bf16 a[160:163], a[16:19], a[56:59], a[160:163]\n" \
"s_barrier\n" \
"v_mfma_f32_16x16x32_bf16 a[164:167], a[20:23], a[56:59], a[164:167]\n" \
"v_and_b32 v52, 0xffff0000, v52\n" \
"v_lshlrev_b32 v26, 16, v36\n" \
"v_mfma_f32_16x16x32_bf16 a[168:171], a[16:19], a[60:63], a[168:171]\n" \
"s_add_u32 m0, s22, 34560\n" \
"v_and_b32 v36, 0xffff0000, v36\n" \
"v_mfma_f32_16x16x32_bf16 a[172:175], a[20:23], a[60:63], a[172:175]\n" \
"buffer_load_dwordx4 v21, s[32:35], s47 offen offset:256 lds\n" \
"v_mfma_f32_16x16x32_bf16 a[176:179], a[24:27], a[56:59], a[176:179]\n" \
"s_add_u32 m0, s22, 35456\n" \
"v_fmac_f32 v23, v26, v25\n" \
"v_mfma_f32_16x16x32_bf16 a[180:183], a[28:31], a[56:59], a[180:183]\n" \
"buffer_load_dwordx4 v21, s[32:35], s47 offen offset:384 lds\n" \
"v_mfma_f32_16x16x32_bf16 a[184:187], a[24:27], a[60:63], a[184:187]\n" \
"s_add_u32 s84, s84, 1048576\ns_addc_u32 s85, s85, 0\n" \
"v_fmac_f32 v23, v36, v52\n" \
"v_mfma_f32_16x16x32_bf16 a[188:191], a[28:31], a[60:63], a[188:191]\n" \
"s_mov_b64 exec, 0xffff\nglobal_load_dword a244, v1, s[84:85]\ns_mov_b64 exec, -1\n" \
"v_mfma_f32_16x16x32_bf16 v[64:67], a[64:67], a[96:99], v[64:67]\n" \
"ds_read_b128 a[0:3], v5 offset:32768\n" \
"v_lshlrev_b32 v26, 16, v44\n" \
"v_and_b32 v44, 0xffff0000, v44\n" \
"v_mfma_f32_16x16x32_bf16 v[68:71], a[68:71], a[96:99], v[68:71]\n" \
"v_fmac_f32 v24, v26, v25\n" \
"v_fmac_f32 v24, v44, v52\n" \
"v_mfma_f32_16x16x32_bf16 v[72:75], a[64:67], a[100:103], v[72:75]\n" \
"s_mov_b32 s46, 384\n" \
"s_add_u32 m0, s50, 65536\n" \
"ds_read_b64_tr_b16 a[32:33], v9 offset:0\n" \
"v_mfma_f32_16x16x32_bf16 v[76:79], a[68:71], a[100:103], v[76:79]\n" \
"buffer_load_dwordx4 v13, s[28:31], s46 offen lds\n" \
"v_mfma_f32_16x16x32_bf16 v[80:83], a[72:75], a[96:99], v[80:83]\n" \
"ds_read_b64_tr_b16 a[34:35], v10 offset:0\n" \
"v_lshlrev_b32 v25, 16, v53\n" \
"v_and_b32 v53, 0xffff0000, v53\n" \
"v_mfma_f32_16x16x32_bf16 v[84:87], a[76:79], a[96:99], v[84:87]\n" \
"v_lshlrev_b32 v26, 16, v37\n" \
"v_and_b32 v37, 0xffff0000, v37\n" \
"v_mfma_f32_16x16x32_bf16 v[88:91], a[72:75], a[100:103], v[88:91]\n" \
"s_add_u32 m0, s50, 69632\n" \
"ds_read_b128 a[4:7], v5 offset:34816\n" \
"v_fmac_f32 v23, v26, v25\n" \
"v_mfma_f32_16x16x32_bf16 v[92:95], a[76:79], a[100:103], v[92:95]\n" \
"buffer_load_dwordx4 v14, s[28:31], s46 offen lds\n" \
"v_mfma_f32_16x16x32_bf16 v[96:99], a[80:83], a[96:99], v[96:99]\n" \
"ds_read_b64_tr_b16 a[36:37], v11 offset:0\n" \
"v_fmac_f32 v23, v37, v53\n" \
"v_lshlrev_b32 v26, 16, v45\n" \
"v_mfma_f32_16x16x32_bf16 v[100:103], a[84:87], a[96:99], v[100:103]\n" \
"v_and_b32 v45, 0xffff0000, v45\n" \
"v_fmac_f32 v24, v26, v25\n" \
"v_mfma_f32_16x16x32_bf16 v[104:107], a[80:83], a[100:103], v[104:107]\n" \
"s_add_u32 m0, s50, 73728\n" \
"ds_read_b64_tr_b16 a[38:39], v12 offset:0\n" \
"v_fmac_f32 v24, v45, v53\n" \
"v_mfma_f32_16x16x32_bf16 v[108:111], a[84:87], a[100:103], v[108:111]\n" \
"buffer_load_dwordx4 v15, s[28:31], s46 offen lds\n" \
"s_waitcnt vmcnt(10)\n" \
"v_mfma_f32_16x16x32_bf16 v[112:115], a[88:91], a[96:99], v[112:115]\n" \
"ds_read_b128 a[8:11], v5 offset:36864\n" \
"v_accvgpr_read_b32 v43, a230\n" \
"v_permlane32_swap_b32 v23, v24\n" \
"v_mfma_f32_16x16x32_bf16 v[116:119], a[92:95], a[96:99], v[116:119]\n" \
"v_add_f32 v23, v23, v24\n" \
"v_mov_b32 v24, v23\n" \
"v_mfma_f32_16x16x32_bf16 v[120:123], a[88:91], a[100:103], v[120:123]\n" \
"s_add_u32 m0, s50, 77824\n" \
"ds_read_b64_tr_b16 a[40:41], v9 offset:512\n" \
"v_lshlrev_b32 v42, 16, v43\n" \
"v_mfma_f32_16x16x32_bf16 v[124:127], a[92:95], a[100:103], v[124:127]\n" \
"buffer_load_dwordx4 v16, s[28:31], s46 offen lds\n" \
"v_mfma_f32_16x16x32_bf16 v[128:131], a[64:67], a[104:107], v[128:131]\n" \
"ds_read_b64_tr_b16 a[42:43], v10 offset:512\n" \
"v_and_b32 v43, 0xffff0000, v43\n" \
"v_permlane16_swap_b32 v23, v24\n" \
"v_mfma_f32_16x16x32_bf16 v[132:135], a[68:71], a[104:107], v[132:135]\n" \
"v_add_f32 v23, v23, v24\n" \
"v_accvgpr_read_b32 v45, a231\n" \
"v_mfma_f32_16x16x32_bf16 v[136:139], a[64:67], a[108:111], v[136:139]\n" \
"s_mov_b32 s46, 384\n" \
"s_add_u32 m0, s50, 81920\n" \
"ds_read_b128 a[12:15], v5 offset:38912\n" \
"v_mfma_f32_16x16x32_bf16 v[140:143], a[68:71], a[108:111], v[140:143]\n" \
"buffer_load_dwordx4 v17, s[28:31], s46 offen lds\n" \
"v_mfma_f32_16x16x32_bf16 v[144:147], a[72:75], a[104:107], v[144:147]\n" \
"ds_read_b64_tr_b16 a[44:45], v11 offset:512\n" \
"v_lshlrev_b32 v44, 16, v45\n" \
"v_add_f32 v23, v23, v23 row_ror:8 row_mask:0xf bank_mask:0xf\n" \
"v_mfma_f32_16x16x32_bf16 v[148:151], a[76:79], a[104:107], v[148:151]\n" \
"v_and_b32 v45, 0xffff0000, v45\n" \
"v_accvgpr_read_b32 v47, a232\n" \
"v_mfma_f32_16x16x32_bf16 v[152:155], a[72:75], a[108:111], v[152:155]\n" \
"s_add_u32 m0, s50, 86016\n" \
"ds_read_b64_tr_b16 a[46:47], v12 offset:512\n" \
"v_add_f32 v23, v23, v23 row_ror:4 row_mask:0xf bank_mask:0xf\n" \
"v_mfma_f32_16x16x32_bf16 v[156:159], a[76:79], a[108:111], v[156:159]\n" \
"buffer_load_dwordx4 v18, s[28:31], s46 offen lds\n" \
"v_mfma_f32_16x16x32_bf16 v[160:163], a[80:83], a[104:107], v[160:163]\n" \
"ds_read_b128 a[16:19], v5 offset:40960\n" \
"v_lshlrev_b32 v46, 16, v47\n" \
"v_and_b32 v47, 0xffff0000, v47\n" \
"v_mfma_f32_16x16x32_bf16 v[164:167], a[84:87], a[104:107], v[164:167]\n" \
"v_add_f32 v23, v23, v23 row_ror:2 row_mask:0xf bank_mask:0xf\n" \
"v_accvgpr_read_b32 v49, a233\n" \
"v_mfma_f32_16x16x32_bf16 v[168:171], a[80:83], a[108:111], v[168:171]\n" \
"s_add_u32 m0, s50, 90112\n" \
"ds_read_b64_tr_b16 a[48:49], v9 offset:1024\n" \
"v_lshlrev_b32 v48, 16, v49\n" \
"v_mfma_f32_16x16x32_bf16 v[172:175], a[84:87], a[108:111], v[172:175]\n" \
"buffer_load_dwordx4 v19, s[28:31], s46 offen lds\n" \
"v_mfma_f32_16x16x32_bf16 v[176:179], a[88:91], a[104:107], v[176:179]\n" \
"ds_read_b64_tr_b16 a[50:51], v10 offset:1024\n" \
"v_add_f32 v23, v23, v23 row_ror:1 row_mask:0xf bank_mask:0xf\n" \
"v_cvt_pk_bf16_f32 v23, v23, v23\n" \
"v_mfma_f32_16x16x32_bf16 v[180:183], a[92:95], a[104:107], v[180:183]\n" \
"v_and_b32 v23, 0xffff0000, v23\n" \
"v_mul_f32 v23, 0xbfb8aa3b, v23\n" \
"v_mfma_f32_16x16x32_bf16 v[184:187], a[88:91], a[108:111], v[184:187]\n" \
"s_add_u32 m0, s50, 94208\n" \
"ds_read_b128 a[20:23], v5 offset:43008\n" \
"v_exp_f32 v23, v23\n" \
"v_mfma_f32_16x16x32_bf16 v[188:191], a[92:95], a[108:111], v[188:191]\n" \
"buffer_load_dwordx4 v20, s[28:31], s46 offen lds\n" \
"v_mfma_f32_16x16x32_bf16 v[192:195], a[64:67], a[112:115], v[192:195]\n" \
"ds_read_b64_tr_b16 a[52:53], v11 offset:1024\n" \
"v_and_b32 v49, 0xffff0000, v49\n" \
"v_accvgpr_read_b32 v36, a192\n" \
"v_mfma_f32_16x16x32_bf16 v[196:199], a[68:71], a[112:115], v[196:199]\n" \
"v_add_f32 v23, 1.0, v23\n" \
"v_rcp_f32 v23, v23\n" \
"v_mfma_f32_16x16x32_bf16 v[200:203], a[64:67], a[116:119], v[200:203]\n" \
"s_mov_b32 s47, 1835008\n" \
"s_add_u32 m0, s22, 49152\n" \
"ds_read_b64_tr_b16 a[54:55], v12 offset:1024\n" \
"v_mfma_f32_16x16x32_bf16 v[204:207], a[68:71], a[116:119], v[204:207]\n" \
"buffer_load_dwordx4 v21, s[32:35], s47 offen offset:0 lds\n" \
"v_mfma_f32_16x16x32_bf16 v[208:211], a[72:75], a[112:115], v[208:211]\n" \
"ds_read_b128 a[24:27], v5 offset:45056\n" \
"v_lshlrev_b32 v30, 16, v36\n" \
"v_and_b32 v36, 0xffff0000, v36\n" \
"v_mfma_f32_16x16x32_bf16 v[212:215], a[76:79], a[112:115], v[212:215]\n" \
"v_cvt_pk_bf16_f32 v23, v23, v23\n" \
"v_and_b32 v23, 0xffff0000, v23\n" \
"v_mfma_f32_16x16x32_bf16 v[216:219], a[72:75], a[116:119], v[216:219]\n" \
"s_add_u32 m0, s22, 50048\n" \
"ds_read_b64_tr_b16 a[56:57], v9 offset:1536\n" \
"v_accvgpr_read_b32 v37, a194\n" \
"v_mfma_f32_16x16x32_bf16 v[220:223], a[76:79], a[116:119], v[220:223]\n" \
"buffer_load_dwordx4 v21, s[32:35], s47 offen offset:128 lds\n" \
"v_mfma_f32_16x16x32_bf16 v[224:227], a[80:83], a[112:115], v[224:227]\n" \
"ds_read_b64_tr_b16 a[58:59], v10 offset:1536\n" \
"v_lshlrev_b32 v31, 16, v37\n" \
"v_and_b32 v37, 0xffff0000, v37\n" \
"v_mfma_f32_16x16x32_bf16 v[228:231], a[84:87], a[112:115], v[228:231]\n" \
"v_accvgpr_read_b32 v38, a196\n" \
"v_lshlrev_b32 v32, 16, v38\n" \
"v_mfma_f32_16x16x32_bf16 v[232:235], a[80:83], a[116:119], v[232:235]\n" \
"s_add_u32 m0, s22, 50944\n" \
"ds_read_b128 a[28:31], v5 offset:47104\n" \
"v_and_b32 v38, 0xffff0000, v38\n" \
"v_mfma_f32_16x16x32_bf16 v[236:239], a[84:87], a[116:119], v[236:239]\n" \
"buffer_load_dwordx4 v21, s[32:35], s47 offen offset:256 lds\n" \
"v_mfma_f32_16x16x32_bf16 v[240:243], a[88:91], a[112:115], v[240:243]\n" \
"ds_read_b64_tr_b16 a[60:61], v11 offset:1536\n" \
"v_accvgpr_read_b32 v39, a198\n" \
"v_lshlrev_b32 v33, 16, v39\n" \
"v_mfma_f32_16x16x32_bf16 v[244:247], a[92:95], a[112:115], v[244:247]\n" \
"v_and_b32 v39, 0xffff0000, v39\n" \
"v_mul_f32 v25, v30, v45\n" \
"v_mfma_f32_16x16x32_bf16 v[248:251], a[88:91], a[116:119], v[248:251]\n" \
"s_add_u32 m0, s22, 51840\n" \
"ds_read_b64_tr_b16 a[62:63], v12 offset:1536\n" \
"v_mul_f32 v26, v36, v49\n" \
"v_mfma_f32_16x16x32_bf16 v[252:255], a[92:95], a[116:119], v[252:255]\n" \
"buffer_load_dwordx4 v21, s[32:35], s47 offen offset:384 lds\n" \
"v_mfma_f32_16x16x32_bf16 a[128:131], a[64:67], a[120:123], a[128:131]\n" \
"v_fmac_f32 v25, v31, v44\n" \
"v_fmac_f32 v26, v37, v48\n" \
"v_mfma_f32_16x16x32_bf16 a[132:135], a[68:71], a[120:123], a[132:135]\n" \
"v_fmac_f32 v25, v32, v43\n" \
"v_fmac_f32 v26, v38, v47\n" \
"v_mfma_f32_16x16x32_bf16 a[136:139], a[64:67], a[124:127], a[136:139]\n" \
"v_fmac_f32 v25, v33, v42\n" \
"v_fmac_f32 v26, v39, v46\n" \
"v_mfma_f32_16x16x32_bf16 a[140:143], a[68:71], a[124:127], a[140:143]\n" \
"buffer_load_dwordx2 a[208:209], v59, s[40:43], s52 offen offset:2048\n" \
"v_mfma_f32_16x16x32_bf16 a[144:147], a[72:75], a[120:123], a[144:147]\n" \
"v_accvgpr_read_b32 v28, a214\n" \
"v_cvt_pk_bf16_f32 v26, v25, v26\n" \
"v_mfma_f32_16x16x32_bf16 a[148:151], a[76:79], a[120:123], a[148:151]\n" \
"v_lshlrev_b32 v27, 16, v28\n" \
"v_lshlrev_b32 v25, 16, v26\n" \
"v_mfma_f32_16x16x32_bf16 a[152:155], a[72:75], a[124:127], a[152:155]\n" \
"v_and_b32 v28, 0xffff0000, v28\n" \
"v_and_b32 v26, 0xffff0000, v26\n" \
"v_mfma_f32_16x16x32_bf16 a[156:159], a[76:79], a[124:127], a[156:159]\n" \
"s_waitcnt vmcnt(25) lgkmcnt(0)\n" \
"v_mul_f32 v29, v25, v27\n" \
"v_mul_f32 v58, v26, v28\n" \
"v_mfma_f32_16x16x32_bf16 a[160:163], a[80:83], a[120:123], a[160:163]\n" \
"s_barrier\n" \
"v_mfma_f32_16x16x32_bf16 a[164:167], a[84:87], a[120:123], a[164:167]\n" \
"v_accvgpr_read_b32 v40, a200\n" \
"v_lshlrev_b32 v34, 16, v40\n" \
"v_mfma_f32_16x16x32_bf16 a[168:171], a[80:83], a[124:127], a[168:171]\n" \
"v_and_b32 v40, 0xffff0000, v40\n" \
"v_mul_f32 v25, v31, v45\n" \
"v_mfma_f32_16x16x32_bf16 a[172:175], a[84:87], a[124:127], a[172:175]\n" \
"buffer_load_dwordx2 a[224:225], v59, s[80:83], s52 offen offset:2048\n" \
"v_mfma_f32_16x16x32_bf16 a[176:179], a[88:91], a[120:123], a[176:179]\n" \
"v_mul_f32 v26, v37, v49\n" \
"v_fmac_f32 v25, v32, v44\n" \
"v_mfma_f32_16x16x32_bf16 a[180:183], a[92:95], a[120:123], a[180:183]\n" \
"v_fmac_f32 v26, v38, v48\n" \
"v_fmac_f32 v25, v33, v43\n" \
"v_mfma_f32_16x16x32_bf16 a[184:187], a[88:91], a[124:127], a[184:187]\n" \
"s_mov_b32 s52, 12288\n" \
"v_fmac_f32 v26, v39, v47\n" \
"v_fmac_f32 v25, v34, v42\n" \
"v_mfma_f32_16x16x32_bf16 a[188:191], a[92:95], a[124:127], a[188:191]\n" \
"buffer_load_dwordx2 a[210:211], v59, s[40:43], s52 offen offset:0\n" \
"v_mfma_f32_16x16x32_bf16 v[64:67], a[0:3], a[32:35], v[64:67]\n" \
"ds_read_b128 a[64:67], v6 offset:32768\n" \
"v_fmac_f32 v26, v40, v46\n" \
"v_accvgpr_read_b32 v28, a216\n" \
"v_mfma_f32_16x16x32_bf16 v[68:71], a[4:7], a[32:35], v[68:71]\n" \
"v_cvt_pk_bf16_f32 v26, v25, v26\n" \
"v_lshlrev_b32 v27, 16, v28\n" \
"v_mfma_f32_16x16x32_bf16 v[72:75], a[0:3], a[36:39], v[72:75]\n" \
"ds_read_b64_tr_b16 a[96:97], v9 offset:16384\n" \
"v_lshlrev_b32 v25, 16, v26\n" \
"v_and_b32 v28, 0xffff0000, v28\n" \
"v_mfma_f32_16x16x32_bf16 v[76:79], a[4:7], a[36:39], v[76:79]\n" \
"buffer_load_dwordx2 a[226:227], v59, s[80:83], s52 offen offset:0\n" \
"v_mfma_f32_16x16x32_bf16 v[80:83], a[8:11], a[32:35], v[80:83]\n" \
"ds_read_b64_tr_b16 a[98:99], v10 offset:16384\n" \
"v_and_b32 v26, 0xffff0000, v26\n" \
"v_mul_f32 v25, v25, v27\n" \
"v_mfma_f32_16x16x32_bf16 v[84:87], a[12:15], a[32:35], v[84:87]\n" \
"v_mul_f32 v26, v26, v28\n" \
"v_cvt_pk_bf16_f32 v53, v25, v29\n" \
"v_mfma_f32_16x16x32_bf16 v[88:91], a[8:11], a[36:39], v[88:91]\n" \
"ds_read_b128 a[68:71], v6 offset:34816\n" \
"v_cvt_pk_bf16_f32 v57, v26, v58\n" \
"v_accvgpr_read_b32 v41, a202\n" \
"v_mfma_f32_16x16x32_bf16 v[92:95], a[12:15], a[36:39], v[92:95]\n" \
"buffer_load_dwordx2 a[212:213], v59, s[40:43], s52 offen offset:2048\n" \
"v_mfma_f32_16x16x32_bf16 v[96:99], a[16:19], a[32:35], v[96:99]\n" \
"ds_read_b64_tr_b16 a[100:101], v11 offset:16384\n" \
"v_lshlrev_b32 v35, 16, v41\n" \
"v_and_b32 v41, 0xffff0000, v41\n" \
"v_mfma_f32_16x16x32_bf16 v[100:103], a[20:23], a[32:35], v[100:103]\n" \
"v_mul_f32 v25, v32, v45\n" \
"v_mul_f32 v26, v38, v49\n" \
"v_mfma_f32_16x16x32_bf16 v[104:107], a[16:19], a[36:39], v[104:107]\n" \
"ds_read_b64_tr_b16 a[102:103], v12 offset:16384\n" \
"v_fmac_f32 v25, v33, v44\n" \
"v_fmac_f32 v26, v39, v48\n" \
"v_mfma_f32_16x16x32_bf16 v[108:111], a[20:23], a[36:39], v[108:111]\n" \
"buffer_load_dwordx2 a[228:229], v59, s[80:83], s52 offen offset:2048\n" \
"v_mfma_f32_16x16x32_bf16 v[112:115], a[24:27], a[32:35], v[112:115]\n" \
"ds_read_b128 a[72:75], v6 offset:36864\n" \
"v_fmac_f32 v25, v34, v43\n" \
"v_fmac_f32 v26, v40, v47\n" \
"v_mfma_f32_16x16x32_bf16 v[116:119], a[28:31], a[32:35], v[116:119]\n" \
"v_fmac_f32 v25, v35, v42\n" \
"v_fmac_f32 v26, v41, v46\n" \
"v_mfma_f32_16x16x32_bf16 v[120:123], a[24:27], a[36:39], v[120:123]\n" \
"s_mov_b32 s46, 512\n" \
"s_add_u32 m0, s50, 0\n" \
"ds_read_b64_tr_b16 a[104:105], v9 offset:16896\n" \
"v_mfma_f32_16x16x32_bf16 v[124:127], a[28:31], a[36:39], v[124:127]\n" \
"buffer_load_dwordx4 v13, s[28:31], s46 offen lds\n" \
"v_mfma_f32_16x16x32_bf16 v[128:131], a[0:3], a[40:43], v[128:131]\n" \
"ds_read_b64_tr_b16 a[106:107], v10 offset:16896\n" \
"v_accvgpr_read_b32 v28, a218\n" \
"v_cvt_pk_bf16_f32 v26, v25, v26\n" \
"v_mfma_f32_16x16x32_bf16 v[132:135], a[4:7], a[40:43], v[132:135]\n" \
"v_lshlrev_b32 v27, 16, v28\n" \
"v_lshlrev_b32 v25, 16, v26\n" \
"v_mfma_f32_16x16x32_bf16 v[136:139], a[0:3], a[44:47], v[136:139]\n" \
"s_add_u32 m0, s50, 4096\n" \
"ds_read_b128 a[76:79], v6 offset:38912\n" \
"v_and_b32 v28, 0xffff0000, v28\n" \
"v_mfma_f32_16x16x32_bf16 v[140:143], a[4:7], a[44:47], v[140:143]\n" \
"buffer_load_dwordx4 v14, s[28:31], s46 offen lds\n" \
"v_mfma_f32_16x16x32_bf16 v[144:147], a[8:11], a[40:43], v[144:147]\n" \
"ds_read_b64_tr_b16 a[108:109], v11 offset:16896\n" \
"v_and_b32 v26, 0xffff0000, v26\n" \
"v_mul_f32 v29, v25, v27\n" \
"v_mfma_f32_16x16x32_bf16 v[148:151], a[12:15], a[40:43], v[148:151]\n" \
"v_mul_f32 v58, v26, v28\n" \
"v_accvgpr_read_b32 v36, a204\n" \
"v_mfma_f32_16x16x32_bf16 v[152:155], a[8:11], a[44:47], v[152:155]\n" \
"s_add_u32 m0, s50, 8192\n" \
"ds_read_b64_tr_b16 a[110:111], v12 offset:16896\n" \
"v_lshlrev_b32 v30, 16, v36\n" \
"v_mfma_f32_16x16x32_bf16 v[156:159], a[12:15], a[44:47], v[156:159]\n" \
"buffer_load_dwordx4 v15, s[28:31], s46 offen lds\n" \
"v_mfma_f32_16x16x32_bf16 v[160:163], a[16:19], a[40:43], v[160:163]\n" \
"ds_read_b128 a[80:83], v6 offset:40960\n" \
"v_and_b32 v36, 0xffff0000, v36\n" \
"v_mul_f32 v25, v33, v45\n" \
"v_mfma_f32_16x16x32_bf16 v[164:167], a[20:23], a[40:43], v[164:167]\n" \
"v_mul_f32 v26, v39, v49\n" \
"v_fmac_f32 v25, v34, v44\n" \
"v_mfma_f32_16x16x32_bf16 v[168:171], a[16:19], a[44:47], v[168:171]\n" \
"s_add_u32 m0, s50, 12288\n" \
"ds_read_b64_tr_b16 a[112:113], v9 offset:17408\n" \
"v_fmac_f32 v26, v40, v48\n" \
"v_mfma_f32_16x16x32_bf16 v[172:175], a[20:23], a[44:47], v[172:175]\n" \
"buffer_load_dwordx4 v16, s[28:31], s46 offen lds\n" \
"v_mfma_f32_16x16x32_bf16 v[176:179], a[24:27], a[40:43], v[176:179]\n" \
"ds_read_b64_tr_b16 a[114:115], v10 offset:17408\n" \
"v_fmac_f32 v25, v35, v43\n" \
"v_fmac_f32 v26, v41, v47\n" \
"v_mfma_f32_16x16x32_bf16 v[180:183], a[28:31], a[40:43], v[180:183]\n" \
"v_fmac_f32 v25, v30, v42\n" \
"v_fmac_f32 v26, v36, v46\n" \
"v_mfma_f32_16x16x32_bf16 v[184:187], a[24:27], a[44:47], v[184:187]\n" \
"s_mov_b32 s46, 512\n" \
"s_add_u32 m0, s50, 16384\n" \
"ds_read_b128 a[84:87], v6 offset:43008\n" \
"v_mfma_f32_16x16x32_bf16 v[188:191], a[28:31], a[44:47], v[188:191]\n" \
"buffer_load_dwordx4 v17, s[28:31], s46 offen lds\n" \
"v_mfma_f32_16x16x32_bf16 v[192:195], a[0:3], a[48:51], v[192:195]\n" \
"ds_read_b64_tr_b16 a[116:117], v11 offset:17408\n" \
"v_accvgpr_read_b32 v28, a220\n" \
"v_cvt_pk_bf16_f32 v26, v25, v26\n" \
"v_mfma_f32_16x16x32_bf16 v[196:199], a[4:7], a[48:51], v[196:199]\n" \
"v_lshlrev_b32 v27, 16, v28\n" \
"v_lshlrev_b32 v25, 16, v26\n" \
"v_mfma_f32_16x16x32_bf16 v[200:203], a[0:3], a[52:55], v[200:203]\n" \
"s_add_u32 m0, s50, 20480\n" \
"ds_read_b64_tr_b16 a[118:119], v12 offset:17408\n" \
"v_and_b32 v28, 0xffff0000, v28\n" \
"v_mfma_f32_16x16x32_bf16 v[204:207], a[4:7], a[52:55], v[204:207]\n" \
"buffer_load_dwordx4 v18, s[28:31], s46 offen lds\n" \
"v_mfma_f32_16x16x32_bf16 v[208:211], a[8:11], a[48:51], v[208:211]\n" \
"ds_read_b128 a[88:91], v6 offset:45056\n" \
"v_and_b32 v26, 0xffff0000, v26\n" \
"v_mul_f32 v25, v25, v27\n" \
"v_mfma_f32_16x16x32_bf16 v[212:215], a[12:15], a[48:51], v[212:215]\n" \
"v_mul_f32 v26, v26, v28\n" \
"v_cvt_pk_bf16_f32 v52, v25, v29\n" \
"v_mfma_f32_16x16x32_bf16 v[216:219], a[8:11], a[52:55], v[216:219]\n" \
"s_add_u32 m0, s50, 24576\n" \
"ds_read_b64_tr_b16 a[120:121], v9 offset:17920\n" \
"v_cvt_pk_bf16_f32 v56, v26, v58\n" \
"v_mfma_f32_16x16x32_bf16 v[220:223], a[12:15], a[52:55], v[220:223]\n" \
"buffer_load_dwordx4 v19, s[28:31], s46 offen lds\n" \
"v_mfma_f32_16x16x32_bf16 v[224:227], a[16:19], a[48:51], v[224:227]\n" \
"ds_read_b64_tr_b16 a[122:123], v10 offset:17920\n" \
"v_mfma_f32_16x16x32_bf16 v[228:231], a[20:23], a[48:51], v[228:231]\n" \
"v_mfma_f32_16x16x32_bf16 v[232:235], a[16:19], a[52:55], v[232:235]\n" \
"s_add_u32 m0, s50, 28672\n" \
"ds_read_b128 a[92:95], v6 offset:47104\n" \
"v_mfma_f32_16x16x32_bf16 v[236:239], a[20:23], a[52:55], v[236:239]\n" \
"buffer_load_dwordx4 v20, s[28:31], s46 offen lds\n" \
"v_mfma_f32_16x16x32_bf16 v[240:243], a[24:27], a[48:51], v[240:243]\n" \
"ds_read_b64_tr_b16 a[124:125], v11 offset:17920\n" \
"v_mfma_f32_16x16x32_bf16 v[244:247], a[28:31], a[48:51], v[244:247]\n" \
"v_mfma_f32_16x16x32_bf16 v[248:251], a[24:27], a[52:55], v[248:251]\n" \
"s_mov_b32 s47, 2097152\n" \
"s_add_u32 m0, s22, 0\n" \
"ds_read_b64_tr_b16 a[126:127], v12 offset:17920\n" \
"v_mfma_f32_16x16x32_bf16 v[252:255], a[28:31], a[52:55], v[252:255]\n" \
"buffer_load_dwordx4 v21, s[32:35], s47 offen offset:0 lds\n" \
"v_mfma_f32_16x16x32_bf16 a[128:131], a[0:3], a[56:59], a[128:131]\n" \
"v_mfma_f32_16x16x32_bf16 a[132:135], a[4:7], a[56:59], a[132:135]\n" \
"v_mfma_f32_16x16x32_bf16 a[136:139], a[0:3], a[60:63], a[136:139]\n" \
"s_add_u32 m0, s22, 896\n" \
"v_mfma_f32_16x16x32_bf16 a[140:143], a[4:7], a[60:63], a[140:143]\n" \
"buffer_load_dwordx4 v21, s[32:35], s47 offen offset:128 lds\n" \
"v_mfma_f32_16x16x32_bf16 a[144:147], a[8:11], a[56:59], a[144:147]\n" \
"v_mfma_f32_16x16x32_bf16 a[148:151], a[12:15], a[56:59], a[148:151]\n" \
"v_mfma_f32_16x16x32_bf16 a[152:155], a[8:11], a[60:63], a[152:155]\n" \
"s_waitcnt vmcnt(10)\n" \
"v_accvgpr_read_b32 v37, a206\n" \
"v_lshlrev_b32 v31, 16, v37\n" \
"v_mfma_f32_16x16x32_bf16 a[156:159], a[12:15], a[60:63], a[156:159]\n" \
"s_waitcnt vmcnt(20) lgkmcnt(0)\n" \
"v_and_b32 v37, 0xffff0000, v37\n" \
"v_mul_f32 v25, v34, v45\n" \
"v_mfma_f32_16x16x32_bf16 a[160:163], a[16:19], a[56:59], a[160:163]\n" \
"s_barrier\n" \
"v_mfma_f32_16x16x32_bf16 a[164:167], a[20:23], a[56:59], a[164:167]\n" \
"v_mul_f32 v26, v40, v49\n" \
"v_fmac_f32 v25, v35, v44\n" \
"v_mfma_f32_16x16x32_bf16 a[168:171], a[16:19], a[60:63], a[168:171]\n" \
"s_add_u32 m0, s22, 1792\n" \
"v_fmac_f32 v26, v41, v48\n" \
"v_fmac_f32 v25, v30, v43\n" \
"v_mfma_f32_16x16x32_bf16 a[172:175], a[20:23], a[60:63], a[172:175]\n" \
"buffer_load_dwordx4 v21, s[32:35], s47 offen offset:256 lds\n" \
"v_mfma_f32_16x16x32_bf16 a[176:179], a[24:27], a[56:59], a[176:179]\n" \
"s_add_u32 m0, s22, 2688\n" \
"v_fmac_f32 v26, v36, v47\n" \
"v_fmac_f32 v25, v31, v42\n" \
"v_mfma_f32_16x16x32_bf16 a[180:183], a[28:31], a[56:59], a[180:183]\n" \
"buffer_load_dwordx4 v21, s[32:35], s47 offen offset:384 lds\n" \
"v_mfma_f32_16x16x32_bf16 a[184:187], a[24:27], a[60:63], a[184:187]\n" \
"s_add_u32 s84, s84, 1048576\ns_addc_u32 s85, s85, 0\n" \
"v_fmac_f32 v26, v37, v46\n" \
"v_accvgpr_read_b32 v28, a222\n" \
"v_mfma_f32_16x16x32_bf16 a[188:191], a[28:31], a[60:63], a[188:191]\n" \
"s_mov_b64 exec, 0xffff\nglobal_load_dword a244, v1, s[84:85]\ns_mov_b64 exec, -1\n" \
"v_mfma_f32_16x16x32_bf16 v[64:67], a[64:67], a[96:99], v[64:67]\n" \
"ds_read_b128 a[0:3], v7 offset:0\n" \
"v_cvt_pk_bf16_f32 v26, v25, v26\n" \
"v_lshlrev_b32 v27, 16, v28\n" \
"v_mfma_f32_16x16x32_bf16 v[68:71], a[68:71], a[96:99], v[68:71]\n" \
"v_lshlrev_b32 v25, 16, v26\n" \
"v_and_b32 v28, 0xffff0000, v28\n" \
"v_mfma_f32_16x16x32_bf16 v[72:75], a[64:67], a[100:103], v[72:75]\n" \
"s_mov_b32 s47, 2359296\n" \
"s_add_u32 m0, s22, 16384\n" \
"ds_read_b64_tr_b16 a[32:33], v9 offset:32768\n" \
"v_mfma_f32_16x16x32_bf16 v[76:79], a[68:71], a[100:103], v[76:79]\n" \
"buffer_load_dwordx4 v21, s[32:35], s47 offen offset:0 lds\n" \
"v_mfma_f32_16x16x32_bf16 v[80:83], a[72:75], a[96:99], v[80:83]\n" \
"ds_read_b64_tr_b16 a[34:35], v10 offset:32768\n" \
"v_and_b32 v26, 0xffff0000, v26\n" \
"v_mul_f32 v29, v25, v27\n" \
"v_mfma_f32_16x16x32_bf16 v[84:87], a[76:79], a[96:99], v[84:87]\n" \
"v_mul_f32 v58, v26, v28\n" \
"v_accvgpr_read_b32 v38, a208\n" \
"v_mfma_f32_16x16x32_bf16 v[88:91], a[72:75], a[100:103], v[88:91]\n" \
"ds_read_b128 a[4:7], v7 offset:2048\n" \
"v_lshlrev_b32 v32, 16, v38\n" \
"v_and_b32 v38, 0xffff0000, v38\n" \
"v_mfma_f32_16x16x32_bf16 v[92:95], a[76:79], a[100:103], v[92:95]\n" \
"v_mul_f32 v25, v35, v45\n" \
"v_mul_f32 v26, v41, v49\n" \
"v_mfma_f32_16x16x32_bf16 v[96:99], a[80:83], a[96:99], v[96:99]\n" \
"s_mov_b32 s46, 640\n" \
"s_add_u32 m0, s50, 32768\n" \
"ds_read_b64_tr_b16 a[36:37], v11 offset:32768\n" \
"v_mfma_f32_16x16x32_bf16 v[100:103], a[84:87], a[96:99], v[100:103]\n" \
"buffer_load_dwordx4 v13, s[28:31], s46 offen lds\n" \
"v_mfma_f32_16x16x32_bf16 v[104:107], a[80:83], a[100:103], v[104:107]\n" \
"ds_read_b64_tr_b16 a[38:39], v12 offset:32768\n" \
"v_fmac_f32 v25, v30, v44\n" \
"v_fmac_f32 v26, v36, v48\n" \
"v_mfma_f32_16x16x32_bf16 v[108:111], a[84:87], a[100:103], v[108:111]\n" \
"v_fmac_f32 v25, v31, v43\n" \
"v_fmac_f32 v26, v37, v47\n" \
"v_mfma_f32_16x16x32_bf16 v[112:115], a[88:91], a[96:99], v[112:115]\n" \
"ds_read_b128 a[8:11], v7 offset:4096\n" \
"v_fmac_f32 v25, v32, v42\n" \
"v_fmac_f32 v26, v38, v46\n" \
"v_mfma_f32_16x16x32_bf16 v[116:119], a[92:95], a[96:99], v[116:119]\n" \
"v_accvgpr_read_b32 v28, a224\n" \
"v_cvt_pk_bf16_f32 v26, v25, v26\n" \
"v_mfma_f32_16x16x32_bf16 v[120:123], a[88:91], a[100:103], v[120:123]\n" \
"s_add_u32 m0, s22, 17280\n" \
"ds_read_b64_tr_b16 a[40:41], v9 offset:33280\n" \
"v_lshlrev_b32 v27, 16, v28\n" \
"v_mfma_f32_16x16x32_bf16 v[124:127], a[92:95], a[100:103], v[124:127]\n" \
"buffer_load_dwordx4 v21, s[32:35], s47 offen offset:128 lds\n" \
"v_mfma_f32_16x16x32_bf16 v[128:131], a[64:67], a[104:107], v[128:131]\n" \
"ds_read_b64_tr_b16 a[42:43], v10 offset:33280\n" \
"v_lshlrev_b32 v25, 16, v26\n" \
"v_and_b32 v28, 0xffff0000, v28\n" \
"v_mfma_f32_16x16x32_bf16 v[132:135], a[68:71], a[104:107], v[132:135]\n" \
"v_and_b32 v26, 0xffff0000, v26\n" \
"v_mul_f32 v25, v25, v27\n" \
"v_mfma_f32_16x16x32_bf16 v[136:139], a[64:67], a[108:111], v[136:139]\n" \
"ds_read_b128 a[12:15], v7 offset:6144\n" \
"v_mul_f32 v26, v26, v28\n" \
"v_cvt_pk_bf16_f32 v51, v25, v29\n" \
"v_mfma_f32_16x16x32_bf16 v[140:143], a[68:71], a[108:111], v[140:143]\n" \
"v_cvt_pk_bf16_f32 v55, v26, v58\n" \
"v_accvgpr_read_b32 v39, a210\n" \
"v_mfma_f32_16x16x32_bf16 v[144:147], a[72:75], a[104:107], v[144:147]\n" \
"s_add_u32 m0, s50, 36864\n" \
"ds_read_b64_tr_b16 a[44:45], v11 offset:33280\n" \
"v_lshlrev_b32 v33, 16, v39\n" \
"v_mfma_f32_16x16x32_bf16 v[148:151], a[76:79], a[104:107], v[148:151]\n" \
"buffer_load_dwordx4 v14, s[28:31], s46 offen lds\n" \
"v_mfma_f32_16x16x32_bf16 v[152:155], a[72:75], a[108:111], v[152:155]\n" \
"ds_read_b64_tr_b16 a[46:47], v12 offset:33280\n" \
"v_and_b32 v39, 0xffff0000, v39\n" \
"v_mul_f32 v25, v30, v45\n" \
"v_mfma_f32_16x16x32_bf16 v[156:159], a[76:79], a[108:111], v[156:159]\n" \
"v_mul_f32 v26, v36, v49\n" \
"v_fmac_f32 v25, v31, v44\n" \
"v_mfma_f32_16x16x32_bf16 v[160:163], a[80:83], a[104:107], v[160:163]\n" \
"ds_read_b128 a[16:19], v7 offset:8192\n" \
"v_fmac_f32 v26, v37, v48\n" \
"v_fmac_f32 v25, v32, v43\n" \
"v_mfma_f32_16x16x32_bf16 v[164:167], a[84:87], a[104:107], v[164:167]\n" \
"v_fmac_f32 v26, v38, v47\n" \
"v_fmac_f32 v25, v33, v42\n" \
"v_mfma_f32_16x16x32_bf16 v[168:171], a[80:83], a[108:111], v[168:171]\n" \
"s_add_u32 m0, s22, 18176\n" \
"ds_read_b64_tr_b16 a[48:49], v9 offset:33792\n" \
"v_fmac_f32 v26, v39, v46\n" \
"v_mfma_f32_16x16x32_bf16 v[172:175], a[84:87], a[108:111], v[172:175]\n" \
"buffer_load_dwordx4 v21, s[32:35], s47 offen offset:256 lds\n" \
"v_mfma_f32_16x16x32_bf16 v[176:179], a[88:91], a[104:107], v[176:179]\n" \
"ds_read_b64_tr_b16 a[50:51], v10 offset:33792\n" \
"v_accvgpr_read_b32 v28, a226\n" \
"v_cvt_pk_bf16_f32 v26, v25, v26\n" \
"v_mfma_f32_16x16x32_bf16 v[180:183], a[92:95], a[104:107], v[180:183]\n" \
"v_lshlrev_b32 v27, 16, v28\n" \
"v_lshlrev_b32 v25, 16, v26\n" \
"v_mfma_f32_16x16x32_bf16 v[184:187], a[88:91], a[108:111], v[184:187]\n" \
"ds_read_b128 a[20:23], v7 offset:10240\n" \
"v_and_b32 v28, 0xffff0000, v28\n" \
"v_and_b32 v26, 0xffff0000, v26\n" \
"v_mfma_f32_16x16x32_bf16 v[188:191], a[92:95], a[108:111], v[188:191]\n" \
"LH5_%=:\ns_cbranch_execz LH9_%=\n" \
"v_mul_f32 v29, v25, v27\n" \
"v_mul_f32 v58, v26, v28\n" \
"v_mfma_f32_16x16x32_bf16 v[192:195], a[64:67], a[112:115], v[192:195]\n" \
"s_add_u32 m0, s50, 40960\n" \
"ds_read_b64_tr_b16 a[52:53], v11 offset:33792\n" \
"v_accvgpr_read_b32 v40, a212\n" \
"v_mfma_f32_16x16x32_bf16 v[196:199], a[68:71], a[112:115], v[196:199]\n" \
"buffer_load_dwordx4 v15, s[28:31], s46 offen lds\n" \
"v_mfma_f32_16x16x32_bf16 v[200:203], a[64:67], a[116:119], v[200:203]\n" \
"ds_read_b64_tr_b16 a[54:55], v12 offset:33792\n" \
"v_lshlrev_b32 v34, 16, v40\n" \
"v_and_b32 v40, 0xffff0000, v40\n" \
"v_mfma_f32_16x16x32_bf16 v[204:207], a[68:71], a[116:119], v[204:207]\n" \
"v_mul_f32 v25, v31, v45\n" \
"v_mul_f32 v26, v37, v49\n" \
"v_mfma_f32_16x16x32_bf16 v[208:211], a[72:75], a[112:115], v[208:211]\n" \
"ds_read_b128 a[24:27], v7 offset:12288\n" \
"v_fmac_f32 v25, v32, v44\n" \
"v_fmac_f32 v26, v38, v48\n" \
"v_mfma_f32_16x16x32_bf16 v[212:215], a[76:79], a[112:115], v[212:215]\n" \
"v_fmac_f32 v25, v33, v43\n" \
"v_fmac_f32 v26, v39, v47\n" \
"v_mfma_f32_16x16x32_bf16 v[216:219], a[72:75], a[116:119], v[216:219]\n" \
"s_add_u32 m0, s22, 19072\n" \
"ds_read_b64_tr_b16 a[56:57], v9 offset:34304\n" \
"v_fmac_f32 v25, v34, v42\n" \
"v_mfma_f32_16x16x32_bf16 v[220:223], a[76:79], a[116:119], v[220:223]\n" \
"buffer_load_dwordx4 v21, s[32:35], s47 offen offset:384 lds\n" \
"v_mfma_f32_16x16x32_bf16 v[224:227], a[80:83], a[112:115], v[224:227]\n" \
"ds_read_b64_tr_b16 a[58:59], v10 offset:34304\n" \
"v_fmac_f32 v26, v40, v46\n" \
"v_accvgpr_read_b32 v28, a228\n" \
"v_mfma_f32_16x16x32_bf16 v[228:231], a[84:87], a[112:115], v[228:231]\n" \
"v_cvt_pk_bf16_f32 v26, v25, v26\n" \
"v_lshlrev_b32 v27, 16, v28\n" \
"v_mfma_f32_16x16x32_bf16 v[232:235], a[80:83], a[116:119], v[232:235]\n" \
"ds_read_b128 a[28:31], v7 offset:14336\n" \
"v_lshlrev_b32 v25, 16, v26\n" \
"v_and_b32 v28, 0xffff0000, v28\n" \
"v_mfma_f32_16x16x32_bf16 v[236:239], a[84:87], a[116:119], v[236:239]\n" \
"v_and_b32 v26, 0xffff0000, v26\n" \
"v_mul_f32 v25, v25, v27\n" \
"v_mfma_f32_16x16x32_bf16 v[240:243], a[88:91], a[112:115], v[240:243]\n" \
"s_add_u32 m0, s50, 45056\n" \
"ds_read_b64_tr_b16 a[60:61], v11 offset:34304\n" \
"v_mul_f32 v26, v26, v28\n" \
"v_mfma_f32_16x16x32_bf16 v[244:247], a[92:95], a[112:115], v[244:247]\n" \
"buffer_load_dwordx4 v16, s[28:31], s46 offen lds\n" \
"v_mfma_f32_16x16x32_bf16 v[248:251], a[88:91], a[116:119], v[248:251]\n" \
"ds_read_b64_tr_b16 a[62:63], v12 offset:34304\n" \
"v_cvt_pk_bf16_f32 v50, v25, v29\n" \
"v_cvt_pk_bf16_f32 v54, v26, v58\n" \
"v_mfma_f32_16x16x32_bf16 v[252:255], a[92:95], a[116:119], v[252:255]\n" \
"v_accvgpr_read_b32 v43, a234\n" \
"v_lshlrev_b32 v42, 16, v43\n" \
"v_mfma_f32_16x16x32_bf16 a[128:131], a[64:67], a[120:123], a[128:131]\n" \
"v_and_b32 v43, 0xffff0000, v43\n" \
"v_accvgpr_read_b32 v45, a235\n" \
"v_mfma_f32_16x16x32_bf16 a[132:135], a[68:71], a[120:123], a[132:135]\n" \
"buffer_store_dword v23, v61, s[36:39], 0 offen sc1\n" \
"s_mov_b32 s94, 79691776\n" \
"v_mfma_f32_16x16x32_bf16 a[136:139], a[64:67], a[124:127], a[136:139]\n" \
"v_lshlrev_b32 v44, 16, v45\n" \
"v_and_b32 v45, 0xffff0000, v45\n" \
"v_mfma_f32_16x16x32_bf16 a[140:143], a[68:71], a[124:127], a[140:143]\n" \
"v_accvgpr_read_b32 v47, a236\n" \
"v_lshlrev_b32 v46, 16, v47\n" \
"v_mfma_f32_16x16x32_bf16 a[144:147], a[72:75], a[120:123], a[144:147]\n" \
"v_and_b32 v47, 0xffff0000, v47\n" \
"v_accvgpr_read_b32 v49, a237\n" \
"v_mfma_f32_16x16x32_bf16 a[148:151], a[76:79], a[120:123], a[148:151]\n" \
"buffer_store_dwordx4 v[50:53], v60, s[36:39], s94 offen offset:0 sc1\n" \
"s_mov_b32 s95, 79695872\n" \
"v_mfma_f32_16x16x32_bf16 a[152:155], a[72:75], a[124:127], a[152:155]\n" \
"v_lshlrev_b32 v48, 16, v49\n" \
"v_and_b32 v49, 0xffff0000, v49\n" \
"v_mfma_f32_16x16x32_bf16 a[156:159], a[76:79], a[124:127], a[156:159]\n" \
"s_waitcnt vmcnt(29) lgkmcnt(0)\n" \
"v_accvgpr_read_b32 v36, a193\n" \
"v_lshlrev_b32 v30, 16, v36\n" \
"v_mfma_f32_16x16x32_bf16 a[160:163], a[80:83], a[120:123], a[160:163]\n" \
"s_barrier\n" \
"v_mfma_f32_16x16x32_bf16 a[164:167], a[84:87], a[120:123], a[164:167]\n" \
"v_and_b32 v36, 0xffff0000, v36\n" \
"v_accvgpr_read_b32 v37, a195\n" \
"v_mfma_f32_16x16x32_bf16 a[168:171], a[80:83], a[124:127], a[168:171]\n" \
"v_lshlrev_b32 v31, 16, v37\n" \
"v_and_b32 v37, 0xffff0000, v37\n" \
"v_mfma_f32_16x16x32_bf16 a[172:175], a[84:87], a[124:127], a[172:175]\n" \
"buffer_store_dwordx4 v[54:57], v60, s[36:39], s95 offen offset:0 sc1\n" \
"v_mfma_f32_16x16x32_bf16 a[176:179], a[88:91], a[120:123], a[176:179]\n" \
"v_accvgpr_read_b32 v38, a197\n" \
"v_lshlrev_b32 v32, 16, v38\n" \
"v_mfma_f32_16x16x32_bf16 a[180:183], a[92:95], a[120:123], a[180:183]\n" \
"v_and_b32 v38, 0xffff0000, v38\n" \
"v_accvgpr_read_b32 v39, a199\n" \
"v_mfma_f32_16x16x32_bf16 a[184:187], a[88:91], a[124:127], a[184:187]\n" \
"v_lshlrev_b32 v33, 16, v39\n" \
"v_and_b32 v39, 0xffff0000, v39\n" \
"v_mfma_f32_16x16x32_bf16 a[188:191], a[92:95], a[124:127], a[188:191]\n" \
"v_mul_f32 v25, v30, v45\n" \
"v_mul_f32 v26, v36, v49\n" \
"v_mfma_f32_16x16x32_bf16 v[64:67], a[0:3], a[32:35], v[64:67]\n" \
"ds_read_b128 a[64:67], v8 offset:0\n" \
"v_fmac_f32 v25, v31, v44\n" \
"v_fmac_f32 v26, v37, v48\n" \
"v_mfma_f32_16x16x32_bf16 v[68:71], a[4:7], a[32:35], v[68:71]\n" \
"v_fmac_f32 v25, v32, v43\n" \
"v_fmac_f32 v26, v38, v47\n" \
"v_mfma_f32_16x16x32_bf16 v[72:75], a[0:3], a[36:39], v[72:75]\n" \
"s_mov_b32 s47, 2621440\n" \
"s_add_u32 m0, s22, 32768\n" \
"ds_read_b64_tr_b16 a[96:97], v9 offset:49152\n" \
"v_mfma_f32_16x16x32_bf16 v[76:79], a[4:7], a[36:39], v[76:79]\n" \
"buffer_load_dwordx4 v21, s[32:35], s47 offen offset:0 lds\n" \
"v_mfma_f32_16x16x32_bf16 v[80:83], a[8:11], a[32:35], v[80:83]\n" \
"ds_read_b64_tr_b16 a[98:99], v10 offset:49152\n" \
"v_fmac_f32 v25, v33, v42\n" \
"v_fmac_f32 v26, v39, v46\n" \
"v_mfma_f32_16x16x32_bf16 v[84:87], a[12:15], a[32:35], v[84:87]\n" \
"v_accvgpr_read_b32 v28, a215\n" \
"v_cvt_pk_bf16_f32 v26, v25, v26\n" \
"v_mfma_f32_16x16x32_bf16 v[88:91], a[8:11], a[36:39], v[88:91]\n" \
"ds_read_b128 a[68:71], v8 offset:2048\n" \
"v_lshlrev_b32 v27, 16, v28\n" \
"v_lshlrev_b32 v25, 16, v26\n" \
"v_mfma_f32_16x16x32_bf16 v[92:95], a[12:15], a[36:39], v[92:95]\n" \
"v_and_b32 v28, 0xffff0000, v28\n" \
"v_and_b32 v26, 0xffff0000, v26\n" \
"v_mfma_f32_16x16x32_bf16 v[96:99], a[16:19], a[32:35], v[96:99]\n" \
"s_mov_b32 s46, 640\n" \
"s_add_u32 m0, s50, 49152\n" \
"ds_read_b64_tr_b16 a[100:101], v11 offset:49152\n" \
"v_mfma_f32_16x16x32_bf16 v[100:103], a[20:23], a[32:35], v[100:103]\n" \
"buffer_load_dwordx4 v17, s[28:31], s46 offen lds\n" \
"v_mfma_f32_16x16x32_bf16 v[104:107], a[16:19], a[36:39], v[104:107]\n" \
"ds_read_b64_tr_b16 a[102:103], v12 offset:49152\n" \
"v_mul_f32 v29, v25, v27\n" \
"v_mul_f32 v58, v26, v28\n" \
"v_mfma_f32_16x16x32_bf16 v[108:111], a[20:23], a[36:39], v[108:111]\n" \
"v_accvgpr_read_b32 v40, a201\n" \
"v_lshlrev_b32 v34, 16, v40\n" \
"v_mfma_f32_16x16x32_bf16 v[112:115], a[24:27], a[32:35], v[112:115]\n" \
"ds_read_b128 a[72:75], v8 offset:4096\n" \
"v_and_b32 v40, 0xffff0000, v40\n" \
"v_mul_f32 v25, v31, v45\n" \
"v_mfma_f32_16x16x32_bf16 v[116:119], a[28:31], a[32:35], v[116:119]\n" \
"v_mul_f32 v26, v37, v49\n" \
"v_fmac_f32 v25, v32, v44\n" \
"v_mfma_f32_16x16x32_bf16 v[120:123], a[24:27], a[36:39], v[120:123]\n" \
"s_add_u32 m0, s22, 33664\n" \
"ds_read_b64_tr_b16 a[104:105], v9 offset:49664\n" \
"v_fmac_f32 v26, v38, v48\n" \
"v_mfma_f32_16x16x32_bf16 v[124:127], a[28:31], a[36:39], v[124:127]\n" \
"buffer_load_dwordx4 v21, s[32:35], s47 offen offset:128 lds\n" \
"v_mfma_f32_16x16x32_bf16 v[128:131], a[0:3], a[40:43], v[128:131]\n" \
"ds_read_b64_tr_b16 a[106:107], v10 offset:49664\n" \
"v_fmac_f32 v25, v33, v43\n" \
"v_fmac_f32 v26, v39, v47\n" \
"v_mfma_f32_16x16x32_bf16 v[132:135], a[4:7], a[40:43], v[132:135]\n" \
"v_fmac_f32 v25, v34, v42\n" \
"v_fmac_f32 v26, v40, v46\n" \
"v_mfma_f32_16x16x32_bf16 v[136:139], a[0:3], a[44:47], v[136:139]\n" \
"ds_read_b128 a[76:79], v8 offset:6144\n" \
"v_accvgpr_read_b32 v28, a217\n" \
"v_cvt_pk_bf16_f32 v26, v25, v26\n" \
"v_mfma_f32_16x16x32_bf16 v[140:143], a[4:7], a[44:47], v[140:143]\n" \
"v_lshlrev_b32 v27, 16, v28\n" \
"v_lshlrev_b32 v25, 16, v26\n" \
"v_mfma_f32_16x16x32_bf16 v[144:147], a[8:11], a[40:43], v[144:147]\n" \
"s_add_u32 m0, s50, 53248\n" \
"ds_read_b64_tr_b16 a[108:109], v11 offset:49664\n" \
"v_and_b32 v28, 0xffff0000, v28\n" \
"v_mfma_f32_16x16x32_bf16 v[148:151], a[12:15], a[40:43], v[148:151]\n" \
"buffer_load_dwordx4 v18, s[28:31], s46 offen lds\n" \
"v_mfma_f32_16x16x32_bf16 v[152:155], a[8:11], a[44:47], v[152:155]\n" \
"ds_read_b64_tr_b16 a[110:111], v12 offset:49664\n" \
"v_and_b32 v26, 0xffff0000, v26\n" \
"v_mul_f32 v25, v25, v27\n" \
"v_mfma_f32_16x16x32_bf16 v[156:159], a[12:15], a[44:47], v[156:159]\n" \
"v_mul_f32 v26, v26, v28\n" \
"v_cvt_pk_bf16_f32 v53, v25, v29\n" \
"v_mfma_f32_16x16x32_bf16 v[160:163], a[16:19], a[40:43], v[160:163]\n" \
"ds_read_b128 a[80:83], v8 offset:8192\n" \
"v_cvt_pk_bf16_f32 v57, v26, v58\n" \
"v_accvgpr_read_b32 v41, a203\n" \
"v_mfma_f32_16x16x32_bf16 v[164:167], a[20:23], a[40:43], v[164:167]\n" \
"v_lshlrev_b32 v35, 16, v41\n" \
"v_and_b32 v41, 0xffff0000, v41\n" \
"v_mfma_f32_16x16x32_bf16 v[168:171], a[16:19], a[44:47], v[168:171]\n" \
"s_add_u32 m0, s22, 34560\n" \
"ds_read_b64_tr_b16 a[112:113], v9 offset:50176\n" \
"v_mul_f32 v25, v32, v45\n" \
"v_mfma_f32_16x16x32_bf16 v[172:175], a[20:23], a[44:47], v[172:175]\n" \
"buffer_load_dwordx4 v21, s[32:35], s47 offen offset:256 lds\n" \
"v_mfma_f32_16x16x32_bf16 v[176:179], a[24:27], a[40:43], v[176:179]\n" \
"ds_read_b64_tr_b16 a[114:115], v10 offset:50176\n" \
"v_mul_f32 v26, v38, v49\n" \
"v_fmac_f32 v25, v33, v44\n" \
"v_mfma_f32_16x16x32_bf16 v[180:183], a[28:31], a[40:43], v[180:183]\n" \
"v_fmac_f32 v26, v39, v48\n" \
"v_fmac_f32 v25, v34, v43\n" \
"v_mfma_f32_16x16x32_bf16 v[184:187], a[24:27], a[44:47], v[184:187]\n" \
"ds_read_b128 a[84:87], v8 offset:10240\n" \
"v_fmac_f32 v26, v40, v47\n" \
"v_fmac_f32 v25, v35, v42\n" \
"v_mfma_f32_16x16x32_bf16 v[188:191], a[28:31], a[44:47], v[188:191]\n" \
"v_fmac_f32 v26, v41, v46\n" \
"v_accvgpr_read_b32 v28, a219\n" \
"v_mfma_f32_16x16x32_bf16 v[192:195], a[0:3], a[48:51], v[192:195]\n" \
"s_add_u32 m0, s50, 57344\n" \
"ds_read_b64_tr_b16 a[116:117], v11 offset:50176\n" \
"v_cvt_pk_bf16_f32 v26, v25, v26\n" \
"v_mfma_f32_16x16x32_bf16 v[196:199], a[4:7], a[48:51], v[196:199]\n" \
"buffer_load_dwordx4 v19, s[28:31], s46 offen lds\n" \
"v_mfma_f32_16x16x32_bf16 v[200:203], a[0:3], a[52:55], v[200:203]\n" \
"ds_read_b64_tr_b16 a[118:119], v12 offset:50176\n" \
"v_lshlrev_b32 v27, 16, v28\n" \
"v_lshlrev_b32 v25, 16, v26\n" \
"v_mfma_f32_16x16x32_bf16 v[204:207], a[4:7], a[52:55], v[204:207]\n" \
"v_and_b32 v28, 0xffff0000, v28\n" \
"v_and_b32 v26, 0xffff0000, v26\n" \
"v_mfma_f32_16x16x32_bf16 v[208:211], a[8:11], a[48:51], v[208:211]\n" \
"ds_read_b128 a[88:91], v8 offset:12288\n" \
"v_mul_f32 v29, v25, v27\n" \
"v_mul_f32 v58, v26, v28\n" \
"v_mfma_f32_16x16x32_bf16 v[212:215], a[12:15], a[48:51], v[212:215]\n" \
"v_accvgpr_read_b32 v36, a205\n" \
"v_lshlrev_b32 v30, 16, v36\n" \
"v_mfma_f32_16x16x32_bf16 v[216:219], a[8:11], a[52:55], v[216:219]\n" \
"s_add_u32 m0, s22, 35456\n" \
"ds_read_b64_tr_b16 a[120:121], v9 offset:50688\n" \
"v_and_b32 v36, 0xffff0000, v36\n" \
"v_mfma_f32_16x16x32_bf16 v[220:223], a[12:15], a[52:55], v[220:223]\n" \
"buffer_load_dwordx4 v21, s[32:35], s47 offen offset:384 lds\n" \
"v_mfma_f32_16x16x32_bf16 v[224:227], a[16:19], a[48:51], v[224:227]\n" \
"ds_read_b64_tr_b16 a[122:123], v10 offset:50688\n" \
"v_mul_f32 v25, v33, v45\n" \
"v_mul_f32 v26, v39, v49\n" \
"v_mfma_f32_16x16x32_bf16 v[228:231], a[20:23], a[48:51], v[228:231]\n" \
"v_fmac_f32 v25, v34, v44\n" \
"v_fmac_f32 v26, v40, v48\n" \
"v_mfma_f32_16x16x32_bf16 v[232:235], a[16:19], a[52:55], v[232:235]\n" \
"ds_read_b128 a[92:95], v8 offset:14336\n" \
"v_fmac_f32 v25, v35, v43\n" \
"v_fmac_f32 v26, v41, v47\n" \
"v_mfma_f32_16x16x32_bf16 v[236:239], a[20:23], a[52:55], v[236:239]\n" \
"v_fmac_f32 v25, v30, v42\n" \
"v_fmac_f32 v26, v36, v46\n" \
"v_mfma_f32_16x16x32_bf16 v[240:243], a[24:27], a[48:51], v[240:243]\n" \
"s_add_u32 m0, s50, 61440\n" \
"ds_read_b64_tr_b16 a[124:125], v11 offset:50688\n" \
"v_accvgpr_read_b32 v28, a221\n" \
"v_mfma_f32_16x16x32_bf16 v[244:247], a[28:31], a[48:51], v[244:247]\n" \
"buffer_load_dwordx4 v20, s[28:31], s46 offen lds\n" \
"v_mfma_f32_16x16x32_bf16 v[248:251], a[24:27], a[52:55], v[248:251]\n" \
"ds_read_b64_tr_b16 a[126:127], v12 offset:50688\n" \
"v_cvt_pk_bf16_f32 v26, v25, v26\n" \
"v_lshlrev_b32 v27, 16, v28\n" \
"v_mfma_f32_16x16x32_bf16 v[252:255], a[28:31], a[52:55], v[252:255]\n" \
"v_lshlrev_b32 v25, 16, v26\n" \
"v_and_b32 v28, 0xffff0000, v28\n" \
"v_mfma_f32_16x16x32_bf16 a[128:131], a[0:3], a[56:59], a[128:131]\n" \
"s_add_u32 s84, s84, 1048576\ns_addc_u32 s85, s85, 0\n" \
"v_and_b32 v26, 0xffff0000, v26\n" \
"v_mul_f32 v25, v25, v27\n" \
"v_mfma_f32_16x16x32_bf16 a[132:135], a[4:7], a[56:59], a[132:135]\n" \
"s_mov_b64 exec, 0xffff\nglobal_load_dword a244, v1, s[84:85]\ns_mov_b64 exec, -1\n" \
"v_mfma_f32_16x16x32_bf16 a[136:139], a[0:3], a[60:63], a[136:139]\n" \
"v_mul_f32 v26, v26, v28\n" \
"v_cvt_pk_bf16_f32 v52, v25, v29\n" \
"v_mfma_f32_16x16x32_bf16 a[140:143], a[4:7], a[60:63], a[140:143]\n" \
"v_cvt_pk_bf16_f32 v56, v26, v58\n" \
"v_accvgpr_read_b32 v37, a207\n" \
"v_mfma_f32_16x16x32_bf16 a[144:147], a[8:11], a[56:59], a[144:147]\n" \
"v_lshlrev_b32 v31, 16, v37\n" \
"v_and_b32 v37, 0xffff0000, v37\n" \
"v_mfma_f32_16x16x32_bf16 a[148:151], a[12:15], a[56:59], a[148:151]\n" \
"v_mul_f32 v25, v34, v45\n" \
"v_mul_f32 v26, v40, v49\n" \
"v_mfma_f32_16x16x32_bf16 a[152:155], a[8:11], a[60:63], a[152:155]\n" \
"v_fmac_f32 v25, v35, v44\n" \
"v_fmac_f32 v26, v41, v48\n" \
"v_mfma_f32_16x16x32_bf16 a[156:159], a[12:15], a[60:63], a[156:159]\n" \
"s_waitcnt vmcnt(21) lgkmcnt(0)\n" \
"v_fmac_f32 v25, v30, v43\n" \
"v_fmac_f32 v26, v36, v47\n" \
"v_mfma_f32_16x16x32_bf16 a[160:163], a[16:19], a[56:59], a[160:163]\n" \
"s_barrier\n" \
"v_mfma_f32_16x16x32_bf16 a[164:167], a[20:23], a[56:59], a[164:167]\n" \
"v_fmac_f32 v25, v31, v42\n" \
"v_fmac_f32 v26, v37, v46\n" \
"v_mfma_f32_16x16x32_bf16 a[168:171], a[16:19], a[60:63], a[168:171]\n" \
"v_accvgpr_read_b32 v28, a223\n" \
"v_cvt_pk_bf16_f32 v26, v25, v26\n" \
"v_mfma_f32_16x16x32_bf16 a[172:175], a[20:23], a[60:63], a[172:175]\n" \
"v_lshlrev_b32 v27, 16, v28\n" \
"v_lshlrev_b32 v25, 16, v26\n" \
"v_mfma_f32_16x16x32_bf16 a[176:179], a[24:27], a[56:59], a[176:179]\n" \
"v_and_b32 v28, 0xffff0000, v28\n" \
"v_and_b32 v26, 0xffff0000, v26\n" \
"v_mfma_f32_16x16x32_bf16 a[180:183], a[28:31], a[56:59], a[180:183]\n" \
"v_mul_f32 v29, v25, v27\n" \
"v_mul_f32 v58, v26, v28\n" \
"v_mfma_f32_16x16x32_bf16 a[184:187], a[24:27], a[60:63], a[184:187]\n" \
"v_accvgpr_read_b32 v38, a209\n" \
"v_lshlrev_b32 v32, 16, v38\n" \
"v_mfma_f32_16x16x32_bf16 a[188:191], a[28:31], a[60:63], a[188:191]\n" \
"v_and_b32 v38, 0xffff0000, v38\n" \
"v_mul_f32 v25, v35, v45\n" \
"v_mfma_f32_16x16x32_bf16 v[64:67], a[64:67], a[96:99], v[64:67]\n" \
"ds_read_b128 a[0:3], v5 offset:0\n" \
"v_mul_f32 v26, v41, v49\n" \
"v_fmac_f32 v25, v30, v44\n" \
"v_mfma_f32_16x16x32_bf16 v[68:71], a[68:71], a[96:99], v[68:71]\n" \
"v_fmac_f32 v26, v36, v48\n" \
"v_fmac_f32 v25, v31, v43\n" \
"v_mfma_f32_16x16x32_bf16 v[72:75], a[64:67], a[100:103], v[72:75]\n" \
"s_mov_b32 s47, 2883584\n" \
"s_add_u32 m0, s22, 49152\n" \
"ds_read_b64_tr_b16 a[32:33], v9 offset:0\n" \
"v_mfma_f32_16x16x32_bf16 v[76:79], a[68:71], a[100:103], v[76:79]\n" \
"buffer_load_dwordx4 v21, s[32:35], s47 offen offset:0 lds\n" \
"v_mfma_f32_16x16x32_bf16 v[80:83], a[72:75], a[96:99], v[80:83]\n" \
"ds_read_b64_tr_b16 a[34:35], v10 offset:0\n" \
"v_fmac_f32 v26, v37, v47\n" \
"v_fmac_f32 v25, v32, v42\n" \
"v_mfma_f32_16x16x32_bf16 v[84:87], a[76:79], a[96:99], v[84:87]\n" \
"v_fmac_f32 v26, v38, v46\n" \
"v_accvgpr_read_b32 v28, a225\n" \
"v_mfma_f32_16x16x32_bf16 v[88:91], a[72:75], a[100:103], v[88:91]\n" \
"ds_read_b128 a[4:7], v5 offset:2048\n" \
"v_cvt_pk_bf16_f32 v26, v25, v26\n" \
"v_lshlrev_b32 v27, 16, v28\n" \
"v_mfma_f32_16x16x32_bf16 v[92:95], a[76:79], a[100:103], v[92:95]\n" \
"v_lshlrev_b32 v25, 16, v26\n" \
"v_and_b32 v28, 0xffff0000, v28\n" \
"v_mfma_f32_16x16x32_bf16 v[96:99], a[80:83], a[96:99], v[96:99]\n" \
"s_mov_b32 s46, 768\n" \
"s_add_u32 m0, s50, 65536\n" \
"ds_read_b64_tr_b16 a[36:37], v11 offset:0\n" \
"v_mfma_f32_16x16x32_bf16 v[100:103], a[84:87], a[96:99], v[100:103]\n" \
"buffer_load_dwordx4 v13, s[28:31], s46 offen lds\n" \
"v_mfma_f32_16x16x32_bf16 v[104:107], a[80:83], a[100:103], v[104:107]\n" \
"ds_read_b64_tr_b16 a[38:39], v12 offset:0\n" \
"v_and_b32 v26, 0xffff0000, v26\n" \
"v_mul_f32 v25, v25, v27\n" \
"v_mfma_f32_16x16x32_bf16 v[108:111], a[84:87], a[100:103], v[108:111]\n" \
"v_mul_f32 v26, v26, v28\n" \
"v_cvt_pk_bf16_f32 v51, v25, v29\n" \
"v_mfma_f32_16x16x32_bf16 v[112:115], a[88:91], a[96:99], v[112:115]\n" \
"ds_read_b128 a[8:11], v5 offset:4096\n" \
"v_cvt_pk_bf16_f32 v55, v26, v58\n" \
"v_accvgpr_read_b32 v39, a211\n" \
"v_mfma_f32_16x16x32_bf16 v[116:119], a[92:95], a[96:99], v[116:119]\n" \
"v_lshlrev_b32 v33, 16, v39\n" \
"v_and_b32 v39, 0xffff0000, v39\n" \
"v_mfma_f32_16x16x32_bf16 v[120:123], a[88:91], a[100:103], v[120:123]\n" \
"s_add_u32 m0, s22, 50048\n" \
"ds_read_b64_tr_b16 a[40:41], v9 offset:512\n" \
"v_mul_f32 v25, v30, v45\n" \
"v_mfma_f32_16x16x32_bf16 v[124:127], a[92:95], a[100:103], v[124:127]\n" \
"buffer_load_dwordx4 v21, s[32:35], s47 offen offset:128 lds\n" \
"v_mfma_f32_16x16x32_bf16 v[128:131], a[64:67], a[104:107], v[128:131]\n" \
"ds_read_b64_tr_b16 a[42:43], v10 offset:512\n" \
"v_mul_f32 v26, v36, v49\n" \
"v_fmac_f32 v25, v31, v44\n" \
"v_mfma_f32_16x16x32_bf16 v[132:135], a[68:71], a[104:107], v[132:135]\n" \
"v_fmac_f32 v26, v37, v48\n" \
"v_fmac_f32 v25, v32, v43\n" \
"v_mfma_f32_16x16x32_bf16 v[136:139], a[64:67], a[108:111], v[136:139]\n" \
"ds_read_b128 a[12:15], v5 offset:6144\n" \
"v_fmac_f32 v26, v38, v47\n" \
"v_fmac_f32 v25, v33, v42\n" \
"v_mfma_f32_16x16x32_bf16 v[140:143], a[68:71], a[108:111], v[140:143]\n" \
"v_fmac_f32 v26, v39, v46\n" \
"v_accvgpr_read_b32 v28, a227\n" \
"v_mfma_f32_16x16x32_bf16 v[144:147], a[72:75], a[104:107], v[144:147]\n" \
"s_add_u32 m0, s50, 69632\n" \
"ds_read_b64_tr_b16 a[44:45], v11 offset:512\n" \
"v_cvt_pk_bf16_f32 v26, v25, v26\n" \
"v_mfma_f32_16x16x32_bf16 v[148:151], a[76:79], a[104:107], v[148:151]\n" \
"buffer_load_dwordx4 v14, s[28:31], s46 offen lds\n" \
"v_mfma_f32_16x16x32_bf16 v[152:155], a[72:75], a[108:111], v[152:155]\n" \
"ds_read_b64_tr_b16 a[46:47], v12 offset:512\n" \
"v_lshlrev_b32 v27, 16, v28\n" \
"v_lshlrev_b32 v25, 16, v26\n" \
"v_mfma_f32_16x16x32_bf16 v[156:159], a[76:79], a[108:111], v[156:159]\n" \
"v_and_b32 v28, 0xffff0000, v28\n" \
"v_and_b32 v26, 0xffff0000, v26\n" \
"v_mfma_f32_16x16x32_bf16 v[160:163], a[80:83], a[104:107], v[160:163]\n" \
"ds_read_b128 a[16:19], v5 offset:8192\n" \
"v_mul_f32 v29, v25, v27\n" \
"v_mul_f32 v58, v26, v28\n" \
"v_mfma_f32_16x16x32_bf16 v[164:167], a[84:87], a[104:107], v[164:167]\n" \
"v_accvgpr_read_b32 v40, a213\n" \
"v_lshlrev_b32 v34, 16, v40\n" \
"v_mfma_f32_16x16x32_bf16 v[168:171], a[80:83], a[108:111], v[168:171]\n" \
"s_add_u32 m0, s22, 50944\n" \
"ds_read_b64_tr_b16 a[48:49], v9 offset:1024\n" \
"v_and_b32 v40, 0xffff0000, v40\n" \
"v_mfma_f32_16x16x32_bf16 v[172:175], a[84:87], a[108:111], v[172:175]\n" \
"buffer_load_dwordx4 v21, s[32:35], s47 offen offset:256 lds\n" \
"v_mfma_f32_16x16x32_bf16 v[176:179], a[88:91], a[104:107], v[176:179]\n" \
"ds_read_b64_tr_b16 a[50:51], v10 offset:1024\n" \
"v_mul_f32 v25, v31, v45\n" \
"v_mul_f32 v26, v37, v49\n" \
"v_mfma_f32_16x16x32_bf16 v[180:183], a[92:95], a[104:107], v[180:183]\n" \
"v_fmac_f32 v25, v32, v44\n" \
"v_fmac_f32 v26, v38, v48\n" \
"v_mfma_f32_16x16x32_bf16 v[184:187], a[88:91], a[108:111], v[184:187]\n" \
"ds_read_b128 a[20:23], v5 offset:10240\n" \
"v_fmac_f32 v25, v33, v43\n" \
"v_fmac_f32 v26, v39, v47\n" \
"v_mfma_f32_16x16x32_bf16 v[188:191], a[92:95], a[108:111], v[188:191]\n" \
"v_fmac_f32 v25, v34, v42\n" \
"v_fmac_f32 v26, v40, v46\n" \
"v_mfma_f32_16x16x32_bf16 v[192:195], a[64:67], a[112:115], v[192:195]\n" \
"s_add_u32 m0, s50, 73728\n" \
"ds_read_b64_tr_b16 a[52:53], v11 offset:1024\n" \
"v_accvgpr_read_b32 v28, a229\n" \
"v_mfma_f32_16x16x32_bf16 v[196:199], a[68:71], a[112:115], v[196:199]\n" \
"buffer_load_dwordx4 v15, s[28:31], s46 offen lds\n" \
"v_mfma_f32_16x16x32_bf16 v[200:203], a[64:67], a[116:119], v[200:203]\n" \
"ds_read_b64_tr_b16 a[54:55], v12 offset:1024\n" \
"v_cvt_pk_bf16_f32 v26, v25, v26\n" \
"v_lshlrev_b32 v27, 16, v28\n" \
"v_mfma_f32_16x16x32_bf16 v[204:207], a[68:71], a[116:119], v[204:207]\n" \
"v_lshlrev_b32 v25, 16, v26\n" \
"v_and_b32 v28, 0xffff0000, v28\n" \
"v_mfma_f32_16x16x32_bf16 v[208:211], a[72:75], a[112:115], v[208:211]\n" \
"ds_read_b128 a[24:27], v5 offset:12288\n" \
"v_and_b32 v26, 0xffff0000, v26\n" \
"v_mul_f32 v25, v25, v27\n" \
"v_mfma_f32_16x16x32_bf16 v[212:215], a[76:79], a[112:115], v[212:215]\n" \
"v_mul_f32 v26, v26, v28\n" \
"v_cvt_pk_bf16_f32 v50, v25, v29\n" \
"v_mfma_f32_16x16x32_bf16 v[216:219], a[72:75], a[116:119], v[216:219]\n" \
"s_add_u32 m0, s22, 51840\n" \
"ds_read_b64_tr_b16 a[56:57], v9 offset:1536\n" \
"v_cvt_pk_bf16_f32 v54, v26, v58\n" \
"v_mfma_f32_16x16x32_bf16 v[220:223], a[76:79], a[116:119], v[220:223]\n" \
"buffer_load_dwordx4 v21, s[32:35], s47 offen offset:384 lds\n" \
"s_mov_b32 s94, 79699968\n" \
"v_mfma_f32_16x16x32_bf16 v[224:227], a[80:83], a[112:115], v[224:227]\n" \
"ds_read_b64_tr_b16 a[58:59], v10 offset:1536\n" \
"v_mfma_f32_16x16x32_bf16 v[228:231], a[84:87], a[112:115], v[228:231]\n" \
"v_mfma_f32_16x16x32_bf16 v[232:235], a[80:83], a[116:119], v[232:235]\n" \
"ds_read_b128 a[28:31], v5 offset:14336\n" \
"v_mfma_f32_16x16x32_bf16 v[236:239], a[84:87], a[116:119], v[236:239]\n" \
"v_mfma_f32_16x16x32_bf16 v[240:243], a[88:91], a[112:115], v[240:243]\n" \
"s_add_u32 m0, s50, 77824\n" \
"ds_read_b64_tr_b16 a[60:61], v11 offset:1536\n" \
"v_mfma_f32_16x16x32_bf16 v[244:247], a[92:95], a[112:115], v[244:247]\n" \
"buffer_load_dwordx4 v16, s[28:31], s46 offen lds\n" \
"v_mfma_f32_16x16x32_bf16 v[248:251], a[88:91], a[116:119], v[248:251]\n" \
"ds_read_b64_tr_b16 a[62:63], v12 offset:1536\n" \
"v_mfma_f32_16x16x32_bf16 v[252:255], a[92:95], a[116:119], v[252:255]\n" \
"v_mfma_f32_16x16x32_bf16 a[128:131], a[64:67], a[120:123], a[128:131]\n" \
"v_mfma_f32_16x16x32_bf16 a[132:135], a[68:71], a[120:123], a[132:135]\n" \
"s_mov_b64 exec, 0xffff\nglobal_load_dword a244, v2, s[88:89]\ns_mov_b64 exec, -1\n" \
"v_mfma_f32_16x16x32_bf16 a[136:139], a[64:67], a[124:127], a[136:139]\n" \
"v_mfma_f32_16x16x32_bf16 a[140:143], a[68:71], a[124:127], a[140:143]\n" \
"v_mfma_f32_16x16x32_bf16 a[144:147], a[72:75], a[120:123], a[144:147]\n" \
"v_mfma_f32_16x16x32_bf16 a[148:151], a[76:79], a[120:123], a[148:151]\n" \
"buffer_store_dwordx4 v[50:53], v60, s[36:39], s94 offen offset:0 sc1\n" \
"s_mov_b32 s95, 79704064\n" \
"v_mfma_f32_16x16x32_bf16 a[152:155], a[72:75], a[124:127], a[152:155]\n" \
"v_mfma_f32_16x16x32_bf16 a[156:159], a[76:79], a[124:127], a[156:159]\n" \
"s_waitcnt vmcnt(23) lgkmcnt(0)\n" \
"v_mfma_f32_16x16x32_bf16 a[160:163], a[80:83], a[120:123], a[160:163]\n" \
"s_barrier\n" \
"v_mfma_f32_16x16x32_bf16 a[164:167], a[84:87], a[120:123], a[164:167]\n" \
"v_mfma_f32_16x16x32_bf16 a[168:171], a[80:83], a[124:127], a[168:171]\n" \
"v_mfma_f32_16x16x32_bf16 a[172:175], a[84:87], a[124:127], a[172:175]\n" \
"buffer_store_dwordx4 v[54:57], v60, s[36:39], s95 offen offset:0 sc1\n" \
"v_mfma_f32_16x16x32_bf16 a[176:179], a[88:91], a[120:123], a[176:179]\n" \
"s_lshl_b32 s62, s8, 2\n" \
"s_add_u32 s62, s62, %2\n" \
"s_lshl_b32 s63, s62, 12\n" \
"v_mfma_f32_16x16x32_bf16 a[180:183], a[92:95], a[120:123], a[180:183]\n" \
"s_add_u32 s68, s10, s63\ns_addc_u32 s69, s11, 0\n" \
"s_and_b32 s69, s69, 0xffff\n" \
"s_mov_b32 s70, 4096\n" \
"v_mfma_f32_16x16x32_bf16 a[184:187], a[88:91], a[124:127], a[184:187]\n" \
"s_mov_b32 s71, 0x00020000\n" \
"s_add_u32 s63, s63, 79691776\n" \
"s_add_u32 s72, s4, s63\ns_addc_u32 s73, s5, 0\n" \
"v_mfma_f32_16x16x32_bf16 a[188:191], a[92:95], a[124:127], a[188:191]\n" \
"s_and_b32 s73, s73, 0xffff\n" \
"s_mov_b32 s74, 4096\n" \
"s_mov_b32 s75, 0x00020000\n" \
"v_mfma_f32_16x16x32_bf16 v[64:67], a[0:3], a[32:35], v[64:67]\n" \
"ds_read_b128 a[64:67], v6 offset:0\n" \
"v_add_u32 v23, -32, v4\n" \
"v_add_u32 v24, 992, v4\n" \
"v_mfma_f32_16x16x32_bf16 v[68:71], a[4:7], a[32:35], v[68:71]\n" \
"v_add_u32 v25, -16, v4\n" \
"v_add_u32 v26, -512, v4\n" \
"v_mfma_f32_16x16x32_bf16 v[72:75], a[0:3], a[36:39], v[72:75]\n" \
"s_mov_b32 s47, 3145728\n" \
"s_add_u32 m0, s22, 0\n" \
"ds_read_b64_tr_b16 a[96:97], v9 offset:16384\n" \
"v_mfma_f32_16x16x32_bf16 v[76:79], a[4:7], a[36:39], v[76:79]\n" \
"buffer_load_dwordx4 v21, s[32:35], s47 offen offset:0 lds\n" \
"v_mfma_f32_16x16x32_bf16 v[80:83], a[8:11], a[32:35], v[80:83]\n" \
"ds_read_b64_tr_b16 a[98:99], v10 offset:16384\n" \
"v_add_u32 v27, 512, v4\n" \
"s_lshr_b32 s63, %2, 1\n" \
"v_mfma_f32_16x16x32_bf16 v[84:87], a[12:15], a[32:35], v[84:87]\n" \
"s_lshl_b32 s63, s63, 16\n" \
"s_lshl_b32 s56, %2, 14\n" \
"s_add_u32 s56, s56, s63\n" \
"v_mfma_f32_16x16x32_bf16 v[88:91], a[8:11], a[36:39], v[88:91]\n" \
"ds_read_b128 a[68:71], v6 offset:2048\n" \
"v_add_u32 v55, s56, v4\n" \
"v_mov_b32 v28, 0\n" \
"v_mfma_f32_16x16x32_bf16 v[92:95], a[12:15], a[36:39], v[92:95]\n" \
"s_lshl_b32 s62, s62, 1\n" \
"v_mov_b32 v29, s62\n" \
"v_lshlrev_b32 v56, 23, v4\n" \
"v_mfma_f32_16x16x32_bf16 v[96:99], a[16:19], a[32:35], v[96:99]\n" \
"s_mov_b32 s46, 768\n" \
"s_add_u32 m0, s50, 81920\n" \
"ds_read_b64_tr_b16 a[100:101], v11 offset:16384\n" \
"v_mfma_f32_16x16x32_bf16 v[100:103], a[20:23], a[32:35], v[100:103]\n" \
"buffer_load_dwordx4 v17, s[28:31], s46 offen lds\n" \
"s_and_b32 s62, %1, 7\n" \
"s_lshl_b32 s62, s62, 8\n" \
"v_mfma_f32_16x16x32_bf16 v[104:107], a[16:19], a[36:39], v[104:107]\n" \
"ds_read_b64_tr_b16 a[102:103], v12 offset:16384\n" \
"s_add_u32 s62, s62, 94380032\n" \
"v_bfe_u32 v57, v4, 4, 5\n" \
"v_mfma_f32_16x16x32_bf16 v[108:111], a[20:23], a[36:39], v[108:111]\n" \
"v_lshl_add_u32 v57, v57, 3, s62\n" \
"v_mfma_f32_16x16x32_bf16 v[112:115], a[24:27], a[32:35], v[112:115]\n" \
"ds_read_b128 a[72:75], v6 offset:4096\n" \
"v_mfma_f32_16x16x32_bf16 v[116:119], a[28:31], a[32:35], v[116:119]\n" \
"v_mfma_f32_16x16x32_bf16 v[120:123], a[24:27], a[36:39], v[120:123]\n" \
"s_add_u32 m0, s22, 896\n" \
"ds_read_b64_tr_b16 a[104:105], v9 offset:16896\n" \
"v_mfma_f32_16x16x32_bf16 v[124:127], a[28:31], a[36:39], v[124:127]\n" \
"buffer_load_dwordx4 v21, s[32:35], s47 offen offset:128 lds\n" \
"v_mfma_f32_16x16x32_bf16 v[128:131], a[0:3], a[40:43], v[128:131]\n" \
"ds_read_b64_tr_b16 a[106:107], v10 offset:16896\n" \
"v_mfma_f32_16x16x32_bf16 v[132:135], a[4:7], a[40:43], v[132:135]\n" \
"v_mfma_f32_16x16x32_bf16 v[136:139], a[0:3], a[44:47], v[136:139]\n" \
"ds_read_b128 a[76:79], v6 offset:6144\n" \
"v_mfma_f32_16x16x32_bf16 v[140:143], a[4:7], a[44:47], v[140:143]\n" \
"v_mfma_f32_16x16x32_bf16 v[144:147], a[8:11], a[40:43], v[144:147]\n" \
"s_add_u32 m0, s50, 86016\n" \
"ds_read_b64_tr_b16 a[108:109], v11 offset:16896\n" \
"v_mfma_f32_16x16x32_bf16 v[148:151], a[12:15], a[40:43], v[148:151]\n" \
"buffer_load_dwordx4 v18, s[28:31], s46 offen lds\n" \
"v_mfma_f32_16x16x32_bf16 v[152:155], a[8:11], a[44:47], v[152:155]\n" \
"ds_read_b64_tr_b16 a[110:111], v12 offset:16896\n" \
"v_mfma_f32_16x16x32_bf16 v[156:159], a[12:15], a[44:47], v[156:159]\n" \
"v_mfma_f32_16x16x32_bf16 v[160:163], a[16:19], a[40:43], v[160:163]\n" \
"ds_read_b128 a[80:83], v6 offset:8192\n" \
"v_mfma_f32_16x16x32_bf16 v[164:167], a[20:23], a[40:43], v[164:167]\n" \
"v_mfma_f32_16x16x32_bf16 v[168:171], a[16:19], a[44:47], v[168:171]\n" \
"s_add_u32 m0, s22, 1792\n" \
"ds_read_b64_tr_b16 a[112:113], v9 offset:17408\n" \
"v_mfma_f32_16x16x32_bf16 v[172:175], a[20:23], a[44:47], v[172:175]\n" \
"buffer_load_dwordx4 v21, s[32:35], s47 offen offset:256 lds\n" \
"v_mfma_f32_16x16x32_bf16 v[176:179], a[24:27], a[40:43], v[176:179]\n" \
"ds_read_b64_tr_b16 a[114:115], v10 offset:17408\n" \
"v_mfma_f32_16x16x32_bf16 v[180:183], a[28:31], a[40:43], v[180:183]\n" \
"v_mfma_f32_16x16x32_bf16 v[184:187], a[24:27], a[44:47], v[184:187]\n" \
"ds_read_b128 a[84:87], v6 offset:10240\n" \
"v_mfma_f32_16x16x32_bf16 v[188:191], a[28:31], a[44:47], v[188:191]\n" \
"v_mfma_f32_16x16x32_bf16 v[192:195], a[0:3], a[48:51], v[192:195]\n" \
"s_add_u32 m0, s50, 90112\n" \
"ds_read_b64_tr_b16 a[116:117], v11 offset:17408\n" \
"v_mfma_f32_16x16x32_bf16 v[196:199], a[4:7], a[48:51], v[196:199]\n" \
"buffer_load_dwordx4 v19, s[28:31], s46 offen lds\n" \
"v_mfma_f32_16x16x32_bf16 v[200:203], a[0:3], a[52:55], v[200:203]\n" \
"ds_read_b64_tr_b16 a[118:119], v12 offset:17408\n" \
"v_mfma_f32_16x16x32_bf16 v[204:207], a[4:7], a[52:55], v[204:207]\n" \
"LH6_%=:\ns_cbranch_execz LH10_%=\n" \
"v_mfma_f32_16x16x32_bf16 v[208:211], a[8:11], a[48:51], v[208:211]\n" \
"ds_read_b128 a[88:91], v6 offset:12288\n" \
"v_mfma_f32_16x16x32_bf16 v[212:215], a[12:15], a[48:51], v[212:215]\n" \
"v_mfma_f32_16x16x32_bf16 v[216:219], a[8:11], a[52:55], v[216:219]\n" \
"s_add_u32 m0, s22, 2688\n" \
"ds_read_b64_tr_b16 a[120:121], v9 offset:17920\n" \
"v_mfma_f32_16x16x32_bf16 v[220:223], a[12:15], a[52:55], v[220:223]\n" \
"buffer_load_dwordx4 v21, s[32:35], s47 offen offset:384 lds\n" \
"v_mfma_f32_16x16x32_bf16 v[224:227], a[16:19], a[48:51], v[224:227]\n" \
"ds_read_b64_tr_b16 a[122:123], v10 offset:17920\n" \
"v_mfma_f32_16x16x32_bf16 v[228:231], a[20:23], a[48:51], v[228:231]\n" \
"v_mfma_f32_16x16x32_bf16 v[232:235], a[16:19], a[52:55], v[232:235]\n" \
"ds_read_b128 a[92:95], v6 offset:14336\n" \
"v_mfma_f32_16x16x32_bf16 v[236:239], a[20:23], a[52:55], v[236:239]\n" \
"v_mfma_f32_16x16x32_bf16 v[240:243], a[24:27], a[48:51], v[240:243]\n" \
"s_add_u32 m0, s50, 94208\n" \
"ds_read_b64_tr_b16 a[124:125], v11 offset:17920\n" \
"v_mfma_f32_16x16x32_bf16 v[244:247], a[28:31], a[48:51], v[244:247]\n" \
"buffer_load_dwordx4 v20, s[28:31], s46 offen lds\n" \
"v_mfma_f32_16x16x32_bf16 v[248:251], a[24:27], a[52:55], v[248:251]\n" \
"ds_read_b64_tr_b16 a[126:127], v12 offset:17920\n" \
"v_mfma_f32_16x16x32_bf16 v[252:255], a[28:31], a[52:55], v[252:255]\n" \
"v_mfma_f32_16x16x32_bf16 a[128:131], a[0:3], a[56:59], a[128:131]\n" \
"s_add_u32 s88, s88, 2048\ns_addc_u32 s89, s89, 0\n" \
"v_mfma_f32_16x16x32_bf16 a[132:135], a[4:7], a[56:59], a[132:135]\n" \
"s_mov_b64 exec, 0xffff\nglobal_load_dword a244, v2, s[88:89]\ns_mov_b64 exec, -1\n" \
"v_mfma_f32_16x16x32_bf16 a[136:139], a[0:3], a[60:63], a[136:139]\n" \
"v_mfma_f32_16x16x32_bf16 a[140:143], a[4:7], a[60:63], a[140:143]\n" \
"v_mfma_f32_16x16x32_bf16 a[144:147], a[8:11], a[56:59], a[144:147]\n" \
"v_mfma_f32_16x16x32_bf16 a[148:151], a[12:15], a[56:59], a[148:151]\n" \
"v_mfma_f32_16x16x32_bf16 a[152:155], a[8:11], a[60:63], a[152:155]\n" \
"v_mfma_f32_16x16x32_bf16 a[156:159], a[12:15], a[60:63], a[156:159]\n" \
"s_waitcnt vmcnt(9) lgkmcnt(0)\n" \
"v_mfma_f32_16x16x32_bf16 a[160:163], a[16:19], a[56:59], a[160:163]\n" \
"s_barrier\n" \
"v_mfma_f32_16x16x32_bf16 a[164:167], a[20:23], a[56:59], a[164:167]\n" \
"v_mov_b32 v58, s14\n" \
"v_mfma_f32_16x16x32_bf16 a[168:171], a[16:19], a[60:63], a[168:171]\n" \
"v_mov_b32 v59, s15\n" \
"v_mfma_f32_16x16x32_bf16 a[172:175], a[20:23], a[60:63], a[172:175]\n" \
"v_mfma_f32_16x16x32_bf16 a[176:179], a[24:27], a[56:59], a[176:179]\n" \
"v_mfma_f32_16x16x32_bf16 a[180:183], a[28:31], a[56:59], a[180:183]\n" \
"v_mfma_f32_16x16x32_bf16 a[184:187], a[24:27], a[60:63], a[184:187]\n" \
"v_mfma_f32_16x16x32_bf16 a[188:191], a[28:31], a[60:63], a[188:191]\n" \
"v_mfma_f32_16x16x32_bf16 v[64:67], a[64:67], a[96:99], v[64:67]\n" \
"ds_read_b128 a[0:3], v5 offset:32768\n" \
"v_mfma_f32_16x16x32_bf16 v[68:71], a[68:71], a[96:99], v[68:71]\n" \
"v_mfma_f32_16x16x32_bf16 v[72:75], a[64:67], a[100:103], v[72:75]\n" \
"ds_read_b64_tr_b16 a[32:33], v9 offset:32768\n" \
"v_mfma_f32_16x16x32_bf16 v[76:79], a[68:71], a[100:103], v[76:79]\n" \
"buffer_store_dwordx2 v[58:59], v56, s[36:39], s23 offen sc1\n" \
"v_mfma_f32_16x16x32_bf16 v[80:83], a[72:75], a[96:99], v[80:83]\n" \
"ds_read_b64_tr_b16 a[34:35], v10 offset:32768\n" \
"v_mfma_f32_16x16x32_bf16 v[84:87], a[76:79], a[96:99], v[84:87]\n" \
"v_mfma_f32_16x16x32_bf16 v[88:91], a[72:75], a[100:103], v[88:91]\n" \
"s_mov_b32 s47, 3407872\n" \
"s_add_u32 m0, s22, 16384\n" \
"ds_read_b128 a[4:7], v5 offset:34816\n" \
"v_mfma_f32_16x16x32_bf16 v[92:95], a[76:79], a[100:103], v[92:95]\n" \
"buffer_load_dwordx4 v21, s[32:35], s47 offen offset:0 lds\n" \
"v_mfma_f32_16x16x32_bf16 v[96:99], a[80:83], a[96:99], v[96:99]\n" \
"ds_read_b64_tr_b16 a[36:37], v11 offset:32768\n" \
"v_mfma_f32_16x16x32_bf16 v[100:103], a[84:87], a[96:99], v[100:103]\n" \
"v_mfma_f32_16x16x32_bf16 v[104:107], a[80:83], a[100:103], v[104:107]\n" \
"s_mov_b32 s46, 896\n" \
"s_add_u32 m0, s50, 0\n" \
"ds_read_b64_tr_b16 a[38:39], v12 offset:32768\n" \
"v_mfma_f32_16x16x32_bf16 v[108:111], a[84:87], a[100:103], v[108:111]\n" \
"buffer_load_dwordx4 v13, s[28:31], s46 offen lds\n" \
"v_mfma_f32_16x16x32_bf16 v[112:115], a[88:91], a[96:99], v[112:115]\n" \
"ds_read_b128 a[8:11], v5 offset:36864\n" \
"v_mfma_f32_16x16x32_bf16 v[116:119], a[92:95], a[96:99], v[116:119]\n" \
"v_mfma_f32_16x16x32_bf16 v[120:123], a[88:91], a[100:103], v[120:123]\n" \
"s_add_u32 m0, s22, 17280\n" \
"ds_read_b64_tr_b16 a[40:41], v9 offset:33280\n" \
"v_mfma_f32_16x16x32_bf16 v[124:127], a[92:95], a[100:103], v[124:127]\n" \
"buffer_load_dwordx4 v21, s[32:35], s47 offen offset:128 lds\n" \
"v_mfma_f32_16x16x32_bf16 v[128:131], a[64:67], a[104:107], v[128:131]\n" \
"ds_read_b64_tr_b16 a[42:43], v10 offset:33280\n" \
"v_mfma_f32_16x16x32_bf16 v[132:135], a[68:71], a[104:107], v[132:135]\n" \
"v_mfma_f32_16x16x32_bf16 v[136:139], a[64:67], a[108:111], v[136:139]\n" \
"s_add_u32 m0, s50, 4096\n" \
"ds_read_b128 a[12:15], v5 offset:38912\n" \
"v_mfma_f32_16x16x32_bf16 v[140:143], a[68:71], a[108:111], v[140:143]\n" \
"buffer_load_dwordx4 v14, s[28:31], s46 offen lds\n" \
"v_mfma_f32_16x16x32_bf16 v[144:147], a[72:75], a[104:107], v[144:147]\n" \
"ds_read_b64_tr_b16 a[44:45], v11 offset:33280\n" \
"v_mfma_f32_16x16x32_bf16 v[148:151], a[76:79], a[104:107], v[148:151]\n" \
"v_mfma_f32_16x16x32_bf16 v[152:155], a[72:75], a[108:111], v[152:155]\n" \
"s_add_u32 m0, s22, 18176\n" \
"ds_read_b64_tr_b16 a[46:47], v12 offset:33280\n" \
"v_mfma_f32_16x16x32_bf16 v[156:159], a[76:79], a[108:111], v[156:159]\n" \
"buffer_load_dwordx4 v21, s[32:35], s47 offen offset:256 lds\n" \
"v_mfma_f32_16x16x32_bf16 v[160:163], a[80:83], a[104:107], v[160:163]\n" \
"ds_read_b128 a[16:19], v5 offset:40960\n" \
"v_mfma_f32_16x16x32_bf16 v[164:167], a[84:87], a[104:107], v[164:167]\n" \
"v_mfma_f32_16x16x32_bf16 v[168:171], a[80:83], a[108:111], v[168:171]\n" \
"s_add_u32 m0, s50, 8192\n" \
"ds_read_b64_tr_b16 a[48:49], v9 offset:33792\n" \
"v_mfma_f32_16x16x32_bf16 v[172:175], a[84:87], a[108:111], v[172:175]\n" \
"buffer_load_dwordx4 v15, s[28:31], s46 offen lds\n" \
"v_mfma_f32_16x16x32_bf16 v[176:179], a[88:91], a[104:107], v[176:179]\n" \
"ds_read_b64_tr_b16 a[50:51], v10 offset:33792\n" \
"v_mfma_f32_16x16x32_bf16 v[180:183], a[92:95], a[104:107], v[180:183]\n" \
"v_mfma_f32_16x16x32_bf16 v[184:187], a[88:91], a[108:111], v[184:187]\n" \
"s_add_u32 m0, s22, 19072\n" \
"ds_read_b128 a[20:23], v5 offset:43008\n" \
"v_mfma_f32_16x16x32_bf16 v[188:191], a[92:95], a[108:111], v[188:191]\n" \
"buffer_load_dwordx4 v21, s[32:35], s47 offen offset:384 lds\n" \
"v_mfma_f32_16x16x32_bf16 v[192:195], a[64:67], a[112:115], v[192:195]\n" \
"ds_read_b64_tr_b16 a[52:53], v11 offset:33792\n" \
"v_mfma_f32_16x16x32_bf16 v[196:199], a[68:71], a[112:115], v[196:199]\n" \
"v_mfma_f32_16x16x32_bf16 v[200:203], a[64:67], a[116:119], v[200:203]\n" \
"s_add_u32 m0, s50, 12288\n" \
"ds_read_b64_tr_b16 a[54:55], v12 offset:33792\n" \
"v_mfma_f32_16x16x32_bf16 v[204:207], a[68:71], a[116:119], v[204:207]\n" \
"buffer_load_dwordx4 v16, s[28:31], s46 offen lds\n" \
"v_mfma_f32_16x16x32_bf16 v[208:211], a[72:75], a[112:115], v[208:211]\n" \
"ds_read_b128 a[24:27], v5 offset:45056\n" \
"v_mfma_f32_16x16x32_bf16 v[212:215], a[76:79], a[112:115], v[212:215]\n" \
"v_mfma_f32_16x16x32_bf16 v[216:219], a[72:75], a[116:119], v[216:219]\n" \
"ds_read_b64_tr_b16 a[56:57], v9 offset:34304\n" \
"v_mfma_f32_16x16x32_bf16 v[220:223], a[76:79], a[116:119], v[220:223]\n" \
"buffer_load_dwordx4 v[30:33], v23, s[68:71], 0 offen offset:0\n" \
"v_mfma_f32_16x16x32_bf16 v[224:227], a[80:83], a[112:115], v[224:227]\n" \
"ds_read_b64_tr_b16 a[58:59], v10 offset:34304\n" \
"v_mfma_f32_16x16x32_bf16 v[228:231], a[84:87], a[112:115], v[228:231]\n" \
"v_mfma_f32_16x16x32_bf16 v[232:235], a[80:83], a[116:119], v[232:235]\n" \
"ds_read_b128 a[28:31], v5 offset:47104\n" \
"v_mfma_f32_16x16x32_bf16 v[236:239], a[84:87], a[116:119], v[236:239]\n" \
"buffer_load_dword v50, v25, s[68:71], 0 offen offset:0\n" \
"v_mfma_f32_16x16x32_bf16 v[240:243], a[88:91], a[112:115], v[240:243]\n" \
"ds_read_b64_tr_b16 a[60:61], v11 offset:34304\n" \
"v_mfma_f32_16x16x32_bf16 v[244:247], a[92:95], a[112:115], v[244:247]\n" \
"v_mfma_f32_16x16x32_bf16 v[248:251], a[88:91], a[116:119], v[248:251]\n" \
"ds_read_b64_tr_b16 a[62:63], v12 offset:34304\n" \
"v_mfma_f32_16x16x32_bf16 v[252:255], a[92:95], a[116:119], v[252:255]\n" \
"buffer_load_dwordx4 v[34:37], v24, s[68:71], 0 offen offset:0\n" \
"v_mfma_f32_16x16x32_bf16 a[128:131], a[64:67], a[120:123], a[128:131]\n" \
"v_mfma_f32_16x16x32_bf16 a[132:135], a[68:71], a[120:123], a[132:135]\n" \
"v_mfma_f32_16x16x32_bf16 a[136:139], a[64:67], a[124:127], a[136:139]\n" \
"v_mfma_f32_16x16x32_bf16 a[140:143], a[68:71], a[124:127], a[140:143]\n" \
"buffer_load_dword v51, v24, s[68:71], 0 offen offset:16\n" \
"v_mfma_f32_16x16x32_bf16 a[144:147], a[72:75], a[120:123], a[144:147]\n" \
"v_mfma_f32_16x16x32_bf16 a[148:151], a[76:79], a[120:123], a[148:151]\n" \
"v_mfma_f32_16x16x32_bf16 a[152:155], a[72:75], a[124:127], a[152:155]\n" \
"v_mfma_f32_16x16x32_bf16 a[156:159], a[76:79], a[124:127], a[156:159]\n" \
"s_waitcnt vmcnt(26) lgkmcnt(0)\n" \
"v_mfma_f32_16x16x32_bf16 a[160:163], a[80:83], a[120:123], a[160:163]\n" \
"s_barrier\n" \
"v_mfma_f32_16x16x32_bf16 a[164:167], a[84:87], a[120:123], a[164:167]\n" \
"v_mfma_f32_16x16x32_bf16 a[168:171], a[80:83], a[124:127], a[168:171]\n" \
"v_mfma_f32_16x16x32_bf16 a[172:175], a[84:87], a[124:127], a[172:175]\n" \
"buffer_load_dwordx4 v[38:41], v24, s[68:71], 0 offen offset:1024\n" \
"v_mfma_f32_16x16x32_bf16 a[176:179], a[88:91], a[120:123], a[176:179]\n" \
"v_mfma_f32_16x16x32_bf16 a[180:183], a[92:95], a[120:123], a[180:183]\n" \
"v_mfma_f32_16x16x32_bf16 a[184:187], a[88:91], a[124:127], a[184:187]\n" \
"v_mfma_f32_16x16x32_bf16 a[188:191], a[92:95], a[124:127], a[188:191]\n" \
"buffer_load_dword v52, v24, s[68:71], 0 offen offset:1040\n" \
"v_mfma_f32_16x16x32_bf16 v[64:67], a[0:3], a[32:35], v[64:67]\n" \
"ds_read_b128 a[64:67], v6 offset:32768\n" \
"v_mfma_f32_16x16x32_bf16 v[68:71], a[4:7], a[32:35], v[68:71]\n" \
"v_mfma_f32_16x16x32_bf16 v[72:75], a[0:3], a[36:39], v[72:75]\n" \
"ds_read_b64_tr_b16 a[96:97], v9 offset:49152\n" \
"v_mfma_f32_16x16x32_bf16 v[76:79], a[4:7], a[36:39], v[76:79]\n" \
"buffer_load_dwordx4 v[42:45], v24, s[68:71], 0 offen offset:2048\n" \
"v_mfma_f32_16x16x32_bf16 v[80:83], a[8:11], a[32:35], v[80:83]\n" \
"ds_read_b64_tr_b16 a[98:99], v10 offset:49152\n" \
"v_mfma_f32_16x16x32_bf16 v[84:87], a[12:15], a[32:35], v[84:87]\n" \
"v_mfma_f32_16x16x32_bf16 v[88:91], a[8:11], a[36:39], v[88:91]\n" \
"ds_read_b128 a[68:71], v6 offset:34816\n" \
"v_mfma_f32_16x16x32_bf16 v[92:95], a[12:15], a[36:39], v[92:95]\n" \
"buffer_load_dword v53, v24, s[68:71], 0 offen offset:2064\n" \
"v_mfma_f32_16x16x32_bf16 v[96:99], a[16:19], a[32:35], v[96:99]\n" \
"ds_read_b64_tr_b16 a[100:101], v11 offset:49152\n" \
"v_mfma_f32_16x16x32_bf16 v[100:103], a[20:23], a[32:35], v[100:103]\n" \
"v_mfma_f32_16x16x32_bf16 v[104:107], a[16:19], a[36:39], v[104:107]\n" \
"ds_read_b64_tr_b16 a[102:103], v12 offset:49152\n" \
"v_mfma_f32_16x16x32_bf16 v[108:111], a[20:23], a[36:39], v[108:111]\n" \
"buffer_load_dwordx4 v[46:49], v24, s[68:71], 0 offen offset:3072\n" \
"v_mfma_f32_16x16x32_bf16 v[112:115], a[24:27], a[32:35], v[112:115]\n" \
"ds_read_b128 a[72:75], v6 offset:36864\n" \
"v_mfma_f32_16x16x32_bf16 v[116:119], a[28:31], a[32:35], v[116:119]\n" \
"v_mfma_f32_16x16x32_bf16 v[120:123], a[24:27], a[36:39], v[120:123]\n" \
"ds_read_b64_tr_b16 a[104:105], v9 offset:49664\n" \
"v_mfma_f32_16x16x32_bf16 v[124:127], a[28:31], a[36:39], v[124:127]\n" \
"buffer_load_dword v54, v24, s[68:71], 0 offen offset:3088\n" \
"v_mfma_f32_16x16x32_bf16 v[128:131], a[0:3], a[40:43], v[128:131]\n" \
"ds_read_b64_tr_b16 a[106:107], v10 offset:49664\n" \
"v_mfma_f32_16x16x32_bf16 v[132:135], a[4:7], a[40:43], v[132:135]\n" \
"v_mfma_f32_16x16x32_bf16 v[136:139], a[0:3], a[44:47], v[136:139]\n" \
"ds_read_b128 a[76:79], v6 offset:38912\n" \
"v_mfma_f32_16x16x32_bf16 v[140:143], a[4:7], a[44:47], v[140:143]\n" \
"global_load_short_d16_hi v28, v29, s[12:13]\n" \
"v_mfma_f32_16x16x32_bf16 v[144:147], a[8:11], a[40:43], v[144:147]\n" \
"ds_read_b64_tr_b16 a[108:109], v11 offset:49664\n" \
"v_mfma_f32_16x16x32_bf16 v[148:151], a[12:15], a[40:43], v[148:151]\n" \
"v_mfma_f32_16x16x32_bf16 v[152:155], a[8:11], a[44:47], v[152:155]\n" \
"s_mov_b32 s47, 3670016\n" \
"s_add_u32 m0, s22, 32768\n" \
"ds_read_b64_tr_b16 a[110:111], v12 offset:49664\n" \
"v_mfma_f32_16x16x32_bf16 v[156:159], a[12:15], a[44:47], v[156:159]\n" \
"buffer_load_dwordx4 v21, s[32:35], s47 offen offset:0 lds\n" \
"v_mfma_f32_16x16x32_bf16 v[160:163], a[16:19], a[40:43], v[160:163]\n" \
"ds_read_b128 a[80:83], v6 offset:40960\n" \
"v_mfma_f32_16x16x32_bf16 v[164:167], a[20:23], a[40:43], v[164:167]\n" \
"v_mfma_f32_16x16x32_bf16 v[168:171], a[16:19], a[44:47], v[168:171]\n" \
"s_mov_b32 s46, 896\n" \
"s_add_u32 m0, s50, 16384\n" \
"ds_read_b64_tr_b16 a[112:113], v9 offset:50176\n" \
"v_mfma_f32_16x16x32_bf16 v[172:175], a[20:23], a[44:47], v[172:175]\n" \
"buffer_load_dwordx4 v17, s[28:31], s46 offen lds\n" \
"v_mfma_f32_16x16x32_bf16 v[176:179], a[24:27], a[40:43], v[176:179]\n" \
"ds_read_b64_tr_b16 a[114:115], v10 offset:50176\n" \
"v_mfma_f32_16x16x32_bf16 v[180:183], a[28:31], a[40:43], v[180:183]\n" \
"v_mfma_f32_16x16x32_bf16 v[184:187], a[24:27], a[44:47], v[184:187]\n" \
"s_add_u32 m0, s22, 33664\n" \
"ds_read_b128 a[84:87], v6 offset:43008\n" \
"v_mfma_f32_16x16x32_bf16 v[188:191], a[28:31], a[44:47], v[188:191]\n" \
"buffer_load_dwordx4 v21, s[32:35], s47 offen offset:128 lds\n" \
"v_mfma_f32_16x16x32_bf16 v[192:195], a[0:3], a[48:51], v[192:195]\n" \
"ds_read_b64_tr_b16 a[116:117], v11 offset:50176\n" \
"v_mfma_f32_16x16x32_bf16 v[196:199], a[4:7], a[48:51], v[196:199]\n" \
"v_mfma_f32_16x16x32_bf16 v[200:203], a[0:3], a[52:55], v[200:203]\n" \
"s_add_u32 m0, s50, 20480\n" \
"ds_read_b64_tr_b16 a[118:119], v12 offset:50176\n" \
"v_mfma_f32_16x16x32_bf16 v[204:207], a[4:7], a[52:55], v[204:207]\n" \
"buffer_load_dwordx4 v18, s[28:31], s46 offen lds\n" \
"v_mfma_f32_16x16x32_bf16 v[208:211], a[8:11], a[48:51], v[208:211]\n" \
"ds_read_b128 a[88:91], v6 offset:45056\n" \
"v_mfma_f32_16x16x32_bf16 v[212:215], a[12:15], a[48:51], v[212:215]\n" \
"v_mfma_f32_16x16x32_bf16 v[216:219], a[8:11], a[52:55], v[216:219]\n" \
"s_add_u32 m0, s22, 34560\n" \
"ds_read_b64_tr_b16 a[120:121], v9 offset:50688\n" \
"v_mfma_f32_16x16x32_bf16 v[220:223], a[12:15], a[52:55], v[220:223]\n" \
"buffer_load_dwordx4 v21, s[32:35], s47 offen offset:256 lds\n" \
"v_mfma_f32_16x16x32_bf16 v[224:227], a[16:19], a[48:51], v[224:227]\n" \
"ds_read_b64_tr_b16 a[122:123], v10 offset:50688\n" \
"v_mfma_f32_16x16x32_bf16 v[228:231], a[20:23], a[48:51], v[228:231]\n" \
"v_mfma_f32_16x16x32_bf16 v[232:235], a[16:19], a[52:55], v[232:235]\n" \
"s_add_u32 m0, s50, 24576\n" \
"ds_read_b128 a[92:95], v6 offset:47104\n" \
"v_mfma_f32_16x16x32_bf16 v[236:239], a[20:23], a[52:55], v[236:239]\n" \
"buffer_load_dwordx4 v19, s[28:31], s46 offen lds\n" \
"v_mfma_f32_16x16x32_bf16 v[240:243], a[24:27], a[48:51], v[240:243]\n" \
"ds_read_b64_tr_b16 a[124:125], v11 offset:50688\n" \
"v_mfma_f32_16x16x32_bf16 v[244:247], a[28:31], a[48:51], v[244:247]\n" \
"v_mfma_f32_16x16x32_bf16 v[248:251], a[24:27], a[52:55], v[248:251]\n" \
"s_add_u32 m0, s22, 35456\n" \
"ds_read_b64_tr_b16 a[126:127], v12 offset:50688\n" \
"v_mfma_f32_16x16x32_bf16 v[252:255], a[28:31], a[52:55], v[252:255]\n" \
"buffer_load_dwordx4 v21, s[32:35], s47 offen offset:384 lds\n" \
"v_mfma_f32_16x16x32_bf16 a[128:131], a[0:3], a[56:59], a[128:131]\n" \
"v_mfma_f32_16x16x32_bf16 a[132:135], a[4:7], a[56:59], a[132:135]\n" \
"v_mfma_f32_16x16x32_bf16 a[136:139], a[0:3], a[60:63], a[136:139]\n" \
"s_add_u32 m0, s50, 28672\n" \
"v_mfma_f32_16x16x32_bf16 a[140:143], a[4:7], a[60:63], a[140:143]\n" \
"buffer_load_dwordx4 v20, s[28:31], s46 offen lds\n" \
"v_mfma_f32_16x16x32_bf16 a[144:147], a[8:11], a[56:59], a[144:147]\n" \
"v_mfma_f32_16x16x32_bf16 a[148:151], a[12:15], a[56:59], a[148:151]\n" \
"v_mfma_f32_16x16x32_bf16 a[152:155], a[8:11], a[60:63], a[152:155]\n" \
"v_mfma_f32_16x16x32_bf16 a[156:159], a[12:15], a[60:63], a[156:159]\n" \
"s_waitcnt vmcnt(29) lgkmcnt(0)\n" \
"v_mfma_f32_16x16x32_bf16 a[160:163], a[16:19], a[56:59], a[160:163]\n" \
"s_barrier\n" \
"v_mfma_f32_16x16x32_bf16 a[164:167], a[20:23], a[56:59], a[164:167]\n" \
"v_mfma_f32_16x16x32_bf16 a[168:171], a[16:19], a[60:63], a[168:171]\n" \
"v_mfma_f32_16x16x32_bf16 a[172:175], a[20:23], a[60:63], a[172:175]\n" \
"v_mfma_f32_16x16x32_bf16 a[176:179], a[24:27], a[56:59], a[176:179]\n" \
"v_mfma_f32_16x16x32_bf16 a[180:183], a[28:31], a[56:59], a[180:183]\n" \
"v_mfma_f32_16x16x32_bf16 a[184:187], a[24:27], a[60:63], a[184:187]\n" \
"v_mfma_f32_16x16x32_bf16 a[188:191], a[28:31], a[60:63], a[188:191]\n" \
"v_mfma_f32_16x16x32_bf16 v[64:67], a[64:67], a[96:99], v[64:67]\n" \
"ds_read_b128 a[0:3], v7 offset:0\n" \
"v_mfma_f32_16x16x32_bf16 v[68:71], a[68:71], a[96:99], v[68:71]\n" \
"s_mov_b64 s[28:29], s[64:65]\n" \
"v_mfma_f32_16x16x32_bf16 v[72:75], a[64:67], a[100:103], v[72:75]\n" \
"s_mov_b32 s47, 3932160\n" \
"s_add_u32 m0, s22, 49152\n" \
"ds_read_b64_tr_b16 a[32:33], v9 offset:0\n" \
"v_mfma_f32_16x16x32_bf16 v[76:79], a[68:71], a[100:103], v[76:79]\n" \
"buffer_load_dwordx4 v21, s[32:35], s47 offen offset:0 lds\n" \
"v_mfma_f32_16x16x32_bf16 v[80:83], a[72:75], a[96:99], v[80:83]\n" \
"ds_read_b64_tr_b16 a[34:35], v10 offset:0\n" \
"v_mfma_f32_16x16x32_bf16 v[84:87], a[76:79], a[96:99], v[84:87]\n" \
"s_and_b32 s29, s29, 0xffff\n" \
"v_mfma_f32_16x16x32_bf16 v[88:91], a[72:75], a[100:103], v[88:91]\n" \
"ds_read_b128 a[4:7], v7 offset:2048\n" \
"v_mfma_f32_16x16x32_bf16 v[92:95], a[76:79], a[100:103], v[92:95]\n" \
"s_mov_b32 s30, 4194304\n" \
"v_mfma_f32_16x16x32_bf16 v[96:99], a[80:83], a[96:99], v[96:99]\n" \
"s_add_u32 m0, s22, 50048\n" \
"ds_read_b64_tr_b16 a[36:37], v11 offset:0\n" \
"v_mfma_f32_16x16x32_bf16 v[100:103], a[84:87], a[96:99], v[100:103]\n" \
"buffer_load_dwordx4 v21, s[32:35], s47 offen offset:128 lds\n" \
"v_mfma_f32_16x16x32_bf16 v[104:107], a[80:83], a[100:103], v[104:107]\n" \
"ds_read_b64_tr_b16 a[38:39], v12 offset:0\n" \
"v_mfma_f32_16x16x32_bf16 v[108:111], a[84:87], a[100:103], v[108:111]\n" \
"s_lshl_b32 s60, s7, 18\n" \
"v_mfma_f32_16x16x32_bf16 v[112:115], a[88:91], a[96:99], v[112:115]\n" \
"ds_read_b128 a[8:11], v7 offset:4096\n" \
"v_mfma_f32_16x16x32_bf16 v[116:119], a[92:95], a[96:99], v[116:119]\n" \
"s_sub_u32 s61, 3932160, s60\n" \
"v_mfma_f32_16x16x32_bf16 v[120:123], a[88:91], a[100:103], v[120:123]\n" \
"s_add_u32 m0, s22, 50944\n" \
"ds_read_b64_tr_b16 a[40:41], v9 offset:512\n" \
"v_mfma_f32_16x16x32_bf16 v[124:127], a[92:95], a[100:103], v[124:127]\n" \
"buffer_load_dwordx4 v21, s[32:35], s47 offen offset:256 lds\n" \
"v_mfma_f32_16x16x32_bf16 v[128:131], a[64:67], a[104:107], v[128:131]\n" \
"ds_read_b64_tr_b16 a[42:43], v10 offset:512\n" \
"v_mfma_f32_16x16x32_bf16 v[132:135], a[68:71], a[104:107], v[132:135]\n" \
"v_add_u32 v13, s60, v63\n" \
"v_mfma_f32_16x16x32_bf16 v[136:139], a[64:67], a[108:111], v[136:139]\n" \
"ds_read_b128 a[12:15], v7 offset:6144\n" \
"v_mfma_f32_16x16x32_bf16 v[140:143], a[68:71], a[108:111], v[140:143]\n" \
"v_add_u32 v17, s61, v63\n" \
"v_mfma_f32_16x16x32_bf16 v[144:147], a[72:75], a[104:107], v[144:147]\n" \
"s_add_u32 m0, s22, 51840\n" \
"ds_read_b64_tr_b16 a[44:45], v11 offset:512\n" \
"v_mfma_f32_16x16x32_bf16 v[148:151], a[76:79], a[104:107], v[148:151]\n" \
"buffer_load_dwordx4 v21, s[32:35], s47 offen offset:384 lds\n" \
"v_mfma_f32_16x16x32_bf16 v[152:155], a[72:75], a[108:111], v[152:155]\n" \
"ds_read_b64_tr_b16 a[46:47], v12 offset:512\n" \
"v_mfma_f32_16x16x32_bf16 v[156:159], a[76:79], a[108:111], v[156:159]\n" \
"v_add_u32 v14, 0x10000, v13\n" \
"v_mfma_f32_16x16x32_bf16 v[160:163], a[80:83], a[104:107], v[160:163]\n" \
"ds_read_b128 a[16:19], v7 offset:8192\n" \
"v_mfma_f32_16x16x32_bf16 v[164:167], a[84:87], a[104:107], v[164:167]\n" \
"v_add_u32 v18, 0x10000, v17\n" \
"v_mfma_f32_16x16x32_bf16 v[168:171], a[80:83], a[108:111], v[168:171]\n" \
"ds_read_b64_tr_b16 a[48:49], v9 offset:1024\n" \
"v_mfma_f32_16x16x32_bf16 v[172:175], a[84:87], a[108:111], v[172:175]\n" \
"v_mfma_f32_16x16x32_bf16 v[176:179], a[88:91], a[104:107], v[176:179]\n" \
"ds_read_b64_tr_b16 a[50:51], v10 offset:1024\n" \
"v_mfma_f32_16x16x32_bf16 v[180:183], a[92:95], a[104:107], v[180:183]\n" \
"v_add_u32 v15, 0x10000, v14\n" \
"v_mfma_f32_16x16x32_bf16 v[184:187], a[88:91], a[108:111], v[184:187]\n" \
"ds_read_b128 a[20:23], v7 offset:10240\n" \
"v_mfma_f32_16x16x32_bf16 v[188:191], a[92:95], a[108:111], v[188:191]\n" \
"v_add_u32 v19, 0x10000, v18\n" \
"v_mfma_f32_16x16x32_bf16 v[192:195], a[64:67], a[112:115], v[192:195]\n" \
"ds_read_b64_tr_b16 a[52:53], v11 offset:1024\n" \
"v_mfma_f32_16x16x32_bf16 v[196:199], a[68:71], a[112:115], v[196:199]\n" \
"v_mfma_f32_16x16x32_bf16 v[200:203], a[64:67], a[116:119], v[200:203]\n" \
"ds_read_b64_tr_b16 a[54:55], v12 offset:1024\n" \
"v_mfma_f32_16x16x32_bf16 v[204:207], a[68:71], a[116:119], v[204:207]\n" \
"v_add_u32 v16, 0x10000, v15\n" \
"v_mfma_f32_16x16x32_bf16 v[208:211], a[72:75], a[112:115], v[208:211]\n" \
"ds_read_b128 a[24:27], v7 offset:12288\n" \
"v_mfma_f32_16x16x32_bf16 v[212:215], a[76:79], a[112:115], v[212:215]\n" \
"v_add_u32 v20, 0x10000, v19\n" \
"v_mfma_f32_16x16x32_bf16 v[216:219], a[72:75], a[116:119], v[216:219]\n" \
"ds_read_b64_tr_b16 a[56:57], v9 offset:1536\n" \
"v_mfma_f32_16x16x32_bf16 v[220:223], a[76:79], a[116:119], v[220:223]\n" \
"v_mfma_f32_16x16x32_bf16 v[224:227], a[80:83], a[112:115], v[224:227]\n" \
"ds_read_b64_tr_b16 a[58:59], v10 offset:1536\n" \
"v_mfma_f32_16x16x32_bf16 v[228:231], a[84:87], a[112:115], v[228:231]\n" \
"v_mfma_f32_16x16x32_bf16 v[232:235], a[80:83], a[116:119], v[232:235]\n" \
"ds_read_b128 a[28:31], v7 offset:14336\n" \
"v_mfma_f32_16x16x32_bf16 v[236:239], a[84:87], a[116:119], v[236:239]\n" \
"v_mfma_f32_16x16x32_bf16 v[240:243], a[88:91], a[112:115], v[240:243]\n" \
"ds_read_b64_tr_b16 a[60:61], v11 offset:1536\n" \
"v_mfma_f32_16x16x32_bf16 v[244:247], a[92:95], a[112:115], v[244:247]\n" \
"v_mfma_f32_16x16x32_bf16 v[248:251], a[88:91], a[116:119], v[248:251]\n" \
"ds_read_b64_tr_b16 a[62:63], v12 offset:1536\n" \
"v_mfma_f32_16x16x32_bf16 v[252:255], a[92:95], a[116:119], v[252:255]\n" \
"v_mfma_f32_16x16x32_bf16 a[128:131], a[64:67], a[120:123], a[128:131]\n" \
"v_mfma_f32_16x16x32_bf16 a[132:135], a[68:71], a[120:123], a[132:135]\n" \
"v_mfma_f32_16x16x32_bf16 a[136:139], a[64:67], a[124:127], a[136:139]\n" \
"v_mfma_f32_16x16x32_bf16 a[140:143], a[68:71], a[124:127], a[140:143]\n" \
"v_mfma_f32_16x16x32_bf16 a[144:147], a[72:75], a[120:123], a[144:147]\n" \
"v_mfma_f32_16x16x32_bf16 a[148:151], a[76:79], a[120:123], a[148:151]\n" \
"v_mfma_f32_16x16x32_bf16 a[152:155], a[72:75], a[124:127], a[152:155]\n" \
"v_mfma_f32_16x16x32_bf16 a[156:159], a[76:79], a[124:127], a[156:159]\n" \
"s_waitcnt vmcnt(24) lgkmcnt(0)\n" \
"v_mfma_f32_16x16x32_bf16 a[160:163], a[80:83], a[120:123], a[160:163]\n" \
"s_barrier\n" \
"v_mfma_f32_16x16x32_bf16 a[164:167], a[84:87], a[120:123], a[164:167]\n" \
"v_mfma_f32_16x16x32_bf16 a[168:171], a[80:83], a[124:127], a[168:171]\n" \
"v_mfma_f32_16x16x32_bf16 a[172:175], a[84:87], a[124:127], a[172:175]\n" \
"v_mfma_f32_16x16x32_bf16 a[176:179], a[88:91], a[120:123], a[176:179]\n" \
"v_mfma_f32_16x16x32_bf16 a[180:183], a[92:95], a[120:123], a[180:183]\n" \
"v_mfma_f32_16x16x32_bf16 a[184:187], a[88:91], a[124:127], a[184:187]\n" \
"v_mfma_f32_16x16x32_bf16 a[188:191], a[92:95], a[124:127], a[188:191]\n" \
"v_mfma_f32_16x16x32_bf16 v[64:67], a[0:3], a[32:35], v[64:67]\n" \
"ds_read_b128 a[64:67], v8 offset:0\n" \
"v_mfma_f32_16x16x32_bf16 v[68:71], a[4:7], a[32:35], v[68:71]\n" \
"s_mov_b64 s[32:33], s[44:45]\n" \
"v_mfma_f32_16x16x32_bf16 v[72:75], a[0:3], a[36:39], v[72:75]\n" \
"ds_read_b64_tr_b16 a[96:97], v9 offset:16384\n" \
"v_mfma_f32_16x16x32_bf16 v[76:79], a[4:7], a[36:39], v[76:79]\n" \
"buffer_load_dwordx2 v[58:59], v57, s[36:39], 0 offen sc1\n" \
"v_mfma_f32_16x16x32_bf16 v[80:83], a[8:11], a[32:35], v[80:83]\n" \
"ds_read_b64_tr_b16 a[98:99], v10 offset:16384\n" \
"v_mfma_f32_16x16x32_bf16 v[84:87], a[12:15], a[32:35], v[84:87]\n" \
"s_mov_b32 s34, s49\n" \
"v_mfma_f32_16x16x32_bf16 v[88:91], a[8:11], a[36:39], v[88:91]\n" \
"ds_read_b128 a[68:71], v8 offset:2048\n" \
"v_mfma_f32_16x16x32_bf16 v[92:95], a[12:15], a[36:39], v[92:95]\n" \
"v_mfma_f32_16x16x32_bf16 v[96:99], a[16:19], a[32:35], v[96:99]\n" \
"ds_read_b64_tr_b16 a[100:101], v11 offset:16384\n" \
"v_mfma_f32_16x16x32_bf16 v[100:103], a[20:23], a[32:35], v[100:103]\n" \
"s_mov_b64 exec, 0xffff\nglobal_load_dword a244, v1, s[76:77]\ns_mov_b64 exec, -1\n" \
"v_mfma_f32_16x16x32_bf16 v[104:107], a[16:19], a[36:39], v[104:107]\n" \
"ds_read_b64_tr_b16 a[102:103], v12 offset:16384\n" \
"v_mfma_f32_16x16x32_bf16 v[108:111], a[20:23], a[36:39], v[108:111]\n" \
"v_mfma_f32_16x16x32_bf16 v[112:115], a[24:27], a[32:35], v[112:115]\n" \
"ds_read_b128 a[72:75], v8 offset:4096\n" \
"v_mfma_f32_16x16x32_bf16 v[116:119], a[28:31], a[32:35], v[116:119]\n" \
"v_mfma_f32_16x16x32_bf16 v[120:123], a[24:27], a[36:39], v[120:123]\n" \
"ds_read_b64_tr_b16 a[104:105], v9 offset:16896\n" \
"v_mfma_f32_16x16x32_bf16 v[124:127], a[28:31], a[36:39], v[124:127]\n" \
"v_mfma_f32_16x16x32_bf16 v[128:131], a[0:3], a[40:43], v[128:131]\n" \
"ds_read_b64_tr_b16 a[106:107], v10 offset:16896\n" \
"v_mfma_f32_16x16x32_bf16 v[132:135], a[4:7], a[40:43], v[132:135]\n" \
"v_mfma_f32_16x16x32_bf16 v[136:139], a[0:3], a[44:47], v[136:139]\n" \
"ds_read_b128 a[76:79], v8 offset:6144\n" \
"v_mfma_f32_16x16x32_bf16 v[140:143], a[4:7], a[44:47], v[140:143]\n" \
"v_mfma_f32_16x16x32_bf16 v[144:147], a[8:11], a[40:43], v[144:147]\n" \
"ds_read_b64_tr_b16 a[108:109], v11 offset:16896\n" \
"v_mfma_f32_16x16x32_bf16 v[148:151], a[12:15], a[40:43], v[148:151]\n" \
"v_mfma_f32_16x16x32_bf16 v[152:155], a[8:11], a[44:47], v[152:155]\n" \
"ds_read_b64_tr_b16 a[110:111], v12 offset:16896\n" \
"v_mfma_f32_16x16x32_bf16 v[156:159], a[12:15], a[44:47], v[156:159]\n" \
"v_mfma_f32_16x16x32_bf16 v[160:163], a[16:19], a[40:43], v[160:163]\n" \
"ds_read_b128 a[80:83], v8 offset:8192\n" \
"v_mfma_f32_16x16x32_bf16 v[164:167], a[20:23], a[40:43], v[164:167]\n" \
"v_mfma_f32_16x16x32_bf16 v[168:171], a[16:19], a[44:47], v[168:171]\n" \
"ds_read_b64_tr_b16 a[112:113], v9 offset:17408\n" \
"v_mfma_f32_16x16x32_bf16 v[172:175], a[20:23], a[44:47], v[172:175]\n" \
"v_mfma_f32_16x16x32_bf16 v[176:179], a[24:27], a[40:43], v[176:179]\n" \
"ds_read_b64_tr_b16 a[114:115], v10 offset:17408\n" \
"v_mfma_f32_16x16x32_bf16 v[180:183], a[28:31], a[40:43], v[180:183]\n" \
"v_mfma_f32_16x16x32_bf16 v[184:187], a[24:27], a[44:47], v[184:187]\n" \
"ds_read_b128 a[84:87], v8 offset:10240\n" \
"v_mfma_f32_16x16x32_bf16 v[188:191], a[28:31], a[44:47], v[188:191]\n" \
"v_mfma_f32_16x16x32_bf16 v[192:195], a[0:3], a[48:51], v[192:195]\n" \
"ds_read_b64_tr_b16 a[116:117], v11 offset:17408\n" \
"v_mfma_f32_16x16x32_bf16 v[196:199], a[4:7], a[48:51], v[196:199]\n" \
"v_mfma_f32_16x16x32_bf16 v[200:203], a[0:3], a[52:55], v[200:203]\n" \
"ds_read_b64_tr_b16 a[118:119], v12 offset:17408\n" \
"v_mfma_f32_16x16x32_bf16 v[204:207], a[4:7], a[52:55], v[204:207]\n" \
"v_mfma_f32_16x16x32_bf16 v[208:211], a[8:11], a[48:51], v[208:211]\n" \
"ds_read_b128 a[88:91], v8 offset:12288\n" \
"v_mfma_f32_16x16x32_bf16 v[212:215], a[12:15], a[48:51], v[212:215]\n" \
"v_mfma_f32_16x16x32_bf16 v[216:219], a[8:11], a[52:55], v[216:219]\n" \
"ds_read_b64_tr_b16 a[120:121], v9 offset:17920\n" \
"v_mfma_f32_16x16x32_bf16 v[220:223], a[12:15], a[52:55], v[220:223]\n" \
"v_mfma_f32_16x16x32_bf16 v[224:227], a[16:19], a[48:51], v[224:227]\n" \
"ds_read_b64_tr_b16 a[122:123], v10 offset:17920\n" \
"v_mfma_f32_16x16x32_bf16 v[228:231], a[20:23], a[48:51], v[228:231]\n" \
"v_mfma_f32_16x16x32_bf16 v[232:235], a[16:19], a[52:55], v[232:235]\n" \
"ds_read_b128 a[92:95], v8 offset:14336\n" \
"v_mfma_f32_16x16x32_bf16 v[236:239], a[20:23], a[52:55], v[236:239]\n" \
"v_mfma_f32_16x16x32_bf16 v[240:243], a[24:27], a[48:51], v[240:243]\n" \
"ds_read_b64_tr_b16 a[124:125], v11 offset:17920\n" \
"v_mfma_f32_16x16x32_bf16 v[244:247], a[28:31], a[48:51], v[244:247]\n" \
"v_mfma_f32_16x16x32_bf16 v[248:251], a[24:27], a[52:55], v[248:251]\n" \
"ds_read_b64_tr_b16 a[126:127], v12 offset:17920\n" \
"v_mfma_f32_16x16x32_bf16 v[252:255], a[28:31], a[52:55], v[252:255]\n" \
"v_mfma_f32_16x16x32_bf16 a[128:131], a[0:3], a[56:59], a[128:131]\n" \
"v_mfma_f32_16x16x32_bf16 a[132:135], a[4:7], a[56:59], a[132:135]\n" \
"v_mfma_f32_16x16x32_bf16 a[136:139], a[0:3], a[60:63], a[136:139]\n" \
"v_mfma_f32_16x16x32_bf16 a[140:143], a[4:7], a[60:63], a[140:143]\n" \
"v_mfma_f32_16x16x32_bf16 a[144:147], a[8:11], a[56:59], a[144:147]\n" \
"v_mfma_f32_16x16x32_bf16 a[148:151], a[12:15], a[56:59], a[148:151]\n" \
"v_mfma_f32_16x16x32_bf16 a[152:155], a[8:11], a[60:63], a[152:155]\n" \
"v_mfma_f32_16x16x32_bf16 a[156:159], a[12:15], a[60:63], a[156:159]\n" \
"s_waitcnt vmcnt(1) lgkmcnt(0)\n" \
"v_mfma_f32_16x16x32_bf16 a[160:163], a[16:19], a[56:59], a[160:163]\n" \
"s_barrier\n" \
"v_mfma_f32_16x16x32_bf16 a[164:167], a[20:23], a[56:59], a[164:167]\n" \
"v_cmp_ne_u64 vcc, s[14:15], v[58:59]\n" \
"v_mfma_f32_16x16x32_bf16 a[168:171], a[16:19], a[60:63], a[168:171]\n" \
"v_mfma_f32_16x16x32_bf16 a[172:175], a[20:23], a[60:63], a[172:175]\n" \
"s_cbranch_vccnz LPUV_%=\nLPUVR_%=:\n" \
"v_mfma_f32_16x16x32_bf16 a[176:179], a[24:27], a[56:59], a[176:179]\n" \
"v_mfma_f32_16x16x32_bf16 a[180:183], a[28:31], a[56:59], a[180:183]\n" \
"v_mfma_f32_16x16x32_bf16 a[184:187], a[24:27], a[60:63], a[184:187]\n" \
"v_mfma_f32_16x16x32_bf16 a[188:191], a[28:31], a[60:63], a[188:191]\n" \
"v_mfma_f32_16x16x32_bf16 v[64:67], a[64:67], a[96:99], v[64:67]\n" \
"ds_read_b128 a[0:3], v5 offset:0\n" \
"v_mfma_f32_16x16x32_bf16 v[68:71], a[68:71], a[96:99], v[68:71]\n" \
"v_mfma_f32_16x16x32_bf16 v[72:75], a[64:67], a[100:103], v[72:75]\n" \
"s_add_u32 m0, s56, 10304\n" \
"ds_read_b64_tr_b16 a[32:33], v9 offset:32768\n" \
"v_mfma_f32_16x16x32_bf16 v[76:79], a[68:71], a[100:103], v[76:79]\n" \
"buffer_load_dwordx4 v26, s[72:75], 0 offen offset:0 sc1 lds\n" \
"v_mfma_f32_16x16x32_bf16 v[80:83], a[72:75], a[96:99], v[80:83]\n" \
"ds_read_b64_tr_b16 a[34:35], v10 offset:32768\n" \
"v_mfma_f32_16x16x32_bf16 v[84:87], a[76:79], a[96:99], v[84:87]\n" \
"v_mfma_f32_16x16x32_bf16 v[88:91], a[72:75], a[100:103], v[88:91]\n" \
"s_add_u32 m0, s56, 11328\n" \
"ds_read_b128 a[4:7], v5 offset:2048\n" \
"v_mfma_f32_16x16x32_bf16 v[92:95], a[76:79], a[100:103], v[92:95]\n" \
"buffer_load_dwordx4 v27, s[72:75], 0 offen offset:0 sc1 lds\n" \
"v_mfma_f32_16x16x32_bf16 v[96:99], a[80:83], a[96:99], v[96:99]\n" \
"ds_read_b64_tr_b16 a[36:37], v11 offset:32768\n" \
"v_mfma_f32_16x16x32_bf16 v[100:103], a[84:87], a[96:99], v[100:103]\n" \
"v_mfma_f32_16x16x32_bf16 v[104:107], a[80:83], a[100:103], v[104:107]\n" \
"s_add_u32 m0, s56, 11328\n" \
"ds_read_b64_tr_b16 a[38:39], v12 offset:32768\n" \
"v_mfma_f32_16x16x32_bf16 v[108:111], a[84:87], a[100:103], v[108:111]\n" \
"buffer_load_dwordx4 v27, s[72:75], 0 offen offset:1024 sc1 lds\n" \
"v_mfma_f32_16x16x32_bf16 v[112:115], a[88:91], a[96:99], v[112:115]\n" \
"ds_read_b128 a[8:11], v5 offset:4096\n" \
"v_mfma_f32_16x16x32_bf16 v[116:119], a[92:95], a[96:99], v[116:119]\n" \
"v_mfma_f32_16x16x32_bf16 v[120:123], a[88:91], a[100:103], v[120:123]\n" \
"s_add_u32 m0, s56, 11328\n" \
"ds_read_b64_tr_b16 a[40:41], v9 offset:33280\n" \
"v_mfma_f32_16x16x32_bf16 v[124:127], a[92:95], a[100:103], v[124:127]\n" \
"buffer_load_dwordx4 v27, s[72:75], 0 offen offset:2048 sc1 lds\n" \
"v_mfma_f32_16x16x32_bf16 v[128:131], a[64:67], a[104:107], v[128:131]\n" \
"ds_read_b64_tr_b16 a[42:43], v10 offset:33280\n" \
"v_mfma_f32_16x16x32_bf16 v[132:135], a[68:71], a[104:107], v[132:135]\n" \
"v_mfma_f32_16x16x32_bf16 v[136:139], a[64:67], a[108:111], v[136:139]\n" \
"s_add_u32 m0, s56, 11328\n" \
"ds_read_b128 a[12:15], v5 offset:6144\n" \
"v_mfma_f32_16x16x32_bf16 v[140:143], a[68:71], a[108:111], v[140:143]\n" \
"LH7_%=:\ns_cbranch_execz LH11_%=\n" \
"buffer_load_dwordx4 v27, s[72:75], 0 offen offset:3072 sc1 lds\n" \
"v_mfma_f32_16x16x32_bf16 v[144:147], a[72:75], a[104:107], v[144:147]\n" \
"ds_read_b64_tr_b16 a[44:45], v11 offset:33280\n" \
"s_waitcnt vmcnt(19)\n" \
"v_alignbyte_b32 v30, v31, v30, 2\n" \
"v_mfma_f32_16x16x32_bf16 v[148:151], a[76:79], a[104:107], v[148:151]\n" \
"v_alignbyte_b32 v31, v32, v31, 2\n" \
"v_alignbyte_b32 v32, v33, v32, 2\n" \
"v_mfma_f32_16x16x32_bf16 v[152:155], a[72:75], a[108:111], v[152:155]\n" \
"s_add_u32 m0, s56, 0\n" \
"ds_read_b64_tr_b16 a[46:47], v12 offset:33280\n" \
"v_alignbyte_b32 v33, v50, v33, 2\n" \
"v_mfma_f32_16x16x32_bf16 v[156:159], a[76:79], a[108:111], v[156:159]\n" \
"buffer_load_dwordx4 v23, s[68:71], 0 offen offset:0 lds\n" \
"v_mfma_f32_16x16x32_bf16 v[160:163], a[80:83], a[104:107], v[160:163]\n" \
"ds_read_b128 a[16:19], v5 offset:8192\n" \
"v_mfma_f32_16x16x32_bf16 v[164:167], a[84:87], a[104:107], v[164:167]\n" \
"ds_write_b128 v55, v[30:33] offset:5184\n" \
"v_alignbyte_b32 v34, v35, v34, 2\n" \
"v_mfma_f32_16x16x32_bf16 v[168:171], a[80:83], a[108:111], v[168:171]\n" \
"s_add_u32 m0, s56, 1024\n" \
"ds_read_b64_tr_b16 a[48:49], v9 offset:33792\n" \
"v_alignbyte_b32 v35, v36, v35, 2\n" \
"v_mfma_f32_16x16x32_bf16 v[172:175], a[84:87], a[108:111], v[172:175]\n" \
"buffer_load_dwordx4 v24, s[68:71], 0 offen offset:0 lds\n" \
"v_mfma_f32_16x16x32_bf16 v[176:179], a[88:91], a[104:107], v[176:179]\n" \
"ds_read_b64_tr_b16 a[50:51], v10 offset:33792\n" \
"v_alignbyte_b32 v36, v37, v36, 2\n" \
"v_mfma_f32_16x16x32_bf16 v[180:183], a[92:95], a[104:107], v[180:183]\n" \
"v_alignbyte_b32 v37, v51, v37, 2\n" \
"ds_write_b128 v55, v[34:37] offset:6208\n" \
"v_mfma_f32_16x16x32_bf16 v[184:187], a[88:91], a[108:111], v[184:187]\n" \
"s_add_u32 m0, s56, 1024\n" \
"ds_read_b128 a[20:23], v5 offset:10240\n" \
"v_alignbyte_b32 v38, v39, v38, 2\n" \
"v_mfma_f32_16x16x32_bf16 v[188:191], a[92:95], a[108:111], v[188:191]\n" \
"buffer_load_dwordx4 v24, s[68:71], 0 offen offset:1024 lds\n" \
"v_mfma_f32_16x16x32_bf16 v[192:195], a[64:67], a[112:115], v[192:195]\n" \
"ds_read_b64_tr_b16 a[52:53], v11 offset:33792\n" \
"v_alignbyte_b32 v39, v40, v39, 2\n" \
"v_mfma_f32_16x16x32_bf16 v[196:199], a[68:71], a[112:115], v[196:199]\n" \
"v_alignbyte_b32 v40, v41, v40, 2\n" \
"v_alignbyte_b32 v41, v52, v41, 2\n" \
"ds_write_b128 v55, v[38:41] offset:7232\n" \
"v_mfma_f32_16x16x32_bf16 v[200:203], a[64:67], a[116:119], v[200:203]\n" \
"s_add_u32 m0, s56, 1024\n" \
"ds_read_b64_tr_b16 a[54:55], v12 offset:33792\n" \
"v_alignbyte_b32 v42, v43, v42, 2\n" \
"v_mfma_f32_16x16x32_bf16 v[204:207], a[68:71], a[116:119], v[204:207]\n" \
"buffer_load_dwordx4 v24, s[68:71], 0 offen offset:2048 lds\n" \
"v_mfma_f32_16x16x32_bf16 v[208:211], a[72:75], a[112:115], v[208:211]\n" \
"ds_read_b128 a[24:27], v5 offset:12288\n" \
"v_alignbyte_b32 v43, v44, v43, 2\n" \
"v_mfma_f32_16x16x32_bf16 v[212:215], a[76:79], a[112:115], v[212:215]\n" \
"v_alignbyte_b32 v44, v45, v44, 2\n" \
"v_alignbyte_b32 v45, v53, v45, 2\n" \
"ds_write_b128 v55, v[42:45] offset:8256\n" \
"v_mfma_f32_16x16x32_bf16 v[216:219], a[72:75], a[116:119], v[216:219]\n" \
"s_add_u32 m0, s56, 1024\n" \
"ds_read_b64_tr_b16 a[56:57], v9 offset:34304\n" \
"v_alignbyte_b32 v46, v47, v46, 2\n" \
"v_mfma_f32_16x16x32_bf16 v[220:223], a[76:79], a[116:119], v[220:223]\n" \
"buffer_load_dwordx4 v24, s[68:71], 0 offen offset:3072 lds\n" \
"v_mfma_f32_16x16x32_bf16 v[224:227], a[80:83], a[112:115], v[224:227]\n" \
"ds_read_b64_tr_b16 a[58:59], v10 offset:34304\n" \
"v_alignbyte_b32 v47, v48, v47, 2\n" \
"v_mfma_f32_16x16x32_bf16 v[228:231], a[84:87], a[112:115], v[228:231]\n" \
"v_alignbyte_b32 v48, v49, v48, 2\n" \
"v_alignbyte_b32 v49, v54, v49, 2\n" \
"ds_write_b128 v55, v[46:49] offset:9280\n" \
"v_mfma_f32_16x16x32_bf16 v[232:235], a[80:83], a[116:119], v[232:235]\n" \
"s_mov_b32 s46, 0\n" \
"s_add_u32 m0, s50, 65536\n" \
"ds_read_b128 a[28:31], v5 offset:14336\n" \
"v_mfma_f32_16x16x32_bf16 v[236:239], a[84:87], a[116:119], v[236:239]\n" \
"buffer_load_dwordx4 v13, s[28:31], s46 offen lds\n" \
"v_mfma_f32_16x16x32_bf16 v[240:243], a[88:91], a[112:115], v[240:243]\n" \
"ds_read_b64_tr_b16 a[60:61], v11 offset:34304\n" \
"v_lshrrev_b32 v30, 4, v4\n" \
"v_mfma_f32_16x16x32_bf16 v[244:247], a[92:95], a[112:115], v[244:247]\n" \
"v_and_b32 v31, 15, v30\n" \
"v_lshrrev_b32 v32, 4, v30\n" \
"v_mfma_f32_16x16x32_bf16 v[248:251], a[88:91], a[116:119], v[248:251]\n" \
"s_add_u32 m0, s50, 69632\n" \
"ds_read_b64_tr_b16 a[62:63], v12 offset:34304\n" \
"v_add_u32 v33, 1, v31\n" \
"v_mfma_f32_16x16x32_bf16 v[252:255], a[92:95], a[116:119], v[252:255]\n" \
"buffer_load_dwordx4 v14, s[28:31], s46 offen lds\n" \
"v_mfma_f32_16x16x32_bf16 a[128:131], a[64:67], a[120:123], a[128:131]\n" \
"v_and_b32 v34, 1, v33\n" \
"v_and_b32 v33, 30, v33\n" \
"v_mfma_f32_16x16x32_bf16 a[132:135], a[68:71], a[120:123], a[132:135]\n" \
"v_mul_u32_u24 v34, 5184, v34\n" \
"v_lshl_add_u32 v33, v33, 1, v34\n" \
"v_mfma_f32_16x16x32_bf16 a[136:139], a[64:67], a[124:127], a[136:139]\n" \
"s_add_u32 m0, s50, 73728\n" \
"v_lshl_add_u32 v33, v32, 4, v33\n" \
"v_mfma_f32_16x16x32_bf16 a[140:143], a[68:71], a[124:127], a[140:143]\n" \
"buffer_load_dwordx4 v15, s[28:31], s46 offen lds\n" \
"v_mfma_f32_16x16x32_bf16 a[144:147], a[72:75], a[120:123], a[144:147]\n" \
"v_add_u32 v46, s56, v33\n" \
"v_add_u32 v47, 1024, v46\n" \
"v_mfma_f32_16x16x32_bf16 a[148:151], a[76:79], a[120:123], a[148:151]\n" \
"v_add_u32 v48, 2048, v46\n" \
"v_add_u32 v49, 3072, v46\n" \
"v_mfma_f32_16x16x32_bf16 a[152:155], a[72:75], a[124:127], a[152:155]\n" \
"v_lshlrev_b32 v33, 1, v31\n" \
"v_sub_u32 v33, v33, v32\n" \
"v_mfma_f32_16x16x32_bf16 a[156:159], a[76:79], a[124:127], a[156:159]\n" \
"s_waitcnt vmcnt(15) lgkmcnt(0)\n" \
"v_lshlrev_b32 v33, 4, v33\n" \
"v_mfma_f32_16x16x32_bf16 a[160:163], a[80:83], a[120:123], a[160:163]\n" \
"s_barrier\n" \
"v_mfma_f32_16x16x32_bf16 a[164:167], a[84:87], a[120:123], a[164:167]\n" \
"v_add_u32 v33, 10320, v33\n" \
"v_add_u32 v50, s56, v33\n" \
"v_mfma_f32_16x16x32_bf16 a[168:171], a[80:83], a[124:127], a[168:171]\n" \
"s_add_u32 m0, s50, 77824\n" \
"v_lshlrev_b32 v33, 5, v31\n" \
"v_mfma_f32_16x16x32_bf16 a[172:175], a[84:87], a[124:127], a[172:175]\n" \
"buffer_load_dwordx4 v16, s[28:31], s46 offen lds\n" \
"v_mfma_f32_16x16x32_bf16 a[176:179], a[88:91], a[120:123], a[176:179]\n" \
"v_add_u32 v33, s56, v33\n" \
"v_lshl_add_u32 v52, v32, 3, v33\n" \
"v_mfma_f32_16x16x32_bf16 a[180:183], a[92:95], a[120:123], a[180:183]\n" \
"v_xor_b32 v32, 1, v32\n" \
"v_lshl_add_u32 v51, v32, 3, v33\n" \
"v_mfma_f32_16x16x32_bf16 a[184:187], a[88:91], a[124:127], a[184:187]\n" \
"v_mfma_f32_16x16x32_bf16 a[188:191], a[92:95], a[124:127], a[188:191]\n" \
"v_mfma_f32_16x16x32_bf16 v[64:67], a[0:3], a[32:35], v[64:67]\n" \
"ds_read_b128 a[64:67], v6 offset:0\n" \
"v_mfma_f32_16x16x32_bf16 v[68:71], a[4:7], a[32:35], v[68:71]\n" \
"v_mfma_f32_16x16x32_bf16 v[72:75], a[0:3], a[36:39], v[72:75]\n" \
"s_mov_b32 s46, 0\n" \
"s_add_u32 m0, s50, 81920\n" \
"ds_read_b64_tr_b16 a[96:97], v9 offset:49152\n" \
"v_mfma_f32_16x16x32_bf16 v[76:79], a[4:7], a[36:39], v[76:79]\n" \
"buffer_load_dwordx4 v17, s[28:31], s46 offen lds\n" \
"v_mfma_f32_16x16x32_bf16 v[80:83], a[8:11], a[32:35], v[80:83]\n" \
"ds_read_b64_tr_b16 a[98:99], v10 offset:49152\n" \
"v_mfma_f32_16x16x32_bf16 v[84:87], a[12:15], a[32:35], v[84:87]\n" \
"v_mfma_f32_16x16x32_bf16 v[88:91], a[8:11], a[36:39], v[88:91]\n" \
"ds_read_b128 a[68:71], v6 offset:2048\n" \
"v_mfma_f32_16x16x32_bf16 v[92:95], a[12:15], a[36:39], v[92:95]\n" \
"v_mfma_f32_16x16x32_bf16 v[96:99], a[16:19], a[32:35], v[96:99]\n" \
"s_add_u32 m0, s50, 86016\n" \
"ds_read_b64_tr_b16 a[100:101], v11 offset:49152\n" \
"v_mfma_f32_16x16x32_bf16 v[100:103], a[20:23], a[32:35], v[100:103]\n" \
"buffer_load_dwordx4 v18, s[28:31], s46 offen lds\n" \
"v_mfma_f32_16x16x32_bf16 v[104:107], a[16:19], a[36:39], v[104:107]\n" \
"ds_read_b64_tr_b16 a[102:103], v12 offset:49152\n" \
"v_mfma_f32_16x16x32_bf16 v[108:111], a[20:23], a[36:39], v[108:111]\n" \
"v_mfma_f32_16x16x32_bf16 v[112:115], a[24:27], a[32:35], v[112:115]\n" \
"ds_read_b128 a[72:75], v6 offset:4096\n" \
"v_mfma_f32_16x16x32_bf16 v[116:119], a[28:31], a[32:35], v[116:119]\n" \
"v_mfma_f32_16x16x32_bf16 v[120:123], a[24:27], a[36:39], v[120:123]\n" \
"s_add_u32 m0, s50, 90112\n" \
"ds_read_b64_tr_b16 a[104:105], v9 offset:49664\n" \
"v_mfma_f32_16x16x32_bf16 v[124:127], a[28:31], a[36:39], v[124:127]\n" \
"buffer_load_dwordx4 v19, s[28:31], s46 offen lds\n" \
"v_mfma_f32_16x16x32_bf16 v[128:131], a[0:3], a[40:43], v[128:131]\n" \
"ds_read_b64_tr_b16 a[106:107], v10 offset:49664\n" \
"v_mfma_f32_16x16x32_bf16 v[132:135], a[4:7], a[40:43], v[132:135]\n" \
"v_mfma_f32_16x16x32_bf16 v[136:139], a[0:3], a[44:47], v[136:139]\n" \
"ds_read_b128 a[76:79], v6 offset:6144\n" \
"v_mfma_f32_16x16x32_bf16 v[140:143], a[4:7], a[44:47], v[140:143]\n" \
"v_mfma_f32_16x16x32_bf16 v[144:147], a[8:11], a[40:43], v[144:147]\n" \
"s_add_u32 m0, s50, 94208\n" \
"ds_read_b64_tr_b16 a[108:109], v11 offset:49664\n" \
"v_mfma_f32_16x16x32_bf16 v[148:151], a[12:15], a[40:43], v[148:151]\n" \
"buffer_load_dwordx4 v20, s[28:31], s46 offen lds\n" \
"v_mfma_f32_16x16x32_bf16 v[152:155], a[8:11], a[44:47], v[152:155]\n" \
"ds_read_b64_tr_b16 a[110:111], v12 offset:49664\n" \
"v_mfma_f32_16x16x32_bf16 v[156:159], a[12:15], a[44:47], v[156:159]\n" \
"v_mfma_f32_16x16x32_bf16 v[160:163], a[16:19], a[40:43], v[160:163]\n" \
"ds_read_b128 a[80:83], v6 offset:8192\n" \
"v_mfma_f32_16x16x32_bf16 v[164:167], a[20:23], a[40:43], v[164:167]\n" \
"v_mfma_f32_16x16x32_bf16 v[168:171], a[16:19], a[44:47], v[168:171]\n" \
"s_mov_b32 s47, 0\n" \
"s_add_u32 m0, s51, 32768\n" \
"ds_read_b64_tr_b16 a[112:113], v9 offset:50176\n" \
"v_mfma_f32_16x16x32_bf16 v[172:175], a[20:23], a[44:47], v[172:175]\n" \
"buffer_load_dwordx4 v21, s[32:35], s47 offen offset:0 lds\n" \
"v_mfma_f32_16x16x32_bf16 v[176:179], a[24:27], a[40:43], v[176:179]\n" \
"ds_read_b64_tr_b16 a[114:115], v10 offset:50176\n" \
"v_mfma_f32_16x16x32_bf16 v[180:183], a[28:31], a[40:43], v[180:183]\n" \
"v_mfma_f32_16x16x32_bf16 v[184:187], a[24:27], a[44:47], v[184:187]\n" \
"ds_read_b128 a[84:87], v6 offset:10240\n" \
"v_mfma_f32_16x16x32_bf16 v[188:191], a[28:31], a[44:47], v[188:191]\n" \
"v_mfma_f32_16x16x32_bf16 v[192:195], a[0:3], a[48:51], v[192:195]\n" \
"s_add_u32 m0, s51, 33664\n" \
"ds_read_b64_tr_b16 a[116:117], v11 offset:50176\n" \
"v_mfma_f32_16x16x32_bf16 v[196:199], a[4:7], a[48:51], v[196:199]\n" \
"buffer_load_dwordx4 v21, s[32:35], s47 offen offset:128 lds\n" \
"v_mfma_f32_16x16x32_bf16 v[200:203], a[0:3], a[52:55], v[200:203]\n" \
"ds_read_b64_tr_b16 a[118:119], v12 offset:50176\n" \
"v_mfma_f32_16x16x32_bf16 v[204:207], a[4:7], a[52:55], v[204:207]\n" \
"v_mfma_f32_16x16x32_bf16 v[208:211], a[8:11], a[48:51], v[208:211]\n" \
"ds_read_b128 a[88:91], v6 offset:12288\n" \
"v_mfma_f32_16x16x32_bf16 v[212:215], a[12:15], a[48:51], v[212:215]\n" \
"v_mfma_f32_16x16x32_bf16 v[216:219], a[8:11], a[52:55], v[216:219]\n" \
"s_mov_b32 s47, 262144\n" \
"s_add_u32 m0, s51, 40960\n" \
"ds_read_b64_tr_b16 a[120:121], v9 offset:50688\n" \
"v_mfma_f32_16x16x32_bf16 v[220:223], a[12:15], a[52:55], v[220:223]\n" \
"buffer_load_dwordx4 v21, s[32:35], s47 offen offset:0 lds\n" \
"v_mfma_f32_16x16x32_bf16 v[224:227], a[16:19], a[48:51], v[224:227]\n" \
"ds_read_b64_tr_b16 a[122:123], v10 offset:50688\n" \
"v_mfma_f32_16x16x32_bf16 v[228:231], a[20:23], a[48:51], v[228:231]\n" \
"v_mfma_f32_16x16x32_bf16 v[232:235], a[16:19], a[52:55], v[232:235]\n" \
"ds_read_b128 a[92:95], v6 offset:14336\n" \
"v_mfma_f32_16x16x32_bf16 v[236:239], a[20:23], a[52:55], v[236:239]\n" \
"v_mfma_f32_16x16x32_bf16 v[240:243], a[24:27], a[48:51], v[240:243]\n" \
"s_add_u32 m0, s51, 41856\n" \
"ds_read_b64_tr_b16 a[124:125], v11 offset:50688\n" \
"v_mfma_f32_16x16x32_bf16 v[244:247], a[28:31], a[48:51], v[244:247]\n" \
"buffer_load_dwordx4 v21, s[32:35], s47 offen offset:128 lds\n" \
"v_mfma_f32_16x16x32_bf16 v[248:251], a[24:27], a[52:55], v[248:251]\n" \
"ds_read_b64_tr_b16 a[126:127], v12 offset:50688\n" \
"v_mfma_f32_16x16x32_bf16 v[252:255], a[28:31], a[52:55], v[252:255]\n" \
"v_add_u32 v5, s54, v5\n" \
"v_mfma_f32_16x16x32_bf16 a[128:131], a[0:3], a[56:59], a[128:131]\n" \
"s_add_u32 s76, s76, 2097152\ns_addc_u32 s77, s77, 0\n" \
"v_add_u32 v6, s54, v6\n" \
"v_mfma_f32_16x16x32_bf16 a[132:135], a[4:7], a[56:59], a[132:135]\n" \
"s_mov_b64 exec, 0xffff\nglobal_load_dword a244, v1, s[76:77]\ns_mov_b64 exec, -1\n" \
"v_mfma_f32_16x16x32_bf16 a[136:139], a[0:3], a[60:63], a[136:139]\n" \
"v_add_u32 v7, s54, v7\n" \
"v_mfma_f32_16x16x32_bf16 a[140:143], a[4:7], a[60:63], a[140:143]\n" \
"v_add_u32 v8, s54, v8\n" \
"v_mfma_f32_16x16x32_bf16 a[144:147], a[8:11], a[56:59], a[144:147]\n" \
"v_sub_u32 v9, v9, v62\n" \
"v_mfma_f32_16x16x32_bf16 a[148:151], a[12:15], a[56:59], a[148:151]\n" \
"v_mfma_f32_16x16x32_bf16 a[152:155], a[8:11], a[60:63], a[152:155]\n" \
"v_sub_u32 v10, v10, v62\n" \
"v_mfma_f32_16x16x32_bf16 a[156:159], a[12:15], a[60:63], a[156:159]\n" \
"s_waitcnt lgkmcnt(0)\n" \
"v_mfma_f32_16x16x32_bf16 a[160:163], a[16:19], a[56:59], a[160:163]\n" \
"s_barrier\n" \
"v_mfma_f32_16x16x32_bf16 a[164:167], a[20:23], a[56:59], a[164:167]\n" \
"v_sub_u32 v11, v11, v62\n" \
"v_mfma_f32_16x16x32_bf16 a[168:171], a[16:19], a[60:63], a[168:171]\n" \
"v_sub_u32 v12, v12, v62\n" \
"v_mfma_f32_16x16x32_bf16 a[172:175], a[20:23], a[60:63], a[172:175]\n" \
"v_mfma_f32_16x16x32_bf16 a[176:179], a[24:27], a[56:59], a[176:179]\n" \
"v_mfma_f32_16x16x32_bf16 a[180:183], a[28:31], a[56:59], a[180:183]\n" \
"v_mfma_f32_16x16x32_bf16 a[184:187], a[24:27], a[60:63], a[184:187]\n" \
"v_mfma_f32_16x16x32_bf16 a[188:191], a[28:31], a[60:63], a[188:191]\n" \
"v_mfma_f32_16x16x32_bf16 v[64:67], a[64:67], a[96:99], v[64:67]\n" \
"s_waitcnt vmcnt(13) lgkmcnt(0)\n" \
"v_mfma_f32_16x16x32_bf16 v[68:71], a[68:71], a[96:99], v[68:71]\n" \
"v_mfma_f32_16x16x32_bf16 v[72:75], a[64:67], a[100:103], v[72:75]\n" \
"s_mov_b32 s46, 128\n" \
"s_add_u32 m0, s50, 0\n" \
"ds_read_b128 a[32:35], v50 offset:512\n" \
"v_mfma_f32_16x16x32_bf16 v[76:79], a[68:71], a[100:103], v[76:79]\n" \
"buffer_load_dwordx4 v13, s[28:31], s46 offen lds\n" \
"s_mov_b32 s96, s57\n" \
"v_mfma_f32_16x16x32_bf16 v[80:83], a[72:75], a[96:99], v[80:83]\n" \
"ds_read2_b32 a[0:1], v46 offset0:0 offset1:1\n" \
"v_mul_f32 v64, s20, v64\n" \
"v_mfma_f32_16x16x32_bf16 v[84:87], a[76:79], a[96:99], v[84:87]\n" \
"v_mul_f32 v65, s20, v65\n" \
"v_mul_f32 v66, s20, v66\n" \
"v_mfma_f32_16x16x32_bf16 v[88:91], a[72:75], a[100:103], v[88:91]\n" \
"s_add_u32 m0, s50, 4096\n" \
"ds_read2_b32 a[2:3], v46 offset0:2 offset1:3\n" \
"v_mul_f32 v67, s20, v67\n" \
"v_mfma_f32_16x16x32_bf16 v[92:95], a[76:79], a[100:103], v[92:95]\n" \
"buffer_load_dwordx4 v14, s[28:31], s46 offen lds\n" \
"v_mfma_f32_16x16x32_bf16 v[96:99], a[80:83], a[96:99], v[96:99]\n" \
"ds_read_b128 a[36:39], v50 offset:1024\n" \
"v_cvt_pk_bf16_f32 v30, v64, v65\n" \
"v_mfma_f32_16x16x32_bf16 v[100:103], a[84:87], a[96:99], v[100:103]\n" \
"v_cvt_pk_bf16_f32 v31, v66, v67\n" \
"v_mul_f32 v68, s20, v68\n" \
"v_mfma_f32_16x16x32_bf16 v[104:107], a[80:83], a[100:103], v[104:107]\n" \
"s_add_u32 m0, s50, 8192\n" \
"ds_read2_b32 a[4:5], v46 offset0:128 offset1:129\n" \
"v_mul_f32 v69, s20, v69\n" \
"v_mfma_f32_16x16x32_bf16 v[108:111], a[84:87], a[100:103], v[108:111]\n" \
"buffer_load_dwordx4 v15, s[28:31], s46 offen lds\n" \
"v_mfma_f32_16x16x32_bf16 v[112:115], a[88:91], a[96:99], v[112:115]\n" \
"ds_read2_b32 a[6:7], v46 offset0:130 offset1:131\n" \
"v_mul_f32 v70, s20, v70\n" \
"v_mfma_f32_16x16x32_bf16 v[116:119], a[92:95], a[96:99], v[116:119]\n" \
"v_mul_f32 v71, s20, v71\n" \
"v_cvt_pk_bf16_f32 v32, v68, v69\n" \
"v_mfma_f32_16x16x32_bf16 v[120:123], a[88:91], a[100:103], v[120:123]\n" \
"s_add_u32 m0, s50, 12288\n" \
"ds_read_b128 a[40:43], v50 offset:1536\n" \
"v_cvt_pk_bf16_f32 v33, v70, v71\n" \
"v_mfma_f32_16x16x32_bf16 v[124:127], a[92:95], a[100:103], v[124:127]\n" \
"buffer_load_dwordx4 v16, s[28:31], s46 offen lds\n" \
"v_mfma_f32_16x16x32_bf16 v[128:131], a[64:67], a[104:107], v[128:131]\n" \
"ds_read2_b32 a[8:9], v47 offset0:0 offset1:1\n" \
"v_mul_f32 v72, s20, v72\n" \
"v_mfma_f32_16x16x32_bf16 v[132:135], a[68:71], a[104:107], v[132:135]\n" \
"v_mul_f32 v73, s20, v73\n" \
"v_mul_f32 v74, s20, v74\n" \
"v_mfma_f32_16x16x32_bf16 v[136:139], a[64:67], a[108:111], v[136:139]\n" \
"s_mov_b32 s46, 128\n" \
"s_add_u32 m0, s50, 16384\n" \
"ds_read2_b32 a[10:11], v47 offset0:2 offset1:3\n" \
"v_mfma_f32_16x16x32_bf16 v[140:143], a[68:71], a[108:111], v[140:143]\n" \
"buffer_load_dwordx4 v17, s[28:31], s46 offen lds\n" \
"v_mfma_f32_16x16x32_bf16 v[144:147], a[72:75], a[104:107], v[144:147]\n" \
"ds_read_b128 a[44:47], v50 offset:2048\n" \
"v_mul_f32 v75, s20, v75\n" \
"v_mfma_f32_16x16x32_bf16 v[148:151], a[76:79], a[104:107], v[148:151]\n" \
"v_cvt_pk_bf16_f32 v34, v72, v73\n" \
"v_cvt_pk_bf16_f32 v35, v74, v75\n" \
"v_mfma_f32_16x16x32_bf16 v[152:155], a[72:75], a[108:111], v[152:155]\n" \
"s_add_u32 m0, s50, 20480\n" \
"ds_read2_b32 a[12:13], v47 offset0:128 offset1:129\n" \
"v_mul_f32 v76, s20, v76\n" \
"v_mfma_f32_16x16x32_bf16 v[156:159], a[76:79], a[108:111], v[156:159]\n" \
"buffer_load_dwordx4 v18, s[28:31], s46 offen lds\n" \
"v_mfma_f32_16x16x32_bf16 v[160:163], a[80:83], a[104:107], v[160:163]\n" \
"ds_read2_b32 a[14:15], v47 offset0:130 offset1:131\n" \
"v_mul_f32 v77, s20, v77\n" \
"v_mfma_f32_16x16x32_bf16 v[164:167], a[84:87], a[104:107], v[164:167]\n" \
"v_mul_f32 v78, s20, v78\n" \
"v_mul_f32 v79, s20, v79\n" \
"v_mfma_f32_16x16x32_bf16 v[168:171], a[80:83], a[108:111], v[168:171]\n" \
"s_add_u32 m0, s50, 24576\n" \
"ds_read_b128 a[48:51], v50 offset:2560\n" \
"v_cvt_pk_bf16_f32 v36, v76, v77\n" \
"v_mfma_f32_16x16x32_bf16 v[172:175], a[84:87], a[108:111], v[172:175]\n" \
"buffer_load_dwordx4 v19, s[28:31], s46 offen lds\n" \
"v_mfma_f32_16x16x32_bf16 v[176:179], a[88:91], a[104:107], v[176:179]\n" \
"ds_read2_b32 a[16:17], v48 offset0:0 offset1:1\n" \
"v_cvt_pk_bf16_f32 v37, v78, v79\n" \
"v_mfma_f32_16x16x32_bf16 v[180:183], a[92:95], a[104:107], v[180:183]\n" \
"v_mul_f32 v80, s20, v80\n" \
"v_mul_f32 v81, s20, v81\n" \
"v_mfma_f32_16x16x32_bf16 v[184:187], a[88:91], a[108:111], v[184:187]\n" \
"s_add_u32 m0, s50, 28672\n" \
"ds_read2_b32 a[18:19], v48 offset0:2 offset1:3\n" \
"v_mul_f32 v82, s20, v82\n" \
"v_mfma_f32_16x16x32_bf16 v[188:191], a[92:95], a[108:111], v[188:191]\n" \
"buffer_load_dwordx4 v20, s[28:31], s46 offen lds\n" \
"v_mfma_f32_16x16x32_bf16 v[192:195], a[64:67], a[112:115], v[192:195]\n" \
"ds_read_b128 a[52:55], v50 offset:3072\n" \
"v_mul_f32 v83, s20, v83\n" \
"v_mfma_f32_16x16x32_bf16 v[196:199], a[68:71], a[112:115], v[196:199]\n" \
"v_cvt_pk_bf16_f32 v38, v80, v81\n" \
"v_cvt_pk_bf16_f32 v39, v82, v83\n" \
"v_mfma_f32_16x16x32_bf16 v[200:203], a[64:67], a[116:119], v[200:203]\n" \
"s_mov_b32 s47, 524288\n" \
"s_add_u32 m0, s51, 49152\n" \
"ds_read2_b32 a[20:21], v48 offset0:128 offset1:129\n" \
"v_mfma_f32_16x16x32_bf16 v[204:207], a[68:71], a[116:119], v[204:207]\n" \
"buffer_load_dwordx4 v21, s[32:35], s47 offen offset:0 lds\n" \
"v_mfma_f32_16x16x32_bf16 v[208:211], a[72:75], a[112:115], v[208:211]\n" \
"ds_read2_b32 a[22:23], v48 offset0:130 offset1:131\n" \
"v_mul_f32 v84, s20, v84\n" \
"v_mfma_f32_16x16x32_bf16 v[212:215], a[76:79], a[112:115], v[212:215]\n" \
"v_mul_f32 v85, s20, v85\n" \
"v_mul_f32 v86, s20, v86\n" \
"v_mfma_f32_16x16x32_bf16 v[216:219], a[72:75], a[116:119], v[216:219]\n" \
"s_add_u32 m0, s51, 50048\n" \
"ds_read_b128 a[56:59], v50 offset:3584\n" \
"v_mul_f32 v87, s20, v87\n" \
"v_mfma_f32_16x16x32_bf16 v[220:223], a[76:79], a[116:119], v[220:223]\n" \
"buffer_load_dwordx4 v21, s[32:35], s47 offen offset:128 lds\n" \
"v_mfma_f32_16x16x32_bf16 v[224:227], a[80:83], a[112:115], v[224:227]\n" \
"ds_read2_b32 a[24:25], v49 offset0:0 offset1:1\n" \
"v_cvt_pk_bf16_f32 v40, v84, v85\n" \
"v_mfma_f32_16x16x32_bf16 v[228:231], a[84:87], a[112:115], v[228:231]\n" \
"v_cvt_pk_bf16_f32 v41, v86, v87\n" \
"v_mul_f32 v88, s20, v88\n" \
"v_mfma_f32_16x16x32_bf16 v[232:235], a[80:83], a[116:119], v[232:235]\n" \
"s_mov_b32 s47, 786432\n" \
"s_add_u32 m0, s51, 57344\n" \
"ds_read2_b32 a[26:27], v49 offset0:2 offset1:3\n" \
"v_mfma_f32_16x16x32_bf16 v[236:239], a[84:87], a[116:119], v[236:239]\n" \
"buffer_load_dwordx4 v21, s[32:35], s47 offen offset:0 lds\n" \
"v_mfma_f32_16x16x32_bf16 v[240:243], a[88:91], a[112:115], v[240:243]\n" \
"ds_read_b128 a[60:63], v50 offset:4096\n" \
"v_mul_f32 v89, s20, v89\n" \
"v_mfma_f32_16x16x32_bf16 v[244:247], a[92:95], a[112:115], v[244:247]\n" \
"v_mul_f32 v90, s20, v90\n" \
"v_mul_f32 v91, s20, v91\n" \
"v_mfma_f32_16x16x32_bf16 v[248:251], a[88:91], a[116:119], v[248:251]\n" \
"s_add_u32 m0, s51, 58240\n" \
"ds_read2_b32 a[28:29], v49 offset0:128 offset1:129\n" \
"v_cvt_pk_bf16_f32 v42, v88, v89\n" \
"v_mfma_f32_16x16x32_bf16 v[252:255], a[92:95], a[116:119], v[252:255]\n" \
"buffer_load_dwordx4 v21, s[32:35], s47 offen offset:128 lds\n" \
"v_mfma_f32_16x16x32_bf16 a[128:131], a[64:67], a[120:123], a[128:131]\n" \
"ds_read2_b32 a[30:31], v49 offset0:130 offset1:131\n" \
"v_cvt_pk_bf16_f32 v43, v90, v91\n" \
"v_mfma_f32_16x16x32_bf16 a[132:135], a[68:71], a[120:123], a[132:135]\n" \
"v_mul_f32 v92, s20, v92\n" \
"v_mul_f32 v93, s20, v93\n" \
"v_mfma_f32_16x16x32_bf16 a[136:139], a[64:67], a[124:127], a[136:139]\n" \
"s_add_u32 s76, s76, 2097152\ns_addc_u32 s77, s77, 0\n" \
"v_mul_f32 v94, s20, v94\n" \
"v_mfma_f32_16x16x32_bf16 a[140:143], a[68:71], a[124:127], a[140:143]\n" \
"s_mov_b64 exec, 0xffff\nglobal_load_dword a244, v1, s[76:77]\ns_mov_b64 exec, -1\n" \
"v_mfma_f32_16x16x32_bf16 a[144:147], a[72:75], a[120:123], a[144:147]\n" \
"v_mul_f32 v95, s20, v95\n" \
"v_cvt_pk_bf16_f32 v44, v92, v93\n" \
"v_mfma_f32_16x16x32_bf16 a[148:151], a[76:79], a[120:123], a[148:151]\n" \
"v_cvt_pk_bf16_f32 v45, v94, v95\n" \
"v_mul_f32 v96, s20, v96\n" \
"v_mfma_f32_16x16x32_bf16 a[152:155], a[72:75], a[124:127], a[152:155]\n" \
"v_mul_f32 v97, s20, v97\n" \
"v_mul_f32 v98, s20, v98\n" \
"v_mfma_f32_16x16x32_bf16 a[156:159], a[76:79], a[124:127], a[156:159]\n" \
"v_mul_f32 v99, s20, v99\n" \
"v_mfma_f32_16x16x32_bf16 a[160:163], a[80:83], a[120:123], a[160:163]\n" \
"v_mfma_f32_16x16x32_bf16 a[164:167], a[84:87], a[120:123], a[164:167]\n" \
"v_mfma_f32_16x16x32_bf16 a[168:171], a[80:83], a[124:127], a[168:171]\n" \
"s_add_u32 s76, s76, 2097152\ns_addc_u32 s77, s77, 0\n" \
"v_mfma_f32_16x16x32_bf16 a[172:175], a[84:87], a[124:127], a[172:175]\n" \
"s_mov_b64 exec, 0xffff\nglobal_load_dword a244, v1, s[76:77]\ns_mov_b64 exec, -1\n" \
"v_mfma_f32_16x16x32_bf16 a[176:179], a[88:91], a[120:123], a[176:179]\n" \
"v_mfma_f32_16x16x32_bf16 a[180:183], a[92:95], a[120:123], a[180:183]\n" \
"v_mfma_f32_16x16x32_bf16 a[184:187], a[88:91], a[124:127], a[184:187]\n" \
"s_waitcnt lgkmcnt(0)\n" \
"v_mfma_f32_16x16x32_bf16 a[188:191], a[92:95], a[124:127], a[188:191]\n" \
"buffer_store_dwordx4 v[30:33], v4, s[36:39], s96 offen offset:0 sc1\n" \
"s_add_u32 s97, s96, s98\n" \
"LC_%=:\n" \
"v_mfma_f32_16x16x32_bf16 v[64:67], a[0:3], a[32:35], 0\n" \
"ds_read_b128 a[96:99], v50 offset:448\n" \
"v_cvt_pk_bf16_f32 v30, v96, v97\n" \
"v_mfma_f32_16x16x32_bf16 v[68:71], a[0:3], a[36:39], 0\n" \
"ds_read2_b32 a[64:65], v46 offset0:16 offset1:17\n" \
"v_cvt_pk_bf16_f32 v31, v98, v99\n" \
"v_mfma_f32_16x16x32_bf16 v[68:71], a[4:7], a[32:35], v[68:71]\n" \
"ds_read2_b32 a[66:67], v46 offset0:18 offset1:19\n" \
"v_mul_f32 v100, s20, v100\n" \
"v_mfma_f32_16x16x32_bf16 v[72:75], a[4:7], a[36:39], 0\n" \
"ds_read_b128 a[100:103], v50 offset:960\n" \
"v_mul_f32 v101, s20, v101\n" \
"v_mfma_f32_16x16x32_bf16 v[72:75], a[0:3], a[40:43], v[72:75]\n" \
"ds_read2_b32 a[68:69], v46 offset0:144 offset1:145\n" \
"v_mul_f32 v102, s20, v102\n" \
"v_mfma_f32_16x16x32_bf16 v[76:79], a[4:7], a[40:43], 0\n" \
"ds_read2_b32 a[70:71], v46 offset0:146 offset1:147\n" \
"v_mul_f32 v103, s20, v103\n" \
"v_mfma_f32_16x16x32_bf16 v[72:75], a[8:11], a[32:35], v[72:75]\n" \
"ds_read_b128 a[104:107], v50 offset:1472\n" \
"v_cvt_pk_bf16_f32 v32, v100, v101\n" \
"v_mfma_f32_16x16x32_bf16 v[76:79], a[8:11], a[36:39], v[76:79]\n" \
"ds_read2_b32 a[72:73], v47 offset0:16 offset1:17\n" \
"v_cvt_pk_bf16_f32 v33, v102, v103\n" \
"v_mfma_f32_16x16x32_bf16 v[80:83], a[8:11], a[40:43], 0\n" \
"ds_read2_b32 a[74:75], v47 offset0:18 offset1:19\n" \
"v_mul_f32 v104, s20, v104\n" \
"v_mfma_f32_16x16x32_bf16 v[76:79], a[0:3], a[44:47], v[76:79]\n" \
"ds_read_b128 a[108:111], v50 offset:1984\n" \
"v_mul_f32 v105, s20, v105\n" \
"v_mfma_f32_16x16x32_bf16 v[80:83], a[4:7], a[44:47], v[80:83]\n" \
"ds_read2_b32 a[76:77], v47 offset0:144 offset1:145\n" \
"v_mul_f32 v106, s20, v106\n" \
"v_mfma_f32_16x16x32_bf16 v[84:87], a[8:11], a[44:47], 0\n" \
"ds_read2_b32 a[78:79], v47 offset0:146 offset1:147\n" \
"v_mul_f32 v107, s20, v107\n" \
"v_mfma_f32_16x16x32_bf16 v[76:79], a[12:15], a[32:35], v[76:79]\n" \
"ds_read_b128 a[112:115], v50 offset:2496\n" \
"v_mfma_f32_16x16x32_bf16 v[80:83], a[12:15], a[36:39], v[80:83]\n" \
"ds_read2_b32 a[80:81], v48 offset0:16 offset1:17\n" \
"v_mfma_f32_16x16x32_bf16 v[84:87], a[12:15], a[40:43], v[84:87]\n" \
"ds_read2_b32 a[82:83], v48 offset0:18 offset1:19\n" \
"v_mfma_f32_16x16x32_bf16 v[88:91], a[12:15], a[44:47], 0\n" \
"ds_read_b128 a[116:119], v50 offset:3008\n" \
"v_mfma_f32_16x16x32_bf16 v[80:83], a[0:3], a[48:51], v[80:83]\n" \
"ds_read2_b32 a[84:85], v48 offset0:144 offset1:145\n" \
"v_mfma_f32_16x16x32_bf16 v[84:87], a[4:7], a[48:51], v[84:87]\n" \
"ds_read2_b32 a[86:87], v48 offset0:146 offset1:147\n" \
"v_mfma_f32_16x16x32_bf16 v[88:91], a[8:11], a[48:51], v[88:91]\n" \
"ds_read_b128 a[120:123], v50 offset:3520\n" \
"v_mfma_f32_16x16x32_bf16 v[92:95], a[12:15], a[48:51], 0\n" \
"ds_read2_b32 a[88:89], v49 offset0:16 offset1:17\n" \
"v_mfma_f32_16x16x32_bf16 v[80:83], a[16:19], a[32:35], v[80:83]\n" \
"ds_read2_b32 a[90:91], v49 offset0:18 offset1:19\n" \
"v_mfma_f32_16x16x32_bf16 v[84:87], a[16:19], a[36:39], v[84:87]\n" \
"ds_read_b128 a[124:127], v50 offset:4032\n" \
"v_mfma_f32_16x16x32_bf16 v[88:91], a[16:19], a[40:43], v[88:91]\n" \
"ds_read2_b32 a[92:93], v49 offset0:144 offset1:145\n" \
"v_mfma_f32_16x16x32_bf16 v[92:95], a[16:19], a[44:47], v[92:95]\n" \
"ds_read2_b32 a[94:95], v49 offset0:146 offset1:147\n" \
"v_mfma_f32_16x16x32_bf16 v[84:87], a[0:3], a[52:55], v[84:87]\n" \
"v_mfma_f32_16x16x32_bf16 v[88:91], a[4:7], a[52:55], v[88:91]\n" \
"buffer_store_dwordx4 v[34:37], v4, s[36:39], s97 offen offset:0 sc1\n" \
"s_add_u32 s96, s96, s92\n" \
"v_mfma_f32_16x16x32_bf16 v[92:95], a[8:11], a[52:55], v[92:95]\n" \
"v_cvt_pk_bf16_f32 v34, v104, v105\n" \
"v_cvt_pk_bf16_f32 v35, v106, v107\n" \
"v_mfma_f32_16x16x32_bf16 v[84:87], a[20:23], a[32:35], v[84:87]\n" \
"v_mul_f32 v108, s20, v108\n" \
"v_mul_f32 v109, s20, v109\n" \
"v_mfma_f32_16x16x32_bf16 v[88:91], a[20:23], a[36:39], v[88:91]\n" \
"v_mul_f32 v110, s20, v110\n" \
"v_mul_f32 v111, s20, v111\n" \
"v_mfma_f32_16x16x32_bf16 v[92:95], a[20:23], a[40:43], v[92:95]\n" \
"buffer_store_dwordx4 v[38:41], v4, s[36:39], s96 offen offset:0 sc1\n" \
"s_add_u32 s97, s96, s98\n" \
"v_mfma_f32_16x16x32_bf16 v[88:91], a[0:3], a[56:59], v[88:91]\n" \
"v_cvt_pk_bf16_f32 v36, v108, v109\n" \
"v_cvt_pk_bf16_f32 v37, v110, v111\n" \
"v_mfma_f32_16x16x32_bf16 v[92:95], a[4:7], a[56:59], v[92:95]\n" \
"v_mul_f32 v112, s20, v112\n" \
"v_mul_f32 v113, s20, v113\n" \
"v_mfma_f32_16x16x32_bf16 v[88:91], a[24:27], a[32:35], v[88:91]\n" \
"v_mul_f32 v114, s20, v114\n" \
"v_mul_f32 v115, s20, v115\n" \
"v_mfma_f32_16x16x32_bf16 v[92:95], a[24:27], a[36:39], v[92:95]\n" \
"buffer_store_dwordx4 v[42:45], v4, s[36:39], s97 offen offset:0 sc1\n" \
"s_add_u32 s96, s96, s92\n" \
"v_mfma_f32_16x16x32_bf16 v[92:95], a[0:3], a[60:63], v[92:95]\n" \
"s_waitcnt lgkmcnt(0)\n" \
"v_cvt_pk_bf16_f32 v38, v112, v113\n" \
"v_mfma_f32_16x16x32_bf16 v[92:95], a[28:31], a[32:35], v[92:95]\n" \
"v_cvt_pk_bf16_f32 v39, v114, v115\n" \
"v_mul_f32 v116, s20, v116\n" \
"v_mfma_f32_16x16x32_bf16 v[64:67], a[64:67], a[96:99], v[64:67]\n" \
"ds_read_b128 a[32:35], v50 offset:384\n" \
"v_mul_f32 v117, s20, v117\n" \
"v_mfma_f32_16x16x32_bf16 v[68:71], a[64:67], a[100:103], v[68:71]\n" \
"ds_read2_b32 a[0:1], v46 offset0:32 offset1:33\n" \
"v_mul_f32 v118, s20, v118\n" \
"v_mfma_f32_16x16x32_bf16 v[68:71], a[68:71], a[96:99], v[68:71]\n" \
"ds_read2_b32 a[2:3], v46 offset0:34 offset1:35\n" \
"v_mul_f32 v119, s20, v119\n" \
"v_mfma_f32_16x16x32_bf16 v[72:75], a[68:71], a[100:103], v[72:75]\n" \
"ds_read_b128 a[36:39], v50 offset:896\n" \
"v_cvt_pk_bf16_f32 v40, v116, v117\n" \
"v_mfma_f32_16x16x32_bf16 v[72:75], a[64:67], a[104:107], v[72:75]\n" \
"ds_read2_b32 a[4:5], v46 offset0:160 offset1:161\n" \
"v_cvt_pk_bf16_f32 v41, v118, v119\n" \
"v_mfma_f32_16x16x32_bf16 v[76:79], a[68:71], a[104:107], v[76:79]\n" \
"ds_read2_b32 a[6:7], v46 offset0:162 offset1:163\n" \
"v_mul_f32 v120, s20, v120\n" \
"v_mfma_f32_16x16x32_bf16 v[72:75], a[72:75], a[96:99], v[72:75]\n" \
"ds_read_b128 a[40:43], v50 offset:1408\n" \
"v_mul_f32 v121, s20, v121\n" \
"v_mfma_f32_16x16x32_bf16 v[76:79], a[72:75], a[100:103], v[76:79]\n" \
"ds_read2_b32 a[8:9], v47 offset0:32 offset1:33\n" \
"v_mul_f32 v122, s20, v122\n" \
"v_mfma_f32_16x16x32_bf16 v[80:83], a[72:75], a[104:107], v[80:83]\n" \
"ds_read2_b32 a[10:11], v47 offset0:34 offset1:35\n" \
"v_mul_f32 v123, s20, v123\n" \
"v_mfma_f32_16x16x32_bf16 v[76:79], a[64:67], a[108:111], v[76:79]\n" \
"ds_read_b128 a[44:47], v50 offset:1920\n" \
"v_cvt_pk_bf16_f32 v42, v120, v121\n" \
"v_mfma_f32_16x16x32_bf16 v[80:83], a[68:71], a[108:111], v[80:83]\n" \
"ds_read2_b32 a[12:13], v47 offset0:160 offset1:161\n" \
"v_cvt_pk_bf16_f32 v43, v122, v123\n" \
"v_mfma_f32_16x16x32_bf16 v[84:87], a[72:75], a[108:111], v[84:87]\n" \
"ds_read2_b32 a[14:15], v47 offset0:162 offset1:163\n" \
"v_mul_f32 v124, s20, v124\n" \
"v_mfma_f32_16x16x32_bf16 v[76:79], a[76:79], a[96:99], v[76:79]\n" \
"LH8_%=:\ns_cbranch_execz LH12_%=\n" \
"ds_read_b128 a[48:51], v50 offset:2432\n" \
"v_mul_f32 v125, s20, v125\n" \
"v_mfma_f32_16x16x32_bf16 v[80:83], a[76:79], a[100:103], v[80:83]\n" \
"ds_read2_b32 a[16:17], v48 offset0:32 offset1:33\n" \
"v_mul_f32 v126, s20, v126\n" \
"v_mfma_f32_16x16x32_bf16 v[84:87], a[76:79], a[104:107], v[84:87]\n" \
"ds_read2_b32 a[18:19], v48 offset0:34 offset1:35\n" \
"v_mul_f32 v127, s20, v127\n" \
"v_mfma_f32_16x16x32_bf16 v[88:91], a[76:79], a[108:111], v[88:91]\n" \
"ds_read_b128 a[52:55], v50 offset:2944\n" \
"v_cvt_pk_bf16_f32 v44, v124, v125\n" \
"v_mfma_f32_16x16x32_bf16 v[80:83], a[64:67], a[112:115], v[80:83]\n" \
"ds_read2_b32 a[20:21], v48 offset0:160 offset1:161\n" \
"v_cvt_pk_bf16_f32 v45, v126, v127\n" \
"v_mfma_f32_16x16x32_bf16 v[84:87], a[68:71], a[112:115], v[84:87]\n" \
"ds_read2_b32 a[22:23], v48 offset0:162 offset1:163\n" \
"v_mul_f32 v128, s20, v128\n" \
"v_mfma_f32_16x16x32_bf16 v[88:91], a[72:75], a[112:115], v[88:91]\n" \
"ds_read_b128 a[56:59], v50 offset:3456\n" \
"v_mul_f32 v129, s20, v129\n" \
"v_mfma_f32_16x16x32_bf16 v[92:95], a[76:79], a[112:115], v[92:95]\n" \
"ds_read2_b32 a[24:25], v49 offset0:32 offset1:33\n" \
"v_mul_f32 v130, s20, v130\n" \
"v_mfma_f32_16x16x32_bf16 v[80:83], a[80:83], a[96:99], v[80:83]\n" \
"ds_read2_b32 a[26:27], v49 offset0:34 offset1:35\n" \
"v_mul_f32 v131, s20, v131\n" \
"v_mfma_f32_16x16x32_bf16 v[84:87], a[80:83], a[100:103], v[84:87]\n" \
"ds_read_b128 a[60:63], v50 offset:3968\n" \
"v_mfma_f32_16x16x32_bf16 v[88:91], a[80:83], a[104:107], v[88:91]\n" \
"ds_read2_b32 a[28:29], v49 offset0:160 offset1:161\n" \
"v_mfma_f32_16x16x32_bf16 v[92:95], a[80:83], a[108:111], v[92:95]\n" \
"ds_read2_b32 a[30:31], v49 offset0:162 offset1:163\n" \
"v_mfma_f32_16x16x32_bf16 v[84:87], a[64:67], a[116:119], v[84:87]\n" \
"v_mfma_f32_16x16x32_bf16 v[88:91], a[68:71], a[116:119], v[88:91]\n" \
"buffer_store_dwordx4 v[30:33], v4, s[36:39], s96 offen offset:0 sc1\n" \
"s_add_u32 s97, s96, s98\n" \
"v_mfma_f32_16x16x32_bf16 v[92:95], a[72:75], a[116:119], v[92:95]\n" \
"v_cvt_pk_bf16_f32 v30, v128, v129\n" \
"v_cvt_pk_bf16_f32 v31, v130, v131\n" \
"v_mfma_f32_16x16x32_bf16 v[84:87], a[84:87], a[96:99], v[84:87]\n" \
"v_mul_f32 v132, s20, v132\n" \
"v_mul_f32 v133, s20, v133\n" \
"v_mfma_f32_16x16x32_bf16 v[88:91], a[84:87], a[100:103], v[88:91]\n" \
"v_mul_f32 v134, s20, v134\n" \
"v_mul_f32 v135, s20, v135\n" \
"v_mfma_f32_16x16x32_bf16 v[92:95], a[84:87], a[104:107], v[92:95]\n" \
"buffer_store_dwordx4 v[34:37], v4, s[36:39], s97 offen offset:0 sc1\n" \
"s_add_u32 s96, s96, s92\n" \
"v_mfma_f32_16x16x32_bf16 v[88:91], a[64:67], a[120:123], v[88:91]\n" \
"v_cvt_pk_bf16_f32 v32, v132, v133\n" \
"v_cvt_pk_bf16_f32 v33, v134, v135\n" \
"v_mfma_f32_16x16x32_bf16 v[92:95], a[68:71], a[120:123], v[92:95]\n" \
"v_mul_f32 v136, s20, v136\n" \
"v_mul_f32 v137, s20, v137\n" \
"v_mfma_f32_16x16x32_bf16 v[88:91], a[88:91], a[96:99], v[88:91]\n" \
"v_mul_f32 v138, s20, v138\n" \
"v_mul_f32 v139, s20, v139\n" \
"v_mfma_f32_16x16x32_bf16 v[92:95], a[88:91], a[100:103], v[92:95]\n" \
"buffer_store_dwordx4 v[38:41], v4, s[36:39], s96 offen offset:0 sc1\n" \
"s_add_u32 s97, s96, s98\n" \
"v_mfma_f32_16x16x32_bf16 v[92:95], a[64:67], a[124:127], v[92:95]\n" \
"s_waitcnt lgkmcnt(0)\n" \
"v_cvt_pk_bf16_f32 v34, v136, v137\n" \
"v_mfma_f32_16x16x32_bf16 v[92:95], a[92:95], a[96:99], v[92:95]\n" \
"v_cvt_pk_bf16_f32 v35, v138, v139\n" \
"v_mul_f32 v140, s20, v140\n" \
"v_mfma_f32_16x16x32_bf16 v[64:67], a[0:3], a[32:35], v[64:67]\n" \
"ds_read_b128 a[96:99], v50 offset:320\n" \
"v_mul_f32 v141, s20, v141\n" \
"v_mfma_f32_16x16x32_bf16 v[68:71], a[0:3], a[36:39], v[68:71]\n" \
"ds_read2_b32 a[64:65], v46 offset0:48 offset1:49\n" \
"v_mul_f32 v142, s20, v142\n" \
"v_mfma_f32_16x16x32_bf16 v[68:71], a[4:7], a[32:35], v[68:71]\n" \
"ds_read2_b32 a[66:67], v46 offset0:50 offset1:51\n" \
"v_mul_f32 v143, s20, v143\n" \
"v_mfma_f32_16x16x32_bf16 v[72:75], a[4:7], a[36:39], v[72:75]\n" \
"ds_read_b128 a[100:103], v50 offset:832\n" \
"v_cvt_pk_bf16_f32 v36, v140, v141\n" \
"v_mfma_f32_16x16x32_bf16 v[72:75], a[0:3], a[40:43], v[72:75]\n" \
"ds_read2_b32 a[68:69], v46 offset0:176 offset1:177\n" \
"v_cvt_pk_bf16_f32 v37, v142, v143\n" \
"v_mfma_f32_16x16x32_bf16 v[76:79], a[4:7], a[40:43], v[76:79]\n" \
"ds_read2_b32 a[70:71], v46 offset0:178 offset1:179\n" \
"v_mul_f32 v144, s20, v144\n" \
"v_mfma_f32_16x16x32_bf16 v[72:75], a[8:11], a[32:35], v[72:75]\n" \
"ds_read_b128 a[104:107], v50 offset:1344\n" \
"v_mul_f32 v145, s20, v145\n" \
"v_mfma_f32_16x16x32_bf16 v[76:79], a[8:11], a[36:39], v[76:79]\n" \
"ds_read2_b32 a[72:73], v47 offset0:48 offset1:49\n" \
"v_mul_f32 v146, s20, v146\n" \
"v_mfma_f32_16x16x32_bf16 v[80:83], a[8:11], a[40:43], v[80:83]\n" \
"ds_read2_b32 a[74:75], v47 offset0:50 offset1:51\n" \
"v_mul_f32 v147, s20, v147\n" \
"v_mfma_f32_16x16x32_bf16 v[76:79], a[0:3], a[44:47], v[76:79]\n" \
"ds_read_b128 a[108:111], v50 offset:1856\n" \
"v_cvt_pk_bf16_f32 v38, v144, v145\n" \
"v_mfma_f32_16x16x32_bf16 v[80:83], a[4:7], a[44:47], v[80:83]\n" \
"ds_read2_b32 a[76:77], v47 offset0:176 offset1:177\n" \
"v_cvt_pk_bf16_f32 v39, v146, v147\n" \
"v_mfma_f32_16x16x32_bf16 v[84:87], a[8:11], a[44:47], v[84:87]\n" \
"ds_read2_b32 a[78:79], v47 offset0:178 offset1:179\n" \
"v_mul_f32 v148, s20, v148\n" \
"v_mfma_f32_16x16x32_bf16 v[76:79], a[12:15], a[32:35], v[76:79]\n" \
"ds_read_b128 a[112:115], v50 offset:2368\n" \
"v_mul_f32 v149, s20, v149\n" \
"v_mfma_f32_16x16x32_bf16 v[80:83], a[12:15], a[36:39], v[80:83]\n" \
"ds_read2_b32 a[80:81], v48 offset0:48 offset1:49\n" \
"v_mul_f32 v150, s20, v150\n" \
"v_mfma_f32_16x16x32_bf16 v[84:87], a[12:15], a[40:43], v[84:87]\n" \
"ds_read2_b32 a[82:83], v48 offset0:50 offset1:51\n" \
"v_mul_f32 v151, s20, v151\n" \
"v_mfma_f32_16x16x32_bf16 v[88:91], a[12:15], a[44:47], v[88:91]\n" \
"ds_read_b128 a[116:119], v50 offset:2880\n" \
"v_cvt_pk_bf16_f32 v40, v148, v149\n" \
"v_mfma_f32_16x16x32_bf16 v[80:83], a[0:3], a[48:51], v[80:83]\n" \
"ds_read2_b32 a[84:85], v48 offset0:176 offset1:177\n" \
"v_cvt_pk_bf16_f32 v41, v150, v151\n" \
"v_mfma_f32_16x16x32_bf16 v[84:87], a[4:7], a[48:51], v[84:87]\n" \
"ds_read2_b32 a[86:87], v48 offset0:178 offset1:179\n" \
"v_mul_f32 v152, s20, v152\n" \
"v_mfma_f32_16x16x32_bf16 v[88:91], a[8:11], a[48:51], v[88:91]\n" \
"ds_read_b128 a[120:123], v50 offset:3392\n" \
"v_mul_f32 v153, s20, v153\n" \
"v_mfma_f32_16x16x32_bf16 v[92:95], a[12:15], a[48:51], v[92:95]\n" \
"ds_read2_b32 a[88:89], v49 offset0:48 offset1:49\n" \
"v_mul_f32 v154, s20, v154\n" \
"v_mfma_f32_16x16x32_bf16 v[80:83], a[16:19], a[32:35], v[80:83]\n" \
"ds_read2_b32 a[90:91], v49 offset0:50 offset1:51\n" \
"v_mul_f32 v155, s20, v155\n" \
"v_mfma_f32_16x16x32_bf16 v[84:87], a[16:19], a[36:39], v[84:87]\n" \
"ds_read_b128 a[124:127], v50 offset:3904\n" \
"v_mfma_f32_16x16x32_bf16 v[88:91], a[16:19], a[40:43], v[88:91]\n" \
"ds_read2_b32 a[92:93], v49 offset0:176 offset1:177\n" \
"v_mfma_f32_16x16x32_bf16 v[92:95], a[16:19], a[44:47], v[92:95]\n" \
"ds_read2_b32 a[94:95], v49 offset0:178 offset1:179\n" \
"v_mfma_f32_16x16x32_bf16 v[84:87], a[0:3], a[52:55], v[84:87]\n" \
"v_mfma_f32_16x16x32_bf16 v[88:91], a[4:7], a[52:55], v[88:91]\n" \
"buffer_store_dwordx4 v[42:45], v4, s[36:39], s97 offen offset:0 sc1\n" \
"s_add_u32 s57, s57, s93\n" \
"s_mov_b32 s96, s57\n" \
"v_mfma_f32_16x16x32_bf16 v[92:95], a[8:11], a[52:55], v[92:95]\n" \
"v_cvt_pk_bf16_f32 v42, v152, v153\n" \
"v_cvt_pk_bf16_f32 v43, v154, v155\n" \
"v_mfma_f32_16x16x32_bf16 v[84:87], a[20:23], a[32:35], v[84:87]\n" \
"v_mul_f32 v156, s20, v156\n" \
"v_mul_f32 v157, s20, v157\n" \
"v_mfma_f32_16x16x32_bf16 v[88:91], a[20:23], a[36:39], v[88:91]\n" \
"v_mul_f32 v158, s20, v158\n" \
"v_mul_f32 v159, s20, v159\n" \
"v_mfma_f32_16x16x32_bf16 v[92:95], a[20:23], a[40:43], v[92:95]\n" \
"buffer_store_dwordx4 v[30:33], v4, s[36:39], s96 offen offset:0 sc1\n" \
"s_add_u32 s97, s96, s98\n" \
"v_mfma_f32_16x16x32_bf16 v[88:91], a[0:3], a[56:59], v[88:91]\n" \
"v_cvt_pk_bf16_f32 v44, v156, v157\n" \
"v_cvt_pk_bf16_f32 v45, v158, v159\n" \
"v_mfma_f32_16x16x32_bf16 v[92:95], a[4:7], a[56:59], v[92:95]\n" \
"v_mul_f32 v160, s20, v160\n" \
"v_mul_f32 v161, s20, v161\n" \
"v_mfma_f32_16x16x32_bf16 v[88:91], a[24:27], a[32:35], v[88:91]\n" \
"v_mul_f32 v162, s20, v162\n" \
"v_mul_f32 v163, s20, v163\n" \
"v_mfma_f32_16x16x32_bf16 v[92:95], a[24:27], a[36:39], v[92:95]\n" \
"buffer_store_dwordx4 v[34:37], v4, s[36:39], s97 offen offset:0 sc1\n" \
"s_add_u32 s96, s96, s92\n" \
"v_mfma_f32_16x16x32_bf16 v[92:95], a[0:3], a[60:63], v[92:95]\n" \
"s_waitcnt lgkmcnt(0)\n" \
"v_cvt_pk_bf16_f32 v30, v160, v161\n" \
"v_mfma_f32_16x16x32_bf16 v[92:95], a[28:31], a[32:35], v[92:95]\n" \
"v_cvt_pk_bf16_f32 v31, v162, v163\n" \
"v_mul_f32 v164, s20, v164\n" \
"v_mfma_f32_16x16x32_bf16 v[64:67], a[64:67], a[96:99], v[64:67]\n" \
"ds_read_b128 a[32:35], v50 offset:256\n" \
"v_mul_f32 v165, s20, v165\n" \
"v_mfma_f32_16x16x32_bf16 v[68:71], a[64:67], a[100:103], v[68:71]\n" \
"ds_read2_b32 a[0:1], v46 offset0:64 offset1:65\n" \
"v_mul_f32 v166, s20, v166\n" \
"v_mfma_f32_16x16x32_bf16 v[68:71], a[68:71], a[96:99], v[68:71]\n" \
"ds_read2_b32 a[2:3], v46 offset0:66 offset1:67\n" \
"v_mul_f32 v167, s20, v167\n" \
"v_mfma_f32_16x16x32_bf16 v[72:75], a[68:71], a[100:103], v[72:75]\n" \
"ds_read_b128 a[36:39], v50 offset:768\n" \
"v_cvt_pk_bf16_f32 v32, v164, v165\n" \
"v_mfma_f32_16x16x32_bf16 v[72:75], a[64:67], a[104:107], v[72:75]\n" \
"ds_read2_b32 a[4:5], v46 offset0:192 offset1:193\n" \
"v_cvt_pk_bf16_f32 v33, v166, v167\n" \
"v_mfma_f32_16x16x32_bf16 v[76:79], a[68:71], a[104:107], v[76:79]\n" \
"ds_read2_b32 a[6:7], v46 offset0:194 offset1:195\n" \
"v_mul_f32 v168, s20, v168\n" \
"v_mfma_f32_16x16x32_bf16 v[72:75], a[72:75], a[96:99], v[72:75]\n" \
"ds_read_b128 a[40:43], v50 offset:1280\n" \
"v_mul_f32 v169, s20, v169\n" \
"v_mfma_f32_16x16x32_bf16 v[76:79], a[72:75], a[100:103], v[76:79]\n" \
"ds_read2_b32 a[8:9], v47 offset0:64 offset1:65\n" \
"v_mul_f32 v170, s20, v170\n" \
"v_mfma_f32_16x16x32_bf16 v[80:83], a[72:75], a[104:107], v[80:83]\n" \
"ds_read2_b32 a[10:11], v47 offset0:66 offset1:67\n" \
"v_mul_f32 v171, s20, v171\n" \
"v_mfma_f32_16x16x32_bf16 v[76:79], a[64:67], a[108:111], v[76:79]\n" \
"ds_read_b128 a[44:47], v50 offset:1792\n" \
"v_cvt_pk_bf16_f32 v34, v168, v169\n" \
"v_mfma_f32_16x16x32_bf16 v[80:83], a[68:71], a[108:111], v[80:83]\n" \
"ds_read2_b32 a[12:13], v47 offset0:192 offset1:193\n" \
"v_cvt_pk_bf16_f32 v35, v170, v171\n" \
"v_mfma_f32_16x16x32_bf16 v[84:87], a[72:75], a[108:111], v[84:87]\n" \
"ds_read2_b32 a[14:15], v47 offset0:194 offset1:195\n" \
"v_mul_f32 v172, s20, v172\n" \
"v_mfma_f32_16x16x32_bf16 v[76:79], a[76:79], a[96:99], v[76:79]\n" \
"ds_read_b128 a[48:51], v50 offset:2304\n" \
"v_mul_f32 v173, s20, v173\n" \
"v_mfma_f32_16x16x32_bf16 v[80:83], a[76:79], a[100:103], v[80:83]\n" \
"ds_read2_b32 a[16:17], v48 offset0:64 offset1:65\n" \
"v_mul_f32 v174, s20, v174\n" \
"v_mfma_f32_16x16x32_bf16 v[84:87], a[76:79], a[104:107], v[84:87]\n" \
"ds_read2_b32 a[18:19], v48 offset0:66 offset1:67\n" \
"v_mul_f32 v175, s20, v175\n" \
"v_mfma_f32_16x16x32_bf16 v[88:91], a[76:79], a[108:111], v[88:91]\n" \
"ds_read_b128 a[52:55], v50 offset:2816\n" \
"v_cvt_pk_bf16_f32 v36, v172, v173\n" \
"v_mfma_f32_16x16x32_bf16 v[80:83], a[64:67], a[112:115], v[80:83]\n" \
"ds_read2_b32 a[20:21], v48 offset0:192 offset1:193\n" \
"v_cvt_pk_bf16_f32 v37, v174, v175\n" \
"v_mfma_f32_16x16x32_bf16 v[84:87], a[68:71], a[112:115], v[84:87]\n" \
"ds_read2_b32 a[22:23], v48 offset0:194 offset1:195\n" \
"v_mul_f32 v176, s20, v176\n" \
"v_mfma_f32_16x16x32_bf16 v[88:91], a[72:75], a[112:115], v[88:91]\n" \
"ds_read_b128 a[56:59], v50 offset:3328\n" \
"v_mul_f32 v177, s20, v177\n" \
"v_mfma_f32_16x16x32_bf16 v[92:95], a[76:79], a[112:115], v[92:95]\n" \
"ds_read2_b32 a[24:25], v49 offset0:64 offset1:65\n" \
"v_mul_f32 v178, s20, v178\n" \
"v_mfma_f32_16x16x32_bf16 v[80:83], a[80:83], a[96:99], v[80:83]\n" \
"ds_read2_b32 a[26:27], v49 offset0:66 offset1:67\n" \
"v_mul_f32 v179, s20, v179\n" \
"v_mfma_f32_16x16x32_bf16 v[84:87], a[80:83], a[100:103], v[84:87]\n" \
"ds_read_b128 a[60:63], v50 offset:3840\n" \
"v_mfma_f32_16x16x32_bf16 v[88:91], a[80:83], a[104:107], v[88:91]\n" \
"ds_read2_b32 a[28:29], v49 offset0:192 offset1:193\n" \
"v_mfma_f32_16x16x32_bf16 v[92:95], a[80:83], a[108:111], v[92:95]\n" \
"ds_read2_b32 a[30:31], v49 offset0:194 offset1:195\n" \
"v_mfma_f32_16x16x32_bf16 v[84:87], a[64:67], a[116:119], v[84:87]\n" \
"v_mfma_f32_16x16x32_bf16 v[88:91], a[68:71], a[116:119], v[88:91]\n" \
"buffer_store_dwordx4 v[38:41], v4, s[36:39], s96 offen offset:0 sc1\n" \
"s_add_u32 s97, s96, s98\n" \
"v_mfma_f32_16x16x32_bf16 v[92:95], a[72:75], a[116:119], v[92:95]\n" \
"v_cvt_pk_bf16_f32 v38, v176, v177\n" \
"v_cvt_pk_bf16_f32 v39, v178, v179\n" \
"v_mfma_f32_16x16x32_bf16 v[84:87], a[84:87], a[96:99], v[84:87]\n" \
"v_mul_f32 v180, s20, v180\n" \
"v_mul_f32 v181, s20, v181\n" \
"v_mfma_f32_16x16x32_bf16 v[88:91], a[84:87], a[100:103], v[88:91]\n" \
"v_mul_f32 v182, s20, v182\n" \
"v_mul_f32 v183, s20, v183\n" \
"v_mfma_f32_16x16x32_bf16 v[92:95], a[84:87], a[104:107], v[92:95]\n" \
"buffer_store_dwordx4 v[42:45], v4, s[36:39], s97 offen offset:0 sc1\n" \
"s_add_u32 s96, s96, s92\n" \
"v_mfma_f32_16x16x32_bf16 v[88:91], a[64:67], a[120:123], v[88:91]\n" \
"v_cvt_pk_bf16_f32 v40, v180, v181\n" \
"v_cvt_pk_bf16_f32 v41, v182, v183\n" \
"v_mfma_f32_16x16x32_bf16 v[92:95], a[68:71], a[120:123], v[92:95]\n" \
"v_mul_f32 v184, s20, v184\n" \
"v_mul_f32 v185, s20, v185\n" \
"v_mfma_f32_16x16x32_bf16 v[88:91], a[88:91], a[96:99], v[88:91]\n" \
"v_mul_f32 v186, s20, v186\n" \
"v_mul_f32 v187, s20, v187\n" \
"v_mfma_f32_16x16x32_bf16 v[92:95], a[88:91], a[100:103], v[92:95]\n" \
"buffer_store_dwordx4 v[30:33], v4, s[36:39], s96 offen offset:0 sc1\n" \
"s_add_u32 s97, s96, s98\n" \
"v_mfma_f32_16x16x32_bf16 v[92:95], a[64:67], a[124:127], v[92:95]\n" \
"s_waitcnt lgkmcnt(0)\n" \
"v_cvt_pk_bf16_f32 v42, v184, v185\n" \
"v_mfma_f32_16x16x32_bf16 v[92:95], a[92:95], a[96:99], v[92:95]\n" \
"v_cvt_pk_bf16_f32 v43, v186, v187\n" \
"v_mul_f32 v188, s20, v188\n" \
"v_mfma_f32_16x16x32_bf16 v[64:67], a[0:3], a[32:35], v[64:67]\n" \
"ds_read_b128 a[96:99], v50 offset:192\n" \
"v_mul_f32 v189, s20, v189\n" \
"v_mfma_f32_16x16x32_bf16 v[68:71], a[0:3], a[36:39], v[68:71]\n" \
"ds_read2_b32 a[64:65], v46 offset0:80 offset1:81\n" \
"v_mul_f32 v190, s20, v190\n" \
"v_mfma_f32_16x16x32_bf16 v[68:71], a[4:7], a[32:35], v[68:71]\n" \
"ds_read2_b32 a[66:67], v46 offset0:82 offset1:83\n" \
"v_mul_f32 v191, s20, v191\n" \
"v_mfma_f32_16x16x32_bf16 v[72:75], a[4:7], a[36:39], v[72:75]\n" \
"ds_read_b128 a[100:103], v50 offset:704\n" \
"v_cvt_pk_bf16_f32 v44, v188, v189\n" \
"v_mfma_f32_16x16x32_bf16 v[72:75], a[0:3], a[40:43], v[72:75]\n" \
"ds_read2_b32 a[68:69], v46 offset0:208 offset1:209\n" \
"v_cvt_pk_bf16_f32 v45, v190, v191\n" \
"v_mfma_f32_16x16x32_bf16 v[76:79], a[4:7], a[40:43], v[76:79]\n" \
"ds_read2_b32 a[70:71], v46 offset0:210 offset1:211\n" \
"v_mul_f32 v192, s20, v192\n" \
"v_mfma_f32_16x16x32_bf16 v[72:75], a[8:11], a[32:35], v[72:75]\n" \
"ds_read_b128 a[104:107], v50 offset:1216\n" \
"v_mul_f32 v193, s20, v193\n" \
"v_mfma_f32_16x16x32_bf16 v[76:79], a[8:11], a[36:39], v[76:79]\n" \
"ds_read2_b32 a[72:73], v47 offset0:80 offset1:81\n" \
"v_mul_f32 v194, s20, v194\n" \
"v_mfma_f32_16x16x32_bf16 v[80:83], a[8:11], a[40:43], v[80:83]\n" \
"ds_read2_b32 a[74:75], v47 offset0:82 offset1:83\n" \
"v_mul_f32 v195, s20, v195\n" \
"v_mfma_f32_16x16x32_bf16 v[76:79], a[0:3], a[44:47], v[76:79]\n" \
"ds_read_b128 a[108:111], v50 offset:1728\n" \
"v_cvt_pk_bf16_f32 v30, v192, v193\n" \
"v_mfma_f32_16x16x32_bf16 v[80:83], a[4:7], a[44:47], v[80:83]\n" \
"ds_read2_b32 a[76:77], v47 offset0:208 offset1:209\n" \
"v_cvt_pk_bf16_f32 v31, v194, v195\n" \
"v_mfma_f32_16x16x32_bf16 v[84:87], a[8:11], a[44:47], v[84:87]\n" \
"ds_read2_b32 a[78:79], v47 offset0:210 offset1:211\n" \
"v_mul_f32 v196, s20, v196\n" \
"v_mfma_f32_16x16x32_bf16 v[76:79], a[12:15], a[32:35], v[76:79]\n" \
"ds_read_b128 a[112:115], v50 offset:2240\n" \
"v_mul_f32 v197, s20, v197\n" \
"v_mfma_f32_16x16x32_bf16 v[80:83], a[12:15], a[36:39], v[80:83]\n" \
"ds_read2_b32 a[80:81], v48 offset0:80 offset1:81\n" \
"v_mul_f32 v198, s20, v198\n" \
"v_mfma_f32_16x16x32_bf16 v[84:87], a[12:15], a[40:43], v[84:87]\n" \
"ds_read2_b32 a[82:83], v48 offset0:82 offset1:83\n" \
"v_mul_f32 v199, s20, v199\n" \
"v_mfma_f32_16x16x32_bf16 v[88:91], a[12:15], a[44:47], v[88:91]\n" \
"ds_read_b128 a[116:119], v50 offset:2752\n" \
"v_cvt_pk_bf16_f32 v32, v196, v197\n" \
"v_mfma_f32_16x16x32_bf16 v[80:83], a[0:3], a[48:51], v[80:83]\n" \
"ds_read2_b32 a[84:85], v48 offset0:208 offset1:209\n" \
"v_cvt_pk_bf16_f32 v33, v198, v199\n" \
"v_mfma_f32_16x16x32_bf16 v[84:87], a[4:7], a[48:51], v[84:87]\n" \
"ds_read2_b32 a[86:87], v48 offset0:210 offset1:211\n" \
"v_mul_f32 v200, s20, v200\n" \
"v_mfma_f32_16x16x32_bf16 v[88:91], a[8:11], a[48:51], v[88:91]\n" \
"ds_read_b128 a[120:123], v50 offset:3264\n" \
"v_mul_f32 v201, s20, v201\n" \
"v_mfma_f32_16x16x32_bf16 v[92:95], a[12:15], a[48:51], v[92:95]\n" \
"ds_read2_b32 a[88:89], v49 offset0:80 offset1:81\n" \
"v_mul_f32 v202, s20, v202\n" \
"v_mfma_f32_16x16x32_bf16 v[80:83], a[16:19], a[32:35], v[80:83]\n" \
"ds_read2_b32 a[90:91], v49 offset0:82 offset1:83\n" \
"v_mul_f32 v203, s20, v203\n" \
"v_mfma_f32_16x16x32_bf16 v[84:87], a[16:19], a[36:39], v[84:87]\n" \
"ds_read_b128 a[124:127], v50 offset:3776\n" \
"v_mfma_f32_16x16x32_bf16 v[88:91], a[16:19], a[40:43], v[88:91]\n" \
"ds_read2_b32 a[92:93], v49 offset0:208 offset1:209\n" \
"v_mfma_f32_16x16x32_bf16 v[92:95], a[16:19], a[44:47], v[92:95]\n" \
"ds_read2_b32 a[94:95], v49 offset0:210 offset1:211\n" \
"v_mfma_f32_16x16x32_bf16 v[84:87], a[0:3], a[52:55], v[84:87]\n" \
"v_mfma_f32_16x16x32_bf16 v[88:91], a[4:7], a[52:55], v[88:91]\n" \
"buffer_store_dwordx4 v[34:37], v4, s[36:39], s97 offen offset:0 sc1\n" \
"s_add_u32 s96, s96, s92\n" \
"v_mfma_f32_16x16x32_bf16 v[92:95], a[8:11], a[52:55], v[92:95]\n" \
"v_cvt_pk_bf16_f32 v34, v200, v201\n" \
"v_cvt_pk_bf16_f32 v35, v202, v203\n" \
"v_mfma_f32_16x16x32_bf16 v[84:87], a[20:23], a[32:35], v[84:87]\n" \
"v_mul_f32 v204, s20, v204\n" \
"v_mul_f32 v205, s20, v205\n" \
"v_mfma_f32_16x16x32_bf16 v[88:91], a[20:23], a[36:39], v[88:91]\n" \
"v_mul_f32 v206, s20, v206\n" \
"v_mul_f32 v207, s20, v207\n" \
"v_mfma_f32_16x16x32_bf16 v[92:95], a[20:23], a[40:43], v[92:95]\n" \
"buffer_store_dwordx4 v[38:41], v4, s[36:39], s96 offen offset:0 sc1\n" \
"s_add_u32 s97, s96, s98\n" \
"v_mfma_f32_16x16x32_bf16 v[88:91], a[0:3], a[56:59], v[88:91]\n" \
"v_cvt_pk_bf16_f32 v36, v204, v205\n" \
"v_cvt_pk_bf16_f32 v37, v206, v207\n" \
"v_mfma_f32_16x16x32_bf16 v[92:95], a[4:7], a[56:59], v[92:95]\n" \
"v_mul_f32 v208, s20, v208\n" \
"v_mul_f32 v209, s20, v209\n" \
"v_mfma_f32_16x16x32_bf16 v[88:91], a[24:27], a[32:35], v[88:91]\n" \
"v_mul_f32 v210, s20, v210\n" \
"v_mul_f32 v211, s20, v211\n" \
"v_mfma_f32_16x16x32_bf16 v[92:95], a[24:27], a[36:39], v[92:95]\n" \
"buffer_store_dwordx4 v[42:45], v4, s[36:39], s97 offen offset:0 sc1\n" \
"s_add_u32 s57, s57, s93\n" \
"s_mov_b32 s96, s57\n" \
"v_mfma_f32_16x16x32_bf16 v[92:95], a[0:3], a[60:63], v[92:95]\n" \
"s_waitcnt lgkmcnt(0)\n" \
"v_cvt_pk_bf16_f32 v38, v208, v209\n" \
"v_mfma_f32_16x16x32_bf16 v[92:95], a[28:31], a[32:35], v[92:95]\n" \
"v_cvt_pk_bf16_f32 v39, v210, v211\n" \
"v_mul_f32 v212, s20, v212\n" \
"v_mfma_f32_16x16x32_bf16 v[64:67], a[64:67], a[96:99], v[64:67]\n" \
"ds_read_b128 a[32:35], v50 offset:128\n" \
"v_mul_f32 v213, s20, v213\n" \
"v_mfma_f32_16x16x32_bf16 v[68:71], a[64:67], a[100:103], v[68:71]\n" \
"ds_read2_b32 a[0:1], v46 offset0:96 offset1:97\n" \
"v_mul_f32 v214, s20, v214\n" \
"v_mfma_f32_16x16x32_bf16 v[68:71], a[68:71], a[96:99], v[68:71]\n" \
"ds_read2_b32 a[2:3], v46 offset0:98 offset1:99\n" \
"v_mul_f32 v215, s20, v215\n" \
"v_mfma_f32_16x16x32_bf16 v[72:75], a[68:71], a[100:103], v[72:75]\n" \
"ds_read_b128 a[36:39], v50 offset:640\n" \
"v_cvt_pk_bf16_f32 v40, v212, v213\n" \
"v_mfma_f32_16x16x32_bf16 v[72:75], a[64:67], a[104:107], v[72:75]\n" \
"ds_read2_b32 a[4:5], v46 offset0:224 offset1:225\n" \
"v_cvt_pk_bf16_f32 v41, v214, v215\n" \
"v_mfma_f32_16x16x32_bf16 v[76:79], a[68:71], a[104:107], v[76:79]\n" \
"ds_read2_b32 a[6:7], v46 offset0:226 offset1:227\n" \
"v_mul_f32 v216, s20, v216\n" \
"v_mfma_f32_16x16x32_bf16 v[72:75], a[72:75], a[96:99], v[72:75]\n" \
"ds_read_b128 a[40:43], v50 offset:1152\n" \
"v_mul_f32 v217, s20, v217\n" \
"v_mfma_f32_16x16x32_bf16 v[76:79], a[72:75], a[100:103], v[76:79]\n" \
"ds_read2_b32 a[8:9], v47 offset0:96 offset1:97\n" \
"v_mul_f32 v218, s20, v218\n" \
"v_mfma_f32_16x16x32_bf16 v[80:83], a[72:75], a[104:107], v[80:83]\n" \
"ds_read2_b32 a[10:11], v47 offset0:98 offset1:99\n" \
"v_mul_f32 v219, s20, v219\n" \
"v_mfma_f32_16x16x32_bf16 v[76:79], a[64:67], a[108:111], v[76:79]\n" \
"ds_read_b128 a[44:47], v50 offset:1664\n" \
"v_cvt_pk_bf16_f32 v42, v216, v217\n" \
"v_mfma_f32_16x16x32_bf16 v[80:83], a[68:71], a[108:111], v[80:83]\n" \
"ds_read2_b32 a[12:13], v47 offset0:224 offset1:225\n" \
"v_cvt_pk_bf16_f32 v43, v218, v219\n" \
"v_mfma_f32_16x16x32_bf16 v[84:87], a[72:75], a[108:111], v[84:87]\n" \
"ds_read2_b32 a[14:15], v47 offset0:226 offset1:227\n" \
"v_mul_f32 v220, s20, v220\n" \
"v_mfma_f32_16x16x32_bf16 v[76:79], a[76:79], a[96:99], v[76:79]\n" \
"ds_read_b128 a[48:51], v50 offset:2176\n" \
"v_mul_f32 v221, s20, v221\n" \
"v_mfma_f32_16x16x32_bf16 v[80:83], a[76:79], a[100:103], v[80:83]\n" \
"ds_read2_b32 a[16:17], v48 offset0:96 offset1:97\n" \
"v_mul_f32 v222, s20, v222\n" \
"v_mfma_f32_16x16x32_bf16 v[84:87], a[76:79], a[104:107], v[84:87]\n" \
"ds_read2_b32 a[18:19], v48 offset0:98 offset1:99\n" \
"v_mul_f32 v223, s20, v223\n" \
"v_mfma_f32_16x16x32_bf16 v[88:91], a[76:79], a[108:111], v[88:91]\n" \
"ds_read_b128 a[52:55], v50 offset:2688\n" \
"v_cvt_pk_bf16_f32 v44, v220, v221\n" \
"v_mfma_f32_16x16x32_bf16 v[80:83], a[64:67], a[112:115], v[80:83]\n" \
"ds_read2_b32 a[20:21], v48 offset0:224 offset1:225\n" \
"v_cvt_pk_bf16_f32 v45, v222, v223\n" \
"v_mfma_f32_16x16x32_bf16 v[84:87], a[68:71], a[112:115], v[84:87]\n" \
"ds_read2_b32 a[22:23], v48 offset0:226 offset1:227\n" \
"v_mul_f32 v224, s20, v224\n" \
"v_mfma_f32_16x16x32_bf16 v[88:91], a[72:75], a[112:115], v[88:91]\n" \
"ds_read_b128 a[56:59], v50 offset:3200\n" \
"v_mul_f32 v225, s20, v225\n" \
"v_mfma_f32_16x16x32_bf16 v[92:95], a[76:79], a[112:115], v[92:95]\n" \
"ds_read2_b32 a[24:25], v49 offset0:96 offset1:97\n" \
"v_mul_f32 v226, s20, v226\n" \
"v_mfma_f32_16x16x32_bf16 v[80:83], a[80:83], a[96:99], v[80:83]\n" \
"ds_read2_b32 a[26:27], v49 offset0:98 offset1:99\n" \
"v_mul_f32 v227, s20, v227\n" \
"v_mfma_f32_16x16x32_bf16 v[84:87], a[80:83], a[100:103], v[84:87]\n" \
"ds_read_b128 a[60:63], v50 offset:3712\n" \
"v_mfma_f32_16x16x32_bf16 v[88:91], a[80:83], a[104:107], v[88:91]\n" \
"ds_read2_b32 a[28:29], v49 offset0:224 offset1:225\n" \
"v_mfma_f32_16x16x32_bf16 v[92:95], a[80:83], a[108:111], v[92:95]\n" \
"ds_read2_b32 a[30:31], v49 offset0:226 offset1:227\n" \
"v_mfma_f32_16x16x32_bf16 v[84:87], a[64:67], a[116:119], v[84:87]\n" \
"v_mfma_f32_16x16x32_bf16 v[88:91], a[68:71], a[116:119], v[88:91]\n" \
"buffer_store_dwordx4 v[30:33], v4, s[36:39], s96 offen offset:0 sc1\n" \
"s_add_u32 s97, s96, s98\n" \
"v_mfma_f32_16x16x32_bf16 v[92:95], a[72:75], a[116:119], v[92:95]\n" \
"v_cvt_pk_bf16_f32 v30, v224, v225\n" \
"v_cvt_pk_bf16_f32 v31, v226, v227\n" \
"v_mfma_f32_16x16x32_bf16 v[84:87], a[84:87], a[96:99], v[84:87]\n" \
"v_mul_f32 v228, s20, v228\n" \
"v_mul_f32 v229, s20, v229\n" \
"v_mfma_f32_16x16x32_bf16 v[88:91], a[84:87], a[100:103], v[88:91]\n" \
"v_mul_f32 v230, s20, v230\n" \
"v_mul_f32 v231, s20, v231\n" \
"v_mfma_f32_16x16x32_bf16 v[92:95], a[84:87], a[104:107], v[92:95]\n" \
"buffer_store_dwordx4 v[34:37], v4, s[36:39], s97 offen offset:0 sc1\n" \
"s_add_u32 s96, s96, s92\n" \
"v_mfma_f32_16x16x32_bf16 v[88:91], a[64:67], a[120:123], v[88:91]\n" \
"v_cvt_pk_bf16_f32 v32, v228, v229\n" \
"v_cvt_pk_bf16_f32 v33, v230, v231\n" \
"v_mfma_f32_16x16x32_bf16 v[92:95], a[68:71], a[120:123], v[92:95]\n" \
"v_mul_f32 v232, s20, v232\n" \
"v_mul_f32 v233, s20, v233\n" \
"v_mfma_f32_16x16x32_bf16 v[88:91], a[88:91], a[96:99], v[88:91]\n" \
"v_mul_f32 v234, s20, v234\n" \
"v_mul_f32 v235, s20, v235\n" \
"v_mfma_f32_16x16x32_bf16 v[92:95], a[88:91], a[100:103], v[92:95]\n" \
"buffer_store_dwordx4 v[38:41], v4, s[36:39], s96 offen offset:0 sc1\n" \
"s_add_u32 s97, s96, s98\n" \
"v_mfma_f32_16x16x32_bf16 v[92:95], a[64:67], a[124:127], v[92:95]\n" \
"s_waitcnt lgkmcnt(0)\n" \
"v_cvt_pk_bf16_f32 v34, v232, v233\n" \
"v_mfma_f32_16x16x32_bf16 v[92:95], a[92:95], a[96:99], v[92:95]\n" \
"v_cvt_pk_bf16_f32 v35, v234, v235\n" \
"v_mul_f32 v236, s20, v236\n" \
"v_mfma_f32_16x16x32_bf16 v[64:67], a[0:3], a[32:35], v[64:67]\n" \
"ds_read_b128 a[96:99], v50 offset:64\n" \
"v_mul_f32 v237, s20, v237\n" \
"v_mfma_f32_16x16x32_bf16 v[68:71], a[0:3], a[36:39], v[68:71]\n" \
"ds_read2_b32 a[64:65], v46 offset0:112 offset1:113\n" \
"v_mul_f32 v238, s20, v238\n" \
"v_mfma_f32_16x16x32_bf16 v[68:71], a[4:7], a[32:35], v[68:71]\n" \
"ds_read2_b32 a[66:67], v46 offset0:114 offset1:115\n" \
"v_mul_f32 v239, s20, v239\n" \
"v_mfma_f32_16x16x32_bf16 v[72:75], a[4:7], a[36:39], v[72:75]\n" \
"ds_read_b128 a[100:103], v50 offset:576\n" \
"v_cvt_pk_bf16_f32 v36, v236, v237\n" \
"v_mfma_f32_16x16x32_bf16 v[72:75], a[0:3], a[40:43], v[72:75]\n" \
"ds_read2_b32 a[68:69], v46 offset0:240 offset1:241\n" \
"v_cvt_pk_bf16_f32 v37, v238, v239\n" \
"v_mfma_f32_16x16x32_bf16 v[76:79], a[4:7], a[40:43], v[76:79]\n" \
"ds_read2_b32 a[70:71], v46 offset0:242 offset1:243\n" \
"v_mul_f32 v240, s20, v240\n" \
"v_mfma_f32_16x16x32_bf16 v[72:75], a[8:11], a[32:35], v[72:75]\n" \
"ds_read_b128 a[104:107], v50 offset:1088\n" \
"v_mul_f32 v241, s20, v241\n" \
"v_mfma_f32_16x16x32_bf16 v[76:79], a[8:11], a[36:39], v[76:79]\n" \
"ds_read2_b32 a[72:73], v47 offset0:112 offset1:113\n" \
"v_mul_f32 v242, s20, v242\n" \
"v_mfma_f32_16x16x32_bf16 v[80:83], a[8:11], a[40:43], v[80:83]\n" \
"ds_read2_b32 a[74:75], v47 offset0:114 offset1:115\n" \
"v_mul_f32 v243, s20, v243\n" \
"v_mfma_f32_16x16x32_bf16 v[76:79], a[0:3], a[44:47], v[76:79]\n" \
"ds_read_b128 a[108:111], v50 offset:1600\n" \
"v_cvt_pk_bf16_f32 v38, v240, v241\n" \
"v_mfma_f32_16x16x32_bf16 v[80:83], a[4:7], a[44:47], v[80:83]\n" \
"ds_read2_b32 a[76:77], v47 offset0:240 offset1:241\n" \
"v_cvt_pk_bf16_f32 v39, v242, v243\n" \
"v_mfma_f32_16x16x32_bf16 v[84:87], a[8:11], a[44:47], v[84:87]\n" \
"ds_read2_b32 a[78:79], v47 offset0:242 offset1:243\n" \
"v_mul_f32 v244, s20, v244\n" \
"v_mfma_f32_16x16x32_bf16 v[76:79], a[12:15], a[32:35], v[76:79]\n" \
"ds_read_b128 a[112:115], v50 offset:2112\n" \
"v_mul_f32 v245, s20, v245\n" \
"v_mfma_f32_16x16x32_bf16 v[80:83], a[12:15], a[36:39], v[80:83]\n" \
"ds_read2_b32 a[80:81], v48 offset0:112 offset1:113\n" \
"v_mul_f32 v246, s20, v246\n" \
"v_mfma_f32_16x16x32_bf16 v[84:87], a[12:15], a[40:43], v[84:87]\n" \
"ds_read2_b32 a[82:83], v48 offset0:114 offset1:115\n" \
"v_mul_f32 v247, s20, v247\n" \
"v_mfma_f32_16x16x32_bf16 v[88:91], a[12:15], a[44:47], v[88:91]\n" \
"ds_read_b128 a[116:119], v50 offset:2624\n" \
"v_cvt_pk_bf16_f32 v40, v244, v245\n" \
"v_mfma_f32_16x16x32_bf16 v[80:83], a[0:3], a[48:51], v[80:83]\n" \
"ds_read2_b32 a[84:85], v48 offset0:240 offset1:241\n" \
"v_cvt_pk_bf16_f32 v41, v246, v247\n" \
"v_mfma_f32_16x16x32_bf16 v[84:87], a[4:7], a[48:51], v[84:87]\n" \
"ds_read2_b32 a[86:87], v48 offset0:242 offset1:243\n" \
"v_mul_f32 v248, s20, v248\n" \
"v_mfma_f32_16x16x32_bf16 v[88:91], a[8:11], a[48:51], v[88:91]\n" \
"ds_read_b128 a[120:123], v50 offset:3136\n" \
"v_mul_f32 v249, s20, v249\n" \
"v_mfma_f32_16x16x32_bf16 v[92:95], a[12:15], a[48:51], v[92:95]\n" \
"ds_read2_b32 a[88:89], v49 offset0:112 offset1:113\n" \
"v_mul_f32 v250, s20, v250\n" \
"v_mfma_f32_16x16x32_bf16 v[80:83], a[16:19], a[32:35], v[80:83]\n" \
"ds_read2_b32 a[90:91], v49 offset0:114 offset1:115\n" \
"v_mul_f32 v251, s20, v251\n" \
"v_mfma_f32_16x16x32_bf16 v[84:87], a[16:19], a[36:39], v[84:87]\n" \
"ds_read_b128 a[124:127], v50 offset:3648\n" \
"v_mfma_f32_16x16x32_bf16 v[88:91], a[16:19], a[40:43], v[88:91]\n" \
"ds_read2_b32 a[92:93], v49 offset0:240 offset1:241\n" \
"v_mfma_f32_16x16x32_bf16 v[92:95], a[16:19], a[44:47], v[92:95]\n" \
"ds_read2_b32 a[94:95], v49 offset0:242 offset1:243\n" \
"v_mfma_f32_16x16x32_bf16 v[84:87], a[0:3], a[52:55], v[84:87]\n" \
"v_mfma_f32_16x16x32_bf16 v[88:91], a[4:7], a[52:55], v[88:91]\n" \
"buffer_store_dwordx4 v[42:45], v4, s[36:39], s97 offen offset:0 sc1\n" \
"s_add_u32 s96, s96, s92\n" \
"v_mfma_f32_16x16x32_bf16 v[92:95], a[8:11], a[52:55], v[92:95]\n" \
"v_cvt_pk_bf16_f32 v42, v248, v249\n" \
"v_cvt_pk_bf16_f32 v43, v250, v251\n" \
"v_mfma_f32_16x16x32_bf16 v[84:87], a[20:23], a[32:35], v[84:87]\n" \
"v_mul_f32 v252, s20, v252\n" \
"v_mul_f32 v253, s20, v253\n" \
"v_mfma_f32_16x16x32_bf16 v[88:91], a[20:23], a[36:39], v[88:91]\n" \
"v_mul_f32 v254, s20, v254\n" \
"v_mul_f32 v255, s20, v255\n" \
"v_mfma_f32_16x16x32_bf16 v[92:95], a[20:23], a[40:43], v[92:95]\n" \
"buffer_store_dwordx4 v[30:33], v4, s[36:39], s96 offen offset:0 sc1\n" \
"s_add_u32 s97, s96, s98\n" \
"v_mfma_f32_16x16x32_bf16 v[88:91], a[0:3], a[56:59], v[88:91]\n" \
"v_cvt_pk_bf16_f32 v44, v252, v253\n" \
"v_cvt_pk_bf16_f32 v45, v254, v255\n" \
"v_mfma_f32_16x16x32_bf16 v[92:95], a[4:7], a[56:59], v[92:95]\n" \
"v_mfma_f32_16x16x32_bf16 v[88:91], a[24:27], a[32:35], v[88:91]\n" \
"v_mfma_f32_16x16x32_bf16 v[92:95], a[24:27], a[36:39], v[92:95]\n" \
"buffer_store_dwordx4 v[34:37], v4, s[36:39], s97 offen offset:0 sc1\n" \
"s_add_u32 s96, s96, s92\n" \
"v_mfma_f32_16x16x32_bf16 v[92:95], a[0:3], a[60:63], v[92:95]\n" \
"s_waitcnt lgkmcnt(0)\n" \
"v_mfma_f32_16x16x32_bf16 v[92:95], a[28:31], a[32:35], v[92:95]\n" \
"v_mfma_f32_16x16x32_bf16 v[64:67], a[64:67], a[96:99], v[64:67]\n" \
"ds_read_b64 v[96:97], v51 offset:10816\n" \
"v_mfma_f32_16x16x32_bf16 v[68:71], a[64:67], a[100:103], v[68:71]\n" \
"ds_read_b64 v[98:99], v51 offset:11328\n" \
"v_mfma_f32_16x16x32_bf16 v[68:71], a[68:71], a[96:99], v[68:71]\n" \
"ds_read_b64 v[100:101], v51 offset:11840\n" \
"s_lshl_b32 s59, %2, 10\n" \
"v_add_u32 v153, s59, v4\n" \
"v_mfma_f32_16x16x32_bf16 v[72:75], a[68:71], a[100:103], v[72:75]\n" \
"ds_read_b64 v[102:103], v51 offset:12352\n" \
"v_and_b32 v144, 0x70, v153\n" \
"v_lshrrev_b32 v153, 7, v153\n" \
"v_mfma_f32_16x16x32_bf16 v[72:75], a[64:67], a[104:107], v[72:75]\n" \
"ds_read_b64 v[104:105], v51 offset:12864\n" \
"v_lshl_add_u32 v144, v153, 8, v144\n" \
"v_lshrrev_b32 v145, 2, v144\n" \
"v_mfma_f32_16x16x32_bf16 v[76:79], a[68:71], a[104:107], v[76:79]\n" \
"LH9_%=:\ns_cbranch_execz LH13_%=\n" \
"ds_read_b64 v[106:107], v51 offset:13376\n" \
"v_add_u32 v146, 16384, v145\n" \
"v_add_u32 v147, 98304, v145\n" \
"v_mfma_f32_16x16x32_bf16 v[72:75], a[72:75], a[96:99], v[72:75]\n" \
"ds_read_b64 v[108:109], v51 offset:13888\n" \
"v_add_u32 v148, 114688, v145\n" \
"v_add_u32 v149, 2048, v145\n" \
"v_mfma_f32_16x16x32_bf16 v[76:79], a[72:75], a[100:103], v[76:79]\n" \
"ds_read_b64 v[110:111], v51 offset:14400\n" \
"v_add_u32 v150, 18432, v145\n" \
"v_add_u32 v151, 100352, v145\n" \
"v_mfma_f32_16x16x32_bf16 v[80:83], a[72:75], a[104:107], v[80:83]\n" \
"v_add_u32 v152, 116736, v145\n" \
"s_lshl_b32 s59, s8, 14\n" \
"v_mfma_f32_16x16x32_bf16 v[76:79], a[64:67], a[108:111], v[76:79]\n" \
"s_add_u32 s59, s59, 67108864\n" \
"s_add_u32 s60, s59, 8192\n" \
"v_mfma_f32_16x16x32_bf16 v[80:83], a[68:71], a[108:111], v[80:83]\n" \
"s_mov_b32 s94, 0x05040100\n" \
"s_mov_b32 s95, 0x07060302\n" \
"v_mfma_f32_16x16x32_bf16 v[84:87], a[72:75], a[108:111], v[84:87]\n" \
"v_mfma_f32_16x16x32_bf16 v[76:79], a[76:79], a[96:99], v[76:79]\n" \
"s_waitcnt lgkmcnt(0)\n" \
"v_mfma_f32_16x16x32_bf16 v[80:83], a[76:79], a[100:103], v[80:83]\n" \
"v_and_b32 v112, 0xffff0000, v97\n" \
"v_lshlrev_b32 v113, 16, v97\n" \
"v_mfma_f32_16x16x32_bf16 v[84:87], a[76:79], a[104:107], v[84:87]\n" \
"v_and_b32 v114, 0xffff0000, v96\n" \
"v_lshlrev_b32 v115, 16, v96\n" \
"v_mfma_f32_16x16x32_bf16 v[88:91], a[76:79], a[108:111], v[88:91]\n" \
"v_and_b32 v116, 0xffff0000, v99\n" \
"v_lshlrev_b32 v117, 16, v99\n" \
"v_mfma_f32_16x16x32_bf16 v[80:83], a[64:67], a[112:115], v[80:83]\n" \
"v_and_b32 v118, 0xffff0000, v98\n" \
"v_lshlrev_b32 v119, 16, v98\n" \
"v_mfma_f32_16x16x32_bf16 v[84:87], a[68:71], a[112:115], v[84:87]\n" \
"v_and_b32 v120, 0xffff0000, v101\n" \
"v_lshlrev_b32 v121, 16, v101\n" \
"v_mfma_f32_16x16x32_bf16 v[88:91], a[72:75], a[112:115], v[88:91]\n" \
"v_and_b32 v122, 0xffff0000, v100\n" \
"v_lshlrev_b32 v123, 16, v100\n" \
"v_mfma_f32_16x16x32_bf16 v[92:95], a[76:79], a[112:115], v[92:95]\n" \
"v_and_b32 v124, 0xffff0000, v103\n" \
"v_lshlrev_b32 v125, 16, v103\n" \
"v_mfma_f32_16x16x32_bf16 v[80:83], a[80:83], a[96:99], v[80:83]\n" \
"v_and_b32 v126, 0xffff0000, v102\n" \
"v_lshlrev_b32 v127, 16, v102\n" \
"v_mfma_f32_16x16x32_bf16 v[84:87], a[80:83], a[100:103], v[84:87]\n" \
"v_and_b32 v128, 0xffff0000, v105\n" \
"v_lshlrev_b32 v129, 16, v105\n" \
"v_mfma_f32_16x16x32_bf16 v[88:91], a[80:83], a[104:107], v[88:91]\n" \
"v_and_b32 v130, 0xffff0000, v104\n" \
"v_lshlrev_b32 v131, 16, v104\n" \
"v_mfma_f32_16x16x32_bf16 v[92:95], a[80:83], a[108:111], v[92:95]\n" \
"v_and_b32 v132, 0xffff0000, v107\n" \
"v_lshlrev_b32 v133, 16, v107\n" \
"v_mfma_f32_16x16x32_bf16 v[84:87], a[64:67], a[116:119], v[84:87]\n" \
"v_and_b32 v134, 0xffff0000, v106\n" \
"v_lshlrev_b32 v135, 16, v106\n" \
"v_mfma_f32_16x16x32_bf16 v[88:91], a[68:71], a[116:119], v[88:91]\n" \
"buffer_store_dwordx4 v[38:41], v4, s[36:39], s96 offen offset:0 sc1\n" \
"s_add_u32 s97, s96, s98\n" \
"v_mfma_f32_16x16x32_bf16 v[92:95], a[72:75], a[116:119], v[92:95]\n" \
"v_and_b32 v136, 0xffff0000, v109\n" \
"v_lshlrev_b32 v137, 16, v109\n" \
"v_mfma_f32_16x16x32_bf16 v[84:87], a[84:87], a[96:99], v[84:87]\n" \
"v_and_b32 v138, 0xffff0000, v108\n" \
"v_lshlrev_b32 v139, 16, v108\n" \
"v_mfma_f32_16x16x32_bf16 v[88:91], a[84:87], a[100:103], v[88:91]\n" \
"v_and_b32 v140, 0xffff0000, v111\n" \
"v_lshlrev_b32 v141, 16, v111\n" \
"v_mfma_f32_16x16x32_bf16 v[92:95], a[84:87], a[104:107], v[92:95]\n" \
"v_and_b32 v142, 0xffff0000, v110\n" \
"v_lshlrev_b32 v143, 16, v110\n" \
"v_mfma_f32_16x16x32_bf16 v[88:91], a[64:67], a[120:123], v[88:91]\n" \
"v_mfma_f32_16x16x32_bf16 v[92:95], a[68:71], a[120:123], v[92:95]\n" \
"v_mfma_f32_16x16x32_bf16 v[88:91], a[88:91], a[96:99], v[88:91]\n" \
"v_mfma_f32_16x16x32_bf16 v[92:95], a[88:91], a[100:103], v[92:95]\n" \
"buffer_store_dwordx4 v[42:45], v4, s[36:39], s97 offen offset:0 sc1\n" \
"s_add_u32 s57, s57, s93\n" \
"s_mov_b32 s96, s57\n" \
"v_mfma_f32_16x16x32_bf16 v[92:95], a[64:67], a[124:127], v[92:95]\n" \
"s_waitcnt lgkmcnt(0)\n" \
"v_mfma_f32_16x16x32_bf16 v[92:95], a[92:95], a[96:99], v[92:95]\n" \
"v_pk_fma_f32 v[112:113], v[112:113], v[28:29], v[64:65] op_sel_hi:[1,0,1]\n" \
"v_pk_fma_f32 v[114:115], v[114:115], v[28:29], v[66:67] op_sel_hi:[1,0,1]\n" \
"v_pk_fma_f32 v[116:117], v[116:117], v[28:29], v[68:69] op_sel_hi:[1,0,1]\n" \
"v_pk_fma_f32 v[118:119], v[118:119], v[28:29], v[70:71] op_sel_hi:[1,0,1]\n" \
"v_pk_fma_f32 v[120:121], v[120:121], v[28:29], v[72:73] op_sel_hi:[1,0,1]\n" \
"v_pk_fma_f32 v[122:123], v[122:123], v[28:29], v[74:75] op_sel_hi:[1,0,1]\n" \
"v_pk_fma_f32 v[124:125], v[124:125], v[28:29], v[76:77] op_sel_hi:[1,0,1]\n" \
"v_pk_fma_f32 v[126:127], v[126:127], v[28:29], v[78:79] op_sel_hi:[1,0,1]\n" \
"v_pk_fma_f32 v[128:129], v[128:129], v[28:29], v[80:81] op_sel_hi:[1,0,1]\n" \
"v_pk_fma_f32 v[130:131], v[130:131], v[28:29], v[82:83] op_sel_hi:[1,0,1]\n" \
"v_pk_fma_f32 v[132:133], v[132:133], v[28:29], v[84:85] op_sel_hi:[1,0,1]\n" \
"v_pk_fma_f32 v[134:135], v[134:135], v[28:29], v[86:87] op_sel_hi:[1,0,1]\n" \
"v_pk_fma_f32 v[136:137], v[136:137], v[28:29], v[88:89] op_sel_hi:[1,0,1]\n" \
"v_pk_fma_f32 v[138:139], v[138:139], v[28:29], v[90:91] op_sel_hi:[1,0,1]\n" \
"v_pk_fma_f32 v[140:141], v[140:141], v[28:29], v[92:93] op_sel_hi:[1,0,1]\n" \
"v_pk_fma_f32 v[142:143], v[142:143], v[28:29], v[94:95] op_sel_hi:[1,0,1]\n" \
"v_cvt_pk_bf16_f32 v96, v112, v113\n" \
"v_cvt_pk_bf16_f32 v97, v114, v115\n" \
"ds_write_b64 v52, v[96:97] offset:0\n" \
"v_cvt_pk_bf16_f32 v98, v116, v117\n" \
"v_cvt_pk_bf16_f32 v99, v118, v119\n" \
"ds_write_b64 v52, v[98:99] offset:512\n" \
"v_cvt_pk_bf16_f32 v100, v120, v121\n" \
"v_cvt_pk_bf16_f32 v101, v122, v123\n" \
"ds_write_b64 v52, v[100:101] offset:1024\n" \
"v_cvt_pk_bf16_f32 v102, v124, v125\n" \
"v_cvt_pk_bf16_f32 v103, v126, v127\n" \
"ds_write_b64 v52, v[102:103] offset:1536\n" \
"v_cvt_pk_bf16_f32 v104, v128, v129\n" \
"v_cvt_pk_bf16_f32 v105, v130, v131\n" \
"ds_write_b64 v52, v[104:105] offset:2048\n" \
"v_cvt_pk_bf16_f32 v106, v132, v133\n" \
"v_cvt_pk_bf16_f32 v107, v134, v135\n" \
"ds_write_b64 v52, v[106:107] offset:2560\n" \
"v_cvt_pk_bf16_f32 v108, v136, v137\n" \
"v_cvt_pk_bf16_f32 v109, v138, v139\n" \
"ds_write_b64 v52, v[108:109] offset:3072\n" \
"v_cvt_pk_bf16_f32 v110, v140, v141\n" \
"v_cvt_pk_bf16_f32 v111, v142, v143\n" \
"ds_write_b64 v52, v[110:111] offset:3584\n" \
"s_waitcnt vmcnt(39) lgkmcnt(0)\n" \
"s_barrier\n" \
"ds_read2_b32 v[64:65], v145 offset1:8\n" \
"ds_read2_b32 v[66:67], v146 offset1:8\n" \
"ds_read2_b32 v[68:69], v147 offset1:8\n" \
"ds_read2_b32 v[70:71], v148 offset1:8\n" \
"ds_read2_b32 v[72:73], v149 offset1:8\n" \
"ds_read2_b32 v[74:75], v150 offset1:8\n" \
"ds_read2_b32 v[76:77], v151 offset1:8\n" \
"ds_read2_b32 v[78:79], v152 offset1:8\n" \
"ds_read_b64_tr_b16 a[24:25], v9 offset:33280\n" \
"ds_read_b64_tr_b16 a[26:27], v10 offset:33280\n" \
"ds_read_b64_tr_b16 a[28:29], v11 offset:33280\n" \
"ds_read_b64_tr_b16 a[30:31], v12 offset:33280\n" \
"ds_read_b128 a[0:3], v7 offset:0\n" \
"ds_read_b128 a[4:7], v7 offset:2048\n" \
"ds_read_b128 a[8:11], v7 offset:4096\n" \
"ds_read_b128 a[12:15], v7 offset:6144\n" \
"ds_read_b64_tr_b16 a[32:33], v9 offset:33792\n" \
"ds_read_b64_tr_b16 a[34:35], v10 offset:33792\n" \
"ds_read_b64_tr_b16 a[36:37], v11 offset:33792\n" \
"ds_read_b64_tr_b16 a[38:39], v12 offset:33792\n" \
"ds_read_b64_tr_b16 a[40:41], v9 offset:34304\n" \
"ds_read_b64_tr_b16 a[42:43], v10 offset:34304\n" \
"s_waitcnt lgkmcnt(14)\n" \
"s_barrier\n" \
"ds_read_b64_tr_b16 a[44:45], v11 offset:34304\n" \
"ds_read_b64_tr_b16 a[46:47], v12 offset:34304\n" \
"ds_read_b64_tr_b16 a[16:17], v9 offset:32768\n" \
"ds_read_b64_tr_b16 a[18:19], v10 offset:32768\n" \
"ds_read_b64_tr_b16 a[20:21], v11 offset:32768\n" \
"ds_read_b64_tr_b16 a[22:23], v12 offset:32768\n" \
"s_mov_b32 s46, 256\n" \
"s_add_u32 m0, s50, 32768\n" \
"v_perm_b32 v80, v66, v64, s94\n" \
"v_perm_b32 v81, v70, v68, s94\n" \
"v_perm_b32 v82, v67, v65, s94\n" \
"v_perm_b32 v83, v71, v69, s94\n" \
"buffer_load_dwordx4 v13, s[28:31], s46 offen lds\n" \
"s_add_u32 m0, s50, 36864\n" \
"buffer_store_dwordx4 v[80:83], v144, s[36:39], s59 offen offset:0 sc1\n" \
"buffer_load_dwordx4 v14, s[28:31], s46 offen lds\n" \
"s_add_u32 m0, s50, 40960\n" \
"v_perm_b32 v84, v66, v64, s95\n" \
"v_perm_b32 v85, v70, v68, s95\n" \
"v_perm_b32 v86, v67, v65, s95\n" \
"v_perm_b32 v87, v71, v69, s95\n" \
"buffer_load_dwordx4 v15, s[28:31], s46 offen lds\n" \
"s_add_u32 m0, s50, 45056\n" \
"buffer_store_dwordx4 v[84:87], v144, s[36:39], s59 offen offset:128 sc1\n" \
"buffer_load_dwordx4 v16, s[28:31], s46 offen lds\n" \
"s_mov_b32 s46, 256\n" \
"s_add_u32 m0, s50, 49152\n" \
"v_perm_b32 v88, v74, v72, s94\n" \
"v_perm_b32 v89, v78, v76, s94\n" \
"v_perm_b32 v90, v75, v73, s94\n" \
"v_perm_b32 v91, v79, v77, s94\n" \
"buffer_load_dwordx4 v17, s[28:31], s46 offen lds\n" \
"s_add_u32 m0, s50, 53248\n" \
"buffer_store_dwordx4 v[88:91], v144, s[36:39], s60 offen offset:0 sc1\n" \
"buffer_load_dwordx4 v18, s[28:31], s46 offen lds\n" \
"s_add_u32 m0, s50, 57344\n" \
"v_perm_b32 v92, v74, v72, s95\n" \
"v_perm_b32 v93, v78, v76, s95\n" \
"v_perm_b32 v94, v75, v73, s95\n" \
"v_perm_b32 v95, v79, v77, s95\n" \
"buffer_load_dwordx4 v19, s[28:31], s46 offen lds\n" \
"s_add_u32 m0, s50, 61440\n" \
"buffer_store_dwordx4 v[92:95], v144, s[36:39], s60 offen offset:128 sc1\n" \
"buffer_load_dwordx4 v20, s[28:31], s46 offen lds\n" \
"s_waitcnt lgkmcnt(0)\n" \
"v_mfma_f32_16x16x32_bf16 v[96:99], a[24:27], a[0:3], 0\n" \
"ds_read_b64_tr_b16 a[72:73], v9 offset:41472\n" \
"v_accvgpr_read_b32 v46, a128\n" \
"v_mfma_f32_16x16x32_bf16 v[100:103], a[28:31], a[0:3], 0\n" \
"ds_read_b64_tr_b16 a[74:75], v10 offset:41472\n" \
"v_accvgpr_read_b32 v47, a129\n" \
"v_mfma_f32_16x16x32_bf16 v[104:107], a[24:27], a[4:7], 0\n" \
"ds_read_b64_tr_b16 a[76:77], v11 offset:41472\n" \
"v_accvgpr_read_b32 v48, a130\n" \
"v_mfma_f32_16x16x32_bf16 v[108:111], a[28:31], a[4:7], 0\n" \
"ds_read_b64_tr_b16 a[78:79], v12 offset:41472\n" \
"v_accvgpr_read_b32 v49, a131\n" \
"v_mfma_f32_16x16x32_bf16 v[112:115], a[24:27], a[8:11], 0\n" \
"ds_read_b128 a[48:51], v8 offset:0\n" \
"v_mul_f32 v46, s20, v46\n" \
"v_mfma_f32_16x16x32_bf16 v[116:119], a[28:31], a[8:11], 0\n" \
"ds_read_b128 a[52:55], v8 offset:2048\n" \
"v_mul_f32 v47, s20, v47\n" \
"v_mfma_f32_16x16x32_bf16 v[120:123], a[24:27], a[12:15], 0\n" \
"ds_read_b128 a[56:59], v8 offset:4096\n" \
"v_mul_f32 v48, s20, v48\n" \
"v_mfma_f32_16x16x32_bf16 v[124:127], a[28:31], a[12:15], 0\n" \
"ds_read_b128 a[60:63], v8 offset:6144\n" \
"v_mul_f32 v49, s20, v49\n" \
"v_mfma_f32_16x16x32_bf16 v[128:131], a[32:35], a[0:3], 0\n" \
"s_mov_b32 s47, 1048576\n" \
"s_add_u32 m0, s51, 0\n" \
"ds_read_b64_tr_b16 a[80:81], v9 offset:41984\n" \
"v_mfma_f32_16x16x32_bf16 v[132:135], a[36:39], a[0:3], 0\n" \
"buffer_load_dwordx4 v21, s[32:35], s47 offen offset:0 lds\n" \
"v_mfma_f32_16x16x32_bf16 v[136:139], a[32:35], a[4:7], 0\n" \
"ds_read_b64_tr_b16 a[82:83], v10 offset:41984\n" \
"v_cvt_pk_bf16_f32 v30, v46, v47\n" \
"v_mfma_f32_16x16x32_bf16 v[140:143], a[36:39], a[4:7], 0\n" \
"ds_read_b64_tr_b16 a[84:85], v11 offset:41984\n" \
"v_cvt_pk_bf16_f32 v31, v48, v49\n" \
"v_mfma_f32_16x16x32_bf16 v[144:147], a[32:35], a[8:11], 0\n" \
"s_add_u32 m0, s51, 896\n" \
"ds_read_b64_tr_b16 a[86:87], v12 offset:41984\n" \
"v_accvgpr_read_b32 v50, a132\n" \
"v_mfma_f32_16x16x32_bf16 v[148:151], a[36:39], a[8:11], 0\n" \
"buffer_load_dwordx4 v21, s[32:35], s47 offen offset:128 lds\n" \
"v_mfma_f32_16x16x32_bf16 v[152:155], a[32:35], a[12:15], 0\n" \
"ds_read_b64_tr_b16 a[88:89], v9 offset:42496\n" \
"v_accvgpr_read_b32 v51, a133\n" \
"v_mfma_f32_16x16x32_bf16 v[156:159], a[36:39], a[12:15], 0\n" \
"ds_read_b64_tr_b16 a[90:91], v10 offset:42496\n" \
"v_accvgpr_read_b32 v52, a134\n" \
"v_mfma_f32_16x16x32_bf16 v[160:163], a[40:43], a[0:3], 0\n" \
"s_mov_b32 s47, 1310720\n" \
"s_add_u32 m0, s51, 8192\n" \
"ds_read_b64_tr_b16 a[92:93], v11 offset:42496\n" \
"v_mfma_f32_16x16x32_bf16 v[164:167], a[44:47], a[0:3], 0\n" \
"buffer_load_dwordx4 v21, s[32:35], s47 offen offset:0 lds\n" \
"v_mfma_f32_16x16x32_bf16 v[168:171], a[40:43], a[4:7], 0\n" \
"ds_read_b64_tr_b16 a[94:95], v12 offset:42496\n" \
"v_accvgpr_read_b32 v53, a135\n" \
"v_mfma_f32_16x16x32_bf16 v[172:175], a[44:47], a[4:7], 0\n" \
"ds_read_b64_tr_b16 a[64:65], v9 offset:40960\n" \
"v_mul_f32 v50, s20, v50\n" \
"v_mfma_f32_16x16x32_bf16 v[176:179], a[40:43], a[8:11], 0\n" \
"s_add_u32 m0, s51, 9088\n" \
"ds_read_b64_tr_b16 a[66:67], v10 offset:40960\n" \
"v_mul_f32 v51, s20, v51\n" \
"v_mfma_f32_16x16x32_bf16 v[180:183], a[44:47], a[8:11], 0\n" \
"buffer_load_dwordx4 v21, s[32:35], s47 offen offset:128 lds\n" \
"v_mfma_f32_16x16x32_bf16 v[184:187], a[40:43], a[12:15], 0\n" \
"ds_read_b64_tr_b16 a[68:69], v11 offset:40960\n" \
"v_mul_f32 v52, s20, v52\n" \
"v_mfma_f32_16x16x32_bf16 v[188:191], a[44:47], a[12:15], 0\n" \
"ds_read_b64_tr_b16 a[70:71], v12 offset:40960\n" \
"v_mul_f32 v53, s20, v53\n" \
"v_mfma_f32_16x16x32_bf16 v[64:67], a[16:19], a[0:3], 0\n" \
"v_cvt_pk_bf16_f32 v32, v50, v51\n" \
"v_cvt_pk_bf16_f32 v33, v52, v53\n" \
"v_mfma_f32_16x16x32_bf16 v[68:71], a[20:23], a[0:3], 0\n" \
"buffer_store_dwordx4 v[30:33], v4, s[36:39], s96 offen offset:0 sc1\n" \
"s_add_u32 s97, s96, s98\n" \
"v_mfma_f32_16x16x32_bf16 v[72:75], a[16:19], a[4:7], 0\n" \
"v_accvgpr_read_b32 v46, a136\n" \
"v_accvgpr_read_b32 v47, a137\n" \
"v_mfma_f32_16x16x32_bf16 v[76:79], a[20:23], a[4:7], 0\n" \
"v_accvgpr_read_b32 v48, a138\n" \
"v_accvgpr_read_b32 v49, a139\n" \
"v_mfma_f32_16x16x32_bf16 v[80:83], a[16:19], a[8:11], 0\n" \
"v_mul_f32 v46, s20, v46\n" \
"v_mul_f32 v47, s20, v47\n" \
"v_mfma_f32_16x16x32_bf16 v[84:87], a[20:23], a[8:11], 0\n" \
"v_mul_f32 v48, s20, v48\n" \
"v_mul_f32 v49, s20, v49\n" \
"v_mfma_f32_16x16x32_bf16 v[88:91], a[16:19], a[12:15], 0\n" \
"s_waitcnt vmcnt(43) lgkmcnt(0)\n" \
"v_cvt_pk_bf16_f32 v34, v46, v47\n" \
"v_mfma_f32_16x16x32_bf16 v[92:95], a[20:23], a[12:15], 0\n" \
"s_barrier\n" \
"v_mfma_f32_16x16x32_bf16 v[96:99], a[72:75], a[48:51], v[96:99]\n" \
"s_mov_b32 s46, 384\n" \
"s_add_u32 m0, s50, 65536\n" \
"ds_read_b64_tr_b16 a[24:25], v9 offset:49664\n" \
"v_mfma_f32_16x16x32_bf16 v[100:103], a[76:79], a[48:51], v[100:103]\n" \
"buffer_load_dwordx4 v13, s[28:31], s46 offen lds\n" \
"v_mfma_f32_16x16x32_bf16 v[104:107], a[72:75], a[52:55], v[104:107]\n" \
"ds_read_b64_tr_b16 a[26:27], v10 offset:49664\n" \
"v_cvt_pk_bf16_f32 v35, v48, v49\n" \
"v_mfma_f32_16x16x32_bf16 v[108:111], a[76:79], a[52:55], v[108:111]\n" \
"ds_read_b64_tr_b16 a[28:29], v11 offset:49664\n" \
"v_accvgpr_read_b32 v50, a140\n" \
"v_mfma_f32_16x16x32_bf16 v[112:115], a[72:75], a[56:59], v[112:115]\n" \
"s_add_u32 m0, s50, 69632\n" \
"ds_read_b64_tr_b16 a[30:31], v12 offset:49664\n" \
"v_accvgpr_read_b32 v51, a141\n" \
"v_mfma_f32_16x16x32_bf16 v[116:119], a[76:79], a[56:59], v[116:119]\n" \
"buffer_load_dwordx4 v14, s[28:31], s46 offen lds\n" \
"v_mfma_f32_16x16x32_bf16 v[120:123], a[72:75], a[60:63], v[120:123]\n" \
"ds_read_b128 a[0:3], v5 offset:0\n" \
"v_accvgpr_read_b32 v52, a142\n" \
"v_mfma_f32_16x16x32_bf16 v[124:127], a[76:79], a[60:63], v[124:127]\n" \
"ds_read_b128 a[4:7], v5 offset:2048\n" \
"v_accvgpr_read_b32 v53, a143\n" \
"v_mfma_f32_16x16x32_bf16 v[128:131], a[80:83], a[48:51], v[128:131]\n" \
"s_add_u32 m0, s50, 73728\n" \
"ds_read_b128 a[8:11], v5 offset:4096\n" \
"v_mul_f32 v50, s20, v50\n" \
"v_mfma_f32_16x16x32_bf16 v[132:135], a[84:87], a[48:51], v[132:135]\n" \
"buffer_load_dwordx4 v15, s[28:31], s46 offen lds\n" \
"v_mfma_f32_16x16x32_bf16 v[136:139], a[80:83], a[52:55], v[136:139]\n" \
"ds_read_b128 a[12:15], v5 offset:6144\n" \
"v_mul_f32 v51, s20, v51\n" \
"v_mfma_f32_16x16x32_bf16 v[140:143], a[84:87], a[52:55], v[140:143]\n" \
"ds_read_b64_tr_b16 a[32:33], v9 offset:50176\n" \
"v_mul_f32 v52, s20, v52\n" \
"v_mfma_f32_16x16x32_bf16 v[144:147], a[80:83], a[56:59], v[144:147]\n" \
"s_add_u32 m0, s50, 77824\n" \
"ds_read_b64_tr_b16 a[34:35], v10 offset:50176\n" \
"v_mul_f32 v53, s20, v53\n" \
"v_mfma_f32_16x16x32_bf16 v[148:151], a[84:87], a[56:59], v[148:151]\n" \
"buffer_load_dwordx4 v16, s[28:31], s46 offen lds\n" \
"v_mfma_f32_16x16x32_bf16 v[152:155], a[80:83], a[60:63], v[152:155]\n" \
"ds_read_b64_tr_b16 a[36:37], v11 offset:50176\n" \
"v_cvt_pk_bf16_f32 v36, v50, v51\n" \
"v_mfma_f32_16x16x32_bf16 v[156:159], a[84:87], a[60:63], v[156:159]\n" \
"ds_read_b64_tr_b16 a[38:39], v12 offset:50176\n" \
"v_cvt_pk_bf16_f32 v37, v52, v53\n" \
"v_mfma_f32_16x16x32_bf16 v[160:163], a[88:91], a[48:51], v[160:163]\n" \
"s_mov_b32 s46, 384\n" \
"s_add_u32 m0, s50, 81920\n" \
"ds_read_b64_tr_b16 a[40:41], v9 offset:50688\n" \
"v_mfma_f32_16x16x32_bf16 v[164:167], a[92:95], a[48:51], v[164:167]\n" \
"buffer_load_dwordx4 v17, s[28:31], s46 offen lds\n" \
"v_mfma_f32_16x16x32_bf16 v[168:171], a[88:91], a[52:55], v[168:171]\n" \
"ds_read_b64_tr_b16 a[42:43], v10 offset:50688\n" \
"v_accvgpr_read_b32 v46, a144\n" \
"v_mfma_f32_16x16x32_bf16 v[172:175], a[92:95], a[52:55], v[172:175]\n" \
"ds_read_b64_tr_b16 a[44:45], v11 offset:50688\n" \
"v_accvgpr_read_b32 v47, a145\n" \
"v_mfma_f32_16x16x32_bf16 v[176:179], a[88:91], a[56:59], v[176:179]\n" \
"s_add_u32 m0, s50, 86016\n" \
"ds_read_b64_tr_b16 a[46:47], v12 offset:50688\n" \
"v_accvgpr_read_b32 v48, a146\n" \
"v_mfma_f32_16x16x32_bf16 v[180:183], a[92:95], a[56:59], v[180:183]\n" \
"buffer_load_dwordx4 v18, s[28:31], s46 offen lds\n" \
"v_mfma_f32_16x16x32_bf16 v[184:187], a[88:91], a[60:63], v[184:187]\n" \
"ds_read_b64_tr_b16 a[16:17], v9 offset:49152\n" \
"v_accvgpr_read_b32 v49, a147\n" \
"v_mfma_f32_16x16x32_bf16 v[188:191], a[92:95], a[60:63], v[188:191]\n" \
"ds_read_b64_tr_b16 a[18:19], v10 offset:49152\n" \
"v_mul_f32 v46, s20, v46\n" \
"v_mfma_f32_16x16x32_bf16 v[64:67], a[64:67], a[48:51], v[64:67]\n" \
"s_add_u32 m0, s50, 90112\n" \
"ds_read_b64_tr_b16 a[20:21], v11 offset:49152\n" \
"v_mul_f32 v47, s20, v47\n" \
"v_mfma_f32_16x16x32_bf16 v[68:71], a[68:71], a[48:51], v[68:71]\n" \
"buffer_load_dwordx4 v19, s[28:31], s46 offen lds\n" \
"v_mfma_f32_16x16x32_bf16 v[72:75], a[64:67], a[52:55], v[72:75]\n" \
"ds_read_b64_tr_b16 a[22:23], v12 offset:49152\n" \
"v_mul_f32 v48, s20, v48\n" \
"v_mfma_f32_16x16x32_bf16 v[76:79], a[68:71], a[52:55], v[76:79]\n" \
"v_mul_f32 v49, s20, v49\n" \
"v_cvt_pk_bf16_f32 v38, v46, v47\n" \
"v_mfma_f32_16x16x32_bf16 v[80:83], a[64:67], a[56:59], v[80:83]\n" \
"s_add_u32 m0, s50, 94208\n" \
"v_cvt_pk_bf16_f32 v39, v48, v49\n" \
"v_mfma_f32_16x16x32_bf16 v[84:87], a[68:71], a[56:59], v[84:87]\n" \
"buffer_load_dwordx4 v20, s[28:31], s46 offen lds\n" \
"v_mfma_f32_16x16x32_bf16 v[88:91], a[64:67], a[60:63], v[88:91]\n" \
"v_accvgpr_read_b32 v50, a148\n" \
"v_accvgpr_read_b32 v51, a149\n" \
"v_mfma_f32_16x16x32_bf16 v[92:95], a[68:71], a[60:63], v[92:95]\n" \
"s_waitcnt lgkmcnt(0)\n" \
"v_accvgpr_read_b32 v52, a150\n" \
"v_mfma_f32_16x16x32_bf16 v[96:99], a[24:27], a[0:3], v[96:99]\n" \
"s_mov_b32 s47, 1572864\n" \
"s_add_u32 m0, s51, 16384\n" \
"ds_read_b64_tr_b16 a[72:73], v9 offset:57856\n" \
"v_mfma_f32_16x16x32_bf16 v[100:103], a[28:31], a[0:3], v[100:103]\n" \
"buffer_load_dwordx4 v21, s[32:35], s47 offen offset:0 lds\n" \
"s_mov_b64 s[40:41], s[4:5]\n" \
"s_and_b32 s41, s41, 0xffff\n" \
"v_mfma_f32_16x16x32_bf16 v[104:107], a[24:27], a[4:7], v[104:107]\n" \
"ds_read_b64_tr_b16 a[74:75], v10 offset:57856\n" \
"v_accvgpr_read_b32 v53, a151\n" \
"s_lshl_b32 s62, s6, 20\n" \
"v_mfma_f32_16x16x32_bf16 v[108:111], a[28:31], a[4:7], v[108:111]\n" \
"ds_read_b64_tr_b16 a[76:77], v11 offset:57856\n" \
"v_mul_f32 v50, s20, v50\n" \
"s_add_u32 s16, s62, 33554432\n" \
"v_mfma_f32_16x16x32_bf16 v[112:115], a[24:27], a[8:11], v[112:115]\n" \
"s_add_u32 m0, s51, 17280\n" \
"ds_read_b64_tr_b16 a[78:79], v12 offset:57856\n" \
"v_mul_f32 v51, s20, v51\n" \
"v_mfma_f32_16x16x32_bf16 v[116:119], a[28:31], a[8:11], v[116:119]\n" \
"buffer_load_dwordx4 v21, s[32:35], s47 offen offset:128 lds\n" \
"s_lshl_b32 s63, s7, 16\n" \
"s_sub_u32 s63, 1048576, s63\n" \
"v_mfma_f32_16x16x32_bf16 v[120:123], a[24:27], a[12:15], v[120:123]\n" \
"ds_read_b128 a[48:51], v6 offset:0\n" \
"v_mul_f32 v52, s20, v52\n" \
"s_add_u32 s42, s16, s63\n" \
"v_mfma_f32_16x16x32_bf16 v[124:127], a[28:31], a[12:15], v[124:127]\n" \
"ds_read_b128 a[52:55], v6 offset:2048\n" \
"v_mul_f32 v53, s20, v53\n" \
"s_cmp_lt_u32 %2, 2\ns_cselect_b32 s62, 1064960, 0\ns_cselect_b32 s9, -1, 1\n" \
"v_mfma_f32_16x16x32_bf16 v[128:131], a[32:35], a[0:3], v[128:131]\n" \
"s_mov_b32 s47, 1835008\n" \
"s_add_u32 m0, s51, 24576\n" \
"ds_read_b128 a[56:59], v6 offset:4096\n" \
"v_mfma_f32_16x16x32_bf16 v[132:135], a[36:39], a[0:3], v[132:135]\n" \
"buffer_load_dwordx4 v21, s[32:35], s47 offen offset:0 lds\n" \
"s_add_u32 s16, s16, s62\n" \
"s_add_u32 s17, s16, 8192\n" \
"v_mfma_f32_16x16x32_bf16 v[136:139], a[32:35], a[4:7], v[136:139]\n" \
"ds_read_b128 a[60:63], v6 offset:6144\n" \
"v_cvt_pk_bf16_f32 v40, v50, v51\n" \
"s_lshl_b32 s9, s9, 14\n" \
"v_mfma_f32_16x16x32_bf16 v[140:143], a[36:39], a[4:7], v[140:143]\n" \
"ds_read_b64_tr_b16 a[80:81], v9 offset:58368\n" \
"v_cvt_pk_bf16_f32 v41, v52, v53\n" \
"s_mov_b32 s19, s9\n" \
"v_mfma_f32_16x16x32_bf16 v[144:147], a[32:35], a[8:11], v[144:147]\n" \
"s_add_u32 m0, s51, 25472\n" \
"ds_read_b64_tr_b16 a[82:83], v10 offset:58368\n" \
"v_accvgpr_read_b32 v46, a152\n" \
"v_mfma_f32_16x16x32_bf16 v[148:151], a[36:39], a[8:11], v[148:151]\n" \
"buffer_load_dwordx4 v21, s[32:35], s47 offen offset:128 lds\n" \
"s_and_b32 s63, %2, 1\n" \
"s_lshl_b32 s63, s63, 12\n" \
"v_mfma_f32_16x16x32_bf16 v[152:155], a[32:35], a[12:15], v[152:155]\n" \
"ds_read_b64_tr_b16 a[84:85], v11 offset:58368\n" \
"v_accvgpr_read_b32 v47, a153\n" \
"v_mfma_f32_16x16x32_bf16 v[156:159], a[36:39], a[12:15], v[156:159]\n" \
"ds_read_b64_tr_b16 a[86:87], v12 offset:58368\n" \
"v_accvgpr_read_b32 v48, a154\n" \
"v_mfma_f32_16x16x32_bf16 v[160:163], a[40:43], a[0:3], v[160:163]\n" \
"s_mov_b32 s47, 2097152\n" \
"s_add_u32 m0, s51, 32768\n" \
"ds_read_b64_tr_b16 a[88:89], v9 offset:58880\n" \
"v_mfma_f32_16x16x32_bf16 v[164:167], a[44:47], a[0:3], v[164:167]\n" \
"buffer_load_dwordx4 v21, s[32:35], s47 offen offset:0 lds\n" \
"v_mfma_f32_16x16x32_bf16 v[168:171], a[40:43], a[4:7], v[168:171]\n" \
"ds_read_b64_tr_b16 a[90:91], v10 offset:58880\n" \
"v_accvgpr_read_b32 v49, a155\n" \
"v_mfma_f32_16x16x32_bf16 v[172:175], a[44:47], a[4:7], v[172:175]\n" \
"ds_read_b64_tr_b16 a[92:93], v11 offset:58880\n" \
"v_mul_f32 v46, s20, v46\n" \
"v_mfma_f32_16x16x32_bf16 v[176:179], a[40:43], a[8:11], v[176:179]\n" \
"s_add_u32 m0, s51, 33664\n" \
"ds_read_b64_tr_b16 a[94:95], v12 offset:58880\n" \
"v_mul_f32 v47, s20, v47\n" \
"v_mfma_f32_16x16x32_bf16 v[180:183], a[44:47], a[8:11], v[180:183]\n" \
"buffer_load_dwordx4 v21, s[32:35], s47 offen offset:128 lds\n" \
"v_mfma_f32_16x16x32_bf16 v[184:187], a[40:43], a[12:15], v[184:187]\n" \
"ds_read_b64_tr_b16 a[64:65], v9 offset:57344\n" \
"v_mul_f32 v48, s20, v48\n" \
"v_mfma_f32_16x16x32_bf16 v[188:191], a[44:47], a[12:15], v[188:191]\n" \
"ds_read_b64_tr_b16 a[66:67], v10 offset:57344\n" \
"v_mul_f32 v49, s20, v49\n" \
"v_mfma_f32_16x16x32_bf16 v[64:67], a[16:19], a[0:3], v[64:67]\n" \
"ds_read_b64_tr_b16 a[68:69], v11 offset:57344\n" \
"v_cvt_pk_bf16_f32 v42, v46, v47\n" \
"v_mfma_f32_16x16x32_bf16 v[68:71], a[20:23], a[0:3], v[68:71]\n" \
"buffer_store_dwordx4 v[34:37], v4, s[36:39], s97 offen offset:0 sc1\n" \
"s_add_u32 s96, s96, s92\n" \
"v_mfma_f32_16x16x32_bf16 v[72:75], a[16:19], a[4:7], v[72:75]\n" \
"ds_read_b64_tr_b16 a[70:71], v12 offset:57344\n" \
"v_cvt_pk_bf16_f32 v43, v48, v49\n" \
"v_mfma_f32_16x16x32_bf16 v[76:79], a[20:23], a[4:7], v[76:79]\n" \
"v_accvgpr_read_b32 v50, a156\n" \
"v_accvgpr_read_b32 v51, a157\n" \
"v_mfma_f32_16x16x32_bf16 v[80:83], a[16:19], a[8:11], v[80:83]\n" \
"v_accvgpr_read_b32 v52, a158\n" \
"v_accvgpr_read_b32 v53, a159\n" \
"v_mfma_f32_16x16x32_bf16 v[84:87], a[20:23], a[8:11], v[84:87]\n" \
"v_mul_f32 v50, s20, v50\n" \
"v_mul_f32 v51, s20, v51\n" \
"v_mfma_f32_16x16x32_bf16 v[88:91], a[16:19], a[12:15], v[88:91]\n" \
"s_waitcnt vmcnt(16) lgkmcnt(0)\n" \
"v_mul_f32 v52, s20, v52\n" \
"v_mfma_f32_16x16x32_bf16 v[92:95], a[20:23], a[12:15], v[92:95]\n" \
"s_barrier\n" \
"v_mfma_f32_16x16x32_bf16 v[96:99], a[72:75], a[48:51], v[96:99]\n" \
"s_mov_b32 s46, 512\n" \
"s_add_u32 m0, s50, 0\n" \
"ds_read_b64_tr_b16 a[24:25], v9 offset:512\n" \
"v_mfma_f32_16x16x32_bf16 v[100:103], a[76:79], a[48:51], v[100:103]\n" \
"buffer_load_dwordx4 v13, s[28:31], s46 offen lds\n" \
"v_mfma_f32_16x16x32_bf16 v[104:107], a[72:75], a[52:55], v[104:107]\n" \
"ds_read_b64_tr_b16 a[26:27], v10 offset:512\n" \
"v_mul_f32 v53, s20, v53\n" \
"v_mfma_f32_16x16x32_bf16 v[108:111], a[76:79], a[52:55], v[108:111]\n" \
"ds_read_b64_tr_b16 a[28:29], v11 offset:512\n" \
"v_cvt_pk_bf16_f32 v44, v50, v51\n" \
"v_mfma_f32_16x16x32_bf16 v[112:115], a[72:75], a[56:59], v[112:115]\n" \
"s_add_u32 m0, s50, 4096\n" \
"ds_read_b64_tr_b16 a[30:31], v12 offset:512\n" \
"v_cvt_pk_bf16_f32 v45, v52, v53\n" \
"v_mfma_f32_16x16x32_bf16 v[116:119], a[76:79], a[56:59], v[116:119]\n" \
"buffer_load_dwordx4 v14, s[28:31], s46 offen lds\n" \
"v_mfma_f32_16x16x32_bf16 v[120:123], a[72:75], a[60:63], v[120:123]\n" \
"ds_read_b128 a[0:3], v5 offset:32768\n" \
"v_accvgpr_read_b32 v46, a160\n" \
"v_mfma_f32_16x16x32_bf16 v[124:127], a[76:79], a[60:63], v[124:127]\n" \
"ds_read_b128 a[4:7], v5 offset:34816\n" \
"v_accvgpr_read_b32 v47, a161\n" \
"v_mfma_f32_16x16x32_bf16 v[128:131], a[80:83], a[48:51], v[128:131]\n" \
"s_add_u32 m0, s50, 8192\n" \
"ds_read_b128 a[8:11], v5 offset:36864\n" \
"v_accvgpr_read_b32 v48, a162\n" \
"v_mfma_f32_16x16x32_bf16 v[132:135], a[84:87], a[48:51], v[132:135]\n" \
"buffer_load_dwordx4 v15, s[28:31], s46 offen lds\n" \
"v_mfma_f32_16x16x32_bf16 v[136:139], a[80:83], a[52:55], v[136:139]\n" \
"ds_read_b128 a[12:15], v5 offset:38912\n" \
"v_accvgpr_read_b32 v49, a163\n" \
"v_mfma_f32_16x16x32_bf16 v[140:143], a[84:87], a[52:55], v[140:143]\n" \
"ds_read_b64_tr_b16 a[32:33], v9 offset:1024\n" \
"v_mul_f32 v46, s20, v46\n" \
"v_mfma_f32_16x16x32_bf16 v[144:147], a[80:83], a[56:59], v[144:147]\n" \
"s_add_u32 m0, s50, 12288\n" \
"ds_read_b64_tr_b16 a[34:35], v10 offset:1024\n" \
"v_mul_f32 v47, s20, v47\n" \
"v_mfma_f32_16x16x32_bf16 v[148:151], a[84:87], a[56:59], v[148:151]\n" \
"buffer_load_dwordx4 v16, s[28:31], s46 offen lds\n" \
"v_mfma_f32_16x16x32_bf16 v[152:155], a[80:83], a[60:63], v[152:155]\n" \
"ds_read_b64_tr_b16 a[36:37], v11 offset:1024\n" \
"v_mul_f32 v48, s20, v48\n" \
"v_mfma_f32_16x16x32_bf16 v[156:159], a[84:87], a[60:63], v[156:159]\n" \
"ds_read_b64_tr_b16 a[38:39], v12 offset:1024\n" \
"v_mul_f32 v49, s20, v49\n" \
"v_mfma_f32_16x16x32_bf16 v[160:163], a[88:91], a[48:51], v[160:163]\n" \
"s_mov_b32 s46, 512\n" \
"s_add_u32 m0, s50, 16384\n" \
"ds_read_b64_tr_b16 a[40:41], v9 offset:1536\n" \
"v_mfma_f32_16x16x32_bf16 v[164:167], a[92:95], a[48:51], v[164:167]\n" \
"buffer_load_dwordx4 v17, s[28:31], s46 offen lds\n" \
"v_mfma_f32_16x16x32_bf16 v[168:171], a[88:91], a[52:55], v[168:171]\n" \
"ds_read_b64_tr_b16 a[42:43], v10 offset:1536\n" \
"v_cvt_pk_bf16_f32 v30, v46, v47\n" \
"v_mfma_f32_16x16x32_bf16 v[172:175], a[92:95], a[52:55], v[172:175]\n" \
"LH10_%=:\ns_cbranch_execz LH14_%=\n" \
"ds_read_b64_tr_b16 a[44:45], v11 offset:1536\n" \
"v_cvt_pk_bf16_f32 v31, v48, v49\n" \
"v_mfma_f32_16x16x32_bf16 v[176:179], a[88:91], a[56:59], v[176:179]\n" \
"s_add_u32 m0, s50, 20480\n" \
"ds_read_b64_tr_b16 a[46:47], v12 offset:1536\n" \
"v_accvgpr_read_b32 v50, a164\n" \
"v_mfma_f32_16x16x32_bf16 v[180:183], a[92:95], a[56:59], v[180:183]\n" \
"buffer_load_dwordx4 v18, s[28:31], s46 offen lds\n" \
"v_mfma_f32_16x16x32_bf16 v[184:187], a[88:91], a[60:63], v[184:187]\n" \
"ds_read_b64_tr_b16 a[16:17], v9 offset:0\n" \
"v_accvgpr_read_b32 v51, a165\n" \
"v_mfma_f32_16x16x32_bf16 v[188:191], a[92:95], a[60:63], v[188:191]\n" \
"ds_read_b64_tr_b16 a[18:19], v10 offset:0\n" \
"v_accvgpr_read_b32 v52, a166\n" \
"v_mfma_f32_16x16x32_bf16 v[64:67], a[64:67], a[48:51], v[64:67]\n" \
"s_add_u32 m0, s50, 24576\n" \
"ds_read_b64_tr_b16 a[20:21], v11 offset:0\n" \
"v_accvgpr_read_b32 v53, a167\n" \
"v_mfma_f32_16x16x32_bf16 v[68:71], a[68:71], a[48:51], v[68:71]\n" \
"buffer_load_dwordx4 v19, s[28:31], s46 offen lds\n" \
"v_mfma_f32_16x16x32_bf16 v[72:75], a[64:67], a[52:55], v[72:75]\n" \
"ds_read_b64_tr_b16 a[22:23], v12 offset:0\n" \
"v_mul_f32 v50, s20, v50\n" \
"v_mfma_f32_16x16x32_bf16 v[76:79], a[68:71], a[52:55], v[76:79]\n" \
"v_mul_f32 v51, s20, v51\n" \
"v_mul_f32 v52, s20, v52\n" \
"v_mfma_f32_16x16x32_bf16 v[80:83], a[64:67], a[56:59], v[80:83]\n" \
"s_add_u32 m0, s50, 28672\n" \
"v_mul_f32 v53, s20, v53\n" \
"v_mfma_f32_16x16x32_bf16 v[84:87], a[68:71], a[56:59], v[84:87]\n" \
"buffer_load_dwordx4 v20, s[28:31], s46 offen lds\n" \
"v_mfma_f32_16x16x32_bf16 v[88:91], a[64:67], a[60:63], v[88:91]\n" \
"v_cvt_pk_bf16_f32 v32, v50, v51\n" \
"v_cvt_pk_bf16_f32 v33, v52, v53\n" \
"v_mfma_f32_16x16x32_bf16 v[92:95], a[68:71], a[60:63], v[92:95]\n" \
"s_waitcnt lgkmcnt(0)\n" \
"v_accvgpr_read_b32 v46, a168\n" \
"v_mfma_f32_16x16x32_bf16 v[96:99], a[24:27], a[0:3], v[96:99]\n" \
"s_mov_b32 s47, 2359296\n" \
"s_add_u32 m0, s51, 40960\n" \
"ds_read_b64_tr_b16 a[72:73], v9 offset:8704\n" \
"v_mfma_f32_16x16x32_bf16 v[100:103], a[28:31], a[0:3], v[100:103]\n" \
"buffer_load_dwordx4 v21, s[32:35], s47 offen offset:0 lds\n" \
"v_mfma_f32_16x16x32_bf16 v[104:107], a[24:27], a[4:7], v[104:107]\n" \
"ds_read_b64_tr_b16 a[74:75], v10 offset:8704\n" \
"v_accvgpr_read_b32 v47, a169\n" \
"v_mfma_f32_16x16x32_bf16 v[108:111], a[28:31], a[4:7], v[108:111]\n" \
"ds_read_b64_tr_b16 a[76:77], v11 offset:8704\n" \
"v_accvgpr_read_b32 v48, a170\n" \
"v_mfma_f32_16x16x32_bf16 v[112:115], a[24:27], a[8:11], v[112:115]\n" \
"s_add_u32 m0, s51, 41856\n" \
"ds_read_b64_tr_b16 a[78:79], v12 offset:8704\n" \
"v_accvgpr_read_b32 v49, a171\n" \
"v_mfma_f32_16x16x32_bf16 v[116:119], a[28:31], a[8:11], v[116:119]\n" \
"buffer_load_dwordx4 v21, s[32:35], s47 offen offset:128 lds\n" \
"v_mfma_f32_16x16x32_bf16 v[120:123], a[24:27], a[12:15], v[120:123]\n" \
"ds_read_b128 a[48:51], v6 offset:32768\n" \
"v_mul_f32 v46, s20, v46\n" \
"v_mfma_f32_16x16x32_bf16 v[124:127], a[28:31], a[12:15], v[124:127]\n" \
"ds_read_b128 a[52:55], v6 offset:34816\n" \
"v_mul_f32 v47, s20, v47\n" \
"v_mfma_f32_16x16x32_bf16 v[128:131], a[32:35], a[0:3], v[128:131]\n" \
"s_mov_b32 s47, 2621440\n" \
"s_add_u32 m0, s51, 49152\n" \
"ds_read_b128 a[56:59], v6 offset:36864\n" \
"v_mfma_f32_16x16x32_bf16 v[132:135], a[36:39], a[0:3], v[132:135]\n" \
"buffer_load_dwordx4 v21, s[32:35], s47 offen offset:0 lds\n" \
"v_mfma_f32_16x16x32_bf16 v[136:139], a[32:35], a[4:7], v[136:139]\n" \
"ds_read_b128 a[60:63], v6 offset:38912\n" \
"v_mul_f32 v48, s20, v48\n" \
"v_mfma_f32_16x16x32_bf16 v[140:143], a[36:39], a[4:7], v[140:143]\n" \
"ds_read_b64_tr_b16 a[80:81], v9 offset:9216\n" \
"v_mul_f32 v49, s20, v49\n" \
"v_mfma_f32_16x16x32_bf16 v[144:147], a[32:35], a[8:11], v[144:147]\n" \
"s_add_u32 m0, s51, 50048\n" \
"ds_read_b64_tr_b16 a[82:83], v10 offset:9216\n" \
"v_cvt_pk_bf16_f32 v34, v46, v47\n" \
"v_mfma_f32_16x16x32_bf16 v[148:151], a[36:39], a[8:11], v[148:151]\n" \
"buffer_load_dwordx4 v21, s[32:35], s47 offen offset:128 lds\n" \
"v_mfma_f32_16x16x32_bf16 v[152:155], a[32:35], a[12:15], v[152:155]\n" \
"ds_read_b64_tr_b16 a[84:85], v11 offset:9216\n" \
"v_cvt_pk_bf16_f32 v35, v48, v49\n" \
"v_mfma_f32_16x16x32_bf16 v[156:159], a[36:39], a[12:15], v[156:159]\n" \
"ds_read_b64_tr_b16 a[86:87], v12 offset:9216\n" \
"v_accvgpr_read_b32 v50, a172\n" \
"v_mfma_f32_16x16x32_bf16 v[160:163], a[40:43], a[0:3], v[160:163]\n" \
"s_mov_b32 s47, 2883584\n" \
"s_add_u32 m0, s51, 57344\n" \
"ds_read_b64_tr_b16 a[88:89], v9 offset:9728\n" \
"v_mfma_f32_16x16x32_bf16 v[164:167], a[44:47], a[0:3], v[164:167]\n" \
"buffer_load_dwordx4 v21, s[32:35], s47 offen offset:0 lds\n" \
"v_mfma_f32_16x16x32_bf16 v[168:171], a[40:43], a[4:7], v[168:171]\n" \
"ds_read_b64_tr_b16 a[90:91], v10 offset:9728\n" \
"v_accvgpr_read_b32 v51, a173\n" \
"v_mfma_f32_16x16x32_bf16 v[172:175], a[44:47], a[4:7], v[172:175]\n" \
"ds_read_b64_tr_b16 a[92:93], v11 offset:9728\n" \
"v_accvgpr_read_b32 v52, a174\n" \
"v_mfma_f32_16x16x32_bf16 v[176:179], a[40:43], a[8:11], v[176:179]\n" \
"s_add_u32 m0, s51, 58240\n" \
"ds_read_b64_tr_b16 a[94:95], v12 offset:9728\n" \
"v_accvgpr_read_b32 v53, a175\n" \
"v_mfma_f32_16x16x32_bf16 v[180:183], a[44:47], a[8:11], v[180:183]\n" \
"buffer_load_dwordx4 v21, s[32:35], s47 offen offset:128 lds\n" \
"v_mfma_f32_16x16x32_bf16 v[184:187], a[40:43], a[12:15], v[184:187]\n" \
"ds_read_b64_tr_b16 a[64:65], v9 offset:8192\n" \
"v_mul_f32 v50, s20, v50\n" \
"v_mfma_f32_16x16x32_bf16 v[188:191], a[44:47], a[12:15], v[188:191]\n" \
"ds_read_b64_tr_b16 a[66:67], v10 offset:8192\n" \
"v_mul_f32 v51, s20, v51\n" \
"v_mfma_f32_16x16x32_bf16 v[64:67], a[16:19], a[0:3], v[64:67]\n" \
"ds_read_b64_tr_b16 a[68:69], v11 offset:8192\n" \
"v_mul_f32 v52, s20, v52\n" \
"v_mfma_f32_16x16x32_bf16 v[68:71], a[20:23], a[0:3], v[68:71]\n" \
"buffer_store_dwordx4 v[38:41], v4, s[36:39], s96 offen offset:0 sc1\n" \
"s_add_u32 s97, s96, s98\n" \
"v_mfma_f32_16x16x32_bf16 v[72:75], a[16:19], a[4:7], v[72:75]\n" \
"ds_read_b64_tr_b16 a[70:71], v12 offset:8192\n" \
"v_mul_f32 v53, s20, v53\n" \
"v_mfma_f32_16x16x32_bf16 v[76:79], a[20:23], a[4:7], v[76:79]\n" \
"v_cvt_pk_bf16_f32 v36, v50, v51\n" \
"v_cvt_pk_bf16_f32 v37, v52, v53\n" \
"v_mfma_f32_16x16x32_bf16 v[80:83], a[16:19], a[8:11], v[80:83]\n" \
"v_accvgpr_read_b32 v46, a176\n" \
"v_accvgpr_read_b32 v47, a177\n" \
"v_mfma_f32_16x16x32_bf16 v[84:87], a[20:23], a[8:11], v[84:87]\n" \
"v_accvgpr_read_b32 v48, a178\n" \
"v_accvgpr_read_b32 v49, a179\n" \
"v_mfma_f32_16x16x32_bf16 v[88:91], a[16:19], a[12:15], v[88:91]\n" \
"s_waitcnt vmcnt(18) lgkmcnt(0)\n" \
"v_mul_f32 v46, s20, v46\n" \
"v_mfma_f32_16x16x32_bf16 v[92:95], a[20:23], a[12:15], v[92:95]\n" \
"s_barrier\n" \
"v_mfma_f32_16x16x32_bf16 v[96:99], a[72:75], a[48:51], v[96:99]\n" \
"s_mov_b32 s46, 640\n" \
"s_add_u32 m0, s50, 32768\n" \
"ds_read_b64_tr_b16 a[24:25], v9 offset:16896\n" \
"v_mfma_f32_16x16x32_bf16 v[100:103], a[76:79], a[48:51], v[100:103]\n" \
"buffer_load_dwordx4 v13, s[28:31], s46 offen lds\n" \
"v_mfma_f32_16x16x32_bf16 v[104:107], a[72:75], a[52:55], v[104:107]\n" \
"ds_read_b64_tr_b16 a[26:27], v10 offset:16896\n" \
"v_mul_f32 v47, s20, v47\n" \
"v_mfma_f32_16x16x32_bf16 v[108:111], a[76:79], a[52:55], v[108:111]\n" \
"ds_read_b64_tr_b16 a[28:29], v11 offset:16896\n" \
"v_mul_f32 v48, s20, v48\n" \
"v_mfma_f32_16x16x32_bf16 v[112:115], a[72:75], a[56:59], v[112:115]\n" \
"s_add_u32 m0, s50, 36864\n" \
"ds_read_b64_tr_b16 a[30:31], v12 offset:16896\n" \
"v_mul_f32 v49, s20, v49\n" \
"v_mfma_f32_16x16x32_bf16 v[116:119], a[76:79], a[56:59], v[116:119]\n" \
"buffer_load_dwordx4 v14, s[28:31], s46 offen lds\n" \
"v_mfma_f32_16x16x32_bf16 v[120:123], a[72:75], a[60:63], v[120:123]\n" \
"ds_read_b128 a[0:3], v7 offset:0\n" \
"v_cvt_pk_bf16_f32 v38, v46, v47\n" \
"v_mfma_f32_16x16x32_bf16 v[124:127], a[76:79], a[60:63], v[124:127]\n" \
"ds_read_b128 a[4:7], v7 offset:2048\n" \
"v_cvt_pk_bf16_f32 v39, v48, v49\n" \
"v_mfma_f32_16x16x32_bf16 v[128:131], a[80:83], a[48:51], v[128:131]\n" \
"s_add_u32 m0, s50, 40960\n" \
"ds_read_b128 a[8:11], v7 offset:4096\n" \
"v_accvgpr_read_b32 v50, a180\n" \
"v_mfma_f32_16x16x32_bf16 v[132:135], a[84:87], a[48:51], v[132:135]\n" \
"buffer_load_dwordx4 v15, s[28:31], s46 offen lds\n" \
"v_mfma_f32_16x16x32_bf16 v[136:139], a[80:83], a[52:55], v[136:139]\n" \
"ds_read_b128 a[12:15], v7 offset:6144\n" \
"v_accvgpr_read_b32 v51, a181\n" \
"v_mfma_f32_16x16x32_bf16 v[140:143], a[84:87], a[52:55], v[140:143]\n" \
"ds_read_b64_tr_b16 a[32:33], v9 offset:17408\n" \
"v_accvgpr_read_b32 v52, a182\n" \
"v_mfma_f32_16x16x32_bf16 v[144:147], a[80:83], a[56:59], v[144:147]\n" \
"s_add_u32 m0, s50, 45056\n" \
"ds_read_b64_tr_b16 a[34:35], v10 offset:17408\n" \
"v_accvgpr_read_b32 v53, a183\n" \
"v_mfma_f32_16x16x32_bf16 v[148:151], a[84:87], a[56:59], v[148:151]\n" \
"buffer_load_dwordx4 v16, s[28:31], s46 offen lds\n" \
"v_mfma_f32_16x16x32_bf16 v[152:155], a[80:83], a[60:63], v[152:155]\n" \
"ds_read_b64_tr_b16 a[36:37], v11 offset:17408\n" \
"v_mul_f32 v50, s20, v50\n" \
"v_mfma_f32_16x16x32_bf16 v[156:159], a[84:87], a[60:63], v[156:159]\n" \
"ds_read_b64_tr_b16 a[38:39], v12 offset:17408\n" \
"v_mul_f32 v51, s20, v51\n" \
"v_mfma_f32_16x16x32_bf16 v[160:163], a[88:91], a[48:51], v[160:163]\n" \
"s_mov_b32 s46, 640\n" \
"s_add_u32 m0, s50, 49152\n" \
"ds_read_b64_tr_b16 a[40:41], v9 offset:17920\n" \
"v_mfma_f32_16x16x32_bf16 v[164:167], a[92:95], a[48:51], v[164:167]\n" \
"buffer_load_dwordx4 v17, s[28:31], s46 offen lds\n" \
"v_mfma_f32_16x16x32_bf16 v[168:171], a[88:91], a[52:55], v[168:171]\n" \
"ds_read_b64_tr_b16 a[42:43], v10 offset:17920\n" \
"v_mul_f32 v52, s20, v52\n" \
"v_mfma_f32_16x16x32_bf16 v[172:175], a[92:95], a[52:55], v[172:175]\n" \
"ds_read_b64_tr_b16 a[44:45], v11 offset:17920\n" \
"v_mul_f32 v53, s20, v53\n" \
"v_mfma_f32_16x16x32_bf16 v[176:179], a[88:91], a[56:59], v[176:179]\n" \
"s_add_u32 m0, s50, 53248\n" \
"ds_read_b64_tr_b16 a[46:47], v12 offset:17920\n" \
"v_cvt_pk_bf16_f32 v40, v50, v51\n" \
"v_mfma_f32_16x16x32_bf16 v[180:183], a[92:95], a[56:59], v[180:183]\n" \
"buffer_load_dwordx4 v18, s[28:31], s46 offen lds\n" \
"v_mfma_f32_16x16x32_bf16 v[184:187], a[88:91], a[60:63], v[184:187]\n" \
"ds_read_b64_tr_b16 a[16:17], v9 offset:16384\n" \
"v_cvt_pk_bf16_f32 v41, v52, v53\n" \
"v_mfma_f32_16x16x32_bf16 v[188:191], a[92:95], a[60:63], v[188:191]\n" \
"ds_read_b64_tr_b16 a[18:19], v10 offset:16384\n" \
"v_accvgpr_read_b32 v46, a184\n" \
"v_mfma_f32_16x16x32_bf16 v[64:67], a[64:67], a[48:51], v[64:67]\n" \
"s_add_u32 m0, s50, 57344\n" \
"ds_read_b64_tr_b16 a[20:21], v11 offset:16384\n" \
"v_accvgpr_read_b32 v47, a185\n" \
"v_mfma_f32_16x16x32_bf16 v[68:71], a[68:71], a[48:51], v[68:71]\n" \
"buffer_load_dwordx4 v19, s[28:31], s46 offen lds\n" \
"v_mfma_f32_16x16x32_bf16 v[72:75], a[64:67], a[52:55], v[72:75]\n" \
"ds_read_b64_tr_b16 a[22:23], v12 offset:16384\n" \
"v_accvgpr_read_b32 v48, a186\n" \
"v_mfma_f32_16x16x32_bf16 v[76:79], a[68:71], a[52:55], v[76:79]\n" \
"v_accvgpr_read_b32 v49, a187\n" \
"v_mul_f32 v46, s20, v46\n" \
"v_mfma_f32_16x16x32_bf16 v[80:83], a[64:67], a[56:59], v[80:83]\n" \
"s_add_u32 m0, s50, 61440\n" \
"v_mul_f32 v47, s20, v47\n" \
"v_mfma_f32_16x16x32_bf16 v[84:87], a[68:71], a[56:59], v[84:87]\n" \
"buffer_load_dwordx4 v20, s[28:31], s46 offen lds\n" \
"v_mfma_f32_16x16x32_bf16 v[88:91], a[64:67], a[60:63], v[88:91]\n" \
"v_mul_f32 v48, s20, v48\n" \
"v_mul_f32 v49, s20, v49\n" \
"v_mfma_f32_16x16x32_bf16 v[92:95], a[68:71], a[60:63], v[92:95]\n" \
"s_waitcnt lgkmcnt(0)\n" \
"v_add_u32 v60, s63, v4\n" \
"s_lshr_b32 s62, s6, 1\n" \
"v_mfma_f32_16x16x32_bf16 v[96:99], a[24:27], a[0:3], v[96:99]\n" \
"ds_read_b64_tr_b16 a[72:73], v9 offset:25088\n" \
"s_lshl_b32 s62, s62, 7\n" \
"s_add_u32 s62, s62, 94375936\n" \
"v_mfma_f32_16x16x32_bf16 v[100:103], a[28:31], a[0:3], v[100:103]\n" \
"ds_read_b64_tr_b16 a[74:75], v10 offset:25088\n" \
"v_bfe_u32 v57, v4, 4, 4\n" \
"v_mfma_f32_16x16x32_bf16 v[104:107], a[24:27], a[4:7], v[104:107]\n" \
"ds_read_b64_tr_b16 a[76:77], v11 offset:25088\n" \
"v_lshl_add_u32 v57, v57, 3, s62\n" \
"v_mfma_f32_16x16x32_bf16 v[108:111], a[28:31], a[4:7], v[108:111]\n" \
"ds_read_b64_tr_b16 a[78:79], v12 offset:25088\n" \
"s_load_dwordx2 s[100:101], %0, 0x50\n" \
"s_lshr_b32 s64, %1, 3\n" \
"v_mfma_f32_16x16x32_bf16 v[112:115], a[24:27], a[8:11], v[112:115]\n" \
"ds_read_b128 a[48:51], v8 offset:0\n" \
"s_lshl_b32 s64, s64, 2\n" \
"s_add_u32 s64, s64, %2\n" \
"v_mfma_f32_16x16x32_bf16 v[116:119], a[28:31], a[8:11], v[116:119]\n" \
"ds_read_b128 a[52:55], v8 offset:2048\n" \
"s_lshl_b32 s64, s64, 11\n" \
"s_bfe_u32 s65, %1, 0x20001\n" \
"v_mfma_f32_16x16x32_bf16 v[120:123], a[24:27], a[12:15], v[120:123]\n" \
"ds_read_b128 a[56:59], v8 offset:4096\n" \
"s_lshl_b32 s65, s65, 21\n" \
"s_add_u32 s64, s64, s65\n" \
"v_mfma_f32_16x16x32_bf16 v[124:127], a[28:31], a[12:15], v[124:127]\n" \
"ds_read_b128 a[60:63], v8 offset:6144\n" \
"v_mfma_f32_16x16x32_bf16 v[128:131], a[32:35], a[0:3], v[128:131]\n" \
"s_mov_b32 s47, 3145728\n" \
"s_add_u32 m0, s51, 0\n" \
"ds_read_b64_tr_b16 a[80:81], v9 offset:25600\n" \
"v_mfma_f32_16x16x32_bf16 v[132:135], a[36:39], a[0:3], v[132:135]\n" \
"buffer_load_dwordx4 v21, s[32:35], s47 offen offset:0 lds\n" \
"v_mfma_f32_16x16x32_bf16 v[136:139], a[32:35], a[4:7], v[136:139]\n" \
"ds_read_b64_tr_b16 a[82:83], v10 offset:25600\n" \
"v_mfma_f32_16x16x32_bf16 v[140:143], a[36:39], a[4:7], v[140:143]\n" \
"ds_read_b64_tr_b16 a[84:85], v11 offset:25600\n" \
"v_mfma_f32_16x16x32_bf16 v[144:147], a[32:35], a[8:11], v[144:147]\n" \
"s_add_u32 m0, s51, 896\n" \
"ds_read_b64_tr_b16 a[86:87], v12 offset:25600\n" \
"v_mfma_f32_16x16x32_bf16 v[148:151], a[36:39], a[8:11], v[148:151]\n" \
"buffer_load_dwordx4 v21, s[32:35], s47 offen offset:128 lds\n" \
"v_mfma_f32_16x16x32_bf16 v[152:155], a[32:35], a[12:15], v[152:155]\n" \
"ds_read_b64_tr_b16 a[88:89], v9 offset:26112\n" \
"v_mfma_f32_16x16x32_bf16 v[156:159], a[36:39], a[12:15], v[156:159]\n" \
"ds_read_b64_tr_b16 a[90:91], v10 offset:26112\n" \
"v_mfma_f32_16x16x32_bf16 v[160:163], a[40:43], a[0:3], v[160:163]\n" \
"s_mov_b32 s47, 3407872\n" \
"s_add_u32 m0, s51, 8192\n" \
"ds_read_b64_tr_b16 a[92:93], v11 offset:26112\n" \
"v_mfma_f32_16x16x32_bf16 v[164:167], a[44:47], a[0:3], v[164:167]\n" \
"buffer_load_dwordx4 v21, s[32:35], s47 offen offset:0 lds\n" \
"v_mfma_f32_16x16x32_bf16 v[168:171], a[40:43], a[4:7], v[168:171]\n" \
"ds_read_b64_tr_b16 a[94:95], v12 offset:26112\n" \
"v_mfma_f32_16x16x32_bf16 v[172:175], a[44:47], a[4:7], v[172:175]\n" \
"ds_read_b64_tr_b16 a[64:65], v9 offset:24576\n" \
"v_mfma_f32_16x16x32_bf16 v[176:179], a[40:43], a[8:11], v[176:179]\n" \
"s_add_u32 m0, s51, 9088\n" \
"ds_read_b64_tr_b16 a[66:67], v10 offset:24576\n" \
"v_mfma_f32_16x16x32_bf16 v[180:183], a[44:47], a[8:11], v[180:183]\n" \
"buffer_load_dwordx4 v21, s[32:35], s47 offen offset:128 lds\n" \
"v_mfma_f32_16x16x32_bf16 v[184:187], a[40:43], a[12:15], v[184:187]\n" \
"ds_read_b64_tr_b16 a[68:69], v11 offset:24576\n" \
"v_mfma_f32_16x16x32_bf16 v[188:191], a[44:47], a[12:15], v[188:191]\n" \
"ds_read_b64_tr_b16 a[70:71], v12 offset:24576\n" \
"v_mfma_f32_16x16x32_bf16 v[64:67], a[16:19], a[0:3], v[64:67]\n" \
"v_mfma_f32_16x16x32_bf16 v[68:71], a[20:23], a[0:3], v[68:71]\n" \
"buffer_store_dwordx4 v[42:45], v4, s[36:39], s97 offen offset:0 sc1\n" \
"s_add_u32 s96, s96, s92\n" \
"v_mfma_f32_16x16x32_bf16 v[72:75], a[16:19], a[4:7], v[72:75]\n" \
"v_cvt_pk_bf16_f32 v42, v46, v47\n" \
"v_cvt_pk_bf16_f32 v43, v48, v49\n" \
"v_mfma_f32_16x16x32_bf16 v[76:79], a[20:23], a[4:7], v[76:79]\n" \
"v_accvgpr_read_b32 v50, a188\n" \
"v_accvgpr_read_b32 v51, a189\n" \
"v_mfma_f32_16x16x32_bf16 v[80:83], a[16:19], a[8:11], v[80:83]\n" \
"v_accvgpr_read_b32 v52, a190\n" \
"v_accvgpr_read_b32 v53, a191\n" \
"v_mfma_f32_16x16x32_bf16 v[84:87], a[20:23], a[8:11], v[84:87]\n" \
"v_mul_f32 v50, s20, v50\n" \
"v_mul_f32 v51, s20, v51\n" \
"v_mfma_f32_16x16x32_bf16 v[88:91], a[16:19], a[12:15], v[88:91]\n" \
"s_waitcnt vmcnt(18) lgkmcnt(0)\n" \
"v_mul_f32 v52, s20, v52\n" \
"v_mfma_f32_16x16x32_bf16 v[92:95], a[20:23], a[12:15], v[92:95]\n" \
"s_barrier\n" \
"v_mfma_f32_16x16x32_bf16 v[96:99], a[72:75], a[48:51], v[96:99]\n" \
"s_mov_b32 s46, 768\n" \
"s_add_u32 m0, s50, 65536\n" \
"ds_read_b64_tr_b16 a[24:25], v9 offset:33280\n" \
"v_mfma_f32_16x16x32_bf16 v[100:103], a[76:79], a[48:51], v[100:103]\n" \
"buffer_load_dwordx4 v13, s[28:31], s46 offen lds\n" \
"v_mfma_f32_16x16x32_bf16 v[104:107], a[72:75], a[52:55], v[104:107]\n" \
"ds_read_b64_tr_b16 a[26:27], v10 offset:33280\n" \
"v_mul_f32 v53, s20, v53\n" \
"v_mfma_f32_16x16x32_bf16 v[108:111], a[76:79], a[52:55], v[108:111]\n" \
"ds_read_b64_tr_b16 a[28:29], v11 offset:33280\n" \
"v_cvt_pk_bf16_f32 v44, v50, v51\n" \
"v_mfma_f32_16x16x32_bf16 v[112:115], a[72:75], a[56:59], v[112:115]\n" \
"s_add_u32 m0, s50, 69632\n" \
"ds_read_b64_tr_b16 a[30:31], v12 offset:33280\n" \
"v_cvt_pk_bf16_f32 v45, v52, v53\n" \
"v_mfma_f32_16x16x32_bf16 v[116:119], a[76:79], a[56:59], v[116:119]\n" \
"buffer_load_dwordx4 v14, s[28:31], s46 offen lds\n" \
"v_mfma_f32_16x16x32_bf16 v[120:123], a[72:75], a[60:63], v[120:123]\n" \
"ds_read_b128 a[0:3], v5 offset:0\n" \
"v_mfma_f32_16x16x32_bf16 v[124:127], a[76:79], a[60:63], v[124:127]\n" \
"ds_read_b128 a[4:7], v5 offset:2048\n" \
"v_mfma_f32_16x16x32_bf16 v[128:131], a[80:83], a[48:51], v[128:131]\n" \
"s_add_u32 m0, s50, 73728\n" \
"ds_read_b128 a[8:11], v5 offset:4096\n" \
"v_mfma_f32_16x16x32_bf16 v[132:135], a[84:87], a[48:51], v[132:135]\n" \
"buffer_load_dwordx4 v15, s[28:31], s46 offen lds\n" \
"v_mfma_f32_16x16x32_bf16 v[136:139], a[80:83], a[52:55], v[136:139]\n" \
"ds_read_b128 a[12:15], v5 offset:6144\n" \
"v_mfma_f32_16x16x32_bf16 v[140:143], a[84:87], a[52:55], v[140:143]\n" \
"ds_read_b64_tr_b16 a[32:33], v9 offset:33792\n" \
"v_mfma_f32_16x16x32_bf16 v[144:147], a[80:83], a[56:59], v[144:147]\n" \
"s_add_u32 m0, s50, 77824\n" \
"ds_read_b64_tr_b16 a[34:35], v10 offset:33792\n" \
"v_mfma_f32_16x16x32_bf16 v[148:151], a[84:87], a[56:59], v[148:151]\n" \
"buffer_load_dwordx4 v16, s[28:31], s46 offen lds\n" \
"v_mfma_f32_16x16x32_bf16 v[152:155], a[80:83], a[60:63], v[152:155]\n" \
"ds_read_b64_tr_b16 a[36:37], v11 offset:33792\n" \
"v_mfma_f32_16x16x32_bf16 v[156:159], a[84:87], a[60:63], v[156:159]\n" \
"ds_read_b64_tr_b16 a[38:39], v12 offset:33792\n" \
"v_mfma_f32_16x16x32_bf16 v[160:163], a[88:91], a[48:51], v[160:163]\n" \
"s_mov_b32 s46, 768\n" \
"s_add_u32 m0, s50, 81920\n" \
"ds_read_b64_tr_b16 a[40:41], v9 offset:34304\n" \
"v_mfma_f32_16x16x32_bf16 v[164:167], a[92:95], a[48:51], v[164:167]\n" \
"buffer_load_dwordx4 v17, s[28:31], s46 offen lds\n" \
"v_mfma_f32_16x16x32_bf16 v[168:171], a[88:91], a[52:55], v[168:171]\n" \
"ds_read_b64_tr_b16 a[42:43], v10 offset:34304\n" \
"v_mfma_f32_16x16x32_bf16 v[172:175], a[92:95], a[52:55], v[172:175]\n" \
"ds_read_b64_tr_b16 a[44:45], v11 offset:34304\n" \
"v_mfma_f32_16x16x32_bf16 v[176:179], a[88:91], a[56:59], v[176:179]\n" \
"s_add_u32 m0, s50, 86016\n" \
"ds_read_b64_tr_b16 a[46:47], v12 offset:34304\n" \
"v_mfma_f32_16x16x32_bf16 v[180:183], a[92:95], a[56:59], v[180:183]\n" \
"buffer_load_dwordx4 v18, s[28:31], s46 offen lds\n" \
"v_mfma_f32_16x16x32_bf16 v[184:187], a[88:91], a[60:63], v[184:187]\n" \
"ds_read_b64_tr_b16 a[16:17], v9 offset:32768\n" \
"v_mfma_f32_16x16x32_bf16 v[188:191], a[92:95], a[60:63], v[188:191]\n" \
"ds_read_b64_tr_b16 a[18:19], v10 offset:32768\n" \
"v_mfma_f32_16x16x32_bf16 v[64:67], a[64:67], a[48:51], v[64:67]\n" \
"s_add_u32 m0, s50, 90112\n" \
"ds_read_b64_tr_b16 a[20:21], v11 offset:32768\n" \
"v_mfma_f32_16x16x32_bf16 v[68:71], a[68:71], a[48:51], v[68:71]\n" \
"buffer_load_dwordx4 v19, s[28:31], s46 offen lds\n" \
"v_mfma_f32_16x16x32_bf16 v[72:75], a[64:67], a[52:55], v[72:75]\n" \
"ds_read_b64_tr_b16 a[22:23], v12 offset:32768\n" \
"v_mfma_f32_16x16x32_bf16 v[76:79], a[68:71], a[52:55], v[76:79]\n" \
"v_mfma_f32_16x16x32_bf16 v[80:83], a[64:67], a[56:59], v[80:83]\n" \
"s_add_u32 m0, s50, 94208\n" \
"v_mfma_f32_16x16x32_bf16 v[84:87], a[68:71], a[56:59], v[84:87]\n" \
"buffer_load_dwordx4 v20, s[28:31], s46 offen lds\n" \
"v_mfma_f32_16x16x32_bf16 v[88:91], a[64:67], a[60:63], v[88:91]\n" \
"v_mfma_f32_16x16x32_bf16 v[92:95], a[68:71], a[60:63], v[92:95]\n" \
"s_waitcnt lgkmcnt(0)\n" \
"v_mfma_f32_16x16x32_bf16 v[96:99], a[24:27], a[0:3], v[96:99]\n" \
"ds_read_b64_tr_b16 a[72:73], v9 offset:41472\n" \
"s_add_u32 s100, s100, s64\ns_addc_u32 s101, s101, 0\n" \
"v_mfma_f32_16x16x32_bf16 v[100:103], a[28:31], a[0:3], v[100:103]\n" \
"ds_read_b64_tr_b16 a[74:75], v10 offset:41472\n" \
"v_mfma_f32_16x16x32_bf16 v[104:107], a[24:27], a[4:7], v[104:107]\n" \
"ds_read_b64_tr_b16 a[76:77], v11 offset:41472\n" \
"v_mfma_f32_16x16x32_bf16 v[108:111], a[28:31], a[4:7], v[108:111]\n" \
"ds_read_b64_tr_b16 a[78:79], v12 offset:41472\n" \
"v_mfma_f32_16x16x32_bf16 v[112:115], a[24:27], a[8:11], v[112:115]\n" \
"ds_read_b128 a[48:51], v6 offset:0\n" \
"v_mfma_f32_16x16x32_bf16 v[116:119], a[28:31], a[8:11], v[116:119]\n" \
"ds_read_b128 a[52:55], v6 offset:2048\n" \
"v_mfma_f32_16x16x32_bf16 v[120:123], a[24:27], a[12:15], v[120:123]\n" \
"ds_read_b128 a[56:59], v6 offset:4096\n" \
"v_mfma_f32_16x16x32_bf16 v[124:127], a[28:31], a[12:15], v[124:127]\n" \
"ds_read_b128 a[60:63], v6 offset:6144\n" \
"v_mfma_f32_16x16x32_bf16 v[128:131], a[32:35], a[0:3], v[128:131]\n" \
"s_mov_b32 s47, 3670016\n" \
"s_add_u32 m0, s51, 16384\n" \
"ds_read_b64_tr_b16 a[80:81], v9 offset:41984\n" \
"v_mfma_f32_16x16x32_bf16 v[132:135], a[36:39], a[0:3], v[132:135]\n" \
"buffer_load_dwordx4 v21, s[32:35], s47 offen offset:0 lds\n" \
"v_mfma_f32_16x16x32_bf16 v[136:139], a[32:35], a[4:7], v[136:139]\n" \
"ds_read_b64_tr_b16 a[82:83], v10 offset:41984\n" \
"v_mfma_f32_16x16x32_bf16 v[140:143], a[36:39], a[4:7], v[140:143]\n" \
"ds_read_b64_tr_b16 a[84:85], v11 offset:41984\n" \
"v_mfma_f32_16x16x32_bf16 v[144:147], a[32:35], a[8:11], v[144:147]\n" \
"s_add_u32 m0, s51, 17280\n" \
"ds_read_b64_tr_b16 a[86:87], v12 offset:41984\n" \
"v_mfma_f32_16x16x32_bf16 v[148:151], a[36:39], a[8:11], v[148:151]\n" \
"buffer_load_dwordx4 v21, s[32:35], s47 offen offset:128 lds\n" \
"v_mfma_f32_16x16x32_bf16 v[152:155], a[32:35], a[12:15], v[152:155]\n" \
"ds_read_b64_tr_b16 a[88:89], v9 offset:42496\n" \
"v_mfma_f32_16x16x32_bf16 v[156:159], a[36:39], a[12:15], v[156:159]\n" \
"ds_read_b64_tr_b16 a[90:91], v10 offset:42496\n" \
"v_mfma_f32_16x16x32_bf16 v[160:163], a[40:43], a[0:3], v[160:163]\n" \
"s_mov_b32 s47, 3932160\n" \
"s_add_u32 m0, s51, 24576\n" \
"ds_read_b64_tr_b16 a[92:93], v11 offset:42496\n" \
"v_mfma_f32_16x16x32_bf16 v[164:167], a[44:47], a[0:3], v[164:167]\n" \
"buffer_load_dwordx4 v21, s[32:35], s47 offen offset:0 lds\n" \
"v_mfma_f32_16x16x32_bf16 v[168:171], a[40:43], a[4:7], v[168:171]\n" \
"ds_read_b64_tr_b16 a[94:95], v12 offset:42496\n" \
"v_mfma_f32_16x16x32_bf16 v[172:175], a[44:47], a[4:7], v[172:175]\n" \
"ds_read_b64_tr_b16 a[64:65], v9 offset:40960\n" \
"v_mfma_f32_16x16x32_bf16 v[176:179], a[40:43], a[8:11], v[176:179]\n" \
"s_add_u32 m0, s51, 25472\n" \
"ds_read_b64_tr_b16 a[66:67], v10 offset:40960\n" \
"v_mfma_f32_16x16x32_bf16 v[180:183], a[44:47], a[8:11], v[180:183]\n" \
"buffer_load_dwordx4 v21, s[32:35], s47 offen offset:128 lds\n" \
"v_mfma_f32_16x16x32_bf16 v[184:187], a[40:43], a[12:15], v[184:187]\n" \
"ds_read_b64_tr_b16 a[68:69], v11 offset:40960\n" \
"v_mfma_f32_16x16x32_bf16 v[188:191], a[44:47], a[12:15], v[188:191]\n" \
"ds_read_b64_tr_b16 a[70:71], v12 offset:40960\n" \
"v_mfma_f32_16x16x32_bf16 v[64:67], a[16:19], a[0:3], v[64:67]\n" \
"v_mfma_f32_16x16x32_bf16 v[68:71], a[20:23], a[0:3], v[68:71]\n" \
"buffer_store_dwordx4 v[30:33], v4, s[36:39], s96 offen offset:0 sc1\n" \
"s_add_u32 s97, s96, s98\n" \
"v_mfma_f32_16x16x32_bf16 v[72:75], a[16:19], a[4:7], v[72:75]\n" \
"v_mfma_f32_16x16x32_bf16 v[76:79], a[20:23], a[4:7], v[76:79]\n" \
"v_mfma_f32_16x16x32_bf16 v[80:83], a[16:19], a[8:11], v[80:83]\n" \
"v_mfma_f32_16x16x32_bf16 v[84:87], a[20:23], a[8:11], v[84:87]\n" \
"v_mfma_f32_16x16x32_bf16 v[88:91], a[16:19], a[12:15], v[88:91]\n" \
"s_waitcnt vmcnt(18) lgkmcnt(0)\n" \
"v_mfma_f32_16x16x32_bf16 v[92:95], a[20:23], a[12:15], v[92:95]\n" \
"s_barrier\n" \
"v_mfma_f32_16x16x32_bf16 v[96:99], a[72:75], a[48:51], v[96:99]\n" \
"s_mov_b32 s46, 896\n" \
"s_add_u32 m0, s50, 0\n" \
"ds_read_b64_tr_b16 a[24:25], v9 offset:49664\n" \
"v_mfma_f32_16x16x32_bf16 v[100:103], a[76:79], a[48:51], v[100:103]\n" \
"buffer_load_dwordx4 v13, s[28:31], s46 offen lds\n" \
"v_mfma_f32_16x16x32_bf16 v[104:107], a[72:75], a[52:55], v[104:107]\n" \
"ds_read_b64_tr_b16 a[26:27], v10 offset:49664\n" \
"v_mfma_f32_16x16x32_bf16 v[108:111], a[76:79], a[52:55], v[108:111]\n" \
"ds_read_b64_tr_b16 a[28:29], v11 offset:49664\n" \
"v_mfma_f32_16x16x32_bf16 v[112:115], a[72:75], a[56:59], v[112:115]\n" \
"s_add_u32 m0, s50, 4096\n" \
"ds_read_b64_tr_b16 a[30:31], v12 offset:49664\n" \
"v_mfma_f32_16x16x32_bf16 v[116:119], a[76:79], a[56:59], v[116:119]\n" \
"buffer_load_dwordx4 v14, s[28:31], s46 offen lds\n" \
"v_mfma_f32_16x16x32_bf16 v[120:123], a[72:75], a[60:63], v[120:123]\n" \
"ds_read_b128 a[0:3], v5 offset:32768\n" \
"v_mfma_f32_16x16x32_bf16 v[124:127], a[76:79], a[60:63], v[124:127]\n" \
"ds_read_b128 a[4:7], v5 offset:34816\n" \
"v_mfma_f32_16x16x32_bf16 v[128:131], a[80:83], a[48:51], v[128:131]\n" \
"s_add_u32 m0, s50, 8192\n" \
"ds_read_b128 a[8:11], v5 offset:36864\n" \
"v_mfma_f32_16x16x32_bf16 v[132:135], a[84:87], a[48:51], v[132:135]\n" \
"buffer_load_dwordx4 v15, s[28:31], s46 offen lds\n" \
"v_mfma_f32_16x16x32_bf16 v[136:139], a[80:83], a[52:55], v[136:139]\n" \
"ds_read_b128 a[12:15], v5 offset:38912\n" \
"v_mfma_f32_16x16x32_bf16 v[140:143], a[84:87], a[52:55], v[140:143]\n" \
"ds_read_b64_tr_b16 a[32:33], v9 offset:50176\n" \
"v_mfma_f32_16x16x32_bf16 v[144:147], a[80:83], a[56:59], v[144:147]\n" \
"s_add_u32 m0, s50, 12288\n" \
"ds_read_b64_tr_b16 a[34:35], v10 offset:50176\n" \
"v_mfma_f32_16x16x32_bf16 v[148:151], a[84:87], a[56:59], v[148:151]\n" \
"buffer_load_dwordx4 v16, s[28:31], s46 offen lds\n" \
"v_mfma_f32_16x16x32_bf16 v[152:155], a[80:83], a[60:63], v[152:155]\n" \
"ds_read_b64_tr_b16 a[36:37], v11 offset:50176\n" \
"v_mfma_f32_16x16x32_bf16 v[156:159], a[84:87], a[60:63], v[156:159]\n" \
"ds_read_b64_tr_b16 a[38:39], v12 offset:50176\n" \
"v_mfma_f32_16x16x32_bf16 v[160:163], a[88:91], a[48:51], v[160:163]\n" \
"s_mov_b32 s46, 896\n" \
"s_add_u32 m0, s50, 16384\n" \
"ds_read_b64_tr_b16 a[40:41], v9 offset:50688\n" \
"v_mfma_f32_16x16x32_bf16 v[164:167], a[92:95], a[48:51], v[164:167]\n" \
"buffer_load_dwordx4 v17, s[28:31], s46 offen lds\n" \
"v_mfma_f32_16x16x32_bf16 v[168:171], a[88:91], a[52:55], v[168:171]\n" \
"ds_read_b64_tr_b16 a[42:43], v10 offset:50688\n" \
"v_mfma_f32_16x16x32_bf16 v[172:175], a[92:95], a[52:55], v[172:175]\n" \
"ds_read_b64_tr_b16 a[44:45], v11 offset:50688\n" \
"v_mfma_f32_16x16x32_bf16 v[176:179], a[88:91], a[56:59], v[176:179]\n" \
"s_add_u32 m0, s50, 20480\n" \
"ds_read_b64_tr_b16 a[46:47], v12 offset:50688\n" \
"v_mfma_f32_16x16x32_bf16 v[180:183], a[92:95], a[56:59], v[180:183]\n" \
"buffer_load_dwordx4 v18, s[28:31], s46 offen lds\n" \
"v_mfma_f32_16x16x32_bf16 v[184:187], a[88:91], a[60:63], v[184:187]\n" \
"ds_read_b64_tr_b16 a[16:17], v9 offset:49152\n" \
"v_mfma_f32_16x16x32_bf16 v[188:191], a[92:95], a[60:63], v[188:191]\n" \
"ds_read_b64_tr_b16 a[18:19], v10 offset:49152\n" \
"v_mfma_f32_16x16x32_bf16 v[64:67], a[64:67], a[48:51], v[64:67]\n" \
"s_add_u32 m0, s50, 24576\n" \
"ds_read_b64_tr_b16 a[20:21], v11 offset:49152\n" \
"v_mfma_f32_16x16x32_bf16 v[68:71], a[68:71], a[48:51], v[68:71]\n" \
"buffer_load_dwordx4 v19, s[28:31], s46 offen lds\n" \
"v_mfma_f32_16x16x32_bf16 v[72:75], a[64:67], a[52:55], v[72:75]\n" \
"ds_read_b64_tr_b16 a[22:23], v12 offset:49152\n" \
"v_mfma_f32_16x16x32_bf16 v[76:79], a[68:71], a[52:55], v[76:79]\n" \
"v_mfma_f32_16x16x32_bf16 v[80:83], a[64:67], a[56:59], v[80:83]\n" \
"s_add_u32 m0, s50, 28672\n" \
"v_mfma_f32_16x16x32_bf16 v[84:87], a[68:71], a[56:59], v[84:87]\n" \
"buffer_load_dwordx4 v20, s[28:31], s46 offen lds\n" \
"v_mfma_f32_16x16x32_bf16 v[88:91], a[64:67], a[60:63], v[88:91]\n" \
"v_mfma_f32_16x16x32_bf16 v[92:95], a[68:71], a[60:63], v[92:95]\n" \
"s_waitcnt lgkmcnt(0)\n" \
"v_mfma_f32_16x16x32_bf16 v[96:99], a[24:27], a[0:3], v[96:99]\n" \
"ds_read_b64_tr_b16 a[72:73], v9 offset:57856\n" \
"v_mfma_f32_16x16x32_bf16 v[100:103], a[28:31], a[0:3], v[100:103]\n" \
"ds_read_b64_tr_b16 a[74:75], v10 offset:57856\n" \
"v_mfma_f32_16x16x32_bf16 v[104:107], a[24:27], a[4:7], v[104:107]\n" \
"ds_read_b64_tr_b16 a[76:77], v11 offset:57856\n" \
"v_mfma_f32_16x16x32_bf16 v[108:111], a[28:31], a[4:7], v[108:111]\n" \
"ds_read_b64_tr_b16 a[78:79], v12 offset:57856\n" \
"v_mfma_f32_16x16x32_bf16 v[112:115], a[24:27], a[8:11], v[112:115]\n" \
"LH11_%=:\ns_cbranch_execz LHR0_%=\n" \
"ds_read_b128 a[48:51], v6 offset:32768\n" \
"v_mfma_f32_16x16x32_bf16 v[116:119], a[28:31], a[8:11], v[116:119]\n" \
"ds_read_b128 a[52:55], v6 offset:34816\n" \
"v_mfma_f32_16x16x32_bf16 v[120:123], a[24:27], a[12:15], v[120:123]\n" \
"ds_read_b128 a[56:59], v6 offset:36864\n" \
"v_mfma_f32_16x16x32_bf16 v[124:127], a[28:31], a[12:15], v[124:127]\n" \
"ds_read_b128 a[60:63], v6 offset:38912\n" \
"v_mfma_f32_16x16x32_bf16 v[128:131], a[32:35], a[0:3], v[128:131]\n" \
"s_mov_b32 s47, 4194304\n" \
"s_add_u32 m0, s51, 32768\n" \
"ds_read_b64_tr_b16 a[80:81], v9 offset:58368\n" \
"v_mfma_f32_16x16x32_bf16 v[132:135], a[36:39], a[0:3], v[132:135]\n" \
"buffer_load_dwordx4 v21, s[32:35], s47 offen offset:0 lds\n" \
"v_mfma_f32_16x16x32_bf16 v[136:139], a[32:35], a[4:7], v[136:139]\n" \
"ds_read_b64_tr_b16 a[82:83], v10 offset:58368\n" \
"v_mfma_f32_16x16x32_bf16 v[140:143], a[36:39], a[4:7], v[140:143]\n" \
"ds_read_b64_tr_b16 a[84:85], v11 offset:58368\n" \
"v_mfma_f32_16x16x32_bf16 v[144:147], a[32:35], a[8:11], v[144:147]\n" \
"s_add_u32 m0, s51, 33664\n" \
"ds_read_b64_tr_b16 a[86:87], v12 offset:58368\n" \
"v_mfma_f32_16x16x32_bf16 v[148:151], a[36:39], a[8:11], v[148:151]\n" \
"buffer_load_dwordx4 v21, s[32:35], s47 offen offset:128 lds\n" \
"v_mfma_f32_16x16x32_bf16 v[152:155], a[32:35], a[12:15], v[152:155]\n" \
"ds_read_b64_tr_b16 a[88:89], v9 offset:58880\n" \
"v_mfma_f32_16x16x32_bf16 v[156:159], a[36:39], a[12:15], v[156:159]\n" \
"ds_read_b64_tr_b16 a[90:91], v10 offset:58880\n" \
"v_mfma_f32_16x16x32_bf16 v[160:163], a[40:43], a[0:3], v[160:163]\n" \
"s_mov_b32 s47, 4456448\n" \
"s_add_u32 m0, s51, 40960\n" \
"ds_read_b64_tr_b16 a[92:93], v11 offset:58880\n" \
"v_mfma_f32_16x16x32_bf16 v[164:167], a[44:47], a[0:3], v[164:167]\n" \
"buffer_load_dwordx4 v21, s[32:35], s47 offen offset:0 lds\n" \
"v_mfma_f32_16x16x32_bf16 v[168:171], a[40:43], a[4:7], v[168:171]\n" \
"ds_read_b64_tr_b16 a[94:95], v12 offset:58880\n" \
"v_mfma_f32_16x16x32_bf16 v[172:175], a[44:47], a[4:7], v[172:175]\n" \
"ds_read_b64_tr_b16 a[64:65], v9 offset:57344\n" \
"v_mfma_f32_16x16x32_bf16 v[176:179], a[40:43], a[8:11], v[176:179]\n" \
"s_add_u32 m0, s51, 41856\n" \
"ds_read_b64_tr_b16 a[66:67], v10 offset:57344\n" \
"v_mfma_f32_16x16x32_bf16 v[180:183], a[44:47], a[8:11], v[180:183]\n" \
"buffer_load_dwordx4 v21, s[32:35], s47 offen offset:128 lds\n" \
"v_mfma_f32_16x16x32_bf16 v[184:187], a[40:43], a[12:15], v[184:187]\n" \
"ds_read_b64_tr_b16 a[68:69], v11 offset:57344\n" \
"v_mfma_f32_16x16x32_bf16 v[188:191], a[44:47], a[12:15], v[188:191]\n" \
"ds_read_b64_tr_b16 a[70:71], v12 offset:57344\n" \
"v_mfma_f32_16x16x32_bf16 v[64:67], a[16:19], a[0:3], v[64:67]\n" \
"v_mfma_f32_16x16x32_bf16 v[68:71], a[20:23], a[0:3], v[68:71]\n" \
"buffer_store_dwordx4 v[34:37], v4, s[36:39], s97 offen offset:0 sc1\n" \
"s_add_u32 s96, s96, s92\n" \
"v_mfma_f32_16x16x32_bf16 v[72:75], a[16:19], a[4:7], v[72:75]\n" \
"v_mfma_f32_16x16x32_bf16 v[76:79], a[20:23], a[4:7], v[76:79]\n" \
"v_mfma_f32_16x16x32_bf16 v[80:83], a[16:19], a[8:11], v[80:83]\n" \
"v_mfma_f32_16x16x32_bf16 v[84:87], a[20:23], a[8:11], v[84:87]\n" \
"v_mfma_f32_16x16x32_bf16 v[88:91], a[16:19], a[12:15], v[88:91]\n" \
"s_waitcnt vmcnt(18) lgkmcnt(0)\n" \
"v_mfma_f32_16x16x32_bf16 v[92:95], a[20:23], a[12:15], v[92:95]\n" \
"s_barrier\n" \
"v_mfma_f32_16x16x32_bf16 v[96:99], a[72:75], a[48:51], v[96:99]\n" \
"s_mov_b32 s46, 1024\n" \
"s_add_u32 m0, s50, 32768\n" \
"ds_read_b64_tr_b16 a[24:25], v9 offset:512\n" \
"v_mfma_f32_16x16x32_bf16 v[100:103], a[76:79], a[48:51], v[100:103]\n" \
"buffer_load_dwordx4 v13, s[28:31], s46 offen lds\n" \
"v_mfma_f32_16x16x32_bf16 v[104:107], a[72:75], a[52:55], v[104:107]\n" \
"ds_read_b64_tr_b16 a[26:27], v10 offset:512\n" \
"v_mfma_f32_16x16x32_bf16 v[108:111], a[76:79], a[52:55], v[108:111]\n" \
"ds_read_b64_tr_b16 a[28:29], v11 offset:512\n" \
"v_mfma_f32_16x16x32_bf16 v[112:115], a[72:75], a[56:59], v[112:115]\n" \
"s_add_u32 m0, s50, 36864\n" \
"ds_read_b64_tr_b16 a[30:31], v12 offset:512\n" \
"v_mfma_f32_16x16x32_bf16 v[116:119], a[76:79], a[56:59], v[116:119]\n" \
"buffer_load_dwordx4 v14, s[28:31], s46 offen lds\n" \
"v_mfma_f32_16x16x32_bf16 v[120:123], a[72:75], a[60:63], v[120:123]\n" \
"ds_read_b128 a[0:3], v7 offset:0\n" \
"v_mfma_f32_16x16x32_bf16 v[124:127], a[76:79], a[60:63], v[124:127]\n" \
"ds_read_b128 a[4:7], v7 offset:2048\n" \
"v_mfma_f32_16x16x32_bf16 v[128:131], a[80:83], a[48:51], v[128:131]\n" \
"s_add_u32 m0, s50, 40960\n" \
"ds_read_b128 a[8:11], v7 offset:4096\n" \
"v_mfma_f32_16x16x32_bf16 v[132:135], a[84:87], a[48:51], v[132:135]\n" \
"buffer_load_dwordx4 v15, s[28:31], s46 offen lds\n" \
"v_mfma_f32_16x16x32_bf16 v[136:139], a[80:83], a[52:55], v[136:139]\n" \
"ds_read_b128 a[12:15], v7 offset:6144\n" \
"v_mfma_f32_16x16x32_bf16 v[140:143], a[84:87], a[52:55], v[140:143]\n" \
"ds_read_b64_tr_b16 a[32:33], v9 offset:1024\n" \
"v_mfma_f32_16x16x32_bf16 v[144:147], a[80:83], a[56:59], v[144:147]\n" \
"s_add_u32 m0, s50, 45056\n" \
"ds_read_b64_tr_b16 a[34:35], v10 offset:1024\n" \
"v_mfma_f32_16x16x32_bf16 v[148:151], a[84:87], a[56:59], v[148:151]\n" \
"buffer_load_dwordx4 v16, s[28:31], s46 offen lds\n" \
"v_mfma_f32_16x16x32_bf16 v[152:155], a[80:83], a[60:63], v[152:155]\n" \
"ds_read_b64_tr_b16 a[36:37], v11 offset:1024\n" \
"v_mfma_f32_16x16x32_bf16 v[156:159], a[84:87], a[60:63], v[156:159]\n" \
"ds_read_b64_tr_b16 a[38:39], v12 offset:1024\n" \
"v_mfma_f32_16x16x32_bf16 v[160:163], a[88:91], a[48:51], v[160:163]\n" \
"s_mov_b32 s46, 1024\n" \
"s_add_u32 m0, s50, 49152\n" \
"ds_read_b64_tr_b16 a[40:41], v9 offset:1536\n" \
"v_mfma_f32_16x16x32_bf16 v[164:167], a[92:95], a[48:51], v[164:167]\n" \
"buffer_load_dwordx4 v17, s[28:31], s46 offen lds\n" \
"v_mfma_f32_16x16x32_bf16 v[168:171], a[88:91], a[52:55], v[168:171]\n" \
"ds_read_b64_tr_b16 a[42:43], v10 offset:1536\n" \
"v_mfma_f32_16x16x32_bf16 v[172:175], a[92:95], a[52:55], v[172:175]\n" \
"ds_read_b64_tr_b16 a[44:45], v11 offset:1536\n" \
"v_mfma_f32_16x16x32_bf16 v[176:179], a[88:91], a[56:59], v[176:179]\n" \
"s_add_u32 m0, s50, 53248\n" \
"ds_read_b64_tr_b16 a[46:47], v12 offset:1536\n" \
"v_mfma_f32_16x16x32_bf16 v[180:183], a[92:95], a[56:59], v[180:183]\n" \
"buffer_load_dwordx4 v18, s[28:31], s46 offen lds\n" \
"v_mfma_f32_16x16x32_bf16 v[184:187], a[88:91], a[60:63], v[184:187]\n" \
"ds_read_b64_tr_b16 a[16:17], v9 offset:0\n" \
"v_mfma_f32_16x16x32_bf16 v[188:191], a[92:95], a[60:63], v[188:191]\n" \
"ds_read_b64_tr_b16 a[18:19], v10 offset:0\n" \
"v_mfma_f32_16x16x32_bf16 v[64:67], a[64:67], a[48:51], v[64:67]\n" \
"s_add_u32 m0, s50, 57344\n" \
"ds_read_b64_tr_b16 a[20:21], v11 offset:0\n" \
"v_mfma_f32_16x16x32_bf16 v[68:71], a[68:71], a[48:51], v[68:71]\n" \
"buffer_load_dwordx4 v19, s[28:31], s46 offen lds\n" \
"v_mfma_f32_16x16x32_bf16 v[72:75], a[64:67], a[52:55], v[72:75]\n" \
"ds_read_b64_tr_b16 a[22:23], v12 offset:0\n" \
"v_mfma_f32_16x16x32_bf16 v[76:79], a[68:71], a[52:55], v[76:79]\n" \
"v_mfma_f32_16x16x32_bf16 v[80:83], a[64:67], a[56:59], v[80:83]\n" \
"s_add_u32 m0, s50, 61440\n" \
"v_mfma_f32_16x16x32_bf16 v[84:87], a[68:71], a[56:59], v[84:87]\n" \
"buffer_load_dwordx4 v20, s[28:31], s46 offen lds\n" \
"v_mfma_f32_16x16x32_bf16 v[88:91], a[64:67], a[60:63], v[88:91]\n" \
"v_mfma_f32_16x16x32_bf16 v[92:95], a[68:71], a[60:63], v[92:95]\n" \
"s_waitcnt lgkmcnt(0)\n" \
"v_mfma_f32_16x16x32_bf16 v[96:99], a[24:27], a[0:3], v[96:99]\n" \
"ds_read_b64_tr_b16 a[72:73], v9 offset:8704\n" \
"v_mfma_f32_16x16x32_bf16 v[100:103], a[28:31], a[0:3], v[100:103]\n" \
"ds_read_b64_tr_b16 a[74:75], v10 offset:8704\n" \
"v_mfma_f32_16x16x32_bf16 v[104:107], a[24:27], a[4:7], v[104:107]\n" \
"ds_read_b64_tr_b16 a[76:77], v11 offset:8704\n" \
"v_mfma_f32_16x16x32_bf16 v[108:111], a[28:31], a[4:7], v[108:111]\n" \
"ds_read_b64_tr_b16 a[78:79], v12 offset:8704\n" \
"v_mfma_f32_16x16x32_bf16 v[112:115], a[24:27], a[8:11], v[112:115]\n" \
"ds_read_b128 a[48:51], v8 offset:0\n" \
"v_mfma_f32_16x16x32_bf16 v[116:119], a[28:31], a[8:11], v[116:119]\n" \
"ds_read_b128 a[52:55], v8 offset:2048\n" \
"v_mfma_f32_16x16x32_bf16 v[120:123], a[24:27], a[12:15], v[120:123]\n" \
"ds_read_b128 a[56:59], v8 offset:4096\n" \
"v_mfma_f32_16x16x32_bf16 v[124:127], a[28:31], a[12:15], v[124:127]\n" \
"ds_read_b128 a[60:63], v8 offset:6144\n" \
"v_mfma_f32_16x16x32_bf16 v[128:131], a[32:35], a[0:3], v[128:131]\n" \
"s_mov_b32 s47, 4718592\n" \
"s_add_u32 m0, s51, 49152\n" \
"ds_read_b64_tr_b16 a[80:81], v9 offset:9216\n" \
"v_mfma_f32_16x16x32_bf16 v[132:135], a[36:39], a[0:3], v[132:135]\n" \
"buffer_load_dwordx4 v21, s[32:35], s47 offen offset:0 lds\n" \
"v_mfma_f32_16x16x32_bf16 v[136:139], a[32:35], a[4:7], v[136:139]\n" \
"ds_read_b64_tr_b16 a[82:83], v10 offset:9216\n" \
"v_mfma_f32_16x16x32_bf16 v[140:143], a[36:39], a[4:7], v[140:143]\n" \
"ds_read_b64_tr_b16 a[84:85], v11 offset:9216\n" \
"v_mfma_f32_16x16x32_bf16 v[144:147], a[32:35], a[8:11], v[144:147]\n" \
"s_add_u32 m0, s51, 50048\n" \
"ds_read_b64_tr_b16 a[86:87], v12 offset:9216\n" \
"v_mfma_f32_16x16x32_bf16 v[148:151], a[36:39], a[8:11], v[148:151]\n" \
"buffer_load_dwordx4 v21, s[32:35], s47 offen offset:128 lds\n" \
"v_mfma_f32_16x16x32_bf16 v[152:155], a[32:35], a[12:15], v[152:155]\n" \
"ds_read_b64_tr_b16 a[88:89], v9 offset:9728\n" \
"v_mfma_f32_16x16x32_bf16 v[156:159], a[36:39], a[12:15], v[156:159]\n" \
"ds_read_b64_tr_b16 a[90:91], v10 offset:9728\n" \
"v_mfma_f32_16x16x32_bf16 v[160:163], a[40:43], a[0:3], v[160:163]\n" \
"s_mov_b32 s47, 4980736\n" \
"s_add_u32 m0, s51, 57344\n" \
"ds_read_b64_tr_b16 a[92:93], v11 offset:9728\n" \
"v_mfma_f32_16x16x32_bf16 v[164:167], a[44:47], a[0:3], v[164:167]\n" \
"buffer_load_dwordx4 v21, s[32:35], s47 offen offset:0 lds\n" \
"v_mfma_f32_16x16x32_bf16 v[168:171], a[40:43], a[4:7], v[168:171]\n" \
"ds_read_b64_tr_b16 a[94:95], v12 offset:9728\n" \
"v_mfma_f32_16x16x32_bf16 v[172:175], a[44:47], a[4:7], v[172:175]\n" \
"ds_read_b64_tr_b16 a[64:65], v9 offset:8192\n" \
"v_mfma_f32_16x16x32_bf16 v[176:179], a[40:43], a[8:11], v[176:179]\n" \
"s_add_u32 m0, s51, 58240\n" \
"ds_read_b64_tr_b16 a[66:67], v10 offset:8192\n" \
"v_mfma_f32_16x16x32_bf16 v[180:183], a[44:47], a[8:11], v[180:183]\n" \
"buffer_load_dwordx4 v21, s[32:35], s47 offen offset:128 lds\n" \
"v_mfma_f32_16x16x32_bf16 v[184:187], a[40:43], a[12:15], v[184:187]\n" \
"ds_read_b64_tr_b16 a[68:69], v11 offset:8192\n" \
"v_mfma_f32_16x16x32_bf16 v[188:191], a[44:47], a[12:15], v[188:191]\n" \
"ds_read_b64_tr_b16 a[70:71], v12 offset:8192\n" \
"v_mfma_f32_16x16x32_bf16 v[64:67], a[16:19], a[0:3], v[64:67]\n" \
"v_mfma_f32_16x16x32_bf16 v[68:71], a[20:23], a[0:3], v[68:71]\n" \
"buffer_store_dwordx4 v[38:41], v4, s[36:39], s96 offen offset:0 sc1\n" \
"s_add_u32 s97, s96, s98\n" \
"v_mfma_f32_16x16x32_bf16 v[72:75], a[16:19], a[4:7], v[72:75]\n" \
"v_mfma_f32_16x16x32_bf16 v[76:79], a[20:23], a[4:7], v[76:79]\n" \
"v_mfma_f32_16x16x32_bf16 v[80:83], a[16:19], a[8:11], v[80:83]\n" \
"v_mfma_f32_16x16x32_bf16 v[84:87], a[20:23], a[8:11], v[84:87]\n" \
"v_mfma_f32_16x16x32_bf16 v[88:91], a[16:19], a[12:15], v[88:91]\n" \
"s_waitcnt vmcnt(18) lgkmcnt(0)\n" \
"v_mfma_f32_16x16x32_bf16 v[92:95], a[20:23], a[12:15], v[92:95]\n" \
"s_barrier\n" \
"v_mfma_f32_16x16x32_bf16 v[96:99], a[72:75], a[48:51], v[96:99]\n" \
"s_mov_b32 s46, 1152\n" \
"s_add_u32 m0, s50, 65536\n" \
"ds_read_b64_tr_b16 a[24:25], v9 offset:16896\n" \
"v_mfma_f32_16x16x32_bf16 v[100:103], a[76:79], a[48:51], v[100:103]\n" \
"buffer_load_dwordx4 v13, s[28:31], s46 offen lds\n" \
"v_mfma_f32_16x16x32_bf16 v[104:107], a[72:75], a[52:55], v[104:107]\n" \
"ds_read_b64_tr_b16 a[26:27], v10 offset:16896\n" \
"v_mfma_f32_16x16x32_bf16 v[108:111], a[76:79], a[52:55], v[108:111]\n" \
"ds_read_b64_tr_b16 a[28:29], v11 offset:16896\n" \
"v_mfma_f32_16x16x32_bf16 v[112:115], a[72:75], a[56:59], v[112:115]\n" \
"s_add_u32 m0, s50, 69632\n" \
"ds_read_b64_tr_b16 a[30:31], v12 offset:16896\n" \
"v_mfma_f32_16x16x32_bf16 v[116:119], a[76:79], a[56:59], v[116:119]\n" \
"buffer_load_dwordx4 v14, s[28:31], s46 offen lds\n" \
"v_mfma_f32_16x16x32_bf16 v[120:123], a[72:75], a[60:63], v[120:123]\n" \
"ds_read_b128 a[0:3], v5 offset:0\n" \
"v_mfma_f32_16x16x32_bf16 v[124:127], a[76:79], a[60:63], v[124:127]\n" \
"ds_read_b128 a[4:7], v5 offset:2048\n" \
"v_mfma_f32_16x16x32_bf16 v[128:131], a[80:83], a[48:51], v[128:131]\n" \
"s_add_u32 m0, s50, 73728\n" \
"ds_read_b128 a[8:11], v5 offset:4096\n" \
"v_mfma_f32_16x16x32_bf16 v[132:135], a[84:87], a[48:51], v[132:135]\n" \
"buffer_load_dwordx4 v15, s[28:31], s46 offen lds\n" \
"v_mfma_f32_16x16x32_bf16 v[136:139], a[80:83], a[52:55], v[136:139]\n" \
"ds_read_b128 a[12:15], v5 offset:6144\n" \
"v_mfma_f32_16x16x32_bf16 v[140:143], a[84:87], a[52:55], v[140:143]\n" \
"ds_read_b64_tr_b16 a[32:33], v9 offset:17408\n" \
"v_mfma_f32_16x16x32_bf16 v[144:147], a[80:83], a[56:59], v[144:147]\n" \
"s_add_u32 m0, s50, 77824\n" \
"ds_read_b64_tr_b16 a[34:35], v10 offset:17408\n" \
"v_mfma_f32_16x16x32_bf16 v[148:151], a[84:87], a[56:59], v[148:151]\n" \
"buffer_load_dwordx4 v16, s[28:31], s46 offen lds\n" \
"v_mfma_f32_16x16x32_bf16 v[152:155], a[80:83], a[60:63], v[152:155]\n" \
"ds_read_b64_tr_b16 a[36:37], v11 offset:17408\n" \
"v_mfma_f32_16x16x32_bf16 v[156:159], a[84:87], a[60:63], v[156:159]\n" \
"ds_read_b64_tr_b16 a[38:39], v12 offset:17408\n" \
"v_mfma_f32_16x16x32_bf16 v[160:163], a[88:91], a[48:51], v[160:163]\n" \
"s_mov_b32 s46, 1152\n" \
"s_add_u32 m0, s50, 81920\n" \
"ds_read_b64_tr_b16 a[40:41], v9 offset:17920\n" \
"v_mfma_f32_16x16x32_bf16 v[164:167], a[92:95], a[48:51], v[164:167]\n" \
"buffer_load_dwordx4 v17, s[28:31], s46 offen lds\n" \
"v_mfma_f32_16x16x32_bf16 v[168:171], a[88:91], a[52:55], v[168:171]\n" \
"ds_read_b64_tr_b16 a[42:43], v10 offset:17920\n" \
"v_mfma_f32_16x16x32_bf16 v[172:175], a[92:95], a[52:55], v[172:175]\n" \
"ds_read_b64_tr_b16 a[44:45], v11 offset:17920\n" \
"v_mfma_f32_16x16x32_bf16 v[176:179], a[88:91], a[56:59], v[176:179]\n" \
"s_add_u32 m0, s50, 86016\n" \
"ds_read_b64_tr_b16 a[46:47], v12 offset:17920\n" \
"v_mfma_f32_16x16x32_bf16 v[180:183], a[92:95], a[56:59], v[180:183]\n" \
"buffer_load_dwordx4 v18, s[28:31], s46 offen lds\n" \
"v_mfma_f32_16x16x32_bf16 v[184:187], a[88:91], a[60:63], v[184:187]\n" \
"ds_read_b64_tr_b16 a[16:17], v9 offset:16384\n" \
"v_mfma_f32_16x16x32_bf16 v[188:191], a[92:95], a[60:63], v[188:191]\n" \
"ds_read_b64_tr_b16 a[18:19], v10 offset:16384\n" \
"v_mfma_f32_16x16x32_bf16 v[64:67], a[64:67], a[48:51], v[64:67]\n" \
"s_add_u32 m0, s50, 90112\n" \
"ds_read_b64_tr_b16 a[20:21], v11 offset:16384\n" \
"v_mfma_f32_16x16x32_bf16 v[68:71], a[68:71], a[48:51], v[68:71]\n" \
"buffer_load_dwordx4 v19, s[28:31], s46 offen lds\n" \
"v_mfma_f32_16x16x32_bf16 v[72:75], a[64:67], a[52:55], v[72:75]\n" \
"ds_read_b64_tr_b16 a[22:23], v12 offset:16384\n" \
"v_mfma_f32_16x16x32_bf16 v[76:79], a[68:71], a[52:55], v[76:79]\n" \
"v_mfma_f32_16x16x32_bf16 v[80:83], a[64:67], a[56:59], v[80:83]\n" \
"s_add_u32 m0, s50, 94208\n" \
"v_mfma_f32_16x16x32_bf16 v[84:87], a[68:71], a[56:59], v[84:87]\n" \
"buffer_load_dwordx4 v20, s[28:31], s46 offen lds\n" \
"v_mfma_f32_16x16x32_bf16 v[88:91], a[64:67], a[60:63], v[88:91]\n" \
"v_mfma_f32_16x16x32_bf16 v[92:95], a[68:71], a[60:63], v[92:95]\n" \
"s_waitcnt lgkmcnt(0)\n" \
"v_mfma_f32_16x16x32_bf16 v[96:99], a[24:27], a[0:3], v[96:99]\n" \
"ds_read_b64_tr_b16 a[72:73], v9 offset:25088\n" \
"v_mfma_f32_16x16x32_bf16 v[100:103], a[28:31], a[0:3], v[100:103]\n" \
"ds_read_b64_tr_b16 a[74:75], v10 offset:25088\n" \
"v_mfma_f32_16x16x32_bf16 v[104:107], a[24:27], a[4:7], v[104:107]\n" \
"ds_read_b64_tr_b16 a[76:77], v11 offset:25088\n" \
"v_mfma_f32_16x16x32_bf16 v[108:111], a[28:31], a[4:7], v[108:111]\n" \
"ds_read_b64_tr_b16 a[78:79], v12 offset:25088\n" \
"v_mfma_f32_16x16x32_bf16 v[112:115], a[24:27], a[8:11], v[112:115]\n" \
"ds_read_b128 a[48:51], v6 offset:0\n" \
"v_mfma_f32_16x16x32_bf16 v[116:119], a[28:31], a[8:11], v[116:119]\n" \
"ds_read_b128 a[52:55], v6 offset:2048\n" \
"v_mfma_f32_16x16x32_bf16 v[120:123], a[24:27], a[12:15], v[120:123]\n" \
"ds_read_b128 a[56:59], v6 offset:4096\n" \
"v_mfma_f32_16x16x32_bf16 v[124:127], a[28:31], a[12:15], v[124:127]\n" \
"ds_read_b128 a[60:63], v6 offset:6144\n" \
"v_mfma_f32_16x16x32_bf16 v[128:131], a[32:35], a[0:3], v[128:131]\n" \
"s_mov_b32 s47, 5242880\n" \
"s_add_u32 m0, s51, 0\n" \
"ds_read_b64_tr_b16 a[80:81], v9 offset:25600\n" \
"v_mfma_f32_16x16x32_bf16 v[132:135], a[36:39], a[0:3], v[132:135]\n" \
"buffer_load_dwordx4 v21, s[32:35], s47 offen offset:0 lds\n" \
"v_mfma_f32_16x16x32_bf16 v[136:139], a[32:35], a[4:7], v[136:139]\n" \
"ds_read_b64_tr_b16 a[82:83], v10 offset:25600\n" \
"v_mfma_f32_16x16x32_bf16 v[140:143], a[36:39], a[4:7], v[140:143]\n" \
"ds_read_b64_tr_b16 a[84:85], v11 offset:25600\n" \
"v_mfma_f32_16x16x32_bf16 v[144:147], a[32:35], a[8:11], v[144:147]\n" \
"s_add_u32 m0, s51, 896\n" \
"ds_read_b64_tr_b16 a[86:87], v12 offset:25600\n" \
"v_mfma_f32_16x16x32_bf16 v[148:151], a[36:39], a[8:11], v[148:151]\n" \
"buffer_load_dwordx4 v21, s[32:35], s47 offen offset:128 lds\n" \
"v_mfma_f32_16x16x32_bf16 v[152:155], a[32:35], a[12:15], v[152:155]\n" \
"ds_read_b64_tr_b16 a[88:89], v9 offset:26112\n" \
"v_mfma_f32_16x16x32_bf16 v[156:159], a[36:39], a[12:15], v[156:159]\n" \
"ds_read_b64_tr_b16 a[90:91], v10 offset:26112\n" \
"v_mfma_f32_16x16x32_bf16 v[160:163], a[40:43], a[0:3], v[160:163]\n" \
"s_mov_b32 s47, 5505024\n" \
"s_add_u32 m0, s51, 8192\n" \
"ds_read_b64_tr_b16 a[92:93], v11 offset:26112\n" \
"v_mfma_f32_16x16x32_bf16 v[164:167], a[44:47], a[0:3], v[164:167]\n" \
"buffer_load_dwordx4 v21, s[32:35], s47 offen offset:0 lds\n" \
"v_mfma_f32_16x16x32_bf16 v[168:171], a[40:43], a[4:7], v[168:171]\n" \
"ds_read_b64_tr_b16 a[94:95], v12 offset:26112\n" \
"v_mfma_f32_16x16x32_bf16 v[172:175], a[44:47], a[4:7], v[172:175]\n" \
"ds_read_b64_tr_b16 a[64:65], v9 offset:24576\n" \
"v_mfma_f32_16x16x32_bf16 v[176:179], a[40:43], a[8:11], v[176:179]\n" \
"s_add_u32 m0, s51, 9088\n" \
"ds_read_b64_tr_b16 a[66:67], v10 offset:24576\n" \
"v_mfma_f32_16x16x32_bf16 v[180:183], a[44:47], a[8:11], v[180:183]\n" \
"buffer_load_dwordx4 v21, s[32:35], s47 offen offset:128 lds\n" \
"v_mfma_f32_16x16x32_bf16 v[184:187], a[40:43], a[12:15], v[184:187]\n" \
"ds_read_b64_tr_b16 a[68:69], v11 offset:24576\n" \
"v_mfma_f32_16x16x32_bf16 v[188:191], a[44:47], a[12:15], v[188:191]\n" \
"ds_read_b64_tr_b16 a[70:71], v12 offset:24576\n" \
"v_mfma_f32_16x16x32_bf16 v[64:67], a[16:19], a[0:3], v[64:67]\n" \
"v_mfma_f32_16x16x32_bf16 v[68:71], a[20:23], a[0:3], v[68:71]\n" \
"buffer_store_dwordx4 v[42:45], v4, s[36:39], s97 offen offset:0 sc1\n" \
"v_mfma_f32_16x16x32_bf16 v[72:75], a[16:19], a[4:7], v[72:75]\n" \
"v_mfma_f32_16x16x32_bf16 v[76:79], a[20:23], a[4:7], v[76:79]\n" \
"v_mfma_f32_16x16x32_bf16 v[80:83], a[16:19], a[8:11], v[80:83]\n" \
"v_mfma_f32_16x16x32_bf16 v[84:87], a[20:23], a[8:11], v[84:87]\n" \
"v_mfma_f32_16x16x32_bf16 v[88:91], a[16:19], a[12:15], v[88:91]\n" \
"s_waitcnt vmcnt(18) lgkmcnt(0)\n" \
"v_mfma_f32_16x16x32_bf16 v[92:95], a[20:23], a[12:15], v[92:95]\n" \
"s_barrier\n" \
"v_mfma_f32_16x16x32_bf16 v[96:99], a[72:75], a[48:51], v[96:99]\n" \
"s_mov_b32 s46, 1280\n" \
"s_add_u32 m0, s50, 0\n" \
"ds_read_b64_tr_b16 a[24:25], v9 offset:33280\n" \
"v_mfma_f32_16x16x32_bf16 v[100:103], a[76:79], a[48:51], v[100:103]\n" \
"buffer_load_dwordx4 v13, s[28:31], s46 offen lds\n" \
"v_mfma_f32_16x16x32_bf16 v[104:107], a[72:75], a[52:55], v[104:107]\n" \
"ds_read_b64_tr_b16 a[26:27], v10 offset:33280\n" \
"v_mfma_f32_16x16x32_bf16 v[108:111], a[76:79], a[52:55], v[108:111]\n" \
"ds_read_b64_tr_b16 a[28:29], v11 offset:33280\n" \
"v_mfma_f32_16x16x32_bf16 v[112:115], a[72:75], a[56:59], v[112:115]\n" \
"s_add_u32 m0, s50, 4096\n" \
"ds_read_b64_tr_b16 a[30:31], v12 offset:33280\n" \
"v_mfma_f32_16x16x32_bf16 v[116:119], a[76:79], a[56:59], v[116:119]\n" \
"buffer_load_dwordx4 v14, s[28:31], s46 offen lds\n" \
"v_mfma_f32_16x16x32_bf16 v[120:123], a[72:75], a[60:63], v[120:123]\n" \
"ds_read_b128 a[0:3], v5 offset:32768\n" \
"v_mfma_f32_16x16x32_bf16 v[124:127], a[76:79], a[60:63], v[124:127]\n" \
"ds_read_b128 a[4:7], v5 offset:34816\n" \
"v_mfma_f32_16x16x32_bf16 v[128:131], a[80:83], a[48:51], v[128:131]\n" \
"s_add_u32 m0, s50, 8192\n" \
"ds_read_b128 a[8:11], v5 offset:36864\n" \
"v_mfma_f32_16x16x32_bf16 v[132:135], a[84:87], a[48:51], v[132:135]\n" \
"buffer_load_dwordx4 v15, s[28:31], s46 offen lds\n" \
"v_mfma_f32_16x16x32_bf16 v[136:139], a[80:83], a[52:55], v[136:139]\n" \
"ds_read_b128 a[12:15], v5 offset:38912\n" \
"v_mfma_f32_16x16x32_bf16 v[140:143], a[84:87], a[52:55], v[140:143]\n" \
"ds_read_b64_tr_b16 a[32:33], v9 offset:33792\n" \
"v_mfma_f32_16x16x32_bf16 v[144:147], a[80:83], a[56:59], v[144:147]\n" \
"s_add_u32 m0, s50, 12288\n" \
"ds_read_b64_tr_b16 a[34:35], v10 offset:33792\n" \
"v_mfma_f32_16x16x32_bf16 v[148:151], a[84:87], a[56:59], v[148:151]\n" \
"buffer_load_dwordx4 v16, s[28:31], s46 offen lds\n" \
"v_mfma_f32_16x16x32_bf16 v[152:155], a[80:83], a[60:63], v[152:155]\n" \
"ds_read_b64_tr_b16 a[36:37], v11 offset:33792\n" \
"v_mfma_f32_16x16x32_bf16 v[156:159], a[84:87], a[60:63], v[156:159]\n" \
"ds_read_b64_tr_b16 a[38:39], v12 offset:33792\n" \
"v_mfma_f32_16x16x32_bf16 v[160:163], a[88:91], a[48:51], v[160:163]\n" \
"s_mov_b32 s46, 1280\n" \
"s_add_u32 m0, s50, 16384\n" \
"ds_read_b64_tr_b16 a[40:41], v9 offset:34304\n" \
"v_mfma_f32_16x16x32_bf16 v[164:167], a[92:95], a[48:51], v[164:167]\n" \
"buffer_load_dwordx4 v17, s[28:31], s46 offen lds\n" \
"v_mfma_f32_16x16x32_bf16 v[168:171], a[88:91], a[52:55], v[168:171]\n" \
"ds_read_b64_tr_b16 a[42:43], v10 offset:34304\n" \
"v_mfma_f32_16x16x32_bf16 v[172:175], a[92:95], a[52:55], v[172:175]\n" \
"ds_read_b64_tr_b16 a[44:45], v11 offset:34304\n" \
"v_mfma_f32_16x16x32_bf16 v[176:179], a[88:91], a[56:59], v[176:179]\n" \
"s_add_u32 m0, s50, 20480\n" \
"ds_read_b64_tr_b16 a[46:47], v12 offset:34304\n" \
"v_mfma_f32_16x16x32_bf16 v[180:183], a[92:95], a[56:59], v[180:183]\n" \
"buffer_load_dwordx4 v18, s[28:31], s46 offen lds\n" \
"v_mfma_f32_16x16x32_bf16 v[184:187], a[88:91], a[60:63], v[184:187]\n" \
"ds_read_b64_tr_b16 a[16:17], v9 offset:32768\n" \
"v_mfma_f32_16x16x32_bf16 v[188:191], a[92:95], a[60:63], v[188:191]\n" \
"ds_read_b64_tr_b16 a[18:19], v10 offset:32768\n" \
"v_mfma_f32_16x16x32_bf16 v[64:67], a[64:67], a[48:51], v[64:67]\n" \
"s_add_u32 m0, s50, 24576\n" \
"ds_read_b64_tr_b16 a[20:21], v11 offset:32768\n" \
"v_mfma_f32_16x16x32_bf16 v[68:71], a[68:71], a[48:51], v[68:71]\n" \
"buffer_load_dwordx4 v19, s[28:31], s46 offen lds\n" \
"v_mfma_f32_16x16x32_bf16 v[72:75], a[64:67], a[52:55], v[72:75]\n" \
"ds_read_b64_tr_b16 a[22:23], v12 offset:32768\n" \
"v_mfma_f32_16x16x32_bf16 v[76:79], a[68:71], a[52:55], v[76:79]\n" \
"v_mfma_f32_16x16x32_bf16 v[80:83], a[64:67], a[56:59], v[80:83]\n" \
"s_add_u32 m0, s50, 28672\n" \
"v_mfma_f32_16x16x32_bf16 v[84:87], a[68:71], a[56:59], v[84:87]\n" \
"buffer_load_dwordx4 v20, s[28:31], s46 offen lds\n" \
"v_mfma_f32_16x16x32_bf16 v[88:91], a[64:67], a[60:63], v[88:91]\n" \
"v_mfma_f32_16x16x32_bf16 v[92:95], a[68:71], a[60:63], v[92:95]\n" \
"s_waitcnt lgkmcnt(0)\n" \
"v_mfma_f32_16x16x32_bf16 v[96:99], a[24:27], a[0:3], v[96:99]\n" \
"ds_read_b64_tr_b16 a[72:73], v9 offset:41472\n" \
"v_mfma_f32_16x16x32_bf16 v[100:103], a[28:31], a[0:3], v[100:103]\n" \
"ds_read_b64_tr_b16 a[74:75], v10 offset:41472\n" \
"v_mfma_f32_16x16x32_bf16 v[104:107], a[24:27], a[4:7], v[104:107]\n" \
"ds_read_b64_tr_b16 a[76:77], v11 offset:41472\n" \
"v_mfma_f32_16x16x32_bf16 v[108:111], a[28:31], a[4:7], v[108:111]\n" \
"ds_read_b64_tr_b16 a[78:79], v12 offset:41472\n" \
"v_mfma_f32_16x16x32_bf16 v[112:115], a[24:27], a[8:11], v[112:115]\n" \
"ds_read_b128 a[48:51], v6 offset:32768\n" \
"v_mfma_f32_16x16x32_bf16 v[116:119], a[28:31], a[8:11], v[116:119]\n" \
"ds_read_b128 a[52:55], v6 offset:34816\n" \
"v_mfma_f32_16x16x32_bf16 v[120:123], a[24:27], a[12:15], v[120:123]\n" \
"ds_read_b128 a[56:59], v6 offset:36864\n" \
"v_mfma_f32_16x16x32_bf16 v[124:127], a[28:31], a[12:15], v[124:127]\n" \
"ds_read_b128 a[60:63], v6 offset:38912\n" \
"v_mfma_f32_16x16x32_bf16 v[128:131], a[32:35], a[0:3], v[128:131]\n" \
"s_mov_b32 s47, 5767168\n" \
"s_add_u32 m0, s51, 16384\n" \
"ds_read_b64_tr_b16 a[80:81], v9 offset:41984\n" \
"v_mfma_f32_16x16x32_bf16 v[132:135], a[36:39], a[0:3], v[132:135]\n" \
"buffer_load_dwordx4 v21, s[32:35], s47 offen offset:0 lds\n" \
"v_mfma_f32_16x16x32_bf16 v[136:139], a[32:35], a[4:7], v[136:139]\n" \
"ds_read_b64_tr_b16 a[82:83], v10 offset:41984\n" \
"v_mfma_f32_16x16x32_bf16 v[140:143], a[36:39], a[4:7], v[140:143]\n" \
"ds_read_b64_tr_b16 a[84:85], v11 offset:41984\n" \
"v_mfma_f32_16x16x32_bf16 v[144:147], a[32:35], a[8:11], v[144:147]\n" \
"s_add_u32 m0, s51, 17280\n" \
"ds_read_b64_tr_b16 a[86:87], v12 offset:41984\n" \
"v_mfma_f32_16x16x32_bf16 v[148:151], a[36:39], a[8:11], v[148:151]\n" \
"buffer_load_dwordx4 v21, s[32:35], s47 offen offset:128 lds\n" \
"v_mfma_f32_16x16x32_bf16 v[152:155], a[32:35], a[12:15], v[152:155]\n" \
"ds_read_b64_tr_b16 a[88:89], v9 offset:42496\n" \
"v_mfma_f32_16x16x32_bf16 v[156:159], a[36:39], a[12:15], v[156:159]\n" \
"ds_read_b64_tr_b16 a[90:91], v10 offset:42496\n" \
"v_mfma_f32_16x16x32_bf16 v[160:163], a[40:43], a[0:3], v[160:163]\n" \
"s_mov_b32 s47, 6029312\n" \
"s_add_u32 m0, s51, 24576\n" \
"ds_read_b64_tr_b16 a[92:93], v11 offset:42496\n" \
"v_mfma_f32_16x16x32_bf16 v[164:167], a[44:47], a[0:3], v[164:167]\n" \
"buffer_load_dwordx4 v21, s[32:35], s47 offen offset:0 lds\n" \
"v_mfma_f32_16x16x32_bf16 v[168:171], a[40:43], a[4:7], v[168:171]\n" \
"ds_read_b64_tr_b16 a[94:95], v12 offset:42496\n" \
"v_mfma_f32_16x16x32_bf16 v[172:175], a[44:47], a[4:7], v[172:175]\n" \
"ds_read_b64_tr_b16 a[64:65], v9 offset:40960\n" \
"v_mfma_f32_16x16x32_bf16 v[176:179], a[40:43], a[8:11], v[176:179]\n" \
"s_add_u32 m0, s51, 25472\n" \
"ds_read_b64_tr_b16 a[66:67], v10 offset:40960\n" \
"v_mfma_f32_16x16x32_bf16 v[180:183], a[44:47], a[8:11], v[180:183]\n" \
"buffer_load_dwordx4 v21, s[32:35], s47 offen offset:128 lds\n" \
"v_mfma_f32_16x16x32_bf16 v[184:187], a[40:43], a[12:15], v[184:187]\n" \
"ds_read_b64_tr_b16 a[68:69], v11 offset:40960\n" \
"v_mfma_f32_16x16x32_bf16 v[188:191], a[44:47], a[12:15], v[188:191]\n" \
"ds_read_b64_tr_b16 a[70:71], v12 offset:40960\n" \
"v_mfma_f32_16x16x32_bf16 v[64:67], a[16:19], a[0:3], v[64:67]\n" \
"v_mfma_f32_16x16x32_bf16 v[68:71], a[20:23], a[0:3], v[68:71]\n" \
"v_mfma_f32_16x16x32_bf16 v[72:75], a[16:19], a[4:7], v[72:75]\n" \
"v_mfma_f32_16x16x32_bf16 v[76:79], a[20:23], a[4:7], v[76:79]\n" \
"v_mfma_f32_16x16x32_bf16 v[80:83], a[16:19], a[8:11], v[80:83]\n" \
"v_mfma_f32_16x16x32_bf16 v[84:87], a[20:23], a[8:11], v[84:87]\n" \
"v_mfma_f32_16x16x32_bf16 v[88:91], a[16:19], a[12:15], v[88:91]\n" \
"s_waitcnt vmcnt(17) lgkmcnt(0)\n" \
"v_mfma_f32_16x16x32_bf16 v[92:95], a[20:23], a[12:15], v[92:95]\n" \
"s_barrier\n" \
"v_mfma_f32_16x16x32_bf16 v[96:99], a[72:75], a[48:51], v[96:99]\n" \
"s_mov_b32 s46, 1408\n" \
"s_add_u32 m0, s50, 32768\n" \
"ds_read_b64_tr_b16 a[24:25], v9 offset:49664\n" \
"v_mfma_f32_16x16x32_bf16 v[100:103], a[76:79], a[48:51], v[100:103]\n" \
"buffer_load_dwordx4 v13, s[28:31], s46 offen lds\n" \
"v_mfma_f32_16x16x32_bf16 v[104:107], a[72:75], a[52:55], v[104:107]\n" \
"ds_read_b64_tr_b16 a[26:27], v10 offset:49664\n" \
"v_mfma_f32_16x16x32_bf16 v[108:111], a[76:79], a[52:55], v[108:111]\n" \
"ds_read_b64_tr_b16 a[28:29], v11 offset:49664\n" \
"v_mfma_f32_16x16x32_bf16 v[112:115], a[72:75], a[56:59], v[112:115]\n" \
"s_add_u32 m0, s50, 36864\n" \
"ds_read_b64_tr_b16 a[30:31], v12 offset:49664\n" \
"v_mfma_f32_16x16x32_bf16 v[116:119], a[76:79], a[56:59], v[116:119]\n" \
"buffer_load_dwordx4 v14, s[28:31], s46 offen lds\n" \
"v_mfma_f32_16x16x32_bf16 v[120:123], a[72:75], a[60:63], v[120:123]\n" \
"ds_read_b128 a[0:3], v7 offset:0\n" \
"v_mfma_f32_16x16x32_bf16 v[124:127], a[76:79], a[60:63], v[124:127]\n" \
"ds_read_b128 a[4:7], v7 offset:2048\n" \
"v_mfma_f32_16x16x32_bf16 v[128:131], a[80:83], a[48:51], v[128:131]\n" \
"s_add_u32 m0, s50, 40960\n" \
"ds_read_b128 a[8:11], v7 offset:4096\n" \
"v_mfma_f32_16x16x32_bf16 v[132:135], a[84:87], a[48:51], v[132:135]\n" \
"buffer_load_dwordx4 v15, s[28:31], s46 offen lds\n" \
"v_mfma_f32_16x16x32_bf16 v[136:139], a[80:83], a[52:55], v[136:139]\n" \
"ds_read_b128 a[12:15], v7 offset:6144\n" \
"v_mfma_f32_16x16x32_bf16 v[140:143], a[84:87], a[52:55], v[140:143]\n" \
"ds_read_b64_tr_b16 a[32:33], v9 offset:50176\n" \
"v_mfma_f32_16x16x32_bf16 v[144:147], a[80:83], a[56:59], v[144:147]\n" \
"s_add_u32 m0, s50, 45056\n" \
"ds_read_b64_tr_b16 a[34:35], v10 offset:50176\n" \
"v_mfma_f32_16x16x32_bf16 v[148:151], a[84:87], a[56:59], v[148:151]\n" \
"buffer_load_dwordx4 v16, s[28:31], s46 offen lds\n" \
"v_mfma_f32_16x16x32_bf16 v[152:155], a[80:83], a[60:63], v[152:155]\n" \
"ds_read_b64_tr_b16 a[36:37], v11 offset:50176\n" \
"v_mfma_f32_16x16x32_bf16 v[156:159], a[84:87], a[60:63], v[156:159]\n" \
"ds_read_b64_tr_b16 a[38:39], v12 offset:50176\n" \
"v_mfma_f32_16x16x32_bf16 v[160:163], a[88:91], a[48:51], v[160:163]\n" \
"s_mov_b32 s46, 1408\n" \
"s_add_u32 m0, s50, 49152\n" \
"ds_read_b64_tr_b16 a[40:41], v9 offset:50688\n" \
"v_mfma_f32_16x16x32_bf16 v[164:167], a[92:95], a[48:51], v[164:167]\n" \
"buffer_load_dwordx4 v17, s[28:31], s46 offen lds\n" \
"v_mfma_f32_16x16x32_bf16 v[168:171], a[88:91], a[52:55], v[168:171]\n" \
"ds_read_b64_tr_b16 a[42:43], v10 offset:50688\n" \
"v_mfma_f32_16x16x32_bf16 v[172:175], a[92:95], a[52:55], v[172:175]\n" \
"ds_read_b64_tr_b16 a[44:45], v11 offset:50688\n" \
"v_mfma_f32_16x16x32_bf16 v[176:179], a[88:91], a[56:59], v[176:179]\n" \
"s_add_u32 m0, s50, 53248\n" \
"ds_read_b64_tr_b16 a[46:47], v12 offset:50688\n" \
"v_mfma_f32_16x16x32_bf16 v[180:183], a[92:95], a[56:59], v[180:183]\n" \
"buffer_load_dwordx4 v18, s[28:31], s46 offen lds\n" \
"v_mfma_f32_16x16x32_bf16 v[184:187], a[88:91], a[60:63], v[184:187]\n" \
"LH12_%=:\ns_cbranch_execz LHR0_%=\n" \
"ds_read_b64_tr_b16 a[16:17], v9 offset:49152\n" \
"v_mfma_f32_16x16x32_bf16 v[188:191], a[92:95], a[60:63], v[188:191]\n" \
"ds_read_b64_tr_b16 a[18:19], v10 offset:49152\n" \
"v_mfma_f32_16x16x32_bf16 v[64:67], a[64:67], a[48:51], v[64:67]\n" \
"s_add_u32 m0, s50, 57344\n" \
"ds_read_b64_tr_b16 a[20:21], v11 offset:49152\n" \
"v_mfma_f32_16x16x32_bf16 v[68:71], a[68:71], a[48:51], v[68:71]\n" \
"buffer_load_dwordx4 v19, s[28:31], s46 offen lds\n" \
"v_mfma_f32_16x16x32_bf16 v[72:75], a[64:67], a[52:55], v[72:75]\n" \
"ds_read_b64_tr_b16 a[22:23], v12 offset:49152\n" \
"v_mfma_f32_16x16x32_bf16 v[76:79], a[68:71], a[52:55], v[76:79]\n" \
"v_mfma_f32_16x16x32_bf16 v[80:83], a[64:67], a[56:59], v[80:83]\n" \
"s_add_u32 m0, s50, 61440\n" \
"v_mfma_f32_16x16x32_bf16 v[84:87], a[68:71], a[56:59], v[84:87]\n" \
"buffer_load_dwordx4 v20, s[28:31], s46 offen lds\n" \
"v_mfma_f32_16x16x32_bf16 v[88:91], a[64:67], a[60:63], v[88:91]\n" \
"v_mfma_f32_16x16x32_bf16 v[92:95], a[68:71], a[60:63], v[92:95]\n" \
"s_waitcnt lgkmcnt(0)\n" \
"v_mfma_f32_16x16x32_bf16 v[96:99], a[24:27], a[0:3], v[96:99]\n" \
"ds_read_b64_tr_b16 a[72:73], v9 offset:57856\n" \
"v_mfma_f32_16x16x32_bf16 v[100:103], a[28:31], a[0:3], v[100:103]\n" \
"ds_read_b64_tr_b16 a[74:75], v10 offset:57856\n" \
"v_mfma_f32_16x16x32_bf16 v[104:107], a[24:27], a[4:7], v[104:107]\n" \
"ds_read_b64_tr_b16 a[76:77], v11 offset:57856\n" \
"v_mfma_f32_16x16x32_bf16 v[108:111], a[28:31], a[4:7], v[108:111]\n" \
"ds_read_b64_tr_b16 a[78:79], v12 offset:57856\n" \
"v_mfma_f32_16x16x32_bf16 v[112:115], a[24:27], a[8:11], v[112:115]\n" \
"ds_read_b128 a[48:51], v8 offset:0\n" \
"v_mfma_f32_16x16x32_bf16 v[116:119], a[28:31], a[8:11], v[116:119]\n" \
"ds_read_b128 a[52:55], v8 offset:2048\n" \
"v_mfma_f32_16x16x32_bf16 v[120:123], a[24:27], a[12:15], v[120:123]\n" \
"ds_read_b128 a[56:59], v8 offset:4096\n" \
"v_mfma_f32_16x16x32_bf16 v[124:127], a[28:31], a[12:15], v[124:127]\n" \
"ds_read_b128 a[60:63], v8 offset:6144\n" \
"v_mfma_f32_16x16x32_bf16 v[128:131], a[32:35], a[0:3], v[128:131]\n" \
"s_mov_b32 s47, 6291456\n" \
"s_add_u32 m0, s51, 32768\n" \
"ds_read_b64_tr_b16 a[80:81], v9 offset:58368\n" \
"v_mfma_f32_16x16x32_bf16 v[132:135], a[36:39], a[0:3], v[132:135]\n" \
"buffer_load_dwordx4 v21, s[32:35], s47 offen offset:0 lds\n" \
"v_mfma_f32_16x16x32_bf16 v[136:139], a[32:35], a[4:7], v[136:139]\n" \
"ds_read_b64_tr_b16 a[82:83], v10 offset:58368\n" \
"v_mfma_f32_16x16x32_bf16 v[140:143], a[36:39], a[4:7], v[140:143]\n" \
"ds_read_b64_tr_b16 a[84:85], v11 offset:58368\n" \
"v_mfma_f32_16x16x32_bf16 v[144:147], a[32:35], a[8:11], v[144:147]\n" \
"s_add_u32 m0, s51, 33664\n" \
"ds_read_b64_tr_b16 a[86:87], v12 offset:58368\n" \
"v_mfma_f32_16x16x32_bf16 v[148:151], a[36:39], a[8:11], v[148:151]\n" \
"buffer_load_dwordx4 v21, s[32:35], s47 offen offset:128 lds\n" \
"v_mfma_f32_16x16x32_bf16 v[152:155], a[32:35], a[12:15], v[152:155]\n" \
"ds_read_b64_tr_b16 a[88:89], v9 offset:58880\n" \
"v_mfma_f32_16x16x32_bf16 v[156:159], a[36:39], a[12:15], v[156:159]\n" \
"ds_read_b64_tr_b16 a[90:91], v10 offset:58880\n" \
"v_mfma_f32_16x16x32_bf16 v[160:163], a[40:43], a[0:3], v[160:163]\n" \
"s_mov_b32 s47, 6553600\n" \
"s_add_u32 m0, s51, 40960\n" \
"ds_read_b64_tr_b16 a[92:93], v11 offset:58880\n" \
"v_mfma_f32_16x16x32_bf16 v[164:167], a[44:47], a[0:3], v[164:167]\n" \
"buffer_load_dwordx4 v21, s[32:35], s47 offen offset:0 lds\n" \
"v_mfma_f32_16x16x32_bf16 v[168:171], a[40:43], a[4:7], v[168:171]\n" \
"ds_read_b64_tr_b16 a[94:95], v12 offset:58880\n" \
"v_mfma_f32_16x16x32_bf16 v[172:175], a[44:47], a[4:7], v[172:175]\n" \
"ds_read_b64_tr_b16 a[64:65], v9 offset:57344\n" \
"v_mfma_f32_16x16x32_bf16 v[176:179], a[40:43], a[8:11], v[176:179]\n" \
"s_add_u32 m0, s51, 41856\n" \
"ds_read_b64_tr_b16 a[66:67], v10 offset:57344\n" \
"v_mfma_f32_16x16x32_bf16 v[180:183], a[44:47], a[8:11], v[180:183]\n" \
"buffer_load_dwordx4 v21, s[32:35], s47 offen offset:128 lds\n" \
"v_mfma_f32_16x16x32_bf16 v[184:187], a[40:43], a[12:15], v[184:187]\n" \
"ds_read_b64_tr_b16 a[68:69], v11 offset:57344\n" \
"v_mfma_f32_16x16x32_bf16 v[188:191], a[44:47], a[12:15], v[188:191]\n" \
"ds_read_b64_tr_b16 a[70:71], v12 offset:57344\n" \
"v_mfma_f32_16x16x32_bf16 v[64:67], a[16:19], a[0:3], v[64:67]\n" \
"v_mfma_f32_16x16x32_bf16 v[68:71], a[20:23], a[0:3], v[68:71]\n" \
"s_mov_b64 exec, 0xffff\nglobal_load_dword v1, v2, s[100:101]\ns_mov_b64 exec, -1\n" \
"v_mfma_f32_16x16x32_bf16 v[72:75], a[16:19], a[4:7], v[72:75]\n" \
"v_mfma_f32_16x16x32_bf16 v[76:79], a[20:23], a[4:7], v[76:79]\n" \
"v_mfma_f32_16x16x32_bf16 v[80:83], a[16:19], a[8:11], v[80:83]\n" \
"v_mfma_f32_16x16x32_bf16 v[84:87], a[20:23], a[8:11], v[84:87]\n" \
"v_mfma_f32_16x16x32_bf16 v[88:91], a[16:19], a[12:15], v[88:91]\n" \
"s_waitcnt vmcnt(17) lgkmcnt(0)\n" \
"v_mfma_f32_16x16x32_bf16 v[92:95], a[20:23], a[12:15], v[92:95]\n" \
"s_barrier\n" \
"v_mfma_f32_16x16x32_bf16 v[96:99], a[72:75], a[48:51], v[96:99]\n" \
"s_mov_b32 s46, 1536\n" \
"s_add_u32 m0, s50, 65536\n" \
"ds_read_b64_tr_b16 a[24:25], v9 offset:512\n" \
"v_mfma_f32_16x16x32_bf16 v[100:103], a[76:79], a[48:51], v[100:103]\n" \
"buffer_load_dwordx4 v13, s[28:31], s46 offen lds\n" \
"v_mfma_f32_16x16x32_bf16 v[104:107], a[72:75], a[52:55], v[104:107]\n" \
"ds_read_b64_tr_b16 a[26:27], v10 offset:512\n" \
"v_mov_b32 v58, s14\n" \
"v_mfma_f32_16x16x32_bf16 v[108:111], a[76:79], a[52:55], v[108:111]\n" \
"ds_read_b64_tr_b16 a[28:29], v11 offset:512\n" \
"v_mov_b32 v59, s15\n" \
"v_mfma_f32_16x16x32_bf16 v[112:115], a[72:75], a[56:59], v[112:115]\n" \
"s_add_u32 m0, s50, 69632\n" \
"ds_read_b64_tr_b16 a[30:31], v12 offset:512\n" \
"v_mfma_f32_16x16x32_bf16 v[116:119], a[76:79], a[56:59], v[116:119]\n" \
"buffer_load_dwordx4 v14, s[28:31], s46 offen lds\n" \
"v_mfma_f32_16x16x32_bf16 v[120:123], a[72:75], a[60:63], v[120:123]\n" \
"ds_read_b128 a[0:3], v5 offset:0\n" \
"v_mfma_f32_16x16x32_bf16 v[124:127], a[76:79], a[60:63], v[124:127]\n" \
"ds_read_b128 a[4:7], v5 offset:2048\n" \
"v_mfma_f32_16x16x32_bf16 v[128:131], a[80:83], a[48:51], v[128:131]\n" \
"s_add_u32 m0, s50, 73728\n" \
"ds_read_b128 a[8:11], v5 offset:4096\n" \
"v_mfma_f32_16x16x32_bf16 v[132:135], a[84:87], a[48:51], v[132:135]\n" \
"buffer_load_dwordx4 v15, s[28:31], s46 offen lds\n" \
"v_mfma_f32_16x16x32_bf16 v[136:139], a[80:83], a[52:55], v[136:139]\n" \
"ds_read_b128 a[12:15], v5 offset:6144\n" \
"v_mfma_f32_16x16x32_bf16 v[140:143], a[84:87], a[52:55], v[140:143]\n" \
"ds_read_b64_tr_b16 a[32:33], v9 offset:1024\n" \
"v_mfma_f32_16x16x32_bf16 v[144:147], a[80:83], a[56:59], v[144:147]\n" \
"s_add_u32 m0, s50, 77824\n" \
"ds_read_b64_tr_b16 a[34:35], v10 offset:1024\n" \
"v_mfma_f32_16x16x32_bf16 v[148:151], a[84:87], a[56:59], v[148:151]\n" \
"buffer_load_dwordx4 v16, s[28:31], s46 offen lds\n" \
"v_mfma_f32_16x16x32_bf16 v[152:155], a[80:83], a[60:63], v[152:155]\n" \
"ds_read_b64_tr_b16 a[36:37], v11 offset:1024\n" \
"v_mfma_f32_16x16x32_bf16 v[156:159], a[84:87], a[60:63], v[156:159]\n" \
"ds_read_b64_tr_b16 a[38:39], v12 offset:1024\n" \
"v_mfma_f32_16x16x32_bf16 v[160:163], a[88:91], a[48:51], v[160:163]\n" \
"s_mov_b32 s46, 1536\n" \
"s_add_u32 m0, s50, 81920\n" \
"ds_read_b64_tr_b16 a[40:41], v9 offset:1536\n" \
"v_mfma_f32_16x16x32_bf16 v[164:167], a[92:95], a[48:51], v[164:167]\n" \
"buffer_load_dwordx4 v17, s[28:31], s46 offen lds\n" \
"v_mfma_f32_16x16x32_bf16 v[168:171], a[88:91], a[52:55], v[168:171]\n" \
"ds_read_b64_tr_b16 a[42:43], v10 offset:1536\n" \
"v_mfma_f32_16x16x32_bf16 v[172:175], a[92:95], a[52:55], v[172:175]\n" \
"ds_read_b64_tr_b16 a[44:45], v11 offset:1536\n" \
"v_mfma_f32_16x16x32_bf16 v[176:179], a[88:91], a[56:59], v[176:179]\n" \
"s_add_u32 m0, s50, 86016\n" \
"ds_read_b64_tr_b16 a[46:47], v12 offset:1536\n" \
"v_mfma_f32_16x16x32_bf16 v[180:183], a[92:95], a[56:59], v[180:183]\n" \
"buffer_load_dwordx4 v18, s[28:31], s46 offen lds\n" \
"v_mfma_f32_16x16x32_bf16 v[184:187], a[88:91], a[60:63], v[184:187]\n" \
"ds_read_b64_tr_b16 a[16:17], v9 offset:0\n" \
"v_mfma_f32_16x16x32_bf16 v[188:191], a[92:95], a[60:63], v[188:191]\n" \
"ds_read_b64_tr_b16 a[18:19], v10 offset:0\n" \
"v_mfma_f32_16x16x32_bf16 v[64:67], a[64:67], a[48:51], v[64:67]\n" \
"s_add_u32 m0, s50, 90112\n" \
"ds_read_b64_tr_b16 a[20:21], v11 offset:0\n" \
"v_mfma_f32_16x16x32_bf16 v[68:71], a[68:71], a[48:51], v[68:71]\n" \
"buffer_load_dwordx4 v19, s[28:31], s46 offen lds\n" \
"v_mfma_f32_16x16x32_bf16 v[72:75], a[64:67], a[52:55], v[72:75]\n" \
"ds_read_b64_tr_b16 a[22:23], v12 offset:0\n" \
"v_mfma_f32_16x16x32_bf16 v[76:79], a[68:71], a[52:55], v[76:79]\n" \
"v_mfma_f32_16x16x32_bf16 v[80:83], a[64:67], a[56:59], v[80:83]\n" \
"s_add_u32 m0, s50, 94208\n" \
"v_mfma_f32_16x16x32_bf16 v[84:87], a[68:71], a[56:59], v[84:87]\n" \
"buffer_load_dwordx4 v20, s[28:31], s46 offen lds\n" \
"v_mfma_f32_16x16x32_bf16 v[88:91], a[64:67], a[60:63], v[88:91]\n" \
"v_mfma_f32_16x16x32_bf16 v[92:95], a[68:71], a[60:63], v[92:95]\n" \
"s_waitcnt lgkmcnt(0)\n" \
"v_mfma_f32_16x16x32_bf16 v[96:99], a[24:27], a[0:3], v[96:99]\n" \
"ds_read_b64_tr_b16 a[72:73], v9 offset:8704\n" \
"v_mfma_f32_16x16x32_bf16 v[100:103], a[28:31], a[0:3], v[100:103]\n" \
"ds_read_b64_tr_b16 a[74:75], v10 offset:8704\n" \
"v_mfma_f32_16x16x32_bf16 v[104:107], a[24:27], a[4:7], v[104:107]\n" \
"ds_read_b64_tr_b16 a[76:77], v11 offset:8704\n" \
"v_mfma_f32_16x16x32_bf16 v[108:111], a[28:31], a[4:7], v[108:111]\n" \
"ds_read_b64_tr_b16 a[78:79], v12 offset:8704\n" \
"v_mfma_f32_16x16x32_bf16 v[112:115], a[24:27], a[8:11], v[112:115]\n" \
"ds_read_b128 a[48:51], v6 offset:0\n" \
"v_mfma_f32_16x16x32_bf16 v[116:119], a[28:31], a[8:11], v[116:119]\n" \
"buffer_store_dwordx2 v[58:59], v56, s[36:39], s24 offen sc1\n" \
"v_mfma_f32_16x16x32_bf16 v[120:123], a[24:27], a[12:15], v[120:123]\n" \
"ds_read_b128 a[52:55], v6 offset:2048\n" \
"v_mfma_f32_16x16x32_bf16 v[124:127], a[28:31], a[12:15], v[124:127]\n" \
"ds_read_b128 a[56:59], v6 offset:4096\n" \
"v_mfma_f32_16x16x32_bf16 v[128:131], a[32:35], a[0:3], v[128:131]\n" \
"s_mov_b32 s47, 6815744\n" \
"s_add_u32 m0, s51, 49152\n" \
"ds_read_b128 a[60:63], v6 offset:6144\n" \
"v_mfma_f32_16x16x32_bf16 v[132:135], a[36:39], a[0:3], v[132:135]\n" \
"buffer_load_dwordx4 v21, s[32:35], s47 offen offset:0 lds\n" \
"v_mfma_f32_16x16x32_bf16 v[136:139], a[32:35], a[4:7], v[136:139]\n" \
"ds_read_b64_tr_b16 a[80:81], v9 offset:9216\n" \
"v_mfma_f32_16x16x32_bf16 v[140:143], a[36:39], a[4:7], v[140:143]\n" \
"ds_read_b64_tr_b16 a[82:83], v10 offset:9216\n" \
"v_mfma_f32_16x16x32_bf16 v[144:147], a[32:35], a[8:11], v[144:147]\n" \
"s_add_u32 m0, s51, 50048\n" \
"ds_read_b64_tr_b16 a[84:85], v11 offset:9216\n" \
"v_mfma_f32_16x16x32_bf16 v[148:151], a[36:39], a[8:11], v[148:151]\n" \
"buffer_load_dwordx4 v21, s[32:35], s47 offen offset:128 lds\n" \
"v_mfma_f32_16x16x32_bf16 v[152:155], a[32:35], a[12:15], v[152:155]\n" \
"ds_read_b64_tr_b16 a[86:87], v12 offset:9216\n" \
"v_mfma_f32_16x16x32_bf16 v[156:159], a[36:39], a[12:15], v[156:159]\n" \
"ds_read_b64_tr_b16 a[88:89], v9 offset:9728\n" \
"v_mfma_f32_16x16x32_bf16 v[160:163], a[40:43], a[0:3], v[160:163]\n" \
"s_mov_b32 s47, 7077888\n" \
"s_add_u32 m0, s51, 57344\n" \
"ds_read_b64_tr_b16 a[90:91], v10 offset:9728\n" \
"v_mfma_f32_16x16x32_bf16 v[164:167], a[44:47], a[0:3], v[164:167]\n" \
"buffer_load_dwordx4 v21, s[32:35], s47 offen offset:0 lds\n" \
"v_mfma_f32_16x16x32_bf16 v[168:171], a[40:43], a[4:7], v[168:171]\n" \
"ds_read_b64_tr_b16 a[92:93], v11 offset:9728\n" \
"v_mfma_f32_16x16x32_bf16 v[172:175], a[44:47], a[4:7], v[172:175]\n" \
"ds_read_b64_tr_b16 a[94:95], v12 offset:9728\n" \
"v_mfma_f32_16x16x32_bf16 v[176:179], a[40:43], a[8:11], v[176:179]\n" \
"s_add_u32 m0, s51, 58240\n" \
"ds_read_b64_tr_b16 a[64:65], v9 offset:8192\n" \
"v_mfma_f32_16x16x32_bf16 v[180:183], a[44:47], a[8:11], v[180:183]\n" \
"buffer_load_dwordx4 v21, s[32:35], s47 offen offset:128 lds\n" \
"v_mfma_f32_16x16x32_bf16 v[184:187], a[40:43], a[12:15], v[184:187]\n" \
"ds_read_b64_tr_b16 a[66:67], v10 offset:8192\n" \
"v_mfma_f32_16x16x32_bf16 v[188:191], a[44:47], a[12:15], v[188:191]\n" \
"ds_read_b64_tr_b16 a[68:69], v11 offset:8192\n" \
"v_mfma_f32_16x16x32_bf16 v[64:67], a[16:19], a[0:3], v[64:67]\n" \
"ds_read_b64_tr_b16 a[70:71], v12 offset:8192\n" \
"v_mfma_f32_16x16x32_bf16 v[68:71], a[20:23], a[0:3], v[68:71]\n" \
"v_mfma_f32_16x16x32_bf16 v[72:75], a[16:19], a[4:7], v[72:75]\n" \
"v_mfma_f32_16x16x32_bf16 v[76:79], a[20:23], a[4:7], v[76:79]\n" \
"v_mfma_f32_16x16x32_bf16 v[80:83], a[16:19], a[8:11], v[80:83]\n" \
"v_mfma_f32_16x16x32_bf16 v[84:87], a[20:23], a[8:11], v[84:87]\n" \
"v_mfma_f32_16x16x32_bf16 v[88:91], a[16:19], a[12:15], v[88:91]\n" \
"s_waitcnt vmcnt(18) lgkmcnt(0)\n" \
"v_mfma_f32_16x16x32_bf16 v[92:95], a[20:23], a[12:15], v[92:95]\n" \
"s_barrier\n" \
"v_mfma_f32_16x16x32_bf16 v[96:99], a[72:75], a[48:51], v[96:99]\n" \
"s_mov_b32 s46, 1664\n" \
"s_add_u32 m0, s50, 0\n" \
"ds_read_b64_tr_b16 a[24:25], v9 offset:16896\n" \
"v_mfma_f32_16x16x32_bf16 v[100:103], a[76:79], a[48:51], v[100:103]\n" \
"buffer_load_dwordx4 v13, s[28:31], s46 offen lds\n" \
"v_mfma_f32_16x16x32_bf16 v[104:107], a[72:75], a[52:55], v[104:107]\n" \
"ds_read_b64_tr_b16 a[26:27], v10 offset:16896\n" \
"v_mfma_f32_16x16x32_bf16 v[108:111], a[76:79], a[52:55], v[108:111]\n" \
"ds_read_b64_tr_b16 a[28:29], v11 offset:16896\n" \
"v_mfma_f32_16x16x32_bf16 v[112:115], a[72:75], a[56:59], v[112:115]\n" \
"s_add_u32 m0, s50, 4096\n" \
"ds_read_b64_tr_b16 a[30:31], v12 offset:16896\n" \
"v_mfma_f32_16x16x32_bf16 v[116:119], a[76:79], a[56:59], v[116:119]\n" \
"buffer_load_dwordx4 v14, s[28:31], s46 offen lds\n" \
"v_mfma_f32_16x16x32_bf16 v[120:123], a[72:75], a[60:63], v[120:123]\n" \
"ds_read_b128 a[0:3], v5 offset:32768\n" \
"v_mfma_f32_16x16x32_bf16 v[124:127], a[76:79], a[60:63], v[124:127]\n" \
"ds_read_b128 a[4:7], v5 offset:34816\n" \
"v_mfma_f32_16x16x32_bf16 v[128:131], a[80:83], a[48:51], v[128:131]\n" \
"s_add_u32 m0, s50, 8192\n" \
"ds_read_b128 a[8:11], v5 offset:36864\n" \
"v_mfma_f32_16x16x32_bf16 v[132:135], a[84:87], a[48:51], v[132:135]\n" \
"buffer_load_dwordx4 v15, s[28:31], s46 offen lds\n" \
"v_mfma_f32_16x16x32_bf16 v[136:139], a[80:83], a[52:55], v[136:139]\n" \
"ds_read_b128 a[12:15], v5 offset:38912\n" \
"v_mfma_f32_16x16x32_bf16 v[140:143], a[84:87], a[52:55], v[140:143]\n" \
"ds_read_b64_tr_b16 a[32:33], v9 offset:17408\n" \
"v_mfma_f32_16x16x32_bf16 v[144:147], a[80:83], a[56:59], v[144:147]\n" \
"s_add_u32 m0, s50, 12288\n" \
"ds_read_b64_tr_b16 a[34:35], v10 offset:17408\n" \
"v_mfma_f32_16x16x32_bf16 v[148:151], a[84:87], a[56:59], v[148:151]\n" \
"buffer_load_dwordx4 v16, s[28:31], s46 offen lds\n" \
"v_mfma_f32_16x16x32_bf16 v[152:155], a[80:83], a[60:63], v[152:155]\n" \
"ds_read_b64_tr_b16 a[36:37], v11 offset:17408\n" \
"v_mfma_f32_16x16x32_bf16 v[156:159], a[84:87], a[60:63], v[156:159]\n" \
"ds_read_b64_tr_b16 a[38:39], v12 offset:17408\n" \
"v_mfma_f32_16x16x32_bf16 v[160:163], a[88:91], a[48:51], v[160:163]\n" \
"s_mov_b32 s46, 1664\n" \
"s_add_u32 m0, s50, 16384\n" \
"ds_read_b64_tr_b16 a[40:41], v9 offset:17920\n" \
"v_mfma_f32_16x16x32_bf16 v[164:167], a[92:95], a[48:51], v[164:167]\n" \
"buffer_load_dwordx4 v17, s[28:31], s46 offen lds\n" \
"v_mfma_f32_16x16x32_bf16 v[168:171], a[88:91], a[52:55], v[168:171]\n" \
"ds_read_b64_tr_b16 a[42:43], v10 offset:17920\n" \
"v_mfma_f32_16x16x32_bf16 v[172:175], a[92:95], a[52:55], v[172:175]\n" \
"ds_read_b64_tr_b16 a[44:45], v11 offset:17920\n" \
"v_mfma_f32_16x16x32_bf16 v[176:179], a[88:91], a[56:59], v[176:179]\n" \
"s_add_u32 m0, s50, 20480\n" \
"ds_read_b64_tr_b16 a[46:47], v12 offset:17920\n" \
"v_mfma_f32_16x16x32_bf16 v[180:183], a[92:95], a[56:59], v[180:183]\n" \
"buffer_load_dwordx4 v18, s[28:31], s46 offen lds\n" \
"v_mfma_f32_16x16x32_bf16 v[184:187], a[88:91], a[60:63], v[184:187]\n" \
"ds_read_b64_tr_b16 a[16:17], v9 offset:16384\n" \
"v_mfma_f32_16x16x32_bf16 v[188:191], a[92:95], a[60:63], v[188:191]\n" \
"ds_read_b64_tr_b16 a[18:19], v10 offset:16384\n" \
"v_mfma_f32_16x16x32_bf16 v[64:67], a[64:67], a[48:51], v[64:67]\n" \
"s_add_u32 m0, s50, 24576\n" \
"ds_read_b64_tr_b16 a[20:21], v11 offset:16384\n" \
"v_mfma_f32_16x16x32_bf16 v[68:71], a[68:71], a[48:51], v[68:71]\n" \
"buffer_load_dwordx4 v19, s[28:31], s46 offen lds\n" \
"v_mfma_f32_16x16x32_bf16 v[72:75], a[64:67], a[52:55], v[72:75]\n" \
"ds_read_b64_tr_b16 a[22:23], v12 offset:16384\n" \
"v_mfma_f32_16x16x32_bf16 v[76:79], a[68:71], a[52:55], v[76:79]\n" \
"v_mfma_f32_16x16x32_bf16 v[80:83], a[64:67], a[56:59], v[80:83]\n" \
"s_add_u32 m0, s50, 28672\n" \
"v_mfma_f32_16x16x32_bf16 v[84:87], a[68:71], a[56:59], v[84:87]\n" \
"buffer_load_dwordx4 v20, s[28:31], s46 offen lds\n" \
"v_mfma_f32_16x16x32_bf16 v[88:91], a[64:67], a[60:63], v[88:91]\n" \
"v_mfma_f32_16x16x32_bf16 v[92:95], a[68:71], a[60:63], v[92:95]\n" \
"s_waitcnt lgkmcnt(0)\n" \
"v_mfma_f32_16x16x32_bf16 v[96:99], a[24:27], a[0:3], v[96:99]\n" \
"ds_read_b64_tr_b16 a[72:73], v9 offset:25088\n" \
"s_add_u32 s100, s100, 262144\ns_addc_u32 s101, s101, 0\n" \
"v_mfma_f32_16x16x32_bf16 v[100:103], a[28:31], a[0:3], v[100:103]\n" \
"ds_read_b64_tr_b16 a[74:75], v10 offset:25088\n" \
"v_mfma_f32_16x16x32_bf16 v[104:107], a[24:27], a[4:7], v[104:107]\n" \
"ds_read_b64_tr_b16 a[76:77], v11 offset:25088\n" \
"v_mfma_f32_16x16x32_bf16 v[108:111], a[28:31], a[4:7], v[108:111]\n" \
"ds_read_b64_tr_b16 a[78:79], v12 offset:25088\n" \
"v_mfma_f32_16x16x32_bf16 v[112:115], a[24:27], a[8:11], v[112:115]\n" \
"ds_read_b128 a[48:51], v6 offset:32768\n" \
"v_mfma_f32_16x16x32_bf16 v[116:119], a[28:31], a[8:11], v[116:119]\n" \
"ds_read_b128 a[52:55], v6 offset:34816\n" \
"v_mfma_f32_16x16x32_bf16 v[120:123], a[24:27], a[12:15], v[120:123]\n" \
"ds_read_b128 a[56:59], v6 offset:36864\n" \
"v_mfma_f32_16x16x32_bf16 v[124:127], a[28:31], a[12:15], v[124:127]\n" \
"ds_read_b128 a[60:63], v6 offset:38912\n" \
"v_mfma_f32_16x16x32_bf16 v[128:131], a[32:35], a[0:3], v[128:131]\n" \
"s_mov_b32 s47, 7340032\n" \
"s_add_u32 m0, s51, 0\n" \
"ds_read_b64_tr_b16 a[80:81], v9 offset:25600\n" \
"v_mfma_f32_16x16x32_bf16 v[132:135], a[36:39], a[0:3], v[132:135]\n" \
"buffer_load_dwordx4 v21, s[32:35], s47 offen offset:0 lds\n" \
"v_mfma_f32_16x16x32_bf16 v[136:139], a[32:35], a[4:7], v[136:139]\n" \
"ds_read_b64_tr_b16 a[82:83], v10 offset:25600\n" \
"v_mfma_f32_16x16x32_bf16 v[140:143], a[36:39], a[4:7], v[140:143]\n" \
"ds_read_b64_tr_b16 a[84:85], v11 offset:25600\n" \
"v_mfma_f32_16x16x32_bf16 v[144:147], a[32:35], a[8:11], v[144:147]\n" \
"s_add_u32 m0, s51, 896\n" \
"ds_read_b64_tr_b16 a[86:87], v12 offset:25600\n" \
"v_mfma_f32_16x16x32_bf16 v[148:151], a[36:39], a[8:11], v[148:151]\n" \
"buffer_load_dwordx4 v21, s[32:35], s47 offen offset:128 lds\n" \
"v_mfma_f32_16x16x32_bf16 v[152:155], a[32:35], a[12:15], v[152:155]\n" \
"ds_read_b64_tr_b16 a[88:89], v9 offset:26112\n" \
"v_mfma_f32_16x16x32_bf16 v[156:159], a[36:39], a[12:15], v[156:159]\n" \
"ds_read_b64_tr_b16 a[90:91], v10 offset:26112\n" \
"v_mfma_f32_16x16x32_bf16 v[160:163], a[40:43], a[0:3], v[160:163]\n" \
"s_mov_b32 s47, 7602176\n" \
"s_add_u32 m0, s51, 8192\n" \
"ds_read_b64_tr_b16 a[92:93], v11 offset:26112\n" \
"v_mfma_f32_16x16x32_bf16 v[164:167], a[44:47], a[0:3], v[164:167]\n" \
"buffer_load_dwordx4 v21, s[32:35], s47 offen offset:0 lds\n" \
"v_mfma_f32_16x16x32_bf16 v[168:171], a[40:43], a[4:7], v[168:171]\n" \
"ds_read_b64_tr_b16 a[94:95], v12 offset:26112\n" \
"v_mfma_f32_16x16x32_bf16 v[172:175], a[44:47], a[4:7], v[172:175]\n" \
"ds_read_b64_tr_b16 a[64:65], v9 offset:24576\n" \
"v_mfma_f32_16x16x32_bf16 v[176:179], a[40:43], a[8:11], v[176:179]\n" \
"s_add_u32 m0, s51, 9088\n" \
"ds_read_b64_tr_b16 a[66:67], v10 offset:24576\n" \
"v_mfma_f32_16x16x32_bf16 v[180:183], a[44:47], a[8:11], v[180:183]\n" \
"buffer_load_dwordx4 v21, s[32:35], s47 offen offset:128 lds\n" \
"v_mfma_f32_16x16x32_bf16 v[184:187], a[40:43], a[12:15], v[184:187]\n" \
"ds_read_b64_tr_b16 a[68:69], v11 offset:24576\n" \
"v_mfma_f32_16x16x32_bf16 v[188:191], a[44:47], a[12:15], v[188:191]\n" \
"ds_read_b64_tr_b16 a[70:71], v12 offset:24576\n" \
"v_mfma_f32_16x16x32_bf16 v[64:67], a[16:19], a[0:3], v[64:67]\n" \
"v_mfma_f32_16x16x32_bf16 v[68:71], a[20:23], a[0:3], v[68:71]\n" \
"s_mov_b64 exec, 0xffff\nglobal_load_dword v1, v2, s[100:101]\ns_mov_b64 exec, -1\n" \
"v_mfma_f32_16x16x32_bf16 v[72:75], a[16:19], a[4:7], v[72:75]\n" \
"v_mfma_f32_16x16x32_bf16 v[76:79], a[20:23], a[4:7], v[76:79]\n" \
"v_mfma_f32_16x16x32_bf16 v[80:83], a[16:19], a[8:11], v[80:83]\n" \
"v_mfma_f32_16x16x32_bf16 v[84:87], a[20:23], a[8:11], v[84:87]\n" \
"v_mfma_f32_16x16x32_bf16 v[88:91], a[16:19], a[12:15], v[88:91]\n" \
"s_waitcnt vmcnt(18) lgkmcnt(0)\n" \
"v_mfma_f32_16x16x32_bf16 v[92:95], a[20:23], a[12:15], v[92:95]\n" \
"s_barrier\n" \
"v_mfma_f32_16x16x32_bf16 v[96:99], a[72:75], a[48:51], v[96:99]\n" \
"s_mov_b32 s46, 1792\n" \
"s_add_u32 m0, s50, 32768\n" \
"ds_read_b64_tr_b16 a[24:25], v9 offset:33280\n" \
"v_mfma_f32_16x16x32_bf16 v[100:103], a[76:79], a[48:51], v[100:103]\n" \
"buffer_load_dwordx4 v13, s[28:31], s46 offen lds\n" \
"v_mfma_f32_16x16x32_bf16 v[104:107], a[72:75], a[52:55], v[104:107]\n" \
"ds_read_b64_tr_b16 a[26:27], v10 offset:33280\n" \
"v_mfma_f32_16x16x32_bf16 v[108:111], a[76:79], a[52:55], v[108:111]\n" \
"ds_read_b64_tr_b16 a[28:29], v11 offset:33280\n" \
"v_mfma_f32_16x16x32_bf16 v[112:115], a[72:75], a[56:59], v[112:115]\n" \
"s_add_u32 m0, s50, 36864\n" \
"ds_read_b64_tr_b16 a[30:31], v12 offset:33280\n" \
"v_mfma_f32_16x16x32_bf16 v[116:119], a[76:79], a[56:59], v[116:119]\n" \
"buffer_load_dwordx4 v14, s[28:31], s46 offen lds\n" \
"v_mfma_f32_16x16x32_bf16 v[120:123], a[72:75], a[60:63], v[120:123]\n" \
"ds_read_b128 a[0:3], v7 offset:0\n" \
"v_mfma_f32_16x16x32_bf16 v[124:127], a[76:79], a[60:63], v[124:127]\n" \
"ds_read_b128 a[4:7], v7 offset:2048\n" \
"v_mfma_f32_16x16x32_bf16 v[128:131], a[80:83], a[48:51], v[128:131]\n" \
"s_add_u32 m0, s50, 40960\n" \
"ds_read_b128 a[8:11], v7 offset:4096\n" \
"v_mfma_f32_16x16x32_bf16 v[132:135], a[84:87], a[48:51], v[132:135]\n" \
"buffer_load_dwordx4 v15, s[28:31], s46 offen lds\n" \
"v_mfma_f32_16x16x32_bf16 v[136:139], a[80:83], a[52:55], v[136:139]\n" \
"ds_read_b128 a[12:15], v7 offset:6144\n" \
"v_mfma_f32_16x16x32_bf16 v[140:143], a[84:87], a[52:55], v[140:143]\n" \
"ds_read_b64_tr_b16 a[32:33], v9 offset:33792\n" \
"v_mfma_f32_16x16x32_bf16 v[144:147], a[80:83], a[56:59], v[144:147]\n" \
"s_add_u32 m0, s50, 45056\n" \
"ds_read_b64_tr_b16 a[34:35], v10 offset:33792\n" \
"v_mfma_f32_16x16x32_bf16 v[148:151], a[84:87], a[56:59], v[148:151]\n" \
"buffer_load_dwordx4 v16, s[28:31], s46 offen lds\n" \
"v_mfma_f32_16x16x32_bf16 v[152:155], a[80:83], a[60:63], v[152:155]\n" \
"ds_read_b64_tr_b16 a[36:37], v11 offset:33792\n" \
"v_mfma_f32_16x16x32_bf16 v[156:159], a[84:87], a[60:63], v[156:159]\n" \
"ds_read_b64_tr_b16 a[38:39], v12 offset:33792\n" \
"v_mfma_f32_16x16x32_bf16 v[160:163], a[88:91], a[48:51], v[160:163]\n" \
"s_mov_b32 s46, 1792\n" \
"s_add_u32 m0, s50, 49152\n" \
"ds_read_b64_tr_b16 a[40:41], v9 offset:34304\n" \
"v_mfma_f32_16x16x32_bf16 v[164:167], a[92:95], a[48:51], v[164:167]\n" \
"buffer_load_dwordx4 v17, s[28:31], s46 offen lds\n" \
"v_mfma_f32_16x16x32_bf16 v[168:171], a[88:91], a[52:55], v[168:171]\n" \
"ds_read_b64_tr_b16 a[42:43], v10 offset:34304\n" \
"v_mfma_f32_16x16x32_bf16 v[172:175], a[92:95], a[52:55], v[172:175]\n" \
"ds_read_b64_tr_b16 a[44:45], v11 offset:34304\n" \
"v_mfma_f32_16x16x32_bf16 v[176:179], a[88:91], a[56:59], v[176:179]\n" \
"s_add_u32 m0, s50, 53248\n" \
"ds_read_b64_tr_b16 a[46:47], v12 offset:34304\n" \
"v_mfma_f32_16x16x32_bf16 v[180:183], a[92:95], a[56:59], v[180:183]\n" \
"buffer_load_dwordx4 v18, s[28:31], s46 offen lds\n" \
"v_mfma_f32_16x16x32_bf16 v[184:187], a[88:91], a[60:63], v[184:187]\n" \
"ds_read_b64_tr_b16 a[16:17], v9 offset:32768\n" \
"v_mfma_f32_16x16x32_bf16 v[188:191], a[92:95], a[60:63], v[188:191]\n" \
"ds_read_b64_tr_b16 a[18:19], v10 offset:32768\n" \
"v_mfma_f32_16x16x32_bf16 v[64:67], a[64:67], a[48:51], v[64:67]\n" \
"s_add_u32 m0, s50, 57344\n" \
"ds_read_b64_tr_b16 a[20:21], v11 offset:32768\n" \
"v_mfma_f32_16x16x32_bf16 v[68:71], a[68:71], a[48:51], v[68:71]\n" \
"buffer_load_dwordx4 v19, s[28:31], s46 offen lds\n" \
"v_mfma_f32_16x16x32_bf16 v[72:75], a[64:67], a[52:55], v[72:75]\n" \
"ds_read_b64_tr_b16 a[22:23], v12 offset:32768\n" \
"v_mfma_f32_16x16x32_bf16 v[76:79], a[68:71], a[52:55], v[76:79]\n" \
"v_mfma_f32_16x16x32_bf16 v[80:83], a[64:67], a[56:59], v[80:83]\n" \
"s_add_u32 m0, s50, 61440\n" \
"v_mfma_f32_16x16x32_bf16 v[84:87], a[68:71], a[56:59], v[84:87]\n" \
"buffer_load_dwordx4 v20, s[28:31], s46 offen lds\n" \
"v_mfma_f32_16x16x32_bf16 v[88:91], a[64:67], a[60:63], v[88:91]\n" \
"v_mfma_f32_16x16x32_bf16 v[92:95], a[68:71], a[60:63], v[92:95]\n" \
"s_waitcnt lgkmcnt(0)\n" \
"v_mfma_f32_16x16x32_bf16 v[96:99], a[24:27], a[0:3], v[96:99]\n" \
"ds_read_b64_tr_b16 a[72:73], v9 offset:41472\n" \
"s_add_u32 s100, s100, 262144\ns_addc_u32 s101, s101, 0\n" \
"v_mfma_f32_16x16x32_bf16 v[100:103], a[28:31], a[0:3], v[100:103]\n" \
"ds_read_b64_tr_b16 a[74:75], v10 offset:41472\n" \
"v_mfma_f32_16x16x32_bf16 v[104:107], a[24:27], a[4:7], v[104:107]\n" \
"ds_read_b64_tr_b16 a[76:77], v11 offset:41472\n" \
"v_mfma_f32_16x16x32_bf16 v[108:111], a[28:31], a[4:7], v[108:111]\n" \
"ds_read_b64_tr_b16 a[78:79], v12 offset:41472\n" \
"v_mfma_f32_16x16x32_bf16 v[112:115], a[24:27], a[8:11], v[112:115]\n" \
"ds_read_b128 a[48:51], v8 offset:0\n" \
"v_mfma_f32_16x16x32_bf16 v[116:119], a[28:31], a[8:11], v[116:119]\n" \
"ds_read_b128 a[52:55], v8 offset:2048\n" \
"v_mfma_f32_16x16x32_bf16 v[120:123], a[24:27], a[12:15], v[120:123]\n" \
"ds_read_b128 a[56:59], v8 offset:4096\n" \
"v_mfma_f32_16x16x32_bf16 v[124:127], a[28:31], a[12:15], v[124:127]\n" \
"ds_read_b128 a[60:63], v8 offset:6144\n" \
"v_mfma_f32_16x16x32_bf16 v[128:131], a[32:35], a[0:3], v[128:131]\n" \
"s_mov_b32 s47, 7864320\n" \
"s_add_u32 m0, s51, 16384\n" \
"ds_read_b64_tr_b16 a[80:81], v9 offset:41984\n" \
"v_mfma_f32_16x16x32_bf16 v[132:135], a[36:39], a[0:3], v[132:135]\n" \
"buffer_load_dwordx4 v21, s[32:35], s47 offen offset:0 lds\n" \
"v_mfma_f32_16x16x32_bf16 v[136:139], a[32:35], a[4:7], v[136:139]\n" \
"ds_read_b64_tr_b16 a[82:83], v10 offset:41984\n" \
"v_mfma_f32_16x16x32_bf16 v[140:143], a[36:39], a[4:7], v[140:143]\n" \
"ds_read_b64_tr_b16 a[84:85], v11 offset:41984\n" \
"v_mfma_f32_16x16x32_bf16 v[144:147], a[32:35], a[8:11], v[144:147]\n" \
"s_add_u32 m0, s51, 17280\n" \
"ds_read_b64_tr_b16 a[86:87], v12 offset:41984\n" \
"v_mfma_f32_16x16x32_bf16 v[148:151], a[36:39], a[8:11], v[148:151]\n" \
"buffer_load_dwordx4 v21, s[32:35], s47 offen offset:128 lds\n" \
"v_mfma_f32_16x16x32_bf16 v[152:155], a[32:35], a[12:15], v[152:155]\n" \
"ds_read_b64_tr_b16 a[88:89], v9 offset:42496\n" \
"v_mfma_f32_16x16x32_bf16 v[156:159], a[36:39], a[12:15], v[156:159]\n" \
"ds_read_b64_tr_b16 a[90:91], v10 offset:42496\n" \
"v_mfma_f32_16x16x32_bf16 v[160:163], a[40:43], a[0:3], v[160:163]\n" \
"s_mov_b32 s47, 8126464\n" \
"s_add_u32 m0, s51, 24576\n" \
"ds_read_b64_tr_b16 a[92:93], v11 offset:42496\n" \
"v_mfma_f32_16x16x32_bf16 v[164:167], a[44:47], a[0:3], v[164:167]\n" \
"buffer_load_dwordx4 v21, s[32:35], s47 offen offset:0 lds\n" \
"v_mfma_f32_16x16x32_bf16 v[168:171], a[40:43], a[4:7], v[168:171]\n" \
"ds_read_b64_tr_b16 a[94:95], v12 offset:42496\n" \
"v_mfma_f32_16x16x32_bf16 v[172:175], a[44:47], a[4:7], v[172:175]\n" \
"ds_read_b64_tr_b16 a[64:65], v9 offset:40960\n" \
"v_mfma_f32_16x16x32_bf16 v[176:179], a[40:43], a[8:11], v[176:179]\n" \
"s_add_u32 m0, s51, 25472\n" \
"ds_read_b64_tr_b16 a[66:67], v10 offset:40960\n" \
"v_mfma_f32_16x16x32_bf16 v[180:183], a[44:47], a[8:11], v[180:183]\n" \
"buffer_load_dwordx4 v21, s[32:35], s47 offen offset:128 lds\n" \
"v_mfma_f32_16x16x32_bf16 v[184:187], a[40:43], a[12:15], v[184:187]\n" \
"ds_read_b64_tr_b16 a[68:69], v11 offset:40960\n" \
"v_mfma_f32_16x16x32_bf16 v[188:191], a[44:47], a[12:15], v[188:191]\n" \
"ds_read_b64_tr_b16 a[70:71], v12 offset:40960\n" \
"v_mfma_f32_16x16x32_bf16 v[64:67], a[16:19], a[0:3], v[64:67]\n" \
"v_mfma_f32_16x16x32_bf16 v[68:71], a[20:23], a[0:3], v[68:71]\n" \
"s_mov_b64 exec, 0xffff\nglobal_load_dword v1, v2, s[100:101]\ns_mov_b64 exec, -1\n" \
"v_mfma_f32_16x16x32_bf16 v[72:75], a[16:19], a[4:7], v[72:75]\n" \
"v_mfma_f32_16x16x32_bf16 v[76:79], a[20:23], a[4:7], v[76:79]\n" \
"v_mfma_f32_16x16x32_bf16 v[80:83], a[16:19], a[8:11], v[80:83]\n" \
"v_mfma_f32_16x16x32_bf16 v[84:87], a[20:23], a[8:11], v[84:87]\n" \
"v_mfma_f32_16x16x32_bf16 v[88:91], a[16:19], a[12:15], v[88:91]\n" \
"s_waitcnt vmcnt(18) lgkmcnt(0)\n" \
"v_mfma_f32_16x16x32_bf16 v[92:95], a[20:23], a[12:15], v[92:95]\n" \
"s_barrier\n" \
"v_mfma_f32_16x16x32_bf16 v[96:99], a[72:75], a[48:51], v[96:99]\n" \
"s_mov_b32 s46, 1920\n" \
"s_add_u32 m0, s50, 65536\n" \
"ds_read_b64_tr_b16 a[24:25], v9 offset:49664\n" \
"v_mfma_f32_16x16x32_bf16 v[100:103], a[76:79], a[48:51], v[100:103]\n" \
"buffer_load_dwordx4 v13, s[28:31], s46 offen lds\n" \
"v_mfma_f32_16x16x32_bf16 v[104:107], a[72:75], a[52:55], v[104:107]\n" \
"ds_read_b64_tr_b16 a[26:27], v10 offset:49664\n" \
"v_mfma_f32_16x16x32_bf16 v[108:111], a[76:79], a[52:55], v[108:111]\n" \
"ds_read_b64_tr_b16 a[28:29], v11 offset:49664\n" \
"v_mfma_f32_16x16x32_bf16 v[112:115], a[72:75], a[56:59], v[112:115]\n" \
"s_add_u32 m0, s50, 69632\n" \
"ds_read_b64_tr_b16 a[30:31], v12 offset:49664\n" \
"v_mfma_f32_16x16x32_bf16 v[116:119], a[76:79], a[56:59], v[116:119]\n" \
"buffer_load_dwordx4 v14, s[28:31], s46 offen lds\n" \
"v_mfma_f32_16x16x32_bf16 v[120:123], a[72:75], a[60:63], v[120:123]\n" \
"ds_read_b128 a[0:3], v5 offset:0\n" \
"v_mfma_f32_16x16x32_bf16 v[124:127], a[76:79], a[60:63], v[124:127]\n" \
"ds_read_b128 a[4:7], v5 offset:2048\n" \
"v_mfma_f32_16x16x32_bf16 v[128:131], a[80:83], a[48:51], v[128:131]\n" \
"LH13_%=:\ns_cbranch_execz LHR0_%=\n" \
"s_add_u32 m0, s50, 73728\n" \
"ds_read_b128 a[8:11], v5 offset:4096\n" \
"v_mfma_f32_16x16x32_bf16 v[132:135], a[84:87], a[48:51], v[132:135]\n" \
"buffer_load_dwordx4 v15, s[28:31], s46 offen lds\n" \
"v_mfma_f32_16x16x32_bf16 v[136:139], a[80:83], a[52:55], v[136:139]\n" \
"ds_read_b128 a[12:15], v5 offset:6144\n" \
"v_mfma_f32_16x16x32_bf16 v[140:143], a[84:87], a[52:55], v[140:143]\n" \
"ds_read_b64_tr_b16 a[32:33], v9 offset:50176\n" \
"v_mfma_f32_16x16x32_bf16 v[144:147], a[80:83], a[56:59], v[144:147]\n" \
"s_add_u32 m0, s50, 77824\n" \
"ds_read_b64_tr_b16 a[34:35], v10 offset:50176\n" \
"v_mfma_f32_16x16x32_bf16 v[148:151], a[84:87], a[56:59], v[148:151]\n" \
"buffer_load_dwordx4 v16, s[28:31], s46 offen lds\n" \
"v_mfma_f32_16x16x32_bf16 v[152:155], a[80:83], a[60:63], v[152:155]\n" \
"ds_read_b64_tr_b16 a[36:37], v11 offset:50176\n" \
"v_mfma_f32_16x16x32_bf16 v[156:159], a[84:87], a[60:63], v[156:159]\n" \
"ds_read_b64_tr_b16 a[38:39], v12 offset:50176\n" \
"v_mfma_f32_16x16x32_bf16 v[160:163], a[88:91], a[48:51], v[160:163]\n" \
"s_mov_b32 s46, 1920\n" \
"s_add_u32 m0, s50, 81920\n" \
"ds_read_b64_tr_b16 a[40:41], v9 offset:50688\n" \
"v_mfma_f32_16x16x32_bf16 v[164:167], a[92:95], a[48:51], v[164:167]\n" \
"buffer_load_dwordx4 v17, s[28:31], s46 offen lds\n" \
"v_mfma_f32_16x16x32_bf16 v[168:171], a[88:91], a[52:55], v[168:171]\n" \
"ds_read_b64_tr_b16 a[42:43], v10 offset:50688\n" \
"v_mfma_f32_16x16x32_bf16 v[172:175], a[92:95], a[52:55], v[172:175]\n" \
"ds_read_b64_tr_b16 a[44:45], v11 offset:50688\n" \
"v_mfma_f32_16x16x32_bf16 v[176:179], a[88:91], a[56:59], v[176:179]\n" \
"s_add_u32 m0, s50, 86016\n" \
"ds_read_b64_tr_b16 a[46:47], v12 offset:50688\n" \
"v_mfma_f32_16x16x32_bf16 v[180:183], a[92:95], a[56:59], v[180:183]\n" \
"buffer_load_dwordx4 v18, s[28:31], s46 offen lds\n" \
"v_mfma_f32_16x16x32_bf16 v[184:187], a[88:91], a[60:63], v[184:187]\n" \
"ds_read_b64_tr_b16 a[16:17], v9 offset:49152\n" \
"v_mfma_f32_16x16x32_bf16 v[188:191], a[92:95], a[60:63], v[188:191]\n" \
"ds_read_b64_tr_b16 a[18:19], v10 offset:49152\n" \
"v_mfma_f32_16x16x32_bf16 v[64:67], a[64:67], a[48:51], v[64:67]\n" \
"s_add_u32 m0, s50, 90112\n" \
"ds_read_b64_tr_b16 a[20:21], v11 offset:49152\n" \
"v_mfma_f32_16x16x32_bf16 v[68:71], a[68:71], a[48:51], v[68:71]\n" \
"buffer_load_dwordx4 v19, s[28:31], s46 offen lds\n" \
"v_mfma_f32_16x16x32_bf16 v[72:75], a[64:67], a[52:55], v[72:75]\n" \
"ds_read_b64_tr_b16 a[22:23], v12 offset:49152\n" \
"v_mfma_f32_16x16x32_bf16 v[76:79], a[68:71], a[52:55], v[76:79]\n" \
"v_mfma_f32_16x16x32_bf16 v[80:83], a[64:67], a[56:59], v[80:83]\n" \
"s_add_u32 m0, s50, 94208\n" \
"v_mfma_f32_16x16x32_bf16 v[84:87], a[68:71], a[56:59], v[84:87]\n" \
"buffer_load_dwordx4 v20, s[28:31], s46 offen lds\n" \
"v_mfma_f32_16x16x32_bf16 v[88:91], a[64:67], a[60:63], v[88:91]\n" \
"v_mfma_f32_16x16x32_bf16 v[92:95], a[68:71], a[60:63], v[92:95]\n" \
"s_waitcnt lgkmcnt(0)\n" \
"v_mfma_f32_16x16x32_bf16 v[96:99], a[24:27], a[0:3], v[96:99]\n" \
"ds_read_b64_tr_b16 a[72:73], v9 offset:57856\n" \
"s_mov_b32 s44, 0\n" \
"s_mov_b64 s[54:55], 0\n" \
"v_mfma_f32_16x16x32_bf16 v[100:103], a[28:31], a[0:3], v[100:103]\n" \
"ds_read_b64_tr_b16 a[74:75], v10 offset:57856\n" \
"s_mov_b32 s49, s22\n" \
"s_add_u32 s48, s22, 0x10000\n" \
"v_mfma_f32_16x16x32_bf16 v[104:107], a[24:27], a[4:7], v[104:107]\n" \
"ds_read_b64_tr_b16 a[76:77], v11 offset:57856\n" \
"s_lshl_b32 s60, s7, 1\n" \
"s_add_u32 s60, s60, %2\n" \
"v_mfma_f32_16x16x32_bf16 v[108:111], a[28:31], a[4:7], v[108:111]\n" \
"ds_read_b64_tr_b16 a[78:79], v12 offset:57856\n" \
"s_sub_u32 s46, s60, 1\n" \
"s_xor_b32 s62, %2, 3\n" \
"v_mfma_f32_16x16x32_bf16 v[112:115], a[24:27], a[8:11], v[112:115]\n" \
"ds_read_b128 a[48:51], v6 offset:0\n" \
"s_cmp_lt_u32 %2, 2\ns_cselect_b32 s46, s46, 14\ns_cselect_b32 s62, s62, %2\ns_cselect_b32 s61, 56, 57\n" \
"s_cmp_lt_u32 s7, 3\ns_cselect_b32 s45, 8, 0\n" \
"v_mfma_f32_16x16x32_bf16 v[116:119], a[28:31], a[8:11], v[116:119]\n" \
"ds_read_b128 a[52:55], v6 offset:2048\n" \
"s_cmp_lt_i32 s46, s45\ns_cselect_b32 s23, 99, s46\ns_cselect_b32 s24, s45, 99\n" \
"s_min_i32 s46, s46, s45\n" \
"v_mfma_f32_16x16x32_bf16 v[120:123], a[24:27], a[12:15], v[120:123]\n" \
"ds_read_b128 a[56:59], v6 offset:4096\n" \
"s_lshl_b32 s60, s60, 6\n" \
"s_add_u32 s60, s60, s6\n" \
"v_mfma_f32_16x16x32_bf16 v[124:127], a[28:31], a[12:15], v[124:127]\n" \
"ds_read_b128 a[60:63], v6 offset:6144\n" \
"s_lshl_b32 s50, s60, 13\n" \
"s_lshl_b32 s52, s60, 3\n" \
"v_mfma_f32_16x16x32_bf16 v[128:131], a[32:35], a[0:3], v[128:131]\n" \
"ds_read_b64_tr_b16 a[80:81], v9 offset:58368\n" \
"s_add_u32 s52, s52, 94388224\n" \
"s_lshr_b32 s60, %2, 1\n" \
"v_mfma_f32_16x16x32_bf16 v[132:135], a[36:39], a[0:3], v[132:135]\n" \
"ds_read_b64_tr_b16 a[82:83], v10 offset:58368\n" \
"s_lshl_b32 s60, s60, 28\n" \
"s_add_u32 s52, s52, s60\n" \
"v_mfma_f32_16x16x32_bf16 v[136:139], a[32:35], a[4:7], v[136:139]\n" \
"ds_read_b64_tr_b16 a[84:85], v11 offset:58368\n" \
"s_lshl_b32 s62, s62, 1\n" \
"s_add_u32 s61, s61, s62\n" \
"v_mfma_f32_16x16x32_bf16 v[140:143], a[36:39], a[4:7], v[140:143]\n" \
"ds_read_b64_tr_b16 a[86:87], v12 offset:58368\n" \
"s_lshl_b32 s60, s7, 2\n" \
"s_sub_u32 s61, s61, s60\n" \
"v_mfma_f32_16x16x32_bf16 v[144:147], a[32:35], a[8:11], v[144:147]\n" \
"ds_read_b64_tr_b16 a[88:89], v9 offset:58880\n" \
"s_lshl_b32 s61, s61, 5\n" \
"s_add_u32 s61, s61, s6\n" \
"v_mfma_f32_16x16x32_bf16 v[148:151], a[36:39], a[8:11], v[148:151]\n" \
"ds_read_b64_tr_b16 a[90:91], v10 offset:58880\n" \
"s_lshl_b32 s51, s61, 13\n" \
"s_lshl_b32 s53, s61, 3\n" \
"v_mfma_f32_16x16x32_bf16 v[152:155], a[32:35], a[12:15], v[152:155]\n" \
"ds_read_b64_tr_b16 a[92:93], v11 offset:58880\n" \
"s_add_u32 s53, s53, 94388224\n" \
"s_and_b32 s62, s62, 2\n" \
"v_mfma_f32_16x16x32_bf16 v[156:159], a[36:39], a[12:15], v[156:159]\n" \
"ds_read_b64_tr_b16 a[94:95], v12 offset:58880\n" \
"s_lshl_b32 s62, s62, 12\n" \
"s_add_u32 s62, s62, 131072\n" \
"v_mfma_f32_16x16x32_bf16 v[160:163], a[40:43], a[0:3], v[160:163]\n" \
"ds_read_b64_tr_b16 a[64:65], v9 offset:57344\n" \
"v_add_u32 v21, s62, v4\n" \
"s_or_b32 s60, s7, %2\n" \
"v_mfma_f32_16x16x32_bf16 v[164:167], a[44:47], a[0:3], v[164:167]\n" \
"ds_read_b64_tr_b16 a[66:67], v10 offset:57344\n" \
"s_cmp_eq_u32 s60, 0\ns_cselect_b32 s60, 0xff800000, 0\n" \
"v_lshrrev_b32 v22, 6, v4\n" \
"v_mfma_f32_16x16x32_bf16 v[168:171], a[40:43], a[4:7], v[168:171]\n" \
"ds_read_b64_tr_b16 a[68:69], v11 offset:57344\n" \
"v_and_b32 v22, 12, v22\n" \
"v_mfma_f32_16x16x32_bf16 v[172:175], a[44:47], a[4:7], v[172:175]\n" \
"ds_read_b64_tr_b16 a[70:71], v12 offset:57344\n" \
"v_bfe_u32 v23, v4, 4, 4\n" \
"v_mfma_f32_16x16x32_bf16 v[176:179], a[40:43], a[8:11], v[176:179]\n" \
"v_sub_u32 v22, v23, v22\n" \
"v_subrev_u32 v240, 0, v22\n" \
"v_mfma_f32_16x16x32_bf16 v[180:183], a[44:47], a[8:11], v[180:183]\n" \
"buffer_load_dwordx2 v[58:59], v57, s[36:39], 0 offen sc1\n" \
"v_mfma_f32_16x16x32_bf16 v[184:187], a[40:43], a[12:15], v[184:187]\n" \
"v_ashrrev_i32 v240, 31, v240\n" \
"v_and_b32 v240, 0xff800000, v240\n" \
"v_mfma_f32_16x16x32_bf16 v[188:191], a[44:47], a[12:15], v[188:191]\n" \
"v_and_b32 v24, s60, v240\n" \
"v_mov_b32 v244, s60\n" \
"v_mfma_f32_16x16x32_bf16 v[64:67], a[16:19], a[0:3], v[64:67]\n" \
"v_subrev_u32 v241, 1, v22\n" \
"v_ashrrev_i32 v241, 31, v241\n" \
"v_mfma_f32_16x16x32_bf16 v[68:71], a[20:23], a[0:3], v[68:71]\n" \
"v_and_b32 v241, 0xff800000, v241\n" \
"v_and_b32 v25, s60, v241\n" \
"v_mfma_f32_16x16x32_bf16 v[72:75], a[16:19], a[4:7], v[72:75]\n" \
"v_mov_b32 v245, s60\n" \
"v_subrev_u32 v242, 2, v22\n" \
"v_mfma_f32_16x16x32_bf16 v[76:79], a[20:23], a[4:7], v[76:79]\n" \
"v_ashrrev_i32 v242, 31, v242\n" \
"v_and_b32 v242, 0xff800000, v242\n" \
"v_mfma_f32_16x16x32_bf16 v[80:83], a[16:19], a[8:11], v[80:83]\n" \
"s_waitcnt vmcnt(0)\n" \
"v_and_b32 v26, s60, v242\n" \
"v_mfma_f32_16x16x32_bf16 v[84:87], a[20:23], a[8:11], v[84:87]\n" \
"v_cmp_ne_u64 vcc, s[14:15], v[58:59]\n" \
"v_mov_b32 v246, s60\n" \
"v_mfma_f32_16x16x32_bf16 v[88:91], a[16:19], a[12:15], v[88:91]\n" \
"s_waitcnt lgkmcnt(0)\n" \
"v_subrev_u32 v243, 3, v22\n" \
"v_mfma_f32_16x16x32_bf16 v[92:95], a[20:23], a[12:15], v[92:95]\n" \
"s_cbranch_vccnz LPK_%=\nLPKR_%=:\n" \
"s_barrier\n" \
"v_mfma_f32_16x16x32_bf16 v[96:99], a[72:75], a[48:51], v[96:99]\n" \
"s_add_u32 m0, s22, 32768\n" \
"ds_read_b64_tr_b16 a[24:25], v9 offset:512\n" \
"v_ashrrev_i32 v243, 31, v243\n" \
"v_mfma_f32_16x16x32_bf16 v[100:103], a[76:79], a[48:51], v[100:103]\n" \
"buffer_load_dwordx4 v60, s[40:43], s16 offen offset:0 sc1 lds\n" \
"v_mfma_f32_16x16x32_bf16 v[104:107], a[72:75], a[52:55], v[104:107]\n" \
"ds_read_b64_tr_b16 a[26:27], v10 offset:512\n" \
"v_and_b32 v243, 0xff800000, v243\n" \
"v_mfma_f32_16x16x32_bf16 v[108:111], a[76:79], a[52:55], v[108:111]\n" \
"ds_read_b64_tr_b16 a[28:29], v11 offset:512\n" \
"v_and_b32 v27, s60, v243\n" \
"v_mfma_f32_16x16x32_bf16 v[112:115], a[72:75], a[56:59], v[112:115]\n" \
"ds_read_b64_tr_b16 a[30:31], v12 offset:512\n" \
"v_mov_b32 v247, s60\n" \
"v_mfma_f32_16x16x32_bf16 v[116:119], a[76:79], a[56:59], v[116:119]\n" \
"buffer_load_dwordx4 v60, s[40:43], s16 offen offset:1024 sc1 lds\n" \
"v_mfma_f32_16x16x32_bf16 v[120:123], a[72:75], a[60:63], v[120:123]\n" \
"ds_read_b128 a[0:3], v5 offset:32768\n" \
"s_load_dwordx2 s[28:29], %0, 0x50\n" \
"s_load_dwordx2 s[32:33], %0, 0x60\n" \
"v_mfma_f32_16x16x32_bf16 v[124:127], a[76:79], a[60:63], v[124:127]\n" \
"ds_read_b128 a[4:7], v5 offset:34816\n" \
"s_load_dwordx2 s[60:61], %0, 0x0\n" \
"s_lshl_b32 s79, %2, 11\n" \
"v_mfma_f32_16x16x32_bf16 v[128:131], a[80:83], a[48:51], v[128:131]\n" \
"ds_read_b128 a[8:11], v5 offset:36864\n" \
"s_bfe_u32 s89, %1, 0x20001\n" \
"s_lshl_b32 s92, s89, 21\n" \
"v_mfma_f32_16x16x32_bf16 v[132:135], a[84:87], a[48:51], v[132:135]\n" \
"buffer_load_dwordx4 v60, s[40:43], s16 offen offset:2048 sc1 lds\n" \
"s_lshr_b32 s93, %1, 5\n" \
"s_lshl_b32 s93, s93, 8\n" \
"v_mfma_f32_16x16x32_bf16 v[136:139], a[80:83], a[52:55], v[136:139]\n" \
"ds_read_b128 a[12:15], v5 offset:38912\n" \
"s_add_u32 s94, s92, s93\n" \
"s_sub_u32 s30, 8388608, s94\n" \
"v_mfma_f32_16x16x32_bf16 v[140:143], a[84:87], a[52:55], v[140:143]\n" \
"ds_read_b64_tr_b16 a[32:33], v9 offset:1024\n" \
"s_lshl_b32 s92, %2, 2\n" \
"s_lshl_b32 s93, %2, 1\n" \
"v_mfma_f32_16x16x32_bf16 v[144:147], a[80:83], a[56:59], v[144:147]\n" \
"ds_read_b64_tr_b16 a[34:35], v10 offset:1024\n" \
"v_bfe_u32 v22, v4, 6, 2\n" \
"v_mfma_f32_16x16x32_bf16 v[148:151], a[84:87], a[56:59], v[148:151]\n" \
"buffer_load_dwordx4 v60, s[40:43], s16 offen offset:3072 sc1 lds\n" \
"v_mfma_f32_16x16x32_bf16 v[152:155], a[80:83], a[60:63], v[152:155]\n" \
"ds_read_b64_tr_b16 a[36:37], v11 offset:1024\n" \
"v_add_u32 v22, s92, v22\n" \
"v_mfma_f32_16x16x32_bf16 v[156:159], a[84:87], a[60:63], v[156:159]\n" \
"ds_read_b64_tr_b16 a[38:39], v12 offset:1024\n" \
"v_bfe_u32 v23, v4, 8, 1\n" \
"v_mfma_f32_16x16x32_bf16 v[160:163], a[88:91], a[48:51], v[160:163]\n" \
"s_add_u32 s16, s16, s9\n" \
"s_add_u32 m0, s22, 65536\n" \
"ds_read_b64_tr_b16 a[40:41], v9 offset:1536\n" \
"v_mfma_f32_16x16x32_bf16 v[164:167], a[92:95], a[48:51], v[164:167]\n" \
"buffer_load_dwordx4 v60, s[40:43], s16 offen offset:0 sc1 lds\n" \
"v_mfma_f32_16x16x32_bf16 v[168:171], a[88:91], a[52:55], v[168:171]\n" \
"ds_read_b64_tr_b16 a[42:43], v10 offset:1536\n" \
"v_lshl_add_u32 v22, v23, 4, v22\n" \
"v_mfma_f32_16x16x32_bf16 v[172:175], a[92:95], a[52:55], v[172:175]\n" \
"ds_read_b64_tr_b16 a[44:45], v11 offset:1536\n" \
"v_lshlrev_b32 v22, 11, v22\n" \
"v_mfma_f32_16x16x32_bf16 v[176:179], a[88:91], a[56:59], v[176:179]\n" \
"ds_read_b64_tr_b16 a[46:47], v12 offset:1536\n" \
"v_add_u32 v23, s93, v23\n" \
"v_mfma_f32_16x16x32_bf16 v[180:183], a[92:95], a[56:59], v[180:183]\n" \
"buffer_load_dwordx4 v60, s[40:43], s16 offen offset:1024 sc1 lds\n" \
"v_mfma_f32_16x16x32_bf16 v[184:187], a[88:91], a[60:63], v[184:187]\n" \
"ds_read_b64_tr_b16 a[16:17], v9 offset:0\n" \
"v_lshrrev_b32 v28, 4, v4\n" \
"v_mfma_f32_16x16x32_bf16 v[188:191], a[92:95], a[60:63], v[188:191]\n" \
"ds_read_b64_tr_b16 a[18:19], v10 offset:0\n" \
"v_xor_b32 v23, v23, v28\n" \
"v_mfma_f32_16x16x32_bf16 v[64:67], a[64:67], a[48:51], v[64:67]\n" \
"ds_read_b64_tr_b16 a[20:21], v11 offset:0\n" \
"v_and_b32 v23, 3, v23\n" \
"v_mfma_f32_16x16x32_bf16 v[68:71], a[68:71], a[48:51], v[68:71]\n" \
"buffer_load_dwordx4 v60, s[40:43], s16 offen offset:2048 sc1 lds\n" \
"v_mfma_f32_16x16x32_bf16 v[72:75], a[64:67], a[52:55], v[72:75]\n" \
"ds_read_b64_tr_b16 a[22:23], v12 offset:0\n" \
"v_lshrrev_b32 v28, 9, v4\n" \
"v_mfma_f32_16x16x32_bf16 v[76:79], a[68:71], a[52:55], v[76:79]\n" \
"v_lshl_add_u32 v23, v28, 2, v23\n" \
"v_lshl_add_u32 v30, v23, 4, v22\n" \
"s_lshl_b32 s92, %2, 9\n" \
"v_mfma_f32_16x16x32_bf16 v[80:83], a[64:67], a[56:59], v[80:83]\n" \
"s_add_u32 s92, s92, 0x20000\n" \
"v_add_u32 v13, s92, v9\n" \
"v_mfma_f32_16x16x32_bf16 v[84:87], a[68:71], a[56:59], v[84:87]\n" \
"buffer_load_dwordx4 v60, s[40:43], s16 offen offset:3072 sc1 lds\n" \
"v_mfma_f32_16x16x32_bf16 v[88:91], a[64:67], a[60:63], v[88:91]\n" \
"v_add_u32 v14, s92, v10\n" \
"v_add_u32 v15, s92, v11\n" \
"v_mfma_f32_16x16x32_bf16 v[92:95], a[68:71], a[60:63], v[92:95]\n" \
"s_waitcnt lgkmcnt(0)\n" \
"v_add_u32 v16, s92, v12\n" \
"v_mfma_f32_16x16x32_bf16 v[96:99], a[24:27], a[0:3], v[96:99]\n" \
"ds_read_b64_tr_b16 a[72:73], v9 offset:8704\n" \
"v_lshlrev_b32 v20, 23, v4\n" \
"v_mfma_f32_16x16x32_bf16 v[100:103], a[28:31], a[0:3], v[100:103]\n" \
"ds_read_b64_tr_b16 a[74:75], v10 offset:8704\n" \
"v_mfma_f32_16x16x32_bf16 v[104:107], a[24:27], a[4:7], v[104:107]\n" \
"ds_read_b64_tr_b16 a[76:77], v11 offset:8704\n" \
"v_mfma_f32_16x16x32_bf16 v[108:111], a[28:31], a[4:7], v[108:111]\n" \
"ds_read_b64_tr_b16 a[78:79], v12 offset:8704\n" \
"v_mfma_f32_16x16x32_bf16 v[112:115], a[24:27], a[8:11], v[112:115]\n" \
"ds_read_b128 a[48:51], v6 offset:32768\n" \
"v_mfma_f32_16x16x32_bf16 v[116:119], a[28:31], a[8:11], v[116:119]\n" \
"ds_read_b128 a[52:55], v6 offset:34816\n" \
"v_mfma_f32_16x16x32_bf16 v[120:123], a[24:27], a[12:15], v[120:123]\n" \
"ds_read_b128 a[56:59], v6 offset:36864\n" \
"v_mfma_f32_16x16x32_bf16 v[124:127], a[28:31], a[12:15], v[124:127]\n" \
"ds_read_b128 a[60:63], v6 offset:38912\n" \
"v_mfma_f32_16x16x32_bf16 v[128:131], a[32:35], a[0:3], v[128:131]\n" \
"s_add_u32 m0, s22, 81920\n" \
"ds_read_b64_tr_b16 a[80:81], v9 offset:9216\n" \
"v_mfma_f32_16x16x32_bf16 v[132:135], a[36:39], a[0:3], v[132:135]\n" \
"buffer_load_dwordx4 v60, s[40:43], s17 offen offset:0 sc1 lds\n" \
"v_mfma_f32_16x16x32_bf16 v[136:139], a[32:35], a[4:7], v[136:139]\n" \
"ds_read_b64_tr_b16 a[82:83], v10 offset:9216\n" \
"v_mfma_f32_16x16x32_bf16 v[140:143], a[36:39], a[4:7], v[140:143]\n" \
"ds_read_b64_tr_b16 a[84:85], v11 offset:9216\n" \
"v_mfma_f32_16x16x32_bf16 v[144:147], a[32:35], a[8:11], v[144:147]\n" \
"ds_read_b64_tr_b16 a[86:87], v12 offset:9216\n" \
"v_mfma_f32_16x16x32_bf16 v[148:151], a[36:39], a[8:11], v[148:151]\n" \
"buffer_load_dwordx4 v60, s[40:43], s17 offen offset:1024 sc1 lds\n" \
"v_mfma_f32_16x16x32_bf16 v[152:155], a[32:35], a[12:15], v[152:155]\n" \
"ds_read_b64_tr_b16 a[88:89], v9 offset:9728\n" \
"v_mfma_f32_16x16x32_bf16 v[156:159], a[36:39], a[12:15], v[156:159]\n" \
"ds_read_b64_tr_b16 a[90:91], v10 offset:9728\n" \
"v_mfma_f32_16x16x32_bf16 v[160:163], a[40:43], a[0:3], v[160:163]\n" \
"ds_read_b64_tr_b16 a[92:93], v11 offset:9728\n" \
"v_mfma_f32_16x16x32_bf16 v[164:167], a[44:47], a[0:3], v[164:167]\n" \
"buffer_load_dwordx4 v60, s[40:43], s17 offen offset:2048 sc1 lds\n" \
"v_mfma_f32_16x16x32_bf16 v[168:171], a[40:43], a[4:7], v[168:171]\n" \
"ds_read_b64_tr_b16 a[94:95], v12 offset:9728\n" \
"v_mfma_f32_16x16x32_bf16 v[172:175], a[44:47], a[4:7], v[172:175]\n" \
"ds_read_b64_tr_b16 a[64:65], v9 offset:8192\n" \
"v_mfma_f32_16x16x32_bf16 v[176:179], a[40:43], a[8:11], v[176:179]\n" \
"ds_read_b64_tr_b16 a[66:67], v10 offset:8192\n" \
"v_mfma_f32_16x16x32_bf16 v[180:183], a[44:47], a[8:11], v[180:183]\n" \
"buffer_load_dwordx4 v60, s[40:43], s17 offen offset:3072 sc1 lds\n" \
"v_mfma_f32_16x16x32_bf16 v[184:187], a[40:43], a[12:15], v[184:187]\n" \
"ds_read_b64_tr_b16 a[68:69], v11 offset:8192\n" \
"v_mfma_f32_16x16x32_bf16 v[188:191], a[44:47], a[12:15], v[188:191]\n" \
"ds_read_b64_tr_b16 a[70:71], v12 offset:8192\n" \
"v_mfma_f32_16x16x32_bf16 v[64:67], a[16:19], a[0:3], v[64:67]\n" \
"v_mfma_f32_16x16x32_bf16 v[68:71], a[20:23], a[0:3], v[68:71]\n" \
"v_mfma_f32_16x16x32_bf16 v[72:75], a[16:19], a[4:7], v[72:75]\n" \
"v_mfma_f32_16x16x32_bf16 v[76:79], a[20:23], a[4:7], v[76:79]\n" \
"v_mfma_f32_16x16x32_bf16 v[80:83], a[16:19], a[8:11], v[80:83]\n" \
"v_mfma_f32_16x16x32_bf16 v[84:87], a[20:23], a[8:11], v[84:87]\n" \
"v_mfma_f32_16x16x32_bf16 v[88:91], a[16:19], a[12:15], v[88:91]\n" \
"s_waitcnt vmcnt(13) lgkmcnt(0)\n" \
"v_mfma_f32_16x16x32_bf16 v[92:95], a[20:23], a[12:15], v[92:95]\n" \
"s_barrier\n" \
"v_mfma_f32_16x16x32_bf16 v[96:99], a[72:75], a[48:51], v[96:99]\n" \
"s_add_u32 s16, s16, s9\n" \
"s_add_u32 m0, s22, 98304\n" \
"ds_read_b64_tr_b16 a[24:25], v9 offset:16896\n" \
"v_mfma_f32_16x16x32_bf16 v[100:103], a[76:79], a[48:51], v[100:103]\n" \
"buffer_load_dwordx4 v60, s[40:43], s16 offen offset:0 sc1 lds\n" \
"s_add_u32 s28, s28, s94\ns_addc_u32 s29, s29, 0\n" \
"s_and_b32 s29, s29, 0xffff\n" \
"v_mfma_f32_16x16x32_bf16 v[104:107], a[72:75], a[52:55], v[104:107]\n" \
"ds_read_b64_tr_b16 a[26:27], v10 offset:16896\n" \
"s_and_b32 s33, s33, 0xffff\n" \
"s_mov_b32 s34, 4194304\n" \
"v_mfma_f32_16x16x32_bf16 v[108:111], a[76:79], a[52:55], v[108:111]\n" \
"ds_read_b64_tr_b16 a[28:29], v11 offset:16896\n" \
"s_mov_b32 s35, 0x00020000\n" \
"v_mfma_f32_16x16x32_bf16 v[112:115], a[72:75], a[56:59], v[112:115]\n" \
"ds_read_b64_tr_b16 a[30:31], v12 offset:16896\n" \
"v_mfma_f32_16x16x32_bf16 v[116:119], a[76:79], a[56:59], v[116:119]\n" \
"buffer_load_dwordx4 v60, s[40:43], s16 offen offset:1024 sc1 lds\n" \
"v_mfma_f32_16x16x32_bf16 v[120:123], a[72:75], a[60:63], v[120:123]\n" \
"ds_read_b128 a[0:3], v7 offset:0\n" \
"v_mfma_f32_16x16x32_bf16 v[124:127], a[76:79], a[60:63], v[124:127]\n" \
"ds_read_b128 a[4:7], v7 offset:2048\n" \
"v_mfma_f32_16x16x32_bf16 v[128:131], a[80:83], a[48:51], v[128:131]\n" \
"ds_read_b128 a[8:11], v7 offset:4096\n" \
"v_mfma_f32_16x16x32_bf16 v[132:135], a[84:87], a[48:51], v[132:135]\n" \
"buffer_load_dwordx4 v60, s[40:43], s16 offen offset:2048 sc1 lds\n" \
"v_mfma_f32_16x16x32_bf16 v[136:139], a[80:83], a[52:55], v[136:139]\n" \
"ds_read_b128 a[12:15], v7 offset:6144\n" \
"v_mfma_f32_16x16x32_bf16 v[140:143], a[84:87], a[52:55], v[140:143]\n" \
"ds_read_b64_tr_b16 a[32:33], v9 offset:17408\n" \
"v_mfma_f32_16x16x32_bf16 v[144:147], a[80:83], a[56:59], v[144:147]\n" \
"ds_read_b64_tr_b16 a[34:35], v10 offset:17408\n" \
"v_mfma_f32_16x16x32_bf16 v[148:151], a[84:87], a[56:59], v[148:151]\n" \
"buffer_load_dwordx4 v60, s[40:43], s16 offen offset:3072 sc1 lds\n" \
"v_mfma_f32_16x16x32_bf16 v[152:155], a[80:83], a[60:63], v[152:155]\n" \
"ds_read_b64_tr_b16 a[36:37], v11 offset:17408\n" \
"v_mfma_f32_16x16x32_bf16 v[156:159], a[84:87], a[60:63], v[156:159]\n" \
"ds_read_b64_tr_b16 a[38:39], v12 offset:17408\n" \
"v_mfma_f32_16x16x32_bf16 v[160:163], a[88:91], a[48:51], v[160:163]\n" \
"s_add_u32 s17, s17, s19\n" \
"s_add_u32 m0, s22, 114688\n" \
"ds_read_b64_tr_b16 a[40:41], v9 offset:17920\n" \
"v_mfma_f32_16x16x32_bf16 v[164:167], a[92:95], a[48:51], v[164:167]\n" \
"buffer_load_dwordx4 v60, s[40:43], s17 offen offset:0 sc1 lds\n" \
"v_mfma_f32_16x16x32_bf16 v[168:171], a[88:91], a[52:55], v[168:171]\n" \
"ds_read_b64_tr_b16 a[42:43], v10 offset:17920\n" \
"v_mfma_f32_16x16x32_bf16 v[172:175], a[92:95], a[52:55], v[172:175]\n" \
"ds_read_b64_tr_b16 a[44:45], v11 offset:17920\n" \
"v_mfma_f32_16x16x32_bf16 v[176:179], a[88:91], a[56:59], v[176:179]\n" \
"ds_read_b64_tr_b16 a[46:47], v12 offset:17920\n" \
"v_mfma_f32_16x16x32_bf16 v[180:183], a[92:95], a[56:59], v[180:183]\n" \
"buffer_load_dwordx4 v60, s[40:43], s17 offen offset:1024 sc1 lds\n" \
"v_mfma_f32_16x16x32_bf16 v[184:187], a[88:91], a[60:63], v[184:187]\n" \
"ds_read_b64_tr_b16 a[16:17], v9 offset:16384\n" \
"v_mfma_f32_16x16x32_bf16 v[188:191], a[92:95], a[60:63], v[188:191]\n" \
"ds_read_b64_tr_b16 a[18:19], v10 offset:16384\n" \
"v_mfma_f32_16x16x32_bf16 v[64:67], a[64:67], a[48:51], v[64:67]\n" \
"ds_read_b64_tr_b16 a[20:21], v11 offset:16384\n" \
"v_mfma_f32_16x16x32_bf16 v[68:71], a[68:71], a[48:51], v[68:71]\n" \
"buffer_load_dwordx4 v60, s[40:43], s17 offen offset:2048 sc1 lds\n" \
"v_mfma_f32_16x16x32_bf16 v[72:75], a[64:67], a[52:55], v[72:75]\n" \
"ds_read_b64_tr_b16 a[22:23], v12 offset:16384\n" \
"v_mfma_f32_16x16x32_bf16 v[76:79], a[68:71], a[52:55], v[76:79]\n" \
"v_mfma_f32_16x16x32_bf16 v[80:83], a[64:67], a[56:59], v[80:83]\n" \
"v_mfma_f32_16x16x32_bf16 v[84:87], a[68:71], a[56:59], v[84:87]\n" \
"buffer_load_dwordx4 v60, s[40:43], s17 offen offset:3072 sc1 lds\n" \
"v_mfma_f32_16x16x32_bf16 v[88:91], a[64:67], a[60:63], v[88:91]\n" \
"v_mfma_f32_16x16x32_bf16 v[92:95], a[68:71], a[60:63], v[92:95]\n" \
"s_waitcnt lgkmcnt(0)\n" \
"s_mov_b64 exec, 0\ns_cmp_eq_u32 %2, 0\ns_cbranch_scc1 LH15_%=\ns_cmp_eq_u32 %2, 1\ns_cbranch_scc1 LH16_%=\ns_cmp_eq_u32 %2, 2\ns_cbranch_scc1 LH17_%=\ns_cmp_eq_u32 %2, 3\ns_cbranch_scc1 LH18_%=\nLHR1_%=:\ns_mov_b64 exec, -1\n" \
"v_mfma_f32_16x16x32_bf16 v[64:67], a[16:19], a[0:3], v[64:67]\n" \
"ds_read_b128 a[48:51], v8 offset:0\n" \
"v_mfma_f32_16x16x32_bf16 v[68:71], a[20:23], a[0:3], v[68:71]\n" \
"ds_read_b128 a[52:55], v8 offset:2048\n" \
"v_mfma_f32_16x16x32_bf16 v[72:75], a[16:19], a[4:7], v[72:75]\n" \
"ds_read_b128 a[56:59], v8 offset:4096\n" \
"v_mfma_f32_16x16x32_bf16 v[76:79], a[20:23], a[4:7], v[76:79]\n" \
"ds_read_b128 a[60:63], v8 offset:6144\n" \
"v_mfma_f32_16x16x32_bf16 v[80:83], a[16:19], a[8:11], v[80:83]\n" \
"ds_read_b64_tr_b16 a[64:65], v9 offset:24576\n" \
"v_mfma_f32_16x16x32_bf16 v[84:87], a[20:23], a[8:11], v[84:87]\n" \
"ds_read_b64_tr_b16 a[66:67], v10 offset:24576\n" \
"v_mfma_f32_16x16x32_bf16 v[88:91], a[16:19], a[12:15], v[88:91]\n" \
"ds_read_b64_tr_b16 a[68:69], v11 offset:24576\n" \
"v_mfma_f32_16x16x32_bf16 v[92:95], a[20:23], a[12:15], v[92:95]\n" \
"ds_read_b64_tr_b16 a[70:71], v12 offset:24576\n" \
"v_mfma_f32_16x16x32_bf16 v[96:99], a[24:27], a[0:3], v[96:99]\n" \
"ds_read_b64_tr_b16 a[72:73], v9 offset:25088\n" \
"v_mfma_f32_16x16x32_bf16 v[100:103], a[28:31], a[0:3], v[100:103]\n" \
"ds_read_b64_tr_b16 a[74:75], v10 offset:25088\n" \
"v_mfma_f32_16x16x32_bf16 v[104:107], a[24:27], a[4:7], v[104:107]\n" \
"ds_read_b64_tr_b16 a[76:77], v11 offset:25088\n" \
"v_mfma_f32_16x16x32_bf16 v[108:111], a[28:31], a[4:7], v[108:111]\n" \
"ds_read_b64_tr_b16 a[78:79], v12 offset:25088\n" \
"v_mfma_f32_16x16x32_bf16 v[112:115], a[24:27], a[8:11], v[112:115]\n" \
"ds_read_b64_tr_b16 a[80:81], v9 offset:25600\n" \
"v_mfma_f32_16x16x32_bf16 v[116:119], a[28:31], a[8:11], v[116:119]\n" \
"ds_read_b64_tr_b16 a[82:83], v10 offset:25600\n" \
"v_mfma_f32_16x16x32_bf16 v[120:123], a[24:27], a[12:15], v[120:123]\n" \
"ds_read_b64_tr_b16 a[84:85], v11 offset:25600\n" \
"v_mfma_f32_16x16x32_bf16 v[124:127], a[28:31], a[12:15], v[124:127]\n" \
"ds_read_b64_tr_b16 a[86:87], v12 offset:25600\n" \
"s_waitcnt lgkmcnt(8)\n" \
"v_mfma_f32_16x16x32_bf16 v[64:67], a[64:67], a[48:51], v[64:67]\n" \
"ds_read_b64_tr_b16 a[88:89], v9 offset:26112\n" \
"v_mfma_f32_16x16x32_bf16 v[68:71], a[68:71], a[48:51], v[68:71]\n" \
"ds_read_b64_tr_b16 a[90:91], v10 offset:26112\n" \
"v_mfma_f32_16x16x32_bf16 v[72:75], a[64:67], a[52:55], v[72:75]\n" \
"ds_read_b64_tr_b16 a[92:93], v11 offset:26112\n" \
"v_mfma_f32_16x16x32_bf16 v[76:79], a[68:71], a[52:55], v[76:79]\n" \
"ds_read_b64_tr_b16 a[94:95], v12 offset:26112\n" \
"v_mfma_f32_16x16x32_bf16 v[80:83], a[64:67], a[56:59], v[80:83]\n" \
"v_cvt_pk_bf16_f32 v32, v64, v65\n" \
"v_cvt_pk_bf16_f32 v33, v66, v67\n" \
"v_mfma_f32_16x16x32_bf16 v[84:87], a[68:71], a[56:59], v[84:87]\n" \
"v_cvt_pk_bf16_f32 v34, v68, v69\n" \
"v_cvt_pk_bf16_f32 v35, v70, v71\n" \
"v_mfma_f32_16x16x32_bf16 v[88:91], a[64:67], a[60:63], v[88:91]\n" \
"v_cvt_pk_bf16_f32 v48, v72, v73\n" \
"v_mfma_f32_16x16x32_bf16 v[92:95], a[68:71], a[60:63], v[92:95]\n" \
"s_waitcnt vmcnt(16) lgkmcnt(0)\n" \
"v_cvt_pk_bf16_f32 v49, v74, v75\n" \
"v_cvt_pk_bf16_f32 v50, v76, v77\n" \
"v_mfma_f32_16x16x32_bf16 v[96:99], a[72:75], a[48:51], v[96:99]\n" \
"s_barrier\n" \
"v_cvt_pk_bf16_f32 v51, v78, v79\n" \
"v_cvt_pk_bf16_f32 v192, v80, v81\n" \
"v_mfma_f32_16x16x32_bf16 v[100:103], a[76:79], a[48:51], v[100:103]\n" \
"v_add_u32 v5, 0x2000, v4\n" \
"v_mov_b32 v6, v60\n" \
"v_mfma_f32_16x16x32_bf16 v[104:107], a[72:75], a[52:55], v[104:107]\n" \
"s_add_u32 s16, s16, s9\n" \
"v_add_u32 v7, 0x1000, v4\n" \
"s_add_u32 m0, s49, 0\n" \
"v_mfma_f32_16x16x32_bf16 v[108:111], a[76:79], a[52:55], v[108:111]\n" \
"buffer_load_dwordx4 v6, s[40:43], s16 offen offset:0 sc1 lds\n" \
"v_mfma_f32_16x16x32_bf16 v[112:115], a[72:75], a[56:59], v[112:115]\n" \
"ds_read_b128 a[128:131], v5 offset:32768\n" \
"v_cvt_pk_bf16_f32 v193, v82, v83\n" \
"v_mfma_f32_16x16x32_bf16 v[116:119], a[76:79], a[56:59], v[116:119]\n" \
"v_cvt_pk_bf16_f32 v194, v84, v85\n" \
"v_cvt_pk_bf16_f32 v195, v86, v87\n" \
"v_mfma_f32_16x16x32_bf16 v[120:123], a[72:75], a[60:63], v[120:123]\n" \
"v_cvt_pk_bf16_f32 v208, v88, v89\n" \
"v_cvt_pk_bf16_f32 v209, v90, v91\n" \
"v_mfma_f32_16x16x32_bf16 v[124:127], a[76:79], a[60:63], v[124:127]\n" \
"buffer_load_dwordx4 v6, s[40:43], s16 offen offset:1024 sc1 lds\n" \
"v_mfma_f32_16x16x32_bf16 v[128:131], a[32:35], a[0:3], v[128:131]\n" \
"ds_read_b128 a[132:135], v5 offset:33792\n" \
"v_cvt_pk_bf16_f32 v210, v92, v93\n" \
"v_mfma_f32_16x16x32_bf16 v[132:135], a[36:39], a[0:3], v[132:135]\n" \
"v_cvt_pk_bf16_f32 v211, v94, v95\n" \
"v_cvt_pk_bf16_f32 v36, v96, v97\n" \
"v_mfma_f32_16x16x32_bf16 v[136:139], a[32:35], a[4:7], v[136:139]\n" \
"v_cvt_pk_bf16_f32 v37, v98, v99\n" \
"v_cvt_pk_bf16_f32 v38, v100, v101\n" \
"v_mfma_f32_16x16x32_bf16 v[140:143], a[36:39], a[4:7], v[140:143]\n" \
"buffer_load_dwordx4 v6, s[40:43], s16 offen offset:2048 sc1 lds\n" \
"v_mfma_f32_16x16x32_bf16 v[144:147], a[32:35], a[8:11], v[144:147]\n" \
"ds_read_b128 a[136:139], v5 offset:34816\n" \
"v_cvt_pk_bf16_f32 v39, v102, v103\n" \
"v_mfma_f32_16x16x32_bf16 v[148:151], a[36:39], a[8:11], v[148:151]\n" \
"v_cvt_pk_bf16_f32 v52, v104, v105\n" \
"v_cvt_pk_bf16_f32 v53, v106, v107\n" \
"v_mfma_f32_16x16x32_bf16 v[152:155], a[32:35], a[12:15], v[152:155]\n" \
"v_cvt_pk_bf16_f32 v54, v108, v109\n" \
"v_cvt_pk_bf16_f32 v55, v110, v111\n" \
"v_mfma_f32_16x16x32_bf16 v[156:159], a[36:39], a[12:15], v[156:159]\n" \
"buffer_load_dwordx4 v6, s[40:43], s16 offen offset:3072 sc1 lds\n" \
"v_mfma_f32_16x16x32_bf16 v[128:131], a[80:83], a[48:51], v[128:131]\n" \
"s_add_u32 s17, s17, s19\n" \
"ds_read_b128 a[144:147], v5 offset:36864\n" \
"s_add_u32 m0, s49, 16384\n" \
"v_mfma_f32_16x16x32_bf16 v[132:135], a[84:87], a[48:51], v[132:135]\n" \
"v_cvt_pk_bf16_f32 v196, v112, v113\n" \
"v_cvt_pk_bf16_f32 v197, v114, v115\n" \
"v_mfma_f32_16x16x32_bf16 v[136:139], a[80:83], a[52:55], v[136:139]\n" \
"v_cvt_pk_bf16_f32 v198, v116, v117\n" \
"v_cvt_pk_bf16_f32 v199, v118, v119\n" \
"v_mfma_f32_16x16x32_bf16 v[140:143], a[84:87], a[52:55], v[140:143]\n" \
"buffer_load_dwordx4 v6, s[40:43], s17 offen offset:0 sc1 lds\n" \
"v_mfma_f32_16x16x32_bf16 v[144:147], a[80:83], a[56:59], v[144:147]\n" \
"ds_read_b128 a[140:143], v5 offset:35840\n" \
"v_cvt_pk_bf16_f32 v212, v120, v121\n" \
"v_mfma_f32_16x16x32_bf16 v[148:151], a[84:87], a[56:59], v[148:151]\n" \
"v_cvt_pk_bf16_f32 v213, v122, v123\n" \
"v_cvt_pk_bf16_f32 v214, v124, v125\n" \
"v_mfma_f32_16x16x32_bf16 v[152:155], a[80:83], a[60:63], v[152:155]\n" \
"v_cvt_pk_bf16_f32 v215, v126, v127\n" \
"v_cvt_pk_bf16_f32 v40, v128, v129\n" \
"v_mfma_f32_16x16x32_bf16 v[156:159], a[84:87], a[60:63], v[156:159]\n" \
"buffer_load_dwordx4 v6, s[40:43], s17 offen offset:1024 sc1 lds\n" \
"v_mfma_f32_16x16x32_bf16 v[160:163], a[40:43], a[0:3], v[160:163]\n" \
"ds_read_b128 a[148:151], v5 offset:37888\n" \
"v_cvt_pk_bf16_f32 v41, v130, v131\n" \
"v_mfma_f32_16x16x32_bf16 v[164:167], a[44:47], a[0:3], v[164:167]\n" \
"v_cvt_pk_bf16_f32 v42, v132, v133\n" \
"v_cvt_pk_bf16_f32 v43, v134, v135\n" \
"v_mfma_f32_16x16x32_bf16 v[168:171], a[40:43], a[4:7], v[168:171]\n" \
"v_cvt_pk_bf16_f32 v56, v136, v137\n" \
"v_cvt_pk_bf16_f32 v57, v138, v139\n" \
"v_mfma_f32_16x16x32_bf16 v[172:175], a[44:47], a[4:7], v[172:175]\n" \
"buffer_load_dwordx4 v6, s[40:43], s17 offen offset:2048 sc1 lds\n" \
"v_mfma_f32_16x16x32_bf16 v[176:179], a[40:43], a[8:11], v[176:179]\n" \
"ds_read_b128 a[152:155], v5 offset:38912\n" \
"v_cvt_pk_bf16_f32 v58, v140, v141\n" \
"v_mfma_f32_16x16x32_bf16 v[180:183], a[44:47], a[8:11], v[180:183]\n" \
"v_cvt_pk_bf16_f32 v59, v142, v143\n" \
"v_cvt_pk_bf16_f32 v200, v144, v145\n" \
"v_mfma_f32_16x16x32_bf16 v[184:187], a[40:43], a[12:15], v[184:187]\n" \
"v_cvt_pk_bf16_f32 v201, v146, v147\n" \
"v_cvt_pk_bf16_f32 v202, v148, v149\n" \
"v_mfma_f32_16x16x32_bf16 v[188:191], a[44:47], a[12:15], v[188:191]\n" \
"buffer_load_dwordx4 v6, s[40:43], s17 offen offset:3072 sc1 lds\n" \
"v_mfma_f32_16x16x32_bf16 v[160:163], a[88:91], a[48:51], v[160:163]\n" \
"ds_read_b128 a[156:159], v5 offset:39936\n" \
"v_cvt_pk_bf16_f32 v203, v150, v151\n" \
"v_mfma_f32_16x16x32_bf16 v[164:167], a[92:95], a[48:51], v[164:167]\n" \
"v_cvt_pk_bf16_f32 v216, v152, v153\n" \
"v_cvt_pk_bf16_f32 v217, v154, v155\n" \
"v_mfma_f32_16x16x32_bf16 v[168:171], a[88:91], a[52:55], v[168:171]\n" \
"v_cvt_pk_bf16_f32 v218, v156, v157\n" \
"v_cvt_pk_bf16_f32 v219, v158, v159\n" \
"v_mfma_f32_16x16x32_bf16 v[172:175], a[92:95], a[52:55], v[172:175]\n" \
"LH14_%=:\ns_cbranch_execz LHR0_%=\n" \
"s_cmp_ge_u32 %2, 2\ns_cselect_b64 vcc, -1, 0\n" \
"v_mfma_f32_16x16x32_bf16 v[176:179], a[88:91], a[56:59], v[176:179]\n" \
"v_cvt_pk_bf16_f32 v44, v160, v161\n" \
"v_cvt_pk_bf16_f32 v45, v162, v163\n" \
"v_mfma_f32_16x16x32_bf16 v[180:183], a[92:95], a[56:59], v[180:183]\n" \
"v_cvt_pk_bf16_f32 v46, v164, v165\n" \
"v_cvt_pk_bf16_f32 v47, v166, v167\n" \
"v_mfma_f32_16x16x32_bf16 v[184:187], a[88:91], a[60:63], v[184:187]\n" \
"v_xor_b32 v5, 0x10000, v5\n" \
"v_mfma_f32_16x16x32_bf16 v[188:191], a[92:95], a[60:63], v[188:191]\n" \
"s_waitcnt lgkmcnt(0)\n" \
"v_cvt_pk_bf16_f32 v60, v168, v169\n" \
"v_cvt_pk_bf16_f32 v61, v170, v171\n" \
"v_mfma_f32_16x16x32_bf16 v[64:67], a[128:131], v[32:35], v[24:27]\n" \
"v_cvt_pk_bf16_f32 v62, v172, v173\n" \
"v_cvt_pk_bf16_f32 v63, v174, v175\n" \
"v_mfma_f32_16x16x32_bf16 v[72:75], a[128:131], v[48:51], 0\n" \
"s_waitcnt vmcnt(16)\n" \
"v_cvt_pk_bf16_f32 v204, v176, v177\n" \
"v_mfma_f32_16x16x32_bf16 v[80:83], a[128:131], v[192:195], 0\n" \
"s_barrier\n" \
"v_cvt_pk_bf16_f32 v205, v178, v179\n" \
"v_cvt_pk_bf16_f32 v206, v180, v181\n" \
"v_mfma_f32_16x16x32_bf16 v[88:91], a[128:131], v[208:211], 0\n" \
"v_cvt_pk_bf16_f32 v207, v182, v183\n" \
"v_cvt_pk_bf16_f32 v220, v184, v185\n" \
"v_mfma_f32_16x16x32_bf16 v[64:67], a[132:135], v[36:39], v[64:67]\n" \
"v_cvt_pk_bf16_f32 v221, v186, v187\n" \
"v_mfma_f32_16x16x32_bf16 v[72:75], a[132:135], v[52:55], v[72:75]\n" \
"ds_read_b128 a[128:131], v5 offset:0\n" \
"v_cvt_pk_bf16_f32 v222, v188, v189\n" \
"v_mfma_f32_16x16x32_bf16 v[80:83], a[132:135], v[196:199], v[80:83]\n" \
"v_cvt_pk_bf16_f32 v223, v190, v191\n" \
"v_accvgpr_write_b32 a64, v192\n" \
"v_mfma_f32_16x16x32_bf16 v[88:91], a[132:135], v[212:215], v[88:91]\n" \
"s_cbranch_vccz LQW1_%=\nds_write_b128 v21, v[32:35] offset:16384\nLQW1_%=:\n" \
"v_mfma_f32_16x16x32_bf16 v[64:67], a[136:139], v[40:43], v[64:67]\n" \
"s_cbranch_vccz LQW2_%=\nds_write_b128 v21, v[48:51] offset:20480\nLQW2_%=:\n" \
"v_mfma_f32_16x16x32_bf16 v[72:75], a[136:139], v[56:59], v[72:75]\n" \
"ds_read_b128 a[132:135], v5 offset:1024\n" \
"v_accvgpr_write_b32 a65, v193\n" \
"v_mfma_f32_16x16x32_bf16 v[80:83], a[136:139], v[200:203], v[80:83]\n" \
"s_cbranch_vccz LQW3_%=\nds_write_b128 v21, v[192:195] offset:0\nLQW3_%=:\n" \
"v_mfma_f32_16x16x32_bf16 v[88:91], a[136:139], v[216:219], v[88:91]\n" \
"s_cbranch_vccz LQW4_%=\nds_write_b128 v21, v[208:211] offset:4096\nLQW4_%=:\n" \
"v_mfma_f32_16x16x32_bf16 v[68:71], a[144:147], v[32:35], v[244:247]\n" \
"s_cbranch_vccz LQW5_%=\nds_write_b128 v21, v[36:39] offset:17408\nLQW5_%=:\n" \
"v_mfma_f32_16x16x32_bf16 v[76:79], a[144:147], v[48:51], v[24:27]\n" \
"ds_read_b128 a[136:139], v5 offset:2048\n" \
"v_accvgpr_write_b32 a66, v194\n" \
"v_mfma_f32_16x16x32_bf16 v[84:87], a[144:147], v[192:195], 0\n" \
"s_cbranch_vccz LQW6_%=\nds_write_b128 v21, v[52:55] offset:21504\nLQW6_%=:\n" \
"v_mfma_f32_16x16x32_bf16 v[92:95], a[144:147], v[208:211], 0\n" \
"s_cbranch_vccz LQW7_%=\nds_write_b128 v21, v[196:199] offset:1024\nLQW7_%=:\n" \
"v_mfma_f32_16x16x32_bf16 v[64:67], a[140:143], v[44:47], v[64:67]\n" \
"s_cbranch_vccz LQW8_%=\nds_write_b128 v21, v[212:215] offset:5120\nLQW8_%=:\n" \
"v_mfma_f32_16x16x32_bf16 v[72:75], a[140:143], v[60:63], v[72:75]\n" \
"ds_read_b128 a[144:147], v5 offset:4096\n" \
"v_accvgpr_write_b32 a67, v195\n" \
"v_mfma_f32_16x16x32_bf16 v[80:83], a[140:143], v[204:207], v[80:83]\n" \
"s_cbranch_vccz LQW9_%=\nds_write_b128 v21, v[40:43] offset:18432\nLQW9_%=:\n" \
"v_mfma_f32_16x16x32_bf16 v[88:91], a[140:143], v[220:223], v[88:91]\n" \
"s_cbranch_vccz LQW10_%=\nds_write_b128 v21, v[56:59] offset:22528\nLQW10_%=:\n" \
"v_mfma_f32_16x16x32_bf16 v[68:71], a[148:151], v[36:39], v[68:71]\n" \
"s_cbranch_vccz LQW11_%=\nds_write_b128 v21, v[200:203] offset:2048\nLQW11_%=:\n" \
"v_mfma_f32_16x16x32_bf16 v[76:79], a[148:151], v[52:55], v[76:79]\n" \
"ds_read_b128 a[140:143], v5 offset:3072\n" \
"v_accvgpr_write_b32 a80, v208\n" \
"v_mfma_f32_16x16x32_bf16 v[84:87], a[148:151], v[196:199], v[84:87]\n" \
"s_cbranch_vccz LQW12_%=\nds_write_b128 v21, v[216:219] offset:6144\nLQW12_%=:\n" \
"v_mfma_f32_16x16x32_bf16 v[92:95], a[148:151], v[212:215], v[92:95]\n" \
"s_cbranch_vccz LQW13_%=\nds_write_b128 v21, v[44:47] offset:19456\nLQW13_%=:\n" \
"v_mfma_f32_16x16x32_bf16 v[68:71], a[152:155], v[40:43], v[68:71]\n" \
"s_cbranch_vccz LQW14_%=\nds_write_b128 v21, v[60:63] offset:23552\nLQW14_%=:\n" \
"v_mfma_f32_16x16x32_bf16 v[76:79], a[152:155], v[56:59], v[76:79]\n" \
"ds_read_b128 a[148:151], v5 offset:5120\n" \
"v_accvgpr_write_b32 a81, v209\n" \
"v_mfma_f32_16x16x32_bf16 v[84:87], a[152:155], v[200:203], v[84:87]\n" \
"s_cbranch_vccz LQW15_%=\nds_write_b128 v21, v[204:207] offset:3072\nLQW15_%=:\n" \
"v_mfma_f32_16x16x32_bf16 v[92:95], a[152:155], v[216:219], v[92:95]\n" \
"s_cbranch_vccz LQW16_%=\nds_write_b128 v21, v[220:223] offset:7168\nLQW16_%=:\n" \
"v_exp_f32 v64, v64\n" \
"v_mfma_f32_16x16x32_bf16 v[68:71], a[156:159], v[44:47], v[68:71]\n" \
"v_exp_f32 v65, v65\n" \
"v_mfma_f32_16x16x32_bf16 v[76:79], a[156:159], v[60:63], v[76:79]\n" \
"v_exp_f32 v66, v66\n" \
"v_mfma_f32_16x16x32_bf16 v[84:87], a[156:159], v[204:207], v[84:87]\n" \
"v_exp_f32 v67, v67\n" \
"v_mfma_f32_16x16x32_bf16 v[92:95], a[156:159], v[220:223], v[92:95]\n" \
"s_waitcnt lgkmcnt(0)\n" \
"v_exp_f32 v72, v72\n" \
"v_mfma_f32_16x16x32_bf16 v[96:99], a[128:131], v[32:35], v[244:247]\n" \
"v_exp_f32 v73, v73\n" \
"v_mfma_f32_16x16x32_bf16 v[104:107], a[128:131], v[48:51], v[244:247]\n" \
"s_add_u32 s16, s16, s9\n" \
"v_exp_f32 v74, v74\n" \
"v_mfma_f32_16x16x32_bf16 v[112:115], a[128:131], v[192:195], v[24:27]\n" \
"ds_read_b128 a[152:155], v5 offset:6144\n" \
"v_cvt_pk_bf16_f32 v128, v64, v65\n" \
"v_mfma_f32_16x16x32_bf16 v[120:123], a[128:131], v[208:211], 0\n" \
"s_add_u32 m0, s49, 32768\n" \
"v_exp_f32 v75, v75\n" \
"v_mfma_f32_16x16x32_bf16 v[96:99], a[132:135], v[36:39], v[96:99]\n" \
"buffer_load_dwordx4 v6, s[40:43], s16 offen offset:0 sc1 lds\n" \
"v_mfma_f32_16x16x32_bf16 v[104:107], a[132:135], v[52:55], v[104:107]\n" \
"v_exp_f32 v80, v80\n" \
"v_mfma_f32_16x16x32_bf16 v[112:115], a[132:135], v[196:199], v[112:115]\n" \
"ds_read_b128 a[156:159], v5 offset:7168\n" \
"v_exp_f32 v81, v81\n" \
"v_mfma_f32_16x16x32_bf16 v[120:123], a[132:135], v[212:215], v[120:123]\n" \
"s_waitcnt lgkmcnt(4)\n" \
"v_exp_f32 v82, v82\n" \
"v_mfma_f32_16x16x32_bf16 v[96:99], a[136:139], v[40:43], v[96:99]\n" \
"v_exp_f32 v83, v83\n" \
"v_mfma_f32_16x16x32_bf16 v[104:107], a[136:139], v[56:59], v[104:107]\n" \
"v_exp_f32 v88, v88\n" \
"v_mfma_f32_16x16x32_bf16 v[112:115], a[136:139], v[200:203], v[112:115]\n" \
"ds_read_b128 a[128:131], v5 offset:16384\n" \
"v_cvt_pk_bf16_f32 v129, v66, v67\n" \
"v_mfma_f32_16x16x32_bf16 v[120:123], a[136:139], v[216:219], v[120:123]\n" \
"v_exp_f32 v89, v89\n" \
"v_mfma_f32_16x16x32_bf16 v[96:99], a[140:143], v[44:47], v[96:99]\n" \
"buffer_load_dwordx4 v6, s[40:43], s16 offen offset:1024 sc1 lds\n" \
"v_exp_f32 v90, v90\n" \
"v_mfma_f32_16x16x32_bf16 v[104:107], a[140:143], v[60:63], v[104:107]\n" \
"v_exp_f32 v91, v91\n" \
"v_mfma_f32_16x16x32_bf16 v[112:115], a[140:143], v[204:207], v[112:115]\n" \
"ds_read_b128 a[132:135], v5 offset:17408\n" \
"v_exp_f32 v68, v68\n" \
"v_mfma_f32_16x16x32_bf16 v[120:123], a[140:143], v[220:223], v[120:123]\n" \
"s_waitcnt lgkmcnt(4)\n" \
"v_exp_f32 v69, v69\n" \
"v_mfma_f32_16x16x32_bf16 v[100:103], a[144:147], v[32:35], v[244:247]\n" \
"v_exp_f32 v70, v70\n" \
"v_mfma_f32_16x16x32_bf16 v[108:111], a[144:147], v[48:51], v[244:247]\n" \
"v_exp_f32 v71, v71\n" \
"v_mfma_f32_16x16x32_bf16 v[116:119], a[144:147], v[192:195], v[244:247]\n" \
"ds_read_b128 a[136:139], v5 offset:18432\n" \
"v_cvt_pk_bf16_f32 v132, v72, v73\n" \
"v_mfma_f32_16x16x32_bf16 v[124:127], a[144:147], v[208:211], v[24:27]\n" \
"v_exp_f32 v76, v76\n" \
"v_mfma_f32_16x16x32_bf16 v[100:103], a[148:151], v[36:39], v[100:103]\n" \
"buffer_load_dwordx4 v6, s[40:43], s16 offen offset:2048 sc1 lds\n" \
"v_exp_f32 v77, v77\n" \
"v_mfma_f32_16x16x32_bf16 v[108:111], a[148:151], v[52:55], v[108:111]\n" \
"v_exp_f32 v78, v78\n" \
"v_accvgpr_write_b32 a82, v210\n" \
"v_accvgpr_write_b32 a83, v211\n" \
"v_mfma_f32_16x16x32_bf16 v[116:119], a[148:151], v[196:199], v[116:119]\n" \
"ds_read_b128 a[140:143], v5 offset:19456\n" \
"v_cvt_pk_bf16_f32 v133, v74, v75\n" \
"v_accvgpr_write_b32 a68, v196\n" \
"v_accvgpr_write_b32 a69, v197\n" \
"v_mfma_f32_16x16x32_bf16 v[124:127], a[148:151], v[212:215], v[124:127]\n" \
"s_waitcnt lgkmcnt(4)\n" \
"v_exp_f32 v79, v79\n" \
"v_accvgpr_write_b32 a70, v198\n" \
"v_accvgpr_write_b32 a71, v199\n" \
"v_mfma_f32_16x16x32_bf16 v[100:103], a[152:155], v[40:43], v[100:103]\n" \
"v_exp_f32 v84, v84\n" \
"v_accvgpr_write_b32 a84, v212\n" \
"v_accvgpr_write_b32 a85, v213\n" \
"v_mfma_f32_16x16x32_bf16 v[108:111], a[152:155], v[56:59], v[108:111]\n" \
"v_exp_f32 v85, v85\n" \
"v_accvgpr_write_b32 a86, v214\n" \
"v_accvgpr_write_b32 a87, v215\n" \
"v_mfma_f32_16x16x32_bf16 v[116:119], a[152:155], v[200:203], v[116:119]\n" \
"ds_read_b128 a[144:147], v5 offset:20480\n" \
"v_exp_f32 v86, v86\n" \
"v_accvgpr_write_b32 a72, v200\n" \
"v_accvgpr_write_b32 a73, v201\n" \
"v_mfma_f32_16x16x32_bf16 v[124:127], a[152:155], v[216:219], v[124:127]\n" \
"v_exp_f32 v87, v87\n" \
"v_accvgpr_write_b32 a74, v202\n" \
"v_accvgpr_write_b32 a75, v203\n" \
"v_mov_b32 v180, 0x3f803f80\n" \
"v_mov_b32 v181, 0x3f803f80\n" \
"v_mov_b32 v182, 0x3f803f80\n" \
"v_mov_b32 v183, 0x3f803f80\n" \
"v_mfma_f32_16x16x32_bf16 v[100:103], a[156:159], v[44:47], v[100:103]\n" \
"buffer_load_dwordx4 v6, s[40:43], s16 offen offset:3072 sc1 lds\n" \
"v_accvgpr_write_b32 a88, v216\n" \
"v_accvgpr_write_b32 a89, v217\n" \
"v_mfma_f32_16x16x32_bf16 v[108:111], a[156:159], v[60:63], v[108:111]\n" \
"v_exp_f32 v92, v92\n" \
"v_accvgpr_write_b32 a90, v218\n" \
"v_accvgpr_write_b32 a91, v219\n" \
"v_mov_b32 v176, v240\n" \
"v_mov_b32 v177, v241\n" \
"v_mov_b32 v178, v242\n" \
"v_mov_b32 v179, v243\n" \
"v_mfma_f32_16x16x32_bf16 v[116:119], a[156:159], v[204:207], v[116:119]\n" \
"ds_read_b128 a[148:151], v5 offset:21504\n" \
"v_exp_f32 v93, v93\n" \
"v_cvt_pk_bf16_f32 v136, v80, v81\n" \
"v_cvt_pk_bf16_f32 v137, v82, v83\n" \
"v_cvt_pk_bf16_f32 v140, v88, v89\n" \
"v_cvt_pk_bf16_f32 v141, v90, v91\n" \
"v_cvt_pk_bf16_f32 v130, v68, v69\n" \
"v_cvt_pk_bf16_f32 v131, v70, v71\n" \
"v_accvgpr_write_b32 a76, v204\n" \
"v_accvgpr_write_b32 a77, v205\n" \
"v_mfma_f32_16x16x32_bf16 v[124:127], a[156:159], v[220:223], v[124:127]\n" \
"s_waitcnt lgkmcnt(4)\n" \
"v_exp_f32 v94, v94\n" \
"v_cvt_pk_bf16_f32 v134, v76, v77\n" \
"v_cvt_pk_bf16_f32 v135, v78, v79\n" \
"v_accvgpr_write_b32 a78, v206\n" \
"v_accvgpr_write_b32 a79, v207\n" \
"v_mfma_f32_16x16x32_bf16 v[160:163], v[180:183], v[128:131], 0\n" \
"v_exp_f32 v95, v95\n" \
"v_cvt_pk_bf16_f32 v138, v84, v85\n" \
"v_cvt_pk_bf16_f32 v139, v86, v87\n" \
"v_accvgpr_write_b32 a92, v220\n" \
"v_accvgpr_write_b32 a93, v221\n" \
"v_mfma_f32_16x16x32_bf16 v[164:167], v[180:183], v[132:135], 0\n" \
"v_cvt_pk_bf16_f32 v142, v92, v93\n" \
"v_cvt_pk_bf16_f32 v143, v94, v95\n" \
"v_accvgpr_write_b32 a94, v222\n" \
"v_accvgpr_write_b32 a95, v223\n" \
"v_mfma_f32_16x16x32_bf16 v[168:171], v[180:183], v[136:139], 0\n" \
"v_exp_f32 v96, v96\n" \
"v_mfma_f32_16x16x32_bf16 v[172:175], v[180:183], v[140:143], 0\n" \
"v_lshrrev_b32 v9, 5, v4\n" \
"v_lshlrev_b32 v9, 27, v9\n" \
"v_mfma_f32_16x16x32_bf16 v[192:195], a[128:131], v[128:131], 0\n" \
"v_exp_f32 v97, v97\n" \
"v_mfma_f32_16x16x32_bf16 v[224:227], a[128:131], v[132:135], 0\n" \
"v_cvt_pk_bf16_f32 v144, v96, v97\n" \
"v_lshl_add_u32 v9, v4, 4, v9\n" \
"v_mfma_f32_16x16x32_bf16 a[0:3], a[128:131], v[136:139], 0\n" \
"ds_read_b128 a[152:155], v5 offset:22528\n" \
"v_mov_b32 v10, s14\n" \
"v_mfma_f32_16x16x32_bf16 a[32:35], a[128:131], v[140:143], 0\n" \
"v_exp_f32 v98, v98\n" \
"v_mfma_f32_16x16x32_bf16 v[196:199], a[132:135], v[128:131], 0\n" \
"v_exp_f32 v99, v99\n" \
"v_mfma_f32_16x16x32_bf16 v[228:231], a[132:135], v[132:135], 0\n" \
"v_cvt_pk_bf16_f32 v145, v98, v99\n" \
"v_mov_b32 v11, s15\n" \
"v_mfma_f32_16x16x32_bf16 a[4:7], a[132:135], v[136:139], 0\n" \
"ds_read_b128 a[156:159], v5 offset:23552\n" \
"s_waitcnt lgkmcnt(10)\n" \
"v_mfma_f32_16x16x32_bf16 a[36:39], a[132:135], v[140:143], 0\n" \
"s_waitcnt vmcnt(12) lgkmcnt(4)\n" \
"v_exp_f32 v100, v100\n" \
"v_mfma_f32_16x16x32_bf16 v[200:203], a[136:139], v[128:131], 0\n" \
"s_barrier\n" \
"v_exp_f32 v101, v101\n" \
"v_mfma_f32_16x16x32_bf16 v[232:235], a[136:139], v[132:135], 0\n" \
"s_add_u32 s17, s17, s19\n" \
"v_cvt_pk_bf16_f32 v146, v100, v101\n" \
"v_mfma_f32_16x16x32_bf16 a[8:11], a[136:139], v[136:139], 0\n" \
"ds_read_b128 a[128:131], v5 offset:32768\n" \
"v_mfma_f32_16x16x32_bf16 a[40:43], a[136:139], v[140:143], 0\n" \
"s_add_u32 m0, s49, 49152\n" \
"v_exp_f32 v102, v102\n" \
"v_mfma_f32_16x16x32_bf16 v[204:207], a[140:143], v[128:131], 0\n" \
"buffer_load_dwordx4 v6, s[40:43], s17 offen offset:0 sc1 lds\n" \
"v_mfma_f32_16x16x32_bf16 v[236:239], a[140:143], v[132:135], 0\n" \
"v_exp_f32 v103, v103\n" \
"v_mfma_f32_16x16x32_bf16 a[12:15], a[140:143], v[136:139], 0\n" \
"ds_read_b128 a[132:135], v5 offset:33792\n" \
"v_cvt_pk_bf16_f32 v147, v102, v103\n" \
"v_mfma_f32_16x16x32_bf16 a[44:47], a[140:143], v[140:143], 0\n" \
"s_waitcnt lgkmcnt(4)\n" \
"v_exp_f32 v104, v104\n" \
"v_mfma_f32_16x16x32_bf16 v[208:211], a[144:147], v[128:131], 0\n" \
"v_exp_f32 v105, v105\n" \
"v_mfma_f32_16x16x32_bf16 v[240:243], a[144:147], v[132:135], 0\n" \
"v_cvt_pk_bf16_f32 v148, v104, v105\n" \
"v_mfma_f32_16x16x32_bf16 a[16:19], a[144:147], v[136:139], 0\n" \
"ds_read_b128 a[136:139], v5 offset:34816\n" \
"v_mfma_f32_16x16x32_bf16 a[48:51], a[144:147], v[140:143], 0\n" \
"v_exp_f32 v106, v106\n" \
"v_mfma_f32_16x16x32_bf16 v[212:215], a[148:151], v[128:131], 0\n" \
"buffer_load_dwordx4 v6, s[40:43], s17 offen offset:1024 sc1 lds\n" \
"v_mfma_f32_16x16x32_bf16 v[244:247], a[148:151], v[132:135], 0\n" \
"v_exp_f32 v107, v107\n" \
"v_mfma_f32_16x16x32_bf16 a[20:23], a[148:151], v[136:139], 0\n" \
"ds_read_b128 a[140:143], v5 offset:35840\n" \
"v_cvt_pk_bf16_f32 v149, v106, v107\n" \
"v_mfma_f32_16x16x32_bf16 a[52:55], a[148:151], v[140:143], 0\n" \
"s_waitcnt lgkmcnt(4)\n" \
"v_exp_f32 v108, v108\n" \
"v_mfma_f32_16x16x32_bf16 v[216:219], a[152:155], v[128:131], 0\n" \
"buffer_load_dwordx4 v6, s[40:43], s17 offen offset:2048 sc1 lds\n" \
"v_mfma_f32_16x16x32_bf16 v[248:251], a[152:155], v[132:135], 0\n" \
"v_exp_f32 v109, v109\n" \
"v_mfma_f32_16x16x32_bf16 a[24:27], a[152:155], v[136:139], 0\n" \
"ds_read_b128 a[144:147], v5 offset:36864\n" \
"v_cvt_pk_bf16_f32 v150, v108, v109\n" \
"v_mfma_f32_16x16x32_bf16 a[56:59], a[152:155], v[140:143], 0\n" \
"v_exp_f32 v110, v110\n" \
"v_mfma_f32_16x16x32_bf16 v[220:223], a[156:159], v[128:131], 0\n" \
"buffer_load_dwordx4 v6, s[40:43], s17 offen offset:3072 sc1 lds\n" \
"v_mfma_f32_16x16x32_bf16 v[252:255], a[156:159], v[132:135], 0\n" \
"v_exp_f32 v111, v111\n" \
"v_mfma_f32_16x16x32_bf16 a[28:31], a[156:159], v[136:139], 0\n" \
"ds_read_b128 a[148:151], v5 offset:37888\n" \
"v_cvt_pk_bf16_f32 v151, v110, v111\n" \
"v_mfma_f32_16x16x32_bf16 a[60:63], a[156:159], v[140:143], 0\n" \
"s_waitcnt lgkmcnt(4)\n" \
"s_cmp_lt_i32 s46, 0\n" \
"s_cbranch_scc1 LAD2_%=\n" \
"LCHK_%=:\n" \
"s_cmp_eq_u32 s44, s46\n" \
"s_cbranch_scc1 LMID_%=\n" \
".p2align 6\n" \
"LODD_%=:\n" \
"v_mfma_f32_16x16x32_bf16 v[64:67], a[128:131], v[32:35], 0\n" \
"v_exp_f32 v112, v112\n" \
"v_mfma_f32_16x16x32_bf16 v[72:75], a[128:131], v[48:51], 0\n" \
"s_add_u32 s16, s16, s9\n" \
"v_mfma_f32_16x16x32_bf16 v[80:83], a[128:131], a[64:67], 0\n" \
"ds_read_b128 a[152:155], v5 offset:38912\n" \
"v_mfma_f32_16x16x32_bf16 v[88:91], a[128:131], a[80:83], 0\n" \
"s_add_u32 m0, s48, 0\n" \
"v_exp_f32 v113, v113\n" \
"v_mfma_f32_16x16x32_bf16 v[64:67], a[132:135], v[36:39], v[64:67]\n" \
"buffer_load_dwordx4 v6, s[40:43], s16 offen offset:0 sc1 lds\n" \
"v_mfma_f32_16x16x32_bf16 v[72:75], a[132:135], v[52:55], v[72:75]\n" \
"v_exp_f32 v114, v114\n" \
"v_mfma_f32_16x16x32_bf16 v[80:83], a[132:135], a[68:71], v[80:83]\n" \
"ds_read_b128 a[156:159], v5 offset:39936\n" \
"v_cvt_pk_bf16_f32 v152, v112, v113\n" \
"v_mfma_f32_16x16x32_bf16 v[88:91], a[132:135], a[84:87], v[88:91]\n" \
"s_waitcnt lgkmcnt(4)\n" \
"v_exp_f32 v115, v115\n" \
"v_mfma_f32_16x16x32_bf16 v[64:67], a[136:139], v[40:43], v[64:67]\n" \
"v_exp_f32 v116, v116\n" \
"v_mfma_f32_16x16x32_bf16 v[72:75], a[136:139], v[56:59], v[72:75]\n" \
"s_xor_b32 s49, s49, 0x10000\n" \
"v_cvt_pk_bf16_f32 v153, v114, v115\n" \
"v_mfma_f32_16x16x32_bf16 v[80:83], a[136:139], a[72:75], v[80:83]\n" \
"ds_read_b128 a[128:131], v5 offset:49152\n" \
"v_mfma_f32_16x16x32_bf16 v[88:91], a[136:139], a[88:91], v[88:91]\n" \
"v_exp_f32 v117, v117\n" \
"v_mfma_f32_16x16x32_bf16 v[64:67], a[140:143], v[44:47], v[64:67]\n" \
"buffer_load_dwordx4 v6, s[40:43], s16 offen offset:1024 sc1 lds\n" \
"v_mfma_f32_16x16x32_bf16 v[72:75], a[140:143], v[60:63], v[72:75]\n" \
"v_exp_f32 v118, v118\n" \
"v_mfma_f32_16x16x32_bf16 v[80:83], a[140:143], a[76:79], v[80:83]\n" \
"ds_read_b128 a[132:135], v5 offset:50176\n" \
"v_cvt_pk_bf16_f32 v154, v116, v117\n" \
"v_mfma_f32_16x16x32_bf16 v[88:91], a[140:143], a[92:95], v[88:91]\n" \
"s_waitcnt lgkmcnt(4)\n" \
"v_exp_f32 v119, v119\n" \
"v_mfma_f32_16x16x32_bf16 v[68:71], a[144:147], v[32:35], 0\n" \
"v_exp_f32 v120, v120\n" \
"v_mfma_f32_16x16x32_bf16 v[76:79], a[144:147], v[48:51], 0\n" \
"v_cvt_pk_bf16_f32 v155, v118, v119\n" \
"v_mfma_f32_16x16x32_bf16 v[84:87], a[144:147], a[64:67], 0\n" \
"ds_read_b128 a[136:139], v5 offset:51200\n" \
"v_mfma_f32_16x16x32_bf16 v[92:95], a[144:147], a[80:83], 0\n" \
"v_exp_f32 v121, v121\n" \
"v_mfma_f32_16x16x32_bf16 v[68:71], a[148:151], v[36:39], v[68:71]\n" \
"buffer_load_dwordx4 v6, s[40:43], s16 offen offset:2048 sc1 lds\n" \
"v_mfma_f32_16x16x32_bf16 v[76:79], a[148:151], v[52:55], v[76:79]\n" \
"v_exp_f32 v122, v122\n" \
"v_mfma_f32_16x16x32_bf16 v[84:87], a[148:151], a[68:71], v[84:87]\n" \
"ds_read_b128 a[140:143], v5 offset:52224\n" \
"v_cvt_pk_bf16_f32 v156, v120, v121\n" \
"v_mfma_f32_16x16x32_bf16 v[92:95], a[148:151], a[84:87], v[92:95]\n" \
"s_waitcnt lgkmcnt(4)\n" \
"v_exp_f32 v123, v123\n" \
"v_mfma_f32_16x16x32_bf16 v[68:71], a[152:155], v[40:43], v[68:71]\n" \
"v_exp_f32 v124, v124\n" \
"v_mfma_f32_16x16x32_bf16 v[76:79], a[152:155], v[56:59], v[76:79]\n" \
"v_cvt_pk_bf16_f32 v157, v122, v123\n" \
"v_mfma_f32_16x16x32_bf16 v[84:87], a[152:155], a[72:75], v[84:87]\n" \
"ds_read_b128 a[144:147], v5 offset:53248\n" \
"v_mfma_f32_16x16x32_bf16 v[92:95], a[152:155], a[88:91], v[92:95]\n" \
"v_exp_f32 v125, v125\n" \
"v_mfma_f32_16x16x32_bf16 v[68:71], a[156:159], v[44:47], v[68:71]\n" \
"buffer_load_dwordx4 v6, s[40:43], s16 offen offset:3072 sc1 lds\n" \
"v_mfma_f32_16x16x32_bf16 v[76:79], a[156:159], v[60:63], v[76:79]\n" \
"v_exp_f32 v126, v126\n" \
"v_mfma_f32_16x16x32_bf16 v[84:87], a[156:159], a[76:79], v[84:87]\n" \
"ds_read_b128 a[148:151], v5 offset:54272\n" \
"v_cvt_pk_bf16_f32 v158, v124, v125\n" \
"v_mfma_f32_16x16x32_bf16 v[92:95], a[156:159], a[92:95], v[92:95]\n" \
"s_waitcnt lgkmcnt(4)\n" \
"v_exp_f32 v127, v127\n" \
"v_mfma_f32_16x16x32_bf16 v[160:163], v[180:183], v[144:147], v[160:163]\n" \
"v_cvt_pk_bf16_f32 v159, v126, v127\n" \
"v_mfma_f32_16x16x32_bf16 v[164:167], v[180:183], v[148:151], v[164:167]\n" \
"v_mfma_f32_16x16x32_bf16 v[168:171], v[180:183], v[152:155], v[168:171]\n" \
"v_exp_f32 v64, v64\n" \
"v_mfma_f32_16x16x32_bf16 v[172:175], v[180:183], v[156:159], v[172:175]\n" \
"v_mfma_f32_16x16x32_bf16 v[192:195], a[128:131], v[144:147], v[192:195]\n" \
"v_exp_f32 v65, v65\n" \
"v_mfma_f32_16x16x32_bf16 v[224:227], a[128:131], v[148:151], v[224:227]\n" \
"v_cvt_pk_bf16_f32 v128, v64, v65\n" \
"v_mfma_f32_16x16x32_bf16 a[0:3], a[128:131], v[152:155], a[0:3]\n" \
"ds_read_b128 a[152:155], v5 offset:55296\n" \
"v_mfma_f32_16x16x32_bf16 a[32:35], a[128:131], v[156:159], a[32:35]\n" \
"v_exp_f32 v66, v66\n" \
"v_mfma_f32_16x16x32_bf16 v[196:199], a[132:135], v[144:147], v[196:199]\n" \
"v_exp_f32 v67, v67\n" \
"v_mfma_f32_16x16x32_bf16 v[228:231], a[132:135], v[148:151], v[228:231]\n" \
"v_cvt_pk_bf16_f32 v129, v66, v67\n" \
"v_mfma_f32_16x16x32_bf16 a[4:7], a[132:135], v[152:155], a[4:7]\n" \
"ds_read_b128 a[156:159], v5 offset:56320\n" \
"v_mfma_f32_16x16x32_bf16 a[36:39], a[132:135], v[156:159], a[36:39]\n" \
"s_waitcnt vmcnt(12) lgkmcnt(4)\n" \
"v_exp_f32 v68, v68\n" \
"v_mfma_f32_16x16x32_bf16 v[200:203], a[136:139], v[144:147], v[200:203]\n" \
"s_barrier\n" \
"v_xor_b32 v5, 0x10000, v5\n" \
"v_mfma_f32_16x16x32_bf16 v[232:235], a[136:139], v[148:151], v[232:235]\n" \
"s_add_u32 s17, s17, s19\n" \
"v_exp_f32 v69, v69\n" \
"v_mfma_f32_16x16x32_bf16 a[8:11], a[136:139], v[152:155], a[8:11]\n" \
"ds_read_b128 a[128:131], v5 offset:0\n" \
"v_cvt_pk_bf16_f32 v130, v68, v69\n" \
"v_mfma_f32_16x16x32_bf16 a[40:43], a[136:139], v[156:159], a[40:43]\n" \
"s_add_u32 m0, s48, 16384\n" \
"v_exp_f32 v70, v70\n" \
"v_mfma_f32_16x16x32_bf16 v[204:207], a[140:143], v[144:147], v[204:207]\n" \
"buffer_load_dwordx4 v6, s[40:43], s17 offen offset:0 sc1 lds\n" \
"v_mfma_f32_16x16x32_bf16 v[236:239], a[140:143], v[148:151], v[236:239]\n" \
"v_exp_f32 v71, v71\n" \
"v_mfma_f32_16x16x32_bf16 a[12:15], a[140:143], v[152:155], a[12:15]\n" \
"ds_read_b128 a[132:135], v5 offset:1024\n" \
"v_cvt_pk_bf16_f32 v131, v70, v71\n" \
"v_mfma_f32_16x16x32_bf16 a[44:47], a[140:143], v[156:159], a[44:47]\n" \
"s_waitcnt lgkmcnt(4)\n" \
"v_exp_f32 v72, v72\n" \
"v_mfma_f32_16x16x32_bf16 v[208:211], a[144:147], v[144:147], v[208:211]\n" \
"v_exp_f32 v73, v73\n" \
"v_mfma_f32_16x16x32_bf16 v[240:243], a[144:147], v[148:151], v[240:243]\n" \
"v_cvt_pk_bf16_f32 v132, v72, v73\n" \
"v_mfma_f32_16x16x32_bf16 a[16:19], a[144:147], v[152:155], a[16:19]\n" \
"ds_read_b128 a[136:139], v5 offset:2048\n" \
"v_mfma_f32_16x16x32_bf16 a[48:51], a[144:147], v[156:159], a[48:51]\n" \
"v_exp_f32 v74, v74\n" \
"v_mfma_f32_16x16x32_bf16 v[212:215], a[148:151], v[144:147], v[212:215]\n" \
"buffer_load_dwordx4 v6, s[40:43], s17 offen offset:1024 sc1 lds\n" \
"v_mfma_f32_16x16x32_bf16 v[244:247], a[148:151], v[148:151], v[244:247]\n" \
"v_exp_f32 v75, v75\n" \
"v_mfma_f32_16x16x32_bf16 a[20:23], a[148:151], v[152:155], a[20:23]\n" \
"ds_read_b128 a[140:143], v5 offset:3072\n" \
"v_cvt_pk_bf16_f32 v133, v74, v75\n" \
"v_mfma_f32_16x16x32_bf16 a[52:55], a[148:151], v[156:159], a[52:55]\n" \
"s_waitcnt lgkmcnt(4)\n" \
"v_exp_f32 v76, v76\n" \
"v_mfma_f32_16x16x32_bf16 v[216:219], a[152:155], v[144:147], v[216:219]\n" \
"buffer_load_dwordx4 v6, s[40:43], s17 offen offset:2048 sc1 lds\n" \
"v_mfma_f32_16x16x32_bf16 v[248:251], a[152:155], v[148:151], v[248:251]\n" \
"s_add_u32 s44, s44, 1\n" \
"v_exp_f32 v77, v77\n" \
"v_mfma_f32_16x16x32_bf16 a[24:27], a[152:155], v[152:155], a[24:27]\n" \
"ds_read_b128 a[144:147], v5 offset:4096\n" \
"v_cvt_pk_bf16_f32 v134, v76, v77\n" \
"v_mfma_f32_16x16x32_bf16 a[56:59], a[152:155], v[156:159], a[56:59]\n" \
"v_exp_f32 v78, v78\n" \
"v_mfma_f32_16x16x32_bf16 v[220:223], a[156:159], v[144:147], v[220:223]\n" \
"buffer_load_dwordx4 v6, s[40:43], s17 offen offset:3072 sc1 lds\n" \
"v_mfma_f32_16x16x32_bf16 v[252:255], a[156:159], v[148:151], v[252:255]\n" \
"s_xor_b32 s48, s48, 0x10000\n" \
"v_exp_f32 v79, v79\n" \
"v_mfma_f32_16x16x32_bf16 a[28:31], a[156:159], v[152:155], a[28:31]\n" \
"ds_read_b128 a[148:151], v5 offset:5120\n" \
"v_cvt_pk_bf16_f32 v135, v78, v79\n" \
"v_mfma_f32_16x16x32_bf16 a[60:63], a[156:159], v[156:159], a[60:63]\n" \
"s_waitcnt lgkmcnt(4)\n" \
"LBOT_%=:\n" \
"s_cmp_ge_u32 s44, 14\n" \
"s_cbranch_scc1 LDISP_%=\n" \
"LEVEN_%=:\n" \
"v_mfma_f32_16x16x32_bf16 v[96:99], a[128:131], v[32:35], 0\n" \
"v_exp_f32 v80, v80\n" \
"v_mfma_f32_16x16x32_bf16 v[104:107], a[128:131], v[48:51], 0\n" \
"s_add_u32 s16, s16, s9\n" \
"v_mfma_f32_16x16x32_bf16 v[112:115], a[128:131], a[64:67], 0\n" \
"ds_read_b128 a[152:155], v5 offset:6144\n" \
"v_mfma_f32_16x16x32_bf16 v[120:123], a[128:131], a[80:83], 0\n" \
"s_add_u32 m0, s49, 32768\n" \
"v_exp_f32 v81, v81\n" \
"v_mfma_f32_16x16x32_bf16 v[96:99], a[132:135], v[36:39], v[96:99]\n" \
"buffer_load_dwordx4 v6, s[40:43], s16 offen offset:0 sc1 lds\n" \
"v_mfma_f32_16x16x32_bf16 v[104:107], a[132:135], v[52:55], v[104:107]\n" \
"v_exp_f32 v82, v82\n" \
"v_mfma_f32_16x16x32_bf16 v[112:115], a[132:135], a[68:71], v[112:115]\n" \
"ds_read_b128 a[156:159], v5 offset:7168\n" \
"v_cvt_pk_bf16_f32 v136, v80, v81\n" \
"v_mfma_f32_16x16x32_bf16 v[120:123], a[132:135], a[84:87], v[120:123]\n" \
"s_waitcnt lgkmcnt(4)\n" \
"v_exp_f32 v83, v83\n" \
"v_mfma_f32_16x16x32_bf16 v[96:99], a[136:139], v[40:43], v[96:99]\n" \
"v_exp_f32 v84, v84\n" \
"v_mfma_f32_16x16x32_bf16 v[104:107], a[136:139], v[56:59], v[104:107]\n" \
"v_cvt_pk_bf16_f32 v137, v82, v83\n" \
"v_mfma_f32_16x16x32_bf16 v[112:115], a[136:139], a[72:75], v[112:115]\n" \
"ds_read_b128 a[128:131], v5 offset:16384\n" \
"v_mfma_f32_16x16x32_bf16 v[120:123], a[136:139], a[88:91], v[120:123]\n" \
"v_exp_f32 v85, v85\n" \
"v_mfma_f32_16x16x32_bf16 v[96:99], a[140:143], v[44:47], v[96:99]\n" \
"buffer_load_dwordx4 v6, s[40:43], s16 offen offset:1024 sc1 lds\n" \
"v_mfma_f32_16x16x32_bf16 v[104:107], a[140:143], v[60:63], v[104:107]\n" \
"v_exp_f32 v86, v86\n" \
"v_mfma_f32_16x16x32_bf16 v[112:115], a[140:143], a[76:79], v[112:115]\n" \
"ds_read_b128 a[132:135], v5 offset:17408\n" \
"v_cvt_pk_bf16_f32 v138, v84, v85\n" \
"v_mfma_f32_16x16x32_bf16 v[120:123], a[140:143], a[92:95], v[120:123]\n" \
"s_waitcnt lgkmcnt(4)\n" \
"v_exp_f32 v87, v87\n" \
"v_mfma_f32_16x16x32_bf16 v[100:103], a[144:147], v[32:35], 0\n" \
"v_exp_f32 v88, v88\n" \
"v_mfma_f32_16x16x32_bf16 v[108:111], a[144:147], v[48:51], 0\n" \
"v_cvt_pk_bf16_f32 v139, v86, v87\n" \
"v_mfma_f32_16x16x32_bf16 v[116:119], a[144:147], a[64:67], 0\n" \
"ds_read_b128 a[136:139], v5 offset:18432\n" \
"v_mfma_f32_16x16x32_bf16 v[124:127], a[144:147], a[80:83], 0\n" \
"v_exp_f32 v89, v89\n" \
"v_mfma_f32_16x16x32_bf16 v[100:103], a[148:151], v[36:39], v[100:103]\n" \
"buffer_load_dwordx4 v6, s[40:43], s16 offen offset:2048 sc1 lds\n" \
"v_mfma_f32_16x16x32_bf16 v[108:111], a[148:151], v[52:55], v[108:111]\n" \
"v_exp_f32 v90, v90\n" \
"v_mfma_f32_16x16x32_bf16 v[116:119], a[148:151], a[68:71], v[116:119]\n" \
"ds_read_b128 a[140:143], v5 offset:19456\n" \
"v_cvt_pk_bf16_f32 v140, v88, v89\n" \
"v_mfma_f32_16x16x32_bf16 v[124:127], a[148:151], a[84:87], v[124:127]\n" \
"s_waitcnt lgkmcnt(4)\n" \
"v_exp_f32 v91, v91\n" \
"v_mfma_f32_16x16x32_bf16 v[100:103], a[152:155], v[40:43], v[100:103]\n" \
"v_exp_f32 v92, v92\n" \
"v_mfma_f32_16x16x32_bf16 v[108:111], a[152:155], v[56:59], v[108:111]\n" \
"v_cvt_pk_bf16_f32 v141, v90, v91\n" \
"v_mfma_f32_16x16x32_bf16 v[116:119], a[152:155], a[72:75], v[116:119]\n" \
"ds_read_b128 a[144:147], v5 offset:20480\n" \
"v_mfma_f32_16x16x32_bf16 v[124:127], a[152:155], a[88:91], v[124:127]\n" \
"v_exp_f32 v93, v93\n" \
"v_mfma_f32_16x16x32_bf16 v[100:103], a[156:159], v[44:47], v[100:103]\n" \
"buffer_load_dwordx4 v6, s[40:43], s16 offen offset:3072 sc1 lds\n" \
"v_mfma_f32_16x16x32_bf16 v[108:111], a[156:159], v[60:63], v[108:111]\n" \
"v_exp_f32 v94, v94\n" \
"v_mfma_f32_16x16x32_bf16 v[116:119], a[156:159], a[76:79], v[116:119]\n" \
"ds_read_b128 a[148:151], v5 offset:21504\n" \
"v_cvt_pk_bf16_f32 v142, v92, v93\n" \
"v_mfma_f32_16x16x32_bf16 v[124:127], a[156:159], a[92:95], v[124:127]\n" \
"s_waitcnt lgkmcnt(4)\n" \
"v_exp_f32 v95, v95\n" \
"v_mfma_f32_16x16x32_bf16 v[160:163], v[180:183], v[128:131], v[160:163]\n" \
"v_cvt_pk_bf16_f32 v143, v94, v95\n" \
"v_mfma_f32_16x16x32_bf16 v[164:167], v[180:183], v[132:135], v[164:167]\n" \
"v_mfma_f32_16x16x32_bf16 v[168:171], v[180:183], v[136:139], v[168:171]\n" \
"v_exp_f32 v96, v96\n" \
"v_mfma_f32_16x16x32_bf16 v[172:175], v[180:183], v[140:143], v[172:175]\n" \
"v_mfma_f32_16x16x32_bf16 v[192:195], a[128:131], v[128:131], v[192:195]\n" \
"v_exp_f32 v97, v97\n" \
"v_mfma_f32_16x16x32_bf16 v[224:227], a[128:131], v[132:135], v[224:227]\n" \
"v_cvt_pk_bf16_f32 v144, v96, v97\n" \
"v_mfma_f32_16x16x32_bf16 a[0:3], a[128:131], v[136:139], a[0:3]\n" \
"ds_read_b128 a[152:155], v5 offset:22528\n" \
"v_mfma_f32_16x16x32_bf16 a[32:35], a[128:131], v[140:143], a[32:35]\n" \
"v_exp_f32 v98, v98\n" \
"v_mfma_f32_16x16x32_bf16 v[196:199], a[132:135], v[128:131], v[196:199]\n" \
"v_exp_f32 v99, v99\n" \
"v_mfma_f32_16x16x32_bf16 v[228:231], a[132:135], v[132:135], v[228:231]\n" \
"v_cvt_pk_bf16_f32 v145, v98, v99\n" \
"v_mfma_f32_16x16x32_bf16 a[4:7], a[132:135], v[136:139], a[4:7]\n" \
"ds_read_b128 a[156:159], v5 offset:23552\n" \
"v_mfma_f32_16x16x32_bf16 a[36:39], a[132:135], v[140:143], a[36:39]\n" \
"s_waitcnt vmcnt(12) lgkmcnt(4)\n" \
"v_exp_f32 v100, v100\n" \
"v_mfma_f32_16x16x32_bf16 v[200:203], a[136:139], v[128:131], v[200:203]\n" \
"s_barrier\n" \
"v_exp_f32 v101, v101\n" \
"v_mfma_f32_16x16x32_bf16 v[232:235], a[136:139], v[132:135], v[232:235]\n" \
"s_add_u32 s17, s17, s19\n" \
"v_cvt_pk_bf16_f32 v146, v100, v101\n" \
"v_mfma_f32_16x16x32_bf16 a[8:11], a[136:139], v[136:139], a[8:11]\n" \
"ds_read_b128 a[128:131], v5 offset:32768\n" \
"v_mfma_f32_16x16x32_bf16 a[40:43], a[136:139], v[140:143], a[40:43]\n" \
"s_add_u32 m0, s49, 49152\n" \
"v_exp_f32 v102, v102\n" \
"v_mfma_f32_16x16x32_bf16 v[204:207], a[140:143], v[128:131], v[204:207]\n" \
"buffer_load_dwordx4 v6, s[40:43], s17 offen offset:0 sc1 lds\n" \
"v_mfma_f32_16x16x32_bf16 v[236:239], a[140:143], v[132:135], v[236:239]\n" \
"v_exp_f32 v103, v103\n" \
"v_mfma_f32_16x16x32_bf16 a[12:15], a[140:143], v[136:139], a[12:15]\n" \
"ds_read_b128 a[132:135], v5 offset:33792\n" \
"v_cvt_pk_bf16_f32 v147, v102, v103\n" \
"v_mfma_f32_16x16x32_bf16 a[44:47], a[140:143], v[140:143], a[44:47]\n" \
"LH15_%=:\ns_cbranch_execz LH19_%=\n" \
"s_waitcnt lgkmcnt(4)\n" \
"v_exp_f32 v104, v104\n" \
"v_mfma_f32_16x16x32_bf16 v[208:211], a[144:147], v[128:131], v[208:211]\n" \
"v_exp_f32 v105, v105\n" \
"v_mfma_f32_16x16x32_bf16 v[240:243], a[144:147], v[132:135], v[240:243]\n" \
"v_cvt_pk_bf16_f32 v148, v104, v105\n" \
"v_mfma_f32_16x16x32_bf16 a[16:19], a[144:147], v[136:139], a[16:19]\n" \
"ds_read_b128 a[136:139], v5 offset:34816\n" \
"v_mfma_f32_16x16x32_bf16 a[48:51], a[144:147], v[140:143], a[48:51]\n" \
"v_exp_f32 v106, v106\n" \
"v_mfma_f32_16x16x32_bf16 v[212:215], a[148:151], v[128:131], v[212:215]\n" \
"buffer_load_dwordx4 v6, s[40:43], s17 offen offset:1024 sc1 lds\n" \
"v_mfma_f32_16x16x32_bf16 v[244:247], a[148:151], v[132:135], v[244:247]\n" \
"v_exp_f32 v107, v107\n" \
"v_mfma_f32_16x16x32_bf16 a[20:23], a[148:151], v[136:139], a[20:23]\n" \
"ds_read_b128 a[140:143], v5 offset:35840\n" \
"v_cvt_pk_bf16_f32 v149, v106, v107\n" \
"v_mfma_f32_16x16x32_bf16 a[52:55], a[148:151], v[140:143], a[52:55]\n" \
"s_waitcnt lgkmcnt(4)\n" \
"v_exp_f32 v108, v108\n" \
"v_mfma_f32_16x16x32_bf16 v[216:219], a[152:155], v[128:131], v[216:219]\n" \
"buffer_load_dwordx4 v6, s[40:43], s17 offen offset:2048 sc1 lds\n" \
"v_mfma_f32_16x16x32_bf16 v[248:251], a[152:155], v[132:135], v[248:251]\n" \
"v_exp_f32 v109, v109\n" \
"v_mfma_f32_16x16x32_bf16 a[24:27], a[152:155], v[136:139], a[24:27]\n" \
"ds_read_b128 a[144:147], v5 offset:36864\n" \
"v_cvt_pk_bf16_f32 v150, v108, v109\n" \
"v_mfma_f32_16x16x32_bf16 a[56:59], a[152:155], v[140:143], a[56:59]\n" \
"v_exp_f32 v110, v110\n" \
"v_mfma_f32_16x16x32_bf16 v[220:223], a[156:159], v[128:131], v[220:223]\n" \
"buffer_load_dwordx4 v6, s[40:43], s17 offen offset:3072 sc1 lds\n" \
"v_mfma_f32_16x16x32_bf16 v[252:255], a[156:159], v[132:135], v[252:255]\n" \
"v_exp_f32 v111, v111\n" \
"v_mfma_f32_16x16x32_bf16 a[28:31], a[156:159], v[136:139], a[28:31]\n" \
"ds_read_b128 a[148:151], v5 offset:37888\n" \
"v_cvt_pk_bf16_f32 v151, v110, v111\n" \
"v_mfma_f32_16x16x32_bf16 a[60:63], a[156:159], v[140:143], a[60:63]\n" \
"s_waitcnt lgkmcnt(4)\n" \
"s_cmp_lg_u32 s44, s46\n" \
"s_cbranch_scc1 LODD_%=\n" \
"LMID_%=:\n" \
"s_cmp_eq_u32 s44, s45\n" \
"s_cbranch_scc1 LWS_%=\n" \
"s_cmp_lt_u32 %2, 2\n" \
"s_cbranch_scc1 LS1_%=\n" \
"s_cmp_eq_u32 s44, 15\n" \
"s_cbranch_scc1 LS1_%=\n" \
"s_lshl_b32 s46, s7, 2\n" \
"s_add_u32 s46, s46, %2\n" \
"s_waitcnt vmcnt(8)\n" \
"v_cmp_ne_u64 vcc, s[14:15], v[18:19]\n" \
"s_cmp_eq_u32 s46, 30\ns_cselect_b32 s46, 15, 99\n" \
"s_cbranch_vccnz LPE0_%=\nLPE0R_%=:\n" \
"s_branch LODD_%=\n" \
"LWS_%=:\n" \
"v_mfma_f32_16x16x32_bf16 v[64:67], a[128:131], v[32:35], 0\n" \
"s_mov_b32 s81, 0\n" \
"v_exp_f32 v112, v112\n" \
"v_mfma_f32_16x16x32_bf16 v[72:75], a[128:131], v[48:51], 0\n" \
"s_add_u32 s16, s16, s9\n" \
"v_mov_b32 v12, v30\n" \
"v_mov_b32 v28, -1\n" \
"v_mfma_f32_16x16x32_bf16 v[80:83], a[128:131], a[64:67], 0\n" \
"ds_read_b128 a[152:155], v5 offset:38912\n" \
"v_mov_b32 v29, -1\n" \
"v_mfma_f32_16x16x32_bf16 v[88:91], a[128:131], a[80:83], 0\n" \
"s_add_u32 m0, s48, 0\n" \
"v_exp_f32 v113, v113\n" \
"v_mfma_f32_16x16x32_bf16 v[64:67], a[132:135], v[36:39], v[64:67]\n" \
"buffer_load_dwordx4 v6, s[40:43], s16 offen offset:0 sc1 lds\n" \
"v_mfma_f32_16x16x32_bf16 v[72:75], a[132:135], v[52:55], v[72:75]\n" \
"v_exp_f32 v114, v114\n" \
"v_mfma_f32_16x16x32_bf16 v[80:83], a[132:135], a[68:71], v[80:83]\n" \
"ds_read_b128 a[156:159], v5 offset:39936\n" \
"v_cvt_pk_bf16_f32 v152, v112, v113\n" \
"v_mfma_f32_16x16x32_bf16 v[88:91], a[132:135], a[84:87], v[88:91]\n" \
"s_waitcnt lgkmcnt(4)\n" \
"v_exp_f32 v115, v115\n" \
"s_add_u32 m0, s79, 131072\n" \
"v_mfma_f32_16x16x32_bf16 v[64:67], a[136:139], v[40:43], v[64:67]\n" \
"buffer_load_dwordx4 v12, s[28:31], s81 offen offset:0 lds\n" \
"v_mfma_f32_16x16x32_bf16 v[72:75], a[136:139], v[56:59], v[72:75]\n" \
"s_xor_b32 s49, s49, 0x10000\n" \
"v_exp_f32 v116, v116\n" \
"v_mfma_f32_16x16x32_bf16 v[80:83], a[136:139], a[72:75], v[80:83]\n" \
"ds_read_b128 a[128:131], v5 offset:49152\n" \
"v_cvt_pk_bf16_f32 v153, v114, v115\n" \
"v_mfma_f32_16x16x32_bf16 v[88:91], a[136:139], a[88:91], v[88:91]\n" \
"s_add_u32 m0, s48, 0\n" \
"v_exp_f32 v117, v117\n" \
"v_mfma_f32_16x16x32_bf16 v[64:67], a[140:143], v[44:47], v[64:67]\n" \
"buffer_load_dwordx4 v6, s[40:43], s16 offen offset:1024 sc1 lds\n" \
"v_mfma_f32_16x16x32_bf16 v[72:75], a[140:143], v[60:63], v[72:75]\n" \
"v_exp_f32 v118, v118\n" \
"v_mfma_f32_16x16x32_bf16 v[80:83], a[140:143], a[76:79], v[80:83]\n" \
"ds_read_b128 a[132:135], v5 offset:50176\n" \
"v_cvt_pk_bf16_f32 v154, v116, v117\n" \
"v_mfma_f32_16x16x32_bf16 v[88:91], a[140:143], a[92:95], v[88:91]\n" \
"s_waitcnt lgkmcnt(4)\n" \
"v_exp_f32 v119, v119\n" \
"s_add_u32 m0, s79, 131968\n" \
"v_mfma_f32_16x16x32_bf16 v[68:71], a[144:147], v[32:35], 0\n" \
"buffer_load_dwordx4 v12, s[28:31], s81 offen offset:128 lds\n" \
"v_mfma_f32_16x16x32_bf16 v[76:79], a[144:147], v[48:51], 0\n" \
"s_mov_b32 s81, 65536\n" \
"v_exp_f32 v120, v120\n" \
"v_mfma_f32_16x16x32_bf16 v[84:87], a[144:147], a[64:67], 0\n" \
"ds_read_b128 a[136:139], v5 offset:51200\n" \
"v_cvt_pk_bf16_f32 v155, v118, v119\n" \
"v_mfma_f32_16x16x32_bf16 v[92:95], a[144:147], a[80:83], 0\n" \
"s_add_u32 m0, s48, 0\n" \
"v_exp_f32 v121, v121\n" \
"v_mfma_f32_16x16x32_bf16 v[68:71], a[148:151], v[36:39], v[68:71]\n" \
"buffer_load_dwordx4 v6, s[40:43], s16 offen offset:2048 sc1 lds\n" \
"v_mfma_f32_16x16x32_bf16 v[76:79], a[148:151], v[52:55], v[76:79]\n" \
"v_exp_f32 v122, v122\n" \
"v_mfma_f32_16x16x32_bf16 v[84:87], a[148:151], a[68:71], v[84:87]\n" \
"ds_read_b128 a[140:143], v5 offset:52224\n" \
"v_cvt_pk_bf16_f32 v156, v120, v121\n" \
"v_mfma_f32_16x16x32_bf16 v[92:95], a[148:151], a[84:87], v[92:95]\n" \
"s_waitcnt lgkmcnt(4)\n" \
"v_exp_f32 v123, v123\n" \
"s_add_u32 m0, s79, 139264\n" \
"v_mfma_f32_16x16x32_bf16 v[68:71], a[152:155], v[40:43], v[68:71]\n" \
"buffer_load_dwordx4 v12, s[28:31], s81 offen offset:0 lds\n" \
"v_mfma_f32_16x16x32_bf16 v[76:79], a[152:155], v[56:59], v[76:79]\n" \
"v_exp_f32 v124, v124\n" \
"v_mfma_f32_16x16x32_bf16 v[84:87], a[152:155], a[72:75], v[84:87]\n" \
"ds_read_b128 a[144:147], v5 offset:53248\n" \
"v_cvt_pk_bf16_f32 v157, v122, v123\n" \
"v_mfma_f32_16x16x32_bf16 v[92:95], a[152:155], a[88:91], v[92:95]\n" \
"s_add_u32 m0, s48, 0\n" \
"v_exp_f32 v125, v125\n" \
"v_mfma_f32_16x16x32_bf16 v[68:71], a[156:159], v[44:47], v[68:71]\n" \
"buffer_load_dwordx4 v6, s[40:43], s16 offen offset:3072 sc1 lds\n" \
"v_mfma_f32_16x16x32_bf16 v[76:79], a[156:159], v[60:63], v[76:79]\n" \
"v_exp_f32 v126, v126\n" \
"v_mfma_f32_16x16x32_bf16 v[84:87], a[156:159], a[76:79], v[84:87]\n" \
"ds_read_b128 a[148:151], v5 offset:54272\n" \
"v_cvt_pk_bf16_f32 v158, v124, v125\n" \
"v_mfma_f32_16x16x32_bf16 v[92:95], a[156:159], a[92:95], v[92:95]\n" \
"s_waitcnt lgkmcnt(4)\n" \
"v_exp_f32 v127, v127\n" \
"s_add_u32 m0, s79, 140160\n" \
"v_mfma_f32_16x16x32_bf16 v[160:163], v[180:183], v[144:147], v[160:163]\n" \
"buffer_load_dwordx4 v12, s[28:31], s81 offen offset:128 lds\n" \
"v_mfma_f32_16x16x32_bf16 v[164:167], v[180:183], v[148:151], v[164:167]\n" \
"s_mov_b32 s81, 131072\n" \
"v_cvt_pk_bf16_f32 v159, v126, v127\n" \
"v_mov_b32 v30, -1\n" \
"v_mfma_f32_16x16x32_bf16 v[168:171], v[180:183], v[152:155], v[168:171]\n" \
"v_exp_f32 v64, v64\n" \
"v_mfma_f32_16x16x32_bf16 v[172:175], v[180:183], v[156:159], v[172:175]\n" \
"s_and_b32 s90, %1, 1\n" \
"v_mov_b32 v31, -1\n" \
"s_lshl_b32 s90, s90, 2\n" \
"v_mfma_f32_16x16x32_bf16 v[192:195], a[128:131], v[144:147], v[192:195]\n" \
"s_bfe_u32 s92, %1, 0x20003\n" \
"v_exp_f32 v65, v65\n" \
"v_mfma_f32_16x16x32_bf16 v[224:227], a[128:131], v[148:151], v[224:227]\n" \
"s_add_u32 s90, s90, s92\n" \
"v_cvt_pk_bf16_f32 v128, v64, v65\n" \
"s_lshl_b32 s92, %1, 2\n" \
"v_mfma_f32_16x16x32_bf16 a[0:3], a[128:131], v[152:155], a[0:3]\n" \
"s_add_u32 s92, s92, %2\n" \
"ds_read_b128 a[152:155], v5 offset:55296\n" \
"s_mul_i32 s93, s92, 12288\n" \
"v_mfma_f32_16x16x32_bf16 a[32:35], a[128:131], v[156:159], a[32:35]\n" \
"s_add_u32 s78, s93, 109051904\n" \
"v_exp_f32 v66, v66\n" \
"v_mfma_f32_16x16x32_bf16 v[196:199], a[132:135], v[144:147], v[196:199]\n" \
"s_lshl_b32 s88, s92, 3\n" \
"v_exp_f32 v67, v67\n" \
"v_mfma_f32_16x16x32_bf16 v[228:231], a[132:135], v[148:151], v[228:231]\n" \
"s_add_u32 s88, s88, 94404608\n" \
"v_cvt_pk_bf16_f32 v129, v66, v67\n" \
"v_mfma_f32_16x16x32_bf16 a[4:7], a[132:135], v[152:155], a[4:7]\n" \
"s_cmp_eq_u32 s92, 0\ns_cselect_b32 s12, 94371840, -1\n" \
"ds_read_b128 a[156:159], v5 offset:56320\n" \
"v_mfma_f32_16x16x32_bf16 a[36:39], a[132:135], v[156:159], a[36:39]\n" \
"s_waitcnt vmcnt(16) lgkmcnt(4)\n" \
"v_exp_f32 v68, v68\n" \
"v_mfma_f32_16x16x32_bf16 v[200:203], a[136:139], v[144:147], v[200:203]\n" \
"s_barrier\n" \
"v_xor_b32 v5, 0x10000, v5\n" \
"s_mul_i32 s80, s90, 12\n" \
"v_mfma_f32_16x16x32_bf16 v[232:235], a[136:139], v[148:151], v[232:235]\n" \
"s_add_u32 s17, s17, s19\n" \
"v_exp_f32 v69, v69\n" \
"v_mfma_f32_16x16x32_bf16 a[8:11], a[136:139], v[152:155], a[8:11]\n" \
"s_add_u32 s80, s80, s89\n" \
"ds_read_b128 a[128:131], v5 offset:0\n" \
"v_cvt_pk_bf16_f32 v130, v68, v69\n" \
"v_mfma_f32_16x16x32_bf16 a[40:43], a[136:139], v[156:159], a[40:43]\n" \
"s_add_u32 m0, s48, 16384\n" \
"v_exp_f32 v70, v70\n" \
"v_mfma_f32_16x16x32_bf16 v[204:207], a[140:143], v[144:147], v[204:207]\n" \
"buffer_load_dwordx4 v6, s[40:43], s17 offen offset:0 sc1 lds\n" \
"v_mfma_f32_16x16x32_bf16 v[236:239], a[140:143], v[148:151], v[236:239]\n" \
"s_lshl_b32 s80, s80, 16\n" \
"v_exp_f32 v71, v71\n" \
"v_mfma_f32_16x16x32_bf16 a[12:15], a[140:143], v[152:155], a[12:15]\n" \
"s_add_u32 s92, %2, 0\n" \
"ds_read_b128 a[132:135], v5 offset:1024\n" \
"v_cvt_pk_bf16_f32 v131, v70, v71\n" \
"v_mfma_f32_16x16x32_bf16 a[44:47], a[140:143], v[156:159], a[44:47]\n" \
"s_waitcnt lgkmcnt(4)\n" \
"v_exp_f32 v72, v72\n" \
"s_add_u32 m0, s79, 147456\n" \
"v_mfma_f32_16x16x32_bf16 v[208:211], a[144:147], v[144:147], v[208:211]\n" \
"buffer_load_dwordx4 v12, s[28:31], s81 offen offset:0 lds\n" \
"v_mfma_f32_16x16x32_bf16 v[240:243], a[144:147], v[148:151], v[240:243]\n" \
"s_mul_i32 s93, s92, 43\n" \
"v_exp_f32 v73, v73\n" \
"v_mfma_f32_16x16x32_bf16 a[16:19], a[144:147], v[152:155], a[16:19]\n" \
"s_lshr_b32 s93, s93, 7\n" \
"ds_read_b128 a[136:139], v5 offset:2048\n" \
"v_cvt_pk_bf16_f32 v132, v72, v73\n" \
"v_mfma_f32_16x16x32_bf16 a[48:51], a[144:147], v[156:159], a[48:51]\n" \
"s_add_u32 m0, s48, 16384\n" \
"v_exp_f32 v74, v74\n" \
"v_mfma_f32_16x16x32_bf16 v[212:215], a[148:151], v[144:147], v[212:215]\n" \
"buffer_load_dwordx4 v6, s[40:43], s17 offen offset:1024 sc1 lds\n" \
"v_mfma_f32_16x16x32_bf16 v[244:247], a[148:151], v[148:151], v[244:247]\n" \
"s_lshl_b32 s93, s93, 12\n" \
"v_exp_f32 v75, v75\n" \
"v_mfma_f32_16x16x32_bf16 a[20:23], a[148:151], v[152:155], a[20:23]\n" \
"s_add_u32 s64, s80, s93\n" \
"ds_read_b128 a[140:143], v5 offset:3072\n" \
"v_cvt_pk_bf16_f32 v133, v74, v75\n" \
"v_mfma_f32_16x16x32_bf16 a[52:55], a[148:151], v[156:159], a[52:55]\n" \
"s_waitcnt lgkmcnt(4)\n" \
"s_add_u32 m0, s48, 16384\n" \
"s_add_u32 s92, s92, s89\n" \
"v_mfma_f32_16x16x32_bf16 v[216:219], a[152:155], v[144:147], v[216:219]\n" \
"buffer_load_dwordx4 v6, s[40:43], s17 offen offset:2048 sc1 lds\n" \
"v_mfma_f32_16x16x32_bf16 v[248:251], a[152:155], v[148:151], v[248:251]\n" \
"s_add_u32 s44, s44, 1\n" \
"v_exp_f32 v76, v76\n" \
"v_mfma_f32_16x16x32_bf16 a[24:27], a[152:155], v[152:155], a[24:27]\n" \
"s_add_u32 s92, s92, 1\n" \
"ds_read_b128 a[144:147], v5 offset:4096\n" \
"s_mul_i32 s93, s92, 43\n" \
"v_mfma_f32_16x16x32_bf16 a[56:59], a[152:155], v[156:159], a[56:59]\n" \
"s_add_u32 m0, s48, 16384\n" \
"v_exp_f32 v77, v77\n" \
"v_mfma_f32_16x16x32_bf16 v[220:223], a[156:159], v[144:147], v[220:223]\n" \
"buffer_load_dwordx4 v6, s[40:43], s17 offen offset:3072 sc1 lds\n" \
"v_mfma_f32_16x16x32_bf16 v[252:255], a[156:159], v[148:151], v[252:255]\n" \
"s_xor_b32 s48, s48, 0x10000\n" \
"v_exp_f32 v78, v78\n" \
"v_mfma_f32_16x16x32_bf16 a[28:31], a[156:159], v[152:155], a[28:31]\n" \
"ds_read_b128 a[148:151], v5 offset:5120\n" \
"v_exp_f32 v79, v79\n" \
"v_mfma_f32_16x16x32_bf16 a[60:63], a[156:159], v[156:159], a[60:63]\n" \
"s_waitcnt lgkmcnt(4)\n" \
"v_cvt_pk_bf16_f32 v134, v76, v77\n" \
"v_cvt_pk_bf16_f32 v135, v78, v79\n" \
"v_mfma_f32_16x16x32_bf16 v[96:99], a[128:131], v[32:35], 0\n" \
"s_lshr_b32 s93, s93, 7\n" \
"v_exp_f32 v80, v80\n" \
"v_mfma_f32_16x16x32_bf16 v[104:107], a[128:131], v[48:51], 0\n" \
"s_add_u32 s16, s16, s9\n" \
"s_mul_i32 s93, s93, 3\n" \
"s_sub_u32 s93, s92, s93\n" \
"v_mfma_f32_16x16x32_bf16 v[112:115], a[128:131], a[64:67], 0\n" \
"s_lshl_b32 s93, s93, 18\n" \
"ds_read_b128 a[152:155], v5 offset:6144\n" \
"s_add_u32 s64, s64, s93\n" \
"v_mfma_f32_16x16x32_bf16 v[120:123], a[128:131], a[80:83], 0\n" \
"s_add_u32 m0, s49, 32768\n" \
"v_exp_f32 v81, v81\n" \
"v_mfma_f32_16x16x32_bf16 v[96:99], a[132:135], v[36:39], v[96:99]\n" \
"buffer_load_dwordx4 v6, s[40:43], s16 offen offset:0 sc1 lds\n" \
"v_mfma_f32_16x16x32_bf16 v[104:107], a[132:135], v[52:55], v[104:107]\n" \
"s_add_u32 s92, %2, 4\n" \
"v_exp_f32 v82, v82\n" \
"v_mfma_f32_16x16x32_bf16 v[112:115], a[132:135], a[68:71], v[112:115]\n" \
"s_mul_i32 s93, s92, 43\n" \
"ds_read_b128 a[156:159], v5 offset:7168\n" \
"v_cvt_pk_bf16_f32 v136, v80, v81\n" \
"v_mfma_f32_16x16x32_bf16 v[120:123], a[132:135], a[84:87], v[120:123]\n" \
"s_waitcnt lgkmcnt(4)\n" \
"v_exp_f32 v83, v83\n" \
"s_add_u32 m0, s79, 148352\n" \
"v_mfma_f32_16x16x32_bf16 v[96:99], a[136:139], v[40:43], v[96:99]\n" \
"buffer_load_dwordx4 v12, s[28:31], s81 offen offset:128 lds\n" \
"v_mfma_f32_16x16x32_bf16 v[104:107], a[136:139], v[56:59], v[104:107]\n" \
"s_mov_b32 s81, 196608\n" \
"v_exp_f32 v84, v84\n" \
"v_mfma_f32_16x16x32_bf16 v[112:115], a[136:139], a[72:75], v[112:115]\n" \
"s_lshr_b32 s93, s93, 7\n" \
"ds_read_b128 a[128:131], v5 offset:16384\n" \
"v_cvt_pk_bf16_f32 v137, v82, v83\n" \
"v_mfma_f32_16x16x32_bf16 v[120:123], a[136:139], a[88:91], v[120:123]\n" \
"s_add_u32 m0, s49, 32768\n" \
"v_exp_f32 v85, v85\n" \
"v_mfma_f32_16x16x32_bf16 v[96:99], a[140:143], v[44:47], v[96:99]\n" \
"buffer_load_dwordx4 v6, s[40:43], s16 offen offset:1024 sc1 lds\n" \
"v_mfma_f32_16x16x32_bf16 v[104:107], a[140:143], v[60:63], v[104:107]\n" \
"s_lshl_b32 s93, s93, 12\n" \
"v_exp_f32 v86, v86\n" \
"v_mfma_f32_16x16x32_bf16 v[112:115], a[140:143], a[76:79], v[112:115]\n" \
"s_add_u32 s65, s80, s93\n" \
"ds_read_b128 a[132:135], v5 offset:17408\n" \
"v_cvt_pk_bf16_f32 v138, v84, v85\n" \
"v_mfma_f32_16x16x32_bf16 v[120:123], a[140:143], a[92:95], v[120:123]\n" \
"s_waitcnt lgkmcnt(4)\n" \
"v_exp_f32 v87, v87\n" \
"s_add_u32 m0, s79, 155648\n" \
"v_mfma_f32_16x16x32_bf16 v[100:103], a[144:147], v[32:35], 0\n" \
"buffer_load_dwordx4 v12, s[28:31], s81 offen offset:0 lds\n" \
"v_mfma_f32_16x16x32_bf16 v[108:111], a[144:147], v[48:51], 0\n" \
"s_add_u32 s92, s92, s89\n" \
"v_exp_f32 v88, v88\n" \
"v_mfma_f32_16x16x32_bf16 v[116:119], a[144:147], a[64:67], 0\n" \
"s_add_u32 s92, s92, 1\n" \
"ds_read_b128 a[136:139], v5 offset:18432\n" \
"v_cvt_pk_bf16_f32 v139, v86, v87\n" \
"v_mfma_f32_16x16x32_bf16 v[124:127], a[144:147], a[80:83], 0\n" \
"s_add_u32 m0, s49, 32768\n" \
"v_exp_f32 v89, v89\n" \
"v_mfma_f32_16x16x32_bf16 v[100:103], a[148:151], v[36:39], v[100:103]\n" \
"buffer_load_dwordx4 v6, s[40:43], s16 offen offset:2048 sc1 lds\n" \
"v_mfma_f32_16x16x32_bf16 v[108:111], a[148:151], v[52:55], v[108:111]\n" \
"s_mul_i32 s93, s92, 43\n" \
"v_exp_f32 v90, v90\n" \
"v_mfma_f32_16x16x32_bf16 v[116:119], a[148:151], a[68:71], v[116:119]\n" \
"s_lshr_b32 s93, s93, 7\n" \
"ds_read_b128 a[140:143], v5 offset:19456\n" \
"v_cvt_pk_bf16_f32 v140, v88, v89\n" \
"v_mfma_f32_16x16x32_bf16 v[124:127], a[148:151], a[84:87], v[124:127]\n" \
"s_waitcnt lgkmcnt(4)\n" \
"v_exp_f32 v91, v91\n" \
"s_add_u32 m0, s79, 156544\n" \
"v_mfma_f32_16x16x32_bf16 v[100:103], a[152:155], v[40:43], v[100:103]\n" \
"buffer_load_dwordx4 v12, s[28:31], s81 offen offset:128 lds\n" \
"v_mfma_f32_16x16x32_bf16 v[108:111], a[152:155], v[56:59], v[108:111]\n" \
"s_mov_b32 s81, 262144\n" \
"v_exp_f32 v92, v92\n" \
"v_mfma_f32_16x16x32_bf16 v[116:119], a[152:155], a[72:75], v[116:119]\n" \
"s_mul_i32 s93, s93, 3\n" \
"ds_read_b128 a[144:147], v5 offset:20480\n" \
"v_cvt_pk_bf16_f32 v141, v90, v91\n" \
"v_mfma_f32_16x16x32_bf16 v[124:127], a[152:155], a[88:91], v[124:127]\n" \
"s_add_u32 m0, s49, 32768\n" \
"v_exp_f32 v93, v93\n" \
"v_mfma_f32_16x16x32_bf16 v[100:103], a[156:159], v[44:47], v[100:103]\n" \
"buffer_load_dwordx4 v6, s[40:43], s16 offen offset:3072 sc1 lds\n" \
"v_mfma_f32_16x16x32_bf16 v[108:111], a[156:159], v[60:63], v[108:111]\n" \
"s_sub_u32 s93, s92, s93\n" \
"v_exp_f32 v94, v94\n" \
"v_mfma_f32_16x16x32_bf16 v[116:119], a[156:159], a[76:79], v[116:119]\n" \
"s_lshl_b32 s93, s93, 18\n" \
"ds_read_b128 a[148:151], v5 offset:21504\n" \
"v_cvt_pk_bf16_f32 v142, v92, v93\n" \
"v_mfma_f32_16x16x32_bf16 v[124:127], a[156:159], a[92:95], v[124:127]\n" \
"s_waitcnt lgkmcnt(4)\n" \
"v_exp_f32 v95, v95\n" \
"v_mfma_f32_16x16x32_bf16 v[160:163], v[180:183], v[128:131], v[160:163]\n" \
"s_add_u32 s65, s65, s93\n" \
"v_cvt_pk_bf16_f32 v143, v94, v95\n" \
"s_add_u32 s92, %2, 8\n" \
"v_mfma_f32_16x16x32_bf16 v[164:167], v[180:183], v[132:135], v[164:167]\n" \
"s_mul_i32 s93, s92, 43\n" \
"s_lshr_b32 s93, s93, 7\n" \
"s_lshl_b32 s93, s93, 12\n" \
"v_mfma_f32_16x16x32_bf16 v[168:171], v[180:183], v[136:139], v[168:171]\n" \
"s_add_u32 s66, s80, s93\n" \
"v_exp_f32 v96, v96\n" \
"v_mfma_f32_16x16x32_bf16 v[172:175], v[180:183], v[140:143], v[172:175]\n" \
"s_add_u32 s92, s92, s89\n" \
"s_add_u32 s92, s92, 1\n" \
"s_mul_i32 s93, s92, 43\n" \
"v_mfma_f32_16x16x32_bf16 v[192:195], a[128:131], v[128:131], v[192:195]\n" \
"s_lshr_b32 s93, s93, 7\n" \
"v_exp_f32 v97, v97\n" \
"v_mfma_f32_16x16x32_bf16 v[224:227], a[128:131], v[132:135], v[224:227]\n" \
"s_mul_i32 s93, s93, 3\n" \
"v_cvt_pk_bf16_f32 v144, v96, v97\n" \
"s_sub_u32 s93, s92, s93\n" \
"v_mfma_f32_16x16x32_bf16 a[0:3], a[128:131], v[136:139], a[0:3]\n" \
"s_lshl_b32 s93, s93, 18\n" \
"ds_read_b128 a[152:155], v5 offset:22528\n" \
"s_add_u32 s66, s66, s93\n" \
"v_mfma_f32_16x16x32_bf16 a[32:35], a[128:131], v[140:143], a[32:35]\n" \
"s_and_b32 s92, %2, 1\n" \
"v_exp_f32 v98, v98\n" \
"v_mfma_f32_16x16x32_bf16 v[196:199], a[132:135], v[128:131], v[196:199]\n" \
"s_lshl_b32 s92, s92, 12\n" \
"v_exp_f32 v99, v99\n" \
"v_mfma_f32_16x16x32_bf16 v[228:231], a[132:135], v[132:135], v[228:231]\n" \
"s_sub_u32 s57, s66, s64\n" \
"v_cvt_pk_bf16_f32 v145, v98, v99\n" \
"s_sub_u32 s56, s64, s57\n" \
"v_mfma_f32_16x16x32_bf16 a[4:7], a[132:135], v[136:139], a[4:7]\n" \
"s_sub_u32 s56, s56, s92\n" \
"ds_read_b128 a[156:159], v5 offset:23552\n" \
"s_sub_u32 s59, s64, s65\n" \
"v_mfma_f32_16x16x32_bf16 a[36:39], a[132:135], v[140:143], a[36:39]\n" \
"s_waitcnt vmcnt(12) lgkmcnt(4)\n" \
"v_exp_f32 v100, v100\n" \
"v_mfma_f32_16x16x32_bf16 v[200:203], a[136:139], v[128:131], v[200:203]\n" \
"s_barrier\n" \
"s_add_u32 s100, s100, 262144\ns_addc_u32 s101, s101, 0\n" \
"v_mfma_f32_16x16x32_bf16 v[232:235], a[136:139], v[132:135], v[232:235]\n" \
"s_add_u32 s17, s17, s19\n" \
"ds_read_b64_tr_b16 a[160:161], v13 offset:0\n" \
"s_add_u32 s59, s59, 16384\n" \
"v_mfma_f32_16x16x32_bf16 a[8:11], a[136:139], v[136:139], a[8:11]\n" \
"s_sub_u32 s58, s65, s59\n" \
"ds_read_b128 a[128:131], v5 offset:32768\n" \
"s_sub_u32 s58, s58, s92\n" \
"v_mfma_f32_16x16x32_bf16 a[40:43], a[136:139], v[140:143], a[40:43]\n" \
"s_add_u32 m0, s49, 49152\n" \
"v_exp_f32 v101, v101\n" \
"v_mfma_f32_16x16x32_bf16 v[204:207], a[140:143], v[128:131], v[204:207]\n" \
"buffer_load_dwordx4 v6, s[40:43], s17 offen offset:0 sc1 lds\n" \
"v_mfma_f32_16x16x32_bf16 v[236:239], a[140:143], v[132:135], v[236:239]\n" \
"s_lshl_b32 s92, s90, 18\n" \
"v_exp_f32 v102, v102\n" \
"v_mfma_f32_16x16x32_bf16 a[12:15], a[140:143], v[136:139], a[12:15]\n" \
"s_lshl_b32 s93, s89, 16\n" \
"ds_read_b128 a[132:135], v5 offset:33792\n" \
"v_cvt_pk_bf16_f32 v146, v100, v101\n" \
"v_mfma_f32_16x16x32_bf16 a[44:47], a[140:143], v[140:143], a[44:47]\n" \
"s_waitcnt lgkmcnt(4)\n" \
"v_exp_f32 v103, v103\n" \
"v_mfma_f32_16x16x32_bf16 v[208:211], a[144:147], v[128:131], v[208:211]\n" \
"s_mov_b64 exec, 0xffff\nglobal_load_dword v1, v2, s[100:101]\ns_mov_b64 exec, -1\n" \
"v_mfma_f32_16x16x32_bf16 v[240:243], a[144:147], v[132:135], v[240:243]\n" \
"s_add_u32 s100, s100, 262144\ns_addc_u32 s101, s101, 0\n" \
"ds_read_b64_tr_b16 a[162:163], v14 offset:0\n" \
"v_mfma_f32_16x16x32_bf16 a[16:19], a[144:147], v[136:139], a[16:19]\n" \
"s_add_u32 s67, s92, s93\n" \
"ds_read_b128 a[136:139], v5 offset:34816\n" \
"v_cvt_pk_bf16_f32 v147, v102, v103\n" \
"v_mfma_f32_16x16x32_bf16 a[48:51], a[144:147], v[140:143], a[48:51]\n" \
"s_add_u32 m0, s49, 49152\n" \
"v_exp_f32 v104, v104\n" \
"v_mfma_f32_16x16x32_bf16 v[212:215], a[148:151], v[128:131], v[212:215]\n" \
"buffer_load_dwordx4 v6, s[40:43], s17 offen offset:1024 sc1 lds\n" \
"v_mfma_f32_16x16x32_bf16 v[244:247], a[148:151], v[132:135], v[244:247]\n" \
"s_add_u32 s67, s67, 6291456\n" \
"v_exp_f32 v105, v105\n" \
"v_mfma_f32_16x16x32_bf16 a[20:23], a[148:151], v[136:139], a[20:23]\n" \
"s_lshl_b32 s92, s90, 20\n" \
"ds_read_b128 a[140:143], v5 offset:35840\n" \
"v_cvt_pk_bf16_f32 v148, v104, v105\n" \
"v_mfma_f32_16x16x32_bf16 a[52:55], a[148:151], v[140:143], a[52:55]\n" \
"s_waitcnt lgkmcnt(4)\n" \
"s_add_u32 m0, s49, 49152\n" \
"ds_read_b64_tr_b16 a[164:165], v15 offset:0\n" \
"v_mfma_f32_16x16x32_bf16 v[216:219], a[152:155], v[128:131], v[216:219]\n" \
"buffer_load_dwordx4 v6, s[40:43], s17 offen offset:2048 sc1 lds\n" \
"v_mfma_f32_16x16x32_bf16 v[248:251], a[152:155], v[132:135], v[248:251]\n" \
"s_add_u32 s94, s92, s93\n" \
"v_exp_f32 v106, v106\n" \
"v_mfma_f32_16x16x32_bf16 a[24:27], a[152:155], v[136:139], a[24:27]\n" \
"ds_read_b128 a[144:147], v5 offset:36864\n" \
"v_exp_f32 v107, v107\n" \
"v_mfma_f32_16x16x32_bf16 a[56:59], a[152:155], v[140:143], a[56:59]\n" \
"s_add_u32 m0, s49, 49152\n" \
"v_exp_f32 v108, v108\n" \
"v_mfma_f32_16x16x32_bf16 v[220:223], a[156:159], v[128:131], v[220:223]\n" \
"buffer_load_dwordx4 v6, s[40:43], s17 offen offset:3072 sc1 lds\n" \
"v_exp_f32 v109, v109\n" \
"v_mfma_f32_16x16x32_bf16 v[252:255], a[156:159], v[132:135], v[252:255]\n" \
"s_add_u32 s94, s94, 8388608\n" \
"v_exp_f32 v110, v110\n" \
"v_mfma_f32_16x16x32_bf16 a[28:31], a[156:159], v[136:139], a[28:31]\n" \
"ds_read_b128 a[148:151], v5 offset:37888\n" \
"v_exp_f32 v111, v111\n" \
"v_mfma_f32_16x16x32_bf16 a[60:63], a[156:159], v[140:143], a[60:63]\n" \
"s_waitcnt lgkmcnt(4)\n" \
"ds_read_b64_tr_b16 a[166:167], v16 offset:0\n" \
"v_cvt_pk_bf16_f32 v149, v106, v107\n" \
"v_cvt_pk_bf16_f32 v150, v108, v109\n" \
"v_cvt_pk_bf16_f32 v151, v110, v111\n" \
"v_mfma_f32_16x16x32_bf16 v[64:67], a[128:131], v[32:35], 0\n" \
"s_mov_b64 exec, 0xffff\nglobal_load_dword v1, v2, s[100:101]\ns_mov_b64 exec, -1\n" \
"v_mfma_f32_16x16x32_bf16 v[72:75], a[128:131], v[48:51], 0\n" \
"s_add_u32 s16, s16, s9\n" \
"v_exp_f32 v112, v112\n" \
"v_mfma_f32_16x16x32_bf16 v[80:83], a[128:131], a[64:67], 0\n" \
"s_add_u32 s95, s89, 1\n" \
"ds_read_b128 a[152:155], v5 offset:38912\n" \
"s_and_b32 s95, s95, 3\n" \
"v_mfma_f32_16x16x32_bf16 v[88:91], a[128:131], a[80:83], 0\n" \
"s_add_u32 m0, s48, 0\n" \
"v_exp_f32 v113, v113\n" \
"v_mfma_f32_16x16x32_bf16 v[64:67], a[132:135], v[36:39], v[64:67]\n" \
"buffer_load_dwordx4 v6, s[40:43], s16 offen offset:0 sc1 lds\n" \
"v_mfma_f32_16x16x32_bf16 v[72:75], a[132:135], v[52:55], v[72:75]\n" \
"s_lshl_b32 s92, s95, 18\n" \
"ds_read_b64_tr_b16 a[168:169], v13 offset:8192\n" \
"v_cvt_pk_bf16_f32 v152, v112, v113\n" \
"v_mfma_f32_16x16x32_bf16 v[80:83], a[132:135], a[68:71], v[80:83]\n" \
"s_add_u32 s68, s94, s92\n" \
"ds_read_b128 a[156:159], v5 offset:39936\n" \
"s_sub_u32 s92, s95, s89\n" \
"v_mfma_f32_16x16x32_bf16 v[88:91], a[132:135], a[84:87], v[88:91]\n" \
"s_waitcnt lgkmcnt(4)\n" \
"v_exp_f32 v114, v114\n" \
"v_mfma_f32_16x16x32_bf16 v[64:67], a[136:139], v[40:43], v[64:67]\n" \
"s_mul_i32 s93, s92, 98304\n" \
"v_exp_f32 v115, v115\n" \
"v_mfma_f32_16x16x32_bf16 v[72:75], a[136:139], v[56:59], v[72:75]\n" \
"s_xor_b32 s49, s49, 0x10000\n" \
"v_exp_f32 v116, v116\n" \
"v_mfma_f32_16x16x32_bf16 v[80:83], a[136:139], a[72:75], v[80:83]\n" \
"s_add_u32 s75, s78, s93\n" \
"ds_read_b128 a[128:131], v5 offset:49152\n" \
"v_cvt_pk_bf16_f32 v153, v114, v115\n" \
"v_mfma_f32_16x16x32_bf16 v[88:91], a[136:139], a[88:91], v[88:91]\n" \
"s_add_u32 m0, s48, 0\n" \
"v_exp_f32 v117, v117\n" \
"v_mfma_f32_16x16x32_bf16 v[64:67], a[140:143], v[44:47], v[64:67]\n" \
"buffer_load_dwordx4 v6, s[40:43], s16 offen offset:1024 sc1 lds\n" \
"v_mfma_f32_16x16x32_bf16 v[72:75], a[140:143], v[60:63], v[72:75]\n" \
"s_add_u32 s75, s75, 6144\n" \
"ds_read_b64_tr_b16 a[170:171], v14 offset:8192\n" \
"v_cvt_pk_bf16_f32 v154, v116, v117\n" \
"v_mfma_f32_16x16x32_bf16 v[80:83], a[140:143], a[76:79], v[80:83]\n" \
"s_add_u32 s95, s89, 2\n" \
"ds_read_b128 a[132:135], v5 offset:50176\n" \
"s_and_b32 s95, s95, 3\n" \
"v_mfma_f32_16x16x32_bf16 v[88:91], a[140:143], a[92:95], v[88:91]\n" \
"s_waitcnt lgkmcnt(4)\n" \
"v_exp_f32 v118, v118\n" \
"v_mfma_f32_16x16x32_bf16 v[68:71], a[144:147], v[32:35], 0\n" \
"s_lshl_b32 s92, s95, 18\n" \
"v_exp_f32 v119, v119\n" \
"v_mfma_f32_16x16x32_bf16 v[76:79], a[144:147], v[48:51], 0\n" \
"s_add_u32 s69, s94, s92\n" \
"v_exp_f32 v120, v120\n" \
"v_mfma_f32_16x16x32_bf16 v[84:87], a[144:147], a[64:67], 0\n" \
"s_sub_u32 s92, s95, s89\n" \
"ds_read_b128 a[136:139], v5 offset:51200\n" \
"v_cvt_pk_bf16_f32 v155, v118, v119\n" \
"v_mfma_f32_16x16x32_bf16 v[92:95], a[144:147], a[80:83], 0\n" \
"s_add_u32 m0, s48, 0\n" \
"ds_read_b64_tr_b16 a[172:173], v15 offset:8192\n" \
"s_mul_i32 s93, s92, 98304\n" \
"v_mfma_f32_16x16x32_bf16 v[68:71], a[148:151], v[36:39], v[68:71]\n" \
"buffer_load_dwordx4 v6, s[40:43], s16 offen offset:2048 sc1 lds\n" \
"v_mfma_f32_16x16x32_bf16 v[76:79], a[148:151], v[52:55], v[76:79]\n" \
"s_add_u32 s76, s78, s93\n" \
"v_exp_f32 v121, v121\n" \
"v_mfma_f32_16x16x32_bf16 v[84:87], a[148:151], a[68:71], v[84:87]\n" \
"s_add_u32 s76, s76, 8192\n" \
"ds_read_b128 a[140:143], v5 offset:52224\n" \
"v_cvt_pk_bf16_f32 v156, v120, v121\n" \
"v_mfma_f32_16x16x32_bf16 v[92:95], a[148:151], a[84:87], v[92:95]\n" \
"s_waitcnt lgkmcnt(4)\n" \
"v_exp_f32 v122, v122\n" \
"v_mfma_f32_16x16x32_bf16 v[68:71], a[152:155], v[40:43], v[68:71]\n" \
"s_add_u32 s95, s89, 3\n" \
"v_exp_f32 v123, v123\n" \
"v_mfma_f32_16x16x32_bf16 v[76:79], a[152:155], v[56:59], v[76:79]\n" \
"s_and_b32 s95, s95, 3\n" \
"v_exp_f32 v124, v124\n" \
"v_mfma_f32_16x16x32_bf16 v[84:87], a[152:155], a[72:75], v[84:87]\n" \
"s_lshl_b32 s92, s95, 18\n" \
"ds_read_b128 a[144:147], v5 offset:53248\n" \
"v_cvt_pk_bf16_f32 v157, v122, v123\n" \
"v_mfma_f32_16x16x32_bf16 v[92:95], a[152:155], a[88:91], v[92:95]\n" \
"s_add_u32 m0, s48, 0\n" \
"ds_read_b64_tr_b16 a[174:175], v16 offset:8192\n" \
"s_add_u32 s70, s94, s92\n" \
"v_mfma_f32_16x16x32_bf16 v[68:71], a[156:159], v[44:47], v[68:71]\n" \
"buffer_load_dwordx4 v6, s[40:43], s16 offen offset:3072 sc1 lds\n" \
"v_mfma_f32_16x16x32_bf16 v[76:79], a[156:159], v[60:63], v[76:79]\n" \
"s_sub_u32 s92, s95, s89\n" \
"v_exp_f32 v125, v125\n" \
"v_mfma_f32_16x16x32_bf16 v[84:87], a[156:159], a[76:79], v[84:87]\n" \
"s_mul_i32 s93, s92, 98304\n" \
"ds_read_b128 a[148:151], v5 offset:54272\n" \
"v_cvt_pk_bf16_f32 v158, v124, v125\n" \
"v_mfma_f32_16x16x32_bf16 v[92:95], a[156:159], a[92:95], v[92:95]\n" \
"s_waitcnt lgkmcnt(4)\n" \
"v_exp_f32 v126, v126\n" \
"v_mfma_f32_16x16x32_bf16 v[160:163], v[180:183], v[144:147], v[160:163]\n" \
"s_add_u32 s77, s78, s93\n" \
"v_exp_f32 v127, v127\n" \
"v_mfma_f32_16x16x32_bf16 v[164:167], v[180:183], v[148:151], v[164:167]\n" \
"s_add_u32 s77, s77, 10240\n" \
"v_cvt_pk_bf16_f32 v159, v126, v127\n" \
"s_add_u32 s95, s89, 4\n" \
"v_mfma_f32_16x16x32_bf16 v[168:171], v[180:183], v[152:155], v[168:171]\n" \
"s_and_b32 s95, s95, 3\n" \
"v_exp_f32 v64, v64\n" \
"v_mfma_f32_16x16x32_bf16 v[172:175], v[180:183], v[156:159], v[172:175]\n" \
"s_lshl_b32 s92, s95, 18\n" \
"s_add_u32 s71, s94, s92\n" \
"s_add_u32 s92, s89, 1\n" \
"v_mfma_f32_16x16x32_bf16 v[192:195], a[128:131], v[144:147], v[192:195]\n" \
"s_mul_i32 s95, s92, 43\n" \
"v_exp_f32 v65, v65\n" \
"v_mfma_f32_16x16x32_bf16 v[224:227], a[128:131], v[148:151], v[224:227]\n" \
"s_lshr_b32 s95, s95, 7\n" \
"v_cvt_pk_bf16_f32 v128, v64, v65\n" \
"s_mul_i32 s95, s95, 3\n" \
"v_mfma_f32_16x16x32_bf16 a[0:3], a[128:131], v[152:155], a[0:3]\n" \
"s_sub_u32 s95, s92, s95\n" \
"ds_read_b128 a[152:155], v5 offset:55296\n" \
"s_sub_u32 s92, s95, s89\n" \
"v_mfma_f32_16x16x32_bf16 a[32:35], a[128:131], v[156:159], a[32:35]\n" \
"s_mul_i32 s93, s92, 98304\n" \
"v_exp_f32 v66, v66\n" \
"v_mfma_f32_16x16x32_bf16 v[196:199], a[132:135], v[144:147], v[196:199]\n" \
"s_add_u32 s72, s78, s93\n" \
"v_exp_f32 v67, v67\n" \
"v_mfma_f32_16x16x32_bf16 v[228:231], a[132:135], v[148:151], v[228:231]\n" \
"s_waitcnt lgkmcnt(2)\n" \
"v_cvt_pk_bf16_f32 v129, v66, v67\n" \
"s_sub_u32 s92, s92, 1\n" \
"v_mfma_f32_16x16x32_bf16 a[4:7], a[132:135], v[152:155], a[4:7]\n" \
"s_and_b32 s92, s92, 3\n" \
"ds_read_b128 a[156:159], v5 offset:56320\n" \
"s_lshl_b32 s92, s92, 11\n" \
"v_mfma_f32_16x16x32_bf16 a[36:39], a[132:135], v[156:159], a[36:39]\n" \
"s_waitcnt vmcnt(11) lgkmcnt(4)\n" \
"v_exp_f32 v68, v68\n" \
"v_mfma_f32_16x16x32_bf16 v[200:203], a[136:139], v[144:147], v[200:203]\n" \
"s_barrier\n" \
"v_xor_b32 v5, 0x10000, v5\n" \
"s_add_u32 s72, s72, s92\n" \
"v_mfma_f32_16x16x32_bf16 v[232:235], a[136:139], v[148:151], v[232:235]\n" \
"s_add_u32 s17, s17, s19\n" \
"ds_read_b64_tr_b16 a[176:177], v13 offset:16384\n" \
"s_add_u32 s92, s89, 2\n" \
"v_mfma_f32_16x16x32_bf16 a[8:11], a[136:139], v[152:155], a[8:11]\n" \
"LH16_%=:\ns_cbranch_execz LH20_%=\n" \
"s_mul_i32 s95, s92, 43\n" \
"ds_read_b128 a[128:131], v5 offset:0\n" \
"s_lshr_b32 s95, s95, 7\n" \
"v_mfma_f32_16x16x32_bf16 a[40:43], a[136:139], v[156:159], a[40:43]\n" \
"s_add_u32 m0, s48, 16384\n" \
"v_exp_f32 v69, v69\n" \
"v_mfma_f32_16x16x32_bf16 v[204:207], a[140:143], v[144:147], v[204:207]\n" \
"buffer_load_dwordx4 v6, s[40:43], s17 offen offset:0 sc1 lds\n" \
"v_mfma_f32_16x16x32_bf16 v[236:239], a[140:143], v[148:151], v[236:239]\n" \
"s_mul_i32 s95, s95, 3\n" \
"v_exp_f32 v70, v70\n" \
"v_mfma_f32_16x16x32_bf16 a[12:15], a[140:143], v[152:155], a[12:15]\n" \
"s_sub_u32 s95, s92, s95\n" \
"ds_read_b128 a[132:135], v5 offset:1024\n" \
"v_cvt_pk_bf16_f32 v130, v68, v69\n" \
"v_mfma_f32_16x16x32_bf16 a[44:47], a[140:143], v[156:159], a[44:47]\n" \
"s_waitcnt lgkmcnt(4)\n" \
"v_exp_f32 v71, v71\n" \
"s_add_u32 m0, s79, 131072\n" \
"v_mfma_f32_16x16x32_bf16 v[208:211], a[144:147], v[144:147], v[208:211]\n" \
"buffer_load_dwordx4 v12, s[28:31], s81 offen offset:0 lds\n" \
"v_mfma_f32_16x16x32_bf16 v[240:243], a[144:147], v[148:151], v[240:243]\n" \
"s_sub_u32 s92, s95, s89\n" \
"ds_read_b64_tr_b16 a[178:179], v14 offset:16384\n" \
"v_cvt_pk_bf16_f32 v131, v70, v71\n" \
"v_mfma_f32_16x16x32_bf16 a[16:19], a[144:147], v[152:155], a[16:19]\n" \
"s_mul_i32 s93, s92, 98304\n" \
"ds_read_b128 a[136:139], v5 offset:2048\n" \
"s_add_u32 s73, s78, s93\n" \
"v_mfma_f32_16x16x32_bf16 a[48:51], a[144:147], v[156:159], a[48:51]\n" \
"s_add_u32 m0, s48, 16384\n" \
"v_exp_f32 v72, v72\n" \
"v_mfma_f32_16x16x32_bf16 v[212:215], a[148:151], v[144:147], v[212:215]\n" \
"buffer_load_dwordx4 v6, s[40:43], s17 offen offset:1024 sc1 lds\n" \
"v_mfma_f32_16x16x32_bf16 v[244:247], a[148:151], v[148:151], v[244:247]\n" \
"s_sub_u32 s92, s92, 1\n" \
"v_exp_f32 v73, v73\n" \
"v_mfma_f32_16x16x32_bf16 a[20:23], a[148:151], v[152:155], a[20:23]\n" \
"s_and_b32 s92, s92, 3\n" \
"ds_read_b128 a[140:143], v5 offset:3072\n" \
"v_cvt_pk_bf16_f32 v132, v72, v73\n" \
"v_mfma_f32_16x16x32_bf16 a[52:55], a[148:151], v[156:159], a[52:55]\n" \
"s_waitcnt lgkmcnt(4)\n" \
"s_add_u32 m0, s48, 16384\n" \
"ds_read_b64_tr_b16 a[180:181], v15 offset:16384\n" \
"v_mfma_f32_16x16x32_bf16 v[216:219], a[152:155], v[144:147], v[216:219]\n" \
"buffer_load_dwordx4 v6, s[40:43], s17 offen offset:2048 sc1 lds\n" \
"v_mfma_f32_16x16x32_bf16 v[248:251], a[152:155], v[148:151], v[248:251]\n" \
"s_add_u32 s44, s44, 1\n" \
"v_exp_f32 v74, v74\n" \
"v_mfma_f32_16x16x32_bf16 a[24:27], a[152:155], v[152:155], a[24:27]\n" \
"ds_read_b128 a[144:147], v5 offset:4096\n" \
"v_exp_f32 v75, v75\n" \
"v_mfma_f32_16x16x32_bf16 a[56:59], a[152:155], v[156:159], a[56:59]\n" \
"s_add_u32 m0, s48, 16384\n" \
"v_exp_f32 v76, v76\n" \
"v_mfma_f32_16x16x32_bf16 v[220:223], a[156:159], v[144:147], v[220:223]\n" \
"buffer_load_dwordx4 v6, s[40:43], s17 offen offset:3072 sc1 lds\n" \
"v_exp_f32 v77, v77\n" \
"v_mfma_f32_16x16x32_bf16 v[252:255], a[156:159], v[148:151], v[252:255]\n" \
"s_xor_b32 s48, s48, 0x10000\n" \
"v_exp_f32 v78, v78\n" \
"v_mfma_f32_16x16x32_bf16 a[28:31], a[156:159], v[152:155], a[28:31]\n" \
"ds_read_b128 a[148:151], v5 offset:5120\n" \
"v_exp_f32 v79, v79\n" \
"v_mfma_f32_16x16x32_bf16 a[60:63], a[156:159], v[156:159], a[60:63]\n" \
"s_waitcnt lgkmcnt(4)\n" \
"ds_read_b64_tr_b16 a[182:183], v16 offset:16384\n" \
"v_cvt_pk_bf16_f32 v133, v74, v75\n" \
"v_cvt_pk_bf16_f32 v134, v76, v77\n" \
"v_cvt_pk_bf16_f32 v135, v78, v79\n" \
"v_mfma_f32_16x16x32_bf16 v[96:99], a[128:131], v[32:35], 0\n" \
"s_lshl_b32 s92, s92, 11\n" \
"v_exp_f32 v80, v80\n" \
"v_mfma_f32_16x16x32_bf16 v[104:107], a[128:131], v[48:51], 0\n" \
"s_add_u32 s16, s16, s9\n" \
"s_add_u32 s73, s73, s92\n" \
"v_mfma_f32_16x16x32_bf16 v[112:115], a[128:131], a[64:67], 0\n" \
"s_cmp_lt_u32 s89, 3\ns_cselect_b32 s95, 3, 0\n" \
"ds_read_b128 a[152:155], v5 offset:6144\n" \
"v_mfma_f32_16x16x32_bf16 v[120:123], a[128:131], a[80:83], 0\n" \
"s_add_u32 m0, s49, 32768\n" \
"v_exp_f32 v81, v81\n" \
"v_mfma_f32_16x16x32_bf16 v[96:99], a[132:135], v[36:39], v[96:99]\n" \
"buffer_load_dwordx4 v6, s[40:43], s16 offen offset:0 sc1 lds\n" \
"v_mfma_f32_16x16x32_bf16 v[104:107], a[132:135], v[52:55], v[104:107]\n" \
"s_sub_u32 s92, s95, s89\n" \
"ds_read_b64_tr_b16 a[184:185], v13 offset:24576\n" \
"v_cvt_pk_bf16_f32 v136, v80, v81\n" \
"v_mfma_f32_16x16x32_bf16 v[112:115], a[132:135], a[68:71], v[112:115]\n" \
"s_mul_i32 s93, s92, 98304\n" \
"ds_read_b128 a[156:159], v5 offset:7168\n" \
"s_add_u32 s74, s78, s93\n" \
"v_mfma_f32_16x16x32_bf16 v[120:123], a[132:135], a[84:87], v[120:123]\n" \
"s_waitcnt lgkmcnt(4)\n" \
"v_exp_f32 v82, v82\n" \
"s_add_u32 m0, s79, 131968\n" \
"v_mfma_f32_16x16x32_bf16 v[96:99], a[136:139], v[40:43], v[96:99]\n" \
"buffer_load_dwordx4 v12, s[28:31], s81 offen offset:128 lds\n" \
"v_mfma_f32_16x16x32_bf16 v[104:107], a[136:139], v[56:59], v[104:107]\n" \
"s_mov_b32 s81, 327680\n" \
"v_exp_f32 v83, v83\n" \
"v_mfma_f32_16x16x32_bf16 v[112:115], a[136:139], a[72:75], v[112:115]\n" \
"s_sub_u32 s92, s92, 1\n" \
"ds_read_b128 a[128:131], v5 offset:16384\n" \
"v_cvt_pk_bf16_f32 v137, v82, v83\n" \
"v_mfma_f32_16x16x32_bf16 v[120:123], a[136:139], a[88:91], v[120:123]\n" \
"s_add_u32 m0, s49, 32768\n" \
"v_exp_f32 v84, v84\n" \
"v_mfma_f32_16x16x32_bf16 v[96:99], a[140:143], v[44:47], v[96:99]\n" \
"buffer_load_dwordx4 v6, s[40:43], s16 offen offset:1024 sc1 lds\n" \
"v_mfma_f32_16x16x32_bf16 v[104:107], a[140:143], v[60:63], v[104:107]\n" \
"s_and_b32 s92, s92, 3\n" \
"ds_read_b64_tr_b16 a[186:187], v14 offset:24576\n" \
"s_lshl_b32 s92, s92, 11\n" \
"v_mfma_f32_16x16x32_bf16 v[112:115], a[140:143], a[76:79], v[112:115]\n" \
"s_add_u32 s74, s74, s92\n" \
"ds_read_b128 a[132:135], v5 offset:17408\n" \
"s_mul_i32 s96, s90, 3\n" \
"v_mfma_f32_16x16x32_bf16 v[120:123], a[140:143], a[92:95], v[120:123]\n" \
"s_waitcnt lgkmcnt(4)\n" \
"v_exp_f32 v85, v85\n" \
"s_add_u32 m0, s79, 139264\n" \
"v_mfma_f32_16x16x32_bf16 v[100:103], a[144:147], v[32:35], 0\n" \
"buffer_load_dwordx4 v12, s[28:31], s81 offen offset:0 lds\n" \
"v_mfma_f32_16x16x32_bf16 v[108:111], a[144:147], v[48:51], 0\n" \
"s_add_u32 s96, s96, s89\n" \
"v_exp_f32 v86, v86\n" \
"v_mfma_f32_16x16x32_bf16 v[116:119], a[144:147], a[64:67], 0\n" \
"s_add_u32 s92, s90, 24\n" \
"ds_read_b128 a[136:139], v5 offset:18432\n" \
"v_cvt_pk_bf16_f32 v138, v84, v85\n" \
"v_mfma_f32_16x16x32_bf16 v[124:127], a[144:147], a[80:83], 0\n" \
"s_add_u32 m0, s49, 32768\n" \
"ds_read_b64_tr_b16 a[188:189], v15 offset:24576\n" \
"v_mfma_f32_16x16x32_bf16 v[100:103], a[148:151], v[36:39], v[100:103]\n" \
"buffer_load_dwordx4 v6, s[40:43], s16 offen offset:2048 sc1 lds\n" \
"v_mfma_f32_16x16x32_bf16 v[108:111], a[148:151], v[52:55], v[108:111]\n" \
"v_exp_f32 v87, v87\n" \
"v_mfma_f32_16x16x32_bf16 v[116:119], a[148:151], a[68:71], v[116:119]\n" \
"ds_read_b128 a[140:143], v5 offset:19456\n" \
"v_cvt_pk_bf16_f32 v139, v86, v87\n" \
"v_mfma_f32_16x16x32_bf16 v[124:127], a[148:151], a[84:87], v[124:127]\n" \
"s_waitcnt lgkmcnt(4)\n" \
"v_exp_f32 v88, v88\n" \
"s_add_u32 m0, s79, 140160\n" \
"v_mfma_f32_16x16x32_bf16 v[100:103], a[152:155], v[40:43], v[100:103]\n" \
"buffer_load_dwordx4 v12, s[28:31], s81 offen offset:128 lds\n" \
"v_mfma_f32_16x16x32_bf16 v[108:111], a[152:155], v[56:59], v[108:111]\n" \
"s_mov_b32 s81, 393216\n" \
"v_exp_f32 v89, v89\n" \
"v_mfma_f32_16x16x32_bf16 v[116:119], a[152:155], a[72:75], v[116:119]\n" \
"ds_read_b128 a[144:147], v5 offset:20480\n" \
"v_cvt_pk_bf16_f32 v140, v88, v89\n" \
"v_mfma_f32_16x16x32_bf16 v[124:127], a[152:155], a[88:91], v[124:127]\n" \
"s_add_u32 m0, s49, 32768\n" \
"ds_read_b64_tr_b16 a[190:191], v16 offset:24576\n" \
"v_exp_f32 v90, v90\n" \
"v_mfma_f32_16x16x32_bf16 v[100:103], a[156:159], v[44:47], v[100:103]\n" \
"buffer_load_dwordx4 v6, s[40:43], s16 offen offset:3072 sc1 lds\n" \
"v_exp_f32 v91, v91\n" \
"v_mfma_f32_16x16x32_bf16 v[108:111], a[156:159], v[60:63], v[108:111]\n" \
"v_exp_f32 v92, v92\n" \
"v_mfma_f32_16x16x32_bf16 v[116:119], a[156:159], a[76:79], v[116:119]\n" \
"ds_read_b128 a[148:151], v5 offset:21504\n" \
"v_exp_f32 v93, v93\n" \
"v_mfma_f32_16x16x32_bf16 v[124:127], a[156:159], a[92:95], v[124:127]\n" \
"s_waitcnt lgkmcnt(4)\n" \
"v_exp_f32 v94, v94\n" \
"v_mfma_f32_16x16x32_bf16 v[160:163], v[180:183], v[128:131], v[160:163]\n" \
"v_exp_f32 v95, v95\n" \
"v_mfma_f32_16x16x32_bf16 v[164:167], v[180:183], v[132:135], v[164:167]\n" \
"v_cvt_pk_bf16_f32 v141, v90, v91\n" \
"v_cvt_pk_bf16_f32 v142, v92, v93\n" \
"v_cvt_pk_bf16_f32 v143, v94, v95\n" \
"v_mfma_f32_16x16x32_bf16 v[168:171], v[180:183], v[136:139], v[168:171]\n" \
"v_exp_f32 v96, v96\n" \
"v_mfma_f32_16x16x32_bf16 v[172:175], v[180:183], v[140:143], v[172:175]\n" \
"s_cmp_lt_u32 s89, 3\ns_cselect_b32 s96, s96, s92\n" \
"s_lshl_b32 s96, s96, 5\n" \
"v_mfma_f32_16x16x32_bf16 v[192:195], a[128:131], v[128:131], v[192:195]\n" \
"s_lshl_b32 s97, s90, 2\n" \
"v_exp_f32 v97, v97\n" \
"v_mfma_f32_16x16x32_bf16 v[224:227], a[128:131], v[132:135], v[224:227]\n" \
"s_add_u32 s97, s97, s89\n" \
"v_cvt_pk_bf16_f32 v144, v96, v97\n" \
"s_add_u32 s97, s97, 32\n" \
"v_mfma_f32_16x16x32_bf16 a[0:3], a[128:131], v[136:139], a[0:3]\n" \
"s_lshl_b32 s97, s97, 5\n" \
"ds_read_b128 a[152:155], v5 offset:22528\n" \
"s_lshr_b32 s92, %1, 5\n" \
"v_mfma_f32_16x16x32_bf16 a[32:35], a[128:131], v[140:143], a[32:35]\n" \
"s_lshl_b32 s92, s92, 2\n" \
"v_exp_f32 v98, v98\n" \
"v_mfma_f32_16x16x32_bf16 v[196:199], a[132:135], v[128:131], v[196:199]\n" \
"s_add_u32 s92, s92, %2\n" \
"v_exp_f32 v99, v99\n" \
"v_mfma_f32_16x16x32_bf16 v[228:231], a[132:135], v[132:135], v[228:231]\n" \
"s_waitcnt lgkmcnt(2)\n" \
"v_cvt_pk_bf16_f32 v145, v98, v99\n" \
"s_lshl_b32 s93, s92, 6\n" \
"v_mfma_f32_16x16x32_bf16 a[4:7], a[132:135], v[136:139], a[4:7]\n" \
"s_lshl_b32 s92, s92, 17\n" \
"ds_read_b128 a[156:159], v5 offset:23552\n" \
"s_add_u32 s92, s92, 67108864\n" \
"v_mfma_f32_16x16x32_bf16 a[36:39], a[132:135], v[140:143], a[36:39]\n" \
"s_waitcnt vmcnt(17) lgkmcnt(4)\n" \
"v_exp_f32 v100, v100\n" \
"v_mfma_f32_16x16x32_bf16 v[200:203], a[136:139], v[128:131], v[200:203]\n" \
"s_barrier\n" \
"v_exp_f32 v101, v101\n" \
"v_mfma_f32_16x16x32_bf16 v[232:235], a[136:139], v[132:135], v[232:235]\n" \
"s_add_u32 s17, s17, s19\n" \
"v_cvt_pk_bf16_f32 v146, v100, v101\n" \
"s_lshl_b32 s82, s96, 11\n" \
"v_mfma_f32_16x16x32_bf16 a[8:11], a[136:139], v[136:139], a[8:11]\n" \
"s_add_u32 s82, s82, s93\n" \
"ds_read_b128 a[128:131], v5 offset:32768\n" \
"s_lshl_b32 s86, s96, 2\n" \
"v_mfma_f32_16x16x32_bf16 a[40:43], a[136:139], v[140:143], a[40:43]\n" \
"s_add_u32 m0, s49, 49152\n" \
"v_exp_f32 v102, v102\n" \
"v_mfma_f32_16x16x32_bf16 v[204:207], a[140:143], v[128:131], v[204:207]\n" \
"buffer_load_dwordx4 v6, s[40:43], s17 offen offset:0 sc1 lds\n" \
"v_mfma_f32_16x16x32_bf16 v[236:239], a[140:143], v[132:135], v[236:239]\n" \
"s_add_u32 s86, s86, 71303168\n" \
"v_exp_f32 v103, v103\n" \
"v_mfma_f32_16x16x32_bf16 a[12:15], a[140:143], v[136:139], a[12:15]\n" \
"s_lshl_b32 s84, s96, 3\n" \
"ds_read_b128 a[132:135], v5 offset:33792\n" \
"v_cvt_pk_bf16_f32 v147, v102, v103\n" \
"v_mfma_f32_16x16x32_bf16 a[44:47], a[140:143], v[140:143], a[44:47]\n" \
"s_waitcnt lgkmcnt(4)\n" \
"v_exp_f32 v104, v104\n" \
"s_add_u32 m0, s79, 147456\n" \
"v_mfma_f32_16x16x32_bf16 v[208:211], a[144:147], v[128:131], v[208:211]\n" \
"buffer_load_dwordx4 v12, s[28:31], s81 offen offset:0 lds\n" \
"v_mfma_f32_16x16x32_bf16 v[240:243], a[144:147], v[132:135], v[240:243]\n" \
"s_add_u32 s84, s84, s92\n" \
"v_exp_f32 v105, v105\n" \
"v_mfma_f32_16x16x32_bf16 a[16:19], a[144:147], v[136:139], a[16:19]\n" \
"s_lshl_b32 s83, s97, 11\n" \
"ds_read_b128 a[136:139], v5 offset:34816\n" \
"v_cvt_pk_bf16_f32 v148, v104, v105\n" \
"v_mfma_f32_16x16x32_bf16 a[48:51], a[144:147], v[140:143], a[48:51]\n" \
"s_add_u32 m0, s49, 49152\n" \
"v_exp_f32 v106, v106\n" \
"v_mfma_f32_16x16x32_bf16 v[212:215], a[148:151], v[128:131], v[212:215]\n" \
"buffer_load_dwordx4 v6, s[40:43], s17 offen offset:1024 sc1 lds\n" \
"v_mfma_f32_16x16x32_bf16 v[244:247], a[148:151], v[132:135], v[244:247]\n" \
"s_add_u32 s83, s83, s93\n" \
"v_exp_f32 v107, v107\n" \
"v_mfma_f32_16x16x32_bf16 a[20:23], a[148:151], v[136:139], a[20:23]\n" \
"s_lshl_b32 s87, s97, 2\n" \
"ds_read_b128 a[140:143], v5 offset:35840\n" \
"v_cvt_pk_bf16_f32 v149, v106, v107\n" \
"v_mfma_f32_16x16x32_bf16 a[52:55], a[148:151], v[140:143], a[52:55]\n" \
"s_waitcnt lgkmcnt(4)\n" \
"s_add_u32 m0, s49, 49152\n" \
"s_add_u32 s87, s87, 71303168\n" \
"v_mfma_f32_16x16x32_bf16 v[216:219], a[152:155], v[128:131], v[216:219]\n" \
"buffer_load_dwordx4 v6, s[40:43], s17 offen offset:2048 sc1 lds\n" \
"v_mfma_f32_16x16x32_bf16 v[248:251], a[152:155], v[132:135], v[248:251]\n" \
"s_lshl_b32 s85, s97, 3\n" \
"v_exp_f32 v108, v108\n" \
"v_mfma_f32_16x16x32_bf16 a[24:27], a[152:155], v[136:139], a[24:27]\n" \
"s_add_u32 s85, s85, s92\n" \
"ds_read_b128 a[144:147], v5 offset:36864\n" \
"s_lshl_b32 s93, s89, 6\n" \
"v_mfma_f32_16x16x32_bf16 a[56:59], a[152:155], v[140:143], a[56:59]\n" \
"s_add_u32 m0, s49, 49152\n" \
"v_exp_f32 v109, v109\n" \
"v_mfma_f32_16x16x32_bf16 v[220:223], a[156:159], v[128:131], v[220:223]\n" \
"buffer_load_dwordx4 v6, s[40:43], s17 offen offset:3072 sc1 lds\n" \
"v_mfma_f32_16x16x32_bf16 v[252:255], a[156:159], v[132:135], v[252:255]\n" \
"s_add_u32 s93, s93, 94388224\n" \
"v_exp_f32 v110, v110\n" \
"v_mfma_f32_16x16x32_bf16 a[28:31], a[156:159], v[136:139], a[28:31]\n" \
"ds_read_b128 a[148:151], v5 offset:37888\n" \
"v_exp_f32 v111, v111\n" \
"v_mfma_f32_16x16x32_bf16 a[60:63], a[156:159], v[140:143], a[60:63]\n" \
"s_waitcnt lgkmcnt(4)\n" \
"v_cvt_pk_bf16_f32 v150, v108, v109\n" \
"v_cvt_pk_bf16_f32 v151, v110, v111\n" \
"v_mfma_f32_16x16x32_bf16 v[64:67], a[128:131], v[32:35], 0\n" \
"s_lshl_b32 s92, s90, 8\n" \
"v_exp_f32 v112, v112\n" \
"v_mfma_f32_16x16x32_bf16 v[72:75], a[128:131], v[48:51], 0\n" \
"s_add_u32 s16, s16, s9\n" \
"s_add_u32 s10, s93, s92\n" \
"s_add_u32 s10, s10, 6144\n" \
"v_mfma_f32_16x16x32_bf16 v[80:83], a[128:131], a[64:67], 0\n" \
"s_lshl_b32 s92, s90, 10\n" \
"ds_read_b128 a[152:155], v5 offset:38912\n" \
"s_add_u32 s11, s93, s92\n" \
"v_mfma_f32_16x16x32_bf16 v[88:91], a[128:131], a[80:83], 0\n" \
"s_add_u32 m0, s48, 0\n" \
"v_exp_f32 v113, v113\n" \
"v_mfma_f32_16x16x32_bf16 v[64:67], a[132:135], v[36:39], v[64:67]\n" \
"buffer_load_dwordx4 v6, s[40:43], s16 offen offset:0 sc1 lds\n" \
"v_mfma_f32_16x16x32_bf16 v[72:75], a[132:135], v[52:55], v[72:75]\n" \
"s_add_u32 s11, s11, 8192\n" \
"v_exp_f32 v114, v114\n" \
"v_mfma_f32_16x16x32_bf16 v[80:83], a[132:135], a[68:71], v[80:83]\n" \
"s_mul_i32 s92, s90, 768\n" \
"ds_read_b128 a[156:159], v5 offset:39936\n" \
"v_cvt_pk_bf16_f32 v152, v112, v113\n" \
"v_mfma_f32_16x16x32_bf16 v[88:91], a[132:135], a[84:87], v[88:91]\n" \
"s_waitcnt lgkmcnt(4)\n" \
"v_exp_f32 v115, v115\n" \
"s_add_u32 m0, s79, 148352\n" \
"v_mfma_f32_16x16x32_bf16 v[64:67], a[136:139], v[40:43], v[64:67]\n" \
"buffer_load_dwordx4 v12, s[28:31], s81 offen offset:128 lds\n" \
"v_mfma_f32_16x16x32_bf16 v[72:75], a[136:139], v[56:59], v[72:75]\n" \
"s_xor_b32 s49, s49, 0x10000\n" \
"s_mov_b32 s81, 458752\n" \
"v_cvt_pk_bf16_f32 v153, v114, v115\n" \
"v_mfma_f32_16x16x32_bf16 v[80:83], a[136:139], a[72:75], v[80:83]\n" \
"s_add_u32 s98, s93, s92\n" \
"ds_read_b128 a[128:131], v5 offset:49152\n" \
"v_bfe_u32 v189, v4, 7, 1\n" \
"v_mfma_f32_16x16x32_bf16 v[88:91], a[136:139], a[88:91], v[88:91]\n" \
"s_add_u32 m0, s48, 0\n" \
"v_bfe_u32 v190, v4, 4, 3\n" \
"v_lshl_add_u32 v189, v190, 1, v189\n" \
"v_mfma_f32_16x16x32_bf16 v[64:67], a[140:143], v[44:47], v[64:67]\n" \
"buffer_load_dwordx4 v6, s[40:43], s16 offen offset:1024 sc1 lds\n" \
"v_mfma_f32_16x16x32_bf16 v[72:75], a[140:143], v[60:63], v[72:75]\n" \
"s_add_u32 s80, s78, 4096\n" \
"v_lshrrev_b32 v190, 8, v4\n" \
"v_lshlrev_b32 v188, 11, v189\n" \
"v_mfma_f32_16x16x32_bf16 v[80:83], a[140:143], a[76:79], v[80:83]\n" \
"ds_read_b128 a[132:135], v5 offset:50176\n" \
"v_and_b32 v189, 1, v190\n" \
"v_mfma_f32_16x16x32_bf16 v[88:91], a[140:143], a[92:95], v[88:91]\n" \
"s_waitcnt lgkmcnt(4)\n" \
"v_lshl_add_u32 v188, v189, 5, v188\n" \
"v_lshrrev_b32 v189, 1, v190\n" \
"s_add_u32 m0, s79, 155648\n" \
"v_mfma_f32_16x16x32_bf16 v[68:71], a[144:147], v[32:35], 0\n" \
"buffer_load_dwordx4 v12, s[28:31], s81 offen offset:0 lds\n" \
"v_mfma_f32_16x16x32_bf16 v[76:79], a[144:147], v[48:51], 0\n" \
"v_lshl_add_u32 v188, v189, 4, v188\n" \
"v_add_u32 v190, s83, v188\n" \
"v_mfma_f32_16x16x32_bf16 v[84:87], a[144:147], a[64:67], 0\n" \
"ds_read_b128 a[136:139], v5 offset:51200\n" \
"v_add_u32 v188, s82, v188\n" \
"v_mfma_f32_16x16x32_bf16 v[92:95], a[144:147], a[80:83], 0\n" \
"s_add_u32 m0, s48, 0\n" \
"v_add_u32 v189, 0x8000, v188\n" \
"v_add_u32 v191, 0x8000, v190\n" \
"v_mfma_f32_16x16x32_bf16 v[68:71], a[148:151], v[36:39], v[68:71]\n" \
"buffer_load_dwordx4 v6, s[40:43], s16 offen offset:2048 sc1 lds\n" \
"v_mfma_f32_16x16x32_bf16 v[76:79], a[148:151], v[52:55], v[76:79]\n" \
"v_exp_f32 v116, v116\n" \
"v_mfma_f32_16x16x32_bf16 v[84:87], a[148:151], a[68:71], v[84:87]\n" \
"ds_read_b128 a[140:143], v5 offset:52224\n" \
"v_exp_f32 v117, v117\n" \
"v_mfma_f32_16x16x32_bf16 v[92:95], a[148:151], a[84:87], v[92:95]\n" \
"s_waitcnt lgkmcnt(4)\n" \
"v_exp_f32 v118, v118\n" \
"s_add_u32 m0, s79, 156544\n" \
"v_mfma_f32_16x16x32_bf16 v[68:71], a[152:155], v[40:43], v[68:71]\n" \
"buffer_load_dwordx4 v12, s[28:31], s81 offen offset:128 lds\n" \
"v_exp_f32 v119, v119\n" \
"v_mfma_f32_16x16x32_bf16 v[76:79], a[152:155], v[56:59], v[76:79]\n" \
"s_mov_b32 s81, 524288\n" \
"v_exp_f32 v120, v120\n" \
"v_mfma_f32_16x16x32_bf16 v[84:87], a[152:155], a[72:75], v[84:87]\n" \
"ds_read_b128 a[144:147], v5 offset:53248\n" \
"v_exp_f32 v121, v121\n" \
"v_mfma_f32_16x16x32_bf16 v[92:95], a[152:155], a[88:91], v[92:95]\n" \
"s_add_u32 m0, s48, 0\n" \
"v_exp_f32 v122, v122\n" \
"v_mfma_f32_16x16x32_bf16 v[68:71], a[156:159], v[44:47], v[68:71]\n" \
"buffer_load_dwordx4 v6, s[40:43], s16 offen offset:3072 sc1 lds\n" \
"v_exp_f32 v123, v123\n" \
"v_mfma_f32_16x16x32_bf16 v[76:79], a[156:159], v[60:63], v[76:79]\n" \
"v_exp_f32 v124, v124\n" \
"v_mfma_f32_16x16x32_bf16 v[84:87], a[156:159], a[76:79], v[84:87]\n" \
"ds_read_b128 a[148:151], v5 offset:54272\n" \
"v_exp_f32 v125, v125\n" \
"v_mfma_f32_16x16x32_bf16 v[92:95], a[156:159], a[92:95], v[92:95]\n" \
"s_waitcnt lgkmcnt(4)\n" \
"v_exp_f32 v126, v126\n" \
"v_mfma_f32_16x16x32_bf16 v[160:163], v[180:183], v[144:147], v[160:163]\n" \
"global_load_dwordx4 a[96:99], v188, s[60:61]\n" \
"v_exp_f32 v127, v127\n" \
"v_cvt_pk_bf16_f32 v154, v116, v117\n" \
"v_cvt_pk_bf16_f32 v155, v118, v119\n" \
"v_mfma_f32_16x16x32_bf16 v[164:167], v[180:183], v[148:151], v[164:167]\n" \
"v_cvt_pk_bf16_f32 v156, v120, v121\n" \
"v_cvt_pk_bf16_f32 v157, v122, v123\n" \
"v_cvt_pk_bf16_f32 v158, v124, v125\n" \
"v_cvt_pk_bf16_f32 v159, v126, v127\n" \
"v_mfma_f32_16x16x32_bf16 v[168:171], v[180:183], v[152:155], v[168:171]\n" \
"buffer_store_dwordx4 v[28:31], v4, s[36:39], s80 offen offset:2048 sc1\n" \
"v_mfma_f32_16x16x32_bf16 v[172:175], v[180:183], v[156:159], v[172:175]\n" \
"v_exp_f32 v64, v64\n" \
"v_mfma_f32_16x16x32_bf16 v[192:195], a[128:131], v[144:147], v[192:195]\n" \
"global_load_dwordx4 a[100:103], v189, s[60:61]\n" \
"v_mfma_f32_16x16x32_bf16 v[224:227], a[128:131], v[148:151], v[224:227]\n" \
"v_exp_f32 v65, v65\n" \
"v_mfma_f32_16x16x32_bf16 a[0:3], a[128:131], v[152:155], a[0:3]\n" \
"ds_read_b128 a[152:155], v5 offset:55296\n" \
"v_cvt_pk_bf16_f32 v128, v64, v65\n" \
"v_mfma_f32_16x16x32_bf16 a[32:35], a[128:131], v[156:159], a[32:35]\n" \
"buffer_store_dwordx4 v[28:31], v4, s[36:39], s80 offen offset:3072 sc1\n" \
"v_mfma_f32_16x16x32_bf16 v[196:199], a[132:135], v[144:147], v[196:199]\n" \
"s_add_u32 s80, s78, 8192\n" \
"v_exp_f32 v66, v66\n" \
"v_mfma_f32_16x16x32_bf16 v[228:231], a[132:135], v[148:151], v[228:231]\n" \
"global_load_dwordx4 a[104:107], v190, s[60:61]\n" \
"v_mfma_f32_16x16x32_bf16 a[4:7], a[132:135], v[152:155], a[4:7]\n" \
"ds_read_b128 a[156:159], v5 offset:56320\n" \
"v_mfma_f32_16x16x32_bf16 a[36:39], a[132:135], v[156:159], a[36:39]\n" \
"s_waitcnt vmcnt(18) lgkmcnt(4)\n" \
"v_exp_f32 v67, v67\n" \
"v_mfma_f32_16x16x32_bf16 v[200:203], a[136:139], v[144:147], v[200:203]\n" \
"s_barrier\n" \
"v_xor_b32 v5, 0x10000, v5\n" \
"v_cvt_pk_bf16_f32 v129, v66, v67\n" \
"v_mfma_f32_16x16x32_bf16 v[232:235], a[136:139], v[148:151], v[232:235]\n" \
"s_add_u32 s17, s17, s19\n" \
"ds_read_b64_tr_b16 a[192:193], v13 offset:0\n" \
"v_mfma_f32_16x16x32_bf16 a[8:11], a[136:139], v[152:155], a[8:11]\n" \
"s_add_u32 s100, s100, 262144\ns_addc_u32 s101, s101, 0\n" \
"ds_read_b128 a[128:131], v5 offset:0\n" \
"v_mfma_f32_16x16x32_bf16 a[40:43], a[136:139], v[156:159], a[40:43]\n" \
"s_add_u32 m0, s48, 16384\n" \
"v_exp_f32 v68, v68\n" \
"v_mfma_f32_16x16x32_bf16 v[204:207], a[140:143], v[144:147], v[204:207]\n" \
"buffer_load_dwordx4 v6, s[40:43], s17 offen offset:0 sc1 lds\n" \
"v_mfma_f32_16x16x32_bf16 v[236:239], a[140:143], v[148:151], v[236:239]\n" \
"v_exp_f32 v69, v69\n" \
"v_mfma_f32_16x16x32_bf16 a[12:15], a[140:143], v[152:155], a[12:15]\n" \
"ds_read_b128 a[132:135], v5 offset:1024\n" \
"v_cvt_pk_bf16_f32 v130, v68, v69\n" \
"v_mfma_f32_16x16x32_bf16 a[44:47], a[140:143], v[156:159], a[44:47]\n" \
"s_waitcnt lgkmcnt(4)\n" \
"v_exp_f32 v70, v70\n" \
"v_mfma_f32_16x16x32_bf16 v[208:211], a[144:147], v[144:147], v[208:211]\n" \
"global_load_dwordx4 a[108:111], v191, s[60:61]\n" \
"v_mfma_f32_16x16x32_bf16 v[240:243], a[144:147], v[148:151], v[240:243]\n" \
"s_lshl_b32 s92, %2, 9\n" \
"ds_read_b64_tr_b16 a[194:195], v14 offset:0\n" \
"s_add_u32 s92, s92, 94375936\n" \
"v_mfma_f32_16x16x32_bf16 a[16:19], a[144:147], v[152:155], a[16:19]\n" \
"ds_read_b128 a[136:139], v5 offset:2048\n" \
"v_lshrrev_b32 v17, 1, v4\n" \
"v_mfma_f32_16x16x32_bf16 a[48:51], a[144:147], v[156:159], a[48:51]\n" \
"s_add_u32 m0, s48, 16384\n" \
"v_add_u32 v17, s92, v17\n" \
"s_mov_b64 s[4:5], 0\n" \
"v_mfma_f32_16x16x32_bf16 v[212:215], a[148:151], v[144:147], v[212:215]\n" \
"buffer_load_dwordx4 v6, s[40:43], s17 offen offset:1024 sc1 lds\n" \
"v_mfma_f32_16x16x32_bf16 v[244:247], a[148:151], v[148:151], v[244:247]\n" \
"v_exp_f32 v71, v71\n" \
"v_mfma_f32_16x16x32_bf16 a[20:23], a[148:151], v[152:155], a[20:23]\n" \
"ds_read_b128 a[140:143], v5 offset:3072\n" \
"v_cvt_pk_bf16_f32 v131, v70, v71\n" \
"v_mfma_f32_16x16x32_bf16 a[52:55], a[148:151], v[156:159], a[52:55]\n" \
"s_waitcnt lgkmcnt(4)\n" \
"s_add_u32 m0, s48, 16384\n" \
"ds_read_b64_tr_b16 a[196:197], v15 offset:0\n" \
"v_exp_f32 v72, v72\n" \
"v_mfma_f32_16x16x32_bf16 v[216:219], a[152:155], v[144:147], v[216:219]\n" \
"buffer_load_dwordx4 v6, s[40:43], s17 offen offset:2048 sc1 lds\n" \
"v_exp_f32 v73, v73\n" \
"v_mfma_f32_16x16x32_bf16 v[248:251], a[152:155], v[148:151], v[248:251]\n" \
"s_add_u32 s44, s44, 1\n" \
"v_exp_f32 v74, v74\n" \
"v_mfma_f32_16x16x32_bf16 a[24:27], a[152:155], v[152:155], a[24:27]\n" \
"ds_read_b128 a[144:147], v5 offset:4096\n" \
"v_exp_f32 v75, v75\n" \
"v_mfma_f32_16x16x32_bf16 a[56:59], a[152:155], v[156:159], a[56:59]\n" \
"s_add_u32 m0, s48, 16384\n" \
"v_exp_f32 v76, v76\n" \
"v_mfma_f32_16x16x32_bf16 v[220:223], a[156:159], v[144:147], v[220:223]\n" \
"buffer_load_dwordx4 v6, s[40:43], s17 offen offset:3072 sc1 lds\n" \
"v_exp_f32 v77, v77\n" \
"v_mfma_f32_16x16x32_bf16 v[252:255], a[156:159], v[148:151], v[252:255]\n" \
"s_xor_b32 s48, s48, 0x10000\n" \
"v_exp_f32 v78, v78\n" \
"v_mfma_f32_16x16x32_bf16 a[28:31], a[156:159], v[152:155], a[28:31]\n" \
"ds_read_b128 a[148:151], v5 offset:5120\n" \
"v_exp_f32 v79, v79\n" \
"v_mfma_f32_16x16x32_bf16 a[60:63], a[156:159], v[156:159], a[60:63]\n" \
"s_waitcnt lgkmcnt(4)\n" \
"ds_read_b64_tr_b16 a[198:199], v16 offset:0\n" \
"v_cvt_pk_bf16_f32 v132, v72, v73\n" \
"v_cvt_pk_bf16_f32 v133, v74, v75\n" \
"v_cvt_pk_bf16_f32 v134, v76, v77\n" \
"v_cvt_pk_bf16_f32 v135, v78, v79\n" \
"v_mfma_f32_16x16x32_bf16 v[96:99], a[128:131], v[32:35], 0\n" \
"buffer_load_dwordx2 v[18:19], v17, s[36:39], 0 offen sc1\n" \
"v_mfma_f32_16x16x32_bf16 v[104:107], a[128:131], v[48:51], 0\n" \
"s_add_u32 s16, s16, s9\n" \
"v_exp_f32 v80, v80\n" \
"v_mfma_f32_16x16x32_bf16 v[112:115], a[128:131], a[64:67], 0\n" \
"ds_read_b128 a[152:155], v5 offset:6144\n" \
"v_mfma_f32_16x16x32_bf16 v[120:123], a[128:131], a[80:83], 0\n" \
"s_add_u32 m0, s49, 32768\n" \
"v_exp_f32 v81, v81\n" \
"v_mfma_f32_16x16x32_bf16 v[96:99], a[132:135], v[36:39], v[96:99]\n" \
"buffer_load_dwordx4 v6, s[40:43], s16 offen offset:0 sc1 lds\n" \
"v_mfma_f32_16x16x32_bf16 v[104:107], a[132:135], v[52:55], v[104:107]\n" \
"ds_read_b64_tr_b16 a[200:201], v13 offset:8192\n" \
"v_cvt_pk_bf16_f32 v136, v80, v81\n" \
"v_mfma_f32_16x16x32_bf16 v[112:115], a[132:135], a[68:71], v[112:115]\n" \
"ds_read_b128 a[156:159], v5 offset:7168\n" \
"v_mfma_f32_16x16x32_bf16 v[120:123], a[132:135], a[84:87], v[120:123]\n" \
"s_waitcnt lgkmcnt(4)\n" \
"v_exp_f32 v82, v82\n" \
"v_mfma_f32_16x16x32_bf16 v[96:99], a[136:139], v[40:43], v[96:99]\n" \
"buffer_store_dwordx4 v[28:31], v4, s[36:39], s80 offen offset:0 sc1\n" \
"v_mfma_f32_16x16x32_bf16 v[104:107], a[136:139], v[56:59], v[104:107]\n" \
"v_exp_f32 v83, v83\n" \
"v_mfma_f32_16x16x32_bf16 v[112:115], a[136:139], a[72:75], v[112:115]\n" \
"ds_read_b128 a[128:131], v5 offset:16384\n" \
"v_cvt_pk_bf16_f32 v137, v82, v83\n" \
"v_mfma_f32_16x16x32_bf16 v[120:123], a[136:139], a[88:91], v[120:123]\n" \
"s_add_u32 m0, s49, 32768\n" \
"v_exp_f32 v84, v84\n" \
"v_mfma_f32_16x16x32_bf16 v[96:99], a[140:143], v[44:47], v[96:99]\n" \
"buffer_load_dwordx4 v6, s[40:43], s16 offen offset:1024 sc1 lds\n" \
"v_mfma_f32_16x16x32_bf16 v[104:107], a[140:143], v[60:63], v[104:107]\n" \
"ds_read_b64_tr_b16 a[202:203], v14 offset:8192\n" \
"v_mfma_f32_16x16x32_bf16 v[112:115], a[140:143], a[76:79], v[112:115]\n" \
"ds_read_b128 a[132:135], v5 offset:17408\n" \
"v_mfma_f32_16x16x32_bf16 v[120:123], a[140:143], a[92:95], v[120:123]\n" \
"s_waitcnt lgkmcnt(4)\n" \
"v_exp_f32 v85, v85\n" \
"v_mfma_f32_16x16x32_bf16 v[100:103], a[144:147], v[32:35], 0\n" \
"buffer_store_dwordx4 v[28:31], v4, s[36:39], s80 offen offset:1024 sc1\n" \
"v_mfma_f32_16x16x32_bf16 v[108:111], a[144:147], v[48:51], 0\n" \
"v_exp_f32 v86, v86\n" \
"v_mfma_f32_16x16x32_bf16 v[116:119], a[144:147], a[64:67], 0\n" \
"ds_read_b128 a[136:139], v5 offset:18432\n" \
"v_cvt_pk_bf16_f32 v138, v84, v85\n" \
"v_mfma_f32_16x16x32_bf16 v[124:127], a[144:147], a[80:83], 0\n" \
"s_add_u32 m0, s49, 32768\n" \
"ds_read_b64_tr_b16 a[204:205], v15 offset:8192\n" \
"v_mfma_f32_16x16x32_bf16 v[100:103], a[148:151], v[36:39], v[100:103]\n" \
"buffer_load_dwordx4 v6, s[40:43], s16 offen offset:2048 sc1 lds\n" \
"v_mfma_f32_16x16x32_bf16 v[108:111], a[148:151], v[52:55], v[108:111]\n" \
"v_exp_f32 v87, v87\n" \
"v_mfma_f32_16x16x32_bf16 v[116:119], a[148:151], a[68:71], v[116:119]\n" \
"ds_read_b128 a[140:143], v5 offset:19456\n" \
"v_cvt_pk_bf16_f32 v139, v86, v87\n" \
"v_mfma_f32_16x16x32_bf16 v[124:127], a[148:151], a[84:87], v[124:127]\n" \
"s_waitcnt lgkmcnt(4)\n" \
"v_exp_f32 v88, v88\n" \
"v_mfma_f32_16x16x32_bf16 v[100:103], a[152:155], v[40:43], v[100:103]\n" \
"buffer_store_dwordx4 v[28:31], v4, s[36:39], s80 offen offset:2048 sc1\n" \
"v_mfma_f32_16x16x32_bf16 v[108:111], a[152:155], v[56:59], v[108:111]\n" \
"v_exp_f32 v89, v89\n" \
"v_mfma_f32_16x16x32_bf16 v[116:119], a[152:155], a[72:75], v[116:119]\n" \
"ds_read_b128 a[144:147], v5 offset:20480\n" \
"v_cvt_pk_bf16_f32 v140, v88, v89\n" \
"v_mfma_f32_16x16x32_bf16 v[124:127], a[152:155], a[88:91], v[124:127]\n" \
"s_add_u32 m0, s49, 32768\n" \
"ds_read_b64_tr_b16 a[206:207], v16 offset:8192\n" \
"v_exp_f32 v90, v90\n" \
"v_mfma_f32_16x16x32_bf16 v[100:103], a[156:159], v[44:47], v[100:103]\n" \
"buffer_load_dwordx4 v6, s[40:43], s16 offen offset:3072 sc1 lds\n" \
"v_exp_f32 v91, v91\n" \
"v_mfma_f32_16x16x32_bf16 v[108:111], a[156:159], v[60:63], v[108:111]\n" \
"v_exp_f32 v92, v92\n" \
"v_mfma_f32_16x16x32_bf16 v[116:119], a[156:159], a[76:79], v[116:119]\n" \
"ds_read_b128 a[148:151], v5 offset:21504\n" \
"v_exp_f32 v93, v93\n" \
"v_mfma_f32_16x16x32_bf16 v[124:127], a[156:159], a[92:95], v[124:127]\n" \
"s_waitcnt lgkmcnt(4)\n" \
"v_exp_f32 v94, v94\n" \
"v_mfma_f32_16x16x32_bf16 v[160:163], v[180:183], v[128:131], v[160:163]\n" \
"buffer_store_dwordx4 v[28:31], v4, s[36:39], s80 offen offset:3072 sc1\n" \
"v_exp_f32 v95, v95\n" \
"v_mfma_f32_16x16x32_bf16 v[164:167], v[180:183], v[132:135], v[164:167]\n" \
"v_bfe_u32 v28, v4, 7, 1\n" \
"v_bfe_u32 v29, v4, 4, 3\n" \
"v_cvt_pk_bf16_f32 v141, v90, v91\n" \
"v_cvt_pk_bf16_f32 v142, v92, v93\n" \
"v_cvt_pk_bf16_f32 v143, v94, v95\n" \
"v_mfma_f32_16x16x32_bf16 v[168:171], v[180:183], v[136:139], v[168:171]\n" \
"s_mov_b64 exec, 0xffff\nglobal_load_dword v1, v2, s[100:101]\ns_mov_b64 exec, -1\n" \
"v_mfma_f32_16x16x32_bf16 v[172:175], v[180:183], v[140:143], v[172:175]\n" \
"v_lshl_add_u32 v28, v29, 1, v28\n" \
"v_lshrrev_b32 v29, 8, v4\n" \
"v_mfma_f32_16x16x32_bf16 v[192:195], a[128:131], v[128:131], v[192:195]\n" \
"v_lshl_add_u32 v28, v29, 4, v28\n" \
"v_lshlrev_b32 v28, 4, v28\n" \
"v_mfma_f32_16x16x32_bf16 v[224:227], a[128:131], v[132:135], v[224:227]\n" \
"s_add_u32 s100, s100, 262144\ns_addc_u32 s101, s101, 0\n" \
"v_add_u32 v29, 0x10000, v28\n" \
"v_mfma_f32_16x16x32_bf16 a[0:3], a[128:131], v[136:139], a[0:3]\n" \
"ds_read_b128 a[152:155], v5 offset:22528\n" \
"v_add_u32 v30, 0x20000, v28\n" \
"v_mfma_f32_16x16x32_bf16 a[32:35], a[128:131], v[140:143], a[32:35]\n" \
"s_mov_b64 exec, 0xffff\nglobal_load_dword v1, v2, s[100:101]\ns_mov_b64 exec, -1\n" \
"v_mfma_f32_16x16x32_bf16 v[196:199], a[132:135], v[128:131], v[196:199]\n" \
"LH17_%=:\ns_cbranch_execz LHR1_%=\n" \
"v_add_u32 v31, s22, v4\n" \
"v_mfma_f32_16x16x32_bf16 v[228:231], a[132:135], v[132:135], v[228:231]\n" \
"s_waitcnt lgkmcnt(2)\n" \
"v_exp_f32 v96, v96\n" \
"v_mfma_f32_16x16x32_bf16 a[4:7], a[132:135], v[136:139], a[4:7]\n" \
"ds_read_b128 a[156:159], v5 offset:23552\n" \
"v_mfma_f32_16x16x32_bf16 a[36:39], a[132:135], v[140:143], a[36:39]\n" \
"s_waitcnt vmcnt(22) lgkmcnt(4)\n" \
"v_exp_f32 v97, v97\n" \
"v_mfma_f32_16x16x32_bf16 v[200:203], a[136:139], v[128:131], v[200:203]\n" \
"s_barrier\n" \
"v_exp_f32 v98, v98\n" \
"v_mfma_f32_16x16x32_bf16 v[232:235], a[136:139], v[132:135], v[232:235]\n" \
"s_add_u32 s17, s17, s19\n" \
"ds_read_b64_tr_b16 a[208:209], v13 offset:16384\n" \
"v_cvt_pk_bf16_f32 v144, v96, v97\n" \
"v_mfma_f32_16x16x32_bf16 a[8:11], a[136:139], v[136:139], a[8:11]\n" \
"ds_read_b128 a[128:131], v5 offset:32768\n" \
"v_mfma_f32_16x16x32_bf16 a[40:43], a[136:139], v[140:143], a[40:43]\n" \
"s_add_u32 m0, s49, 49152\n" \
"v_exp_f32 v99, v99\n" \
"v_mfma_f32_16x16x32_bf16 v[204:207], a[140:143], v[128:131], v[204:207]\n" \
"buffer_load_dwordx4 v6, s[40:43], s17 offen offset:0 sc1 lds\n" \
"v_mfma_f32_16x16x32_bf16 v[236:239], a[140:143], v[132:135], v[236:239]\n" \
"v_exp_f32 v100, v100\n" \
"v_mfma_f32_16x16x32_bf16 a[12:15], a[140:143], v[136:139], a[12:15]\n" \
"ds_read_b128 a[132:135], v5 offset:33792\n" \
"v_cvt_pk_bf16_f32 v145, v98, v99\n" \
"v_mfma_f32_16x16x32_bf16 a[44:47], a[140:143], v[140:143], a[44:47]\n" \
"s_waitcnt lgkmcnt(4)\n" \
"v_exp_f32 v101, v101\n" \
"s_add_u32 m0, s79, 131072\n" \
"v_mfma_f32_16x16x32_bf16 v[208:211], a[144:147], v[128:131], v[208:211]\n" \
"buffer_load_dwordx4 v12, s[28:31], s81 offen offset:0 lds\n" \
"v_mfma_f32_16x16x32_bf16 v[240:243], a[144:147], v[132:135], v[240:243]\n" \
"ds_read_b64_tr_b16 a[210:211], v14 offset:16384\n" \
"v_cvt_pk_bf16_f32 v146, v100, v101\n" \
"v_mfma_f32_16x16x32_bf16 a[16:19], a[144:147], v[136:139], a[16:19]\n" \
"ds_read_b128 a[136:139], v5 offset:34816\n" \
"v_mfma_f32_16x16x32_bf16 a[48:51], a[144:147], v[140:143], a[48:51]\n" \
"s_add_u32 m0, s49, 49152\n" \
"v_exp_f32 v102, v102\n" \
"v_mfma_f32_16x16x32_bf16 v[212:215], a[148:151], v[128:131], v[212:215]\n" \
"buffer_load_dwordx4 v6, s[40:43], s17 offen offset:1024 sc1 lds\n" \
"v_mfma_f32_16x16x32_bf16 v[244:247], a[148:151], v[132:135], v[244:247]\n" \
"v_exp_f32 v103, v103\n" \
"v_mfma_f32_16x16x32_bf16 a[20:23], a[148:151], v[136:139], a[20:23]\n" \
"ds_read_b128 a[140:143], v5 offset:35840\n" \
"v_cvt_pk_bf16_f32 v147, v102, v103\n" \
"v_mfma_f32_16x16x32_bf16 a[52:55], a[148:151], v[140:143], a[52:55]\n" \
"s_waitcnt lgkmcnt(4)\n" \
"s_add_u32 m0, s49, 49152\n" \
"ds_read_b64_tr_b16 a[212:213], v15 offset:16384\n" \
"v_exp_f32 v104, v104\n" \
"v_mfma_f32_16x16x32_bf16 v[216:219], a[152:155], v[128:131], v[216:219]\n" \
"buffer_load_dwordx4 v6, s[40:43], s17 offen offset:2048 sc1 lds\n" \
"v_exp_f32 v105, v105\n" \
"v_mfma_f32_16x16x32_bf16 v[248:251], a[152:155], v[132:135], v[248:251]\n" \
"v_exp_f32 v106, v106\n" \
"v_mfma_f32_16x16x32_bf16 a[24:27], a[152:155], v[136:139], a[24:27]\n" \
"ds_read_b128 a[144:147], v5 offset:36864\n" \
"v_exp_f32 v107, v107\n" \
"v_mfma_f32_16x16x32_bf16 a[56:59], a[152:155], v[140:143], a[56:59]\n" \
"s_add_u32 m0, s49, 49152\n" \
"v_exp_f32 v108, v108\n" \
"v_mfma_f32_16x16x32_bf16 v[220:223], a[156:159], v[128:131], v[220:223]\n" \
"buffer_load_dwordx4 v6, s[40:43], s17 offen offset:3072 sc1 lds\n" \
"v_exp_f32 v109, v109\n" \
"v_mfma_f32_16x16x32_bf16 v[252:255], a[156:159], v[132:135], v[252:255]\n" \
"v_exp_f32 v110, v110\n" \
"v_mfma_f32_16x16x32_bf16 a[28:31], a[156:159], v[136:139], a[28:31]\n" \
"ds_read_b128 a[148:151], v5 offset:37888\n" \
"v_exp_f32 v111, v111\n" \
"v_mfma_f32_16x16x32_bf16 a[60:63], a[156:159], v[140:143], a[60:63]\n" \
"s_waitcnt lgkmcnt(4)\n" \
"ds_read_b64_tr_b16 a[214:215], v16 offset:16384\n" \
"v_cvt_pk_bf16_f32 v148, v104, v105\n" \
"v_cvt_pk_bf16_f32 v149, v106, v107\n" \
"v_cvt_pk_bf16_f32 v150, v108, v109\n" \
"v_cvt_pk_bf16_f32 v151, v110, v111\n" \
"v_mfma_f32_16x16x32_bf16 v[64:67], a[128:131], v[32:35], 0\n" \
"v_exp_f32 v112, v112\n" \
"v_mfma_f32_16x16x32_bf16 v[72:75], a[128:131], v[48:51], 0\n" \
"s_add_u32 s16, s16, s9\n" \
"v_mfma_f32_16x16x32_bf16 v[80:83], a[128:131], a[64:67], 0\n" \
"ds_read_b128 a[152:155], v5 offset:38912\n" \
"v_mfma_f32_16x16x32_bf16 v[88:91], a[128:131], a[80:83], 0\n" \
"s_add_u32 m0, s48, 0\n" \
"v_exp_f32 v113, v113\n" \
"v_mfma_f32_16x16x32_bf16 v[64:67], a[132:135], v[36:39], v[64:67]\n" \
"buffer_load_dwordx4 v6, s[40:43], s16 offen offset:0 sc1 lds\n" \
"v_mfma_f32_16x16x32_bf16 v[72:75], a[132:135], v[52:55], v[72:75]\n" \
"ds_read_b64_tr_b16 a[216:217], v13 offset:24576\n" \
"v_cvt_pk_bf16_f32 v152, v112, v113\n" \
"v_mfma_f32_16x16x32_bf16 v[80:83], a[132:135], a[68:71], v[80:83]\n" \
"ds_read_b128 a[156:159], v5 offset:39936\n" \
"v_mfma_f32_16x16x32_bf16 v[88:91], a[132:135], a[84:87], v[88:91]\n" \
"s_waitcnt lgkmcnt(4)\n" \
"v_exp_f32 v114, v114\n" \
"s_add_u32 m0, s79, 131968\n" \
"v_mfma_f32_16x16x32_bf16 v[64:67], a[136:139], v[40:43], v[64:67]\n" \
"buffer_load_dwordx4 v12, s[28:31], s81 offen offset:128 lds\n" \
"v_mfma_f32_16x16x32_bf16 v[72:75], a[136:139], v[56:59], v[72:75]\n" \
"s_xor_b32 s49, s49, 0x10000\n" \
"s_mov_b32 s81, 589824\n" \
"v_mfma_f32_16x16x32_bf16 v[80:83], a[136:139], a[72:75], v[80:83]\n" \
"ds_read_b128 a[128:131], v5 offset:49152\n" \
"v_mfma_f32_16x16x32_bf16 v[88:91], a[136:139], a[88:91], v[88:91]\n" \
"s_add_u32 m0, s48, 0\n" \
"v_exp_f32 v115, v115\n" \
"v_mfma_f32_16x16x32_bf16 v[64:67], a[140:143], v[44:47], v[64:67]\n" \
"buffer_load_dwordx4 v6, s[40:43], s16 offen offset:1024 sc1 lds\n" \
"v_mfma_f32_16x16x32_bf16 v[72:75], a[140:143], v[60:63], v[72:75]\n" \
"ds_read_b64_tr_b16 a[218:219], v14 offset:24576\n" \
"v_cvt_pk_bf16_f32 v153, v114, v115\n" \
"v_mfma_f32_16x16x32_bf16 v[80:83], a[140:143], a[76:79], v[80:83]\n" \
"ds_read_b128 a[132:135], v5 offset:50176\n" \
"v_mfma_f32_16x16x32_bf16 v[88:91], a[140:143], a[92:95], v[88:91]\n" \
"s_waitcnt lgkmcnt(4)\n" \
"v_exp_f32 v116, v116\n" \
"s_add_u32 m0, s79, 139264\n" \
"v_mfma_f32_16x16x32_bf16 v[68:71], a[144:147], v[32:35], 0\n" \
"buffer_load_dwordx4 v12, s[28:31], s81 offen offset:0 lds\n" \
"v_mfma_f32_16x16x32_bf16 v[76:79], a[144:147], v[48:51], 0\n" \
"v_exp_f32 v117, v117\n" \
"v_mfma_f32_16x16x32_bf16 v[84:87], a[144:147], a[64:67], 0\n" \
"ds_read_b128 a[136:139], v5 offset:51200\n" \
"v_cvt_pk_bf16_f32 v154, v116, v117\n" \
"v_mfma_f32_16x16x32_bf16 v[92:95], a[144:147], a[80:83], 0\n" \
"s_add_u32 m0, s48, 0\n" \
"ds_read_b64_tr_b16 a[220:221], v15 offset:24576\n" \
"v_mfma_f32_16x16x32_bf16 v[68:71], a[148:151], v[36:39], v[68:71]\n" \
"buffer_load_dwordx4 v6, s[40:43], s16 offen offset:2048 sc1 lds\n" \
"v_mfma_f32_16x16x32_bf16 v[76:79], a[148:151], v[52:55], v[76:79]\n" \
"v_exp_f32 v118, v118\n" \
"v_mfma_f32_16x16x32_bf16 v[84:87], a[148:151], a[68:71], v[84:87]\n" \
"ds_read_b128 a[140:143], v5 offset:52224\n" \
"v_mfma_f32_16x16x32_bf16 v[92:95], a[148:151], a[84:87], v[92:95]\n" \
"s_waitcnt lgkmcnt(4)\n" \
"v_exp_f32 v119, v119\n" \
"s_add_u32 m0, s79, 140160\n" \
"v_mfma_f32_16x16x32_bf16 v[68:71], a[152:155], v[40:43], v[68:71]\n" \
"buffer_load_dwordx4 v12, s[28:31], s81 offen offset:128 lds\n" \
"v_mfma_f32_16x16x32_bf16 v[76:79], a[152:155], v[56:59], v[76:79]\n" \
"s_mov_b32 s81, 655360\n" \
"v_exp_f32 v120, v120\n" \
"v_mfma_f32_16x16x32_bf16 v[84:87], a[152:155], a[72:75], v[84:87]\n" \
"ds_read_b128 a[144:147], v5 offset:53248\n" \
"v_exp_f32 v121, v121\n" \
"v_mfma_f32_16x16x32_bf16 v[92:95], a[152:155], a[88:91], v[92:95]\n" \
"s_add_u32 m0, s48, 0\n" \
"ds_read_b64_tr_b16 a[222:223], v16 offset:24576\n" \
"v_exp_f32 v122, v122\n" \
"v_mfma_f32_16x16x32_bf16 v[68:71], a[156:159], v[44:47], v[68:71]\n" \
"buffer_load_dwordx4 v6, s[40:43], s16 offen offset:3072 sc1 lds\n" \
"v_exp_f32 v123, v123\n" \
"v_mfma_f32_16x16x32_bf16 v[76:79], a[156:159], v[60:63], v[76:79]\n" \
"v_exp_f32 v124, v124\n" \
"v_mfma_f32_16x16x32_bf16 v[84:87], a[156:159], a[76:79], v[84:87]\n" \
"ds_read_b128 a[148:151], v5 offset:54272\n" \
"v_exp_f32 v125, v125\n" \
"v_mfma_f32_16x16x32_bf16 v[92:95], a[156:159], a[92:95], v[92:95]\n" \
"s_waitcnt lgkmcnt(4)\n" \
"v_exp_f32 v126, v126\n" \
"v_mfma_f32_16x16x32_bf16 v[160:163], v[180:183], v[144:147], v[160:163]\n" \
"s_waitcnt vmcnt(22)\n" \
"v_cmp_ne_u64 vcc, s[14:15], v[18:19]\n" \
"s_nop 0\n" \
"v_exp_f32 v127, v127\n" \
"v_cvt_pk_bf16_f32 v155, v118, v119\n" \
"v_mfma_f32_16x16x32_bf16 v[164:167], v[180:183], v[148:151], v[164:167]\n" \
"s_cbranch_vccnz LPZ_%=\nLPZR_%=:\n" \
"v_and_b32 v188, 0xf0, v4\n" \
"v_lshrrev_b32 v189, 8, v4\n" \
"v_cvt_pk_bf16_f32 v156, v120, v121\n" \
"v_cvt_pk_bf16_f32 v157, v122, v123\n" \
"v_cvt_pk_bf16_f32 v158, v124, v125\n" \
"v_cvt_pk_bf16_f32 v159, v126, v127\n" \
"v_mfma_f32_16x16x32_bf16 v[168:171], v[180:183], v[152:155], v[168:171]\n" \
"v_lshl_add_u32 v188, v189, 14, v188\n" \
"v_mfma_f32_16x16x32_bf16 v[172:175], v[180:183], v[156:159], v[172:175]\n" \
"v_add_u32 v189, 0x10000, v188\n" \
"v_mfma_f32_16x16x32_bf16 v[192:195], a[128:131], v[144:147], v[192:195]\n" \
"v_exp_f32 v64, v64\n" \
"v_mfma_f32_16x16x32_bf16 v[224:227], a[128:131], v[148:151], v[224:227]\n" \
"v_exp_f32 v65, v65\n" \
"v_mfma_f32_16x16x32_bf16 a[0:3], a[128:131], v[152:155], a[0:3]\n" \
"ds_read_b128 a[152:155], v5 offset:55296\n" \
"v_cvt_pk_bf16_f32 v128, v64, v65\n" \
"v_mfma_f32_16x16x32_bf16 a[32:35], a[128:131], v[156:159], a[32:35]\n" \
"v_exp_f32 v66, v66\n" \
"v_mfma_f32_16x16x32_bf16 v[196:199], a[132:135], v[144:147], v[196:199]\n" \
"v_exp_f32 v67, v67\n" \
"v_mfma_f32_16x16x32_bf16 v[228:231], a[132:135], v[148:151], v[228:231]\n" \
"s_waitcnt lgkmcnt(2)\n" \
"v_cvt_pk_bf16_f32 v129, v66, v67\n" \
"v_mfma_f32_16x16x32_bf16 a[4:7], a[132:135], v[152:155], a[4:7]\n" \
"ds_read_b128 a[156:159], v5 offset:56320\n" \
"v_mfma_f32_16x16x32_bf16 a[36:39], a[132:135], v[156:159], a[36:39]\n" \
"s_waitcnt vmcnt(23) lgkmcnt(4)\n" \
"v_exp_f32 v68, v68\n" \
"v_mfma_f32_16x16x32_bf16 v[200:203], a[136:139], v[144:147], v[200:203]\n" \
"s_barrier\n" \
"v_xor_b32 v5, 0x10000, v5\n" \
"v_mfma_f32_16x16x32_bf16 v[232:235], a[136:139], v[148:151], v[232:235]\n" \
"s_add_u32 s17, s17, s19\n" \
"v_exp_f32 v69, v69\n" \
"v_mfma_f32_16x16x32_bf16 a[8:11], a[136:139], v[152:155], a[8:11]\n" \
"ds_read_b128 a[128:131], v5 offset:0\n" \
"v_cvt_pk_bf16_f32 v130, v68, v69\n" \
"v_mfma_f32_16x16x32_bf16 a[40:43], a[136:139], v[156:159], a[40:43]\n" \
"s_add_u32 m0, s48, 16384\n" \
"v_exp_f32 v70, v70\n" \
"v_mfma_f32_16x16x32_bf16 v[204:207], a[140:143], v[144:147], v[204:207]\n" \
"buffer_load_dwordx4 v6, s[40:43], s17 offen offset:0 sc1 lds\n" \
"v_mfma_f32_16x16x32_bf16 v[236:239], a[140:143], v[148:151], v[236:239]\n" \
"v_exp_f32 v71, v71\n" \
"v_mfma_f32_16x16x32_bf16 a[12:15], a[140:143], v[152:155], a[12:15]\n" \
"ds_read_b128 a[132:135], v5 offset:1024\n" \
"v_cvt_pk_bf16_f32 v131, v70, v71\n" \
"v_mfma_f32_16x16x32_bf16 a[44:47], a[140:143], v[156:159], a[44:47]\n" \
"s_waitcnt lgkmcnt(4)\n" \
"v_exp_f32 v72, v72\n" \
"s_add_u32 m0, s79, 147456\n" \
"v_mfma_f32_16x16x32_bf16 v[208:211], a[144:147], v[144:147], v[208:211]\n" \
"buffer_load_dwordx4 v12, s[28:31], s81 offen offset:0 lds\n" \
"v_mfma_f32_16x16x32_bf16 v[240:243], a[144:147], v[148:151], v[240:243]\n" \
"v_exp_f32 v73, v73\n" \
"v_mfma_f32_16x16x32_bf16 a[16:19], a[144:147], v[152:155], a[16:19]\n" \
"ds_read_b128 a[136:139], v5 offset:2048\n" \
"v_cvt_pk_bf16_f32 v132, v72, v73\n" \
"v_mfma_f32_16x16x32_bf16 a[48:51], a[144:147], v[156:159], a[48:51]\n" \
"s_add_u32 m0, s48, 16384\n" \
"v_exp_f32 v74, v74\n" \
"v_mfma_f32_16x16x32_bf16 v[212:215], a[148:151], v[144:147], v[212:215]\n" \
"buffer_load_dwordx4 v6, s[40:43], s17 offen offset:1024 sc1 lds\n" \
"v_mfma_f32_16x16x32_bf16 v[244:247], a[148:151], v[148:151], v[244:247]\n" \
"v_exp_f32 v75, v75\n" \
"v_mfma_f32_16x16x32_bf16 a[20:23], a[148:151], v[152:155], a[20:23]\n" \
"ds_read_b128 a[140:143], v5 offset:3072\n" \
"v_cvt_pk_bf16_f32 v133, v74, v75\n" \
"v_mfma_f32_16x16x32_bf16 a[52:55], a[148:151], v[156:159], a[52:55]\n" \
"s_waitcnt lgkmcnt(4)\n" \
"s_add_u32 m0, s48, 16384\n" \
"v_mfma_f32_16x16x32_bf16 v[216:219], a[152:155], v[144:147], v[216:219]\n" \
"buffer_load_dwordx4 v6, s[40:43], s17 offen offset:2048 sc1 lds\n" \
"v_mfma_f32_16x16x32_bf16 v[248:251], a[152:155], v[148:151], v[248:251]\n" \
"s_add_u32 s44, s44, 1\n" \
"v_exp_f32 v76, v76\n" \
"v_mfma_f32_16x16x32_bf16 a[24:27], a[152:155], v[152:155], a[24:27]\n" \
"ds_read_b128 a[144:147], v5 offset:4096\n" \
"v_mfma_f32_16x16x32_bf16 a[56:59], a[152:155], v[156:159], a[56:59]\n" \
"s_add_u32 m0, s48, 16384\n" \
"v_exp_f32 v77, v77\n" \
"v_mfma_f32_16x16x32_bf16 v[220:223], a[156:159], v[144:147], v[220:223]\n" \
"buffer_load_dwordx4 v6, s[40:43], s17 offen offset:3072 sc1 lds\n" \
"v_mfma_f32_16x16x32_bf16 v[252:255], a[156:159], v[148:151], v[252:255]\n" \
"s_xor_b32 s48, s48, 0x10000\n" \
"v_exp_f32 v78, v78\n" \
"v_mfma_f32_16x16x32_bf16 a[28:31], a[156:159], v[152:155], a[28:31]\n" \
"ds_read_b128 a[148:151], v5 offset:5120\n" \
"v_exp_f32 v79, v79\n" \
"v_mfma_f32_16x16x32_bf16 a[60:63], a[156:159], v[156:159], a[60:63]\n" \
"s_waitcnt lgkmcnt(4)\n" \
"v_cvt_pk_bf16_f32 v134, v76, v77\n" \
"v_cvt_pk_bf16_f32 v135, v78, v79\n" \
"v_mfma_f32_16x16x32_bf16 v[96:99], a[128:131], v[32:35], 0\n" \
"v_exp_f32 v80, v80\n" \
"v_mfma_f32_16x16x32_bf16 v[104:107], a[128:131], v[48:51], 0\n" \
"s_add_u32 s16, s16, s9\n" \
"v_mfma_f32_16x16x32_bf16 v[112:115], a[128:131], a[64:67], 0\n" \
"ds_read_b128 a[152:155], v5 offset:6144\n" \
"v_mfma_f32_16x16x32_bf16 v[120:123], a[128:131], a[80:83], 0\n" \
"s_add_u32 m0, s49, 32768\n" \
"v_exp_f32 v81, v81\n" \
"v_mfma_f32_16x16x32_bf16 v[96:99], a[132:135], v[36:39], v[96:99]\n" \
"buffer_load_dwordx4 v6, s[40:43], s16 offen offset:0 sc1 lds\n" \
"v_mfma_f32_16x16x32_bf16 v[104:107], a[132:135], v[52:55], v[104:107]\n" \
"v_exp_f32 v82, v82\n" \
"v_mfma_f32_16x16x32_bf16 v[112:115], a[132:135], a[68:71], v[112:115]\n" \
"ds_read_b128 a[156:159], v5 offset:7168\n" \
"v_cvt_pk_bf16_f32 v136, v80, v81\n" \
"v_mfma_f32_16x16x32_bf16 v[120:123], a[132:135], a[84:87], v[120:123]\n" \
"s_waitcnt lgkmcnt(4)\n" \
"v_exp_f32 v83, v83\n" \
"s_add_u32 m0, s79, 148352\n" \
"v_mfma_f32_16x16x32_bf16 v[96:99], a[136:139], v[40:43], v[96:99]\n" \
"buffer_load_dwordx4 v12, s[28:31], s81 offen offset:128 lds\n" \
"v_mfma_f32_16x16x32_bf16 v[104:107], a[136:139], v[56:59], v[104:107]\n" \
"s_mov_b32 s81, 720896\n" \
"v_exp_f32 v84, v84\n" \
"v_mfma_f32_16x16x32_bf16 v[112:115], a[136:139], a[72:75], v[112:115]\n" \
"ds_read_b128 a[128:131], v5 offset:16384\n" \
"v_cvt_pk_bf16_f32 v137, v82, v83\n" \
"v_mfma_f32_16x16x32_bf16 v[120:123], a[136:139], a[88:91], v[120:123]\n" \
"s_add_u32 m0, s49, 32768\n" \
"v_exp_f32 v85, v85\n" \
"v_mfma_f32_16x16x32_bf16 v[96:99], a[140:143], v[44:47], v[96:99]\n" \
"buffer_load_dwordx4 v6, s[40:43], s16 offen offset:1024 sc1 lds\n" \
"v_mfma_f32_16x16x32_bf16 v[104:107], a[140:143], v[60:63], v[104:107]\n" \
"v_exp_f32 v86, v86\n" \
"v_mfma_f32_16x16x32_bf16 v[112:115], a[140:143], a[76:79], v[112:115]\n" \
"ds_read_b128 a[132:135], v5 offset:17408\n" \
"v_cvt_pk_bf16_f32 v138, v84, v85\n" \
"v_mfma_f32_16x16x32_bf16 v[120:123], a[140:143], a[92:95], v[120:123]\n" \
"s_waitcnt lgkmcnt(4)\n" \
"v_exp_f32 v87, v87\n" \
"s_add_u32 m0, s79, 155648\n" \
"v_mfma_f32_16x16x32_bf16 v[100:103], a[144:147], v[32:35], 0\n" \
"buffer_load_dwordx4 v12, s[28:31], s81 offen offset:0 lds\n" \
"v_mfma_f32_16x16x32_bf16 v[108:111], a[144:147], v[48:51], 0\n" \
"v_exp_f32 v88, v88\n" \
"v_mfma_f32_16x16x32_bf16 v[116:119], a[144:147], a[64:67], 0\n" \
"ds_read_b128 a[136:139], v5 offset:18432\n" \
"v_cvt_pk_bf16_f32 v139, v86, v87\n" \
"v_mfma_f32_16x16x32_bf16 v[124:127], a[144:147], a[80:83], 0\n" \
"s_add_u32 m0, s49, 32768\n" \
"v_exp_f32 v89, v89\n" \
"v_mfma_f32_16x16x32_bf16 v[100:103], a[148:151], v[36:39], v[100:103]\n" \
"buffer_load_dwordx4 v6, s[40:43], s16 offen offset:2048 sc1 lds\n" \
"v_mfma_f32_16x16x32_bf16 v[108:111], a[148:151], v[52:55], v[108:111]\n" \
"v_exp_f32 v90, v90\n" \
"v_mfma_f32_16x16x32_bf16 v[116:119], a[148:151], a[68:71], v[116:119]\n" \
"ds_read_b128 a[140:143], v5 offset:19456\n" \
"v_cvt_pk_bf16_f32 v140, v88, v89\n" \
"v_mfma_f32_16x16x32_bf16 v[124:127], a[148:151], a[84:87], v[124:127]\n" \
"s_waitcnt lgkmcnt(4)\n" \
"v_exp_f32 v91, v91\n" \
"s_add_u32 m0, s79, 156544\n" \
"v_mfma_f32_16x16x32_bf16 v[100:103], a[152:155], v[40:43], v[100:103]\n" \
"buffer_load_dwordx4 v12, s[28:31], s81 offen offset:128 lds\n" \
"v_mfma_f32_16x16x32_bf16 v[108:111], a[152:155], v[56:59], v[108:111]\n" \
"v_exp_f32 v92, v92\n" \
"v_mfma_f32_16x16x32_bf16 v[116:119], a[152:155], a[72:75], v[116:119]\n" \
"ds_read_b128 a[144:147], v5 offset:20480\n" \
"v_cvt_pk_bf16_f32 v141, v90, v91\n" \
"v_mfma_f32_16x16x32_bf16 v[124:127], a[152:155], a[88:91], v[124:127]\n" \
"s_add_u32 m0, s49, 32768\n" \
"v_exp_f32 v93, v93\n" \
"v_mfma_f32_16x16x32_bf16 v[100:103], a[156:159], v[44:47], v[100:103]\n" \
"buffer_load_dwordx4 v6, s[40:43], s16 offen offset:3072 sc1 lds\n" \
"v_mfma_f32_16x16x32_bf16 v[108:111], a[156:159], v[60:63], v[108:111]\n" \
"v_exp_f32 v94, v94\n" \
"v_mfma_f32_16x16x32_bf16 v[116:119], a[156:159], a[76:79], v[116:119]\n" \
"ds_read_b128 a[148:151], v5 offset:21504\n" \
"v_cvt_pk_bf16_f32 v142, v92, v93\n" \
"v_mfma_f32_16x16x32_bf16 v[124:127], a[156:159], a[92:95], v[124:127]\n" \
"s_waitcnt lgkmcnt(4)\n" \
"v_exp_f32 v95, v95\n" \
"v_mfma_f32_16x16x32_bf16 v[160:163], v[180:183], v[128:131], v[160:163]\n" \
"v_cvt_pk_bf16_f32 v143, v94, v95\n" \
"v_mfma_f32_16x16x32_bf16 v[164:167], v[180:183], v[132:135], v[164:167]\n" \
"v_mfma_f32_16x16x32_bf16 v[168:171], v[180:183], v[136:139], v[168:171]\n" \
"v_exp_f32 v96, v96\n" \
"v_mfma_f32_16x16x32_bf16 v[172:175], v[180:183], v[140:143], v[172:175]\n" \
"v_mfma_f32_16x16x32_bf16 v[192:195], a[128:131], v[128:131], v[192:195]\n" \
"v_exp_f32 v97, v97\n" \
"v_mfma_f32_16x16x32_bf16 v[224:227], a[128:131], v[132:135], v[224:227]\n" \
"v_cvt_pk_bf16_f32 v144, v96, v97\n" \
"v_mfma_f32_16x16x32_bf16 a[0:3], a[128:131], v[136:139], a[0:3]\n" \
"ds_read_b128 a[152:155], v5 offset:22528\n" \
"v_mfma_f32_16x16x32_bf16 a[32:35], a[128:131], v[140:143], a[32:35]\n" \
"v_exp_f32 v98, v98\n" \
"v_mfma_f32_16x16x32_bf16 v[196:199], a[132:135], v[128:131], v[196:199]\n" \
"v_exp_f32 v99, v99\n" \
"v_mfma_f32_16x16x32_bf16 v[228:231], a[132:135], v[132:135], v[228:231]\n" \
"v_cvt_pk_bf16_f32 v145, v98, v99\n" \
"v_mfma_f32_16x16x32_bf16 a[4:7], a[132:135], v[136:139], a[4:7]\n" \
"ds_read_b128 a[156:159], v5 offset:23552\n" \
"v_mfma_f32_16x16x32_bf16 a[36:39], a[132:135], v[140:143], a[36:39]\n" \
"s_waitcnt vmcnt(13) lgkmcnt(4)\n" \
"v_exp_f32 v100, v100\n" \
"v_mfma_f32_16x16x32_bf16 v[200:203], a[136:139], v[128:131], v[200:203]\n" \
"s_barrier\n" \
"s_add_u32 s100, s100, 262144\ns_addc_u32 s101, s101, 0\n" \
"v_mfma_f32_16x16x32_bf16 v[232:235], a[136:139], v[132:135], v[232:235]\n" \
"s_add_u32 s17, s17, s19\n" \
"ds_read_b64_tr_b16 a[224:225], v13 offset:0\n" \
"v_mfma_f32_16x16x32_bf16 a[8:11], a[136:139], v[136:139], a[8:11]\n" \
"ds_read_b128 a[128:131], v5 offset:32768\n" \
"v_mfma_f32_16x16x32_bf16 a[40:43], a[136:139], v[140:143], a[40:43]\n" \
"s_add_u32 m0, s49, 49152\n" \
"v_exp_f32 v101, v101\n" \
"v_mfma_f32_16x16x32_bf16 v[204:207], a[140:143], v[128:131], v[204:207]\n" \
"buffer_load_dwordx4 v6, s[40:43], s17 offen offset:0 sc1 lds\n" \
"v_mfma_f32_16x16x32_bf16 v[236:239], a[140:143], v[132:135], v[236:239]\n" \
"v_exp_f32 v102, v102\n" \
"v_mfma_f32_16x16x32_bf16 a[12:15], a[140:143], v[136:139], a[12:15]\n" \
"ds_read_b128 a[132:135], v5 offset:33792\n" \
"v_cvt_pk_bf16_f32 v146, v100, v101\n" \
"v_mfma_f32_16x16x32_bf16 a[44:47], a[140:143], v[140:143], a[44:47]\n" \
"s_waitcnt lgkmcnt(4)\n" \
"v_exp_f32 v103, v103\n" \
"v_mfma_f32_16x16x32_bf16 v[208:211], a[144:147], v[128:131], v[208:211]\n" \
"buffer_load_dwordx4 a[112:115], v188, s[36:39], s84 offen sc1\n" \
"v_mfma_f32_16x16x32_bf16 v[240:243], a[144:147], v[132:135], v[240:243]\n" \
"ds_read_b64_tr_b16 a[226:227], v14 offset:0\n" \
"v_cvt_pk_bf16_f32 v147, v102, v103\n" \
"v_mfma_f32_16x16x32_bf16 a[16:19], a[144:147], v[136:139], a[16:19]\n" \
"ds_read_b128 a[136:139], v5 offset:34816\n" \
"v_mfma_f32_16x16x32_bf16 a[48:51], a[144:147], v[140:143], a[48:51]\n" \
"s_add_u32 m0, s49, 49152\n" \
"v_exp_f32 v104, v104\n" \
"v_mfma_f32_16x16x32_bf16 v[212:215], a[148:151], v[128:131], v[212:215]\n" \
"buffer_load_dwordx4 v6, s[40:43], s17 offen offset:1024 sc1 lds\n" \
"v_mfma_f32_16x16x32_bf16 v[244:247], a[148:151], v[132:135], v[244:247]\n" \
"v_exp_f32 v105, v105\n" \
"v_mfma_f32_16x16x32_bf16 a[20:23], a[148:151], v[136:139], a[20:23]\n" \
"ds_read_b128 a[140:143], v5 offset:35840\n" \
"v_cvt_pk_bf16_f32 v148, v104, v105\n" \
"v_mfma_f32_16x16x32_bf16 a[52:55], a[148:151], v[140:143], a[52:55]\n" \
"s_waitcnt lgkmcnt(4)\n" \
"s_add_u32 m0, s49, 49152\n" \
"ds_read_b64_tr_b16 a[228:229], v15 offset:0\n" \
"v_mfma_f32_16x16x32_bf16 v[216:219], a[152:155], v[128:131], v[216:219]\n" \
"buffer_load_dwordx4 v6, s[40:43], s17 offen offset:2048 sc1 lds\n" \
"v_mfma_f32_16x16x32_bf16 v[248:251], a[152:155], v[132:135], v[248:251]\n" \
"v_exp_f32 v106, v106\n" \
"v_mfma_f32_16x16x32_bf16 a[24:27], a[152:155], v[136:139], a[24:27]\n" \
"ds_read_b128 a[144:147], v5 offset:36864\n" \
"v_exp_f32 v107, v107\n" \
"v_mfma_f32_16x16x32_bf16 a[56:59], a[152:155], v[140:143], a[56:59]\n" \
"s_add_u32 m0, s49, 49152\n" \
"v_exp_f32 v108, v108\n" \
"v_mfma_f32_16x16x32_bf16 v[220:223], a[156:159], v[128:131], v[220:223]\n" \
"buffer_load_dwordx4 v6, s[40:43], s17 offen offset:3072 sc1 lds\n" \
"v_exp_f32 v109, v109\n" \
"v_mfma_f32_16x16x32_bf16 v[252:255], a[156:159], v[132:135], v[252:255]\n" \
"v_exp_f32 v110, v110\n" \
"v_mfma_f32_16x16x32_bf16 a[28:31], a[156:159], v[136:139], a[28:31]\n" \
"ds_read_b128 a[148:151], v5 offset:37888\n" \
"v_exp_f32 v111, v111\n" \
"v_mfma_f32_16x16x32_bf16 a[60:63], a[156:159], v[140:143], a[60:63]\n" \
"s_waitcnt lgkmcnt(4)\n" \
"ds_read_b64_tr_b16 a[230:231], v16 offset:0\n" \
"v_cvt_pk_bf16_f32 v149, v106, v107\n" \
"v_cvt_pk_bf16_f32 v150, v108, v109\n" \
"v_cvt_pk_bf16_f32 v151, v110, v111\n" \
"v_mfma_f32_16x16x32_bf16 v[64:67], a[128:131], v[32:35], 0\n" \
"buffer_load_dwordx4 a[116:119], v189, s[36:39], s84 offen sc1\n" \
"v_mfma_f32_16x16x32_bf16 v[72:75], a[128:131], v[48:51], 0\n" \
"s_add_u32 s16, s16, s9\n" \
"v_exp_f32 v112, v112\n" \
"v_mfma_f32_16x16x32_bf16 v[80:83], a[128:131], a[64:67], 0\n" \
"ds_read_b128 a[152:155], v5 offset:38912\n" \
"v_mfma_f32_16x16x32_bf16 v[88:91], a[128:131], a[80:83], 0\n" \
"s_add_u32 m0, s48, 0\n" \
"v_exp_f32 v113, v113\n" \
"v_mfma_f32_16x16x32_bf16 v[64:67], a[132:135], v[36:39], v[64:67]\n" \
"buffer_load_dwordx4 v6, s[40:43], s16 offen offset:0 sc1 lds\n" \
"v_mfma_f32_16x16x32_bf16 v[72:75], a[132:135], v[52:55], v[72:75]\n" \
"ds_read_b64_tr_b16 a[232:233], v13 offset:8192\n" \
"v_cvt_pk_bf16_f32 v152, v112, v113\n" \
"v_mfma_f32_16x16x32_bf16 v[80:83], a[132:135], a[68:71], v[80:83]\n" \
"ds_read_b128 a[156:159], v5 offset:39936\n" \
"v_mfma_f32_16x16x32_bf16 v[88:91], a[132:135], a[84:87], v[88:91]\n" \
"s_waitcnt lgkmcnt(4)\n" \
"v_exp_f32 v114, v114\n" \
"v_mfma_f32_16x16x32_bf16 v[64:67], a[136:139], v[40:43], v[64:67]\n" \
"s_mov_b64 exec, 0xffff\nglobal_load_dword v1, v2, s[100:101]\ns_mov_b64 exec, -1\n" \
"v_mfma_f32_16x16x32_bf16 v[72:75], a[136:139], v[56:59], v[72:75]\n" \
"s_xor_b32 s49, s49, 0x10000\n" \
"v_exp_f32 v115, v115\n" \
"v_mfma_f32_16x16x32_bf16 v[80:83], a[136:139], a[72:75], v[80:83]\n" \
"ds_read_b128 a[128:131], v5 offset:49152\n" \
"v_cvt_pk_bf16_f32 v153, v114, v115\n" \
"v_mfma_f32_16x16x32_bf16 v[88:91], a[136:139], a[88:91], v[88:91]\n" \
"s_add_u32 m0, s48, 0\n" \
"v_exp_f32 v116, v116\n" \
"v_mfma_f32_16x16x32_bf16 v[64:67], a[140:143], v[44:47], v[64:67]\n" \
"buffer_load_dwordx4 v6, s[40:43], s16 offen offset:1024 sc1 lds\n" \
"v_mfma_f32_16x16x32_bf16 v[72:75], a[140:143], v[60:63], v[72:75]\n" \
"ds_read_b64_tr_b16 a[234:235], v14 offset:8192\n" \
"v_mfma_f32_16x16x32_bf16 v[80:83], a[140:143], a[76:79], v[80:83]\n" \
"ds_read_b128 a[132:135], v5 offset:50176\n" \
"v_mfma_f32_16x16x32_bf16 v[88:91], a[140:143], a[92:95], v[88:91]\n" \
"s_waitcnt lgkmcnt(4)\n" \
"v_exp_f32 v117, v117\n" \
"v_mfma_f32_16x16x32_bf16 v[68:71], a[144:147], v[32:35], 0\n" \
"buffer_load_dwordx4 a[120:123], v188, s[36:39], s85 offen sc1\n" \
"v_mfma_f32_16x16x32_bf16 v[76:79], a[144:147], v[48:51], 0\n" \
"v_exp_f32 v118, v118\n" \
"v_mfma_f32_16x16x32_bf16 v[84:87], a[144:147], a[64:67], 0\n" \
"ds_read_b128 a[136:139], v5 offset:51200\n" \
"v_cvt_pk_bf16_f32 v154, v116, v117\n" \
"v_mfma_f32_16x16x32_bf16 v[92:95], a[144:147], a[80:83], 0\n" \
"s_add_u32 m0, s48, 0\n" \
"ds_read_b64_tr_b16 a[236:237], v15 offset:8192\n" \
"v_mfma_f32_16x16x32_bf16 v[68:71], a[148:151], v[36:39], v[68:71]\n" \
"buffer_load_dwordx4 v6, s[40:43], s16 offen offset:2048 sc1 lds\n" \
"v_mfma_f32_16x16x32_bf16 v[76:79], a[148:151], v[52:55], v[76:79]\n" \
"v_exp_f32 v119, v119\n" \
"v_mfma_f32_16x16x32_bf16 v[84:87], a[148:151], a[68:71], v[84:87]\n" \
"ds_read_b128 a[140:143], v5 offset:52224\n" \
"v_cvt_pk_bf16_f32 v155, v118, v119\n" \
"v_mfma_f32_16x16x32_bf16 v[92:95], a[148:151], a[84:87], v[92:95]\n" \
"s_waitcnt lgkmcnt(4)\n" \
"v_exp_f32 v120, v120\n" \
"v_mfma_f32_16x16x32_bf16 v[68:71], a[152:155], v[40:43], v[68:71]\n" \
"v_exp_f32 v121, v121\n" \
"v_mfma_f32_16x16x32_bf16 v[76:79], a[152:155], v[56:59], v[76:79]\n" \
"v_exp_f32 v122, v122\n" \
"v_mfma_f32_16x16x32_bf16 v[84:87], a[152:155], a[72:75], v[84:87]\n" \
"ds_read_b128 a[144:147], v5 offset:53248\n" \
"v_cvt_pk_bf16_f32 v156, v120, v121\n" \
"v_mfma_f32_16x16x32_bf16 v[92:95], a[152:155], a[88:91], v[92:95]\n" \
"s_add_u32 m0, s48, 0\n" \
"ds_read_b64_tr_b16 a[238:239], v16 offset:8192\n" \
"v_mfma_f32_16x16x32_bf16 v[68:71], a[156:159], v[44:47], v[68:71]\n" \
"buffer_load_dwordx4 v6, s[40:43], s16 offen offset:3072 sc1 lds\n" \
"v_exp_f32 v123, v123\n" \
"v_mfma_f32_16x16x32_bf16 v[76:79], a[156:159], v[60:63], v[76:79]\n" \
"v_exp_f32 v124, v124\n" \
"v_mfma_f32_16x16x32_bf16 v[84:87], a[156:159], a[76:79], v[84:87]\n" \
"ds_read_b128 a[148:151], v5 offset:54272\n" \
"v_exp_f32 v125, v125\n" \
"v_mfma_f32_16x16x32_bf16 v[92:95], a[156:159], a[92:95], v[92:95]\n" \
"s_waitcnt lgkmcnt(4)\n" \
"v_exp_f32 v126, v126\n" \
"v_mfma_f32_16x16x32_bf16 v[160:163], v[180:183], v[144:147], v[160:163]\n" \
"buffer_load_dwordx4 a[124:127], v189, s[36:39], s85 offen sc1\n" \
"v_exp_f32 v127, v127\n" \
"v_mfma_f32_16x16x32_bf16 v[164:167], v[180:183], v[148:151], v[164:167]\n" \
"v_cvt_pk_bf16_f32 v157, v122, v123\n" \
"v_cvt_pk_bf16_f32 v158, v124, v125\n" \
"v_cvt_pk_bf16_f32 v159, v126, v127\n" \
"v_mfma_f32_16x16x32_bf16 v[168:171], v[180:183], v[152:155], v[168:171]\n" \
"buffer_store_dwordx2 v[10:11], v20, s[36:39], s12 offen sc1\n" \
"v_mfma_f32_16x16x32_bf16 v[172:175], v[180:183], v[156:159], v[172:175]\n" \
"v_bfe_u32 v190, v4, 7, 2\n" \
"v_min_u32 v190, 2, v190\n" \
"v_mfma_f32_16x16x32_bf16 v[192:195], a[128:131], v[144:147], v[192:195]\n" \
"v_lshl_add_u32 v17, v190, 8, s98\n" \
"v_bfe_u32 v190, v4, 4, 3\n" \
"v_mfma_f32_16x16x32_bf16 v[224:227], a[128:131], v[148:151], v[224:227]\n" \
"s_lshr_b32 s92, %1, 5\n" \
"v_lshl_add_u32 v17, v190, 3, v17\n" \
"s_lshl_b32 s92, s92, 2\n" \
"v_mfma_f32_16x16x32_bf16 a[0:3], a[128:131], v[152:155], a[0:3]\n" \
"s_add_u32 s92, s92, %2\n" \
"ds_read_b128 a[152:155], v5 offset:55296\n" \
"s_lshl_b32 s92, s92, 11\n" \
"v_mfma_f32_16x16x32_bf16 a[32:35], a[128:131], v[156:159], a[32:35]\n" \
"v_and_b32 v188, 0xf0, v4\n" \
"v_lshl_add_u32 v188, v188, 3, s92\n" \
"v_mfma_f32_16x16x32_bf16 v[196:199], a[132:135], v[144:147], v[196:199]\n" \
"s_cmp_ge_u32 %2, 2\ns_cselect_b64 vcc, -1, 0\n" \
"v_lshrrev_b32 v189, 8, v4\n" \
"v_mfma_f32_16x16x32_bf16 v[228:231], a[132:135], v[148:151], v[228:231]\n" \
"s_waitcnt lgkmcnt(2)\n" \
"v_lshl_add_u32 v190, v189, 18, v188\n" \
"v_min_u32 v189, 2, v189\n" \
"v_mfma_f32_16x16x32_bf16 a[4:7], a[132:135], v[152:155], a[4:7]\n" \
"s_mul_i32 s92, s90, 12\n" \
"ds_read_b128 a[156:159], v5 offset:56320\n" \
"s_add_u32 s92, s92, s89\n" \
"v_mfma_f32_16x16x32_bf16 a[36:39], a[132:135], v[156:159], a[36:39]\n" \
"s_waitcnt vmcnt(15) lgkmcnt(4)\n" \
"s_lshl_b32 s92, s92, 16\n" \
"v_lshl_add_u32 v191, v189, 18, v188\n" \
"v_mfma_f32_16x16x32_bf16 v[200:203], a[136:139], v[144:147], v[200:203]\n" \
"s_barrier\n" \
"v_xor_b32 v5, 0x10000, v5\n" \
"v_add_u32 v191, s92, v191\n" \
"v_mfma_f32_16x16x32_bf16 v[232:235], a[136:139], v[148:151], v[232:235]\n" \
"s_add_u32 s17, s17, s19\n" \
"ds_read_b64_tr_b16 a[240:241], v13 offset:16384\n" \
"s_lshl_b32 s92, %2, 13\n" \
"v_mfma_f32_16x16x32_bf16 a[8:11], a[136:139], v[152:155], a[8:11]\n" \
"s_add_u32 s92, s92, 131072\n" \
"ds_read_b128 a[128:131], v5 offset:0\n" \
"v_add_u32 v186, s92, v4\n" \
"v_mfma_f32_16x16x32_bf16 a[40:43], a[136:139], v[156:159], a[40:43]\n" \
"s_add_u32 m0, s48, 16384\n" \
"s_cbranch_vccz LQW17_%=\nds_write_b128 v21, a[64:67] offset:0\nLQW17_%=:\n" \
"v_mfma_f32_16x16x32_bf16 v[204:207], a[140:143], v[144:147], v[204:207]\n" \
"buffer_load_dwordx4 v6, s[40:43], s17 offen offset:0 sc1 lds\n" \
"v_mfma_f32_16x16x32_bf16 v[236:239], a[140:143], v[148:151], v[236:239]\n" \
"s_cbranch_vccz LQW18_%=\nds_write_b128 v21, a[68:71] offset:1024\nLQW18_%=:\n" \
"v_mfma_f32_16x16x32_bf16 a[12:15], a[140:143], v[152:155], a[12:15]\n" \
"s_lshl_b32 s92, %2, 9\n" \
"ds_read_b128 a[132:135], v5 offset:1024\n" \
"v_xor_b32 v187, 0x6000, v186\n" \
"v_mfma_f32_16x16x32_bf16 a[44:47], a[140:143], v[156:159], a[44:47]\n" \
"s_waitcnt lgkmcnt(4)\n" \
"s_cbranch_vccz LQW19_%=\nds_write_b128 v21, a[72:75] offset:2048\nLQW19_%=:\n" \
"v_exp_f32 v64, v64\n" \
"v_mfma_f32_16x16x32_bf16 v[208:211], a[144:147], v[144:147], v[208:211]\n" \
"s_add_u32 s92, s92, 81920\n" \
"ds_read_b64_tr_b16 a[242:243], v14 offset:16384\n" \
"v_lshrrev_b32 v184, 2, v4\n" \
"v_exp_f32 v65, v65\n" \
"v_mfma_f32_16x16x32_bf16 v[240:243], a[144:147], v[148:151], v[240:243]\n" \
"LH18_%=:\ns_cbranch_execz LHR1_%=\n" \
"s_cbranch_vccz LQW20_%=\nds_write_b128 v21, a[76:79] offset:3072\nLQW20_%=:\n" \
"v_exp_f32 v66, v66\n" \
"v_mfma_f32_16x16x32_bf16 a[16:19], a[144:147], v[152:155], a[16:19]\n" \
"ds_read_b128 a[136:139], v5 offset:2048\n" \
"v_add_u32 v184, s92, v184\n" \
"v_exp_f32 v67, v67\n" \
"v_mfma_f32_16x16x32_bf16 a[48:51], a[144:147], v[156:159], a[48:51]\n" \
"s_add_u32 m0, s48, 16384\n" \
"s_cbranch_vccz LQW21_%=\nds_write_b128 v21, a[80:83] offset:4096\nLQW21_%=:\n" \
"v_exp_f32 v68, v68\n" \
"v_mfma_f32_16x16x32_bf16 v[212:215], a[148:151], v[144:147], v[212:215]\n" \
"buffer_load_dwordx4 v6, s[40:43], s17 offen offset:1024 sc1 lds\n" \
"v_exp_f32 v69, v69\n" \
"v_mfma_f32_16x16x32_bf16 v[244:247], a[148:151], v[148:151], v[244:247]\n" \
"s_cbranch_vccz LQW22_%=\nds_write_b128 v21, a[84:87] offset:5120\nLQW22_%=:\n" \
"v_exp_f32 v70, v70\n" \
"v_mfma_f32_16x16x32_bf16 a[20:23], a[148:151], v[152:155], a[20:23]\n" \
"ds_read_b128 a[140:143], v5 offset:3072\n" \
"v_xor_b32 v185, 0x600, v184\n" \
"v_exp_f32 v71, v71\n" \
"v_mfma_f32_16x16x32_bf16 a[52:55], a[148:151], v[156:159], a[52:55]\n" \
"s_waitcnt lgkmcnt(4)\n" \
"s_add_u32 m0, s48, 16384\n" \
"ds_read_b64_tr_b16 a[244:245], v15 offset:16384\n" \
"v_exp_f32 v72, v72\n" \
"v_mfma_f32_16x16x32_bf16 v[216:219], a[152:155], v[144:147], v[216:219]\n" \
"buffer_load_dwordx4 v6, s[40:43], s17 offen offset:2048 sc1 lds\n" \
"v_exp_f32 v73, v73\n" \
"v_mfma_f32_16x16x32_bf16 v[248:251], a[152:155], v[148:151], v[248:251]\n" \
"s_add_u32 s44, s44, 1\n" \
"s_cbranch_vccz LQW23_%=\nds_write_b128 v21, a[88:91] offset:6144\nLQW23_%=:\n" \
"v_exp_f32 v74, v74\n" \
"v_mfma_f32_16x16x32_bf16 a[24:27], a[152:155], v[152:155], a[24:27]\n" \
"ds_read_b128 a[144:147], v5 offset:4096\n" \
"v_mov_b32 v2, v190\n" \
"v_exp_f32 v75, v75\n" \
"v_mfma_f32_16x16x32_bf16 a[56:59], a[152:155], v[156:159], a[56:59]\n" \
"s_add_u32 m0, s48, 16384\n" \
"s_cbranch_vccz LQW24_%=\nds_write_b128 v21, a[92:95] offset:7168\nLQW24_%=:\n" \
"v_exp_f32 v76, v76\n" \
"v_mfma_f32_16x16x32_bf16 v[220:223], a[156:159], v[144:147], v[220:223]\n" \
"buffer_load_dwordx4 v6, s[40:43], s17 offen offset:3072 sc1 lds\n" \
"v_exp_f32 v77, v77\n" \
"v_mfma_f32_16x16x32_bf16 v[252:255], a[156:159], v[148:151], v[252:255]\n" \
"s_xor_b32 s48, s48, 0x10000\n" \
"s_waitcnt vmcnt(44)\n" \
"v_exp_f32 v78, v78\n" \
"v_mfma_f32_16x16x32_bf16 a[28:31], a[156:159], v[152:155], a[28:31]\n" \
"ds_read_b128 a[148:151], v5 offset:5120\n" \
"v_exp_f32 v79, v79\n" \
"v_mfma_f32_16x16x32_bf16 a[60:63], a[156:159], v[156:159], a[60:63]\n" \
"s_waitcnt lgkmcnt(4)\n" \
"ds_read_b64_tr_b16 a[246:247], v16 offset:16384\n" \
"v_cvt_pk_bf16_f32 v128, v64, v65\n" \
"v_cvt_pk_bf16_f32 v129, v66, v67\n" \
"v_cvt_pk_bf16_f32 v130, v68, v69\n" \
"v_cvt_pk_bf16_f32 v131, v70, v71\n" \
"v_cvt_pk_bf16_f32 v132, v72, v73\n" \
"v_cvt_pk_bf16_f32 v133, v74, v75\n" \
"v_cvt_pk_bf16_f32 v134, v76, v77\n" \
"v_cvt_pk_bf16_f32 v135, v78, v79\n" \
"v_mfma_f32_16x16x32_bf16 v[96:99], a[128:131], v[32:35], 0\n" \
"buffer_store_dwordx2 v[10:11], v20, s[36:39], s88 offen sc1\n" \
"v_mfma_f32_16x16x32_bf16 v[104:107], a[128:131], v[48:51], 0\n" \
"s_add_u32 s16, s16, s9\n" \
"s_mov_b32 s46, s23\n" \
"v_mfma_f32_16x16x32_bf16 v[112:115], a[128:131], a[64:67], 0\n" \
"ds_read_b128 a[152:155], v5 offset:6144\n" \
"v_mfma_f32_16x16x32_bf16 v[120:123], a[128:131], a[80:83], 0\n" \
"s_add_u32 m0, s49, 32768\n" \
"v_exp_f32 v80, v80\n" \
"v_mfma_f32_16x16x32_bf16 v[96:99], a[132:135], v[36:39], v[96:99]\n" \
"buffer_load_dwordx4 v6, s[40:43], s16 offen offset:0 sc1 lds\n" \
"v_mfma_f32_16x16x32_bf16 v[104:107], a[132:135], v[52:55], v[104:107]\n" \
"ds_read_b64_tr_b16 a[248:249], v13 offset:24576\n" \
"v_mfma_f32_16x16x32_bf16 v[112:115], a[132:135], a[68:71], v[112:115]\n" \
"ds_read_b128 a[156:159], v5 offset:7168\n" \
"v_mfma_f32_16x16x32_bf16 v[120:123], a[132:135], a[84:87], v[120:123]\n" \
"s_waitcnt lgkmcnt(4)\n" \
"v_exp_f32 v81, v81\n" \
"v_mfma_f32_16x16x32_bf16 v[96:99], a[136:139], v[40:43], v[96:99]\n" \
"v_exp_f32 v82, v82\n" \
"v_mfma_f32_16x16x32_bf16 v[104:107], a[136:139], v[56:59], v[104:107]\n" \
"v_exp_f32 v83, v83\n" \
"v_mfma_f32_16x16x32_bf16 v[112:115], a[136:139], a[72:75], v[112:115]\n" \
"ds_read_b128 a[128:131], v5 offset:16384\n" \
"v_cvt_pk_bf16_f32 v136, v80, v81\n" \
"v_mfma_f32_16x16x32_bf16 v[120:123], a[136:139], a[88:91], v[120:123]\n" \
"s_add_u32 m0, s49, 32768\n" \
"v_exp_f32 v84, v84\n" \
"v_mfma_f32_16x16x32_bf16 v[96:99], a[140:143], v[44:47], v[96:99]\n" \
"buffer_load_dwordx4 v6, s[40:43], s16 offen offset:1024 sc1 lds\n" \
"v_mfma_f32_16x16x32_bf16 v[104:107], a[140:143], v[60:63], v[104:107]\n" \
"ds_read_b64_tr_b16 a[250:251], v14 offset:24576\n" \
"v_cvt_pk_bf16_f32 v137, v82, v83\n" \
"v_mfma_f32_16x16x32_bf16 v[112:115], a[140:143], a[76:79], v[112:115]\n" \
"ds_read_b128 a[132:135], v5 offset:17408\n" \
"v_mfma_f32_16x16x32_bf16 v[120:123], a[140:143], a[92:95], v[120:123]\n" \
"s_waitcnt lgkmcnt(4)\n" \
"v_exp_f32 v85, v85\n" \
"v_mfma_f32_16x16x32_bf16 v[100:103], a[144:147], v[32:35], 0\n" \
"v_exp_f32 v86, v86\n" \
"v_mfma_f32_16x16x32_bf16 v[108:111], a[144:147], v[48:51], 0\n" \
"v_exp_f32 v87, v87\n" \
"v_mfma_f32_16x16x32_bf16 v[116:119], a[144:147], a[64:67], 0\n" \
"ds_read_b128 a[136:139], v5 offset:18432\n" \
"v_cvt_pk_bf16_f32 v138, v84, v85\n" \
"v_mfma_f32_16x16x32_bf16 v[124:127], a[144:147], a[80:83], 0\n" \
"s_add_u32 m0, s49, 32768\n" \
"ds_read_b64_tr_b16 a[252:253], v15 offset:24576\n" \
"v_cvt_pk_bf16_f32 v139, v86, v87\n" \
"v_mfma_f32_16x16x32_bf16 v[100:103], a[148:151], v[36:39], v[100:103]\n" \
"buffer_load_dwordx4 v6, s[40:43], s16 offen offset:2048 sc1 lds\n" \
"v_mfma_f32_16x16x32_bf16 v[108:111], a[148:151], v[52:55], v[108:111]\n" \
"v_exp_f32 v88, v88\n" \
"v_mfma_f32_16x16x32_bf16 v[116:119], a[148:151], a[68:71], v[116:119]\n" \
"ds_read_b128 a[140:143], v5 offset:19456\n" \
"v_mfma_f32_16x16x32_bf16 v[124:127], a[148:151], a[84:87], v[124:127]\n" \
"s_waitcnt lgkmcnt(4)\n" \
"v_exp_f32 v89, v89\n" \
"v_mfma_f32_16x16x32_bf16 v[100:103], a[152:155], v[40:43], v[100:103]\n" \
"v_exp_f32 v90, v90\n" \
"v_mfma_f32_16x16x32_bf16 v[108:111], a[152:155], v[56:59], v[108:111]\n" \
"v_exp_f32 v91, v91\n" \
"v_mfma_f32_16x16x32_bf16 v[116:119], a[152:155], a[72:75], v[116:119]\n" \
"ds_read_b128 a[144:147], v5 offset:20480\n" \
"v_cvt_pk_bf16_f32 v140, v88, v89\n" \
"v_mfma_f32_16x16x32_bf16 v[124:127], a[152:155], a[88:91], v[124:127]\n" \
"s_add_u32 m0, s49, 32768\n" \
"ds_read_b64_tr_b16 a[254:255], v16 offset:24576\n" \
"v_cvt_pk_bf16_f32 v141, v90, v91\n" \
"v_mfma_f32_16x16x32_bf16 v[100:103], a[156:159], v[44:47], v[100:103]\n" \
"buffer_load_dwordx4 v6, s[40:43], s16 offen offset:3072 sc1 lds\n" \
"v_mfma_f32_16x16x32_bf16 v[108:111], a[156:159], v[60:63], v[108:111]\n" \
"v_exp_f32 v92, v92\n" \
"v_mfma_f32_16x16x32_bf16 v[116:119], a[156:159], a[76:79], v[116:119]\n" \
"ds_read_b128 a[148:151], v5 offset:21504\n" \
"v_exp_f32 v93, v93\n" \
"v_mfma_f32_16x16x32_bf16 v[124:127], a[156:159], a[92:95], v[124:127]\n" \
"s_waitcnt lgkmcnt(4)\n" \
"v_exp_f32 v94, v94\n" \
"v_mfma_f32_16x16x32_bf16 v[160:163], v[180:183], v[128:131], v[160:163]\n" \
"v_exp_f32 v95, v95\n" \
"v_mfma_f32_16x16x32_bf16 v[164:167], v[180:183], v[132:135], v[164:167]\n" \
"v_cvt_pk_bf16_f32 v142, v92, v93\n" \
"v_cvt_pk_bf16_f32 v143, v94, v95\n" \
"v_mfma_f32_16x16x32_bf16 v[168:171], v[180:183], v[136:139], v[168:171]\n" \
"v_exp_f32 v96, v96\n" \
"v_mfma_f32_16x16x32_bf16 v[172:175], v[180:183], v[140:143], v[172:175]\n" \
"v_mfma_f32_16x16x32_bf16 v[192:195], a[128:131], v[128:131], v[192:195]\n" \
"v_exp_f32 v97, v97\n" \
"v_mfma_f32_16x16x32_bf16 v[224:227], a[128:131], v[132:135], v[224:227]\n" \
"v_cvt_pk_bf16_f32 v144, v96, v97\n" \
"v_mfma_f32_16x16x32_bf16 a[0:3], a[128:131], v[136:139], a[0:3]\n" \
"ds_read_b128 a[152:155], v5 offset:22528\n" \
"v_mfma_f32_16x16x32_bf16 a[32:35], a[128:131], v[140:143], a[32:35]\n" \
"v_exp_f32 v98, v98\n" \
"v_mfma_f32_16x16x32_bf16 v[196:199], a[132:135], v[128:131], v[196:199]\n" \
"v_exp_f32 v99, v99\n" \
"v_mfma_f32_16x16x32_bf16 v[228:231], a[132:135], v[132:135], v[228:231]\n" \
"s_waitcnt lgkmcnt(2)\n" \
"v_cvt_pk_bf16_f32 v145, v98, v99\n" \
"v_mfma_f32_16x16x32_bf16 a[4:7], a[132:135], v[136:139], a[4:7]\n" \
"ds_read_b128 a[156:159], v5 offset:23552\n" \
"v_mfma_f32_16x16x32_bf16 a[36:39], a[132:135], v[140:143], a[36:39]\n" \
"s_waitcnt vmcnt(18) lgkmcnt(4)\n" \
"v_exp_f32 v100, v100\n" \
"v_mfma_f32_16x16x32_bf16 v[200:203], a[136:139], v[128:131], v[200:203]\n" \
"s_barrier\n" \
"v_exp_f32 v101, v101\n" \
"v_mfma_f32_16x16x32_bf16 v[232:235], a[136:139], v[132:135], v[232:235]\n" \
"s_add_u32 s17, s17, s19\n" \
"s_cbranch_vccz LQW25_%=\nds_write_b128 v21, v[32:35] offset:16384\nLQW25_%=:\n" \
"v_mfma_f32_16x16x32_bf16 a[8:11], a[136:139], v[136:139], a[8:11]\n" \
"ds_read_b128 a[128:131], v5 offset:32768\n" \
"v_cvt_pk_bf16_f32 v146, v100, v101\n" \
"v_mfma_f32_16x16x32_bf16 a[40:43], a[136:139], v[140:143], a[40:43]\n" \
"s_add_u32 m0, s49, 49152\n" \
"s_cbranch_vccz LQW26_%=\nds_write_b128 v21, v[36:39] offset:17408\nLQW26_%=:\n" \
"v_mfma_f32_16x16x32_bf16 v[204:207], a[140:143], v[128:131], v[204:207]\n" \
"buffer_load_dwordx4 v6, s[40:43], s17 offen offset:0 sc1 lds\n" \
"v_mfma_f32_16x16x32_bf16 v[236:239], a[140:143], v[132:135], v[236:239]\n" \
"s_cbranch_vccz LQW27_%=\nds_write_b128 v21, v[40:43] offset:18432\nLQW27_%=:\n" \
"v_mfma_f32_16x16x32_bf16 a[12:15], a[140:143], v[136:139], a[12:15]\n" \
"ds_read_b128 a[132:135], v5 offset:33792\n" \
"v_mfma_f32_16x16x32_bf16 a[44:47], a[140:143], v[140:143], a[44:47]\n" \
"s_waitcnt lgkmcnt(4)\n" \
"s_cbranch_vccz LQW28_%=\nds_write_b128 v21, v[44:47] offset:19456\nLQW28_%=:\n" \
"v_mfma_f32_16x16x32_bf16 v[208:211], a[144:147], v[128:131], v[208:211]\n" \
"s_cbranch_vccz LQW29_%=\nds_write_b128 v21, v[48:51] offset:20480\nLQW29_%=:\n" \
"v_mfma_f32_16x16x32_bf16 v[240:243], a[144:147], v[132:135], v[240:243]\n" \
"s_cbranch_vccz LQW30_%=\nds_write_b128 v21, v[52:55] offset:21504\nLQW30_%=:\n" \
"v_mfma_f32_16x16x32_bf16 a[16:19], a[144:147], v[136:139], a[16:19]\n" \
"ds_read_b128 a[136:139], v5 offset:34816\n" \
"v_mfma_f32_16x16x32_bf16 a[48:51], a[144:147], v[140:143], a[48:51]\n" \
"s_add_u32 m0, s49, 49152\n" \
"s_cbranch_vccz LQW31_%=\nds_write_b128 v21, v[56:59] offset:22528\nLQW31_%=:\n" \
"v_mfma_f32_16x16x32_bf16 v[212:215], a[148:151], v[128:131], v[212:215]\n" \
"buffer_load_dwordx4 v6, s[40:43], s17 offen offset:1024 sc1 lds\n" \
"v_mfma_f32_16x16x32_bf16 v[244:247], a[148:151], v[132:135], v[244:247]\n" \
"s_cbranch_vccz LQW32_%=\nds_write_b128 v21, v[60:63] offset:23552\nLQW32_%=:\n" \
"v_exp_f32 v102, v102\n" \
"v_mfma_f32_16x16x32_bf16 a[20:23], a[148:151], v[136:139], a[20:23]\n" \
"ds_read_b128 a[140:143], v5 offset:35840\n" \
"v_exp_f32 v103, v103\n" \
"v_mfma_f32_16x16x32_bf16 a[52:55], a[148:151], v[140:143], a[52:55]\n" \
"s_waitcnt lgkmcnt(4)\n" \
"s_add_u32 m0, s49, 49152\n" \
"v_exp_f32 v104, v104\n" \
"v_mfma_f32_16x16x32_bf16 v[216:219], a[152:155], v[128:131], v[216:219]\n" \
"buffer_load_dwordx4 v6, s[40:43], s17 offen offset:2048 sc1 lds\n" \
"v_exp_f32 v105, v105\n" \
"v_mfma_f32_16x16x32_bf16 v[248:251], a[152:155], v[132:135], v[248:251]\n" \
"v_exp_f32 v106, v106\n" \
"v_mfma_f32_16x16x32_bf16 a[24:27], a[152:155], v[136:139], a[24:27]\n" \
"ds_read_b128 a[144:147], v5 offset:36864\n" \
"v_exp_f32 v107, v107\n" \
"v_mfma_f32_16x16x32_bf16 a[56:59], a[152:155], v[140:143], a[56:59]\n" \
"s_add_u32 m0, s49, 49152\n" \
"v_exp_f32 v108, v108\n" \
"v_mfma_f32_16x16x32_bf16 v[220:223], a[156:159], v[128:131], v[220:223]\n" \
"buffer_load_dwordx4 v6, s[40:43], s17 offen offset:3072 sc1 lds\n" \
"v_exp_f32 v109, v109\n" \
"v_mfma_f32_16x16x32_bf16 v[252:255], a[156:159], v[132:135], v[252:255]\n" \
"v_exp_f32 v110, v110\n" \
"v_mfma_f32_16x16x32_bf16 a[28:31], a[156:159], v[136:139], a[28:31]\n" \
"ds_read_b128 a[148:151], v5 offset:37888\n" \
"v_exp_f32 v111, v111\n" \
"v_mfma_f32_16x16x32_bf16 a[60:63], a[156:159], v[140:143], a[60:63]\n" \
"s_waitcnt lgkmcnt(4)\n" \
"v_cvt_pk_bf16_f32 v147, v102, v103\n" \
"v_cvt_pk_bf16_f32 v148, v104, v105\n" \
"v_cvt_pk_bf16_f32 v149, v106, v107\n" \
"v_cvt_pk_bf16_f32 v150, v108, v109\n" \
"v_cvt_pk_bf16_f32 v151, v110, v111\n" \
"s_mov_b64 exec, 0\ns_cmp_eq_u32 %2, 0\ns_cbranch_scc1 LH21_%=\ns_cmp_eq_u32 %2, 1\ns_cbranch_scc1 LH22_%=\ns_cmp_eq_u32 %2, 2\ns_cbranch_scc1 LH23_%=\ns_cmp_eq_u32 %2, 3\ns_cbranch_scc1 LH24_%=\nLHR2_%=:\ns_mov_b64 exec, -1\n" \
"s_branch LCHK_%=\n" \
"LAD2_%=:\n" \
"v_xor_b32 v8, 0x2000, v5\n" \
"ds_read_b128 v[32:35], v21 offset:0\n" \
"v_exp_f32 v112, v112\n" \
"v_subrev_u32 v24, 0x1f800000, v160\n" \
"ds_read_b128 v[36:39], v21 offset:1024\n" \
"v_exp_f32 v113, v113\n" \
"v_subrev_u32 v25, 0x1f800000, v164\n" \
"ds_read_b128 v[40:43], v21 offset:2048\n" \
"v_exp_f32 v114, v114\n" \
"v_cvt_pk_bf16_f32 v152, v112, v113\n" \
"ds_read_b128 v[44:47], v21 offset:3072\n" \
"v_exp_f32 v115, v115\n" \
"v_max_u32 v24, v24, v25\n" \
"ds_read_b128 v[48:51], v21 offset:4096\n" \
"v_exp_f32 v120, v120\n" \
"v_cvt_pk_bf16_f32 v153, v114, v115\n" \
"ds_read_b128 v[52:55], v21 offset:5120\n" \
"v_exp_f32 v121, v121\n" \
"v_mov_b32 v154, 0\n" \
"ds_read_b128 v[56:59], v21 offset:6144\n" \
"v_exp_f32 v122, v122\n" \
"v_mov_b32 v155, 0\n" \
"ds_read_b128 v[60:63], v21 offset:7168\n" \
"v_exp_f32 v123, v123\n" \
"v_cvt_pk_bf16_f32 v156, v120, v121\n" \
"ds_read_b128 a[64:67], v21 offset:16384\n" \
"v_exp_f32 v124, v124\n" \
"v_cvt_pk_bf16_f32 v157, v122, v123\n" \
"ds_read_b128 a[68:71], v21 offset:17408\n" \
"v_exp_f32 v125, v125\n" \
"v_cmp_le_u32 vcc, 0x40000000, v24\n" \
"ds_read_b128 a[72:75], v21 offset:18432\n" \
"v_exp_f32 v126, v126\n" \
"v_cvt_pk_bf16_f32 v158, v124, v125\n" \
"ds_read_b128 a[76:79], v21 offset:19456\n" \
"v_exp_f32 v127, v127\n" \
"s_nop 0\n" \
"s_or_b64 s[54:55], s[54:55], vcc\n" \
"ds_read_b128 a[80:83], v21 offset:20480\n" \
"v_cvt_pk_bf16_f32 v159, v126, v127\n" \
"ds_read_b128 a[84:87], v21 offset:21504\n" \
"v_rcp_f32 v22, v160\n" \
"ds_read_b128 a[88:91], v21 offset:22528\n" \
"v_rcp_f32 v26, v164\n" \
"ds_read_b128 a[92:95], v21 offset:23552\n" \
"ds_read_b128 a[128:131], v8 offset:32768\n" \
"ds_read_b128 a[132:135], v8 offset:33792\n" \
"ds_read_b128 a[136:139], v8 offset:34816\n" \
"ds_read_b128 a[140:143], v8 offset:35840\n" \
"ds_read_b128 a[144:147], v8 offset:36864\n" \
"ds_read_b128 a[148:151], v8 offset:37888\n" \
"s_waitcnt lgkmcnt(4)\n" \
"s_branch LS3_%=\n" \
"LS1_%=:\n" \
"v_mfma_f32_16x16x32_bf16 v[64:67], a[128:131], v[32:35], v[176:179]\n" \
"v_exp_f32 v112, v112\n" \
"v_mfma_f32_16x16x32_bf16 v[72:75], a[128:131], v[48:51], 0\n" \
"s_add_u32 s16, s16, s9\n" \
"v_mfma_f32_16x16x32_bf16 v[80:83], a[128:131], a[64:67], 0\n" \
"ds_read_b128 a[152:155], v5 offset:38912\n" \
"v_mfma_f32_16x16x32_bf16 v[88:91], a[128:131], a[80:83], 0\n" \
"s_add_u32 m0, s48, 0\n" \
"v_exp_f32 v113, v113\n" \
"v_mfma_f32_16x16x32_bf16 v[64:67], a[132:135], v[36:39], v[64:67]\n" \
"buffer_load_dwordx4 v6, s[40:43], s16 offen offset:0 sc1 lds\n" \
"v_mfma_f32_16x16x32_bf16 v[72:75], a[132:135], v[52:55], v[72:75]\n" \
"v_exp_f32 v114, v114\n" \
"v_mfma_f32_16x16x32_bf16 v[80:83], a[132:135], a[68:71], v[80:83]\n" \
"ds_read_b128 a[156:159], v5 offset:39936\n" \
"v_cvt_pk_bf16_f32 v152, v112, v113\n" \
"v_mfma_f32_16x16x32_bf16 v[88:91], a[132:135], a[84:87], v[88:91]\n" \
"s_waitcnt lgkmcnt(4)\n" \
"v_exp_f32 v115, v115\n" \
"v_mfma_f32_16x16x32_bf16 v[64:67], a[136:139], v[40:43], v[64:67]\n" \
"v_exp_f32 v116, v116\n" \
"v_mfma_f32_16x16x32_bf16 v[72:75], a[136:139], v[56:59], v[72:75]\n" \
"s_xor_b32 s49, s49, 0x10000\n" \
"v_cvt_pk_bf16_f32 v153, v114, v115\n" \
"v_mfma_f32_16x16x32_bf16 v[80:83], a[136:139], a[72:75], v[80:83]\n" \
"ds_read_b128 a[128:131], v5 offset:49152\n" \
"v_mfma_f32_16x16x32_bf16 v[88:91], a[136:139], a[88:91], v[88:91]\n" \
"v_exp_f32 v117, v117\n" \
"v_mfma_f32_16x16x32_bf16 v[64:67], a[140:143], v[44:47], v[64:67]\n" \
"buffer_load_dwordx4 v6, s[40:43], s16 offen offset:1024 sc1 lds\n" \
"v_mfma_f32_16x16x32_bf16 v[72:75], a[140:143], v[60:63], v[72:75]\n" \
"v_exp_f32 v118, v118\n" \
"v_mfma_f32_16x16x32_bf16 v[80:83], a[140:143], a[76:79], v[80:83]\n" \
"ds_read_b128 a[132:135], v5 offset:50176\n" \
"v_cvt_pk_bf16_f32 v154, v116, v117\n" \
"v_mfma_f32_16x16x32_bf16 v[88:91], a[140:143], a[92:95], v[88:91]\n" \
"s_waitcnt lgkmcnt(4)\n" \
"v_exp_f32 v119, v119\n" \
"v_mov_b32 v68, 0xff800000\n" \
"v_mov_b32 v69, 0xff800000\n" \
"v_exp_f32 v120, v120\n" \
"v_mfma_f32_16x16x32_bf16 v[76:79], a[144:147], v[48:51], v[176:179]\n" \
"v_cvt_pk_bf16_f32 v155, v118, v119\n" \
"v_mfma_f32_16x16x32_bf16 v[84:87], a[144:147], a[64:67], 0\n" \
"ds_read_b128 a[136:139], v5 offset:51200\n" \
"v_mfma_f32_16x16x32_bf16 v[92:95], a[144:147], a[80:83], 0\n" \
"v_exp_f32 v121, v121\n" \
"buffer_load_dwordx4 v6, s[40:43], s16 offen offset:2048 sc1 lds\n" \
"v_exp_f32 v122, v122\n" \
"v_mfma_f32_16x16x32_bf16 v[76:79], a[148:151], v[52:55], v[76:79]\n" \
"v_cvt_pk_bf16_f32 v156, v120, v121\n" \
"v_mfma_f32_16x16x32_bf16 v[84:87], a[148:151], a[68:71], v[84:87]\n" \
"ds_read_b128 a[140:143], v5 offset:52224\n" \
"v_mfma_f32_16x16x32_bf16 v[92:95], a[148:151], a[84:87], v[92:95]\n" \
"s_waitcnt lgkmcnt(4)\n" \
"v_exp_f32 v123, v123\n" \
"v_mov_b32 v70, 0xff800000\n" \
"v_mov_b32 v71, 0xff800000\n" \
"v_exp_f32 v124, v124\n" \
"v_mfma_f32_16x16x32_bf16 v[76:79], a[152:155], v[56:59], v[76:79]\n" \
"v_cvt_pk_bf16_f32 v157, v122, v123\n" \
"v_mfma_f32_16x16x32_bf16 v[84:87], a[152:155], a[72:75], v[84:87]\n" \
"ds_read_b128 a[144:147], v5 offset:53248\n" \
"v_mfma_f32_16x16x32_bf16 v[92:95], a[152:155], a[88:91], v[92:95]\n" \
"v_exp_f32 v125, v125\n" \
"buffer_load_dwordx4 v6, s[40:43], s16 offen offset:3072 sc1 lds\n" \
"v_exp_f32 v126, v126\n" \
"v_mfma_f32_16x16x32_bf16 v[76:79], a[156:159], v[60:63], v[76:79]\n" \
"v_cvt_pk_bf16_f32 v158, v124, v125\n" \
"v_mfma_f32_16x16x32_bf16 v[84:87], a[156:159], a[76:79], v[84:87]\n" \
"ds_read_b128 a[148:151], v5 offset:54272\n" \
"v_mfma_f32_16x16x32_bf16 v[92:95], a[156:159], a[92:95], v[92:95]\n" \
"s_waitcnt lgkmcnt(4)\n" \
"v_exp_f32 v127, v127\n" \
"v_mfma_f32_16x16x32_bf16 v[160:163], v[180:183], v[144:147], v[160:163]\n" \
"v_cvt_pk_bf16_f32 v159, v126, v127\n" \
"v_mfma_f32_16x16x32_bf16 v[164:167], v[180:183], v[148:151], v[164:167]\n" \
"v_mfma_f32_16x16x32_bf16 v[168:171], v[180:183], v[152:155], v[168:171]\n" \
"v_exp_f32 v64, v64\n" \
"v_mfma_f32_16x16x32_bf16 v[172:175], v[180:183], v[156:159], v[172:175]\n" \
"v_mfma_f32_16x16x32_bf16 v[192:195], a[128:131], v[144:147], v[192:195]\n" \
"v_exp_f32 v65, v65\n" \
"v_mfma_f32_16x16x32_bf16 v[224:227], a[128:131], v[148:151], v[224:227]\n" \
"v_cvt_pk_bf16_f32 v128, v64, v65\n" \
"v_mfma_f32_16x16x32_bf16 a[0:3], a[128:131], v[152:155], a[0:3]\n" \
"ds_read_b128 a[152:155], v5 offset:55296\n" \
"v_mfma_f32_16x16x32_bf16 a[32:35], a[128:131], v[156:159], a[32:35]\n" \
"v_exp_f32 v66, v66\n" \
"v_mfma_f32_16x16x32_bf16 v[196:199], a[132:135], v[144:147], v[196:199]\n" \
"v_exp_f32 v67, v67\n" \
"v_mfma_f32_16x16x32_bf16 v[228:231], a[132:135], v[148:151], v[228:231]\n" \
"v_cvt_pk_bf16_f32 v129, v66, v67\n" \
"v_mfma_f32_16x16x32_bf16 a[4:7], a[132:135], v[152:155], a[4:7]\n" \
"ds_read_b128 a[156:159], v5 offset:56320\n" \
"v_mfma_f32_16x16x32_bf16 a[36:39], a[132:135], v[156:159], a[36:39]\n" \
"s_waitcnt vmcnt(12) lgkmcnt(4)\n" \
"v_exp_f32 v68, v68\n" \
"v_mfma_f32_16x16x32_bf16 v[200:203], a[136:139], v[144:147], v[200:203]\n" \
"s_barrier\n" \
"v_xor_b32 v5, 0x10000, v5\n" \
"v_mfma_f32_16x16x32_bf16 v[232:235], a[136:139], v[148:151], v[232:235]\n" \
"s_add_u32 s17, s17, s19\n" \
"v_exp_f32 v69, v69\n" \
"v_mfma_f32_16x16x32_bf16 a[8:11], a[136:139], v[152:155], a[8:11]\n" \
"ds_read_b128 a[128:131], v5 offset:0\n" \
"v_cvt_pk_bf16_f32 v130, v68, v69\n" \
"v_mfma_f32_16x16x32_bf16 a[40:43], a[136:139], v[156:159], a[40:43]\n" \
"s_add_u32 m0, s48, 16384\n" \
"v_exp_f32 v70, v70\n" \
"v_mfma_f32_16x16x32_bf16 v[204:207], a[140:143], v[144:147], v[204:207]\n" \
"buffer_load_dwordx4 v6, s[40:43], s17 offen offset:0 sc1 lds\n" \
"v_mfma_f32_16x16x32_bf16 v[236:239], a[140:143], v[148:151], v[236:239]\n" \
"v_exp_f32 v71, v71\n" \
"v_mfma_f32_16x16x32_bf16 a[12:15], a[140:143], v[152:155], a[12:15]\n" \
"ds_read_b128 a[132:135], v5 offset:1024\n" \
"v_cvt_pk_bf16_f32 v131, v70, v71\n" \
"v_mfma_f32_16x16x32_bf16 a[44:47], a[140:143], v[156:159], a[44:47]\n" \
"s_waitcnt lgkmcnt(4)\n" \
"v_exp_f32 v72, v72\n" \
"v_mfma_f32_16x16x32_bf16 v[208:211], a[144:147], v[144:147], v[208:211]\n" \
"v_exp_f32 v73, v73\n" \
"v_mfma_f32_16x16x32_bf16 v[240:243], a[144:147], v[148:151], v[240:243]\n" \
"v_cvt_pk_bf16_f32 v132, v72, v73\n" \
"v_mfma_f32_16x16x32_bf16 a[16:19], a[144:147], v[152:155], a[16:19]\n" \
"ds_read_b128 a[136:139], v5 offset:2048\n" \
"v_mfma_f32_16x16x32_bf16 a[48:51], a[144:147], v[156:159], a[48:51]\n" \
"v_exp_f32 v74, v74\n" \
"v_mfma_f32_16x16x32_bf16 v[212:215], a[148:151], v[144:147], v[212:215]\n" \
"buffer_load_dwordx4 v6, s[40:43], s17 offen offset:1024 sc1 lds\n" \
"v_mfma_f32_16x16x32_bf16 v[244:247], a[148:151], v[148:151], v[244:247]\n" \
"v_exp_f32 v75, v75\n" \
"v_mfma_f32_16x16x32_bf16 a[20:23], a[148:151], v[152:155], a[20:23]\n" \
"ds_read_b128 a[140:143], v5 offset:3072\n" \
"v_cvt_pk_bf16_f32 v133, v74, v75\n" \
"v_mfma_f32_16x16x32_bf16 a[52:55], a[148:151], v[156:159], a[52:55]\n" \
"s_waitcnt lgkmcnt(4)\n" \
"v_exp_f32 v76, v76\n" \
"v_mfma_f32_16x16x32_bf16 v[216:219], a[152:155], v[144:147], v[216:219]\n" \
"buffer_load_dwordx4 v6, s[40:43], s17 offen offset:2048 sc1 lds\n" \
"v_mfma_f32_16x16x32_bf16 v[248:251], a[152:155], v[148:151], v[248:251]\n" \
"s_add_u32 s44, s44, 1\n" \
"v_exp_f32 v77, v77\n" \
"v_mfma_f32_16x16x32_bf16 a[24:27], a[152:155], v[152:155], a[24:27]\n" \
"ds_read_b128 a[144:147], v5 offset:4096\n" \
"v_cvt_pk_bf16_f32 v134, v76, v77\n" \
"v_mfma_f32_16x16x32_bf16 a[56:59], a[152:155], v[156:159], a[56:59]\n" \
"v_exp_f32 v78, v78\n" \
"v_mfma_f32_16x16x32_bf16 v[220:223], a[156:159], v[144:147], v[220:223]\n" \
"buffer_load_dwordx4 v6, s[40:43], s17 offen offset:3072 sc1 lds\n" \
"v_mfma_f32_16x16x32_bf16 v[252:255], a[156:159], v[148:151], v[252:255]\n" \
"s_xor_b32 s48, s48, 0x10000\n" \
"v_exp_f32 v79, v79\n" \
"v_mfma_f32_16x16x32_bf16 a[28:31], a[156:159], v[152:155], a[28:31]\n" \
"ds_read_b128 a[148:151], v5 offset:5120\n" \
"v_cvt_pk_bf16_f32 v135, v78, v79\n" \
"v_mfma_f32_16x16x32_bf16 a[60:63], a[156:159], v[156:159], a[60:63]\n" \
"s_waitcnt lgkmcnt(4)\n" \
"s_cmp_ge_u32 %2, 2\n" \
"s_cbranch_scc1 LFIN_%=\n" \
"s_cmp_eq_u32 s44, 15\ns_cselect_b32 s16, s56, s16\ns_cselect_b32 s9, s57, s9\ns_cselect_b32 s17, s58, s17\ns_cselect_b32 s19, s59, s19\n" \
"v_xor_b32 v8, 0x2000, v5\n" \
"v_exp_f32 v80, v80\n" \
"ds_read_b128 v[32:35], v21 offset:0\n" \
"s_add_u32 s16, s16, s9\n" \
"v_exp_f32 v81, v81\n" \
"ds_read_b128 v[36:39], v21 offset:1024\n" \
"v_mfma_f32_16x16x32_bf16 v[112:115], a[128:131], a[64:67], v[176:179]\n" \
"ds_read_b128 a[152:155], v5 offset:6144\n" \
"v_cvt_pk_bf16_f32 v136, v80, v81\n" \
"v_mfma_f32_16x16x32_bf16 v[120:123], a[128:131], a[80:83], 0\n" \
"s_add_u32 m0, s49, 32768\n" \
"v_exp_f32 v82, v82\n" \
"buffer_load_dwordx4 v6, s[40:43], s16 offen offset:0 sc1 lds\n" \
"v_exp_f32 v83, v83\n" \
"v_exp_f32 v84, v84\n" \
"v_exp_f32 v85, v85\n" \
"v_mfma_f32_16x16x32_bf16 v[112:115], a[132:135], a[68:71], v[112:115]\n" \
"ds_read_b128 a[156:159], v5 offset:7168\n" \
"v_cvt_pk_bf16_f32 v137, v82, v83\n" \
"v_mfma_f32_16x16x32_bf16 v[120:123], a[132:135], a[84:87], v[120:123]\n" \
"s_waitcnt lgkmcnt(4)\n" \
"v_exp_f32 v86, v86\n" \
"v_exp_f32 v87, v87\n" \
"v_exp_f32 v88, v88\n" \
"v_exp_f32 v89, v89\n" \
"v_cvt_pk_bf16_f32 v138, v84, v85\n" \
"v_cvt_pk_bf16_f32 v139, v86, v87\n" \
"v_mfma_f32_16x16x32_bf16 v[112:115], a[136:139], a[72:75], v[112:115]\n" \
"ds_read_b128 a[128:131], v5 offset:16384\n" \
"v_cvt_pk_bf16_f32 v140, v88, v89\n" \
"v_mfma_f32_16x16x32_bf16 v[120:123], a[136:139], a[88:91], v[120:123]\n" \
"v_exp_f32 v90, v90\n" \
"buffer_load_dwordx4 v6, s[40:43], s16 offen offset:1024 sc1 lds\n" \
"v_exp_f32 v91, v91\n" \
"v_exp_f32 v92, v92\n" \
"v_exp_f32 v93, v93\n" \
"v_mfma_f32_16x16x32_bf16 v[112:115], a[140:143], a[76:79], v[112:115]\n" \
"ds_read_b128 a[132:135], v5 offset:17408\n" \
"v_cvt_pk_bf16_f32 v141, v90, v91\n" \
"v_mfma_f32_16x16x32_bf16 v[120:123], a[140:143], a[92:95], v[120:123]\n" \
"s_waitcnt lgkmcnt(4)\n" \
"v_exp_f32 v94, v94\n" \
"v_exp_f32 v95, v95\n" \
"v_cvt_pk_bf16_f32 v142, v92, v93\n" \
"ds_read_b128 v[40:43], v21 offset:2048\n" \
"v_cvt_pk_bf16_f32 v143, v94, v95\n" \
"ds_read_b128 v[44:47], v21 offset:3072\n" \
"ds_read_b128 a[136:139], v5 offset:18432\n" \
"v_mfma_f32_16x16x32_bf16 v[124:127], a[144:147], a[80:83], v[176:179]\n" \
"ds_read_b128 v[48:51], v21 offset:4096\n" \
"buffer_load_dwordx4 v6, s[40:43], s16 offen offset:2048 sc1 lds\n" \
"ds_read_b128 v[52:55], v21 offset:5120\n" \
"ds_read_b128 v[56:59], v21 offset:6144\n" \
"ds_read_b128 a[140:143], v5 offset:19456\n" \
"v_mfma_f32_16x16x32_bf16 v[124:127], a[148:151], a[84:87], v[124:127]\n" \
"s_waitcnt lgkmcnt(4)\n" \
"ds_read_b128 v[60:63], v21 offset:7168\n" \
"ds_read_b128 a[144:147], v5 offset:20480\n" \
"v_mfma_f32_16x16x32_bf16 v[124:127], a[152:155], a[88:91], v[124:127]\n" \
"buffer_load_dwordx4 v6, s[40:43], s16 offen offset:3072 sc1 lds\n" \
"ds_read_b128 a[148:151], v5 offset:21504\n" \
"v_mfma_f32_16x16x32_bf16 v[124:127], a[156:159], a[92:95], v[124:127]\n" \
"s_waitcnt lgkmcnt(4)\n" \
"v_mfma_f32_16x16x32_bf16 v[160:163], v[180:183], v[128:131], v[160:163]\n" \
"v_mfma_f32_16x16x32_bf16 v[164:167], v[180:183], v[132:135], v[164:167]\n" \
"ds_read_b128 a[64:67], v21 offset:16384\n" \
"v_mfma_f32_16x16x32_bf16 v[168:171], v[180:183], v[136:139], v[168:171]\n" \
"ds_read_b128 a[68:71], v21 offset:17408\n" \
"v_mfma_f32_16x16x32_bf16 v[172:175], v[180:183], v[140:143], v[172:175]\n" \
"ds_read_b128 a[72:75], v21 offset:18432\n" \
"v_mfma_f32_16x16x32_bf16 v[192:195], a[128:131], v[128:131], v[192:195]\n" \
"v_exp_f32 v112, v112\n" \
"v_mfma_f32_16x16x32_bf16 v[224:227], a[128:131], v[132:135], v[224:227]\n" \
"v_exp_f32 v113, v113\n" \
"v_mfma_f32_16x16x32_bf16 a[0:3], a[128:131], v[136:139], a[0:3]\n" \
"ds_read_b128 a[152:155], v5 offset:22528\n" \
"v_cvt_pk_bf16_f32 v152, v112, v113\n" \
"v_mfma_f32_16x16x32_bf16 a[32:35], a[128:131], v[140:143], a[32:35]\n" \
"v_exp_f32 v114, v114\n" \
"v_mfma_f32_16x16x32_bf16 v[196:199], a[132:135], v[128:131], v[196:199]\n" \
"v_exp_f32 v115, v115\n" \
"v_mfma_f32_16x16x32_bf16 v[228:231], a[132:135], v[132:135], v[228:231]\n" \
"v_exp_f32 v120, v120\n" \
"v_mfma_f32_16x16x32_bf16 a[4:7], a[132:135], v[136:139], a[4:7]\n" \
"ds_read_b128 a[156:159], v5 offset:23552\n" \
"v_cvt_pk_bf16_f32 v153, v114, v115\n" \
"v_mfma_f32_16x16x32_bf16 a[36:39], a[132:135], v[140:143], a[36:39]\n" \
"s_waitcnt vmcnt(12) lgkmcnt(4)\n" \
"v_exp_f32 v121, v121\n" \
"v_mfma_f32_16x16x32_bf16 v[200:203], a[136:139], v[128:131], v[200:203]\n" \
"s_barrier\n" \
"v_exp_f32 v122, v122\n" \
"v_mfma_f32_16x16x32_bf16 v[232:235], a[136:139], v[132:135], v[232:235]\n" \
"s_add_u32 s17, s17, s19\n" \
"v_exp_f32 v123, v123\n" \
"v_mfma_f32_16x16x32_bf16 a[8:11], a[136:139], v[136:139], a[8:11]\n" \
"ds_read_b128 a[128:131], v8 offset:32768\n" \
"v_mov_b32 v154, 0\n" \
"v_mfma_f32_16x16x32_bf16 a[40:43], a[136:139], v[140:143], a[40:43]\n" \
"s_add_u32 m0, s49, 49152\n" \
"v_exp_f32 v124, v124\n" \
"v_mfma_f32_16x16x32_bf16 v[204:207], a[140:143], v[128:131], v[204:207]\n" \
"buffer_load_dwordx4 v6, s[40:43], s17 offen offset:0 sc1 lds\n" \
"v_mfma_f32_16x16x32_bf16 v[236:239], a[140:143], v[132:135], v[236:239]\n" \
"v_exp_f32 v125, v125\n" \
"v_mfma_f32_16x16x32_bf16 a[12:15], a[140:143], v[136:139], a[12:15]\n" \
"ds_read_b128 a[132:135], v8 offset:33792\n" \
"v_mov_b32 v155, 0\n" \
"v_mfma_f32_16x16x32_bf16 a[44:47], a[140:143], v[140:143], a[44:47]\n" \
"s_waitcnt lgkmcnt(4)\n" \
"v_exp_f32 v126, v126\n" \
"v_mfma_f32_16x16x32_bf16 v[208:211], a[144:147], v[128:131], v[208:211]\n" \
"v_exp_f32 v127, v127\n" \
"v_mfma_f32_16x16x32_bf16 v[240:243], a[144:147], v[132:135], v[240:243]\n" \
"v_cvt_pk_bf16_f32 v156, v120, v121\n" \
"v_cvt_pk_bf16_f32 v157, v122, v123\n" \
"v_mfma_f32_16x16x32_bf16 a[16:19], a[144:147], v[136:139], a[16:19]\n" \
"ds_read_b128 a[136:139], v8 offset:34816\n" \
"v_cvt_pk_bf16_f32 v158, v124, v125\n" \
"v_mfma_f32_16x16x32_bf16 a[48:51], a[144:147], v[140:143], a[48:51]\n" \
"v_cvt_pk_bf16_f32 v159, v126, v127\n" \
"ds_read_b128 a[76:79], v21 offset:19456\n" \
"v_mfma_f32_16x16x32_bf16 v[212:215], a[148:151], v[128:131], v[212:215]\n" \
"buffer_load_dwordx4 v6, s[40:43], s17 offen offset:1024 sc1 lds\n" \
"v_mfma_f32_16x16x32_bf16 v[244:247], a[148:151], v[132:135], v[244:247]\n" \
"ds_read_b128 a[80:83], v21 offset:20480\n" \
"v_subrev_u32 v24, 0x1f800000, v160\n" \
"v_mfma_f32_16x16x32_bf16 a[20:23], a[148:151], v[136:139], a[20:23]\n" \
"ds_read_b128 a[140:143], v8 offset:35840\n" \
"v_subrev_u32 v25, 0x1f800000, v164\n" \
"v_mfma_f32_16x16x32_bf16 a[52:55], a[148:151], v[140:143], a[52:55]\n" \
"s_waitcnt lgkmcnt(4)\n" \
"ds_read_b128 a[84:87], v21 offset:21504\n" \
"v_max_u32 v24, v24, v25\n" \
"v_mfma_f32_16x16x32_bf16 v[216:219], a[152:155], v[128:131], v[216:219]\n" \
"LH19_%=:\ns_cbranch_execz LHR1_%=\n" \
"buffer_load_dwordx4 v6, s[40:43], s17 offen offset:2048 sc1 lds\n" \
"v_mfma_f32_16x16x32_bf16 v[248:251], a[152:155], v[132:135], v[248:251]\n" \
"ds_read_b128 a[88:91], v21 offset:22528\n" \
"v_cmp_le_u32 vcc, 0x40000000, v24\n" \
"v_mfma_f32_16x16x32_bf16 a[24:27], a[152:155], v[136:139], a[24:27]\n" \
"s_or_b64 s[54:55], s[54:55], vcc\n" \
"ds_read_b128 a[144:147], v8 offset:36864\n" \
"s_nop 0\n" \
"v_mfma_f32_16x16x32_bf16 a[56:59], a[152:155], v[140:143], a[56:59]\n" \
"ds_read_b128 a[92:95], v21 offset:23552\n" \
"v_mfma_f32_16x16x32_bf16 v[220:223], a[156:159], v[128:131], v[220:223]\n" \
"buffer_load_dwordx4 v6, s[40:43], s17 offen offset:3072 sc1 lds\n" \
"v_mfma_f32_16x16x32_bf16 v[252:255], a[156:159], v[132:135], v[252:255]\n" \
"v_rcp_f32 v22, v160\n" \
"v_mfma_f32_16x16x32_bf16 a[28:31], a[156:159], v[136:139], a[28:31]\n" \
"ds_read_b128 a[148:151], v8 offset:37888\n" \
"v_mfma_f32_16x16x32_bf16 a[60:63], a[156:159], v[140:143], a[60:63]\n" \
"s_waitcnt lgkmcnt(4)\n" \
"v_rcp_f32 v26, v164\n" \
"LS3_%=:\n" \
"v_xor_b32 v8, 0x2000, v8\n" \
"v_xor_b32 v5, 0x2000, v5\n" \
"v_mul_f32 v192, v22, v192\n" \
"v_mul_f32 v193, v22, v193\n" \
"v_mul_f32 v194, v22, v194\n" \
"v_mul_f32 v195, v22, v195\n" \
"v_mul_f32 v196, v22, v196\n" \
"v_mul_f32 v197, v22, v197\n" \
"v_mul_f32 v198, v22, v198\n" \
"v_mul_f32 v199, v22, v199\n" \
"v_mfma_f32_16x16x32_bf16 v[64:67], a[128:131], v[32:35], v[176:179]\n" \
"v_cvt_pk_bf16_f32 v192, v192, v193\n" \
"v_mfma_f32_16x16x32_bf16 v[72:75], a[128:131], v[48:51], 0\n" \
"s_add_u32 s16, s16, s9\n" \
"v_cvt_pk_bf16_f32 v193, v194, v195\n" \
"v_cvt_pk_bf16_f32 v194, v196, v197\n" \
"ds_read_b128 a[152:155], v5 offset:38912\n" \
"v_cvt_pk_bf16_f32 v195, v198, v199\n" \
"v_mul_f32 v224, v26, v224\n" \
"v_mul_f32 v225, v26, v225\n" \
"s_add_u32 m0, s48, 0\n" \
"v_mul_f32 v226, v26, v226\n" \
"v_mul_f32 v227, v26, v227\n" \
"v_mul_f32 v228, v26, v228\n" \
"v_mfma_f32_16x16x32_bf16 v[64:67], a[132:135], v[36:39], v[64:67]\n" \
"buffer_load_dwordx4 v6, s[40:43], s16 offen offset:0 sc1 lds\n" \
"v_mfma_f32_16x16x32_bf16 v[72:75], a[132:135], v[52:55], v[72:75]\n" \
"v_mul_f32 v229, v26, v229\n" \
"v_mul_f32 v230, v26, v230\n" \
"ds_read_b128 a[156:159], v5 offset:39936\n" \
"buffer_store_dwordx4 v[192:195], v4, s[40:43], s50 offen offset:0 sc1\n" \
"v_mul_f32 v231, v26, v231\n" \
"s_waitcnt lgkmcnt(4)\n" \
"v_cvt_pk_bf16_f32 v224, v224, v225\n" \
"v_cvt_pk_bf16_f32 v225, v226, v227\n" \
"v_cvt_pk_bf16_f32 v226, v228, v229\n" \
"v_mfma_f32_16x16x32_bf16 v[64:67], a[136:139], v[40:43], v[64:67]\n" \
"v_cvt_pk_bf16_f32 v227, v230, v231\n" \
"v_mul_f32 v200, v22, v200\n" \
"v_mfma_f32_16x16x32_bf16 v[72:75], a[136:139], v[56:59], v[72:75]\n" \
"s_xor_b32 s49, s49, 0x10000\n" \
"v_mul_f32 v201, v22, v201\n" \
"ds_read_b128 a[128:131], v8 offset:49152\n" \
"buffer_store_dwordx4 v[224:227], v4, s[40:43], s50 offen offset:1024 sc1\n" \
"v_mul_f32 v202, v22, v202\n" \
"v_mul_f32 v203, v22, v203\n" \
"v_mul_f32 v204, v22, v204\n" \
"v_mul_f32 v205, v22, v205\n" \
"v_mul_f32 v206, v22, v206\n" \
"v_mfma_f32_16x16x32_bf16 v[64:67], a[140:143], v[44:47], v[64:67]\n" \
"buffer_load_dwordx4 v6, s[40:43], s16 offen offset:1024 sc1 lds\n" \
"v_mfma_f32_16x16x32_bf16 v[72:75], a[140:143], v[60:63], v[72:75]\n" \
"v_mul_f32 v207, v22, v207\n" \
"v_cvt_pk_bf16_f32 v200, v200, v201\n" \
"ds_read_b128 a[132:135], v8 offset:50176\n" \
"v_cvt_pk_bf16_f32 v201, v202, v203\n" \
"v_cvt_pk_bf16_f32 v202, v204, v205\n" \
"v_cvt_pk_bf16_f32 v203, v206, v207\n" \
"s_waitcnt lgkmcnt(4)\n" \
"buffer_store_dwordx4 v[200:203], v4, s[40:43], s50 offen offset:2048 sc1\n" \
"v_mul_f32 v232, v26, v232\n" \
"v_mul_f32 v233, v26, v233\n" \
"v_mul_f32 v234, v26, v234\n" \
"v_mul_f32 v235, v26, v235\n" \
"v_mul_f32 v236, v26, v236\n" \
"v_mfma_f32_16x16x32_bf16 v[76:79], a[144:147], v[48:51], v[176:179]\n" \
"v_mul_f32 v237, v26, v237\n" \
"v_mul_f32 v238, v26, v238\n" \
"ds_read_b128 a[136:139], v8 offset:51200\n" \
"v_mul_f32 v239, v26, v239\n" \
"v_cvt_pk_bf16_f32 v232, v232, v233\n" \
"v_cvt_pk_bf16_f32 v233, v234, v235\n" \
"v_cvt_pk_bf16_f32 v234, v236, v237\n" \
"v_cvt_pk_bf16_f32 v235, v238, v239\n" \
"v_mul_f32 v208, v22, v208\n" \
"v_mul_f32 v209, v22, v209\n" \
"buffer_load_dwordx4 v6, s[40:43], s16 offen offset:2048 sc1 lds\n" \
"v_mul_f32 v210, v22, v210\n" \
"v_mul_f32 v211, v22, v211\n" \
"v_mfma_f32_16x16x32_bf16 v[76:79], a[148:151], v[52:55], v[76:79]\n" \
"v_mul_f32 v212, v22, v212\n" \
"ds_read_b128 a[140:143], v8 offset:52224\n" \
"buffer_store_dwordx4 v[232:235], v4, s[40:43], s50 offen offset:3072 sc1\n" \
"v_mul_f32 v213, v22, v213\n" \
"s_waitcnt lgkmcnt(4)\n" \
"v_mul_f32 v214, v22, v214\n" \
"v_mul_f32 v215, v22, v215\n" \
"v_cvt_pk_bf16_f32 v208, v208, v209\n" \
"v_cvt_pk_bf16_f32 v209, v210, v211\n" \
"v_cvt_pk_bf16_f32 v210, v212, v213\n" \
"v_cvt_pk_bf16_f32 v211, v214, v215\n" \
"v_mul_f32 v240, v26, v240\n" \
"v_mfma_f32_16x16x32_bf16 v[76:79], a[152:155], v[56:59], v[76:79]\n" \
"buffer_store_dwordx4 v[208:211], v7, s[40:43], s50 offen offset:0 sc1\n" \
"ds_read_b128 a[144:147], v8 offset:53248\n" \
"v_mul_f32 v241, v26, v241\n" \
"v_mul_f32 v242, v26, v242\n" \
"v_mul_f32 v243, v26, v243\n" \
"v_mul_f32 v244, v26, v244\n" \
"v_mul_f32 v245, v26, v245\n" \
"v_mul_f32 v246, v26, v246\n" \
"v_mul_f32 v247, v26, v247\n" \
"buffer_load_dwordx4 v6, s[40:43], s16 offen offset:3072 sc1 lds\n" \
"v_cvt_pk_bf16_f32 v240, v240, v241\n" \
"v_cvt_pk_bf16_f32 v241, v242, v243\n" \
"v_mfma_f32_16x16x32_bf16 v[76:79], a[156:159], v[60:63], v[76:79]\n" \
"v_cvt_pk_bf16_f32 v242, v244, v245\n" \
"ds_read_b128 a[148:151], v8 offset:54272\n" \
"v_cvt_pk_bf16_f32 v243, v246, v247\n" \
"v_mul_f32 v216, v22, v216\n" \
"v_mul_f32 v217, v22, v217\n" \
"s_waitcnt lgkmcnt(4)\n" \
"buffer_store_dwordx4 v[240:243], v7, s[40:43], s50 offen offset:1024 sc1\n" \
"v_mul_f32 v218, v22, v218\n" \
"v_mul_f32 v219, v22, v219\n" \
"v_mul_f32 v220, v22, v220\n" \
"v_mul_f32 v221, v22, v221\n" \
"v_mul_f32 v222, v22, v222\n" \
"v_mul_f32 v223, v22, v223\n" \
"v_cvt_pk_bf16_f32 v216, v216, v217\n" \
"v_cvt_pk_bf16_f32 v217, v218, v219\n" \
"v_cvt_pk_bf16_f32 v218, v220, v221\n" \
"v_mfma_f32_16x16x32_bf16 v[168:171], v[180:183], v[152:155], v[168:171]\n" \
"v_cvt_pk_bf16_f32 v219, v222, v223\n" \
"v_mfma_f32_16x16x32_bf16 v[172:175], v[180:183], v[156:159], v[172:175]\n" \
"buffer_store_dwordx4 v[216:219], v7, s[40:43], s50 offen offset:2048 sc1\n" \
"v_mul_f32 v248, v26, v248\n" \
"v_mul_f32 v249, v26, v249\n" \
"v_mul_f32 v250, v26, v250\n" \
"v_mul_f32 v251, v26, v251\n" \
"v_mul_f32 v252, v26, v252\n" \
"v_mul_f32 v253, v26, v253\n" \
"v_mul_f32 v254, v26, v254\n" \
"v_mul_f32 v255, v26, v255\n" \
"v_mfma_f32_16x16x32_bf16 a[0:3], a[128:131], v[152:155], a[0:3]\n" \
"ds_read_b128 a[152:155], v8 offset:55296\n" \
"v_cvt_pk_bf16_f32 v248, v248, v249\n" \
"v_mfma_f32_16x16x32_bf16 a[32:35], a[128:131], v[156:159], a[32:35]\n" \
"v_cvt_pk_bf16_f32 v249, v250, v251\n" \
"v_cvt_pk_bf16_f32 v250, v252, v253\n" \
"v_exp_f32 v64, v64\n" \
"v_cvt_pk_bf16_f32 v251, v254, v255\n" \
"buffer_store_dwordx4 v[248:251], v7, s[40:43], s50 offen offset:3072 sc1\n" \
"v_exp_f32 v65, v65\n" \
"v_mfma_f32_16x16x32_bf16 a[4:7], a[132:135], v[152:155], a[4:7]\n" \
"ds_read_b128 a[156:159], v8 offset:56320\n" \
"v_cvt_pk_bf16_f32 v128, v64, v65\n" \
"v_mfma_f32_16x16x32_bf16 a[36:39], a[132:135], v[156:159], a[36:39]\n" \
"s_waitcnt vmcnt(20) lgkmcnt(4)\n" \
"v_exp_f32 v66, v66\n" \
"s_barrier\n" \
"v_xor_b32 v5, 0x10000, v5\n" \
"v_exp_f32 v67, v67\n" \
"s_add_u32 s17, s17, s19\n" \
"v_exp_f32 v72, v72\n" \
"v_cvt_pk_bf16_f32 v129, v66, v67\n" \
"v_mfma_f32_16x16x32_bf16 a[8:11], a[136:139], v[152:155], a[8:11]\n" \
"ds_read_b128 a[128:131], v5 offset:0\n" \
"v_mov_b32 v130, 0\n" \
"v_mfma_f32_16x16x32_bf16 a[40:43], a[136:139], v[156:159], a[40:43]\n" \
"s_add_u32 m0, s48, 16384\n" \
"v_exp_f32 v73, v73\n" \
"buffer_load_dwordx4 v6, s[40:43], s17 offen offset:0 sc1 lds\n" \
"v_exp_f32 v74, v74\n" \
"v_exp_f32 v75, v75\n" \
"v_exp_f32 v76, v76\n" \
"v_mfma_f32_16x16x32_bf16 a[12:15], a[140:143], v[152:155], a[12:15]\n" \
"ds_read_b128 a[132:135], v5 offset:1024\n" \
"v_mov_b32 v131, 0\n" \
"v_mfma_f32_16x16x32_bf16 a[44:47], a[140:143], v[156:159], a[44:47]\n" \
"s_waitcnt lgkmcnt(4)\n" \
"v_exp_f32 v77, v77\n" \
"v_exp_f32 v78, v78\n" \
"v_exp_f32 v79, v79\n" \
"v_cvt_pk_bf16_f32 v132, v72, v73\n" \
"v_cvt_pk_bf16_f32 v133, v74, v75\n" \
"v_cvt_pk_bf16_f32 v134, v76, v77\n" \
"v_cvt_pk_bf16_f32 v135, v78, v79\n" \
"v_mfma_f32_16x16x32_bf16 a[16:19], a[144:147], v[152:155], a[16:19]\n" \
"ds_read_b128 a[136:139], v5 offset:2048\n" \
"v_mfma_f32_16x16x32_bf16 a[48:51], a[144:147], v[156:159], a[48:51]\n" \
"buffer_load_dwordx4 v6, s[40:43], s17 offen offset:1024 sc1 lds\n" \
"v_mfma_f32_16x16x32_bf16 a[20:23], a[148:151], v[152:155], a[20:23]\n" \
"ds_read_b128 a[140:143], v5 offset:3072\n" \
"v_mfma_f32_16x16x32_bf16 a[52:55], a[148:151], v[156:159], a[52:55]\n" \
"s_waitcnt lgkmcnt(4)\n" \
"buffer_load_dwordx4 v6, s[40:43], s17 offen offset:2048 sc1 lds\n" \
"s_add_u32 s44, s44, 1\n" \
"v_mfma_f32_16x16x32_bf16 a[24:27], a[152:155], v[152:155], a[24:27]\n" \
"ds_read_b128 a[144:147], v5 offset:4096\n" \
"v_mfma_f32_16x16x32_bf16 a[56:59], a[152:155], v[156:159], a[56:59]\n" \
"buffer_load_dwordx4 v6, s[40:43], s17 offen offset:3072 sc1 lds\n" \
"s_xor_b32 s48, s48, 0x10000\n" \
"v_mfma_f32_16x16x32_bf16 a[28:31], a[156:159], v[152:155], a[28:31]\n" \
"ds_read_b128 a[148:151], v5 offset:5120\n" \
"v_mfma_f32_16x16x32_bf16 a[60:63], a[156:159], v[156:159], a[60:63]\n" \
"s_waitcnt lgkmcnt(4)\n" \
"s_cmp_eq_u32 s44, 16\n" \
"s_cbranch_scc1 LAD32_%=\n" \
"s_cmp_eq_u32 s44, 15\ns_cselect_b32 s16, s56, s16\ns_cselect_b32 s9, s57, s9\ns_cselect_b32 s17, s58, s17\ns_cselect_b32 s19, s59, s19\n" \
"s_add_u32 s50, s50, 262144\n" \
"v_mov_b32 v116, 0xff800000\n" \
"v_subrev_u32 v24, 0x1f800000, v168\n" \
"v_subrev_u32 v25, 0x1f800000, v172\n" \
"v_mov_b32 v117, 0xff800000\n" \
"v_max_u32 v24, v24, v25\n" \
"v_cmp_le_u32 vcc, 0x40000000, v24\n" \
"s_nop 0\n" \
"s_or_b64 s[54:55], s[54:55], vcc\n" \
"v_mov_b32 v118, 0xff800000\n" \
"v_rcp_f32 v22, v168\n" \
"v_mov_b32 v119, 0xff800000\n" \
"v_rcp_f32 v26, v172\n" \
"v_accvgpr_read_b32 v80, a0\n" \
"v_mfma_f32_16x16x32_bf16 v[96:99], a[128:131], v[32:35], 0\n" \
"v_accvgpr_read_b32 v81, a1\n" \
"v_accvgpr_read_b32 v82, a2\n" \
"v_mfma_f32_16x16x32_bf16 v[104:107], a[128:131], v[48:51], 0\n" \
"s_add_u32 s16, s16, s9\n" \
"v_accvgpr_read_b32 v83, a3\n" \
"v_mfma_f32_16x16x32_bf16 v[112:115], a[128:131], a[64:67], v[176:179]\n" \
"ds_read_b128 a[152:155], v5 offset:6144\n" \
"v_accvgpr_read_b32 v84, a4\n" \
"v_mfma_f32_16x16x32_bf16 v[120:123], a[128:131], a[80:83], 0\n" \
"s_add_u32 m0, s49, 32768\n" \
"v_accvgpr_read_b32 v85, a5\n" \
"v_accvgpr_read_b32 v86, a6\n" \
"v_mfma_f32_16x16x32_bf16 v[96:99], a[132:135], v[36:39], v[96:99]\n" \
"buffer_load_dwordx4 v6, s[40:43], s16 offen offset:0 sc1 lds\n" \
"v_mfma_f32_16x16x32_bf16 v[104:107], a[132:135], v[52:55], v[104:107]\n" \
"v_accvgpr_read_b32 v87, a7\n" \
"v_mul_f32 v80, v22, v80\n" \
"v_mfma_f32_16x16x32_bf16 v[112:115], a[132:135], a[68:71], v[112:115]\n" \
"ds_read_b128 a[156:159], v5 offset:7168\n" \
"v_mul_f32 v81, v22, v81\n" \
"v_mfma_f32_16x16x32_bf16 v[120:123], a[132:135], a[84:87], v[120:123]\n" \
"s_waitcnt lgkmcnt(4)\n" \
"v_mul_f32 v82, v22, v82\n" \
"v_mul_f32 v83, v22, v83\n" \
"v_mfma_f32_16x16x32_bf16 v[96:99], a[136:139], v[40:43], v[96:99]\n" \
"v_mul_f32 v84, v22, v84\n" \
"v_mul_f32 v85, v22, v85\n" \
"v_mfma_f32_16x16x32_bf16 v[104:107], a[136:139], v[56:59], v[104:107]\n" \
"v_mul_f32 v86, v22, v86\n" \
"v_mfma_f32_16x16x32_bf16 v[112:115], a[136:139], a[72:75], v[112:115]\n" \
"ds_read_b128 a[128:131], v5 offset:16384\n" \
"v_mul_f32 v87, v22, v87\n" \
"v_mfma_f32_16x16x32_bf16 v[120:123], a[136:139], a[88:91], v[120:123]\n" \
"v_cvt_pk_bf16_f32 v80, v80, v81\n" \
"v_cvt_pk_bf16_f32 v81, v82, v83\n" \
"v_mfma_f32_16x16x32_bf16 v[96:99], a[140:143], v[44:47], v[96:99]\n" \
"buffer_load_dwordx4 v6, s[40:43], s16 offen offset:1024 sc1 lds\n" \
"v_mfma_f32_16x16x32_bf16 v[104:107], a[140:143], v[60:63], v[104:107]\n" \
"v_cvt_pk_bf16_f32 v82, v84, v85\n" \
"v_cvt_pk_bf16_f32 v83, v86, v87\n" \
"v_mfma_f32_16x16x32_bf16 v[112:115], a[140:143], a[76:79], v[112:115]\n" \
"ds_read_b128 a[132:135], v5 offset:17408\n" \
"v_accvgpr_read_b32 v88, a32\n" \
"v_mfma_f32_16x16x32_bf16 v[120:123], a[140:143], a[92:95], v[120:123]\n" \
"s_waitcnt lgkmcnt(4)\n" \
"v_accvgpr_read_b32 v89, a33\n" \
"v_accvgpr_read_b32 v90, a34\n" \
"v_mfma_f32_16x16x32_bf16 v[100:103], a[144:147], v[32:35], 0\n" \
"buffer_store_dwordx4 v[80:83], v4, s[40:43], s50 offen offset:0 sc1\n" \
"v_mfma_f32_16x16x32_bf16 v[108:111], a[144:147], v[48:51], 0\n" \
"v_accvgpr_read_b32 v91, a35\n" \
"v_accvgpr_read_b32 v92, a36\n" \
"ds_read_b128 a[136:139], v5 offset:18432\n" \
"v_accvgpr_read_b32 v93, a37\n" \
"v_accvgpr_read_b32 v94, a38\n" \
"v_accvgpr_read_b32 v95, a39\n" \
"v_mfma_f32_16x16x32_bf16 v[124:127], a[144:147], a[80:83], v[176:179]\n" \
"v_mul_f32 v88, v26, v88\n" \
"v_mfma_f32_16x16x32_bf16 v[100:103], a[148:151], v[36:39], v[100:103]\n" \
"buffer_load_dwordx4 v6, s[40:43], s16 offen offset:2048 sc1 lds\n" \
"v_mfma_f32_16x16x32_bf16 v[108:111], a[148:151], v[52:55], v[108:111]\n" \
"v_mul_f32 v89, v26, v89\n" \
"v_mul_f32 v90, v26, v90\n" \
"ds_read_b128 a[140:143], v5 offset:19456\n" \
"v_mul_f32 v91, v26, v91\n" \
"v_mul_f32 v92, v26, v92\n" \
"v_mul_f32 v93, v26, v93\n" \
"v_mfma_f32_16x16x32_bf16 v[124:127], a[148:151], a[84:87], v[124:127]\n" \
"s_waitcnt lgkmcnt(4)\n" \
"v_mul_f32 v94, v26, v94\n" \
"v_mfma_f32_16x16x32_bf16 v[100:103], a[152:155], v[40:43], v[100:103]\n" \
"v_mul_f32 v95, v26, v95\n" \
"v_cvt_pk_bf16_f32 v88, v88, v89\n" \
"v_mfma_f32_16x16x32_bf16 v[108:111], a[152:155], v[56:59], v[108:111]\n" \
"v_cvt_pk_bf16_f32 v89, v90, v91\n" \
"v_cvt_pk_bf16_f32 v90, v92, v93\n" \
"ds_read_b128 a[144:147], v5 offset:20480\n" \
"v_cvt_pk_bf16_f32 v91, v94, v95\n" \
"v_accvgpr_read_b32 v136, a8\n" \
"v_accvgpr_read_b32 v137, a9\n" \
"v_mfma_f32_16x16x32_bf16 v[124:127], a[152:155], a[88:91], v[124:127]\n" \
"v_accvgpr_read_b32 v138, a10\n" \
"v_mfma_f32_16x16x32_bf16 v[100:103], a[156:159], v[44:47], v[100:103]\n" \
"buffer_load_dwordx4 v6, s[40:43], s16 offen offset:3072 sc1 lds\n" \
"v_mfma_f32_16x16x32_bf16 v[108:111], a[156:159], v[60:63], v[108:111]\n" \
"v_accvgpr_read_b32 v139, a11\n" \
"v_accvgpr_read_b32 v140, a12\n" \
"ds_read_b128 a[148:151], v5 offset:21504\n" \
"buffer_store_dwordx4 v[88:91], v4, s[40:43], s50 offen offset:1024 sc1\n" \
"v_accvgpr_read_b32 v141, a13\n" \
"v_mfma_f32_16x16x32_bf16 v[124:127], a[156:159], a[92:95], v[124:127]\n" \
"s_waitcnt lgkmcnt(4)\n" \
"v_accvgpr_read_b32 v142, a14\n" \
"v_accvgpr_read_b32 v143, a15\n" \
"v_mfma_f32_16x16x32_bf16 v[160:163], v[180:183], v[128:131], 0\n" \
"v_mul_f32 v136, v22, v136\n" \
"v_mul_f32 v137, v22, v137\n" \
"v_mfma_f32_16x16x32_bf16 v[164:167], v[180:183], v[132:135], 0\n" \
"v_mul_f32 v138, v22, v138\n" \
"v_mul_f32 v139, v22, v139\n" \
"v_mul_f32 v140, v22, v140\n" \
"v_mul_f32 v141, v22, v141\n" \
"v_mul_f32 v142, v22, v142\n" \
"v_mul_f32 v143, v22, v143\n" \
"v_cvt_pk_bf16_f32 v136, v136, v137\n" \
"v_cvt_pk_bf16_f32 v137, v138, v139\n" \
"v_cvt_pk_bf16_f32 v138, v140, v141\n" \
"v_mfma_f32_16x16x32_bf16 v[192:195], a[128:131], v[128:131], 0\n" \
"v_exp_f32 v96, v96\n" \
"v_mfma_f32_16x16x32_bf16 v[224:227], a[128:131], v[132:135], 0\n" \
"v_exp_f32 v97, v97\n" \
"ds_read_b128 a[152:155], v5 offset:22528\n" \
"v_exp_f32 v98, v98\n" \
"v_cvt_pk_bf16_f32 v144, v96, v97\n" \
"v_exp_f32 v99, v99\n" \
"v_cvt_pk_bf16_f32 v139, v142, v143\n" \
"v_accvgpr_read_b32 v80, a40\n" \
"v_mfma_f32_16x16x32_bf16 v[196:199], a[132:135], v[128:131], 0\n" \
"buffer_store_dwordx4 v[136:139], v4, s[40:43], s50 offen offset:2048 sc1\n" \
"v_mfma_f32_16x16x32_bf16 v[228:231], a[132:135], v[132:135], 0\n" \
"v_exp_f32 v100, v100\n" \
"ds_read_b128 a[156:159], v5 offset:23552\n" \
"v_exp_f32 v101, v101\n" \
"v_cvt_pk_bf16_f32 v145, v98, v99\n" \
"s_waitcnt vmcnt(23) lgkmcnt(4)\n" \
"v_exp_f32 v102, v102\n" \
"v_cvt_pk_bf16_f32 v146, v100, v101\n" \
"v_mfma_f32_16x16x32_bf16 v[200:203], a[136:139], v[128:131], 0\n" \
"s_barrier\n" \
"v_exp_f32 v103, v103\n" \
"v_mfma_f32_16x16x32_bf16 v[232:235], a[136:139], v[132:135], 0\n" \
"s_add_u32 s17, s17, s19\n" \
"v_exp_f32 v104, v104\n" \
"ds_read_b128 a[128:131], v5 offset:32768\n" \
"v_exp_f32 v105, v105\n" \
"v_cvt_pk_bf16_f32 v147, v102, v103\n" \
"s_add_u32 m0, s49, 49152\n" \
"v_exp_f32 v106, v106\n" \
"v_cvt_pk_bf16_f32 v148, v104, v105\n" \
"v_mfma_f32_16x16x32_bf16 v[204:207], a[140:143], v[128:131], 0\n" \
"buffer_load_dwordx4 v6, s[40:43], s17 offen offset:0 sc1 lds\n" \
"v_mfma_f32_16x16x32_bf16 v[236:239], a[140:143], v[132:135], 0\n" \
"v_exp_f32 v107, v107\n" \
"ds_read_b128 a[132:135], v5 offset:33792\n" \
"v_exp_f32 v108, v108\n" \
"v_cvt_pk_bf16_f32 v149, v106, v107\n" \
"s_waitcnt lgkmcnt(4)\n" \
"v_exp_f32 v109, v109\n" \
"v_accvgpr_read_b32 v81, a41\n" \
"v_mfma_f32_16x16x32_bf16 v[208:211], a[144:147], v[128:131], 0\n" \
"v_exp_f32 v110, v110\n" \
"v_mfma_f32_16x16x32_bf16 v[240:243], a[144:147], v[132:135], 0\n" \
"v_exp_f32 v111, v111\n" \
"ds_read_b128 a[136:139], v5 offset:34816\n" \
"v_cvt_pk_bf16_f32 v150, v108, v109\n" \
"v_cvt_pk_bf16_f32 v151, v110, v111\n" \
"v_accvgpr_read_b32 v82, a42\n" \
"v_accvgpr_read_b32 v83, a43\n" \
"v_accvgpr_read_b32 v84, a44\n" \
"v_accvgpr_read_b32 v85, a45\n" \
"v_accvgpr_read_b32 v86, a46\n" \
"v_mfma_f32_16x16x32_bf16 v[212:215], a[148:151], v[128:131], 0\n" \
"buffer_load_dwordx4 v6, s[40:43], s17 offen offset:1024 sc1 lds\n" \
"v_mfma_f32_16x16x32_bf16 v[244:247], a[148:151], v[132:135], 0\n" \
"v_accvgpr_read_b32 v87, a47\n" \
"v_mul_f32 v80, v26, v80\n" \
"ds_read_b128 a[140:143], v5 offset:35840\n" \
"v_mul_f32 v81, v26, v81\n" \
"v_mul_f32 v82, v26, v82\n" \
"v_mul_f32 v83, v26, v83\n" \
"s_waitcnt lgkmcnt(4)\n" \
"v_mul_f32 v84, v26, v84\n" \
"v_mul_f32 v85, v26, v85\n" \
"v_mul_f32 v86, v26, v86\n" \
"v_mfma_f32_16x16x32_bf16 v[216:219], a[152:155], v[128:131], 0\n" \
"buffer_load_dwordx4 v6, s[40:43], s17 offen offset:2048 sc1 lds\n" \
"v_mfma_f32_16x16x32_bf16 v[248:251], a[152:155], v[132:135], 0\n" \
"v_mul_f32 v87, v26, v87\n" \
"v_cvt_pk_bf16_f32 v80, v80, v81\n" \
"ds_read_b128 a[144:147], v5 offset:36864\n" \
"v_cvt_pk_bf16_f32 v81, v82, v83\n" \
"v_cvt_pk_bf16_f32 v82, v84, v85\n" \
"v_cvt_pk_bf16_f32 v83, v86, v87\n" \
"v_accvgpr_read_b32 v88, a16\n" \
"v_accvgpr_read_b32 v89, a17\n" \
"v_accvgpr_read_b32 v90, a18\n" \
"v_accvgpr_read_b32 v91, a19\n" \
"v_mfma_f32_16x16x32_bf16 v[220:223], a[156:159], v[128:131], 0\n" \
"buffer_load_dwordx4 v6, s[40:43], s17 offen offset:3072 sc1 lds\n" \
"v_mfma_f32_16x16x32_bf16 v[252:255], a[156:159], v[132:135], 0\n" \
"v_accvgpr_read_b32 v92, a20\n" \
"v_accvgpr_read_b32 v93, a21\n" \
"ds_read_b128 a[148:151], v5 offset:37888\n" \
"buffer_store_dwordx4 v[80:83], v4, s[40:43], s50 offen offset:3072 sc1\n" \
"v_accvgpr_read_b32 v94, a22\n" \
"s_waitcnt lgkmcnt(4)\n" \
"v_accvgpr_read_b32 v95, a23\n" \
"v_mul_f32 v88, v22, v88\n" \
"v_mul_f32 v89, v22, v89\n" \
"v_mul_f32 v90, v22, v90\n" \
"v_mul_f32 v91, v22, v91\n" \
"v_mul_f32 v92, v22, v92\n" \
"v_mul_f32 v93, v22, v93\n" \
"v_mul_f32 v94, v22, v94\n" \
"v_mul_f32 v95, v22, v95\n" \
"v_cvt_pk_bf16_f32 v88, v88, v89\n" \
"v_cvt_pk_bf16_f32 v89, v90, v91\n" \
"v_cvt_pk_bf16_f32 v90, v92, v93\n" \
"v_cvt_pk_bf16_f32 v91, v94, v95\n" \
"v_accvgpr_read_b32 v136, a48\n" \
"v_accvgpr_read_b32 v137, a49\n" \
"buffer_store_dwordx4 v[88:91], v7, s[40:43], s50 offen offset:0 sc1\n" \
"v_accvgpr_read_b32 v138, a50\n" \
"v_accvgpr_read_b32 v139, a51\n" \
"v_accvgpr_read_b32 v140, a52\n" \
"v_accvgpr_read_b32 v141, a53\n" \
"v_accvgpr_read_b32 v142, a54\n" \
"v_accvgpr_read_b32 v143, a55\n" \
"v_mul_f32 v136, v26, v136\n" \
"v_mul_f32 v137, v26, v137\n" \
"v_mul_f32 v138, v26, v138\n" \
"v_mul_f32 v139, v26, v139\n" \
"v_mul_f32 v140, v26, v140\n" \
"v_mul_f32 v141, v26, v141\n" \
"v_mul_f32 v142, v26, v142\n" \
"v_mul_f32 v143, v26, v143\n" \
"v_cvt_pk_bf16_f32 v136, v136, v137\n" \
"v_cvt_pk_bf16_f32 v137, v138, v139\n" \
"v_cvt_pk_bf16_f32 v138, v140, v141\n" \
"v_cvt_pk_bf16_f32 v139, v142, v143\n" \
"buffer_store_dwordx4 v[136:139], v7, s[40:43], s50 offen offset:1024 sc1\n" \
"v_accvgpr_read_b32 v80, a24\n" \
"v_accvgpr_read_b32 v81, a25\n" \
"v_accvgpr_read_b32 v82, a26\n" \
"v_accvgpr_read_b32 v83, a27\n" \
"v_accvgpr_read_b32 v84, a28\n" \
"v_accvgpr_read_b32 v85, a29\n" \
"v_accvgpr_read_b32 v86, a30\n" \
"v_accvgpr_read_b32 v87, a31\n" \
"v_mul_f32 v80, v22, v80\n" \
"v_mul_f32 v81, v22, v81\n" \
"v_mul_f32 v82, v22, v82\n" \
"v_mul_f32 v83, v22, v83\n" \
"v_mul_f32 v84, v22, v84\n" \
"v_mul_f32 v85, v22, v85\n" \
"v_mul_f32 v86, v22, v86\n" \
"v_mul_f32 v87, v22, v87\n" \
"v_cvt_pk_bf16_f32 v80, v80, v81\n" \
"v_cvt_pk_bf16_f32 v81, v82, v83\n" \
"v_cvt_pk_bf16_f32 v82, v84, v85\n" \
"v_cvt_pk_bf16_f32 v83, v86, v87\n" \
"v_accvgpr_read_b32 v88, a56\n" \
"v_accvgpr_read_b32 v89, a57\n" \
"buffer_store_dwordx4 v[80:83], v7, s[40:43], s50 offen offset:2048 sc1\n" \
"v_accvgpr_read_b32 v90, a58\n" \
"v_accvgpr_read_b32 v91, a59\n" \
"v_accvgpr_read_b32 v92, a60\n" \
"v_accvgpr_read_b32 v93, a61\n" \
"v_accvgpr_read_b32 v94, a62\n" \
"v_accvgpr_read_b32 v95, a63\n" \
"v_mul_f32 v88, v26, v88\n" \
"v_mul_f32 v89, v26, v89\n" \
"v_mul_f32 v90, v26, v90\n" \
"v_mul_f32 v91, v26, v91\n" \
"v_mul_f32 v92, v26, v92\n" \
"v_mul_f32 v93, v26, v93\n" \
"v_mul_f32 v94, v26, v94\n" \
"v_mul_f32 v95, v26, v95\n" \
"v_cvt_pk_bf16_f32 v88, v88, v89\n" \
"v_cvt_pk_bf16_f32 v89, v90, v91\n" \
"v_cvt_pk_bf16_f32 v90, v92, v93\n" \
"v_cvt_pk_bf16_f32 v91, v94, v95\n" \
"buffer_store_dwordx4 v[88:91], v7, s[40:43], s50 offen offset:3072 sc1\n" \
"v_mfma_f32_16x16x32_bf16 v[64:67], a[128:131], v[32:35], 0\n" \
"v_exp_f32 v112, v112\n" \
"v_mfma_f32_16x16x32_bf16 v[72:75], a[128:131], v[48:51], 0\n" \
"s_add_u32 s16, s16, s9\n" \
"v_mfma_f32_16x16x32_bf16 v[80:83], a[128:131], a[64:67], 0\n" \
"ds_read_b128 a[152:155], v5 offset:38912\n" \
"v_mfma_f32_16x16x32_bf16 v[88:91], a[128:131], a[80:83], 0\n" \
"s_add_u32 m0, s48, 0\n" \
"v_exp_f32 v113, v113\n" \
"v_mfma_f32_16x16x32_bf16 v[64:67], a[132:135], v[36:39], v[64:67]\n" \
"buffer_load_dwordx4 v6, s[40:43], s16 offen offset:0 sc1 lds\n" \
"v_mfma_f32_16x16x32_bf16 v[72:75], a[132:135], v[52:55], v[72:75]\n" \
"v_exp_f32 v114, v114\n" \
"v_mfma_f32_16x16x32_bf16 v[80:83], a[132:135], a[68:71], v[80:83]\n" \
"ds_read_b128 a[156:159], v5 offset:39936\n" \
"v_cvt_pk_bf16_f32 v152, v112, v113\n" \
"v_mfma_f32_16x16x32_bf16 v[88:91], a[132:135], a[84:87], v[88:91]\n" \
"s_waitcnt lgkmcnt(4)\n" \
"v_exp_f32 v115, v115\n" \
"v_mfma_f32_16x16x32_bf16 v[64:67], a[136:139], v[40:43], v[64:67]\n" \
"v_exp_f32 v116, v116\n" \
"v_mfma_f32_16x16x32_bf16 v[72:75], a[136:139], v[56:59], v[72:75]\n" \
"s_xor_b32 s49, s49, 0x10000\n" \
"v_cvt_pk_bf16_f32 v153, v114, v115\n" \
"v_mfma_f32_16x16x32_bf16 v[80:83], a[136:139], a[72:75], v[80:83]\n" \
"ds_read_b128 a[128:131], v5 offset:49152\n" \
"v_mfma_f32_16x16x32_bf16 v[88:91], a[136:139], a[88:91], v[88:91]\n" \
"v_exp_f32 v117, v117\n" \
"v_mfma_f32_16x16x32_bf16 v[64:67], a[140:143], v[44:47], v[64:67]\n" \
"buffer_load_dwordx4 v6, s[40:43], s16 offen offset:1024 sc1 lds\n" \
"v_mfma_f32_16x16x32_bf16 v[72:75], a[140:143], v[60:63], v[72:75]\n" \
"v_exp_f32 v118, v118\n" \
"v_mfma_f32_16x16x32_bf16 v[80:83], a[140:143], a[76:79], v[80:83]\n" \
"ds_read_b128 a[132:135], v5 offset:50176\n" \
"v_cvt_pk_bf16_f32 v154, v116, v117\n" \
"v_mfma_f32_16x16x32_bf16 v[88:91], a[140:143], a[92:95], v[88:91]\n" \
"s_waitcnt lgkmcnt(4)\n" \
"v_exp_f32 v119, v119\n" \
"v_mfma_f32_16x16x32_bf16 v[68:71], a[144:147], v[32:35], 0\n" \
"v_exp_f32 v120, v120\n" \
"v_mfma_f32_16x16x32_bf16 v[76:79], a[144:147], v[48:51], 0\n" \
"v_cvt_pk_bf16_f32 v155, v118, v119\n" \
"v_mfma_f32_16x16x32_bf16 v[84:87], a[144:147], a[64:67], 0\n" \
"ds_read_b128 a[136:139], v5 offset:51200\n" \
"v_mfma_f32_16x16x32_bf16 v[92:95], a[144:147], a[80:83], 0\n" \
"v_exp_f32 v121, v121\n" \
"v_mfma_f32_16x16x32_bf16 v[68:71], a[148:151], v[36:39], v[68:71]\n" \
"buffer_load_dwordx4 v6, s[40:43], s16 offen offset:2048 sc1 lds\n" \
"v_mfma_f32_16x16x32_bf16 v[76:79], a[148:151], v[52:55], v[76:79]\n" \
"v_exp_f32 v122, v122\n" \
"v_mfma_f32_16x16x32_bf16 v[84:87], a[148:151], a[68:71], v[84:87]\n" \
"ds_read_b128 a[140:143], v5 offset:52224\n" \
"v_cvt_pk_bf16_f32 v156, v120, v121\n" \
"v_mfma_f32_16x16x32_bf16 v[92:95], a[148:151], a[84:87], v[92:95]\n" \
"s_waitcnt lgkmcnt(4)\n" \
"v_exp_f32 v123, v123\n" \
"v_mfma_f32_16x16x32_bf16 v[68:71], a[152:155], v[40:43], v[68:71]\n" \
"v_exp_f32 v124, v124\n" \
"v_mfma_f32_16x16x32_bf16 v[76:79], a[152:155], v[56:59], v[76:79]\n" \
"v_cvt_pk_bf16_f32 v157, v122, v123\n" \
"v_mfma_f32_16x16x32_bf16 v[84:87], a[152:155], a[72:75], v[84:87]\n" \
"ds_read_b128 a[144:147], v5 offset:53248\n" \
"v_mfma_f32_16x16x32_bf16 v[92:95], a[152:155], a[88:91], v[92:95]\n" \
"v_exp_f32 v125, v125\n" \
"v_mfma_f32_16x16x32_bf16 v[68:71], a[156:159], v[44:47], v[68:71]\n" \
"buffer_load_dwordx4 v6, s[40:43], s16 offen offset:3072 sc1 lds\n" \
"v_mfma_f32_16x16x32_bf16 v[76:79], a[156:159], v[60:63], v[76:79]\n" \
"v_exp_f32 v126, v126\n" \
"v_mfma_f32_16x16x32_bf16 v[84:87], a[156:159], a[76:79], v[84:87]\n" \
"ds_read_b128 a[148:151], v5 offset:54272\n" \
"v_cvt_pk_bf16_f32 v158, v124, v125\n" \
"v_mfma_f32_16x16x32_bf16 v[92:95], a[156:159], a[92:95], v[92:95]\n" \
"s_waitcnt lgkmcnt(4)\n" \
"v_exp_f32 v127, v127\n" \
"v_mfma_f32_16x16x32_bf16 v[160:163], v[180:183], v[144:147], v[160:163]\n" \
"v_cvt_pk_bf16_f32 v159, v126, v127\n" \
"v_mfma_f32_16x16x32_bf16 v[164:167], v[180:183], v[148:151], v[164:167]\n" \
"v_mfma_f32_16x16x32_bf16 v[168:171], v[180:183], v[152:155], 0\n" \
"v_exp_f32 v64, v64\n" \
"v_mfma_f32_16x16x32_bf16 v[172:175], v[180:183], v[156:159], 0\n" \
"v_mfma_f32_16x16x32_bf16 v[192:195], a[128:131], v[144:147], v[192:195]\n" \
"v_exp_f32 v65, v65\n" \
"v_mfma_f32_16x16x32_bf16 v[224:227], a[128:131], v[148:151], v[224:227]\n" \
"v_cvt_pk_bf16_f32 v128, v64, v65\n" \
"v_mfma_f32_16x16x32_bf16 a[0:3], a[128:131], v[152:155], 0\n" \
"ds_read_b128 a[152:155], v5 offset:55296\n" \
"v_mfma_f32_16x16x32_bf16 a[32:35], a[128:131], v[156:159], 0\n" \
"v_exp_f32 v66, v66\n" \
"v_mfma_f32_16x16x32_bf16 v[196:199], a[132:135], v[144:147], v[196:199]\n" \
"v_exp_f32 v67, v67\n" \
"v_mfma_f32_16x16x32_bf16 v[228:231], a[132:135], v[148:151], v[228:231]\n" \
"v_cvt_pk_bf16_f32 v129, v66, v67\n" \
"v_mfma_f32_16x16x32_bf16 a[4:7], a[132:135], v[152:155], 0\n" \
"ds_read_b128 a[156:159], v5 offset:56320\n" \
"v_mfma_f32_16x16x32_bf16 a[36:39], a[132:135], v[156:159], 0\n" \
"s_waitcnt vmcnt(20) lgkmcnt(4)\n" \
"v_exp_f32 v68, v68\n" \
"v_mfma_f32_16x16x32_bf16 v[200:203], a[136:139], v[144:147], v[200:203]\n" \
"s_barrier\n" \
"v_xor_b32 v5, 0x10000, v5\n" \
"v_mfma_f32_16x16x32_bf16 v[232:235], a[136:139], v[148:151], v[232:235]\n" \
"s_add_u32 s17, s17, s19\n" \
"v_exp_f32 v69, v69\n" \
"v_mfma_f32_16x16x32_bf16 a[8:11], a[136:139], v[152:155], 0\n" \
"ds_read_b128 a[128:131], v5 offset:0\n" \
"v_cvt_pk_bf16_f32 v130, v68, v69\n" \
"v_mfma_f32_16x16x32_bf16 a[40:43], a[136:139], v[156:159], 0\n" \
"s_add_u32 m0, s48, 16384\n" \
"v_exp_f32 v70, v70\n" \
"v_mfma_f32_16x16x32_bf16 v[204:207], a[140:143], v[144:147], v[204:207]\n" \
"buffer_load_dwordx4 v6, s[40:43], s17 offen offset:0 sc1 lds\n" \
"v_mfma_f32_16x16x32_bf16 v[236:239], a[140:143], v[148:151], v[236:239]\n" \
"s_waitcnt vmcnt(5)\n" \
"s_cmp_lg_u64 s[54:55], 0\ns_cbranch_scc1 LFBA_%=\nLFBAR_%=:\n" \
"v_mfma_f32_16x16x32_bf16 a[12:15], a[140:143], v[152:155], 0\n" \
"ds_read_b128 a[132:135], v5 offset:1024\n" \
"v_mfma_f32_16x16x32_bf16 a[44:47], a[140:143], v[156:159], 0\n" \
"LH20_%=:\ns_cbranch_execz LHR1_%=\n" \
"s_waitcnt lgkmcnt(4)\n" \
"v_exp_f32 v71, v71\n" \
"v_mfma_f32_16x16x32_bf16 v[208:211], a[144:147], v[144:147], v[208:211]\n" \
"buffer_store_dwordx2 v[10:11], v9, s[36:39], s52 offen sc1\n" \
"v_mfma_f32_16x16x32_bf16 v[240:243], a[144:147], v[148:151], v[240:243]\n" \
"s_mov_b32 s46, s24\n" \
"v_exp_f32 v72, v72\n" \
"v_mfma_f32_16x16x32_bf16 a[16:19], a[144:147], v[152:155], 0\n" \
"ds_read_b128 a[136:139], v5 offset:2048\n" \
"v_cvt_pk_bf16_f32 v131, v70, v71\n" \
"v_mfma_f32_16x16x32_bf16 a[48:51], a[144:147], v[156:159], 0\n" \
"v_exp_f32 v73, v73\n" \
"v_mfma_f32_16x16x32_bf16 v[212:215], a[148:151], v[144:147], v[212:215]\n" \
"buffer_load_dwordx4 v6, s[40:43], s17 offen offset:1024 sc1 lds\n" \
"v_mfma_f32_16x16x32_bf16 v[244:247], a[148:151], v[148:151], v[244:247]\n" \
"v_exp_f32 v74, v74\n" \
"v_mfma_f32_16x16x32_bf16 a[20:23], a[148:151], v[152:155], 0\n" \
"ds_read_b128 a[140:143], v5 offset:3072\n" \
"v_cvt_pk_bf16_f32 v132, v72, v73\n" \
"v_mfma_f32_16x16x32_bf16 a[52:55], a[148:151], v[156:159], 0\n" \
"s_waitcnt lgkmcnt(4)\n" \
"v_exp_f32 v75, v75\n" \
"v_mfma_f32_16x16x32_bf16 v[216:219], a[152:155], v[144:147], v[216:219]\n" \
"buffer_load_dwordx4 v6, s[40:43], s17 offen offset:2048 sc1 lds\n" \
"v_mfma_f32_16x16x32_bf16 v[248:251], a[152:155], v[148:151], v[248:251]\n" \
"s_add_u32 s44, s44, 1\n" \
"v_exp_f32 v76, v76\n" \
"v_mfma_f32_16x16x32_bf16 a[24:27], a[152:155], v[152:155], 0\n" \
"ds_read_b128 a[144:147], v5 offset:4096\n" \
"v_cvt_pk_bf16_f32 v133, v74, v75\n" \
"v_mfma_f32_16x16x32_bf16 a[56:59], a[152:155], v[156:159], 0\n" \
"v_exp_f32 v77, v77\n" \
"v_mfma_f32_16x16x32_bf16 v[220:223], a[156:159], v[144:147], v[220:223]\n" \
"buffer_load_dwordx4 v6, s[40:43], s17 offen offset:3072 sc1 lds\n" \
"v_mfma_f32_16x16x32_bf16 v[252:255], a[156:159], v[148:151], v[252:255]\n" \
"s_xor_b32 s48, s48, 0x10000\n" \
"v_exp_f32 v78, v78\n" \
"v_mfma_f32_16x16x32_bf16 a[28:31], a[156:159], v[152:155], 0\n" \
"ds_read_b128 a[148:151], v5 offset:5120\n" \
"v_exp_f32 v79, v79\n" \
"v_mfma_f32_16x16x32_bf16 a[60:63], a[156:159], v[156:159], 0\n" \
"s_waitcnt lgkmcnt(4)\n" \
"v_cvt_pk_bf16_f32 v134, v76, v77\n" \
"v_cvt_pk_bf16_f32 v135, v78, v79\n" \
"s_branch LBOT_%=\n" \
"LAD32_%=:\n" \
"v_mov_b32 v96, 0\n" \
"v_mov_b32 v97, 0\n" \
"v_mov_b32 v98, 0\n" \
"v_mov_b32 v99, 0\n" \
"s_nop 1\n" \
"v_mfma_f32_32x32x16_bf16 v[192:207], v[96:99], v[96:99], 0\n" \
"v_mfma_f32_32x32x16_bf16 v[208:223], v[96:99], v[96:99], 0\n" \
"v_mfma_f32_32x32x16_bf16 v[224:239], v[96:99], v[96:99], 0\n" \
"v_mfma_f32_32x32x16_bf16 v[240:255], v[96:99], v[96:99], 0\n" \
"s_add_u32 s50, s50, 262144\n" \
"v_subrev_u32 v24, 0x1f800000, v168\n" \
"v_subrev_u32 v25, 0x1f800000, v172\n" \
"v_max_u32 v24, v24, v25\n" \
"v_cmp_le_u32 vcc, 0x40000000, v24\n" \
"s_nop 0\n" \
"s_or_b64 s[54:55], s[54:55], vcc\n" \
"v_rcp_f32 v22, v168\n" \
"v_rcp_f32 v26, v172\n" \
"v_accvgpr_read_b32 v80, a0\n" \
"v_accvgpr_read_b32 v81, a1\n" \
"v_accvgpr_read_b32 v82, a2\n" \
"v_accvgpr_read_b32 v83, a3\n" \
"v_accvgpr_read_b32 v84, a4\n" \
"v_accvgpr_read_b32 v85, a5\n" \
"v_accvgpr_read_b32 v86, a6\n" \
"v_accvgpr_read_b32 v87, a7\n" \
"v_pk_mul_f32 v[80:81], v[80:81], v[22:23] op_sel_hi:[1,0]\n" \
"v_pk_mul_f32 v[82:83], v[82:83], v[22:23] op_sel_hi:[1,0]\n" \
"v_pk_mul_f32 v[84:85], v[84:85], v[22:23] op_sel_hi:[1,0]\n" \
"v_pk_mul_f32 v[86:87], v[86:87], v[22:23] op_sel_hi:[1,0]\n" \
"v_cvt_pk_bf16_f32 v80, v80, v81\n" \
"v_cvt_pk_bf16_f32 v81, v82, v83\n" \
"v_cvt_pk_bf16_f32 v82, v84, v85\n" \
"v_cvt_pk_bf16_f32 v83, v86, v87\n" \
"buffer_store_dwordx4 v[80:83], v4, s[40:43], s50 offen offset:0 sc1\n" \
"v_accvgpr_read_b32 v88, a32\n" \
"v_accvgpr_read_b32 v89, a33\n" \
"v_accvgpr_read_b32 v90, a34\n" \
"v_accvgpr_read_b32 v91, a35\n" \
"v_accvgpr_read_b32 v92, a36\n" \
"v_accvgpr_read_b32 v93, a37\n" \
"v_accvgpr_read_b32 v94, a38\n" \
"v_accvgpr_read_b32 v95, a39\n" \
"v_pk_mul_f32 v[88:89], v[88:89], v[26:27] op_sel_hi:[1,0]\n" \
"v_pk_mul_f32 v[90:91], v[90:91], v[26:27] op_sel_hi:[1,0]\n" \
"v_pk_mul_f32 v[92:93], v[92:93], v[26:27] op_sel_hi:[1,0]\n" \
"v_pk_mul_f32 v[94:95], v[94:95], v[26:27] op_sel_hi:[1,0]\n" \
"v_cvt_pk_bf16_f32 v88, v88, v89\n" \
"v_cvt_pk_bf16_f32 v89, v90, v91\n" \
"v_cvt_pk_bf16_f32 v90, v92, v93\n" \
"v_cvt_pk_bf16_f32 v91, v94, v95\n" \
"v_accvgpr_read_b32 v136, a8\n" \
"v_accvgpr_read_b32 v137, a9\n" \
"buffer_store_dwordx4 v[88:91], v4, s[40:43], s50 offen offset:1024 sc1\n" \
"v_accvgpr_read_b32 v138, a10\n" \
"v_accvgpr_read_b32 v139, a11\n" \
"v_accvgpr_read_b32 v140, a12\n" \
"v_accvgpr_read_b32 v141, a13\n" \
"v_accvgpr_read_b32 v142, a14\n" \
"v_accvgpr_read_b32 v143, a15\n" \
"v_pk_mul_f32 v[136:137], v[136:137], v[22:23] op_sel_hi:[1,0]\n" \
"v_pk_mul_f32 v[138:139], v[138:139], v[22:23] op_sel_hi:[1,0]\n" \
"v_pk_mul_f32 v[140:141], v[140:141], v[22:23] op_sel_hi:[1,0]\n" \
"v_pk_mul_f32 v[142:143], v[142:143], v[22:23] op_sel_hi:[1,0]\n" \
"v_cvt_pk_bf16_f32 v136, v136, v137\n" \
"v_cvt_pk_bf16_f32 v137, v138, v139\n" \
"v_cvt_pk_bf16_f32 v138, v140, v141\n" \
"v_cvt_pk_bf16_f32 v139, v142, v143\n" \
"buffer_store_dwordx4 v[136:139], v4, s[40:43], s50 offen offset:2048 sc1\n" \
"v_accvgpr_read_b32 v80, a40\n" \
"v_accvgpr_read_b32 v81, a41\n" \
"v_accvgpr_read_b32 v82, a42\n" \
"v_accvgpr_read_b32 v83, a43\n" \
"v_accvgpr_read_b32 v84, a44\n" \
"v_accvgpr_read_b32 v85, a45\n" \
"v_accvgpr_read_b32 v86, a46\n" \
"v_accvgpr_read_b32 v87, a47\n" \
"v_pk_mul_f32 v[80:81], v[80:81], v[26:27] op_sel_hi:[1,0]\n" \
"v_pk_mul_f32 v[82:83], v[82:83], v[26:27] op_sel_hi:[1,0]\n" \
"v_pk_mul_f32 v[84:85], v[84:85], v[26:27] op_sel_hi:[1,0]\n" \
"v_pk_mul_f32 v[86:87], v[86:87], v[26:27] op_sel_hi:[1,0]\n" \
"v_cvt_pk_bf16_f32 v80, v80, v81\n" \
"v_cvt_pk_bf16_f32 v81, v82, v83\n" \
"v_cvt_pk_bf16_f32 v82, v84, v85\n" \
"v_cvt_pk_bf16_f32 v83, v86, v87\n" \
"v_accvgpr_read_b32 v88, a16\n" \
"v_accvgpr_read_b32 v89, a17\n" \
"buffer_store_dwordx4 v[80:83], v4, s[40:43], s50 offen offset:3072 sc1\n" \
"v_accvgpr_read_b32 v90, a18\n" \
"v_accvgpr_read_b32 v91, a19\n" \
"v_accvgpr_read_b32 v92, a20\n" \
"v_accvgpr_read_b32 v93, a21\n" \
"v_accvgpr_read_b32 v94, a22\n" \
"v_accvgpr_read_b32 v95, a23\n" \
"v_pk_mul_f32 v[88:89], v[88:89], v[22:23] op_sel_hi:[1,0]\n" \
"v_pk_mul_f32 v[90:91], v[90:91], v[22:23] op_sel_hi:[1,0]\n" \
"v_pk_mul_f32 v[92:93], v[92:93], v[22:23] op_sel_hi:[1,0]\n" \
"v_pk_mul_f32 v[94:95], v[94:95], v[22:23] op_sel_hi:[1,0]\n" \
"v_cvt_pk_bf16_f32 v88, v88, v89\n" \
"v_cvt_pk_bf16_f32 v89, v90, v91\n" \
"v_cvt_pk_bf16_f32 v90, v92, v93\n" \
"v_cvt_pk_bf16_f32 v91, v94, v95\n" \
"buffer_store_dwordx4 v[88:91], v7, s[40:43], s50 offen offset:0 sc1\n" \
"v_accvgpr_read_b32 v136, a48\n" \
"v_accvgpr_read_b32 v137, a49\n" \
"v_accvgpr_read_b32 v138, a50\n" \
"v_accvgpr_read_b32 v139, a51\n" \
"v_accvgpr_read_b32 v140, a52\n" \
"v_accvgpr_read_b32 v141, a53\n" \
"v_accvgpr_read_b32 v142, a54\n" \
"v_accvgpr_read_b32 v143, a55\n" \
"v_pk_mul_f32 v[136:137], v[136:137], v[26:27] op_sel_hi:[1,0]\n" \
"v_pk_mul_f32 v[138:139], v[138:139], v[26:27] op_sel_hi:[1,0]\n" \
"v_pk_mul_f32 v[140:141], v[140:141], v[26:27] op_sel_hi:[1,0]\n" \
"v_pk_mul_f32 v[142:143], v[142:143], v[26:27] op_sel_hi:[1,0]\n" \
"v_cvt_pk_bf16_f32 v136, v136, v137\n" \
"v_cvt_pk_bf16_f32 v137, v138, v139\n" \
"v_cvt_pk_bf16_f32 v138, v140, v141\n" \
"v_cvt_pk_bf16_f32 v139, v142, v143\n" \
"v_accvgpr_read_b32 v80, a24\n" \
"v_accvgpr_read_b32 v81, a25\n" \
"buffer_store_dwordx4 v[136:139], v7, s[40:43], s50 offen offset:1024 sc1\n" \
"v_accvgpr_read_b32 v82, a26\n" \
"v_accvgpr_read_b32 v83, a27\n" \
"v_accvgpr_read_b32 v84, a28\n" \
"v_accvgpr_read_b32 v85, a29\n" \
"v_accvgpr_read_b32 v86, a30\n" \
"v_accvgpr_read_b32 v87, a31\n" \
"v_pk_mul_f32 v[80:81], v[80:81], v[22:23] op_sel_hi:[1,0]\n" \
"v_pk_mul_f32 v[82:83], v[82:83], v[22:23] op_sel_hi:[1,0]\n" \
"v_pk_mul_f32 v[84:85], v[84:85], v[22:23] op_sel_hi:[1,0]\n" \
"v_pk_mul_f32 v[86:87], v[86:87], v[22:23] op_sel_hi:[1,0]\n" \
"v_cvt_pk_bf16_f32 v80, v80, v81\n" \
"v_cvt_pk_bf16_f32 v81, v82, v83\n" \
"v_cvt_pk_bf16_f32 v82, v84, v85\n" \
"v_cvt_pk_bf16_f32 v83, v86, v87\n" \
"buffer_store_dwordx4 v[80:83], v7, s[40:43], s50 offen offset:2048 sc1\n" \
"v_accvgpr_read_b32 v88, a56\n" \
"v_accvgpr_read_b32 v89, a57\n" \
"v_accvgpr_read_b32 v90, a58\n" \
"v_accvgpr_read_b32 v91, a59\n" \
"v_accvgpr_read_b32 v92, a60\n" \
"v_accvgpr_read_b32 v93, a61\n" \
"v_accvgpr_read_b32 v94, a62\n" \
"v_accvgpr_read_b32 v95, a63\n" \
"v_pk_mul_f32 v[88:89], v[88:89], v[26:27] op_sel_hi:[1,0]\n" \
"v_pk_mul_f32 v[90:91], v[90:91], v[26:27] op_sel_hi:[1,0]\n" \
"v_pk_mul_f32 v[92:93], v[92:93], v[26:27] op_sel_hi:[1,0]\n" \
"v_pk_mul_f32 v[94:95], v[94:95], v[26:27] op_sel_hi:[1,0]\n" \
"v_cvt_pk_bf16_f32 v88, v88, v89\n" \
"v_cvt_pk_bf16_f32 v89, v90, v91\n" \
"v_cvt_pk_bf16_f32 v90, v92, v93\n" \
"v_cvt_pk_bf16_f32 v91, v94, v95\n" \
"buffer_store_dwordx4 v[88:91], v7, s[40:43], s50 offen offset:3072 sc1\n" \
"v_mfma_f32_32x32x16_bf16 a[0:15], v[96:99], v[96:99], 0\n" \
"v_mfma_f32_32x32x16_bf16 a[16:31], v[96:99], v[96:99], 0\n" \
"v_mfma_f32_32x32x16_bf16 a[32:47], v[96:99], v[96:99], 0\n" \
"v_mfma_f32_32x32x16_bf16 a[48:63], v[96:99], v[96:99], 0\n" \
"v_mfma_f32_32x32x16_bf16 v[160:175], v[96:99], v[96:99], 0\n" \
"v_mov_b32 v80, 0xff800000\n" \
"v_mov_b32 v81, 0xff800000\n" \
"v_mov_b32 v82, 0xff800000\n" \
"v_mov_b32 v83, 0xff800000\n" \
"v_mov_b32 v84, 0xff800000\n" \
"v_mov_b32 v85, 0xff800000\n" \
"v_mov_b32 v86, 0xff800000\n" \
"v_mov_b32 v87, 0xff800000\n" \
"v_mov_b32 v88, 0xff800000\n" \
"v_mov_b32 v89, 0xff800000\n" \
"v_mov_b32 v90, 0xff800000\n" \
"v_mov_b32 v91, 0xff800000\n" \
"v_mov_b32 v92, 0xff800000\n" \
"v_mov_b32 v93, 0xff800000\n" \
"v_mov_b32 v94, 0xff800000\n" \
"v_mov_b32 v95, 0xff800000\n" \
"s_branch LFIN_%=\n" \
"LDISP_%=:\n" \
"s_cmp_eq_u32 s44, 16\n" \
"s_cbranch_scc1 LFIN_%=\n" \
"s_cmp_eq_u32 s44, 15\ns_cselect_b32 s16, s56, s16\ns_cselect_b32 s9, s57, s9\ns_cselect_b32 s17, s58, s17\ns_cselect_b32 s19, s59, s19\n" \
"s_cbranch_scc0 LD14_%=\n" \
"buffer_load_dword v1, v191, s[36:39], 0 offen sc1\n" \
"s_branch LEVEN_%=\n" \
"LD14_%=:\n" \
"buffer_load_dwordx2 v[18:19], v17, s[36:39], 0 offen sc1\n" \
"s_branch LEVEN_%=\n" \
"LFIN_%=:\n" \
"ds_read_b128 a[128:131], v5 offset:16384\n" \
"ds_read_b128 a[132:135], v5 offset:17408\n" \
"v_exp_f32 v80, v80\n" \
"ds_read_b128 a[136:139], v5 offset:18432\n" \
"ds_read_b128 a[140:143], v5 offset:19456\n" \
"v_exp_f32 v81, v81\n" \
"ds_read_b128 a[144:147], v5 offset:20480\n" \
"ds_read_b128 a[148:151], v5 offset:21504\n" \
"v_exp_f32 v82, v82\n" \
"ds_read_b128 a[152:155], v5 offset:22528\n" \
"ds_read_b128 a[156:159], v5 offset:23552\n" \
"v_exp_f32 v83, v83\n" \
"v_exp_f32 v84, v84\n" \
"v_exp_f32 v85, v85\n" \
"v_mfma_f32_16x16x32_bf16 v[160:163], v[180:183], v[128:131], v[160:163]\n" \
"s_add_u32 s80, s65, 16384\n" \
"v_exp_f32 v86, v86\n" \
"v_cvt_pk_bf16_f32 v136, v80, v81\n" \
"v_mfma_f32_16x16x32_bf16 v[164:167], v[180:183], v[132:135], v[164:167]\n" \
"s_waitcnt lgkmcnt(6)\n" \
"v_exp_f32 v87, v87\n" \
"v_cvt_pk_bf16_f32 v137, v82, v83\n" \
"v_mfma_f32_16x16x32_bf16 v[192:195], a[128:131], v[128:131], v[192:195]\n" \
"s_add_u32 m0, s22, 32768\n" \
"v_exp_f32 v88, v88\n" \
"v_cvt_pk_bf16_f32 v138, v84, v85\n" \
"v_mfma_f32_16x16x32_bf16 v[224:227], a[128:131], v[132:135], v[224:227]\n" \
"v_exp_f32 v89, v89\n" \
"v_cvt_pk_bf16_f32 v139, v86, v87\n" \
"v_mfma_f32_16x16x32_bf16 v[196:199], a[132:135], v[128:131], v[196:199]\n" \
"s_waitcnt vmcnt(8) lgkmcnt(0)\n" \
"v_exp_f32 v90, v90\n" \
"v_cvt_pk_bf16_f32 v140, v88, v89\n" \
"v_mfma_f32_16x16x32_bf16 v[228:231], a[132:135], v[132:135], v[228:231]\n" \
"s_barrier\n" \
"v_exp_f32 v91, v91\n" \
"v_mfma_f32_16x16x32_bf16 v[200:203], a[136:139], v[128:131], v[200:203]\n" \
"ds_read_b128 v[144:147], v29 offset:32768\n" \
"v_exp_f32 v92, v92\n" \
"v_mfma_f32_16x16x32_bf16 v[232:235], a[136:139], v[132:135], v[232:235]\n" \
"ds_read_b128 v[148:151], v29 offset:33792\n" \
"v_exp_f32 v93, v93\n" \
"v_mfma_f32_16x16x32_bf16 v[204:207], a[140:143], v[128:131], v[204:207]\n" \
"ds_read_b128 v[152:155], v29 offset:36864\n" \
"v_exp_f32 v94, v94\n" \
"v_mfma_f32_16x16x32_bf16 v[236:239], a[140:143], v[132:135], v[236:239]\n" \
"ds_read_b128 v[156:159], v29 offset:37888\n" \
"v_exp_f32 v95, v95\n" \
"v_mfma_f32_16x16x32_bf16 v[208:211], a[144:147], v[128:131], v[208:211]\n" \
"ds_read_b128 v[112:115], v29 offset:40960\n" \
"v_cvt_pk_bf16_f32 v141, v90, v91\n" \
"v_cvt_pk_bf16_f32 v142, v92, v93\n" \
"v_mfma_f32_16x16x32_bf16 v[240:243], a[144:147], v[132:135], v[240:243]\n" \
"ds_read_b128 v[116:119], v29 offset:41984\n" \
"v_cvt_pk_bf16_f32 v143, v94, v95\n" \
"v_cvt_pk_bf16_f32 v192, v192, v193\n" \
"v_mfma_f32_16x16x32_bf16 v[212:215], a[148:151], v[128:131], v[212:215]\n" \
"ds_read_b128 v[120:123], v29 offset:34816\n" \
"v_cvt_pk_bf16_f32 v193, v194, v195\n" \
"v_cvt_pk_bf16_f32 v194, v196, v197\n" \
"v_mfma_f32_16x16x32_bf16 v[244:247], a[148:151], v[132:135], v[244:247]\n" \
"ds_read_b128 v[124:127], v29 offset:35840\n" \
"v_cvt_pk_bf16_f32 v195, v198, v199\n" \
"v_cvt_pk_bf16_f32 v224, v224, v225\n" \
"v_mfma_f32_16x16x32_bf16 v[216:219], a[152:155], v[128:131], v[216:219]\n" \
"ds_read_b128 v[96:99], v29 offset:38912\n" \
"v_cvt_pk_bf16_f32 v225, v226, v227\n" \
"v_cvt_pk_bf16_f32 v226, v228, v229\n" \
"v_mfma_f32_16x16x32_bf16 v[248:251], a[152:155], v[132:135], v[248:251]\n" \
"ds_read_b128 v[100:103], v29 offset:39936\n" \
"v_cvt_pk_bf16_f32 v227, v230, v231\n" \
"v_cvt_pk_bf16_f32 v200, v200, v201\n" \
"v_mfma_f32_16x16x32_bf16 v[220:223], a[156:159], v[128:131], v[220:223]\n" \
"ds_read_b128 v[104:107], v29 offset:43008\n" \
"v_cvt_pk_bf16_f32 v201, v202, v203\n" \
"v_cvt_pk_bf16_f32 v202, v204, v205\n" \
"v_mfma_f32_16x16x32_bf16 v[252:255], a[156:159], v[132:135], v[252:255]\n" \
"ds_read_b128 v[108:111], v29 offset:44032\n" \
"v_cvt_pk_bf16_f32 v203, v206, v207\n" \
"v_cvt_pk_bf16_f32 v232, v232, v233\n" \
"v_mfma_f32_16x16x32_bf16 v[168:171], v[180:183], v[136:139], v[168:171]\n" \
"ds_write2_b32 v184, v160, v164 offset1:64\n" \
"v_cvt_pk_bf16_f32 v233, v234, v235\n" \
"v_cvt_pk_bf16_f32 v234, v236, v237\n" \
"v_mfma_f32_16x16x32_bf16 v[172:175], v[180:183], v[140:143], v[172:175]\n" \
"buffer_load_dwordx4 v4, s[36:39], s80 offen offset:0 sc1 lds\n" \
"v_mfma_f32_16x16x32_bf16 a[0:3], a[128:131], v[136:139], a[0:3]\n" \
"ds_write_b128 v186, v[192:195] offset:0\n" \
"v_cvt_pk_bf16_f32 v235, v238, v239\n" \
"v_cvt_pk_bf16_f32 v208, v208, v209\n" \
"v_mfma_f32_16x16x32_bf16 a[32:35], a[128:131], v[140:143], a[32:35]\n" \
"ds_write_b128 v186, v[224:227] offset:4096\n" \
"v_cvt_pk_bf16_f32 v209, v210, v211\n" \
"v_cvt_pk_bf16_f32 v210, v212, v213\n" \
"v_mfma_f32_16x16x32_bf16 a[4:7], a[132:135], v[136:139], a[4:7]\n" \
"ds_write_b128 v186, v[200:203] offset:1024\n" \
"v_cvt_pk_bf16_f32 v211, v214, v215\n" \
"v_cvt_pk_bf16_f32 v240, v240, v241\n" \
"v_mfma_f32_16x16x32_bf16 a[36:39], a[132:135], v[140:143], a[36:39]\n" \
"buffer_load_dwordx4 v4, s[36:39], s80 offen offset:1024 sc1 lds\n" \
"v_mfma_f32_16x16x32_bf16 a[8:11], a[136:139], v[136:139], a[8:11]\n" \
"ds_write_b128 v186, v[232:235] offset:5120\n" \
"v_cvt_pk_bf16_f32 v241, v242, v243\n" \
"v_cvt_pk_bf16_f32 v242, v244, v245\n" \
"v_mfma_f32_16x16x32_bf16 a[40:43], a[136:139], v[140:143], a[40:43]\n" \
"ds_write_b128 v186, v[208:211] offset:2048\n" \
"v_cvt_pk_bf16_f32 v243, v246, v247\n" \
"v_cvt_pk_bf16_f32 v216, v216, v217\n" \
"v_mfma_f32_16x16x32_bf16 a[12:15], a[140:143], v[136:139], a[12:15]\n" \
"ds_write_b128 v186, v[240:243] offset:6144\n" \
"v_cvt_pk_bf16_f32 v217, v218, v219\n" \
"v_cvt_pk_bf16_f32 v218, v220, v221\n" \
"v_mfma_f32_16x16x32_bf16 a[44:47], a[140:143], v[140:143], a[44:47]\n" \
"buffer_load_dwordx4 v4, s[36:39], s80 offen offset:2048 sc1 lds\n" \
"v_mfma_f32_16x16x32_bf16 a[16:19], a[144:147], v[136:139], a[16:19]\n" \
"v_cvt_pk_bf16_f32 v219, v222, v223\n" \
"v_cvt_pk_bf16_f32 v248, v248, v249\n" \
"v_cvt_pk_bf16_f32 v249, v250, v251\n" \
"v_mfma_f32_16x16x32_bf16 a[48:51], a[144:147], v[140:143], a[48:51]\n" \
"ds_write_b128 v186, v[216:219] offset:3072\n" \
"v_cvt_pk_bf16_f32 v250, v252, v253\n" \
"v_cvt_pk_bf16_f32 v251, v254, v255\n" \
"v_mfma_f32_16x16x32_bf16 a[20:23], a[148:151], v[136:139], a[20:23]\n" \
"ds_write_b128 v186, v[248:251] offset:7168\n" \
"v_and_b32 v188, 16, v4\n" \
"v_mov_b32 v189, 0x3f80\n" \
"v_mfma_f32_16x16x32_bf16 a[52:55], a[148:151], v[140:143], a[52:55]\n" \
"buffer_load_dwordx4 v4, s[36:39], s80 offen offset:3072 sc1 lds\n" \
"v_mfma_f32_16x16x32_bf16 a[24:27], a[152:155], v[136:139], a[24:27]\n" \
"s_mov_b32 s81, 786432\n" \
"s_add_u32 m0, s79, 49152\n" \
"v_lshlrev_b32 v188, v188, v189\n" \
"v_mov_b32 v22, 0\n" \
"v_mfma_f32_16x16x32_bf16 a[56:59], a[152:155], v[140:143], a[56:59]\n" \
"s_mov_b32 s94, 0x300003\n" \
"v_mov_b32 v23, 0\n" \
"v_mov_b32 v26, 0\n" \
"v_mov_b32 v27, 0\n" \
"v_mfma_f32_16x16x32_bf16 a[28:31], a[156:159], v[136:139], a[28:31]\n" \
"s_waitcnt lgkmcnt(15)\n" \
"s_mov_b32 s95, 0x30000300\n" \
"v_cndmask_b32 v24, 0, v188, s[94:95]\n" \
"s_mov_b32 s94, 0xc0000c\n" \
"v_mfma_f32_16x16x32_bf16 a[60:63], a[156:159], v[140:143], a[60:63]\n" \
"buffer_load_dwordx4 v12, s[28:31], s81 offen offset:0 lds\n" \
"v_mfma_f32_16x16x32_bf16 v[32:35], a[160:163], v[144:147], 0\n" \
"s_add_u32 m0, s79, 50048\n" \
"s_mov_b32 s95, 0xc0000c00\n" \
"v_cndmask_b32 v25, 0, v188, s[94:95]\n" \
"v_mfma_f32_16x16x32_bf16 v[36:39], a[164:167], v[144:147], 0\n" \
"s_waitcnt lgkmcnt(0)\n" \
"v_add_u32 v176, 0xfffe0000, v13\n" \
"v_add_u32 v177, 0xfffe0000, v14\n" \
"v_mfma_f32_16x16x32_bf16 v[40:43], a[160:163], v[148:151], 0\n" \
"s_waitcnt lgkmcnt(15)\n" \
"s_barrier\n" \
"ds_read2_b32 v[190:191], v185 offset1:64\n" \
"v_mfma_f32_16x16x32_bf16 v[44:47], a[164:167], v[148:151], 0\n" \
"buffer_load_dwordx4 v12, s[28:31], s81 offen offset:128 lds\n" \
"v_mfma_f32_16x16x32_bf16 v[48:51], a[160:163], v[152:155], 0\n" \
"s_mov_b32 s81, 851968\n" \
"s_add_u32 m0, s79, 57344\n" \
"ds_read_b128 v[128:131], v187 offset:0\n" \
"v_mfma_f32_16x16x32_bf16 v[52:55], a[164:167], v[152:155], 0\n" \
"ds_read_b128 v[132:135], v187 offset:1024\n" \
"v_add_u32 v178, 0xfffe0000, v15\n" \
"v_mfma_f32_16x16x32_bf16 v[56:59], a[160:163], v[156:159], 0\n" \
"s_waitcnt lgkmcnt(15)\n" \
"ds_read_b128 v[136:139], v187 offset:2048\n" \
"v_add_u32 v179, 0xfffe0000, v16\n" \
"v_mfma_f32_16x16x32_bf16 v[60:63], a[164:167], v[156:159], 0\n" \
"buffer_load_dwordx4 v12, s[28:31], s81 offen offset:0 lds\n" \
"v_mfma_f32_16x16x32_bf16 v[64:67], a[160:163], v[112:115], 0\n" \
"s_add_u32 m0, s79, 58240\n" \
"ds_read_b128 v[140:143], v187 offset:3072\n" \
"v_add_u32 v180, 0xffff0000, v13\n" \
"v_mfma_f32_16x16x32_bf16 v[68:71], a[164:167], v[112:115], 0\n" \
"ds_read_b128 v[144:147], v187 offset:4096\n" \
"v_add_u32 v181, 0xffff0000, v14\n" \
"v_mfma_f32_16x16x32_bf16 v[72:75], a[160:163], v[116:119], 0\n" \
"s_waitcnt lgkmcnt(15)\n" \
"ds_read_b128 v[112:115], v29 offset:45056\n" \
"v_add_u32 v182, 0xffff0000, v15\n" \
"v_mfma_f32_16x16x32_bf16 v[76:79], a[164:167], v[116:119], 0\n" \
"buffer_load_dwordx4 v12, s[28:31], s81 offen offset:128 lds\n" \
"v_mfma_f32_16x16x32_bf16 v[32:35], a[168:171], v[120:123], v[32:35]\n" \
"s_mov_b32 s81, 917504\n" \
"ds_read_b128 v[116:119], v29 offset:46080\n" \
"s_add_u32 m0, s79, 65536\n" \
"v_mfma_f32_16x16x32_bf16 v[36:39], a[172:175], v[120:123], v[36:39]\n" \
"ds_read_b128 v[148:151], v187 offset:5120\n" \
"v_add_u32 v183, 0xffff0000, v16\n" \
"v_mfma_f32_16x16x32_bf16 v[40:43], a[168:171], v[124:127], v[40:43]\n" \
"ds_read_b128 v[120:123], v29 offset:49152\n" \
"s_waitcnt lgkmcnt(9)\n" \
"v_mfma_f32_16x16x32_bf16 v[44:47], a[172:175], v[124:127], v[44:47]\n" \
"s_waitcnt lgkmcnt(9)\n" \
"s_barrier\n" \
"ds_read_b128 v[152:155], v187 offset:6144\n" \
"v_mfma_f32_16x16x32_bf16 v[48:51], a[168:171], v[96:99], v[48:51]\n" \
"ds_read_b128 v[124:127], v29 offset:50176\n" \
"v_add_f32 v190, v168, v190\n" \
"v_mfma_f32_16x16x32_bf16 v[52:55], a[172:175], v[96:99], v[52:55]\n" \
"buffer_load_dwordx4 v12, s[28:31], s81 offen offset:0 lds\n" \
"v_mfma_f32_16x16x32_bf16 v[56:59], a[168:171], v[100:103], v[56:59]\n" \
"s_waitcnt lgkmcnt(15)\n" \
"ds_read_b128 v[96:99], v29 offset:53248\n" \
"s_add_u32 m0, s79, 66432\n" \
"v_mfma_f32_16x16x32_bf16 v[60:63], a[172:175], v[100:103], v[60:63]\n" \
"ds_read_b128 v[156:159], v187 offset:7168\n" \
"v_add_f32 v191, v172, v191\n" \
"v_mfma_f32_16x16x32_bf16 v[64:67], a[168:171], v[104:107], v[64:67]\n" \
"ds_read_b128 v[100:103], v29 offset:54272\n" \
"v_subrev_u32 v188, 0x1f800000, v190\n" \
"v_mfma_f32_16x16x32_bf16 v[68:71], a[172:175], v[104:107], v[68:71]\n" \
"buffer_load_dwordx4 v12, s[28:31], s81 offen offset:128 lds\n" \
"v_mfma_f32_16x16x32_bf16 v[72:75], a[168:171], v[108:111], v[72:75]\n" \
"s_waitcnt lgkmcnt(7)\n" \
"ds_read_b128 v[104:107], v29 offset:47104\n" \
"s_mov_b32 s81, 983040\n" \
"v_mfma_f32_16x16x32_bf16 v[76:79], a[172:175], v[108:111], v[76:79]\n" \
"s_add_u32 m0, s79, 73728\n" \
"v_subrev_u32 v189, 0x1f800000, v191\n" \
"v_max_u32 v188, v188, v189\n" \
"v_mfma_f32_16x16x32_bf16 v[32:35], a[176:179], v[112:115], v[32:35]\n" \
"ds_read_b128 v[108:111], v29 offset:48128\n" \
"v_cmp_le_u32 vcc, 0x40000000, v188\n" \
"v_accvgpr_read_b32 v192, a0\n" \
"v_accvgpr_read_b32 v193, a1\n" \
"v_accvgpr_read_b32 v194, a2\n" \
"v_accvgpr_read_b32 v195, a3\n" \
"v_accvgpr_read_b32 v196, a4\n" \
"v_accvgpr_read_b32 v197, a5\n" \
"v_accvgpr_read_b32 v198, a6\n" \
"v_accvgpr_read_b32 v199, a7\n" \
"v_mfma_f32_16x16x32_bf16 v[36:39], a[180:183], v[112:115], v[36:39]\n" \
"s_waitcnt lgkmcnt(15)\n" \
"s_nop 0\n" \
"s_or_b64 s[4:5], s[4:5], vcc\n" \
"v_mfma_f32_16x16x32_bf16 v[192:195], v[24:27], v[128:131], v[192:195]\n" \
"buffer_load_dwordx4 v12, s[28:31], s81 offen offset:0 lds\n" \
"v_mfma_f32_16x16x32_bf16 v[196:199], v[22:25], v[128:131], v[196:199]\n" \
"s_add_u32 m0, s79, 74624\n" \
"v_rcp_f32 v186, v190\n" \
"v_mfma_f32_16x16x32_bf16 v[40:43], a[176:179], v[116:119], v[40:43]\n" \
"s_waitcnt lgkmcnt(5)\n" \
"ds_read_b128 v[112:115], v29 offset:51200\n" \
"v_accvgpr_read_b32 v200, a8\n" \
"v_mfma_f32_16x16x32_bf16 v[44:47], a[180:183], v[116:119], v[44:47]\n" \
"v_rcp_f32 v187, v191\n" \
"v_mfma_f32_16x16x32_bf16 v[48:51], a[176:179], v[120:123], v[48:51]\n" \
"ds_read_b128 v[116:119], v29 offset:52224\n" \
"s_waitcnt vmcnt(7)\n" \
"v_mfma_f32_16x16x32_bf16 v[52:55], a[180:183], v[120:123], v[52:55]\n" \
"buffer_load_dwordx4 v12, s[28:31], s81 offen offset:128 lds\n" \
"v_mfma_f32_16x16x32_bf16 v[56:59], a[176:179], v[124:127], v[56:59]\n" \
"s_add_u32 s80, s66, 16384\n" \
"ds_read_b128 v[120:123], v29 offset:55296\n" \
"s_add_u32 m0, s22, 81920\n" \
"v_mfma_f32_16x16x32_bf16 v[60:63], a[180:183], v[124:127], v[60:63]\n" \
"s_waitcnt vmcnt(16) lgkmcnt(5)\n" \
"s_barrier\n" \
"v_bfe_u32 v188, v4, 7, 2\n" \
"v_mfma_f32_16x16x32_bf16 v[64:67], a[176:179], v[96:99], v[64:67]\n" \
"ds_read_b128 v[124:127], v29 offset:56320\n" \
"v_min_u32 v188, 0, v188\n" \
"v_mfma_f32_16x16x32_bf16 v[68:71], a[180:183], v[96:99], v[68:71]\n" \
"buffer_load_dwordx4 v4, s[36:39], s80 offen offset:0 sc1 lds\n" \
"v_mfma_f32_16x16x32_bf16 v[72:75], a[176:179], v[100:103], v[72:75]\n" \
"s_waitcnt lgkmcnt(4)\n" \
"ds_read_b128 v[96:99], v29 offset:57344\n" \
"v_lshl_add_u32 v17, v188, 8, s10\n" \
"v_mfma_f32_16x16x32_bf16 v[76:79], a[180:183], v[100:103], v[76:79]\n" \
"s_cmp_lg_u64 s[54:55], 0\ns_cbranch_scc1 LFBC_%=\nLFBCR_%=:\n" \
"v_bfe_u32 v188, v4, 4, 3\n" \
"v_mfma_f32_16x16x32_bf16 v[32:35], a[184:187], v[104:107], v[32:35]\n" \
"s_andn2_b32 s94, s88, 0xc0\n" \
"ds_read_b128 v[100:103], v29 offset:58368\n" \
"v_lshl_add_u32 v17, v188, 3, v17\n" \
"v_mfma_f32_16x16x32_bf16 v[36:39], a[188:191], v[104:107], v[36:39]\n" \
"buffer_store_dwordx2 v[10:11], v9, s[36:39], s52 offen sc1\n" \
"v_mfma_f32_16x16x32_bf16 v[40:43], a[184:187], v[108:111], v[40:43]\n" \
"s_waitcnt lgkmcnt(4)\n" \
"ds_read_b128 v[104:107], v29 offset:61440\n" \
"v_and_b32 v188, 0x30, v4\n" \
"v_mfma_f32_16x16x32_bf16 v[44:47], a[188:191], v[108:111], v[44:47]\n" \
"buffer_load_dwordx4 v4, s[36:39], s80 offen offset:1024 sc1 lds\n" \
"v_mfma_f32_16x16x32_bf16 v[48:51], a[184:187], v[112:115], v[48:51]\n" \
"ds_read_b128 v[108:111], v29 offset:62464\n" \
"v_mul_f32 v192, v186, v192\n" \
"v_accvgpr_read_b32 v201, a9\n" \
"v_accvgpr_read_b32 v202, a10\n" \
"v_accvgpr_read_b32 v203, a11\n" \
"v_accvgpr_read_b32 v204, a12\n" \
"v_accvgpr_read_b32 v205, a13\n" \
"v_accvgpr_read_b32 v206, a14\n" \
"v_accvgpr_read_b32 v207, a15\n" \
"v_mfma_f32_16x16x32_bf16 v[52:55], a[188:191], v[112:115], v[52:55]\n" \
"s_waitcnt lgkmcnt(15)\n" \
"v_mul_f32 v193, v186, v193\n" \
"v_mul_f32 v194, v186, v194\n" \
"v_mfma_f32_16x16x32_bf16 v[200:203], v[24:27], v[132:135], v[200:203]\n" \
"s_mov_b32 exec_lo, 0\nv_lshl_add_u32 v17, v188, 2, s94\ns_mov_b32 exec_lo, -1\n" \
"v_mfma_f32_16x16x32_bf16 v[204:207], v[22:25], v[132:135], v[204:207]\n" \
"buffer_load_dwordx4 v4, s[36:39], s80 offen offset:2048 sc1 lds\n" \
"v_mfma_f32_16x16x32_bf16 v[56:59], a[184:187], v[116:119], v[56:59]\n" \
"s_waitcnt lgkmcnt(4)\n" \
"ds_read_b128 v[112:115], v28 offset:0\n" \
"s_sub_u32 s96, s72, 109051904\n" \
"v_mfma_f32_16x16x32_bf16 v[60:63], a[188:191], v[116:119], v[60:63]\n" \
"s_lshr_b32 s96, s96, 11\n" \
"s_mul_i32 s97, s96, 10923\n" \
"s_lshr_b32 s97, s97, 16\n" \
"v_mfma_f32_16x16x32_bf16 v[64:67], a[184:187], v[120:123], v[64:67]\n" \
"s_lshl_b32 s97, s97, 1\n" \
"ds_read_b128 v[116:119], v28 offset:1024\n" \
"s_sub_u32 s96, s96, s97\n" \
"v_mfma_f32_16x16x32_bf16 v[68:71], a[188:191], v[120:123], v[68:71]\n" \
"buffer_load_dwordx4 v4, s[36:39], s80 offen offset:3072 sc1 lds\n" \
"v_mfma_f32_16x16x32_bf16 v[72:75], a[184:187], v[124:127], v[72:75]\n" \
"s_mov_b32 s81, 1048576\n" \
"ds_read_b128 v[120:123], v29 offset:59392\n" \
"s_add_u32 m0, s79, 131072\n" \
"v_mfma_f32_16x16x32_bf16 v[76:79], a[188:191], v[124:127], v[76:79]\n" \
"s_waitcnt vmcnt(9) lgkmcnt(5)\n" \
"s_barrier\n" \
"s_lshl_b32 s96, s96, 3\n" \
"v_mfma_f32_16x16x32_bf16 v[32:35], a[192:195], v[96:99], v[32:35]\n" \
"LH21_%=:\ns_cbranch_execz LH25_%=\n" \
"s_add_u32 s23, s96, 94437376\n" \
"ds_read_b128 v[124:127], v29 offset:60416\n" \
"s_sub_u32 s96, s73, 109051904\n" \
"v_mfma_f32_16x16x32_bf16 v[36:39], a[196:199], v[96:99], v[36:39]\n" \
"buffer_load_dwordx4 v12, s[28:31], s81 offen offset:0 lds\n" \
"v_mfma_f32_16x16x32_bf16 v[40:43], a[192:195], v[100:103], v[40:43]\n" \
"s_waitcnt lgkmcnt(4)\n" \
"ds_read_b128 v[96:99], v29 offset:63488\n" \
"s_add_u32 m0, s79, 131968\n" \
"v_mfma_f32_16x16x32_bf16 v[44:47], a[196:199], v[100:103], v[44:47]\n" \
"s_lshr_b32 s96, s96, 11\n" \
"ds_read_b64_tr_b16 a[64:65], v176 offset:49152\n" \
"s_mul_i32 s97, s96, 10923\n" \
"v_mfma_f32_16x16x32_bf16 v[48:51], a[192:195], v[104:107], v[48:51]\n" \
"s_lshr_b32 s97, s97, 16\n" \
"ds_read_b128 v[100:103], v29 offset:64512\n" \
"s_lshl_b32 s97, s97, 1\n" \
"v_mfma_f32_16x16x32_bf16 v[52:55], a[196:199], v[104:107], v[52:55]\n" \
"buffer_load_dwordx4 v12, s[28:31], s81 offen offset:128 lds\n" \
"v_mfma_f32_16x16x32_bf16 v[56:59], a[192:195], v[108:111], v[56:59]\n" \
"s_waitcnt lgkmcnt(5)\n" \
"ds_read_b128 v[104:107], v28 offset:2048\n" \
"s_mov_b32 s81, 1114112\n" \
"v_mfma_f32_16x16x32_bf16 v[60:63], a[196:199], v[108:111], v[60:63]\n" \
"s_add_u32 m0, s79, 139264\n" \
"ds_read_b64_tr_b16 a[66:67], v177 offset:49152\n" \
"s_sub_u32 s96, s96, s97\n" \
"v_mfma_f32_16x16x32_bf16 v[64:67], a[192:195], v[112:115], v[64:67]\n" \
"s_lshl_b32 s96, s96, 3\n" \
"ds_read_b128 v[108:111], v28 offset:3072\n" \
"s_add_u32 s24, s96, 94437376\n" \
"v_accvgpr_read_b32 v208, a16\n" \
"v_accvgpr_read_b32 v209, a17\n" \
"v_accvgpr_read_b32 v210, a18\n" \
"v_accvgpr_read_b32 v211, a19\n" \
"v_accvgpr_read_b32 v212, a20\n" \
"v_accvgpr_read_b32 v213, a21\n" \
"v_accvgpr_read_b32 v214, a22\n" \
"v_accvgpr_read_b32 v215, a23\n" \
"v_mfma_f32_16x16x32_bf16 v[68:71], a[196:199], v[112:115], v[68:71]\n" \
"s_waitcnt lgkmcnt(15)\n" \
"ds_read_b64_tr_b16 a[68:69], v178 offset:49152\n" \
"s_sub_u32 s96, s74, 109051904\n" \
"v_mfma_f32_16x16x32_bf16 v[208:211], v[24:27], v[136:139], v[208:211]\n" \
"buffer_load_dwordx4 v12, s[28:31], s81 offen offset:0 lds\n" \
"v_mfma_f32_16x16x32_bf16 v[212:215], v[22:25], v[136:139], v[212:215]\n" \
"s_add_u32 m0, s79, 140160\n" \
"ds_read_b64_tr_b16 a[70:71], v179 offset:49152\n" \
"s_lshr_b32 s96, s96, 11\n" \
"v_mfma_f32_16x16x32_bf16 v[72:75], a[192:195], v[116:119], v[72:75]\n" \
"s_waitcnt lgkmcnt(8)\n" \
"ds_read_b128 v[112:115], v28 offset:4096\n" \
"s_mul_i32 s97, s96, 10923\n" \
"v_mfma_f32_16x16x32_bf16 v[76:79], a[196:199], v[116:119], v[76:79]\n" \
"s_lshr_b32 s97, s97, 16\n" \
"ds_read_b64_tr_b16 a[72:73], v176 offset:57344\n" \
"s_lshl_b32 s97, s97, 1\n" \
"v_mfma_f32_16x16x32_bf16 v[32:35], a[200:203], v[120:123], v[32:35]\n" \
"s_sub_u32 s96, s96, s97\n" \
"ds_read_b128 v[116:119], v28 offset:5120\n" \
"s_lshl_b32 s96, s96, 3\n" \
"v_mfma_f32_16x16x32_bf16 v[36:39], a[204:207], v[120:123], v[36:39]\n" \
"buffer_load_dwordx4 v12, s[28:31], s81 offen offset:128 lds\n" \
"v_mfma_f32_16x16x32_bf16 v[40:43], a[200:203], v[124:127], v[40:43]\n" \
"s_mov_b32 s81, 1179648\n" \
"ds_read_b128 v[120:123], v28 offset:8192\n" \
"s_add_u32 m0, s79, 147456\n" \
"v_mfma_f32_16x16x32_bf16 v[44:47], a[204:207], v[124:127], v[44:47]\n" \
"s_waitcnt lgkmcnt(9)\n" \
"s_barrier\n" \
"ds_read_b64_tr_b16 a[74:75], v177 offset:57344\n" \
"v_mfma_f32_16x16x32_bf16 v[48:51], a[200:203], v[96:99], v[48:51]\n" \
"s_add_u32 s45, s96, 94437376\n" \
"ds_read_b128 v[124:127], v28 offset:9216\n" \
"v_mul_f32 v195, v186, v195\n" \
"v_mfma_f32_16x16x32_bf16 v[52:55], a[204:207], v[96:99], v[52:55]\n" \
"buffer_load_dwordx4 v12, s[28:31], s81 offen offset:0 lds\n" \
"v_mfma_f32_16x16x32_bf16 v[56:59], a[200:203], v[100:103], v[56:59]\n" \
"s_waitcnt lgkmcnt(8)\n" \
"ds_read_b128 v[96:99], v28 offset:12288\n" \
"s_add_u32 m0, s79, 148352\n" \
"v_mfma_f32_16x16x32_bf16 v[60:63], a[204:207], v[100:103], v[60:63]\n" \
"ds_read_b64_tr_b16 a[76:77], v178 offset:57344\n" \
"v_mul_f32 v196, v186, v196\n" \
"v_mfma_f32_16x16x32_bf16 v[64:67], a[200:203], v[104:107], v[64:67]\n" \
"ds_read_b128 v[100:103], v28 offset:13312\n" \
"v_mul_f32 v197, v186, v197\n" \
"v_mfma_f32_16x16x32_bf16 v[68:71], a[204:207], v[104:107], v[68:71]\n" \
"buffer_load_dwordx4 v12, s[28:31], s81 offen offset:128 lds\n" \
"v_mfma_f32_16x16x32_bf16 v[72:75], a[200:203], v[108:111], v[72:75]\n" \
"s_waitcnt lgkmcnt(6)\n" \
"ds_read_b128 v[104:107], v28 offset:6144\n" \
"s_mov_b32 s81, 1245184\n" \
"v_mfma_f32_16x16x32_bf16 v[76:79], a[204:207], v[108:111], v[76:79]\n" \
"s_add_u32 m0, s79, 155648\n" \
"ds_read_b64_tr_b16 a[78:79], v179 offset:57344\n" \
"v_mul_f32 v198, v186, v198\n" \
"v_mfma_f32_16x16x32_bf16 v[32:35], a[208:211], v[112:115], v[32:35]\n" \
"ds_read_b128 v[108:111], v28 offset:7168\n" \
"v_mul_f32 v199, v186, v199\n" \
"v_accvgpr_read_b32 v216, a24\n" \
"v_accvgpr_read_b32 v217, a25\n" \
"v_accvgpr_read_b32 v218, a26\n" \
"v_accvgpr_read_b32 v219, a27\n" \
"v_accvgpr_read_b32 v220, a28\n" \
"v_accvgpr_read_b32 v221, a29\n" \
"v_accvgpr_read_b32 v222, a30\n" \
"v_accvgpr_read_b32 v223, a31\n" \
"v_mfma_f32_16x16x32_bf16 v[36:39], a[212:215], v[112:115], v[36:39]\n" \
"s_waitcnt lgkmcnt(15)\n" \
"v_cvt_pk_bf16_f32 v192, v192, v193\n" \
"v_cvt_pk_bf16_f32 v193, v194, v195\n" \
"v_mfma_f32_16x16x32_bf16 v[216:219], v[24:27], v[140:143], v[216:219]\n" \
"buffer_load_dwordx4 v12, s[28:31], s81 offen offset:0 lds\n" \
"v_mfma_f32_16x16x32_bf16 v[220:223], v[22:25], v[140:143], v[220:223]\n" \
"s_add_u32 m0, s79, 156544\n" \
"v_cvt_pk_bf16_f32 v194, v196, v197\n" \
"v_cvt_pk_bf16_f32 v195, v198, v199\n" \
"v_mfma_f32_16x16x32_bf16 v[40:43], a[208:211], v[116:119], v[40:43]\n" \
"s_waitcnt lgkmcnt(6)\n" \
"ds_read_b128 v[112:115], v28 offset:10240\n" \
"v_mul_f32 v200, v186, v200\n" \
"v_mfma_f32_16x16x32_bf16 v[44:47], a[212:215], v[116:119], v[44:47]\n" \
"buffer_store_dwordx4 v[192:195], v4, s[40:43], s51 offen offset:0 sc1\n" \
"v_mfma_f32_16x16x32_bf16 v[48:51], a[208:211], v[120:123], v[48:51]\n" \
"ds_read_b128 v[116:119], v28 offset:11264\n" \
"v_mul_f32 v201, v186, v201\n" \
"v_mfma_f32_16x16x32_bf16 v[52:55], a[212:215], v[120:123], v[52:55]\n" \
"buffer_load_dwordx4 v12, s[28:31], s81 offen offset:128 lds\n" \
"v_mfma_f32_16x16x32_bf16 v[56:59], a[208:211], v[124:127], v[56:59]\n" \
"s_add_u32 s80, s64, 32768\n" \
"ds_read_b128 v[120:123], v28 offset:14336\n" \
"s_add_u32 m0, s22, 98304\n" \
"v_mfma_f32_16x16x32_bf16 v[60:63], a[212:215], v[124:127], v[60:63]\n" \
"s_waitcnt vmcnt(14) lgkmcnt(4)\n" \
"s_barrier\n" \
"v_mul_f32 v202, v186, v202\n" \
"v_mfma_f32_16x16x32_bf16 v[64:67], a[208:211], v[96:99], v[64:67]\n" \
"ds_read_b128 v[124:127], v28 offset:15360\n" \
"v_mul_f32 v203, v186, v203\n" \
"v_mfma_f32_16x16x32_bf16 v[68:71], a[212:215], v[96:99], v[68:71]\n" \
"buffer_load_dwordx4 v4, s[36:39], s80 offen offset:0 sc1 lds\n" \
"v_mfma_f32_16x16x32_bf16 v[72:75], a[208:211], v[100:103], v[72:75]\n" \
"s_waitcnt lgkmcnt(4)\n" \
"ds_read_b128 v[96:99], v28 offset:16384\n" \
"v_mul_f32 v204, v186, v204\n" \
"v_mfma_f32_16x16x32_bf16 v[76:79], a[212:215], v[100:103], v[76:79]\n" \
"ds_read_b64_tr_b16 a[80:81], v180 offset:0\n" \
"v_mul_f32 v205, v186, v205\n" \
"v_mfma_f32_16x16x32_bf16 v[32:35], a[216:219], v[104:107], v[32:35]\n" \
"ds_read_b128 v[100:103], v28 offset:17408\n" \
"v_mul_f32 v206, v186, v206\n" \
"v_mfma_f32_16x16x32_bf16 v[36:39], a[220:223], v[104:107], v[36:39]\n" \
"buffer_load_dwordx4 v4, s[36:39], s80 offen offset:1024 sc1 lds\n" \
"v_mfma_f32_16x16x32_bf16 v[40:43], a[216:219], v[108:111], v[40:43]\n" \
"s_waitcnt lgkmcnt(5)\n" \
"ds_read_b128 v[104:107], v28 offset:20480\n" \
"v_mul_f32 v207, v186, v207\n" \
"v_mfma_f32_16x16x32_bf16 v[44:47], a[220:223], v[108:111], v[44:47]\n" \
"ds_read_b64_tr_b16 a[82:83], v181 offset:0\n" \
"v_cvt_pk_bf16_f32 v200, v200, v201\n" \
"v_mfma_f32_16x16x32_bf16 v[48:51], a[216:219], v[112:115], v[48:51]\n" \
"ds_read_b128 v[108:111], v28 offset:21504\n" \
"v_cvt_pk_bf16_f32 v201, v202, v203\n" \
"v_accvgpr_read_b32 v224, a32\n" \
"v_accvgpr_read_b32 v225, a33\n" \
"v_accvgpr_read_b32 v226, a34\n" \
"v_accvgpr_read_b32 v227, a35\n" \
"v_accvgpr_read_b32 v228, a36\n" \
"v_accvgpr_read_b32 v229, a37\n" \
"v_accvgpr_read_b32 v230, a38\n" \
"v_accvgpr_read_b32 v231, a39\n" \
"v_mfma_f32_16x16x32_bf16 v[52:55], a[220:223], v[112:115], v[52:55]\n" \
"s_waitcnt lgkmcnt(15)\n" \
"ds_read_b64_tr_b16 a[84:85], v182 offset:0\n" \
"v_cvt_pk_bf16_f32 v202, v204, v205\n" \
"v_mfma_f32_16x16x32_bf16 v[224:227], v[24:27], v[144:147], v[224:227]\n" \
"buffer_load_dwordx4 v4, s[36:39], s80 offen offset:2048 sc1 lds\n" \
"v_mfma_f32_16x16x32_bf16 v[228:231], v[22:25], v[144:147], v[228:231]\n" \
"ds_read_b64_tr_b16 a[86:87], v183 offset:0\n" \
"v_cvt_pk_bf16_f32 v203, v206, v207\n" \
"v_mfma_f32_16x16x32_bf16 v[56:59], a[216:219], v[116:119], v[56:59]\n" \
"s_waitcnt lgkmcnt(8)\n" \
"ds_read_b128 v[112:115], v28 offset:24576\n" \
"v_mul_f32 v208, v186, v208\n" \
"v_mfma_f32_16x16x32_bf16 v[60:63], a[220:223], v[116:119], v[60:63]\n" \
"buffer_store_dwordx4 v[200:203], v4, s[40:43], s51 offen offset:2048 sc1\n" \
"v_mfma_f32_16x16x32_bf16 v[64:67], a[216:219], v[120:123], v[64:67]\n" \
"ds_read_b128 v[116:119], v28 offset:25600\n" \
"v_mul_f32 v209, v186, v209\n" \
"v_mfma_f32_16x16x32_bf16 v[68:71], a[220:223], v[120:123], v[68:71]\n" \
"buffer_load_dwordx4 v4, s[36:39], s80 offen offset:3072 sc1 lds\n" \
"v_mfma_f32_16x16x32_bf16 v[72:75], a[216:219], v[124:127], v[72:75]\n" \
"s_add_u32 s80, s65, 32768\n" \
"ds_read_b128 v[120:123], v28 offset:18432\n" \
"s_add_u32 m0, s22, 114688\n" \
"v_mfma_f32_16x16x32_bf16 v[76:79], a[220:223], v[124:127], v[76:79]\n" \
"s_waitcnt vmcnt(27) lgkmcnt(8)\n" \
"s_barrier\n" \
"ds_read_b64_tr_b16 a[88:89], v180 offset:8192\n" \
"v_mfma_f32_16x16x32_bf16 v[32:35], a[224:227], v[96:99], v[32:35]\n" \
"ds_read_b128 v[124:127], v28 offset:19456\n" \
"v_mul_f32 v210, v186, v210\n" \
"v_mfma_f32_16x16x32_bf16 v[36:39], a[228:231], v[96:99], v[36:39]\n" \
"buffer_load_dwordx4 v4, s[36:39], s80 offen offset:0 sc1 lds\n" \
"v_mfma_f32_16x16x32_bf16 v[40:43], a[224:227], v[100:103], v[40:43]\n" \
"s_waitcnt lgkmcnt(7)\n" \
"ds_read_b128 v[96:99], v28 offset:22528\n" \
"v_mul_f32 v211, v186, v211\n" \
"v_mfma_f32_16x16x32_bf16 v[44:47], a[228:231], v[100:103], v[44:47]\n" \
"ds_read_b64_tr_b16 a[90:91], v181 offset:8192\n" \
"v_mul_f32 v212, v186, v212\n" \
"v_mfma_f32_16x16x32_bf16 v[48:51], a[224:227], v[104:107], v[48:51]\n" \
"ds_read_b128 v[100:103], v28 offset:23552\n" \
"v_mul_f32 v213, v186, v213\n" \
"v_mfma_f32_16x16x32_bf16 v[52:55], a[228:231], v[104:107], v[52:55]\n" \
"buffer_load_dwordx4 v4, s[36:39], s80 offen offset:1024 sc1 lds\n" \
"v_mfma_f32_16x16x32_bf16 v[56:59], a[224:227], v[108:111], v[56:59]\n" \
"s_waitcnt lgkmcnt(6)\n" \
"ds_read_b128 v[104:107], v28 offset:26624\n" \
"v_mul_f32 v214, v186, v214\n" \
"v_mfma_f32_16x16x32_bf16 v[60:63], a[228:231], v[108:111], v[60:63]\n" \
"ds_read_b64_tr_b16 a[92:93], v182 offset:8192\n" \
"v_mul_f32 v215, v186, v215\n" \
"v_mfma_f32_16x16x32_bf16 v[64:67], a[224:227], v[112:115], v[64:67]\n" \
"ds_read_b128 v[108:111], v28 offset:27648\n" \
"v_cvt_pk_bf16_f32 v208, v208, v209\n" \
"v_accvgpr_read_b32 v232, a40\n" \
"v_accvgpr_read_b32 v233, a41\n" \
"v_accvgpr_read_b32 v234, a42\n" \
"v_accvgpr_read_b32 v235, a43\n" \
"v_accvgpr_read_b32 v236, a44\n" \
"v_accvgpr_read_b32 v237, a45\n" \
"v_accvgpr_read_b32 v238, a46\n" \
"v_accvgpr_read_b32 v239, a47\n" \
"v_mfma_f32_16x16x32_bf16 v[68:71], a[228:231], v[112:115], v[68:71]\n" \
"s_waitcnt lgkmcnt(15)\n" \
"ds_read_b64_tr_b16 a[94:95], v183 offset:8192\n" \
"v_cvt_pk_bf16_f32 v209, v210, v211\n" \
"v_mfma_f32_16x16x32_bf16 v[232:235], v[24:27], v[148:151], v[232:235]\n" \
"buffer_load_dwordx4 v4, s[36:39], s80 offen offset:2048 sc1 lds\n" \
"v_mfma_f32_16x16x32_bf16 v[236:239], v[22:25], v[148:151], v[236:239]\n" \
"v_cvt_pk_bf16_f32 v210, v212, v213\n" \
"v_cvt_pk_bf16_f32 v211, v214, v215\n" \
"v_mfma_f32_16x16x32_bf16 v[72:75], a[224:227], v[116:119], v[72:75]\n" \
"s_waitcnt lgkmcnt(7)\n" \
"ds_read_b128 v[112:115], v28 offset:28672\n" \
"v_mul_f32 v216, v186, v216\n" \
"v_mfma_f32_16x16x32_bf16 v[76:79], a[228:231], v[116:119], v[76:79]\n" \
"buffer_store_dwordx4 v[208:211], v7, s[40:43], s51 offen offset:0 sc1\n" \
"v_mfma_f32_16x16x32_bf16 v[32:35], a[232:235], v[120:123], v[32:35]\n" \
"ds_read_b128 v[116:119], v28 offset:29696\n" \
"v_mul_f32 v217, v186, v217\n" \
"v_mfma_f32_16x16x32_bf16 v[36:39], a[236:239], v[120:123], v[36:39]\n" \
"buffer_load_dwordx4 v4, s[36:39], s80 offen offset:3072 sc1 lds\n" \
"v_mfma_f32_16x16x32_bf16 v[40:43], a[232:235], v[124:127], v[40:43]\n" \
"s_mov_b32 s81, 1310720\n" \
"ds_read_b128 v[120:123], v28 offset:32768\n" \
"s_add_u32 m0, s79, 49152\n" \
"v_mfma_f32_16x16x32_bf16 v[44:47], a[236:239], v[124:127], v[44:47]\n" \
"s_waitcnt vmcnt(15) lgkmcnt(3)\n" \
"s_barrier\n" \
"v_mul_f32 v218, v186, v218\n" \
"v_mfma_f32_16x16x32_bf16 v[48:51], a[232:235], v[96:99], v[48:51]\n" \
"ds_read_b128 v[124:127], v28 offset:33792\n" \
"v_mul_f32 v219, v186, v219\n" \
"v_mfma_f32_16x16x32_bf16 v[52:55], a[236:239], v[96:99], v[52:55]\n" \
"buffer_load_dwordx4 v12, s[28:31], s81 offen offset:0 lds\n" \
"v_mfma_f32_16x16x32_bf16 v[56:59], a[232:235], v[100:103], v[56:59]\n" \
"s_waitcnt lgkmcnt(5)\n" \
"ds_read_b128 v[96:99], v28 offset:36864\n" \
"s_add_u32 m0, s79, 50048\n" \
"v_mfma_f32_16x16x32_bf16 v[60:63], a[236:239], v[100:103], v[60:63]\n" \
"ds_read_b64_tr_b16 a[128:129], v13 offset:0\n" \
"v_mul_f32 v220, v186, v220\n" \
"v_mfma_f32_16x16x32_bf16 v[64:67], a[232:235], v[104:107], v[64:67]\n" \
"ds_read_b128 v[100:103], v28 offset:37888\n" \
"v_mul_f32 v221, v186, v221\n" \
"v_mfma_f32_16x16x32_bf16 v[68:71], a[236:239], v[104:107], v[68:71]\n" \
"buffer_load_dwordx4 v12, s[28:31], s81 offen offset:128 lds\n" \
"v_mfma_f32_16x16x32_bf16 v[72:75], a[232:235], v[108:111], v[72:75]\n" \
"s_waitcnt lgkmcnt(5)\n" \
"ds_read_b128 v[104:107], v28 offset:30720\n" \
"s_mov_b32 s81, 1376256\n" \
"v_mfma_f32_16x16x32_bf16 v[76:79], a[236:239], v[108:111], v[76:79]\n" \
"s_add_u32 m0, s79, 57344\n" \
"ds_read_b64_tr_b16 a[130:131], v14 offset:0\n" \
"v_mul_f32 v222, v186, v222\n" \
"v_mfma_f32_16x16x32_bf16 v[32:35], a[240:243], v[112:115], v[32:35]\n" \
"ds_read_b128 v[108:111], v28 offset:31744\n" \
"v_mul_f32 v223, v186, v223\n" \
"v_accvgpr_read_b32 v240, a48\n" \
"v_accvgpr_read_b32 v241, a49\n" \
"v_accvgpr_read_b32 v242, a50\n" \
"v_accvgpr_read_b32 v243, a51\n" \
"v_accvgpr_read_b32 v244, a52\n" \
"v_accvgpr_read_b32 v245, a53\n" \
"v_accvgpr_read_b32 v246, a54\n" \
"v_accvgpr_read_b32 v247, a55\n" \
"v_mfma_f32_16x16x32_bf16 v[36:39], a[244:247], v[112:115], v[36:39]\n" \
"s_waitcnt lgkmcnt(15)\n" \
"ds_read_b64_tr_b16 a[132:133], v15 offset:0\n" \
"v_cvt_pk_bf16_f32 v216, v216, v217\n" \
"v_mfma_f32_16x16x32_bf16 v[240:243], v[24:27], v[152:155], v[240:243]\n" \
"buffer_load_dwordx4 v12, s[28:31], s81 offen offset:0 lds\n" \
"v_mfma_f32_16x16x32_bf16 v[244:247], v[22:25], v[152:155], v[244:247]\n" \
"s_add_u32 m0, s79, 58240\n" \
"ds_read_b64_tr_b16 a[134:135], v16 offset:0\n" \
"v_cvt_pk_bf16_f32 v217, v218, v219\n" \
"v_mfma_f32_16x16x32_bf16 v[40:43], a[240:243], v[116:119], v[40:43]\n" \
"s_waitcnt lgkmcnt(8)\n" \
"ds_read_b128 v[112:115], v28 offset:34816\n" \
"v_cvt_pk_bf16_f32 v218, v220, v221\n" \
"v_mfma_f32_16x16x32_bf16 v[44:47], a[244:247], v[116:119], v[44:47]\n" \
"ds_read_b64_tr_b16 a[136:137], v13 offset:8192\n" \
"v_cvt_pk_bf16_f32 v219, v222, v223\n" \
"v_mfma_f32_16x16x32_bf16 v[48:51], a[240:243], v[120:123], v[48:51]\n" \
"ds_read_b128 v[116:119], v28 offset:35840\n" \
"v_mul_f32 v224, v187, v224\n" \
"v_mfma_f32_16x16x32_bf16 v[52:55], a[244:247], v[120:123], v[52:55]\n" \
"buffer_store_dwordx4 v[216:219], v7, s[40:43], s51 offen offset:2048 sc1\n" \
"v_mfma_f32_16x16x32_bf16 v[56:59], a[240:243], v[124:127], v[56:59]\n" \
"ds_read_b128 v[120:123], v28 offset:38912\n" \
"v_mul_f32 v225, v187, v225\n" \
"v_mfma_f32_16x16x32_bf16 v[60:63], a[244:247], v[124:127], v[60:63]\n" \
"s_waitcnt vmcnt(14) lgkmcnt(9)\n" \
"s_barrier\n" \
"ds_read_b64_tr_b16 a[138:139], v14 offset:8192\n" \
"v_mfma_f32_16x16x32_bf16 v[64:67], a[240:243], v[96:99], v[64:67]\n" \
"ds_read_b128 v[124:127], v28 offset:39936\n" \
"v_mul_f32 v226, v187, v226\n" \
"v_mfma_f32_16x16x32_bf16 v[68:71], a[244:247], v[96:99], v[68:71]\n" \
"buffer_load_dwordx4 v12, s[28:31], s81 offen offset:128 lds\n" \
"v_mfma_f32_16x16x32_bf16 v[72:75], a[240:243], v[100:103], v[72:75]\n" \
"s_waitcnt lgkmcnt(8)\n" \
"ds_read_b128 v[96:99], v28 offset:40960\n" \
"s_mov_b32 s81, 1441792\n" \
"v_mfma_f32_16x16x32_bf16 v[76:79], a[244:247], v[100:103], v[76:79]\n" \
"s_add_u32 m0, s79, 0\n" \
"ds_read_b64_tr_b16 a[140:141], v15 offset:8192\n" \
"v_mul_f32 v227, v187, v227\n" \
"v_mfma_f32_16x16x32_bf16 v[32:35], a[248:251], v[104:107], v[32:35]\n" \
"ds_read_b128 v[100:103], v28 offset:41984\n" \
"v_mul_f32 v228, v187, v228\n" \
"v_mfma_f32_16x16x32_bf16 v[36:39], a[252:255], v[104:107], v[36:39]\n" \
"buffer_load_dwordx4 v12, s[28:31], s81 offen offset:0 lds\n" \
"v_mfma_f32_16x16x32_bf16 v[40:43], a[248:251], v[108:111], v[40:43]\n" \
"s_waitcnt lgkmcnt(6)\n" \
"ds_read_b128 v[104:107], v28 offset:45056\n" \
"s_add_u32 m0, s79, 896\n" \
"v_mfma_f32_16x16x32_bf16 v[44:47], a[252:255], v[108:111], v[44:47]\n" \
"ds_read_b64_tr_b16 a[142:143], v16 offset:8192\n" \
"v_mul_f32 v229, v187, v229\n" \
"v_mfma_f32_16x16x32_bf16 v[48:51], a[248:251], v[112:115], v[48:51]\n" \
"ds_read_b128 v[108:111], v28 offset:46080\n" \
"v_mul_f32 v230, v187, v230\n" \
"v_accvgpr_read_b32 v248, a56\n" \
"v_accvgpr_read_b32 v249, a57\n" \
"v_accvgpr_read_b32 v250, a58\n" \
"v_accvgpr_read_b32 v251, a59\n" \
"v_accvgpr_read_b32 v252, a60\n" \
"v_accvgpr_read_b32 v253, a61\n" \
"v_accvgpr_read_b32 v254, a62\n" \
"v_accvgpr_read_b32 v255, a63\n" \
"v_mfma_f32_16x16x32_bf16 v[52:55], a[252:255], v[112:115], v[52:55]\n" \
"s_waitcnt lgkmcnt(15)\n" \
"ds_read_b64_tr_b16 a[144:145], v13 offset:16384\n" \
"v_mul_f32 v231, v187, v231\n" \
"v_mfma_f32_16x16x32_bf16 v[248:251], v[24:27], v[156:159], v[248:251]\n" \
"buffer_load_dwordx4 v12, s[28:31], s81 offen offset:128 lds\n" \
"v_mfma_f32_16x16x32_bf16 v[252:255], v[22:25], v[156:159], v[252:255]\n" \
"s_mov_b32 s81, 1507328\n" \
"s_add_u32 m0, s79, 8192\n" \
"ds_read_b64_tr_b16 a[146:147], v14 offset:16384\n" \
"v_mfma_f32_16x16x32_bf16 v[56:59], a[248:251], v[116:119], v[56:59]\n" \
"s_waitcnt lgkmcnt(8)\n" \
"ds_read_b128 v[112:115], v29 offset:16384\n" \
"v_cvt_pk_bf16_f32 v224, v224, v225\n" \
"v_mfma_f32_16x16x32_bf16 v[60:63], a[252:255], v[116:119], v[60:63]\n" \
"ds_read_b64_tr_b16 a[148:149], v15 offset:16384\n" \
"v_cvt_pk_bf16_f32 v225, v226, v227\n" \
"v_mfma_f32_16x16x32_bf16 v[64:67], a[248:251], v[120:123], v[64:67]\n" \
"ds_read_b128 v[116:119], v29 offset:17408\n" \
"v_cvt_pk_bf16_f32 v226, v228, v229\n" \
"v_mfma_f32_16x16x32_bf16 v[68:71], a[252:255], v[120:123], v[68:71]\n" \
"buffer_load_dwordx4 v12, s[28:31], s81 offen offset:0 lds\n" \
"v_mfma_f32_16x16x32_bf16 v[72:75], a[248:251], v[124:127], v[72:75]\n" \
"s_add_u32 m0, s79, 9088\n" \
"ds_read_b128 v[120:123], v28 offset:43008\n" \
"v_cvt_pk_bf16_f32 v227, v230, v231\n" \
"v_mfma_f32_16x16x32_bf16 v[76:79], a[252:255], v[124:127], v[76:79]\n" \
"s_waitcnt lgkmcnt(7)\n" \
"s_barrier\n" \
"ds_read_b64_tr_b16 a[150:151], v16 offset:16384\n" \
"v_mfma_f32_16x16x32_bf16 v[32:35], a[64:67], v[96:99], v[32:35]\n" \
"ds_read_b128 v[124:127], v28 offset:44032\n" \
"v_mul_f32 v232, v187, v232\n" \
"v_mfma_f32_16x16x32_bf16 v[36:39], a[68:71], v[96:99], v[36:39]\n" \
"buffer_store_dwordx4 v[224:227], v4, s[40:43], s51 offen offset:1024 sc1\n" \
"v_mfma_f32_16x16x32_bf16 v[40:43], a[64:67], v[100:103], v[40:43]\n" \
"s_waitcnt lgkmcnt(8)\n" \
"ds_read_b128 v[96:99], v28 offset:47104\n" \
"v_mul_f32 v233, v187, v233\n" \
"v_mfma_f32_16x16x32_bf16 v[44:47], a[68:71], v[100:103], v[44:47]\n" \
"buffer_load_dwordx4 v12, s[28:31], s81 offen offset:128 lds\n" \
"v_mfma_f32_16x16x32_bf16 v[48:51], a[64:67], v[104:107], v[48:51]\n" \
"s_add_u32 s80, s66, 32768\n" \
"ds_read_b128 v[100:103], v28 offset:48128\n" \
"s_add_u32 m0, s22, 65536\n" \
"v_mfma_f32_16x16x32_bf16 v[52:55], a[68:71], v[104:107], v[52:55]\n" \
"ds_read_b64_tr_b16 a[152:153], v13 offset:24576\n" \
"v_mul_f32 v234, v187, v234\n" \
"v_mfma_f32_16x16x32_bf16 v[56:59], a[64:67], v[108:111], v[56:59]\n" \
"s_waitcnt lgkmcnt(6)\n" \
"ds_read_b128 v[104:107], v29 offset:18432\n" \
"v_mul_f32 v235, v187, v235\n" \
"v_mfma_f32_16x16x32_bf16 v[60:63], a[68:71], v[108:111], v[60:63]\n" \
"buffer_load_dwordx4 v4, s[36:39], s80 offen offset:0 sc1 lds\n" \
"v_mfma_f32_16x16x32_bf16 v[64:67], a[64:67], v[112:115], v[64:67]\n" \
"ds_read_b128 v[108:111], v29 offset:19456\n" \
"v_mul_f32 v236, v187, v236\n" \
"v_mfma_f32_16x16x32_bf16 v[68:71], a[68:71], v[112:115], v[68:71]\n" \
"ds_read_b64_tr_b16 a[154:155], v14 offset:24576\n" \
"v_mul_f32 v237, v187, v237\n" \
"v_mfma_f32_16x16x32_bf16 v[72:75], a[64:67], v[116:119], v[72:75]\n" \
"s_waitcnt lgkmcnt(6)\n" \
"ds_read_b128 v[112:115], v29 offset:20480\n" \
"ds_read_b64_tr_b16 a[156:157], v15 offset:24576\n" \
"ds_read_b64_tr_b16 a[158:159], v16 offset:24576\n" \
"v_mfma_f32_16x16x32_bf16 v[76:79], a[68:71], v[116:119], v[76:79]\n" \
"buffer_load_dwordx4 v4, s[36:39], s80 offen offset:1024 sc1 lds\n" \
"v_mfma_f32_16x16x32_bf16 v[32:35], a[72:75], v[120:123], v[32:35]\n" \
"ds_read_b128 v[116:119], v29 offset:21504\n" \
"v_mul_f32 v238, v187, v238\n" \
"v_mfma_f32_16x16x32_bf16 v[36:39], a[76:79], v[120:123], v[36:39]\n" \
"v_mul_f32 v239, v187, v239\n" \
"v_cvt_pk_bf16_f32 v232, v232, v233\n" \
"v_mfma_f32_16x16x32_bf16 v[40:43], a[72:75], v[124:127], v[40:43]\n" \
"ds_read_b128 v[120:123], v29 offset:24576\n" \
"v_cvt_pk_bf16_f32 v233, v234, v235\n" \
"v_mfma_f32_16x16x32_bf16 v[44:47], a[76:79], v[124:127], v[44:47]\n" \
"s_waitcnt lgkmcnt(2)\n" \
"s_barrier\n" \
"v_cvt_pk_bf16_f32 v234, v236, v237\n" \
"v_mfma_f32_16x16x32_bf16 v[48:51], a[72:75], v[96:99], v[48:51]\n" \
"ds_read_b128 v[124:127], v29 offset:25600\n" \
"v_cvt_pk_bf16_f32 v235, v238, v239\n" \
"v_mfma_f32_16x16x32_bf16 v[52:55], a[76:79], v[96:99], v[52:55]\n" \
"buffer_store_dwordx4 v[232:235], v4, s[40:43], s51 offen offset:3072 sc1\n" \
"v_mfma_f32_16x16x32_bf16 v[56:59], a[72:75], v[100:103], v[56:59]\n" \
"s_waitcnt lgkmcnt(7)\n" \
"ds_read_b128 v[96:99], v29 offset:28672\n" \
"v_mul_f32 v240, v187, v240\n" \
"v_mfma_f32_16x16x32_bf16 v[60:63], a[76:79], v[100:103], v[60:63]\n" \
"buffer_load_dwordx4 v4, s[36:39], s80 offen offset:2048 sc1 lds\n" \
"v_mfma_f32_16x16x32_bf16 v[64:67], a[72:75], v[104:107], v[64:67]\n" \
"ds_read_b128 v[100:103], v29 offset:29696\n" \
"v_mul_f32 v241, v187, v241\n" \
"v_mfma_f32_16x16x32_bf16 v[68:71], a[76:79], v[104:107], v[68:71]\n" \
"v_mul_f32 v242, v187, v242\n" \
"v_mul_f32 v243, v187, v243\n" \
"v_mfma_f32_16x16x32_bf16 v[72:75], a[72:75], v[108:111], v[72:75]\n" \
"s_waitcnt lgkmcnt(4)\n" \
"ds_read_b128 v[104:107], v29 offset:22528\n" \
"v_mul_f32 v244, v187, v244\n" \
"v_mfma_f32_16x16x32_bf16 v[76:79], a[76:79], v[108:111], v[76:79]\n" \
"buffer_load_dwordx4 v4, s[36:39], s80 offen offset:3072 sc1 lds\n" \
"v_mfma_f32_16x16x32_bf16 v[32:35], a[80:83], v[112:115], v[32:35]\n" \
"s_mov_b32 s81, 1572864\n" \
"ds_read_b128 v[108:111], v29 offset:23552\n" \
"s_add_u32 m0, s79, 16384\n" \
"v_mfma_f32_16x16x32_bf16 v[36:39], a[84:87], v[112:115], v[36:39]\n" \
"v_mul_f32 v245, v187, v245\n" \
"v_mul_f32 v246, v187, v246\n" \
"v_mfma_f32_16x16x32_bf16 v[40:43], a[80:83], v[116:119], v[40:43]\n" \
"s_waitcnt lgkmcnt(4)\n" \
"ds_read_b128 v[112:115], v29 offset:26624\n" \
"v_mul_f32 v247, v187, v247\n" \
"v_mfma_f32_16x16x32_bf16 v[44:47], a[84:87], v[116:119], v[44:47]\n" \
"buffer_load_dwordx4 v12, s[28:31], s81 offen offset:0 lds\n" \
"v_mfma_f32_16x16x32_bf16 v[48:51], a[80:83], v[120:123], v[48:51]\n" \
"s_add_u32 m0, s79, 17280\n" \
"ds_read_b128 v[116:119], v29 offset:27648\n" \
"v_cvt_pk_bf16_f32 v240, v240, v241\n" \
"v_mfma_f32_16x16x32_bf16 v[52:55], a[84:87], v[120:123], v[52:55]\n" \
"v_cvt_pk_bf16_f32 v241, v242, v243\n" \
"v_cvt_pk_bf16_f32 v242, v244, v245\n" \
"v_mfma_f32_16x16x32_bf16 v[56:59], a[80:83], v[124:127], v[56:59]\n" \
"ds_read_b128 v[120:123], v29 offset:30720\n" \
"v_cvt_pk_bf16_f32 v243, v246, v247\n" \
"v_mfma_f32_16x16x32_bf16 v[60:63], a[84:87], v[124:127], v[60:63]\n" \
"s_waitcnt vmcnt(11) lgkmcnt(5)\n" \
"s_barrier\n" \
"v_mul_f32 v248, v187, v248\n" \
"v_mfma_f32_16x16x32_bf16 v[64:67], a[80:83], v[96:99], v[64:67]\n" \
"ds_read_b128 v[124:127], v29 offset:31744\n" \
"v_mul_f32 v249, v187, v249\n" \
"v_mfma_f32_16x16x32_bf16 v[68:71], a[84:87], v[96:99], v[68:71]\n" \
"buffer_store_dwordx4 v[240:243], v7, s[40:43], s51 offen offset:1024 sc1\n" \
"v_mfma_f32_16x16x32_bf16 v[72:75], a[80:83], v[100:103], v[72:75]\n" \
"s_waitcnt lgkmcnt(4)\n" \
"ds_read_b128 v[96:99], v29 offset:32768\n" \
"v_mul_f32 v250, v187, v250\n" \
"v_mfma_f32_16x16x32_bf16 v[76:79], a[84:87], v[100:103], v[76:79]\n" \
"buffer_load_dwordx4 v12, s[28:31], s81 offen offset:128 lds\n" \
"v_mfma_f32_16x16x32_bf16 v[32:35], a[88:91], v[104:107], v[32:35]\n" \
"s_mov_b32 s81, 1638400\n" \
"ds_read_b128 v[100:103], v29 offset:33792\n" \
"s_add_u32 m0, s79, 24576\n" \
"v_mfma_f32_16x16x32_bf16 v[36:39], a[92:95], v[104:107], v[36:39]\n" \
"ds_read_b64_tr_b16 a[0:1], v176 offset:49152\n" \
"v_mul_f32 v251, v187, v251\n" \
"v_mfma_f32_16x16x32_bf16 v[40:43], a[88:91], v[108:111], v[40:43]\n" \
"s_waitcnt lgkmcnt(5)\n" \
"ds_read_b128 v[104:107], v29 offset:36864\n" \
"v_mul_f32 v252, v187, v252\n" \
"v_mfma_f32_16x16x32_bf16 v[44:47], a[92:95], v[108:111], v[44:47]\n" \
"buffer_load_dwordx4 v12, s[28:31], s81 offen offset:0 lds\n" \
"v_mfma_f32_16x16x32_bf16 v[48:51], a[88:91], v[112:115], v[48:51]\n" \
"s_add_u32 m0, s79, 25472\n" \
"ds_read_b128 v[108:111], v29 offset:37888\n" \
"v_mul_f32 v253, v187, v253\n" \
"v_mfma_f32_16x16x32_bf16 v[52:55], a[92:95], v[112:115], v[52:55]\n" \
"ds_read_b64_tr_b16 a[2:3], v177 offset:49152\n" \
"v_mul_f32 v254, v187, v254\n" \
"v_mfma_f32_16x16x32_bf16 v[56:59], a[88:91], v[116:119], v[56:59]\n" \
"s_waitcnt lgkmcnt(6)\n" \
"ds_read_b128 v[112:115], v29 offset:40960\n" \
"v_mul_f32 v255, v187, v255\n" \
"v_mfma_f32_16x16x32_bf16 v[60:63], a[92:95], v[116:119], v[60:63]\n" \
"buffer_load_dwordx4 v12, s[28:31], s81 offen offset:128 lds\n" \
"v_mfma_f32_16x16x32_bf16 v[64:67], a[88:91], v[120:123], v[64:67]\n" \
"s_mov_b32 s81, 1703936\n" \
"ds_read_b128 v[116:119], v29 offset:41984\n" \
"s_add_u32 m0, s79, 131072\n" \
"v_mfma_f32_16x16x32_bf16 v[68:71], a[92:95], v[120:123], v[68:71]\n" \
"ds_read_b64_tr_b16 a[4:5], v178 offset:49152\n" \
"v_cvt_pk_bf16_f32 v248, v248, v249\n" \
"v_mfma_f32_16x16x32_bf16 v[72:75], a[88:91], v[124:127], v[72:75]\n" \
"ds_read_b128 v[120:123], v29 offset:34816\n" \
"v_cvt_pk_bf16_f32 v249, v250, v251\n" \
"v_mfma_f32_16x16x32_bf16 v[76:79], a[92:95], v[124:127], v[76:79]\n" \
"s_waitcnt vmcnt(20) lgkmcnt(8)\n" \
"s_barrier\n" \
"ds_read_b64_tr_b16 a[6:7], v179 offset:49152\n" \
"v_mfma_f32_16x16x32_bf16 v[32:35], a[128:131], v[96:99], v[32:35]\n" \
"ds_read_b128 v[124:127], v29 offset:35840\n" \
"v_cvt_pk_bf16_f32 v250, v252, v253\n" \
"v_mfma_f32_16x16x32_bf16 v[36:39], a[132:135], v[96:99], v[36:39]\n" \
"buffer_load_dwordx4 v12, s[28:31], s81 offen offset:0 lds\n" \
"v_mfma_f32_16x16x32_bf16 v[40:43], a[128:131], v[100:103], v[40:43]\n" \
"s_waitcnt lgkmcnt(7)\n" \
"ds_read_b128 v[96:99], v29 offset:38912\n" \
"s_add_u32 m0, s79, 131968\n" \
"v_mfma_f32_16x16x32_bf16 v[44:47], a[132:135], v[100:103], v[44:47]\n" \
"ds_read_b64_tr_b16 a[8:9], v176 offset:57344\n" \
"v_cvt_pk_bf16_f32 v251, v254, v255\n" \
"v_mfma_f32_16x16x32_bf16 v[48:51], a[128:131], v[104:107], v[48:51]\n" \
"ds_read_b128 v[100:103], v29 offset:39936\n" \
"v_bfe_u32 v188, v4, 7, 1\n" \
"v_mfma_f32_16x16x32_bf16 v[52:55], a[132:135], v[104:107], v[52:55]\n" \
"buffer_store_dwordx4 v[248:251], v7, s[40:43], s51 offen offset:3072 sc1\n" \
"v_mfma_f32_16x16x32_bf16 v[56:59], a[128:131], v[108:111], v[56:59]\n" \
"s_waitcnt lgkmcnt(7)\n" \
"ds_read_b128 v[104:107], v29 offset:43008\n" \
"v_bfe_u32 v189, v4, 4, 3\n" \
"v_mfma_f32_16x16x32_bf16 v[60:63], a[132:135], v[108:111], v[60:63]\n" \
"buffer_load_dwordx4 v12, s[28:31], s81 offen offset:128 lds\n" \
"v_mfma_f32_16x16x32_bf16 v[64:67], a[128:131], v[112:115], v[64:67]\n" \
"s_mov_b32 s81, 1769472\n" \
"ds_read_b128 v[108:111], v29 offset:44032\n" \
"s_add_u32 m0, s79, 139264\n" \
"v_mfma_f32_16x16x32_bf16 v[68:71], a[132:135], v[112:115], v[68:71]\n" \
"ds_read_b64_tr_b16 a[10:11], v177 offset:57344\n" \
"v_lshl_add_u32 v188, v189, 1, v188\n" \
"v_mfma_f32_16x16x32_bf16 v[72:75], a[128:131], v[116:119], v[72:75]\n" \
"LH22_%=:\ns_cbranch_execz LHR2_%=\n" \
"s_waitcnt lgkmcnt(6)\n" \
"ds_read_b128 v[112:115], v29 offset:45056\n" \
"ds_read_b64_tr_b16 a[12:13], v178 offset:57344\n" \
"ds_read_b64_tr_b16 a[14:15], v179 offset:57344\n" \
"v_mfma_f32_16x16x32_bf16 v[76:79], a[132:135], v[116:119], v[76:79]\n" \
"buffer_load_dwordx4 v12, s[28:31], s81 offen offset:0 lds\n" \
"v_mfma_f32_16x16x32_bf16 v[32:35], a[136:139], v[120:123], v[32:35]\n" \
"s_add_u32 m0, s79, 140160\n" \
"ds_read_b128 v[116:119], v29 offset:46080\n" \
"v_lshrrev_b32 v189, 8, v4\n" \
"v_mfma_f32_16x16x32_bf16 v[36:39], a[140:143], v[120:123], v[36:39]\n" \
"v_lshlrev_b32 v184, 11, v188\n" \
"v_and_b32 v188, 1, v189\n" \
"v_mfma_f32_16x16x32_bf16 v[40:43], a[136:139], v[124:127], v[40:43]\n" \
"ds_read_b128 v[120:123], v29 offset:49152\n" \
"v_lshl_add_u32 v184, v188, 5, v184\n" \
"v_mfma_f32_16x16x32_bf16 v[44:47], a[140:143], v[124:127], v[44:47]\n" \
"s_waitcnt vmcnt(14) lgkmcnt(2)\n" \
"s_barrier\n" \
"v_lshrrev_b32 v188, 1, v189\n" \
"v_mfma_f32_16x16x32_bf16 v[48:51], a[136:139], v[96:99], v[48:51]\n" \
"ds_read_b128 v[124:127], v29 offset:50176\n" \
"v_lshl_add_u32 v184, v188, 4, v184\n" \
"v_mfma_f32_16x16x32_bf16 v[52:55], a[140:143], v[96:99], v[52:55]\n" \
"buffer_load_dwordx4 v12, s[28:31], s81 offen offset:128 lds\n" \
"v_mfma_f32_16x16x32_bf16 v[56:59], a[136:139], v[100:103], v[56:59]\n" \
"s_waitcnt lgkmcnt(7)\n" \
"ds_read_b128 v[96:99], v29 offset:53248\n" \
"s_add_u32 s80, s64, 49152\n" \
"v_mfma_f32_16x16x32_bf16 v[60:63], a[140:143], v[100:103], v[60:63]\n" \
"s_add_u32 m0, s22, 32768\n" \
"ds_read_b64_tr_b16 a[16:17], v176 offset:0\n" \
"v_bfe_u32 v188, v4, 7, 1\n" \
"v_mfma_f32_16x16x32_bf16 v[64:67], a[136:139], v[104:107], v[64:67]\n" \
"ds_read_b128 v[100:103], v29 offset:54272\n" \
"v_bfe_u32 v189, v4, 4, 3\n" \
"v_mfma_f32_16x16x32_bf16 v[68:71], a[140:143], v[104:107], v[68:71]\n" \
"buffer_load_dwordx4 v4, s[36:39], s80 offen offset:0 sc1 lds\n" \
"v_mfma_f32_16x16x32_bf16 v[72:75], a[136:139], v[108:111], v[72:75]\n" \
"s_waitcnt lgkmcnt(5)\n" \
"ds_read_b128 v[104:107], v29 offset:47104\n" \
"v_lshl_add_u32 v188, v189, 1, v188\n" \
"v_mfma_f32_16x16x32_bf16 v[76:79], a[140:143], v[108:111], v[76:79]\n" \
"ds_read_b64_tr_b16 a[18:19], v177 offset:0\n" \
"v_lshlrev_b32 v185, 3, v188\n" \
"v_mfma_f32_16x16x32_bf16 v[32:35], a[144:147], v[112:115], v[32:35]\n" \
"ds_read_b128 v[108:111], v29 offset:48128\n" \
"v_accvgpr_read_b32 v144, a96\n" \
"v_mfma_f32_16x16x32_bf16 v[36:39], a[148:151], v[112:115], v[36:39]\n" \
"buffer_load_dwordx4 v4, s[36:39], s80 offen offset:1024 sc1 lds\n" \
"v_mfma_f32_16x16x32_bf16 v[40:43], a[144:147], v[116:119], v[40:43]\n" \
"s_waitcnt lgkmcnt(6)\n" \
"ds_read_b128 v[112:115], v29 offset:51200\n" \
"v_accvgpr_read_b32 v145, a97\n" \
"v_mfma_f32_16x16x32_bf16 v[44:47], a[148:151], v[116:119], v[44:47]\n" \
"ds_read_b64_tr_b16 a[20:21], v178 offset:0\n" \
"v_accvgpr_read_b32 v146, a98\n" \
"v_mfma_f32_16x16x32_bf16 v[48:51], a[144:147], v[120:123], v[48:51]\n" \
"ds_read_b128 v[116:119], v29 offset:52224\n" \
"v_accvgpr_read_b32 v147, a99\n" \
"v_mfma_f32_16x16x32_bf16 v[52:55], a[148:151], v[120:123], v[52:55]\n" \
"buffer_load_dwordx4 v4, s[36:39], s80 offen offset:2048 sc1 lds\n" \
"v_mfma_f32_16x16x32_bf16 v[56:59], a[144:147], v[124:127], v[56:59]\n" \
"ds_read_b128 v[120:123], v29 offset:55296\n" \
"v_accvgpr_read_b32 v148, a100\n" \
"v_mfma_f32_16x16x32_bf16 v[60:63], a[148:151], v[124:127], v[60:63]\n" \
"s_waitcnt vmcnt(13) lgkmcnt(7)\n" \
"s_barrier\n" \
"ds_read_b64_tr_b16 a[22:23], v179 offset:0\n" \
"v_mfma_f32_16x16x32_bf16 v[64:67], a[144:147], v[96:99], v[64:67]\n" \
"ds_read_b128 v[124:127], v29 offset:56320\n" \
"v_accvgpr_read_b32 v149, a101\n" \
"v_mfma_f32_16x16x32_bf16 v[68:71], a[148:151], v[96:99], v[68:71]\n" \
"buffer_load_dwordx4 v4, s[36:39], s80 offen offset:3072 sc1 lds\n" \
"v_mfma_f32_16x16x32_bf16 v[72:75], a[144:147], v[100:103], v[72:75]\n" \
"s_waitcnt lgkmcnt(6)\n" \
"ds_read_b128 v[96:99], v29 offset:57344\n" \
"s_add_u32 s80, s65, 49152\n" \
"v_mfma_f32_16x16x32_bf16 v[76:79], a[148:151], v[100:103], v[76:79]\n" \
"s_add_u32 m0, s22, 147456\n" \
"ds_read_b64_tr_b16 a[24:25], v176 offset:8192\n" \
"v_accvgpr_read_b32 v150, a102\n" \
"v_mfma_f32_16x16x32_bf16 v[32:35], a[152:155], v[104:107], v[32:35]\n" \
"ds_read_b128 v[100:103], v29 offset:58368\n" \
"v_accvgpr_read_b32 v151, a103\n" \
"v_mfma_f32_16x16x32_bf16 v[36:39], a[156:159], v[104:107], v[36:39]\n" \
"buffer_load_dwordx2 v[18:19], v17, s[36:39], 0 offen sc1\n" \
"v_mfma_f32_16x16x32_bf16 v[40:43], a[152:155], v[108:111], v[40:43]\n" \
"s_waitcnt lgkmcnt(6)\n" \
"ds_read_b128 v[104:107], v29 offset:61440\n" \
"v_accvgpr_read_b32 v152, a104\n" \
"v_mfma_f32_16x16x32_bf16 v[44:47], a[156:159], v[108:111], v[44:47]\n" \
"ds_read_b64_tr_b16 a[26:27], v177 offset:8192\n" \
"v_accvgpr_read_b32 v153, a105\n" \
"v_mfma_f32_16x16x32_bf16 v[48:51], a[152:155], v[112:115], v[48:51]\n" \
"ds_read_b128 v[108:111], v29 offset:62464\n" \
"v_accvgpr_read_b32 v154, a106\n" \
"v_mfma_f32_16x16x32_bf16 v[52:55], a[156:159], v[112:115], v[52:55]\n" \
"buffer_load_dwordx4 v4, s[36:39], s80 offen offset:0 sc1 lds\n" \
"v_mfma_f32_16x16x32_bf16 v[56:59], a[152:155], v[116:119], v[56:59]\n" \
"s_waitcnt lgkmcnt(6)\n" \
"ds_read_b128 v[112:115], v29 offset:0\n" \
"ds_read_b64_tr_b16 a[28:29], v178 offset:8192\n" \
"ds_read_b64_tr_b16 a[30:31], v179 offset:8192\n" \
"v_mfma_f32_16x16x32_bf16 v[60:63], a[156:159], v[116:119], v[60:63]\n" \
"v_accvgpr_read_b32 v155, a107\n" \
"v_accvgpr_read_b32 v156, a108\n" \
"v_mfma_f32_16x16x32_bf16 v[64:67], a[152:155], v[120:123], v[64:67]\n" \
"ds_read_b128 v[116:119], v29 offset:1024\n" \
"v_accvgpr_read_b32 v157, a109\n" \
"v_mfma_f32_16x16x32_bf16 v[68:71], a[156:159], v[120:123], v[68:71]\n" \
"buffer_load_dwordx4 v4, s[36:39], s80 offen offset:1024 sc1 lds\n" \
"v_mfma_f32_16x16x32_bf16 v[72:75], a[152:155], v[124:127], v[72:75]\n" \
"ds_read_b128 v[120:123], v29 offset:59392\n" \
"v_accvgpr_read_b32 v158, a110\n" \
"v_mfma_f32_16x16x32_bf16 v[76:79], a[156:159], v[124:127], v[76:79]\n" \
"s_waitcnt vmcnt(12) lgkmcnt(2)\n" \
"s_barrier\n" \
"v_accvgpr_read_b32 v159, a111\n" \
"v_mfma_f32_16x16x32_bf16 v[32:35], a[0:3], v[96:99], v[32:35]\n" \
"ds_read_b128 v[124:127], v29 offset:60416\n" \
"v_accvgpr_read_b32 v128, a112\n" \
"v_mfma_f32_16x16x32_bf16 v[36:39], a[4:7], v[96:99], v[36:39]\n" \
"buffer_load_dwordx4 v4, s[36:39], s80 offen offset:2048 sc1 lds\n" \
"v_mfma_f32_16x16x32_bf16 v[40:43], a[0:3], v[100:103], v[40:43]\n" \
"s_waitcnt lgkmcnt(6)\n" \
"ds_read_b128 v[96:99], v29 offset:63488\n" \
"v_accvgpr_read_b32 v129, a113\n" \
"v_mfma_f32_16x16x32_bf16 v[44:47], a[4:7], v[100:103], v[44:47]\n" \
"ds_read_b64_tr_b16 a[32:33], v176 offset:16384\n" \
"v_accvgpr_read_b32 v130, a114\n" \
"v_mfma_f32_16x16x32_bf16 v[48:51], a[0:3], v[104:107], v[48:51]\n" \
"ds_read_b128 v[100:103], v29 offset:64512\n" \
"v_accvgpr_read_b32 v131, a115\n" \
"v_mfma_f32_16x16x32_bf16 v[52:55], a[4:7], v[104:107], v[52:55]\n" \
"buffer_load_dwordx4 v4, s[36:39], s80 offen offset:3072 sc1 lds\n" \
"v_mfma_f32_16x16x32_bf16 v[56:59], a[0:3], v[108:111], v[56:59]\n" \
"s_waitcnt lgkmcnt(5)\n" \
"ds_read_b128 v[104:107], v29 offset:2048\n" \
"s_mov_b32 s81, 1835008\n" \
"v_mfma_f32_16x16x32_bf16 v[60:63], a[4:7], v[108:111], v[60:63]\n" \
"s_add_u32 m0, s79, 81920\n" \
"ds_read_b64_tr_b16 a[34:35], v177 offset:16384\n" \
"v_accvgpr_read_b32 v132, a116\n" \
"v_mfma_f32_16x16x32_bf16 v[64:67], a[0:3], v[112:115], v[64:67]\n" \
"ds_read_b128 v[108:111], v29 offset:3072\n" \
"v_accvgpr_read_b32 v133, a117\n" \
"v_mfma_f32_16x16x32_bf16 v[68:71], a[4:7], v[112:115], v[68:71]\n" \
"buffer_load_dwordx4 v12, s[28:31], s81 offen offset:0 lds\n" \
"v_mfma_f32_16x16x32_bf16 v[72:75], a[0:3], v[116:119], v[72:75]\n" \
"s_waitcnt lgkmcnt(6)\n" \
"ds_read_b128 v[112:115], v29 offset:4096\n" \
"s_add_u32 m0, s79, 82816\n" \
"v_mfma_f32_16x16x32_bf16 v[76:79], a[4:7], v[116:119], v[76:79]\n" \
"ds_read_b64_tr_b16 a[36:37], v178 offset:16384\n" \
"v_accvgpr_read_b32 v134, a118\n" \
"v_mfma_f32_16x16x32_bf16 v[32:35], a[8:11], v[120:123], v[32:35]\n" \
"ds_read_b128 v[116:119], v29 offset:5120\n" \
"v_accvgpr_read_b32 v135, a119\n" \
"v_mfma_f32_16x16x32_bf16 v[36:39], a[12:15], v[120:123], v[36:39]\n" \
"buffer_load_dwordx4 v12, s[28:31], s81 offen offset:128 lds\n" \
"v_mfma_f32_16x16x32_bf16 v[40:43], a[8:11], v[124:127], v[40:43]\n" \
"s_mov_b32 s81, 1900544\n" \
"ds_read_b128 v[120:123], v29 offset:8192\n" \
"s_add_u32 m0, s79, 90112\n" \
"v_mfma_f32_16x16x32_bf16 v[44:47], a[12:15], v[124:127], v[44:47]\n" \
"s_waitcnt vmcnt(11) lgkmcnt(7)\n" \
"s_barrier\n" \
"ds_read_b64_tr_b16 a[38:39], v179 offset:16384\n" \
"v_mfma_f32_16x16x32_bf16 v[48:51], a[8:11], v[96:99], v[48:51]\n" \
"ds_read_b128 v[124:127], v29 offset:9216\n" \
"v_accvgpr_read_b32 v136, a120\n" \
"v_mfma_f32_16x16x32_bf16 v[52:55], a[12:15], v[96:99], v[52:55]\n" \
"buffer_load_dwordx4 v12, s[28:31], s81 offen offset:0 lds\n" \
"v_mfma_f32_16x16x32_bf16 v[56:59], a[8:11], v[100:103], v[56:59]\n" \
"s_waitcnt lgkmcnt(6)\n" \
"ds_read_b128 v[96:99], v29 offset:12288\n" \
"s_add_u32 m0, s79, 91008\n" \
"v_mfma_f32_16x16x32_bf16 v[60:63], a[12:15], v[100:103], v[60:63]\n" \
"ds_read_b64_tr_b16 a[40:41], v176 offset:24576\n" \
"v_accvgpr_read_b32 v137, a121\n" \
"v_mfma_f32_16x16x32_bf16 v[64:67], a[8:11], v[104:107], v[64:67]\n" \
"ds_read_b128 v[100:103], v29 offset:13312\n" \
"v_accvgpr_read_b32 v138, a122\n" \
"v_mfma_f32_16x16x32_bf16 v[68:71], a[12:15], v[104:107], v[68:71]\n" \
"buffer_load_dwordx4 v12, s[28:31], s81 offen offset:128 lds\n" \
"v_mfma_f32_16x16x32_bf16 v[72:75], a[8:11], v[108:111], v[72:75]\n" \
"s_waitcnt lgkmcnt(6)\n" \
"ds_read_b128 v[104:107], v29 offset:6144\n" \
"s_mov_b32 s81, 1966080\n" \
"v_mfma_f32_16x16x32_bf16 v[76:79], a[12:15], v[108:111], v[76:79]\n" \
"s_add_u32 m0, s79, 49152\n" \
"ds_read_b64_tr_b16 a[42:43], v177 offset:24576\n" \
"v_accvgpr_read_b32 v139, a123\n" \
"v_mfma_f32_16x16x32_bf16 v[32:35], a[16:19], v[112:115], v[32:35]\n" \
"ds_read_b128 v[108:111], v29 offset:7168\n" \
"v_accvgpr_read_b32 v140, a124\n" \
"v_mfma_f32_16x16x32_bf16 v[36:39], a[20:23], v[112:115], v[36:39]\n" \
"buffer_load_dwordx4 v12, s[28:31], s81 offen offset:0 lds\n" \
"v_mfma_f32_16x16x32_bf16 v[40:43], a[16:19], v[116:119], v[40:43]\n" \
"s_waitcnt lgkmcnt(6)\n" \
"ds_read_b128 v[112:115], v29 offset:10240\n" \
"s_add_u32 m0, s79, 50048\n" \
"ds_read_b64_tr_b16 a[44:45], v178 offset:24576\n" \
"ds_read_b64_tr_b16 a[46:47], v179 offset:24576\n" \
"v_mfma_f32_16x16x32_bf16 v[44:47], a[20:23], v[116:119], v[44:47]\n" \
"s_waitcnt vmcnt(17)\n" \
"s_cmp_lg_u64 s[4:5], 0\ns_cbranch_scc1 LFBB_%=\nLFBBR_%=:\n" \
"v_mfma_f32_16x16x32_bf16 v[48:51], a[16:19], v[120:123], v[48:51]\n" \
"ds_read_b128 v[116:119], v29 offset:11264\n" \
"v_accvgpr_read_b32 v141, a125\n" \
"v_mfma_f32_16x16x32_bf16 v[52:55], a[20:23], v[120:123], v[52:55]\n" \
"buffer_store_dwordx2 v[10:11], v20, s[36:39], s53 offen sc1\n" \
"v_mfma_f32_16x16x32_bf16 v[56:59], a[16:19], v[124:127], v[56:59]\n" \
"ds_read_b128 v[120:123], v29 offset:14336\n" \
"s_waitcnt vmcnt(10)\n" \
"v_mfma_f32_16x16x32_bf16 v[60:63], a[20:23], v[124:127], v[60:63]\n" \
"s_waitcnt vmcnt(11) lgkmcnt(2)\n" \
"s_barrier\n" \
"v_cmp_ne_u64 vcc, s[14:15], v[18:19]\n" \
"v_mfma_f32_16x16x32_bf16 v[64:67], a[16:19], v[96:99], v[64:67]\n" \
"ds_read_b128 v[124:127], v29 offset:15360\n" \
"s_nop 0\n" \
"v_mfma_f32_16x16x32_bf16 v[68:71], a[20:23], v[96:99], v[68:71]\n" \
"buffer_load_dwordx4 v12, s[28:31], s81 offen offset:128 lds\n" \
"v_mfma_f32_16x16x32_bf16 v[72:75], a[16:19], v[100:103], v[72:75]\n" \
"s_waitcnt lgkmcnt(6)\n" \
"ds_read_b128 v[96:99], v28 offset:32768\n" \
"s_cbranch_vccnz LPS_%=\nLPSR_%=:\n" \
"v_mfma_f32_16x16x32_bf16 v[76:79], a[20:23], v[100:103], v[76:79]\n" \
"s_mov_b32 s81, 2031616\n" \
"s_add_u32 m0, s79, 57344\n" \
"ds_read_b64_tr_b16 a[48:49], v13 offset:0\n" \
"v_mfma_f32_16x16x32_bf16 v[32:35], a[24:27], v[104:107], v[32:35]\n" \
"ds_read_b128 v[100:103], v28 offset:33792\n" \
"v_accvgpr_read_b32 v142, a126\n" \
"v_mfma_f32_16x16x32_bf16 v[36:39], a[28:31], v[104:107], v[36:39]\n" \
"s_mov_b64 exec, 0xffff\nbuffer_load_dword v1, v2, s[36:39], s67 offen sc1\ns_mov_b64 exec, -1\n" \
"v_mfma_f32_16x16x32_bf16 v[40:43], a[24:27], v[108:111], v[40:43]\n" \
"s_waitcnt lgkmcnt(5)\n" \
"ds_read_b128 v[104:107], v28 offset:36864\n" \
"v_accvgpr_read_b32 v143, a127\n" \
"v_mfma_f32_16x16x32_bf16 v[44:47], a[28:31], v[108:111], v[44:47]\n" \
"ds_read_b64_tr_b16 a[50:51], v14 offset:0\n" \
"v_mfma_f32_16x16x32_bf16 v[48:51], a[24:27], v[112:115], v[48:51]\n" \
"ds_read_b128 v[108:111], v28 offset:37888\n" \
"v_mfma_f32_16x16x32_bf16 v[52:55], a[28:31], v[112:115], v[52:55]\n" \
"buffer_load_dwordx4 v12, s[28:31], s81 offen offset:0 lds\n" \
"v_mfma_f32_16x16x32_bf16 v[56:59], a[24:27], v[116:119], v[56:59]\n" \
"s_waitcnt lgkmcnt(6)\n" \
"ds_read_b128 v[112:115], v28 offset:40960\n" \
"s_add_u32 m0, s79, 58240\n" \
"ds_read_b64_tr_b16 a[52:53], v15 offset:0\n" \
"ds_read_b64_tr_b16 a[54:55], v16 offset:0\n" \
"ds_read_b64_tr_b16 a[56:57], v13 offset:8192\n" \
"ds_read_b64_tr_b16 a[58:59], v14 offset:8192\n" \
"ds_read_b64_tr_b16 a[60:61], v15 offset:8192\n" \
"ds_read_b64_tr_b16 a[62:63], v16 offset:8192\n" \
"v_mfma_f32_16x16x32_bf16 v[60:63], a[28:31], v[116:119], v[60:63]\n" \
"v_mfma_f32_16x16x32_bf16 v[64:67], a[24:27], v[120:123], v[64:67]\n" \
"ds_read_b128 v[116:119], v28 offset:41984\n" \
"v_mfma_f32_16x16x32_bf16 v[68:71], a[28:31], v[120:123], v[68:71]\n" \
"buffer_load_dwordx4 v12, s[28:31], s81 offen offset:128 lds\n" \
"v_mfma_f32_16x16x32_bf16 v[72:75], a[24:27], v[124:127], v[72:75]\n" \
"s_add_u32 s80, s66, 49152\n" \
"ds_read_b128 v[120:123], v28 offset:34816\n" \
"s_add_u32 m0, s22, 0\n" \
"v_mfma_f32_16x16x32_bf16 v[76:79], a[28:31], v[124:127], v[76:79]\n" \
"s_waitcnt vmcnt(10) lgkmcnt(2)\n" \
"s_barrier\n" \
"v_mfma_f32_16x16x32_bf16 v[32:35], a[32:35], v[96:99], v[32:35]\n" \
"ds_read_b128 v[124:127], v28 offset:35840\n" \
"buffer_load_dwordx4 v4, s[36:39], s80 offen offset:0 sc1 lds\n" \
"v_mfma_f32_16x16x32_bf16 v[36:39], a[36:39], v[96:99], v[36:39]\n" \
"buffer_load_dwordx4 v4, s[36:39], s80 offen offset:1024 sc1 lds\n" \
"v_mfma_f32_16x16x32_bf16 v[40:43], a[32:35], v[100:103], v[40:43]\n" \
"s_waitcnt lgkmcnt(10)\n" \
"ds_read_b128 v[96:99], v28 offset:38912\n" \
"buffer_load_dwordx4 v4, s[36:39], s80 offen offset:2048 sc1 lds\n" \
"v_mfma_f32_16x16x32_bf16 v[44:47], a[36:39], v[100:103], v[44:47]\n" \
"buffer_load_dwordx4 v4, s[36:39], s80 offen offset:3072 sc1 lds\n" \
"v_mfma_f32_16x16x32_bf16 v[48:51], a[32:35], v[104:107], v[48:51]\n" \
"s_add_u32 s80, s67, 0\n" \
"ds_read_b128 v[100:103], v28 offset:39936\n" \
"s_add_u32 m0, s22, 98304\n" \
"v_mfma_f32_16x16x32_bf16 v[52:55], a[36:39], v[104:107], v[52:55]\n" \
"v_mfma_f32_16x16x32_bf16 v[56:59], a[32:35], v[108:111], v[56:59]\n" \
"s_waitcnt lgkmcnt(4)\n" \
"ds_read_b128 v[104:107], v28 offset:43008\n" \
"v_mfma_f32_16x16x32_bf16 v[60:63], a[36:39], v[108:111], v[60:63]\n" \
"buffer_load_dwordx4 v31, s[36:39], s80 offen offset:0 sc1 lds\n" \
"v_mfma_f32_16x16x32_bf16 v[64:67], a[32:35], v[112:115], v[64:67]\n" \
"ds_read_b128 v[108:111], v28 offset:44032\n" \
"v_mfma_f32_16x16x32_bf16 v[68:71], a[36:39], v[112:115], v[68:71]\n" \
"v_mfma_f32_16x16x32_bf16 v[72:75], a[32:35], v[116:119], v[72:75]\n" \
"s_waitcnt lgkmcnt(4)\n" \
"ds_read_b128 v[112:115], v28 offset:45056\n" \
"v_mfma_f32_16x16x32_bf16 v[76:79], a[36:39], v[116:119], v[76:79]\n" \
"buffer_load_dwordx4 v31, s[36:39], s80 offen offset:1024 sc1 lds\n" \
"v_mfma_f32_16x16x32_bf16 v[32:35], a[40:43], v[120:123], v[32:35]\n" \
"ds_read_b128 v[116:119], v28 offset:46080\n" \
"v_mfma_f32_16x16x32_bf16 v[36:39], a[44:47], v[120:123], v[36:39]\n" \
"v_mfma_f32_16x16x32_bf16 v[40:43], a[40:43], v[124:127], v[40:43]\n" \
"ds_read_b128 v[120:123], v30 offset:16384\n" \
"v_mfma_f32_16x16x32_bf16 v[44:47], a[44:47], v[124:127], v[44:47]\n" \
"s_waitcnt vmcnt(12) lgkmcnt(5)\n" \
"s_barrier\n" \
"v_mfma_f32_16x16x32_bf16 v[48:51], a[40:43], v[96:99], v[48:51]\n" \
"ds_read_b128 v[124:127], v30 offset:17408\n" \
"v_mfma_f32_16x16x32_bf16 v[52:55], a[44:47], v[96:99], v[52:55]\n" \
"buffer_load_dwordx4 v31, s[36:39], s80 offen offset:2048 sc1 lds\n" \
"v_mfma_f32_16x16x32_bf16 v[56:59], a[40:43], v[100:103], v[56:59]\n" \
"s_waitcnt lgkmcnt(4)\n" \
"ds_read_b128 v[96:99], v30 offset:20480\n" \
"v_mfma_f32_16x16x32_bf16 v[60:63], a[44:47], v[100:103], v[60:63]\n" \
"ds_read_b64_tr_b16 a[96:97], v180 offset:16384\n" \
"v_mfma_f32_16x16x32_bf16 v[64:67], a[40:43], v[104:107], v[64:67]\n" \
"ds_read_b128 v[100:103], v30 offset:21504\n" \
"v_mfma_f32_16x16x32_bf16 v[68:71], a[44:47], v[104:107], v[68:71]\n" \
"buffer_load_dwordx4 v31, s[36:39], s80 offen offset:3072 sc1 lds\n" \
"v_mfma_f32_16x16x32_bf16 v[72:75], a[40:43], v[108:111], v[72:75]\n" \
"s_waitcnt lgkmcnt(5)\n" \
"ds_read_b128 v[104:107], v28 offset:47104\n" \
"s_add_u32 s80, s67, 16384\n" \
"v_mfma_f32_16x16x32_bf16 v[76:79], a[44:47], v[108:111], v[76:79]\n" \
"s_add_u32 m0, s22, 114688\n" \
"ds_read_b64_tr_b16 a[98:99], v181 offset:16384\n" \
"v_mfma_f32_16x16x32_bf16 v[32:35], a[48:51], v[112:115], v[32:35]\n" \
"ds_read_b128 v[108:111], v28 offset:48128\n" \
"v_mfma_f32_16x16x32_bf16 v[36:39], a[52:55], v[112:115], v[36:39]\n" \
"buffer_load_dwordx4 v31, s[36:39], s80 offen offset:0 sc1 lds\n" \
"v_mfma_f32_16x16x32_bf16 v[40:43], a[48:51], v[116:119], v[40:43]\n" \
"s_waitcnt lgkmcnt(6)\n" \
"ds_read_b128 v[112:115], v30 offset:18432\n" \
"v_mfma_f32_16x16x32_bf16 v[44:47], a[52:55], v[116:119], v[44:47]\n" \
"ds_read_b64_tr_b16 a[100:101], v182 offset:16384\n" \
"v_mfma_f32_16x16x32_bf16 v[48:51], a[48:51], v[120:123], v[48:51]\n" \
"ds_read_b128 v[116:119], v30 offset:19456\n" \
"v_mfma_f32_16x16x32_bf16 v[52:55], a[52:55], v[120:123], v[52:55]\n" \
"buffer_load_dwordx4 v31, s[36:39], s80 offen offset:1024 sc1 lds\n" \
"v_mfma_f32_16x16x32_bf16 v[56:59], a[48:51], v[124:127], v[56:59]\n" \
"ds_read_b128 v[120:123], v30 offset:22528\n" \
"v_mfma_f32_16x16x32_bf16 v[60:63], a[52:55], v[124:127], v[60:63]\n" \
"s_waitcnt vmcnt(6) lgkmcnt(7)\n" \
"s_barrier\n" \
"ds_read_b64_tr_b16 a[102:103], v183 offset:16384\n" \
"v_mfma_f32_16x16x32_bf16 v[64:67], a[48:51], v[96:99], v[64:67]\n" \
"ds_read_b128 v[124:127], v30 offset:23552\n" \
"v_mfma_f32_16x16x32_bf16 v[68:71], a[52:55], v[96:99], v[68:71]\n" \
"buffer_load_dwordx4 v31, s[36:39], s80 offen offset:2048 sc1 lds\n" \
"v_mfma_f32_16x16x32_bf16 v[72:75], a[48:51], v[100:103], v[72:75]\n" \
"s_waitcnt lgkmcnt(6)\n" \
"ds_read_b128 v[96:99], v30 offset:24576\n" \
"v_mfma_f32_16x16x32_bf16 v[76:79], a[52:55], v[100:103], v[76:79]\n" \
"ds_read_b64_tr_b16 a[104:105], v180 offset:24576\n" \
"v_mfma_f32_16x16x32_bf16 v[32:35], a[56:59], v[104:107], v[32:35]\n" \
"ds_read_b128 v[100:103], v30 offset:25600\n" \
"v_mfma_f32_16x16x32_bf16 v[36:39], a[60:63], v[104:107], v[36:39]\n" \
"buffer_load_dwordx4 v31, s[36:39], s80 offen offset:3072 sc1 lds\n" \
"v_mfma_f32_16x16x32_bf16 v[40:43], a[56:59], v[108:111], v[40:43]\n" \
"s_waitcnt lgkmcnt(6)\n" \
"ds_read_b128 v[104:107], v30 offset:28672\n" \
"s_add_u32 s80, s67, 32768\n" \
"v_mfma_f32_16x16x32_bf16 v[44:47], a[60:63], v[108:111], v[44:47]\n" \
"s_add_u32 m0, s22, 16384\n" \
"ds_read_b64_tr_b16 a[106:107], v181 offset:24576\n" \
"v_mfma_f32_16x16x32_bf16 v[48:51], a[56:59], v[112:115], v[48:51]\n" \
"ds_read_b128 v[108:111], v30 offset:29696\n" \
"v_mfma_f32_16x16x32_bf16 v[52:55], a[60:63], v[112:115], v[52:55]\n" \
"buffer_load_dwordx4 v31, s[36:39], s80 offen offset:0 sc1 lds\n" \
"v_mfma_f32_16x16x32_bf16 v[56:59], a[56:59], v[116:119], v[56:59]\n" \
"s_waitcnt lgkmcnt(6)\n" \
"ds_read_b128 v[112:115], v28 offset:0\n" \
"ds_read_b64_tr_b16 a[108:109], v182 offset:24576\n" \
"ds_read_b64_tr_b16 a[110:111], v183 offset:24576\n" \
"v_mfma_f32_16x16x32_bf16 v[60:63], a[60:63], v[116:119], v[60:63]\n" \
"ds_read_b64_tr_b16 a[112:113], v176 offset:49152\n" \
"v_mfma_f32_16x16x32_bf16 v[64:67], a[56:59], v[120:123], v[64:67]\n" \
"ds_read_b128 v[116:119], v28 offset:1024\n" \
"v_mfma_f32_16x16x32_bf16 v[68:71], a[60:63], v[120:123], v[68:71]\n" \
"buffer_load_dwordx4 v31, s[36:39], s80 offen offset:1024 sc1 lds\n" \
"v_mfma_f32_16x16x32_bf16 v[72:75], a[56:59], v[124:127], v[72:75]\n" \
"ds_read_b128 v[120:123], v30 offset:26624\n" \
"v_mfma_f32_16x16x32_bf16 v[76:79], a[60:63], v[124:127], v[76:79]\n" \
"s_waitcnt lgkmcnt(3)\n" \
"s_barrier\n" \
"ds_read_b64_tr_b16 a[114:115], v177 offset:49152\n" \
"v_mfma_f32_16x16x32_bf16 v[32:35], a[96:99], v[96:99], v[32:35]\n" \
"ds_read_b128 v[124:127], v30 offset:27648\n" \
"v_mfma_f32_16x16x32_bf16 v[36:39], a[100:103], v[96:99], v[36:39]\n" \
"buffer_load_dwordx4 v31, s[36:39], s80 offen offset:2048 sc1 lds\n" \
"v_mfma_f32_16x16x32_bf16 v[40:43], a[96:99], v[100:103], v[40:43]\n" \
"s_waitcnt lgkmcnt(8)\n" \
"ds_read_b128 v[96:99], v30 offset:30720\n" \
"v_mfma_f32_16x16x32_bf16 v[44:47], a[100:103], v[100:103], v[44:47]\n" \
"ds_read_b64_tr_b16 a[116:117], v178 offset:49152\n" \
"v_mfma_f32_16x16x32_bf16 v[48:51], a[96:99], v[104:107], v[48:51]\n" \
"ds_read_b128 v[100:103], v30 offset:31744\n" \
"v_mfma_f32_16x16x32_bf16 v[52:55], a[100:103], v[104:107], v[52:55]\n" \
"buffer_load_dwordx4 v31, s[36:39], s80 offen offset:3072 sc1 lds\n" \
"v_mfma_f32_16x16x32_bf16 v[56:59], a[96:99], v[108:111], v[56:59]\n" \
"s_waitcnt lgkmcnt(6)\n" \
"ds_read_b128 v[104:107], v28 offset:2048\n" \
"s_add_u32 s80, s67, 49152\n" \
"v_mfma_f32_16x16x32_bf16 v[60:63], a[100:103], v[108:111], v[60:63]\n" \
"s_add_u32 m0, s22, 65536\n" \
"ds_read_b64_tr_b16 a[118:119], v179 offset:49152\n" \
"v_mfma_f32_16x16x32_bf16 v[64:67], a[96:99], v[112:115], v[64:67]\n" \
"ds_read_b128 v[108:111], v28 offset:3072\n" \
"v_mfma_f32_16x16x32_bf16 v[68:71], a[100:103], v[112:115], v[68:71]\n" \
"buffer_load_dwordx4 v31, s[36:39], s80 offen offset:0 sc1 lds\n" \
"v_mfma_f32_16x16x32_bf16 v[72:75], a[96:99], v[116:119], v[72:75]\n" \
"s_waitcnt lgkmcnt(6)\n" \
"ds_read_b128 v[112:115], v28 offset:4096\n" \
"v_bfe_u32 v186, v4, 7, 2\n" \
"ds_read_b64_tr_b16 a[120:121], v176 offset:57344\n" \
"ds_read_b64_tr_b16 a[122:123], v177 offset:57344\n" \
"ds_read_b64_tr_b16 a[124:125], v178 offset:57344\n" \
"ds_read_b64_tr_b16 a[126:127], v179 offset:57344\n" \
"v_mfma_f32_16x16x32_bf16 v[76:79], a[100:103], v[116:119], v[76:79]\n" \
"v_min_u32 v186, 3, v186\n" \
"v_lshl_add_u32 v17, v186, 8, s11\n" \
"v_mfma_f32_16x16x32_bf16 v[32:35], a[104:107], v[120:123], v[32:35]\n" \
"ds_read_b128 v[116:119], v28 offset:5120\n" \
"v_bfe_u32 v186, v4, 4, 3\n" \
"v_mfma_f32_16x16x32_bf16 v[36:39], a[108:111], v[120:123], v[36:39]\n" \
"buffer_load_dwordx4 v31, s[36:39], s80 offen offset:1024 sc1 lds\n" \
"v_mfma_f32_16x16x32_bf16 v[40:43], a[104:107], v[124:127], v[40:43]\n" \
"ds_read_b128 v[120:123], v28 offset:8192\n" \
"v_lshl_add_u32 v17, v186, 3, v17\n" \
"v_mfma_f32_16x16x32_bf16 v[44:47], a[108:111], v[124:127], v[44:47]\n" \
"s_waitcnt lgkmcnt(2)\n" \
"s_barrier\n" \
"v_mfma_f32_16x16x32_bf16 v[48:51], a[104:107], v[96:99], v[48:51]\n" \
"ds_read_b128 v[124:127], v28 offset:9216\n" \
"v_mfma_f32_16x16x32_bf16 v[52:55], a[108:111], v[96:99], v[52:55]\n" \
"buffer_load_dwordx4 v31, s[36:39], s80 offen offset:2048 sc1 lds\n" \
"v_mfma_f32_16x16x32_bf16 v[56:59], a[104:107], v[100:103], v[56:59]\n" \
"s_waitcnt lgkmcnt(8)\n" \
"ds_read_b128 v[96:99], v28 offset:12288\n" \
"v_mfma_f32_16x16x32_bf16 v[60:63], a[108:111], v[100:103], v[60:63]\n" \
"v_mfma_f32_16x16x32_bf16 v[64:67], a[104:107], v[104:107], v[64:67]\n" \
"ds_read_b128 v[100:103], v28 offset:13312\n" \
"v_mfma_f32_16x16x32_bf16 v[68:71], a[108:111], v[104:107], v[68:71]\n" \
"buffer_load_dwordx4 v31, s[36:39], s80 offen offset:3072 sc1 lds\n" \
"v_mfma_f32_16x16x32_bf16 v[72:75], a[104:107], v[108:111], v[72:75]\n" \
"s_waitcnt lgkmcnt(4)\n" \
"ds_read_b128 v[104:107], v28 offset:6144\n" \
"v_mfma_f32_16x16x32_bf16 v[76:79], a[108:111], v[108:111], v[76:79]\n" \
"v_mfma_f32_16x16x32_bf16 v[32:35], a[112:115], v[112:115], v[32:35]\n" \
"ds_read_b128 v[108:111], v28 offset:7168\n" \
"v_mfma_f32_16x16x32_bf16 v[36:39], a[116:119], v[112:115], v[36:39]\n" \
"buffer_load_dwordx2 v[18:19], v17, s[36:39], 0 offen sc1\n" \
"v_mfma_f32_16x16x32_bf16 v[40:43], a[112:115], v[116:119], v[40:43]\n" \
"s_waitcnt lgkmcnt(4)\n" \
"ds_read_b128 v[112:115], v28 offset:10240\n" \
"v_mfma_f32_16x16x32_bf16 v[44:47], a[116:119], v[116:119], v[44:47]\n" \
"v_mfma_f32_16x16x32_bf16 v[48:51], a[112:115], v[120:123], v[48:51]\n" \
"ds_read_b128 v[116:119], v28 offset:11264\n" \
"v_mfma_f32_16x16x32_bf16 v[52:55], a[116:119], v[120:123], v[52:55]\n" \
"buffer_load_dwordx2 v[176:177], v185, s[36:39], s86 offen offset:0 sc1\n" \
"v_mfma_f32_16x16x32_bf16 v[56:59], a[112:115], v[124:127], v[56:59]\n" \
"ds_read_b128 v[120:123], v28 offset:14336\n" \
"v_mfma_f32_16x16x32_bf16 v[60:63], a[116:119], v[124:127], v[60:63]\n" \
"s_waitcnt vmcnt(14) lgkmcnt(5)\n" \
"s_barrier\n" \
"v_mfma_f32_16x16x32_bf16 v[64:67], a[112:115], v[96:99], v[64:67]\n" \
"ds_read_b128 v[124:127], v28 offset:15360\n" \
"v_mfma_f32_16x16x32_bf16 v[68:71], a[116:119], v[96:99], v[68:71]\n" \
"buffer_load_dwordx2 v[178:179], v185, s[36:39], s87 offen offset:0 sc1\n" \
"v_mfma_f32_16x16x32_bf16 v[72:75], a[112:115], v[100:103], v[72:75]\n" \
"s_waitcnt lgkmcnt(4)\n" \
"ds_read_b128 v[96:99], v29 offset:32768\n" \
"v_mfma_f32_16x16x32_bf16 v[76:79], a[116:119], v[100:103], v[76:79]\n" \
"v_mfma_f32_16x16x32_bf16 v[32:35], a[120:123], v[104:107], v[32:35]\n" \
"ds_read_b128 v[100:103], v29 offset:33792\n" \
"v_mfma_f32_16x16x32_bf16 v[36:39], a[124:127], v[104:107], v[36:39]\n" \
"v_mfma_f32_16x16x32_bf16 v[40:43], a[120:123], v[108:111], v[40:43]\n" \
"s_waitcnt lgkmcnt(4)\n" \
"ds_read_b128 v[104:107], v29 offset:34816\n" \
"v_mfma_f32_16x16x32_bf16 v[44:47], a[124:127], v[108:111], v[44:47]\n" \
"v_mfma_f32_16x16x32_bf16 v[48:51], a[120:123], v[112:115], v[48:51]\n" \
"ds_read_b128 v[108:111], v29 offset:35840\n" \
"v_mfma_f32_16x16x32_bf16 v[52:55], a[124:127], v[112:115], v[52:55]\n" \
"v_mfma_f32_16x16x32_bf16 v[56:59], a[120:123], v[116:119], v[56:59]\n" \
"s_waitcnt lgkmcnt(4)\n" \
"ds_read_b128 v[112:115], v29 offset:36864\n" \
"v_mfma_f32_16x16x32_bf16 v[60:63], a[124:127], v[116:119], v[60:63]\n" \
"v_mfma_f32_16x16x32_bf16 v[64:67], a[120:123], v[120:123], v[64:67]\n" \
"ds_read_b128 v[116:119], v29 offset:37888\n" \
"v_mfma_f32_16x16x32_bf16 v[68:71], a[124:127], v[120:123], v[68:71]\n" \
"v_mfma_f32_16x16x32_bf16 v[72:75], a[120:123], v[124:127], v[72:75]\n" \
"ds_read_b128 v[120:123], v29 offset:38912\n" \
"v_mfma_f32_16x16x32_bf16 v[76:79], a[124:127], v[124:127], v[76:79]\n" \
"s_waitcnt lgkmcnt(5)\n" \
"s_barrier\n" \
"v_mfma_f32_16x16x32_bf16 v[80:83], a[160:163], v[96:99], 0\n" \
"ds_read_b128 v[124:127], v29 offset:39936\n" \
"v_mfma_f32_16x16x32_bf16 v[84:87], a[164:167], v[96:99], 0\n" \
"v_mfma_f32_16x16x32_bf16 v[88:91], a[160:163], v[100:103], 0\n" \
"s_waitcnt lgkmcnt(4)\n" \
"ds_read_b128 v[96:99], v29 offset:40960\n" \
"v_cvt_pk_bf16_f32 v32, v32, v33\n" \
"v_mfma_f32_16x16x32_bf16 v[92:95], a[164:167], v[100:103], 0\n" \
"v_cvt_pk_bf16_f32 v33, v34, v35\n" \
"v_cvt_pk_bf16_f32 v34, v36, v37\n" \
"v_mfma_f32_16x16x32_bf16 v[80:83], a[168:171], v[104:107], v[80:83]\n" \
"ds_read_b128 v[100:103], v29 offset:41984\n" \
"v_cvt_pk_bf16_f32 v35, v38, v39\n" \
"v_mfma_f32_16x16x32_bf16 v[84:87], a[172:175], v[104:107], v[84:87]\n" \
"buffer_store_dwordx4 v[32:35], v4, s[36:39], s72 offen offset:0 sc1\n" \
"v_mfma_f32_16x16x32_bf16 v[88:91], a[168:171], v[108:111], v[88:91]\n" \
"s_waitcnt lgkmcnt(4)\n" \
"ds_read_b128 v[104:107], v29 offset:43008\n" \
"v_cvt_pk_bf16_f32 v40, v40, v41\n" \
"v_mfma_f32_16x16x32_bf16 v[92:95], a[172:175], v[108:111], v[92:95]\n" \
"v_cvt_pk_bf16_f32 v41, v42, v43\n" \
"v_cvt_pk_bf16_f32 v42, v44, v45\n" \
"v_mfma_f32_16x16x32_bf16 v[80:83], a[176:179], v[112:115], v[80:83]\n" \
"ds_read_b128 v[108:111], v29 offset:44032\n" \
"v_cvt_pk_bf16_f32 v43, v46, v47\n" \
"v_mfma_f32_16x16x32_bf16 v[84:87], a[180:183], v[112:115], v[84:87]\n" \
"v_mfma_f32_16x16x32_bf16 v[88:91], a[176:179], v[116:119], v[88:91]\n" \
"s_waitcnt lgkmcnt(4)\n" \
"ds_read_b128 v[112:115], v29 offset:45056\n" \
"v_mfma_f32_16x16x32_bf16 v[92:95], a[180:183], v[116:119], v[92:95]\n" \
"v_mfma_f32_16x16x32_bf16 v[80:83], a[184:187], v[120:123], v[80:83]\n" \
"ds_read_b128 v[116:119], v29 offset:46080\n" \
"v_mfma_f32_16x16x32_bf16 v[84:87], a[188:191], v[120:123], v[84:87]\n" \
"buffer_store_dwordx4 v[40:43], v4, s[36:39], s72 offen offset:1024 sc1\n" \
"v_mfma_f32_16x16x32_bf16 v[88:91], a[184:187], v[124:127], v[88:91]\n" \
"ds_read_b128 v[120:123], v29 offset:47104\n" \
"v_mfma_f32_16x16x32_bf16 v[92:95], a[188:191], v[124:127], v[92:95]\n" \
"s_waitcnt vmcnt(13) lgkmcnt(5)\n" \
"s_barrier\n" \
"s_add_u32 s80, s68, 0\n" \
"v_mfma_f32_16x16x32_bf16 v[80:83], a[192:195], v[96:99], v[80:83]\n" \
"ds_read_b128 v[124:127], v29 offset:48128\n" \
"s_waitcnt vmcnt(4)\n" \
"v_mfma_f32_16x16x32_bf16 v[84:87], a[196:199], v[96:99], v[84:87]\n" \
"s_add_u32 m0, s22, 131072\n" \
"v_cmp_ne_u64 vcc, s[14:15], v[18:19]\n" \
"s_nop 0\n" \
"v_mfma_f32_16x16x32_bf16 v[88:91], a[192:195], v[100:103], v[88:91]\n" \
"s_waitcnt lgkmcnt(4)\n" \
"ds_read_b128 v[96:99], v29 offset:49152\n" \
"s_cbranch_vccnz LPL_%=\nLPLR_%=:\n" \
"v_mfma_f32_16x16x32_bf16 v[92:95], a[196:199], v[100:103], v[92:95]\n" \
"v_mfma_f32_16x16x32_bf16 v[80:83], a[200:203], v[104:107], v[80:83]\n" \
"LH23_%=:\ns_cbranch_execz LHR2_%=\n" \
"ds_read_b128 v[100:103], v29 offset:50176\n" \
"v_mfma_f32_16x16x32_bf16 v[84:87], a[204:207], v[104:107], v[84:87]\n" \
"v_mfma_f32_16x16x32_bf16 v[88:91], a[200:203], v[108:111], v[88:91]\n" \
"s_waitcnt lgkmcnt(4)\n" \
"ds_read_b128 v[104:107], v29 offset:51200\n" \
"v_mfma_f32_16x16x32_bf16 v[92:95], a[204:207], v[108:111], v[92:95]\n" \
"buffer_load_dwordx4 v31, s[36:39], s80 offen offset:0 sc1 lds\n" \
"v_mfma_f32_16x16x32_bf16 v[80:83], a[208:211], v[112:115], v[80:83]\n" \
"ds_read_b128 v[108:111], v29 offset:52224\n" \
"v_mfma_f32_16x16x32_bf16 v[84:87], a[212:215], v[112:115], v[84:87]\n" \
"v_mfma_f32_16x16x32_bf16 v[88:91], a[208:211], v[116:119], v[88:91]\n" \
"s_waitcnt lgkmcnt(4)\n" \
"ds_read_b128 v[112:115], v29 offset:53248\n" \
"v_mfma_f32_16x16x32_bf16 v[92:95], a[212:215], v[116:119], v[92:95]\n" \
"buffer_load_dwordx4 v31, s[36:39], s80 offen offset:1024 sc1 lds\n" \
"v_mfma_f32_16x16x32_bf16 v[80:83], a[216:219], v[120:123], v[80:83]\n" \
"ds_read_b128 v[116:119], v29 offset:54272\n" \
"v_mfma_f32_16x16x32_bf16 v[84:87], a[220:223], v[120:123], v[84:87]\n" \
"v_mfma_f32_16x16x32_bf16 v[88:91], a[216:219], v[124:127], v[88:91]\n" \
"s_waitcnt lgkmcnt(4)\n" \
"ds_read_b128 v[120:123], v29 offset:55296\n" \
"v_mfma_f32_16x16x32_bf16 v[92:95], a[220:223], v[124:127], v[92:95]\n" \
"buffer_load_dwordx4 v31, s[36:39], s80 offen offset:2048 sc1 lds\n" \
"v_mfma_f32_16x16x32_bf16 v[80:83], a[224:227], v[96:99], v[80:83]\n" \
"ds_read_b128 v[124:127], v29 offset:56320\n" \
"v_mfma_f32_16x16x32_bf16 v[84:87], a[228:231], v[96:99], v[84:87]\n" \
"v_mfma_f32_16x16x32_bf16 v[88:91], a[224:227], v[100:103], v[88:91]\n" \
"s_waitcnt lgkmcnt(4)\n" \
"ds_read_b128 v[96:99], v29 offset:57344\n" \
"v_mfma_f32_16x16x32_bf16 v[92:95], a[228:231], v[100:103], v[92:95]\n" \
"buffer_load_dwordx4 v31, s[36:39], s80 offen offset:3072 sc1 lds\n" \
"v_mfma_f32_16x16x32_bf16 v[80:83], a[232:235], v[104:107], v[80:83]\n" \
"s_add_u32 s80, s68, 16384\n" \
"ds_read_b128 v[100:103], v29 offset:58368\n" \
"s_add_u32 m0, s22, 32768\n" \
"v_mfma_f32_16x16x32_bf16 v[84:87], a[236:239], v[104:107], v[84:87]\n" \
"s_waitcnt vmcnt(6)\n" \
"v_permlane16_swap_b32 v144, v146\n" \
"v_permlane16_swap_b32 v145, v147\n" \
"v_mfma_f32_16x16x32_bf16 v[88:91], a[232:235], v[108:111], v[88:91]\n" \
"s_waitcnt lgkmcnt(4)\n" \
"ds_read_b128 v[104:107], v29 offset:59392\n" \
"v_cvt_pk_bf16_f32 v48, v48, v49\n" \
"v_mfma_f32_16x16x32_bf16 v[92:95], a[236:239], v[108:111], v[92:95]\n" \
"buffer_load_dwordx4 v31, s[36:39], s80 offen offset:0 sc1 lds\n" \
"v_mfma_f32_16x16x32_bf16 v[80:83], a[240:243], v[112:115], v[80:83]\n" \
"ds_read_b128 v[108:111], v29 offset:60416\n" \
"v_cvt_pk_bf16_f32 v49, v50, v51\n" \
"v_mfma_f32_16x16x32_bf16 v[84:87], a[244:247], v[112:115], v[84:87]\n" \
"v_cvt_pk_bf16_f32 v50, v52, v53\n" \
"v_cvt_pk_bf16_f32 v51, v54, v55\n" \
"v_mfma_f32_16x16x32_bf16 v[88:91], a[240:243], v[116:119], v[88:91]\n" \
"s_waitcnt lgkmcnt(4)\n" \
"ds_read_b128 v[112:115], v29 offset:61440\n" \
"v_cvt_pk_bf16_f32 v56, v56, v57\n" \
"v_mfma_f32_16x16x32_bf16 v[92:95], a[244:247], v[116:119], v[92:95]\n" \
"buffer_store_dwordx4 v[48:51], v4, s[36:39], s73 offen offset:0 sc1\n" \
"v_mfma_f32_16x16x32_bf16 v[80:83], a[248:251], v[120:123], v[80:83]\n" \
"ds_read_b128 v[116:119], v29 offset:62464\n" \
"v_cvt_pk_bf16_f32 v57, v58, v59\n" \
"v_mfma_f32_16x16x32_bf16 v[84:87], a[252:255], v[120:123], v[84:87]\n" \
"buffer_load_dwordx4 v31, s[36:39], s80 offen offset:1024 sc1 lds\n" \
"v_mfma_f32_16x16x32_bf16 v[88:91], a[248:251], v[124:127], v[88:91]\n" \
"ds_read_b128 v[120:123], v29 offset:63488\n" \
"v_cvt_pk_bf16_f32 v58, v60, v61\n" \
"v_mfma_f32_16x16x32_bf16 v[92:95], a[252:255], v[124:127], v[92:95]\n" \
"s_waitcnt vmcnt(16) lgkmcnt(5)\n" \
"s_barrier\n" \
"v_cvt_pk_bf16_f32 v59, v62, v63\n" \
"v_mfma_f32_16x16x32_bf16 v[80:83], a[64:67], v[96:99], v[80:83]\n" \
"ds_read_b128 v[124:127], v29 offset:64512\n" \
"v_sub_f32 v5, 1.0, v176\n" \
"v_mfma_f32_16x16x32_bf16 v[84:87], a[68:71], v[96:99], v[84:87]\n" \
"buffer_load_dwordx4 v31, s[36:39], s80 offen offset:2048 sc1 lds\n" \
"v_mfma_f32_16x16x32_bf16 v[88:91], a[64:67], v[100:103], v[88:91]\n" \
"s_waitcnt lgkmcnt(4)\n" \
"ds_read_b128 v[96:99], v28 offset:16384\n" \
"v_cvt_pk_bf16_f32 v5, v5, v5\n" \
"v_mfma_f32_16x16x32_bf16 v[92:95], a[68:71], v[100:103], v[92:95]\n" \
"buffer_store_dwordx4 v[56:59], v4, s[36:39], s73 offen offset:1024 sc1\n" \
"v_mfma_f32_16x16x32_bf16 v[80:83], a[72:75], v[104:107], v[80:83]\n" \
"ds_read_b128 v[100:103], v28 offset:17408\n" \
"v_and_b32 v5, 0xffff0000, v5\n" \
"v_mfma_f32_16x16x32_bf16 v[84:87], a[76:79], v[104:107], v[84:87]\n" \
"buffer_load_dwordx4 v31, s[36:39], s80 offen offset:3072 sc1 lds\n" \
"v_mfma_f32_16x16x32_bf16 v[88:91], a[72:75], v[108:111], v[88:91]\n" \
"s_waitcnt lgkmcnt(4)\n" \
"ds_read_b128 v[104:107], v28 offset:18432\n" \
"s_add_u32 s80, s68, 32768\n" \
"v_mfma_f32_16x16x32_bf16 v[92:95], a[76:79], v[108:111], v[92:95]\n" \
"s_add_u32 m0, s22, 81920\n" \
"v_lshlrev_b32 v188, 16, v144\n" \
"v_and_b32 v189, 0xffff0000, v144\n" \
"v_mfma_f32_16x16x32_bf16 v[80:83], a[80:83], v[112:115], v[80:83]\n" \
"ds_read_b128 v[108:111], v28 offset:19456\n" \
"v_lshlrev_b32 v190, 16, v128\n" \
"v_mfma_f32_16x16x32_bf16 v[84:87], a[84:87], v[112:115], v[84:87]\n" \
"buffer_load_dwordx4 v31, s[36:39], s80 offen offset:0 sc1 lds\n" \
"v_mfma_f32_16x16x32_bf16 v[88:91], a[80:83], v[116:119], v[88:91]\n" \
"s_waitcnt lgkmcnt(4)\n" \
"ds_read_b128 v[112:115], v28 offset:20480\n" \
"v_and_b32 v191, 0xffff0000, v128\n" \
"v_mfma_f32_16x16x32_bf16 v[92:95], a[84:87], v[116:119], v[92:95]\n" \
"v_mul_f32 v188, v188, v190\n" \
"v_mul_f32 v189, v189, v191\n" \
"v_mfma_f32_16x16x32_bf16 v[80:83], a[88:91], v[120:123], v[80:83]\n" \
"ds_read_b128 v[116:119], v28 offset:21504\n" \
"v_cvt_pk_bf16_f32 v189, v188, v189\n" \
"v_mfma_f32_16x16x32_bf16 v[84:87], a[92:95], v[120:123], v[84:87]\n" \
"buffer_load_dwordx4 v31, s[36:39], s80 offen offset:1024 sc1 lds\n" \
"v_mfma_f32_16x16x32_bf16 v[88:91], a[88:91], v[124:127], v[88:91]\n" \
"s_waitcnt lgkmcnt(4)\n" \
"ds_read_b128 v[120:123], v28 offset:22528\n" \
"v_lshlrev_b32 v188, 16, v189\n" \
"v_mfma_f32_16x16x32_bf16 v[92:95], a[92:95], v[124:127], v[92:95]\n" \
"v_and_b32 v189, 0xffff0000, v189\n" \
"v_mul_f32 v188, v176, v188\n" \
"v_mfma_f32_16x16x32_bf16 v[80:83], a[128:131], v[96:99], v[80:83]\n" \
"ds_read_b128 v[124:127], v28 offset:23552\n" \
"v_mul_f32 v189, v176, v189\n" \
"v_mfma_f32_16x16x32_bf16 v[84:87], a[132:135], v[96:99], v[84:87]\n" \
"buffer_load_dwordx4 v31, s[36:39], s80 offen offset:2048 sc1 lds\n" \
"v_mfma_f32_16x16x32_bf16 v[88:91], a[128:131], v[100:103], v[88:91]\n" \
"s_waitcnt lgkmcnt(4)\n" \
"ds_read_b128 v[96:99], v28 offset:24576\n" \
"v_cvt_pk_bf16_f32 v144, v188, v189\n" \
"v_mfma_f32_16x16x32_bf16 v[92:95], a[132:135], v[100:103], v[92:95]\n" \
"v_lshlrev_b32 v188, 16, v145\n" \
"v_and_b32 v189, 0xffff0000, v145\n" \
"v_mfma_f32_16x16x32_bf16 v[80:83], a[136:139], v[104:107], v[80:83]\n" \
"ds_read_b128 v[100:103], v28 offset:25600\n" \
"v_lshlrev_b32 v190, 16, v129\n" \
"v_mfma_f32_16x16x32_bf16 v[84:87], a[140:143], v[104:107], v[84:87]\n" \
"buffer_load_dwordx4 v31, s[36:39], s80 offen offset:3072 sc1 lds\n" \
"v_mfma_f32_16x16x32_bf16 v[88:91], a[136:139], v[108:111], v[88:91]\n" \
"s_waitcnt lgkmcnt(4)\n" \
"ds_read_b128 v[104:107], v28 offset:26624\n" \
"s_add_u32 s80, s68, 49152\n" \
"v_mfma_f32_16x16x32_bf16 v[92:95], a[140:143], v[108:111], v[92:95]\n" \
"s_add_u32 m0, s22, 49152\n" \
"v_and_b32 v191, 0xffff0000, v129\n" \
"v_mul_f32 v188, v188, v190\n" \
"v_mfma_f32_16x16x32_bf16 v[80:83], a[144:147], v[112:115], v[80:83]\n" \
"ds_read_b128 v[108:111], v28 offset:27648\n" \
"v_mul_f32 v189, v189, v191\n" \
"v_mfma_f32_16x16x32_bf16 v[84:87], a[148:151], v[112:115], v[84:87]\n" \
"buffer_load_dwordx4 v31, s[36:39], s80 offen offset:0 sc1 lds\n" \
"v_mfma_f32_16x16x32_bf16 v[88:91], a[144:147], v[116:119], v[88:91]\n" \
"s_waitcnt lgkmcnt(4)\n" \
"ds_read_b128 v[112:115], v28 offset:28672\n" \
"v_cvt_pk_bf16_f32 v189, v188, v189\n" \
"v_mfma_f32_16x16x32_bf16 v[92:95], a[148:151], v[116:119], v[92:95]\n" \
"v_lshlrev_b32 v188, 16, v189\n" \
"v_and_b32 v189, 0xffff0000, v189\n" \
"v_mfma_f32_16x16x32_bf16 v[80:83], a[152:155], v[120:123], v[80:83]\n" \
"ds_read_b128 v[116:119], v28 offset:29696\n" \
"v_mul_f32 v188, v176, v188\n" \
"v_mfma_f32_16x16x32_bf16 v[84:87], a[156:159], v[120:123], v[84:87]\n" \
"buffer_load_dwordx4 v31, s[36:39], s80 offen offset:1024 sc1 lds\n" \
"v_mfma_f32_16x16x32_bf16 v[88:91], a[152:155], v[124:127], v[88:91]\n" \
"ds_read_b128 v[120:123], v28 offset:30720\n" \
"v_mul_f32 v189, v176, v189\n" \
"v_mfma_f32_16x16x32_bf16 v[92:95], a[156:159], v[124:127], v[92:95]\n" \
"s_waitcnt vmcnt(21) lgkmcnt(5)\n" \
"s_barrier\n" \
"v_cvt_pk_bf16_f32 v145, v188, v189\n" \
"v_mfma_f32_16x16x32_bf16 v[80:83], a[0:3], v[96:99], v[80:83]\n" \
"ds_read_b128 v[124:127], v28 offset:31744\n" \
"v_lshlrev_b32 v188, 16, v146\n" \
"v_mfma_f32_16x16x32_bf16 v[84:87], a[4:7], v[96:99], v[84:87]\n" \
"buffer_load_dwordx4 v31, s[36:39], s80 offen offset:2048 sc1 lds\n" \
"v_mfma_f32_16x16x32_bf16 v[88:91], a[0:3], v[100:103], v[88:91]\n" \
"s_waitcnt lgkmcnt(4)\n" \
"ds_read_b128 v[96:99], v29 offset:0\n" \
"v_and_b32 v189, 0xffff0000, v146\n" \
"v_mfma_f32_16x16x32_bf16 v[92:95], a[4:7], v[100:103], v[92:95]\n" \
"s_waitcnt vmcnt(17)\n" \
"v_lshlrev_b32 v190, 16, v132\n" \
"v_and_b32 v191, 0xffff0000, v132\n" \
"v_mfma_f32_16x16x32_bf16 v[80:83], a[8:11], v[104:107], v[80:83]\n" \
"ds_read_b128 v[100:103], v29 offset:1024\n" \
"v_mul_f32 v188, v188, v190\n" \
"v_mfma_f32_16x16x32_bf16 v[84:87], a[12:15], v[104:107], v[84:87]\n" \
"buffer_store_dwordx2 v[10:11], v20, s[36:39], s23 offen sc1\n" \
"v_mfma_f32_16x16x32_bf16 v[88:91], a[8:11], v[108:111], v[88:91]\n" \
"s_waitcnt lgkmcnt(4)\n" \
"ds_read_b128 v[104:107], v29 offset:2048\n" \
"v_mul_f32 v189, v189, v191\n" \
"v_mfma_f32_16x16x32_bf16 v[92:95], a[12:15], v[108:111], v[92:95]\n" \
"buffer_load_dwordx4 v31, s[36:39], s80 offen offset:3072 sc1 lds\n" \
"v_mfma_f32_16x16x32_bf16 v[80:83], a[16:19], v[112:115], v[80:83]\n" \
"s_add_u32 s80, s69, 0\n" \
"ds_read_b128 v[108:111], v29 offset:3072\n" \
"s_add_u32 m0, s22, 147456\n" \
"v_mfma_f32_16x16x32_bf16 v[84:87], a[20:23], v[112:115], v[84:87]\n" \
"v_cvt_pk_bf16_f32 v189, v188, v189\n" \
"v_lshlrev_b32 v188, 16, v189\n" \
"v_mfma_f32_16x16x32_bf16 v[88:91], a[16:19], v[116:119], v[88:91]\n" \
"s_waitcnt lgkmcnt(4)\n" \
"ds_read_b128 v[112:115], v29 offset:4096\n" \
"v_and_b32 v189, 0xffff0000, v189\n" \
"v_mfma_f32_16x16x32_bf16 v[92:95], a[20:23], v[116:119], v[92:95]\n" \
"buffer_load_dwordx4 v31, s[36:39], s80 offen offset:0 sc1 lds\n" \
"v_mfma_f32_16x16x32_bf16 v[80:83], a[24:27], v[120:123], v[80:83]\n" \
"ds_read_b128 v[116:119], v29 offset:5120\n" \
"v_mul_f32 v188, v176, v188\n" \
"v_mfma_f32_16x16x32_bf16 v[84:87], a[28:31], v[120:123], v[84:87]\n" \
"v_mul_f32 v189, v176, v189\n" \
"v_cvt_pk_bf16_f32 v146, v188, v189\n" \
"v_mfma_f32_16x16x32_bf16 v[88:91], a[24:27], v[124:127], v[88:91]\n" \
"s_waitcnt lgkmcnt(4)\n" \
"ds_read_b128 v[120:123], v29 offset:6144\n" \
"v_lshlrev_b32 v188, 16, v147\n" \
"v_mfma_f32_16x16x32_bf16 v[92:95], a[28:31], v[124:127], v[92:95]\n" \
"buffer_load_dwordx4 v31, s[36:39], s80 offen offset:1024 sc1 lds\n" \
"v_mfma_f32_16x16x32_bf16 v[80:83], a[32:35], v[96:99], v[80:83]\n" \
"ds_read_b128 v[124:127], v29 offset:7168\n" \
"v_and_b32 v189, 0xffff0000, v147\n" \
"v_mfma_f32_16x16x32_bf16 v[84:87], a[36:39], v[96:99], v[84:87]\n" \
"v_lshlrev_b32 v190, 16, v133\n" \
"v_and_b32 v191, 0xffff0000, v133\n" \
"v_mfma_f32_16x16x32_bf16 v[88:91], a[32:35], v[100:103], v[88:91]\n" \
"s_waitcnt lgkmcnt(4)\n" \
"ds_read_b128 v[96:99], v29 offset:8192\n" \
"v_mul_f32 v188, v188, v190\n" \
"v_mfma_f32_16x16x32_bf16 v[92:95], a[36:39], v[100:103], v[92:95]\n" \
"buffer_load_dwordx4 v31, s[36:39], s80 offen offset:2048 sc1 lds\n" \
"v_mfma_f32_16x16x32_bf16 v[80:83], a[40:43], v[104:107], v[80:83]\n" \
"ds_read_b128 v[100:103], v29 offset:9216\n" \
"v_mul_f32 v189, v189, v191\n" \
"v_mfma_f32_16x16x32_bf16 v[84:87], a[44:47], v[104:107], v[84:87]\n" \
"v_cvt_pk_bf16_f32 v189, v188, v189\n" \
"v_lshlrev_b32 v188, 16, v189\n" \
"v_mfma_f32_16x16x32_bf16 v[88:91], a[40:43], v[108:111], v[88:91]\n" \
"s_waitcnt lgkmcnt(4)\n" \
"ds_read_b128 v[104:107], v29 offset:10240\n" \
"v_and_b32 v189, 0xffff0000, v189\n" \
"v_mfma_f32_16x16x32_bf16 v[92:95], a[44:47], v[108:111], v[92:95]\n" \
"buffer_load_dwordx4 v31, s[36:39], s80 offen offset:3072 sc1 lds\n" \
"v_mfma_f32_16x16x32_bf16 v[80:83], a[48:51], v[112:115], v[80:83]\n" \
"s_add_u32 s80, s69, 16384\n" \
"ds_read_b128 v[108:111], v29 offset:11264\n" \
"s_add_u32 m0, s22, 0\n" \
"v_mfma_f32_16x16x32_bf16 v[84:87], a[52:55], v[112:115], v[84:87]\n" \
"v_mul_f32 v188, v176, v188\n" \
"v_mul_f32 v189, v176, v189\n" \
"v_mfma_f32_16x16x32_bf16 v[88:91], a[48:51], v[116:119], v[88:91]\n" \
"s_waitcnt lgkmcnt(4)\n" \
"ds_read_b128 v[112:115], v29 offset:12288\n" \
"v_cvt_pk_bf16_f32 v147, v188, v189\n" \
"v_mfma_f32_16x16x32_bf16 v[92:95], a[52:55], v[116:119], v[92:95]\n" \
"buffer_load_dwordx4 v31, s[36:39], s80 offen offset:0 sc1 lds\n" \
"v_mfma_f32_16x16x32_bf16 v[80:83], a[56:59], v[120:123], v[80:83]\n" \
"ds_read_b128 v[116:119], v29 offset:13312\n" \
"v_permlane16_swap_b32 v148, v150\n" \
"v_mfma_f32_16x16x32_bf16 v[84:87], a[60:63], v[120:123], v[84:87]\n" \
"v_permlane16_swap_b32 v149, v151\n" \
"v_sub_f32 v21, 1.0, v177\n" \
"v_mfma_f32_16x16x32_bf16 v[88:91], a[56:59], v[124:127], v[88:91]\n" \
"ds_read_b128 v[120:123], v29 offset:14336\n" \
"v_cvt_pk_bf16_f32 v21, v21, v21\n" \
"v_mfma_f32_16x16x32_bf16 v[92:95], a[60:63], v[124:127], v[92:95]\n" \
"s_waitcnt vmcnt(20) lgkmcnt(5)\n" \
"s_barrier\n" \
"v_and_b32 v21, 0xffff0000, v21\n" \
"v_mfma_f32_16x16x32_bf16 v[80:83], a[96:99], v[96:99], v[80:83]\n" \
"ds_read_b128 v[124:127], v29 offset:15360\n" \
"v_lshlrev_b32 v188, 16, v148\n" \
"v_mfma_f32_16x16x32_bf16 v[84:87], a[100:103], v[96:99], v[84:87]\n" \
"buffer_load_dwordx4 v31, s[36:39], s80 offen offset:1024 sc1 lds\n" \
"v_mfma_f32_16x16x32_bf16 v[88:91], a[96:99], v[100:103], v[88:91]\n" \
"s_waitcnt lgkmcnt(4)\n" \
"ds_read_b128 v[96:99], v30 offset:0\n" \
"v_and_b32 v189, 0xffff0000, v148\n" \
"v_mfma_f32_16x16x32_bf16 v[92:95], a[100:103], v[100:103], v[92:95]\n" \
"v_lshlrev_b32 v190, 16, v130\n" \
"v_and_b32 v191, 0xffff0000, v130\n" \
"v_mfma_f32_16x16x32_bf16 v[80:83], a[104:107], v[104:107], v[80:83]\n" \
"ds_read_b128 v[100:103], v30 offset:1024\n" \
"v_mul_f32 v188, v188, v190\n" \
"v_mfma_f32_16x16x32_bf16 v[84:87], a[108:111], v[104:107], v[84:87]\n" \
"buffer_load_dwordx4 v31, s[36:39], s80 offen offset:2048 sc1 lds\n" \
"v_mfma_f32_16x16x32_bf16 v[88:91], a[104:107], v[108:111], v[88:91]\n" \
"s_waitcnt lgkmcnt(4)\n" \
"ds_read_b128 v[104:107], v30 offset:2048\n" \
"v_mul_f32 v189, v189, v191\n" \
"v_mfma_f32_16x16x32_bf16 v[92:95], a[108:111], v[108:111], v[92:95]\n" \
"v_cvt_pk_bf16_f32 v189, v188, v189\n" \
"v_lshlrev_b32 v188, 16, v189\n" \
"v_mfma_f32_16x16x32_bf16 v[80:83], a[112:115], v[112:115], v[80:83]\n" \
"ds_read_b128 v[108:111], v30 offset:3072\n" \
"v_and_b32 v189, 0xffff0000, v189\n" \
"v_mfma_f32_16x16x32_bf16 v[84:87], a[116:119], v[112:115], v[84:87]\n" \
"buffer_load_dwordx4 v31, s[36:39], s80 offen offset:3072 sc1 lds\n" \
"v_mfma_f32_16x16x32_bf16 v[88:91], a[112:115], v[116:119], v[88:91]\n" \
"s_waitcnt lgkmcnt(4)\n" \
"ds_read_b128 v[112:115], v30 offset:4096\n" \
"s_waitcnt vmcnt(18)\n" \
"v_mfma_f32_16x16x32_bf16 v[92:95], a[116:119], v[116:119], v[92:95]\n" \
"buffer_store_dwordx2 v[10:11], v20, s[36:39], s24 offen sc1\n" \
"v_mfma_f32_16x16x32_bf16 v[80:83], a[120:123], v[120:123], v[80:83]\n" \
"s_add_u32 s80, s69, 32768\n" \
"ds_read_b128 v[116:119], v30 offset:5120\n" \
"s_add_u32 m0, s22, 98304\n" \
"v_mfma_f32_16x16x32_bf16 v[84:87], a[124:127], v[120:123], v[84:87]\n" \
"buffer_load_dwordx4 v31, s[36:39], s80 offen offset:0 sc1 lds\n" \
"v_mfma_f32_16x16x32_bf16 v[88:91], a[120:123], v[124:127], v[88:91]\n" \
"s_waitcnt lgkmcnt(4)\n" \
"ds_read_b128 v[120:123], v30 offset:6144\n" \
"v_mul_f32 v188, v177, v188\n" \
"v_mfma_f32_16x16x32_bf16 v[92:95], a[124:127], v[124:127], v[92:95]\n" \
"v_mul_f32 v189, v177, v189\n" \
"v_cvt_pk_bf16_f32 v148, v188, v189\n" \
"v_mfma_f32_16x16x32_bf16 v[192:195], a[160:163], v[96:99], 0\n" \
"ds_read_b128 v[124:127], v30 offset:7168\n" \
"v_lshlrev_b32 v188, 16, v149\n" \
"v_mfma_f32_16x16x32_bf16 v[196:199], a[164:167], v[96:99], 0\n" \
"buffer_load_dwordx4 v31, s[36:39], s80 offen offset:1024 sc1 lds\n" \
"v_mfma_f32_16x16x32_bf16 v[200:203], a[160:163], v[100:103], 0\n" \
"s_waitcnt lgkmcnt(4)\n" \
"ds_read_b128 v[96:99], v30 offset:8192\n" \
"v_and_b32 v189, 0xffff0000, v149\n" \
"v_mfma_f32_16x16x32_bf16 v[204:207], a[164:167], v[100:103], 0\n" \
"s_cmp_eq_u32 s89, 3\ns_cbranch_scc1 LSW_%=\nLSWR_%=:\n" \
"v_cvt_pk_bf16_f32 v80, v80, v81\n" \
"v_mfma_f32_16x16x32_bf16 v[192:195], a[168:171], v[104:107], v[192:195]\n" \
"ds_read_b128 v[100:103], v30 offset:9216\n" \
"v_cvt_pk_bf16_f32 v81, v82, v83\n" \
"v_mfma_f32_16x16x32_bf16 v[196:199], a[172:175], v[104:107], v[196:199]\n" \
"buffer_load_dwordx4 v31, s[36:39], s80 offen offset:2048 sc1 lds\n" \
"v_mfma_f32_16x16x32_bf16 v[200:203], a[168:171], v[108:111], v[200:203]\n" \
"s_waitcnt lgkmcnt(4)\n" \
"ds_read_b128 v[104:107], v30 offset:10240\n" \
"v_cvt_pk_bf16_f32 v82, v84, v85\n" \
"v_mfma_f32_16x16x32_bf16 v[204:207], a[172:175], v[108:111], v[204:207]\n" \
"v_cvt_pk_bf16_f32 v83, v86, v87\n" \
"v_cvt_pk_bf16_f32 v88, v88, v89\n" \
"v_mfma_f32_16x16x32_bf16 v[192:195], a[176:179], v[112:115], v[192:195]\n" \
"ds_read_b128 v[108:111], v30 offset:11264\n" \
"v_cvt_pk_bf16_f32 v89, v90, v91\n" \
"v_mfma_f32_16x16x32_bf16 v[196:199], a[180:183], v[112:115], v[196:199]\n" \
"buffer_store_dwordx4 v[80:83], v4, s[36:39], s74 offen offset:0 sc1\n" \
"v_mfma_f32_16x16x32_bf16 v[200:203], a[176:179], v[116:119], v[200:203]\n" \
"s_waitcnt lgkmcnt(4)\n" \
"ds_read_b128 v[112:115], v30 offset:12288\n" \
"v_cvt_pk_bf16_f32 v90, v92, v93\n" \
"v_mfma_f32_16x16x32_bf16 v[204:207], a[180:183], v[116:119], v[204:207]\n" \
"buffer_load_dwordx4 v31, s[36:39], s80 offen offset:3072 sc1 lds\n" \
"v_mfma_f32_16x16x32_bf16 v[192:195], a[184:187], v[120:123], v[192:195]\n" \
"s_add_u32 s80, s69, 49152\n" \
"ds_read_b128 v[116:119], v30 offset:13312\n" \
"s_add_u32 m0, s22, 114688\n" \
"v_mfma_f32_16x16x32_bf16 v[196:199], a[188:191], v[120:123], v[196:199]\n" \
"v_cvt_pk_bf16_f32 v91, v94, v95\n" \
"v_lshlrev_b32 v190, 16, v131\n" \
"v_mfma_f32_16x16x32_bf16 v[200:203], a[184:187], v[124:127], v[200:203]\n" \
"ds_read_b128 v[120:123], v30 offset:14336\n" \
"v_and_b32 v191, 0xffff0000, v131\n" \
"v_mfma_f32_16x16x32_bf16 v[204:207], a[188:191], v[124:127], v[204:207]\n" \
"s_waitcnt vmcnt(23) lgkmcnt(5)\n" \
"s_barrier\n" \
"v_mul_f32 v188, v188, v190\n" \
"v_mfma_f32_16x16x32_bf16 v[192:195], a[192:195], v[96:99], v[192:195]\n" \
"ds_read_b128 v[124:127], v30 offset:15360\n" \
"v_mul_f32 v189, v189, v191\n" \
"v_mfma_f32_16x16x32_bf16 v[196:199], a[196:199], v[96:99], v[196:199]\n" \
"buffer_store_dwordx4 v[88:91], v4, s[36:39], s74 offen offset:1024 sc1\n" \
"v_mfma_f32_16x16x32_bf16 v[200:203], a[192:195], v[100:103], v[200:203]\n" \
"s_waitcnt lgkmcnt(4)\n" \
"ds_read_b128 v[96:99], v28 offset:32768\n" \
"v_cvt_pk_bf16_f32 v189, v188, v189\n" \
"v_mfma_f32_16x16x32_bf16 v[204:207], a[196:199], v[100:103], v[204:207]\n" \
"buffer_load_dwordx4 v31, s[36:39], s80 offen offset:0 sc1 lds\n" \
"v_mfma_f32_16x16x32_bf16 v[192:195], a[200:203], v[104:107], v[192:195]\n" \
"ds_read_b128 v[100:103], v28 offset:33792\n" \
"v_lshlrev_b32 v188, 16, v189\n" \
"v_mfma_f32_16x16x32_bf16 v[196:199], a[204:207], v[104:107], v[196:199]\n" \
"v_and_b32 v189, 0xffff0000, v189\n" \
"v_mul_f32 v188, v177, v188\n" \
"v_mfma_f32_16x16x32_bf16 v[200:203], a[200:203], v[108:111], v[200:203]\n" \
"s_waitcnt lgkmcnt(4)\n" \
"ds_read_b128 v[104:107], v28 offset:34816\n" \
"v_mul_f32 v189, v177, v189\n" \
"v_mfma_f32_16x16x32_bf16 v[204:207], a[204:207], v[108:111], v[204:207]\n" \
"buffer_load_dwordx4 v31, s[36:39], s80 offen offset:1024 sc1 lds\n" \
"v_mfma_f32_16x16x32_bf16 v[192:195], a[208:211], v[112:115], v[192:195]\n" \
"ds_read_b128 v[108:111], v28 offset:35840\n" \
"v_cvt_pk_bf16_f32 v149, v188, v189\n" \
"v_mfma_f32_16x16x32_bf16 v[196:199], a[212:215], v[112:115], v[196:199]\n" \
"v_lshlrev_b32 v188, 16, v150\n" \
"v_and_b32 v189, 0xffff0000, v150\n" \
"v_mfma_f32_16x16x32_bf16 v[200:203], a[208:211], v[116:119], v[200:203]\n" \
"s_waitcnt lgkmcnt(4)\n" \
"ds_read_b128 v[112:115], v28 offset:36864\n" \
"v_lshlrev_b32 v190, 16, v134\n" \
"v_mfma_f32_16x16x32_bf16 v[204:207], a[212:215], v[116:119], v[204:207]\n" \
"buffer_load_dwordx4 v31, s[36:39], s80 offen offset:2048 sc1 lds\n" \
"v_mfma_f32_16x16x32_bf16 v[192:195], a[216:219], v[120:123], v[192:195]\n" \
"ds_read_b128 v[116:119], v28 offset:37888\n" \
"v_and_b32 v191, 0xffff0000, v134\n" \
"v_mfma_f32_16x16x32_bf16 v[196:199], a[220:223], v[120:123], v[196:199]\n" \
"v_mul_f32 v188, v188, v190\n" \
"v_mul_f32 v189, v189, v191\n" \
"v_mfma_f32_16x16x32_bf16 v[200:203], a[216:219], v[124:127], v[200:203]\n" \
"s_waitcnt lgkmcnt(4)\n" \
"ds_read_b128 v[120:123], v28 offset:38912\n" \
"v_cvt_pk_bf16_f32 v189, v188, v189\n" \
"v_mfma_f32_16x16x32_bf16 v[204:207], a[220:223], v[124:127], v[204:207]\n" \
"buffer_load_dwordx4 v31, s[36:39], s80 offen offset:3072 sc1 lds\n" \
"v_mfma_f32_16x16x32_bf16 v[192:195], a[224:227], v[96:99], v[192:195]\n" \
"s_add_u32 s80, s70, 0\n" \
"ds_read_b128 v[124:127], v28 offset:39936\n" \
"s_add_u32 m0, s22, 16384\n" \
"v_mfma_f32_16x16x32_bf16 v[196:199], a[228:231], v[96:99], v[196:199]\n" \
"v_lshlrev_b32 v188, 16, v189\n" \
"v_and_b32 v189, 0xffff0000, v189\n" \
"v_mfma_f32_16x16x32_bf16 v[200:203], a[224:227], v[100:103], v[200:203]\n" \
"s_waitcnt lgkmcnt(4)\n" \
"ds_read_b128 v[96:99], v28 offset:40960\n" \
"v_mul_f32 v188, v177, v188\n" \
"v_mfma_f32_16x16x32_bf16 v[204:207], a[228:231], v[100:103], v[204:207]\n" \
"v_mul_f32 v189, v177, v189\n" \
"v_cvt_pk_bf16_f32 v150, v188, v189\n" \
"v_mfma_f32_16x16x32_bf16 v[192:195], a[232:235], v[104:107], v[192:195]\n" \
"ds_read_b128 v[100:103], v28 offset:41984\n" \
"v_lshlrev_b32 v188, 16, v151\n" \
"v_mfma_f32_16x16x32_bf16 v[196:199], a[236:239], v[104:107], v[196:199]\n" \
"v_and_b32 v189, 0xffff0000, v151\n" \
"v_lshlrev_b32 v190, 16, v135\n" \
"v_mfma_f32_16x16x32_bf16 v[200:203], a[232:235], v[108:111], v[200:203]\n" \
"s_waitcnt lgkmcnt(4)\n" \
"ds_read_b128 v[104:107], v28 offset:43008\n" \
"v_and_b32 v191, 0xffff0000, v135\n" \
"v_mfma_f32_16x16x32_bf16 v[204:207], a[236:239], v[108:111], v[204:207]\n" \
"buffer_load_dwordx4 v31, s[36:39], s80 offen offset:0 sc1 lds\n" \
"v_mfma_f32_16x16x32_bf16 v[192:195], a[240:243], v[112:115], v[192:195]\n" \
"ds_read_b128 v[108:111], v28 offset:44032\n" \
"v_mul_f32 v188, v188, v190\n" \
"v_mfma_f32_16x16x32_bf16 v[196:199], a[244:247], v[112:115], v[196:199]\n" \
"v_mul_f32 v189, v189, v191\n" \
"v_cvt_pk_bf16_f32 v189, v188, v189\n" \
"v_mfma_f32_16x16x32_bf16 v[200:203], a[240:243], v[116:119], v[200:203]\n" \
"s_waitcnt lgkmcnt(4)\n" \
"ds_read_b128 v[112:115], v28 offset:45056\n" \
"v_lshlrev_b32 v188, 16, v189\n" \
"v_mfma_f32_16x16x32_bf16 v[204:207], a[244:247], v[116:119], v[204:207]\n" \
"buffer_load_dwordx4 v31, s[36:39], s80 offen offset:1024 sc1 lds\n" \
"v_mfma_f32_16x16x32_bf16 v[192:195], a[248:251], v[120:123], v[192:195]\n" \
"ds_read_b128 v[116:119], v28 offset:46080\n" \
"v_and_b32 v189, 0xffff0000, v189\n" \
"v_mfma_f32_16x16x32_bf16 v[196:199], a[252:255], v[120:123], v[196:199]\n" \
"v_mul_f32 v188, v177, v188\n" \
"v_mul_f32 v189, v177, v189\n" \
"v_mfma_f32_16x16x32_bf16 v[200:203], a[248:251], v[124:127], v[200:203]\n" \
"ds_read_b128 v[120:123], v28 offset:47104\n" \
"v_cvt_pk_bf16_f32 v151, v188, v189\n" \
"v_mfma_f32_16x16x32_bf16 v[204:207], a[252:255], v[124:127], v[204:207]\n" \
"s_waitcnt vmcnt(26) lgkmcnt(5)\n" \
"s_barrier\n" \
"v_permlane16_swap_b32 v152, v154\n" \
"v_mfma_f32_16x16x32_bf16 v[192:195], a[64:67], v[96:99], v[192:195]\n" \
"ds_read_b128 v[124:127], v28 offset:48128\n" \
"v_permlane16_swap_b32 v153, v155\n" \
"v_mfma_f32_16x16x32_bf16 v[196:199], a[68:71], v[96:99], v[196:199]\n" \
"buffer_load_dwordx4 v31, s[36:39], s80 offen offset:2048 sc1 lds\n" \
"v_mfma_f32_16x16x32_bf16 v[200:203], a[64:67], v[100:103], v[200:203]\n" \
"s_waitcnt lgkmcnt(4)\n" \
"ds_read_b128 v[96:99], v29 offset:16384\n" \
"v_sub_f32 v6, 1.0, v178\n" \
"v_mfma_f32_16x16x32_bf16 v[204:207], a[68:71], v[100:103], v[204:207]\n" \
"v_cvt_pk_bf16_f32 v6, v6, v6\n" \
"v_and_b32 v6, 0xffff0000, v6\n" \
"v_mfma_f32_16x16x32_bf16 v[192:195], a[72:75], v[104:107], v[192:195]\n" \
"ds_read_b128 v[100:103], v29 offset:17408\n" \
"v_lshlrev_b32 v188, 16, v152\n" \
"v_mfma_f32_16x16x32_bf16 v[196:199], a[76:79], v[104:107], v[196:199]\n" \
"buffer_load_dwordx4 v31, s[36:39], s80 offen offset:3072 sc1 lds\n" \
"v_mfma_f32_16x16x32_bf16 v[200:203], a[72:75], v[108:111], v[200:203]\n" \
"s_waitcnt lgkmcnt(4)\n" \
"ds_read_b128 v[104:107], v29 offset:18432\n" \
"v_and_b32 v189, 0xffff0000, v152\n" \
"v_mfma_f32_16x16x32_bf16 v[204:207], a[76:79], v[108:111], v[204:207]\n" \
"v_lshlrev_b32 v190, 16, v136\n" \
"v_and_b32 v191, 0xffff0000, v136\n" \
"v_mfma_f32_16x16x32_bf16 v[192:195], a[80:83], v[112:115], v[192:195]\n" \
"ds_read_b128 v[108:111], v29 offset:19456\n" \
"v_mul_f32 v188, v188, v190\n" \
"v_mfma_f32_16x16x32_bf16 v[196:199], a[84:87], v[112:115], v[196:199]\n" \
"v_mul_f32 v189, v189, v191\n" \
"v_cvt_pk_bf16_f32 v189, v188, v189\n" \
"v_mfma_f32_16x16x32_bf16 v[200:203], a[80:83], v[116:119], v[200:203]\n" \
"s_waitcnt lgkmcnt(4)\n" \
"ds_read_b128 v[112:115], v29 offset:20480\n" \
"v_lshlrev_b32 v188, 16, v189\n" \
"v_mfma_f32_16x16x32_bf16 v[204:207], a[84:87], v[116:119], v[204:207]\n" \
"v_and_b32 v189, 0xffff0000, v189\n" \
"v_mul_f32 v188, v178, v188\n" \
"v_mfma_f32_16x16x32_bf16 v[192:195], a[88:91], v[120:123], v[192:195]\n" \
"ds_read_b128 v[116:119], v29 offset:21504\n" \
"v_mul_f32 v189, v178, v189\n" \
"v_mfma_f32_16x16x32_bf16 v[196:199], a[92:95], v[120:123], v[196:199]\n" \
"v_cvt_pk_bf16_f32 v152, v188, v189\n" \
"v_lshlrev_b32 v188, 16, v153\n" \
"v_mfma_f32_16x16x32_bf16 v[200:203], a[88:91], v[124:127], v[200:203]\n" \
"s_waitcnt lgkmcnt(4)\n" \
"ds_read_b128 v[120:123], v29 offset:22528\n" \
"v_and_b32 v189, 0xffff0000, v153\n" \
"v_mfma_f32_16x16x32_bf16 v[204:207], a[92:95], v[124:127], v[204:207]\n" \
"s_add_u32 s80, s70, 16384\n" \
"s_add_u32 m0, s22, 65536\n" \
"v_lshlrev_b32 v190, 16, v137\n" \
"v_mfma_f32_16x16x32_bf16 v[192:195], a[128:131], v[96:99], v[192:195]\n" \
"ds_read_b128 v[124:127], v29 offset:23552\n" \
"v_and_b32 v191, 0xffff0000, v137\n" \
"v_mfma_f32_16x16x32_bf16 v[196:199], a[132:135], v[96:99], v[196:199]\n" \
"v_mul_f32 v188, v188, v190\n" \
"v_mul_f32 v189, v189, v191\n" \
"v_mfma_f32_16x16x32_bf16 v[200:203], a[128:131], v[100:103], v[200:203]\n" \
"s_waitcnt lgkmcnt(4)\n" \
"ds_read_b128 v[96:99], v29 offset:24576\n" \
"v_cvt_pk_bf16_f32 v189, v188, v189\n" \
"v_mfma_f32_16x16x32_bf16 v[204:207], a[132:135], v[100:103], v[204:207]\n" \
"v_lshlrev_b32 v188, 16, v189\n" \
"v_and_b32 v189, 0xffff0000, v189\n" \
"v_mfma_f32_16x16x32_bf16 v[192:195], a[136:139], v[104:107], v[192:195]\n" \
"ds_read_b128 v[100:103], v29 offset:25600\n" \
"v_mul_f32 v188, v178, v188\n" \
"v_mfma_f32_16x16x32_bf16 v[196:199], a[140:143], v[104:107], v[196:199]\n" \
"v_mul_f32 v189, v178, v189\n" \
"v_cvt_pk_bf16_f32 v153, v188, v189\n" \
"v_mfma_f32_16x16x32_bf16 v[200:203], a[136:139], v[108:111], v[200:203]\n" \
"s_waitcnt lgkmcnt(4)\n" \
"ds_read_b128 v[104:107], v29 offset:26624\n" \
"v_lshlrev_b32 v188, 16, v154\n" \
"v_mfma_f32_16x16x32_bf16 v[204:207], a[140:143], v[108:111], v[204:207]\n" \
"buffer_load_dwordx4 v31, s[36:39], s80 offen offset:0 sc1 lds\n" \
"v_mfma_f32_16x16x32_bf16 v[192:195], a[144:147], v[112:115], v[192:195]\n" \
"ds_read_b128 v[108:111], v29 offset:27648\n" \
"v_and_b32 v189, 0xffff0000, v154\n" \
"v_mfma_f32_16x16x32_bf16 v[196:199], a[148:151], v[112:115], v[196:199]\n" \
"v_lshlrev_b32 v190, 16, v140\n" \
"v_and_b32 v191, 0xffff0000, v140\n" \
"v_mfma_f32_16x16x32_bf16 v[200:203], a[144:147], v[116:119], v[200:203]\n" \
"s_waitcnt lgkmcnt(4)\n" \
"ds_read_b128 v[112:115], v29 offset:28672\n" \
"v_mul_f32 v188, v188, v190\n" \
"v_mfma_f32_16x16x32_bf16 v[204:207], a[148:151], v[116:119], v[204:207]\n" \
"buffer_load_dwordx4 v31, s[36:39], s80 offen offset:1024 sc1 lds\n" \
"v_mfma_f32_16x16x32_bf16 v[192:195], a[152:155], v[120:123], v[192:195]\n" \
"ds_read_b128 v[116:119], v29 offset:29696\n" \
"v_mul_f32 v189, v189, v191\n" \
"v_mfma_f32_16x16x32_bf16 v[196:199], a[156:159], v[120:123], v[196:199]\n" \
"v_cvt_pk_bf16_f32 v189, v188, v189\n" \
"v_lshlrev_b32 v188, 16, v189\n" \
"v_mfma_f32_16x16x32_bf16 v[200:203], a[152:155], v[124:127], v[200:203]\n" \
"ds_read_b128 v[120:123], v29 offset:30720\n" \
"v_and_b32 v189, 0xffff0000, v189\n" \
"v_mfma_f32_16x16x32_bf16 v[204:207], a[156:159], v[124:127], v[204:207]\n" \
"s_waitcnt vmcnt(25) lgkmcnt(5)\n" \
"s_barrier\n" \
"v_mul_f32 v188, v178, v188\n" \
"v_mfma_f32_16x16x32_bf16 v[192:195], a[0:3], v[96:99], v[192:195]\n" \
"ds_read_b128 v[124:127], v29 offset:31744\n" \
"v_mul_f32 v189, v178, v189\n" \
"v_mfma_f32_16x16x32_bf16 v[196:199], a[4:7], v[96:99], v[196:199]\n" \
"buffer_load_dwordx4 v31, s[36:39], s80 offen offset:2048 sc1 lds\n" \
"v_mfma_f32_16x16x32_bf16 v[200:203], a[0:3], v[100:103], v[200:203]\n" \
"s_waitcnt lgkmcnt(4)\n" \
"ds_read_b128 v[96:99], v28 offset:49152\n" \
"v_cvt_pk_bf16_f32 v154, v188, v189\n" \
"v_mfma_f32_16x16x32_bf16 v[204:207], a[4:7], v[100:103], v[204:207]\n" \
"v_lshlrev_b32 v188, 16, v155\n" \
"v_and_b32 v189, 0xffff0000, v155\n" \
"v_mfma_f32_16x16x32_bf16 v[192:195], a[8:11], v[104:107], v[192:195]\n" \
"ds_read_b128 v[100:103], v28 offset:50176\n" \
"v_lshlrev_b32 v190, 16, v141\n" \
"v_mfma_f32_16x16x32_bf16 v[196:199], a[12:15], v[104:107], v[196:199]\n" \
"buffer_load_dwordx4 v31, s[36:39], s80 offen offset:3072 sc1 lds\n" \
"v_mfma_f32_16x16x32_bf16 v[200:203], a[8:11], v[108:111], v[200:203]\n" \
"s_waitcnt lgkmcnt(4)\n" \
"ds_read_b128 v[104:107], v28 offset:51200\n" \
"v_and_b32 v191, 0xffff0000, v141\n" \
"v_mfma_f32_16x16x32_bf16 v[204:207], a[12:15], v[108:111], v[204:207]\n" \
"s_waitcnt vmcnt(12)\n" \
"v_mul_f32 v188, v188, v190\n" \
"v_mul_f32 v189, v189, v191\n" \
"v_mfma_f32_16x16x32_bf16 v[192:195], a[16:19], v[112:115], v[192:195]\n" \
"ds_read_b128 v[108:111], v28 offset:52224\n" \
"v_cvt_pk_bf16_f32 v189, v188, v189\n" \
"v_mfma_f32_16x16x32_bf16 v[196:199], a[20:23], v[112:115], v[196:199]\n" \
"LH24_%=:\ns_cbranch_execz LHR2_%=\n" \
"buffer_store_dwordx2 v[10:11], v20, s[36:39], s45 offen sc1\n" \
"v_mfma_f32_16x16x32_bf16 v[200:203], a[16:19], v[116:119], v[200:203]\n" \
"s_waitcnt lgkmcnt(4)\n" \
"ds_read_b128 v[112:115], v28 offset:53248\n" \
"v_lshlrev_b32 v188, 16, v189\n" \
"v_mfma_f32_16x16x32_bf16 v[204:207], a[20:23], v[116:119], v[204:207]\n" \
"v_and_b32 v189, 0xffff0000, v189\n" \
"v_mul_f32 v188, v178, v188\n" \
"v_mfma_f32_16x16x32_bf16 v[192:195], a[24:27], v[120:123], v[192:195]\n" \
"ds_read_b128 v[116:119], v28 offset:54272\n" \
"v_mul_f32 v189, v178, v189\n" \
"v_mfma_f32_16x16x32_bf16 v[196:199], a[28:31], v[120:123], v[196:199]\n" \
"v_cvt_pk_bf16_f32 v155, v188, v189\n" \
"v_permlane16_swap_b32 v156, v158\n" \
"v_mfma_f32_16x16x32_bf16 v[200:203], a[24:27], v[124:127], v[200:203]\n" \
"s_waitcnt lgkmcnt(4)\n" \
"ds_read_b128 v[120:123], v28 offset:55296\n" \
"v_permlane16_swap_b32 v157, v159\n" \
"v_mfma_f32_16x16x32_bf16 v[204:207], a[28:31], v[124:127], v[204:207]\n" \
"s_add_u32 s80, s70, 32768\n" \
"s_add_u32 m0, s22, 131072\n" \
"v_sub_f32 v8, 1.0, v179\n" \
"v_mfma_f32_16x16x32_bf16 v[192:195], a[32:35], v[96:99], v[192:195]\n" \
"ds_read_b128 v[124:127], v28 offset:56320\n" \
"v_cvt_pk_bf16_f32 v8, v8, v8\n" \
"v_mfma_f32_16x16x32_bf16 v[196:199], a[36:39], v[96:99], v[196:199]\n" \
"v_and_b32 v8, 0xffff0000, v8\n" \
"v_lshlrev_b32 v188, 16, v156\n" \
"v_mfma_f32_16x16x32_bf16 v[200:203], a[32:35], v[100:103], v[200:203]\n" \
"s_waitcnt lgkmcnt(4)\n" \
"ds_read_b128 v[96:99], v28 offset:57344\n" \
"v_and_b32 v189, 0xffff0000, v156\n" \
"v_mfma_f32_16x16x32_bf16 v[204:207], a[36:39], v[100:103], v[204:207]\n" \
"v_lshlrev_b32 v190, 16, v138\n" \
"v_and_b32 v191, 0xffff0000, v138\n" \
"v_mfma_f32_16x16x32_bf16 v[192:195], a[40:43], v[104:107], v[192:195]\n" \
"ds_read_b128 v[100:103], v28 offset:58368\n" \
"v_mul_f32 v188, v188, v190\n" \
"v_mfma_f32_16x16x32_bf16 v[196:199], a[44:47], v[104:107], v[196:199]\n" \
"v_mul_f32 v189, v189, v191\n" \
"v_cvt_pk_bf16_f32 v189, v188, v189\n" \
"v_mfma_f32_16x16x32_bf16 v[200:203], a[40:43], v[108:111], v[200:203]\n" \
"s_waitcnt lgkmcnt(4)\n" \
"ds_read_b128 v[104:107], v28 offset:59392\n" \
"v_lshlrev_b32 v188, 16, v189\n" \
"v_mfma_f32_16x16x32_bf16 v[204:207], a[44:47], v[108:111], v[204:207]\n" \
"buffer_load_dwordx4 v31, s[36:39], s80 offen offset:0 sc1 lds\n" \
"v_mfma_f32_16x16x32_bf16 v[192:195], a[48:51], v[112:115], v[192:195]\n" \
"ds_read_b128 v[108:111], v28 offset:60416\n" \
"v_and_b32 v189, 0xffff0000, v189\n" \
"v_mfma_f32_16x16x32_bf16 v[196:199], a[52:55], v[112:115], v[196:199]\n" \
"v_mul_f32 v188, v179, v188\n" \
"v_mul_f32 v189, v179, v189\n" \
"v_mfma_f32_16x16x32_bf16 v[200:203], a[48:51], v[116:119], v[200:203]\n" \
"s_waitcnt lgkmcnt(4)\n" \
"ds_read_b128 v[112:115], v28 offset:61440\n" \
"v_cvt_pk_bf16_f32 v156, v188, v189\n" \
"v_mfma_f32_16x16x32_bf16 v[204:207], a[52:55], v[116:119], v[204:207]\n" \
"buffer_load_dwordx4 v31, s[36:39], s80 offen offset:1024 sc1 lds\n" \
"v_mfma_f32_16x16x32_bf16 v[192:195], a[56:59], v[120:123], v[192:195]\n" \
"ds_read_b128 v[116:119], v28 offset:62464\n" \
"v_lshlrev_b32 v188, 16, v157\n" \
"v_mfma_f32_16x16x32_bf16 v[196:199], a[60:63], v[120:123], v[196:199]\n" \
"v_and_b32 v189, 0xffff0000, v157\n" \
"v_lshlrev_b32 v190, 16, v139\n" \
"v_mfma_f32_16x16x32_bf16 v[200:203], a[56:59], v[124:127], v[200:203]\n" \
"ds_read_b128 v[120:123], v28 offset:63488\n" \
"v_and_b32 v191, 0xffff0000, v139\n" \
"v_mfma_f32_16x16x32_bf16 v[204:207], a[60:63], v[124:127], v[204:207]\n" \
"s_waitcnt vmcnt(26) lgkmcnt(5)\n" \
"s_barrier\n" \
"v_mul_f32 v188, v188, v190\n" \
"v_mfma_f32_16x16x32_bf16 v[192:195], a[96:99], v[96:99], v[192:195]\n" \
"ds_read_b128 v[124:127], v28 offset:64512\n" \
"v_mul_f32 v189, v189, v191\n" \
"v_mfma_f32_16x16x32_bf16 v[196:199], a[100:103], v[96:99], v[196:199]\n" \
"buffer_load_dwordx4 v31, s[36:39], s80 offen offset:2048 sc1 lds\n" \
"v_mfma_f32_16x16x32_bf16 v[200:203], a[96:99], v[100:103], v[200:203]\n" \
"s_waitcnt lgkmcnt(4)\n" \
"ds_read_b128 v[96:99], v30 offset:16384\n" \
"v_cvt_pk_bf16_f32 v189, v188, v189\n" \
"v_mfma_f32_16x16x32_bf16 v[204:207], a[100:103], v[100:103], v[204:207]\n" \
"v_lshlrev_b32 v188, 16, v189\n" \
"v_and_b32 v189, 0xffff0000, v189\n" \
"v_mfma_f32_16x16x32_bf16 v[192:195], a[104:107], v[104:107], v[192:195]\n" \
"ds_read_b128 v[100:103], v30 offset:17408\n" \
"v_mul_f32 v188, v179, v188\n" \
"v_mfma_f32_16x16x32_bf16 v[196:199], a[108:111], v[104:107], v[196:199]\n" \
"buffer_load_dwordx4 v31, s[36:39], s80 offen offset:3072 sc1 lds\n" \
"v_mfma_f32_16x16x32_bf16 v[200:203], a[104:107], v[108:111], v[200:203]\n" \
"s_waitcnt lgkmcnt(4)\n" \
"ds_read_b128 v[104:107], v30 offset:18432\n" \
"v_mul_f32 v189, v179, v189\n" \
"v_mfma_f32_16x16x32_bf16 v[204:207], a[108:111], v[108:111], v[204:207]\n" \
"v_cvt_pk_bf16_f32 v157, v188, v189\n" \
"v_lshlrev_b32 v188, 16, v158\n" \
"v_mfma_f32_16x16x32_bf16 v[192:195], a[112:115], v[112:115], v[192:195]\n" \
"ds_read_b128 v[108:111], v30 offset:19456\n" \
"v_and_b32 v189, 0xffff0000, v158\n" \
"v_mfma_f32_16x16x32_bf16 v[196:199], a[116:119], v[112:115], v[196:199]\n" \
"v_lshlrev_b32 v190, 16, v142\n" \
"v_and_b32 v191, 0xffff0000, v142\n" \
"v_mfma_f32_16x16x32_bf16 v[200:203], a[112:115], v[116:119], v[200:203]\n" \
"s_waitcnt lgkmcnt(4)\n" \
"ds_read_b128 v[112:115], v30 offset:20480\n" \
"v_mul_f32 v188, v188, v190\n" \
"v_mfma_f32_16x16x32_bf16 v[204:207], a[116:119], v[116:119], v[204:207]\n" \
"v_mul_f32 v189, v189, v191\n" \
"v_cvt_pk_bf16_f32 v189, v188, v189\n" \
"v_mfma_f32_16x16x32_bf16 v[192:195], a[120:123], v[120:123], v[192:195]\n" \
"ds_read_b128 v[116:119], v30 offset:21504\n" \
"v_lshlrev_b32 v188, 16, v189\n" \
"v_mfma_f32_16x16x32_bf16 v[196:199], a[124:127], v[120:123], v[196:199]\n" \
"v_and_b32 v189, 0xffff0000, v189\n" \
"v_mul_f32 v188, v179, v188\n" \
"v_mfma_f32_16x16x32_bf16 v[200:203], a[120:123], v[124:127], v[200:203]\n" \
"s_waitcnt lgkmcnt(4)\n" \
"ds_read_b128 v[120:123], v30 offset:22528\n" \
"v_mul_f32 v189, v179, v189\n" \
"v_mfma_f32_16x16x32_bf16 v[204:207], a[124:127], v[124:127], v[204:207]\n" \
"s_add_u32 s80, s70, 49152\n" \
"s_add_u32 m0, s22, 32768\n" \
"v_cvt_pk_bf16_f32 v158, v188, v189\n" \
"v_mfma_f32_16x16x32_bf16 v[208:211], a[160:163], v[96:99], 0\n" \
"ds_read_b128 v[124:127], v30 offset:23552\n" \
"v_lshlrev_b32 v188, 16, v159\n" \
"v_mfma_f32_16x16x32_bf16 v[212:215], a[164:167], v[96:99], 0\n" \
"v_and_b32 v189, 0xffff0000, v159\n" \
"v_lshlrev_b32 v190, 16, v143\n" \
"v_mfma_f32_16x16x32_bf16 v[216:219], a[160:163], v[100:103], 0\n" \
"s_waitcnt lgkmcnt(4)\n" \
"ds_read_b128 v[96:99], v30 offset:24576\n" \
"v_cvt_pk_bf16_f32 v192, v192, v193\n" \
"v_mfma_f32_16x16x32_bf16 v[220:223], a[164:167], v[100:103], 0\n" \
"v_cvt_pk_bf16_f32 v193, v194, v195\n" \
"v_cvt_pk_bf16_f32 v194, v196, v197\n" \
"v_mfma_f32_16x16x32_bf16 v[208:211], a[168:171], v[104:107], v[208:211]\n" \
"ds_read_b128 v[100:103], v30 offset:25600\n" \
"v_cvt_pk_bf16_f32 v195, v198, v199\n" \
"v_mfma_f32_16x16x32_bf16 v[212:215], a[172:175], v[104:107], v[212:215]\n" \
"buffer_store_dwordx4 v[192:195], v4, s[36:39], s75 offen offset:0 sc1\n" \
"v_mfma_f32_16x16x32_bf16 v[216:219], a[168:171], v[108:111], v[216:219]\n" \
"s_waitcnt lgkmcnt(4)\n" \
"ds_read_b128 v[104:107], v30 offset:26624\n" \
"v_cvt_pk_bf16_f32 v200, v200, v201\n" \
"v_mfma_f32_16x16x32_bf16 v[220:223], a[172:175], v[108:111], v[220:223]\n" \
"buffer_load_dwordx4 v31, s[36:39], s80 offen offset:0 sc1 lds\n" \
"v_mfma_f32_16x16x32_bf16 v[208:211], a[176:179], v[112:115], v[208:211]\n" \
"ds_read_b128 v[108:111], v30 offset:27648\n" \
"v_cvt_pk_bf16_f32 v201, v202, v203\n" \
"v_mfma_f32_16x16x32_bf16 v[212:215], a[180:183], v[112:115], v[212:215]\n" \
"v_cvt_pk_bf16_f32 v202, v204, v205\n" \
"v_cvt_pk_bf16_f32 v203, v206, v207\n" \
"v_mfma_f32_16x16x32_bf16 v[216:219], a[176:179], v[116:119], v[216:219]\n" \
"s_waitcnt lgkmcnt(4)\n" \
"ds_read_b128 v[112:115], v30 offset:28672\n" \
"v_and_b32 v191, 0xffff0000, v143\n" \
"v_mfma_f32_16x16x32_bf16 v[220:223], a[180:183], v[116:119], v[220:223]\n" \
"buffer_load_dwordx4 v31, s[36:39], s80 offen offset:1024 sc1 lds\n" \
"v_mfma_f32_16x16x32_bf16 v[208:211], a[184:187], v[120:123], v[208:211]\n" \
"ds_read_b128 v[116:119], v30 offset:29696\n" \
"v_mul_f32 v188, v188, v190\n" \
"v_mfma_f32_16x16x32_bf16 v[212:215], a[188:191], v[120:123], v[212:215]\n" \
"buffer_store_dwordx4 v[200:203], v4, s[36:39], s75 offen offset:1024 sc1\n" \
"v_mfma_f32_16x16x32_bf16 v[216:219], a[184:187], v[124:127], v[216:219]\n" \
"ds_read_b128 v[120:123], v30 offset:30720\n" \
"v_mul_f32 v189, v189, v191\n" \
"v_mfma_f32_16x16x32_bf16 v[220:223], a[188:191], v[124:127], v[220:223]\n" \
"s_waitcnt vmcnt(28) lgkmcnt(5)\n" \
"s_barrier\n" \
"v_cvt_pk_bf16_f32 v189, v188, v189\n" \
"v_mfma_f32_16x16x32_bf16 v[208:211], a[192:195], v[96:99], v[208:211]\n" \
"ds_read_b128 v[124:127], v30 offset:31744\n" \
"v_lshlrev_b32 v188, 16, v189\n" \
"v_mfma_f32_16x16x32_bf16 v[212:215], a[196:199], v[96:99], v[212:215]\n" \
"buffer_load_dwordx4 v31, s[36:39], s80 offen offset:2048 sc1 lds\n" \
"v_mfma_f32_16x16x32_bf16 v[216:219], a[192:195], v[100:103], v[216:219]\n" \
"s_waitcnt lgkmcnt(4)\n" \
"ds_read_b128 v[96:99], v28 offset:0\n" \
"v_and_b32 v189, 0xffff0000, v189\n" \
"v_mfma_f32_16x16x32_bf16 v[220:223], a[196:199], v[100:103], v[220:223]\n" \
"v_mul_f32 v188, v179, v188\n" \
"v_mul_f32 v189, v179, v189\n" \
"v_mfma_f32_16x16x32_bf16 v[208:211], a[200:203], v[104:107], v[208:211]\n" \
"ds_read_b128 v[100:103], v28 offset:1024\n" \
"v_cvt_pk_bf16_f32 v159, v188, v189\n" \
"v_mfma_f32_16x16x32_bf16 v[212:215], a[204:207], v[104:107], v[212:215]\n" \
"buffer_load_dwordx4 v31, s[36:39], s80 offen offset:3072 sc1 lds\n" \
"v_mfma_f32_16x16x32_bf16 v[216:219], a[200:203], v[108:111], v[216:219]\n" \
"s_waitcnt lgkmcnt(4)\n" \
"ds_read_b128 v[104:107], v28 offset:2048\n" \
"v_lshlrev_b32 v32, 16, v144\n" \
"v_mfma_f32_16x16x32_bf16 v[220:223], a[204:207], v[108:111], v[220:223]\n" \
"v_and_b32 v33, 0xffff0000, v144\n" \
"v_lshlrev_b32 v34, 16, v145\n" \
"v_mfma_f32_16x16x32_bf16 v[208:211], a[208:211], v[112:115], v[208:211]\n" \
"ds_read_b128 v[108:111], v28 offset:3072\n" \
"v_and_b32 v35, 0xffff0000, v145\n" \
"v_mfma_f32_16x16x32_bf16 v[212:215], a[212:215], v[112:115], v[212:215]\n" \
"v_lshlrev_b32 v36, 16, v146\n" \
"v_and_b32 v37, 0xffff0000, v146\n" \
"v_mfma_f32_16x16x32_bf16 v[216:219], a[208:211], v[116:119], v[216:219]\n" \
"s_waitcnt lgkmcnt(4)\n" \
"ds_read_b128 v[112:115], v28 offset:4096\n" \
"v_lshlrev_b32 v38, 16, v147\n" \
"v_mfma_f32_16x16x32_bf16 v[220:223], a[212:215], v[116:119], v[220:223]\n" \
"v_and_b32 v39, 0xffff0000, v147\n" \
"v_lshlrev_b32 v40, 16, v148\n" \
"v_mfma_f32_16x16x32_bf16 v[208:211], a[216:219], v[120:123], v[208:211]\n" \
"ds_read_b128 v[116:119], v28 offset:5120\n" \
"v_and_b32 v41, 0xffff0000, v148\n" \
"v_mfma_f32_16x16x32_bf16 v[212:215], a[220:223], v[120:123], v[212:215]\n" \
"v_lshlrev_b32 v42, 16, v149\n" \
"v_and_b32 v43, 0xffff0000, v149\n" \
"v_mfma_f32_16x16x32_bf16 v[216:219], a[216:219], v[124:127], v[216:219]\n" \
"s_waitcnt lgkmcnt(4)\n" \
"ds_read_b128 v[120:123], v28 offset:6144\n" \
"v_lshlrev_b32 v44, 16, v150\n" \
"v_mfma_f32_16x16x32_bf16 v[220:223], a[220:223], v[124:127], v[220:223]\n" \
"s_add_u32 s80, s71, 0\n" \
"s_add_u32 m0, s22, 81920\n" \
"v_and_b32 v45, 0xffff0000, v150\n" \
"v_mfma_f32_16x16x32_bf16 v[208:211], a[224:227], v[96:99], v[208:211]\n" \
"ds_read_b128 v[124:127], v28 offset:7168\n" \
"v_lshlrev_b32 v46, 16, v151\n" \
"v_mfma_f32_16x16x32_bf16 v[212:215], a[228:231], v[96:99], v[212:215]\n" \
"v_and_b32 v47, 0xffff0000, v151\n" \
"v_lshlrev_b32 v48, 16, v152\n" \
"v_mfma_f32_16x16x32_bf16 v[216:219], a[224:227], v[100:103], v[216:219]\n" \
"s_waitcnt lgkmcnt(4)\n" \
"ds_read_b128 v[96:99], v28 offset:8192\n" \
"v_and_b32 v49, 0xffff0000, v152\n" \
"v_mfma_f32_16x16x32_bf16 v[220:223], a[228:231], v[100:103], v[220:223]\n" \
"v_lshlrev_b32 v50, 16, v153\n" \
"v_and_b32 v51, 0xffff0000, v153\n" \
"v_mfma_f32_16x16x32_bf16 v[208:211], a[232:235], v[104:107], v[208:211]\n" \
"ds_read_b128 v[100:103], v28 offset:9216\n" \
"v_lshlrev_b32 v52, 16, v154\n" \
"v_mfma_f32_16x16x32_bf16 v[212:215], a[236:239], v[104:107], v[212:215]\n" \
"v_and_b32 v53, 0xffff0000, v154\n" \
"v_lshlrev_b32 v54, 16, v155\n" \
"v_mfma_f32_16x16x32_bf16 v[216:219], a[232:235], v[108:111], v[216:219]\n" \
"s_waitcnt lgkmcnt(4)\n" \
"ds_read_b128 v[104:107], v28 offset:10240\n" \
"v_and_b32 v55, 0xffff0000, v155\n" \
"v_mfma_f32_16x16x32_bf16 v[220:223], a[236:239], v[108:111], v[220:223]\n" \
"buffer_load_dwordx4 v31, s[36:39], s80 offen offset:0 sc1 lds\n" \
"v_mfma_f32_16x16x32_bf16 v[208:211], a[240:243], v[112:115], v[208:211]\n" \
"ds_read_b128 v[108:111], v28 offset:11264\n" \
"v_lshlrev_b32 v56, 16, v156\n" \
"v_mfma_f32_16x16x32_bf16 v[212:215], a[244:247], v[112:115], v[212:215]\n" \
"v_and_b32 v57, 0xffff0000, v156\n" \
"v_lshlrev_b32 v58, 16, v157\n" \
"v_mfma_f32_16x16x32_bf16 v[216:219], a[240:243], v[116:119], v[216:219]\n" \
"s_waitcnt lgkmcnt(4)\n" \
"ds_read_b128 v[112:115], v28 offset:12288\n" \
"v_and_b32 v59, 0xffff0000, v157\n" \
"v_mfma_f32_16x16x32_bf16 v[220:223], a[244:247], v[116:119], v[220:223]\n" \
"buffer_load_dwordx4 v31, s[36:39], s80 offen offset:1024 sc1 lds\n" \
"v_mfma_f32_16x16x32_bf16 v[208:211], a[248:251], v[120:123], v[208:211]\n" \
"ds_read_b128 v[116:119], v28 offset:13312\n" \
"v_lshlrev_b32 v60, 16, v158\n" \
"v_mfma_f32_16x16x32_bf16 v[212:215], a[252:255], v[120:123], v[212:215]\n" \
"v_and_b32 v61, 0xffff0000, v158\n" \
"v_lshlrev_b32 v62, 16, v159\n" \
"v_mfma_f32_16x16x32_bf16 v[216:219], a[248:251], v[124:127], v[216:219]\n" \
"ds_read_b128 v[120:123], v28 offset:14336\n" \
"v_and_b32 v63, 0xffff0000, v159\n" \
"v_mfma_f32_16x16x32_bf16 v[220:223], a[252:255], v[124:127], v[220:223]\n" \
"s_waitcnt vmcnt(26) lgkmcnt(5)\n" \
"s_barrier\n" \
"v_mfma_f32_16x16x32_bf16 v[208:211], a[64:67], v[96:99], v[208:211]\n" \
"s_sub_u32 s96, s88, 94404608\n" \
"ds_read_b128 v[124:127], v28 offset:15360\n" \
"s_lshl_b32 s96, s96, 2\n" \
"v_mfma_f32_16x16x32_bf16 v[212:215], a[68:71], v[96:99], v[212:215]\n" \
"buffer_load_dwordx4 v31, s[36:39], s80 offen offset:2048 sc1 lds\n" \
"v_mfma_f32_16x16x32_bf16 v[216:219], a[64:67], v[100:103], v[216:219]\n" \
"s_waitcnt lgkmcnt(4)\n" \
"ds_read_b128 v[96:99], v29 offset:32768\n" \
"s_add_u32 s96, s96, 94437376\n" \
"v_mfma_f32_16x16x32_bf16 v[220:223], a[68:71], v[100:103], v[220:223]\n" \
"v_bfe_u32 v17, v4, 4, 2\n" \
"v_min_u32 v17, 2, v17\n" \
"v_mfma_f32_16x16x32_bf16 v[208:211], a[72:75], v[104:107], v[208:211]\n" \
"ds_read_b128 v[100:103], v29 offset:33792\n" \
"v_lshl_add_u32 v17, v17, 3, s96\n" \
"v_mfma_f32_16x16x32_bf16 v[212:215], a[76:79], v[104:107], v[212:215]\n" \
"buffer_load_dwordx4 v31, s[36:39], s80 offen offset:3072 sc1 lds\n" \
"v_mfma_f32_16x16x32_bf16 v[216:219], a[72:75], v[108:111], v[216:219]\n" \
"s_waitcnt lgkmcnt(4)\n" \
"ds_read_b128 v[104:107], v29 offset:34816\n" \
"v_mfma_f32_16x16x32_bf16 v[220:223], a[76:79], v[108:111], v[220:223]\n" \
"v_mfma_f32_16x16x32_bf16 v[208:211], a[80:83], v[112:115], v[208:211]\n" \
"ds_read_b128 v[108:111], v29 offset:35840\n" \
"v_mfma_f32_16x16x32_bf16 v[212:215], a[84:87], v[112:115], v[212:215]\n" \
"v_mfma_f32_16x16x32_bf16 v[216:219], a[80:83], v[116:119], v[216:219]\n" \
"s_waitcnt lgkmcnt(4)\n" \
"ds_read_b128 v[112:115], v29 offset:36864\n" \
"v_mfma_f32_16x16x32_bf16 v[220:223], a[84:87], v[116:119], v[220:223]\n" \
"v_mfma_f32_16x16x32_bf16 v[208:211], a[88:91], v[120:123], v[208:211]\n" \
"ds_read_b128 v[116:119], v29 offset:37888\n" \
"v_mfma_f32_16x16x32_bf16 v[212:215], a[92:95], v[120:123], v[212:215]\n" \
"buffer_load_dwordx2 v[18:19], v17, s[36:39], 0 offen sc1\n" \
"v_mfma_f32_16x16x32_bf16 v[216:219], a[88:91], v[124:127], v[216:219]\n" \
"s_waitcnt lgkmcnt(4)\n" \
"ds_read_b128 v[120:123], v29 offset:38912\n" \
"v_mfma_f32_16x16x32_bf16 v[220:223], a[92:95], v[124:127], v[220:223]\n" \
"v_mfma_f32_16x16x32_bf16 v[208:211], a[128:131], v[96:99], v[208:211]\n" \
"ds_read_b128 v[124:127], v29 offset:39936\n" \
"v_mfma_f32_16x16x32_bf16 v[212:215], a[132:135], v[96:99], v[212:215]\n" \
"v_mfma_f32_16x16x32_bf16 v[216:219], a[128:131], v[100:103], v[216:219]\n" \
"s_waitcnt lgkmcnt(4)\n" \
"ds_read_b128 v[96:99], v29 offset:40960\n" \
"v_mfma_f32_16x16x32_bf16 v[220:223], a[132:135], v[100:103], v[220:223]\n" \
"v_mfma_f32_16x16x32_bf16 v[208:211], a[136:139], v[104:107], v[208:211]\n" \
"ds_read_b128 v[100:103], v29 offset:41984\n" \
"v_mfma_f32_16x16x32_bf16 v[212:215], a[140:143], v[104:107], v[212:215]\n" \
"v_mfma_f32_16x16x32_bf16 v[216:219], a[136:139], v[108:111], v[216:219]\n" \
"s_waitcnt lgkmcnt(4)\n" \
"ds_read_b128 v[104:107], v29 offset:43008\n" \
"v_mfma_f32_16x16x32_bf16 v[220:223], a[140:143], v[108:111], v[220:223]\n" \
"v_mfma_f32_16x16x32_bf16 v[208:211], a[144:147], v[112:115], v[208:211]\n" \
"ds_read_b128 v[108:111], v29 offset:44032\n" \
"v_mfma_f32_16x16x32_bf16 v[212:215], a[148:151], v[112:115], v[212:215]\n" \
"v_mfma_f32_16x16x32_bf16 v[216:219], a[144:147], v[116:119], v[216:219]\n" \
"s_waitcnt lgkmcnt(4)\n" \
"ds_read_b128 v[112:115], v29 offset:45056\n" \
"v_mfma_f32_16x16x32_bf16 v[220:223], a[148:151], v[116:119], v[220:223]\n" \
"s_add_u32 s80, s71, 16384\n" \
"s_add_u32 m0, s22, 49152\n" \
"v_mfma_f32_16x16x32_bf16 v[208:211], a[152:155], v[120:123], v[208:211]\n" \
"ds_read_b128 v[116:119], v29 offset:46080\n" \
"v_mfma_f32_16x16x32_bf16 v[212:215], a[156:159], v[120:123], v[212:215]\n" \
"v_mfma_f32_16x16x32_bf16 v[216:219], a[152:155], v[124:127], v[216:219]\n" \
"ds_read_b128 v[120:123], v29 offset:47104\n" \
"v_mfma_f32_16x16x32_bf16 v[220:223], a[156:159], v[124:127], v[220:223]\n" \
"s_waitcnt vmcnt(24) lgkmcnt(5)\n" \
"s_barrier\n" \
"v_mfma_f32_16x16x32_bf16 v[208:211], a[0:3], v[96:99], v[208:211]\n" \
"ds_read_b128 v[124:127], v29 offset:48128\n" \
"v_mfma_f32_16x16x32_bf16 v[212:215], a[4:7], v[96:99], v[212:215]\n" \
"v_mfma_f32_16x16x32_bf16 v[216:219], a[0:3], v[100:103], v[216:219]\n" \
"s_waitcnt lgkmcnt(4)\n" \
"ds_read_b128 v[96:99], v29 offset:49152\n" \
"v_mfma_f32_16x16x32_bf16 v[220:223], a[4:7], v[100:103], v[220:223]\n" \
"buffer_load_dwordx4 v31, s[36:39], s80 offen offset:0 sc1 lds\n" \
"v_mfma_f32_16x16x32_bf16 v[208:211], a[8:11], v[104:107], v[208:211]\n" \
"ds_read_b128 v[100:103], v29 offset:50176\n" \
"v_mfma_f32_16x16x32_bf16 v[212:215], a[12:15], v[104:107], v[212:215]\n" \
"v_mfma_f32_16x16x32_bf16 v[216:219], a[8:11], v[108:111], v[216:219]\n" \
"s_waitcnt lgkmcnt(4)\n" \
"ds_read_b128 v[104:107], v29 offset:51200\n" \
"v_mfma_f32_16x16x32_bf16 v[220:223], a[12:15], v[108:111], v[220:223]\n" \
"buffer_load_dwordx4 v31, s[36:39], s80 offen offset:1024 sc1 lds\n" \
"v_mfma_f32_16x16x32_bf16 v[208:211], a[16:19], v[112:115], v[208:211]\n" \
"ds_read_b128 v[108:111], v29 offset:52224\n" \
"v_mfma_f32_16x16x32_bf16 v[212:215], a[20:23], v[112:115], v[212:215]\n" \
"v_mfma_f32_16x16x32_bf16 v[216:219], a[16:19], v[116:119], v[216:219]\n" \
"s_waitcnt lgkmcnt(4)\n" \
"ds_read_b128 v[112:115], v29 offset:53248\n" \
"v_mfma_f32_16x16x32_bf16 v[220:223], a[20:23], v[116:119], v[220:223]\n" \
"buffer_load_dwordx4 v31, s[36:39], s80 offen offset:2048 sc1 lds\n" \
"v_mfma_f32_16x16x32_bf16 v[208:211], a[24:27], v[120:123], v[208:211]\n" \
"ds_read_b128 v[116:119], v29 offset:54272\n" \
"v_mfma_f32_16x16x32_bf16 v[212:215], a[28:31], v[120:123], v[212:215]\n" \
"v_mfma_f32_16x16x32_bf16 v[216:219], a[24:27], v[124:127], v[216:219]\n" \
"s_waitcnt lgkmcnt(4)\n" \
"ds_read_b128 v[120:123], v29 offset:55296\n" \
"v_mfma_f32_16x16x32_bf16 v[220:223], a[28:31], v[124:127], v[220:223]\n" \
"buffer_load_dwordx4 v31, s[36:39], s80 offen offset:3072 sc1 lds\n" \
"v_mfma_f32_16x16x32_bf16 v[208:211], a[32:35], v[96:99], v[208:211]\n" \
"ds_read_b128 v[124:127], v29 offset:56320\n" \
"v_mfma_f32_16x16x32_bf16 v[212:215], a[36:39], v[96:99], v[212:215]\n" \
"v_mfma_f32_16x16x32_bf16 v[216:219], a[32:35], v[100:103], v[216:219]\n" \
"s_waitcnt lgkmcnt(4)\n" \
"ds_read_b128 v[96:99], v29 offset:57344\n" \
"v_mfma_f32_16x16x32_bf16 v[220:223], a[36:39], v[100:103], v[220:223]\n" \
"v_mfma_f32_16x16x32_bf16 v[208:211], a[40:43], v[104:107], v[208:211]\n" \
"ds_read_b128 v[100:103], v29 offset:58368\n" \
"v_mfma_f32_16x16x32_bf16 v[212:215], a[44:47], v[104:107], v[212:215]\n" \
"v_mfma_f32_16x16x32_bf16 v[216:219], a[40:43], v[108:111], v[216:219]\n" \
"s_waitcnt lgkmcnt(4)\n" \
"ds_read_b128 v[104:107], v29 offset:59392\n" \
"v_mfma_f32_16x16x32_bf16 v[220:223], a[44:47], v[108:111], v[220:223]\n" \
"v_mfma_f32_16x16x32_bf16 v[208:211], a[48:51], v[112:115], v[208:211]\n" \
"ds_read_b128 v[108:111], v29 offset:60416\n" \
"v_mfma_f32_16x16x32_bf16 v[212:215], a[52:55], v[112:115], v[212:215]\n" \
"v_mfma_f32_16x16x32_bf16 v[216:219], a[48:51], v[116:119], v[216:219]\n" \
"s_waitcnt lgkmcnt(4)\n" \
"ds_read_b128 v[112:115], v29 offset:61440\n" \
"s_waitcnt vmcnt(4)\n" \
"v_mfma_f32_16x16x32_bf16 v[220:223], a[52:55], v[116:119], v[220:223]\n" \
"s_add_u32 s80, s71, 32768\n" \
"v_cmp_ne_u64 vcc, s[14:15], v[18:19]\n" \
"s_nop 0\n" \
"v_mfma_f32_16x16x32_bf16 v[208:211], a[56:59], v[120:123], v[208:211]\n" \
"ds_read_b128 v[116:119], v29 offset:62464\n" \
"s_cbranch_vccnz LPE_%=\nLPER_%=:\n" \
"v_mfma_f32_16x16x32_bf16 v[212:215], a[60:63], v[120:123], v[212:215]\n" \
"s_add_u32 m0, s22, 147456\n" \
"s_add_u32 s97, s78, 0\n" \
"v_mfma_f32_16x16x32_bf16 v[216:219], a[56:59], v[124:127], v[216:219]\n" \
"ds_read_b128 v[120:123], v29 offset:63488\n" \
"v_mfma_f32_16x16x32_bf16 v[220:223], a[60:63], v[124:127], v[220:223]\n" \
"s_waitcnt vmcnt(24) lgkmcnt(5)\n" \
"s_barrier\n" \
"v_mfma_f32_16x16x32_bf16 v[208:211], a[96:99], v[96:99], v[208:211]\n" \
"ds_read_b128 v[124:127], v29 offset:64512\n" \
"v_mfma_f32_16x16x32_bf16 v[212:215], a[100:103], v[96:99], v[212:215]\n" \
"v_mfma_f32_16x16x32_bf16 v[216:219], a[96:99], v[100:103], v[216:219]\n" \
"s_waitcnt lgkmcnt(4)\n" \
"ds_read_b128 v[96:99], v28 offset:16384\n" \
"v_mfma_f32_16x16x32_bf16 v[220:223], a[100:103], v[100:103], v[220:223]\n" \
"buffer_load_dwordx4 v31, s[36:39], s80 offen offset:0 sc1 lds\n" \
"v_mfma_f32_16x16x32_bf16 v[208:211], a[104:107], v[104:107], v[208:211]\n" \
"ds_read_b128 v[100:103], v28 offset:17408\n" \
"v_mfma_f32_16x16x32_bf16 v[212:215], a[108:111], v[104:107], v[212:215]\n" \
"v_mfma_f32_16x16x32_bf16 v[216:219], a[104:107], v[108:111], v[216:219]\n" \
"s_waitcnt lgkmcnt(4)\n" \
"ds_read_b128 v[104:107], v28 offset:18432\n" \
"v_mfma_f32_16x16x32_bf16 v[220:223], a[108:111], v[108:111], v[220:223]\n" \
"buffer_load_dwordx4 v31, s[36:39], s80 offen offset:1024 sc1 lds\n" \
"v_mfma_f32_16x16x32_bf16 v[208:211], a[112:115], v[112:115], v[208:211]\n" \
"ds_read_b128 v[108:111], v28 offset:19456\n" \
"v_mfma_f32_16x16x32_bf16 v[212:215], a[116:119], v[112:115], v[212:215]\n" \
"v_mfma_f32_16x16x32_bf16 v[216:219], a[112:115], v[116:119], v[216:219]\n" \
"s_waitcnt lgkmcnt(4)\n" \
"ds_read_b128 v[112:115], v28 offset:20480\n" \
"v_mfma_f32_16x16x32_bf16 v[220:223], a[116:119], v[116:119], v[220:223]\n" \
"buffer_load_dwordx4 v31, s[36:39], s80 offen offset:2048 sc1 lds\n" \
"v_mfma_f32_16x16x32_bf16 v[208:211], a[120:123], v[120:123], v[208:211]\n" \
"ds_read_b128 v[116:119], v28 offset:21504\n" \
"v_mfma_f32_16x16x32_bf16 v[212:215], a[124:127], v[120:123], v[212:215]\n" \
"v_mfma_f32_16x16x32_bf16 v[216:219], a[120:123], v[124:127], v[216:219]\n" \
"s_waitcnt lgkmcnt(4)\n" \
"ds_read_b128 v[120:123], v28 offset:22528\n" \
"v_mfma_f32_16x16x32_bf16 v[220:223], a[124:127], v[124:127], v[220:223]\n" \
"buffer_load_dwordx4 v31, s[36:39], s80 offen offset:3072 sc1 lds\n" \
"v_mfma_f32_16x16x32_bf16 v[192:195], a[160:163], v[96:99], 0\n" \
"ds_read_b128 v[124:127], v28 offset:23552\n" \
"v_mfma_f32_16x16x32_bf16 v[196:199], a[164:167], v[96:99], 0\n" \
"v_mfma_f32_16x16x32_bf16 v[200:203], a[160:163], v[100:103], 0\n" \
"s_waitcnt lgkmcnt(4)\n" \
"ds_read_b128 v[96:99], v28 offset:24576\n" \
"v_cvt_pk_bf16_f32 v208, v208, v209\n" \
"v_mfma_f32_16x16x32_bf16 v[204:207], a[164:167], v[100:103], 0\n" \
"buffer_load_dwordx4 v[128:131], v4, s[36:39], s97 offen offset:0 sc1\n" \
"v_mfma_f32_16x16x32_bf16 v[192:195], a[168:171], v[104:107], v[192:195]\n" \
"ds_read_b128 v[100:103], v28 offset:25600\n" \
"v_cvt_pk_bf16_f32 v209, v210, v211\n" \
"v_mfma_f32_16x16x32_bf16 v[196:199], a[172:175], v[104:107], v[196:199]\n" \
"v_cvt_pk_bf16_f32 v210, v212, v213\n" \
"v_cvt_pk_bf16_f32 v211, v214, v215\n" \
"v_mfma_f32_16x16x32_bf16 v[200:203], a[168:171], v[108:111], v[200:203]\n" \
"s_waitcnt lgkmcnt(4)\n" \
"ds_read_b128 v[104:107], v28 offset:26624\n" \
"v_cvt_pk_bf16_f32 v216, v216, v217\n" \
"v_mfma_f32_16x16x32_bf16 v[204:207], a[172:175], v[108:111], v[204:207]\n" \
"buffer_store_dwordx4 v[208:211], v4, s[36:39], s76 offen offset:0 sc1\n" \
"v_mfma_f32_16x16x32_bf16 v[192:195], a[176:179], v[112:115], v[192:195]\n" \
"ds_read_b128 v[108:111], v28 offset:27648\n" \
"v_cvt_pk_bf16_f32 v217, v218, v219\n" \
"v_mfma_f32_16x16x32_bf16 v[196:199], a[180:183], v[112:115], v[196:199]\n" \
"buffer_load_dwordx4 v[132:135], v4, s[36:39], s97 offen offset:1024 sc1\n" \
"v_mfma_f32_16x16x32_bf16 v[200:203], a[176:179], v[116:119], v[200:203]\n" \
"s_waitcnt lgkmcnt(4)\n" \
"ds_read_b128 v[112:115], v28 offset:28672\n" \
"v_cvt_pk_bf16_f32 v218, v220, v221\n" \
"v_mfma_f32_16x16x32_bf16 v[204:207], a[180:183], v[116:119], v[204:207]\n" \
"s_add_u32 s80, s71, 49152\n" \
"s_add_u32 m0, s22, 0\n" \
"v_cvt_pk_bf16_f32 v219, v222, v223\n" \
"v_mfma_f32_16x16x32_bf16 v[192:195], a[184:187], v[120:123], v[192:195]\n" \
"ds_read_b128 v[116:119], v28 offset:29696\n" \
"v_mfma_f32_16x16x32_bf16 v[196:199], a[188:191], v[120:123], v[196:199]\n" \
"buffer_load_dwordx4 v[136:139], v4, s[36:39], s97 offen offset:2048 sc1\n" \
"v_mfma_f32_16x16x32_bf16 v[200:203], a[184:187], v[124:127], v[200:203]\n" \
"ds_read_b128 v[120:123], v28 offset:30720\n" \
"v_mfma_f32_16x16x32_bf16 v[204:207], a[188:191], v[124:127], v[204:207]\n" \
"s_waitcnt vmcnt(28) lgkmcnt(5)\n" \
"s_barrier\n" \
"v_mfma_f32_16x16x32_bf16 v[192:195], a[192:195], v[96:99], v[192:195]\n" \
"ds_read_b128 v[124:127], v28 offset:31744\n" \
"v_mfma_f32_16x16x32_bf16 v[196:199], a[196:199], v[96:99], v[196:199]\n" \
"buffer_store_dwordx4 v[216:219], v4, s[36:39], s76 offen offset:1024 sc1\n" \
"v_mfma_f32_16x16x32_bf16 v[200:203], a[192:195], v[100:103], v[200:203]\n" \
"s_waitcnt lgkmcnt(4)\n" \
"ds_read_b128 v[96:99], v29 offset:0\n" \
"v_mfma_f32_16x16x32_bf16 v[204:207], a[196:199], v[100:103], v[204:207]\n" \
"buffer_load_dwordx4 v31, s[36:39], s80 offen offset:0 sc1 lds\n" \
"v_mfma_f32_16x16x32_bf16 v[192:195], a[200:203], v[104:107], v[192:195]\n" \
"ds_read_b128 v[100:103], v29 offset:1024\n" \
"v_mfma_f32_16x16x32_bf16 v[196:199], a[204:207], v[104:107], v[196:199]\n" \
"v_mfma_f32_16x16x32_bf16 v[200:203], a[200:203], v[108:111], v[200:203]\n" \
"s_waitcnt lgkmcnt(4)\n" \
"ds_read_b128 v[104:107], v29 offset:2048\n" \
"v_mfma_f32_16x16x32_bf16 v[204:207], a[204:207], v[108:111], v[204:207]\n" \
"buffer_load_dwordx4 v31, s[36:39], s80 offen offset:1024 sc1 lds\n" \
"v_mfma_f32_16x16x32_bf16 v[192:195], a[208:211], v[112:115], v[192:195]\n" \
"ds_read_b128 v[108:111], v29 offset:3072\n" \
"v_mfma_f32_16x16x32_bf16 v[196:199], a[212:215], v[112:115], v[196:199]\n" \
"v_mfma_f32_16x16x32_bf16 v[200:203], a[208:211], v[116:119], v[200:203]\n" \
"s_waitcnt lgkmcnt(4)\n" \
"ds_read_b128 v[112:115], v29 offset:4096\n" \
"v_mfma_f32_16x16x32_bf16 v[204:207], a[212:215], v[116:119], v[204:207]\n" \
"buffer_load_dwordx4 v31, s[36:39], s80 offen offset:2048 sc1 lds\n" \
"v_mfma_f32_16x16x32_bf16 v[192:195], a[216:219], v[120:123], v[192:195]\n" \
"ds_read_b128 v[116:119], v29 offset:5120\n" \
"v_mfma_f32_16x16x32_bf16 v[196:199], a[220:223], v[120:123], v[196:199]\n" \
"v_mfma_f32_16x16x32_bf16 v[200:203], a[216:219], v[124:127], v[200:203]\n" \
"s_waitcnt lgkmcnt(4)\n" \
"ds_read_b128 v[120:123], v29 offset:6144\n" \
"v_mfma_f32_16x16x32_bf16 v[204:207], a[220:223], v[124:127], v[204:207]\n" \
"buffer_load_dwordx4 v31, s[36:39], s80 offen offset:3072 sc1 lds\n" \
"v_mfma_f32_16x16x32_bf16 v[192:195], a[224:227], v[96:99], v[192:195]\n" \
"ds_read_b128 v[124:127], v29 offset:7168\n" \
"v_mfma_f32_16x16x32_bf16 v[196:199], a[228:231], v[96:99], v[196:199]\n" \
"v_mfma_f32_16x16x32_bf16 v[200:203], a[224:227], v[100:103], v[200:203]\n" \
"s_waitcnt lgkmcnt(4)\n" \
"ds_read_b128 v[96:99], v29 offset:8192\n" \
"v_mfma_f32_16x16x32_bf16 v[204:207], a[228:231], v[100:103], v[204:207]\n" \
"buffer_load_dwordx4 v[140:143], v4, s[36:39], s97 offen offset:3072 sc1\n" \
"v_mfma_f32_16x16x32_bf16 v[192:195], a[232:235], v[104:107], v[192:195]\n" \
"s_add_u32 s97, s78, 4096\n" \
"ds_read_b128 v[100:103], v29 offset:9216\n" \
"v_mfma_f32_16x16x32_bf16 v[196:199], a[236:239], v[104:107], v[196:199]\n" \
"v_mfma_f32_16x16x32_bf16 v[200:203], a[232:235], v[108:111], v[200:203]\n" \
"s_waitcnt lgkmcnt(4)\n" \
"ds_read_b128 v[104:107], v29 offset:10240\n" \
"v_mfma_f32_16x16x32_bf16 v[204:207], a[236:239], v[108:111], v[204:207]\n" \
"buffer_load_dwordx4 v[160:163], v4, s[36:39], s97 offen offset:0 sc1\n" \
"v_mfma_f32_16x16x32_bf16 v[192:195], a[240:243], v[112:115], v[192:195]\n" \
"ds_read_b128 v[108:111], v29 offset:11264\n" \
"v_mfma_f32_16x16x32_bf16 v[196:199], a[244:247], v[112:115], v[196:199]\n" \
"v_mfma_f32_16x16x32_bf16 v[200:203], a[240:243], v[116:119], v[200:203]\n" \
"s_waitcnt lgkmcnt(4)\n" \
"ds_read_b128 v[112:115], v29 offset:12288\n" \
"v_mfma_f32_16x16x32_bf16 v[204:207], a[244:247], v[116:119], v[204:207]\n" \
"buffer_load_dwordx4 v[164:167], v4, s[36:39], s97 offen offset:1024 sc1\n" \
"v_mfma_f32_16x16x32_bf16 v[192:195], a[248:251], v[120:123], v[192:195]\n" \
"ds_read_b128 v[116:119], v29 offset:13312\n" \
"v_mfma_f32_16x16x32_bf16 v[196:199], a[252:255], v[120:123], v[196:199]\n" \
"v_mfma_f32_16x16x32_bf16 v[200:203], a[248:251], v[124:127], v[200:203]\n" \
"ds_read_b128 v[120:123], v29 offset:14336\n" \
"v_mfma_f32_16x16x32_bf16 v[204:207], a[252:255], v[124:127], v[204:207]\n" \
"s_waitcnt vmcnt(31) lgkmcnt(5)\n" \
"s_barrier\n" \
"v_mfma_f32_16x16x32_bf16 v[192:195], a[64:67], v[96:99], v[192:195]\n" \
"ds_read_b128 v[124:127], v29 offset:15360\n" \
"v_mfma_f32_16x16x32_bf16 v[196:199], a[68:71], v[96:99], v[196:199]\n" \
"v_mfma_f32_16x16x32_bf16 v[200:203], a[64:67], v[100:103], v[200:203]\n" \
"s_waitcnt lgkmcnt(4)\n" \
"ds_read_b128 v[96:99], v30 offset:0\n" \
"v_mfma_f32_16x16x32_bf16 v[204:207], a[68:71], v[100:103], v[204:207]\n" \
"v_mfma_f32_16x16x32_bf16 v[192:195], a[72:75], v[104:107], v[192:195]\n" \
"ds_read_b128 v[100:103], v30 offset:1024\n" \
"v_mfma_f32_16x16x32_bf16 v[196:199], a[76:79], v[104:107], v[196:199]\n" \
"v_mfma_f32_16x16x32_bf16 v[200:203], a[72:75], v[108:111], v[200:203]\n" \
"s_waitcnt lgkmcnt(4)\n" \
"ds_read_b128 v[104:107], v30 offset:2048\n" \
"v_mfma_f32_16x16x32_bf16 v[204:207], a[76:79], v[108:111], v[204:207]\n" \
"v_mfma_f32_16x16x32_bf16 v[192:195], a[80:83], v[112:115], v[192:195]\n" \
"ds_read_b128 v[108:111], v30 offset:3072\n" \
"v_mfma_f32_16x16x32_bf16 v[196:199], a[84:87], v[112:115], v[196:199]\n" \
"v_mfma_f32_16x16x32_bf16 v[200:203], a[80:83], v[116:119], v[200:203]\n" \
"s_waitcnt lgkmcnt(4)\n" \
"ds_read_b128 v[112:115], v30 offset:4096\n" \
"v_mfma_f32_16x16x32_bf16 v[204:207], a[84:87], v[116:119], v[204:207]\n" \
"v_mfma_f32_16x16x32_bf16 v[192:195], a[88:91], v[120:123], v[192:195]\n" \
"ds_read_b128 v[116:119], v30 offset:5120\n" \
"v_mfma_f32_16x16x32_bf16 v[196:199], a[92:95], v[120:123], v[196:199]\n" \
"LH25_%=:\ns_cbranch_execz LHR2_%=\n" \
"v_mfma_f32_16x16x32_bf16 v[200:203], a[88:91], v[124:127], v[200:203]\n" \
"s_waitcnt lgkmcnt(4)\n" \
"ds_read_b128 v[120:123], v30 offset:6144\n" \
"v_mfma_f32_16x16x32_bf16 v[204:207], a[92:95], v[124:127], v[204:207]\n" \
"v_mfma_f32_16x16x32_bf16 v[192:195], a[128:131], v[96:99], v[192:195]\n" \
"ds_read_b128 v[124:127], v30 offset:7168\n" \
"v_mfma_f32_16x16x32_bf16 v[196:199], a[132:135], v[96:99], v[196:199]\n" \
"v_mfma_f32_16x16x32_bf16 v[200:203], a[128:131], v[100:103], v[200:203]\n" \
"s_waitcnt lgkmcnt(4)\n" \
"ds_read_b128 v[96:99], v30 offset:8192\n" \
"v_mfma_f32_16x16x32_bf16 v[204:207], a[132:135], v[100:103], v[204:207]\n" \
"v_mfma_f32_16x16x32_bf16 v[192:195], a[136:139], v[104:107], v[192:195]\n" \
"ds_read_b128 v[100:103], v30 offset:9216\n" \
"v_mfma_f32_16x16x32_bf16 v[196:199], a[140:143], v[104:107], v[196:199]\n" \
"v_mfma_f32_16x16x32_bf16 v[200:203], a[136:139], v[108:111], v[200:203]\n" \
"s_waitcnt lgkmcnt(4)\n" \
"ds_read_b128 v[104:107], v30 offset:10240\n" \
"v_mfma_f32_16x16x32_bf16 v[204:207], a[140:143], v[108:111], v[204:207]\n" \
"v_mfma_f32_16x16x32_bf16 v[192:195], a[144:147], v[112:115], v[192:195]\n" \
"ds_read_b128 v[108:111], v30 offset:11264\n" \
"v_mfma_f32_16x16x32_bf16 v[196:199], a[148:151], v[112:115], v[196:199]\n" \
"v_mfma_f32_16x16x32_bf16 v[200:203], a[144:147], v[116:119], v[200:203]\n" \
"s_waitcnt lgkmcnt(4)\n" \
"ds_read_b128 v[112:115], v30 offset:12288\n" \
"v_mfma_f32_16x16x32_bf16 v[204:207], a[148:151], v[116:119], v[204:207]\n" \
"v_mfma_f32_16x16x32_bf16 v[192:195], a[152:155], v[120:123], v[192:195]\n" \
"ds_read_b128 v[116:119], v30 offset:13312\n" \
"v_mfma_f32_16x16x32_bf16 v[196:199], a[156:159], v[120:123], v[196:199]\n" \
"v_mfma_f32_16x16x32_bf16 v[200:203], a[152:155], v[124:127], v[200:203]\n" \
"ds_read_b128 v[120:123], v30 offset:14336\n" \
"v_mfma_f32_16x16x32_bf16 v[204:207], a[156:159], v[124:127], v[204:207]\n" \
"s_waitcnt vmcnt(25) lgkmcnt(5)\n" \
"s_barrier\n" \
"v_mfma_f32_16x16x32_bf16 v[192:195], a[0:3], v[96:99], v[192:195]\n" \
"ds_read_b128 v[124:127], v30 offset:15360\n" \
"v_mfma_f32_16x16x32_bf16 v[196:199], a[4:7], v[96:99], v[196:199]\n" \
"v_mfma_f32_16x16x32_bf16 v[200:203], a[0:3], v[100:103], v[200:203]\n" \
"s_waitcnt lgkmcnt(4)\n" \
"ds_read_b128 v[96:99], v28 offset:32768\n" \
"v_mfma_f32_16x16x32_bf16 v[204:207], a[4:7], v[100:103], v[204:207]\n" \
"v_mfma_f32_16x16x32_bf16 v[192:195], a[8:11], v[104:107], v[192:195]\n" \
"ds_read_b128 v[100:103], v28 offset:33792\n" \
"v_mfma_f32_16x16x32_bf16 v[196:199], a[12:15], v[104:107], v[196:199]\n" \
"v_mfma_f32_16x16x32_bf16 v[200:203], a[8:11], v[108:111], v[200:203]\n" \
"s_waitcnt lgkmcnt(4)\n" \
"ds_read_b128 v[104:107], v28 offset:34816\n" \
"v_mfma_f32_16x16x32_bf16 v[204:207], a[12:15], v[108:111], v[204:207]\n" \
"v_mfma_f32_16x16x32_bf16 v[192:195], a[16:19], v[112:115], v[192:195]\n" \
"ds_read_b128 v[108:111], v28 offset:35840\n" \
"v_mfma_f32_16x16x32_bf16 v[196:199], a[20:23], v[112:115], v[196:199]\n" \
"v_mfma_f32_16x16x32_bf16 v[200:203], a[16:19], v[116:119], v[200:203]\n" \
"s_waitcnt lgkmcnt(4)\n" \
"ds_read_b128 v[112:115], v28 offset:36864\n" \
"v_mfma_f32_16x16x32_bf16 v[204:207], a[20:23], v[116:119], v[204:207]\n" \
"v_mfma_f32_16x16x32_bf16 v[192:195], a[24:27], v[120:123], v[192:195]\n" \
"ds_read_b128 v[116:119], v28 offset:37888\n" \
"v_mfma_f32_16x16x32_bf16 v[196:199], a[28:31], v[120:123], v[196:199]\n" \
"v_mfma_f32_16x16x32_bf16 v[200:203], a[24:27], v[124:127], v[200:203]\n" \
"s_waitcnt lgkmcnt(4)\n" \
"ds_read_b128 v[120:123], v28 offset:38912\n" \
"v_mfma_f32_16x16x32_bf16 v[204:207], a[28:31], v[124:127], v[204:207]\n" \
"v_mfma_f32_16x16x32_bf16 v[192:195], a[32:35], v[96:99], v[192:195]\n" \
"ds_read_b128 v[124:127], v28 offset:39936\n" \
"v_mfma_f32_16x16x32_bf16 v[196:199], a[36:39], v[96:99], v[196:199]\n" \
"v_mfma_f32_16x16x32_bf16 v[200:203], a[32:35], v[100:103], v[200:203]\n" \
"s_waitcnt lgkmcnt(4)\n" \
"ds_read_b128 v[96:99], v28 offset:40960\n" \
"v_mfma_f32_16x16x32_bf16 v[204:207], a[36:39], v[100:103], v[204:207]\n" \
"v_mfma_f32_16x16x32_bf16 v[192:195], a[40:43], v[104:107], v[192:195]\n" \
"ds_read_b128 v[100:103], v28 offset:41984\n" \
"v_mfma_f32_16x16x32_bf16 v[196:199], a[44:47], v[104:107], v[196:199]\n" \
"v_mfma_f32_16x16x32_bf16 v[200:203], a[40:43], v[108:111], v[200:203]\n" \
"s_waitcnt lgkmcnt(4)\n" \
"ds_read_b128 v[104:107], v28 offset:43008\n" \
"v_mfma_f32_16x16x32_bf16 v[204:207], a[44:47], v[108:111], v[204:207]\n" \
"v_mfma_f32_16x16x32_bf16 v[192:195], a[48:51], v[112:115], v[192:195]\n" \
"ds_read_b128 v[108:111], v28 offset:44032\n" \
"v_mfma_f32_16x16x32_bf16 v[196:199], a[52:55], v[112:115], v[196:199]\n" \
"v_mfma_f32_16x16x32_bf16 v[200:203], a[48:51], v[116:119], v[200:203]\n" \
"s_waitcnt lgkmcnt(4)\n" \
"ds_read_b128 v[112:115], v28 offset:45056\n" \
"v_mfma_f32_16x16x32_bf16 v[204:207], a[52:55], v[116:119], v[204:207]\n" \
"v_mfma_f32_16x16x32_bf16 v[192:195], a[56:59], v[120:123], v[192:195]\n" \
"ds_read_b128 v[116:119], v28 offset:46080\n" \
"v_mfma_f32_16x16x32_bf16 v[196:199], a[60:63], v[120:123], v[196:199]\n" \
"v_mfma_f32_16x16x32_bf16 v[200:203], a[56:59], v[124:127], v[200:203]\n" \
"ds_read_b128 v[120:123], v28 offset:47104\n" \
"v_mfma_f32_16x16x32_bf16 v[204:207], a[60:63], v[124:127], v[204:207]\n" \
"s_waitcnt vmcnt(21) lgkmcnt(5)\n" \
"s_barrier\n" \
"v_mfma_f32_16x16x32_bf16 v[192:195], a[96:99], v[96:99], v[192:195]\n" \
"ds_read_b128 v[124:127], v28 offset:48128\n" \
"v_mfma_f32_16x16x32_bf16 v[196:199], a[100:103], v[96:99], v[196:199]\n" \
"v_mfma_f32_16x16x32_bf16 v[200:203], a[96:99], v[100:103], v[200:203]\n" \
"s_waitcnt lgkmcnt(4)\n" \
"ds_read_b128 v[96:99], v29 offset:16384\n" \
"v_mfma_f32_16x16x32_bf16 v[204:207], a[100:103], v[100:103], v[204:207]\n" \
"v_mfma_f32_16x16x32_bf16 v[192:195], a[104:107], v[104:107], v[192:195]\n" \
"ds_read_b128 v[100:103], v29 offset:17408\n" \
"v_mfma_f32_16x16x32_bf16 v[196:199], a[108:111], v[104:107], v[196:199]\n" \
"v_mfma_f32_16x16x32_bf16 v[200:203], a[104:107], v[108:111], v[200:203]\n" \
"s_waitcnt lgkmcnt(4)\n" \
"ds_read_b128 v[104:107], v29 offset:18432\n" \
"v_mfma_f32_16x16x32_bf16 v[204:207], a[108:111], v[108:111], v[204:207]\n" \
"v_mfma_f32_16x16x32_bf16 v[192:195], a[112:115], v[112:115], v[192:195]\n" \
"ds_read_b128 v[108:111], v29 offset:19456\n" \
"v_mfma_f32_16x16x32_bf16 v[196:199], a[116:119], v[112:115], v[196:199]\n" \
"v_mfma_f32_16x16x32_bf16 v[200:203], a[112:115], v[116:119], v[200:203]\n" \
"s_waitcnt lgkmcnt(4)\n" \
"ds_read_b128 v[112:115], v29 offset:20480\n" \
"v_mfma_f32_16x16x32_bf16 v[204:207], a[116:119], v[116:119], v[204:207]\n" \
"v_mfma_f32_16x16x32_bf16 v[192:195], a[120:123], v[120:123], v[192:195]\n" \
"ds_read_b128 v[116:119], v29 offset:21504\n" \
"v_mfma_f32_16x16x32_bf16 v[196:199], a[124:127], v[120:123], v[196:199]\n" \
"v_mfma_f32_16x16x32_bf16 v[200:203], a[120:123], v[124:127], v[200:203]\n" \
"ds_read_b128 v[120:123], v29 offset:22528\n" \
"v_mfma_f32_16x16x32_bf16 v[204:207], a[124:127], v[124:127], v[204:207]\n" \
"s_waitcnt vmcnt(0)\n" \
"v_mfma_f32_16x16x32_bf16 v[64:67], v[24:27], v[128:131], v[64:67]\n" \
"v_mfma_f32_16x16x32_bf16 v[68:71], v[22:25], v[128:131], v[68:71]\n" \
"v_mfma_f32_16x16x32_bf16 v[72:75], v[24:27], v[132:135], v[72:75]\n" \
"v_cvt_pk_bf16_f32 v192, v192, v193\n" \
"v_cvt_pk_bf16_f32 v193, v194, v195\n" \
"v_mfma_f32_16x16x32_bf16 v[76:79], v[22:25], v[132:135], v[76:79]\n" \
"v_cvt_pk_bf16_f32 v194, v196, v197\n" \
"v_cvt_pk_bf16_f32 v195, v198, v199\n" \
"v_mfma_f32_16x16x32_bf16 v[64:67], v[24:27], v[136:139], v[64:67]\n" \
"buffer_store_dwordx4 v[192:195], v4, s[36:39], s77 offen offset:0 sc1\n" \
"v_mfma_f32_16x16x32_bf16 v[68:71], v[22:25], v[136:139], v[68:71]\n" \
"v_cvt_pk_bf16_f32 v200, v200, v201\n" \
"v_cvt_pk_bf16_f32 v201, v202, v203\n" \
"v_mfma_f32_16x16x32_bf16 v[72:75], v[24:27], v[140:143], v[72:75]\n" \
"v_cvt_pk_bf16_f32 v202, v204, v205\n" \
"v_cvt_pk_bf16_f32 v203, v206, v207\n" \
"v_mfma_f32_16x16x32_bf16 v[76:79], v[22:25], v[140:143], v[76:79]\n" \
"v_mfma_f32_16x16x32_bf16 v[64:67], v[24:27], v[160:163], v[64:67]\n" \
"v_mfma_f32_16x16x32_bf16 v[68:71], v[22:25], v[160:163], v[68:71]\n" \
"v_mfma_f32_16x16x32_bf16 v[72:75], v[24:27], v[164:167], v[72:75]\n" \
"s_waitcnt lgkmcnt(5)\n" \
"v_mfma_f32_16x16x32_bf16 v[76:79], v[22:25], v[164:167], v[76:79]\n" \
"v_mfma_f32_16x16x32_bf16 v[240:243], a[160:163], v[96:99], 0\n" \
"ds_read_b128 v[124:127], v29 offset:23552\n" \
"v_mfma_f32_16x16x32_bf16 v[244:247], a[164:167], v[96:99], 0\n" \
"buffer_store_dwordx4 v[200:203], v4, s[36:39], s77 offen offset:1024 sc1\n" \
"v_mfma_f32_16x16x32_bf16 v[248:251], a[160:163], v[100:103], 0\n" \
"s_waitcnt lgkmcnt(4)\n" \
"ds_read_b128 v[96:99], v29 offset:24576\n" \
"v_cvt_pk_bf16_f32 v80, v64, v65\n" \
"v_mfma_f32_16x16x32_bf16 v[252:255], a[164:167], v[100:103], 0\n" \
"v_lshlrev_b32 v64, 16, v80\n" \
"v_and_b32 v65, 0xffff0000, v80\n" \
"v_mfma_f32_16x16x32_bf16 v[240:243], a[168:171], v[104:107], v[240:243]\n" \
"ds_read_b128 v[100:103], v29 offset:25600\n" \
"v_mul_f32 v64, v5, v64\n" \
"v_mfma_f32_16x16x32_bf16 v[244:247], a[172:175], v[104:107], v[244:247]\n" \
"v_mul_f32 v65, v5, v65\n" \
"v_cvt_pk_bf16_f32 v80, v64, v65\n" \
"v_mfma_f32_16x16x32_bf16 v[248:251], a[168:171], v[108:111], v[248:251]\n" \
"s_waitcnt lgkmcnt(4)\n" \
"ds_read_b128 v[104:107], v29 offset:26624\n" \
"v_lshlrev_b32 v64, 16, v80\n" \
"v_mfma_f32_16x16x32_bf16 v[252:255], a[172:175], v[108:111], v[252:255]\n" \
"v_and_b32 v65, 0xffff0000, v80\n" \
"v_add_f32 v64, v64, v32\n" \
"v_mfma_f32_16x16x32_bf16 v[240:243], a[176:179], v[112:115], v[240:243]\n" \
"ds_read_b128 v[108:111], v29 offset:27648\n" \
"v_add_f32 v65, v65, v33\n" \
"v_mfma_f32_16x16x32_bf16 v[244:247], a[180:183], v[112:115], v[244:247]\n" \
"v_cvt_pk_bf16_f32 v80, v64, v65\n" \
"v_cvt_pk_bf16_f32 v81, v66, v67\n" \
"v_mfma_f32_16x16x32_bf16 v[248:251], a[176:179], v[116:119], v[248:251]\n" \
"s_waitcnt lgkmcnt(4)\n" \
"ds_read_b128 v[112:115], v29 offset:28672\n" \
"v_lshlrev_b32 v66, 16, v81\n" \
"v_mfma_f32_16x16x32_bf16 v[252:255], a[180:183], v[116:119], v[252:255]\n" \
"v_and_b32 v67, 0xffff0000, v81\n" \
"v_mul_f32 v66, v5, v66\n" \
"v_mfma_f32_16x16x32_bf16 v[240:243], a[184:187], v[120:123], v[240:243]\n" \
"ds_read_b128 v[116:119], v29 offset:29696\n" \
"v_mul_f32 v67, v5, v67\n" \
"v_mfma_f32_16x16x32_bf16 v[244:247], a[188:191], v[120:123], v[244:247]\n" \
"v_cvt_pk_bf16_f32 v81, v66, v67\n" \
"v_lshlrev_b32 v66, 16, v81\n" \
"v_mfma_f32_16x16x32_bf16 v[248:251], a[184:187], v[124:127], v[248:251]\n" \
"ds_read_b128 v[120:123], v29 offset:30720\n" \
"v_and_b32 v67, 0xffff0000, v81\n" \
"v_mfma_f32_16x16x32_bf16 v[252:255], a[188:191], v[124:127], v[252:255]\n" \
"s_waitcnt vmcnt(18) lgkmcnt(5)\n" \
"s_barrier\n" \
"v_add_f32 v66, v66, v34\n" \
"v_mfma_f32_16x16x32_bf16 v[240:243], a[192:195], v[96:99], v[240:243]\n" \
"ds_read_b128 v[124:127], v29 offset:31744\n" \
"v_add_f32 v67, v67, v35\n" \
"v_mfma_f32_16x16x32_bf16 v[244:247], a[196:199], v[96:99], v[244:247]\n" \
"v_cvt_pk_bf16_f32 v81, v66, v67\n" \
"v_cvt_pk_bf16_f32 v82, v68, v69\n" \
"v_mfma_f32_16x16x32_bf16 v[248:251], a[192:195], v[100:103], v[248:251]\n" \
"s_waitcnt lgkmcnt(4)\n" \
"ds_read_b128 v[96:99], v28 offset:49152\n" \
"v_lshlrev_b32 v68, 16, v82\n" \
"v_mfma_f32_16x16x32_bf16 v[252:255], a[196:199], v[100:103], v[252:255]\n" \
"v_and_b32 v69, 0xffff0000, v82\n" \
"v_mul_f32 v68, v5, v68\n" \
"v_mfma_f32_16x16x32_bf16 v[240:243], a[200:203], v[104:107], v[240:243]\n" \
"s_add_u32 s97, s78, 4096\n" \
"ds_read_b128 v[100:103], v28 offset:50176\n" \
"v_mul_f32 v69, v5, v69\n" \
"v_mfma_f32_16x16x32_bf16 v[244:247], a[204:207], v[104:107], v[244:247]\n" \
"buffer_load_dwordx4 v[168:171], v4, s[36:39], s97 offen offset:2048 sc1\n" \
"v_mfma_f32_16x16x32_bf16 v[248:251], a[200:203], v[108:111], v[248:251]\n" \
"s_waitcnt lgkmcnt(4)\n" \
"ds_read_b128 v[104:107], v28 offset:51200\n" \
"v_cvt_pk_bf16_f32 v82, v68, v69\n" \
"v_mfma_f32_16x16x32_bf16 v[252:255], a[204:207], v[108:111], v[252:255]\n" \
"v_lshlrev_b32 v68, 16, v82\n" \
"v_and_b32 v69, 0xffff0000, v82\n" \
"v_mfma_f32_16x16x32_bf16 v[240:243], a[208:211], v[112:115], v[240:243]\n" \
"ds_read_b128 v[108:111], v28 offset:52224\n" \
"v_add_f32 v68, v68, v36\n" \
"v_mfma_f32_16x16x32_bf16 v[244:247], a[212:215], v[112:115], v[244:247]\n" \
"buffer_load_dwordx4 v[172:175], v4, s[36:39], s97 offen offset:3072 sc1\n" \
"v_mfma_f32_16x16x32_bf16 v[248:251], a[208:211], v[116:119], v[248:251]\n" \
"s_waitcnt lgkmcnt(4)\n" \
"ds_read_b128 v[112:115], v28 offset:53248\n" \
"s_add_u32 s97, s78, 8192\n" \
"v_mfma_f32_16x16x32_bf16 v[252:255], a[212:215], v[116:119], v[252:255]\n" \
"v_add_f32 v69, v69, v37\n" \
"v_cvt_pk_bf16_f32 v82, v68, v69\n" \
"v_mfma_f32_16x16x32_bf16 v[240:243], a[216:219], v[120:123], v[240:243]\n" \
"ds_read_b128 v[116:119], v28 offset:54272\n" \
"v_cvt_pk_bf16_f32 v83, v70, v71\n" \
"v_mfma_f32_16x16x32_bf16 v[244:247], a[220:223], v[120:123], v[244:247]\n" \
"buffer_load_dwordx4 v[224:227], v4, s[36:39], s97 offen offset:0 sc1\n" \
"v_mfma_f32_16x16x32_bf16 v[248:251], a[216:219], v[124:127], v[248:251]\n" \
"s_waitcnt lgkmcnt(4)\n" \
"ds_read_b128 v[120:123], v28 offset:55296\n" \
"v_lshlrev_b32 v70, 16, v83\n" \
"v_mfma_f32_16x16x32_bf16 v[252:255], a[220:223], v[124:127], v[252:255]\n" \
"v_and_b32 v71, 0xffff0000, v83\n" \
"v_mul_f32 v70, v5, v70\n" \
"v_mfma_f32_16x16x32_bf16 v[240:243], a[224:227], v[96:99], v[240:243]\n" \
"ds_read_b128 v[124:127], v28 offset:56320\n" \
"v_mul_f32 v71, v5, v71\n" \
"v_mfma_f32_16x16x32_bf16 v[244:247], a[228:231], v[96:99], v[244:247]\n" \
"buffer_load_dwordx4 v[228:231], v4, s[36:39], s97 offen offset:1024 sc1\n" \
"v_mfma_f32_16x16x32_bf16 v[248:251], a[224:227], v[100:103], v[248:251]\n" \
"s_waitcnt lgkmcnt(4)\n" \
"ds_read_b128 v[96:99], v28 offset:57344\n" \
"v_cvt_pk_bf16_f32 v83, v70, v71\n" \
"v_mfma_f32_16x16x32_bf16 v[252:255], a[228:231], v[100:103], v[252:255]\n" \
"v_lshlrev_b32 v70, 16, v83\n" \
"v_and_b32 v71, 0xffff0000, v83\n" \
"v_mfma_f32_16x16x32_bf16 v[240:243], a[232:235], v[104:107], v[240:243]\n" \
"ds_read_b128 v[100:103], v28 offset:58368\n" \
"v_add_f32 v70, v70, v38\n" \
"v_mfma_f32_16x16x32_bf16 v[244:247], a[236:239], v[104:107], v[244:247]\n" \
"s_add_u32 s92, s82, 0\n" \
"v_add_f32 v71, v71, v39\n" \
"v_cvt_pk_bf16_f32 v83, v70, v71\n" \
"v_mfma_f32_16x16x32_bf16 v[248:251], a[232:235], v[108:111], v[248:251]\n" \
"s_waitcnt lgkmcnt(4)\n" \
"ds_read_b128 v[104:107], v28 offset:59392\n" \
"s_nop 0\n" \
"v_mfma_f32_16x16x32_bf16 v[252:255], a[236:239], v[108:111], v[252:255]\n" \
"v_permlane16_swap_b32 v80, v82\n" \
"v_permlane16_swap_b32 v81, v83\n" \
"v_mfma_f32_16x16x32_bf16 v[240:243], a[240:243], v[112:115], v[240:243]\n" \
"ds_read_b128 v[108:111], v28 offset:60416\n" \
"v_cvt_pk_bf16_f32 v84, v72, v73\n" \
"v_mfma_f32_16x16x32_bf16 v[244:247], a[244:247], v[112:115], v[244:247]\n" \
"v_lshlrev_b32 v72, 16, v84\n" \
"v_and_b32 v73, 0xffff0000, v84\n" \
"v_mfma_f32_16x16x32_bf16 v[248:251], a[240:243], v[116:119], v[248:251]\n" \
"s_waitcnt lgkmcnt(4)\n" \
"ds_read_b128 v[112:115], v28 offset:61440\n" \
"v_mul_f32 v72, v21, v72\n" \
"v_mfma_f32_16x16x32_bf16 v[252:255], a[244:247], v[116:119], v[252:255]\n" \
"v_mul_f32 v73, v21, v73\n" \
"v_cvt_pk_bf16_f32 v84, v72, v73\n" \
"v_mfma_f32_16x16x32_bf16 v[240:243], a[248:251], v[120:123], v[240:243]\n" \
"ds_read_b128 v[116:119], v28 offset:62464\n" \
"v_lshlrev_b32 v72, 16, v84\n" \
"v_mfma_f32_16x16x32_bf16 v[244:247], a[252:255], v[120:123], v[244:247]\n" \
"v_and_b32 v73, 0xffff0000, v84\n" \
"v_add_f32 v72, v72, v40\n" \
"v_mfma_f32_16x16x32_bf16 v[248:251], a[248:251], v[124:127], v[248:251]\n" \
"ds_read_b128 v[120:123], v28 offset:63488\n" \
"v_add_f32 v73, v73, v41\n" \
"v_mfma_f32_16x16x32_bf16 v[252:255], a[252:255], v[124:127], v[252:255]\n" \
"s_waitcnt vmcnt(18) lgkmcnt(5)\n" \
"s_barrier\n" \
"v_cvt_pk_bf16_f32 v84, v72, v73\n" \
"v_mfma_f32_16x16x32_bf16 v[240:243], a[64:67], v[96:99], v[240:243]\n" \
"ds_read_b128 v[124:127], v28 offset:64512\n" \
"v_cvt_pk_bf16_f32 v85, v74, v75\n" \
"v_mfma_f32_16x16x32_bf16 v[244:247], a[68:71], v[96:99], v[244:247]\n" \
"v_lshlrev_b32 v74, 16, v85\n" \
"v_and_b32 v75, 0xffff0000, v85\n" \
"v_mfma_f32_16x16x32_bf16 v[248:251], a[64:67], v[100:103], v[248:251]\n" \
"s_waitcnt lgkmcnt(4)\n" \
"ds_read_b128 v[96:99], v30 offset:16384\n" \
"v_mul_f32 v74, v21, v74\n" \
"v_mfma_f32_16x16x32_bf16 v[252:255], a[68:71], v[100:103], v[252:255]\n" \
"v_mul_f32 v75, v21, v75\n" \
"v_cvt_pk_bf16_f32 v85, v74, v75\n" \
"v_mfma_f32_16x16x32_bf16 v[240:243], a[72:75], v[104:107], v[240:243]\n" \
"ds_read_b128 v[100:103], v30 offset:17408\n" \
"v_lshlrev_b32 v74, 16, v85\n" \
"v_mfma_f32_16x16x32_bf16 v[244:247], a[76:79], v[104:107], v[244:247]\n" \
"v_and_b32 v75, 0xffff0000, v85\n" \
"v_add_f32 v74, v74, v42\n" \
"v_mfma_f32_16x16x32_bf16 v[248:251], a[72:75], v[108:111], v[248:251]\n" \
"s_waitcnt lgkmcnt(4)\n" \
"ds_read_b128 v[104:107], v30 offset:18432\n" \
"v_add_f32 v75, v75, v43\n" \
"v_mfma_f32_16x16x32_bf16 v[252:255], a[76:79], v[108:111], v[252:255]\n" \
"v_cvt_pk_bf16_f32 v85, v74, v75\n" \
"v_cvt_pk_bf16_f32 v86, v76, v77\n" \
"v_mfma_f32_16x16x32_bf16 v[240:243], a[80:83], v[112:115], v[240:243]\n" \
"ds_read_b128 v[108:111], v30 offset:19456\n" \
"v_lshlrev_b32 v76, 16, v86\n" \
"v_mfma_f32_16x16x32_bf16 v[244:247], a[84:87], v[112:115], v[244:247]\n" \
"v_and_b32 v77, 0xffff0000, v86\n" \
"v_mul_f32 v76, v21, v76\n" \
"v_mfma_f32_16x16x32_bf16 v[248:251], a[80:83], v[116:119], v[248:251]\n" \
"s_waitcnt lgkmcnt(4)\n" \
"ds_read_b128 v[112:115], v30 offset:20480\n" \
"v_mul_f32 v77, v21, v77\n" \
"v_mfma_f32_16x16x32_bf16 v[252:255], a[84:87], v[116:119], v[252:255]\n" \
"v_cvt_pk_bf16_f32 v86, v76, v77\n" \
"v_lshlrev_b32 v76, 16, v86\n" \
"v_mfma_f32_16x16x32_bf16 v[240:243], a[88:91], v[120:123], v[240:243]\n" \
"ds_read_b128 v[116:119], v30 offset:21504\n" \
"v_and_b32 v77, 0xffff0000, v86\n" \
"v_mfma_f32_16x16x32_bf16 v[244:247], a[92:95], v[120:123], v[244:247]\n" \
"v_add_f32 v76, v76, v44\n" \
"v_add_f32 v77, v77, v45\n" \
"v_mfma_f32_16x16x32_bf16 v[248:251], a[88:91], v[124:127], v[248:251]\n" \
"s_waitcnt lgkmcnt(4)\n" \
"ds_read_b128 v[120:123], v30 offset:22528\n" \
"v_cvt_pk_bf16_f32 v86, v76, v77\n" \
"v_mfma_f32_16x16x32_bf16 v[252:255], a[92:95], v[124:127], v[252:255]\n" \
"v_cvt_pk_bf16_f32 v87, v78, v79\n" \
"v_lshlrev_b32 v78, 16, v87\n" \
"v_mfma_f32_16x16x32_bf16 v[240:243], a[128:131], v[96:99], v[240:243]\n" \
"ds_read_b128 v[124:127], v30 offset:23552\n" \
"v_and_b32 v79, 0xffff0000, v87\n" \
"v_mfma_f32_16x16x32_bf16 v[244:247], a[132:135], v[96:99], v[244:247]\n" \
"v_mul_f32 v78, v21, v78\n" \
"v_mul_f32 v79, v21, v79\n" \
"v_mfma_f32_16x16x32_bf16 v[248:251], a[128:131], v[100:103], v[248:251]\n" \
"s_waitcnt lgkmcnt(4)\n" \
"ds_read_b128 v[96:99], v30 offset:24576\n" \
"v_cvt_pk_bf16_f32 v87, v78, v79\n" \
"v_mfma_f32_16x16x32_bf16 v[252:255], a[132:135], v[100:103], v[252:255]\n" \
"v_lshlrev_b32 v78, 16, v87\n" \
"v_and_b32 v79, 0xffff0000, v87\n" \
"v_mfma_f32_16x16x32_bf16 v[240:243], a[136:139], v[104:107], v[240:243]\n" \
"ds_read_b128 v[100:103], v30 offset:25600\n" \
"v_add_f32 v78, v78, v46\n" \
"v_mfma_f32_16x16x32_bf16 v[244:247], a[140:143], v[104:107], v[244:247]\n" \
"s_add_u32 s93, s82, 32768\n" \
"v_add_f32 v79, v79, v47\n" \
"v_cvt_pk_bf16_f32 v87, v78, v79\n" \
"v_mfma_f32_16x16x32_bf16 v[248:251], a[136:139], v[108:111], v[248:251]\n" \
"s_waitcnt lgkmcnt(4)\n" \
"ds_read_b128 v[104:107], v30 offset:26624\n" \
"s_nop 0\n" \
"v_mfma_f32_16x16x32_bf16 v[252:255], a[140:143], v[108:111], v[252:255]\n" \
"v_permlane16_swap_b32 v84, v86\n" \
"v_permlane16_swap_b32 v85, v87\n" \
"v_mfma_f32_16x16x32_bf16 v[240:243], a[144:147], v[112:115], v[240:243]\n" \
"ds_read_b128 v[108:111], v30 offset:27648\n" \
"v_mfma_f32_16x16x32_bf16 v[244:247], a[148:151], v[112:115], v[244:247]\n" \
"v_mfma_f32_16x16x32_bf16 v[248:251], a[144:147], v[116:119], v[248:251]\n" \
"s_waitcnt lgkmcnt(4)\n" \
"ds_read_b128 v[112:115], v30 offset:28672\n" \
"v_mfma_f32_16x16x32_bf16 v[252:255], a[148:151], v[116:119], v[252:255]\n" \
"v_mfma_f32_16x16x32_bf16 v[240:243], a[152:155], v[120:123], v[240:243]\n" \
"ds_read_b128 v[116:119], v30 offset:29696\n" \
"v_mfma_f32_16x16x32_bf16 v[244:247], a[156:159], v[120:123], v[244:247]\n" \
"v_mfma_f32_16x16x32_bf16 v[248:251], a[152:155], v[124:127], v[248:251]\n" \
"ds_read_b128 v[120:123], v30 offset:30720\n" \
"v_mfma_f32_16x16x32_bf16 v[252:255], a[156:159], v[124:127], v[252:255]\n" \
"s_waitcnt vmcnt(9) lgkmcnt(5)\n" \
"s_barrier\n" \
"v_mfma_f32_16x16x32_bf16 v[240:243], a[0:3], v[96:99], v[240:243]\n" \
"ds_read_b128 v[124:127], v30 offset:31744\n" \
"v_mfma_f32_16x16x32_bf16 v[244:247], a[4:7], v[96:99], v[244:247]\n" \
"v_mfma_f32_16x16x32_bf16 v[248:251], a[0:3], v[100:103], v[248:251]\n" \
"s_waitcnt lgkmcnt(4)\n" \
"ds_read_b128 v[96:99], v28 offset:0\n" \
"v_mfma_f32_16x16x32_bf16 v[252:255], a[4:7], v[100:103], v[252:255]\n" \
"v_mfma_f32_16x16x32_bf16 v[240:243], a[8:11], v[104:107], v[240:243]\n" \
"ds_read_b128 v[100:103], v28 offset:1024\n" \
"v_mfma_f32_16x16x32_bf16 v[244:247], a[12:15], v[104:107], v[244:247]\n" \
"v_mfma_f32_16x16x32_bf16 v[248:251], a[8:11], v[108:111], v[248:251]\n" \
"s_waitcnt lgkmcnt(4)\n" \
"ds_read_b128 v[104:107], v28 offset:2048\n" \
"v_mfma_f32_16x16x32_bf16 v[252:255], a[12:15], v[108:111], v[252:255]\n" \
"v_mfma_f32_16x16x32_bf16 v[240:243], a[16:19], v[112:115], v[240:243]\n" \
"ds_read_b128 v[108:111], v28 offset:3072\n" \
"v_mfma_f32_16x16x32_bf16 v[244:247], a[20:23], v[112:115], v[244:247]\n" \
"v_mfma_f32_16x16x32_bf16 v[248:251], a[16:19], v[116:119], v[248:251]\n" \
"s_waitcnt lgkmcnt(4)\n" \
"ds_read_b128 v[112:115], v28 offset:4096\n" \
"s_add_u32 s97, s78, 8192\n" \
"v_mfma_f32_16x16x32_bf16 v[252:255], a[20:23], v[116:119], v[252:255]\n" \
"buffer_load_dwordx4 v[232:235], v4, s[36:39], s97 offen offset:2048 sc1\n" \
"v_mfma_f32_16x16x32_bf16 v[240:243], a[24:27], v[120:123], v[240:243]\n" \
"ds_read_b128 v[116:119], v28 offset:5120\n" \
"s_waitcnt vmcnt(1)\n" \
"v_mfma_f32_16x16x32_bf16 v[244:247], a[28:31], v[120:123], v[244:247]\n" \
"v_max_u32 v18, v168, v169\n" \
"v_max3_u32 v18, v18, v170, v171\n" \
"v_mfma_f32_16x16x32_bf16 v[248:251], a[24:27], v[124:127], v[248:251]\n" \
"s_waitcnt lgkmcnt(4)\n" \
"ds_read_b128 v[120:123], v28 offset:6144\n" \
"v_max3_u32 v18, v18, v172, v173\n" \
"v_mfma_f32_16x16x32_bf16 v[252:255], a[28:31], v[124:127], v[252:255]\n" \
"buffer_load_dwordx4 v[236:239], v4, s[36:39], s97 offen offset:3072 sc1\n" \
"v_mfma_f32_16x16x32_bf16 v[240:243], a[32:35], v[96:99], v[240:243]\n" \
"ds_read_b128 v[124:127], v28 offset:7168\n" \
"v_max3_u32 v18, v18, v174, v175\n" \
"v_mfma_f32_16x16x32_bf16 v[244:247], a[36:39], v[96:99], v[244:247]\n" \
"buffer_store_dwordx4 v[80:83], v184, s[32:35], s92 offen\n" \
"v_mfma_f32_16x16x32_bf16 v[248:251], a[32:35], v[100:103], v[248:251]\n" \
"s_waitcnt lgkmcnt(4)\n" \
"ds_read_b128 v[96:99], v28 offset:8192\n" \
"v_max3_u32 v18, v18, v224, v225\n" \
"v_mfma_f32_16x16x32_bf16 v[252:255], a[36:39], v[100:103], v[252:255]\n" \
"v_max3_u32 v18, v18, v226, v227\n" \
"v_max3_u32 v18, v18, v228, v229\n" \
"v_mfma_f32_16x16x32_bf16 v[240:243], a[40:43], v[104:107], v[240:243]\n" \
"ds_read_b128 v[100:103], v28 offset:9216\n" \
"v_max3_u32 v18, v18, v230, v231\n" \
"v_mfma_f32_16x16x32_bf16 v[244:247], a[44:47], v[104:107], v[244:247]\n" \
"v_cmp_eq_u32 vcc, -1, v18\n" \
"s_cbranch_vccnz LIM_%=\nLIMR_%=:\n" \
"v_mfma_f32_16x16x32_bf16 v[248:251], a[40:43], v[108:111], v[248:251]\n" \
"s_waitcnt lgkmcnt(4)\n" \
"ds_read_b128 v[104:107], v28 offset:10240\n" \
"v_mfma_f32_16x16x32_bf16 v[252:255], a[44:47], v[108:111], v[252:255]\n" \
"v_mfma_f32_16x16x32_bf16 v[240:243], a[48:51], v[112:115], v[240:243]\n" \
"ds_read_b128 v[108:111], v28 offset:11264\n" \
"v_mfma_f32_16x16x32_bf16 v[244:247], a[52:55], v[112:115], v[244:247]\n" \
"buffer_store_dwordx4 v[84:87], v184, s[32:35], s93 offen\n" \
"v_mfma_f32_16x16x32_bf16 v[248:251], a[48:51], v[116:119], v[248:251]\n" \
"s_waitcnt lgkmcnt(4)\n" \
"ds_read_b128 v[112:115], v28 offset:12288\n" \
"v_mfma_f32_16x16x32_bf16 v[252:255], a[52:55], v[116:119], v[252:255]\n" \
"v_mfma_f32_16x16x32_bf16 v[240:243], a[56:59], v[120:123], v[240:243]\n" \
"ds_read_b128 v[116:119], v28 offset:13312\n" \
"v_mfma_f32_16x16x32_bf16 v[244:247], a[60:63], v[120:123], v[244:247]\n" \
"v_mfma_f32_16x16x32_bf16 v[248:251], a[56:59], v[124:127], v[248:251]\n" \
"ds_read_b128 v[120:123], v28 offset:14336\n" \
"v_mfma_f32_16x16x32_bf16 v[252:255], a[60:63], v[124:127], v[252:255]\n" \
"s_waitcnt lgkmcnt(5)\n" \
"s_barrier\n" \
"v_mfma_f32_16x16x32_bf16 v[240:243], a[96:99], v[96:99], v[240:243]\n" \
"ds_read_b128 v[124:127], v28 offset:15360\n" \
"v_mfma_f32_16x16x32_bf16 v[244:247], a[100:103], v[96:99], v[244:247]\n" \
"v_mfma_f32_16x16x32_bf16 v[248:251], a[96:99], v[100:103], v[248:251]\n" \
"s_waitcnt lgkmcnt(4)\n" \
"v_mfma_f32_16x16x32_bf16 v[252:255], a[100:103], v[100:103], v[252:255]\n" \
"v_mfma_f32_16x16x32_bf16 v[240:243], a[104:107], v[104:107], v[240:243]\n" \
"v_mfma_f32_16x16x32_bf16 v[244:247], a[108:111], v[104:107], v[244:247]\n" \
"v_mfma_f32_16x16x32_bf16 v[248:251], a[104:107], v[108:111], v[248:251]\n" \
"s_waitcnt lgkmcnt(2)\n" \
"v_mfma_f32_16x16x32_bf16 v[252:255], a[108:111], v[108:111], v[252:255]\n" \
"v_mfma_f32_16x16x32_bf16 v[240:243], a[112:115], v[112:115], v[240:243]\n" \
"v_mfma_f32_16x16x32_bf16 v[244:247], a[116:119], v[112:115], v[244:247]\n" \
"v_mfma_f32_16x16x32_bf16 v[248:251], a[112:115], v[116:119], v[248:251]\n" \
"s_waitcnt lgkmcnt(0)\n" \
"v_mfma_f32_16x16x32_bf16 v[252:255], a[116:119], v[116:119], v[252:255]\n" \
"v_mfma_f32_16x16x32_bf16 v[240:243], a[120:123], v[120:123], v[240:243]\n" \
"v_mfma_f32_16x16x32_bf16 v[244:247], a[124:127], v[120:123], v[244:247]\n" \
"v_mfma_f32_16x16x32_bf16 v[248:251], a[120:123], v[124:127], v[248:251]\n" \
"v_mfma_f32_16x16x32_bf16 v[252:255], a[124:127], v[124:127], v[252:255]\n" \
"v_mfma_f32_16x16x32_bf16 v[240:243], v[24:27], v[168:171], v[240:243]\n" \
"v_mfma_f32_16x16x32_bf16 v[244:247], v[22:25], v[168:171], v[244:247]\n" \
"v_mfma_f32_16x16x32_bf16 v[248:251], v[24:27], v[172:175], v[248:251]\n" \
"v_mfma_f32_16x16x32_bf16 v[252:255], v[22:25], v[172:175], v[252:255]\n" \
"v_mfma_f32_16x16x32_bf16 v[240:243], v[24:27], v[224:227], v[240:243]\n" \
"v_mfma_f32_16x16x32_bf16 v[244:247], v[22:25], v[224:227], v[244:247]\n" \
"v_mfma_f32_16x16x32_bf16 v[248:251], v[24:27], v[228:231], v[248:251]\n" \
"v_mfma_f32_16x16x32_bf16 v[252:255], v[22:25], v[228:231], v[252:255]\n" \
"s_mov_b32 s92, s83\n" \
"s_add_u32 s93, s83, 32768\n" \
"s_waitcnt vmcnt(2)\n" \
"v_max_u32 v18, v232, v233\n" \
"v_max3_u32 v18, v18, v234, v235\n" \
"v_max3_u32 v18, v18, v236, v237\n" \
"v_max3_u32 v18, v18, v238, v239\n" \
"v_cmp_eq_u32 vcc, -1, v18\n" \
"s_cbranch_vccnz LIN_%=\nLINR_%=:\n" \
"v_mfma_f32_16x16x32_bf16 v[240:243], v[24:27], v[232:235], v[240:243]\n" \
"v_mfma_f32_16x16x32_bf16 v[244:247], v[22:25], v[232:235], v[244:247]\n" \
"v_mfma_f32_16x16x32_bf16 v[248:251], v[24:27], v[236:239], v[248:251]\n" \
"v_mfma_f32_16x16x32_bf16 v[252:255], v[22:25], v[236:239], v[252:255]\n" \
"s_nop 1\n" \
"v_cvt_pk_bf16_f32 v80, v240, v241\n" \
"v_cvt_pk_bf16_f32 v81, v242, v243\n" \
"v_cvt_pk_bf16_f32 v82, v244, v245\n" \
"v_cvt_pk_bf16_f32 v83, v246, v247\n" \
"v_lshlrev_b32 v240, 16, v80\n" \
"v_lshlrev_b32 v242, 16, v81\n" \
"v_lshlrev_b32 v244, 16, v82\n" \
"v_lshlrev_b32 v246, 16, v83\n" \
"v_and_b32 v241, 0xffff0000, v80\n" \
"v_and_b32 v243, 0xffff0000, v81\n" \
"v_and_b32 v245, 0xffff0000, v82\n" \
"v_and_b32 v247, 0xffff0000, v83\n" \
"v_pk_mul_f32 v[240:241], v[240:241], v[6:7] op_sel_hi:[1,0]\n" \
"v_pk_mul_f32 v[242:243], v[242:243], v[6:7] op_sel_hi:[1,0]\n" \
"v_pk_mul_f32 v[244:245], v[244:245], v[6:7] op_sel_hi:[1,0]\n" \
"v_pk_mul_f32 v[246:247], v[246:247], v[6:7] op_sel_hi:[1,0]\n" \
"v_cvt_pk_bf16_f32 v80, v240, v241\n" \
"v_cvt_pk_bf16_f32 v81, v242, v243\n" \
"v_cvt_pk_bf16_f32 v82, v244, v245\n" \
"v_cvt_pk_bf16_f32 v83, v246, v247\n" \
"v_lshlrev_b32 v240, 16, v80\n" \
"v_lshlrev_b32 v242, 16, v81\n" \
"v_lshlrev_b32 v244, 16, v82\n" \
"v_lshlrev_b32 v246, 16, v83\n" \
"v_and_b32 v241, 0xffff0000, v80\n" \
"v_and_b32 v243, 0xffff0000, v81\n" \
"v_and_b32 v245, 0xffff0000, v82\n" \
"v_and_b32 v247, 0xffff0000, v83\n" \
"v_pk_add_f32 v[240:241], v[240:241], v[48:49]\n" \
"v_pk_add_f32 v[242:243], v[242:243], v[50:51]\n" \
"v_pk_add_f32 v[244:245], v[244:245], v[52:53]\n" \
"v_pk_add_f32 v[246:247], v[246:247], v[54:55]\n" \
"v_cvt_pk_bf16_f32 v80, v240, v241\n" \
"v_cvt_pk_bf16_f32 v81, v242, v243\n" \
"v_cvt_pk_bf16_f32 v82, v244, v245\n" \
"v_cvt_pk_bf16_f32 v83, v246, v247\n" \
"s_nop 0\n" \
"v_permlane16_swap_b32 v80, v82\n" \
"v_permlane16_swap_b32 v81, v83\n" \
"buffer_store_dwordx4 v[80:83], v184, s[32:35], s92 offen\n" \
"v_cvt_pk_bf16_f32 v84, v248, v249\n" \
"v_cvt_pk_bf16_f32 v85, v250, v251\n" \
"v_cvt_pk_bf16_f32 v86, v252, v253\n" \
"v_cvt_pk_bf16_f32 v87, v254, v255\n" \
"v_lshlrev_b32 v248, 16, v84\n" \
"v_lshlrev_b32 v250, 16, v85\n" \
"v_lshlrev_b32 v252, 16, v86\n" \
"v_lshlrev_b32 v254, 16, v87\n" \
"v_and_b32 v249, 0xffff0000, v84\n" \
"v_and_b32 v251, 0xffff0000, v85\n" \
"v_and_b32 v253, 0xffff0000, v86\n" \
"v_and_b32 v255, 0xffff0000, v87\n" \
"v_pk_mul_f32 v[248:249], v[248:249], v[8:9] op_sel_hi:[1,0]\n" \
"v_pk_mul_f32 v[250:251], v[250:251], v[8:9] op_sel_hi:[1,0]\n" \
"v_pk_mul_f32 v[252:253], v[252:253], v[8:9] op_sel_hi:[1,0]\n" \
"v_pk_mul_f32 v[254:255], v[254:255], v[8:9] op_sel_hi:[1,0]\n" \
"v_cvt_pk_bf16_f32 v84, v248, v249\n" \
"v_cvt_pk_bf16_f32 v85, v250, v251\n" \
"v_cvt_pk_bf16_f32 v86, v252, v253\n" \
"v_cvt_pk_bf16_f32 v87, v254, v255\n" \
"v_lshlrev_b32 v248, 16, v84\n" \
"v_lshlrev_b32 v250, 16, v85\n" \
"v_lshlrev_b32 v252, 16, v86\n" \
"v_lshlrev_b32 v254, 16, v87\n" \
"v_and_b32 v249, 0xffff0000, v84\n" \
"v_and_b32 v251, 0xffff0000, v85\n" \
"v_and_b32 v253, 0xffff0000, v86\n" \
"v_and_b32 v255, 0xffff0000, v87\n" \
"v_pk_add_f32 v[248:249], v[248:249], v[56:57]\n" \
"v_pk_add_f32 v[250:251], v[250:251], v[58:59]\n" \
"v_pk_add_f32 v[252:253], v[252:253], v[60:61]\n" \
"v_pk_add_f32 v[254:255], v[254:255], v[62:63]\n" \
"v_cvt_pk_bf16_f32 v84, v248, v249\n" \
"v_cvt_pk_bf16_f32 v85, v250, v251\n" \
"v_cvt_pk_bf16_f32 v86, v252, v253\n" \
"v_cvt_pk_bf16_f32 v87, v254, v255\n" \
"s_nop 0\n" \
"v_permlane16_swap_b32 v84, v86\n" \
"v_permlane16_swap_b32 v85, v87\n" \
"buffer_store_dwordx4 v[84:87], v184, s[32:35], s93 offen\n" \
"s_branch LEND_%=\n" \
"LPUK_%=:\n" \
"s_mov_b32 s25, 0\n" \
"LPUKL_%=:\n" \
"s_sleep 2\n" \
"buffer_load_dwordx2 v[58:59], v57, s[36:39], 0 offen sc1\n" \
"s_add_u32 s25, s25, 1\n" \
"s_waitcnt vmcnt(0)\n" \
"v_cmp_ne_u64 vcc, s[14:15], v[58:59]\n" \
"s_cmp_gt_u32 s25, 262144\n" \
"s_cbranch_scc1 LPUKR_%=\n" \
"s_cbranch_vccnz LPUKL_%=\n" \
"s_branch LPUKR_%=\n" \
"LPUV_%=:\n" \
"s_mov_b32 s25, 0\n" \
"LPUVL_%=:\n" \
"s_sleep 2\n" \
"buffer_load_dwordx2 v[58:59], v57, s[36:39], 0 offen sc1\n" \
"s_add_u32 s25, s25, 1\n" \
"s_waitcnt vmcnt(0)\n" \
"v_cmp_ne_u64 vcc, s[14:15], v[58:59]\n" \
"s_cmp_gt_u32 s25, 262144\n" \
"s_cbranch_scc1 LPUVR_%=\n" \
"s_cbranch_vccnz LPUVL_%=\n" \
"s_branch LPUVR_%=\n" \
"LPK_%=:\n" \
"s_mov_b32 s25, 0\n" \
"LPKL_%=:\n" \
"s_sleep 2\n" \
"buffer_load_dwordx2 v[58:59], v57, s[36:39], 0 offen sc1\n" \
"s_add_u32 s25, s25, 1\n" \
"s_waitcnt vmcnt(0)\n" \
"v_cmp_ne_u64 vcc, s[14:15], v[58:59]\n" \
"s_cmp_gt_u32 s25, 262144\n" \
"s_cbranch_scc1 LPKR_%=\n" \
"s_cbranch_vccnz LPKL_%=\n" \
"s_branch LPKR_%=\n" \
"LSW_%=:\n" \
"v_swap_b32 v64, v80\n" \
"v_swap_b32 v65, v81\n" \
"v_swap_b32 v66, v82\n" \
"v_swap_b32 v67, v83\n" \
"v_swap_b32 v68, v84\n" \
"v_swap_b32 v69, v85\n" \
"v_swap_b32 v70, v86\n" \
"v_swap_b32 v71, v87\n" \
"v_swap_b32 v72, v88\n" \
"v_swap_b32 v73, v89\n" \
"v_swap_b32 v74, v90\n" \
"v_swap_b32 v75, v91\n" \
"v_swap_b32 v76, v92\n" \
"v_swap_b32 v77, v93\n" \
"v_swap_b32 v78, v94\n" \
"v_swap_b32 v79, v95\n" \
"s_branch LSWR_%=\n" \
"LIM_%=:\n" \
"s_mov_b32 s25, 0\n" \
"LIML_%=:\n" \
"s_sleep 1\n" \
"s_add_u32 s98, s78, 4096\n" \
"buffer_load_dwordx4 v[168:171], v4, s[36:39], s98 offen offset:2048 sc1\n" \
"buffer_load_dwordx4 v[172:175], v4, s[36:39], s98 offen offset:3072 sc1\n" \
"s_add_u32 s98, s78, 8192\n" \
"buffer_load_dwordx4 v[224:227], v4, s[36:39], s98 offen offset:0 sc1\n" \
"buffer_load_dwordx4 v[228:231], v4, s[36:39], s98 offen offset:1024 sc1\n" \
"s_add_u32 s25, s25, 1\n" \
"s_waitcnt vmcnt(0)\n" \
"v_max_u32 v18, v168, v169\n" \
"v_max3_u32 v18, v18, v170, v171\n" \
"v_max3_u32 v18, v18, v172, v173\n" \
"v_max3_u32 v18, v18, v174, v175\n" \
"v_max3_u32 v18, v18, v224, v225\n" \
"v_max3_u32 v18, v18, v226, v227\n" \
"v_max3_u32 v18, v18, v228, v229\n" \
"v_max3_u32 v18, v18, v230, v231\n" \
"v_cmp_eq_u32 vcc, -1, v18\n" \
"s_cmp_gt_u32 s25, 262144\n" \
"s_cbranch_scc1 LIMR_%=\n" \
"s_cbranch_vccnz LIML_%=\n" \
"s_branch LIMR_%=\n" \
"LIN_%=:\n" \
"s_mov_b32 s25, 0\n" \
"LINL_%=:\n" \
"s_sleep 1\n" \
"s_add_u32 s98, s78, 8192\n" \
"buffer_load_dwordx4 v[232:235], v4, s[36:39], s98 offen offset:2048 sc1\n" \
"buffer_load_dwordx4 v[236:239], v4, s[36:39], s98 offen offset:3072 sc1\n" \
"s_add_u32 s25, s25, 1\n" \
"s_waitcnt vmcnt(0)\n" \
"v_max_u32 v18, v232, v233\n" \
"v_max3_u32 v18, v18, v234, v235\n" \
"v_max3_u32 v18, v18, v236, v237\n" \
"v_max3_u32 v18, v18, v238, v239\n" \
"v_cmp_eq_u32 vcc, -1, v18\n" \
"s_cmp_gt_u32 s25, 262144\n" \
"s_cbranch_scc1 LINR_%=\n" \
"s_cbranch_vccnz LINL_%=\n" \
"s_branch LINR_%=\n" \
"LPZ_%=:\n" \
"s_mov_b32 s25, 0\n" \
"LPZL_%=:\n" \
"s_sleep 2\n" \
"buffer_load_dwordx2 v[18:19], v17, s[36:39], 0 offen sc1\n" \
"s_add_u32 s25, s25, 1\n" \
"s_waitcnt vmcnt(0)\n" \
"v_cmp_ne_u64 vcc, s[14:15], v[18:19]\n" \
"s_cmp_gt_u32 s25, 262144\n" \
"s_cbranch_scc1 LPZR_%=\n" \
"s_cbranch_vccnz LPZL_%=\n" \
"s_branch LPZR_%=\n" \
"LPS_%=:\n" \
"s_mov_b32 s25, 0\n" \
"LPSL_%=:\n" \
"s_sleep 2\n" \
"buffer_load_dwordx2 v[18:19], v17, s[36:39], 0 offen sc1\n" \
"s_add_u32 s25, s25, 1\n" \
"s_waitcnt vmcnt(0)\n" \
"v_cmp_ne_u64 vcc, s[14:15], v[18:19]\n" \
"s_cmp_gt_u32 s25, 262144\n" \
"s_cbranch_scc1 LPSR_%=\n" \
"s_cbranch_vccnz LPSL_%=\n" \
"s_branch LPSR_%=\n" \
"LPL_%=:\n" \
"s_mov_b32 s25, 0\n" \
"LPLL_%=:\n" \
"s_sleep 2\n" \
"buffer_load_dwordx2 v[18:19], v17, s[36:39], 0 offen sc1\n" \
"s_add_u32 s25, s25, 1\n" \
"s_waitcnt vmcnt(0)\n" \
"v_cmp_ne_u64 vcc, s[14:15], v[18:19]\n" \
"s_cmp_gt_u32 s25, 262144\n" \
"s_cbranch_scc1 LPLR_%=\n" \
"s_cbranch_vccnz LPLL_%=\n" \
"s_branch LPLR_%=\n" \
"LPE_%=:\n" \
"s_mov_b32 s25, 0\n" \
"LPEL_%=:\n" \
"s_sleep 2\n" \
"buffer_load_dwordx2 v[18:19], v17, s[36:39], 0 offen sc1\n" \
"s_add_u32 s25, s25, 1\n" \
"s_waitcnt vmcnt(0)\n" \
"v_cmp_ne_u64 vcc, s[14:15], v[18:19]\n" \
"s_cmp_gt_u32 s25, 262144\n" \
"s_cbranch_scc1 LPER_%=\n" \
"s_cbranch_vccnz LPEL_%=\n" \
"s_branch LPER_%=\n" \
"LPE0_%=:\n" \
"s_mov_b32 s25, 0\n" \
"LPE0L_%=:\n" \
"s_sleep 2\n" \
"buffer_load_dwordx2 v[18:19], v17, s[36:39], 0 offen sc1\n" \
"s_add_u32 s25, s25, 1\n" \
"s_waitcnt vmcnt(0)\n" \
"v_cmp_ne_u64 vcc, s[14:15], v[18:19]\n" \
"s_cmp_gt_u32 s25, 262144\n" \
"s_cbranch_scc1 LPE0R_%=\n" \
"s_cbranch_vccnz LPE0L_%=\n" \
"s_branch LPE0R_%=\n" \
"LFBA_%=:\n" \
"s_mov_b32 s47, 0\n" \
"LFA_%=:\n" \
"s_lshr_b32 s13, s50, 18\n" \
"s_sub_u32 s13, s13, 1\n" \
"s_mov_b32 s18, 4\n" \
"s_mov_b64 s[54:55], 0\n" \
"s_branch LFB_%=\n" \
"LFBC_%=:\n" \
"s_mov_b32 s47, 2\n" \
"s_branch LFA_%=\n" \
"LFBB_%=:\n" \
"s_mov_b32 s47, 1\n" \
"s_lshr_b32 s13, s51, 18\n" \
"s_mov_b32 s18, 2\n" \
"s_mov_b64 s[4:5], 0\n" \
"LFB_%=:\n" \
"s_mov_b64 s[26:27], vcc\n" \
"s_lshl_b32 s13, s13, 5\n" \
"s_waitcnt vmcnt(0) lgkmcnt(0)\n" \
"s_nop 15\n" \
"s_lshl_b32 s99, %1, 2\n" \
"s_add_u32 s99, s99, %2\n" \
"s_lshl_b32 s99, s99, 13\n" \
"s_add_u32 s99, s99, 16777216\n" \
"buffer_store_dwordx4 v[64:67], v4, s[36:39], s99 offen offset:0\n" \
"buffer_store_dwordx4 v[68:71], v4, s[36:39], s99 offen offset:1024\n" \
"buffer_store_dwordx4 v[72:75], v4, s[36:39], s99 offen offset:2048\n" \
"buffer_store_dwordx4 v[76:79], v4, s[36:39], s99 offen offset:3072\n" \
"buffer_store_dwordx4 v[80:83], v7, s[36:39], s99 offen offset:0\n" \
"buffer_store_dwordx4 v[84:87], v7, s[36:39], s99 offen offset:1024\n" \
"buffer_store_dwordx4 v[88:91], v7, s[36:39], s99 offen offset:2048\n" \
"buffer_store_dwordx4 v[92:95], v7, s[36:39], s99 offen offset:3072\n" \
"s_add_u32 s99, s99, 8388608\n" \
"buffer_store_dwordx4 v[96:99], v4, s[36:39], s99 offen offset:0\n" \
"buffer_store_dwordx4 v[100:103], v4, s[36:39], s99 offen offset:1024\n" \
"buffer_store_dwordx4 v[104:107], v4, s[36:39], s99 offen offset:2048\n" \
"buffer_store_dwordx4 v[108:111], v4, s[36:39], s99 offen offset:3072\n" \
"buffer_store_dwordx4 v[112:115], v7, s[36:39], s99 offen offset:0\n" \
"buffer_store_dwordx4 v[116:119], v7, s[36:39], s99 offen offset:1024\n" \
"buffer_store_dwordx4 v[120:123], v7, s[36:39], s99 offen offset:2048\n" \
"buffer_store_dwordx4 v[124:127], v7, s[36:39], s99 offen offset:3072\n" \
"s_add_u32 s99, s99, 58720256\n" \
"buffer_store_dwordx4 v[128:131], v4, s[36:39], s99 offen offset:0\n" \
"buffer_store_dwordx4 v[132:135], v4, s[36:39], s99 offen offset:1024\n" \
"buffer_store_dwordx4 v[136:139], v4, s[36:39], s99 offen offset:2048\n" \
"buffer_store_dwordx4 v[140:143], v4, s[36:39], s99 offen offset:3072\n" \
"buffer_store_dwordx4 v[144:147], v7, s[36:39], s99 offen offset:0\n" \
"buffer_store_dwordx4 v[148:151], v7, s[36:39], s99 offen offset:1024\n" \
"buffer_store_dwordx4 v[152:155], v7, s[36:39], s99 offen offset:2048\n" \
"buffer_store_dwordx4 v[156:159], v7, s[36:39], s99 offen offset:3072\n" \
"s_add_u32 s99, s99, 37748736\n" \
"buffer_store_dwordx4 v[160:163], v4, s[36:39], s99 offen offset:0\n" \
"buffer_store_dwordx4 v[164:167], v4, s[36:39], s99 offen offset:1024\n" \
"buffer_store_dwordx4 v[168:171], v4, s[36:39], s99 offen offset:2048\n" \
"buffer_store_dwordx4 v[172:175], v4, s[36:39], s99 offen offset:3072\n" \
"s_waitcnt vmcnt(0)\n" \
"v_mov_b32 v160, 0x3f803f80\n" \
"v_mov_b32 v161, 0x3f803f80\n" \
"v_mov_b32 v162, 0x3f803f80\n" \
"v_mov_b32 v163, 0x3f803f80\n" \
"LFG_%=:\n" \
"s_mov_b32 s8, 0\n" \
"v_bfe_u32 v167, v4, 4, 4\n" \
"v_lshrrev_b32 v168, 8, v4\n" \
"v_add_u32 v165, s13, v167\n" \
"s_lshl_b32 s21, s6, 8\n" \
"v_lshl_add_u32 v128, v167, 1, s21\n" \
"v_lshl_add_u32 v128, v168, 16, v128\n" \
"v_add_u32 v129, 0x2000, v128\n" \
"v_add_u32 v130, 0x2000, v129\n" \
"v_add_u32 v131, 0x2000, v130\n" \
"v_add_u32 v132, 0x2000, v131\n" \
"v_add_u32 v133, 0x2000, v132\n" \
"v_add_u32 v134, 0x2000, v133\n" \
"v_add_u32 v135, 0x2000, v134\n" \
"LFQ_%=:\n" \
"s_load_dwordx2 s[60:61], %0, 0x0\n" \
"s_load_dwordx2 s[62:63], %0, 0x38\n" \
"v_lshlrev_b32 v166, 11, v165\n" \
"v_lshl_add_u32 v166, v168, 4, v166\n" \
"s_mov_b32 s20, 0\n" \
"v_mov_b32 v136, 0\n" \
"v_mov_b32 v137, 0\n" \
"v_mov_b32 v138, 0\n" \
"v_mov_b32 v139, 0\n" \
"v_mov_b32 v140, 0\n" \
"v_mov_b32 v141, 0\n" \
"v_mov_b32 v142, 0\n" \
"v_mov_b32 v143, 0\n" \
"s_waitcnt lgkmcnt(0)\n" \
"LFK_%=:\n" \
"global_load_dwordx4 v[112:115], v166, s[60:61]\n" \
"global_load_ushort v96, v128, s[62:63] offset:0\n" \
"global_load_ushort v97, v129, s[62:63] offset:0\n" \
"global_load_ushort v98, v130, s[62:63] offset:0\n" \
"global_load_ushort v99, v131, s[62:63] offset:0\n" \
"global_load_ushort v100, v132, s[62:63] offset:0\n" \
"global_load_ushort v101, v133, s[62:63] offset:0\n" \
"global_load_ushort v102, v134, s[62:63] offset:0\n" \
"global_load_ushort v103, v135, s[62:63] offset:0\n" \
"global_load_ushort v104, v128, s[62:63] offset:32\n" \
"global_load_ushort v105, v129, s[62:63] offset:32\n" \
"global_load_ushort v106, v130, s[62:63] offset:32\n" \
"global_load_ushort v107, v131, s[62:63] offset:32\n" \
"global_load_ushort v108, v132, s[62:63] offset:32\n" \
"global_load_ushort v109, v133, s[62:63] offset:32\n" \
"global_load_ushort v110, v134, s[62:63] offset:32\n" \
"global_load_ushort v111, v135, s[62:63] offset:32\n" \
"v_add_u32 v166, 64, v166\n" \
"s_add_u32 s62, s62, 262144\ns_addc_u32 s63, s63, 0\n" \
"s_add_u32 s20, s20, 1\n" \
"s_waitcnt vmcnt(0)\n" \
"v_lshl_or_b32 v116, v97, 16, v96\n" \
"v_lshl_or_b32 v117, v99, 16, v98\n" \
"v_lshl_or_b32 v118, v101, 16, v100\n" \
"v_lshl_or_b32 v119, v103, 16, v102\n" \
"v_lshl_or_b32 v120, v105, 16, v104\n" \
"v_lshl_or_b32 v121, v107, 16, v106\n" \
"v_lshl_or_b32 v122, v109, 16, v108\n" \
"v_lshl_or_b32 v123, v111, 16, v110\n" \
"s_nop 1\n" \
"v_mfma_f32_16x16x32_bf16 v[136:139], v[116:119], v[112:115], v[136:139]\n" \
"v_mfma_f32_16x16x32_bf16 v[140:143], v[120:123], v[112:115], v[140:143]\n" \
"s_cmp_lt_u32 s20, 32\n" \
"s_cbranch_scc1 LFK_%=\n" \
"s_nop 7\n" \
"v_mov_b32 v144, v148\n" \
"v_mov_b32 v145, v149\n" \
"v_mov_b32 v146, v150\n" \
"v_mov_b32 v147, v151\n" \
"v_mov_b32 v148, v152\n" \
"v_mov_b32 v149, v153\n" \
"v_mov_b32 v150, v154\n" \
"v_mov_b32 v151, v155\n" \
"v_mov_b32 v152, v156\n" \
"v_mov_b32 v153, v157\n" \
"v_mov_b32 v154, v158\n" \
"v_mov_b32 v155, v159\n" \
"v_cvt_pk_bf16_f32 v156, v136, v137\n" \
"v_cvt_pk_bf16_f32 v157, v138, v139\n" \
"v_cvt_pk_bf16_f32 v158, v140, v141\n" \
"v_cvt_pk_bf16_f32 v159, v142, v143\n" \
"v_add_u32 v128, 64, v128\n" \
"v_add_u32 v129, 64, v129\n" \
"v_add_u32 v130, 64, v130\n" \
"v_add_u32 v131, 64, v131\n" \
"v_add_u32 v132, 64, v132\n" \
"v_add_u32 v133, 64, v133\n" \
"v_add_u32 v134, 64, v134\n" \
"v_add_u32 v135, 64, v135\n" \
"s_add_u32 s8, s8, 1\n" \
"s_cmp_lt_u32 s8, 4\n" \
"s_cbranch_scc1 LFQ_%=\n" \
"v_mov_b32 v64, 0\n" \
"v_mov_b32 v65, 0\n" \
"v_mov_b32 v66, 0\n" \
"v_mov_b32 v67, 0\n" \
"v_mov_b32 v68, 0\n" \
"v_mov_b32 v69, 0\n" \
"v_mov_b32 v70, 0\n" \
"v_mov_b32 v71, 0\n" \
"v_mov_b32 v72, 0\n" \
"v_mov_b32 v73, 0\n" \
"v_mov_b32 v74, 0\n" \
"v_mov_b32 v75, 0\n" \
"v_mov_b32 v76, 0\n" \
"v_mov_b32 v77, 0\n" \
"v_mov_b32 v78, 0\n" \
"v_mov_b32 v79, 0\n" \
"v_mov_b32 v80, 0\n" \
"v_mov_b32 v81, 0\n" \
"v_mov_b32 v82, 0\n" \
"v_mov_b32 v83, 0\n" \
"v_mov_b32 v84, 0\n" \
"v_mov_b32 v85, 0\n" \
"v_mov_b32 v86, 0\n" \
"v_mov_b32 v87, 0\n" \
"v_mov_b32 v88, 0\n" \
"v_mov_b32 v89, 0\n" \
"v_mov_b32 v90, 0\n" \
"v_mov_b32 v91, 0\n" \
"v_mov_b32 v92, 0\n" \
"v_mov_b32 v93, 0\n" \
"v_mov_b32 v94, 0\n" \
"v_mov_b32 v95, 0\n" \
"v_mov_b32 v132, 0\n" \
"v_mov_b32 v133, 0\n" \
"v_mov_b32 v134, 0\n" \
"v_mov_b32 v135, 0\n" \
"v_mov_b32 v164, 0xff800000\n" \
"v_mov_b32 v169, 0xff800000\n" \
"s_mov_b32 s8, 0\n" \
"s_lshr_b32 s91, s13, 5\n" \
"LFP_%=:\n" \
"s_mov_b32 s20, 0\n" \
"s_lshl_b32 s21, s6, 20\n" \
"s_add_u32 s21, s21, 33554432\n" \
"v_lshlrev_b32 v166, 2, v168\n" \
"v_sub_u32 v166, v165, v166\n" \
"LFT_%=:\n" \
"buffer_load_dwordx4 v[96:99], v4, s[36:39], s21 offen offset:0 sc1\n" \
"buffer_load_dwordx4 v[100:103], v4, s[36:39], s21 offen offset:1024 sc1\n" \
"buffer_load_dwordx4 v[104:107], v4, s[36:39], s21 offen offset:2048 sc1\n" \
"buffer_load_dwordx4 v[108:111], v4, s[36:39], s21 offen offset:3072 sc1\n" \
"buffer_load_dwordx4 v[112:115], v7, s[36:39], s21 offen offset:0 sc1\n" \
"buffer_load_dwordx4 v[116:119], v7, s[36:39], s21 offen offset:1024 sc1\n" \
"buffer_load_dwordx4 v[120:123], v7, s[36:39], s21 offen offset:2048 sc1\n" \
"buffer_load_dwordx4 v[124:127], v7, s[36:39], s21 offen offset:3072 sc1\n" \
"s_add_u32 s21, s21, 8192\n" \
"s_waitcnt vmcnt(0)\n" \
"v_mfma_f32_16x16x32_bf16 v[136:139], v[96:99], v[144:147], 0\n" \
"v_mfma_f32_16x16x32_bf16 v[136:139], v[100:103], v[148:151], v[136:139]\n" \
"v_mfma_f32_16x16x32_bf16 v[136:139], v[104:107], v[152:155], v[136:139]\n" \
"v_mfma_f32_16x16x32_bf16 v[136:139], v[108:111], v[156:159], v[136:139]\n" \
"v_mfma_f32_16x16x32_bf16 v[140:143], v[112:115], v[144:147], 0\n" \
"v_mfma_f32_16x16x32_bf16 v[140:143], v[116:119], v[148:151], v[140:143]\n" \
"v_mfma_f32_16x16x32_bf16 v[140:143], v[120:123], v[152:155], v[140:143]\n" \
"v_mfma_f32_16x16x32_bf16 v[140:143], v[124:127], v[156:159], v[140:143]\n" \
"s_nop 7\n" \
"v_cmp_gt_i32 vcc, 0, v166\n" \
"s_nop 1\n" \
"v_cndmask_b32 v136, v136, v169, vcc\n" \
"v_cmp_gt_i32 vcc, 1, v166\n" \
"s_nop 1\n" \
"v_cndmask_b32 v137, v137, v169, vcc\n" \
"v_cmp_gt_i32 vcc, 2, v166\n" \
"s_nop 1\n" \
"v_cndmask_b32 v138, v138, v169, vcc\n" \
"v_cmp_gt_i32 vcc, 3, v166\n" \
"s_nop 1\n" \
"v_cndmask_b32 v139, v139, v169, vcc\n" \
"v_cmp_gt_i32 vcc, 16, v166\n" \
"s_nop 1\n" \
"v_cndmask_b32 v140, v140, v169, vcc\n" \
"v_cmp_gt_i32 vcc, 17, v166\n" \
"s_nop 1\n" \
"v_cndmask_b32 v141, v141, v169, vcc\n" \
"v_cmp_gt_i32 vcc, 18, v166\n" \
"s_nop 1\n" \
"v_cndmask_b32 v142, v142, v169, vcc\n" \
"v_cmp_gt_i32 vcc, 19, v166\n" \
"s_nop 1\n" \
"v_cndmask_b32 v143, v143, v169, vcc\n" \
"s_cmp_lg_u32 s8, 0\n" \
"s_cbranch_scc1 LF2_%=\n" \
"v_max3_f32 v164, v164, v136, v137\n" \
"v_max3_f32 v164, v164, v138, v139\n" \
"v_max3_f32 v164, v164, v140, v141\n" \
"v_max3_f32 v164, v164, v142, v143\n" \
"s_branch LFN_%=\n" \
"LF2_%=:\n" \
"buffer_load_dwordx4 v[96:99], v4, s[36:39], s21 offen offset:0 sc1\n" \
"buffer_load_dwordx4 v[100:103], v4, s[36:39], s21 offen offset:1024 sc1\n" \
"buffer_load_dwordx4 v[104:107], v4, s[36:39], s21 offen offset:2048 sc1\n" \
"buffer_load_dwordx4 v[108:111], v4, s[36:39], s21 offen offset:3072 sc1\n" \
"buffer_load_dwordx4 v[112:115], v7, s[36:39], s21 offen offset:0 sc1\n" \
"buffer_load_dwordx4 v[116:119], v7, s[36:39], s21 offen offset:1024 sc1\n" \
"buffer_load_dwordx4 v[120:123], v7, s[36:39], s21 offen offset:2048 sc1\n" \
"buffer_load_dwordx4 v[124:127], v7, s[36:39], s21 offen offset:3072 sc1\n" \
"v_sub_f32 v136, v136, v164\n" \
"v_sub_f32 v137, v137, v164\n" \
"v_sub_f32 v138, v138, v164\n" \
"v_sub_f32 v139, v139, v164\n" \
"v_sub_f32 v140, v140, v164\n" \
"v_sub_f32 v141, v141, v164\n" \
"v_sub_f32 v142, v142, v164\n" \
"v_sub_f32 v143, v143, v164\n" \
"v_exp_f32 v136, v136\n" \
"v_exp_f32 v137, v137\n" \
"v_exp_f32 v138, v138\n" \
"v_exp_f32 v139, v139\n" \
"v_exp_f32 v140, v140\n" \
"v_exp_f32 v141, v141\n" \
"v_exp_f32 v142, v142\n" \
"v_exp_f32 v143, v143\n" \
"s_nop 0\n" \
"v_cvt_pk_bf16_f32 v128, v136, v137\n" \
"v_cvt_pk_bf16_f32 v129, v138, v139\n" \
"v_cvt_pk_bf16_f32 v130, v140, v141\n" \
"v_cvt_pk_bf16_f32 v131, v142, v143\n" \
"s_waitcnt vmcnt(0)\n" \
"s_nop 1\n" \
"v_mfma_f32_16x16x32_bf16 v[132:135], v[160:163], v[128:131], v[132:135]\n" \
"v_mfma_f32_16x16x32_bf16 v[64:67], v[96:99], v[128:131], v[64:67]\n" \
"v_mfma_f32_16x16x32_bf16 v[68:71], v[100:103], v[128:131], v[68:71]\n" \
"v_mfma_f32_16x16x32_bf16 v[72:75], v[104:107], v[128:131], v[72:75]\n" \
"v_mfma_f32_16x16x32_bf16 v[76:79], v[108:111], v[128:131], v[76:79]\n" \
"v_mfma_f32_16x16x32_bf16 v[80:83], v[112:115], v[128:131], v[80:83]\n" \
"v_mfma_f32_16x16x32_bf16 v[84:87], v[116:119], v[128:131], v[84:87]\n" \
"v_mfma_f32_16x16x32_bf16 v[88:91], v[120:123], v[128:131], v[88:91]\n" \
"v_mfma_f32_16x16x32_bf16 v[92:95], v[124:127], v[128:131], v[92:95]\n" \
"LFN_%=:\n" \
"s_add_u32 s21, s21, 8192\n" \
"v_subrev_u32 v166, 32, v166\n" \
"s_add_u32 s20, s20, 1\n" \
"s_cmp_le_u32 s20, s91\n" \
"s_cbranch_scc1 LFT_%=\n" \
"v_mov_b32 v170, v164\n" \
"s_nop 1\n" \
"v_permlane32_swap_b32 v164, v170\n" \
"s_nop 0\n" \
"v_max_f32 v164, v164, v170\n" \
"v_mov_b32 v170, v164\n" \
"s_nop 1\n" \
"v_permlane16_swap_b32 v164, v170\n" \
"s_nop 0\n" \
"v_max_f32 v164, v164, v170\n" \
"s_add_u32 s8, s8, 1\n" \
"s_cmp_lt_u32 s8, 2\n" \
"s_cbranch_scc1 LFP_%=\n" \
"s_nop 7\n" \
"v_rcp_f32 v170, v132\n" \
"s_lshl_b32 s21, s91, 5\n" \
"s_add_u32 s21, s21, s6\n" \
"s_lshl_b32 s21, s21, 13\n" \
"s_and_b32 s20, s13, 16\n" \
"s_lshl_b32 s20, s20, 6\n" \
"s_add_u32 s21, s21, s20\n" \
"v_mul_f32 v64, v170, v64\n" \
"v_mul_f32 v65, v170, v65\n" \
"v_mul_f32 v66, v170, v66\n" \
"v_mul_f32 v67, v170, v67\n" \
"v_mul_f32 v68, v170, v68\n" \
"v_mul_f32 v69, v170, v69\n" \
"v_mul_f32 v70, v170, v70\n" \
"v_mul_f32 v71, v170, v71\n" \
"v_mul_f32 v72, v170, v72\n" \
"v_mul_f32 v73, v170, v73\n" \
"v_mul_f32 v74, v170, v74\n" \
"v_mul_f32 v75, v170, v75\n" \
"v_mul_f32 v76, v170, v76\n" \
"v_mul_f32 v77, v170, v77\n" \
"v_mul_f32 v78, v170, v78\n" \
"v_mul_f32 v79, v170, v79\n" \
"v_mul_f32 v80, v170, v80\n" \
"v_mul_f32 v81, v170, v81\n" \
"v_mul_f32 v82, v170, v82\n" \
"v_mul_f32 v83, v170, v83\n" \
"v_mul_f32 v84, v170, v84\n" \
"v_mul_f32 v85, v170, v85\n" \
"v_mul_f32 v86, v170, v86\n" \
"v_mul_f32 v87, v170, v87\n" \
"v_mul_f32 v88, v170, v88\n" \
"v_mul_f32 v89, v170, v89\n" \
"v_mul_f32 v90, v170, v90\n" \
"v_mul_f32 v91, v170, v91\n" \
"v_mul_f32 v92, v170, v92\n" \
"v_mul_f32 v93, v170, v93\n" \
"v_mul_f32 v94, v170, v94\n" \
"v_mul_f32 v95, v170, v95\n" \
"v_cvt_pk_bf16_f32 v64, v64, v65\n" \
"v_cvt_pk_bf16_f32 v65, v66, v67\n" \
"v_cvt_pk_bf16_f32 v66, v68, v69\n" \
"v_cvt_pk_bf16_f32 v67, v70, v71\n" \
"v_cvt_pk_bf16_f32 v68, v72, v73\n" \
"v_cvt_pk_bf16_f32 v69, v74, v75\n" \
"v_cvt_pk_bf16_f32 v70, v76, v77\n" \
"v_cvt_pk_bf16_f32 v71, v78, v79\n" \
"v_cvt_pk_bf16_f32 v72, v80, v81\n" \
"v_cvt_pk_bf16_f32 v73, v82, v83\n" \
"v_cvt_pk_bf16_f32 v74, v84, v85\n" \
"v_cvt_pk_bf16_f32 v75, v86, v87\n" \
"v_cvt_pk_bf16_f32 v76, v88, v89\n" \
"v_cvt_pk_bf16_f32 v77, v90, v91\n" \
"v_cvt_pk_bf16_f32 v78, v92, v93\n" \
"v_cvt_pk_bf16_f32 v79, v94, v95\n" \
"buffer_store_dwordx4 v[64:67], v4, s[36:39], s21 offen offset:0 sc1\n" \
"buffer_store_dwordx4 v[68:71], v4, s[36:39], s21 offen offset:2048 sc1\n" \
"buffer_store_dwordx4 v[72:75], v7, s[36:39], s21 offen offset:0 sc1\n" \
"buffer_store_dwordx4 v[76:79], v7, s[36:39], s21 offen offset:2048 sc1\n" \
"s_waitcnt vmcnt(0)\n" \
"s_add_u32 s13, s13, 16\n" \
"s_sub_u32 s18, s18, 1\n" \
"s_cmp_lg_u32 s18, 0\n" \
"s_cbranch_scc1 LFG_%=\n" \
"s_lshl_b32 s99, %1, 2\n" \
"s_add_u32 s99, s99, %2\n" \
"s_lshl_b32 s99, s99, 13\n" \
"s_add_u32 s99, s99, 16777216\n" \
"buffer_load_dwordx4 v[64:67], v4, s[36:39], s99 offen offset:0\n" \
"buffer_load_dwordx4 v[68:71], v4, s[36:39], s99 offen offset:1024\n" \
"buffer_load_dwordx4 v[72:75], v4, s[36:39], s99 offen offset:2048\n" \
"buffer_load_dwordx4 v[76:79], v4, s[36:39], s99 offen offset:3072\n" \
"buffer_load_dwordx4 v[80:83], v7, s[36:39], s99 offen offset:0\n" \
"buffer_load_dwordx4 v[84:87], v7, s[36:39], s99 offen offset:1024\n" \
"buffer_load_dwordx4 v[88:91], v7, s[36:39], s99 offen offset:2048\n" \
"buffer_load_dwordx4 v[92:95], v7, s[36:39], s99 offen offset:3072\n" \
"s_add_u32 s99, s99, 8388608\n" \
"buffer_load_dwordx4 v[96:99], v4, s[36:39], s99 offen offset:0\n" \
"buffer_load_dwordx4 v[100:103], v4, s[36:39], s99 offen offset:1024\n" \
"buffer_load_dwordx4 v[104:107], v4, s[36:39], s99 offen offset:2048\n" \
"buffer_load_dwordx4 v[108:111], v4, s[36:39], s99 offen offset:3072\n" \
"buffer_load_dwordx4 v[112:115], v7, s[36:39], s99 offen offset:0\n" \
"buffer_load_dwordx4 v[116:119], v7, s[36:39], s99 offen offset:1024\n" \
"buffer_load_dwordx4 v[120:123], v7, s[36:39], s99 offen offset:2048\n" \
"buffer_load_dwordx4 v[124:127], v7, s[36:39], s99 offen offset:3072\n" \
"s_add_u32 s99, s99, 58720256\n" \
"buffer_load_dwordx4 v[128:131], v4, s[36:39], s99 offen offset:0\n" \
"buffer_load_dwordx4 v[132:135], v4, s[36:39], s99 offen offset:1024\n" \
"buffer_load_dwordx4 v[136:139], v4, s[36:39], s99 offen offset:2048\n" \
"buffer_load_dwordx4 v[140:143], v4, s[36:39], s99 offen offset:3072\n" \
"buffer_load_dwordx4 v[144:147], v7, s[36:39], s99 offen offset:0\n" \
"buffer_load_dwordx4 v[148:151], v7, s[36:39], s99 offen offset:1024\n" \
"buffer_load_dwordx4 v[152:155], v7, s[36:39], s99 offen offset:2048\n" \
"buffer_load_dwordx4 v[156:159], v7, s[36:39], s99 offen offset:3072\n" \
"s_add_u32 s99, s99, 37748736\n" \
"buffer_load_dwordx4 v[160:163], v4, s[36:39], s99 offen offset:0\n" \
"buffer_load_dwordx4 v[164:167], v4, s[36:39], s99 offen offset:1024\n" \
"buffer_load_dwordx4 v[168:171], v4, s[36:39], s99 offen offset:2048\n" \
"buffer_load_dwordx4 v[172:175], v4, s[36:39], s99 offen offset:3072\n" \
"s_waitcnt vmcnt(0)\n" \
"s_mov_b64 vcc, s[26:27]\n" \
"s_cmp_eq_u32 s47, 0\n" \
"s_cbranch_scc1 LFBAR_%=\n" \
"s_cmp_eq_u32 s47, 1\n" \
"s_cbranch_scc1 LFBBR_%=\n" \
"s_branch LFBCR_%=\n" \
"LEND_%=:\n" \
"s_waitcnt vmcnt(0) lgkmcnt(0)\n" \
""
#define QKV_ASM_CLOBBERS "v1", "v2", "v3", "v4", "v5", "v6", "v7", "v8", "v9", "v10", "v11", "v12", "v13", "v14", "v15", "v16", "v17", "v18", "v19", "v20", "v21", "v22", "v23", "v24", "v25", "v26", "v27", "v28", "v29", "v30", "v31", "v32", "v33", "v34", "v35", "v36", "v37", "v38", "v39", "v40", "v41", "v42", "v43", "v44", "v45", "v46", "v47", "v48", "v49", "v50", "v51", "v52", "v53", "v54", "v55", "v56", "v57", "v58", "v59", "v60", "v61", "v62", "v63", "v64", "v65", "v66", "v67", "v68", "v69", "v70", "v71", "v72", "v73", "v74", "v75", "v76", "v77", "v78", "v79", "v80", "v81", "v82", "v83", "v84", "v85", "v86", "v87", "v88", "v89", "v90", "v91", "v92", "v93", "v94", "v95", "v96", "v97", "v98", "v99", "v100", "v101", "v102", "v103", "v104", "v105", "v106", "v107", "v108", "v109", "v110", "v111", "v112", "v113", "v114", "v115", "v116", "v117", "v118", "v119", "v120", "v121", "v122", "v123", "v124", "v125", "v126", "v127", "v128", "v129", "v130", "v131", "v132", "v133", "v134", "v135", "v136", "v137", "v138", "v139", "v140", "v141", "v142", "v143", "v144", "v145", "v146", "v147", "v148", "v149", "v150", "v151", "v152", "v153", "v154", "v155", "v156", "v157", "v158", "v159", "v160", "v161", "v162", "v163", "v164", "v165", "v166", "v167", "v168", "v169", "v170", "v171", "v172", "v173", "v174", "v175", "v176", "v177", "v178", "v179", "v180", "v181", "v182", "v183", "v184", "v185", "v186", "v187", "v188", "v189", "v190", "v191", "v192", "v193", "v194", "v195", "v196", "v197", "v198", "v199", "v200", "v201", "v202", "v203", "v204", "v205", "v206", "v207", "v208", "v209", "v210", "v211", "v212", "v213", "v214", "v215", "v216", "v217", "v218", "v219", "v220", "v221", "v222", "v223", "v224", "v225", "v226", "v227", "v228", "v229", "v230", "v231", "v232", "v233", "v234", "v235", "v236", "v237", "v238", "v239", "v240", "v241", "v242", "v243", "v244", "v245", "v246", "v247", "v248", "v249", "v250", "v251", "v252", "v253", "v254", "v255", "a0", "a1", "a2", "a3", "a4", "a5", "a6", "a7", "a8", "a9", "a10", "a11", "a12", "a13", "a14", "a15", "a16", "a17", "a18", "a19", "a20", "a21", "a22", "a23", "a24", "a25", "a26", "a27", "a28", "a29", "a30", "a31", "a32", "a33", "a34", "a35", "a36", "a37", "a38", "a39", "a40", "a41", "a42", "a43", "a44", "a45", "a46", "a47", "a48", "a49", "a50", "a51", "a52", "a53", "a54", "a55", "a56", "a57", "a58", "a59", "a60", "a61", "a62", "a63", "a64", "a65", "a66", "a67", "a68", "a69", "a70", "a71", "a72", "a73", "a74", "a75", "a76", "a77", "a78", "a79", "a80", "a81", "a82", "a83", "a84", "a85", "a86", "a87", "a88", "a89", "a90", "a91", "a92", "a93", "a94", "a95", "a96", "a97", "a98", "a99", "a100", "a101", "a102", "a103", "a104", "a105", "a106", "a107", "a108", "a109", "a110", "a111", "a112", "a113", "a114", "a115", "a116", "a117", "a118", "a119", "a120", "a121", "a122", "a123", "a124", "a125", "a126", "a127", "a128", "a129", "a130", "a131", "a132", "a133", "a134", "a135", "a136", "a137", "a138", "a139", "a140", "a141", "a142", "a143", "a144", "a145", "a146", "a147", "a148", "a149", "a150", "a151", "a152", "a153", "a154", "a155", "a156", "a157", "a158", "a159", "a160", "a161", "a162", "a163", "a164", "a165", "a166", "a167", "a168", "a169", "a170", "a171", "a172", "a173", "a174", "a175", "a176", "a177", "a178", "a179", "a180", "a181", "a182", "a183", "a184", "a185", "a186", "a187", "a188", "a189", "a190", "a191", "a192", "a193", "a194", "a195", "a196", "a197", "a198", "a199", "a200", "a201", "a202", "a203", "a204", "a205", "a206", "a207", "a208", "a209", "a210", "a211", "a212", "a213", "a214", "a215", "a216", "a217", "a218", "a219", "a220", "a221", "a222", "a223", "a224", "a225", "a226", "a227", "a228", "a229", "a230", "a231", "a232", "a233", "a234", "a235", "a236", "a237", "a238", "a239", "a240", "a241", "a242", "a243", "a244", "a245", "a246", "a247", "a248", "a249", "a250", "a251", "a252", "a253", "a254", "a255", "s4", "s5", "s6", "s7", "s8", "s9", "s10", "s11", "s12", "s13", "s14", "s15", "s16", "s17", "s18", "s19", "s20", "s21", "s22", "s23", "s24", "s25", "s26", "s27", "s28", "s29", "s30", "s31", "s32", "s33", "s34", "s35", "s36", "s37", "s38", "s39", "s40", "s41", "s42", "s43", "s44", "s45", "s46", "s47", "s48", "s49", "s50", "s51", "s52", "s53", "s54", "s55", "s56", "s57", "s58", "s59", "s60", "s61", "s62", "s63", "s64", "s65", "s66", "s67", "s68", "s69", "s70", "s71", "s72", "s73", "s74", "s75", "s76", "s77", "s78", "s79", "s80", "s81", "s82", "s83", "s84", "s85", "s86", "s87", "s88", "s89", "s90", "s91", "s92", "s93", "s94", "s95", "s96", "s97", "s98", "s99", "s100", "s101", "vcc", "scc", "memory"
constexpr int Q_LDS = 163840;
__global__ __launch_bounds__(256, 1) void fused_kernel(const void* x, const void* k, const void* v, const void* shk, const void* ssmk, const void* ssmD, const void* ckv,
                                                       const void* wq, const void* wuk, const void* wuv, const void* wo, const void* gw, void* out, void* ws) {
    const int w = __builtin_amdgcn_readfirstlane(threadIdx.x >> 6);
    const unsigned long long ka = (unsigned long long)__builtin_amdgcn_kernarg_segment_ptr();
    asm volatile(QKV_ASM_TEXT :: "s"(ka), "s"(blockIdx.x), "s"(w) : QKV_ASM_CLOBBERS);
}

extern "C" void kernel_launch(void* const* d_in, const int* in_sizes, int n_in, void* d_out, int out_size, void* d_ws, size_t ws_size,
                              hipStream_t stream) {
    (void)hipFuncSetAttribute((const void*)fused_kernel, hipFuncAttributeMaxDynamicSharedMemorySize, Q_LDS);
    fused_kernel<<<256, 256, Q_LDS, stream>>>(d_in[0], d_in[1], d_in[2], d_in[3], d_in[4], d_in[5], d_in[7], d_in[8], d_in[9], d_in[10], d_in[11], d_in[12], d_out, d_ws);
}
```

```cpp
#include <hip/hip_runtime.h>
#include <stdint.h>
#define QKV_ASM_TEXT \
"s_nop 0\n" \
"s_load_dwordx16 s[64:79], %0, 0x0\n" \
"s_load_dwordx8 s[80:87], %0, 0x40\n" \
"s_load_dwordx4 s[88:91], %0, 0x60\n" \
"v_mbcnt_lo_u32_b32 v23, -1, 0\n" \
"v_mbcnt_hi_u32_b32 v23, -1, v23\n" \
"s_bfe_u32 s58, %1, 0x30003\n" \
"s_and_b32 s59, %1, 7\n" \
"s_lshl_b32 s59, s59, 1\n" \
"s_bfe_u32 s60, %1, 0x10006\n" \
"s_add_u32 s59, s59, s60\n" \
"v_lshlrev_b32 v4, 4, v23\n" \
"s_getpc_b64 s[40:41]\nLCTBEG_%=:\n" \
"s_add_u32 s40, s40, LBEG_%=-LCTBEG_%=\ns_addc_u32 s41, s41, 0\n" \
"s_and_b32 s41, s41, 0xffff\n" \
"s_mov_b32 s42, LEND_%=-LBEG_%=\n" \
"s_mov_b32 s43, 0x00020000\n" \
"s_lshr_b32 s62, %1, 3\n" \
"s_lshl_b32 s62, s62, 2\n" \
"s_add_u32 s62, s62, %2\n" \
"s_and_b32 s62, s62, 31\n" \
"s_lshl_b32 s62, s62, 13\n" \
"v_lshlrev_b32 v64, 3, v4\n" \
"buffer_load_dword v64, v64, s[40:43], s62 offen\n" \
"s_lshl_b32 s60, %2, 2\n" \
"v_lshrrev_b32 v24, 4, v23\n" \
"v_add_u32 v25, s60, v24\n" \
"v_xor_b32 v25, v25, v23\n" \
"v_and_b32 v25, 7, v25\n" \
"s_lshl_b32 s61, %2, 3\n" \
"v_lshrrev_b32 v26, 3, v23\n" \
"v_add_u32 v26, s61, v26\n" \
"v_lshlrev_b32 v26, 10, v26\n" \
"v_lshl_add_u32 v13, v25, 4, v26\n" \
"v_add_u32 v63, v13, v26\n" \
"v_add_u32 v14, 0x8000, v13\n" \
"v_add_u32 v15, 0x8000, v14\n" \
"v_add_u32 v16, 0x8000, v15\n" \
"v_add_u32 v17, 0x8000, v16\n" \
"v_add_u32 v18, 0x8000, v17\n" \
"v_add_u32 v19, 0x8000, v18\n" \
"v_add_u32 v20, 0x8000, v19\n" \
"v_bfe_u32 v25, v23, 2, 3\n" \
"v_add_u32 v25, s61, v25\n" \
"v_lshlrev_b32 v25, 13, v25\n" \
"s_lshl_b32 s61, %2, 1\n" \
"v_and_b32 v26, 1, v24\n" \
"v_add_u32 v26, s61, v26\n" \
"v_xor_b32 v26, v26, v23\n" \
"v_and_b32 v26, 3, v26\n" \
"v_lshrrev_b32 v27, 5, v23\n" \
"v_lshl_add_u32 v26, v27, 2, v26\n" \
"v_lshl_add_u32 v21, v26, 4, v25\n" \
"s_lshl_b32 s50, %2, 10\n" \
"s_add_u32 s50, s50, 65536\n" \
"s_lshl_b32 s22, %2, 12\n" \
"s_lshl_b32 s51, %2, 11\n" \
"s_mov_b32 s31, 0x00020000\n" \
"s_mov_b32 s35, 0x00020000\n" \
"s_mov_b32 s30, 262144\n" \
"s_lshl_b32 s60, s58, 18\n" \
"s_lshl_b32 s61, s59, 9\n" \
"s_sub_u32 s34, 4194304, s61\n" \
"s_mov_b32 s46, s60\n" \
"s_mov_b32 s47, s61\n" \
"s_lshl_b32 s57, s59, 1\n" \
"s_and_b32 s60, %2, 1\n" \
"s_add_u32 s57, s57, s60\n" \
"s_lshl_b32 s57, s57, 20\n" \
"s_lshl_b32 s60, s58, 17\n" \
"s_add_u32 s57, s57, s60\n" \
"s_lshr_b32 s60, %2, 1\n" \
"s_lshl_b32 s60, s60, 16\n" \
"s_add_u32 s57, s57, s60\n" \
"s_add_u32 s57, s57, 33554432\n" \
"s_lshl_b32 s24, s59, 7\n" \
"s_lshl_b32 s60, s58, 3\n" \
"s_add_u32 s24, s24, s60\n" \
"s_lshl_b32 s60, %2, 27\n" \
"s_add_u32 s24, s24, s60\n" \
"s_and_b32 s23, %1, 7\n" \
"s_lshl_b32 s23, s23, 8\n" \
"s_add_u32 s23, s23, s60\n" \
"s_and_b32 s60, %1, 0xf8\n" \
"s_add_u32 s23, s23, s60\n" \
"s_add_u32 s23, s23, 94380032\n" \
"s_cmp_lt_u32 %1, 128\n" \
"s_cselect_b32 s60, 0, 8192\n" \
"s_cselect_b32 s61, 0, 64\n" \
"s_cselect_b32 s62, 10, 14\n" \
"s_cselect_b32 s63, 14, 11\n" \
"s_cselect_b32 s98, 12, 10\n" \
"s_cselect_b32 s20, 0x3e0293ee, 1.0\n" \
"s_add_u32 s57, s57, s60\n" \
"s_add_u32 s24, s24, s61\n" \
"s_add_u32 s24, s24, 94375936\n" \
"s_lshl_b32 s92, 1, s62\n" \
"s_lshl_b32 s93, 1, s63\n" \
"s_lshl_b32 s98, 1, s98\n" \
"s_and_b32 s6, %1, 7\n" \
"s_lshl_b32 s8, s6, 5\n" \
"s_lshl_b32 s6, s6, 2\n" \
"s_lshr_b32 s58, %1, 6\n" \
"s_add_u32 s6, s6, s58\n" \
"s_lshr_b32 s58, %1, 3\n" \
"s_add_u32 s8, s8, s58\n" \
"s_and_b32 s7, s58, 7\n" \
"s_lshl_b32 s58, s6, 8\n" \
"s_mov_b32 s94, s58\n" \
"s_sub_u32 s49, 8388608, s58\n" \
"s_waitcnt lgkmcnt(0)\n" \
"s_cmp_lt_u32 %1, 128\n" \
"s_cselect_b64 s[32:33], s[80:81], s[82:83]\n" \
"s_add_u32 s28, s76, s46\ns_addc_u32 s29, s77, 0\n" \
"s_and_b32 s29, s29, 0xffff\n" \
"s_add_u32 s32, s32, s47\ns_addc_u32 s33, s33, 0\n" \
"s_and_b32 s33, s33, 0xffff\n" \
"s_mov_b32 s99, 94371840\n" \
"s_load_dwordx2 s[14:15], s[90:91], s99\n" \
"s_mov_b32 s46, 0\n" \
"s_add_u32 m0, s50, 65536\n" \
"s_mov_b64 s[36:37], s[90:91]\n" \
"buffer_load_dwordx4 v13, s[28:31], s46 offen lds\n" \
"s_and_b32 s37, s37, 0xffff\n" \
"s_mov_b32 s38, 134217728\n" \
"s_mov_b32 s39, 0x00020000\n" \
"v_lshrrev_b32 v23, 4, v4\n" \
"s_add_u32 m0, s50, 69632\n" \
"v_and_b32 v24, 15, v23\n" \
"buffer_load_dwordx4 v14, s[28:31], s46 offen lds\n" \
"v_lshrrev_b32 v25, 4, v23\n" \
"s_lshr_b32 s58, %2, 1\n" \
"s_lshl_b32 s58, s58, 14\n" \
"s_and_b32 s54, %2, 1\n" \
"s_add_u32 m0, s50, 73728\n" \
"s_lshl_b32 s54, s54, 13\n" \
"buffer_load_dwordx4 v15, s[28:31], s46 offen lds\n" \
"s_add_u32 s58, s58, 65536\n" \
"v_lshrrev_b32 v26, 1, v24\n" \
"v_xor_b32 v26, v26, v25\n" \
"v_lshlrev_b32 v26, 4, v26\n" \
"s_add_u32 m0, s50, 77824\n" \
"v_lshl_add_u32 v26, v24, 7, v26\n" \
"buffer_load_dwordx4 v16, s[28:31], s46 offen lds\n" \
"v_add_u32 v5, s58, v26\n" \
"v_xor_b32 v6, 64, v5\n" \
"v_add_u32 v7, 0x10000, v5\n" \
"v_add_u32 v8, 0x10000, v6\n" \
"s_mov_b32 s47, 0\n" \
"s_add_u32 m0, s22, 0\n" \
"s_and_b32 s58, %2, 1\n" \
"buffer_load_dwordx4 v21, s[32:35], s47 offen offset:0 lds\n" \
"s_lshl_b32 s58, s58, 11\n" \
"v_lshrrev_b32 v26, 2, v24\n" \
"v_lshlrev_b32 v26, 6, v26\n" \
"v_and_b32 v27, 1, v23\n" \
"s_mov_b32 s46, 0\n" \
"s_add_u32 m0, s50, 81920\n" \
"v_lshl_add_u32 v26, v27, 3, v26\n" \
"buffer_load_dwordx4 v17, s[28:31], s46 offen lds\n" \
"v_bfe_u32 v27, v23, 1, 1\n" \
"v_lshlrev_b32 v28, 1, v25\n" \
"v_xor_b32 v27, v27, v28\n" \
"v_and_b32 v27, 3, v27\n" \
"s_add_u32 m0, s22, 896\n" \
"v_lshl_add_u32 v26, v27, 4, v26\n" \
"buffer_load_dwordx4 v21, s[32:35], s47 offen offset:128 lds\n" \
"v_lshl_add_u32 v9, v25, 12, v26\n" \
"v_add_u32 v9, s58, v9\n" \
"v_lshl_add_u32 v62, v25, 11, s58\n" \
"v_xor_b32 v10, 0x110, v9\n" \
"s_add_u32 m0, s50, 86016\n" \
"v_xor_b32 v11, 32, v9\n" \
"buffer_load_dwordx4 v18, s[28:31], s46 offen lds\n" \
"v_xor_b32 v12, 0x130, v9\n" \
"s_and_b32 s62, %1, 7\n" \
"s_lshl_b32 s62, s62, 7\n" \
"s_lshl_b32 s63, %2, 6\n" \
"s_add_u32 m0, s22, 1792\n" \
"s_and_b32 s63, s63, 127\n" \
"buffer_load_dwordx4 v21, s[32:35], s47 offen offset:256 lds\n" \
"s_add_u32 s62, s62, s63\n" \
"v_bfe_u32 v24, v4, 4, 4\n" \
"v_lshl_add_u32 v24, v24, 2, s62\n" \
"s_lshr_b32 s62, %2, 1\n" \
"s_add_u32 m0, s50, 90112\n" \
"s_lshl_b32 s62, s62, 2\n" \
"buffer_load_dwordx4 v19, s[28:31], s46 offen lds\n" \
"s_and_b32 s63, %1, 0xf8\n" \
"s_add_u32 s63, s63, s62\n" \
"v_lshrrev_b32 v25, 8, v4\n" \
"v_lshrrev_b32 v23, 7, v4\n" \
"s_add_u32 m0, s22, 2688\n" \
"v_add_u32 v25, s63, v25\n" \
"buffer_load_dwordx4 v21, s[32:35], s47 offen offset:384 lds\n" \
"v_lshlrev_b32 v26, 14, v25\n" \
"v_lshl_add_u32 v59, v24, 1, v26\n" \
"v_add_u32 v54, 0xffffe800, v59\n" \
"v_add_u32 v55, 0xfffff800, v59\n" \
"s_add_u32 m0, s50, 94208\n" \
"v_lshlrev_b32 v60, 12, v24\n" \
"buffer_load_dwordx4 v20, s[28:31], s46 offen lds\n" \
"v_lshl_add_u32 v60, v25, 4, v60\n" \
"v_lshlrev_b32 v27, 3, v24\n" \
"s_lshl_b32 s62, %1, 2\n" \
"s_add_u32 s62, s62, %2\n" \
"s_mov_b32 s47, 262144\n" \
"s_add_u32 m0, s22, 16384\n" \
"s_lshl_b32 s63, s62, 12\n" \
"buffer_load_dwordx4 v21, s[32:35], s47 offen offset:0 lds\n" \
"v_add_u32 v26, s63, v4\n" \
"s_and_b32 s63, s62, 7\n" \
"s_lshl_b32 s63, s63, 4\n" \
"s_add_u32 s63, s63, 71303168\n" \
"s_add_u32 m0, s22, 17280\n" \
"v_and_b32 v61, 4, v23\n" \
"buffer_load_dwordx4 v21, s[32:35], s47 offen offset:128 lds\n" \
"v_lshl_add_u32 v61, v61, 1, s63\n" \
"s_bfe_u32 s63, s62, 0x10003\n" \
"s_lshl_b32 s63, s63, 2\n" \
"s_lshr_b32 s62, s62, 4\n" \
"s_add_u32 m0, s22, 18176\n" \
"s_lshl_b32 s62, s62, 7\n" \
"buffer_load_dwordx4 v21, s[32:35], s47 offen offset:256 lds\n" \
"s_add_u32 s62, s62, s63\n" \
"v_add_u32 v61, s62, v61\n" \
"v_lshlrev_b32 v2, 3, v4\n" \
"v_and_b32 v1, 0x380, v2\n" \
"s_add_u32 m0, s22, 19072\n" \
"v_lshrrev_b32 v23, 7, v4\n" \
"buffer_load_dwordx4 v21, s[32:35], s47 offen offset:384 lds\n" \
"v_lshl_add_u32 v1, v23, 13, v1\n" \
"s_lshr_b32 s58, %1, 3\n" \
"s_lshl_b32 s58, s58, 2\n" \
"s_add_u32 s58, s58, %2\n" \
"s_mov_b32 s47, 524288\n" \
"s_add_u32 m0, s22, 32768\n" \
"s_and_b32 s60, %1, 7\n" \
"buffer_load_dwordx4 v21, s[32:35], s47 offen offset:0 lds\n" \
"s_lshl_b32 s60, s60, 10\n" \
"s_lshl_b32 s59, s58, 14\n" \
"s_add_u32 s59, s59, s60\n" \
"s_mov_b64 s[4:5], s[90:91]\n" \
"s_add_u32 m0, s22, 33664\n" \
"s_lshl_b32 s95, s58, 11\n" \
"buffer_load_dwordx4 v21, s[32:35], s47 offen offset:128 lds\n" \
"s_lshl_b32 s96, s60, 8\n" \
"s_add_u32 s95, s95, s96\n" \
"s_add_u32 s96, s76, s95\ns_addc_u32 s97, s77, 0\n" \
"s_add_u32 s76, s78, s59\ns_addc_u32 s77, s79, 0\n" \
"s_add_u32 m0, s22, 34560\n" \
"s_and_b32 s59, s59, 1048575\n" \
"buffer_load_dwordx4 v21, s[32:35], s47 offen offset:256 lds\n" \
"s_cmp_lt_u32 s58, 64\ns_cselect_b64 s[84:85], s[80:81], s[82:83]\n" \
"s_add_u32 s84, s84, s59\ns_addc_u32 s85, s85, 0\n" \
"s_lshl_b32 s59, s8, 2\n" \
"s_add_u32 s59, s59, %2\n" \
"s_add_u32 m0, s22, 35456\n" \
"s_lshl_b32 s59, s59, 12\n" \
"buffer_load_dwordx4 v21, s[32:35], s47 offen offset:384 lds\n" \
"s_add_u32 s88, s72, s59\ns_addc_u32 s89, s73, 0\n" \
"s_mov_b64 s[4:5], s[90:91]\n" \
"s_mov_b64 s[10:11], s[72:73]\n" \
"s_mov_b64 s[12:13], s[74:75]\n" \
"s_mov_b32 s46, 128\n" \
"s_add_u32 m0, s50, 0\n" \
"s_add_u32 s44, s78, s94\ns_addc_u32 s45, s79, 0\n" \
"buffer_load_dwordx4 v13, s[28:31], s46 offen lds\n" \
"s_mov_b64 s[40:41], s[66:67]\n" \
"s_mov_b64 s[80:81], s[68:69]\n" \
"s_and_b32 s81, s81, 0xffff\n" \
"s_mov_b32 s82, 4194304\n" \
"s_add_u32 m0, s50, 4096\n" \
"s_mov_b32 s83, 0x00020000\n" \
"buffer_load_dwordx4 v14, s[28:31], s46 offen lds\n" \
"s_and_b32 s45, s45, 0xffff\n" \
"s_and_b32 s41, s41, 0xffff\n" \
"s_mov_b32 s42, 4194304\n" \
"s_mov_b32 s43, 0x00020000\n" \
"s_mov_b64 exec, 0xffff\nglobal_load_dword v22, v1, s[84:85]\ns_mov_b64 exec, -1\n" \
"s_mov_b64 exec, 0xffff\nglobal_load_dword v22, v2, s[96:97]\ns_mov_b64 exec, -1\n" \
"s_cmp_lt_u32 %1, 128\ns_cbranch_scc0 LHV_%=\ns_mov_b64 exec, 0\ns_cmp_eq_u32 %2, 0\ns_cbranch_scc1 LH0_%=\ns_cmp_eq_u32 %2, 1\ns_cbranch_scc1 LH1_%=\ns_cmp_eq_u32 %2, 2\ns_cbranch_scc1 LH2_%=\ns_cmp_eq_u32 %2, 3\ns_cbranch_scc1 LH3_%=\nLHV_%=:\ns_mov_b64 exec, 0\ns_cmp_eq_u32 %2, 0\ns_cbranch_scc1 LH4_%=\ns_cmp_eq_u32 %2, 1\ns_cbranch_scc1 LH5_%=\ns_cmp_eq_u32 %2, 2\ns_cbranch_scc1 LH6_%=\ns_cmp_eq_u32 %2, 3\ns_cbranch_scc1 LH7_%=\nLHR0_%=:\ns_mov_b64 exec, -1\n" \
"s_waitcnt vmcnt(8)\n" \
"s_barrier\n" \
"s_add_u32 m0, s50, 8192\n" \
"ds_read_b128 a[0:3], v7 offset:0\n" \
"ds_read_b64_tr_b16 a[32:33], v9 offset:0\n" \
"ds_read_b64_tr_b16 a[34:35], v10 offset:0\n" \
"ds_read_b128 a[4:7], v7 offset:2048\n" \
"buffer_load_dwordx4 v15, s[28:31], s46 offen lds\n" \
"s_add_u32 m0, s50, 12288\n" \
"ds_read_b64_tr_b16 a[36:37], v11 offset:0\n" \
"ds_read_b64_tr_b16 a[38:39], v12 offset:0\n" \
"ds_read_b128 a[8:11], v7 offset:4096\n" \
"ds_read_b64_tr_b16 a[40:41], v9 offset:512\n" \
"buffer_load_dwordx4 v16, s[28:31], s46 offen lds\n" \
"s_mov_b32 s46, 128\n" \
"s_add_u32 m0, s50, 16384\n" \
"ds_read_b64_tr_b16 a[42:43], v10 offset:512\n" \
"ds_read_b128 a[12:15], v7 offset:6144\n" \
"ds_read_b64_tr_b16 a[44:45], v11 offset:512\n" \
"ds_read_b64_tr_b16 a[46:47], v12 offset:512\n" \
"buffer_load_dwordx4 v17, s[28:31], s46 offen lds\n" \
"s_add_u32 m0, s50, 20480\n" \
"ds_read_b128 a[16:19], v7 offset:8192\n" \
"ds_read_b64_tr_b16 a[48:49], v9 offset:1024\n" \
"ds_read_b64_tr_b16 a[50:51], v10 offset:1024\n" \
"ds_read_b128 a[20:23], v7 offset:10240\n" \
"buffer_load_dwordx4 v18, s[28:31], s46 offen lds\n" \
"s_add_u32 m0, s50, 24576\n" \
"ds_read_b64_tr_b16 a[52:53], v11 offset:1024\n" \
"ds_read_b64_tr_b16 a[54:55], v12 offset:1024\n" \
"ds_read_b128 a[24:27], v7 offset:12288\n" \
"ds_read_b64_tr_b16 a[56:57], v9 offset:1536\n" \
"buffer_load_dwordx4 v19, s[28:31], s46 offen lds\n" \
"s_add_u32 m0, s50, 28672\n" \
"ds_read_b64_tr_b16 a[58:59], v10 offset:1536\n" \
"ds_read_b128 a[28:31], v7 offset:14336\n" \
"ds_read_b64_tr_b16 a[60:61], v11 offset:1536\n" \
"ds_read_b64_tr_b16 a[62:63], v12 offset:1536\n" \
"buffer_load_dwordx4 v20, s[28:31], s46 offen lds\n" \
"s_waitcnt lgkmcnt(0)\n" \
"s_branch LBEG_%=\n" \
".p2align 12\n" \
"LBEG_%=:\n" \
"s_cmp_lt_u32 %1, 128\n" \
"s_cbranch_scc0 LAV_%=\n" \
"v_mfma_f32_16x16x32_bf16 v[64:67], a[32:35], a[0:3], 0\n" \
"s_add_u32 s14, s14, 1\ns_addc_u32 s15, s15, 0\n" \
"ds_read_b128 a[64:67], v8 offset:0\n" \
"v_mfma_f32_16x16x32_bf16 v[68:71], a[36:39], a[0:3], 0\n" \
"v_mfma_f32_16x16x32_bf16 v[72:75], a[32:35], a[4:7], 0\n" \
"s_mov_b32 s47, 786432\n" \
"s_add_u32 m0, s22, 49152\n" \
"ds_read_b64_tr_b16 a[96:97], v9 offset:16384\n" \
"v_mfma_f32_16x16x32_bf16 v[76:79], a[36:39], a[4:7], 0\n" \
"buffer_load_dwordx4 v21, s[32:35], s47 offen offset:0 lds\n" \
"v_mfma_f32_16x16x32_bf16 v[80:83], a[40:43], a[0:3], 0\n" \
"ds_read_b64_tr_b16 a[98:99], v10 offset:16384\n" \
"v_mfma_f32_16x16x32_bf16 v[84:87], a[44:47], a[0:3], 0\n" \
"v_mfma_f32_16x16x32_bf16 v[88:91], a[40:43], a[4:7], 0\n" \
"s_add_u32 m0, s22, 50048\n" \
"ds_read_b128 a[68:71], v8 offset:2048\n" \
"v_mfma_f32_16x16x32_bf16 v[92:95], a[44:47], a[4:7], 0\n" \
"buffer_load_dwordx4 v21, s[32:35], s47 offen offset:128 lds\n" \
"v_mfma_f32_16x16x32_bf16 v[96:99], a[48:51], a[0:3], 0\n" \
"ds_read_b64_tr_b16 a[100:101], v11 offset:16384\n" \
"v_mfma_f32_16x16x32_bf16 v[100:103], a[52:55], a[0:3], 0\n" \
"v_mfma_f32_16x16x32_bf16 v[104:107], a[48:51], a[4:7], 0\n" \
"s_add_u32 m0, s22, 50944\n" \
"ds_read_b64_tr_b16 a[102:103], v12 offset:16384\n" \
"v_mfma_f32_16x16x32_bf16 v[108:111], a[52:55], a[4:7], 0\n" \
"buffer_load_dwordx4 v21, s[32:35], s47 offen offset:256 lds\n" \
"v_mfma_f32_16x16x32_bf16 v[112:115], a[56:59], a[0:3], 0\n" \
"ds_read_b128 a[72:75], v8 offset:4096\n" \
"v_mfma_f32_16x16x32_bf16 v[116:119], a[60:63], a[0:3], 0\n" \
"v_mfma_f32_16x16x32_bf16 v[120:123], a[56:59], a[4:7], 0\n" \
"s_add_u32 m0, s22, 51840\n" \
"ds_read_b64_tr_b16 a[104:105], v9 offset:16896\n" \
"v_mfma_f32_16x16x32_bf16 v[124:127], a[60:63], a[4:7], 0\n" \
"buffer_load_dwordx4 v21, s[32:35], s47 offen offset:384 lds\n" \
"v_mfma_f32_16x16x32_bf16 v[128:131], a[32:35], a[8:11], 0\n" \
"ds_read_b64_tr_b16 a[106:107], v10 offset:16896\n" \
"v_mfma_f32_16x16x32_bf16 v[132:135], a[36:39], a[8:11], 0\n" \
"v_mfma_f32_16x16x32_bf16 v[136:139], a[32:35], a[12:15], 0\n" \
"s_mov_b32 s46, 256\n" \
"s_add_u32 m0, s50, 32768\n" \
"ds_read_b128 a[76:79], v8 offset:6144\n" \
"v_mfma_f32_16x16x32_bf16 v[140:143], a[36:39], a[12:15], 0\n" \
"buffer_load_dwordx4 v13, s[28:31], s46 offen lds\n" \
"v_mfma_f32_16x16x32_bf16 v[144:147], a[40:43], a[8:11], 0\n" \
"ds_read_b64_tr_b16 a[108:109], v11 offset:16896\n" \
"v_mfma_f32_16x16x32_bf16 v[148:151], a[44:47], a[8:11], 0\n" \
"v_mfma_f32_16x16x32_bf16 v[152:155], a[40:43], a[12:15], 0\n" \
"s_add_u32 m0, s50, 36864\n" \
"ds_read_b64_tr_b16 a[110:111], v12 offset:16896\n" \
"v_mfma_f32_16x16x32_bf16 v[156:159], a[44:47], a[12:15], 0\n" \
"buffer_load_dwordx4 v14, s[28:31], s46 offen lds\n" \
"v_mfma_f32_16x16x32_bf16 v[160:163], a[48:51], a[8:11], 0\n" \
"ds_read_b128 a[80:83], v8 offset:8192\n" \
"v_mfma_f32_16x16x32_bf16 v[164:167], a[52:55], a[8:11], 0\n" \
"v_mfma_f32_16x16x32_bf16 v[168:171], a[48:51], a[12:15], 0\n" \
"s_add_u32 m0, s50, 40960\n" \
"ds_read_b64_tr_b16 a[112:113], v9 offset:17408\n" \
"v_mfma_f32_16x16x32_bf16 v[172:175], a[52:55], a[12:15], 0\n" \
"buffer_load_dwordx4 v15, s[28:31], s46 offen lds\n" \
"v_mfma_f32_16x16x32_bf16 v[176:179], a[56:59], a[8:11], 0\n" \
"ds_read_b64_tr_b16 a[114:115], v10 offset:17408\n" \
"v_mfma_f32_16x16x32_bf16 v[180:183], a[60:63], a[8:11], 0\n" \
"v_mfma_f32_16x16x32_bf16 v[184:187], a[56:59], a[12:15], 0\n" \
"s_add_u32 m0, s50, 45056\n" \
"ds_read_b128 a[84:87], v8 offset:10240\n" \
"v_mfma_f32_16x16x32_bf16 v[188:191], a[60:63], a[12:15], 0\n" \
"buffer_load_dwordx4 v16, s[28:31], s46 offen lds\n" \
"v_mfma_f32_16x16x32_bf16 v[192:195], a[32:35], a[16:19], 0\n" \
"ds_read_b64_tr_b16 a[116:117], v11 offset:17408\n" \
"v_mfma_f32_16x16x32_bf16 v[196:199], a[36:39], a[16:19], 0\n" \
"v_mfma_f32_16x16x32_bf16 v[200:203], a[32:35], a[20:23], 0\n" \
"s_mov_b32 s46, 256\n" \
"s_add_u32 m0, s50, 49152\n" \
"ds_read_b64_tr_b16 a[118:119], v12 offset:17408\n" \
"v_mfma_f32_16x16x32_bf16 v[204:207], a[36:39], a[20:23], 0\n" \
"buffer_load_dwordx4 v17, s[28:31], s46 offen lds\n" \
"v_mfma_f32_16x16x32_bf16 v[208:211], a[40:43], a[16:19], 0\n" \
"ds_read_b128 a[88:91], v8 offset:12288\n" \
"v_mfma_f32_16x16x32_bf16 v[212:215], a[44:47], a[16:19], 0\n" \
"v_mfma_f32_16x16x32_bf16 v[216:219], a[40:43], a[20:23], 0\n" \
"s_add_u32 m0, s50, 53248\n" \
"ds_read_b64_tr_b16 a[120:121], v9 offset:17920\n" \
"v_mfma_f32_16x16x32_bf16 v[220:223], a[44:47], a[20:23], 0\n" \
"buffer_load_dwordx4 v18, s[28:31], s46 offen lds\n" \
"v_mfma_f32_16x16x32_bf16 v[224:227], a[48:51], a[16:19], 0\n" \
"ds_read_b64_tr_b16 a[122:123], v10 offset:17920\n" \
"v_mfma_f32_16x16x32_bf16 v[228:231], a[52:55], a[16:19], 0\n" \
"v_mfma_f32_16x16x32_bf16 v[232:235], a[48:51], a[20:23], 0\n" \
"s_add_u32 m0, s50, 57344\n" \
"ds_read_b128 a[92:95], v8 offset:14336\n" \
"v_mfma_f32_16x16x32_bf16 v[236:239], a[52:55], a[20:23], 0\n" \
"buffer_load_dwordx4 v19, s[28:31], s46 offen lds\n" \
"v_mfma_f32_16x16x32_bf16 v[240:243], a[56:59], a[16:19], 0\n" \
"ds_read_b64_tr_b16 a[124:125], v11 offset:17920\n" \
"v_mfma_f32_16x16x32_bf16 v[244:247], a[60:63], a[16:19], 0\n" \
"v_mfma_f32_16x16x32_bf16 v[248:251], a[56:59], a[20:23], 0\n" \
"s_add_u32 m0, s50, 61440\n" \
"ds_read_b64_tr_b16 a[126:127], v12 offset:17920\n" \
"v_mfma_f32_16x16x32_bf16 v[252:255], a[60:63], a[20:23], 0\n" \
"buffer_load_dwordx4 v20, s[28:31], s46 offen lds\n" \
"v_mfma_f32_16x16x32_bf16 a[128:131], a[32:35], a[24:27], 0\n" \
"v_mfma_f32_16x16x32_bf16 a[132:135], a[36:39], a[24:27], 0\n" \
"v_mfma_f32_16x16x32_bf16 a[136:139], a[32:35], a[28:31], 0\n" \
"v_mfma_f32_16x16x32_bf16 a[140:143], a[36:39], a[28:31], 0\n" \
"v_mfma_f32_16x16x32_bf16 a[144:147], a[40:43], a[24:27], 0\n" \
"global_load_dwordx4 v[46:49], v4, s[86:87] offset:0\n" \
"v_mfma_f32_16x16x32_bf16 a[148:151], a[44:47], a[24:27], 0\n" \
"v_mfma_f32_16x16x32_bf16 a[152:155], a[40:43], a[28:31], 0\n" \
"v_mfma_f32_16x16x32_bf16 a[156:159], a[44:47], a[28:31], 0\n" \
"s_waitcnt vmcnt(13) lgkmcnt(0)\n" \
"v_mfma_f32_16x16x32_bf16 a[160:163], a[48:51], a[24:27], 0\n" \
"s_barrier\n" \
"v_mfma_f32_16x16x32_bf16 a[164:167], a[52:55], a[24:27], 0\n" \
"global_load_dwordx4 v[50:53], v4, s[86:87] offset:1024\n" \
"v_mfma_f32_16x16x32_bf16 a[168:171], a[48:51], a[28:31], 0\n" \
"v_mfma_f32_16x16x32_bf16 a[172:175], a[52:55], a[28:31], 0\n" \
"v_mfma_f32_16x16x32_bf16 a[176:179], a[56:59], a[24:27], 0\n" \
"v_mfma_f32_16x16x32_bf16 a[180:183], a[60:63], a[24:27], 0\n" \
"global_load_dwordx4 v[30:33], v26, s[64:65] offset:0\n" \
"v_mfma_f32_16x16x32_bf16 a[184:187], a[56:59], a[28:31], 0\n" \
"v_mfma_f32_16x16x32_bf16 a[188:191], a[60:63], a[28:31], 0\n" \
"v_mfma_f32_16x16x32_bf16 v[64:67], a[96:99], a[64:67], v[64:67]\n" \
"ds_read_b128 a[0:3], v5 offset:0\n" \
"v_mfma_f32_16x16x32_bf16 v[68:71], a[100:103], a[64:67], v[68:71]\n" \
"v_mfma_f32_16x16x32_bf16 v[72:75], a[96:99], a[68:71], v[72:75]\n" \
"s_mov_b32 s47, 1048576\n" \
"s_add_u32 m0, s22, 0\n" \
"ds_read_b64_tr_b16 a[32:33], v9 offset:32768\n" \
"v_mfma_f32_16x16x32_bf16 v[76:79], a[100:103], a[68:71], v[76:79]\n" \
"buffer_load_dwordx4 v21, s[32:35], s47 offen offset:0 lds\n" \
"v_mfma_f32_16x16x32_bf16 v[80:83], a[104:107], a[64:67], v[80:83]\n" \
"ds_read_b64_tr_b16 a[34:35], v10 offset:32768\n" \
"v_mfma_f32_16x16x32_bf16 v[84:87], a[108:111], a[64:67], v[84:87]\n" \
"v_mfma_f32_16x16x32_bf16 v[88:91], a[104:107], a[68:71], v[88:91]\n" \
"s_add_u32 m0, s22, 896\n" \
"ds_read_b128 a[4:7], v5 offset:2048\n" \
"v_mfma_f32_16x16x32_bf16 v[92:95], a[108:111], a[68:71], v[92:95]\n" \
"buffer_load_dwordx4 v21, s[32:35], s47 offen offset:128 lds\n" \
"v_mfma_f32_16x16x32_bf16 v[96:99], a[112:115], a[64:67], v[96:99]\n" \
"ds_read_b64_tr_b16 a[36:37], v11 offset:32768\n" \
"v_mfma_f32_16x16x32_bf16 v[100:103], a[116:119], a[64:67], v[100:103]\n" \
"v_mfma_f32_16x16x32_bf16 v[104:107], a[112:115], a[68:71], v[104:107]\n" \
"s_add_u32 m0, s22, 1792\n" \
"ds_read_b64_tr_b16 a[38:39], v12 offset:32768\n" \
"v_mfma_f32_16x16x32_bf16 v[108:111], a[116:119], a[68:71], v[108:111]\n" \
"buffer_load_dwordx4 v21, s[32:35], s47 offen offset:256 lds\n" \
"v_mfma_f32_16x16x32_bf16 v[112:115], a[120:123], a[64:67], v[112:115]\n" \
"ds_read_b128 a[8:11], v5 offset:4096\n" \
"v_mfma_f32_16x16x32_bf16 v[116:119], a[124:127], a[64:67], v[116:119]\n" \
"v_mfma_f32_16x16x32_bf16 v[120:123], a[120:123], a[68:71], v[120:123]\n" \
"s_add_u32 m0, s22, 2688\n" \
"ds_read_b64_tr_b16 a[40:41], v9 offset:33280\n" \
"v_mfma_f32_16x16x32_bf16 v[124:127], a[124:127], a[68:71], v[124:127]\n" \
"buffer_load_dwordx4 v21, s[32:35], s47 offen offset:384 lds\n" \
"v_mfma_f32_16x16x32_bf16 v[128:131], a[96:99], a[72:75], v[128:131]\n" \
"ds_read_b64_tr_b16 a[42:43], v10 offset:33280\n" \
"v_mfma_f32_16x16x32_bf16 v[132:135], a[100:103], a[72:75], v[132:135]\n" \
"v_mfma_f32_16x16x32_bf16 v[136:139], a[96:99], a[76:79], v[136:139]\n" \
"s_mov_b32 s47, 1310720\n" \
"s_add_u32 m0, s22, 16384\n" \
"ds_read_b128 a[12:15], v5 offset:6144\n" \
"v_mfma_f32_16x16x32_bf16 v[140:143], a[100:103], a[76:79], v[140:143]\n" \
"buffer_load_dwordx4 v21, s[32:35], s47 offen offset:0 lds\n" \
"v_mfma_f32_16x16x32_bf16 v[144:147], a[104:107], a[72:75], v[144:147]\n" \
"ds_read_b64_tr_b16 a[44:45], v11 offset:33280\n" \
"v_mfma_f32_16x16x32_bf16 v[148:151], a[108:111], a[72:75], v[148:151]\n" \
"v_mfma_f32_16x16x32_bf16 v[152:155], a[104:107], a[76:79], v[152:155]\n" \
"s_mov_b32 s46, 384\n" \
"s_add_u32 m0, s50, 65536\n" \
"ds_read_b64_tr_b16 a[46:47], v12 offset:33280\n" \
"v_mfma_f32_16x16x32_bf16 v[156:159], a[108:111], a[76:79], v[156:159]\n" \
"buffer_load_dwordx4 v13, s[28:31], s46 offen lds\n" \
"v_mfma_f32_16x16x32_bf16 v[160:163], a[112:115], a[72:75], v[160:163]\n" \
"ds_read_b128 a[16:19], v5 offset:8192\n" \
"v_mfma_f32_16x16x32_bf16 v[164:167], a[116:119], a[72:75], v[164:167]\n" \
"v_mfma_f32_16x16x32_bf16 v[168:171], a[112:115], a[76:79], v[168:171]\n" \
"s_add_u32 m0, s22, 17280\n" \
"ds_read_b64_tr_b16 a[48:49], v9 offset:33792\n" \
"v_mfma_f32_16x16x32_bf16 v[172:175], a[116:119], a[76:79], v[172:175]\n" \
"buffer_load_dwordx4 v21, s[32:35], s47 offen offset:128 lds\n" \
"v_mfma_f32_16x16x32_bf16 v[176:179], a[120:123], a[72:75], v[176:179]\n" \
"ds_read_b64_tr_b16 a[50:51], v10 offset:33792\n" \
"v_mfma_f32_16x16x32_bf16 v[180:183], a[124:127], a[72:75], v[180:183]\n" \
"v_mfma_f32_16x16x32_bf16 v[184:187], a[120:123], a[76:79], v[184:187]\n" \
"s_add_u32 m0, s50, 69632\n" \
"ds_read_b128 a[20:23], v5 offset:10240\n" \
"v_mfma_f32_16x16x32_bf16 v[188:191], a[124:127], a[76:79], v[188:191]\n" \
"buffer_load_dwordx4 v14, s[28:31], s46 offen lds\n" \
"v_mfma_f32_16x16x32_bf16 v[192:195], a[96:99], a[80:83], v[192:195]\n" \
"ds_read_b64_tr_b16 a[52:53], v11 offset:33792\n" \
"v_mfma_f32_16x16x32_bf16 v[196:199], a[100:103], a[80:83], v[196:199]\n" \
"v_mfma_f32_16x16x32_bf16 v[200:203], a[96:99], a[84:87], v[200:203]\n" \
"s_add_u32 m0, s22, 18176\n" \
"ds_read_b64_tr_b16 a[54:55], v12 offset:33792\n" \
"v_mfma_f32_16x16x32_bf16 v[204:207], a[100:103], a[84:87], v[204:207]\n" \
"buffer_load_dwordx4 v21, s[32:35], s47 offen offset:256 lds\n" \
"v_mfma_f32_16x16x32_bf16 v[208:211], a[104:107], a[80:83], v[208:211]\n" \
"ds_read_b128 a[24:27], v5 offset:12288\n" \
"v_mfma_f32_16x16x32_bf16 v[212:215], a[108:111], a[80:83], v[212:215]\n" \
"v_mfma_f32_16x16x32_bf16 v[216:219], a[104:107], a[84:87], v[216:219]\n" \
"s_add_u32 m0, s50, 73728\n" \
"ds_read_b64_tr_b16 a[56:57], v9 offset:34304\n" \
"v_mfma_f32_16x16x32_bf16 v[220:223], a[108:111], a[84:87], v[220:223]\n" \
"buffer_load_dwordx4 v15, s[28:31], s46 offen lds\n" \
"v_mfma_f32_16x16x32_bf16 v[224:227], a[112:115], a[80:83], v[224:227]\n" \
"ds_read_b64_tr_b16 a[58:59], v10 offset:34304\n" \
"v_mfma_f32_16x16x32_bf16 v[228:231], a[116:119], a[80:83], v[228:231]\n" \
"v_mfma_f32_16x16x32_bf16 v[232:235], a[112:115], a[84:87], v[232:235]\n" \
"s_add_u32 m0, s22, 19072\n" \
"ds_read_b128 a[28:31], v5 offset:14336\n" \
"v_mfma_f32_16x16x32_bf16 v[236:239], a[116:119], a[84:87], v[236:239]\n" \
"buffer_load_dwordx4 v21, s[32:35], s47 offen offset:384 lds\n" \
"v_mfma_f32_16x16x32_bf16 v[240:243], a[120:123], a[80:83], v[240:243]\n" \
"ds_read_b64_tr_b16 a[60:61], v11 offset:34304\n" \
"v_mfma_f32_16x16x32_bf16 v[244:247], a[124:127], a[80:83], v[244:247]\n" \
"v_mfma_f32_16x16x32_bf16 v[248:251], a[120:123], a[84:87], v[248:251]\n" \
"s_add_u32 m0, s50, 77824\n" \
"ds_read_b64_tr_b16 a[62:63], v12 offset:34304\n" \
"v_mfma_f32_16x16x32_bf16 v[252:255], a[124:127], a[84:87], v[252:255]\n" \
"buffer_load_dwordx4 v16, s[28:31], s46 offen lds\n" \
"v_mfma_f32_16x16x32_bf16 a[128:131], a[96:99], a[88:91], a[128:131]\n" \
"v_mfma_f32_16x16x32_bf16 a[132:135], a[100:103], a[88:91], a[132:135]\n" \
"v_mfma_f32_16x16x32_bf16 a[136:139], a[96:99], a[92:95], a[136:139]\n" \
"v_mfma_f32_16x16x32_bf16 a[140:143], a[100:103], a[92:95], a[140:143]\n" \
"global_load_dwordx4 v[34:37], v26, s[64:65] offset:1024\n" \
"v_mfma_f32_16x16x32_bf16 a[144:147], a[104:107], a[88:91], a[144:147]\n" \
"v_mfma_f32_16x16x32_bf16 a[148:151], a[108:111], a[88:91], a[148:151]\n" \
"v_mfma_f32_16x16x32_bf16 a[152:155], a[104:107], a[92:95], a[152:155]\n" \
"v_mfma_f32_16x16x32_bf16 a[156:159], a[108:111], a[92:95], a[156:159]\n" \
"s_waitcnt vmcnt(24) lgkmcnt(0)\n" \
"v_mfma_f32_16x16x32_bf16 a[160:163], a[112:115], a[88:91], a[160:163]\n" \
"s_barrier\n" \
"v_mfma_f32_16x16x32_bf16 a[164:167], a[116:119], a[88:91], a[164:167]\n" \
"global_load_dwordx4 v[38:41], v26, s[64:65] offset:2048\n" \
"v_mfma_f32_16x16x32_bf16 a[168:171], a[112:115], a[92:95], a[168:171]\n" \
"v_mfma_f32_16x16x32_bf16 a[172:175], a[116:119], a[92:95], a[172:175]\n" \
"v_mfma_f32_16x16x32_bf16 a[176:179], a[120:123], a[88:91], a[176:179]\n" \
"v_mfma_f32_16x16x32_bf16 a[180:183], a[124:127], a[88:91], a[180:183]\n" \
"global_load_dwordx4 v[42:45], v26, s[64:65] offset:3072\n" \
"v_mfma_f32_16x16x32_bf16 a[184:187], a[120:123], a[92:95], a[184:187]\n" \
"v_mfma_f32_16x16x32_bf16 a[188:191], a[124:127], a[92:95], a[188:191]\n" \
"v_mfma_f32_16x16x32_bf16 v[64:67], a[32:35], a[0:3], v[64:67]\n" \
"ds_read_b128 a[64:67], v6 offset:0\n" \
"v_mfma_f32_16x16x32_bf16 v[68:71], a[36:39], a[0:3], v[68:71]\n" \
"v_mfma_f32_16x16x32_bf16 v[72:75], a[32:35], a[4:7], v[72:75]\n" \
"s_mov_b32 s47, 1572864\n" \
"s_add_u32 m0, s22, 32768\n" \
"ds_read_b64_tr_b16 a[96:97], v9 offset:49152\n" \
"v_mfma_f32_16x16x32_bf16 v[76:79], a[36:39], a[4:7], v[76:79]\n" \
"buffer_load_dwordx4 v21, s[32:35], s47 offen offset:0 lds\n" \
"v_mfma_f32_16x16x32_bf16 v[80:83], a[40:43], a[0:3], v[80:83]\n" \
"ds_read_b64_tr_b16 a[98:99], v10 offset:49152\n" \
"v_mfma_f32_16x16x32_bf16 v[84:87], a[44:47], a[0:3], v[84:87]\n" \
"v_mfma_f32_16x16x32_bf16 v[88:91], a[40:43], a[4:7], v[88:91]\n" \
"ds_read_b128 a[68:71], v6 offset:2048\n" \
"v_mfma_f32_16x16x32_bf16 v[92:95], a[44:47], a[4:7], v[92:95]\n" \
"global_load_dwordx4 a[230:233], v27, s[70:71] offset:0\n" \
"v_mfma_f32_16x16x32_bf16 v[96:99], a[48:51], a[0:3], v[96:99]\n" \
"ds_read_b64_tr_b16 a[100:101], v11 offset:49152\n" \
"v_mfma_f32_16x16x32_bf16 v[100:103], a[52:55], a[0:3], v[100:103]\n" \
"v_mfma_f32_16x16x32_bf16 v[104:107], a[48:51], a[4:7], v[104:107]\n" \
"s_mov_b32 s46, 384\n" \
"s_add_u32 m0, s50, 81920\n" \
"ds_read_b64_tr_b16 a[102:103], v12 offset:49152\n" \
"v_mfma_f32_16x16x32_bf16 v[108:111], a[52:55], a[4:7], v[108:111]\n" \
"buffer_load_dwordx4 v17, s[28:31], s46 offen lds\n" \
"v_mfma_f32_16x16x32_bf16 v[112:115], a[56:59], a[0:3], v[112:115]\n" \
"ds_read_b128 a[72:75], v6 offset:4096\n" \
"v_mfma_f32_16x16x32_bf16 v[116:119], a[60:63], a[0:3], v[116:119]\n" \
"v_mfma_f32_16x16x32_bf16 v[120:123], a[56:59], a[4:7], v[120:123]\n" \
"ds_read_b64_tr_b16 a[104:105], v9 offset:49664\n" \
"v_mfma_f32_16x16x32_bf16 v[124:127], a[60:63], a[4:7], v[124:127]\n" \
"global_load_dwordx4 a[234:237], v27, s[70:71] offset:16\n" \
"v_mfma_f32_16x16x32_bf16 v[128:131], a[32:35], a[8:11], v[128:131]\n" \
"ds_read_b64_tr_b16 a[106:107], v10 offset:49664\n" \
"v_mfma_f32_16x16x32_bf16 v[132:135], a[36:39], a[8:11], v[132:135]\n" \
"v_mfma_f32_16x16x32_bf16 v[136:139], a[32:35], a[12:15], v[136:139]\n" \
"s_add_u32 m0, s22, 33664\n" \
"ds_read_b128 a[76:79], v6 offset:6144\n" \
"v_mfma_f32_16x16x32_bf16 v[140:143], a[36:39], a[12:15], v[140:143]\n" \
"buffer_load_dwordx4 v21, s[32:35], s47 offen offset:128 lds\n" \
"v_mfma_f32_16x16x32_bf16 v[144:147], a[40:43], a[8:11], v[144:147]\n" \
"ds_read_b64_tr_b16 a[108:109], v11 offset:49664\n" \
"v_mfma_f32_16x16x32_bf16 v[148:151], a[44:47], a[8:11], v[148:151]\n" \
"v_mfma_f32_16x16x32_bf16 v[152:155], a[40:43], a[12:15], v[152:155]\n" \
"ds_read_b64_tr_b16 a[110:111], v12 offset:49664\n" \
"v_mfma_f32_16x16x32_bf16 v[156:159], a[44:47], a[12:15], v[156:159]\n" \
"buffer_load_dwordx2 a[192:193], v54, s[40:43], 0 offen offset:0 nt\n" \
"v_mfma_f32_16x16x32_bf16 v[160:163], a[48:51], a[8:11], v[160:163]\n" \
"ds_read_b128 a[80:83], v6 offset:8192\n" \
"v_mfma_f32_16x16x32_bf16 v[164:167], a[52:55], a[8:11], v[164:167]\n" \
"v_mfma_f32_16x16x32_bf16 v[168:171], a[48:51], a[12:15], v[168:171]\n" \
"s_add_u32 m0, s50, 86016\n" \
"ds_read_b64_tr_b16 a[112:113], v9 offset:50176\n" \
"v_mfma_f32_16x16x32_bf16 v[172:175], a[52:55], a[12:15], v[172:175]\n" \
"buffer_load_dwordx4 v18, s[28:31], s46 offen lds\n" \
"v_mfma_f32_16x16x32_bf16 v[176:179], a[56:59], a[8:11], v[176:179]\n" \
"ds_read_b64_tr_b16 a[114:115], v10 offset:50176\n" \
"v_mfma_f32_16x16x32_bf16 v[180:183], a[60:63], a[8:11], v[180:183]\n" \
"v_mfma_f32_16x16x32_bf16 v[184:187], a[56:59], a[12:15], v[184:187]\n" \
"ds_read_b128 a[84:87], v6 offset:10240\n" \
"v_mfma_f32_16x16x32_bf16 v[188:191], a[60:63], a[12:15], v[188:191]\n" \
"buffer_load_dwordx2 a[194:195], v54, s[40:43], 0 offen offset:2048 nt\n" \
"v_mfma_f32_16x16x32_bf16 v[192:195], a[32:35], a[16:19], v[192:195]\n" \
"ds_read_b64_tr_b16 a[116:117], v11 offset:50176\n" \
"v_mfma_f32_16x16x32_bf16 v[196:199], a[36:39], a[16:19], v[196:199]\n" \
"v_mfma_f32_16x16x32_bf16 v[200:203], a[32:35], a[20:23], v[200:203]\n" \
"s_add_u32 m0, s22, 34560\n" \
"ds_read_b64_tr_b16 a[118:119], v12 offset:50176\n" \
"v_mfma_f32_16x16x32_bf16 v[204:207], a[36:39], a[20:23], v[204:207]\n" \
"buffer_load_dwordx4 v21, s[32:35], s47 offen offset:256 lds\n" \
"v_mfma_f32_16x16x32_bf16 v[208:211], a[40:43], a[16:19], v[208:211]\n" \
"ds_read_b128 a[88:91], v6 offset:12288\n" \
"v_mfma_f32_16x16x32_bf16 v[212:215], a[44:47], a[16:19], v[212:215]\n" \
"v_mfma_f32_16x16x32_bf16 v[216:219], a[40:43], a[20:23], v[216:219]\n" \
"ds_read_b64_tr_b16 a[120:121], v9 offset:50688\n" \
"v_mfma_f32_16x16x32_bf16 v[220:223], a[44:47], a[20:23], v[220:223]\n" \
"buffer_load_dwordx2 a[196:197], v55, s[40:43], 0 offen offset:0 nt\n" \
"v_mfma_f32_16x16x32_bf16 v[224:227], a[48:51], a[16:19], v[224:227]\n" \
"s_mov_b32 s52, 0\n" \
"ds_read_b64_tr_b16 a[122:123], v10 offset:50688\n" \
"v_mfma_f32_16x16x32_bf16 v[228:231], a[52:55], a[16:19], v[228:231]\n" \
"v_mfma_f32_16x16x32_bf16 v[232:235], a[48:51], a[20:23], v[232:235]\n" \
"s_add_u32 m0, s50, 90112\n" \
"ds_read_b128 a[92:95], v6 offset:14336\n" \
"v_mfma_f32_16x16x32_bf16 v[236:239], a[52:55], a[20:23], v[236:239]\n" \
"buffer_load_dwordx4 v19, s[28:31], s46 offen lds\n" \
"v_mfma_f32_16x16x32_bf16 v[240:243], a[56:59], a[16:19], v[240:243]\n" \
"ds_read_b64_tr_b16 a[124:125], v11 offset:50688\n" \
"v_mfma_f32_16x16x32_bf16 v[244:247], a[60:63], a[16:19], v[244:247]\n" \
"v_mfma_f32_16x16x32_bf16 v[248:251], a[56:59], a[20:23], v[248:251]\n" \
"ds_read_b64_tr_b16 a[126:127], v12 offset:50688\n" \
"v_mfma_f32_16x16x32_bf16 v[252:255], a[60:63], a[20:23], v[252:255]\n" \
"buffer_load_dwordx2 a[198:199], v59, s[40:43], s52 offen offset:0 nt\n" \
"v_mfma_f32_16x16x32_bf16 a[128:131], a[32:35], a[24:27], a[128:131]\n" \
"s_waitcnt vmcnt(12)\n" \
"v_lshlrev_b32 v25, 16, v46\n" \
"v_and_b32 v46, 0xffff0000, v46\n" \
"v_mfma_f32_16x16x32_bf16 a[132:135], a[36:39], a[24:27], a[132:135]\n" \
"v_lshlrev_b32 v26, 16, v30\n" \
"v_and_b32 v30, 0xffff0000, v30\n" \
"v_mfma_f32_16x16x32_bf16 a[136:139], a[32:35], a[28:31], a[136:139]\n" \
"s_add_u32 m0, s22, 35456\n" \
"s_add_u32 s84, s84, 1048576\ns_addc_u32 s85, s85, 0\n" \
"v_mfma_f32_16x16x32_bf16 a[140:143], a[36:39], a[28:31], a[140:143]\n" \
"buffer_load_dwordx4 v21, s[32:35], s47 offen offset:384 lds\n" \
"v_mfma_f32_16x16x32_bf16 a[144:147], a[40:43], a[24:27], a[144:147]\n" \
"v_mul_f32 v23, v26, v25\n" \
"v_fmac_f32 v23, v30, v46\n" \
"v_mfma_f32_16x16x32_bf16 a[148:151], a[44:47], a[24:27], a[148:151]\n" \
"v_lshlrev_b32 v26, 16, v38\n" \
"v_and_b32 v38, 0xffff0000, v38\n" \
"v_mfma_f32_16x16x32_bf16 a[152:155], a[40:43], a[28:31], a[152:155]\n" \
"v_mul_f32 v24, v26, v25\n" \
"v_mfma_f32_16x16x32_bf16 a[156:159], a[44:47], a[28:31], a[156:159]\n" \
"s_waitcnt vmcnt(24) lgkmcnt(0)\n" \
"v_fmac_f32 v24, v38, v46\n" \
"v_lshlrev_b32 v25, 16, v47\n" \
"v_mfma_f32_16x16x32_bf16 a[160:163], a[48:51], a[24:27], a[160:163]\n" \
"s_barrier\n" \
"v_and_b32 v47, 0xffff0000, v47\n" \
"v_lshlrev_b32 v26, 16, v31\n" \
"v_mfma_f32_16x16x32_bf16 a[164:167], a[52:55], a[24:27], a[164:167]\n" \
"v_and_b32 v31, 0xffff0000, v31\n" \
"v_mfma_f32_16x16x32_bf16 a[168:171], a[48:51], a[28:31], a[168:171]\n" \
"s_add_u32 m0, s50, 94208\n" \
"v_fmac_f32 v23, v26, v25\n" \
"v_fmac_f32 v23, v31, v47\n" \
"v_mfma_f32_16x16x32_bf16 a[172:175], a[52:55], a[28:31], a[172:175]\n" \
"buffer_load_dwordx4 v20, s[28:31], s46 offen lds\n" \
"v_mfma_f32_16x16x32_bf16 a[176:179], a[56:59], a[24:27], a[176:179]\n" \
"v_lshlrev_b32 v26, 16, v39\n" \
"v_and_b32 v39, 0xffff0000, v39\n" \
"v_mfma_f32_16x16x32_bf16 a[180:183], a[60:63], a[24:27], a[180:183]\n" \
"v_fmac_f32 v24, v26, v25\n" \
"v_fmac_f32 v24, v39, v47\n" \
"v_mfma_f32_16x16x32_bf16 a[184:187], a[56:59], a[28:31], a[184:187]\n" \
"v_lshlrev_b32 v25, 16, v48\n" \
"v_mfma_f32_16x16x32_bf16 a[188:191], a[60:63], a[28:31], a[188:191]\n" \
"s_mov_b64 exec, 0xffff\nglobal_load_dword v22, v1, s[84:85]\ns_mov_b64 exec, -1\n" \
"v_mfma_f32_16x16x32_bf16 v[64:67], a[96:99], a[64:67], v[64:67]\n" \
"ds_read_b128 a[0:3], v5 offset:32768\n" \
"v_and_b32 v48, 0xffff0000, v48\n" \
"v_mfma_f32_16x16x32_bf16 v[68:71], a[100:103], a[64:67], v[68:71]\n" \
"v_lshlrev_b32 v26, 16, v32\n" \
"v_and_b32 v32, 0xffff0000, v32\n" \
"v_mfma_f32_16x16x32_bf16 v[72:75], a[96:99], a[68:71], v[72:75]\n" \
"s_mov_b32 s47, 1835008\n" \
"s_add_u32 m0, s22, 49152\n" \
"ds_read_b64_tr_b16 a[32:33], v9 offset:0\n" \
"v_mfma_f32_16x16x32_bf16 v[76:79], a[100:103], a[68:71], v[76:79]\n" \
"buffer_load_dwordx4 v21, s[32:35], s47 offen offset:0 lds\n" \
"v_mfma_f32_16x16x32_bf16 v[80:83], a[104:107], a[64:67], v[80:83]\n" \
"ds_read_b64_tr_b16 a[34:35], v10 offset:0\n" \
"v_fmac_f32 v23, v26, v25\n" \
"v_mfma_f32_16x16x32_bf16 v[84:87], a[108:111], a[64:67], v[84:87]\n" \
"v_fmac_f32 v23, v32, v48\n" \
"v_lshlrev_b32 v26, 16, v40\n" \
"v_mfma_f32_16x16x32_bf16 v[88:91], a[104:107], a[68:71], v[88:91]\n" \
"ds_read_b128 a[4:7], v5 offset:34816\n" \
"v_and_b32 v40, 0xffff0000, v40\n" \
"v_mfma_f32_16x16x32_bf16 v[92:95], a[108:111], a[68:71], v[92:95]\n" \
"buffer_load_dwordx2 a[214:215], v59, s[80:83], s52 offen offset:0 nt\n" \
"v_mfma_f32_16x16x32_bf16 v[96:99], a[112:115], a[64:67], v[96:99]\n" \
"ds_read_b64_tr_b16 a[36:37], v11 offset:0\n" \
"v_fmac_f32 v24, v26, v25\n" \
"v_mfma_f32_16x16x32_bf16 v[100:103], a[116:119], a[64:67], v[100:103]\n" \
"v_fmac_f32 v24, v40, v48\n" \
"v_lshlrev_b32 v25, 16, v49\n" \
"v_mfma_f32_16x16x32_bf16 v[104:107], a[112:115], a[68:71], v[104:107]\n" \
"s_mov_b32 s46, 512\n" \
"s_add_u32 m0, s50, 0\n" \
"ds_read_b64_tr_b16 a[38:39], v12 offset:0\n" \
"v_mfma_f32_16x16x32_bf16 v[108:111], a[116:119], a[68:71], v[108:111]\n" \
"buffer_load_dwordx4 v13, s[28:31], s46 offen lds\n" \
"v_mfma_f32_16x16x32_bf16 v[112:115], a[120:123], a[64:67], v[112:115]\n" \
"ds_read_b128 a[8:11], v5 offset:36864\n" \
"v_and_b32 v49, 0xffff0000, v49\n" \
"v_mfma_f32_16x16x32_bf16 v[116:119], a[124:127], a[64:67], v[116:119]\n" \
"v_lshlrev_b32 v26, 16, v33\n" \
"v_and_b32 v33, 0xffff0000, v33\n" \
"v_mfma_f32_16x16x32_bf16 v[120:123], a[120:123], a[68:71], v[120:123]\n" \
"ds_read_b64_tr_b16 a[40:41], v9 offset:512\n" \
"v_fmac_f32 v23, v26, v25\n" \
"v_mfma_f32_16x16x32_bf16 v[124:127], a[124:127], a[68:71], v[124:127]\n" \
"buffer_load_dwordx2 a[200:201], v59, s[40:43], s52 offen offset:2048 nt\n" \
"v_mfma_f32_16x16x32_bf16 v[128:131], a[96:99], a[72:75], v[128:131]\n" \
"ds_read_b64_tr_b16 a[42:43], v10 offset:512\n" \
"v_fmac_f32 v23, v33, v49\n" \
"v_mfma_f32_16x16x32_bf16 v[132:135], a[100:103], a[72:75], v[132:135]\n" \
"v_lshlrev_b32 v26, 16, v41\n" \
"v_and_b32 v41, 0xffff0000, v41\n" \
"v_mfma_f32_16x16x32_bf16 v[136:139], a[96:99], a[76:79], v[136:139]\n" \
"s_add_u32 m0, s22, 50048\n" \
"ds_read_b128 a[12:15], v5 offset:38912\n" \
"v_fmac_f32 v24, v26, v25\n" \
"v_mfma_f32_16x16x32_bf16 v[140:143], a[100:103], a[76:79], v[140:143]\n" \
"buffer_load_dwordx4 v21, s[32:35], s47 offen offset:128 lds\n" \
"v_mfma_f32_16x16x32_bf16 v[144:147], a[104:107], a[72:75], v[144:147]\n" \
"ds_read_b64_tr_b16 a[44:45], v11 offset:512\n" \
"v_fmac_f32 v24, v41, v49\n" \
"v_mfma_f32_16x16x32_bf16 v[148:151], a[108:111], a[72:75], v[148:151]\n" \
"v_lshlrev_b32 v25, 16, v50\n" \
"v_and_b32 v50, 0xffff0000, v50\n" \
"v_mfma_f32_16x16x32_bf16 v[152:155], a[104:107], a[76:79], v[152:155]\n" \
"ds_read_b64_tr_b16 a[46:47], v12 offset:512\n" \
"v_lshlrev_b32 v26, 16, v34\n" \
"v_mfma_f32_16x16x32_bf16 v[156:159], a[108:111], a[76:79], v[156:159]\n" \
"buffer_load_dwordx2 a[216:217], v59, s[80:83], s52 offen offset:2048 nt\n" \
"v_mfma_f32_16x16x32_bf16 v[160:163], a[112:115], a[72:75], v[160:163]\n" \
"s_mov_b32 s52, 4096\n" \
"ds_read_b128 a[16:19], v5 offset:40960\n" \
"v_and_b32 v34, 0xffff0000, v34\n" \
"v_mfma_f32_16x16x32_bf16 v[164:167], a[116:119], a[72:75], v[164:167]\n" \
"v_fmac_f32 v23, v26, v25\n" \
"v_fmac_f32 v23, v34, v50\n" \
"v_mfma_f32_16x16x32_bf16 v[168:171], a[112:115], a[76:79], v[168:171]\n" \
"s_add_u32 m0, s50, 4096\n" \
"ds_read_b64_tr_b16 a[48:49], v9 offset:1024\n" \
"v_lshlrev_b32 v26, 16, v42\n" \
"v_mfma_f32_16x16x32_bf16 v[172:175], a[116:119], a[76:79], v[172:175]\n" \
"buffer_load_dwordx4 v14, s[28:31], s46 offen lds\n" \
"v_mfma_f32_16x16x32_bf16 v[176:179], a[120:123], a[72:75], v[176:179]\n" \
"ds_read_b64_tr_b16 a[50:51], v10 offset:1024\n" \
"v_and_b32 v42, 0xffff0000, v42\n" \
"v_mfma_f32_16x16x32_bf16 v[180:183], a[124:127], a[72:75], v[180:183]\n" \
"v_fmac_f32 v24, v26, v25\n" \
"v_fmac_f32 v24, v42, v50\n" \
"v_mfma_f32_16x16x32_bf16 v[184:187], a[120:123], a[76:79], v[184:187]\n" \
"ds_read_b128 a[20:23], v5 offset:43008\n" \
"v_lshlrev_b32 v25, 16, v51\n" \
"v_mfma_f32_16x16x32_bf16 v[188:191], a[124:127], a[76:79], v[188:191]\n" \
"buffer_load_dwordx2 a[202:203], v59, s[40:43], s52 offen offset:0 nt\n" \
"v_mfma_f32_16x16x32_bf16 v[192:195], a[96:99], a[80:83], v[192:195]\n" \
"ds_read_b64_tr_b16 a[52:53], v11 offset:1024\n" \
"v_and_b32 v51, 0xffff0000, v51\n" \
"v_mfma_f32_16x16x32_bf16 v[196:199], a[100:103], a[80:83], v[196:199]\n" \
"v_lshlrev_b32 v26, 16, v35\n" \
"v_and_b32 v35, 0xffff0000, v35\n" \
"v_mfma_f32_16x16x32_bf16 v[200:203], a[96:99], a[84:87], v[200:203]\n" \
"s_add_u32 m0, s22, 50944\n" \
"ds_read_b64_tr_b16 a[54:55], v12 offset:1024\n" \
"v_fmac_f32 v23, v26, v25\n" \
"v_mfma_f32_16x16x32_bf16 v[204:207], a[100:103], a[84:87], v[204:207]\n" \
"buffer_load_dwordx4 v21, s[32:35], s47 offen offset:256 lds\n" \
"v_mfma_f32_16x16x32_bf16 v[208:211], a[104:107], a[80:83], v[208:211]\n" \
"ds_read_b128 a[24:27], v5 offset:45056\n" \
"v_fmac_f32 v23, v35, v51\n" \
"v_mfma_f32_16x16x32_bf16 v[212:215], a[108:111], a[80:83], v[212:215]\n" \
"v_lshlrev_b32 v26, 16, v43\n" \
"v_and_b32 v43, 0xffff0000, v43\n" \
"v_mfma_f32_16x16x32_bf16 v[216:219], a[104:107], a[84:87], v[216:219]\n" \
"ds_read_b64_tr_b16 a[56:57], v9 offset:1536\n" \
"v_fmac_f32 v24, v26, v25\n" \
"v_mfma_f32_16x16x32_bf16 v[220:223], a[108:111], a[84:87], v[220:223]\n" \
"buffer_load_dwordx2 a[218:219], v59, s[80:83], s52 offen offset:0 nt\n" \
"v_mfma_f32_16x16x32_bf16 v[224:227], a[112:115], a[80:83], v[224:227]\n" \
"ds_read_b64_tr_b16 a[58:59], v10 offset:1536\n" \
"v_fmac_f32 v24, v43, v51\n" \
"v_mfma_f32_16x16x32_bf16 v[228:231], a[116:119], a[80:83], v[228:231]\n" \
"v_lshlrev_b32 v25, 16, v52\n" \
"v_and_b32 v52, 0xffff0000, v52\n" \
"v_mfma_f32_16x16x32_bf16 v[232:235], a[112:115], a[84:87], v[232:235]\n" \
"s_add_u32 m0, s50, 8192\n" \
"ds_read_b128 a[28:31], v5 offset:47104\n" \
"v_lshlrev_b32 v26, 16, v36\n" \
"v_mfma_f32_16x16x32_bf16 v[236:239], a[116:119], a[84:87], v[236:239]\n" \
"buffer_load_dwordx4 v15, s[28:31], s46 offen lds\n" \
"v_mfma_f32_16x16x32_bf16 v[240:243], a[120:123], a[80:83], v[240:243]\n" \
"ds_read_b64_tr_b16 a[60:61], v11 offset:1536\n" \
"v_and_b32 v36, 0xffff0000, v36\n" \
"v_mfma_f32_16x16x32_bf16 v[244:247], a[124:127], a[80:83], v[244:247]\n" \
"v_fmac_f32 v23, v26, v25\n" \
"v_fmac_f32 v23, v36, v52\n" \
"v_mfma_f32_16x16x32_bf16 v[248:251], a[120:123], a[84:87], v[248:251]\n" \
"ds_read_b64_tr_b16 a[62:63], v12 offset:1536\n" \
"v_lshlrev_b32 v26, 16, v44\n" \
"v_mfma_f32_16x16x32_bf16 v[252:255], a[124:127], a[84:87], v[252:255]\n" \
"buffer_load_dwordx2 a[204:205], v59, s[40:43], s52 offen offset:2048 nt\n" \
"v_mfma_f32_16x16x32_bf16 a[128:131], a[96:99], a[88:91], a[128:131]\n" \
"v_and_b32 v44, 0xffff0000, v44\n" \
"v_fmac_f32 v24, v26, v25\n" \
"v_mfma_f32_16x16x32_bf16 a[132:135], a[100:103], a[88:91], a[132:135]\n" \
"v_fmac_f32 v24, v44, v52\n" \
"v_lshlrev_b32 v25, 16, v53\n" \
"v_mfma_f32_16x16x32_bf16 a[136:139], a[96:99], a[92:95], a[136:139]\n" \
"s_add_u32 m0, s22, 51840\n" \
"v_and_b32 v53, 0xffff0000, v53\n" \
"v_mfma_f32_16x16x32_bf16 a[140:143], a[100:103], a[92:95], a[140:143]\n" \
"buffer_load_dwordx4 v21, s[32:35], s47 offen offset:384 lds\n" \
"v_mfma_f32_16x16x32_bf16 a[144:147], a[104:107], a[88:91], a[144:147]\n" \
"v_lshlrev_b32 v26, 16, v37\n" \
"v_and_b32 v37, 0xffff0000, v37\n" \
"v_mfma_f32_16x16x32_bf16 a[148:151], a[108:111], a[88:91], a[148:151]\n" \
"v_fmac_f32 v23, v26, v25\n" \
"v_fmac_f32 v23, v37, v53\n" \
"v_mfma_f32_16x16x32_bf16 a[152:155], a[104:107], a[92:95], a[152:155]\n" \
"v_lshlrev_b32 v26, 16, v45\n" \
"v_mfma_f32_16x16x32_bf16 a[156:159], a[108:111], a[92:95], a[156:159]\n" \
"s_waitcnt vmcnt(32) lgkmcnt(0)\n" \
"v_and_b32 v45, 0xffff0000, v45\n" \
"v_fmac_f32 v24, v26, v25\n" \
"v_mfma_f32_16x16x32_bf16 a[160:163], a[112:115], a[88:91], a[160:163]\n" \
"LH0_%=:\ns_cbranch_execz LH8_%=\n" \
"s_barrier\n" \
"v_fmac_f32 v24, v45, v53\n" \
"v_mfma_f32_16x16x32_bf16 a[164:167], a[116:119], a[88:91], a[164:167]\n" \
"v_mfma_f32_16x16x32_bf16 a[168:171], a[112:115], a[92:95], a[168:171]\n" \
"s_add_u32 m0, s50, 12288\n" \
"v_mfma_f32_16x16x32_bf16 a[172:175], a[116:119], a[92:95], a[172:175]\n" \
"buffer_load_dwordx4 v16, s[28:31], s46 offen lds\n" \
"v_mfma_f32_16x16x32_bf16 a[176:179], a[120:123], a[88:91], a[176:179]\n" \
"v_mfma_f32_16x16x32_bf16 a[180:183], a[124:127], a[88:91], a[180:183]\n" \
"v_mfma_f32_16x16x32_bf16 a[184:187], a[120:123], a[92:95], a[184:187]\n" \
"v_mfma_f32_16x16x32_bf16 a[188:191], a[124:127], a[92:95], a[188:191]\n" \
"buffer_load_dwordx2 a[220:221], v59, s[80:83], s52 offen offset:2048 nt\n" \
"v_mfma_f32_16x16x32_bf16 v[64:67], a[32:35], a[0:3], v[64:67]\n" \
"s_mov_b32 s52, 8192\n" \
"ds_read_b128 a[64:67], v6 offset:32768\n" \
"v_mfma_f32_16x16x32_bf16 v[68:71], a[36:39], a[0:3], v[68:71]\n" \
"v_mfma_f32_16x16x32_bf16 v[72:75], a[32:35], a[4:7], v[72:75]\n" \
"s_mov_b32 s47, 2097152\n" \
"s_add_u32 m0, s22, 0\n" \
"ds_read_b64_tr_b16 a[96:97], v9 offset:16384\n" \
"v_mfma_f32_16x16x32_bf16 v[76:79], a[36:39], a[4:7], v[76:79]\n" \
"buffer_load_dwordx4 v21, s[32:35], s47 offen offset:0 lds\n" \
"v_mfma_f32_16x16x32_bf16 v[80:83], a[40:43], a[0:3], v[80:83]\n" \
"ds_read_b64_tr_b16 a[98:99], v10 offset:16384\n" \
"v_mfma_f32_16x16x32_bf16 v[84:87], a[44:47], a[0:3], v[84:87]\n" \
"v_mfma_f32_16x16x32_bf16 v[88:91], a[40:43], a[4:7], v[88:91]\n" \
"ds_read_b128 a[68:71], v6 offset:34816\n" \
"v_mfma_f32_16x16x32_bf16 v[92:95], a[44:47], a[4:7], v[92:95]\n" \
"buffer_load_dwordx2 a[206:207], v59, s[40:43], s52 offen offset:0 nt\n" \
"v_mfma_f32_16x16x32_bf16 v[96:99], a[48:51], a[0:3], v[96:99]\n" \
"ds_read_b64_tr_b16 a[100:101], v11 offset:16384\n" \
"v_mfma_f32_16x16x32_bf16 v[100:103], a[52:55], a[0:3], v[100:103]\n" \
"v_mfma_f32_16x16x32_bf16 v[104:107], a[48:51], a[4:7], v[104:107]\n" \
"s_mov_b32 s46, 512\n" \
"s_add_u32 m0, s50, 16384\n" \
"ds_read_b64_tr_b16 a[102:103], v12 offset:16384\n" \
"v_mfma_f32_16x16x32_bf16 v[108:111], a[52:55], a[4:7], v[108:111]\n" \
"buffer_load_dwordx4 v17, s[28:31], s46 offen lds\n" \
"v_mfma_f32_16x16x32_bf16 v[112:115], a[56:59], a[0:3], v[112:115]\n" \
"ds_read_b128 a[72:75], v6 offset:36864\n" \
"v_mfma_f32_16x16x32_bf16 v[116:119], a[60:63], a[0:3], v[116:119]\n" \
"v_mfma_f32_16x16x32_bf16 v[120:123], a[56:59], a[4:7], v[120:123]\n" \
"ds_read_b64_tr_b16 a[104:105], v9 offset:16896\n" \
"v_mfma_f32_16x16x32_bf16 v[124:127], a[60:63], a[4:7], v[124:127]\n" \
"buffer_load_dwordx2 a[222:223], v59, s[80:83], s52 offen offset:0 nt\n" \
"v_mfma_f32_16x16x32_bf16 v[128:131], a[32:35], a[8:11], v[128:131]\n" \
"ds_read_b64_tr_b16 a[106:107], v10 offset:16896\n" \
"v_mfma_f32_16x16x32_bf16 v[132:135], a[36:39], a[8:11], v[132:135]\n" \
"v_mfma_f32_16x16x32_bf16 v[136:139], a[32:35], a[12:15], v[136:139]\n" \
"s_add_u32 m0, s22, 896\n" \
"ds_read_b128 a[76:79], v6 offset:38912\n" \
"v_mfma_f32_16x16x32_bf16 v[140:143], a[36:39], a[12:15], v[140:143]\n" \
"buffer_load_dwordx4 v21, s[32:35], s47 offen offset:128 lds\n" \
"v_mfma_f32_16x16x32_bf16 v[144:147], a[40:43], a[8:11], v[144:147]\n" \
"ds_read_b64_tr_b16 a[108:109], v11 offset:16896\n" \
"v_mfma_f32_16x16x32_bf16 v[148:151], a[44:47], a[8:11], v[148:151]\n" \
"v_mfma_f32_16x16x32_bf16 v[152:155], a[40:43], a[12:15], v[152:155]\n" \
"ds_read_b64_tr_b16 a[110:111], v12 offset:16896\n" \
"v_mfma_f32_16x16x32_bf16 v[156:159], a[44:47], a[12:15], v[156:159]\n" \
"buffer_load_dwordx2 a[208:209], v59, s[40:43], s52 offen offset:2048 nt\n" \
"v_mfma_f32_16x16x32_bf16 v[160:163], a[48:51], a[8:11], v[160:163]\n" \
"ds_read_b128 a[80:83], v6 offset:40960\n" \
"v_mfma_f32_16x16x32_bf16 v[164:167], a[52:55], a[8:11], v[164:167]\n" \
"v_mfma_f32_16x16x32_bf16 v[168:171], a[48:51], a[12:15], v[168:171]\n" \
"s_add_u32 m0, s50, 20480\n" \
"ds_read_b64_tr_b16 a[112:113], v9 offset:17408\n" \
"v_mfma_f32_16x16x32_bf16 v[172:175], a[52:55], a[12:15], v[172:175]\n" \
"buffer_load_dwordx4 v18, s[28:31], s46 offen lds\n" \
"v_mfma_f32_16x16x32_bf16 v[176:179], a[56:59], a[8:11], v[176:179]\n" \
"ds_read_b64_tr_b16 a[114:115], v10 offset:17408\n" \
"v_mfma_f32_16x16x32_bf16 v[180:183], a[60:63], a[8:11], v[180:183]\n" \
"v_mfma_f32_16x16x32_bf16 v[184:187], a[56:59], a[12:15], v[184:187]\n" \
"ds_read_b128 a[84:87], v6 offset:43008\n" \
"v_mfma_f32_16x16x32_bf16 v[188:191], a[60:63], a[12:15], v[188:191]\n" \
"buffer_load_dwordx2 a[224:225], v59, s[80:83], s52 offen offset:2048 nt\n" \
"v_mfma_f32_16x16x32_bf16 v[192:195], a[32:35], a[16:19], v[192:195]\n" \
"s_mov_b32 s52, 12288\n" \
"ds_read_b64_tr_b16 a[116:117], v11 offset:17408\n" \
"v_mfma_f32_16x16x32_bf16 v[196:199], a[36:39], a[16:19], v[196:199]\n" \
"v_mfma_f32_16x16x32_bf16 v[200:203], a[32:35], a[20:23], v[200:203]\n" \
"s_add_u32 m0, s22, 1792\n" \
"ds_read_b64_tr_b16 a[118:119], v12 offset:17408\n" \
"v_mfma_f32_16x16x32_bf16 v[204:207], a[36:39], a[20:23], v[204:207]\n" \
"buffer_load_dwordx4 v21, s[32:35], s47 offen offset:256 lds\n" \
"v_mfma_f32_16x16x32_bf16 v[208:211], a[40:43], a[16:19], v[208:211]\n" \
"ds_read_b128 a[88:91], v6 offset:45056\n" \
"v_mfma_f32_16x16x32_bf16 v[212:215], a[44:47], a[16:19], v[212:215]\n" \
"v_mfma_f32_16x16x32_bf16 v[216:219], a[40:43], a[20:23], v[216:219]\n" \
"ds_read_b64_tr_b16 a[120:121], v9 offset:17920\n" \
"v_mfma_f32_16x16x32_bf16 v[220:223], a[44:47], a[20:23], v[220:223]\n" \
"buffer_load_dwordx2 a[210:211], v59, s[40:43], s52 offen offset:0 nt\n" \
"v_mfma_f32_16x16x32_bf16 v[224:227], a[48:51], a[16:19], v[224:227]\n" \
"ds_read_b64_tr_b16 a[122:123], v10 offset:17920\n" \
"v_mfma_f32_16x16x32_bf16 v[228:231], a[52:55], a[16:19], v[228:231]\n" \
"v_mfma_f32_16x16x32_bf16 v[232:235], a[48:51], a[20:23], v[232:235]\n" \
"s_add_u32 m0, s50, 24576\n" \
"ds_read_b128 a[92:95], v6 offset:47104\n" \
"v_mfma_f32_16x16x32_bf16 v[236:239], a[52:55], a[20:23], v[236:239]\n" \
"buffer_load_dwordx4 v19, s[28:31], s46 offen lds\n" \
"v_mfma_f32_16x16x32_bf16 v[240:243], a[56:59], a[16:19], v[240:243]\n" \
"ds_read_b64_tr_b16 a[124:125], v11 offset:17920\n" \
"v_mfma_f32_16x16x32_bf16 v[244:247], a[60:63], a[16:19], v[244:247]\n" \
"v_mfma_f32_16x16x32_bf16 v[248:251], a[56:59], a[20:23], v[248:251]\n" \
"ds_read_b64_tr_b16 a[126:127], v12 offset:17920\n" \
"v_mfma_f32_16x16x32_bf16 v[252:255], a[60:63], a[20:23], v[252:255]\n" \
"buffer_load_dwordx2 a[226:227], v59, s[80:83], s52 offen offset:0 nt\n" \
"v_mfma_f32_16x16x32_bf16 a[128:131], a[32:35], a[24:27], a[128:131]\n" \
"v_mfma_f32_16x16x32_bf16 a[132:135], a[36:39], a[24:27], a[132:135]\n" \
"s_waitcnt vmcnt(12)\n" \
"v_accvgpr_read_b32 v43, a230\n" \
"v_permlane32_swap_b32 v23, v24\n" \
"v_mfma_f32_16x16x32_bf16 a[136:139], a[32:35], a[28:31], a[136:139]\n" \
"s_add_u32 m0, s22, 2688\n" \
"s_add_u32 s84, s84, 1048576\ns_addc_u32 s85, s85, 0\n" \
"v_mfma_f32_16x16x32_bf16 a[140:143], a[36:39], a[28:31], a[140:143]\n" \
"buffer_load_dwordx4 v21, s[32:35], s47 offen offset:384 lds\n" \
"v_mfma_f32_16x16x32_bf16 a[144:147], a[40:43], a[24:27], a[144:147]\n" \
"v_add_f32 v23, v23, v24\n" \
"v_mov_b32 v24, v23\n" \
"v_mfma_f32_16x16x32_bf16 a[148:151], a[44:47], a[24:27], a[148:151]\n" \
"v_lshlrev_b32 v42, 16, v43\n" \
"v_and_b32 v43, 0xffff0000, v43\n" \
"v_mfma_f32_16x16x32_bf16 a[152:155], a[40:43], a[28:31], a[152:155]\n" \
"v_permlane16_swap_b32 v23, v24\n" \
"v_mfma_f32_16x16x32_bf16 a[156:159], a[44:47], a[28:31], a[156:159]\n" \
"s_waitcnt vmcnt(29) lgkmcnt(0)\n" \
"v_add_f32 v23, v23, v24\n" \
"v_accvgpr_read_b32 v45, a231\n" \
"v_mfma_f32_16x16x32_bf16 a[160:163], a[48:51], a[24:27], a[160:163]\n" \
"s_barrier\n" \
"v_lshlrev_b32 v44, 16, v45\n" \
"v_add_f32 v23, v23, v23 row_ror:8 row_mask:0xf bank_mask:0xf\n" \
"v_mfma_f32_16x16x32_bf16 a[164:167], a[52:55], a[24:27], a[164:167]\n" \
"v_and_b32 v45, 0xffff0000, v45\n" \
"v_mfma_f32_16x16x32_bf16 a[168:171], a[48:51], a[28:31], a[168:171]\n" \
"s_add_u32 m0, s50, 28672\n" \
"v_accvgpr_read_b32 v47, a232\n" \
"v_add_f32 v23, v23, v23 row_ror:4 row_mask:0xf bank_mask:0xf\n" \
"v_mfma_f32_16x16x32_bf16 a[172:175], a[52:55], a[28:31], a[172:175]\n" \
"buffer_load_dwordx4 v20, s[28:31], s46 offen lds\n" \
"v_mfma_f32_16x16x32_bf16 a[176:179], a[56:59], a[24:27], a[176:179]\n" \
"v_lshlrev_b32 v46, 16, v47\n" \
"v_and_b32 v47, 0xffff0000, v47\n" \
"v_mfma_f32_16x16x32_bf16 a[180:183], a[60:63], a[24:27], a[180:183]\n" \
"v_add_f32 v23, v23, v23 row_ror:2 row_mask:0xf bank_mask:0xf\n" \
"v_accvgpr_read_b32 v49, a233\n" \
"v_mfma_f32_16x16x32_bf16 a[184:187], a[56:59], a[28:31], a[184:187]\n" \
"v_lshlrev_b32 v48, 16, v49\n" \
"v_mfma_f32_16x16x32_bf16 a[188:191], a[60:63], a[28:31], a[188:191]\n" \
"s_mov_b64 exec, 0xffff\nglobal_load_dword v22, v1, s[84:85]\ns_mov_b64 exec, -1\n" \
"v_mfma_f32_16x16x32_bf16 v[64:67], a[96:99], a[64:67], v[64:67]\n" \
"ds_read_b128 a[0:3], v7 offset:0\n" \
"v_add_f32 v23, v23, v23 row_ror:1 row_mask:0xf bank_mask:0xf\n" \
"v_cvt_pk_bf16_f32 v23, v23, v23\n" \
"v_mfma_f32_16x16x32_bf16 v[68:71], a[100:103], a[64:67], v[68:71]\n" \
"v_and_b32 v23, 0xffff0000, v23\n" \
"v_mul_f32 v23, 0xbfb8aa3b, v23\n" \
"v_mfma_f32_16x16x32_bf16 v[72:75], a[96:99], a[68:71], v[72:75]\n" \
"s_mov_b32 s47, 2359296\n" \
"s_add_u32 m0, s22, 16384\n" \
"ds_read_b64_tr_b16 a[32:33], v9 offset:32768\n" \
"v_mfma_f32_16x16x32_bf16 v[76:79], a[100:103], a[68:71], v[76:79]\n" \
"buffer_load_dwordx4 v21, s[32:35], s47 offen offset:0 lds\n" \
"v_mfma_f32_16x16x32_bf16 v[80:83], a[104:107], a[64:67], v[80:83]\n" \
"ds_read_b64_tr_b16 a[34:35], v10 offset:32768\n" \
"v_exp_f32 v23, v23\n" \
"v_mfma_f32_16x16x32_bf16 v[84:87], a[108:111], a[64:67], v[84:87]\n" \
"v_and_b32 v49, 0xffff0000, v49\n" \
"v_accvgpr_read_b32 v36, a192\n" \
"v_add_f32 v23, 1.0, v23\n" \
"v_mfma_f32_16x16x32_bf16 v[88:91], a[104:107], a[68:71], v[88:91]\n" \
"ds_read_b128 a[4:7], v7 offset:2048\n" \
"v_rcp_f32 v23, v23\n" \
"v_mfma_f32_16x16x32_bf16 v[92:95], a[108:111], a[68:71], v[92:95]\n" \
"buffer_load_dwordx2 a[212:213], v59, s[40:43], s52 offen offset:2048 nt\n" \
"v_mfma_f32_16x16x32_bf16 v[96:99], a[112:115], a[64:67], v[96:99]\n" \
"ds_read_b64_tr_b16 a[36:37], v11 offset:32768\n" \
"v_lshlrev_b32 v30, 16, v36\n" \
"v_and_b32 v36, 0xffff0000, v36\n" \
"v_mfma_f32_16x16x32_bf16 v[100:103], a[116:119], a[64:67], v[100:103]\n" \
"v_cvt_pk_bf16_f32 v23, v23, v23\n" \
"v_and_b32 v23, 0xffff0000, v23\n" \
"v_accvgpr_read_b32 v37, a194\n" \
"v_mfma_f32_16x16x32_bf16 v[104:107], a[112:115], a[68:71], v[104:107]\n" \
"s_mov_b32 s46, 640\n" \
"s_add_u32 m0, s50, 32768\n" \
"ds_read_b64_tr_b16 a[38:39], v12 offset:32768\n" \
"v_lshlrev_b32 v31, 16, v37\n" \
"v_mfma_f32_16x16x32_bf16 v[108:111], a[116:119], a[68:71], v[108:111]\n" \
"buffer_load_dwordx4 v13, s[28:31], s46 offen lds\n" \
"v_mfma_f32_16x16x32_bf16 v[112:115], a[120:123], a[64:67], v[112:115]\n" \
"ds_read_b128 a[8:11], v7 offset:4096\n" \
"v_and_b32 v37, 0xffff0000, v37\n" \
"v_accvgpr_read_b32 v38, a196\n" \
"v_mfma_f32_16x16x32_bf16 v[116:119], a[124:127], a[64:67], v[116:119]\n" \
"buffer_store_dword v23, v61, s[36:39], 0 offen sc1\n" \
"v_mfma_f32_16x16x32_bf16 v[120:123], a[120:123], a[68:71], v[120:123]\n" \
"ds_read_b64_tr_b16 a[40:41], v9 offset:33280\n" \
"v_lshlrev_b32 v32, 16, v38\n" \
"v_and_b32 v38, 0xffff0000, v38\n" \
"v_mfma_f32_16x16x32_bf16 v[124:127], a[124:127], a[68:71], v[124:127]\n" \
"buffer_load_dwordx2 a[228:229], v59, s[80:83], s52 offen offset:2048 nt\n" \
"v_mfma_f32_16x16x32_bf16 v[128:131], a[96:99], a[72:75], v[128:131]\n" \
"ds_read_b64_tr_b16 a[42:43], v10 offset:33280\n" \
"v_accvgpr_read_b32 v39, a198\n" \
"v_lshlrev_b32 v33, 16, v39\n" \
"v_mfma_f32_16x16x32_bf16 v[132:135], a[100:103], a[72:75], v[132:135]\n" \
"v_and_b32 v39, 0xffff0000, v39\n" \
"v_mul_f32 v25, v30, v45\n" \
"v_mul_f32 v26, v36, v49\n" \
"v_mfma_f32_16x16x32_bf16 v[136:139], a[96:99], a[76:79], v[136:139]\n" \
"s_add_u32 m0, s22, 17280\n" \
"ds_read_b128 a[12:15], v7 offset:6144\n" \
"v_fmac_f32 v25, v31, v44\n" \
"v_fmac_f32 v26, v37, v48\n" \
"v_mfma_f32_16x16x32_bf16 v[140:143], a[100:103], a[76:79], v[140:143]\n" \
"buffer_load_dwordx4 v21, s[32:35], s47 offen offset:128 lds\n" \
"v_mfma_f32_16x16x32_bf16 v[144:147], a[104:107], a[72:75], v[144:147]\n" \
"ds_read_b64_tr_b16 a[44:45], v11 offset:33280\n" \
"v_fmac_f32 v25, v32, v43\n" \
"v_fmac_f32 v26, v38, v47\n" \
"v_mfma_f32_16x16x32_bf16 v[148:151], a[108:111], a[72:75], v[148:151]\n" \
"v_fmac_f32 v25, v33, v42\n" \
"v_fmac_f32 v26, v39, v46\n" \
"v_accvgpr_read_b32 v28, a214\n" \
"v_mfma_f32_16x16x32_bf16 v[152:155], a[104:107], a[76:79], v[152:155]\n" \
"ds_read_b64_tr_b16 a[46:47], v12 offset:33280\n" \
"v_cvt_pk_bf16_f32 v26, v25, v26\n" \
"v_lshlrev_b32 v27, 16, v28\n" \
"v_mfma_f32_16x16x32_bf16 v[156:159], a[108:111], a[76:79], v[156:159]\n" \
"v_lshlrev_b32 v25, 16, v26\n" \
"v_and_b32 v28, 0xffff0000, v28\n" \
"v_and_b32 v26, 0xffff0000, v26\n" \
"v_mfma_f32_16x16x32_bf16 v[160:163], a[112:115], a[72:75], v[160:163]\n" \
"ds_read_b128 a[16:19], v7 offset:8192\n" \
"v_mul_f32 v29, v25, v27\n" \
"v_mul_f32 v58, v26, v28\n" \
"v_mfma_f32_16x16x32_bf16 v[164:167], a[116:119], a[72:75], v[164:167]\n" \
"v_accvgpr_read_b32 v40, a200\n" \
"v_lshlrev_b32 v34, 16, v40\n" \
"v_and_b32 v40, 0xffff0000, v40\n" \
"v_mfma_f32_16x16x32_bf16 v[168:171], a[112:115], a[76:79], v[168:171]\n" \
"s_add_u32 m0, s50, 36864\n" \
"ds_read_b64_tr_b16 a[48:49], v9 offset:33792\n" \
"v_mul_f32 v25, v31, v45\n" \
"v_mul_f32 v26, v37, v49\n" \
"v_mfma_f32_16x16x32_bf16 v[172:175], a[116:119], a[76:79], v[172:175]\n" \
"buffer_load_dwordx4 v14, s[28:31], s46 offen lds\n" \
"v_mfma_f32_16x16x32_bf16 v[176:179], a[120:123], a[72:75], v[176:179]\n" \
"ds_read_b64_tr_b16 a[50:51], v10 offset:33792\n" \
"v_fmac_f32 v25, v32, v44\n" \
"v_fmac_f32 v26, v38, v48\n" \
"v_mfma_f32_16x16x32_bf16 v[180:183], a[124:127], a[72:75], v[180:183]\n" \
"v_fmac_f32 v25, v33, v43\n" \
"v_fmac_f32 v26, v39, v47\n" \
"v_fmac_f32 v25, v34, v42\n" \
"v_mfma_f32_16x16x32_bf16 v[184:187], a[120:123], a[76:79], v[184:187]\n" \
"ds_read_b128 a[20:23], v7 offset:10240\n" \
"v_fmac_f32 v26, v40, v46\n" \
"v_accvgpr_read_b32 v28, a216\n" \
"v_mfma_f32_16x16x32_bf16 v[188:191], a[124:127], a[76:79], v[188:191]\n" \
"v_cvt_pk_bf16_f32 v26, v25, v26\n" \
"v_lshlrev_b32 v27, 16, v28\n" \
"v_lshlrev_b32 v25, 16, v26\n" \
"v_mfma_f32_16x16x32_bf16 v[192:195], a[96:99], a[80:83], v[192:195]\n" \
"ds_read_b64_tr_b16 a[52:53], v11 offset:33792\n" \
"v_and_b32 v28, 0xffff0000, v28\n" \
"v_and_b32 v26, 0xffff0000, v26\n" \
"v_mfma_f32_16x16x32_bf16 v[196:199], a[100:103], a[80:83], v[196:199]\n" \
"v_mul_f32 v25, v25, v27\n" \
"v_mul_f32 v26, v26, v28\n" \
"v_cvt_pk_bf16_f32 v53, v25, v29\n" \
"v_mfma_f32_16x16x32_bf16 v[200:203], a[96:99], a[84:87], v[200:203]\n" \
"s_add_u32 m0, s22, 18176\n" \
"ds_read_b64_tr_b16 a[54:55], v12 offset:33792\n" \
"v_cvt_pk_bf16_f32 v57, v26, v58\n" \
"v_accvgpr_read_b32 v41, a202\n" \
"v_mfma_f32_16x16x32_bf16 v[204:207], a[100:103], a[84:87], v[204:207]\n" \
"buffer_load_dwordx4 v21, s[32:35], s47 offen offset:256 lds\n" \
"v_mfma_f32_16x16x32_bf16 v[208:211], a[104:107], a[80:83], v[208:211]\n" \
"ds_read_b128 a[24:27], v7 offset:12288\n" \
"v_lshlrev_b32 v35, 16, v41\n" \
"v_and_b32 v41, 0xffff0000, v41\n" \
"v_mfma_f32_16x16x32_bf16 v[212:215], a[108:111], a[80:83], v[212:215]\n" \
"v_mul_f32 v25, v32, v45\n" \
"v_mul_f32 v26, v38, v49\n" \
"v_fmac_f32 v25, v33, v44\n" \
"v_mfma_f32_16x16x32_bf16 v[216:219], a[104:107], a[84:87], v[216:219]\n" \
"ds_read_b64_tr_b16 a[56:57], v9 offset:34304\n" \
"v_fmac_f32 v26, v39, v48\n" \
"v_fmac_f32 v25, v34, v43\n" \
"v_mfma_f32_16x16x32_bf16 v[220:223], a[108:111], a[84:87], v[220:223]\n" \
"v_fmac_f32 v26, v40, v47\n" \
"v_fmac_f32 v25, v35, v42\n" \
"v_fmac_f32 v26, v41, v46\n" \
"v_mfma_f32_16x16x32_bf16 v[224:227], a[112:115], a[80:83], v[224:227]\n" \
"ds_read_b64_tr_b16 a[58:59], v10 offset:34304\n" \
"v_accvgpr_read_b32 v28, a218\n" \
"v_cvt_pk_bf16_f32 v26, v25, v26\n" \
"v_mfma_f32_16x16x32_bf16 v[228:231], a[116:119], a[80:83], v[228:231]\n" \
"v_lshlrev_b32 v27, 16, v28\n" \
"v_lshlrev_b32 v25, 16, v26\n" \
"v_and_b32 v28, 0xffff0000, v28\n" \
"v_mfma_f32_16x16x32_bf16 v[232:235], a[112:115], a[84:87], v[232:235]\n" \
"s_add_u32 m0, s50, 40960\n" \
"ds_read_b128 a[28:31], v7 offset:14336\n" \
"v_and_b32 v26, 0xffff0000, v26\n" \
"v_mul_f32 v29, v25, v27\n" \
"v_mfma_f32_16x16x32_bf16 v[236:239], a[116:119], a[84:87], v[236:239]\n" \
"buffer_load_dwordx4 v15, s[28:31], s46 offen lds\n" \
"v_mfma_f32_16x16x32_bf16 v[240:243], a[120:123], a[80:83], v[240:243]\n" \
"ds_read_b64_tr_b16 a[60:61], v11 offset:34304\n" \
"v_mul_f32 v58, v26, v28\n" \
"v_accvgpr_read_b32 v36, a204\n" \
"v_mfma_f32_16x16x32_bf16 v[244:247], a[124:127], a[80:83], v[244:247]\n" \
"v_lshlrev_b32 v30, 16, v36\n" \
"v_and_b32 v36, 0xffff0000, v36\n" \
"v_mul_f32 v25, v33, v45\n" \
"v_mfma_f32_16x16x32_bf16 v[248:251], a[120:123], a[84:87], v[248:251]\n" \
"ds_read_b64_tr_b16 a[62:63], v12 offset:34304\n" \
"v_mul_f32 v26, v39, v49\n" \
"v_fmac_f32 v25, v34, v44\n" \
"v_mfma_f32_16x16x32_bf16 v[252:255], a[124:127], a[84:87], v[252:255]\n" \
"v_fmac_f32 v26, v40, v48\n" \
"v_fmac_f32 v25, v35, v43\n" \
"v_fmac_f32 v26, v41, v47\n" \
"v_mfma_f32_16x16x32_bf16 a[128:131], a[96:99], a[88:91], a[128:131]\n" \
"v_fmac_f32 v25, v30, v42\n" \
"v_fmac_f32 v26, v36, v46\n" \
"v_accvgpr_read_b32 v28, a220\n" \
"v_mfma_f32_16x16x32_bf16 a[132:135], a[100:103], a[88:91], a[132:135]\n" \
"v_cvt_pk_bf16_f32 v26, v25, v26\n" \
"v_lshlrev_b32 v27, 16, v28\n" \
"v_lshlrev_b32 v25, 16, v26\n" \
"v_mfma_f32_16x16x32_bf16 a[136:139], a[96:99], a[92:95], a[136:139]\n" \
"s_add_u32 m0, s22, 19072\n" \
"v_and_b32 v28, 0xffff0000, v28\n" \
"v_and_b32 v26, 0xffff0000, v26\n" \
"v_mul_f32 v25, v25, v27\n" \
"v_mfma_f32_16x16x32_bf16 a[140:143], a[100:103], a[92:95], a[140:143]\n" \
"buffer_load_dwordx4 v21, s[32:35], s47 offen offset:384 lds\n" \
"v_mfma_f32_16x16x32_bf16 a[144:147], a[104:107], a[88:91], a[144:147]\n" \
"v_mul_f32 v26, v26, v28\n" \
"v_cvt_pk_bf16_f32 v52, v25, v29\n" \
"v_cvt_pk_bf16_f32 v56, v26, v58\n" \
"v_mfma_f32_16x16x32_bf16 a[148:151], a[108:111], a[88:91], a[148:151]\n" \
"v_mfma_f32_16x16x32_bf16 a[152:155], a[104:107], a[92:95], a[152:155]\n" \
"v_mfma_f32_16x16x32_bf16 a[156:159], a[108:111], a[92:95], a[156:159]\n" \
"s_waitcnt vmcnt(27) lgkmcnt(0)\n" \
"v_mfma_f32_16x16x32_bf16 a[160:163], a[112:115], a[88:91], a[160:163]\n" \
"s_barrier\n" \
"v_mfma_f32_16x16x32_bf16 a[164:167], a[116:119], a[88:91], a[164:167]\n" \
"v_mfma_f32_16x16x32_bf16 a[168:171], a[112:115], a[92:95], a[168:171]\n" \
"s_add_u32 m0, s50, 45056\n" \
"v_mfma_f32_16x16x32_bf16 a[172:175], a[116:119], a[92:95], a[172:175]\n" \
"buffer_load_dwordx4 v16, s[28:31], s46 offen lds\n" \
"v_mfma_f32_16x16x32_bf16 a[176:179], a[120:123], a[88:91], a[176:179]\n" \
"v_mfma_f32_16x16x32_bf16 a[180:183], a[124:127], a[88:91], a[180:183]\n" \
"v_mfma_f32_16x16x32_bf16 a[184:187], a[120:123], a[92:95], a[184:187]\n" \
"v_mfma_f32_16x16x32_bf16 a[188:191], a[124:127], a[92:95], a[188:191]\n" \
"v_mfma_f32_16x16x32_bf16 v[64:67], a[32:35], a[0:3], v[64:67]\n" \
"ds_read_b128 a[64:67], v8 offset:0\n" \
"v_mfma_f32_16x16x32_bf16 v[68:71], a[36:39], a[0:3], v[68:71]\n" \
"s_waitcnt vmcnt(6)\n" \
"v_accvgpr_read_b32 v37, a206\n" \
"v_lshlrev_b32 v31, 16, v37\n" \
"v_and_b32 v37, 0xffff0000, v37\n" \
"v_mfma_f32_16x16x32_bf16 v[72:75], a[32:35], a[4:7], v[72:75]\n" \
"s_mov_b32 s47, 2621440\n" \
"s_add_u32 m0, s22, 32768\n" \
"ds_read_b64_tr_b16 a[96:97], v9 offset:49152\n" \
"v_mul_f32 v25, v34, v45\n" \
"v_mfma_f32_16x16x32_bf16 v[76:79], a[36:39], a[4:7], v[76:79]\n" \
"buffer_load_dwordx4 v21, s[32:35], s47 offen offset:0 lds\n" \
"v_mfma_f32_16x16x32_bf16 v[80:83], a[40:43], a[0:3], v[80:83]\n" \
"ds_read_b64_tr_b16 a[98:99], v10 offset:49152\n" \
"v_mul_f32 v26, v40, v49\n" \
"v_fmac_f32 v25, v35, v44\n" \
"v_mfma_f32_16x16x32_bf16 v[84:87], a[44:47], a[0:3], v[84:87]\n" \
"v_fmac_f32 v26, v41, v48\n" \
"v_fmac_f32 v25, v30, v43\n" \
"v_fmac_f32 v26, v36, v47\n" \
"v_mfma_f32_16x16x32_bf16 v[88:91], a[40:43], a[4:7], v[88:91]\n" \
"ds_read_b128 a[68:71], v8 offset:2048\n" \
"v_fmac_f32 v25, v31, v42\n" \
"v_fmac_f32 v26, v37, v46\n" \
"v_mfma_f32_16x16x32_bf16 v[92:95], a[44:47], a[4:7], v[92:95]\n" \
"v_accvgpr_read_b32 v28, a222\n" \
"v_cvt_pk_bf16_f32 v26, v25, v26\n" \
"v_lshlrev_b32 v27, 16, v28\n" \
"v_mfma_f32_16x16x32_bf16 v[96:99], a[48:51], a[0:3], v[96:99]\n" \
"ds_read_b64_tr_b16 a[100:101], v11 offset:49152\n" \
"v_lshlrev_b32 v25, 16, v26\n" \
"v_and_b32 v28, 0xffff0000, v28\n" \
"v_mfma_f32_16x16x32_bf16 v[100:103], a[52:55], a[0:3], v[100:103]\n" \
"v_and_b32 v26, 0xffff0000, v26\n" \
"v_mul_f32 v29, v25, v27\n" \
"v_mul_f32 v58, v26, v28\n" \
"v_mfma_f32_16x16x32_bf16 v[104:107], a[48:51], a[4:7], v[104:107]\n" \
"s_mov_b32 s46, 640\n" \
"s_add_u32 m0, s50, 49152\n" \
"ds_read_b64_tr_b16 a[102:103], v12 offset:49152\n" \
"v_accvgpr_read_b32 v38, a208\n" \
"v_mfma_f32_16x16x32_bf16 v[108:111], a[52:55], a[4:7], v[108:111]\n" \
"buffer_load_dwordx4 v17, s[28:31], s46 offen lds\n" \
"v_mfma_f32_16x16x32_bf16 v[112:115], a[56:59], a[0:3], v[112:115]\n" \
"ds_read_b128 a[72:75], v8 offset:4096\n" \
"v_lshlrev_b32 v32, 16, v38\n" \
"v_and_b32 v38, 0xffff0000, v38\n" \
"v_mfma_f32_16x16x32_bf16 v[116:119], a[60:63], a[0:3], v[116:119]\n" \
"v_mul_f32 v25, v35, v45\n" \
"v_mul_f32 v26, v41, v49\n" \
"v_fmac_f32 v25, v30, v44\n" \
"v_mfma_f32_16x16x32_bf16 v[120:123], a[56:59], a[4:7], v[120:123]\n" \
"ds_read_b64_tr_b16 a[104:105], v9 offset:49664\n" \
"v_fmac_f32 v26, v36, v48\n" \
"v_fmac_f32 v25, v31, v43\n" \
"v_mfma_f32_16x16x32_bf16 v[124:127], a[60:63], a[4:7], v[124:127]\n" \
"v_fmac_f32 v26, v37, v47\n" \
"v_fmac_f32 v25, v32, v42\n" \
"v_fmac_f32 v26, v38, v46\n" \
"v_mfma_f32_16x16x32_bf16 v[128:131], a[32:35], a[8:11], v[128:131]\n" \
"ds_read_b64_tr_b16 a[106:107], v10 offset:49664\n" \
"v_accvgpr_read_b32 v28, a224\n" \
"v_cvt_pk_bf16_f32 v26, v25, v26\n" \
"v_mfma_f32_16x16x32_bf16 v[132:135], a[36:39], a[8:11], v[132:135]\n" \
"v_lshlrev_b32 v27, 16, v28\n" \
"v_lshlrev_b32 v25, 16, v26\n" \
"v_and_b32 v28, 0xffff0000, v28\n" \
"v_mfma_f32_16x16x32_bf16 v[136:139], a[32:35], a[12:15], v[136:139]\n" \
"s_add_u32 m0, s22, 33664\n" \
"ds_read_b128 a[76:79], v8 offset:6144\n" \
"v_and_b32 v26, 0xffff0000, v26\n" \
"v_mul_f32 v25, v25, v27\n" \
"v_mfma_f32_16x16x32_bf16 v[140:143], a[36:39], a[12:15], v[140:143]\n" \
"buffer_load_dwordx4 v21, s[32:35], s47 offen offset:128 lds\n" \
"v_mfma_f32_16x16x32_bf16 v[144:147], a[40:43], a[8:11], v[144:147]\n" \
"ds_read_b64_tr_b16 a[108:109], v11 offset:49664\n" \
"v_mul_f32 v26, v26, v28\n" \
"v_cvt_pk_bf16_f32 v51, v25, v29\n" \
"v_mfma_f32_16x16x32_bf16 v[148:151], a[44:47], a[8:11], v[148:151]\n" \
"v_cvt_pk_bf16_f32 v55, v26, v58\n" \
"v_accvgpr_read_b32 v39, a210\n" \
"v_lshlrev_b32 v33, 16, v39\n" \
"v_mfma_f32_16x16x32_bf16 v[152:155], a[40:43], a[12:15], v[152:155]\n" \
"ds_read_b64_tr_b16 a[110:111], v12 offset:49664\n" \
"v_and_b32 v39, 0xffff0000, v39\n" \
"v_mul_f32 v25, v30, v45\n" \
"v_mfma_f32_16x16x32_bf16 v[156:159], a[44:47], a[12:15], v[156:159]\n" \
"v_mul_f32 v26, v36, v49\n" \
"v_fmac_f32 v25, v31, v44\n" \
"v_fmac_f32 v26, v37, v48\n" \
"v_mfma_f32_16x16x32_bf16 v[160:163], a[48:51], a[8:11], v[160:163]\n" \
"ds_read_b128 a[80:83], v8 offset:8192\n" \
"v_fmac_f32 v25, v32, v43\n" \
"v_fmac_f32 v26, v38, v47\n" \
"v_mfma_f32_16x16x32_bf16 v[164:167], a[52:55], a[8:11], v[164:167]\n" \
"v_fmac_f32 v25, v33, v42\n" \
"v_fmac_f32 v26, v39, v46\n" \
"v_accvgpr_read_b32 v28, a226\n" \
"v_mfma_f32_16x16x32_bf16 v[168:171], a[48:51], a[12:15], v[168:171]\n" \
"s_add_u32 m0, s50, 53248\n" \
"ds_read_b64_tr_b16 a[112:113], v9 offset:50176\n" \
"v_cvt_pk_bf16_f32 v26, v25, v26\n" \
"v_lshlrev_b32 v27, 16, v28\n" \
"v_mfma_f32_16x16x32_bf16 v[172:175], a[52:55], a[12:15], v[172:175]\n" \
"buffer_load_dwordx4 v18, s[28:31], s46 offen lds\n" \
"v_mfma_f32_16x16x32_bf16 v[176:179], a[56:59], a[8:11], v[176:179]\n" \
"ds_read_b64_tr_b16 a[114:115], v10 offset:50176\n" \
"v_lshlrev_b32 v25, 16, v26\n" \
"v_and_b32 v28, 0xffff0000, v28\n" \
"v_mfma_f32_16x16x32_bf16 v[180:183], a[60:63], a[8:11], v[180:183]\n" \
"v_and_b32 v26, 0xffff0000, v26\n" \
"v_mul_f32 v29, v25, v27\n" \
"v_mul_f32 v58, v26, v28\n" \
"v_mfma_f32_16x16x32_bf16 v[184:187], a[56:59], a[12:15], v[184:187]\n" \
"ds_read_b128 a[84:87], v8 offset:10240\n" \
"v_accvgpr_read_b32 v40, a212\n" \
"v_lshlrev_b32 v34, 16, v40\n" \
"v_mfma_f32_16x16x32_bf16 v[188:191], a[60:63], a[12:15], v[188:191]\n" \
"v_and_b32 v40, 0xffff0000, v40\n" \
"v_mul_f32 v25, v31, v45\n" \
"v_mul_f32 v26, v37, v49\n" \
"v_mfma_f32_16x16x32_bf16 v[192:195], a[32:35], a[16:19], v[192:195]\n" \
"ds_read_b64_tr_b16 a[116:117], v11 offset:50176\n" \
"v_fmac_f32 v25, v32, v44\n" \
"v_fmac_f32 v26, v38, v48\n" \
"v_mfma_f32_16x16x32_bf16 v[196:199], a[36:39], a[16:19], v[196:199]\n" \
"v_fmac_f32 v25, v33, v43\n" \
"v_fmac_f32 v26, v39, v47\n" \
"v_fmac_f32 v25, v34, v42\n" \
"v_mfma_f32_16x16x32_bf16 v[200:203], a[32:35], a[20:23], v[200:203]\n" \
"s_add_u32 m0, s22, 34560\n" \
"ds_read_b64_tr_b16 a[118:119], v12 offset:50176\n" \
"v_fmac_f32 v26, v40, v46\n" \
"v_accvgpr_read_b32 v28, a228\n" \
"v_mfma_f32_16x16x32_bf16 v[204:207], a[36:39], a[20:23], v[204:207]\n" \
"buffer_load_dwordx4 v21, s[32:35], s47 offen offset:256 lds\n" \
"v_mfma_f32_16x16x32_bf16 v[208:211], a[40:43], a[16:19], v[208:211]\n" \
"ds_read_b128 a[88:91], v8 offset:12288\n" \
"v_cvt_pk_bf16_f32 v26, v25, v26\n" \
"v_lshlrev_b32 v27, 16, v28\n" \
"v_mfma_f32_16x16x32_bf16 v[212:215], a[44:47], a[16:19], v[212:215]\n" \
"v_lshlrev_b32 v25, 16, v26\n" \
"v_and_b32 v28, 0xffff0000, v28\n" \
"v_and_b32 v26, 0xffff0000, v26\n" \
"v_mfma_f32_16x16x32_bf16 v[216:219], a[40:43], a[20:23], v[216:219]\n" \
"ds_read_b64_tr_b16 a[120:121], v9 offset:50688\n" \
"v_mul_f32 v25, v25, v27\n" \
"v_mul_f32 v26, v26, v28\n" \
"v_mfma_f32_16x16x32_bf16 v[220:223], a[44:47], a[20:23], v[220:223]\n" \
"s_mov_b32 s94, 79691776\n" \
"v_cvt_pk_bf16_f32 v50, v25, v29\n" \
"v_cvt_pk_bf16_f32 v54, v26, v58\n" \
"v_accvgpr_read_b32 v43, a234\n" \
"v_mfma_f32_16x16x32_bf16 v[224:227], a[48:51], a[16:19], v[224:227]\n" \
"ds_read_b64_tr_b16 a[122:123], v10 offset:50688\n" \
"v_lshlrev_b32 v42, 16, v43\n" \
"v_and_b32 v43, 0xffff0000, v43\n" \
"v_mfma_f32_16x16x32_bf16 v[228:231], a[52:55], a[16:19], v[228:231]\n" \
"buffer_store_dwordx4 v[50:53], v60, s[36:39], s94 offen offset:0 sc1\n" \
"v_mfma_f32_16x16x32_bf16 v[232:235], a[48:51], a[20:23], v[232:235]\n" \
"s_add_u32 m0, s50, 57344\n" \
"ds_read_b128 a[92:95], v8 offset:14336\n" \
"s_mov_b32 s95, 79695872\n" \
"v_accvgpr_read_b32 v45, a235\n" \
"v_mfma_f32_16x16x32_bf16 v[236:239], a[52:55], a[20:23], v[236:239]\n" \
"buffer_load_dwordx4 v19, s[28:31], s46 offen lds\n" \
"v_mfma_f32_16x16x32_bf16 v[240:243], a[56:59], a[16:19], v[240:243]\n" \
"ds_read_b64_tr_b16 a[124:125], v11 offset:50688\n" \
"v_lshlrev_b32 v44, 16, v45\n" \
"v_and_b32 v45, 0xffff0000, v45\n" \
"v_mfma_f32_16x16x32_bf16 v[244:247], a[60:63], a[16:19], v[244:247]\n" \
"v_accvgpr_read_b32 v47, a236\n" \
"v_lshlrev_b32 v46, 16, v47\n" \
"v_and_b32 v47, 0xffff0000, v47\n" \
"v_mfma_f32_16x16x32_bf16 v[248:251], a[56:59], a[20:23], v[248:251]\n" \
"ds_read_b64_tr_b16 a[126:127], v12 offset:50688\n" \
"v_accvgpr_read_b32 v49, a237\n" \
"v_lshlrev_b32 v48, 16, v49\n" \
"v_mfma_f32_16x16x32_bf16 v[252:255], a[60:63], a[20:23], v[252:255]\n" \
"v_and_b32 v49, 0xffff0000, v49\n" \
"v_accvgpr_read_b32 v36, a193\n" \
"v_lshlrev_b32 v30, 16, v36\n" \
"v_mfma_f32_16x16x32_bf16 a[128:131], a[32:35], a[24:27], a[128:131]\n" \
"v_and_b32 v36, 0xffff0000, v36\n" \
"v_accvgpr_read_b32 v37, a195\n" \
"v_lshlrev_b32 v31, 16, v37\n" \
"v_mfma_f32_16x16x32_bf16 a[132:135], a[36:39], a[24:27], a[132:135]\n" \
"v_and_b32 v37, 0xffff0000, v37\n" \
"v_accvgpr_read_b32 v38, a197\n" \
"v_lshlrev_b32 v32, 16, v38\n" \
"v_mfma_f32_16x16x32_bf16 a[136:139], a[32:35], a[28:31], a[136:139]\n" \
"s_add_u32 m0, s22, 35456\n" \
"s_add_u32 s84, s84, 1048576\ns_addc_u32 s85, s85, 0\n" \
"v_and_b32 v38, 0xffff0000, v38\n" \
"v_mfma_f32_16x16x32_bf16 a[140:143], a[36:39], a[28:31], a[140:143]\n" \
"buffer_load_dwordx4 v21, s[32:35], s47 offen offset:384 lds\n" \
"v_mfma_f32_16x16x32_bf16 a[144:147], a[40:43], a[24:27], a[144:147]\n" \
"v_accvgpr_read_b32 v39, a199\n" \
"v_lshlrev_b32 v33, 16, v39\n" \
"v_and_b32 v39, 0xffff0000, v39\n" \
"v_mfma_f32_16x16x32_bf16 a[148:151], a[44:47], a[24:27], a[148:151]\n" \
"v_mul_f32 v25, v30, v45\n" \
"v_mul_f32 v26, v36, v49\n" \
"v_fmac_f32 v25, v31, v44\n" \
"v_mfma_f32_16x16x32_bf16 a[152:155], a[40:43], a[28:31], a[152:155]\n" \
"v_fmac_f32 v26, v37, v48\n" \
"v_fmac_f32 v25, v32, v43\n" \
"v_fmac_f32 v26, v38, v47\n" \
"v_mfma_f32_16x16x32_bf16 a[156:159], a[44:47], a[28:31], a[156:159]\n" \
"s_waitcnt vmcnt(20) lgkmcnt(0)\n" \
"v_fmac_f32 v25, v33, v42\n" \
"v_fmac_f32 v26, v39, v46\n" \
"v_accvgpr_read_b32 v28, a215\n" \
"v_mfma_f32_16x16x32_bf16 a[160:163], a[48:51], a[24:27], a[160:163]\n" \
"s_barrier\n" \
"v_cvt_pk_bf16_f32 v26, v25, v26\n" \
"v_lshlrev_b32 v27, 16, v28\n" \
"v_lshlrev_b32 v25, 16, v26\n" \
"v_mfma_f32_16x16x32_bf16 a[164:167], a[52:55], a[24:27], a[164:167]\n" \
"buffer_store_dwordx4 v[54:57], v60, s[36:39], s95 offen offset:0 sc1\n" \
"v_mfma_f32_16x16x32_bf16 a[168:171], a[48:51], a[28:31], a[168:171]\n" \
"LH1_%=:\ns_cbranch_execz LH9_%=\n" \
"s_add_u32 m0, s50, 61440\n" \
"v_and_b32 v28, 0xffff0000, v28\n" \
"v_and_b32 v26, 0xffff0000, v26\n" \
"v_mul_f32 v29, v25, v27\n" \
"v_mfma_f32_16x16x32_bf16 a[172:175], a[52:55], a[28:31], a[172:175]\n" \
"buffer_load_dwordx4 v20, s[28:31], s46 offen lds\n" \
"v_mfma_f32_16x16x32_bf16 a[176:179], a[56:59], a[24:27], a[176:179]\n" \
"v_mul_f32 v58, v26, v28\n" \
"v_accvgpr_read_b32 v40, a201\n" \
"v_lshlrev_b32 v34, 16, v40\n" \
"v_mfma_f32_16x16x32_bf16 a[180:183], a[60:63], a[24:27], a[180:183]\n" \
"v_and_b32 v40, 0xffff0000, v40\n" \
"v_mul_f32 v25, v31, v45\n" \
"v_mul_f32 v26, v37, v49\n" \
"v_mfma_f32_16x16x32_bf16 a[184:187], a[56:59], a[28:31], a[184:187]\n" \
"v_fmac_f32 v25, v32, v44\n" \
"v_fmac_f32 v26, v38, v48\n" \
"v_fmac_f32 v25, v33, v43\n" \
"v_mfma_f32_16x16x32_bf16 a[188:191], a[60:63], a[28:31], a[188:191]\n" \
"s_mov_b64 exec, 0xffff\nglobal_load_dword v22, v1, s[84:85]\ns_mov_b64 exec, -1\n" \
"v_mfma_f32_16x16x32_bf16 v[64:67], a[96:99], a[64:67], v[64:67]\n" \
"ds_read_b128 a[0:3], v5 offset:0\n" \
"v_fmac_f32 v26, v39, v47\n" \
"v_fmac_f32 v25, v34, v42\n" \
"v_mfma_f32_16x16x32_bf16 v[68:71], a[100:103], a[64:67], v[68:71]\n" \
"v_fmac_f32 v26, v40, v46\n" \
"v_accvgpr_read_b32 v28, a217\n" \
"v_cvt_pk_bf16_f32 v26, v25, v26\n" \
"v_mfma_f32_16x16x32_bf16 v[72:75], a[96:99], a[68:71], v[72:75]\n" \
"s_mov_b32 s47, 2883584\n" \
"s_add_u32 m0, s22, 49152\n" \
"ds_read_b64_tr_b16 a[32:33], v9 offset:0\n" \
"v_lshlrev_b32 v27, 16, v28\n" \
"v_mfma_f32_16x16x32_bf16 v[76:79], a[100:103], a[68:71], v[76:79]\n" \
"buffer_load_dwordx4 v21, s[32:35], s47 offen offset:0 lds\n" \
"v_mfma_f32_16x16x32_bf16 v[80:83], a[104:107], a[64:67], v[80:83]\n" \
"ds_read_b64_tr_b16 a[34:35], v10 offset:0\n" \
"v_lshlrev_b32 v25, 16, v26\n" \
"v_and_b32 v28, 0xffff0000, v28\n" \
"v_mfma_f32_16x16x32_bf16 v[84:87], a[108:111], a[64:67], v[84:87]\n" \
"v_and_b32 v26, 0xffff0000, v26\n" \
"v_mul_f32 v25, v25, v27\n" \
"v_mul_f32 v26, v26, v28\n" \
"v_mfma_f32_16x16x32_bf16 v[88:91], a[104:107], a[68:71], v[88:91]\n" \
"ds_read_b128 a[4:7], v5 offset:2048\n" \
"v_cvt_pk_bf16_f32 v53, v25, v29\n" \
"v_cvt_pk_bf16_f32 v57, v26, v58\n" \
"v_mfma_f32_16x16x32_bf16 v[92:95], a[108:111], a[68:71], v[92:95]\n" \
"v_accvgpr_read_b32 v41, a203\n" \
"v_lshlrev_b32 v35, 16, v41\n" \
"v_and_b32 v41, 0xffff0000, v41\n" \
"v_mfma_f32_16x16x32_bf16 v[96:99], a[112:115], a[64:67], v[96:99]\n" \
"ds_read_b64_tr_b16 a[36:37], v11 offset:0\n" \
"v_mul_f32 v25, v32, v45\n" \
"v_mul_f32 v26, v38, v49\n" \
"v_mfma_f32_16x16x32_bf16 v[100:103], a[116:119], a[64:67], v[100:103]\n" \
"v_fmac_f32 v25, v33, v44\n" \
"v_fmac_f32 v26, v39, v48\n" \
"v_fmac_f32 v25, v34, v43\n" \
"v_mfma_f32_16x16x32_bf16 v[104:107], a[112:115], a[68:71], v[104:107]\n" \
"s_mov_b32 s46, 768\n" \
"s_add_u32 m0, s50, 65536\n" \
"ds_read_b64_tr_b16 a[38:39], v12 offset:0\n" \
"v_fmac_f32 v26, v40, v47\n" \
"v_mfma_f32_16x16x32_bf16 v[108:111], a[116:119], a[68:71], v[108:111]\n" \
"buffer_load_dwordx4 v13, s[28:31], s46 offen lds\n" \
"v_mfma_f32_16x16x32_bf16 v[112:115], a[120:123], a[64:67], v[112:115]\n" \
"ds_read_b128 a[8:11], v5 offset:4096\n" \
"v_fmac_f32 v25, v35, v42\n" \
"v_fmac_f32 v26, v41, v46\n" \
"v_mfma_f32_16x16x32_bf16 v[116:119], a[124:127], a[64:67], v[116:119]\n" \
"v_accvgpr_read_b32 v28, a219\n" \
"v_cvt_pk_bf16_f32 v26, v25, v26\n" \
"v_lshlrev_b32 v27, 16, v28\n" \
"v_mfma_f32_16x16x32_bf16 v[120:123], a[120:123], a[68:71], v[120:123]\n" \
"ds_read_b64_tr_b16 a[40:41], v9 offset:512\n" \
"v_lshlrev_b32 v25, 16, v26\n" \
"v_and_b32 v28, 0xffff0000, v28\n" \
"v_mfma_f32_16x16x32_bf16 v[124:127], a[124:127], a[68:71], v[124:127]\n" \
"v_and_b32 v26, 0xffff0000, v26\n" \
"v_mul_f32 v29, v25, v27\n" \
"v_mul_f32 v58, v26, v28\n" \
"v_mfma_f32_16x16x32_bf16 v[128:131], a[96:99], a[72:75], v[128:131]\n" \
"ds_read_b64_tr_b16 a[42:43], v10 offset:512\n" \
"v_accvgpr_read_b32 v36, a205\n" \
"v_lshlrev_b32 v30, 16, v36\n" \
"v_mfma_f32_16x16x32_bf16 v[132:135], a[100:103], a[72:75], v[132:135]\n" \
"v_and_b32 v36, 0xffff0000, v36\n" \
"v_mul_f32 v25, v33, v45\n" \
"v_mul_f32 v26, v39, v49\n" \
"v_mfma_f32_16x16x32_bf16 v[136:139], a[96:99], a[76:79], v[136:139]\n" \
"s_add_u32 m0, s22, 50048\n" \
"ds_read_b128 a[12:15], v5 offset:6144\n" \
"v_fmac_f32 v25, v34, v44\n" \
"v_fmac_f32 v26, v40, v48\n" \
"v_mfma_f32_16x16x32_bf16 v[140:143], a[100:103], a[76:79], v[140:143]\n" \
"buffer_load_dwordx4 v21, s[32:35], s47 offen offset:128 lds\n" \
"v_mfma_f32_16x16x32_bf16 v[144:147], a[104:107], a[72:75], v[144:147]\n" \
"ds_read_b64_tr_b16 a[44:45], v11 offset:512\n" \
"v_fmac_f32 v25, v35, v43\n" \
"v_fmac_f32 v26, v41, v47\n" \
"v_mfma_f32_16x16x32_bf16 v[148:151], a[108:111], a[72:75], v[148:151]\n" \
"v_fmac_f32 v25, v30, v42\n" \
"v_fmac_f32 v26, v36, v46\n" \
"v_accvgpr_read_b32 v28, a221\n" \
"v_mfma_f32_16x16x32_bf16 v[152:155], a[104:107], a[76:79], v[152:155]\n" \
"ds_read_b64_tr_b16 a[46:47], v12 offset:512\n" \
"v_cvt_pk_bf16_f32 v26, v25, v26\n" \
"v_lshlrev_b32 v27, 16, v28\n" \
"v_mfma_f32_16x16x32_bf16 v[156:159], a[108:111], a[76:79], v[156:159]\n" \
"v_lshlrev_b32 v25, 16, v26\n" \
"v_and_b32 v28, 0xffff0000, v28\n" \
"v_and_b32 v26, 0xffff0000, v26\n" \
"v_mfma_f32_16x16x32_bf16 v[160:163], a[112:115], a[72:75], v[160:163]\n" \
"ds_read_b128 a[16:19], v5 offset:8192\n" \
"v_mul_f32 v25, v25, v27\n" \
"v_mul_f32 v26, v26, v28\n" \
"v_mfma_f32_16x16x32_bf16 v[164:167], a[116:119], a[72:75], v[164:167]\n" \
"v_cvt_pk_bf16_f32 v52, v25, v29\n" \
"v_cvt_pk_bf16_f32 v56, v26, v58\n" \
"v_accvgpr_read_b32 v37, a207\n" \
"v_mfma_f32_16x16x32_bf16 v[168:171], a[112:115], a[76:79], v[168:171]\n" \
"s_add_u32 m0, s50, 69632\n" \
"ds_read_b64_tr_b16 a[48:49], v9 offset:1024\n" \
"v_lshlrev_b32 v31, 16, v37\n" \
"v_and_b32 v37, 0xffff0000, v37\n" \
"v_mfma_f32_16x16x32_bf16 v[172:175], a[116:119], a[76:79], v[172:175]\n" \
"buffer_load_dwordx4 v14, s[28:31], s46 offen lds\n" \
"v_mfma_f32_16x16x32_bf16 v[176:179], a[120:123], a[72:75], v[176:179]\n" \
"ds_read_b64_tr_b16 a[50:51], v10 offset:1024\n" \
"v_mul_f32 v25, v34, v45\n" \
"v_mul_f32 v26, v40, v49\n" \
"v_mfma_f32_16x16x32_bf16 v[180:183], a[124:127], a[72:75], v[180:183]\n" \
"v_fmac_f32 v25, v35, v44\n" \
"v_fmac_f32 v26, v41, v48\n" \
"v_fmac_f32 v25, v30, v43\n" \
"v_mfma_f32_16x16x32_bf16 v[184:187], a[120:123], a[76:79], v[184:187]\n" \
"ds_read_b128 a[20:23], v5 offset:10240\n" \
"v_fmac_f32 v26, v36, v47\n" \
"v_fmac_f32 v25, v31, v42\n" \
"v_mfma_f32_16x16x32_bf16 v[188:191], a[124:127], a[76:79], v[188:191]\n" \
"v_fmac_f32 v26, v37, v46\n" \
"v_accvgpr_read_b32 v28, a223\n" \
"v_cvt_pk_bf16_f32 v26, v25, v26\n" \
"v_mfma_f32_16x16x32_bf16 v[192:195], a[96:99], a[80:83], v[192:195]\n" \
"ds_read_b64_tr_b16 a[52:53], v11 offset:1024\n" \
"v_lshlrev_b32 v27, 16, v28\n" \
"v_lshlrev_b32 v25, 16, v26\n" \
"v_mfma_f32_16x16x32_bf16 v[196:199], a[100:103], a[80:83], v[196:199]\n" \
"v_and_b32 v28, 0xffff0000, v28\n" \
"v_and_b32 v26, 0xffff0000, v26\n" \
"v_mul_f32 v29, v25, v27\n" \
"v_mfma_f32_16x16x32_bf16 v[200:203], a[96:99], a[84:87], v[200:203]\n" \
"s_add_u32 m0, s22, 50944\n" \
"ds_read_b64_tr_b16 a[54:55], v12 offset:1024\n" \
"v_mul_f32 v58, v26, v28\n" \
"v_accvgpr_read_b32 v38, a209\n" \
"v_mfma_f32_16x16x32_bf16 v[204:207], a[100:103], a[84:87], v[204:207]\n" \
"buffer_load_dwordx4 v21, s[32:35], s47 offen offset:256 lds\n" \
"v_mfma_f32_16x16x32_bf16 v[208:211], a[104:107], a[80:83], v[208:211]\n" \
"ds_read_b128 a[24:27], v5 offset:12288\n" \
"v_lshlrev_b32 v32, 16, v38\n" \
"v_and_b32 v38, 0xffff0000, v38\n" \
"v_mfma_f32_16x16x32_bf16 v[212:215], a[108:111], a[80:83], v[212:215]\n" \
"v_mul_f32 v25, v35, v45\n" \
"v_mul_f32 v26, v41, v49\n" \
"v_fmac_f32 v25, v30, v44\n" \
"v_mfma_f32_16x16x32_bf16 v[216:219], a[104:107], a[84:87], v[216:219]\n" \
"ds_read_b64_tr_b16 a[56:57], v9 offset:1536\n" \
"v_fmac_f32 v26, v36, v48\n" \
"v_fmac_f32 v25, v31, v43\n" \
"v_mfma_f32_16x16x32_bf16 v[220:223], a[108:111], a[84:87], v[220:223]\n" \
"v_fmac_f32 v26, v37, v47\n" \
"v_fmac_f32 v25, v32, v42\n" \
"v_fmac_f32 v26, v38, v46\n" \
"v_mfma_f32_16x16x32_bf16 v[224:227], a[112:115], a[80:83], v[224:227]\n" \
"ds_read_b64_tr_b16 a[58:59], v10 offset:1536\n" \
"v_accvgpr_read_b32 v28, a225\n" \
"v_cvt_pk_bf16_f32 v26, v25, v26\n" \
"v_mfma_f32_16x16x32_bf16 v[228:231], a[116:119], a[80:83], v[228:231]\n" \
"v_lshlrev_b32 v27, 16, v28\n" \
"v_lshlrev_b32 v25, 16, v26\n" \
"v_and_b32 v28, 0xffff0000, v28\n" \
"v_mfma_f32_16x16x32_bf16 v[232:235], a[112:115], a[84:87], v[232:235]\n" \
"s_add_u32 m0, s50, 73728\n" \
"ds_read_b128 a[28:31], v5 offset:14336\n" \
"v_and_b32 v26, 0xffff0000, v26\n" \
"v_mul_f32 v25, v25, v27\n" \
"v_mfma_f32_16x16x32_bf16 v[236:239], a[116:119], a[84:87], v[236:239]\n" \
"buffer_load_dwordx4 v15, s[28:31], s46 offen lds\n" \
"v_mfma_f32_16x16x32_bf16 v[240:243], a[120:123], a[80:83], v[240:243]\n" \
"ds_read_b64_tr_b16 a[60:61], v11 offset:1536\n" \
"v_mul_f32 v26, v26, v28\n" \
"v_cvt_pk_bf16_f32 v51, v25, v29\n" \
"v_mfma_f32_16x16x32_bf16 v[244:247], a[124:127], a[80:83], v[244:247]\n" \
"v_cvt_pk_bf16_f32 v55, v26, v58\n" \
"v_accvgpr_read_b32 v39, a211\n" \
"v_lshlrev_b32 v33, 16, v39\n" \
"v_mfma_f32_16x16x32_bf16 v[248:251], a[120:123], a[84:87], v[248:251]\n" \
"ds_read_b64_tr_b16 a[62:63], v12 offset:1536\n" \
"v_and_b32 v39, 0xffff0000, v39\n" \
"v_mul_f32 v25, v30, v45\n" \
"v_mfma_f32_16x16x32_bf16 v[252:255], a[124:127], a[84:87], v[252:255]\n" \
"v_mul_f32 v26, v36, v49\n" \
"v_fmac_f32 v25, v31, v44\n" \
"v_fmac_f32 v26, v37, v48\n" \
"v_mfma_f32_16x16x32_bf16 a[128:131], a[96:99], a[88:91], a[128:131]\n" \
"v_fmac_f32 v25, v32, v43\n" \
"v_fmac_f32 v26, v38, v47\n" \
"v_fmac_f32 v25, v33, v42\n" \
"v_mfma_f32_16x16x32_bf16 a[132:135], a[100:103], a[88:91], a[132:135]\n" \
"v_fmac_f32 v26, v39, v46\n" \
"v_accvgpr_read_b32 v28, a227\n" \
"v_cvt_pk_bf16_f32 v26, v25, v26\n" \
"v_mfma_f32_16x16x32_bf16 a[136:139], a[96:99], a[92:95], a[136:139]\n" \
"s_add_u32 m0, s22, 51840\n" \
"v_lshlrev_b32 v27, 16, v28\n" \
"v_lshlrev_b32 v25, 16, v26\n" \
"v_and_b32 v28, 0xffff0000, v28\n" \
"v_mfma_f32_16x16x32_bf16 a[140:143], a[100:103], a[92:95], a[140:143]\n" \
"buffer_load_dwordx4 v21, s[32:35], s47 offen offset:384 lds\n" \
"v_mfma_f32_16x16x32_bf16 a[144:147], a[104:107], a[88:91], a[144:147]\n" \
"v_and_b32 v26, 0xffff0000, v26\n" \
"v_mul_f32 v29, v25, v27\n" \
"v_mul_f32 v58, v26, v28\n" \
"v_mfma_f32_16x16x32_bf16 a[148:151], a[108:111], a[88:91], a[148:151]\n" \
"v_accvgpr_read_b32 v40, a213\n" \
"v_lshlrev_b32 v34, 16, v40\n" \
"v_and_b32 v40, 0xffff0000, v40\n" \
"v_mfma_f32_16x16x32_bf16 a[152:155], a[104:107], a[92:95], a[152:155]\n" \
"v_mul_f32 v25, v31, v45\n" \
"v_mul_f32 v26, v37, v49\n" \
"v_fmac_f32 v25, v32, v44\n" \
"v_mfma_f32_16x16x32_bf16 a[156:159], a[108:111], a[92:95], a[156:159]\n" \
"s_waitcnt vmcnt(19) lgkmcnt(0)\n" \
"v_fmac_f32 v26, v38, v48\n" \
"v_fmac_f32 v25, v33, v43\n" \
"v_fmac_f32 v26, v39, v47\n" \
"v_mfma_f32_16x16x32_bf16 a[160:163], a[112:115], a[88:91], a[160:163]\n" \
"s_barrier\n" \
"v_fmac_f32 v25, v34, v42\n" \
"v_fmac_f32 v26, v40, v46\n" \
"v_accvgpr_read_b32 v28, a229\n" \
"v_mfma_f32_16x16x32_bf16 a[164:167], a[116:119], a[88:91], a[164:167]\n" \
"v_cvt_pk_bf16_f32 v26, v25, v26\n" \
"v_lshlrev_b32 v27, 16, v28\n" \
"v_lshlrev_b32 v25, 16, v26\n" \
"v_mfma_f32_16x16x32_bf16 a[168:171], a[112:115], a[92:95], a[168:171]\n" \
"s_add_u32 m0, s50, 77824\n" \
"v_and_b32 v28, 0xffff0000, v28\n" \
"v_and_b32 v26, 0xffff0000, v26\n" \
"v_mul_f32 v25, v25, v27\n" \
"v_mfma_f32_16x16x32_bf16 a[172:175], a[116:119], a[92:95], a[172:175]\n" \
"buffer_load_dwordx4 v16, s[28:31], s46 offen lds\n" \
"v_mfma_f32_16x16x32_bf16 a[176:179], a[120:123], a[88:91], a[176:179]\n" \
"s_mov_b32 s94, 79699968\n" \
"v_mul_f32 v26, v26, v28\n" \
"v_cvt_pk_bf16_f32 v50, v25, v29\n" \
"v_cvt_pk_bf16_f32 v54, v26, v58\n" \
"v_mfma_f32_16x16x32_bf16 a[180:183], a[124:127], a[88:91], a[180:183]\n" \
"buffer_store_dwordx4 v[50:53], v60, s[36:39], s94 offen offset:0 sc1\n" \
"v_mfma_f32_16x16x32_bf16 a[184:187], a[120:123], a[92:95], a[184:187]\n" \
"s_mov_b32 s95, 79704064\n" \
"v_mfma_f32_16x16x32_bf16 a[188:191], a[124:127], a[92:95], a[188:191]\n" \
"s_mov_b64 exec, 0xffff\nglobal_load_dword v22, v2, s[88:89]\ns_mov_b64 exec, -1\n" \
"v_mfma_f32_16x16x32_bf16 v[64:67], a[32:35], a[0:3], v[64:67]\n" \
"ds_read_b128 a[64:67], v6 offset:0\n" \
"v_mfma_f32_16x16x32_bf16 v[68:71], a[36:39], a[0:3], v[68:71]\n" \
"v_mfma_f32_16x16x32_bf16 v[72:75], a[32:35], a[4:7], v[72:75]\n" \
"s_mov_b32 s47, 3145728\n" \
"s_add_u32 m0, s22, 0\n" \
"ds_read_b64_tr_b16 a[96:97], v9 offset:16384\n" \
"v_mfma_f32_16x16x32_bf16 v[76:79], a[36:39], a[4:7], v[76:79]\n" \
"buffer_load_dwordx4 v21, s[32:35], s47 offen offset:0 lds\n" \
"v_mfma_f32_16x16x32_bf16 v[80:83], a[40:43], a[0:3], v[80:83]\n" \
"ds_read_b64_tr_b16 a[98:99], v10 offset:16384\n" \
"v_mfma_f32_16x16x32_bf16 v[84:87], a[44:47], a[0:3], v[84:87]\n" \
"v_mfma_f32_16x16x32_bf16 v[88:91], a[40:43], a[4:7], v[88:91]\n" \
"ds_read_b128 a[68:71], v6 offset:2048\n" \
"v_mfma_f32_16x16x32_bf16 v[92:95], a[44:47], a[4:7], v[92:95]\n" \
"v_mfma_f32_16x16x32_bf16 v[96:99], a[48:51], a[0:3], v[96:99]\n" \
"ds_read_b64_tr_b16 a[100:101], v11 offset:16384\n" \
"v_mfma_f32_16x16x32_bf16 v[100:103], a[52:55], a[0:3], v[100:103]\n" \
"v_mfma_f32_16x16x32_bf16 v[104:107], a[48:51], a[4:7], v[104:107]\n" \
"s_mov_b32 s46, 768\n" \
"s_add_u32 m0, s50, 81920\n" \
"ds_read_b64_tr_b16 a[102:103], v12 offset:16384\n" \
"v_mfma_f32_16x16x32_bf16 v[108:111], a[52:55], a[4:7], v[108:111]\n" \
"buffer_load_dwordx4 v17, s[28:31], s46 offen lds\n" \
"v_mfma_f32_16x16x32_bf16 v[112:115], a[56:59], a[0:3], v[112:115]\n" \
"ds_read_b128 a[72:75], v6 offset:4096\n" \
"v_mfma_f32_16x16x32_bf16 v[116:119], a[60:63], a[0:3], v[116:119]\n" \
"buffer_store_dwordx4 v[54:57], v60, s[36:39], s95 offen offset:0 sc1\n" \
"v_mfma_f32_16x16x32_bf16 v[120:123], a[56:59], a[4:7], v[120:123]\n" \
"s_lshl_b32 s62, s8, 2\n" \
"ds_read_b64_tr_b16 a[104:105], v9 offset:16896\n" \
"s_add_u32 s62, s62, %2\n" \
"s_lshl_b32 s63, s62, 12\n" \
"v_mfma_f32_16x16x32_bf16 v[124:127], a[60:63], a[4:7], v[124:127]\n" \
"s_add_u32 s68, s10, s63\ns_addc_u32 s69, s11, 0\n" \
"s_and_b32 s69, s69, 0xffff\n" \
"s_mov_b32 s70, 4096\n" \
"v_mfma_f32_16x16x32_bf16 v[128:131], a[32:35], a[8:11], v[128:131]\n" \
"s_mov_b32 s71, 0x00020000\n" \
"ds_read_b64_tr_b16 a[106:107], v10 offset:16896\n" \
"s_add_u32 s63, s63, 79691776\n" \
"v_mfma_f32_16x16x32_bf16 v[132:135], a[36:39], a[8:11], v[132:135]\n" \
"s_add_u32 s72, s4, s63\ns_addc_u32 s73, s5, 0\n" \
"s_and_b32 s73, s73, 0xffff\n" \
"s_mov_b32 s74, 4096\n" \
"v_mfma_f32_16x16x32_bf16 v[136:139], a[32:35], a[12:15], v[136:139]\n" \
"s_add_u32 m0, s22, 896\n" \
"ds_read_b128 a[76:79], v6 offset:6144\n" \
"s_mov_b32 s75, 0x00020000\n" \
"v_add_u32 v23, -32, v4\n" \
"v_mfma_f32_16x16x32_bf16 v[140:143], a[36:39], a[12:15], v[140:143]\n" \
"buffer_load_dwordx4 v21, s[32:35], s47 offen offset:128 lds\n" \
"v_mfma_f32_16x16x32_bf16 v[144:147], a[40:43], a[8:11], v[144:147]\n" \
"ds_read_b64_tr_b16 a[108:109], v11 offset:16896\n" \
"v_add_u32 v24, 992, v4\n" \
"v_add_u32 v25, -16, v4\n" \
"v_mfma_f32_16x16x32_bf16 v[148:151], a[44:47], a[8:11], v[148:151]\n" \
"s_lshr_b32 s63, %2, 1\n" \
"v_add_u32 v26, -512, v4\n" \
"v_add_u32 v27, 512, v4\n" \
"s_lshl_b32 s63, s63, 16\n" \
"v_mfma_f32_16x16x32_bf16 v[152:155], a[40:43], a[12:15], v[152:155]\n" \
"s_lshl_b32 s56, %2, 14\n" \
"ds_read_b64_tr_b16 a[110:111], v12 offset:16896\n" \
"s_add_u32 s56, s56, s63\n" \
"v_add_u32 v55, s56, v4\n" \
"v_mfma_f32_16x16x32_bf16 v[156:159], a[44:47], a[12:15], v[156:159]\n" \
"s_lshl_b32 s62, s62, 1\n" \
"v_mov_b32 v28, 0\n" \
"v_mov_b32 v29, s62\n" \
"v_lshlrev_b32 v56, 23, v4\n" \
"v_mfma_f32_16x16x32_bf16 v[160:163], a[48:51], a[8:11], v[160:163]\n" \
"s_and_b32 s62, %1, 7\n" \
"ds_read_b128 a[80:83], v6 offset:8192\n" \
"s_lshl_b32 s62, s62, 8\n" \
"s_add_u32 s62, s62, 94380032\n" \
"v_mfma_f32_16x16x32_bf16 v[164:167], a[52:55], a[8:11], v[164:167]\n" \
"v_bfe_u32 v57, v4, 4, 5\n" \
"v_lshl_add_u32 v57, v57, 3, s62\n" \
"v_mfma_f32_16x16x32_bf16 v[168:171], a[48:51], a[12:15], v[168:171]\n" \
"s_add_u32 m0, s50, 86016\n" \
"ds_read_b64_tr_b16 a[112:113], v9 offset:17408\n" \
"v_mfma_f32_16x16x32_bf16 v[172:175], a[52:55], a[12:15], v[172:175]\n" \
"buffer_load_dwordx4 v18, s[28:31], s46 offen lds\n" \
"v_mfma_f32_16x16x32_bf16 v[176:179], a[56:59], a[8:11], v[176:179]\n" \
"ds_read_b64_tr_b16 a[114:115], v10 offset:17408\n" \
"v_mfma_f32_16x16x32_bf16 v[180:183], a[60:63], a[8:11], v[180:183]\n" \
"v_mfma_f32_16x16x32_bf16 v[184:187], a[56:59], a[12:15], v[184:187]\n" \
"ds_read_b128 a[84:87], v6 offset:10240\n" \
"v_mfma_f32_16x16x32_bf16 v[188:191], a[60:63], a[12:15], v[188:191]\n" \
"v_mfma_f32_16x16x32_bf16 v[192:195], a[32:35], a[16:19], v[192:195]\n" \
"ds_read_b64_tr_b16 a[116:117], v11 offset:17408\n" \
"v_mfma_f32_16x16x32_bf16 v[196:199], a[36:39], a[16:19], v[196:199]\n" \
"v_mfma_f32_16x16x32_bf16 v[200:203], a[32:35], a[20:23], v[200:203]\n" \
"s_add_u32 m0, s22, 1792\n" \
"ds_read_b64_tr_b16 a[118:119], v12 offset:17408\n" \
"v_mfma_f32_16x16x32_bf16 v[204:207], a[36:39], a[20:23], v[204:207]\n" \
"buffer_load_dwordx4 v21, s[32:35], s47 offen offset:256 lds\n" \
"v_mfma_f32_16x16x32_bf16 v[208:211], a[40:43], a[16:19], v[208:211]\n" \
"ds_read_b128 a[88:91], v6 offset:12288\n" \
"v_mfma_f32_16x16x32_bf16 v[212:215], a[44:47], a[16:19], v[212:215]\n" \
"v_mfma_f32_16x16x32_bf16 v[216:219], a[40:43], a[20:23], v[216:219]\n" \
"ds_read_b64_tr_b16 a[120:121], v9 offset:17920\n" \
"v_mfma_f32_16x16x32_bf16 v[220:223], a[44:47], a[20:23], v[220:223]\n" \
"v_mfma_f32_16x16x32_bf16 v[224:227], a[48:51], a[16:19], v[224:227]\n" \
"ds_read_b64_tr_b16 a[122:123], v10 offset:17920\n" \
"v_mfma_f32_16x16x32_bf16 v[228:231], a[52:55], a[16:19], v[228:231]\n" \
"v_mfma_f32_16x16x32_bf16 v[232:235], a[48:51], a[20:23], v[232:235]\n" \
"s_add_u32 m0, s50, 90112\n" \
"ds_read_b128 a[92:95], v6 offset:14336\n" \
"v_mfma_f32_16x16x32_bf16 v[236:239], a[52:55], a[20:23], v[236:239]\n" \
"buffer_load_dwordx4 v19, s[28:31], s46 offen lds\n" \
"v_mfma_f32_16x16x32_bf16 v[240:243], a[56:59], a[16:19], v[240:243]\n" \
"ds_read_b64_tr_b16 a[124:125], v11 offset:17920\n" \
"v_mfma_f32_16x16x32_bf16 v[244:247], a[60:63], a[16:19], v[244:247]\n" \
"v_mfma_f32_16x16x32_bf16 v[248:251], a[56:59], a[20:23], v[248:251]\n" \
"ds_read_b64_tr_b16 a[126:127], v12 offset:17920\n" \
"v_mfma_f32_16x16x32_bf16 v[252:255], a[60:63], a[20:23], v[252:255]\n" \
"v_mfma_f32_16x16x32_bf16 a[128:131], a[32:35], a[24:27], a[128:131]\n" \
"v_mfma_f32_16x16x32_bf16 a[132:135], a[36:39], a[24:27], a[132:135]\n" \
"v_mfma_f32_16x16x32_bf16 a[136:139], a[32:35], a[28:31], a[136:139]\n" \
"s_add_u32 m0, s22, 2688\n" \
"s_add_u32 s88, s88, 2048\ns_addc_u32 s89, s89, 0\n" \
"v_mfma_f32_16x16x32_bf16 a[140:143], a[36:39], a[28:31], a[140:143]\n" \
"buffer_load_dwordx4 v21, s[32:35], s47 offen offset:384 lds\n" \
"v_mfma_f32_16x16x32_bf16 a[144:147], a[40:43], a[24:27], a[144:147]\n" \
"v_mfma_f32_16x16x32_bf16 a[148:151], a[44:47], a[24:27], a[148:151]\n" \
"v_mfma_f32_16x16x32_bf16 a[152:155], a[40:43], a[28:31], a[152:155]\n" \
"v_mfma_f32_16x16x32_bf16 a[156:159], a[44:47], a[28:31], a[156:159]\n" \
"s_waitcnt vmcnt(19) lgkmcnt(0)\n" \
"v_mfma_f32_16x16x32_bf16 a[160:163], a[48:51], a[24:27], a[160:163]\n" \
"s_barrier\n" \
"v_mfma_f32_16x16x32_bf16 a[164:167], a[52:55], a[24:27], a[164:167]\n" \
"v_mfma_f32_16x16x32_bf16 a[168:171], a[48:51], a[28:31], a[168:171]\n" \
"s_add_u32 m0, s50, 94208\n" \
"v_mfma_f32_16x16x32_bf16 a[172:175], a[52:55], a[28:31], a[172:175]\n" \
"buffer_load_dwordx4 v20, s[28:31], s46 offen lds\n" \
"v_mfma_f32_16x16x32_bf16 a[176:179], a[56:59], a[24:27], a[176:179]\n" \
"v_mfma_f32_16x16x32_bf16 a[180:183], a[60:63], a[24:27], a[180:183]\n" \
"v_mfma_f32_16x16x32_bf16 a[184:187], a[56:59], a[28:31], a[184:187]\n" \
"v_mfma_f32_16x16x32_bf16 a[188:191], a[60:63], a[28:31], a[188:191]\n" \
"s_mov_b64 exec, 0xffff\nglobal_load_dword v22, v2, s[88:89]\ns_mov_b64 exec, -1\n" \
"v_mfma_f32_16x16x32_bf16 v[64:67], a[96:99], a[64:67], v[64:67]\n" \
"ds_read_b128 a[0:3], v5 offset:32768\n" \
"v_mfma_f32_16x16x32_bf16 v[68:71], a[100:103], a[64:67], v[68:71]\n" \
"v_mfma_f32_16x16x32_bf16 v[72:75], a[96:99], a[68:71], v[72:75]\n" \
"s_mov_b32 s47, 3407872\n" \
"s_add_u32 m0, s22, 16384\n" \
"ds_read_b64_tr_b16 a[32:33], v9 offset:32768\n" \
"v_mfma_f32_16x16x32_bf16 v[76:79], a[100:103], a[68:71], v[76:79]\n" \
"buffer_load_dwordx4 v21, s[32:35], s47 offen offset:0 lds\n" \
"v_mfma_f32_16x16x32_bf16 v[80:83], a[104:107], a[64:67], v[80:83]\n" \
"ds_read_b64_tr_b16 a[34:35], v10 offset:32768\n" \
"v_mfma_f32_16x16x32_bf16 v[84:87], a[108:111], a[64:67], v[84:87]\n" \
"v_mfma_f32_16x16x32_bf16 v[88:91], a[104:107], a[68:71], v[88:91]\n" \
"ds_read_b128 a[4:7], v5 offset:34816\n" \
"v_mfma_f32_16x16x32_bf16 v[92:95], a[108:111], a[68:71], v[92:95]\n" \
"v_mfma_f32_16x16x32_bf16 v[96:99], a[112:115], a[64:67], v[96:99]\n" \
"ds_read_b64_tr_b16 a[36:37], v11 offset:32768\n" \
"v_mfma_f32_16x16x32_bf16 v[100:103], a[116:119], a[64:67], v[100:103]\n" \
"v_mfma_f32_16x16x32_bf16 v[104:107], a[112:115], a[68:71], v[104:107]\n" \
"s_mov_b32 s46, 896\n" \
"s_add_u32 m0, s50, 0\n" \
"ds_read_b64_tr_b16 a[38:39], v12 offset:32768\n" \
"v_mfma_f32_16x16x32_bf16 v[108:111], a[116:119], a[68:71], v[108:111]\n" \
"buffer_load_dwordx4 v13, s[28:31], s46 offen lds\n" \
"v_mfma_f32_16x16x32_bf16 v[112:115], a[120:123], a[64:67], v[112:115]\n" \
"ds_read_b128 a[8:11], v5 offset:36864\n" \
"v_mfma_f32_16x16x32_bf16 v[116:119], a[124:127], a[64:67], v[116:119]\n" \
"v_mfma_f32_16x16x32_bf16 v[120:123], a[120:123], a[68:71], v[120:123]\n" \
"ds_read_b64_tr_b16 a[40:41], v9 offset:33280\n" \
"v_mfma_f32_16x16x32_bf16 v[124:127], a[124:127], a[68:71], v[124:127]\n" \
"v_mfma_f32_16x16x32_bf16 v[128:131], a[96:99], a[72:75], v[128:131]\n" \
"ds_read_b64_tr_b16 a[42:43], v10 offset:33280\n" \
"v_mfma_f32_16x16x32_bf16 v[132:135], a[100:103], a[72:75], v[132:135]\n" \
"v_mfma_f32_16x16x32_bf16 v[136:139], a[96:99], a[76:79], v[136:139]\n" \
"s_add_u32 m0, s22, 17280\n" \
"ds_read_b128 a[12:15], v5 offset:38912\n" \
"v_mfma_f32_16x16x32_bf16 v[140:143], a[100:103], a[76:79], v[140:143]\n" \
"buffer_load_dwordx4 v21, s[32:35], s47 offen offset:128 lds\n" \
"v_mfma_f32_16x16x32_bf16 v[144:147], a[104:107], a[72:75], v[144:147]\n" \
"ds_read_b64_tr_b16 a[44:45], v11 offset:33280\n" \
"v_mfma_f32_16x16x32_bf16 v[148:151], a[108:111], a[72:75], v[148:151]\n" \
"v_mfma_f32_16x16x32_bf16 v[152:155], a[104:107], a[76:79], v[152:155]\n" \
"ds_read_b64_tr_b16 a[46:47], v12 offset:33280\n" \
"v_mfma_f32_16x16x32_bf16 v[156:159], a[108:111], a[76:79], v[156:159]\n" \
"v_mfma_f32_16x16x32_bf16 v[160:163], a[112:115], a[72:75], v[160:163]\n" \
"ds_read_b128 a[16:19], v5 offset:40960\n" \
"v_mfma_f32_16x16x32_bf16 v[164:167], a[116:119], a[72:75], v[164:167]\n" \
"v_mfma_f32_16x16x32_bf16 v[168:171], a[112:115], a[76:79], v[168:171]\n" \
"s_add_u32 m0, s50, 4096\n" \
"ds_read_b64_tr_b16 a[48:49], v9 offset:33792\n" \
"v_mfma_f32_16x16x32_bf16 v[172:175], a[116:119], a[76:79], v[172:175]\n" \
"buffer_load_dwordx4 v14, s[28:31], s46 offen lds\n" \
"v_mfma_f32_16x16x32_bf16 v[176:179], a[120:123], a[72:75], v[176:179]\n" \
"ds_read_b64_tr_b16 a[50:51], v10 offset:33792\n" \
"v_mfma_f32_16x16x32_bf16 v[180:183], a[124:127], a[72:75], v[180:183]\n" \
"v_mfma_f32_16x16x32_bf16 v[184:187], a[120:123], a[76:79], v[184:187]\n" \
"ds_read_b128 a[20:23], v5 offset:43008\n" \
"v_mfma_f32_16x16x32_bf16 v[188:191], a[124:127], a[76:79], v[188:191]\n" \
"v_mfma_f32_16x16x32_bf16 v[192:195], a[96:99], a[80:83], v[192:195]\n" \
"ds_read_b64_tr_b16 a[52:53], v11 offset:33792\n" \
"v_mfma_f32_16x16x32_bf16 v[196:199], a[100:103], a[80:83], v[196:199]\n" \
"v_mfma_f32_16x16x32_bf16 v[200:203], a[96:99], a[84:87], v[200:203]\n" \
"s_add_u32 m0, s22, 18176\n" \
"ds_read_b64_tr_b16 a[54:55], v12 offset:33792\n" \
"v_mfma_f32_16x16x32_bf16 v[204:207], a[100:103], a[84:87], v[204:207]\n" \
"buffer_load_dwordx4 v21, s[32:35], s47 offen offset:256 lds\n" \
"v_mfma_f32_16x16x32_bf16 v[208:211], a[104:107], a[80:83], v[208:211]\n" \
"ds_read_b128 a[24:27], v5 offset:45056\n" \
"v_mfma_f32_16x16x32_bf16 v[212:215], a[108:111], a[80:83], v[212:215]\n" \
"v_mfma_f32_16x16x32_bf16 v[216:219], a[104:107], a[84:87], v[216:219]\n" \
"ds_read_b64_tr_b16 a[56:57], v9 offset:34304\n" \
"v_mfma_f32_16x16x32_bf16 v[220:223], a[108:111], a[84:87], v[220:223]\n" \
"v_mfma_f32_16x16x32_bf16 v[224:227], a[112:115], a[80:83], v[224:227]\n" \
"ds_read_b64_tr_b16 a[58:59], v10 offset:34304\n" \
"v_mfma_f32_16x16x32_bf16 v[228:231], a[116:119], a[80:83], v[228:231]\n" \
"v_mfma_f32_16x16x32_bf16 v[232:235], a[112:115], a[84:87], v[232:235]\n" \
"s_add_u32 m0, s50, 8192\n" \
"ds_read_b128 a[28:31], v5 offset:47104\n" \
"v_mfma_f32_16x16x32_bf16 v[236:239], a[116:119], a[84:87], v[236:239]\n" \
"buffer_load_dwordx4 v15, s[28:31], s46 offen lds\n" \
"v_mfma_f32_16x16x32_bf16 v[240:243], a[120:123], a[80:83], v[240:243]\n" \
"ds_read_b64_tr_b16 a[60:61], v11 offset:34304\n" \
"v_mfma_f32_16x16x32_bf16 v[244:247], a[124:127], a[80:83], v[244:247]\n" \
"v_mfma_f32_16x16x32_bf16 v[248:251], a[120:123], a[84:87], v[248:251]\n" \
"ds_read_b64_tr_b16 a[62:63], v12 offset:34304\n" \
"v_mfma_f32_16x16x32_bf16 v[252:255], a[124:127], a[84:87], v[252:255]\n" \
"v_mfma_f32_16x16x32_bf16 a[128:131], a[96:99], a[88:91], a[128:131]\n" \
"v_mfma_f32_16x16x32_bf16 a[132:135], a[100:103], a[88:91], a[132:135]\n" \
"v_mfma_f32_16x16x32_bf16 a[136:139], a[96:99], a[92:95], a[136:139]\n" \
"s_add_u32 m0, s22, 19072\n" \
"v_mfma_f32_16x16x32_bf16 a[140:143], a[100:103], a[92:95], a[140:143]\n" \
"buffer_load_dwordx4 v21, s[32:35], s47 offen offset:384 lds\n" \
"v_mfma_f32_16x16x32_bf16 a[144:147], a[104:107], a[88:91], a[144:147]\n" \
"v_mov_b32 v58, s14\n" \
"v_mfma_f32_16x16x32_bf16 a[148:151], a[108:111], a[88:91], a[148:151]\n" \
"v_mov_b32 v59, s15\n" \
"v_mfma_f32_16x16x32_bf16 a[152:155], a[104:107], a[92:95], a[152:155]\n" \
"v_mfma_f32_16x16x32_bf16 a[156:159], a[108:111], a[92:95], a[156:159]\n" \
"s_waitcnt vmcnt(14) lgkmcnt(0)\n" \
"v_mfma_f32_16x16x32_bf16 a[160:163], a[112:115], a[88:91], a[160:163]\n" \
"s_barrier\n" \
"v_mfma_f32_16x16x32_bf16 a[164:167], a[116:119], a[88:91], a[164:167]\n" \
"v_mfma_f32_16x16x32_bf16 a[168:171], a[112:115], a[92:95], a[168:171]\n" \
"s_add_u32 m0, s50, 12288\n" \
"v_mfma_f32_16x16x32_bf16 a[172:175], a[116:119], a[92:95], a[172:175]\n" \
"buffer_load_dwordx4 v16, s[28:31], s46 offen lds\n" \
"v_mfma_f32_16x16x32_bf16 a[176:179], a[120:123], a[88:91], a[176:179]\n" \
"v_mfma_f32_16x16x32_bf16 a[180:183], a[124:127], a[88:91], a[180:183]\n" \
"v_mfma_f32_16x16x32_bf16 a[184:187], a[120:123], a[92:95], a[184:187]\n" \
"v_mfma_f32_16x16x32_bf16 a[188:191], a[124:127], a[92:95], a[188:191]\n" \
"buffer_store_dwordx2 v[58:59], v56, s[36:39], s23 offen sc1\n" \
"v_mfma_f32_16x16x32_bf16 v[64:67], a[32:35], a[0:3], v[64:67]\n" \
"ds_read_b128 a[64:67], v6 offset:32768\n" \
"v_mfma_f32_16x16x32_bf16 v[68:71], a[36:39], a[0:3], v[68:71]\n" \
"v_mfma_f32_16x16x32_bf16 v[72:75], a[32:35], a[4:7], v[72:75]\n" \
"s_mov_b32 s47, 3670016\n" \
"s_add_u32 m0, s22, 32768\n" \
"ds_read_b64_tr_b16 a[96:97], v9 offset:49152\n" \
"v_mfma_f32_16x16x32_bf16 v[76:79], a[36:39], a[4:7], v[76:79]\n" \
"buffer_load_dwordx4 v21, s[32:35], s47 offen offset:0 lds\n" \
"v_mfma_f32_16x16x32_bf16 v[80:83], a[40:43], a[0:3], v[80:83]\n" \
"ds_read_b64_tr_b16 a[98:99], v10 offset:49152\n" \
"v_mfma_f32_16x16x32_bf16 v[84:87], a[44:47], a[0:3], v[84:87]\n" \
"v_mfma_f32_16x16x32_bf16 v[88:91], a[40:43], a[4:7], v[88:91]\n" \
"ds_read_b128 a[68:71], v6 offset:34816\n" \
"v_mfma_f32_16x16x32_bf16 v[92:95], a[44:47], a[4:7], v[92:95]\n" \
"buffer_load_dwordx4 v[30:33], v23, s[68:71], 0 offen offset:0\n" \
"v_mfma_f32_16x16x32_bf16 v[96:99], a[48:51], a[0:3], v[96:99]\n" \
"ds_read_b64_tr_b16 a[100:101], v11 offset:49152\n" \
"v_mfma_f32_16x16x32_bf16 v[100:103], a[52:55], a[0:3], v[100:103]\n" \
"v_mfma_f32_16x16x32_bf16 v[104:107], a[48:51], a[4:7], v[104:107]\n" \
"s_mov_b32 s46, 896\n" \
"s_add_u32 m0, s50, 16384\n" \
"ds_read_b64_tr_b16 a[102:103], v12 offset:49152\n" \
"v_mfma_f32_16x16x32_bf16 v[108:111], a[52:55], a[4:7], v[108:111]\n" \
"buffer_load_dwordx4 v17, s[28:31], s46 offen lds\n" \
"v_mfma_f32_16x16x32_bf16 v[112:115], a[56:59], a[0:3], v[112:115]\n" \
"ds_read_b128 a[72:75], v6 offset:36864\n" \
"v_mfma_f32_16x16x32_bf16 v[116:119], a[60:63], a[0:3], v[116:119]\n" \
"v_mfma_f32_16x16x32_bf16 v[120:123], a[56:59], a[4:7], v[120:123]\n" \
"ds_read_b64_tr_b16 a[104:105], v9 offset:49664\n" \
"v_mfma_f32_16x16x32_bf16 v[124:127], a[60:63], a[4:7], v[124:127]\n" \
"buffer_load_dword v50, v25, s[68:71], 0 offen offset:0\n" \
"v_mfma_f32_16x16x32_bf16 v[128:131], a[32:35], a[8:11], v[128:131]\n" \
"ds_read_b64_tr_b16 a[106:107], v10 offset:49664\n" \
"v_mfma_f32_16x16x32_bf16 v[132:135], a[36:39], a[8:11], v[132:135]\n" \
"v_mfma_f32_16x16x32_bf16 v[136:139], a[32:35], a[12:15], v[136:139]\n" \
"s_add_u32 m0, s22, 33664\n" \
"ds_read_b128 a[76:79], v6 offset:38912\n" \
"v_mfma_f32_16x16x32_bf16 v[140:143], a[36:39], a[12:15], v[140:143]\n" \
"buffer_load_dwordx4 v21, s[32:35], s47 offen offset:128 lds\n" \
"v_mfma_f32_16x16x32_bf16 v[144:147], a[40:43], a[8:11], v[144:147]\n" \
"ds_read_b64_tr_b16 a[108:109], v11 offset:49664\n" \
"v_mfma_f32_16x16x32_bf16 v[148:151], a[44:47], a[8:11], v[148:151]\n" \
"v_mfma_f32_16x16x32_bf16 v[152:155], a[40:43], a[12:15], v[152:155]\n" \
"ds_read_b64_tr_b16 a[110:111], v12 offset:49664\n" \
"v_mfma_f32_16x16x32_bf16 v[156:159], a[44:47], a[12:15], v[156:159]\n" \
"LH2_%=:\ns_cbranch_execz LH10_%=\n" \
"buffer_load_dwordx4 v[34:37], v24, s[68:71], 0 offen offset:0\n" \
"v_mfma_f32_16x16x32_bf16 v[160:163], a[48:51], a[8:11], v[160:163]\n" \
"ds_read_b128 a[80:83], v6 offset:40960\n" \
"v_mfma_f32_16x16x32_bf16 v[164:167], a[52:55], a[8:11], v[164:167]\n" \
"v_mfma_f32_16x16x32_bf16 v[168:171], a[48:51], a[12:15], v[168:171]\n" \
"s_add_u32 m0, s50, 20480\n" \
"ds_read_b64_tr_b16 a[112:113], v9 offset:50176\n" \
"v_mfma_f32_16x16x32_bf16 v[172:175], a[52:55], a[12:15], v[172:175]\n" \
"buffer_load_dwordx4 v18, s[28:31], s46 offen lds\n" \
"v_mfma_f32_16x16x32_bf16 v[176:179], a[56:59], a[8:11], v[176:179]\n" \
"ds_read_b64_tr_b16 a[114:115], v10 offset:50176\n" \
"v_mfma_f32_16x16x32_bf16 v[180:183], a[60:63], a[8:11], v[180:183]\n" \
"v_mfma_f32_16x16x32_bf16 v[184:187], a[56:59], a[12:15], v[184:187]\n" \
"ds_read_b128 a[84:87], v6 offset:43008\n" \
"v_mfma_f32_16x16x32_bf16 v[188:191], a[60:63], a[12:15], v[188:191]\n" \
"buffer_load_dword v51, v24, s[68:71], 0 offen offset:16\n" \
"v_mfma_f32_16x16x32_bf16 v[192:195], a[32:35], a[16:19], v[192:195]\n" \
"ds_read_b64_tr_b16 a[116:117], v11 offset:50176\n" \
"v_mfma_f32_16x16x32_bf16 v[196:199], a[36:39], a[16:19], v[196:199]\n" \
"v_mfma_f32_16x16x32_bf16 v[200:203], a[32:35], a[20:23], v[200:203]\n" \
"s_add_u32 m0, s22, 34560\n" \
"ds_read_b64_tr_b16 a[118:119], v12 offset:50176\n" \
"v_mfma_f32_16x16x32_bf16 v[204:207], a[36:39], a[20:23], v[204:207]\n" \
"buffer_load_dwordx4 v21, s[32:35], s47 offen offset:256 lds\n" \
"v_mfma_f32_16x16x32_bf16 v[208:211], a[40:43], a[16:19], v[208:211]\n" \
"ds_read_b128 a[88:91], v6 offset:45056\n" \
"v_mfma_f32_16x16x32_bf16 v[212:215], a[44:47], a[16:19], v[212:215]\n" \
"v_mfma_f32_16x16x32_bf16 v[216:219], a[40:43], a[20:23], v[216:219]\n" \
"ds_read_b64_tr_b16 a[120:121], v9 offset:50688\n" \
"v_mfma_f32_16x16x32_bf16 v[220:223], a[44:47], a[20:23], v[220:223]\n" \
"buffer_load_dwordx4 v[38:41], v24, s[68:71], 0 offen offset:1024\n" \
"v_mfma_f32_16x16x32_bf16 v[224:227], a[48:51], a[16:19], v[224:227]\n" \
"ds_read_b64_tr_b16 a[122:123], v10 offset:50688\n" \
"v_mfma_f32_16x16x32_bf16 v[228:231], a[52:55], a[16:19], v[228:231]\n" \
"v_mfma_f32_16x16x32_bf16 v[232:235], a[48:51], a[20:23], v[232:235]\n" \
"s_add_u32 m0, s50, 24576\n" \
"ds_read_b128 a[92:95], v6 offset:47104\n" \
"v_mfma_f32_16x16x32_bf16 v[236:239], a[52:55], a[20:23], v[236:239]\n" \
"buffer_load_dwordx4 v19, s[28:31], s46 offen lds\n" \
"v_mfma_f32_16x16x32_bf16 v[240:243], a[56:59], a[16:19], v[240:243]\n" \
"ds_read_b64_tr_b16 a[124:125], v11 offset:50688\n" \
"v_mfma_f32_16x16x32_bf16 v[244:247], a[60:63], a[16:19], v[244:247]\n" \
"v_mfma_f32_16x16x32_bf16 v[248:251], a[56:59], a[20:23], v[248:251]\n" \
"ds_read_b64_tr_b16 a[126:127], v12 offset:50688\n" \
"v_mfma_f32_16x16x32_bf16 v[252:255], a[60:63], a[20:23], v[252:255]\n" \
"buffer_load_dword v52, v24, s[68:71], 0 offen offset:1040\n" \
"v_mfma_f32_16x16x32_bf16 a[128:131], a[32:35], a[24:27], a[128:131]\n" \
"v_mfma_f32_16x16x32_bf16 a[132:135], a[36:39], a[24:27], a[132:135]\n" \
"v_mfma_f32_16x16x32_bf16 a[136:139], a[32:35], a[28:31], a[136:139]\n" \
"s_add_u32 m0, s22, 35456\n" \
"v_mfma_f32_16x16x32_bf16 a[140:143], a[36:39], a[28:31], a[140:143]\n" \
"buffer_load_dwordx4 v21, s[32:35], s47 offen offset:384 lds\n" \
"v_mfma_f32_16x16x32_bf16 a[144:147], a[40:43], a[24:27], a[144:147]\n" \
"v_mfma_f32_16x16x32_bf16 a[148:151], a[44:47], a[24:27], a[148:151]\n" \
"v_mfma_f32_16x16x32_bf16 a[152:155], a[40:43], a[28:31], a[152:155]\n" \
"v_mfma_f32_16x16x32_bf16 a[156:159], a[44:47], a[28:31], a[156:159]\n" \
"s_waitcnt vmcnt(23) lgkmcnt(0)\n" \
"v_mfma_f32_16x16x32_bf16 a[160:163], a[48:51], a[24:27], a[160:163]\n" \
"s_barrier\n" \
"v_mfma_f32_16x16x32_bf16 a[164:167], a[52:55], a[24:27], a[164:167]\n" \
"v_mfma_f32_16x16x32_bf16 a[168:171], a[48:51], a[28:31], a[168:171]\n" \
"s_add_u32 m0, s50, 28672\n" \
"v_mfma_f32_16x16x32_bf16 a[172:175], a[52:55], a[28:31], a[172:175]\n" \
"buffer_load_dwordx4 v20, s[28:31], s46 offen lds\n" \
"v_mfma_f32_16x16x32_bf16 a[176:179], a[56:59], a[24:27], a[176:179]\n" \
"v_mfma_f32_16x16x32_bf16 a[180:183], a[60:63], a[24:27], a[180:183]\n" \
"v_mfma_f32_16x16x32_bf16 a[184:187], a[56:59], a[28:31], a[184:187]\n" \
"v_mfma_f32_16x16x32_bf16 a[188:191], a[60:63], a[28:31], a[188:191]\n" \
"s_mov_b64 exec, 0xffff\nglobal_load_dword v22, v1, s[76:77]\ns_mov_b64 exec, -1\n" \
"v_mfma_f32_16x16x32_bf16 v[64:67], a[96:99], a[64:67], v[64:67]\n" \
"s_mov_b64 s[28:29], s[64:65]\n" \
"ds_read_b128 a[0:3], v7 offset:0\n" \
"v_mfma_f32_16x16x32_bf16 v[68:71], a[100:103], a[64:67], v[68:71]\n" \
"v_mfma_f32_16x16x32_bf16 v[72:75], a[96:99], a[68:71], v[72:75]\n" \
"s_mov_b32 s47, 3932160\n" \
"s_add_u32 m0, s22, 49152\n" \
"ds_read_b64_tr_b16 a[32:33], v9 offset:0\n" \
"v_mfma_f32_16x16x32_bf16 v[76:79], a[100:103], a[68:71], v[76:79]\n" \
"buffer_load_dwordx4 v21, s[32:35], s47 offen offset:0 lds\n" \
"v_mfma_f32_16x16x32_bf16 v[80:83], a[104:107], a[64:67], v[80:83]\n" \
"s_and_b32 s29, s29, 0xffff\n" \
"ds_read_b64_tr_b16 a[34:35], v10 offset:0\n" \
"v_mfma_f32_16x16x32_bf16 v[84:87], a[108:111], a[64:67], v[84:87]\n" \
"v_mfma_f32_16x16x32_bf16 v[88:91], a[104:107], a[68:71], v[88:91]\n" \
"s_mov_b32 s30, 4194304\n" \
"ds_read_b128 a[4:7], v7 offset:2048\n" \
"v_mfma_f32_16x16x32_bf16 v[92:95], a[108:111], a[68:71], v[92:95]\n" \
"buffer_load_dwordx4 v[42:45], v24, s[68:71], 0 offen offset:2048\n" \
"v_mfma_f32_16x16x32_bf16 v[96:99], a[112:115], a[64:67], v[96:99]\n" \
"s_lshl_b32 s60, s7, 18\n" \
"ds_read_b64_tr_b16 a[36:37], v11 offset:0\n" \
"v_mfma_f32_16x16x32_bf16 v[100:103], a[116:119], a[64:67], v[100:103]\n" \
"v_mfma_f32_16x16x32_bf16 v[104:107], a[112:115], a[68:71], v[104:107]\n" \
"s_add_u32 m0, s22, 50048\n" \
"ds_read_b64_tr_b16 a[38:39], v12 offset:0\n" \
"v_mfma_f32_16x16x32_bf16 v[108:111], a[116:119], a[68:71], v[108:111]\n" \
"buffer_load_dwordx4 v21, s[32:35], s47 offen offset:128 lds\n" \
"v_mfma_f32_16x16x32_bf16 v[112:115], a[120:123], a[64:67], v[112:115]\n" \
"s_sub_u32 s61, 3932160, s60\n" \
"ds_read_b128 a[8:11], v7 offset:4096\n" \
"v_mfma_f32_16x16x32_bf16 v[116:119], a[124:127], a[64:67], v[116:119]\n" \
"v_mfma_f32_16x16x32_bf16 v[120:123], a[120:123], a[68:71], v[120:123]\n" \
"ds_read_b64_tr_b16 a[40:41], v9 offset:512\n" \
"v_add_u32 v13, s60, v63\n" \
"v_mfma_f32_16x16x32_bf16 v[124:127], a[124:127], a[68:71], v[124:127]\n" \
"buffer_load_dword v53, v24, s[68:71], 0 offen offset:2064\n" \
"v_mfma_f32_16x16x32_bf16 v[128:131], a[96:99], a[72:75], v[128:131]\n" \
"ds_read_b64_tr_b16 a[42:43], v10 offset:512\n" \
"v_add_u32 v17, s61, v63\n" \
"v_mfma_f32_16x16x32_bf16 v[132:135], a[100:103], a[72:75], v[132:135]\n" \
"v_mfma_f32_16x16x32_bf16 v[136:139], a[96:99], a[76:79], v[136:139]\n" \
"s_add_u32 m0, s22, 50944\n" \
"ds_read_b128 a[12:15], v7 offset:6144\n" \
"v_mfma_f32_16x16x32_bf16 v[140:143], a[100:103], a[76:79], v[140:143]\n" \
"buffer_load_dwordx4 v21, s[32:35], s47 offen offset:256 lds\n" \
"v_mfma_f32_16x16x32_bf16 v[144:147], a[104:107], a[72:75], v[144:147]\n" \
"ds_read_b64_tr_b16 a[44:45], v11 offset:512\n" \
"v_add_u32 v14, 0x10000, v13\n" \
"v_mfma_f32_16x16x32_bf16 v[148:151], a[108:111], a[72:75], v[148:151]\n" \
"v_mfma_f32_16x16x32_bf16 v[152:155], a[104:107], a[76:79], v[152:155]\n" \
"ds_read_b64_tr_b16 a[46:47], v12 offset:512\n" \
"v_add_u32 v18, 0x10000, v17\n" \
"v_mfma_f32_16x16x32_bf16 v[156:159], a[108:111], a[76:79], v[156:159]\n" \
"buffer_load_dwordx4 v[46:49], v24, s[68:71], 0 offen offset:3072\n" \
"v_mfma_f32_16x16x32_bf16 v[160:163], a[112:115], a[72:75], v[160:163]\n" \
"ds_read_b128 a[16:19], v7 offset:8192\n" \
"v_add_u32 v15, 0x10000, v14\n" \
"v_mfma_f32_16x16x32_bf16 v[164:167], a[116:119], a[72:75], v[164:167]\n" \
"s_add_u32 s76, s76, 2097152\ns_addc_u32 s77, s77, 0\n" \
"v_mfma_f32_16x16x32_bf16 v[168:171], a[112:115], a[76:79], v[168:171]\n" \
"s_add_u32 m0, s22, 51840\n" \
"ds_read_b64_tr_b16 a[48:49], v9 offset:1024\n" \
"v_mfma_f32_16x16x32_bf16 v[172:175], a[116:119], a[76:79], v[172:175]\n" \
"buffer_load_dwordx4 v21, s[32:35], s47 offen offset:384 lds\n" \
"v_mfma_f32_16x16x32_bf16 v[176:179], a[120:123], a[72:75], v[176:179]\n" \
"ds_read_b64_tr_b16 a[50:51], v10 offset:1024\n" \
"v_add_u32 v19, 0x10000, v18\n" \
"v_mfma_f32_16x16x32_bf16 v[180:183], a[124:127], a[72:75], v[180:183]\n" \
"v_mfma_f32_16x16x32_bf16 v[184:187], a[120:123], a[76:79], v[184:187]\n" \
"ds_read_b128 a[20:23], v7 offset:10240\n" \
"v_add_u32 v16, 0x10000, v15\n" \
"v_mfma_f32_16x16x32_bf16 v[188:191], a[124:127], a[76:79], v[188:191]\n" \
"buffer_load_dword v54, v24, s[68:71], 0 offen offset:3088\n" \
"v_mfma_f32_16x16x32_bf16 v[192:195], a[96:99], a[80:83], v[192:195]\n" \
"ds_read_b64_tr_b16 a[52:53], v11 offset:1024\n" \
"v_add_u32 v20, 0x10000, v19\n" \
"v_mfma_f32_16x16x32_bf16 v[196:199], a[100:103], a[80:83], v[196:199]\n" \
"v_mfma_f32_16x16x32_bf16 v[200:203], a[96:99], a[84:87], v[200:203]\n" \
"ds_read_b64_tr_b16 a[54:55], v12 offset:1024\n" \
"v_mfma_f32_16x16x32_bf16 v[204:207], a[100:103], a[84:87], v[204:207]\n" \
"s_mov_b64 exec, 0xffff\nglobal_load_dword v22, v1, s[76:77]\ns_mov_b64 exec, -1\n" \
"v_mfma_f32_16x16x32_bf16 v[208:211], a[104:107], a[80:83], v[208:211]\n" \
"ds_read_b128 a[24:27], v7 offset:12288\n" \
"v_mfma_f32_16x16x32_bf16 v[212:215], a[108:111], a[80:83], v[212:215]\n" \
"v_mfma_f32_16x16x32_bf16 v[216:219], a[104:107], a[84:87], v[216:219]\n" \
"ds_read_b64_tr_b16 a[56:57], v9 offset:1536\n" \
"v_mfma_f32_16x16x32_bf16 v[220:223], a[108:111], a[84:87], v[220:223]\n" \
"global_load_short_d16_hi v28, v29, s[12:13]\n" \
"v_mfma_f32_16x16x32_bf16 v[224:227], a[112:115], a[80:83], v[224:227]\n" \
"ds_read_b64_tr_b16 a[58:59], v10 offset:1536\n" \
"v_mfma_f32_16x16x32_bf16 v[228:231], a[116:119], a[80:83], v[228:231]\n" \
"v_mfma_f32_16x16x32_bf16 v[232:235], a[112:115], a[84:87], v[232:235]\n" \
"ds_read_b128 a[28:31], v7 offset:14336\n" \
"v_mfma_f32_16x16x32_bf16 v[236:239], a[116:119], a[84:87], v[236:239]\n" \
"v_mfma_f32_16x16x32_bf16 v[240:243], a[120:123], a[80:83], v[240:243]\n" \
"ds_read_b64_tr_b16 a[60:61], v11 offset:1536\n" \
"v_mfma_f32_16x16x32_bf16 v[244:247], a[124:127], a[80:83], v[244:247]\n" \
"v_mfma_f32_16x16x32_bf16 v[248:251], a[120:123], a[84:87], v[248:251]\n" \
"ds_read_b64_tr_b16 a[62:63], v12 offset:1536\n" \
"v_mfma_f32_16x16x32_bf16 v[252:255], a[124:127], a[84:87], v[252:255]\n" \
"v_mfma_f32_16x16x32_bf16 a[128:131], a[96:99], a[88:91], a[128:131]\n" \
"v_mfma_f32_16x16x32_bf16 a[132:135], a[100:103], a[88:91], a[132:135]\n" \
"v_mfma_f32_16x16x32_bf16 a[136:139], a[96:99], a[92:95], a[136:139]\n" \
"v_mfma_f32_16x16x32_bf16 a[140:143], a[100:103], a[92:95], a[140:143]\n" \
"v_mfma_f32_16x16x32_bf16 a[144:147], a[104:107], a[88:91], a[144:147]\n" \
"v_mfma_f32_16x16x32_bf16 a[148:151], a[108:111], a[88:91], a[148:151]\n" \
"v_mfma_f32_16x16x32_bf16 a[152:155], a[104:107], a[92:95], a[152:155]\n" \
"v_mfma_f32_16x16x32_bf16 a[156:159], a[108:111], a[92:95], a[156:159]\n" \
"s_waitcnt vmcnt(27) lgkmcnt(0)\n" \
"v_mfma_f32_16x16x32_bf16 a[160:163], a[112:115], a[88:91], a[160:163]\n" \
"s_barrier\n" \
"v_mfma_f32_16x16x32_bf16 a[164:167], a[116:119], a[88:91], a[164:167]\n" \
"v_mfma_f32_16x16x32_bf16 a[168:171], a[112:115], a[92:95], a[168:171]\n" \
"v_mfma_f32_16x16x32_bf16 a[172:175], a[116:119], a[92:95], a[172:175]\n" \
"v_mfma_f32_16x16x32_bf16 a[176:179], a[120:123], a[88:91], a[176:179]\n" \
"v_mfma_f32_16x16x32_bf16 a[180:183], a[124:127], a[88:91], a[180:183]\n" \
"v_mfma_f32_16x16x32_bf16 a[184:187], a[120:123], a[92:95], a[184:187]\n" \
"v_mfma_f32_16x16x32_bf16 a[188:191], a[124:127], a[92:95], a[188:191]\n" \
"v_mfma_f32_16x16x32_bf16 v[64:67], a[32:35], a[0:3], v[64:67]\n" \
"ds_read_b128 a[64:67], v8 offset:0\n" \
"v_mfma_f32_16x16x32_bf16 v[68:71], a[36:39], a[0:3], v[68:71]\n" \
"v_mfma_f32_16x16x32_bf16 v[72:75], a[32:35], a[4:7], v[72:75]\n" \
"ds_read_b64_tr_b16 a[96:97], v9 offset:16384\n" \
"v_mfma_f32_16x16x32_bf16 v[76:79], a[36:39], a[4:7], v[76:79]\n" \
"v_mfma_f32_16x16x32_bf16 v[80:83], a[40:43], a[0:3], v[80:83]\n" \
"ds_read_b64_tr_b16 a[98:99], v10 offset:16384\n" \
"v_mfma_f32_16x16x32_bf16 v[84:87], a[44:47], a[0:3], v[84:87]\n" \
"v_mfma_f32_16x16x32_bf16 v[88:91], a[40:43], a[4:7], v[88:91]\n" \
"ds_read_b128 a[68:71], v8 offset:2048\n" \
"v_mfma_f32_16x16x32_bf16 v[92:95], a[44:47], a[4:7], v[92:95]\n" \
"v_mfma_f32_16x16x32_bf16 v[96:99], a[48:51], a[0:3], v[96:99]\n" \
"ds_read_b64_tr_b16 a[100:101], v11 offset:16384\n" \
"v_mfma_f32_16x16x32_bf16 v[100:103], a[52:55], a[0:3], v[100:103]\n" \
"v_mfma_f32_16x16x32_bf16 v[104:107], a[48:51], a[4:7], v[104:107]\n" \
"ds_read_b64_tr_b16 a[102:103], v12 offset:16384\n" \
"v_mfma_f32_16x16x32_bf16 v[108:111], a[52:55], a[4:7], v[108:111]\n" \
"v_mfma_f32_16x16x32_bf16 v[112:115], a[56:59], a[0:3], v[112:115]\n" \
"ds_read_b128 a[72:75], v8 offset:4096\n" \
"v_mfma_f32_16x16x32_bf16 v[116:119], a[60:63], a[0:3], v[116:119]\n" \
"v_mfma_f32_16x16x32_bf16 v[120:123], a[56:59], a[4:7], v[120:123]\n" \
"ds_read_b64_tr_b16 a[104:105], v9 offset:16896\n" \
"v_mfma_f32_16x16x32_bf16 v[124:127], a[60:63], a[4:7], v[124:127]\n" \
"v_mfma_f32_16x16x32_bf16 v[128:131], a[32:35], a[8:11], v[128:131]\n" \
"ds_read_b64_tr_b16 a[106:107], v10 offset:16896\n" \
"v_mfma_f32_16x16x32_bf16 v[132:135], a[36:39], a[8:11], v[132:135]\n" \
"v_mfma_f32_16x16x32_bf16 v[136:139], a[32:35], a[12:15], v[136:139]\n" \
"ds_read_b128 a[76:79], v8 offset:6144\n" \
"v_mfma_f32_16x16x32_bf16 v[140:143], a[36:39], a[12:15], v[140:143]\n" \
"buffer_load_dwordx2 v[58:59], v57, s[36:39], 0 offen sc1\n" \
"v_mfma_f32_16x16x32_bf16 v[144:147], a[40:43], a[8:11], v[144:147]\n" \
"ds_read_b64_tr_b16 a[108:109], v11 offset:16896\n" \
"v_mfma_f32_16x16x32_bf16 v[148:151], a[44:47], a[8:11], v[148:151]\n" \
"v_mfma_f32_16x16x32_bf16 v[152:155], a[40:43], a[12:15], v[152:155]\n" \
"ds_read_b64_tr_b16 a[110:111], v12 offset:16896\n" \
"v_mfma_f32_16x16x32_bf16 v[156:159], a[44:47], a[12:15], v[156:159]\n" \
"v_mfma_f32_16x16x32_bf16 v[160:163], a[48:51], a[8:11], v[160:163]\n" \
"ds_read_b128 a[80:83], v8 offset:8192\n" \
"v_mfma_f32_16x16x32_bf16 v[164:167], a[52:55], a[8:11], v[164:167]\n" \
"v_mfma_f32_16x16x32_bf16 v[168:171], a[48:51], a[12:15], v[168:171]\n" \
"ds_read_b64_tr_b16 a[112:113], v9 offset:17408\n" \
"v_mfma_f32_16x16x32_bf16 v[172:175], a[52:55], a[12:15], v[172:175]\n" \
"v_mfma_f32_16x16x32_bf16 v[176:179], a[56:59], a[8:11], v[176:179]\n" \
"ds_read_b64_tr_b16 a[114:115], v10 offset:17408\n" \
"v_mfma_f32_16x16x32_bf16 v[180:183], a[60:63], a[8:11], v[180:183]\n" \
"v_mfma_f32_16x16x32_bf16 v[184:187], a[56:59], a[12:15], v[184:187]\n" \
"ds_read_b128 a[84:87], v8 offset:10240\n" \
"v_mfma_f32_16x16x32_bf16 v[188:191], a[60:63], a[12:15], v[188:191]\n" \
"v_mfma_f32_16x16x32_bf16 v[192:195], a[32:35], a[16:19], v[192:195]\n" \
"ds_read_b64_tr_b16 a[116:117], v11 offset:17408\n" \
"v_mfma_f32_16x16x32_bf16 v[196:199], a[36:39], a[16:19], v[196:199]\n" \
"v_mfma_f32_16x16x32_bf16 v[200:203], a[32:35], a[20:23], v[200:203]\n" \
"ds_read_b64_tr_b16 a[118:119], v12 offset:17408\n" \
"v_mfma_f32_16x16x32_bf16 v[204:207], a[36:39], a[20:23], v[204:207]\n" \
"v_mfma_f32_16x16x32_bf16 v[208:211], a[40:43], a[16:19], v[208:211]\n" \
"ds_read_b128 a[88:91], v8 offset:12288\n" \
"v_mfma_f32_16x16x32_bf16 v[212:215], a[44:47], a[16:19], v[212:215]\n" \
"v_mfma_f32_16x16x32_bf16 v[216:219], a[40:43], a[20:23], v[216:219]\n" \
"ds_read_b64_tr_b16 a[120:121], v9 offset:17920\n" \
"v_mfma_f32_16x16x32_bf16 v[220:223], a[44:47], a[20:23], v[220:223]\n" \
"v_mfma_f32_16x16x32_bf16 v[224:227], a[48:51], a[16:19], v[224:227]\n" \
"ds_read_b64_tr_b16 a[122:123], v10 offset:17920\n" \
"v_mfma_f32_16x16x32_bf16 v[228:231], a[52:55], a[16:19], v[228:231]\n" \
"v_mfma_f32_16x16x32_bf16 v[232:235], a[48:51], a[20:23], v[232:235]\n" \
"ds_read_b128 a[92:95], v8 offset:14336\n" \
"v_mfma_f32_16x16x32_bf16 v[236:239], a[52:55], a[20:23], v[236:239]\n" \
"v_mfma_f32_16x16x32_bf16 v[240:243], a[56:59], a[16:19], v[240:243]\n" \
"ds_read_b64_tr_b16 a[124:125], v11 offset:17920\n" \
"v_mfma_f32_16x16x32_bf16 v[244:247], a[60:63], a[16:19], v[244:247]\n" \
"v_mfma_f32_16x16x32_bf16 v[248:251], a[56:59], a[20:23], v[248:251]\n" \
"ds_read_b64_tr_b16 a[126:127], v12 offset:17920\n" \
"v_mfma_f32_16x16x32_bf16 v[252:255], a[60:63], a[20:23], v[252:255]\n" \
"v_mfma_f32_16x16x32_bf16 a[128:131], a[32:35], a[24:27], a[128:131]\n" \
"v_mfma_f32_16x16x32_bf16 a[132:135], a[36:39], a[24:27], a[132:135]\n" \
"v_mfma_f32_16x16x32_bf16 a[136:139], a[32:35], a[28:31], a[136:139]\n" \
"s_add_u32 s76, s76, 2097152\ns_addc_u32 s77, s77, 0\n" \
"v_mfma_f32_16x16x32_bf16 a[140:143], a[36:39], a[28:31], a[140:143]\n" \
"v_mfma_f32_16x16x32_bf16 a[144:147], a[40:43], a[24:27], a[144:147]\n" \
"v_mfma_f32_16x16x32_bf16 a[148:151], a[44:47], a[24:27], a[148:151]\n" \
"v_mfma_f32_16x16x32_bf16 a[152:155], a[40:43], a[28:31], a[152:155]\n" \
"v_mfma_f32_16x16x32_bf16 a[156:159], a[44:47], a[28:31], a[156:159]\n" \
"s_waitcnt vmcnt(0) lgkmcnt(0)\n" \
"v_mfma_f32_16x16x32_bf16 a[160:163], a[48:51], a[24:27], a[160:163]\n" \
"s_barrier\n" \
"v_mfma_f32_16x16x32_bf16 a[164:167], a[52:55], a[24:27], a[164:167]\n" \
"v_cmp_ne_u64 vcc, s[14:15], v[58:59]\n" \
"v_mfma_f32_16x16x32_bf16 a[168:171], a[48:51], a[28:31], a[168:171]\n" \
"s_mov_b64 exec, 0xffff\nglobal_load_dword v22, v1, s[76:77]\ns_mov_b64 exec, -1\n" \
"v_mfma_f32_16x16x32_bf16 a[172:175], a[52:55], a[28:31], a[172:175]\n" \
"s_cbranch_vccnz LPUK_%=\nLPUKR_%=:\n" \
"v_mfma_f32_16x16x32_bf16 a[176:179], a[56:59], a[24:27], a[176:179]\n" \
"v_mfma_f32_16x16x32_bf16 a[180:183], a[60:63], a[24:27], a[180:183]\n" \
"v_mfma_f32_16x16x32_bf16 a[184:187], a[56:59], a[28:31], a[184:187]\n" \
"v_mfma_f32_16x16x32_bf16 a[188:191], a[60:63], a[28:31], a[188:191]\n" \
"v_mfma_f32_16x16x32_bf16 v[64:67], a[96:99], a[64:67], v[64:67]\n" \
"s_mov_b64 s[32:33], s[44:45]\n" \
"ds_read_b128 a[0:3], v5 offset:0\n" \
"v_mfma_f32_16x16x32_bf16 v[68:71], a[100:103], a[64:67], v[68:71]\n" \
"v_mfma_f32_16x16x32_bf16 v[72:75], a[96:99], a[68:71], v[72:75]\n" \
"s_add_u32 m0, s56, 10304\n" \
"ds_read_b64_tr_b16 a[32:33], v9 offset:32768\n" \
"v_mfma_f32_16x16x32_bf16 v[76:79], a[100:103], a[68:71], v[76:79]\n" \
"buffer_load_dwordx4 v26, s[72:75], 0 offen offset:0 sc1 lds\n" \
"v_mfma_f32_16x16x32_bf16 v[80:83], a[104:107], a[64:67], v[80:83]\n" \
"s_mov_b32 s34, s49\n" \
"ds_read_b64_tr_b16 a[34:35], v10 offset:32768\n" \
"s_waitcnt vmcnt(3)\n" \
"v_mfma_f32_16x16x32_bf16 v[84:87], a[108:111], a[64:67], v[84:87]\n" \
"v_alignbyte_b32 v30, v31, v30, 2\n" \
"v_alignbyte_b32 v31, v32, v31, 2\n" \
"v_mfma_f32_16x16x32_bf16 v[88:91], a[104:107], a[68:71], v[88:91]\n" \
"s_add_u32 m0, s56, 11328\n" \
"ds_read_b128 a[4:7], v5 offset:2048\n" \
"v_alignbyte_b32 v32, v33, v32, 2\n" \
"v_mfma_f32_16x16x32_bf16 v[92:95], a[108:111], a[68:71], v[92:95]\n" \
"buffer_load_dwordx4 v27, s[72:75], 0 offen offset:0 sc1 lds\n" \
"v_mfma_f32_16x16x32_bf16 v[96:99], a[112:115], a[64:67], v[96:99]\n" \
"ds_read_b64_tr_b16 a[36:37], v11 offset:32768\n" \
"v_alignbyte_b32 v33, v50, v33, 2\n" \
"v_mfma_f32_16x16x32_bf16 v[100:103], a[116:119], a[64:67], v[100:103]\n" \
"ds_write_b128 v55, v[30:33] offset:5184\n" \
"v_alignbyte_b32 v34, v35, v34, 2\n" \
"v_mfma_f32_16x16x32_bf16 v[104:107], a[112:115], a[68:71], v[104:107]\n" \
"s_add_u32 m0, s56, 11328\n" \
"ds_read_b64_tr_b16 a[38:39], v12 offset:32768\n" \
"v_alignbyte_b32 v35, v36, v35, 2\n" \
"v_mfma_f32_16x16x32_bf16 v[108:111], a[116:119], a[68:71], v[108:111]\n" \
"buffer_load_dwordx4 v27, s[72:75], 0 offen offset:1024 sc1 lds\n" \
"v_mfma_f32_16x16x32_bf16 v[112:115], a[120:123], a[64:67], v[112:115]\n" \
"ds_read_b128 a[8:11], v5 offset:4096\n" \
"v_alignbyte_b32 v36, v37, v36, 2\n" \
"v_mfma_f32_16x16x32_bf16 v[116:119], a[124:127], a[64:67], v[116:119]\n" \
"v_alignbyte_b32 v37, v51, v37, 2\n" \
"ds_write_b128 v55, v[34:37] offset:6208\n" \
"v_mfma_f32_16x16x32_bf16 v[120:123], a[120:123], a[68:71], v[120:123]\n" \
"s_add_u32 m0, s56, 11328\n" \
"ds_read_b64_tr_b16 a[40:41], v9 offset:33280\n" \
"v_alignbyte_b32 v38, v39, v38, 2\n" \
"v_mfma_f32_16x16x32_bf16 v[124:127], a[124:127], a[68:71], v[124:127]\n" \
"buffer_load_dwordx4 v27, s[72:75], 0 offen offset:2048 sc1 lds\n" \
"v_mfma_f32_16x16x32_bf16 v[128:131], a[96:99], a[72:75], v[128:131]\n" \
"ds_read_b64_tr_b16 a[42:43], v10 offset:33280\n" \
"v_alignbyte_b32 v39, v40, v39, 2\n" \
"v_mfma_f32_16x16x32_bf16 v[132:135], a[100:103], a[72:75], v[132:135]\n" \
"v_alignbyte_b32 v40, v41, v40, 2\n" \
"v_alignbyte_b32 v41, v52, v41, 2\n" \
"v_mfma_f32_16x16x32_bf16 v[136:139], a[96:99], a[76:79], v[136:139]\n" \
"s_add_u32 m0, s56, 11328\n" \
"ds_read_b128 a[12:15], v5 offset:6144\n" \
"v_mfma_f32_16x16x32_bf16 v[140:143], a[100:103], a[76:79], v[140:143]\n" \
"buffer_load_dwordx4 v27, s[72:75], 0 offen offset:3072 sc1 lds\n" \
"v_mfma_f32_16x16x32_bf16 v[144:147], a[104:107], a[72:75], v[144:147]\n" \
"ds_read_b64_tr_b16 a[44:45], v11 offset:33280\n" \
"v_mfma_f32_16x16x32_bf16 v[148:151], a[108:111], a[72:75], v[148:151]\n" \
"ds_write_b128 v55, v[38:41] offset:7232\n" \
"v_alignbyte_b32 v42, v43, v42, 2\n" \
"v_mfma_f32_16x16x32_bf16 v[152:155], a[104:107], a[76:79], v[152:155]\n" \
"s_add_u32 m0, s56, 0\n" \
"ds_read_b64_tr_b16 a[46:47], v12 offset:33280\n" \
"v_alignbyte_b32 v43, v44, v43, 2\n" \
"v_mfma_f32_16x16x32_bf16 v[156:159], a[108:111], a[76:79], v[156:159]\n" \
"buffer_load_dwordx4 v23, s[68:71], 0 offen offset:0 lds\n" \
"v_mfma_f32_16x16x32_bf16 v[160:163], a[112:115], a[72:75], v[160:163]\n" \
"ds_read_b128 a[16:19], v5 offset:8192\n" \
"v_alignbyte_b32 v44, v45, v44, 2\n" \
"v_mfma_f32_16x16x32_bf16 v[164:167], a[116:119], a[72:75], v[164:167]\n" \
"v_alignbyte_b32 v45, v53, v45, 2\n" \
"ds_write_b128 v55, v[42:45] offset:8256\n" \
"v_mfma_f32_16x16x32_bf16 v[168:171], a[112:115], a[76:79], v[168:171]\n" \
"s_add_u32 m0, s56, 1024\n" \
"ds_read_b64_tr_b16 a[48:49], v9 offset:33792\n" \
"v_alignbyte_b32 v46, v47, v46, 2\n" \
"v_mfma_f32_16x16x32_bf16 v[172:175], a[116:119], a[76:79], v[172:175]\n" \
"buffer_load_dwordx4 v24, s[68:71], 0 offen offset:0 lds\n" \
"v_mfma_f32_16x16x32_bf16 v[176:179], a[120:123], a[72:75], v[176:179]\n" \
"ds_read_b64_tr_b16 a[50:51], v10 offset:33792\n" \
"v_alignbyte_b32 v47, v48, v47, 2\n" \
"v_mfma_f32_16x16x32_bf16 v[180:183], a[124:127], a[72:75], v[180:183]\n" \
"v_alignbyte_b32 v48, v49, v48, 2\n" \
"v_alignbyte_b32 v49, v54, v49, 2\n" \
"v_mfma_f32_16x16x32_bf16 v[184:187], a[120:123], a[76:79], v[184:187]\n" \
"s_add_u32 m0, s56, 1024\n" \
"ds_read_b128 a[20:23], v5 offset:10240\n" \
"v_mfma_f32_16x16x32_bf16 v[188:191], a[124:127], a[76:79], v[188:191]\n" \
"buffer_load_dwordx4 v24, s[68:71], 0 offen offset:1024 lds\n" \
"v_mfma_f32_16x16x32_bf16 v[192:195], a[96:99], a[80:83], v[192:195]\n" \
"ds_read_b64_tr_b16 a[52:53], v11 offset:33792\n" \
"v_mfma_f32_16x16x32_bf16 v[196:199], a[100:103], a[80:83], v[196:199]\n" \
"ds_write_b128 v55, v[46:49] offset:9280\n" \
"v_lshrrev_b32 v30, 4, v4\n" \
"v_mfma_f32_16x16x32_bf16 v[200:203], a[96:99], a[84:87], v[200:203]\n" \
"s_add_u32 m0, s56, 1024\n" \
"ds_read_b64_tr_b16 a[54:55], v12 offset:33792\n" \
"v_and_b32 v31, 15, v30\n" \
"v_mfma_f32_16x16x32_bf16 v[204:207], a[100:103], a[84:87], v[204:207]\n" \
"buffer_load_dwordx4 v24, s[68:71], 0 offen offset:2048 lds\n" \
"v_mfma_f32_16x16x32_bf16 v[208:211], a[104:107], a[80:83], v[208:211]\n" \
"ds_read_b128 a[24:27], v5 offset:12288\n" \
"v_lshrrev_b32 v32, 4, v30\n" \
"v_mfma_f32_16x16x32_bf16 v[212:215], a[108:111], a[80:83], v[212:215]\n" \
"v_add_u32 v33, 1, v31\n" \
"v_and_b32 v34, 1, v33\n" \
"v_mfma_f32_16x16x32_bf16 v[216:219], a[104:107], a[84:87], v[216:219]\n" \
"s_add_u32 m0, s56, 1024\n" \
"ds_read_b64_tr_b16 a[56:57], v9 offset:34304\n" \
"v_and_b32 v33, 30, v33\n" \
"v_mfma_f32_16x16x32_bf16 v[220:223], a[108:111], a[84:87], v[220:223]\n" \
"buffer_load_dwordx4 v24, s[68:71], 0 offen offset:3072 lds\n" \
"v_mfma_f32_16x16x32_bf16 v[224:227], a[112:115], a[80:83], v[224:227]\n" \
"ds_read_b64_tr_b16 a[58:59], v10 offset:34304\n" \
"v_mul_u32_u24 v34, 5184, v34\n" \
"v_mfma_f32_16x16x32_bf16 v[228:231], a[116:119], a[80:83], v[228:231]\n" \
"v_lshl_add_u32 v33, v33, 1, v34\n" \
"v_lshl_add_u32 v33, v32, 4, v33\n" \
"v_mfma_f32_16x16x32_bf16 v[232:235], a[112:115], a[84:87], v[232:235]\n" \
"s_mov_b32 s46, 0\n" \
"s_add_u32 m0, s50, 65536\n" \
"ds_read_b128 a[28:31], v5 offset:14336\n" \
"v_mfma_f32_16x16x32_bf16 v[236:239], a[116:119], a[84:87], v[236:239]\n" \
"buffer_load_dwordx4 v13, s[28:31], s46 offen lds\n" \
"v_mfma_f32_16x16x32_bf16 v[240:243], a[120:123], a[80:83], v[240:243]\n" \
"ds_read_b64_tr_b16 a[60:61], v11 offset:34304\n" \
"v_add_u32 v46, s56, v33\n" \
"v_mfma_f32_16x16x32_bf16 v[244:247], a[124:127], a[80:83], v[244:247]\n" \
"v_add_u32 v47, 1024, v46\n" \
"v_add_u32 v48, 2048, v46\n" \
"v_mfma_f32_16x16x32_bf16 v[248:251], a[120:123], a[84:87], v[248:251]\n" \
"s_add_u32 m0, s50, 69632\n" \
"ds_read_b64_tr_b16 a[62:63], v12 offset:34304\n" \
"v_add_u32 v49, 3072, v46\n" \
"v_mfma_f32_16x16x32_bf16 v[252:255], a[124:127], a[84:87], v[252:255]\n" \
"buffer_load_dwordx4 v14, s[28:31], s46 offen lds\n" \
"v_mfma_f32_16x16x32_bf16 a[128:131], a[96:99], a[88:91], a[128:131]\n" \
"v_lshlrev_b32 v33, 1, v31\n" \
"v_sub_u32 v33, v33, v32\n" \
"v_mfma_f32_16x16x32_bf16 a[132:135], a[100:103], a[88:91], a[132:135]\n" \
"v_lshlrev_b32 v33, 4, v33\n" \
"v_add_u32 v33, 10320, v33\n" \
"v_mfma_f32_16x16x32_bf16 a[136:139], a[96:99], a[92:95], a[136:139]\n" \
"s_add_u32 m0, s50, 73728\n" \
"v_add_u32 v50, s56, v33\n" \
"v_mfma_f32_16x16x32_bf16 a[140:143], a[100:103], a[92:95], a[140:143]\n" \
"buffer_load_dwordx4 v15, s[28:31], s46 offen lds\n" \
"v_mfma_f32_16x16x32_bf16 a[144:147], a[104:107], a[88:91], a[144:147]\n" \
"v_lshlrev_b32 v33, 5, v31\n" \
"v_lshl_add_u32 v55, v32, 3, v33\n" \
"v_mfma_f32_16x16x32_bf16 a[148:151], a[108:111], a[88:91], a[148:151]\n" \
"v_add_u32 v55, s22, v55\n" \
"v_add_u32 v33, s56, v33\n" \
"v_mfma_f32_16x16x32_bf16 a[152:155], a[104:107], a[92:95], a[152:155]\n" \
"v_xor_b32 v32, 1, v32\n" \
"v_mfma_f32_16x16x32_bf16 a[156:159], a[108:111], a[92:95], a[156:159]\n" \
"s_waitcnt vmcnt(18) lgkmcnt(0)\n" \
"v_lshl_add_u32 v51, v32, 3, v33\n" \
"v_mfma_f32_16x16x32_bf16 a[160:163], a[112:115], a[88:91], a[160:163]\n" \
"s_barrier\n" \
"v_mfma_f32_16x16x32_bf16 a[164:167], a[116:119], a[88:91], a[164:167]\n" \
"v_mfma_f32_16x16x32_bf16 a[168:171], a[112:115], a[92:95], a[168:171]\n" \
"s_add_u32 m0, s50, 77824\n" \
"v_mfma_f32_16x16x32_bf16 a[172:175], a[116:119], a[92:95], a[172:175]\n" \
"buffer_load_dwordx4 v16, s[28:31], s46 offen lds\n" \
"v_mfma_f32_16x16x32_bf16 a[176:179], a[120:123], a[88:91], a[176:179]\n" \
"v_mfma_f32_16x16x32_bf16 a[180:183], a[124:127], a[88:91], a[180:183]\n" \
"v_mfma_f32_16x16x32_bf16 a[184:187], a[120:123], a[92:95], a[184:187]\n" \
"v_mfma_f32_16x16x32_bf16 a[188:191], a[124:127], a[92:95], a[188:191]\n" \
"v_mfma_f32_16x16x32_bf16 v[64:67], a[32:35], a[0:3], v[64:67]\n" \
"ds_read_b128 a[64:67], v6 offset:0\n" \
"v_mfma_f32_16x16x32_bf16 v[68:71], a[36:39], a[0:3], v[68:71]\n" \
"v_mfma_f32_16x16x32_bf16 v[72:75], a[32:35], a[4:7], v[72:75]\n" \
"s_mov_b32 s46, 0\n" \
"s_add_u32 m0, s50, 81920\n" \
"ds_read_b64_tr_b16 a[96:97], v9 offset:49152\n" \
"v_mfma_f32_16x16x32_bf16 v[76:79], a[36:39], a[4:7], v[76:79]\n" \
"buffer_load_dwordx4 v17, s[28:31], s46 offen lds\n" \
"v_mfma_f32_16x16x32_bf16 v[80:83], a[40:43], a[0:3], v[80:83]\n" \
"ds_read_b64_tr_b16 a[98:99], v10 offset:49152\n" \
"v_mfma_f32_16x16x32_bf16 v[84:87], a[44:47], a[0:3], v[84:87]\n" \
"v_mfma_f32_16x16x32_bf16 v[88:91], a[40:43], a[4:7], v[88:91]\n" \
"ds_read_b128 a[68:71], v6 offset:2048\n" \
"v_mfma_f32_16x16x32_bf16 v[92:95], a[44:47], a[4:7], v[92:95]\n" \
"v_mfma_f32_16x16x32_bf16 v[96:99], a[48:51], a[0:3], v[96:99]\n" \
"ds_read_b64_tr_b16 a[100:101], v11 offset:49152\n" \
"v_mfma_f32_16x16x32_bf16 v[100:103], a[52:55], a[0:3], v[100:103]\n" \
"v_mfma_f32_16x16x32_bf16 v[104:107], a[48:51], a[4:7], v[104:107]\n" \
"s_add_u32 m0, s50, 86016\n" \
"ds_read_b64_tr_b16 a[102:103], v12 offset:49152\n" \
"v_mfma_f32_16x16x32_bf16 v[108:111], a[52:55], a[4:7], v[108:111]\n" \
"buffer_load_dwordx4 v18, s[28:31], s46 offen lds\n" \
"v_mfma_f32_16x16x32_bf16 v[112:115], a[56:59], a[0:3], v[112:115]\n" \
"ds_read_b128 a[72:75], v6 offset:4096\n" \
"v_mfma_f32_16x16x32_bf16 v[116:119], a[60:63], a[0:3], v[116:119]\n" \
"v_mfma_f32_16x16x32_bf16 v[120:123], a[56:59], a[4:7], v[120:123]\n" \
"ds_read_b64_tr_b16 a[104:105], v9 offset:49664\n" \
"v_mfma_f32_16x16x32_bf16 v[124:127], a[60:63], a[4:7], v[124:127]\n" \
"v_mfma_f32_16x16x32_bf16 v[128:131], a[32:35], a[8:11], v[128:131]\n" \
"ds_read_b64_tr_b16 a[106:107], v10 offset:49664\n" \
"v_mfma_f32_16x16x32_bf16 v[132:135], a[36:39], a[8:11], v[132:135]\n" \
"v_mfma_f32_16x16x32_bf16 v[136:139], a[32:35], a[12:15], v[136:139]\n" \
"s_add_u32 m0, s50, 90112\n" \
"ds_read_b128 a[76:79], v6 offset:6144\n" \
"v_mfma_f32_16x16x32_bf16 v[140:143], a[36:39], a[12:15], v[140:143]\n" \
"buffer_load_dwordx4 v19, s[28:31], s46 offen lds\n" \
"v_mfma_f32_16x16x32_bf16 v[144:147], a[40:43], a[8:11], v[144:147]\n" \
"ds_read_b64_tr_b16 a[108:109], v11 offset:49664\n" \
"v_mfma_f32_16x16x32_bf16 v[148:151], a[44:47], a[8:11], v[148:151]\n" \
"v_mfma_f32_16x16x32_bf16 v[152:155], a[40:43], a[12:15], v[152:155]\n" \
"ds_read_b64_tr_b16 a[110:111], v12 offset:49664\n" \
"v_mfma_f32_16x16x32_bf16 v[156:159], a[44:47], a[12:15], v[156:159]\n" \
"v_mfma_f32_16x16x32_bf16 v[160:163], a[48:51], a[8:11], v[160:163]\n" \
"s_add_u32 s76, s76, 2097152\ns_addc_u32 s77, s77, 0\n" \
"ds_read_b128 a[80:83], v6 offset:8192\n" \
"v_mfma_f32_16x16x32_bf16 v[164:167], a[52:55], a[8:11], v[164:167]\n" \
"v_mfma_f32_16x16x32_bf16 v[168:171], a[48:51], a[12:15], v[168:171]\n" \
"s_add_u32 m0, s50, 94208\n" \
"ds_read_b64_tr_b16 a[112:113], v9 offset:50176\n" \
"v_mfma_f32_16x16x32_bf16 v[172:175], a[52:55], a[12:15], v[172:175]\n" \
"buffer_load_dwordx4 v20, s[28:31], s46 offen lds\n" \
"v_mfma_f32_16x16x32_bf16 v[176:179], a[56:59], a[8:11], v[176:179]\n" \
"ds_read_b64_tr_b16 a[114:115], v10 offset:50176\n" \
"v_mfma_f32_16x16x32_bf16 v[180:183], a[60:63], a[8:11], v[180:183]\n" \
"v_mfma_f32_16x16x32_bf16 v[184:187], a[56:59], a[12:15], v[184:187]\n" \
"ds_read_b128 a[84:87], v6 offset:10240\n" \
"v_mfma_f32_16x16x32_bf16 v[188:191], a[60:63], a[12:15], v[188:191]\n" \
"v_mfma_f32_16x16x32_bf16 v[192:195], a[32:35], a[16:19], v[192:195]\n" \
"ds_read_b64_tr_b16 a[116:117], v11 offset:50176\n" \
"v_mfma_f32_16x16x32_bf16 v[196:199], a[36:39], a[16:19], v[196:199]\n" \
"v_mfma_f32_16x16x32_bf16 v[200:203], a[32:35], a[20:23], v[200:203]\n" \
"s_mov_b32 s47, 0\n" \
"s_add_u32 m0, s51, 32768\n" \
"ds_read_b64_tr_b16 a[118:119], v12 offset:50176\n" \
"v_mfma_f32_16x16x32_bf16 v[204:207], a[36:39], a[20:23], v[204:207]\n" \
"buffer_load_dwordx4 v21, s[32:35], s47 offen offset:0 lds\n" \
"v_mfma_f32_16x16x32_bf16 v[208:211], a[40:43], a[16:19], v[208:211]\n" \
"ds_read_b128 a[88:91], v6 offset:12288\n" \
"v_mfma_f32_16x16x32_bf16 v[212:215], a[44:47], a[16:19], v[212:215]\n" \
"LH3_%=:\ns_cbranch_execz LH11_%=\n" \
"v_mfma_f32_16x16x32_bf16 v[216:219], a[40:43], a[20:23], v[216:219]\n" \
"ds_read_b64_tr_b16 a[120:121], v9 offset:50688\n" \
"v_mfma_f32_16x16x32_bf16 v[220:223], a[44:47], a[20:23], v[220:223]\n" \
"s_mov_b64 exec, 0xffff\nglobal_load_dword v22, v1, s[76:77]\ns_mov_b64 exec, -1\n" \
"v_mfma_f32_16x16x32_bf16 v[224:227], a[48:51], a[16:19], v[224:227]\n" \
"ds_read_b64_tr_b16 a[122:123], v10 offset:50688\n" \
"v_mfma_f32_16x16x32_bf16 v[228:231], a[52:55], a[16:19], v[228:231]\n" \
"v_mfma_f32_16x16x32_bf16 v[232:235], a[48:51], a[20:23], v[232:235]\n" \
"s_add_u32 m0, s51, 33664\n" \
"ds_read_b128 a[92:95], v6 offset:14336\n" \
"v_mfma_f32_16x16x32_bf16 v[236:239], a[52:55], a[20:23], v[236:239]\n" \
"buffer_load_dwordx4 v21, s[32:35], s47 offen offset:128 lds\n" \
"v_mfma_f32_16x16x32_bf16 v[240:243], a[56:59], a[16:19], v[240:243]\n" \
"ds_read_b64_tr_b16 a[124:125], v11 offset:50688\n" \
"v_mfma_f32_16x16x32_bf16 v[244:247], a[60:63], a[16:19], v[244:247]\n" \
"v_mfma_f32_16x16x32_bf16 v[248:251], a[56:59], a[20:23], v[248:251]\n" \
"ds_read_b64_tr_b16 a[126:127], v12 offset:50688\n" \
"v_mfma_f32_16x16x32_bf16 v[252:255], a[60:63], a[20:23], v[252:255]\n" \
"v_add_u32 v5, s54, v5\n" \
"v_mfma_f32_16x16x32_bf16 a[128:131], a[32:35], a[24:27], a[128:131]\n" \
"v_add_u32 v6, s54, v6\n" \
"v_mfma_f32_16x16x32_bf16 a[132:135], a[36:39], a[24:27], a[132:135]\n" \
"v_add_u32 v7, s54, v7\n" \
"v_mfma_f32_16x16x32_bf16 a[136:139], a[32:35], a[28:31], a[136:139]\n" \
"s_mov_b32 s47, 262144\n" \
"s_add_u32 m0, s51, 40960\n" \
"v_mfma_f32_16x16x32_bf16 a[140:143], a[36:39], a[28:31], a[140:143]\n" \
"buffer_load_dwordx4 v21, s[32:35], s47 offen offset:0 lds\n" \
"v_mfma_f32_16x16x32_bf16 a[144:147], a[40:43], a[24:27], a[144:147]\n" \
"v_add_u32 v8, s54, v8\n" \
"v_mfma_f32_16x16x32_bf16 a[148:151], a[44:47], a[24:27], a[148:151]\n" \
"v_sub_u32 v9, v9, v62\n" \
"v_mfma_f32_16x16x32_bf16 a[152:155], a[40:43], a[28:31], a[152:155]\n" \
"v_sub_u32 v10, v10, v62\n" \
"v_mfma_f32_16x16x32_bf16 a[156:159], a[44:47], a[28:31], a[156:159]\n" \
"s_waitcnt lgkmcnt(0)\n" \
"v_mfma_f32_16x16x32_bf16 a[160:163], a[48:51], a[24:27], a[160:163]\n" \
"s_barrier\n" \
"v_mfma_f32_16x16x32_bf16 a[164:167], a[52:55], a[24:27], a[164:167]\n" \
"v_sub_u32 v11, v11, v62\n" \
"v_mfma_f32_16x16x32_bf16 a[168:171], a[48:51], a[28:31], a[168:171]\n" \
"s_add_u32 m0, s51, 41856\n" \
"v_mfma_f32_16x16x32_bf16 a[172:175], a[52:55], a[28:31], a[172:175]\n" \
"buffer_load_dwordx4 v21, s[32:35], s47 offen offset:128 lds\n" \
"v_mfma_f32_16x16x32_bf16 a[176:179], a[56:59], a[24:27], a[176:179]\n" \
"v_sub_u32 v12, v12, v62\n" \
"v_mfma_f32_16x16x32_bf16 a[180:183], a[60:63], a[24:27], a[180:183]\n" \
"v_mfma_f32_16x16x32_bf16 a[184:187], a[56:59], a[28:31], a[184:187]\n" \
"v_mfma_f32_16x16x32_bf16 a[188:191], a[60:63], a[28:31], a[188:191]\n" \
"v_mfma_f32_16x16x32_bf16 v[64:67], a[96:99], a[64:67], v[64:67]\n" \
"s_waitcnt vmcnt(13) lgkmcnt(0)\n" \
"v_mfma_f32_16x16x32_bf16 v[68:71], a[100:103], a[64:67], v[68:71]\n" \
"v_mfma_f32_16x16x32_bf16 v[72:75], a[96:99], a[68:71], v[72:75]\n" \
"s_mov_b32 s46, 128\n" \
"s_add_u32 m0, s50, 0\n" \
"ds_read_b128 a[32:35], v50 offset:512\n" \
"v_mfma_f32_16x16x32_bf16 v[76:79], a[100:103], a[68:71], v[76:79]\n" \
"buffer_load_dwordx4 v13, s[28:31], s46 offen lds\n" \
"v_mfma_f32_16x16x32_bf16 v[80:83], a[104:107], a[64:67], v[80:83]\n" \
"s_mov_b32 s96, s57\n" \
"ds_read2_b32 a[0:1], v46 offset0:0 offset1:1\n" \
"v_mul_f32 v64, s20, v64\n" \
"v_mfma_f32_16x16x32_bf16 v[84:87], a[108:111], a[64:67], v[84:87]\n" \
"v_mul_f32 v65, s20, v65\n" \
"v_mul_f32 v66, s20, v66\n" \
"v_mfma_f32_16x16x32_bf16 v[88:91], a[104:107], a[68:71], v[88:91]\n" \
"ds_read2_b32 a[2:3], v46 offset0:2 offset1:3\n" \
"v_mul_f32 v67, s20, v67\n" \
"v_mfma_f32_16x16x32_bf16 v[92:95], a[108:111], a[68:71], v[92:95]\n" \
"v_cvt_pk_bf16_f32 v30, v64, v65\n" \
"v_cvt_pk_bf16_f32 v31, v66, v67\n" \
"v_mfma_f32_16x16x32_bf16 v[96:99], a[112:115], a[64:67], v[96:99]\n" \
"ds_read_b128 a[36:39], v50 offset:1024\n" \
"v_mul_f32 v68, s20, v68\n" \
"v_mfma_f32_16x16x32_bf16 v[100:103], a[116:119], a[64:67], v[100:103]\n" \
"v_mul_f32 v69, s20, v69\n" \
"v_mul_f32 v70, s20, v70\n" \
"v_mfma_f32_16x16x32_bf16 v[104:107], a[112:115], a[68:71], v[104:107]\n" \
"s_add_u32 m0, s50, 4096\n" \
"ds_read2_b32 a[4:5], v46 offset0:128 offset1:129\n" \
"v_mul_f32 v71, s20, v71\n" \
"v_mfma_f32_16x16x32_bf16 v[108:111], a[116:119], a[68:71], v[108:111]\n" \
"buffer_load_dwordx4 v14, s[28:31], s46 offen lds\n" \
"v_mfma_f32_16x16x32_bf16 v[112:115], a[120:123], a[64:67], v[112:115]\n" \
"ds_read2_b32 a[6:7], v46 offset0:130 offset1:131\n" \
"v_cvt_pk_bf16_f32 v32, v68, v69\n" \
"v_mfma_f32_16x16x32_bf16 v[116:119], a[124:127], a[64:67], v[116:119]\n" \
"v_cvt_pk_bf16_f32 v33, v70, v71\n" \
"v_mul_f32 v72, s20, v72\n" \
"v_mfma_f32_16x16x32_bf16 v[120:123], a[120:123], a[68:71], v[120:123]\n" \
"ds_read_b128 a[40:43], v50 offset:1536\n" \
"v_mul_f32 v73, s20, v73\n" \
"v_mfma_f32_16x16x32_bf16 v[124:127], a[124:127], a[68:71], v[124:127]\n" \
"buffer_store_dwordx4 v[30:33], v4, s[36:39], s96 offen offset:0 sc1\n" \
"v_mfma_f32_16x16x32_bf16 v[128:131], a[96:99], a[72:75], v[128:131]\n" \
"s_add_u32 s97, s96, s98\n" \
"ds_read2_b32 a[8:9], v47 offset0:0 offset1:1\n" \
"v_mul_f32 v74, s20, v74\n" \
"v_mfma_f32_16x16x32_bf16 v[132:135], a[100:103], a[72:75], v[132:135]\n" \
"v_mul_f32 v75, s20, v75\n" \
"v_cvt_pk_bf16_f32 v34, v72, v73\n" \
"v_mfma_f32_16x16x32_bf16 v[136:139], a[96:99], a[76:79], v[136:139]\n" \
"s_add_u32 m0, s50, 8192\n" \
"ds_read2_b32 a[10:11], v47 offset0:2 offset1:3\n" \
"v_cvt_pk_bf16_f32 v35, v74, v75\n" \
"v_mfma_f32_16x16x32_bf16 v[140:143], a[100:103], a[76:79], v[140:143]\n" \
"buffer_load_dwordx4 v15, s[28:31], s46 offen lds\n" \
"v_mfma_f32_16x16x32_bf16 v[144:147], a[104:107], a[72:75], v[144:147]\n" \
"ds_read_b128 a[44:47], v50 offset:2048\n" \
"v_mul_f32 v76, s20, v76\n" \
"v_mfma_f32_16x16x32_bf16 v[148:151], a[108:111], a[72:75], v[148:151]\n" \
"v_mul_f32 v77, s20, v77\n" \
"v_mul_f32 v78, s20, v78\n" \
"v_mfma_f32_16x16x32_bf16 v[152:155], a[104:107], a[76:79], v[152:155]\n" \
"ds_read2_b32 a[12:13], v47 offset0:128 offset1:129\n" \
"v_mul_f32 v79, s20, v79\n" \
"v_mfma_f32_16x16x32_bf16 v[156:159], a[108:111], a[76:79], v[156:159]\n" \
"v_cvt_pk_bf16_f32 v36, v76, v77\n" \
"v_cvt_pk_bf16_f32 v37, v78, v79\n" \
"v_mfma_f32_16x16x32_bf16 v[160:163], a[112:115], a[72:75], v[160:163]\n" \
"ds_read2_b32 a[14:15], v47 offset0:130 offset1:131\n" \
"v_mul_f32 v80, s20, v80\n" \
"v_mfma_f32_16x16x32_bf16 v[164:167], a[116:119], a[72:75], v[164:167]\n" \
"v_mul_f32 v81, s20, v81\n" \
"v_mul_f32 v82, s20, v82\n" \
"v_mfma_f32_16x16x32_bf16 v[168:171], a[112:115], a[76:79], v[168:171]\n" \
"s_add_u32 m0, s50, 12288\n" \
"ds_read_b128 a[48:51], v50 offset:2560\n" \
"v_mul_f32 v83, s20, v83\n" \
"v_mfma_f32_16x16x32_bf16 v[172:175], a[116:119], a[76:79], v[172:175]\n" \
"buffer_load_dwordx4 v16, s[28:31], s46 offen lds\n" \
"v_mfma_f32_16x16x32_bf16 v[176:179], a[120:123], a[72:75], v[176:179]\n" \
"ds_read2_b32 a[16:17], v48 offset0:0 offset1:1\n" \
"v_cvt_pk_bf16_f32 v38, v80, v81\n" \
"v_mfma_f32_16x16x32_bf16 v[180:183], a[124:127], a[72:75], v[180:183]\n" \
"buffer_store_dwordx4 v[34:37], v4, s[36:39], s97 offen offset:0 sc1\n" \
"v_mfma_f32_16x16x32_bf16 v[184:187], a[120:123], a[76:79], v[184:187]\n" \
"s_add_u32 s96, s96, s92\n" \
"ds_read2_b32 a[18:19], v48 offset0:2 offset1:3\n" \
"v_cvt_pk_bf16_f32 v39, v82, v83\n" \
"v_mfma_f32_16x16x32_bf16 v[188:191], a[124:127], a[76:79], v[188:191]\n" \
"v_mul_f32 v84, s20, v84\n" \
"v_mul_f32 v85, s20, v85\n" \
"v_mfma_f32_16x16x32_bf16 v[192:195], a[96:99], a[80:83], v[192:195]\n" \
"ds_read_b128 a[52:55], v50 offset:3072\n" \
"v_mul_f32 v86, s20, v86\n" \
"v_mfma_f32_16x16x32_bf16 v[196:199], a[100:103], a[80:83], v[196:199]\n" \
"v_mul_f32 v87, s20, v87\n" \
"v_cvt_pk_bf16_f32 v40, v84, v85\n" \
"v_mfma_f32_16x16x32_bf16 v[200:203], a[96:99], a[84:87], v[200:203]\n" \
"s_mov_b32 s46, 128\n" \
"s_add_u32 m0, s50, 16384\n" \
"ds_read2_b32 a[20:21], v48 offset0:128 offset1:129\n" \
"v_mfma_f32_16x16x32_bf16 v[204:207], a[100:103], a[84:87], v[204:207]\n" \
"buffer_load_dwordx4 v17, s[28:31], s46 offen lds\n" \
"v_mfma_f32_16x16x32_bf16 v[208:211], a[104:107], a[80:83], v[208:211]\n" \
"ds_read2_b32 a[22:23], v48 offset0:130 offset1:131\n" \
"v_cvt_pk_bf16_f32 v41, v86, v87\n" \
"v_mfma_f32_16x16x32_bf16 v[212:215], a[108:111], a[80:83], v[212:215]\n" \
"v_mul_f32 v88, s20, v88\n" \
"v_mul_f32 v89, s20, v89\n" \
"v_mfma_f32_16x16x32_bf16 v[216:219], a[104:107], a[84:87], v[216:219]\n" \
"ds_read_b128 a[56:59], v50 offset:3584\n" \
"v_mul_f32 v90, s20, v90\n" \
"v_mfma_f32_16x16x32_bf16 v[220:223], a[108:111], a[84:87], v[220:223]\n" \
"v_mul_f32 v91, s20, v91\n" \
"v_cvt_pk_bf16_f32 v42, v88, v89\n" \
"v_mfma_f32_16x16x32_bf16 v[224:227], a[112:115], a[80:83], v[224:227]\n" \
"ds_read2_b32 a[24:25], v49 offset0:0 offset1:1\n" \
"v_cvt_pk_bf16_f32 v43, v90, v91\n" \
"v_mfma_f32_16x16x32_bf16 v[228:231], a[116:119], a[80:83], v[228:231]\n" \
"v_mul_f32 v92, s20, v92\n" \
"v_mul_f32 v93, s20, v93\n" \
"v_mfma_f32_16x16x32_bf16 v[232:235], a[112:115], a[84:87], v[232:235]\n" \
"s_add_u32 m0, s50, 20480\n" \
"ds_read2_b32 a[26:27], v49 offset0:2 offset1:3\n" \
"v_mul_f32 v94, s20, v94\n" \
"v_mfma_f32_16x16x32_bf16 v[236:239], a[116:119], a[84:87], v[236:239]\n" \
"buffer_load_dwordx4 v18, s[28:31], s46 offen lds\n" \
"v_mfma_f32_16x16x32_bf16 v[240:243], a[120:123], a[80:83], v[240:243]\n" \
"ds_read_b128 a[60:63], v50 offset:4096\n" \
"v_mul_f32 v95, s20, v95\n" \
"v_mfma_f32_16x16x32_bf16 v[244:247], a[124:127], a[80:83], v[244:247]\n" \
"buffer_store_dwordx4 v[38:41], v4, s[36:39], s96 offen offset:0 sc1\n" \
"v_mfma_f32_16x16x32_bf16 v[248:251], a[120:123], a[84:87], v[248:251]\n" \
"s_add_u32 s97, s96, s98\n" \
"ds_read2_b32 a[28:29], v49 offset0:128 offset1:129\n" \
"v_cvt_pk_bf16_f32 v44, v92, v93\n" \
"v_mfma_f32_16x16x32_bf16 v[252:255], a[124:127], a[84:87], v[252:255]\n" \
"v_cvt_pk_bf16_f32 v45, v94, v95\n" \
"v_mul_f32 v96, s20, v96\n" \
"v_mfma_f32_16x16x32_bf16 a[128:131], a[96:99], a[88:91], a[128:131]\n" \
"ds_read2_b32 a[30:31], v49 offset0:130 offset1:131\n" \
"v_mul_f32 v97, s20, v97\n" \
"v_mfma_f32_16x16x32_bf16 a[132:135], a[100:103], a[88:91], a[132:135]\n" \
"v_mul_f32 v98, s20, v98\n" \
"v_mul_f32 v99, s20, v99\n" \
"v_mfma_f32_16x16x32_bf16 a[136:139], a[96:99], a[92:95], a[136:139]\n" \
"s_add_u32 m0, s50, 24576\n" \
"v_cvt_pk_bf16_f32 v30, v96, v97\n" \
"v_cvt_pk_bf16_f32 v31, v98, v99\n" \
"v_mfma_f32_16x16x32_bf16 a[140:143], a[100:103], a[92:95], a[140:143]\n" \
"buffer_load_dwordx4 v19, s[28:31], s46 offen lds\n" \
"v_mfma_f32_16x16x32_bf16 a[144:147], a[104:107], a[88:91], a[144:147]\n" \
"v_mul_f32 v100, s20, v100\n" \
"v_mul_f32 v101, s20, v101\n" \
"v_mfma_f32_16x16x32_bf16 a[148:151], a[108:111], a[88:91], a[148:151]\n" \
"v_mul_f32 v102, s20, v102\n" \
"v_mul_f32 v103, s20, v103\n" \
"v_mfma_f32_16x16x32_bf16 a[152:155], a[104:107], a[92:95], a[152:155]\n" \
"v_cvt_pk_bf16_f32 v32, v100, v101\n" \
"v_mfma_f32_16x16x32_bf16 a[156:159], a[108:111], a[92:95], a[156:159]\n" \
"v_cvt_pk_bf16_f32 v33, v102, v103\n" \
"v_mul_f32 v104, s20, v104\n" \
"v_mfma_f32_16x16x32_bf16 a[160:163], a[112:115], a[88:91], a[160:163]\n" \
"v_mul_f32 v105, s20, v105\n" \
"v_mul_f32 v106, s20, v106\n" \
"v_mfma_f32_16x16x32_bf16 a[164:167], a[116:119], a[88:91], a[164:167]\n" \
"v_mul_f32 v107, s20, v107\n" \
"v_mfma_f32_16x16x32_bf16 a[168:171], a[112:115], a[92:95], a[168:171]\n" \
"s_add_u32 m0, s50, 28672\n" \
"v_cvt_pk_bf16_f32 v34, v104, v105\n" \
"v_cvt_pk_bf16_f32 v35, v106, v107\n" \
"v_mfma_f32_16x16x32_bf16 a[172:175], a[116:119], a[92:95], a[172:175]\n" \
"buffer_load_dwordx4 v20, s[28:31], s46 offen lds\n" \
"v_mfma_f32_16x16x32_bf16 a[176:179], a[120:123], a[88:91], a[176:179]\n" \
"v_mul_f32 v108, s20, v108\n" \
"v_mul_f32 v109, s20, v109\n" \
"v_mfma_f32_16x16x32_bf16 a[180:183], a[124:127], a[88:91], a[180:183]\n" \
"buffer_store_dwordx4 v[42:45], v4, s[36:39], s97 offen offset:0 sc1\n" \
"v_mfma_f32_16x16x32_bf16 a[184:187], a[120:123], a[92:95], a[184:187]\n" \
"s_waitcnt lgkmcnt(0)\n" \
"s_add_u32 s96, s96, s92\n" \
"v_mul_f32 v110, s20, v110\n" \
"v_mfma_f32_16x16x32_bf16 a[188:191], a[124:127], a[92:95], a[188:191]\n" \
"v_mul_f32 v111, s20, v111\n" \
"v_cvt_pk_bf16_f32 v36, v108, v109\n" \
"s_branch LC_%=\n" \
"LAV_%=:\n" \
"v_mfma_f32_16x16x32_bf16 v[64:67], a[0:3], a[32:35], 0\n" \
"s_add_u32 s14, s14, 1\ns_addc_u32 s15, s15, 0\n" \
"ds_read_b128 a[64:67], v8 offset:0\n" \
"v_mfma_f32_16x16x32_bf16 v[68:71], a[4:7], a[32:35], 0\n" \
"v_mfma_f32_16x16x32_bf16 v[72:75], a[0:3], a[36:39], 0\n" \
"s_mov_b32 s47, 786432\n" \
"s_add_u32 m0, s22, 49152\n" \
"ds_read_b64_tr_b16 a[96:97], v9 offset:16384\n" \
"v_mfma_f32_16x16x32_bf16 v[76:79], a[4:7], a[36:39], 0\n" \
"buffer_load_dwordx4 v21, s[32:35], s47 offen offset:0 lds\n" \
"v_mfma_f32_16x16x32_bf16 v[80:83], a[8:11], a[32:35], 0\n" \
"ds_read_b64_tr_b16 a[98:99], v10 offset:16384\n" \
"v_mfma_f32_16x16x32_bf16 v[84:87], a[12:15], a[32:35], 0\n" \
"v_mfma_f32_16x16x32_bf16 v[88:91], a[8:11], a[36:39], 0\n" \
"s_add_u32 m0, s22, 50048\n" \
"ds_read_b128 a[68:71], v8 offset:2048\n" \
"v_mfma_f32_16x16x32_bf16 v[92:95], a[12:15], a[36:39], 0\n" \
"buffer_load_dwordx4 v21, s[32:35], s47 offen offset:128 lds\n" \
"v_mfma_f32_16x16x32_bf16 v[96:99], a[16:19], a[32:35], 0\n" \
"ds_read_b64_tr_b16 a[100:101], v11 offset:16384\n" \
"v_mfma_f32_16x16x32_bf16 v[100:103], a[20:23], a[32:35], 0\n" \
"v_mfma_f32_16x16x32_bf16 v[104:107], a[16:19], a[36:39], 0\n" \
"s_add_u32 m0, s22, 50944\n" \
"ds_read_b64_tr_b16 a[102:103], v12 offset:16384\n" \
"v_mfma_f32_16x16x32_bf16 v[108:111], a[20:23], a[36:39], 0\n" \
"buffer_load_dwordx4 v21, s[32:35], s47 offen offset:256 lds\n" \
"v_mfma_f32_16x16x32_bf16 v[112:115], a[24:27], a[32:35], 0\n" \
"ds_read_b128 a[72:75], v8 offset:4096\n" \
"v_mfma_f32_16x16x32_bf16 v[116:119], a[28:31], a[32:35], 0\n" \
"v_mfma_f32_16x16x32_bf16 v[120:123], a[24:27], a[36:39], 0\n" \
"s_add_u32 m0, s22, 51840\n" \
"ds_read_b64_tr_b16 a[104:105], v9 offset:16896\n" \
"v_mfma_f32_16x16x32_bf16 v[124:127], a[28:31], a[36:39], 0\n" \
"buffer_load_dwordx4 v21, s[32:35], s47 offen offset:384 lds\n" \
"v_mfma_f32_16x16x32_bf16 v[128:131], a[0:3], a[40:43], 0\n" \
"ds_read_b64_tr_b16 a[106:107], v10 offset:16896\n" \
"v_mfma_f32_16x16x32_bf16 v[132:135], a[4:7], a[40:43], 0\n" \
"v_mfma_f32_16x16x32_bf16 v[136:139], a[0:3], a[44:47], 0\n" \
"s_mov_b32 s46, 256\n" \
"s_add_u32 m0, s50, 32768\n" \
"ds_read_b128 a[76:79], v8 offset:6144\n" \
"v_mfma_f32_16x16x32_bf16 v[140:143], a[4:7], a[44:47], 0\n" \
"buffer_load_dwordx4 v13, s[28:31], s46 offen lds\n" \
"v_mfma_f32_16x16x32_bf16 v[144:147], a[8:11], a[40:43], 0\n" \
"ds_read_b64_tr_b16 a[108:109], v11 offset:16896\n" \
"v_mfma_f32_16x16x32_bf16 v[148:151], a[12:15], a[40:43], 0\n" \
"v_mfma_f32_16x16x32_bf16 v[152:155], a[8:11], a[44:47], 0\n" \
"s_add_u32 m0, s50, 36864\n" \
"ds_read_b64_tr_b16 a[110:111], v12 offset:16896\n" \
"v_mfma_f32_16x16x32_bf16 v[156:159], a[12:15], a[44:47], 0\n" \
"buffer_load_dwordx4 v14, s[28:31], s46 offen lds\n" \
"v_mfma_f32_16x16x32_bf16 v[160:163], a[16:19], a[40:43], 0\n" \
"ds_read_b128 a[80:83], v8 offset:8192\n" \
"v_mfma_f32_16x16x32_bf16 v[164:167], a[20:23], a[40:43], 0\n" \
"v_mfma_f32_16x16x32_bf16 v[168:171], a[16:19], a[44:47], 0\n" \
"s_add_u32 m0, s50, 40960\n" \
"ds_read_b64_tr_b16 a[112:113], v9 offset:17408\n" \
"v_mfma_f32_16x16x32_bf16 v[172:175], a[20:23], a[44:47], 0\n" \
"buffer_load_dwordx4 v15, s[28:31], s46 offen lds\n" \
"v_mfma_f32_16x16x32_bf16 v[176:179], a[24:27], a[40:43], 0\n" \
"ds_read_b64_tr_b16 a[114:115], v10 offset:17408\n" \
"v_mfma_f32_16x16x32_bf16 v[180:183], a[28:31], a[40:43], 0\n" \
"v_mfma_f32_16x16x32_bf16 v[184:187], a[24:27], a[44:47], 0\n" \
"s_add_u32 m0, s50, 45056\n" \
"ds_read_b128 a[84:87], v8 offset:10240\n" \
"v_mfma_f32_16x16x32_bf16 v[188:191], a[28:31], a[44:47], 0\n" \
"buffer_load_dwordx4 v16, s[28:31], s46 offen lds\n" \
"v_mfma_f32_16x16x32_bf16 v[192:195], a[0:3], a[48:51], 0\n" \
"ds_read_b64_tr_b16 a[116:117], v11 offset:17408\n" \
"v_mfma_f32_16x16x32_bf16 v[196:199], a[4:7], a[48:51], 0\n" \
"v_mfma_f32_16x16x32_bf16 v[200:203], a[0:3], a[52:55], 0\n" \
"s_mov_b32 s46, 256\n" \
"s_add_u32 m0, s50, 49152\n" \
"ds_read_b64_tr_b16 a[118:119], v12 offset:17408\n" \
"v_mfma_f32_16x16x32_bf16 v[204:207], a[4:7], a[52:55], 0\n" \
"buffer_load_dwordx4 v17, s[28:31], s46 offen lds\n" \
"v_mfma_f32_16x16x32_bf16 v[208:211], a[8:11], a[48:51], 0\n" \
"ds_read_b128 a[88:91], v8 offset:12288\n" \
"v_mfma_f32_16x16x32_bf16 v[212:215], a[12:15], a[48:51], 0\n" \
"v_mfma_f32_16x16x32_bf16 v[216:219], a[8:11], a[52:55], 0\n" \
"s_add_u32 m0, s50, 53248\n" \
"ds_read_b64_tr_b16 a[120:121], v9 offset:17920\n" \
"v_mfma_f32_16x16x32_bf16 v[220:223], a[12:15], a[52:55], 0\n" \
"buffer_load_dwordx4 v18, s[28:31], s46 offen lds\n" \
"v_mfma_f32_16x16x32_bf16 v[224:227], a[16:19], a[48:51], 0\n" \
"ds_read_b64_tr_b16 a[122:123], v10 offset:17920\n" \
"v_mfma_f32_16x16x32_bf16 v[228:231], a[20:23], a[48:51], 0\n" \
"v_mfma_f32_16x16x32_bf16 v[232:235], a[16:19], a[52:55], 0\n" \
"s_add_u32 m0, s50, 57344\n" \
"ds_read_b128 a[92:95], v8 offset:14336\n" \
"v_mfma_f32_16x16x32_bf16 v[236:239], a[20:23], a[52:55], 0\n" \
"buffer_load_dwordx4 v19, s[28:31], s46 offen lds\n" \
"v_mfma_f32_16x16x32_bf16 v[240:243], a[24:27], a[48:51], 0\n" \
"ds_read_b64_tr_b16 a[124:125], v11 offset:17920\n" \
"v_mfma_f32_16x16x32_bf16 v[244:247], a[28:31], a[48:51], 0\n" \
"v_mfma_f32_16x16x32_bf16 v[248:251], a[24:27], a[52:55], 0\n" \
"s_add_u32 m0, s50, 61440\n" \
"ds_read_b64_tr_b16 a[126:127], v12 offset:17920\n" \
"v_mfma_f32_16x16x32_bf16 v[252:255], a[28:31], a[52:55], 0\n" \
"buffer_load_dwordx4 v20, s[28:31], s46 offen lds\n" \
"v_mfma_f32_16x16x32_bf16 a[128:131], a[0:3], a[56:59], 0\n" \
"v_mfma_f32_16x16x32_bf16 a[132:135], a[4:7], a[56:59], 0\n" \
"v_mfma_f32_16x16x32_bf16 a[136:139], a[0:3], a[60:63], 0\n" \
"v_mfma_f32_16x16x32_bf16 a[140:143], a[4:7], a[60:63], 0\n" \
"v_mfma_f32_16x16x32_bf16 a[144:147], a[8:11], a[56:59], 0\n" \
"global_load_dwordx4 v[46:49], v4, s[86:87] offset:0\n" \
"v_mfma_f32_16x16x32_bf16 a[148:151], a[12:15], a[56:59], 0\n" \
"v_mfma_f32_16x16x32_bf16 a[152:155], a[8:11], a[60:63], 0\n" \
"v_mfma_f32_16x16x32_bf16 a[156:159], a[12:15], a[60:63], 0\n" \
"s_waitcnt vmcnt(13) lgkmcnt(0)\n" \
"v_mfma_f32_16x16x32_bf16 a[160:163], a[16:19], a[56:59], 0\n" \
"s_barrier\n" \
"v_mfma_f32_16x16x32_bf16 a[164:167], a[20:23], a[56:59], 0\n" \
"global_load_dwordx4 v[50:53], v4, s[86:87] offset:1024\n" \
"v_mfma_f32_16x16x32_bf16 a[168:171], a[16:19], a[60:63], 0\n" \
"v_mfma_f32_16x16x32_bf16 a[172:175], a[20:23], a[60:63], 0\n" \
"v_mfma_f32_16x16x32_bf16 a[176:179], a[24:27], a[56:59], 0\n" \
"v_mfma_f32_16x16x32_bf16 a[180:183], a[28:31], a[56:59], 0\n" \
"global_load_dwordx4 v[30:33], v26, s[64:65] offset:0\n" \
"v_mfma_f32_16x16x32_bf16 a[184:187], a[24:27], a[60:63], 0\n" \
"v_mfma_f32_16x16x32_bf16 a[188:191], a[28:31], a[60:63], 0\n" \
"v_mfma_f32_16x16x32_bf16 v[64:67], a[64:67], a[96:99], v[64:67]\n" \
"ds_read_b128 a[0:3], v5 offset:0\n" \
"v_mfma_f32_16x16x32_bf16 v[68:71], a[68:71], a[96:99], v[68:71]\n" \
"v_mfma_f32_16x16x32_bf16 v[72:75], a[64:67], a[100:103], v[72:75]\n" \
"s_mov_b32 s47, 1048576\n" \
"s_add_u32 m0, s22, 0\n" \
"ds_read_b64_tr_b16 a[32:33], v9 offset:32768\n" \
"v_mfma_f32_16x16x32_bf16 v[76:79], a[68:71], a[100:103], v[76:79]\n" \
"buffer_load_dwordx4 v21, s[32:35], s47 offen offset:0 lds\n" \
"v_mfma_f32_16x16x32_bf16 v[80:83], a[72:75], a[96:99], v[80:83]\n" \
"ds_read_b64_tr_b16 a[34:35], v10 offset:32768\n" \
"v_mfma_f32_16x16x32_bf16 v[84:87], a[76:79], a[96:99], v[84:87]\n" \
"v_mfma_f32_16x16x32_bf16 v[88:91], a[72:75], a[100:103], v[88:91]\n" \
"s_add_u32 m0, s22, 896\n" \
"ds_read_b128 a[4:7], v5 offset:2048\n" \
"v_mfma_f32_16x16x32_bf16 v[92:95], a[76:79], a[100:103], v[92:95]\n" \
"buffer_load_dwordx4 v21, s[32:35], s47 offen offset:128 lds\n" \
"v_mfma_f32_16x16x32_bf16 v[96:99], a[80:83], a[96:99], v[96:99]\n" \
"ds_read_b64_tr_b16 a[36:37], v11 offset:32768\n" \
"v_mfma_f32_16x16x32_bf16 v[100:103], a[84:87], a[96:99], v[100:103]\n" \
"v_mfma_f32_16x16x32_bf16 v[104:107], a[80:83], a[100:103], v[104:107]\n" \
"s_add_u32 m0, s22, 1792\n" \
"ds_read_b64_tr_b16 a[38:39], v12 offset:32768\n" \
"v_mfma_f32_16x16x32_bf16 v[108:111], a[84:87], a[100:103], v[108:111]\n" \
"buffer_load_dwordx4 v21, s[32:35], s47 offen offset:256 lds\n" \
"v_mfma_f32_16x16x32_bf16 v[112:115], a[88:91], a[96:99], v[112:115]\n" \
"ds_read_b128 a[8:11], v5 offset:4096\n" \
"v_mfma_f32_16x16x32_bf16 v[116:119], a[92:95], a[96:99], v[116:119]\n" \
"v_mfma_f32_16x16x32_bf16 v[120:123], a[88:91], a[100:103], v[120:123]\n" \
"s_add_u32 m0, s22, 2688\n" \
"ds_read_b64_tr_b16 a[40:41], v9 offset:33280\n" \
"v_mfma_f32_16x16x32_bf16 v[124:127], a[92:95], a[100:103], v[124:127]\n" \
"buffer_load_dwordx4 v21, s[32:35], s47 offen offset:384 lds\n" \
"v_mfma_f32_16x16x32_bf16 v[128:131], a[64:67], a[104:107], v[128:131]\n" \
"ds_read_b64_tr_b16 a[42:43], v10 offset:33280\n" \
"v_mfma_f32_16x16x32_bf16 v[132:135], a[68:71], a[104:107], v[132:135]\n" \
"v_mfma_f32_16x16x32_bf16 v[136:139], a[64:67], a[108:111], v[136:139]\n" \
"s_mov_b32 s47, 1310720\n" \
"s_add_u32 m0, s22, 16384\n" \
"ds_read_b128 a[12:15], v5 offset:6144\n" \
"v_mfma_f32_16x16x32_bf16 v[140:143], a[68:71], a[108:111], v[140:143]\n" \
"buffer_load_dwordx4 v21, s[32:35], s47 offen offset:0 lds\n" \
"v_mfma_f32_16x16x32_bf16 v[144:147], a[72:75], a[104:107], v[144:147]\n" \
"ds_read_b64_tr_b16 a[44:45], v11 offset:33280\n" \
"v_mfma_f32_16x16x32_bf16 v[148:151], a[76:79], a[104:107], v[148:151]\n" \
"v_mfma_f32_16x16x32_bf16 v[152:155], a[72:75], a[108:111], v[152:155]\n" \
"s_mov_b32 s46, 384\n" \
"s_add_u32 m0, s50, 65536\n" \
"ds_read_b64_tr_b16 a[46:47], v12 offset:33280\n" \
"v_mfma_f32_16x16x32_bf16 v[156:159], a[76:79], a[108:111], v[156:159]\n" \
"buffer_load_dwordx4 v13, s[28:31], s46 offen lds\n" \
"v_mfma_f32_16x16x32_bf16 v[160:163], a[80:83], a[104:107], v[160:163]\n" \
"ds_read_b128 a[16:19], v5 offset:8192\n" \
"v_mfma_f32_16x16x32_bf16 v[164:167], a[84:87], a[104:107], v[164:167]\n" \
"v_mfma_f32_16x16x32_bf16 v[168:171], a[80:83], a[108:111], v[168:171]\n" \
"s_add_u32 m0, s22, 17280\n" \
"ds_read_b64_tr_b16 a[48:49], v9 offset:33792\n" \
"v_mfma_f32_16x16x32_bf16 v[172:175], a[84:87], a[108:111], v[172:175]\n" \
"buffer_load_dwordx4 v21, s[32:35], s47 offen offset:128 lds\n" \
"v_mfma_f32_16x16x32_bf16 v[176:179], a[88:91], a[104:107], v[176:179]\n" \
"ds_read_b64_tr_b16 a[50:51], v10 offset:33792\n" \
"v_mfma_f32_16x16x32_bf16 v[180:183], a[92:95], a[104:107], v[180:183]\n" \
"v_mfma_f32_16x16x32_bf16 v[184:187], a[88:91], a[108:111], v[184:187]\n" \
"s_add_u32 m0, s50, 69632\n" \
"ds_read_b128 a[20:23], v5 offset:10240\n" \
"v_mfma_f32_16x16x32_bf16 v[188:191], a[92:95], a[108:111], v[188:191]\n" \
"buffer_load_dwordx4 v14, s[28:31], s46 offen lds\n" \
"v_mfma_f32_16x16x32_bf16 v[192:195], a[64:67], a[112:115], v[192:195]\n" \
"ds_read_b64_tr_b16 a[52:53], v11 offset:33792\n" \
"v_mfma_f32_16x16x32_bf16 v[196:199], a[68:71], a[112:115], v[196:199]\n" \
"v_mfma_f32_16x16x32_bf16 v[200:203], a[64:67], a[116:119], v[200:203]\n" \
"s_add_u32 m0, s22, 18176\n" \
"ds_read_b64_tr_b16 a[54:55], v12 offset:33792\n" \
"v_mfma_f32_16x16x32_bf16 v[204:207], a[68:71], a[116:119], v[204:207]\n" \
"buffer_load_dwordx4 v21, s[32:35], s47 offen offset:256 lds\n" \
"v_mfma_f32_16x16x32_bf16 v[208:211], a[72:75], a[112:115], v[208:211]\n" \
"ds_read_b128 a[24:27], v5 offset:12288\n" \
"v_mfma_f32_16x16x32_bf16 v[212:215], a[76:79], a[112:115], v[212:215]\n" \
"v_mfma_f32_16x16x32_bf16 v[216:219], a[72:75], a[116:119], v[216:219]\n" \
"s_add_u32 m0, s50, 73728\n" \
"ds_read_b64_tr_b16 a[56:57], v9 offset:34304\n" \
"v_mfma_f32_16x16x32_bf16 v[220:223], a[76:79], a[116:119], v[220:223]\n" \
"buffer_load_dwordx4 v15, s[28:31], s46 offen lds\n" \
"v_mfma_f32_16x16x32_bf16 v[224:227], a[80:83], a[112:115], v[224:227]\n" \
"ds_read_b64_tr_b16 a[58:59], v10 offset:34304\n" \
"v_mfma_f32_16x16x32_bf16 v[228:231], a[84:87], a[112:115], v[228:231]\n" \
"v_mfma_f32_16x16x32_bf16 v[232:235], a[80:83], a[116:119], v[232:235]\n" \
"s_add_u32 m0, s22, 19072\n" \
"ds_read_b128 a[28:31], v5 offset:14336\n" \
"v_mfma_f32_16x16x32_bf16 v[236:239], a[84:87], a[116:119], v[236:239]\n" \
"buffer_load_dwordx4 v21, s[32:35], s47 offen offset:384 lds\n" \
"v_mfma_f32_16x16x32_bf16 v[240:243], a[88:91], a[112:115], v[240:243]\n" \
"ds_read_b64_tr_b16 a[60:61], v11 offset:34304\n" \
"v_mfma_f32_16x16x32_bf16 v[244:247], a[92:95], a[112:115], v[244:247]\n" \
"v_mfma_f32_16x16x32_bf16 v[248:251], a[88:91], a[116:119], v[248:251]\n" \
"s_add_u32 m0, s50, 77824\n" \
"ds_read_b64_tr_b16 a[62:63], v12 offset:34304\n" \
"v_mfma_f32_16x16x32_bf16 v[252:255], a[92:95], a[116:119], v[252:255]\n" \
"buffer_load_dwordx4 v16, s[28:31], s46 offen lds\n" \
"v_mfma_f32_16x16x32_bf16 a[128:131], a[64:67], a[120:123], a[128:131]\n" \
"v_mfma_f32_16x16x32_bf16 a[132:135], a[68:71], a[120:123], a[132:135]\n" \
"v_mfma_f32_16x16x32_bf16 a[136:139], a[64:67], a[124:127], a[136:139]\n" \
"v_mfma_f32_16x16x32_bf16 a[140:143], a[68:71], a[124:127], a[140:143]\n" \
"global_load_dwordx4 v[34:37], v26, s[64:65] offset:1024\n" \
"v_mfma_f32_16x16x32_bf16 a[144:147], a[72:75], a[120:123], a[144:147]\n" \
"v_mfma_f32_16x16x32_bf16 a[148:151], a[76:79], a[120:123], a[148:151]\n" \
"v_mfma_f32_16x16x32_bf16 a[152:155], a[72:75], a[124:127], a[152:155]\n" \
"v_mfma_f32_16x16x32_bf16 a[156:159], a[76:79], a[124:127], a[156:159]\n" \
"s_waitcnt vmcnt(24) lgkmcnt(0)\n" \
"v_mfma_f32_16x16x32_bf16 a[160:163], a[80:83], a[120:123], a[160:163]\n" \
"s_barrier\n" \
"v_mfma_f32_16x16x32_bf16 a[164:167], a[84:87], a[120:123], a[164:167]\n" \
"global_load_dwordx4 v[38:41], v26, s[64:65] offset:2048\n" \
"v_mfma_f32_16x16x32_bf16 a[168:171], a[80:83], a[124:127], a[168:171]\n" \
"v_mfma_f32_16x16x32_bf16 a[172:175], a[84:87], a[124:127], a[172:175]\n" \
"v_mfma_f32_16x16x32_bf16 a[176:179], a[88:91], a[120:123], a[176:179]\n" \
"v_mfma_f32_16x16x32_bf16 a[180:183], a[92:95], a[120:123], a[180:183]\n" \
"global_load_dwordx4 v[42:45], v26, s[64:65] offset:3072\n" \
"v_mfma_f32_16x16x32_bf16 a[184:187], a[88:91], a[124:127], a[184:187]\n" \
"v_mfma_f32_16x16x32_bf16 a[188:191], a[92:95], a[124:127], a[188:191]\n" \
"v_mfma_f32_16x16x32_bf16 v[64:67], a[0:3], a[32:35], v[64:67]\n" \
"ds_read_b128 a[64:67], v6 offset:0\n" \
"v_mfma_f32_16x16x32_bf16 v[68:71], a[4:7], a[32:35], v[68:71]\n" \
"v_mfma_f32_16x16x32_bf16 v[72:75], a[0:3], a[36:39], v[72:75]\n" \
"s_mov_b32 s47, 1572864\n" \
"s_add_u32 m0, s22, 32768\n" \
"ds_read_b64_tr_b16 a[96:97], v9 offset:49152\n" \
"v_mfma_f32_16x16x32_bf16 v[76:79], a[4:7], a[36:39], v[76:79]\n" \
"buffer_load_dwordx4 v21, s[32:35], s47 offen offset:0 lds\n" \
"v_mfma_f32_16x16x32_bf16 v[80:83], a[8:11], a[32:35], v[80:83]\n" \
"ds_read_b64_tr_b16 a[98:99], v10 offset:49152\n" \
"v_mfma_f32_16x16x32_bf16 v[84:87], a[12:15], a[32:35], v[84:87]\n" \
"v_mfma_f32_16x16x32_bf16 v[88:91], a[8:11], a[36:39], v[88:91]\n" \
"ds_read_b128 a[68:71], v6 offset:2048\n" \
"v_mfma_f32_16x16x32_bf16 v[92:95], a[12:15], a[36:39], v[92:95]\n" \
"global_load_dwordx4 a[230:233], v27, s[70:71] offset:0\n" \
"v_mfma_f32_16x16x32_bf16 v[96:99], a[16:19], a[32:35], v[96:99]\n" \
"ds_read_b64_tr_b16 a[100:101], v11 offset:49152\n" \
"v_mfma_f32_16x16x32_bf16 v[100:103], a[20:23], a[32:35], v[100:103]\n" \
"v_mfma_f32_16x16x32_bf16 v[104:107], a[16:19], a[36:39], v[104:107]\n" \
"s_mov_b32 s46, 384\n" \
"s_add_u32 m0, s50, 81920\n" \
"ds_read_b64_tr_b16 a[102:103], v12 offset:49152\n" \
"v_mfma_f32_16x16x32_bf16 v[108:111], a[20:23], a[36:39], v[108:111]\n" \
"buffer_load_dwordx4 v17, s[28:31], s46 offen lds\n" \
"v_mfma_f32_16x16x32_bf16 v[112:115], a[24:27], a[32:35], v[112:115]\n" \
"ds_read_b128 a[72:75], v6 offset:4096\n" \
"v_mfma_f32_16x16x32_bf16 v[116:119], a[28:31], a[32:35], v[116:119]\n" \
"v_mfma_f32_16x16x32_bf16 v[120:123], a[24:27], a[36:39], v[120:123]\n" \
"ds_read_b64_tr_b16 a[104:105], v9 offset:49664\n" \
"v_mfma_f32_16x16x32_bf16 v[124:127], a[28:31], a[36:39], v[124:127]\n" \
"global_load_dwordx4 a[234:237], v27, s[70:71] offset:16\n" \
"v_mfma_f32_16x16x32_bf16 v[128:131], a[0:3], a[40:43], v[128:131]\n" \
"ds_read_b64_tr_b16 a[106:107], v10 offset:49664\n" \
"v_mfma_f32_16x16x32_bf16 v[132:135], a[4:7], a[40:43], v[132:135]\n" \
"v_mfma_f32_16x16x32_bf16 v[136:139], a[0:3], a[44:47], v[136:139]\n" \
"s_add_u32 m0, s22, 33664\n" \
"ds_read_b128 a[76:79], v6 offset:6144\n" \
"v_mfma_f32_16x16x32_bf16 v[140:143], a[4:7], a[44:47], v[140:143]\n" \
"buffer_load_dwordx4 v21, s[32:35], s47 offen offset:128 lds\n" \
"v_mfma_f32_16x16x32_bf16 v[144:147], a[8:11], a[40:43], v[144:147]\n" \
"ds_read_b64_tr_b16 a[108:109], v11 offset:49664\n" \
"v_mfma_f32_16x16x32_bf16 v[148:151], a[12:15], a[40:43], v[148:151]\n" \
"v_mfma_f32_16x16x32_bf16 v[152:155], a[8:11], a[44:47], v[152:155]\n" \
"ds_read_b64_tr_b16 a[110:111], v12 offset:49664\n" \
"v_mfma_f32_16x16x32_bf16 v[156:159], a[12:15], a[44:47], v[156:159]\n" \
"buffer_load_dwordx2 a[192:193], v54, s[40:43], 0 offen offset:0 nt\n" \
"v_mfma_f32_16x16x32_bf16 v[160:163], a[16:19], a[40:43], v[160:163]\n" \
"ds_read_b128 a[80:83], v6 offset:8192\n" \
"v_mfma_f32_16x16x32_bf16 v[164:167], a[20:23], a[40:43], v[164:167]\n" \
"v_mfma_f32_16x16x32_bf16 v[168:171], a[16:19], a[44:47], v[168:171]\n" \
"s_add_u32 m0, s50, 86016\n" \
"ds_read_b64_tr_b16 a[112:113], v9 offset:50176\n" \
"v_mfma_f32_16x16x32_bf16 v[172:175], a[20:23], a[44:47], v[172:175]\n" \
"buffer_load_dwordx4 v18, s[28:31], s46 offen lds\n" \
"v_mfma_f32_16x16x32_bf16 v[176:179], a[24:27], a[40:43], v[176:179]\n" \
"ds_read_b64_tr_b16 a[114:115], v10 offset:50176\n" \
"v_mfma_f32_16x16x32_bf16 v[180:183], a[28:31], a[40:43], v[180:183]\n" \
"v_mfma_f32_16x16x32_bf16 v[184:187], a[24:27], a[44:47], v[184:187]\n" \
"ds_read_b128 a[84:87], v6 offset:10240\n" \
"v_mfma_f32_16x16x32_bf16 v[188:191], a[28:31], a[44:47], v[188:191]\n" \
"buffer_load_dwordx2 a[194:195], v54, s[40:43], 0 offen offset:2048 nt\n" \
"v_mfma_f32_16x16x32_bf16 v[192:195], a[0:3], a[48:51], v[192:195]\n" \
"ds_read_b64_tr_b16 a[116:117], v11 offset:50176\n" \
"v_mfma_f32_16x16x32_bf16 v[196:199], a[4:7], a[48:51], v[196:199]\n" \
"v_mfma_f32_16x16x32_bf16 v[200:203], a[0:3], a[52:55], v[200:203]\n" \
"s_add_u32 m0, s22, 34560\n" \
"ds_read_b64_tr_b16 a[118:119], v12 offset:50176\n" \
"v_mfma_f32_16x16x32_bf16 v[204:207], a[4:7], a[52:55], v[204:207]\n" \
"LH4_%=:\ns_cbranch_execz LH8_%=\n" \
"buffer_load_dwordx4 v21, s[32:35], s47 offen offset:256 lds\n" \
"v_mfma_f32_16x16x32_bf16 v[208:211], a[8:11], a[48:51], v[208:211]\n" \
"ds_read_b128 a[88:91], v6 offset:12288\n" \
"v_mfma_f32_16x16x32_bf16 v[212:215], a[12:15], a[48:51], v[212:215]\n" \
"v_mfma_f32_16x16x32_bf16 v[216:219], a[8:11], a[52:55], v[216:219]\n" \
"ds_read_b64_tr_b16 a[120:121], v9 offset:50688\n" \
"v_mfma_f32_16x16x32_bf16 v[220:223], a[12:15], a[52:55], v[220:223]\n" \
"buffer_load_dwordx2 a[196:197], v55, s[40:43], 0 offen offset:0 nt\n" \
"v_mfma_f32_16x16x32_bf16 v[224:227], a[16:19], a[48:51], v[224:227]\n" \
"s_mov_b32 s52, 0\n" \
"ds_read_b64_tr_b16 a[122:123], v10 offset:50688\n" \
"v_mfma_f32_16x16x32_bf16 v[228:231], a[20:23], a[48:51], v[228:231]\n" \
"v_mfma_f32_16x16x32_bf16 v[232:235], a[16:19], a[52:55], v[232:235]\n" \
"s_add_u32 m0, s50, 90112\n" \
"ds_read_b128 a[92:95], v6 offset:14336\n" \
"v_mfma_f32_16x16x32_bf16 v[236:239], a[20:23], a[52:55], v[236:239]\n" \
"buffer_load_dwordx4 v19, s[28:31], s46 offen lds\n" \
"v_mfma_f32_16x16x32_bf16 v[240:243], a[24:27], a[48:51], v[240:243]\n" \
"ds_read_b64_tr_b16 a[124:125], v11 offset:50688\n" \
"v_mfma_f32_16x16x32_bf16 v[244:247], a[28:31], a[48:51], v[244:247]\n" \
"v_mfma_f32_16x16x32_bf16 v[248:251], a[24:27], a[52:55], v[248:251]\n" \
"ds_read_b64_tr_b16 a[126:127], v12 offset:50688\n" \
"v_mfma_f32_16x16x32_bf16 v[252:255], a[28:31], a[52:55], v[252:255]\n" \
"buffer_load_dwordx2 a[198:199], v59, s[40:43], s52 offen offset:0 nt\n" \
"v_mfma_f32_16x16x32_bf16 a[128:131], a[0:3], a[56:59], a[128:131]\n" \
"s_waitcnt vmcnt(12)\n" \
"v_lshlrev_b32 v25, 16, v46\n" \
"v_and_b32 v46, 0xffff0000, v46\n" \
"v_mfma_f32_16x16x32_bf16 a[132:135], a[4:7], a[56:59], a[132:135]\n" \
"v_lshlrev_b32 v26, 16, v30\n" \
"v_and_b32 v30, 0xffff0000, v30\n" \
"v_mfma_f32_16x16x32_bf16 a[136:139], a[0:3], a[60:63], a[136:139]\n" \
"s_add_u32 m0, s22, 35456\n" \
"s_add_u32 s84, s84, 1048576\ns_addc_u32 s85, s85, 0\n" \
"v_mfma_f32_16x16x32_bf16 a[140:143], a[4:7], a[60:63], a[140:143]\n" \
"buffer_load_dwordx4 v21, s[32:35], s47 offen offset:384 lds\n" \
"v_mfma_f32_16x16x32_bf16 a[144:147], a[8:11], a[56:59], a[144:147]\n" \
"v_mul_f32 v23, v26, v25\n" \
"v_fmac_f32 v23, v30, v46\n" \
"v_mfma_f32_16x16x32_bf16 a[148:151], a[12:15], a[56:59], a[148:151]\n" \
"v_lshlrev_b32 v26, 16, v38\n" \
"v_and_b32 v38, 0xffff0000, v38\n" \
"v_mfma_f32_16x16x32_bf16 a[152:155], a[8:11], a[60:63], a[152:155]\n" \
"v_mul_f32 v24, v26, v25\n" \
"v_mfma_f32_16x16x32_bf16 a[156:159], a[12:15], a[60:63], a[156:159]\n" \
"s_waitcnt vmcnt(24) lgkmcnt(0)\n" \
"v_fmac_f32 v24, v38, v46\n" \
"v_lshlrev_b32 v25, 16, v47\n" \
"v_mfma_f32_16x16x32_bf16 a[160:163], a[16:19], a[56:59], a[160:163]\n" \
"s_barrier\n" \
"v_and_b32 v47, 0xffff0000, v47\n" \
"v_lshlrev_b32 v26, 16, v31\n" \
"v_mfma_f32_16x16x32_bf16 a[164:167], a[20:23], a[56:59], a[164:167]\n" \
"v_and_b32 v31, 0xffff0000, v31\n" \
"v_mfma_f32_16x16x32_bf16 a[168:171], a[16:19], a[60:63], a[168:171]\n" \
"s_add_u32 m0, s50, 94208\n" \
"v_fmac_f32 v23, v26, v25\n" \
"v_fmac_f32 v23, v31, v47\n" \
"v_mfma_f32_16x16x32_bf16 a[172:175], a[20:23], a[60:63], a[172:175]\n" \
"buffer_load_dwordx4 v20, s[28:31], s46 offen lds\n" \
"v_mfma_f32_16x16x32_bf16 a[176:179], a[24:27], a[56:59], a[176:179]\n" \
"v_lshlrev_b32 v26, 16, v39\n" \
"v_and_b32 v39, 0xffff0000, v39\n" \
"v_mfma_f32_16x16x32_bf16 a[180:183], a[28:31], a[56:59], a[180:183]\n" \
"v_fmac_f32 v24, v26, v25\n" \
"v_fmac_f32 v24, v39, v47\n" \
"v_mfma_f32_16x16x32_bf16 a[184:187], a[24:27], a[60:63], a[184:187]\n" \
"v_lshlrev_b32 v25, 16, v48\n" \
"v_mfma_f32_16x16x32_bf16 a[188:191], a[28:31], a[60:63], a[188:191]\n" \
"s_mov_b64 exec, 0xffff\nglobal_load_dword v22, v1, s[84:85]\ns_mov_b64 exec, -1\n" \
"v_mfma_f32_16x16x32_bf16 v[64:67], a[64:67], a[96:99], v[64:67]\n" \
"ds_read_b128 a[0:3], v5 offset:32768\n" \
"v_and_b32 v48, 0xffff0000, v48\n" \
"v_mfma_f32_16x16x32_bf16 v[68:71], a[68:71], a[96:99], v[68:71]\n" \
"v_lshlrev_b32 v26, 16, v32\n" \
"v_and_b32 v32, 0xffff0000, v32\n" \
"v_mfma_f32_16x16x32_bf16 v[72:75], a[64:67], a[100:103], v[72:75]\n" \
"s_mov_b32 s47, 1835008\n" \
"s_add_u32 m0, s22, 49152\n" \
"ds_read_b64_tr_b16 a[32:33], v9 offset:0\n" \
"v_mfma_f32_16x16x32_bf16 v[76:79], a[68:71], a[100:103], v[76:79]\n" \
"buffer_load_dwordx4 v21, s[32:35], s47 offen offset:0 lds\n" \
"v_mfma_f32_16x16x32_bf16 v[80:83], a[72:75], a[96:99], v[80:83]\n" \
"ds_read_b64_tr_b16 a[34:35], v10 offset:0\n" \
"v_fmac_f32 v23, v26, v25\n" \
"v_mfma_f32_16x16x32_bf16 v[84:87], a[76:79], a[96:99], v[84:87]\n" \
"v_fmac_f32 v23, v32, v48\n" \
"v_lshlrev_b32 v26, 16, v40\n" \
"v_mfma_f32_16x16x32_bf16 v[88:91], a[72:75], a[100:103], v[88:91]\n" \
"ds_read_b128 a[4:7], v5 offset:34816\n" \
"v_and_b32 v40, 0xffff0000, v40\n" \
"v_mfma_f32_16x16x32_bf16 v[92:95], a[76:79], a[100:103], v[92:95]\n" \
"buffer_load_dwordx2 a[214:215], v59, s[80:83], s52 offen offset:0 nt\n" \
"v_mfma_f32_16x16x32_bf16 v[96:99], a[80:83], a[96:99], v[96:99]\n" \
"ds_read_b64_tr_b16 a[36:37], v11 offset:0\n" \
"v_fmac_f32 v24, v26, v25\n" \
"v_mfma_f32_16x16x32_bf16 v[100:103], a[84:87], a[96:99], v[100:103]\n" \
"v_fmac_f32 v24, v40, v48\n" \
"v_lshlrev_b32 v25, 16, v49\n" \
"v_mfma_f32_16x16x32_bf16 v[104:107], a[80:83], a[100:103], v[104:107]\n" \
"s_mov_b32 s46, 512\n" \
"s_add_u32 m0, s50, 0\n" \
"ds_read_b64_tr_b16 a[38:39], v12 offset:0\n" \
"v_mfma_f32_16x16x32_bf16 v[108:111], a[84:87], a[100:103], v[108:111]\n" \
"buffer_load_dwordx4 v13, s[28:31], s46 offen lds\n" \
"v_mfma_f32_16x16x32_bf16 v[112:115], a[88:91], a[96:99], v[112:115]\n" \
"ds_read_b128 a[8:11], v5 offset:36864\n" \
"v_and_b32 v49, 0xffff0000, v49\n" \
"v_mfma_f32_16x16x32_bf16 v[116:119], a[92:95], a[96:99], v[116:119]\n" \
"v_lshlrev_b32 v26, 16, v33\n" \
"v_and_b32 v33, 0xffff0000, v33\n" \
"v_mfma_f32_16x16x32_bf16 v[120:123], a[88:91], a[100:103], v[120:123]\n" \
"ds_read_b64_tr_b16 a[40:41], v9 offset:512\n" \
"v_fmac_f32 v23, v26, v25\n" \
"v_mfma_f32_16x16x32_bf16 v[124:127], a[92:95], a[100:103], v[124:127]\n" \
"buffer_load_dwordx2 a[200:201], v59, s[40:43], s52 offen offset:2048 nt\n" \
"v_mfma_f32_16x16x32_bf16 v[128:131], a[64:67], a[104:107], v[128:131]\n" \
"ds_read_b64_tr_b16 a[42:43], v10 offset:512\n" \
"v_fmac_f32 v23, v33, v49\n" \
"v_mfma_f32_16x16x32_bf16 v[132:135], a[68:71], a[104:107], v[132:135]\n" \
"v_lshlrev_b32 v26, 16, v41\n" \
"v_and_b32 v41, 0xffff0000, v41\n" \
"v_mfma_f32_16x16x32_bf16 v[136:139], a[64:67], a[108:111], v[136:139]\n" \
"s_add_u32 m0, s22, 50048\n" \
"ds_read_b128 a[12:15], v5 offset:38912\n" \
"v_fmac_f32 v24, v26, v25\n" \
"v_mfma_f32_16x16x32_bf16 v[140:143], a[68:71], a[108:111], v[140:143]\n" \
"buffer_load_dwordx4 v21, s[32:35], s47 offen offset:128 lds\n" \
"v_mfma_f32_16x16x32_bf16 v[144:147], a[72:75], a[104:107], v[144:147]\n" \
"ds_read_b64_tr_b16 a[44:45], v11 offset:512\n" \
"v_fmac_f32 v24, v41, v49\n" \
"v_mfma_f32_16x16x32_bf16 v[148:151], a[76:79], a[104:107], v[148:151]\n" \
"v_lshlrev_b32 v25, 16, v50\n" \
"v_and_b32 v50, 0xffff0000, v50\n" \
"v_mfma_f32_16x16x32_bf16 v[152:155], a[72:75], a[108:111], v[152:155]\n" \
"ds_read_b64_tr_b16 a[46:47], v12 offset:512\n" \
"v_lshlrev_b32 v26, 16, v34\n" \
"v_mfma_f32_16x16x32_bf16 v[156:159], a[76:79], a[108:111], v[156:159]\n" \
"buffer_load_dwordx2 a[216:217], v59, s[80:83], s52 offen offset:2048 nt\n" \
"v_mfma_f32_16x16x32_bf16 v[160:163], a[80:83], a[104:107], v[160:163]\n" \
"s_mov_b32 s52, 4096\n" \
"ds_read_b128 a[16:19], v5 offset:40960\n" \
"v_and_b32 v34, 0xffff0000, v34\n" \
"v_mfma_f32_16x16x32_bf16 v[164:167], a[84:87], a[104:107], v[164:167]\n" \
"v_fmac_f32 v23, v26, v25\n" \
"v_fmac_f32 v23, v34, v50\n" \
"v_mfma_f32_16x16x32_bf16 v[168:171], a[80:83], a[108:111], v[168:171]\n" \
"s_add_u32 m0, s50, 4096\n" \
"ds_read_b64_tr_b16 a[48:49], v9 offset:1024\n" \
"v_lshlrev_b32 v26, 16, v42\n" \
"v_mfma_f32_16x16x32_bf16 v[172:175], a[84:87], a[108:111], v[172:175]\n" \
"buffer_load_dwordx4 v14, s[28:31], s46 offen lds\n" \
"v_mfma_f32_16x16x32_bf16 v[176:179], a[88:91], a[104:107], v[176:179]\n" \
"ds_read_b64_tr_b16 a[50:51], v10 offset:1024\n" \
"v_and_b32 v42, 0xffff0000, v42\n" \
"v_mfma_f32_16x16x32_bf16 v[180:183], a[92:95], a[104:107], v[180:183]\n" \
"v_fmac_f32 v24, v26, v25\n" \
"v_fmac_f32 v24, v42, v50\n" \
"v_mfma_f32_16x16x32_bf16 v[184:187], a[88:91], a[108:111], v[184:187]\n" \
"ds_read_b128 a[20:23], v5 offset:43008\n" \
"v_lshlrev_b32 v25, 16, v51\n" \
"v_mfma_f32_16x16x32_bf16 v[188:191], a[92:95], a[108:111], v[188:191]\n" \
"buffer_load_dwordx2 a[202:203], v59, s[40:43], s52 offen offset:0 nt\n" \
"v_mfma_f32_16x16x32_bf16 v[192:195], a[64:67], a[112:115], v[192:195]\n" \
"ds_read_b64_tr_b16 a[52:53], v11 offset:1024\n" \
"v_and_b32 v51, 0xffff0000, v51\n" \
"v_mfma_f32_16x16x32_bf16 v[196:199], a[68:71], a[112:115], v[196:199]\n" \
"v_lshlrev_b32 v26, 16, v35\n" \
"v_and_b32 v35, 0xffff0000, v35\n" \
"v_mfma_f32_16x16x32_bf16 v[200:203], a[64:67], a[116:119], v[200:203]\n" \
"s_add_u32 m0, s22, 50944\n" \
"ds_read_b64_tr_b16 a[54:55], v12 offset:1024\n" \
"v_fmac_f32 v23, v26, v25\n" \
"v_mfma_f32_16x16x32_bf16 v[204:207], a[68:71], a[116:119], v[204:207]\n" \
"buffer_load_dwordx4 v21, s[32:35], s47 offen offset:256 lds\n" \
"v_mfma_f32_16x16x32_bf16 v[208:211], a[72:75], a[112:115], v[208:211]\n" \
"ds_read_b128 a[24:27], v5 offset:45056\n" \
"v_fmac_f32 v23, v35, v51\n" \
"v_mfma_f32_16x16x32_bf16 v[212:215], a[76:79], a[112:115], v[212:215]\n" \
"v_lshlrev_b32 v26, 16, v43\n" \
"v_and_b32 v43, 0xffff0000, v43\n" \
"v_mfma_f32_16x16x32_bf16 v[216:219], a[72:75], a[116:119], v[216:219]\n" \
"ds_read_b64_tr_b16 a[56:57], v9 offset:1536\n" \
"v_fmac_f32 v24, v26, v25\n" \
"v_mfma_f32_16x16x32_bf16 v[220:223], a[76:79], a[116:119], v[220:223]\n" \
"buffer_load_dwordx2 a[218:219], v59, s[80:83], s52 offen offset:0 nt\n" \
"v_mfma_f32_16x16x32_bf16 v[224:227], a[80:83], a[112:115], v[224:227]\n" \
"ds_read_b64_tr_b16 a[58:59], v10 offset:1536\n" \
"v_fmac_f32 v24, v43, v51\n" \
"v_mfma_f32_16x16x32_bf16 v[228:231], a[84:87], a[112:115], v[228:231]\n" \
"v_lshlrev_b32 v25, 16, v52\n" \
"v_and_b32 v52, 0xffff0000, v52\n" \
"v_mfma_f32_16x16x32_bf16 v[232:235], a[80:83], a[116:119], v[232:235]\n" \
"s_add_u32 m0, s50, 8192\n" \
"ds_read_b128 a[28:31], v5 offset:47104\n" \
"v_lshlrev_b32 v26, 16, v36\n" \
"v_mfma_f32_16x16x32_bf16 v[236:239], a[84:87], a[116:119], v[236:239]\n" \
"buffer_load_dwordx4 v15, s[28:31], s46 offen lds\n" \
"v_mfma_f32_16x16x32_bf16 v[240:243], a[88:91], a[112:115], v[240:243]\n" \
"ds_read_b64_tr_b16 a[60:61], v11 offset:1536\n" \
"v_and_b32 v36, 0xffff0000, v36\n" \
"v_mfma_f32_16x16x32_bf16 v[244:247], a[92:95], a[112:115], v[244:247]\n" \
"v_fmac_f32 v23, v26, v25\n" \
"v_fmac_f32 v23, v36, v52\n" \
"v_mfma_f32_16x16x32_bf16 v[248:251], a[88:91], a[116:119], v[248:251]\n" \
"ds_read_b64_tr_b16 a[62:63], v12 offset:1536\n" \
"v_lshlrev_b32 v26, 16, v44\n" \
"v_mfma_f32_16x16x32_bf16 v[252:255], a[92:95], a[116:119], v[252:255]\n" \
"buffer_load_dwordx2 a[204:205], v59, s[40:43], s52 offen offset:2048 nt\n" \
"v_mfma_f32_16x16x32_bf16 a[128:131], a[64:67], a[120:123], a[128:131]\n" \
"v_and_b32 v44, 0xffff0000, v44\n" \
"v_fmac_f32 v24, v26, v25\n" \
"v_mfma_f32_16x16x32_bf16 a[132:135], a[68:71], a[120:123], a[132:135]\n" \
"v_fmac_f32 v24, v44, v52\n" \
"v_lshlrev_b32 v25, 16, v53\n" \
"v_mfma_f32_16x16x32_bf16 a[136:139], a[64:67], a[124:127], a[136:139]\n" \
"s_add_u32 m0, s22, 51840\n" \
"v_and_b32 v53, 0xffff0000, v53\n" \
"v_mfma_f32_16x16x32_bf16 a[140:143], a[68:71], a[124:127], a[140:143]\n" \
"buffer_load_dwordx4 v21, s[32:35], s47 offen offset:384 lds\n" \
"v_mfma_f32_16x16x32_bf16 a[144:147], a[72:75], a[120:123], a[144:147]\n" \
"v_lshlrev_b32 v26, 16, v37\n" \
"v_and_b32 v37, 0xffff0000, v37\n" \
"v_mfma_f32_16x16x32_bf16 a[148:151], a[76:79], a[120:123], a[148:151]\n" \
"v_fmac_f32 v23, v26, v25\n" \
"v_fmac_f32 v23, v37, v53\n" \
"v_mfma_f32_16x16x32_bf16 a[152:155], a[72:75], a[124:127], a[152:155]\n" \
"v_lshlrev_b32 v26, 16, v45\n" \
"v_mfma_f32_16x16x32_bf16 a[156:159], a[76:79], a[124:127], a[156:159]\n" \
"s_waitcnt vmcnt(32) lgkmcnt(0)\n" \
"v_and_b32 v45, 0xffff0000, v45\n" \
"v_fmac_f32 v24, v26, v25\n" \
"v_mfma_f32_16x16x32_bf16 a[160:163], a[80:83], a[120:123], a[160:163]\n" \
"s_barrier\n" \
"v_fmac_f32 v24, v45, v53\n" \
"v_mfma_f32_16x16x32_bf16 a[164:167], a[84:87], a[120:123], a[164:167]\n" \
"v_mfma_f32_16x16x32_bf16 a[168:171], a[80:83], a[124:127], a[168:171]\n" \
"s_add_u32 m0, s50, 12288\n" \
"v_mfma_f32_16x16x32_bf16 a[172:175], a[84:87], a[124:127], a[172:175]\n" \
"buffer_load_dwordx4 v16, s[28:31], s46 offen lds\n" \
"v_mfma_f32_16x16x32_bf16 a[176:179], a[88:91], a[120:123], a[176:179]\n" \
"v_mfma_f32_16x16x32_bf16 a[180:183], a[92:95], a[120:123], a[180:183]\n" \
"v_mfma_f32_16x16x32_bf16 a[184:187], a[88:91], a[124:127], a[184:187]\n" \
"v_mfma_f32_16x16x32_bf16 a[188:191], a[92:95], a[124:127], a[188:191]\n" \
"buffer_load_dwordx2 a[220:221], v59, s[80:83], s52 offen offset:2048 nt\n" \
"v_mfma_f32_16x16x32_bf16 v[64:67], a[0:3], a[32:35], v[64:67]\n" \
"s_mov_b32 s52, 8192\n" \
"ds_read_b128 a[64:67], v6 offset:32768\n" \
"v_mfma_f32_16x16x32_bf16 v[68:71], a[4:7], a[32:35], v[68:71]\n" \
"v_mfma_f32_16x16x32_bf16 v[72:75], a[0:3], a[36:39], v[72:75]\n" \
"s_mov_b32 s47, 2097152\n" \
"s_add_u32 m0, s22, 0\n" \
"ds_read_b64_tr_b16 a[96:97], v9 offset:16384\n" \
"v_mfma_f32_16x16x32_bf16 v[76:79], a[4:7], a[36:39], v[76:79]\n" \
"buffer_load_dwordx4 v21, s[32:35], s47 offen offset:0 lds\n" \
"v_mfma_f32_16x16x32_bf16 v[80:83], a[8:11], a[32:35], v[80:83]\n" \
"ds_read_b64_tr_b16 a[98:99], v10 offset:16384\n" \
"v_mfma_f32_16x16x32_bf16 v[84:87], a[12:15], a[32:35], v[84:87]\n" \
"v_mfma_f32_16x16x32_bf16 v[88:91], a[8:11], a[36:39], v[88:91]\n" \
"ds_read_b128 a[68:71], v6 offset:34816\n" \
"v_mfma_f32_16x16x32_bf16 v[92:95], a[12:15], a[36:39], v[92:95]\n" \
"buffer_load_dwordx2 a[206:207], v59, s[40:43], s52 offen offset:0 nt\n" \
"v_mfma_f32_16x16x32_bf16 v[96:99], a[16:19], a[32:35], v[96:99]\n" \
"ds_read_b64_tr_b16 a[100:101], v11 offset:16384\n" \
"v_mfma_f32_16x16x32_bf16 v[100:103], a[20:23], a[32:35], v[100:103]\n" \
"v_mfma_f32_16x16x32_bf16 v[104:107], a[16:19], a[36:39], v[104:107]\n" \
"s_mov_b32 s46, 512\n" \
"s_add_u32 m0, s50, 16384\n" \
"ds_read_b64_tr_b16 a[102:103], v12 offset:16384\n" \
"v_mfma_f32_16x16x32_bf16 v[108:111], a[20:23], a[36:39], v[108:111]\n" \
"buffer_load_dwordx4 v17, s[28:31], s46 offen lds\n" \
"v_mfma_f32_16x16x32_bf16 v[112:115], a[24:27], a[32:35], v[112:115]\n" \
"ds_read_b128 a[72:75], v6 offset:36864\n" \
"v_mfma_f32_16x16x32_bf16 v[116:119], a[28:31], a[32:35], v[116:119]\n" \
"v_mfma_f32_16x16x32_bf16 v[120:123], a[24:27], a[36:39], v[120:123]\n" \
"ds_read_b64_tr_b16 a[104:105], v9 offset:16896\n" \
"v_mfma_f32_16x16x32_bf16 v[124:127], a[28:31], a[36:39], v[124:127]\n" \
"buffer_load_dwordx2 a[222:223], v59, s[80:83], s52 offen offset:0 nt\n" \
"v_mfma_f32_16x16x32_bf16 v[128:131], a[0:3], a[40:43], v[128:131]\n" \
"ds_read_b64_tr_b16 a[106:107], v10 offset:16896\n" \
"v_mfma_f32_16x16x32_bf16 v[132:135], a[4:7], a[40:43], v[132:135]\n" \
"v_mfma_f32_16x16x32_bf16 v[136:139], a[0:3], a[44:47], v[136:139]\n" \
"s_add_u32 m0, s22, 896\n" \
"ds_read_b128 a[76:79], v6 offset:38912\n" \
"v_mfma_f32_16x16x32_bf16 v[140:143], a[4:7], a[44:47], v[140:143]\n" \
"buffer_load_dwordx4 v21, s[32:35], s47 offen offset:128 lds\n" \
"v_mfma_f32_16x16x32_bf16 v[144:147], a[8:11], a[40:43], v[144:147]\n" \
"ds_read_b64_tr_b16 a[108:109], v11 offset:16896\n" \
"v_mfma_f32_16x16x32_bf16 v[148:151], a[12:15], a[40:43], v[148:151]\n" \
"v_mfma_f32_16x16x32_bf16 v[152:155], a[8:11], a[44:47], v[152:155]\n" \
"ds_read_b64_tr_b16 a[110:111], v12 offset:16896\n" \
"v_mfma_f32_16x16x32_bf16 v[156:159], a[12:15], a[44:47], v[156:159]\n" \
"buffer_load_dwordx2 a[208:209], v59, s[40:43], s52 offen offset:2048 nt\n" \
"v_mfma_f32_16x16x32_bf16 v[160:163], a[16:19], a[40:43], v[160:163]\n" \
"ds_read_b128 a[80:83], v6 offset:40960\n" \
"v_mfma_f32_16x16x32_bf16 v[164:167], a[20:23], a[40:43], v[164:167]\n" \
"v_mfma_f32_16x16x32_bf16 v[168:171], a[16:19], a[44:47], v[168:171]\n" \
"s_add_u32 m0, s50, 20480\n" \
"ds_read_b64_tr_b16 a[112:113], v9 offset:17408\n" \
"v_mfma_f32_16x16x32_bf16 v[172:175], a[20:23], a[44:47], v[172:175]\n" \
"buffer_load_dwordx4 v18, s[28:31], s46 offen lds\n" \
"v_mfma_f32_16x16x32_bf16 v[176:179], a[24:27], a[40:43], v[176:179]\n" \
"ds_read_b64_tr_b16 a[114:115], v10 offset:17408\n" \
"v_mfma_f32_16x16x32_bf16 v[180:183], a[28:31], a[40:43], v[180:183]\n" \
"v_mfma_f32_16x16x32_bf16 v[184:187], a[24:27], a[44:47], v[184:187]\n" \
"ds_read_b128 a[84:87], v6 offset:43008\n" \
"v_mfma_f32_16x16x32_bf16 v[188:191], a[28:31], a[44:47], v[188:191]\n" \
"buffer_load_dwordx2 a[224:225], v59, s[80:83], s52 offen offset:2048 nt\n" \
"v_mfma_f32_16x16x32_bf16 v[192:195], a[0:3], a[48:51], v[192:195]\n" \
"s_mov_b32 s52, 12288\n" \
"ds_read_b64_tr_b16 a[116:117], v11 offset:17408\n" \
"v_mfma_f32_16x16x32_bf16 v[196:199], a[4:7], a[48:51], v[196:199]\n" \
"v_mfma_f32_16x16x32_bf16 v[200:203], a[0:3], a[52:55], v[200:203]\n" \
"s_add_u32 m0, s22, 1792\n" \
"ds_read_b64_tr_b16 a[118:119], v12 offset:17408\n" \
"v_mfma_f32_16x16x32_bf16 v[204:207], a[4:7], a[52:55], v[204:207]\n" \
"buffer_load_dwordx4 v21, s[32:35], s47 offen offset:256 lds\n" \
"v_mfma_f32_16x16x32_bf16 v[208:211], a[8:11], a[48:51], v[208:211]\n" \
"ds_read_b128 a[88:91], v6 offset:45056\n" \
"v_mfma_f32_16x16x32_bf16 v[212:215], a[12:15], a[48:51], v[212:215]\n" \
"v_mfma_f32_16x16x32_bf16 v[216:219], a[8:11], a[52:55], v[216:219]\n" \
"ds_read_b64_tr_b16 a[120:121], v9 offset:17920\n" \
"v_mfma_f32_16x16x32_bf16 v[220:223], a[12:15], a[52:55], v[220:223]\n" \
"buffer_load_dwordx2 a[210:211], v59, s[40:43], s52 offen offset:0 nt\n" \
"v_mfma_f32_16x16x32_bf16 v[224:227], a[16:19], a[48:51], v[224:227]\n" \
"ds_read_b64_tr_b16 a[122:123], v10 offset:17920\n" \
"v_mfma_f32_16x16x32_bf16 v[228:231], a[20:23], a[48:51], v[228:231]\n" \
"v_mfma_f32_16x16x32_bf16 v[232:235], a[16:19], a[52:55], v[232:235]\n" \
"s_add_u32 m0, s50, 24576\n" \
"ds_read_b128 a[92:95], v6 offset:47104\n" \
"v_mfma_f32_16x16x32_bf16 v[236:239], a[20:23], a[52:55], v[236:239]\n" \
"buffer_load_dwordx4 v19, s[28:31], s46 offen lds\n" \
"v_mfma_f32_16x16x32_bf16 v[240:243], a[24:27], a[48:51], v[240:243]\n" \
"ds_read_b64_tr_b16 a[124:125], v11 offset:17920\n" \
"v_mfma_f32_16x16x32_bf16 v[244:247], a[28:31], a[48:51], v[244:247]\n" \
"v_mfma_f32_16x16x32_bf16 v[248:251], a[24:27], a[52:55], v[248:251]\n" \
"ds_read_b64_tr_b16 a[126:127], v12 offset:17920\n" \
"v_mfma_f32_16x16x32_bf16 v[252:255], a[28:31], a[52:55], v[252:255]\n" \
"buffer_load_dwordx2 a[226:227], v59, s[80:83], s52 offen offset:0 nt\n" \
"v_mfma_f32_16x16x32_bf16 a[128:131], a[0:3], a[56:59], a[128:131]\n" \
"v_mfma_f32_16x16x32_bf16 a[132:135], a[4:7], a[56:59], a[132:135]\n" \
"s_waitcnt vmcnt(12)\n" \
"v_accvgpr_read_b32 v43, a230\n" \
"v_permlane32_swap_b32 v23, v24\n" \
"v_mfma_f32_16x16x32_bf16 a[136:139], a[0:3], a[60:63], a[136:139]\n" \
"s_add_u32 m0, s22, 2688\n" \
"s_add_u32 s84, s84, 1048576\ns_addc_u32 s85, s85, 0\n" \
"v_mfma_f32_16x16x32_bf16 a[140:143], a[4:7], a[60:63], a[140:143]\n" \
"buffer_load_dwordx4 v21, s[32:35], s47 offen offset:384 lds\n" \
"v_mfma_f32_16x16x32_bf16 a[144:147], a[8:11], a[56:59], a[144:147]\n" \
"v_add_f32 v23, v23, v24\n" \
"v_mov_b32 v24, v23\n" \
"v_mfma_f32_16x16x32_bf16 a[148:151], a[12:15], a[56:59], a[148:151]\n" \
"v_lshlrev_b32 v42, 16, v43\n" \
"v_and_b32 v43, 0xffff0000, v43\n" \
"v_mfma_f32_16x16x32_bf16 a[152:155], a[8:11], a[60:63], a[152:155]\n" \
"v_permlane16_swap_b32 v23, v24\n" \
"v_mfma_f32_16x16x32_bf16 a[156:159], a[12:15], a[60:63], a[156:159]\n" \
"s_waitcnt vmcnt(29) lgkmcnt(0)\n" \
"v_add_f32 v23, v23, v24\n" \
"v_accvgpr_read_b32 v45, a231\n" \
"v_mfma_f32_16x16x32_bf16 a[160:163], a[16:19], a[56:59], a[160:163]\n" \
"s_barrier\n" \
"v_lshlrev_b32 v44, 16, v45\n" \
"v_add_f32 v23, v23, v23 row_ror:8 row_mask:0xf bank_mask:0xf\n" \
"v_mfma_f32_16x16x32_bf16 a[164:167], a[20:23], a[56:59], a[164:167]\n" \
"v_and_b32 v45, 0xffff0000, v45\n" \
"v_mfma_f32_16x16x32_bf16 a[168:171], a[16:19], a[60:63], a[168:171]\n" \
"s_add_u32 m0, s50, 28672\n" \
"v_accvgpr_read_b32 v47, a232\n" \
"v_add_f32 v23, v23, v23 row_ror:4 row_mask:0xf bank_mask:0xf\n" \
"v_mfma_f32_16x16x32_bf16 a[172:175], a[20:23], a[60:63], a[172:175]\n" \
"buffer_load_dwordx4 v20, s[28:31], s46 offen lds\n" \
"v_mfma_f32_16x16x32_bf16 a[176:179], a[24:27], a[56:59], a[176:179]\n" \
"v_lshlrev_b32 v46, 16, v47\n" \
"v_and_b32 v47, 0xffff0000, v47\n" \
"v_mfma_f32_16x16x32_bf16 a[180:183], a[28:31], a[56:59], a[180:183]\n" \
"v_add_f32 v23, v23, v23 row_ror:2 row_mask:0xf bank_mask:0xf\n" \
"v_accvgpr_read_b32 v49, a233\n" \
"v_mfma_f32_16x16x32_bf16 a[184:187], a[24:27], a[60:63], a[184:187]\n" \
"v_lshlrev_b32 v48, 16, v49\n" \
"v_mfma_f32_16x16x32_bf16 a[188:191], a[28:31], a[60:63], a[188:191]\n" \
"s_mov_b64 exec, 0xffff\nglobal_load_dword v22, v1, s[84:85]\ns_mov_b64 exec, -1\n" \
"v_mfma_f32_16x16x32_bf16 v[64:67], a[64:67], a[96:99], v[64:67]\n" \
"ds_read_b128 a[0:3], v7 offset:0\n" \
"v_add_f32 v23, v23, v23 row_ror:1 row_mask:0xf bank_mask:0xf\n" \
"v_cvt_pk_bf16_f32 v23, v23, v23\n" \
"v_mfma_f32_16x16x32_bf16 v[68:71], a[68:71], a[96:99], v[68:71]\n" \
"v_and_b32 v23, 0xffff0000, v23\n" \
"v_mul_f32 v23, 0xbfb8aa3b, v23\n" \
"v_mfma_f32_16x16x32_bf16 v[72:75], a[64:67], a[100:103], v[72:75]\n" \
"s_mov_b32 s47, 2359296\n" \
"s_add_u32 m0, s22, 16384\n" \
"ds_read_b64_tr_b16 a[32:33], v9 offset:32768\n" \
"v_mfma_f32_16x16x32_bf16 v[76:79], a[68:71], a[100:103], v[76:79]\n" \
"buffer_load_dwordx4 v21, s[32:35], s47 offen offset:0 lds\n" \
"v_mfma_f32_16x16x32_bf16 v[80:83], a[72:75], a[96:99], v[80:83]\n" \
"ds_read_b64_tr_b16 a[34:35], v10 offset:32768\n" \
"v_exp_f32 v23, v23\n" \
"v_mfma_f32_16x16x32_bf16 v[84:87], a[76:79], a[96:99], v[84:87]\n" \
"v_and_b32 v49, 0xffff0000, v49\n" \
"v_accvgpr_read_b32 v36, a192\n" \
"v_add_f32 v23, 1.0, v23\n" \
"v_mfma_f32_16x16x32_bf16 v[88:91], a[72:75], a[100:103], v[88:91]\n" \
"ds_read_b128 a[4:7], v7 offset:2048\n" \
"v_rcp_f32 v23, v23\n" \
"v_mfma_f32_16x16x32_bf16 v[92:95], a[76:79], a[100:103], v[92:95]\n" \
"buffer_load_dwordx2 a[212:213], v59, s[40:43], s52 offen offset:2048 nt\n" \
"v_mfma_f32_16x16x32_bf16 v[96:99], a[80:83], a[96:99], v[96:99]\n" \
"ds_read_b64_tr_b16 a[36:37], v11 offset:32768\n" \
"v_lshlrev_b32 v30, 16, v36\n" \
"v_and_b32 v36, 0xffff0000, v36\n" \
"v_mfma_f32_16x16x32_bf16 v[100:103], a[84:87], a[96:99], v[100:103]\n" \
"v_cvt_pk_bf16_f32 v23, v23, v23\n" \
"v_and_b32 v23, 0xffff0000, v23\n" \
"v_accvgpr_read_b32 v37, a194\n" \
"v_mfma_f32_16x16x32_bf16 v[104:107], a[80:83], a[100:103], v[104:107]\n" \
"s_mov_b32 s46, 640\n" \
"s_add_u32 m0, s50, 32768\n" \
"ds_read_b64_tr_b16 a[38:39], v12 offset:32768\n" \
"v_lshlrev_b32 v31, 16, v37\n" \
"v_mfma_f32_16x16x32_bf16 v[108:111], a[84:87], a[100:103], v[108:111]\n" \
"buffer_load_dwordx4 v13, s[28:31], s46 offen lds\n" \
"v_mfma_f32_16x16x32_bf16 v[112:115], a[88:91], a[96:99], v[112:115]\n" \
"ds_read_b128 a[8:11], v7 offset:4096\n" \
"v_and_b32 v37, 0xffff0000, v37\n" \
"v_accvgpr_read_b32 v38, a196\n" \
"v_mfma_f32_16x16x32_bf16 v[116:119], a[92:95], a[96:99], v[116:119]\n" \
"buffer_store_dword v23, v61, s[36:39], 0 offen sc1\n" \
"v_mfma_f32_16x16x32_bf16 v[120:123], a[88:91], a[100:103], v[120:123]\n" \
"ds_read_b64_tr_b16 a[40:41], v9 offset:33280\n" \
"v_lshlrev_b32 v32, 16, v38\n" \
"v_and_b32 v38, 0xffff0000, v38\n" \
"v_mfma_f32_16x16x32_bf16 v[124:127], a[92:95], a[100:103], v[124:127]\n" \
"buffer_load_dwordx2 a[228:229], v59, s[80:83], s52 offen offset:2048 nt\n" \
"v_mfma_f32_16x16x32_bf16 v[128:131], a[64:67], a[104:107], v[128:131]\n" \
"ds_read_b64_tr_b16 a[42:43], v10 offset:33280\n" \
"v_accvgpr_read_b32 v39, a198\n" \
"v_lshlrev_b32 v33, 16, v39\n" \
"v_mfma_f32_16x16x32_bf16 v[132:135], a[68:71], a[104:107], v[132:135]\n" \
"v_and_b32 v39, 0xffff0000, v39\n" \
"v_mul_f32 v25, v30, v45\n" \
"v_mul_f32 v26, v36, v49\n" \
"v_mfma_f32_16x16x32_bf16 v[136:139], a[64:67], a[108:111], v[136:139]\n" \
"s_add_u32 m0, s22, 17280\n" \
"ds_read_b128 a[12:15], v7 offset:6144\n" \
"v_fmac_f32 v25, v31, v44\n" \
"v_fmac_f32 v26, v37, v48\n" \
"v_mfma_f32_16x16x32_bf16 v[140:143], a[68:71], a[108:111], v[140:143]\n" \
"buffer_load_dwordx4 v21, s[32:35], s47 offen offset:128 lds\n" \
"v_mfma_f32_16x16x32_bf16 v[144:147], a[72:75], a[104:107], v[144:147]\n" \
"ds_read_b64_tr_b16 a[44:45], v11 offset:33280\n" \
"v_fmac_f32 v25, v32, v43\n" \
"v_fmac_f32 v26, v38, v47\n" \
"v_mfma_f32_16x16x32_bf16 v[148:151], a[76:79], a[104:107], v[148:151]\n" \
"v_fmac_f32 v25, v33, v42\n" \
"v_fmac_f32 v26, v39, v46\n" \
"v_accvgpr_read_b32 v28, a214\n" \
"v_mfma_f32_16x16x32_bf16 v[152:155], a[72:75], a[108:111], v[152:155]\n" \
"ds_read_b64_tr_b16 a[46:47], v12 offset:33280\n" \
"v_cvt_pk_bf16_f32 v26, v25, v26\n" \
"v_lshlrev_b32 v27, 16, v28\n" \
"v_mfma_f32_16x16x32_bf16 v[156:159], a[76:79], a[108:111], v[156:159]\n" \
"v_lshlrev_b32 v25, 16, v26\n" \
"v_and_b32 v28, 0xffff0000, v28\n" \
"v_and_b32 v26, 0xffff0000, v26\n" \
"v_mfma_f32_16x16x32_bf16 v[160:163], a[80:83], a[104:107], v[160:163]\n" \
"ds_read_b128 a[16:19], v7 offset:8192\n" \
"v_mul_f32 v29, v25, v27\n" \
"v_mul_f32 v58, v26, v28\n" \
"v_mfma_f32_16x16x32_bf16 v[164:167], a[84:87], a[104:107], v[164:167]\n" \
"v_accvgpr_read_b32 v40, a200\n" \
"v_lshlrev_b32 v34, 16, v40\n" \
"v_and_b32 v40, 0xffff0000, v40\n" \
"v_mfma_f32_16x16x32_bf16 v[168:171], a[80:83], a[108:111], v[168:171]\n" \
"s_add_u32 m0, s50, 36864\n" \
"ds_read_b64_tr_b16 a[48:49], v9 offset:33792\n" \
"v_mul_f32 v25, v31, v45\n" \
"v_mul_f32 v26, v37, v49\n" \
"v_mfma_f32_16x16x32_bf16 v[172:175], a[84:87], a[108:111], v[172:175]\n" \
"buffer_load_dwordx4 v14, s[28:31], s46 offen lds\n" \
"v_mfma_f32_16x16x32_bf16 v[176:179], a[88:91], a[104:107], v[176:179]\n" \
"ds_read_b64_tr_b16 a[50:51], v10 offset:33792\n" \
"v_fmac_f32 v25, v32, v44\n" \
"v_fmac_f32 v26, v38, v48\n" \
"v_mfma_f32_16x16x32_bf16 v[180:183], a[92:95], a[104:107], v[180:183]\n" \
"v_fmac_f32 v25, v33, v43\n" \
"v_fmac_f32 v26, v39, v47\n" \
"v_fmac_f32 v25, v34, v42\n" \
"v_mfma_f32_16x16x32_bf16 v[184:187], a[88:91], a[108:111], v[184:187]\n" \
"ds_read_b128 a[20:23], v7 offset:10240\n" \
"v_fmac_f32 v26, v40, v46\n" \
"v_accvgpr_read_b32 v28, a216\n" \
"v_mfma_f32_16x16x32_bf16 v[188:191], a[92:95], a[108:111], v[188:191]\n" \
"v_cvt_pk_bf16_f32 v26, v25, v26\n" \
"v_lshlrev_b32 v27, 16, v28\n" \
"v_lshlrev_b32 v25, 16, v26\n" \
"v_mfma_f32_16x16x32_bf16 v[192:195], a[64:67], a[112:115], v[192:195]\n" \
"ds_read_b64_tr_b16 a[52:53], v11 offset:33792\n" \
"v_and_b32 v28, 0xffff0000, v28\n" \
"v_and_b32 v26, 0xffff0000, v26\n" \
"v_mfma_f32_16x16x32_bf16 v[196:199], a[68:71], a[112:115], v[196:199]\n" \
"v_mul_f32 v25, v25, v27\n" \
"v_mul_f32 v26, v26, v28\n" \
"v_cvt_pk_bf16_f32 v53, v25, v29\n" \
"v_mfma_f32_16x16x32_bf16 v[200:203], a[64:67], a[116:119], v[200:203]\n" \
"s_add_u32 m0, s22, 18176\n" \
"ds_read_b64_tr_b16 a[54:55], v12 offset:33792\n" \
"v_cvt_pk_bf16_f32 v57, v26, v58\n" \
"v_accvgpr_read_b32 v41, a202\n" \
"v_mfma_f32_16x16x32_bf16 v[204:207], a[68:71], a[116:119], v[204:207]\n" \
"buffer_load_dwordx4 v21, s[32:35], s47 offen offset:256 lds\n" \
"v_mfma_f32_16x16x32_bf16 v[208:211], a[72:75], a[112:115], v[208:211]\n" \
"ds_read_b128 a[24:27], v7 offset:12288\n" \
"v_lshlrev_b32 v35, 16, v41\n" \
"v_and_b32 v41, 0xffff0000, v41\n" \
"v_mfma_f32_16x16x32_bf16 v[212:215], a[76:79], a[112:115], v[212:215]\n" \
"v_mul_f32 v25, v32, v45\n" \
"v_mul_f32 v26, v38, v49\n" \
"v_fmac_f32 v25, v33, v44\n" \
"v_mfma_f32_16x16x32_bf16 v[216:219], a[72:75], a[116:119], v[216:219]\n" \
"ds_read_b64_tr_b16 a[56:57], v9 offset:34304\n" \
"v_fmac_f32 v26, v39, v48\n" \
"v_fmac_f32 v25, v34, v43\n" \
"v_mfma_f32_16x16x32_bf16 v[220:223], a[76:79], a[116:119], v[220:223]\n" \
"v_fmac_f32 v26, v40, v47\n" \
"v_fmac_f32 v25, v35, v42\n" \
"v_fmac_f32 v26, v41, v46\n" \
"v_mfma_f32_16x16x32_bf16 v[224:227], a[80:83], a[112:115], v[224:227]\n" \
"ds_read_b64_tr_b16 a[58:59], v10 offset:34304\n" \
"v_accvgpr_read_b32 v28, a218\n" \
"v_cvt_pk_bf16_f32 v26, v25, v26\n" \
"v_mfma_f32_16x16x32_bf16 v[228:231], a[84:87], a[112:115], v[228:231]\n" \
"v_lshlrev_b32 v27, 16, v28\n" \
"v_lshlrev_b32 v25, 16, v26\n" \
"v_and_b32 v28, 0xffff0000, v28\n" \
"v_mfma_f32_16x16x32_bf16 v[232:235], a[80:83], a[116:119], v[232:235]\n" \
"s_add_u32 m0, s50, 40960\n" \
"ds_read_b128 a[28:31], v7 offset:14336\n" \
"v_and_b32 v26, 0xffff0000, v26\n" \
"v_mul_f32 v29, v25, v27\n" \
"v_mfma_f32_16x16x32_bf16 v[236:239], a[84:87], a[116:119], v[236:239]\n" \
"buffer_load_dwordx4 v15, s[28:31], s46 offen lds\n" \
"v_mfma_f32_16x16x32_bf16 v[240:243], a[88:91], a[112:115], v[240:243]\n" \
"ds_read_b64_tr_b16 a[60:61], v11 offset:34304\n" \
"v_mul_f32 v58, v26, v28\n" \
"v_accvgpr_read_b32 v36, a204\n" \
"v_mfma_f32_16x16x32_bf16 v[244:247], a[92:95], a[112:115], v[244:247]\n" \
"v_lshlrev_b32 v30, 16, v36\n" \
"v_and_b32 v36, 0xffff0000, v36\n" \
"v_mul_f32 v25, v33, v45\n" \
"v_mfma_f32_16x16x32_bf16 v[248:251], a[88:91], a[116:119], v[248:251]\n" \
"ds_read_b64_tr_b16 a[62:63], v12 offset:34304\n" \
"v_mul_f32 v26, v39, v49\n" \
"v_fmac_f32 v25, v34, v44\n" \
"v_mfma_f32_16x16x32_bf16 v[252:255], a[92:95], a[116:119], v[252:255]\n" \
"v_fmac_f32 v26, v40, v48\n" \
"v_fmac_f32 v25, v35, v43\n" \
"v_fmac_f32 v26, v41, v47\n" \
"v_mfma_f32_16x16x32_bf16 a[128:131], a[64:67], a[120:123], a[128:131]\n" \
"v_fmac_f32 v25, v30, v42\n" \
"v_fmac_f32 v26, v36, v46\n" \
"v_accvgpr_read_b32 v28, a220\n" \
"v_mfma_f32_16x16x32_bf16 a[132:135], a[68:71], a[120:123], a[132:135]\n" \
"v_cvt_pk_bf16_f32 v26, v25, v26\n" \
"v_lshlrev_b32 v27, 16, v28\n" \
"v_lshlrev_b32 v25, 16, v26\n" \
"v_mfma_f32_16x16x32_bf16 a[136:139], a[64:67], a[124:127], a[136:139]\n" \
"s_add_u32 m0, s22, 19072\n" \
"v_and_b32 v28, 0xffff0000, v28\n" \
"v_and_b32 v26, 0xffff0000, v26\n" \
"v_mul_f32 v25, v25, v27\n" \
"v_mfma_f32_16x16x32_bf16 a[140:143], a[68:71], a[124:127], a[140:143]\n" \
"LH5_%=:\ns_cbranch_execz LH9_%=\n" \
"buffer_load_dwordx4 v21, s[32:35], s47 offen offset:384 lds\n" \
"v_mfma_f32_16x16x32_bf16 a[144:147], a[72:75], a[120:123], a[144:147]\n" \
"v_mul_f32 v26, v26, v28\n" \
"v_cvt_pk_bf16_f32 v52, v25, v29\n" \
"v_cvt_pk_bf16_f32 v56, v26, v58\n" \
"v_mfma_f32_16x16x32_bf16 a[148:151], a[76:79], a[120:123], a[148:151]\n" \
"v_mfma_f32_16x16x32_bf16 a[152:155], a[72:75], a[124:127], a[152:155]\n" \
"v_mfma_f32_16x16x32_bf16 a[156:159], a[76:79], a[124:127], a[156:159]\n" \
"s_waitcnt vmcnt(27) lgkmcnt(0)\n" \
"v_mfma_f32_16x16x32_bf16 a[160:163], a[80:83], a[120:123], a[160:163]\n" \
"s_barrier\n" \
"v_mfma_f32_16x16x32_bf16 a[164:167], a[84:87], a[120:123], a[164:167]\n" \
"v_mfma_f32_16x16x32_bf16 a[168:171], a[80:83], a[124:127], a[168:171]\n" \
"s_add_u32 m0, s50, 45056\n" \
"v_mfma_f32_16x16x32_bf16 a[172:175], a[84:87], a[124:127], a[172:175]\n" \
"buffer_load_dwordx4 v16, s[28:31], s46 offen lds\n" \
"v_mfma_f32_16x16x32_bf16 a[176:179], a[88:91], a[120:123], a[176:179]\n" \
"v_mfma_f32_16x16x32_bf16 a[180:183], a[92:95], a[120:123], a[180:183]\n" \
"v_mfma_f32_16x16x32_bf16 a[184:187], a[88:91], a[124:127], a[184:187]\n" \
"v_mfma_f32_16x16x32_bf16 a[188:191], a[92:95], a[124:127], a[188:191]\n" \
"v_mfma_f32_16x16x32_bf16 v[64:67], a[0:3], a[32:35], v[64:67]\n" \
"ds_read_b128 a[64:67], v8 offset:0\n" \
"v_mfma_f32_16x16x32_bf16 v[68:71], a[4:7], a[32:35], v[68:71]\n" \
"s_waitcnt vmcnt(6)\n" \
"v_accvgpr_read_b32 v37, a206\n" \
"v_lshlrev_b32 v31, 16, v37\n" \
"v_and_b32 v37, 0xffff0000, v37\n" \
"v_mfma_f32_16x16x32_bf16 v[72:75], a[0:3], a[36:39], v[72:75]\n" \
"s_mov_b32 s47, 2621440\n" \
"s_add_u32 m0, s22, 32768\n" \
"ds_read_b64_tr_b16 a[96:97], v9 offset:49152\n" \
"v_mul_f32 v25, v34, v45\n" \
"v_mfma_f32_16x16x32_bf16 v[76:79], a[4:7], a[36:39], v[76:79]\n" \
"buffer_load_dwordx4 v21, s[32:35], s47 offen offset:0 lds\n" \
"v_mfma_f32_16x16x32_bf16 v[80:83], a[8:11], a[32:35], v[80:83]\n" \
"ds_read_b64_tr_b16 a[98:99], v10 offset:49152\n" \
"v_mul_f32 v26, v40, v49\n" \
"v_fmac_f32 v25, v35, v44\n" \
"v_mfma_f32_16x16x32_bf16 v[84:87], a[12:15], a[32:35], v[84:87]\n" \
"v_fmac_f32 v26, v41, v48\n" \
"v_fmac_f32 v25, v30, v43\n" \
"v_fmac_f32 v26, v36, v47\n" \
"v_mfma_f32_16x16x32_bf16 v[88:91], a[8:11], a[36:39], v[88:91]\n" \
"ds_read_b128 a[68:71], v8 offset:2048\n" \
"v_fmac_f32 v25, v31, v42\n" \
"v_fmac_f32 v26, v37, v46\n" \
"v_mfma_f32_16x16x32_bf16 v[92:95], a[12:15], a[36:39], v[92:95]\n" \
"v_accvgpr_read_b32 v28, a222\n" \
"v_cvt_pk_bf16_f32 v26, v25, v26\n" \
"v_lshlrev_b32 v27, 16, v28\n" \
"v_mfma_f32_16x16x32_bf16 v[96:99], a[16:19], a[32:35], v[96:99]\n" \
"ds_read_b64_tr_b16 a[100:101], v11 offset:49152\n" \
"v_lshlrev_b32 v25, 16, v26\n" \
"v_and_b32 v28, 0xffff0000, v28\n" \
"v_mfma_f32_16x16x32_bf16 v[100:103], a[20:23], a[32:35], v[100:103]\n" \
"v_and_b32 v26, 0xffff0000, v26\n" \
"v_mul_f32 v29, v25, v27\n" \
"v_mul_f32 v58, v26, v28\n" \
"v_mfma_f32_16x16x32_bf16 v[104:107], a[16:19], a[36:39], v[104:107]\n" \
"s_mov_b32 s46, 640\n" \
"s_add_u32 m0, s50, 49152\n" \
"ds_read_b64_tr_b16 a[102:103], v12 offset:49152\n" \
"v_accvgpr_read_b32 v38, a208\n" \
"v_mfma_f32_16x16x32_bf16 v[108:111], a[20:23], a[36:39], v[108:111]\n" \
"buffer_load_dwordx4 v17, s[28:31], s46 offen lds\n" \
"v_mfma_f32_16x16x32_bf16 v[112:115], a[24:27], a[32:35], v[112:115]\n" \
"ds_read_b128 a[72:75], v8 offset:4096\n" \
"v_lshlrev_b32 v32, 16, v38\n" \
"v_and_b32 v38, 0xffff0000, v38\n" \
"v_mfma_f32_16x16x32_bf16 v[116:119], a[28:31], a[32:35], v[116:119]\n" \
"v_mul_f32 v25, v35, v45\n" \
"v_mul_f32 v26, v41, v49\n" \
"v_fmac_f32 v25, v30, v44\n" \
"v_mfma_f32_16x16x32_bf16 v[120:123], a[24:27], a[36:39], v[120:123]\n" \
"ds_read_b64_tr_b16 a[104:105], v9 offset:49664\n" \
"v_fmac_f32 v26, v36, v48\n" \
"v_fmac_f32 v25, v31, v43\n" \
"v_mfma_f32_16x16x32_bf16 v[124:127], a[28:31], a[36:39], v[124:127]\n" \
"v_fmac_f32 v26, v37, v47\n" \
"v_fmac_f32 v25, v32, v42\n" \
"v_fmac_f32 v26, v38, v46\n" \
"v_mfma_f32_16x16x32_bf16 v[128:131], a[0:3], a[40:43], v[128:131]\n" \
"ds_read_b64_tr_b16 a[106:107], v10 offset:49664\n" \
"v_accvgpr_read_b32 v28, a224\n" \
"v_cvt_pk_bf16_f32 v26, v25, v26\n" \
"v_mfma_f32_16x16x32_bf16 v[132:135], a[4:7], a[40:43], v[132:135]\n" \
"v_lshlrev_b32 v27, 16, v28\n" \
"v_lshlrev_b32 v25, 16, v26\n" \
"v_and_b32 v28, 0xffff0000, v28\n" \
"v_mfma_f32_16x16x32_bf16 v[136:139], a[0:3], a[44:47], v[136:139]\n" \
"s_add_u32 m0, s22, 33664\n" \
"ds_read_b128 a[76:79], v8 offset:6144\n" \
"v_and_b32 v26, 0xffff0000, v26\n" \
"v_mul_f32 v25, v25, v27\n" \
"v_mfma_f32_16x16x32_bf16 v[140:143], a[4:7], a[44:47], v[140:143]\n" \
"buffer_load_dwordx4 v21, s[32:35], s47 offen offset:128 lds\n" \
"v_mfma_f32_16x16x32_bf16 v[144:147], a[8:11], a[40:43], v[144:147]\n" \
"ds_read_b64_tr_b16 a[108:109], v11 offset:49664\n" \
"v_mul_f32 v26, v26, v28\n" \
"v_cvt_pk_bf16_f32 v51, v25, v29\n" \
"v_mfma_f32_16x16x32_bf16 v[148:151], a[12:15], a[40:43], v[148:151]\n" \
"v_cvt_pk_bf16_f32 v55, v26, v58\n" \
"v_accvgpr_read_b32 v39, a210\n" \
"v_lshlrev_b32 v33, 16, v39\n" \
"v_mfma_f32_16x16x32_bf16 v[152:155], a[8:11], a[44:47], v[152:155]\n" \
"ds_read_b64_tr_b16 a[110:111], v12 offset:49664\n" \
"v_and_b32 v39, 0xffff0000, v39\n" \
"v_mul_f32 v25, v30, v45\n" \
"v_mfma_f32_16x16x32_bf16 v[156:159], a[12:15], a[44:47], v[156:159]\n" \
"v_mul_f32 v26, v36, v49\n" \
"v_fmac_f32 v25, v31, v44\n" \
"v_fmac_f32 v26, v37, v48\n" \
"v_mfma_f32_16x16x32_bf16 v[160:163], a[16:19], a[40:43], v[160:163]\n" \
"ds_read_b128 a[80:83], v8 offset:8192\n" \
"v_fmac_f32 v25, v32, v43\n" \
"v_fmac_f32 v26, v38, v47\n" \
"v_mfma_f32_16x16x32_bf16 v[164:167], a[20:23], a[40:43], v[164:167]\n" \
"v_fmac_f32 v25, v33, v42\n" \
"v_fmac_f32 v26, v39, v46\n" \
"v_accvgpr_read_b32 v28, a226\n" \
"v_mfma_f32_16x16x32_bf16 v[168:171], a[16:19], a[44:47], v[168:171]\n" \
"s_add_u32 m0, s50, 53248\n" \
"ds_read_b64_tr_b16 a[112:113], v9 offset:50176\n" \
"v_cvt_pk_bf16_f32 v26, v25, v26\n" \
"v_lshlrev_b32 v27, 16, v28\n" \
"v_mfma_f32_16x16x32_bf16 v[172:175], a[20:23], a[44:47], v[172:175]\n" \
"buffer_load_dwordx4 v18, s[28:31], s46 offen lds\n" \
"v_mfma_f32_16x16x32_bf16 v[176:179], a[24:27], a[40:43], v[176:179]\n" \
"ds_read_b64_tr_b16 a[114:115], v10 offset:50176\n" \
"v_lshlrev_b32 v25, 16, v26\n" \
"v_and_b32 v28, 0xffff0000, v28\n" \
"v_mfma_f32_16x16x32_bf16 v[180:183], a[28:31], a[40:43], v[180:183]\n" \
"v_and_b32 v26, 0xffff0000, v26\n" \
"v_mul_f32 v29, v25, v27\n" \
"v_mul_f32 v58, v26, v28\n" \
"v_mfma_f32_16x16x32_bf16 v[184:187], a[24:27], a[44:47], v[184:187]\n" \
"ds_read_b128 a[84:87], v8 offset:10240\n" \
"v_accvgpr_read_b32 v40, a212\n" \
"v_lshlrev_b32 v34, 16, v40\n" \
"v_mfma_f32_16x16x32_bf16 v[188:191], a[28:31], a[44:47], v[188:191]\n" \
"v_and_b32 v40, 0xffff0000, v40\n" \
"v_mul_f32 v25, v31, v45\n" \
"v_mul_f32 v26, v37, v49\n" \
"v_mfma_f32_16x16x32_bf16 v[192:195], a[0:3], a[48:51], v[192:195]\n" \
"ds_read_b64_tr_b16 a[116:117], v11 offset:50176\n" \
"v_fmac_f32 v25, v32, v44\n" \
"v_fmac_f32 v26, v38, v48\n" \
"v_mfma_f32_16x16x32_bf16 v[196:199], a[4:7], a[48:51], v[196:199]\n" \
"v_fmac_f32 v25, v33, v43\n" \
"v_fmac_f32 v26, v39, v47\n" \
"v_fmac_f32 v25, v34, v42\n" \
"v_mfma_f32_16x16x32_bf16 v[200:203], a[0:3], a[52:55], v[200:203]\n" \
"s_add_u32 m0, s22, 34560\n" \
"ds_read_b64_tr_b16 a[118:119], v12 offset:50176\n" \
"v_fmac_f32 v26, v40, v46\n" \
"v_accvgpr_read_b32 v28, a228\n" \
"v_mfma_f32_16x16x32_bf16 v[204:207], a[4:7], a[52:55], v[204:207]\n" \
"buffer_load_dwordx4 v21, s[32:35], s47 offen offset:256 lds\n" \
"v_mfma_f32_16x16x32_bf16 v[208:211], a[8:11], a[48:51], v[208:211]\n" \
"ds_read_b128 a[88:91], v8 offset:12288\n" \
"v_cvt_pk_bf16_f32 v26, v25, v26\n" \
"v_lshlrev_b32 v27, 16, v28\n" \
"v_mfma_f32_16x16x32_bf16 v[212:215], a[12:15], a[48:51], v[212:215]\n" \
"v_lshlrev_b32 v25, 16, v26\n" \
"v_and_b32 v28, 0xffff0000, v28\n" \
"v_and_b32 v26, 0xffff0000, v26\n" \
"v_mfma_f32_16x16x32_bf16 v[216:219], a[8:11], a[52:55], v[216:219]\n" \
"ds_read_b64_tr_b16 a[120:121], v9 offset:50688\n" \
"v_mul_f32 v25, v25, v27\n" \
"v_mul_f32 v26, v26, v28\n" \
"v_mfma_f32_16x16x32_bf16 v[220:223], a[12:15], a[52:55], v[220:223]\n" \
"s_mov_b32 s94, 79691776\n" \
"v_cvt_pk_bf16_f32 v50, v25, v29\n" \
"v_cvt_pk_bf16_f32 v54, v26, v58\n" \
"v_accvgpr_read_b32 v43, a234\n" \
"v_mfma_f32_16x16x32_bf16 v[224:227], a[16:19], a[48:51], v[224:227]\n" \
"ds_read_b64_tr_b16 a[122:123], v10 offset:50688\n" \
"v_lshlrev_b32 v42, 16, v43\n" \
"v_and_b32 v43, 0xffff0000, v43\n" \
"v_mfma_f32_16x16x32_bf16 v[228:231], a[20:23], a[48:51], v[228:231]\n" \
"buffer_store_dwordx4 v[50:53], v60, s[36:39], s94 offen offset:0 sc1\n" \
"v_mfma_f32_16x16x32_bf16 v[232:235], a[16:19], a[52:55], v[232:235]\n" \
"s_add_u32 m0, s50, 57344\n" \
"ds_read_b128 a[92:95], v8 offset:14336\n" \
"s_mov_b32 s95, 79695872\n" \
"v_accvgpr_read_b32 v45, a235\n" \
"v_mfma_f32_16x16x32_bf16 v[236:239], a[20:23], a[52:55], v[236:239]\n" \
"buffer_load_dwordx4 v19, s[28:31], s46 offen lds\n" \
"v_mfma_f32_16x16x32_bf16 v[240:243], a[24:27], a[48:51], v[240:243]\n" \
"ds_read_b64_tr_b16 a[124:125], v11 offset:50688\n" \
"v_lshlrev_b32 v44, 16, v45\n" \
"v_and_b32 v45, 0xffff0000, v45\n" \
"v_mfma_f32_16x16x32_bf16 v[244:247], a[28:31], a[48:51], v[244:247]\n" \
"v_accvgpr_read_b32 v47, a236\n" \
"v_lshlrev_b32 v46, 16, v47\n" \
"v_and_b32 v47, 0xffff0000, v47\n" \
"v_mfma_f32_16x16x32_bf16 v[248:251], a[24:27], a[52:55], v[248:251]\n" \
"ds_read_b64_tr_b16 a[126:127], v12 offset:50688\n" \
"v_accvgpr_read_b32 v49, a237\n" \
"v_lshlrev_b32 v48, 16, v49\n" \
"v_mfma_f32_16x16x32_bf16 v[252:255], a[28:31], a[52:55], v[252:255]\n" \
"v_and_b32 v49, 0xffff0000, v49\n" \
"v_accvgpr_read_b32 v36, a193\n" \
"v_lshlrev_b32 v30, 16, v36\n" \
"v_mfma_f32_16x16x32_bf16 a[128:131], a[0:3], a[56:59], a[128:131]\n" \
"v_and_b32 v36, 0xffff0000, v36\n" \
"v_accvgpr_read_b32 v37, a195\n" \
"v_lshlrev_b32 v31, 16, v37\n" \
"v_mfma_f32_16x16x32_bf16 a[132:135], a[4:7], a[56:59], a[132:135]\n" \
"v_and_b32 v37, 0xffff0000, v37\n" \
"v_accvgpr_read_b32 v38, a197\n" \
"v_lshlrev_b32 v32, 16, v38\n" \
"v_mfma_f32_16x16x32_bf16 a[136:139], a[0:3], a[60:63], a[136:139]\n" \
"s_add_u32 m0, s22, 35456\n" \
"s_add_u32 s84, s84, 1048576\ns_addc_u32 s85, s85, 0\n" \
"v_and_b32 v38, 0xffff0000, v38\n" \
"v_mfma_f32_16x16x32_bf16 a[140:143], a[4:7], a[60:63], a[140:143]\n" \
"buffer_load_dwordx4 v21, s[32:35], s47 offen offset:384 lds\n" \
"v_mfma_f32_16x16x32_bf16 a[144:147], a[8:11], a[56:59], a[144:147]\n" \
"v_accvgpr_read_b32 v39, a199\n" \
"v_lshlrev_b32 v33, 16, v39\n" \
"v_and_b32 v39, 0xffff0000, v39\n" \
"v_mfma_f32_16x16x32_bf16 a[148:151], a[12:15], a[56:59], a[148:151]\n" \
"v_mul_f32 v25, v30, v45\n" \
"v_mul_f32 v26, v36, v49\n" \
"v_fmac_f32 v25, v31, v44\n" \
"v_mfma_f32_16x16x32_bf16 a[152:155], a[8:11], a[60:63], a[152:155]\n" \
"v_fmac_f32 v26, v37, v48\n" \
"v_fmac_f32 v25, v32, v43\n" \
"v_fmac_f32 v26, v38, v47\n" \
"v_mfma_f32_16x16x32_bf16 a[156:159], a[12:15], a[60:63], a[156:159]\n" \
"s_waitcnt vmcnt(20) lgkmcnt(0)\n" \
"v_fmac_f32 v25, v33, v42\n" \
"v_fmac_f32 v26, v39, v46\n" \
"v_accvgpr_read_b32 v28, a215\n" \
"v_mfma_f32_16x16x32_bf16 a[160:163], a[16:19], a[56:59], a[160:163]\n" \
"s_barrier\n" \
"v_cvt_pk_bf16_f32 v26, v25, v26\n" \
"v_lshlrev_b32 v27, 16, v28\n" \
"v_lshlrev_b32 v25, 16, v26\n" \
"v_mfma_f32_16x16x32_bf16 a[164:167], a[20:23], a[56:59], a[164:167]\n" \
"buffer_store_dwordx4 v[54:57], v60, s[36:39], s95 offen offset:0 sc1\n" \
"v_mfma_f32_16x16x32_bf16 a[168:171], a[16:19], a[60:63], a[168:171]\n" \
"s_add_u32 m0, s50, 61440\n" \
"v_and_b32 v28, 0xffff0000, v28\n" \
"v_and_b32 v26, 0xffff0000, v26\n" \
"v_mul_f32 v29, v25, v27\n" \
"v_mfma_f32_16x16x32_bf16 a[172:175], a[20:23], a[60:63], a[172:175]\n" \
"buffer_load_dwordx4 v20, s[28:31], s46 offen lds\n" \
"v_mfma_f32_16x16x32_bf16 a[176:179], a[24:27], a[56:59], a[176:179]\n" \
"v_mul_f32 v58, v26, v28\n" \
"v_accvgpr_read_b32 v40, a201\n" \
"v_lshlrev_b32 v34, 16, v40\n" \
"v_mfma_f32_16x16x32_bf16 a[180:183], a[28:31], a[56:59], a[180:183]\n" \
"v_and_b32 v40, 0xffff0000, v40\n" \
"v_mul_f32 v25, v31, v45\n" \
"v_mul_f32 v26, v37, v49\n" \
"v_mfma_f32_16x16x32_bf16 a[184:187], a[24:27], a[60:63], a[184:187]\n" \
"v_fmac_f32 v25, v32, v44\n" \
"v_fmac_f32 v26, v38, v48\n" \
"v_fmac_f32 v25, v33, v43\n" \
"v_mfma_f32_16x16x32_bf16 a[188:191], a[28:31], a[60:63], a[188:191]\n" \
"s_mov_b64 exec, 0xffff\nglobal_load_dword v22, v1, s[84:85]\ns_mov_b64 exec, -1\n" \
"v_mfma_f32_16x16x32_bf16 v[64:67], a[64:67], a[96:99], v[64:67]\n" \
"ds_read_b128 a[0:3], v5 offset:0\n" \
"v_fmac_f32 v26, v39, v47\n" \
"v_fmac_f32 v25, v34, v42\n" \
"v_mfma_f32_16x16x32_bf16 v[68:71], a[68:71], a[96:99], v[68:71]\n" \
"v_fmac_f32 v26, v40, v46\n" \
"v_accvgpr_read_b32 v28, a217\n" \
"v_cvt_pk_bf16_f32 v26, v25, v26\n" \
"v_mfma_f32_16x16x32_bf16 v[72:75], a[64:67], a[100:103], v[72:75]\n" \
"s_mov_b32 s47, 2883584\n" \
"s_add_u32 m0, s22, 49152\n" \
"ds_read_b64_tr_b16 a[32:33], v9 offset:0\n" \
"v_lshlrev_b32 v27, 16, v28\n" \
"v_mfma_f32_16x16x32_bf16 v[76:79], a[68:71], a[100:103], v[76:79]\n" \
"buffer_load_dwordx4 v21, s[32:35], s47 offen offset:0 lds\n" \
"v_mfma_f32_16x16x32_bf16 v[80:83], a[72:75], a[96:99], v[80:83]\n" \
"ds_read_b64_tr_b16 a[34:35], v10 offset:0\n" \
"v_lshlrev_b32 v25, 16, v26\n" \
"v_and_b32 v28, 0xffff0000, v28\n" \
"v_mfma_f32_16x16x32_bf16 v[84:87], a[76:79], a[96:99], v[84:87]\n" \
"v_and_b32 v26, 0xffff0000, v26\n" \
"v_mul_f32 v25, v25, v27\n" \
"v_mul_f32 v26, v26, v28\n" \
"v_mfma_f32_16x16x32_bf16 v[88:91], a[72:75], a[100:103], v[88:91]\n" \
"ds_read_b128 a[4:7], v5 offset:2048\n" \
"v_cvt_pk_bf16_f32 v53, v25, v29\n" \
"v_cvt_pk_bf16_f32 v57, v26, v58\n" \
"v_mfma_f32_16x16x32_bf16 v[92:95], a[76:79], a[100:103], v[92:95]\n" \
"v_accvgpr_read_b32 v41, a203\n" \
"v_lshlrev_b32 v35, 16, v41\n" \
"v_and_b32 v41, 0xffff0000, v41\n" \
"v_mfma_f32_16x16x32_bf16 v[96:99], a[80:83], a[96:99], v[96:99]\n" \
"ds_read_b64_tr_b16 a[36:37], v11 offset:0\n" \
"v_mul_f32 v25, v32, v45\n" \
"v_mul_f32 v26, v38, v49\n" \
"v_mfma_f32_16x16x32_bf16 v[100:103], a[84:87], a[96:99], v[100:103]\n" \
"v_fmac_f32 v25, v33, v44\n" \
"v_fmac_f32 v26, v39, v48\n" \
"v_fmac_f32 v25, v34, v43\n" \
"v_mfma_f32_16x16x32_bf16 v[104:107], a[80:83], a[100:103], v[104:107]\n" \
"s_mov_b32 s46, 768\n" \
"s_add_u32 m0, s50, 65536\n" \
"ds_read_b64_tr_b16 a[38:39], v12 offset:0\n" \
"v_fmac_f32 v26, v40, v47\n" \
"v_mfma_f32_16x16x32_bf16 v[108:111], a[84:87], a[100:103], v[108:111]\n" \
"buffer_load_dwordx4 v13, s[28:31], s46 offen lds\n" \
"v_mfma_f32_16x16x32_bf16 v[112:115], a[88:91], a[96:99], v[112:115]\n" \
"ds_read_b128 a[8:11], v5 offset:4096\n" \
"v_fmac_f32 v25, v35, v42\n" \
"v_fmac_f32 v26, v41, v46\n" \
"v_mfma_f32_16x16x32_bf16 v[116:119], a[92:95], a[96:99], v[116:119]\n" \
"v_accvgpr_read_b32 v28, a219\n" \
"v_cvt_pk_bf16_f32 v26, v25, v26\n" \
"v_lshlrev_b32 v27, 16, v28\n" \
"v_mfma_f32_16x16x32_bf16 v[120:123], a[88:91], a[100:103], v[120:123]\n" \
"ds_read_b64_tr_b16 a[40:41], v9 offset:512\n" \
"v_lshlrev_b32 v25, 16, v26\n" \
"v_and_b32 v28, 0xffff0000, v28\n" \
"v_mfma_f32_16x16x32_bf16 v[124:127], a[92:95], a[100:103], v[124:127]\n" \
"v_and_b32 v26, 0xffff0000, v26\n" \
"v_mul_f32 v29, v25, v27\n" \
"v_mul_f32 v58, v26, v28\n" \
"v_mfma_f32_16x16x32_bf16 v[128:131], a[64:67], a[104:107], v[128:131]\n" \
"ds_read_b64_tr_b16 a[42:43], v10 offset:512\n" \
"v_accvgpr_read_b32 v36, a205\n" \
"v_lshlrev_b32 v30, 16, v36\n" \
"v_mfma_f32_16x16x32_bf16 v[132:135], a[68:71], a[104:107], v[132:135]\n" \
"v_and_b32 v36, 0xffff0000, v36\n" \
"v_mul_f32 v25, v33, v45\n" \
"v_mul_f32 v26, v39, v49\n" \
"v_mfma_f32_16x16x32_bf16 v[136:139], a[64:67], a[108:111], v[136:139]\n" \
"s_add_u32 m0, s22, 50048\n" \
"ds_read_b128 a[12:15], v5 offset:6144\n" \
"v_fmac_f32 v25, v34, v44\n" \
"v_fmac_f32 v26, v40, v48\n" \
"v_mfma_f32_16x16x32_bf16 v[140:143], a[68:71], a[108:111], v[140:143]\n" \
"buffer_load_dwordx4 v21, s[32:35], s47 offen offset:128 lds\n" \
"v_mfma_f32_16x16x32_bf16 v[144:147], a[72:75], a[104:107], v[144:147]\n" \
"ds_read_b64_tr_b16 a[44:45], v11 offset:512\n" \
"v_fmac_f32 v25, v35, v43\n" \
"v_fmac_f32 v26, v41, v47\n" \
"v_mfma_f32_16x16x32_bf16 v[148:151], a[76:79], a[104:107], v[148:151]\n" \
"v_fmac_f32 v25, v30, v42\n" \
"v_fmac_f32 v26, v36, v46\n" \
"v_accvgpr_read_b32 v28, a221\n" \
"v_mfma_f32_16x16x32_bf16 v[152:155], a[72:75], a[108:111], v[152:155]\n" \
"ds_read_b64_tr_b16 a[46:47], v12 offset:512\n" \
"v_cvt_pk_bf16_f32 v26, v25, v26\n" \
"v_lshlrev_b32 v27, 16, v28\n" \
"v_mfma_f32_16x16x32_bf16 v[156:159], a[76:79], a[108:111], v[156:159]\n" \
"v_lshlrev_b32 v25, 16, v26\n" \
"v_and_b32 v28, 0xffff0000, v28\n" \
"v_and_b32 v26, 0xffff0000, v26\n" \
"v_mfma_f32_16x16x32_bf16 v[160:163], a[80:83], a[104:107], v[160:163]\n" \
"ds_read_b128 a[16:19], v5 offset:8192\n" \
"v_mul_f32 v25, v25, v27\n" \
"v_mul_f32 v26, v26, v28\n" \
"v_mfma_f32_16x16x32_bf16 v[164:167], a[84:87], a[104:107], v[164:167]\n" \
"v_cvt_pk_bf16_f32 v52, v25, v29\n" \
"v_cvt_pk_bf16_f32 v56, v26, v58\n" \
"v_accvgpr_read_b32 v37, a207\n" \
"v_mfma_f32_16x16x32_bf16 v[168:171], a[80:83], a[108:111], v[168:171]\n" \
"s_add_u32 m0, s50, 69632\n" \
"ds_read_b64_tr_b16 a[48:49], v9 offset:1024\n" \
"v_lshlrev_b32 v31, 16, v37\n" \
"v_and_b32 v37, 0xffff0000, v37\n" \
"v_mfma_f32_16x16x32_bf16 v[172:175], a[84:87], a[108:111], v[172:175]\n" \
"buffer_load_dwordx4 v14, s[28:31], s46 offen lds\n" \
"v_mfma_f32_16x16x32_bf16 v[176:179], a[88:91], a[104:107], v[176:179]\n" \
"ds_read_b64_tr_b16 a[50:51], v10 offset:1024\n" \
"v_mul_f32 v25, v34, v45\n" \
"v_mul_f32 v26, v40, v49\n" \
"v_mfma_f32_16x16x32_bf16 v[180:183], a[92:95], a[104:107], v[180:183]\n" \
"v_fmac_f32 v25, v35, v44\n" \
"v_fmac_f32 v26, v41, v48\n" \
"v_fmac_f32 v25, v30, v43\n" \
"v_mfma_f32_16x16x32_bf16 v[184:187], a[88:91], a[108:111], v[184:187]\n" \
"ds_read_b128 a[20:23], v5 offset:10240\n" \
"v_fmac_f32 v26, v36, v47\n" \
"v_fmac_f32 v25, v31, v42\n" \
"v_mfma_f32_16x16x32_bf16 v[188:191], a[92:95], a[108:111], v[188:191]\n" \
"v_fmac_f32 v26, v37, v46\n" \
"v_accvgpr_read_b32 v28, a223\n" \
"v_cvt_pk_bf16_f32 v26, v25, v26\n" \
"v_mfma_f32_16x16x32_bf16 v[192:195], a[64:67], a[112:115], v[192:195]\n" \
"ds_read_b64_tr_b16 a[52:53], v11 offset:1024\n" \
"v_lshlrev_b32 v27, 16, v28\n" \
"v_lshlrev_b32 v25, 16, v26\n" \
"v_mfma_f32_16x16x32_bf16 v[196:199], a[68:71], a[112:115], v[196:199]\n" \
"v_and_b32 v28, 0xffff0000, v28\n" \
"v_and_b32 v26, 0xffff0000, v26\n" \
"v_mul_f32 v29, v25, v27\n" \
"v_mfma_f32_16x16x32_bf16 v[200:203], a[64:67], a[116:119], v[200:203]\n" \
"s_add_u32 m0, s22, 50944\n" \
"ds_read_b64_tr_b16 a[54:55], v12 offset:1024\n" \
"v_mul_f32 v58, v26, v28\n" \
"v_accvgpr_read_b32 v38, a209\n" \
"v_mfma_f32_16x16x32_bf16 v[204:207], a[68:71], a[116:119], v[204:207]\n" \
"buffer_load_dwordx4 v21, s[32:35], s47 offen offset:256 lds\n" \
"v_mfma_f32_16x16x32_bf16 v[208:211], a[72:75], a[112:115], v[208:211]\n" \
"ds_read_b128 a[24:27], v5 offset:12288\n" \
"v_lshlrev_b32 v32, 16, v38\n" \
"v_and_b32 v38, 0xffff0000, v38\n" \
"v_mfma_f32_16x16x32_bf16 v[212:215], a[76:79], a[112:115], v[212:215]\n" \
"v_mul_f32 v25, v35, v45\n" \
"v_mul_f32 v26, v41, v49\n" \
"v_fmac_f32 v25, v30, v44\n" \
"v_mfma_f32_16x16x32_bf16 v[216:219], a[72:75], a[116:119], v[216:219]\n" \
"ds_read_b64_tr_b16 a[56:57], v9 offset:1536\n" \
"v_fmac_f32 v26, v36, v48\n" \
"v_fmac_f32 v25, v31, v43\n" \
"v_mfma_f32_16x16x32_bf16 v[220:223], a[76:79], a[116:119], v[220:223]\n" \
"v_fmac_f32 v26, v37, v47\n" \
"v_fmac_f32 v25, v32, v42\n" \
"v_fmac_f32 v26, v38, v46\n" \
"v_mfma_f32_16x16x32_bf16 v[224:227], a[80:83], a[112:115], v[224:227]\n" \
"ds_read_b64_tr_b16 a[58:59], v10 offset:1536\n" \
"v_accvgpr_read_b32 v28, a225\n" \
"v_cvt_pk_bf16_f32 v26, v25, v26\n" \
"v_mfma_f32_16x16x32_bf16 v[228:231], a[84:87], a[112:115], v[228:231]\n" \
"v_lshlrev_b32 v27, 16, v28\n" \
"v_lshlrev_b32 v25, 16, v26\n" \
"v_and_b32 v28, 0xffff0000, v28\n" \
"v_mfma_f32_16x16x32_bf16 v[232:235], a[80:83], a[116:119], v[232:235]\n" \
"s_add_u32 m0, s50, 73728\n" \
"ds_read_b128 a[28:31], v5 offset:14336\n" \
"v_and_b32 v26, 0xffff0000, v26\n" \
"v_mul_f32 v25, v25, v27\n" \
"v_mfma_f32_16x16x32_bf16 v[236:239], a[84:87], a[116:119], v[236:239]\n" \
"buffer_load_dwordx4 v15, s[28:31], s46 offen lds\n" \
"v_mfma_f32_16x16x32_bf16 v[240:243], a[88:91], a[112:115], v[240:243]\n" \
"ds_read_b64_tr_b16 a[60:61], v11 offset:1536\n" \
"v_mul_f32 v26, v26, v28\n" \
"v_cvt_pk_bf16_f32 v51, v25, v29\n" \
"v_mfma_f32_16x16x32_bf16 v[244:247], a[92:95], a[112:115], v[244:247]\n" \
"v_cvt_pk_bf16_f32 v55, v26, v58\n" \
"v_accvgpr_read_b32 v39, a211\n" \
"v_lshlrev_b32 v33, 16, v39\n" \
"v_mfma_f32_16x16x32_bf16 v[248:251], a[88:91], a[116:119], v[248:251]\n" \
"ds_read_b64_tr_b16 a[62:63], v12 offset:1536\n" \
"v_and_b32 v39, 0xffff0000, v39\n" \
"v_mul_f32 v25, v30, v45\n" \
"v_mfma_f32_16x16x32_bf16 v[252:255], a[92:95], a[116:119], v[252:255]\n" \
"v_mul_f32 v26, v36, v49\n" \
"v_fmac_f32 v25, v31, v44\n" \
"v_fmac_f32 v26, v37, v48\n" \
"v_mfma_f32_16x16x32_bf16 a[128:131], a[64:67], a[120:123], a[128:131]\n" \
"v_fmac_f32 v25, v32, v43\n" \
"v_fmac_f32 v26, v38, v47\n" \
"v_fmac_f32 v25, v33, v42\n" \
"v_mfma_f32_16x16x32_bf16 a[132:135], a[68:71], a[120:123], a[132:135]\n" \
"v_fmac_f32 v26, v39, v46\n" \
"v_accvgpr_read_b32 v28, a227\n" \
"v_cvt_pk_bf16_f32 v26, v25, v26\n" \
"v_mfma_f32_16x16x32_bf16 a[136:139], a[64:67], a[124:127], a[136:139]\n" \
"s_add_u32 m0, s22, 51840\n" \
"v_lshlrev_b32 v27, 16, v28\n" \
"v_lshlrev_b32 v25, 16, v26\n" \
"v_and_b32 v28, 0xffff0000, v28\n" \
"v_mfma_f32_16x16x32_bf16 a[140:143], a[68:71], a[124:127], a[140:143]\n" \
"buffer_load_dwordx4 v21, s[32:35], s47 offen offset:384 lds\n" \
"v_mfma_f32_16x16x32_bf16 a[144:147], a[72:75], a[120:123], a[144:147]\n" \
"v_and_b32 v26, 0xffff0000, v26\n" \
"v_mul_f32 v29, v25, v27\n" \
"v_mul_f32 v58, v26, v28\n" \
"v_mfma_f32_16x16x32_bf16 a[148:151], a[76:79], a[120:123], a[148:151]\n" \
"v_accvgpr_read_b32 v40, a213\n" \
"v_lshlrev_b32 v34, 16, v40\n" \
"v_and_b32 v40, 0xffff0000, v40\n" \
"v_mfma_f32_16x16x32_bf16 a[152:155], a[72:75], a[124:127], a[152:155]\n" \
"v_mul_f32 v25, v31, v45\n" \
"v_mul_f32 v26, v37, v49\n" \
"v_fmac_f32 v25, v32, v44\n" \
"v_mfma_f32_16x16x32_bf16 a[156:159], a[76:79], a[124:127], a[156:159]\n" \
"s_waitcnt vmcnt(19) lgkmcnt(0)\n" \
"v_fmac_f32 v26, v38, v48\n" \
"v_fmac_f32 v25, v33, v43\n" \
"v_fmac_f32 v26, v39, v47\n" \
"v_mfma_f32_16x16x32_bf16 a[160:163], a[80:83], a[120:123], a[160:163]\n" \
"s_barrier\n" \
"v_fmac_f32 v25, v34, v42\n" \
"v_fmac_f32 v26, v40, v46\n" \
"v_accvgpr_read_b32 v28, a229\n" \
"v_mfma_f32_16x16x32_bf16 a[164:167], a[84:87], a[120:123], a[164:167]\n" \
"v_cvt_pk_bf16_f32 v26, v25, v26\n" \
"v_lshlrev_b32 v27, 16, v28\n" \
"v_lshlrev_b32 v25, 16, v26\n" \
"v_mfma_f32_16x16x32_bf16 a[168:171], a[80:83], a[124:127], a[168:171]\n" \
"s_add_u32 m0, s50, 77824\n" \
"v_and_b32 v28, 0xffff0000, v28\n" \
"v_and_b32 v26, 0xffff0000, v26\n" \
"v_mul_f32 v25, v25, v27\n" \
"v_mfma_f32_16x16x32_bf16 a[172:175], a[84:87], a[124:127], a[172:175]\n" \
"buffer_load_dwordx4 v16, s[28:31], s46 offen lds\n" \
"v_mfma_f32_16x16x32_bf16 a[176:179], a[88:91], a[120:123], a[176:179]\n" \
"s_mov_b32 s94, 79699968\n" \
"v_mul_f32 v26, v26, v28\n" \
"v_cvt_pk_bf16_f32 v50, v25, v29\n" \
"v_cvt_pk_bf16_f32 v54, v26, v58\n" \
"v_mfma_f32_16x16x32_bf16 a[180:183], a[92:95], a[120:123], a[180:183]\n" \
"buffer_store_dwordx4 v[50:53], v60, s[36:39], s94 offen offset:0 sc1\n" \
"v_mfma_f32_16x16x32_bf16 a[184:187], a[88:91], a[124:127], a[184:187]\n" \
"s_mov_b32 s95, 79704064\n" \
"v_mfma_f32_16x16x32_bf16 a[188:191], a[92:95], a[124:127], a[188:191]\n" \
"s_mov_b64 exec, 0xffff\nglobal_load_dword v22, v2, s[88:89]\ns_mov_b64 exec, -1\n" \
"v_mfma_f32_16x16x32_bf16 v[64:67], a[0:3], a[32:35], v[64:67]\n" \
"ds_read_b128 a[64:67], v6 offset:0\n" \
"v_mfma_f32_16x16x32_bf16 v[68:71], a[4:7], a[32:35], v[68:71]\n" \
"v_mfma_f32_16x16x32_bf16 v[72:75], a[0:3], a[36:39], v[72:75]\n" \
"s_mov_b32 s47, 3145728\n" \
"s_add_u32 m0, s22, 0\n" \
"ds_read_b64_tr_b16 a[96:97], v9 offset:16384\n" \
"v_mfma_f32_16x16x32_bf16 v[76:79], a[4:7], a[36:39], v[76:79]\n" \
"buffer_load_dwordx4 v21, s[32:35], s47 offen offset:0 lds\n" \
"v_mfma_f32_16x16x32_bf16 v[80:83], a[8:11], a[32:35], v[80:83]\n" \
"ds_read_b64_tr_b16 a[98:99], v10 offset:16384\n" \
"v_mfma_f32_16x16x32_bf16 v[84:87], a[12:15], a[32:35], v[84:87]\n" \
"v_mfma_f32_16x16x32_bf16 v[88:91], a[8:11], a[36:39], v[88:91]\n" \
"ds_read_b128 a[68:71], v6 offset:2048\n" \
"v_mfma_f32_16x16x32_bf16 v[92:95], a[12:15], a[36:39], v[92:95]\n" \
"v_mfma_f32_16x16x32_bf16 v[96:99], a[16:19], a[32:35], v[96:99]\n" \
"ds_read_b64_tr_b16 a[100:101], v11 offset:16384\n" \
"v_mfma_f32_16x16x32_bf16 v[100:103], a[20:23], a[32:35], v[100:103]\n" \
"v_mfma_f32_16x16x32_bf16 v[104:107], a[16:19], a[36:39], v[104:107]\n" \
"s_mov_b32 s46, 768\n" \
"s_add_u32 m0, s50, 81920\n" \
"ds_read_b64_tr_b16 a[102:103], v12 offset:16384\n" \
"v_mfma_f32_16x16x32_bf16 v[108:111], a[20:23], a[36:39], v[108:111]\n" \
"buffer_load_dwordx4 v17, s[28:31], s46 offen lds\n" \
"v_mfma_f32_16x16x32_bf16 v[112:115], a[24:27], a[32:35], v[112:115]\n" \
"ds_read_b128 a[72:75], v6 offset:4096\n" \
"v_mfma_f32_16x16x32_bf16 v[116:119], a[28:31], a[32:35], v[116:119]\n" \
"buffer_store_dwordx4 v[54:57], v60, s[36:39], s95 offen offset:0 sc1\n" \
"v_mfma_f32_16x16x32_bf16 v[120:123], a[24:27], a[36:39], v[120:123]\n" \
"s_lshl_b32 s62, s8, 2\n" \
"ds_read_b64_tr_b16 a[104:105], v9 offset:16896\n" \
"s_add_u32 s62, s62, %2\n" \
"s_lshl_b32 s63, s62, 12\n" \
"v_mfma_f32_16x16x32_bf16 v[124:127], a[28:31], a[36:39], v[124:127]\n" \
"s_add_u32 s68, s10, s63\ns_addc_u32 s69, s11, 0\n" \
"s_and_b32 s69, s69, 0xffff\n" \
"s_mov_b32 s70, 4096\n" \
"v_mfma_f32_16x16x32_bf16 v[128:131], a[0:3], a[40:43], v[128:131]\n" \
"s_mov_b32 s71, 0x00020000\n" \
"ds_read_b64_tr_b16 a[106:107], v10 offset:16896\n" \
"s_add_u32 s63, s63, 79691776\n" \
"v_mfma_f32_16x16x32_bf16 v[132:135], a[4:7], a[40:43], v[132:135]\n" \
"s_add_u32 s72, s4, s63\ns_addc_u32 s73, s5, 0\n" \
"s_and_b32 s73, s73, 0xffff\n" \
"s_mov_b32 s74, 4096\n" \
"v_mfma_f32_16x16x32_bf16 v[136:139], a[0:3], a[44:47], v[136:139]\n" \
"s_add_u32 m0, s22, 896\n" \
"ds_read_b128 a[76:79], v6 offset:6144\n" \
"s_mov_b32 s75, 0x00020000\n" \
"v_add_u32 v23, -32, v4\n" \
"v_mfma_f32_16x16x32_bf16 v[140:143], a[4:7], a[44:47], v[140:143]\n" \
"buffer_load_dwordx4 v21, s[32:35], s47 offen offset:128 lds\n" \
"v_mfma_f32_16x16x32_bf16 v[144:147], a[8:11], a[40:43], v[144:147]\n" \
"ds_read_b64_tr_b16 a[108:109], v11 offset:16896\n" \
"v_add_u32 v24, 992, v4\n" \
"v_add_u32 v25, -16, v4\n" \
"v_mfma_f32_16x16x32_bf16 v[148:151], a[12:15], a[40:43], v[148:151]\n" \
"s_lshr_b32 s63, %2, 1\n" \
"v_add_u32 v26, -512, v4\n" \
"v_add_u32 v27, 512, v4\n" \
"s_lshl_b32 s63, s63, 16\n" \
"v_mfma_f32_16x16x32_bf16 v[152:155], a[8:11], a[44:47], v[152:155]\n" \
"s_lshl_b32 s56, %2, 14\n" \
"ds_read_b64_tr_b16 a[110:111], v12 offset:16896\n" \
"s_add_u32 s56, s56, s63\n" \
"v_add_u32 v55, s56, v4\n" \
"v_mfma_f32_16x16x32_bf16 v[156:159], a[12:15], a[44:47], v[156:159]\n" \
"s_lshl_b32 s62, s62, 1\n" \
"v_mov_b32 v28, 0\n" \
"v_mov_b32 v29, s62\n" \
"v_lshlrev_b32 v56, 23, v4\n" \
"v_mfma_f32_16x16x32_bf16 v[160:163], a[16:19], a[40:43], v[160:163]\n" \
"s_and_b32 s62, %1, 7\n" \
"ds_read_b128 a[80:83], v6 offset:8192\n" \
"s_lshl_b32 s62, s62, 8\n" \
"s_add_u32 s62, s62, 94380032\n" \
"v_mfma_f32_16x16x32_bf16 v[164:167], a[20:23], a[40:43], v[164:167]\n" \
"v_bfe_u32 v57, v4, 4, 5\n" \
"v_lshl_add_u32 v57, v57, 3, s62\n" \
"v_mfma_f32_16x16x32_bf16 v[168:171], a[16:19], a[44:47], v[168:171]\n" \
"s_add_u32 m0, s50, 86016\n" \
"ds_read_b64_tr_b16 a[112:113], v9 offset:17408\n" \
"v_mfma_f32_16x16x32_bf16 v[172:175], a[20:23], a[44:47], v[172:175]\n" \
"buffer_load_dwordx4 v18, s[28:31], s46 offen lds\n" \
"v_mfma_f32_16x16x32_bf16 v[176:179], a[24:27], a[40:43], v[176:179]\n" \
"ds_read_b64_tr_b16 a[114:115], v10 offset:17408\n" \
"v_mfma_f32_16x16x32_bf16 v[180:183], a[28:31], a[40:43], v[180:183]\n" \
"v_mfma_f32_16x16x32_bf16 v[184:187], a[24:27], a[44:47], v[184:187]\n" \
"ds_read_b128 a[84:87], v6 offset:10240\n" \
"v_mfma_f32_16x16x32_bf16 v[188:191], a[28:31], a[44:47], v[188:191]\n" \
"v_mfma_f32_16x16x32_bf16 v[192:195], a[0:3], a[48:51], v[192:195]\n" \
"ds_read_b64_tr_b16 a[116:117], v11 offset:17408\n" \
"v_mfma_f32_16x16x32_bf16 v[196:199], a[4:7], a[48:51], v[196:199]\n" \
"LH6_%=:\ns_cbranch_execz LH10_%=\n" \
"v_mfma_f32_16x16x32_bf16 v[200:203], a[0:3], a[52:55], v[200:203]\n" \
"s_add_u32 m0, s22, 1792\n" \
"ds_read_b64_tr_b16 a[118:119], v12 offset:17408\n" \
"v_mfma_f32_16x16x32_bf16 v[204:207], a[4:7], a[52:55], v[204:207]\n" \
"buffer_load_dwordx4 v21, s[32:35], s47 offen offset:256 lds\n" \
"v_mfma_f32_16x16x32_bf16 v[208:211], a[8:11], a[48:51], v[208:211]\n" \
"ds_read_b128 a[88:91], v6 offset:12288\n" \
"v_mfma_f32_16x16x32_bf16 v[212:215], a[12:15], a[48:51], v[212:215]\n" \
"v_mfma_f32_16x16x32_bf16 v[216:219], a[8:11], a[52:55], v[216:219]\n" \
"ds_read_b64_tr_b16 a[120:121], v9 offset:17920\n" \
"v_mfma_f32_16x16x32_bf16 v[220:223], a[12:15], a[52:55], v[220:223]\n" \
"v_mfma_f32_16x16x32_bf16 v[224:227], a[16:19], a[48:51], v[224:227]\n" \
"ds_read_b64_tr_b16 a[122:123], v10 offset:17920\n" \
"v_mfma_f32_16x16x32_bf16 v[228:231], a[20:23], a[48:51], v[228:231]\n" \
"v_mfma_f32_16x16x32_bf16 v[232:235], a[16:19], a[52:55], v[232:235]\n" \
"s_add_u32 m0, s50, 90112\n" \
"ds_read_b128 a[92:95], v6 offset:14336\n" \
"v_mfma_f32_16x16x32_bf16 v[236:239], a[20:23], a[52:55], v[236:239]\n" \
"buffer_load_dwordx4 v19, s[28:31], s46 offen lds\n" \
"v_mfma_f32_16x16x32_bf16 v[240:243], a[24:27], a[48:51], v[240:243]\n" \
"ds_read_b64_tr_b16 a[124:125], v11 offset:17920\n" \
"v_mfma_f32_16x16x32_bf16 v[244:247], a[28:31], a[48:51], v[244:247]\n" \
"v_mfma_f32_16x16x32_bf16 v[248:251], a[24:27], a[52:55], v[248:251]\n" \
"ds_read_b64_tr_b16 a[126:127], v12 offset:17920\n" \
"v_mfma_f32_16x16x32_bf16 v[252:255], a[28:31], a[52:55], v[252:255]\n" \
"v_mfma_f32_16x16x32_bf16 a[128:131], a[0:3], a[56:59], a[128:131]\n" \
"v_mfma_f32_16x16x32_bf16 a[132:135], a[4:7], a[56:59], a[132:135]\n" \
"v_mfma_f32_16x16x32_bf16 a[136:139], a[0:3], a[60:63], a[136:139]\n" \
"s_add_u32 m0, s22, 2688\n" \
"s_add_u32 s88, s88, 2048\ns_addc_u32 s89, s89, 0\n" \
"v_mfma_f32_16x16x32_bf16 a[140:143], a[4:7], a[60:63], a[140:143]\n" \
"buffer_load_dwordx4 v21, s[32:35], s47 offen offset:384 lds\n" \
"v_mfma_f32_16x16x32_bf16 a[144:147], a[8:11], a[56:59], a[144:147]\n" \
"v_mfma_f32_16x16x32_bf16 a[148:151], a[12:15], a[56:59], a[148:151]\n" \
"v_mfma_f32_16x16x32_bf16 a[152:155], a[8:11], a[60:63], a[152:155]\n" \
"v_mfma_f32_16x16x32_bf16 a[156:159], a[12:15], a[60:63], a[156:159]\n" \
"s_waitcnt vmcnt(19) lgkmcnt(0)\n" \
"v_mfma_f32_16x16x32_bf16 a[160:163], a[16:19], a[56:59], a[160:163]\n" \
"s_barrier\n" \
"v_mfma_f32_16x16x32_bf16 a[164:167], a[20:23], a[56:59], a[164:167]\n" \
"v_mfma_f32_16x16x32_bf16 a[168:171], a[16:19], a[60:63], a[168:171]\n" \
"s_add_u32 m0, s50, 94208\n" \
"v_mfma_f32_16x16x32_bf16 a[172:175], a[20:23], a[60:63], a[172:175]\n" \
"buffer_load_dwordx4 v20, s[28:31], s46 offen lds\n" \
"v_mfma_f32_16x16x32_bf16 a[176:179], a[24:27], a[56:59], a[176:179]\n" \
"v_mfma_f32_16x16x32_bf16 a[180:183], a[28:31], a[56:59], a[180:183]\n" \
"v_mfma_f32_16x16x32_bf16 a[184:187], a[24:27], a[60:63], a[184:187]\n" \
"v_mfma_f32_16x16x32_bf16 a[188:191], a[28:31], a[60:63], a[188:191]\n" \
"s_mov_b64 exec, 0xffff\nglobal_load_dword v22, v2, s[88:89]\ns_mov_b64 exec, -1\n" \
"v_mfma_f32_16x16x32_bf16 v[64:67], a[64:67], a[96:99], v[64:67]\n" \
"ds_read_b128 a[0:3], v5 offset:32768\n" \
"v_mfma_f32_16x16x32_bf16 v[68:71], a[68:71], a[96:99], v[68:71]\n" \
"v_mfma_f32_16x16x32_bf16 v[72:75], a[64:67], a[100:103], v[72:75]\n" \
"s_mov_b32 s47, 3407872\n" \
"s_add_u32 m0, s22, 16384\n" \
"ds_read_b64_tr_b16 a[32:33], v9 offset:32768\n" \
"v_mfma_f32_16x16x32_bf16 v[76:79], a[68:71], a[100:103], v[76:79]\n" \
"buffer_load_dwordx4 v21, s[32:35], s47 offen offset:0 lds\n" \
"v_mfma_f32_16x16x32_bf16 v[80:83], a[72:75], a[96:99], v[80:83]\n" \
"ds_read_b64_tr_b16 a[34:35], v10 offset:32768\n" \
"v_mfma_f32_16x16x32_bf16 v[84:87], a[76:79], a[96:99], v[84:87]\n" \
"v_mfma_f32_16x16x32_bf16 v[88:91], a[72:75], a[100:103], v[88:91]\n" \
"ds_read_b128 a[4:7], v5 offset:34816\n" \
"v_mfma_f32_16x16x32_bf16 v[92:95], a[76:79], a[100:103], v[92:95]\n" \
"v_mfma_f32_16x16x32_bf16 v[96:99], a[80:83], a[96:99], v[96:99]\n" \
"ds_read_b64_tr_b16 a[36:37], v11 offset:32768\n" \
"v_mfma_f32_16x16x32_bf16 v[100:103], a[84:87], a[96:99], v[100:103]\n" \
"v_mfma_f32_16x16x32_bf16 v[104:107], a[80:83], a[100:103], v[104:107]\n" \
"s_mov_b32 s46, 896\n" \
"s_add_u32 m0, s50, 0\n" \
"ds_read_b64_tr_b16 a[38:39], v12 offset:32768\n" \
"v_mfma_f32_16x16x32_bf16 v[108:111], a[84:87], a[100:103], v[108:111]\n" \
"buffer_load_dwordx4 v13, s[28:31], s46 offen lds\n" \
"v_mfma_f32_16x16x32_bf16 v[112:115], a[88:91], a[96:99], v[112:115]\n" \
"ds_read_b128 a[8:11], v5 offset:36864\n" \
"v_mfma_f32_16x16x32_bf16 v[116:119], a[92:95], a[96:99], v[116:119]\n" \
"v_mfma_f32_16x16x32_bf16 v[120:123], a[88:91], a[100:103], v[120:123]\n" \
"ds_read_b64_tr_b16 a[40:41], v9 offset:33280\n" \
"v_mfma_f32_16x16x32_bf16 v[124:127], a[92:95], a[100:103], v[124:127]\n" \
"v_mfma_f32_16x16x32_bf16 v[128:131], a[64:67], a[104:107], v[128:131]\n" \
"ds_read_b64_tr_b16 a[42:43], v10 offset:33280\n" \
"v_mfma_f32_16x16x32_bf16 v[132:135], a[68:71], a[104:107], v[132:135]\n" \
"v_mfma_f32_16x16x32_bf16 v[136:139], a[64:67], a[108:111], v[136:139]\n" \
"s_add_u32 m0, s22, 17280\n" \
"ds_read_b128 a[12:15], v5 offset:38912\n" \
"v_mfma_f32_16x16x32_bf16 v[140:143], a[68:71], a[108:111], v[140:143]\n" \
"buffer_load_dwordx4 v21, s[32:35], s47 offen offset:128 lds\n" \
"v_mfma_f32_16x16x32_bf16 v[144:147], a[72:75], a[104:107], v[144:147]\n" \
"ds_read_b64_tr_b16 a[44:45], v11 offset:33280\n" \
"v_mfma_f32_16x16x32_bf16 v[148:151], a[76:79], a[104:107], v[148:151]\n" \
"v_mfma_f32_16x16x32_bf16 v[152:155], a[72:75], a[108:111], v[152:155]\n" \
"ds_read_b64_tr_b16 a[46:47], v12 offset:33280\n" \
"v_mfma_f32_16x16x32_bf16 v[156:159], a[76:79], a[108:111], v[156:159]\n" \
"v_mfma_f32_16x16x32_bf16 v[160:163], a[80:83], a[104:107], v[160:163]\n" \
"ds_read_b128 a[16:19], v5 offset:40960\n" \
"v_mfma_f32_16x16x32_bf16 v[164:167], a[84:87], a[104:107], v[164:167]\n" \
"v_mfma_f32_16x16x32_bf16 v[168:171], a[80:83], a[108:111], v[168:171]\n" \
"s_add_u32 m0, s50, 4096\n" \
"ds_read_b64_tr_b16 a[48:49], v9 offset:33792\n" \
"v_mfma_f32_16x16x32_bf16 v[172:175], a[84:87], a[108:111], v[172:175]\n" \
"buffer_load_dwordx4 v14, s[28:31], s46 offen lds\n" \
"v_mfma_f32_16x16x32_bf16 v[176:179], a[88:91], a[104:107], v[176:179]\n" \
"ds_read_b64_tr_b16 a[50:51], v10 offset:33792\n" \
"v_mfma_f32_16x16x32_bf16 v[180:183], a[92:95], a[104:107], v[180:183]\n" \
"v_mfma_f32_16x16x32_bf16 v[184:187], a[88:91], a[108:111], v[184:187]\n" \
"ds_read_b128 a[20:23], v5 offset:43008\n" \
"v_mfma_f32_16x16x32_bf16 v[188:191], a[92:95], a[108:111], v[188:191]\n" \
"v_mfma_f32_16x16x32_bf16 v[192:195], a[64:67], a[112:115], v[192:195]\n" \
"ds_read_b64_tr_b16 a[52:53], v11 offset:33792\n" \
"v_mfma_f32_16x16x32_bf16 v[196:199], a[68:71], a[112:115], v[196:199]\n" \
"v_mfma_f32_16x16x32_bf16 v[200:203], a[64:67], a[116:119], v[200:203]\n" \
"s_add_u32 m0, s22, 18176\n" \
"ds_read_b64_tr_b16 a[54:55], v12 offset:33792\n" \
"v_mfma_f32_16x16x32_bf16 v[204:207], a[68:71], a[116:119], v[204:207]\n" \
"buffer_load_dwordx4 v21, s[32:35], s47 offen offset:256 lds\n" \
"v_mfma_f32_16x16x32_bf16 v[208:211], a[72:75], a[112:115], v[208:211]\n" \
"ds_read_b128 a[24:27], v5 offset:45056\n" \
"v_mfma_f32_16x16x32_bf16 v[212:215], a[76:79], a[112:115], v[212:215]\n" \
"v_mfma_f32_16x16x32_bf16 v[216:219], a[72:75], a[116:119], v[216:219]\n" \
"ds_read_b64_tr_b16 a[56:57], v9 offset:34304\n" \
"v_mfma_f32_16x16x32_bf16 v[220:223], a[76:79], a[116:119], v[220:223]\n" \
"v_mfma_f32_16x16x32_bf16 v[224:227], a[80:83], a[112:115], v[224:227]\n" \
"ds_read_b64_tr_b16 a[58:59], v10 offset:34304\n" \
"v_mfma_f32_16x16x32_bf16 v[228:231], a[84:87], a[112:115], v[228:231]\n" \
"v_mfma_f32_16x16x32_bf16 v[232:235], a[80:83], a[116:119], v[232:235]\n" \
"s_add_u32 m0, s50, 8192\n" \
"ds_read_b128 a[28:31], v5 offset:47104\n" \
"v_mfma_f32_16x16x32_bf16 v[236:239], a[84:87], a[116:119], v[236:239]\n" \
"buffer_load_dwordx4 v15, s[28:31], s46 offen lds\n" \
"v_mfma_f32_16x16x32_bf16 v[240:243], a[88:91], a[112:115], v[240:243]\n" \
"ds_read_b64_tr_b16 a[60:61], v11 offset:34304\n" \
"v_mfma_f32_16x16x32_bf16 v[244:247], a[92:95], a[112:115], v[244:247]\n" \
"v_mfma_f32_16x16x32_bf16 v[248:251], a[88:91], a[116:119], v[248:251]\n" \
"ds_read_b64_tr_b16 a[62:63], v12 offset:34304\n" \
"v_mfma_f32_16x16x32_bf16 v[252:255], a[92:95], a[116:119], v[252:255]\n" \
"v_mfma_f32_16x16x32_bf16 a[128:131], a[64:67], a[120:123], a[128:131]\n" \
"v_mfma_f32_16x16x32_bf16 a[132:135], a[68:71], a[120:123], a[132:135]\n" \
"v_mfma_f32_16x16x32_bf16 a[136:139], a[64:67], a[124:127], a[136:139]\n" \
"s_add_u32 m0, s22, 19072\n" \
"v_mfma_f32_16x16x32_bf16 a[140:143], a[68:71], a[124:127], a[140:143]\n" \
"buffer_load_dwordx4 v21, s[32:35], s47 offen offset:384 lds\n" \
"v_mfma_f32_16x16x32_bf16 a[144:147], a[72:75], a[120:123], a[144:147]\n" \
"v_mov_b32 v58, s14\n" \
"v_mfma_f32_16x16x32_bf16 a[148:151], a[76:79], a[120:123], a[148:151]\n" \
"v_mov_b32 v59, s15\n" \
"v_mfma_f32_16x16x32_bf16 a[152:155], a[72:75], a[124:127], a[152:155]\n" \
"v_mfma_f32_16x16x32_bf16 a[156:159], a[76:79], a[124:127], a[156:159]\n" \
"s_waitcnt vmcnt(14) lgkmcnt(0)\n" \
"v_mfma_f32_16x16x32_bf16 a[160:163], a[80:83], a[120:123], a[160:163]\n" \
"s_barrier\n" \
"v_mfma_f32_16x16x32_bf16 a[164:167], a[84:87], a[120:123], a[164:167]\n" \
"v_mfma_f32_16x16x32_bf16 a[168:171], a[80:83], a[124:127], a[168:171]\n" \
"s_add_u32 m0, s50, 12288\n" \
"v_mfma_f32_16x16x32_bf16 a[172:175], a[84:87], a[124:127], a[172:175]\n" \
"buffer_load_dwordx4 v16, s[28:31], s46 offen lds\n" \
"v_mfma_f32_16x16x32_bf16 a[176:179], a[88:91], a[120:123], a[176:179]\n" \
"v_mfma_f32_16x16x32_bf16 a[180:183], a[92:95], a[120:123], a[180:183]\n" \
"v_mfma_f32_16x16x32_bf16 a[184:187], a[88:91], a[124:127], a[184:187]\n" \
"v_mfma_f32_16x16x32_bf16 a[188:191], a[92:95], a[124:127], a[188:191]\n" \
"buffer_store_dwordx2 v[58:59], v56, s[36:39], s23 offen sc1\n" \
"v_mfma_f32_16x16x32_bf16 v[64:67], a[0:3], a[32:35], v[64:67]\n" \
"ds_read_b128 a[64:67], v6 offset:32768\n" \
"v_mfma_f32_16x16x32_bf16 v[68:71], a[4:7], a[32:35], v[68:71]\n" \
"v_mfma_f32_16x16x32_bf16 v[72:75], a[0:3], a[36:39], v[72:75]\n" \
"s_mov_b32 s47, 3670016\n" \
"s_add_u32 m0, s22, 32768\n" \
"ds_read_b64_tr_b16 a[96:97], v9 offset:49152\n" \
"v_mfma_f32_16x16x32_bf16 v[76:79], a[4:7], a[36:39], v[76:79]\n" \
"buffer_load_dwordx4 v21, s[32:35], s47 offen offset:0 lds\n" \
"v_mfma_f32_16x16x32_bf16 v[80:83], a[8:11], a[32:35], v[80:83]\n" \
"ds_read_b64_tr_b16 a[98:99], v10 offset:49152\n" \
"v_mfma_f32_16x16x32_bf16 v[84:87], a[12:15], a[32:35], v[84:87]\n" \
"v_mfma_f32_16x16x32_bf16 v[88:91], a[8:11], a[36:39], v[88:91]\n" \
"ds_read_b128 a[68:71], v6 offset:34816\n" \
"v_mfma_f32_16x16x32_bf16 v[92:95], a[12:15], a[36:39], v[92:95]\n" \
"buffer_load_dwordx4 v[30:33], v23, s[68:71], 0 offen offset:0\n" \
"v_mfma_f32_16x16x32_bf16 v[96:99], a[16:19], a[32:35], v[96:99]\n" \
"ds_read_b64_tr_b16 a[100:101], v11 offset:49152\n" \
"v_mfma_f32_16x16x32_bf16 v[100:103], a[20:23], a[32:35], v[100:103]\n" \
"v_mfma_f32_16x16x32_bf16 v[104:107], a[16:19], a[36:39], v[104:107]\n" \
"s_mov_b32 s46, 896\n" \
"s_add_u32 m0, s50, 16384\n" \
"ds_read_b64_tr_b16 a[102:103], v12 offset:49152\n" \
"v_mfma_f32_16x16x32_bf16 v[108:111], a[20:23], a[36:39], v[108:111]\n" \
"buffer_load_dwordx4 v17, s[28:31], s46 offen lds\n" \
"v_mfma_f32_16x16x32_bf16 v[112:115], a[24:27], a[32:35], v[112:115]\n" \
"ds_read_b128 a[72:75], v6 offset:36864\n" \
"v_mfma_f32_16x16x32_bf16 v[116:119], a[28:31], a[32:35], v[116:119]\n" \
"v_mfma_f32_16x16x32_bf16 v[120:123], a[24:27], a[36:39], v[120:123]\n" \
"ds_read_b64_tr_b16 a[104:105], v9 offset:49664\n" \
"v_mfma_f32_16x16x32_bf16 v[124:127], a[28:31], a[36:39], v[124:127]\n" \
"buffer_load_dword v50, v25, s[68:71], 0 offen offset:0\n" \
"v_mfma_f32_16x16x32_bf16 v[128:131], a[0:3], a[40:43], v[128:131]\n" \
"ds_read_b64_tr_b16 a[106:107], v10 offset:49664\n" \
"v_mfma_f32_16x16x32_bf16 v[132:135], a[4:7], a[40:43], v[132:135]\n" \
"v_mfma_f32_16x16x32_bf16 v[136:139], a[0:3], a[44:47], v[136:139]\n" \
"s_add_u32 m0, s22, 33664\n" \
"ds_read_b128 a[76:79], v6 offset:38912\n" \
"v_mfma_f32_16x16x32_bf16 v[140:143], a[4:7], a[44:47], v[140:143]\n" \
"buffer_load_dwordx4 v21, s[32:35], s47 offen offset:128 lds\n" \
"v_mfma_f32_16x16x32_bf16 v[144:147], a[8:11], a[40:43], v[144:147]\n" \
"ds_read_b64_tr_b16 a[108:109], v11 offset:49664\n" \
"v_mfma_f32_16x16x32_bf16 v[148:151], a[12:15], a[40:43], v[148:151]\n" \
"v_mfma_f32_16x16x32_bf16 v[152:155], a[8:11], a[44:47], v[152:155]\n" \
"ds_read_b64_tr_b16 a[110:111], v12 offset:49664\n" \
"v_mfma_f32_16x16x32_bf16 v[156:159], a[12:15], a[44:47], v[156:159]\n" \
"buffer_load_dwordx4 v[34:37], v24, s[68:71], 0 offen offset:0\n" \
"v_mfma_f32_16x16x32_bf16 v[160:163], a[16:19], a[40:43], v[160:163]\n" \
"ds_read_b128 a[80:83], v6 offset:40960\n" \
"v_mfma_f32_16x16x32_bf16 v[164:167], a[20:23], a[40:43], v[164:167]\n" \
"v_mfma_f32_16x16x32_bf16 v[168:171], a[16:19], a[44:47], v[168:171]\n" \
"s_add_u32 m0, s50, 20480\n" \
"ds_read_b64_tr_b16 a[112:113], v9 offset:50176\n" \
"v_mfma_f32_16x16x32_bf16 v[172:175], a[20:23], a[44:47], v[172:175]\n" \
"buffer_load_dwordx4 v18, s[28:31], s46 offen lds\n" \
"v_mfma_f32_16x16x32_bf16 v[176:179], a[24:27], a[40:43], v[176:179]\n" \
"ds_read_b64_tr_b16 a[114:115], v10 offset:50176\n" \
"v_mfma_f32_16x16x32_bf16 v[180:183], a[28:31], a[40:43], v[180:183]\n" \
"v_mfma_f32_16x16x32_bf16 v[184:187], a[24:27], a[44:47], v[184:187]\n" \
"ds_read_b128 a[84:87], v6 offset:43008\n" \
"v_mfma_f32_16x16x32_bf16 v[188:191], a[28:31], a[44:47], v[188:191]\n" \
"buffer_load_dword v51, v24, s[68:71], 0 offen offset:16\n" \
"v_mfma_f32_16x16x32_bf16 v[192:195], a[0:3], a[48:51], v[192:195]\n" \
"ds_read_b64_tr_b16 a[116:117], v11 offset:50176\n" \
"v_mfma_f32_16x16x32_bf16 v[196:199], a[4:7], a[48:51], v[196:199]\n" \
"v_mfma_f32_16x16x32_bf16 v[200:203], a[0:3], a[52:55], v[200:203]\n" \
"s_add_u32 m0, s22, 34560\n" \
"ds_read_b64_tr_b16 a[118:119], v12 offset:50176\n" \
"v_mfma_f32_16x16x32_bf16 v[204:207], a[4:7], a[52:55], v[204:207]\n" \
"buffer_load_dwordx4 v21, s[32:35], s47 offen offset:256 lds\n" \
"v_mfma_f32_16x16x32_bf16 v[208:211], a[8:11], a[48:51], v[208:211]\n" \
"ds_read_b128 a[88:91], v6 offset:45056\n" \
"v_mfma_f32_16x16x32_bf16 v[212:215], a[12:15], a[48:51], v[212:215]\n" \
"v_mfma_f32_16x16x32_bf16 v[216:219], a[8:11], a[52:55], v[216:219]\n" \
"ds_read_b64_tr_b16 a[120:121], v9 offset:50688\n" \
"v_mfma_f32_16x16x32_bf16 v[220:223], a[12:15], a[52:55], v[220:223]\n" \
"buffer_load_dwordx4 v[38:41], v24, s[68:71], 0 offen offset:1024\n" \
"v_mfma_f32_16x16x32_bf16 v[224:227], a[16:19], a[48:51], v[224:227]\n" \
"ds_read_b64_tr_b16 a[122:123], v10 offset:50688\n" \
"v_mfma_f32_16x16x32_bf16 v[228:231], a[20:23], a[48:51], v[228:231]\n" \
"v_mfma_f32_16x16x32_bf16 v[232:235], a[16:19], a[52:55], v[232:235]\n" \
"s_add_u32 m0, s50, 24576\n" \
"ds_read_b128 a[92:95], v6 offset:47104\n" \
"v_mfma_f32_16x16x32_bf16 v[236:239], a[20:23], a[52:55], v[236:239]\n" \
"buffer_load_dwordx4 v19, s[28:31], s46 offen lds\n" \
"v_mfma_f32_16x16x32_bf16 v[240:243], a[24:27], a[48:51], v[240:243]\n" \
"ds_read_b64_tr_b16 a[124:125], v11 offset:50688\n" \
"v_mfma_f32_16x16x32_bf16 v[244:247], a[28:31], a[48:51], v[244:247]\n" \
"v_mfma_f32_16x16x32_bf16 v[248:251], a[24:27], a[52:55], v[248:251]\n" \
"ds_read_b64_tr_b16 a[126:127], v12 offset:50688\n" \
"v_mfma_f32_16x16x32_bf16 v[252:255], a[28:31], a[52:55], v[252:255]\n" \
"buffer_load_dword v52, v24, s[68:71], 0 offen offset:1040\n" \
"v_mfma_f32_16x16x32_bf16 a[128:131], a[0:3], a[56:59], a[128:131]\n" \
"v_mfma_f32_16x16x32_bf16 a[132:135], a[4:7], a[56:59], a[132:135]\n" \
"v_mfma_f32_16x16x32_bf16 a[136:139], a[0:3], a[60:63], a[136:139]\n" \
"s_add_u32 m0, s22, 35456\n" \
"v_mfma_f32_16x16x32_bf16 a[140:143], a[4:7], a[60:63], a[140:143]\n" \
"buffer_load_dwordx4 v21, s[32:35], s47 offen offset:384 lds\n" \
"v_mfma_f32_16x16x32_bf16 a[144:147], a[8:11], a[56:59], a[144:147]\n" \
"v_mfma_f32_16x16x32_bf16 a[148:151], a[12:15], a[56:59], a[148:151]\n" \
"v_mfma_f32_16x16x32_bf16 a[152:155], a[8:11], a[60:63], a[152:155]\n" \
"v_mfma_f32_16x16x32_bf16 a[156:159], a[12:15], a[60:63], a[156:159]\n" \
"s_waitcnt vmcnt(23) lgkmcnt(0)\n" \
"v_mfma_f32_16x16x32_bf16 a[160:163], a[16:19], a[56:59], a[160:163]\n" \
"s_barrier\n" \
"v_mfma_f32_16x16x32_bf16 a[164:167], a[20:23], a[56:59], a[164:167]\n" \
"v_mfma_f32_16x16x32_bf16 a[168:171], a[16:19], a[60:63], a[168:171]\n" \
"s_add_u32 m0, s50, 28672\n" \
"v_mfma_f32_16x16x32_bf16 a[172:175], a[20:23], a[60:63], a[172:175]\n" \
"buffer_load_dwordx4 v20, s[28:31], s46 offen lds\n" \
"v_mfma_f32_16x16x32_bf16 a[176:179], a[24:27], a[56:59], a[176:179]\n" \
"v_mfma_f32_16x16x32_bf16 a[180:183], a[28:31], a[56:59], a[180:183]\n" \
"v_mfma_f32_16x16x32_bf16 a[184:187], a[24:27], a[60:63], a[184:187]\n" \
"v_mfma_f32_16x16x32_bf16 a[188:191], a[28:31], a[60:63], a[188:191]\n" \
"s_mov_b64 exec, 0xffff\nglobal_load_dword v22, v1, s[76:77]\ns_mov_b64 exec, -1\n" \
"v_mfma_f32_16x16x32_bf16 v[64:67], a[64:67], a[96:99], v[64:67]\n" \
"s_mov_b64 s[28:29], s[64:65]\n" \
"ds_read_b128 a[0:3], v7 offset:0\n" \
"v_mfma_f32_16x16x32_bf16 v[68:71], a[68:71], a[96:99], v[68:71]\n" \
"v_mfma_f32_16x16x32_bf16 v[72:75], a[64:67], a[100:103], v[72:75]\n" \
"s_mov_b32 s47, 3932160\n" \
"s_add_u32 m0, s22, 49152\n" \
"ds_read_b64_tr_b16 a[32:33], v9 offset:0\n" \
"v_mfma_f32_16x16x32_bf16 v[76:79], a[68:71], a[100:103], v[76:79]\n" \
"buffer_load_dwordx4 v21, s[32:35], s47 offen offset:0 lds\n" \
"v_mfma_f32_16x16x32_bf16 v[80:83], a[72:75], a[96:99], v[80:83]\n" \
"s_and_b32 s29, s29, 0xffff\n" \
"ds_read_b64_tr_b16 a[34:35], v10 offset:0\n" \
"v_mfma_f32_16x16x32_bf16 v[84:87], a[76:79], a[96:99], v[84:87]\n" \
"v_mfma_f32_16x16x32_bf16 v[88:91], a[72:75], a[100:103], v[88:91]\n" \
"s_mov_b32 s30, 4194304\n" \
"ds_read_b128 a[4:7], v7 offset:2048\n" \
"v_mfma_f32_16x16x32_bf16 v[92:95], a[76:79], a[100:103], v[92:95]\n" \
"buffer_load_dwordx4 v[42:45], v24, s[68:71], 0 offen offset:2048\n" \
"v_mfma_f32_16x16x32_bf16 v[96:99], a[80:83], a[96:99], v[96:99]\n" \
"s_lshl_b32 s60, s7, 18\n" \
"ds_read_b64_tr_b16 a[36:37], v11 offset:0\n" \
"v_mfma_f32_16x16x32_bf16 v[100:103], a[84:87], a[96:99], v[100:103]\n" \
"v_mfma_f32_16x16x32_bf16 v[104:107], a[80:83], a[100:103], v[104:107]\n" \
"s_add_u32 m0, s22, 50048\n" \
"ds_read_b64_tr_b16 a[38:39], v12 offset:0\n" \
"v_mfma_f32_16x16x32_bf16 v[108:111], a[84:87], a[100:103], v[108:111]\n" \
"buffer_load_dwordx4 v21, s[32:35], s47 offen offset:128 lds\n" \
"v_mfma_f32_16x16x32_bf16 v[112:115], a[88:91], a[96:99], v[112:115]\n" \
"s_sub_u32 s61, 3932160, s60\n" \
"ds_read_b128 a[8:11], v7 offset:4096\n" \
"v_mfma_f32_16x16x32_bf16 v[116:119], a[92:95], a[96:99], v[116:119]\n" \
"v_mfma_f32_16x16x32_bf16 v[120:123], a[88:91], a[100:103], v[120:123]\n" \
"ds_read_b64_tr_b16 a[40:41], v9 offset:512\n" \
"v_add_u32 v13, s60, v63\n" \
"v_mfma_f32_16x16x32_bf16 v[124:127], a[92:95], a[100:103], v[124:127]\n" \
"buffer_load_dword v53, v24, s[68:71], 0 offen offset:2064\n" \
"v_mfma_f32_16x16x32_bf16 v[128:131], a[64:67], a[104:107], v[128:131]\n" \
"ds_read_b64_tr_b16 a[42:43], v10 offset:512\n" \
"v_add_u32 v17, s61, v63\n" \
"v_mfma_f32_16x16x32_bf16 v[132:135], a[68:71], a[104:107], v[132:135]\n" \
"v_mfma_f32_16x16x32_bf16 v[136:139], a[64:67], a[108:111], v[136:139]\n" \
"s_add_u32 m0, s22, 50944\n" \
"ds_read_b128 a[12:15], v7 offset:6144\n" \
"v_mfma_f32_16x16x32_bf16 v[140:143], a[68:71], a[108:111], v[140:143]\n" \
"buffer_load_dwordx4 v21, s[32:35], s47 offen offset:256 lds\n" \
"v_mfma_f32_16x16x32_bf16 v[144:147], a[72:75], a[104:107], v[144:147]\n" \
"ds_read_b64_tr_b16 a[44:45], v11 offset:512\n" \
"v_add_u32 v14, 0x10000, v13\n" \
"v_mfma_f32_16x16x32_bf16 v[148:151], a[76:79], a[104:107], v[148:151]\n" \
"v_mfma_f32_16x16x32_bf16 v[152:155], a[72:75], a[108:111], v[152:155]\n" \
"ds_read_b64_tr_b16 a[46:47], v12 offset:512\n" \
"v_add_u32 v18, 0x10000, v17\n" \
"v_mfma_f32_16x16x32_bf16 v[156:159], a[76:79], a[108:111], v[156:159]\n" \
"buffer_load_dwordx4 v[46:49], v24, s[68:71], 0 offen offset:3072\n" \
"v_mfma_f32_16x16x32_bf16 v[160:163], a[80:83], a[104:107], v[160:163]\n" \
"ds_read_b128 a[16:19], v7 offset:8192\n" \
"v_add_u32 v15, 0x10000, v14\n" \
"v_mfma_f32_16x16x32_bf16 v[164:167], a[84:87], a[104:107], v[164:167]\n" \
"s_add_u32 s76, s76, 2097152\ns_addc_u32 s77, s77, 0\n" \
"v_mfma_f32_16x16x32_bf16 v[168:171], a[80:83], a[108:111], v[168:171]\n" \
"s_add_u32 m0, s22, 51840\n" \
"ds_read_b64_tr_b16 a[48:49], v9 offset:1024\n" \
"v_mfma_f32_16x16x32_bf16 v[172:175], a[84:87], a[108:111], v[172:175]\n" \
"buffer_load_dwordx4 v21, s[32:35], s47 offen offset:384 lds\n" \
"v_mfma_f32_16x16x32_bf16 v[176:179], a[88:91], a[104:107], v[176:179]\n" \
"ds_read_b64_tr_b16 a[50:51], v10 offset:1024\n" \
"v_add_u32 v19, 0x10000, v18\n" \
"v_mfma_f32_16x16x32_bf16 v[180:183], a[92:95], a[104:107], v[180:183]\n" \
"v_mfma_f32_16x16x32_bf16 v[184:187], a[88:91], a[108:111], v[184:187]\n" \
"ds_read_b128 a[20:23], v7 offset:10240\n" \
"v_add_u32 v16, 0x10000, v15\n" \
"v_mfma_f32_16x16x32_bf16 v[188:191], a[92:95], a[108:111], v[188:191]\n" \
"buffer_load_dword v54, v24, s[68:71], 0 offen offset:3088\n" \
"v_mfma_f32_16x16x32_bf16 v[192:195], a[64:67], a[112:115], v[192:195]\n" \
"ds_read_b64_tr_b16 a[52:53], v11 offset:1024\n" \
"v_add_u32 v20, 0x10000, v19\n" \
"v_mfma_f32_16x16x32_bf16 v[196:199], a[68:71], a[112:115], v[196:199]\n" \
"v_mfma_f32_16x16x32_bf16 v[200:203], a[64:67], a[116:119], v[200:203]\n" \
"ds_read_b64_tr_b16 a[54:55], v12 offset:1024\n" \
"v_mfma_f32_16x16x32_bf16 v[204:207], a[68:71], a[116:119], v[204:207]\n" \
"s_mov_b64 exec, 0xffff\nglobal_load_dword v22, v1, s[76:77]\ns_mov_b64 exec, -1\n" \
"v_mfma_f32_16x16x32_bf16 v[208:211], a[72:75], a[112:115], v[208:211]\n" \
"ds_read_b128 a[24:27], v7 offset:12288\n" \
"v_mfma_f32_16x16x32_bf16 v[212:215], a[76:79], a[112:115], v[212:215]\n" \
"v_mfma_f32_16x16x32_bf16 v[216:219], a[72:75], a[116:119], v[216:219]\n" \
"ds_read_b64_tr_b16 a[56:57], v9 offset:1536\n" \
"v_mfma_f32_16x16x32_bf16 v[220:223], a[76:79], a[116:119], v[220:223]\n" \
"global_load_short_d16_hi v28, v29, s[12:13]\n" \
"v_mfma_f32_16x16x32_bf16 v[224:227], a[80:83], a[112:115], v[224:227]\n" \
"ds_read_b64_tr_b16 a[58:59], v10 offset:1536\n" \
"v_mfma_f32_16x16x32_bf16 v[228:231], a[84:87], a[112:115], v[228:231]\n" \
"v_mfma_f32_16x16x32_bf16 v[232:235], a[80:83], a[116:119], v[232:235]\n" \
"ds_read_b128 a[28:31], v7 offset:14336\n" \
"v_mfma_f32_16x16x32_bf16 v[236:239], a[84:87], a[116:119], v[236:239]\n" \
"v_mfma_f32_16x16x32_bf16 v[240:243], a[88:91], a[112:115], v[240:243]\n" \
"ds_read_b64_tr_b16 a[60:61], v11 offset:1536\n" \
"v_mfma_f32_16x16x32_bf16 v[244:247], a[92:95], a[112:115], v[244:247]\n" \
"v_mfma_f32_16x16x32_bf16 v[248:251], a[88:91], a[116:119], v[248:251]\n" \
"ds_read_b64_tr_b16 a[62:63], v12 offset:1536\n" \
"v_mfma_f32_16x16x32_bf16 v[252:255], a[92:95], a[116:119], v[252:255]\n" \
"v_mfma_f32_16x16x32_bf16 a[128:131], a[64:67], a[120:123], a[128:131]\n" \
"v_mfma_f32_16x16x32_bf16 a[132:135], a[68:71], a[120:123], a[132:135]\n" \
"v_mfma_f32_16x16x32_bf16 a[136:139], a[64:67], a[124:127], a[136:139]\n" \
"v_mfma_f32_16x16x32_bf16 a[140:143], a[68:71], a[124:127], a[140:143]\n" \
"v_mfma_f32_16x16x32_bf16 a[144:147], a[72:75], a[120:123], a[144:147]\n" \
"v_mfma_f32_16x16x32_bf16 a[148:151], a[76:79], a[120:123], a[148:151]\n" \
"v_mfma_f32_16x16x32_bf16 a[152:155], a[72:75], a[124:127], a[152:155]\n" \
"v_mfma_f32_16x16x32_bf16 a[156:159], a[76:79], a[124:127], a[156:159]\n" \
"s_waitcnt vmcnt(27) lgkmcnt(0)\n" \
"v_mfma_f32_16x16x32_bf16 a[160:163], a[80:83], a[120:123], a[160:163]\n" \
"s_barrier\n" \
"v_mfma_f32_16x16x32_bf16 a[164:167], a[84:87], a[120:123], a[164:167]\n" \
"v_mfma_f32_16x16x32_bf16 a[168:171], a[80:83], a[124:127], a[168:171]\n" \
"v_mfma_f32_16x16x32_bf16 a[172:175], a[84:87], a[124:127], a[172:175]\n" \
"v_mfma_f32_16x16x32_bf16 a[176:179], a[88:91], a[120:123], a[176:179]\n" \
"v_mfma_f32_16x16x32_bf16 a[180:183], a[92:95], a[120:123], a[180:183]\n" \
"v_mfma_f32_16x16x32_bf16 a[184:187], a[88:91], a[124:127], a[184:187]\n" \
"v_mfma_f32_16x16x32_bf16 a[188:191], a[92:95], a[124:127], a[188:191]\n" \
"v_mfma_f32_16x16x32_bf16 v[64:67], a[0:3], a[32:35], v[64:67]\n" \
"ds_read_b128 a[64:67], v8 offset:0\n" \
"v_mfma_f32_16x16x32_bf16 v[68:71], a[4:7], a[32:35], v[68:71]\n" \
"v_mfma_f32_16x16x32_bf16 v[72:75], a[0:3], a[36:39], v[72:75]\n" \
"ds_read_b64_tr_b16 a[96:97], v9 offset:16384\n" \
"v_mfma_f32_16x16x32_bf16 v[76:79], a[4:7], a[36:39], v[76:79]\n" \
"v_mfma_f32_16x16x32_bf16 v[80:83], a[8:11], a[32:35], v[80:83]\n" \
"ds_read_b64_tr_b16 a[98:99], v10 offset:16384\n" \
"v_mfma_f32_16x16x32_bf16 v[84:87], a[12:15], a[32:35], v[84:87]\n" \
"v_mfma_f32_16x16x32_bf16 v[88:91], a[8:11], a[36:39], v[88:91]\n" \
"ds_read_b128 a[68:71], v8 offset:2048\n" \
"v_mfma_f32_16x16x32_bf16 v[92:95], a[12:15], a[36:39], v[92:95]\n" \
"v_mfma_f32_16x16x32_bf16 v[96:99], a[16:19], a[32:35], v[96:99]\n" \
"ds_read_b64_tr_b16 a[100:101], v11 offset:16384\n" \
"v_mfma_f32_16x16x32_bf16 v[100:103], a[20:23], a[32:35], v[100:103]\n" \
"v_mfma_f32_16x16x32_bf16 v[104:107], a[16:19], a[36:39], v[104:107]\n" \
"ds_read_b64_tr_b16 a[102:103], v12 offset:16384\n" \
"v_mfma_f32_16x16x32_bf16 v[108:111], a[20:23], a[36:39], v[108:111]\n" \
"v_mfma_f32_16x16x32_bf16 v[112:115], a[24:27], a[32:35], v[112:115]\n" \
"ds_read_b128 a[72:75], v8 offset:4096\n" \
"v_mfma_f32_16x16x32_bf16 v[116:119], a[28:31], a[32:35], v[116:119]\n" \
"v_mfma_f32_16x16x32_bf16 v[120:123], a[24:27], a[36:39], v[120:123]\n" \
"ds_read_b64_tr_b16 a[104:105], v9 offset:16896\n" \
"v_mfma_f32_16x16x32_bf16 v[124:127], a[28:31], a[36:39], v[124:127]\n" \
"v_mfma_f32_16x16x32_bf16 v[128:131], a[0:3], a[40:43], v[128:131]\n" \
"ds_read_b64_tr_b16 a[106:107], v10 offset:16896\n" \
"v_mfma_f32_16x16x32_bf16 v[132:135], a[4:7], a[40:43], v[132:135]\n" \
"v_mfma_f32_16x16x32_bf16 v[136:139], a[0:3], a[44:47], v[136:139]\n" \
"ds_read_b128 a[76:79], v8 offset:6144\n" \
"v_mfma_f32_16x16x32_bf16 v[140:143], a[4:7], a[44:47], v[140:143]\n" \
"buffer_load_dwordx2 v[58:59], v57, s[36:39], 0 offen sc1\n" \
"v_mfma_f32_16x16x32_bf16 v[144:147], a[8:11], a[40:43], v[144:147]\n" \
"ds_read_b64_tr_b16 a[108:109], v11 offset:16896\n" \
"v_mfma_f32_16x16x32_bf16 v[148:151], a[12:15], a[40:43], v[148:151]\n" \
"v_mfma_f32_16x16x32_bf16 v[152:155], a[8:11], a[44:47], v[152:155]\n" \
"ds_read_b64_tr_b16 a[110:111], v12 offset:16896\n" \
"v_mfma_f32_16x16x32_bf16 v[156:159], a[12:15], a[44:47], v[156:159]\n" \
"v_mfma_f32_16x16x32_bf16 v[160:163], a[16:19], a[40:43], v[160:163]\n" \
"ds_read_b128 a[80:83], v8 offset:8192\n" \
"v_mfma_f32_16x16x32_bf16 v[164:167], a[20:23], a[40:43], v[164:167]\n" \
"v_mfma_f32_16x16x32_bf16 v[168:171], a[16:19], a[44:47], v[168:171]\n" \
"ds_read_b64_tr_b16 a[112:113], v9 offset:17408\n" \
"v_mfma_f32_16x16x32_bf16 v[172:175], a[20:23], a[44:47], v[172:175]\n" \
"v_mfma_f32_16x16x32_bf16 v[176:179], a[24:27], a[40:43], v[176:179]\n" \
"ds_read_b64_tr_b16 a[114:115], v10 offset:17408\n" \
"v_mfma_f32_16x16x32_bf16 v[180:183], a[28:31], a[40:43], v[180:183]\n" \
"v_mfma_f32_16x16x32_bf16 v[184:187], a[24:27], a[44:47], v[184:187]\n" \
"ds_read_b128 a[84:87], v8 offset:10240\n" \
"v_mfma_f32_16x16x32_bf16 v[188:191], a[28:31], a[44:47], v[188:191]\n" \
"v_mfma_f32_16x16x32_bf16 v[192:195], a[0:3], a[48:51], v[192:195]\n" \
"ds_read_b64_tr_b16 a[116:117], v11 offset:17408\n" \
"v_mfma_f32_16x16x32_bf16 v[196:199], a[4:7], a[48:51], v[196:199]\n" \
"v_mfma_f32_16x16x32_bf16 v[200:203], a[0:3], a[52:55], v[200:203]\n" \
"ds_read_b64_tr_b16 a[118:119], v12 offset:17408\n" \
"v_mfma_f32_16x16x32_bf16 v[204:207], a[4:7], a[52:55], v[204:207]\n" \
"v_mfma_f32_16x16x32_bf16 v[208:211], a[8:11], a[48:51], v[208:211]\n" \
"ds_read_b128 a[88:91], v8 offset:12288\n" \
"v_mfma_f32_16x16x32_bf16 v[212:215], a[12:15], a[48:51], v[212:215]\n" \
"v_mfma_f32_16x16x32_bf16 v[216:219], a[8:11], a[52:55], v[216:219]\n" \
"ds_read_b64_tr_b16 a[120:121], v9 offset:17920\n" \
"v_mfma_f32_16x16x32_bf16 v[220:223], a[12:15], a[52:55], v[220:223]\n" \
"v_mfma_f32_16x16x32_bf16 v[224:227], a[16:19], a[48:51], v[224:227]\n" \
"ds_read_b64_tr_b16 a[122:123], v10 offset:17920\n" \
"v_mfma_f32_16x16x32_bf16 v[228:231], a[20:23], a[48:51], v[228:231]\n" \
"v_mfma_f32_16x16x32_bf16 v[232:235], a[16:19], a[52:55], v[232:235]\n" \
"ds_read_b128 a[92:95], v8 offset:14336\n" \
"v_mfma_f32_16x16x32_bf16 v[236:239], a[20:23], a[52:55], v[236:239]\n" \
"v_mfma_f32_16x16x32_bf16 v[240:243], a[24:27], a[48:51], v[240:243]\n" \
"ds_read_b64_tr_b16 a[124:125], v11 offset:17920\n" \
"v_mfma_f32_16x16x32_bf16 v[244:247], a[28:31], a[48:51], v[244:247]\n" \
"v_mfma_f32_16x16x32_bf16 v[248:251], a[24:27], a[52:55], v[248:251]\n" \
"ds_read_b64_tr_b16 a[126:127], v12 offset:17920\n" \
"v_mfma_f32_16x16x32_bf16 v[252:255], a[28:31], a[52:55], v[252:255]\n" \
"v_mfma_f32_16x16x32_bf16 a[128:131], a[0:3], a[56:59], a[128:131]\n" \
"v_mfma_f32_16x16x32_bf16 a[132:135], a[4:7], a[56:59], a[132:135]\n" \
"v_mfma_f32_16x16x32_bf16 a[136:139], a[0:3], a[60:63], a[136:139]\n" \
"s_add_u32 s76, s76, 2097152\ns_addc_u32 s77, s77, 0\n" \
"v_mfma_f32_16x16x32_bf16 a[140:143], a[4:7], a[60:63], a[140:143]\n" \
"v_mfma_f32_16x16x32_bf16 a[144:147], a[8:11], a[56:59], a[144:147]\n" \
"v_mfma_f32_16x16x32_bf16 a[148:151], a[12:15], a[56:59], a[148:151]\n" \
"v_mfma_f32_16x16x32_bf16 a[152:155], a[8:11], a[60:63], a[152:155]\n" \
"v_mfma_f32_16x16x32_bf16 a[156:159], a[12:15], a[60:63], a[156:159]\n" \
"s_waitcnt vmcnt(0) lgkmcnt(0)\n" \
"v_mfma_f32_16x16x32_bf16 a[160:163], a[16:19], a[56:59], a[160:163]\n" \
"s_barrier\n" \
"v_mfma_f32_16x16x32_bf16 a[164:167], a[20:23], a[56:59], a[164:167]\n" \
"v_cmp_ne_u64 vcc, s[14:15], v[58:59]\n" \
"v_mfma_f32_16x16x32_bf16 a[168:171], a[16:19], a[60:63], a[168:171]\n" \
"s_mov_b64 exec, 0xffff\nglobal_load_dword v22, v1, s[76:77]\ns_mov_b64 exec, -1\n" \
"v_mfma_f32_16x16x32_bf16 a[172:175], a[20:23], a[60:63], a[172:175]\n" \
"s_cbranch_vccnz LPUV_%=\nLPUVR_%=:\n" \
"v_mfma_f32_16x16x32_bf16 a[176:179], a[24:27], a[56:59], a[176:179]\n" \
"v_mfma_f32_16x16x32_bf16 a[180:183], a[28:31], a[56:59], a[180:183]\n" \
"v_mfma_f32_16x16x32_bf16 a[184:187], a[24:27], a[60:63], a[184:187]\n" \
"v_mfma_f32_16x16x32_bf16 a[188:191], a[28:31], a[60:63], a[188:191]\n" \
"v_mfma_f32_16x16x32_bf16 v[64:67], a[64:67], a[96:99], v[64:67]\n" \
"s_mov_b64 s[32:33], s[44:45]\n" \
"ds_read_b128 a[0:3], v5 offset:0\n" \
"v_mfma_f32_16x16x32_bf16 v[68:71], a[68:71], a[96:99], v[68:71]\n" \
"v_mfma_f32_16x16x32_bf16 v[72:75], a[64:67], a[100:103], v[72:75]\n" \
"s_add_u32 m0, s56, 10304\n" \
"ds_read_b64_tr_b16 a[32:33], v9 offset:32768\n" \
"v_mfma_f32_16x16x32_bf16 v[76:79], a[68:71], a[100:103], v[76:79]\n" \
"buffer_load_dwordx4 v26, s[72:75], 0 offen offset:0 sc1 lds\n" \
"v_mfma_f32_16x16x32_bf16 v[80:83], a[72:75], a[96:99], v[80:83]\n" \
"s_mov_b32 s34, s49\n" \
"ds_read_b64_tr_b16 a[34:35], v10 offset:32768\n" \
"s_waitcnt vmcnt(3)\n" \
"v_mfma_f32_16x16x32_bf16 v[84:87], a[76:79], a[96:99], v[84:87]\n" \
"v_alignbyte_b32 v30, v31, v30, 2\n" \
"v_alignbyte_b32 v31, v32, v31, 2\n" \
"v_mfma_f32_16x16x32_bf16 v[88:91], a[72:75], a[100:103], v[88:91]\n" \
"s_add_u32 m0, s56, 11328\n" \
"ds_read_b128 a[4:7], v5 offset:2048\n" \
"v_alignbyte_b32 v32, v33, v32, 2\n" \
"v_mfma_f32_16x16x32_bf16 v[92:95], a[76:79], a[100:103], v[92:95]\n" \
"buffer_load_dwordx4 v27, s[72:75], 0 offen offset:0 sc1 lds\n" \
"v_mfma_f32_16x16x32_bf16 v[96:99], a[80:83], a[96:99], v[96:99]\n" \
"ds_read_b64_tr_b16 a[36:37], v11 offset:32768\n" \
"v_alignbyte_b32 v33, v50, v33, 2\n" \
"v_mfma_f32_16x16x32_bf16 v[100:103], a[84:87], a[96:99], v[100:103]\n" \
"LH7_%=:\ns_cbranch_execz LH11_%=\n" \
"ds_write_b128 v55, v[30:33] offset:5184\n" \
"v_alignbyte_b32 v34, v35, v34, 2\n" \
"v_mfma_f32_16x16x32_bf16 v[104:107], a[80:83], a[100:103], v[104:107]\n" \
"s_add_u32 m0, s56, 11328\n" \
"ds_read_b64_tr_b16 a[38:39], v12 offset:32768\n" \
"v_alignbyte_b32 v35, v36, v35, 2\n" \
"v_mfma_f32_16x16x32_bf16 v[108:111], a[84:87], a[100:103], v[108:111]\n" \
"buffer_load_dwordx4 v27, s[72:75], 0 offen offset:1024 sc1 lds\n" \
"v_mfma_f32_16x16x32_bf16 v[112:115], a[88:91], a[96:99], v[112:115]\n" \
"ds_read_b128 a[8:11], v5 offset:4096\n" \
"v_alignbyte_b32 v36, v37, v36, 2\n" \
"v_mfma_f32_16x16x32_bf16 v[116:119], a[92:95], a[96:99], v[116:119]\n" \
"v_alignbyte_b32 v37, v51, v37, 2\n" \
"ds_write_b128 v55, v[34:37] offset:6208\n" \
"v_mfma_f32_16x16x32_bf16 v[120:123], a[88:91], a[100:103], v[120:123]\n" \
"s_add_u32 m0, s56, 11328\n" \
"ds_read_b64_tr_b16 a[40:41], v9 offset:33280\n" \
"v_alignbyte_b32 v38, v39, v38, 2\n" \
"v_mfma_f32_16x16x32_bf16 v[124:127], a[92:95], a[100:103], v[124:127]\n" \
"buffer_load_dwordx4 v27, s[72:75], 0 offen offset:2048 sc1 lds\n" \
"v_mfma_f32_16x16x32_bf16 v[128:131], a[64:67], a[104:107], v[128:131]\n" \
"ds_read_b64_tr_b16 a[42:43], v10 offset:33280\n" \
"v_alignbyte_b32 v39, v40, v39, 2\n" \
"v_mfma_f32_16x16x32_bf16 v[132:135], a[68:71], a[104:107], v[132:135]\n" \
"v_alignbyte_b32 v40, v41, v40, 2\n" \
"v_alignbyte_b32 v41, v52, v41, 2\n" \
"v_mfma_f32_16x16x32_bf16 v[136:139], a[64:67], a[108:111], v[136:139]\n" \
"s_add_u32 m0, s56, 11328\n" \
"ds_read_b128 a[12:15], v5 offset:6144\n" \
"v_mfma_f32_16x16x32_bf16 v[140:143], a[68:71], a[108:111], v[140:143]\n" \
"buffer_load_dwordx4 v27, s[72:75], 0 offen offset:3072 sc1 lds\n" \
"v_mfma_f32_16x16x32_bf16 v[144:147], a[72:75], a[104:107], v[144:147]\n" \
"ds_read_b64_tr_b16 a[44:45], v11 offset:33280\n" \
"v_mfma_f32_16x16x32_bf16 v[148:151], a[76:79], a[104:107], v[148:151]\n" \
"ds_write_b128 v55, v[38:41] offset:7232\n" \
"v_alignbyte_b32 v42, v43, v42, 2\n" \
"v_mfma_f32_16x16x32_bf16 v[152:155], a[72:75], a[108:111], v[152:155]\n" \
"s_add_u32 m0, s56, 0\n" \
"ds_read_b64_tr_b16 a[46:47], v12 offset:33280\n" \
"v_alignbyte_b32 v43, v44, v43, 2\n" \
"v_mfma_f32_16x16x32_bf16 v[156:159], a[76:79], a[108:111], v[156:159]\n" \
"buffer_load_dwordx4 v23, s[68:71], 0 offen offset:0 lds\n" \
"v_mfma_f32_16x16x32_bf16 v[160:163], a[80:83], a[104:107], v[160:163]\n" \
"ds_read_b128 a[16:19], v5 offset:8192\n" \
"v_alignbyte_b32 v44, v45, v44, 2\n" \
"v_mfma_f32_16x16x32_bf16 v[164:167], a[84:87], a[104:107], v[164:167]\n" \
"v_alignbyte_b32 v45, v53, v45, 2\n" \
"ds_write_b128 v55, v[42:45] offset:8256\n" \
"v_mfma_f32_16x16x32_bf16 v[168:171], a[80:83], a[108:111], v[168:171]\n" \
"s_add_u32 m0, s56, 1024\n" \
"ds_read_b64_tr_b16 a[48:49], v9 offset:33792\n" \
"v_alignbyte_b32 v46, v47, v46, 2\n" \
"v_mfma_f32_16x16x32_bf16 v[172:175], a[84:87], a[108:111], v[172:175]\n" \
"buffer_load_dwordx4 v24, s[68:71], 0 offen offset:0 lds\n" \
"v_mfma_f32_16x16x32_bf16 v[176:179], a[88:91], a[104:107], v[176:179]\n" \
"ds_read_b64_tr_b16 a[50:51], v10 offset:33792\n" \
"v_alignbyte_b32 v47, v48, v47, 2\n" \
"v_mfma_f32_16x16x32_bf16 v[180:183], a[92:95], a[104:107], v[180:183]\n" \
"v_alignbyte_b32 v48, v49, v48, 2\n" \
"v_alignbyte_b32 v49, v54, v49, 2\n" \
"v_mfma_f32_16x16x32_bf16 v[184:187], a[88:91], a[108:111], v[184:187]\n" \
"s_add_u32 m0, s56, 1024\n" \
"ds_read_b128 a[20:23], v5 offset:10240\n" \
"v_mfma_f32_16x16x32_bf16 v[188:191], a[92:95], a[108:111], v[188:191]\n" \
"buffer_load_dwordx4 v24, s[68:71], 0 offen offset:1024 lds\n" \
"v_mfma_f32_16x16x32_bf16 v[192:195], a[64:67], a[112:115], v[192:195]\n" \
"ds_read_b64_tr_b16 a[52:53], v11 offset:33792\n" \
"v_mfma_f32_16x16x32_bf16 v[196:199], a[68:71], a[112:115], v[196:199]\n" \
"ds_write_b128 v55, v[46:49] offset:9280\n" \
"v_lshrrev_b32 v30, 4, v4\n" \
"v_mfma_f32_16x16x32_bf16 v[200:203], a[64:67], a[116:119], v[200:203]\n" \
"s_add_u32 m0, s56, 1024\n" \
"ds_read_b64_tr_b16 a[54:55], v12 offset:33792\n" \
"v_and_b32 v31, 15, v30\n" \
"v_mfma_f32_16x16x32_bf16 v[204:207], a[68:71], a[116:119], v[204:207]\n" \
"buffer_load_dwordx4 v24, s[68:71], 0 offen offset:2048 lds\n" \
"v_mfma_f32_16x16x32_bf16 v[208:211], a[72:75], a[112:115], v[208:211]\n" \
"ds_read_b128 a[24:27], v5 offset:12288\n" \
"v_lshrrev_b32 v32, 4, v30\n" \
"v_mfma_f32_16x16x32_bf16 v[212:215], a[76:79], a[112:115], v[212:215]\n" \
"v_add_u32 v33, 1, v31\n" \
"v_and_b32 v34, 1, v33\n" \
"v_mfma_f32_16x16x32_bf16 v[216:219], a[72:75], a[116:119], v[216:219]\n" \
"s_add_u32 m0, s56, 1024\n" \
"ds_read_b64_tr_b16 a[56:57], v9 offset:34304\n" \
"v_and_b32 v33, 30, v33\n" \
"v_mfma_f32_16x16x32_bf16 v[220:223], a[76:79], a[116:119], v[220:223]\n" \
"buffer_load_dwordx4 v24, s[68:71], 0 offen offset:3072 lds\n" \
"v_mfma_f32_16x16x32_bf16 v[224:227], a[80:83], a[112:115], v[224:227]\n" \
"ds_read_b64_tr_b16 a[58:59], v10 offset:34304\n" \
"v_mul_u32_u24 v34, 5184, v34\n" \
"v_mfma_f32_16x16x32_bf16 v[228:231], a[84:87], a[112:115], v[228:231]\n" \
"v_lshl_add_u32 v33, v33, 1, v34\n" \
"v_lshl_add_u32 v33, v32, 4, v33\n" \
"v_mfma_f32_16x16x32_bf16 v[232:235], a[80:83], a[116:119], v[232:235]\n" \
"s_mov_b32 s46, 0\n" \
"s_add_u32 m0, s50, 65536\n" \
"ds_read_b128 a[28:31], v5 offset:14336\n" \
"v_mfma_f32_16x16x32_bf16 v[236:239], a[84:87], a[116:119], v[236:239]\n" \
"buffer_load_dwordx4 v13, s[28:31], s46 offen lds\n" \
"v_mfma_f32_16x16x32_bf16 v[240:243], a[88:91], a[112:115], v[240:243]\n" \
"ds_read_b64_tr_b16 a[60:61], v11 offset:34304\n" \
"v_add_u32 v46, s56, v33\n" \
"v_mfma_f32_16x16x32_bf16 v[244:247], a[92:95], a[112:115], v[244:247]\n" \
"v_add_u32 v47, 1024, v46\n" \
"v_add_u32 v48, 2048, v46\n" \
"v_mfma_f32_16x16x32_bf16 v[248:251], a[88:91], a[116:119], v[248:251]\n" \
"s_add_u32 m0, s50, 69632\n" \
"ds_read_b64_tr_b16 a[62:63], v12 offset:34304\n" \
"v_add_u32 v49, 3072, v46\n" \
"v_mfma_f32_16x16x32_bf16 v[252:255], a[92:95], a[116:119], v[252:255]\n" \
"buffer_load_dwordx4 v14, s[28:31], s46 offen lds\n" \
"v_mfma_f32_16x16x32_bf16 a[128:131], a[64:67], a[120:123], a[128:131]\n" \
"v_lshlrev_b32 v33, 1, v31\n" \
"v_sub_u32 v33, v33, v32\n" \
"v_mfma_f32_16x16x32_bf16 a[132:135], a[68:71], a[120:123], a[132:135]\n" \
"v_lshlrev_b32 v33, 4, v33\n" \
"v_add_u32 v33, 10320, v33\n" \
"v_mfma_f32_16x16x32_bf16 a[136:139], a[64:67], a[124:127], a[136:139]\n" \
"s_add_u32 m0, s50, 73728\n" \
"v_add_u32 v50, s56, v33\n" \
"v_mfma_f32_16x16x32_bf16 a[140:143], a[68:71], a[124:127], a[140:143]\n" \
"buffer_load_dwordx4 v15, s[28:31], s46 offen lds\n" \
"v_mfma_f32_16x16x32_bf16 a[144:147], a[72:75], a[120:123], a[144:147]\n" \
"v_lshlrev_b32 v33, 5, v31\n" \
"v_lshl_add_u32 v55, v32, 3, v33\n" \
"v_mfma_f32_16x16x32_bf16 a[148:151], a[76:79], a[120:123], a[148:151]\n" \
"v_add_u32 v55, s22, v55\n" \
"v_add_u32 v33, s56, v33\n" \
"v_mfma_f32_16x16x32_bf16 a[152:155], a[72:75], a[124:127], a[152:155]\n" \
"v_xor_b32 v32, 1, v32\n" \
"v_mfma_f32_16x16x32_bf16 a[156:159], a[76:79], a[124:127], a[156:159]\n" \
"s_waitcnt vmcnt(18) lgkmcnt(0)\n" \
"v_lshl_add_u32 v51, v32, 3, v33\n" \
"v_mfma_f32_16x16x32_bf16 a[160:163], a[80:83], a[120:123], a[160:163]\n" \
"s_barrier\n" \
"v_mfma_f32_16x16x32_bf16 a[164:167], a[84:87], a[120:123], a[164:167]\n" \
"v_mfma_f32_16x16x32_bf16 a[168:171], a[80:83], a[124:127], a[168:171]\n" \
"s_add_u32 m0, s50, 77824\n" \
"v_mfma_f32_16x16x32_bf16 a[172:175], a[84:87], a[124:127], a[172:175]\n" \
"buffer_load_dwordx4 v16, s[28:31], s46 offen lds\n" \
"v_mfma_f32_16x16x32_bf16 a[176:179], a[88:91], a[120:123], a[176:179]\n" \
"v_mfma_f32_16x16x32_bf16 a[180:183], a[92:95], a[120:123], a[180:183]\n" \
"v_mfma_f32_16x16x32_bf16 a[184:187], a[88:91], a[124:127], a[184:187]\n" \
"v_mfma_f32_16x16x32_bf16 a[188:191], a[92:95], a[124:127], a[188:191]\n" \
"v_mfma_f32_16x16x32_bf16 v[64:67], a[0:3], a[32:35], v[64:67]\n" \
"ds_read_b128 a[64:67], v6 offset:0\n" \
"v_mfma_f32_16x16x32_bf16 v[68:71], a[4:7], a[32:35], v[68:71]\n" \
"v_mfma_f32_16x16x32_bf16 v[72:75], a[0:3], a[36:39], v[72:75]\n" \
"s_mov_b32 s46, 0\n" \
"s_add_u32 m0, s50, 81920\n" \
"ds_read_b64_tr_b16 a[96:97], v9 offset:49152\n" \
"v_mfma_f32_16x16x32_bf16 v[76:79], a[4:7], a[36:39], v[76:79]\n" \
"buffer_load_dwordx4 v17, s[28:31], s46 offen lds\n" \
"v_mfma_f32_16x16x32_bf16 v[80:83], a[8:11], a[32:35], v[80:83]\n" \
"ds_read_b64_tr_b16 a[98:99], v10 offset:49152\n" \
"v_mfma_f32_16x16x32_bf16 v[84:87], a[12:15], a[32:35], v[84:87]\n" \
"v_mfma_f32_16x16x32_bf16 v[88:91], a[8:11], a[36:39], v[88:91]\n" \
"ds_read_b128 a[68:71], v6 offset:2048\n" \
"v_mfma_f32_16x16x32_bf16 v[92:95], a[12:15], a[36:39], v[92:95]\n" \
"v_mfma_f32_16x16x32_bf16 v[96:99], a[16:19], a[32:35], v[96:99]\n" \
"ds_read_b64_tr_b16 a[100:101], v11 offset:49152\n" \
"v_mfma_f32_16x16x32_bf16 v[100:103], a[20:23], a[32:35], v[100:103]\n" \
"v_mfma_f32_16x16x32_bf16 v[104:107], a[16:19], a[36:39], v[104:107]\n" \
"s_add_u32 m0, s50, 86016\n" \
"ds_read_b64_tr_b16 a[102:103], v12 offset:49152\n" \
"v_mfma_f32_16x16x32_bf16 v[108:111], a[20:23], a[36:39], v[108:111]\n" \
"buffer_load_dwordx4 v18, s[28:31], s46 offen lds\n" \
"v_mfma_f32_16x16x32_bf16 v[112:115], a[24:27], a[32:35], v[112:115]\n" \
"ds_read_b128 a[72:75], v6 offset:4096\n" \
"v_mfma_f32_16x16x32_bf16 v[116:119], a[28:31], a[32:35], v[116:119]\n" \
"v_mfma_f32_16x16x32_bf16 v[120:123], a[24:27], a[36:39], v[120:123]\n" \
"ds_read_b64_tr_b16 a[104:105], v9 offset:49664\n" \
"v_mfma_f32_16x16x32_bf16 v[124:127], a[28:31], a[36:39], v[124:127]\n" \
"v_mfma_f32_16x16x32_bf16 v[128:131], a[0:3], a[40:43], v[128:131]\n" \
"ds_read_b64_tr_b16 a[106:107], v10 offset:49664\n" \
"v_mfma_f32_16x16x32_bf16 v[132:135], a[4:7], a[40:43], v[132:135]\n" \
"v_mfma_f32_16x16x32_bf16 v[136:139], a[0:3], a[44:47], v[136:139]\n" \
"s_add_u32 m0, s50, 90112\n" \
"ds_read_b128 a[76:79], v6 offset:6144\n" \
"v_mfma_f32_16x16x32_bf16 v[140:143], a[4:7], a[44:47], v[140:143]\n" \
"buffer_load_dwordx4 v19, s[28:31], s46 offen lds\n" \
"v_mfma_f32_16x16x32_bf16 v[144:147], a[8:11], a[40:43], v[144:147]\n" \
"ds_read_b64_tr_b16 a[108:109], v11 offset:49664\n" \
"v_mfma_f32_16x16x32_bf16 v[148:151], a[12:15], a[40:43], v[148:151]\n" \
"v_mfma_f32_16x16x32_bf16 v[152:155], a[8:11], a[44:47], v[152:155]\n" \
"ds_read_b64_tr_b16 a[110:111], v12 offset:49664\n" \
"v_mfma_f32_16x16x32_bf16 v[156:159], a[12:15], a[44:47], v[156:159]\n" \
"v_mfma_f32_16x16x32_bf16 v[160:163], a[16:19], a[40:43], v[160:163]\n" \
"s_add_u32 s76, s76, 2097152\ns_addc_u32 s77, s77, 0\n" \
"ds_read_b128 a[80:83], v6 offset:8192\n" \
"v_mfma_f32_16x16x32_bf16 v[164:167], a[20:23], a[40:43], v[164:167]\n" \
"v_mfma_f32_16x16x32_bf16 v[168:171], a[16:19], a[44:47], v[168:171]\n" \
"s_add_u32 m0, s50, 94208\n" \
"ds_read_b64_tr_b16 a[112:113], v9 offset:50176\n" \
"v_mfma_f32_16x16x32_bf16 v[172:175], a[20:23], a[44:47], v[172:175]\n" \
"buffer_load_dwordx4 v20, s[28:31], s46 offen lds\n" \
"v_mfma_f32_16x16x32_bf16 v[176:179], a[24:27], a[40:43], v[176:179]\n" \
"ds_read_b64_tr_b16 a[114:115], v10 offset:50176\n" \
"v_mfma_f32_16x16x32_bf16 v[180:183], a[28:31], a[40:43], v[180:183]\n" \
"v_mfma_f32_16x16x32_bf16 v[184:187], a[24:27], a[44:47], v[184:187]\n" \
"ds_read_b128 a[84:87], v6 offset:10240\n" \
"v_mfma_f32_16x16x32_bf16 v[188:191], a[28:31], a[44:47], v[188:191]\n" \
"v_mfma_f32_16x16x32_bf16 v[192:195], a[0:3], a[48:51], v[192:195]\n" \
"ds_read_b64_tr_b16 a[116:117], v11 offset:50176\n" \
"v_mfma_f32_16x16x32_bf16 v[196:199], a[4:7], a[48:51], v[196:199]\n" \
"v_mfma_f32_16x16x32_bf16 v[200:203], a[0:3], a[52:55], v[200:203]\n" \
"s_mov_b32 s47, 0\n" \
"s_add_u32 m0, s51, 32768\n" \
"ds_read_b64_tr_b16 a[118:119], v12 offset:50176\n" \
"v_mfma_f32_16x16x32_bf16 v[204:207], a[4:7], a[52:55], v[204:207]\n" \
"buffer_load_dwordx4 v21, s[32:35], s47 offen offset:0 lds\n" \
"v_mfma_f32_16x16x32_bf16 v[208:211], a[8:11], a[48:51], v[208:211]\n" \
"ds_read_b128 a[88:91], v6 offset:12288\n" \
"v_mfma_f32_16x16x32_bf16 v[212:215], a[12:15], a[48:51], v[212:215]\n" \
"v_mfma_f32_16x16x32_bf16 v[216:219], a[8:11], a[52:55], v[216:219]\n" \
"ds_read_b64_tr_b16 a[120:121], v9 offset:50688\n" \
"v_mfma_f32_16x16x32_bf16 v[220:223], a[12:15], a[52:55], v[220:223]\n" \
"s_mov_b64 exec, 0xffff\nglobal_load_dword v22, v1, s[76:77]\ns_mov_b64 exec, -1\n" \
"v_mfma_f32_16x16x32_bf16 v[224:227], a[16:19], a[48:51], v[224:227]\n" \
"ds_read_b64_tr_b16 a[122:123], v10 offset:50688\n" \
"v_mfma_f32_16x16x32_bf16 v[228:231], a[20:23], a[48:51], v[228:231]\n" \
"v_mfma_f32_16x16x32_bf16 v[232:235], a[16:19], a[52:55], v[232:235]\n" \
"s_add_u32 m0, s51, 33664\n" \
"ds_read_b128 a[92:95], v6 offset:14336\n" \
"v_mfma_f32_16x16x32_bf16 v[236:239], a[20:23], a[52:55], v[236:239]\n" \
"buffer_load_dwordx4 v21, s[32:35], s47 offen offset:128 lds\n" \
"v_mfma_f32_16x16x32_bf16 v[240:243], a[24:27], a[48:51], v[240:243]\n" \
"ds_read_b64_tr_b16 a[124:125], v11 offset:50688\n" \
"v_mfma_f32_16x16x32_bf16 v[244:247], a[28:31], a[48:51], v[244:247]\n" \
"v_mfma_f32_16x16x32_bf16 v[248:251], a[24:27], a[52:55], v[248:251]\n" \
"ds_read_b64_tr_b16 a[126:127], v12 offset:50688\n" \
"v_mfma_f32_16x16x32_bf16 v[252:255], a[28:31], a[52:55], v[252:255]\n" \
"v_add_u32 v5, s54, v5\n" \
"v_mfma_f32_16x16x32_bf16 a[128:131], a[0:3], a[56:59], a[128:131]\n" \
"v_add_u32 v6, s54, v6\n" \
"v_mfma_f32_16x16x32_bf16 a[132:135], a[4:7], a[56:59], a[132:135]\n" \
"v_add_u32 v7, s54, v7\n" \
"v_mfma_f32_16x16x32_bf16 a[136:139], a[0:3], a[60:63], a[136:139]\n" \
"s_mov_b32 s47, 262144\n" \
"s_add_u32 m0, s51, 40960\n" \
"v_mfma_f32_16x16x32_bf16 a[140:143], a[4:7], a[60:63], a[140:143]\n" \
"buffer_load_dwordx4 v21, s[32:35], s47 offen offset:0 lds\n" \
"v_mfma_f32_16x16x32_bf16 a[144:147], a[8:11], a[56:59], a[144:147]\n" \
"v_add_u32 v8, s54, v8\n" \
"v_mfma_f32_16x16x32_bf16 a[148:151], a[12:15], a[56:59], a[148:151]\n" \
"v_sub_u32 v9, v9, v62\n" \
"v_mfma_f32_16x16x32_bf16 a[152:155], a[8:11], a[60:63], a[152:155]\n" \
"v_sub_u32 v10, v10, v62\n" \
"v_mfma_f32_16x16x32_bf16 a[156:159], a[12:15], a[60:63], a[156:159]\n" \
"s_waitcnt lgkmcnt(0)\n" \
"v_mfma_f32_16x16x32_bf16 a[160:163], a[16:19], a[56:59], a[160:163]\n" \
"s_barrier\n" \
"v_mfma_f32_16x16x32_bf16 a[164:167], a[20:23], a[56:59], a[164:167]\n" \
"v_sub_u32 v11, v11, v62\n" \
"v_mfma_f32_16x16x32_bf16 a[168:171], a[16:19], a[60:63], a[168:171]\n" \
"s_add_u32 m0, s51, 41856\n" \
"v_mfma_f32_16x16x32_bf16 a[172:175], a[20:23], a[60:63], a[172:175]\n" \
"buffer_load_dwordx4 v21, s[32:35], s47 offen offset:128 lds\n" \
"v_mfma_f32_16x16x32_bf16 a[176:179], a[24:27], a[56:59], a[176:179]\n" \
"v_sub_u32 v12, v12, v62\n" \
"v_mfma_f32_16x16x32_bf16 a[180:183], a[28:31], a[56:59], a[180:183]\n" \
"v_mfma_f32_16x16x32_bf16 a[184:187], a[24:27], a[60:63], a[184:187]\n" \
"v_mfma_f32_16x16x32_bf16 a[188:191], a[28:31], a[60:63], a[188:191]\n" \
"v_mfma_f32_16x16x32_bf16 v[64:67], a[64:67], a[96:99], v[64:67]\n" \
"s_waitcnt vmcnt(13) lgkmcnt(0)\n" \
"v_mfma_f32_16x16x32_bf16 v[68:71], a[68:71], a[96:99], v[68:71]\n" \
"v_mfma_f32_16x16x32_bf16 v[72:75], a[64:67], a[100:103], v[72:75]\n" \
"s_mov_b32 s46, 128\n" \
"s_add_u32 m0, s50, 0\n" \
"ds_read_b128 a[32:35], v50 offset:512\n" \
"v_mfma_f32_16x16x32_bf16 v[76:79], a[68:71], a[100:103], v[76:79]\n" \
"buffer_load_dwordx4 v13, s[28:31], s46 offen lds\n" \
"v_mfma_f32_16x16x32_bf16 v[80:83], a[72:75], a[96:99], v[80:83]\n" \
"s_mov_b32 s96, s57\n" \
"ds_read2_b32 a[0:1], v46 offset0:0 offset1:1\n" \
"v_mul_f32 v64, s20, v64\n" \
"v_mfma_f32_16x16x32_bf16 v[84:87], a[76:79], a[96:99], v[84:87]\n" \
"v_mul_f32 v65, s20, v65\n" \
"v_mul_f32 v66, s20, v66\n" \
"v_mfma_f32_16x16x32_bf16 v[88:91], a[72:75], a[100:103], v[88:91]\n" \
"ds_read2_b32 a[2:3], v46 offset0:2 offset1:3\n" \
"v_mul_f32 v67, s20, v67\n" \
"v_mfma_f32_16x16x32_bf16 v[92:95], a[76:79], a[100:103], v[92:95]\n" \
"v_cvt_pk_bf16_f32 v30, v64, v65\n" \
"v_cvt_pk_bf16_f32 v31, v66, v67\n" \
"v_mfma_f32_16x16x32_bf16 v[96:99], a[80:83], a[96:99], v[96:99]\n" \
"ds_read_b128 a[36:39], v50 offset:1024\n" \
"v_mul_f32 v68, s20, v68\n" \
"v_mfma_f32_16x16x32_bf16 v[100:103], a[84:87], a[96:99], v[100:103]\n" \
"v_mul_f32 v69, s20, v69\n" \
"v_mul_f32 v70, s20, v70\n" \
"v_mfma_f32_16x16x32_bf16 v[104:107], a[80:83], a[100:103], v[104:107]\n" \
"s_add_u32 m0, s50, 4096\n" \
"ds_read2_b32 a[4:5], v46 offset0:128 offset1:129\n" \
"v_mul_f32 v71, s20, v71\n" \
"v_mfma_f32_16x16x32_bf16 v[108:111], a[84:87], a[100:103], v[108:111]\n" \
"buffer_load_dwordx4 v14, s[28:31], s46 offen lds\n" \
"v_mfma_f32_16x16x32_bf16 v[112:115], a[88:91], a[96:99], v[112:115]\n" \
"ds_read2_b32 a[6:7], v46 offset0:130 offset1:131\n" \
"v_cvt_pk_bf16_f32 v32, v68, v69\n" \
"v_mfma_f32_16x16x32_bf16 v[116:119], a[92:95], a[96:99], v[116:119]\n" \
"v_cvt_pk_bf16_f32 v33, v70, v71\n" \
"v_mul_f32 v72, s20, v72\n" \
"v_mfma_f32_16x16x32_bf16 v[120:123], a[88:91], a[100:103], v[120:123]\n" \
"ds_read_b128 a[40:43], v50 offset:1536\n" \
"v_mul_f32 v73, s20, v73\n" \
"v_mfma_f32_16x16x32_bf16 v[124:127], a[92:95], a[100:103], v[124:127]\n" \
"buffer_store_dwordx4 v[30:33], v4, s[36:39], s96 offen offset:0 sc1\n" \
"v_mfma_f32_16x16x32_bf16 v[128:131], a[64:67], a[104:107], v[128:131]\n" \
"s_add_u32 s97, s96, s98\n" \
"ds_read2_b32 a[8:9], v47 offset0:0 offset1:1\n" \
"v_mul_f32 v74, s20, v74\n" \
"v_mfma_f32_16x16x32_bf16 v[132:135], a[68:71], a[104:107], v[132:135]\n" \
"v_mul_f32 v75, s20, v75\n" \
"v_cvt_pk_bf16_f32 v34, v72, v73\n" \
"v_mfma_f32_16x16x32_bf16 v[136:139], a[64:67], a[108:111], v[136:139]\n" \
"s_add_u32 m0, s50, 8192\n" \
"ds_read2_b32 a[10:11], v47 offset0:2 offset1:3\n" \
"v_cvt_pk_bf16_f32 v35, v74, v75\n" \
"v_mfma_f32_16x16x32_bf16 v[140:143], a[68:71], a[108:111], v[140:143]\n" \
"buffer_load_dwordx4 v15, s[28:31], s46 offen lds\n" \
"v_mfma_f32_16x16x32_bf16 v[144:147], a[72:75], a[104:107], v[144:147]\n" \
"ds_read_b128 a[44:47], v50 offset:2048\n" \
"v_mul_f32 v76, s20, v76\n" \
"v_mfma_f32_16x16x32_bf16 v[148:151], a[76:79], a[104:107], v[148:151]\n" \
"v_mul_f32 v77, s20, v77\n" \
"v_mul_f32 v78, s20, v78\n" \
"v_mfma_f32_16x16x32_bf16 v[152:155], a[72:75], a[108:111], v[152:155]\n" \
"ds_read2_b32 a[12:13], v47 offset0:128 offset1:129\n" \
"v_mul_f32 v79, s20, v79\n" \
"v_mfma_f32_16x16x32_bf16 v[156:159], a[76:79], a[108:111], v[156:159]\n" \
"v_cvt_pk_bf16_f32 v36, v76, v77\n" \
"v_cvt_pk_bf16_f32 v37, v78, v79\n" \
"v_mfma_f32_16x16x32_bf16 v[160:163], a[80:83], a[104:107], v[160:163]\n" \
"ds_read2_b32 a[14:15], v47 offset0:130 offset1:131\n" \
"v_mul_f32 v80, s20, v80\n" \
"v_mfma_f32_16x16x32_bf16 v[164:167], a[84:87], a[104:107], v[164:167]\n" \
"v_mul_f32 v81, s20, v81\n" \
"v_mul_f32 v82, s20, v82\n" \
"v_mfma_f32_16x16x32_bf16 v[168:171], a[80:83], a[108:111], v[168:171]\n" \
"s_add_u32 m0, s50, 12288\n" \
"ds_read_b128 a[48:51], v50 offset:2560\n" \
"v_mul_f32 v83, s20, v83\n" \
"v_mfma_f32_16x16x32_bf16 v[172:175], a[84:87], a[108:111], v[172:175]\n" \
"buffer_load_dwordx4 v16, s[28:31], s46 offen lds\n" \
"v_mfma_f32_16x16x32_bf16 v[176:179], a[88:91], a[104:107], v[176:179]\n" \
"ds_read2_b32 a[16:17], v48 offset0:0 offset1:1\n" \
"v_cvt_pk_bf16_f32 v38, v80, v81\n" \
"v_mfma_f32_16x16x32_bf16 v[180:183], a[92:95], a[104:107], v[180:183]\n" \
"buffer_store_dwordx4 v[34:37], v4, s[36:39], s97 offen offset:0 sc1\n" \
"v_mfma_f32_16x16x32_bf16 v[184:187], a[88:91], a[108:111], v[184:187]\n" \
"s_add_u32 s96, s96, s92\n" \
"ds_read2_b32 a[18:19], v48 offset0:2 offset1:3\n" \
"v_cvt_pk_bf16_f32 v39, v82, v83\n" \
"v_mfma_f32_16x16x32_bf16 v[188:191], a[92:95], a[108:111], v[188:191]\n" \
"v_mul_f32 v84, s20, v84\n" \
"v_mul_f32 v85, s20, v85\n" \
"v_mfma_f32_16x16x32_bf16 v[192:195], a[64:67], a[112:115], v[192:195]\n" \
"ds_read_b128 a[52:55], v50 offset:3072\n" \
"v_mul_f32 v86, s20, v86\n" \
"v_mfma_f32_16x16x32_bf16 v[196:199], a[68:71], a[112:115], v[196:199]\n" \
"v_mul_f32 v87, s20, v87\n" \
"v_cvt_pk_bf16_f32 v40, v84, v85\n" \
"v_mfma_f32_16x16x32_bf16 v[200:203], a[64:67], a[116:119], v[200:203]\n" \
"s_mov_b32 s46, 128\n" \
"s_add_u32 m0, s50, 16384\n" \
"ds_read2_b32 a[20:21], v48 offset0:128 offset1:129\n" \
"v_mfma_f32_16x16x32_bf16 v[204:207], a[68:71], a[116:119], v[204:207]\n" \
"buffer_load_dwordx4 v17, s[28:31], s46 offen lds\n" \
"v_mfma_f32_16x16x32_bf16 v[208:211], a[72:75], a[112:115], v[208:211]\n" \
"ds_read2_b32 a[22:23], v48 offset0:130 offset1:131\n" \
"v_cvt_pk_bf16_f32 v41, v86, v87\n" \
"v_mfma_f32_16x16x32_bf16 v[212:215], a[76:79], a[112:115], v[212:215]\n" \
"v_mul_f32 v88, s20, v88\n" \
"v_mul_f32 v89, s20, v89\n" \
"v_mfma_f32_16x16x32_bf16 v[216:219], a[72:75], a[116:119], v[216:219]\n" \
"ds_read_b128 a[56:59], v50 offset:3584\n" \
"v_mul_f32 v90, s20, v90\n" \
"v_mfma_f32_16x16x32_bf16 v[220:223], a[76:79], a[116:119], v[220:223]\n" \
"v_mul_f32 v91, s20, v91\n" \
"v_cvt_pk_bf16_f32 v42, v88, v89\n" \
"v_mfma_f32_16x16x32_bf16 v[224:227], a[80:83], a[112:115], v[224:227]\n" \
"ds_read2_b32 a[24:25], v49 offset0:0 offset1:1\n" \
"v_cvt_pk_bf16_f32 v43, v90, v91\n" \
"v_mfma_f32_16x16x32_bf16 v[228:231], a[84:87], a[112:115], v[228:231]\n" \
"v_mul_f32 v92, s20, v92\n" \
"v_mul_f32 v93, s20, v93\n" \
"v_mfma_f32_16x16x32_bf16 v[232:235], a[80:83], a[116:119], v[232:235]\n" \
"s_add_u32 m0, s50, 20480\n" \
"ds_read2_b32 a[26:27], v49 offset0:2 offset1:3\n" \
"v_mul_f32 v94, s20, v94\n" \
"v_mfma_f32_16x16x32_bf16 v[236:239], a[84:87], a[116:119], v[236:239]\n" \
"buffer_load_dwordx4 v18, s[28:31], s46 offen lds\n" \
"v_mfma_f32_16x16x32_bf16 v[240:243], a[88:91], a[112:115], v[240:243]\n" \
"ds_read_b128 a[60:63], v50 offset:4096\n" \
"v_mul_f32 v95, s20, v95\n" \
"v_mfma_f32_16x16x32_bf16 v[244:247], a[92:95], a[112:115], v[244:247]\n" \
"buffer_store_dwordx4 v[38:41], v4, s[36:39], s96 offen offset:0 sc1\n" \
"v_mfma_f32_16x16x32_bf16 v[248:251], a[88:91], a[116:119], v[248:251]\n" \
"s_add_u32 s97, s96, s98\n" \
"ds_read2_b32 a[28:29], v49 offset0:128 offset1:129\n" \
"v_cvt_pk_bf16_f32 v44, v92, v93\n" \
"v_mfma_f32_16x16x32_bf16 v[252:255], a[92:95], a[116:119], v[252:255]\n" \
"v_cvt_pk_bf16_f32 v45, v94, v95\n" \
"v_mul_f32 v96, s20, v96\n" \
"v_mfma_f32_16x16x32_bf16 a[128:131], a[64:67], a[120:123], a[128:131]\n" \
"ds_read2_b32 a[30:31], v49 offset0:130 offset1:131\n" \
"v_mul_f32 v97, s20, v97\n" \
"v_mfma_f32_16x16x32_bf16 a[132:135], a[68:71], a[120:123], a[132:135]\n" \
"v_mul_f32 v98, s20, v98\n" \
"v_mul_f32 v99, s20, v99\n" \
"v_mfma_f32_16x16x32_bf16 a[136:139], a[64:67], a[124:127], a[136:139]\n" \
"s_add_u32 m0, s50, 24576\n" \
"v_cvt_pk_bf16_f32 v30, v96, v97\n" \
"v_cvt_pk_bf16_f32 v31, v98, v99\n" \
"v_mfma_f32_16x16x32_bf16 a[140:143], a[68:71], a[124:127], a[140:143]\n" \
"buffer_load_dwordx4 v19, s[28:31], s46 offen lds\n" \
"v_mfma_f32_16x16x32_bf16 a[144:147], a[72:75], a[120:123], a[144:147]\n" \
"v_mul_f32 v100, s20, v100\n" \
"v_mul_f32 v101, s20, v101\n" \
"v_mfma_f32_16x16x32_bf16 a[148:151], a[76:79], a[120:123], a[148:151]\n" \
"v_mul_f32 v102, s20, v102\n" \
"v_mul_f32 v103, s20, v103\n" \
"v_mfma_f32_16x16x32_bf16 a[152:155], a[72:75], a[124:127], a[152:155]\n" \
"v_cvt_pk_bf16_f32 v32, v100, v101\n" \
"v_mfma_f32_16x16x32_bf16 a[156:159], a[76:79], a[124:127], a[156:159]\n" \
"v_cvt_pk_bf16_f32 v33, v102, v103\n" \
"v_mul_f32 v104, s20, v104\n" \
"v_mfma_f32_16x16x32_bf16 a[160:163], a[80:83], a[120:123], a[160:163]\n" \
"v_mul_f32 v105, s20, v105\n" \
"v_mul_f32 v106, s20, v106\n" \
"v_mfma_f32_16x16x32_bf16 a[164:167], a[84:87], a[120:123], a[164:167]\n" \
"v_mul_f32 v107, s20, v107\n" \
"v_mfma_f32_16x16x32_bf16 a[168:171], a[80:83], a[124:127], a[168:171]\n" \
"s_add_u32 m0, s50, 28672\n" \
"v_cvt_pk_bf16_f32 v34, v104, v105\n" \
"v_cvt_pk_bf16_f32 v35, v106, v107\n" \
"v_mfma_f32_16x16x32_bf16 a[172:175], a[84:87], a[124:127], a[172:175]\n" \
"buffer_load_dwordx4 v20, s[28:31], s46 offen lds\n" \
"v_mfma_f32_16x16x32_bf16 a[176:179], a[88:91], a[120:123], a[176:179]\n" \
"v_mul_f32 v108, s20, v108\n" \
"v_mul_f32 v109, s20, v109\n" \
"v_mfma_f32_16x16x32_bf16 a[180:183], a[92:95], a[120:123], a[180:183]\n" \
"buffer_store_dwordx4 v[42:45], v4, s[36:39], s97 offen offset:0 sc1\n" \
"v_mfma_f32_16x16x32_bf16 a[184:187], a[88:91], a[124:127], a[184:187]\n" \
"s_waitcnt lgkmcnt(0)\n" \
"s_add_u32 s96, s96, s92\n" \
"v_mul_f32 v110, s20, v110\n" \
"v_mfma_f32_16x16x32_bf16 a[188:191], a[92:95], a[124:127], a[188:191]\n" \
"v_mul_f32 v111, s20, v111\n" \
"v_cvt_pk_bf16_f32 v36, v108, v109\n" \
"LC_%=:\n" \
"v_mfma_f32_16x16x32_bf16 v[64:67], a[0:3], a[32:35], 0\n" \
"ds_read_b128 a[96:99], v50 offset:448\n" \
"v_cvt_pk_bf16_f32 v37, v110, v111\n" \
"v_mfma_f32_16x16x32_bf16 v[68:71], a[0:3], a[36:39], 0\n" \
"v_mul_f32 v112, s20, v112\n" \
"v_mul_f32 v113, s20, v113\n" \
"v_mfma_f32_16x16x32_bf16 v[68:71], a[4:7], a[32:35], v[68:71]\n" \
"ds_read2_b32 a[64:65], v46 offset0:16 offset1:17\n" \
"v_mul_f32 v114, s20, v114\n" \
"v_mfma_f32_16x16x32_bf16 v[72:75], a[4:7], a[36:39], 0\n" \
"ds_read2_b32 a[66:67], v46 offset0:18 offset1:19\n" \
"v_mul_f32 v115, s20, v115\n" \
"v_mfma_f32_16x16x32_bf16 v[72:75], a[0:3], a[40:43], v[72:75]\n" \
"s_mov_b32 s47, 524288\n" \
"s_add_u32 m0, s51, 49152\n" \
"ds_read_b128 a[100:103], v50 offset:960\n" \
"v_mfma_f32_16x16x32_bf16 v[76:79], a[4:7], a[40:43], 0\n" \
"buffer_load_dwordx4 v21, s[32:35], s47 offen offset:0 lds\n" \
"v_mfma_f32_16x16x32_bf16 v[72:75], a[8:11], a[32:35], v[72:75]\n" \
"ds_read2_b32 a[68:69], v46 offset0:144 offset1:145\n" \
"v_cvt_pk_bf16_f32 v38, v112, v113\n" \
"v_mfma_f32_16x16x32_bf16 v[76:79], a[8:11], a[36:39], v[76:79]\n" \
"ds_read2_b32 a[70:71], v46 offset0:146 offset1:147\n" \
"v_cvt_pk_bf16_f32 v39, v114, v115\n" \
"v_mfma_f32_16x16x32_bf16 v[80:83], a[8:11], a[40:43], 0\n" \
"ds_read_b128 a[104:107], v50 offset:1472\n" \
"v_mul_f32 v116, s20, v116\n" \
"v_mfma_f32_16x16x32_bf16 v[76:79], a[0:3], a[44:47], v[76:79]\n" \
"buffer_store_dwordx4 v[30:33], v4, s[36:39], s96 offen offset:0 sc1\n" \
"v_mfma_f32_16x16x32_bf16 v[80:83], a[4:7], a[44:47], v[80:83]\n" \
"s_add_u32 s97, s96, s98\n" \
"ds_read2_b32 a[72:73], v47 offset0:16 offset1:17\n" \
"v_mul_f32 v117, s20, v117\n" \
"v_mfma_f32_16x16x32_bf16 v[84:87], a[8:11], a[44:47], 0\n" \
"ds_read2_b32 a[74:75], v47 offset0:18 offset1:19\n" \
"v_mul_f32 v118, s20, v118\n" \
"v_mfma_f32_16x16x32_bf16 v[76:79], a[12:15], a[32:35], v[76:79]\n" \
"s_add_u32 m0, s51, 50048\n" \
"ds_read_b128 a[108:111], v50 offset:1984\n" \
"v_mul_f32 v119, s20, v119\n" \
"v_mfma_f32_16x16x32_bf16 v[80:83], a[12:15], a[36:39], v[80:83]\n" \
"buffer_load_dwordx4 v21, s[32:35], s47 offen offset:128 lds\n" \
"v_mfma_f32_16x16x32_bf16 v[84:87], a[12:15], a[40:43], v[84:87]\n" \
"ds_read2_b32 a[76:77], v47 offset0:144 offset1:145\n" \
"v_cvt_pk_bf16_f32 v40, v116, v117\n" \
"v_mfma_f32_16x16x32_bf16 v[88:91], a[12:15], a[44:47], 0\n" \
"ds_read2_b32 a[78:79], v47 offset0:146 offset1:147\n" \
"v_cvt_pk_bf16_f32 v41, v118, v119\n" \
"v_mfma_f32_16x16x32_bf16 v[80:83], a[0:3], a[48:51], v[80:83]\n" \
"ds_read_b128 a[112:115], v50 offset:2496\n" \
"v_mul_f32 v120, s20, v120\n" \
"v_mfma_f32_16x16x32_bf16 v[84:87], a[4:7], a[48:51], v[84:87]\n" \
"v_mul_f32 v121, s20, v121\n" \
"v_mul_f32 v122, s20, v122\n" \
"v_mfma_f32_16x16x32_bf16 v[88:91], a[8:11], a[48:51], v[88:91]\n" \
"ds_read2_b32 a[80:81], v48 offset0:16 offset1:17\n" \
"v_mul_f32 v123, s20, v123\n" \
"v_mfma_f32_16x16x32_bf16 v[92:95], a[12:15], a[48:51], 0\n" \
"ds_read2_b32 a[82:83], v48 offset0:18 offset1:19\n" \
"v_cvt_pk_bf16_f32 v42, v120, v121\n" \
"v_mfma_f32_16x16x32_bf16 v[80:83], a[16:19], a[32:35], v[80:83]\n" \
"s_mov_b32 s47, 786432\n" \
"s_add_u32 m0, s51, 57344\n" \
"ds_read_b128 a[116:119], v50 offset:3008\n" \
"v_mfma_f32_16x16x32_bf16 v[84:87], a[16:19], a[36:39], v[84:87]\n" \
"buffer_load_dwordx4 v21, s[32:35], s47 offen offset:0 lds\n" \
"v_mfma_f32_16x16x32_bf16 v[88:91], a[16:19], a[40:43], v[88:91]\n" \
"ds_read2_b32 a[84:85], v48 offset0:144 offset1:145\n" \
"v_cvt_pk_bf16_f32 v43, v122, v123\n" \
"v_mfma_f32_16x16x32_bf16 v[92:95], a[16:19], a[44:47], v[92:95]\n" \
"ds_read2_b32 a[86:87], v48 offset0:146 offset1:147\n" \
"v_mul_f32 v124, s20, v124\n" \
"v_mfma_f32_16x16x32_bf16 v[84:87], a[0:3], a[52:55], v[84:87]\n" \
"ds_read_b128 a[120:123], v50 offset:3520\n" \
"v_mul_f32 v125, s20, v125\n" \
"v_mfma_f32_16x16x32_bf16 v[88:91], a[4:7], a[52:55], v[88:91]\n" \
"buffer_store_dwordx4 v[34:37], v4, s[36:39], s97 offen offset:0 sc1\n" \
"v_mfma_f32_16x16x32_bf16 v[92:95], a[8:11], a[52:55], v[92:95]\n" \
"s_add_u32 s96, s96, s92\n" \
"ds_read2_b32 a[88:89], v49 offset0:16 offset1:17\n" \
"v_mul_f32 v126, s20, v126\n" \
"v_mfma_f32_16x16x32_bf16 v[84:87], a[20:23], a[32:35], v[84:87]\n" \
"ds_read2_b32 a[90:91], v49 offset0:18 offset1:19\n" \
"v_mul_f32 v127, s20, v127\n" \
"v_mfma_f32_16x16x32_bf16 v[88:91], a[20:23], a[36:39], v[88:91]\n" \
"s_add_u32 m0, s51, 58240\n" \
"ds_read_b128 a[124:127], v50 offset:4032\n" \
"v_cvt_pk_bf16_f32 v44, v124, v125\n" \
"v_mfma_f32_16x16x32_bf16 v[92:95], a[20:23], a[40:43], v[92:95]\n" \
"buffer_load_dwordx4 v21, s[32:35], s47 offen offset:128 lds\n" \
"v_mfma_f32_16x16x32_bf16 v[88:91], a[0:3], a[56:59], v[88:91]\n" \
"ds_read2_b32 a[92:93], v49 offset0:144 offset1:145\n" \
"v_cvt_pk_bf16_f32 v45, v126, v127\n" \
"v_mfma_f32_16x16x32_bf16 v[92:95], a[4:7], a[56:59], v[92:95]\n" \
"ds_read2_b32 a[94:95], v49 offset0:146 offset1:147\n" \
"v_mul_f32 v128, s20, v128\n" \
"v_mfma_f32_16x16x32_bf16 v[88:91], a[24:27], a[32:35], v[88:91]\n" \
"v_mul_f32 v129, s20, v129\n" \
"v_mul_f32 v130, s20, v130\n" \
"v_mfma_f32_16x16x32_bf16 v[92:95], a[24:27], a[36:39], v[92:95]\n" \
"v_mul_f32 v131, s20, v131\n" \
"v_cvt_pk_bf16_f32 v30, v128, v129\n" \
"v_mfma_f32_16x16x32_bf16 v[92:95], a[0:3], a[60:63], v[92:95]\n" \
"v_cvt_pk_bf16_f32 v31, v130, v131\n" \
"v_mfma_f32_16x16x32_bf16 v[92:95], a[28:31], a[32:35], v[92:95]\n" \
"s_waitcnt lgkmcnt(0)\n" \
"v_mul_f32 v132, s20, v132\n" \
"v_mul_f32 v133, s20, v133\n" \
"v_mfma_f32_16x16x32_bf16 v[64:67], a[64:67], a[96:99], v[64:67]\n" \
"ds_read_b128 a[32:35], v50 offset:384\n" \
"v_mul_f32 v134, s20, v134\n" \
"v_mfma_f32_16x16x32_bf16 v[68:71], a[64:67], a[100:103], v[68:71]\n" \
"buffer_store_dwordx4 v[38:41], v4, s[36:39], s96 offen offset:0 sc1\n" \
"v_mfma_f32_16x16x32_bf16 v[68:71], a[68:71], a[96:99], v[68:71]\n" \
"LH8_%=:\ns_cbranch_execz LH12_%=\n" \
"s_add_u32 s97, s96, s98\n" \
"ds_read2_b32 a[0:1], v46 offset0:32 offset1:33\n" \
"v_mul_f32 v135, s20, v135\n" \
"v_mfma_f32_16x16x32_bf16 v[72:75], a[68:71], a[100:103], v[72:75]\n" \
"ds_read2_b32 a[2:3], v46 offset0:34 offset1:35\n" \
"v_cvt_pk_bf16_f32 v32, v132, v133\n" \
"v_mfma_f32_16x16x32_bf16 v[72:75], a[64:67], a[104:107], v[72:75]\n" \
"ds_read_b128 a[36:39], v50 offset:896\n" \
"v_cvt_pk_bf16_f32 v33, v134, v135\n" \
"v_mfma_f32_16x16x32_bf16 v[76:79], a[68:71], a[104:107], v[76:79]\n" \
"v_mul_f32 v136, s20, v136\n" \
"v_mul_f32 v137, s20, v137\n" \
"v_mfma_f32_16x16x32_bf16 v[72:75], a[72:75], a[96:99], v[72:75]\n" \
"ds_read2_b32 a[4:5], v46 offset0:160 offset1:161\n" \
"v_mul_f32 v138, s20, v138\n" \
"v_mfma_f32_16x16x32_bf16 v[76:79], a[72:75], a[100:103], v[76:79]\n" \
"ds_read2_b32 a[6:7], v46 offset0:162 offset1:163\n" \
"v_mul_f32 v139, s20, v139\n" \
"v_mfma_f32_16x16x32_bf16 v[80:83], a[72:75], a[104:107], v[80:83]\n" \
"ds_read_b128 a[40:43], v50 offset:1408\n" \
"v_cvt_pk_bf16_f32 v34, v136, v137\n" \
"v_mfma_f32_16x16x32_bf16 v[76:79], a[64:67], a[108:111], v[76:79]\n" \
"v_cvt_pk_bf16_f32 v35, v138, v139\n" \
"v_mul_f32 v140, s20, v140\n" \
"v_mfma_f32_16x16x32_bf16 v[80:83], a[68:71], a[108:111], v[80:83]\n" \
"ds_read2_b32 a[8:9], v47 offset0:32 offset1:33\n" \
"v_mul_f32 v141, s20, v141\n" \
"v_mfma_f32_16x16x32_bf16 v[84:87], a[72:75], a[108:111], v[84:87]\n" \
"ds_read2_b32 a[10:11], v47 offset0:34 offset1:35\n" \
"v_mul_f32 v142, s20, v142\n" \
"v_mfma_f32_16x16x32_bf16 v[76:79], a[76:79], a[96:99], v[76:79]\n" \
"ds_read_b128 a[44:47], v50 offset:1920\n" \
"v_mul_f32 v143, s20, v143\n" \
"v_mfma_f32_16x16x32_bf16 v[80:83], a[76:79], a[100:103], v[80:83]\n" \
"buffer_store_dwordx4 v[42:45], v4, s[36:39], s97 offen offset:0 sc1\n" \
"v_mfma_f32_16x16x32_bf16 v[84:87], a[76:79], a[104:107], v[84:87]\n" \
"s_add_u32 s57, s57, s93\n" \
"ds_read2_b32 a[12:13], v47 offset0:160 offset1:161\n" \
"s_mov_b32 s96, s57\n" \
"v_mfma_f32_16x16x32_bf16 v[88:91], a[76:79], a[108:111], v[88:91]\n" \
"ds_read2_b32 a[14:15], v47 offset0:162 offset1:163\n" \
"v_cvt_pk_bf16_f32 v36, v140, v141\n" \
"v_mfma_f32_16x16x32_bf16 v[80:83], a[64:67], a[112:115], v[80:83]\n" \
"ds_read_b128 a[48:51], v50 offset:2432\n" \
"v_cvt_pk_bf16_f32 v37, v142, v143\n" \
"v_mfma_f32_16x16x32_bf16 v[84:87], a[68:71], a[112:115], v[84:87]\n" \
"v_mul_f32 v144, s20, v144\n" \
"v_mul_f32 v145, s20, v145\n" \
"v_mfma_f32_16x16x32_bf16 v[88:91], a[72:75], a[112:115], v[88:91]\n" \
"ds_read2_b32 a[16:17], v48 offset0:32 offset1:33\n" \
"v_mul_f32 v146, s20, v146\n" \
"v_mfma_f32_16x16x32_bf16 v[92:95], a[76:79], a[112:115], v[92:95]\n" \
"ds_read2_b32 a[18:19], v48 offset0:34 offset1:35\n" \
"v_mul_f32 v147, s20, v147\n" \
"v_mfma_f32_16x16x32_bf16 v[80:83], a[80:83], a[96:99], v[80:83]\n" \
"ds_read_b128 a[52:55], v50 offset:2944\n" \
"v_cvt_pk_bf16_f32 v38, v144, v145\n" \
"v_mfma_f32_16x16x32_bf16 v[84:87], a[80:83], a[100:103], v[84:87]\n" \
"v_cvt_pk_bf16_f32 v39, v146, v147\n" \
"v_mul_f32 v148, s20, v148\n" \
"v_mfma_f32_16x16x32_bf16 v[88:91], a[80:83], a[104:107], v[88:91]\n" \
"ds_read2_b32 a[20:21], v48 offset0:160 offset1:161\n" \
"v_mul_f32 v149, s20, v149\n" \
"v_mfma_f32_16x16x32_bf16 v[92:95], a[80:83], a[108:111], v[92:95]\n" \
"ds_read2_b32 a[22:23], v48 offset0:162 offset1:163\n" \
"v_mul_f32 v150, s20, v150\n" \
"v_mfma_f32_16x16x32_bf16 v[84:87], a[64:67], a[116:119], v[84:87]\n" \
"ds_read_b128 a[56:59], v50 offset:3456\n" \
"v_mul_f32 v151, s20, v151\n" \
"v_mfma_f32_16x16x32_bf16 v[88:91], a[68:71], a[116:119], v[88:91]\n" \
"buffer_store_dwordx4 v[30:33], v4, s[36:39], s96 offen offset:0 sc1\n" \
"v_mfma_f32_16x16x32_bf16 v[92:95], a[72:75], a[116:119], v[92:95]\n" \
"s_add_u32 s97, s96, s98\n" \
"ds_read2_b32 a[24:25], v49 offset0:32 offset1:33\n" \
"v_cvt_pk_bf16_f32 v40, v148, v149\n" \
"v_mfma_f32_16x16x32_bf16 v[84:87], a[84:87], a[96:99], v[84:87]\n" \
"ds_read2_b32 a[26:27], v49 offset0:34 offset1:35\n" \
"v_cvt_pk_bf16_f32 v41, v150, v151\n" \
"v_mfma_f32_16x16x32_bf16 v[88:91], a[84:87], a[100:103], v[88:91]\n" \
"ds_read_b128 a[60:63], v50 offset:3968\n" \
"v_mul_f32 v152, s20, v152\n" \
"v_mfma_f32_16x16x32_bf16 v[92:95], a[84:87], a[104:107], v[92:95]\n" \
"v_mul_f32 v153, s20, v153\n" \
"v_mul_f32 v154, s20, v154\n" \
"v_mfma_f32_16x16x32_bf16 v[88:91], a[64:67], a[120:123], v[88:91]\n" \
"ds_read2_b32 a[28:29], v49 offset0:160 offset1:161\n" \
"v_mul_f32 v155, s20, v155\n" \
"v_mfma_f32_16x16x32_bf16 v[92:95], a[68:71], a[120:123], v[92:95]\n" \
"ds_read2_b32 a[30:31], v49 offset0:162 offset1:163\n" \
"v_cvt_pk_bf16_f32 v42, v152, v153\n" \
"v_mfma_f32_16x16x32_bf16 v[88:91], a[88:91], a[96:99], v[88:91]\n" \
"v_cvt_pk_bf16_f32 v43, v154, v155\n" \
"v_mul_f32 v156, s20, v156\n" \
"v_mfma_f32_16x16x32_bf16 v[92:95], a[88:91], a[100:103], v[92:95]\n" \
"v_mul_f32 v157, s20, v157\n" \
"v_mul_f32 v158, s20, v158\n" \
"v_mfma_f32_16x16x32_bf16 v[92:95], a[64:67], a[124:127], v[92:95]\n" \
"v_mul_f32 v159, s20, v159\n" \
"v_mfma_f32_16x16x32_bf16 v[92:95], a[92:95], a[96:99], v[92:95]\n" \
"s_waitcnt lgkmcnt(0)\n" \
"v_cvt_pk_bf16_f32 v44, v156, v157\n" \
"v_cvt_pk_bf16_f32 v45, v158, v159\n" \
"v_mfma_f32_16x16x32_bf16 v[64:67], a[0:3], a[32:35], v[64:67]\n" \
"ds_read_b128 a[96:99], v50 offset:320\n" \
"v_mul_f32 v160, s20, v160\n" \
"v_mfma_f32_16x16x32_bf16 v[68:71], a[0:3], a[36:39], v[68:71]\n" \
"buffer_store_dwordx4 v[34:37], v4, s[36:39], s97 offen offset:0 sc1\n" \
"v_mfma_f32_16x16x32_bf16 v[68:71], a[4:7], a[32:35], v[68:71]\n" \
"s_add_u32 s96, s96, s92\n" \
"ds_read2_b32 a[64:65], v46 offset0:48 offset1:49\n" \
"v_mul_f32 v161, s20, v161\n" \
"v_mfma_f32_16x16x32_bf16 v[72:75], a[4:7], a[36:39], v[72:75]\n" \
"ds_read2_b32 a[66:67], v46 offset0:50 offset1:51\n" \
"v_mul_f32 v162, s20, v162\n" \
"v_mfma_f32_16x16x32_bf16 v[72:75], a[0:3], a[40:43], v[72:75]\n" \
"ds_read_b128 a[100:103], v50 offset:832\n" \
"v_mul_f32 v163, s20, v163\n" \
"v_mfma_f32_16x16x32_bf16 v[76:79], a[4:7], a[40:43], v[76:79]\n" \
"v_cvt_pk_bf16_f32 v30, v160, v161\n" \
"v_cvt_pk_bf16_f32 v31, v162, v163\n" \
"v_mfma_f32_16x16x32_bf16 v[72:75], a[8:11], a[32:35], v[72:75]\n" \
"ds_read2_b32 a[68:69], v46 offset0:176 offset1:177\n" \
"v_mul_f32 v164, s20, v164\n" \
"v_mfma_f32_16x16x32_bf16 v[76:79], a[8:11], a[36:39], v[76:79]\n" \
"ds_read2_b32 a[70:71], v46 offset0:178 offset1:179\n" \
"v_mul_f32 v165, s20, v165\n" \
"v_mfma_f32_16x16x32_bf16 v[80:83], a[8:11], a[40:43], v[80:83]\n" \
"ds_read_b128 a[104:107], v50 offset:1344\n" \
"v_mul_f32 v166, s20, v166\n" \
"v_mfma_f32_16x16x32_bf16 v[76:79], a[0:3], a[44:47], v[76:79]\n" \
"v_mul_f32 v167, s20, v167\n" \
"v_cvt_pk_bf16_f32 v32, v164, v165\n" \
"v_mfma_f32_16x16x32_bf16 v[80:83], a[4:7], a[44:47], v[80:83]\n" \
"ds_read2_b32 a[72:73], v47 offset0:48 offset1:49\n" \
"v_cvt_pk_bf16_f32 v33, v166, v167\n" \
"v_mfma_f32_16x16x32_bf16 v[84:87], a[8:11], a[44:47], v[84:87]\n" \
"ds_read2_b32 a[74:75], v47 offset0:50 offset1:51\n" \
"v_mul_f32 v168, s20, v168\n" \
"v_mfma_f32_16x16x32_bf16 v[76:79], a[12:15], a[32:35], v[76:79]\n" \
"ds_read_b128 a[108:111], v50 offset:1856\n" \
"v_mul_f32 v169, s20, v169\n" \
"v_mfma_f32_16x16x32_bf16 v[80:83], a[12:15], a[36:39], v[80:83]\n" \
"buffer_store_dwordx4 v[38:41], v4, s[36:39], s96 offen offset:0 sc1\n" \
"v_mfma_f32_16x16x32_bf16 v[84:87], a[12:15], a[40:43], v[84:87]\n" \
"s_add_u32 s97, s96, s98\n" \
"ds_read2_b32 a[76:77], v47 offset0:176 offset1:177\n" \
"v_mul_f32 v170, s20, v170\n" \
"v_mfma_f32_16x16x32_bf16 v[88:91], a[12:15], a[44:47], v[88:91]\n" \
"ds_read2_b32 a[78:79], v47 offset0:178 offset1:179\n" \
"v_mul_f32 v171, s20, v171\n" \
"v_mfma_f32_16x16x32_bf16 v[80:83], a[0:3], a[48:51], v[80:83]\n" \
"ds_read_b128 a[112:115], v50 offset:2368\n" \
"v_cvt_pk_bf16_f32 v34, v168, v169\n" \
"v_mfma_f32_16x16x32_bf16 v[84:87], a[4:7], a[48:51], v[84:87]\n" \
"v_cvt_pk_bf16_f32 v35, v170, v171\n" \
"v_mul_f32 v172, s20, v172\n" \
"v_mfma_f32_16x16x32_bf16 v[88:91], a[8:11], a[48:51], v[88:91]\n" \
"ds_read2_b32 a[80:81], v48 offset0:48 offset1:49\n" \
"v_mul_f32 v173, s20, v173\n" \
"v_mfma_f32_16x16x32_bf16 v[92:95], a[12:15], a[48:51], v[92:95]\n" \
"ds_read2_b32 a[82:83], v48 offset0:50 offset1:51\n" \
"v_mul_f32 v174, s20, v174\n" \
"v_mfma_f32_16x16x32_bf16 v[80:83], a[16:19], a[32:35], v[80:83]\n" \
"ds_read_b128 a[116:119], v50 offset:2880\n" \
"v_mul_f32 v175, s20, v175\n" \
"v_mfma_f32_16x16x32_bf16 v[84:87], a[16:19], a[36:39], v[84:87]\n" \
"v_cvt_pk_bf16_f32 v36, v172, v173\n" \
"v_cvt_pk_bf16_f32 v37, v174, v175\n" \
"v_mfma_f32_16x16x32_bf16 v[88:91], a[16:19], a[40:43], v[88:91]\n" \
"ds_read2_b32 a[84:85], v48 offset0:176 offset1:177\n" \
"v_mul_f32 v176, s20, v176\n" \
"v_mfma_f32_16x16x32_bf16 v[92:95], a[16:19], a[44:47], v[92:95]\n" \
"ds_read2_b32 a[86:87], v48 offset0:178 offset1:179\n" \
"v_mul_f32 v177, s20, v177\n" \
"v_mfma_f32_16x16x32_bf16 v[84:87], a[0:3], a[52:55], v[84:87]\n" \
"ds_read_b128 a[120:123], v50 offset:3392\n" \
"v_mul_f32 v178, s20, v178\n" \
"v_mfma_f32_16x16x32_bf16 v[88:91], a[4:7], a[52:55], v[88:91]\n" \
"buffer_store_dwordx4 v[42:45], v4, s[36:39], s97 offen offset:0 sc1\n" \
"v_mfma_f32_16x16x32_bf16 v[92:95], a[8:11], a[52:55], v[92:95]\n" \
"s_add_u32 s96, s96, s92\n" \
"ds_read2_b32 a[88:89], v49 offset0:48 offset1:49\n" \
"v_mul_f32 v179, s20, v179\n" \
"v_mfma_f32_16x16x32_bf16 v[84:87], a[20:23], a[32:35], v[84:87]\n" \
"ds_read2_b32 a[90:91], v49 offset0:50 offset1:51\n" \
"v_cvt_pk_bf16_f32 v38, v176, v177\n" \
"v_mfma_f32_16x16x32_bf16 v[88:91], a[20:23], a[36:39], v[88:91]\n" \
"ds_read_b128 a[124:127], v50 offset:3904\n" \
"v_cvt_pk_bf16_f32 v39, v178, v179\n" \
"v_mfma_f32_16x16x32_bf16 v[92:95], a[20:23], a[40:43], v[92:95]\n" \
"v_mul_f32 v180, s20, v180\n" \
"v_mul_f32 v181, s20, v181\n" \
"v_mfma_f32_16x16x32_bf16 v[88:91], a[0:3], a[56:59], v[88:91]\n" \
"ds_read2_b32 a[92:93], v49 offset0:176 offset1:177\n" \
"v_mul_f32 v182, s20, v182\n" \
"v_mfma_f32_16x16x32_bf16 v[92:95], a[4:7], a[56:59], v[92:95]\n" \
"ds_read2_b32 a[94:95], v49 offset0:178 offset1:179\n" \
"v_mul_f32 v183, s20, v183\n" \
"v_mfma_f32_16x16x32_bf16 v[88:91], a[24:27], a[32:35], v[88:91]\n" \
"v_cvt_pk_bf16_f32 v40, v180, v181\n" \
"v_cvt_pk_bf16_f32 v41, v182, v183\n" \
"v_mfma_f32_16x16x32_bf16 v[92:95], a[24:27], a[36:39], v[92:95]\n" \
"v_mul_f32 v184, s20, v184\n" \
"v_mul_f32 v185, s20, v185\n" \
"v_mfma_f32_16x16x32_bf16 v[92:95], a[0:3], a[60:63], v[92:95]\n" \
"v_mul_f32 v186, s20, v186\n" \
"v_mfma_f32_16x16x32_bf16 v[92:95], a[28:31], a[32:35], v[92:95]\n" \
"s_waitcnt lgkmcnt(0)\n" \
"v_mul_f32 v187, s20, v187\n" \
"v_cvt_pk_bf16_f32 v42, v184, v185\n" \
"v_mfma_f32_16x16x32_bf16 v[64:67], a[64:67], a[96:99], v[64:67]\n" \
"ds_read_b128 a[32:35], v50 offset:256\n" \
"v_cvt_pk_bf16_f32 v43, v186, v187\n" \
"v_mfma_f32_16x16x32_bf16 v[68:71], a[64:67], a[100:103], v[68:71]\n" \
"buffer_store_dwordx4 v[30:33], v4, s[36:39], s96 offen offset:0 sc1\n" \
"v_mfma_f32_16x16x32_bf16 v[68:71], a[68:71], a[96:99], v[68:71]\n" \
"s_add_u32 s97, s96, s98\n" \
"ds_read2_b32 a[0:1], v46 offset0:64 offset1:65\n" \
"v_mul_f32 v188, s20, v188\n" \
"v_mfma_f32_16x16x32_bf16 v[72:75], a[68:71], a[100:103], v[72:75]\n" \
"ds_read2_b32 a[2:3], v46 offset0:66 offset1:67\n" \
"v_mul_f32 v189, s20, v189\n" \
"v_mfma_f32_16x16x32_bf16 v[72:75], a[64:67], a[104:107], v[72:75]\n" \
"ds_read_b128 a[36:39], v50 offset:768\n" \
"v_mul_f32 v190, s20, v190\n" \
"v_mfma_f32_16x16x32_bf16 v[76:79], a[68:71], a[104:107], v[76:79]\n" \
"v_mul_f32 v191, s20, v191\n" \
"v_cvt_pk_bf16_f32 v44, v188, v189\n" \
"v_mfma_f32_16x16x32_bf16 v[72:75], a[72:75], a[96:99], v[72:75]\n" \
"ds_read2_b32 a[4:5], v46 offset0:192 offset1:193\n" \
"v_cvt_pk_bf16_f32 v45, v190, v191\n" \
"v_mfma_f32_16x16x32_bf16 v[76:79], a[72:75], a[100:103], v[76:79]\n" \
"ds_read2_b32 a[6:7], v46 offset0:194 offset1:195\n" \
"v_mul_f32 v192, s20, v192\n" \
"v_mfma_f32_16x16x32_bf16 v[80:83], a[72:75], a[104:107], v[80:83]\n" \
"ds_read_b128 a[40:43], v50 offset:1280\n" \
"v_mul_f32 v193, s20, v193\n" \
"v_mfma_f32_16x16x32_bf16 v[76:79], a[64:67], a[108:111], v[76:79]\n" \
"v_mul_f32 v194, s20, v194\n" \
"v_mul_f32 v195, s20, v195\n" \
"v_mfma_f32_16x16x32_bf16 v[80:83], a[68:71], a[108:111], v[80:83]\n" \
"ds_read2_b32 a[8:9], v47 offset0:64 offset1:65\n" \
"v_cvt_pk_bf16_f32 v30, v192, v193\n" \
"v_mfma_f32_16x16x32_bf16 v[84:87], a[72:75], a[108:111], v[84:87]\n" \
"ds_read2_b32 a[10:11], v47 offset0:66 offset1:67\n" \
"v_cvt_pk_bf16_f32 v31, v194, v195\n" \
"v_mfma_f32_16x16x32_bf16 v[76:79], a[76:79], a[96:99], v[76:79]\n" \
"ds_read_b128 a[44:47], v50 offset:1792\n" \
"v_mul_f32 v196, s20, v196\n" \
"v_mfma_f32_16x16x32_bf16 v[80:83], a[76:79], a[100:103], v[80:83]\n" \
"buffer_store_dwordx4 v[34:37], v4, s[36:39], s97 offen offset:0 sc1\n" \
"v_mfma_f32_16x16x32_bf16 v[84:87], a[76:79], a[104:107], v[84:87]\n" \
"s_add_u32 s96, s96, s92\n" \
"ds_read2_b32 a[12:13], v47 offset0:192 offset1:193\n" \
"v_mul_f32 v197, s20, v197\n" \
"v_mfma_f32_16x16x32_bf16 v[88:91], a[76:79], a[108:111], v[88:91]\n" \
"ds_read2_b32 a[14:15], v47 offset0:194 offset1:195\n" \
"v_mul_f32 v198, s20, v198\n" \
"v_mfma_f32_16x16x32_bf16 v[80:83], a[64:67], a[112:115], v[80:83]\n" \
"ds_read_b128 a[48:51], v50 offset:2304\n" \
"v_mul_f32 v199, s20, v199\n" \
"v_mfma_f32_16x16x32_bf16 v[84:87], a[68:71], a[112:115], v[84:87]\n" \
"v_cvt_pk_bf16_f32 v32, v196, v197\n" \
"v_cvt_pk_bf16_f32 v33, v198, v199\n" \
"v_mfma_f32_16x16x32_bf16 v[88:91], a[72:75], a[112:115], v[88:91]\n" \
"ds_read2_b32 a[16:17], v48 offset0:64 offset1:65\n" \
"v_mul_f32 v200, s20, v200\n" \
"v_mfma_f32_16x16x32_bf16 v[92:95], a[76:79], a[112:115], v[92:95]\n" \
"ds_read2_b32 a[18:19], v48 offset0:66 offset1:67\n" \
"v_mul_f32 v201, s20, v201\n" \
"v_mfma_f32_16x16x32_bf16 v[80:83], a[80:83], a[96:99], v[80:83]\n" \
"ds_read_b128 a[52:55], v50 offset:2816\n" \
"v_mul_f32 v202, s20, v202\n" \
"v_mfma_f32_16x16x32_bf16 v[84:87], a[80:83], a[100:103], v[84:87]\n" \
"v_mul_f32 v203, s20, v203\n" \
"v_cvt_pk_bf16_f32 v34, v200, v201\n" \
"v_mfma_f32_16x16x32_bf16 v[88:91], a[80:83], a[104:107], v[88:91]\n" \
"ds_read2_b32 a[20:21], v48 offset0:192 offset1:193\n" \
"v_cvt_pk_bf16_f32 v35, v202, v203\n" \
"v_mfma_f32_16x16x32_bf16 v[92:95], a[80:83], a[108:111], v[92:95]\n" \
"ds_read2_b32 a[22:23], v48 offset0:194 offset1:195\n" \
"v_mul_f32 v204, s20, v204\n" \
"v_mfma_f32_16x16x32_bf16 v[84:87], a[64:67], a[116:119], v[84:87]\n" \
"ds_read_b128 a[56:59], v50 offset:3328\n" \
"v_mul_f32 v205, s20, v205\n" \
"v_mfma_f32_16x16x32_bf16 v[88:91], a[68:71], a[116:119], v[88:91]\n" \
"buffer_store_dwordx4 v[38:41], v4, s[36:39], s96 offen offset:0 sc1\n" \
"v_mfma_f32_16x16x32_bf16 v[92:95], a[72:75], a[116:119], v[92:95]\n" \
"s_add_u32 s97, s96, s98\n" \
"ds_read2_b32 a[24:25], v49 offset0:64 offset1:65\n" \
"v_mul_f32 v206, s20, v206\n" \
"v_mfma_f32_16x16x32_bf16 v[84:87], a[84:87], a[96:99], v[84:87]\n" \
"ds_read2_b32 a[26:27], v49 offset0:66 offset1:67\n" \
"v_mul_f32 v207, s20, v207\n" \
"v_mfma_f32_16x16x32_bf16 v[88:91], a[84:87], a[100:103], v[88:91]\n" \
"ds_read_b128 a[60:63], v50 offset:3840\n" \
"v_cvt_pk_bf16_f32 v36, v204, v205\n" \
"v_mfma_f32_16x16x32_bf16 v[92:95], a[84:87], a[104:107], v[92:95]\n" \
"v_cvt_pk_bf16_f32 v37, v206, v207\n" \
"v_mul_f32 v208, s20, v208\n" \
"v_mfma_f32_16x16x32_bf16 v[88:91], a[64:67], a[120:123], v[88:91]\n" \
"ds_read2_b32 a[28:29], v49 offset0:192 offset1:193\n" \
"v_mul_f32 v209, s20, v209\n" \
"v_mfma_f32_16x16x32_bf16 v[92:95], a[68:71], a[120:123], v[92:95]\n" \
"ds_read2_b32 a[30:31], v49 offset0:194 offset1:195\n" \
"v_mul_f32 v210, s20, v210\n" \
"v_mfma_f32_16x16x32_bf16 v[88:91], a[88:91], a[96:99], v[88:91]\n" \
"v_mul_f32 v211, s20, v211\n" \
"v_cvt_pk_bf16_f32 v38, v208, v209\n" \
"v_mfma_f32_16x16x32_bf16 v[92:95], a[88:91], a[100:103], v[92:95]\n" \
"v_cvt_pk_bf16_f32 v39, v210, v211\n" \
"v_mul_f32 v212, s20, v212\n" \
"v_mfma_f32_16x16x32_bf16 v[92:95], a[64:67], a[124:127], v[92:95]\n" \
"v_mul_f32 v213, s20, v213\n" \
"v_mfma_f32_16x16x32_bf16 v[92:95], a[92:95], a[96:99], v[92:95]\n" \
"s_waitcnt lgkmcnt(0)\n" \
"v_mul_f32 v214, s20, v214\n" \
"v_mul_f32 v215, s20, v215\n" \
"v_mfma_f32_16x16x32_bf16 v[64:67], a[0:3], a[32:35], v[64:67]\n" \
"ds_read_b128 a[96:99], v50 offset:192\n" \
"v_cvt_pk_bf16_f32 v40, v212, v213\n" \
"v_mfma_f32_16x16x32_bf16 v[68:71], a[0:3], a[36:39], v[68:71]\n" \
"buffer_store_dwordx4 v[42:45], v4, s[36:39], s97 offen offset:0 sc1\n" \
"v_mfma_f32_16x16x32_bf16 v[68:71], a[4:7], a[32:35], v[68:71]\n" \
"s_add_u32 s57, s57, s93\n" \
"ds_read2_b32 a[64:65], v46 offset0:80 offset1:81\n" \
"s_mov_b32 s96, s57\n" \
"v_mfma_f32_16x16x32_bf16 v[72:75], a[4:7], a[36:39], v[72:75]\n" \
"ds_read2_b32 a[66:67], v46 offset0:82 offset1:83\n" \
"v_cvt_pk_bf16_f32 v41, v214, v215\n" \
"v_mfma_f32_16x16x32_bf16 v[72:75], a[0:3], a[40:43], v[72:75]\n" \
"ds_read_b128 a[100:103], v50 offset:704\n" \
"v_mul_f32 v216, s20, v216\n" \
"v_mfma_f32_16x16x32_bf16 v[76:79], a[4:7], a[40:43], v[76:79]\n" \
"v_mul_f32 v217, s20, v217\n" \
"v_mul_f32 v218, s20, v218\n" \
"v_mfma_f32_16x16x32_bf16 v[72:75], a[8:11], a[32:35], v[72:75]\n" \
"ds_read2_b32 a[68:69], v46 offset0:208 offset1:209\n" \
"v_mul_f32 v219, s20, v219\n" \
"v_mfma_f32_16x16x32_bf16 v[76:79], a[8:11], a[36:39], v[76:79]\n" \
"ds_read2_b32 a[70:71], v46 offset0:210 offset1:211\n" \
"v_cvt_pk_bf16_f32 v42, v216, v217\n" \
"v_mfma_f32_16x16x32_bf16 v[80:83], a[8:11], a[40:43], v[80:83]\n" \
"ds_read_b128 a[104:107], v50 offset:1216\n" \
"v_cvt_pk_bf16_f32 v43, v218, v219\n" \
"v_mfma_f32_16x16x32_bf16 v[76:79], a[0:3], a[44:47], v[76:79]\n" \
"v_mul_f32 v220, s20, v220\n" \
"v_mul_f32 v221, s20, v221\n" \
"v_mfma_f32_16x16x32_bf16 v[80:83], a[4:7], a[44:47], v[80:83]\n" \
"ds_read2_b32 a[72:73], v47 offset0:80 offset1:81\n" \
"v_mul_f32 v222, s20, v222\n" \
"v_mfma_f32_16x16x32_bf16 v[84:87], a[8:11], a[44:47], v[84:87]\n" \
"ds_read2_b32 a[74:75], v47 offset0:82 offset1:83\n" \
"v_mul_f32 v223, s20, v223\n" \
"v_mfma_f32_16x16x32_bf16 v[76:79], a[12:15], a[32:35], v[76:79]\n" \
"ds_read_b128 a[108:111], v50 offset:1728\n" \
"v_cvt_pk_bf16_f32 v44, v220, v221\n" \
"v_mfma_f32_16x16x32_bf16 v[80:83], a[12:15], a[36:39], v[80:83]\n" \
"buffer_store_dwordx4 v[30:33], v4, s[36:39], s96 offen offset:0 sc1\n" \
"v_mfma_f32_16x16x32_bf16 v[84:87], a[12:15], a[40:43], v[84:87]\n" \
"s_add_u32 s97, s96, s98\n" \
"ds_read2_b32 a[76:77], v47 offset0:208 offset1:209\n" \
"v_cvt_pk_bf16_f32 v45, v222, v223\n" \
"v_mfma_f32_16x16x32_bf16 v[88:91], a[12:15], a[44:47], v[88:91]\n" \
"ds_read2_b32 a[78:79], v47 offset0:210 offset1:211\n" \
"v_mul_f32 v224, s20, v224\n" \
"v_mfma_f32_16x16x32_bf16 v[80:83], a[0:3], a[48:51], v[80:83]\n" \
"ds_read_b128 a[112:115], v50 offset:2240\n" \
"v_mul_f32 v225, s20, v225\n" \
"v_mfma_f32_16x16x32_bf16 v[84:87], a[4:7], a[48:51], v[84:87]\n" \
"v_mul_f32 v226, s20, v226\n" \
"v_mul_f32 v227, s20, v227\n" \
"v_mfma_f32_16x16x32_bf16 v[88:91], a[8:11], a[48:51], v[88:91]\n" \
"ds_read2_b32 a[80:81], v48 offset0:80 offset1:81\n" \
"v_cvt_pk_bf16_f32 v30, v224, v225\n" \
"v_mfma_f32_16x16x32_bf16 v[92:95], a[12:15], a[48:51], v[92:95]\n" \
"ds_read2_b32 a[82:83], v48 offset0:82 offset1:83\n" \
"v_cvt_pk_bf16_f32 v31, v226, v227\n" \
"v_mfma_f32_16x16x32_bf16 v[80:83], a[16:19], a[32:35], v[80:83]\n" \
"ds_read_b128 a[116:119], v50 offset:2752\n" \
"v_mul_f32 v228, s20, v228\n" \
"v_mfma_f32_16x16x32_bf16 v[84:87], a[16:19], a[36:39], v[84:87]\n" \
"v_mul_f32 v229, s20, v229\n" \
"v_mul_f32 v230, s20, v230\n" \
"v_mfma_f32_16x16x32_bf16 v[88:91], a[16:19], a[40:43], v[88:91]\n" \
"ds_read2_b32 a[84:85], v48 offset0:208 offset1:209\n" \
"v_mul_f32 v231, s20, v231\n" \
"v_mfma_f32_16x16x32_bf16 v[92:95], a[16:19], a[44:47], v[92:95]\n" \
"ds_read2_b32 a[86:87], v48 offset0:210 offset1:211\n" \
"v_cvt_pk_bf16_f32 v32, v228, v229\n" \
"v_mfma_f32_16x16x32_bf16 v[84:87], a[0:3], a[52:55], v[84:87]\n" \
"ds_read_b128 a[120:123], v50 offset:3264\n" \
"v_cvt_pk_bf16_f32 v33, v230, v231\n" \
"v_mfma_f32_16x16x32_bf16 v[88:91], a[4:7], a[52:55], v[88:91]\n" \
"buffer_store_dwordx4 v[34:37], v4, s[36:39], s97 offen offset:0 sc1\n" \
"v_mfma_f32_16x16x32_bf16 v[92:95], a[8:11], a[52:55], v[92:95]\n" \
"s_add_u32 s96, s96, s92\n" \
"ds_read2_b32 a[88:89], v49 offset0:80 offset1:81\n" \
"v_mul_f32 v232, s20, v232\n" \
"v_mfma_f32_16x16x32_bf16 v[84:87], a[20:23], a[32:35], v[84:87]\n" \
"ds_read2_b32 a[90:91], v49 offset0:82 offset1:83\n" \
"v_mul_f32 v233, s20, v233\n" \
"v_mfma_f32_16x16x32_bf16 v[88:91], a[20:23], a[36:39], v[88:91]\n" \
"ds_read_b128 a[124:127], v50 offset:3776\n" \
"v_mul_f32 v234, s20, v234\n" \
"v_mfma_f32_16x16x32_bf16 v[92:95], a[20:23], a[40:43], v[92:95]\n" \
"v_mul_f32 v235, s20, v235\n" \
"v_cvt_pk_bf16_f32 v34, v232, v233\n" \
"v_mfma_f32_16x16x32_bf16 v[88:91], a[0:3], a[56:59], v[88:91]\n" \
"ds_read2_b32 a[92:93], v49 offset0:208 offset1:209\n" \
"v_cvt_pk_bf16_f32 v35, v234, v235\n" \
"v_mfma_f32_16x16x32_bf16 v[92:95], a[4:7], a[56:59], v[92:95]\n" \
"ds_read2_b32 a[94:95], v49 offset0:210 offset1:211\n" \
"v_mul_f32 v236, s20, v236\n" \
"v_mfma_f32_16x16x32_bf16 v[88:91], a[24:27], a[32:35], v[88:91]\n" \
"v_mul_f32 v237, s20, v237\n" \
"v_mul_f32 v238, s20, v238\n" \
"v_mfma_f32_16x16x32_bf16 v[92:95], a[24:27], a[36:39], v[92:95]\n" \
"v_mul_f32 v239, s20, v239\n" \
"v_cvt_pk_bf16_f32 v36, v236, v237\n" \
"v_mfma_f32_16x16x32_bf16 v[92:95], a[0:3], a[60:63], v[92:95]\n" \
"v_cvt_pk_bf16_f32 v37, v238, v239\n" \
"v_mfma_f32_16x16x32_bf16 v[92:95], a[28:31], a[32:35], v[92:95]\n" \
"s_waitcnt lgkmcnt(0)\n" \
"v_mul_f32 v240, s20, v240\n" \
"v_mul_f32 v241, s20, v241\n" \
"v_mfma_f32_16x16x32_bf16 v[64:67], a[64:67], a[96:99], v[64:67]\n" \
"ds_read_b128 a[32:35], v50 offset:128\n" \
"v_mul_f32 v242, s20, v242\n" \
"v_mfma_f32_16x16x32_bf16 v[68:71], a[64:67], a[100:103], v[68:71]\n" \
"buffer_store_dwordx4 v[38:41], v4, s[36:39], s96 offen offset:0 sc1\n" \
"v_mfma_f32_16x16x32_bf16 v[68:71], a[68:71], a[96:99], v[68:71]\n" \
"s_add_u32 s97, s96, s98\n" \
"ds_read2_b32 a[0:1], v46 offset0:96 offset1:97\n" \
"v_mul_f32 v243, s20, v243\n" \
"v_mfma_f32_16x16x32_bf16 v[72:75], a[68:71], a[100:103], v[72:75]\n" \
"ds_read2_b32 a[2:3], v46 offset0:98 offset1:99\n" \
"v_cvt_pk_bf16_f32 v38, v240, v241\n" \
"v_mfma_f32_16x16x32_bf16 v[72:75], a[64:67], a[104:107], v[72:75]\n" \
"ds_read_b128 a[36:39], v50 offset:640\n" \
"v_cvt_pk_bf16_f32 v39, v242, v243\n" \
"v_mfma_f32_16x16x32_bf16 v[76:79], a[68:71], a[104:107], v[76:79]\n" \
"v_mul_f32 v244, s20, v244\n" \
"v_mul_f32 v245, s20, v245\n" \
"v_mfma_f32_16x16x32_bf16 v[72:75], a[72:75], a[96:99], v[72:75]\n" \
"ds_read2_b32 a[4:5], v46 offset0:224 offset1:225\n" \
"v_mul_f32 v246, s20, v246\n" \
"v_mfma_f32_16x16x32_bf16 v[76:79], a[72:75], a[100:103], v[76:79]\n" \
"ds_read2_b32 a[6:7], v46 offset0:226 offset1:227\n" \
"v_mul_f32 v247, s20, v247\n" \
"v_mfma_f32_16x16x32_bf16 v[80:83], a[72:75], a[104:107], v[80:83]\n" \
"ds_read_b128 a[40:43], v50 offset:1152\n" \
"v_cvt_pk_bf16_f32 v40, v244, v245\n" \
"v_mfma_f32_16x16x32_bf16 v[76:79], a[64:67], a[108:111], v[76:79]\n" \
"v_cvt_pk_bf16_f32 v41, v246, v247\n" \
"v_mul_f32 v248, s20, v248\n" \
"v_mfma_f32_16x16x32_bf16 v[80:83], a[68:71], a[108:111], v[80:83]\n" \
"ds_read2_b32 a[8:9], v47 offset0:96 offset1:97\n" \
"v_mul_f32 v249, s20, v249\n" \
"v_mfma_f32_16x16x32_bf16 v[84:87], a[72:75], a[108:111], v[84:87]\n" \
"ds_read2_b32 a[10:11], v47 offset0:98 offset1:99\n" \
"v_mul_f32 v250, s20, v250\n" \
"v_mfma_f32_16x16x32_bf16 v[76:79], a[76:79], a[96:99], v[76:79]\n" \
"ds_read_b128 a[44:47], v50 offset:1664\n" \
"v_mul_f32 v251, s20, v251\n" \
"v_mfma_f32_16x16x32_bf16 v[80:83], a[76:79], a[100:103], v[80:83]\n" \
"buffer_store_dwordx4 v[42:45], v4, s[36:39], s97 offen offset:0 sc1\n" \
"v_mfma_f32_16x16x32_bf16 v[84:87], a[76:79], a[104:107], v[84:87]\n" \
"s_add_u32 s96, s96, s92\n" \
"ds_read2_b32 a[12:13], v47 offset0:224 offset1:225\n" \
"v_cvt_pk_bf16_f32 v42, v248, v249\n" \
"v_mfma_f32_16x16x32_bf16 v[88:91], a[76:79], a[108:111], v[88:91]\n" \
"ds_read2_b32 a[14:15], v47 offset0:226 offset1:227\n" \
"v_cvt_pk_bf16_f32 v43, v250, v251\n" \
"v_mfma_f32_16x16x32_bf16 v[80:83], a[64:67], a[112:115], v[80:83]\n" \
"ds_read_b128 a[48:51], v50 offset:2176\n" \
"v_mul_f32 v252, s20, v252\n" \
"v_mfma_f32_16x16x32_bf16 v[84:87], a[68:71], a[112:115], v[84:87]\n" \
"v_mul_f32 v253, s20, v253\n" \
"v_mul_f32 v254, s20, v254\n" \
"v_mfma_f32_16x16x32_bf16 v[88:91], a[72:75], a[112:115], v[88:91]\n" \
"ds_read2_b32 a[16:17], v48 offset0:96 offset1:97\n" \
"v_mul_f32 v255, s20, v255\n" \
"v_mfma_f32_16x16x32_bf16 v[92:95], a[76:79], a[112:115], v[92:95]\n" \
"ds_read2_b32 a[18:19], v48 offset0:98 offset1:99\n" \
"v_cvt_pk_bf16_f32 v44, v252, v253\n" \
"v_mfma_f32_16x16x32_bf16 v[80:83], a[80:83], a[96:99], v[80:83]\n" \
"ds_read_b128 a[52:55], v50 offset:2688\n" \
"v_cvt_pk_bf16_f32 v45, v254, v255\n" \
"v_mfma_f32_16x16x32_bf16 v[84:87], a[80:83], a[100:103], v[84:87]\n" \
"v_mfma_f32_16x16x32_bf16 v[88:91], a[80:83], a[104:107], v[88:91]\n" \
"ds_read2_b32 a[20:21], v48 offset0:224 offset1:225\n" \
"v_mfma_f32_16x16x32_bf16 v[92:95], a[80:83], a[108:111], v[92:95]\n" \
"ds_read2_b32 a[22:23], v48 offset0:226 offset1:227\n" \
"v_mfma_f32_16x16x32_bf16 v[84:87], a[64:67], a[116:119], v[84:87]\n" \
"ds_read_b128 a[56:59], v50 offset:3200\n" \
"v_mfma_f32_16x16x32_bf16 v[88:91], a[68:71], a[116:119], v[88:91]\n" \
"buffer_store_dwordx4 v[30:33], v4, s[36:39], s96 offen offset:0 sc1\n" \
"v_mfma_f32_16x16x32_bf16 v[92:95], a[72:75], a[116:119], v[92:95]\n" \
"s_add_u32 s97, s96, s98\n" \
"ds_read2_b32 a[24:25], v49 offset0:96 offset1:97\n" \
"v_mfma_f32_16x16x32_bf16 v[84:87], a[84:87], a[96:99], v[84:87]\n" \
"ds_read2_b32 a[26:27], v49 offset0:98 offset1:99\n" \
"v_mfma_f32_16x16x32_bf16 v[88:91], a[84:87], a[100:103], v[88:91]\n" \
"ds_read_b128 a[60:63], v50 offset:3712\n" \
"v_mfma_f32_16x16x32_bf16 v[92:95], a[84:87], a[104:107], v[92:95]\n" \
"v_mfma_f32_16x16x32_bf16 v[88:91], a[64:67], a[120:123], v[88:91]\n" \
"ds_read2_b32 a[28:29], v49 offset0:224 offset1:225\n" \
"v_mfma_f32_16x16x32_bf16 v[92:95], a[68:71], a[120:123], v[92:95]\n" \
"ds_read2_b32 a[30:31], v49 offset0:226 offset1:227\n" \
"v_mfma_f32_16x16x32_bf16 v[88:91], a[88:91], a[96:99], v[88:91]\n" \
"v_mfma_f32_16x16x32_bf16 v[92:95], a[88:91], a[100:103], v[92:95]\n" \
"v_mfma_f32_16x16x32_bf16 v[92:95], a[64:67], a[124:127], v[92:95]\n" \
"v_mfma_f32_16x16x32_bf16 v[92:95], a[92:95], a[96:99], v[92:95]\n" \
"s_waitcnt lgkmcnt(0)\n" \
"v_mfma_f32_16x16x32_bf16 v[64:67], a[0:3], a[32:35], v[64:67]\n" \
"ds_read_b128 a[96:99], v50 offset:64\n" \
"v_mfma_f32_16x16x32_bf16 v[68:71], a[0:3], a[36:39], v[68:71]\n" \
"buffer_store_dwordx4 v[34:37], v4, s[36:39], s97 offen offset:0 sc1\n" \
"v_mfma_f32_16x16x32_bf16 v[68:71], a[4:7], a[32:35], v[68:71]\n" \
"s_add_u32 s96, s96, s92\n" \
"ds_read2_b32 a[64:65], v46 offset0:112 offset1:113\n" \
"v_mfma_f32_16x16x32_bf16 v[72:75], a[4:7], a[36:39], v[72:75]\n" \
"ds_read2_b32 a[66:67], v46 offset0:114 offset1:115\n" \
"v_mfma_f32_16x16x32_bf16 v[72:75], a[0:3], a[40:43], v[72:75]\n" \
"ds_read_b128 a[100:103], v50 offset:576\n" \
"v_mfma_f32_16x16x32_bf16 v[76:79], a[4:7], a[40:43], v[76:79]\n" \
"v_mfma_f32_16x16x32_bf16 v[72:75], a[8:11], a[32:35], v[72:75]\n" \
"ds_read2_b32 a[68:69], v46 offset0:240 offset1:241\n" \
"v_mfma_f32_16x16x32_bf16 v[76:79], a[8:11], a[36:39], v[76:79]\n" \
"ds_read2_b32 a[70:71], v46 offset0:242 offset1:243\n" \
"v_mfma_f32_16x16x32_bf16 v[80:83], a[8:11], a[40:43], v[80:83]\n" \
"ds_read_b128 a[104:107], v50 offset:1088\n" \
"v_mfma_f32_16x16x32_bf16 v[76:79], a[0:3], a[44:47], v[76:79]\n" \
"v_mfma_f32_16x16x32_bf16 v[80:83], a[4:7], a[44:47], v[80:83]\n" \
"ds_read2_b32 a[72:73], v47 offset0:112 offset1:113\n" \
"v_mfma_f32_16x16x32_bf16 v[84:87], a[8:11], a[44:47], v[84:87]\n" \
"ds_read2_b32 a[74:75], v47 offset0:114 offset1:115\n" \
"v_mfma_f32_16x16x32_bf16 v[76:79], a[12:15], a[32:35], v[76:79]\n" \
"ds_read_b128 a[108:111], v50 offset:1600\n" \
"v_mfma_f32_16x16x32_bf16 v[80:83], a[12:15], a[36:39], v[80:83]\n" \
"buffer_store_dwordx4 v[38:41], v4, s[36:39], s96 offen offset:0 sc1\n" \
"v_mfma_f32_16x16x32_bf16 v[84:87], a[12:15], a[40:43], v[84:87]\n" \
"s_add_u32 s97, s96, s98\n" \
"ds_read2_b32 a[76:77], v47 offset0:240 offset1:241\n" \
"v_mfma_f32_16x16x32_bf16 v[88:91], a[12:15], a[44:47], v[88:91]\n" \
"ds_read2_b32 a[78:79], v47 offset0:242 offset1:243\n" \
"v_mfma_f32_16x16x32_bf16 v[80:83], a[0:3], a[48:51], v[80:83]\n" \
"ds_read_b128 a[112:115], v50 offset:2112\n" \
"v_mfma_f32_16x16x32_bf16 v[84:87], a[4:7], a[48:51], v[84:87]\n" \
"v_mfma_f32_16x16x32_bf16 v[88:91], a[8:11], a[48:51], v[88:91]\n" \
"ds_read2_b32 a[80:81], v48 offset0:112 offset1:113\n" \
"v_mfma_f32_16x16x32_bf16 v[92:95], a[12:15], a[48:51], v[92:95]\n" \
"ds_read2_b32 a[82:83], v48 offset0:114 offset1:115\n" \
"v_mfma_f32_16x16x32_bf16 v[80:83], a[16:19], a[32:35], v[80:83]\n" \
"ds_read_b128 a[116:119], v50 offset:2624\n" \
"v_mfma_f32_16x16x32_bf16 v[84:87], a[16:19], a[36:39], v[84:87]\n" \
"v_mfma_f32_16x16x32_bf16 v[88:91], a[16:19], a[40:43], v[88:91]\n" \
"ds_read2_b32 a[84:85], v48 offset0:240 offset1:241\n" \
"v_mfma_f32_16x16x32_bf16 v[92:95], a[16:19], a[44:47], v[92:95]\n" \
"ds_read2_b32 a[86:87], v48 offset0:242 offset1:243\n" \
"v_mfma_f32_16x16x32_bf16 v[84:87], a[0:3], a[52:55], v[84:87]\n" \
"ds_read_b128 a[120:123], v50 offset:3136\n" \
"v_mfma_f32_16x16x32_bf16 v[88:91], a[4:7], a[52:55], v[88:91]\n" \
"buffer_store_dwordx4 v[42:45], v4, s[36:39], s97 offen offset:0 sc1\n" \
"v_mfma_f32_16x16x32_bf16 v[92:95], a[8:11], a[52:55], v[92:95]\n" \
"s_add_u32 s57, s57, s93\n" \
"ds_read2_b32 a[88:89], v49 offset0:112 offset1:113\n" \
"s_mov_b32 s96, s57\n" \
"v_mfma_f32_16x16x32_bf16 v[84:87], a[20:23], a[32:35], v[84:87]\n" \
"ds_read2_b32 a[90:91], v49 offset0:114 offset1:115\n" \
"v_mfma_f32_16x16x32_bf16 v[88:91], a[20:23], a[36:39], v[88:91]\n" \
"ds_read_b128 a[124:127], v50 offset:3648\n" \
"v_mfma_f32_16x16x32_bf16 v[92:95], a[20:23], a[40:43], v[92:95]\n" \
"v_mfma_f32_16x16x32_bf16 v[88:91], a[0:3], a[56:59], v[88:91]\n" \
"ds_read2_b32 a[92:93], v49 offset0:240 offset1:241\n" \
"v_mfma_f32_16x16x32_bf16 v[92:95], a[4:7], a[56:59], v[92:95]\n" \
"ds_read2_b32 a[94:95], v49 offset0:242 offset1:243\n" \
"v_mfma_f32_16x16x32_bf16 v[88:91], a[24:27], a[32:35], v[88:91]\n" \
"v_mfma_f32_16x16x32_bf16 v[92:95], a[24:27], a[36:39], v[92:95]\n" \
"v_mfma_f32_16x16x32_bf16 v[92:95], a[0:3], a[60:63], v[92:95]\n" \
"v_mfma_f32_16x16x32_bf16 v[92:95], a[28:31], a[32:35], v[92:95]\n" \
"s_waitcnt lgkmcnt(0)\n" \
"v_mfma_f32_16x16x32_bf16 v[64:67], a[64:67], a[96:99], v[64:67]\n" \
"s_lshl_b32 s59, %2, 10\n" \
"ds_read_b64 v[192:193], v51 offset:10816\n" \
"v_add_u32 v243, s59, v4\n" \
"v_mfma_f32_16x16x32_bf16 v[68:71], a[64:67], a[100:103], v[68:71]\n" \
"LH9_%=:\ns_cbranch_execz LH13_%=\n" \
"ds_read_b64 v[194:195], v51 offset:11328\n" \
"v_and_b32 v240, 0x70, v243\n" \
"v_mfma_f32_16x16x32_bf16 v[68:71], a[68:71], a[96:99], v[68:71]\n" \
"ds_read_b64 v[196:197], v51 offset:11840\n" \
"v_lshrrev_b32 v243, 7, v243\n" \
"v_mfma_f32_16x16x32_bf16 v[72:75], a[68:71], a[100:103], v[72:75]\n" \
"ds_read_b64 v[198:199], v51 offset:12352\n" \
"v_lshl_add_u32 v240, v243, 8, v240\n" \
"v_mfma_f32_16x16x32_bf16 v[72:75], a[64:67], a[104:107], v[72:75]\n" \
"ds_read_b64 v[200:201], v51 offset:12864\n" \
"v_lshrrev_b32 v241, 2, v240\n" \
"v_mfma_f32_16x16x32_bf16 v[76:79], a[68:71], a[104:107], v[76:79]\n" \
"s_lshl_b32 s59, s8, 14\n" \
"ds_read_b64 v[202:203], v51 offset:13376\n" \
"v_add_u32 v242, 32, v241\n" \
"v_mfma_f32_16x16x32_bf16 v[72:75], a[72:75], a[96:99], v[72:75]\n" \
"s_add_u32 s59, s59, 67108864\n" \
"ds_read_b64 v[204:205], v51 offset:13888\n" \
"s_add_u32 s60, s59, 8192\n" \
"v_mfma_f32_16x16x32_bf16 v[76:79], a[72:75], a[100:103], v[76:79]\n" \
"s_mov_b32 s94, 0x05040100\n" \
"ds_read_b64 v[206:207], v51 offset:14400\n" \
"s_mov_b32 s95, 0x07060302\n" \
"v_mfma_f32_16x16x32_bf16 v[80:83], a[72:75], a[104:107], v[80:83]\n" \
"v_mfma_f32_16x16x32_bf16 v[76:79], a[64:67], a[108:111], v[76:79]\n" \
"v_mfma_f32_16x16x32_bf16 v[80:83], a[68:71], a[108:111], v[80:83]\n" \
"v_mfma_f32_16x16x32_bf16 v[84:87], a[72:75], a[108:111], v[84:87]\n" \
"s_waitcnt vmcnt(36) lgkmcnt(0)\n" \
"v_mfma_f32_16x16x32_bf16 v[76:79], a[76:79], a[96:99], v[76:79]\n" \
"s_barrier\n" \
"ds_read_b64_tr_b16 a[24:25], v9 offset:33280\n" \
"v_and_b32 v208, 0xffff0000, v193\n" \
"v_mfma_f32_16x16x32_bf16 v[80:83], a[76:79], a[100:103], v[80:83]\n" \
"ds_read_b64_tr_b16 a[26:27], v10 offset:33280\n" \
"v_lshlrev_b32 v209, 16, v193\n" \
"v_mfma_f32_16x16x32_bf16 v[84:87], a[76:79], a[104:107], v[84:87]\n" \
"ds_read_b64_tr_b16 a[28:29], v11 offset:33280\n" \
"v_and_b32 v210, 0xffff0000, v192\n" \
"v_mfma_f32_16x16x32_bf16 v[88:91], a[76:79], a[108:111], v[88:91]\n" \
"ds_read_b64_tr_b16 a[30:31], v12 offset:33280\n" \
"v_lshlrev_b32 v211, 16, v192\n" \
"v_mfma_f32_16x16x32_bf16 v[80:83], a[64:67], a[112:115], v[80:83]\n" \
"s_mov_b32 s46, 256\n" \
"s_add_u32 m0, s50, 32768\n" \
"ds_read_b128 a[0:3], v7 offset:0\n" \
"v_mfma_f32_16x16x32_bf16 v[84:87], a[68:71], a[112:115], v[84:87]\n" \
"buffer_load_dwordx4 v13, s[28:31], s46 offen lds\n" \
"v_mfma_f32_16x16x32_bf16 v[88:91], a[72:75], a[112:115], v[88:91]\n" \
"ds_read_b128 a[4:7], v7 offset:2048\n" \
"v_fma_f32 v208, v208, v28, v64\n" \
"v_mfma_f32_16x16x32_bf16 v[92:95], a[76:79], a[112:115], v[92:95]\n" \
"ds_read_b128 a[8:11], v7 offset:4096\n" \
"v_fma_f32 v209, v209, v28, v65\n" \
"v_mfma_f32_16x16x32_bf16 v[80:83], a[80:83], a[96:99], v[80:83]\n" \
"ds_read_b128 a[12:15], v7 offset:6144\n" \
"v_fma_f32 v210, v210, v28, v66\n" \
"v_mfma_f32_16x16x32_bf16 v[84:87], a[80:83], a[100:103], v[84:87]\n" \
"ds_read_b64_tr_b16 a[32:33], v9 offset:33792\n" \
"v_fma_f32 v211, v211, v28, v67\n" \
"v_mfma_f32_16x16x32_bf16 v[88:91], a[80:83], a[104:107], v[88:91]\n" \
"s_add_u32 m0, s50, 36864\n" \
"ds_read_b64_tr_b16 a[34:35], v10 offset:33792\n" \
"v_and_b32 v212, 0xffff0000, v195\n" \
"v_mfma_f32_16x16x32_bf16 v[92:95], a[80:83], a[108:111], v[92:95]\n" \
"buffer_load_dwordx4 v14, s[28:31], s46 offen lds\n" \
"v_mfma_f32_16x16x32_bf16 v[84:87], a[64:67], a[116:119], v[84:87]\n" \
"ds_read_b64_tr_b16 a[36:37], v11 offset:33792\n" \
"v_lshlrev_b32 v213, 16, v195\n" \
"v_mfma_f32_16x16x32_bf16 v[88:91], a[68:71], a[116:119], v[88:91]\n" \
"ds_read_b64_tr_b16 a[38:39], v12 offset:33792\n" \
"v_and_b32 v214, 0xffff0000, v194\n" \
"v_mfma_f32_16x16x32_bf16 v[92:95], a[72:75], a[116:119], v[92:95]\n" \
"ds_read_b64_tr_b16 a[40:41], v9 offset:34304\n" \
"v_lshlrev_b32 v215, 16, v194\n" \
"v_mfma_f32_16x16x32_bf16 v[84:87], a[84:87], a[96:99], v[84:87]\n" \
"ds_read_b64_tr_b16 a[42:43], v10 offset:34304\n" \
"v_fma_f32 v212, v212, v28, v68\n" \
"v_mfma_f32_16x16x32_bf16 v[88:91], a[84:87], a[100:103], v[88:91]\n" \
"s_add_u32 m0, s50, 40960\n" \
"ds_read_b64_tr_b16 a[44:45], v11 offset:34304\n" \
"v_fma_f32 v213, v213, v28, v69\n" \
"v_mfma_f32_16x16x32_bf16 v[92:95], a[84:87], a[104:107], v[92:95]\n" \
"buffer_load_dwordx4 v15, s[28:31], s46 offen lds\n" \
"v_mfma_f32_16x16x32_bf16 v[88:91], a[64:67], a[120:123], v[88:91]\n" \
"ds_read_b64_tr_b16 a[46:47], v12 offset:34304\n" \
"v_fma_f32 v214, v214, v28, v70\n" \
"v_mfma_f32_16x16x32_bf16 v[92:95], a[68:71], a[120:123], v[92:95]\n" \
"ds_read_b64_tr_b16 a[16:17], v9 offset:32768\n" \
"v_fma_f32 v215, v215, v28, v71\n" \
"v_mfma_f32_16x16x32_bf16 v[88:91], a[88:91], a[96:99], v[88:91]\n" \
"ds_read_b64_tr_b16 a[18:19], v10 offset:32768\n" \
"v_and_b32 v216, 0xffff0000, v197\n" \
"v_mfma_f32_16x16x32_bf16 v[92:95], a[88:91], a[100:103], v[92:95]\n" \
"ds_read_b64_tr_b16 a[20:21], v11 offset:32768\n" \
"v_lshlrev_b32 v217, 16, v197\n" \
"v_mfma_f32_16x16x32_bf16 v[92:95], a[64:67], a[124:127], v[92:95]\n" \
"ds_read_b64_tr_b16 a[22:23], v12 offset:32768\n" \
"v_and_b32 v218, 0xffff0000, v196\n" \
"v_mfma_f32_16x16x32_bf16 v[92:95], a[92:95], a[96:99], v[92:95]\n" \
"s_waitcnt lgkmcnt(8)\n" \
"v_lshlrev_b32 v219, 16, v196\n" \
"v_fma_f32 v216, v216, v28, v72\n" \
"v_mfma_f32_16x16x32_bf16 v[96:99], a[24:27], a[0:3], 0\n" \
"s_add_u32 m0, s50, 45056\n" \
"ds_read_b64_tr_b16 a[72:73], v9 offset:41472\n" \
"v_fma_f32 v217, v217, v28, v73\n" \
"v_mfma_f32_16x16x32_bf16 v[100:103], a[28:31], a[0:3], 0\n" \
"buffer_load_dwordx4 v16, s[28:31], s46 offen lds\n" \
"v_mfma_f32_16x16x32_bf16 v[104:107], a[24:27], a[4:7], 0\n" \
"ds_read_b64_tr_b16 a[74:75], v10 offset:41472\n" \
"v_fma_f32 v218, v218, v28, v74\n" \
"v_mfma_f32_16x16x32_bf16 v[108:111], a[28:31], a[4:7], 0\n" \
"ds_read_b64_tr_b16 a[76:77], v11 offset:41472\n" \
"v_fma_f32 v219, v219, v28, v75\n" \
"v_mfma_f32_16x16x32_bf16 v[112:115], a[24:27], a[8:11], 0\n" \
"s_mov_b32 s46, 256\n" \
"s_add_u32 m0, s50, 49152\n" \
"ds_read_b64_tr_b16 a[78:79], v12 offset:41472\n" \
"v_mfma_f32_16x16x32_bf16 v[116:119], a[28:31], a[8:11], 0\n" \
"buffer_load_dwordx4 v17, s[28:31], s46 offen lds\n" \
"v_mfma_f32_16x16x32_bf16 v[120:123], a[24:27], a[12:15], 0\n" \
"ds_read_b128 a[48:51], v8 offset:0\n" \
"v_and_b32 v220, 0xffff0000, v199\n" \
"v_mfma_f32_16x16x32_bf16 v[124:127], a[28:31], a[12:15], 0\n" \
"ds_read_b128 a[52:55], v8 offset:2048\n" \
"s_waitcnt lgkmcnt(6)\n" \
"v_mfma_f32_16x16x32_bf16 v[128:131], a[32:35], a[0:3], 0\n" \
"s_add_u32 m0, s50, 53248\n" \
"ds_read_b128 a[56:59], v8 offset:4096\n" \
"v_lshlrev_b32 v221, 16, v199\n" \
"v_mfma_f32_16x16x32_bf16 v[132:135], a[36:39], a[0:3], 0\n" \
"buffer_load_dwordx4 v18, s[28:31], s46 offen lds\n" \
"v_mfma_f32_16x16x32_bf16 v[136:139], a[32:35], a[4:7], 0\n" \
"ds_read_b128 a[60:63], v8 offset:6144\n" \
"v_and_b32 v222, 0xffff0000, v198\n" \
"v_mfma_f32_16x16x32_bf16 v[140:143], a[36:39], a[4:7], 0\n" \
"ds_read_b64_tr_b16 a[80:81], v9 offset:41984\n" \
"v_lshlrev_b32 v223, 16, v198\n" \
"v_mfma_f32_16x16x32_bf16 v[144:147], a[32:35], a[8:11], 0\n" \
"s_add_u32 m0, s50, 57344\n" \
"ds_read_b64_tr_b16 a[82:83], v10 offset:41984\n" \
"v_fma_f32 v220, v220, v28, v76\n" \
"v_mfma_f32_16x16x32_bf16 v[148:151], a[36:39], a[8:11], 0\n" \
"buffer_load_dwordx4 v19, s[28:31], s46 offen lds\n" \
"v_mfma_f32_16x16x32_bf16 v[152:155], a[32:35], a[12:15], 0\n" \
"ds_read_b64_tr_b16 a[84:85], v11 offset:41984\n" \
"v_fma_f32 v221, v221, v28, v77\n" \
"v_mfma_f32_16x16x32_bf16 v[156:159], a[36:39], a[12:15], 0\n" \
"ds_read_b64_tr_b16 a[86:87], v12 offset:41984\n" \
"v_fma_f32 v222, v222, v28, v78\n" \
"v_mfma_f32_16x16x32_bf16 v[160:163], a[40:43], a[0:3], 0\n" \
"s_add_u32 m0, s50, 61440\n" \
"ds_read_b64_tr_b16 a[88:89], v9 offset:42496\n" \
"v_fma_f32 v223, v223, v28, v79\n" \
"v_mfma_f32_16x16x32_bf16 v[164:167], a[44:47], a[0:3], 0\n" \
"buffer_load_dwordx4 v20, s[28:31], s46 offen lds\n" \
"v_mfma_f32_16x16x32_bf16 v[168:171], a[40:43], a[4:7], 0\n" \
"ds_read_b64_tr_b16 a[90:91], v10 offset:42496\n" \
"v_and_b32 v224, 0xffff0000, v201\n" \
"v_mfma_f32_16x16x32_bf16 v[172:175], a[44:47], a[4:7], 0\n" \
"ds_read_b64_tr_b16 a[92:93], v11 offset:42496\n" \
"v_lshlrev_b32 v225, 16, v201\n" \
"v_mfma_f32_16x16x32_bf16 v[176:179], a[40:43], a[8:11], 0\n" \
"s_mov_b32 s47, 1048576\n" \
"s_add_u32 m0, s51, 0\n" \
"ds_read_b64_tr_b16 a[94:95], v12 offset:42496\n" \
"v_mfma_f32_16x16x32_bf16 v[180:183], a[44:47], a[8:11], 0\n" \
"buffer_load_dwordx4 v21, s[32:35], s47 offen offset:0 lds\n" \
"v_mfma_f32_16x16x32_bf16 v[184:187], a[40:43], a[12:15], 0\n" \
"ds_read_b64_tr_b16 a[64:65], v9 offset:40960\n" \
"v_and_b32 v226, 0xffff0000, v200\n" \
"v_mfma_f32_16x16x32_bf16 v[188:191], a[44:47], a[12:15], 0\n" \
"ds_read_b64_tr_b16 a[66:67], v10 offset:40960\n" \
"v_lshlrev_b32 v227, 16, v200\n" \
"v_mfma_f32_16x16x32_bf16 v[64:67], a[16:19], a[0:3], 0\n" \
"s_add_u32 m0, s51, 896\n" \
"ds_read_b64_tr_b16 a[68:69], v11 offset:40960\n" \
"v_fma_f32 v224, v224, v28, v80\n" \
"v_mfma_f32_16x16x32_bf16 v[68:71], a[20:23], a[0:3], 0\n" \
"buffer_load_dwordx4 v21, s[32:35], s47 offen offset:128 lds\n" \
"v_mfma_f32_16x16x32_bf16 v[72:75], a[16:19], a[4:7], 0\n" \
"ds_read_b64_tr_b16 a[70:71], v12 offset:40960\n" \
"v_fma_f32 v225, v225, v28, v81\n" \
"v_fma_f32 v226, v226, v28, v82\n" \
"v_fma_f32 v227, v227, v28, v83\n" \
"v_mfma_f32_16x16x32_bf16 v[76:79], a[20:23], a[4:7], 0\n" \
"v_and_b32 v228, 0xffff0000, v203\n" \
"v_lshlrev_b32 v229, 16, v203\n" \
"v_and_b32 v230, 0xffff0000, v202\n" \
"v_lshlrev_b32 v231, 16, v202\n" \
"v_fma_f32 v228, v228, v28, v84\n" \
"v_fma_f32 v229, v229, v28, v85\n" \
"v_fma_f32 v230, v230, v28, v86\n" \
"v_fma_f32 v231, v231, v28, v87\n" \
"v_mfma_f32_16x16x32_bf16 v[80:83], a[16:19], a[8:11], 0\n" \
"v_and_b32 v232, 0xffff0000, v205\n" \
"v_lshlrev_b32 v233, 16, v205\n" \
"v_and_b32 v234, 0xffff0000, v204\n" \
"v_lshlrev_b32 v235, 16, v204\n" \
"v_fma_f32 v232, v232, v28, v88\n" \
"v_fma_f32 v233, v233, v28, v89\n" \
"v_fma_f32 v234, v234, v28, v90\n" \
"v_fma_f32 v235, v235, v28, v91\n" \
"v_mfma_f32_16x16x32_bf16 v[84:87], a[20:23], a[8:11], 0\n" \
"v_and_b32 v236, 0xffff0000, v207\n" \
"v_lshlrev_b32 v237, 16, v207\n" \
"v_and_b32 v238, 0xffff0000, v206\n" \
"v_lshlrev_b32 v239, 16, v206\n" \
"v_fma_f32 v236, v236, v28, v92\n" \
"v_fma_f32 v237, v237, v28, v93\n" \
"v_fma_f32 v238, v238, v28, v94\n" \
"v_fma_f32 v239, v239, v28, v95\n" \
"v_mfma_f32_16x16x32_bf16 v[88:91], a[16:19], a[12:15], 0\n" \
"s_waitcnt vmcnt(28) lgkmcnt(0)\n" \
"v_cvt_pk_bf16_f32 v192, v208, v209\n" \
"v_mfma_f32_16x16x32_bf16 v[92:95], a[20:23], a[12:15], 0\n" \
"s_barrier\n" \
"v_cvt_pk_bf16_f32 v193, v210, v211\n" \
"ds_write_b64 v55, v[192:193] offset:16384\n" \
"v_mfma_f32_16x16x32_bf16 v[96:99], a[72:75], a[48:51], v[96:99]\n" \
"s_mov_b32 s47, 1310720\n" \
"s_add_u32 m0, s51, 8192\n" \
"ds_read_b64_tr_b16 a[24:25], v9 offset:49664\n" \
"v_mfma_f32_16x16x32_bf16 v[100:103], a[76:79], a[48:51], v[100:103]\n" \
"buffer_load_dwordx4 v21, s[32:35], s47 offen offset:0 lds\n" \
"v_mfma_f32_16x16x32_bf16 v[104:107], a[72:75], a[52:55], v[104:107]\n" \
"ds_read_b64_tr_b16 a[26:27], v10 offset:49664\n" \
"v_cvt_pk_bf16_f32 v194, v212, v213\n" \
"v_mfma_f32_16x16x32_bf16 v[108:111], a[76:79], a[52:55], v[108:111]\n" \
"ds_read_b64_tr_b16 a[28:29], v11 offset:49664\n" \
"v_cvt_pk_bf16_f32 v195, v214, v215\n" \
"v_mfma_f32_16x16x32_bf16 v[112:115], a[72:75], a[56:59], v[112:115]\n" \
"s_add_u32 m0, s51, 9088\n" \
"ds_read_b64_tr_b16 a[30:31], v12 offset:49664\n" \
"v_accvgpr_read_b32 v46, a128\n" \
"v_mfma_f32_16x16x32_bf16 v[116:119], a[76:79], a[56:59], v[116:119]\n" \
"buffer_load_dwordx4 v21, s[32:35], s47 offen offset:128 lds\n" \
"v_mfma_f32_16x16x32_bf16 v[120:123], a[72:75], a[60:63], v[120:123]\n" \
"ds_read_b128 a[0:3], v5 offset:0\n" \
"v_accvgpr_read_b32 v47, a129\n" \
"v_mfma_f32_16x16x32_bf16 v[124:127], a[76:79], a[60:63], v[124:127]\n" \
"ds_read_b128 a[4:7], v5 offset:2048\n" \
"v_accvgpr_read_b32 v48, a130\n" \
"v_mfma_f32_16x16x32_bf16 v[128:131], a[80:83], a[48:51], v[128:131]\n" \
"s_mov_b32 s46, 384\n" \
"s_add_u32 m0, s50, 65536\n" \
"ds_read_b128 a[8:11], v5 offset:4096\n" \
"v_mfma_f32_16x16x32_bf16 v[132:135], a[84:87], a[48:51], v[132:135]\n" \
"buffer_load_dwordx4 v13, s[28:31], s46 offen lds\n" \
"v_mfma_f32_16x16x32_bf16 v[136:139], a[80:83], a[52:55], v[136:139]\n" \
"ds_read_b128 a[12:15], v5 offset:6144\n" \
"v_accvgpr_read_b32 v49, a131\n" \
"v_mfma_f32_16x16x32_bf16 v[140:143], a[84:87], a[52:55], v[140:143]\n" \
"ds_read_b64_tr_b16 a[32:33], v9 offset:50176\n" \
"v_mul_f32 v46, s20, v46\n" \
"v_mfma_f32_16x16x32_bf16 v[144:147], a[80:83], a[56:59], v[144:147]\n" \
"s_add_u32 m0, s50, 69632\n" \
"ds_read_b64_tr_b16 a[34:35], v10 offset:50176\n" \
"v_mul_f32 v47, s20, v47\n" \
"v_mfma_f32_16x16x32_bf16 v[148:151], a[84:87], a[56:59], v[148:151]\n" \
"buffer_load_dwordx4 v14, s[28:31], s46 offen lds\n" \
"v_mfma_f32_16x16x32_bf16 v[152:155], a[80:83], a[60:63], v[152:155]\n" \
"ds_read_b64_tr_b16 a[36:37], v11 offset:50176\n" \
"v_mul_f32 v48, s20, v48\n" \
"v_mfma_f32_16x16x32_bf16 v[156:159], a[84:87], a[60:63], v[156:159]\n" \
"ds_read_b64_tr_b16 a[38:39], v12 offset:50176\n" \
"v_mul_f32 v49, s20, v49\n" \
"v_mfma_f32_16x16x32_bf16 v[160:163], a[88:91], a[48:51], v[160:163]\n" \
"s_add_u32 m0, s50, 73728\n" \
"ds_read_b64_tr_b16 a[40:41], v9 offset:50688\n" \
"v_cvt_pk_bf16_f32 v30, v46, v47\n" \
"v_mfma_f32_16x16x32_bf16 v[164:167], a[92:95], a[48:51], v[164:167]\n" \
"buffer_load_dwordx4 v15, s[28:31], s46 offen lds\n" \
"v_mfma_f32_16x16x32_bf16 v[168:171], a[88:91], a[52:55], v[168:171]\n" \
"ds_read_b64_tr_b16 a[42:43], v10 offset:50688\n" \
"v_cvt_pk_bf16_f32 v31, v48, v49\n" \
"v_mfma_f32_16x16x32_bf16 v[172:175], a[92:95], a[52:55], v[172:175]\n" \
"ds_read_b64_tr_b16 a[44:45], v11 offset:50688\n" \
"v_accvgpr_read_b32 v50, a132\n" \
"v_mfma_f32_16x16x32_bf16 v[176:179], a[88:91], a[56:59], v[176:179]\n" \
"s_add_u32 m0, s50, 77824\n" \
"ds_read_b64_tr_b16 a[46:47], v12 offset:50688\n" \
"v_accvgpr_read_b32 v51, a133\n" \
"v_mfma_f32_16x16x32_bf16 v[180:183], a[92:95], a[56:59], v[180:183]\n" \
"buffer_load_dwordx4 v16, s[28:31], s46 offen lds\n" \
"v_mfma_f32_16x16x32_bf16 v[184:187], a[88:91], a[60:63], v[184:187]\n" \
"ds_read_b64_tr_b16 a[16:17], v9 offset:49152\n" \
"v_accvgpr_read_b32 v52, a134\n" \
"v_mfma_f32_16x16x32_bf16 v[188:191], a[92:95], a[60:63], v[188:191]\n" \
"ds_read_b64_tr_b16 a[18:19], v10 offset:49152\n" \
"v_accvgpr_read_b32 v53, a135\n" \
"v_mfma_f32_16x16x32_bf16 v[64:67], a[64:67], a[48:51], v[64:67]\n" \
"ds_read_b64_tr_b16 a[20:21], v11 offset:49152\n" \
"v_mul_f32 v50, s20, v50\n" \
"v_mfma_f32_16x16x32_bf16 v[68:71], a[68:71], a[48:51], v[68:71]\n" \
"ds_read_b64_tr_b16 a[22:23], v12 offset:49152\n" \
"v_mul_f32 v51, s20, v51\n" \
"v_mfma_f32_16x16x32_bf16 v[72:75], a[64:67], a[52:55], v[72:75]\n" \
"ds_write_b64 v55, v[194:195] offset:16896\n" \
"v_cvt_pk_bf16_f32 v196, v216, v217\n" \
"v_mfma_f32_16x16x32_bf16 v[76:79], a[68:71], a[52:55], v[76:79]\n" \
"v_cvt_pk_bf16_f32 v197, v218, v219\n" \
"ds_write_b64 v55, v[196:197] offset:17408\n" \
"v_cvt_pk_bf16_f32 v198, v220, v221\n" \
"v_cvt_pk_bf16_f32 v199, v222, v223\n" \
"ds_write_b64 v55, v[198:199] offset:17920\n" \
"v_cvt_pk_bf16_f32 v200, v224, v225\n" \
"v_cvt_pk_bf16_f32 v201, v226, v227\n" \
"ds_write_b64 v55, v[200:201] offset:18432\n" \
"v_cvt_pk_bf16_f32 v202, v228, v229\n" \
"v_cvt_pk_bf16_f32 v203, v230, v231\n" \
"ds_write_b64 v55, v[202:203] offset:18944\n" \
"v_cvt_pk_bf16_f32 v204, v232, v233\n" \
"v_cvt_pk_bf16_f32 v205, v234, v235\n" \
"ds_write_b64 v55, v[204:205] offset:19456\n" \
"v_cvt_pk_bf16_f32 v206, v236, v237\n" \
"v_cvt_pk_bf16_f32 v207, v238, v239\n" \
"ds_write_b64 v55, v[206:207] offset:19968\n" \
"v_mfma_f32_16x16x32_bf16 v[80:83], a[64:67], a[56:59], v[80:83]\n" \
"v_mul_f32 v52, s20, v52\n" \
"v_mul_f32 v53, s20, v53\n" \
"v_mfma_f32_16x16x32_bf16 v[84:87], a[68:71], a[56:59], v[84:87]\n" \
"v_cvt_pk_bf16_f32 v32, v50, v51\n" \
"v_mfma_f32_16x16x32_bf16 v[88:91], a[64:67], a[60:63], v[88:91]\n" \
"v_cvt_pk_bf16_f32 v33, v52, v53\n" \
"v_accvgpr_read_b32 v46, a136\n" \
"v_mfma_f32_16x16x32_bf16 v[92:95], a[68:71], a[60:63], v[92:95]\n" \
"s_waitcnt lgkmcnt(0)\n" \
"s_barrier\n" \
"v_accvgpr_read_b32 v47, a137\n" \
"v_mfma_f32_16x16x32_bf16 v[96:99], a[24:27], a[0:3], v[96:99]\n" \
"s_mov_b32 s46, 384\n" \
"s_add_u32 m0, s50, 81920\n" \
"ds_read_b64_tr_b16 a[72:73], v9 offset:57856\n" \
"v_mfma_f32_16x16x32_bf16 v[100:103], a[28:31], a[0:3], v[100:103]\n" \
"buffer_load_dwordx4 v17, s[28:31], s46 offen lds\n" \
"v_mfma_f32_16x16x32_bf16 v[104:107], a[24:27], a[4:7], v[104:107]\n" \
"ds_read_b64_tr_b16 a[74:75], v10 offset:57856\n" \
"v_accvgpr_read_b32 v48, a138\n" \
"v_mfma_f32_16x16x32_bf16 v[108:111], a[28:31], a[4:7], v[108:111]\n" \
"ds_read_b64_tr_b16 a[76:77], v11 offset:57856\n" \
"v_accvgpr_read_b32 v49, a139\n" \
"v_mfma_f32_16x16x32_bf16 v[112:115], a[24:27], a[8:11], v[112:115]\n" \
"s_add_u32 m0, s50, 86016\n" \
"ds_read_b64_tr_b16 a[78:79], v12 offset:57856\n" \
"v_mul_f32 v46, s20, v46\n" \
"v_mfma_f32_16x16x32_bf16 v[116:119], a[28:31], a[8:11], v[116:119]\n" \
"buffer_load_dwordx4 v18, s[28:31], s46 offen lds\n" \
"v_mfma_f32_16x16x32_bf16 v[120:123], a[24:27], a[12:15], v[120:123]\n" \
"ds_read_b128 a[48:51], v6 offset:0\n" \
"v_mul_f32 v47, s20, v47\n" \
"v_mfma_f32_16x16x32_bf16 v[124:127], a[28:31], a[12:15], v[124:127]\n" \
"ds_read_b128 a[52:55], v6 offset:2048\n" \
"v_mul_f32 v48, s20, v48\n" \
"v_mfma_f32_16x16x32_bf16 v[128:131], a[32:35], a[0:3], v[128:131]\n" \
"s_add_u32 m0, s50, 90112\n" \
"ds_read_b128 a[56:59], v6 offset:4096\n" \
"v_mul_f32 v49, s20, v49\n" \
"v_mfma_f32_16x16x32_bf16 v[132:135], a[36:39], a[0:3], v[132:135]\n" \
"buffer_load_dwordx4 v19, s[28:31], s46 offen lds\n" \
"v_mfma_f32_16x16x32_bf16 v[136:139], a[32:35], a[4:7], v[136:139]\n" \
"ds_read_b128 a[60:63], v6 offset:6144\n" \
"v_cvt_pk_bf16_f32 v34, v46, v47\n" \
"v_mfma_f32_16x16x32_bf16 v[140:143], a[36:39], a[4:7], v[140:143]\n" \
"ds_read_b64_tr_b16 a[80:81], v9 offset:58368\n" \
"v_cvt_pk_bf16_f32 v35, v48, v49\n" \
"v_mfma_f32_16x16x32_bf16 v[144:147], a[32:35], a[8:11], v[144:147]\n" \
"s_add_u32 m0, s50, 94208\n" \
"ds_read_b64_tr_b16 a[82:83], v10 offset:58368\n" \
"v_accvgpr_read_b32 v50, a140\n" \
"v_mfma_f32_16x16x32_bf16 v[148:151], a[36:39], a[8:11], v[148:151]\n" \
"buffer_load_dwordx4 v20, s[28:31], s46 offen lds\n" \
"v_mfma_f32_16x16x32_bf16 v[152:155], a[32:35], a[12:15], v[152:155]\n" \
"ds_read_b64_tr_b16 a[84:85], v11 offset:58368\n" \
"v_accvgpr_read_b32 v51, a141\n" \
"v_mfma_f32_16x16x32_bf16 v[156:159], a[36:39], a[12:15], v[156:159]\n" \
"ds_read_b64_tr_b16 a[86:87], v12 offset:58368\n" \
"v_accvgpr_read_b32 v52, a142\n" \
"v_mfma_f32_16x16x32_bf16 v[160:163], a[40:43], a[0:3], v[160:163]\n" \
"s_mov_b32 s47, 2097152\n" \
"s_add_u32 m0, s51, 32768\n" \
"ds_read_b64_tr_b16 a[88:89], v9 offset:58880\n" \
"v_mfma_f32_16x16x32_bf16 v[164:167], a[44:47], a[0:3], v[164:167]\n" \
"buffer_load_dwordx4 v21, s[32:35], s47 offen offset:0 lds\n" \
"v_mfma_f32_16x16x32_bf16 v[168:171], a[40:43], a[4:7], v[168:171]\n" \
"ds_read_b64_tr_b16 a[90:91], v10 offset:58880\n" \
"v_accvgpr_read_b32 v53, a143\n" \
"v_mfma_f32_16x16x32_bf16 v[172:175], a[44:47], a[4:7], v[172:175]\n" \
"ds_read_b64_tr_b16 a[92:93], v11 offset:58880\n" \
"v_mul_f32 v50, s20, v50\n" \
"v_mfma_f32_16x16x32_bf16 v[176:179], a[40:43], a[8:11], v[176:179]\n" \
"s_add_u32 m0, s51, 33664\n" \
"ds_read_b64_tr_b16 a[94:95], v12 offset:58880\n" \
"v_mul_f32 v51, s20, v51\n" \
"v_mfma_f32_16x16x32_bf16 v[180:183], a[44:47], a[8:11], v[180:183]\n" \
"buffer_load_dwordx4 v21, s[32:35], s47 offen offset:128 lds\n" \
"v_mfma_f32_16x16x32_bf16 v[184:187], a[40:43], a[12:15], v[184:187]\n" \
"ds_read_b64_tr_b16 a[64:65], v9 offset:57344\n" \
"v_mul_f32 v52, s20, v52\n" \
"v_mfma_f32_16x16x32_bf16 v[188:191], a[44:47], a[12:15], v[188:191]\n" \
"ds_read_b64_tr_b16 a[66:67], v10 offset:57344\n" \
"v_mul_f32 v53, s20, v53\n" \
"v_mfma_f32_16x16x32_bf16 v[64:67], a[16:19], a[0:3], v[64:67]\n" \
"ds_read_b64_tr_b16 a[68:69], v11 offset:57344\n" \
"v_cvt_pk_bf16_f32 v36, v50, v51\n" \
"v_mfma_f32_16x16x32_bf16 v[68:71], a[20:23], a[0:3], v[68:71]\n" \
"ds_read_b64_tr_b16 a[70:71], v12 offset:57344\n" \
"v_cvt_pk_bf16_f32 v37, v52, v53\n" \
"v_mfma_f32_16x16x32_bf16 v[72:75], a[16:19], a[4:7], v[72:75]\n" \
"buffer_store_dwordx4 v[30:33], v4, s[36:39], s96 offen offset:0 sc1\n" \
"v_mfma_f32_16x16x32_bf16 v[76:79], a[20:23], a[4:7], v[76:79]\n" \
"s_add_u32 s97, s96, s98\n" \
"ds_read2st64_b32 v[208:209], v241 offset0:64 offset1:80\n" \
"ds_read2st64_b32 v[210:211], v241 offset0:96 offset1:112\n" \
"ds_read2st64_b32 v[212:213], v242 offset0:64 offset1:80\n" \
"ds_read2st64_b32 v[214:215], v242 offset0:96 offset1:112\n" \
"ds_read2st64_b32 v[216:217], v241 offset0:72 offset1:88\n" \
"ds_read2st64_b32 v[218:219], v241 offset0:104 offset1:120\n" \
"ds_read2st64_b32 v[220:221], v242 offset0:72 offset1:88\n" \
"ds_read2st64_b32 v[222:223], v242 offset0:104 offset1:120\n" \
"v_mfma_f32_16x16x32_bf16 v[80:83], a[16:19], a[8:11], v[80:83]\n" \
"v_accvgpr_read_b32 v46, a144\n" \
"v_accvgpr_read_b32 v47, a145\n" \
"v_mfma_f32_16x16x32_bf16 v[84:87], a[20:23], a[8:11], v[84:87]\n" \
"v_accvgpr_read_b32 v48, a146\n" \
"v_accvgpr_read_b32 v49, a147\n" \
"v_mfma_f32_16x16x32_bf16 v[88:91], a[16:19], a[12:15], v[88:91]\n" \
"s_waitcnt vmcnt(11) lgkmcnt(0)\n" \
"v_mul_f32 v46, s20, v46\n" \
"v_mfma_f32_16x16x32_bf16 v[92:95], a[20:23], a[12:15], v[92:95]\n" \
"s_barrier\n" \
"v_mul_f32 v47, s20, v47\n" \
"v_mul_f32 v48, s20, v48\n" \
"v_mfma_f32_16x16x32_bf16 v[96:99], a[72:75], a[48:51], v[96:99]\n" \
"s_mov_b32 s47, 1572864\n" \
"s_add_u32 m0, s51, 16384\n" \
"ds_read_b64_tr_b16 a[24:25], v9 offset:512\n" \
"v_mfma_f32_16x16x32_bf16 v[100:103], a[76:79], a[48:51], v[100:103]\n" \
"buffer_load_dwordx4 v21, s[32:35], s47 offen offset:0 lds\n" \
"v_mfma_f32_16x16x32_bf16 v[104:107], a[72:75], a[52:55], v[104:107]\n" \
"ds_read_b64_tr_b16 a[26:27], v10 offset:512\n" \
"v_perm_b32 v224, v209, v208, s94\n" \
"v_mfma_f32_16x16x32_bf16 v[108:111], a[76:79], a[52:55], v[108:111]\n" \
"ds_read_b64_tr_b16 a[28:29], v11 offset:512\n" \
"v_perm_b32 v225, v211, v210, s94\n" \
"v_mfma_f32_16x16x32_bf16 v[112:115], a[72:75], a[56:59], v[112:115]\n" \
"s_add_u32 m0, s51, 17280\n" \
"ds_read_b64_tr_b16 a[30:31], v12 offset:512\n" \
"v_perm_b32 v226, v213, v212, s94\n" \
"v_mfma_f32_16x16x32_bf16 v[116:119], a[76:79], a[56:59], v[116:119]\n" \
"buffer_load_dwordx4 v21, s[32:35], s47 offen offset:128 lds\n" \
"v_mfma_f32_16x16x32_bf16 v[120:123], a[72:75], a[60:63], v[120:123]\n" \
"ds_read_b128 a[0:3], v5 offset:32768\n" \
"v_perm_b32 v227, v215, v214, s94\n" \
"v_mfma_f32_16x16x32_bf16 v[124:127], a[76:79], a[60:63], v[124:127]\n" \
"ds_read_b128 a[4:7], v5 offset:34816\n" \
"v_perm_b32 v228, v209, v208, s95\n" \
"v_mfma_f32_16x16x32_bf16 v[128:131], a[80:83], a[48:51], v[128:131]\n" \
"s_mov_b32 s47, 1835008\n" \
"s_add_u32 m0, s51, 24576\n" \
"ds_read_b128 a[8:11], v5 offset:36864\n" \
"v_mfma_f32_16x16x32_bf16 v[132:135], a[84:87], a[48:51], v[132:135]\n" \
"buffer_load_dwordx4 v21, s[32:35], s47 offen offset:0 lds\n" \
"v_mfma_f32_16x16x32_bf16 v[136:139], a[80:83], a[52:55], v[136:139]\n" \
"ds_read_b128 a[12:15], v5 offset:38912\n" \
"v_perm_b32 v229, v211, v210, s95\n" \
"v_mfma_f32_16x16x32_bf16 v[140:143], a[84:87], a[52:55], v[140:143]\n" \
"ds_read_b64_tr_b16 a[32:33], v9 offset:1024\n" \
"v_perm_b32 v230, v213, v212, s95\n" \
"v_mfma_f32_16x16x32_bf16 v[144:147], a[80:83], a[56:59], v[144:147]\n" \
"s_add_u32 m0, s51, 25472\n" \
"ds_read_b64_tr_b16 a[34:35], v10 offset:1024\n" \
"v_perm_b32 v231, v215, v214, s95\n" \
"v_mfma_f32_16x16x32_bf16 v[148:151], a[84:87], a[56:59], v[148:151]\n" \
"buffer_load_dwordx4 v21, s[32:35], s47 offen offset:128 lds\n" \
"v_mfma_f32_16x16x32_bf16 v[152:155], a[80:83], a[60:63], v[152:155]\n" \
"ds_read_b64_tr_b16 a[36:37], v11 offset:1024\n" \
"v_perm_b32 v232, v217, v216, s94\n" \
"v_mfma_f32_16x16x32_bf16 v[156:159], a[84:87], a[60:63], v[156:159]\n" \
"ds_read_b64_tr_b16 a[38:39], v12 offset:1024\n" \
"v_perm_b32 v233, v219, v218, s94\n" \
"v_mfma_f32_16x16x32_bf16 v[160:163], a[88:91], a[48:51], v[160:163]\n" \
"s_mov_b32 s46, 512\n" \
"s_add_u32 m0, s50, 0\n" \
"ds_read_b64_tr_b16 a[40:41], v9 offset:1536\n" \
"v_mfma_f32_16x16x32_bf16 v[164:167], a[92:95], a[48:51], v[164:167]\n" \
"buffer_load_dwordx4 v13, s[28:31], s46 offen lds\n" \
"v_mfma_f32_16x16x32_bf16 v[168:171], a[88:91], a[52:55], v[168:171]\n" \
"ds_read_b64_tr_b16 a[42:43], v10 offset:1536\n" \
"v_perm_b32 v234, v221, v220, s94\n" \
"v_mfma_f32_16x16x32_bf16 v[172:175], a[92:95], a[52:55], v[172:175]\n" \
"ds_read_b64_tr_b16 a[44:45], v11 offset:1536\n" \
"v_perm_b32 v235, v223, v222, s94\n" \
"v_mfma_f32_16x16x32_bf16 v[176:179], a[88:91], a[56:59], v[176:179]\n" \
"s_add_u32 m0, s50, 4096\n" \
"ds_read_b64_tr_b16 a[46:47], v12 offset:1536\n" \
"v_perm_b32 v236, v217, v216, s95\n" \
"v_mfma_f32_16x16x32_bf16 v[180:183], a[92:95], a[56:59], v[180:183]\n" \
"buffer_load_dwordx4 v14, s[28:31], s46 offen lds\n" \
"v_mfma_f32_16x16x32_bf16 v[184:187], a[88:91], a[60:63], v[184:187]\n" \
"ds_read_b64_tr_b16 a[16:17], v9 offset:0\n" \
"v_perm_b32 v237, v219, v218, s95\n" \
"v_mfma_f32_16x16x32_bf16 v[188:191], a[92:95], a[60:63], v[188:191]\n" \
"ds_read_b64_tr_b16 a[18:19], v10 offset:0\n" \
"v_perm_b32 v238, v221, v220, s95\n" \
"v_mfma_f32_16x16x32_bf16 v[64:67], a[64:67], a[48:51], v[64:67]\n" \
"s_add_u32 m0, s50, 8192\n" \
"ds_read_b64_tr_b16 a[20:21], v11 offset:0\n" \
"v_perm_b32 v239, v223, v222, s95\n" \
"v_mfma_f32_16x16x32_bf16 v[68:71], a[68:71], a[48:51], v[68:71]\n" \
"buffer_load_dwordx4 v15, s[28:31], s46 offen lds\n" \
"v_mfma_f32_16x16x32_bf16 v[72:75], a[64:67], a[52:55], v[72:75]\n" \
"ds_read_b64_tr_b16 a[22:23], v12 offset:0\n" \
"v_mul_f32 v49, s20, v49\n" \
"v_mfma_f32_16x16x32_bf16 v[76:79], a[68:71], a[52:55], v[76:79]\n" \
"buffer_store_dwordx4 v[224:227], v240, s[36:39], s59 offen offset:0 sc1\n" \
"v_mfma_f32_16x16x32_bf16 v[80:83], a[64:67], a[56:59], v[80:83]\n" \
"s_add_u32 m0, s50, 12288\n" \
"v_cvt_pk_bf16_f32 v38, v46, v47\n" \
"v_cvt_pk_bf16_f32 v39, v48, v49\n" \
"v_mfma_f32_16x16x32_bf16 v[84:87], a[68:71], a[56:59], v[84:87]\n" \
"buffer_load_dwordx4 v16, s[28:31], s46 offen lds\n" \
"v_mfma_f32_16x16x32_bf16 v[88:91], a[64:67], a[60:63], v[88:91]\n" \
"v_accvgpr_read_b32 v50, a148\n" \
"v_accvgpr_read_b32 v51, a149\n" \
"v_mfma_f32_16x16x32_bf16 v[92:95], a[68:71], a[60:63], v[92:95]\n" \
"s_waitcnt lgkmcnt(0)\n" \
"v_accvgpr_read_b32 v52, a150\n" \
"v_accvgpr_read_b32 v53, a151\n" \
"v_mfma_f32_16x16x32_bf16 v[96:99], a[24:27], a[0:3], v[96:99]\n" \
"s_mov_b32 s47, 2359296\n" \
"s_add_u32 m0, s51, 40960\n" \
"ds_read_b64_tr_b16 a[72:73], v9 offset:8704\n" \
"v_mfma_f32_16x16x32_bf16 v[100:103], a[28:31], a[0:3], v[100:103]\n" \
"buffer_load_dwordx4 v21, s[32:35], s47 offen offset:0 lds\n" \
"v_mfma_f32_16x16x32_bf16 v[104:107], a[24:27], a[4:7], v[104:107]\n" \
"LH10_%=:\ns_cbranch_execz LH14_%=\n" \
"ds_read_b64_tr_b16 a[74:75], v10 offset:8704\n" \
"v_mul_f32 v50, s20, v50\n" \
"v_mfma_f32_16x16x32_bf16 v[108:111], a[28:31], a[4:7], v[108:111]\n" \
"ds_read_b64_tr_b16 a[76:77], v11 offset:8704\n" \
"v_mul_f32 v51, s20, v51\n" \
"v_mfma_f32_16x16x32_bf16 v[112:115], a[24:27], a[8:11], v[112:115]\n" \
"s_add_u32 m0, s51, 41856\n" \
"ds_read_b64_tr_b16 a[78:79], v12 offset:8704\n" \
"v_mul_f32 v52, s20, v52\n" \
"v_mfma_f32_16x16x32_bf16 v[116:119], a[28:31], a[8:11], v[116:119]\n" \
"buffer_load_dwordx4 v21, s[32:35], s47 offen offset:128 lds\n" \
"v_mfma_f32_16x16x32_bf16 v[120:123], a[24:27], a[12:15], v[120:123]\n" \
"ds_read_b128 a[48:51], v6 offset:32768\n" \
"v_mul_f32 v53, s20, v53\n" \
"v_mfma_f32_16x16x32_bf16 v[124:127], a[28:31], a[12:15], v[124:127]\n" \
"ds_read_b128 a[52:55], v6 offset:34816\n" \
"v_cvt_pk_bf16_f32 v40, v50, v51\n" \
"v_mfma_f32_16x16x32_bf16 v[128:131], a[32:35], a[0:3], v[128:131]\n" \
"s_mov_b32 s46, 512\n" \
"s_add_u32 m0, s50, 16384\n" \
"ds_read_b128 a[56:59], v6 offset:36864\n" \
"v_mfma_f32_16x16x32_bf16 v[132:135], a[36:39], a[0:3], v[132:135]\n" \
"buffer_load_dwordx4 v17, s[28:31], s46 offen lds\n" \
"v_mfma_f32_16x16x32_bf16 v[136:139], a[32:35], a[4:7], v[136:139]\n" \
"ds_read_b128 a[60:63], v6 offset:38912\n" \
"v_cvt_pk_bf16_f32 v41, v52, v53\n" \
"v_mfma_f32_16x16x32_bf16 v[140:143], a[36:39], a[4:7], v[140:143]\n" \
"ds_read_b64_tr_b16 a[80:81], v9 offset:9216\n" \
"v_accvgpr_read_b32 v46, a152\n" \
"v_mfma_f32_16x16x32_bf16 v[144:147], a[32:35], a[8:11], v[144:147]\n" \
"s_add_u32 m0, s50, 20480\n" \
"ds_read_b64_tr_b16 a[82:83], v10 offset:9216\n" \
"v_accvgpr_read_b32 v47, a153\n" \
"v_mfma_f32_16x16x32_bf16 v[148:151], a[36:39], a[8:11], v[148:151]\n" \
"buffer_load_dwordx4 v18, s[28:31], s46 offen lds\n" \
"v_mfma_f32_16x16x32_bf16 v[152:155], a[32:35], a[12:15], v[152:155]\n" \
"ds_read_b64_tr_b16 a[84:85], v11 offset:9216\n" \
"v_accvgpr_read_b32 v48, a154\n" \
"v_mfma_f32_16x16x32_bf16 v[156:159], a[36:39], a[12:15], v[156:159]\n" \
"ds_read_b64_tr_b16 a[86:87], v12 offset:9216\n" \
"v_accvgpr_read_b32 v49, a155\n" \
"v_mfma_f32_16x16x32_bf16 v[160:163], a[40:43], a[0:3], v[160:163]\n" \
"s_add_u32 m0, s50, 24576\n" \
"ds_read_b64_tr_b16 a[88:89], v9 offset:9728\n" \
"v_mul_f32 v46, s20, v46\n" \
"v_mfma_f32_16x16x32_bf16 v[164:167], a[44:47], a[0:3], v[164:167]\n" \
"buffer_load_dwordx4 v19, s[28:31], s46 offen lds\n" \
"v_mfma_f32_16x16x32_bf16 v[168:171], a[40:43], a[4:7], v[168:171]\n" \
"ds_read_b64_tr_b16 a[90:91], v10 offset:9728\n" \
"v_mul_f32 v47, s20, v47\n" \
"v_mfma_f32_16x16x32_bf16 v[172:175], a[44:47], a[4:7], v[172:175]\n" \
"ds_read_b64_tr_b16 a[92:93], v11 offset:9728\n" \
"v_mul_f32 v48, s20, v48\n" \
"v_mfma_f32_16x16x32_bf16 v[176:179], a[40:43], a[8:11], v[176:179]\n" \
"s_add_u32 m0, s50, 28672\n" \
"ds_read_b64_tr_b16 a[94:95], v12 offset:9728\n" \
"v_mul_f32 v49, s20, v49\n" \
"v_mfma_f32_16x16x32_bf16 v[180:183], a[44:47], a[8:11], v[180:183]\n" \
"buffer_load_dwordx4 v20, s[28:31], s46 offen lds\n" \
"v_mfma_f32_16x16x32_bf16 v[184:187], a[40:43], a[12:15], v[184:187]\n" \
"ds_read_b64_tr_b16 a[64:65], v9 offset:8192\n" \
"v_cvt_pk_bf16_f32 v42, v46, v47\n" \
"v_mfma_f32_16x16x32_bf16 v[188:191], a[44:47], a[12:15], v[188:191]\n" \
"ds_read_b64_tr_b16 a[66:67], v10 offset:8192\n" \
"v_cvt_pk_bf16_f32 v43, v48, v49\n" \
"v_mfma_f32_16x16x32_bf16 v[64:67], a[16:19], a[0:3], v[64:67]\n" \
"s_mov_b32 s47, 2621440\n" \
"s_add_u32 m0, s51, 49152\n" \
"ds_read_b64_tr_b16 a[68:69], v11 offset:8192\n" \
"v_mfma_f32_16x16x32_bf16 v[68:71], a[20:23], a[0:3], v[68:71]\n" \
"buffer_load_dwordx4 v21, s[32:35], s47 offen offset:0 lds\n" \
"v_mfma_f32_16x16x32_bf16 v[72:75], a[16:19], a[4:7], v[72:75]\n" \
"ds_read_b64_tr_b16 a[70:71], v12 offset:8192\n" \
"v_accvgpr_read_b32 v50, a156\n" \
"v_mfma_f32_16x16x32_bf16 v[76:79], a[20:23], a[4:7], v[76:79]\n" \
"buffer_store_dwordx4 v[228:231], v240, s[36:39], s59 offen offset:128 sc1\n" \
"v_mfma_f32_16x16x32_bf16 v[80:83], a[16:19], a[8:11], v[80:83]\n" \
"s_add_u32 m0, s51, 50048\n" \
"v_accvgpr_read_b32 v51, a157\n" \
"v_accvgpr_read_b32 v52, a158\n" \
"v_mfma_f32_16x16x32_bf16 v[84:87], a[20:23], a[8:11], v[84:87]\n" \
"buffer_load_dwordx4 v21, s[32:35], s47 offen offset:128 lds\n" \
"v_mfma_f32_16x16x32_bf16 v[88:91], a[16:19], a[12:15], v[88:91]\n" \
"s_waitcnt vmcnt(14) lgkmcnt(0)\n" \
"v_accvgpr_read_b32 v53, a159\n" \
"v_mul_f32 v50, s20, v50\n" \
"v_mfma_f32_16x16x32_bf16 v[92:95], a[20:23], a[12:15], v[92:95]\n" \
"s_barrier\n" \
"v_mul_f32 v51, s20, v51\n" \
"v_mul_f32 v52, s20, v52\n" \
"v_mfma_f32_16x16x32_bf16 v[96:99], a[72:75], a[48:51], v[96:99]\n" \
"s_mov_b32 s47, 2883584\n" \
"s_add_u32 m0, s51, 57344\n" \
"ds_read_b64_tr_b16 a[24:25], v9 offset:16896\n" \
"v_mfma_f32_16x16x32_bf16 v[100:103], a[76:79], a[48:51], v[100:103]\n" \
"buffer_load_dwordx4 v21, s[32:35], s47 offen offset:0 lds\n" \
"v_mfma_f32_16x16x32_bf16 v[104:107], a[72:75], a[52:55], v[104:107]\n" \
"ds_read_b64_tr_b16 a[26:27], v10 offset:16896\n" \
"v_mul_f32 v53, s20, v53\n" \
"v_mfma_f32_16x16x32_bf16 v[108:111], a[76:79], a[52:55], v[108:111]\n" \
"ds_read_b64_tr_b16 a[28:29], v11 offset:16896\n" \
"v_cvt_pk_bf16_f32 v44, v50, v51\n" \
"v_mfma_f32_16x16x32_bf16 v[112:115], a[72:75], a[56:59], v[112:115]\n" \
"s_add_u32 m0, s51, 58240\n" \
"ds_read_b64_tr_b16 a[30:31], v12 offset:16896\n" \
"v_cvt_pk_bf16_f32 v45, v52, v53\n" \
"v_mfma_f32_16x16x32_bf16 v[116:119], a[76:79], a[56:59], v[116:119]\n" \
"buffer_load_dwordx4 v21, s[32:35], s47 offen offset:128 lds\n" \
"v_mfma_f32_16x16x32_bf16 v[120:123], a[72:75], a[60:63], v[120:123]\n" \
"ds_read_b128 a[0:3], v7 offset:0\n" \
"v_accvgpr_read_b32 v46, a160\n" \
"v_mfma_f32_16x16x32_bf16 v[124:127], a[76:79], a[60:63], v[124:127]\n" \
"ds_read_b128 a[4:7], v7 offset:2048\n" \
"v_accvgpr_read_b32 v47, a161\n" \
"v_mfma_f32_16x16x32_bf16 v[128:131], a[80:83], a[48:51], v[128:131]\n" \
"s_mov_b32 s46, 640\n" \
"s_add_u32 m0, s50, 32768\n" \
"ds_read_b128 a[8:11], v7 offset:4096\n" \
"v_mfma_f32_16x16x32_bf16 v[132:135], a[84:87], a[48:51], v[132:135]\n" \
"buffer_load_dwordx4 v13, s[28:31], s46 offen lds\n" \
"v_mfma_f32_16x16x32_bf16 v[136:139], a[80:83], a[52:55], v[136:139]\n" \
"ds_read_b128 a[12:15], v7 offset:6144\n" \
"v_accvgpr_read_b32 v48, a162\n" \
"v_mfma_f32_16x16x32_bf16 v[140:143], a[84:87], a[52:55], v[140:143]\n" \
"ds_read_b64_tr_b16 a[32:33], v9 offset:17408\n" \
"v_accvgpr_read_b32 v49, a163\n" \
"v_mfma_f32_16x16x32_bf16 v[144:147], a[80:83], a[56:59], v[144:147]\n" \
"s_add_u32 m0, s50, 36864\n" \
"ds_read_b64_tr_b16 a[34:35], v10 offset:17408\n" \
"v_mul_f32 v46, s20, v46\n" \
"v_mfma_f32_16x16x32_bf16 v[148:151], a[84:87], a[56:59], v[148:151]\n" \
"buffer_load_dwordx4 v14, s[28:31], s46 offen lds\n" \
"v_mfma_f32_16x16x32_bf16 v[152:155], a[80:83], a[60:63], v[152:155]\n" \
"ds_read_b64_tr_b16 a[36:37], v11 offset:17408\n" \
"v_mul_f32 v47, s20, v47\n" \
"v_mfma_f32_16x16x32_bf16 v[156:159], a[84:87], a[60:63], v[156:159]\n" \
"ds_read_b64_tr_b16 a[38:39], v12 offset:17408\n" \
"v_mul_f32 v48, s20, v48\n" \
"v_mfma_f32_16x16x32_bf16 v[160:163], a[88:91], a[48:51], v[160:163]\n" \
"s_add_u32 m0, s50, 40960\n" \
"ds_read_b64_tr_b16 a[40:41], v9 offset:17920\n" \
"v_mul_f32 v49, s20, v49\n" \
"v_mfma_f32_16x16x32_bf16 v[164:167], a[92:95], a[48:51], v[164:167]\n" \
"buffer_load_dwordx4 v15, s[28:31], s46 offen lds\n" \
"v_mfma_f32_16x16x32_bf16 v[168:171], a[88:91], a[52:55], v[168:171]\n" \
"ds_read_b64_tr_b16 a[42:43], v10 offset:17920\n" \
"v_cvt_pk_bf16_f32 v30, v46, v47\n" \
"v_mfma_f32_16x16x32_bf16 v[172:175], a[92:95], a[52:55], v[172:175]\n" \
"ds_read_b64_tr_b16 a[44:45], v11 offset:17920\n" \
"v_cvt_pk_bf16_f32 v31, v48, v49\n" \
"v_mfma_f32_16x16x32_bf16 v[176:179], a[88:91], a[56:59], v[176:179]\n" \
"s_add_u32 m0, s50, 45056\n" \
"ds_read_b64_tr_b16 a[46:47], v12 offset:17920\n" \
"v_accvgpr_read_b32 v50, a164\n" \
"v_mfma_f32_16x16x32_bf16 v[180:183], a[92:95], a[56:59], v[180:183]\n" \
"buffer_load_dwordx4 v16, s[28:31], s46 offen lds\n" \
"v_mfma_f32_16x16x32_bf16 v[184:187], a[88:91], a[60:63], v[184:187]\n" \
"ds_read_b64_tr_b16 a[16:17], v9 offset:16384\n" \
"v_accvgpr_read_b32 v51, a165\n" \
"v_mfma_f32_16x16x32_bf16 v[188:191], a[92:95], a[60:63], v[188:191]\n" \
"ds_read_b64_tr_b16 a[18:19], v10 offset:16384\n" \
"v_accvgpr_read_b32 v52, a166\n" \
"v_mfma_f32_16x16x32_bf16 v[64:67], a[64:67], a[48:51], v[64:67]\n" \
"s_mov_b32 s46, 640\n" \
"s_add_u32 m0, s50, 49152\n" \
"ds_read_b64_tr_b16 a[20:21], v11 offset:16384\n" \
"v_mfma_f32_16x16x32_bf16 v[68:71], a[68:71], a[48:51], v[68:71]\n" \
"buffer_load_dwordx4 v17, s[28:31], s46 offen lds\n" \
"v_mfma_f32_16x16x32_bf16 v[72:75], a[64:67], a[52:55], v[72:75]\n" \
"ds_read_b64_tr_b16 a[22:23], v12 offset:16384\n" \
"v_accvgpr_read_b32 v53, a167\n" \
"v_mfma_f32_16x16x32_bf16 v[76:79], a[68:71], a[52:55], v[76:79]\n" \
"buffer_store_dwordx4 v[232:235], v240, s[36:39], s60 offen offset:0 sc1\n" \
"v_mfma_f32_16x16x32_bf16 v[80:83], a[64:67], a[56:59], v[80:83]\n" \
"s_add_u32 m0, s50, 53248\n" \
"v_mul_f32 v50, s20, v50\n" \
"v_mul_f32 v51, s20, v51\n" \
"v_mfma_f32_16x16x32_bf16 v[84:87], a[68:71], a[56:59], v[84:87]\n" \
"buffer_load_dwordx4 v18, s[28:31], s46 offen lds\n" \
"v_mfma_f32_16x16x32_bf16 v[88:91], a[64:67], a[60:63], v[88:91]\n" \
"v_mul_f32 v52, s20, v52\n" \
"v_mul_f32 v53, s20, v53\n" \
"v_mfma_f32_16x16x32_bf16 v[92:95], a[68:71], a[60:63], v[92:95]\n" \
"s_waitcnt lgkmcnt(0)\n" \
"v_cvt_pk_bf16_f32 v32, v50, v51\n" \
"v_cvt_pk_bf16_f32 v33, v52, v53\n" \
"v_mfma_f32_16x16x32_bf16 v[96:99], a[24:27], a[0:3], v[96:99]\n" \
"s_add_u32 m0, s50, 57344\n" \
"ds_read_b64_tr_b16 a[72:73], v9 offset:25088\n" \
"v_accvgpr_read_b32 v46, a168\n" \
"v_mfma_f32_16x16x32_bf16 v[100:103], a[28:31], a[0:3], v[100:103]\n" \
"buffer_load_dwordx4 v19, s[28:31], s46 offen lds\n" \
"v_mfma_f32_16x16x32_bf16 v[104:107], a[24:27], a[4:7], v[104:107]\n" \
"ds_read_b64_tr_b16 a[74:75], v10 offset:25088\n" \
"v_accvgpr_read_b32 v47, a169\n" \
"v_mfma_f32_16x16x32_bf16 v[108:111], a[28:31], a[4:7], v[108:111]\n" \
"ds_read_b64_tr_b16 a[76:77], v11 offset:25088\n" \
"v_accvgpr_read_b32 v48, a170\n" \
"v_mfma_f32_16x16x32_bf16 v[112:115], a[24:27], a[8:11], v[112:115]\n" \
"ds_read_b64_tr_b16 a[78:79], v12 offset:25088\n" \
"v_accvgpr_read_b32 v49, a171\n" \
"v_mfma_f32_16x16x32_bf16 v[116:119], a[28:31], a[8:11], v[116:119]\n" \
"s_add_u32 m0, s50, 61440\n" \
"ds_read_b128 a[48:51], v8 offset:0\n" \
"v_mul_f32 v46, s20, v46\n" \
"v_mfma_f32_16x16x32_bf16 v[120:123], a[24:27], a[12:15], v[120:123]\n" \
"buffer_load_dwordx4 v20, s[28:31], s46 offen lds\n" \
"v_mfma_f32_16x16x32_bf16 v[124:127], a[28:31], a[12:15], v[124:127]\n" \
"ds_read_b128 a[52:55], v8 offset:2048\n" \
"v_mul_f32 v47, s20, v47\n" \
"v_mfma_f32_16x16x32_bf16 v[128:131], a[32:35], a[0:3], v[128:131]\n" \
"v_mul_f32 v48, s20, v48\n" \
"v_mul_f32 v49, s20, v49\n" \
"v_mfma_f32_16x16x32_bf16 v[132:135], a[36:39], a[0:3], v[132:135]\n" \
"ds_read_b128 a[56:59], v8 offset:4096\n" \
"s_load_dwordx2 s[100:101], %0, 0x50\n" \
"v_mfma_f32_16x16x32_bf16 v[136:139], a[32:35], a[4:7], v[136:139]\n" \
"s_mov_b32 s47, 3145728\n" \
"s_add_u32 m0, s51, 0\n" \
"ds_read_b128 a[60:63], v8 offset:6144\n" \
"v_mfma_f32_16x16x32_bf16 v[140:143], a[36:39], a[4:7], v[140:143]\n" \
"buffer_load_dwordx4 v21, s[32:35], s47 offen offset:0 lds\n" \
"v_mfma_f32_16x16x32_bf16 v[144:147], a[32:35], a[8:11], v[144:147]\n" \
"s_lshr_b32 s64, %1, 3\n" \
"ds_read_b64_tr_b16 a[80:81], v9 offset:25600\n" \
"s_lshl_b32 s64, s64, 2\n" \
"v_mfma_f32_16x16x32_bf16 v[148:151], a[36:39], a[8:11], v[148:151]\n" \
"s_add_u32 s64, s64, %2\n" \
"ds_read_b64_tr_b16 a[82:83], v10 offset:25600\n" \
"s_lshl_b32 s64, s64, 11\n" \
"v_mfma_f32_16x16x32_bf16 v[152:155], a[32:35], a[12:15], v[152:155]\n" \
"s_bfe_u32 s65, %1, 0x20001\n" \
"ds_read_b64_tr_b16 a[84:85], v11 offset:25600\n" \
"s_lshl_b32 s65, s65, 21\n" \
"v_mfma_f32_16x16x32_bf16 v[156:159], a[36:39], a[12:15], v[156:159]\n" \
"s_add_u32 m0, s51, 896\n" \
"ds_read_b64_tr_b16 a[86:87], v12 offset:25600\n" \
"s_add_u32 s64, s64, s65\n" \
"v_mfma_f32_16x16x32_bf16 v[160:163], a[40:43], a[0:3], v[160:163]\n" \
"buffer_load_dwordx4 v21, s[32:35], s47 offen offset:128 lds\n" \
"v_mfma_f32_16x16x32_bf16 v[164:167], a[44:47], a[0:3], v[164:167]\n" \
"ds_read_b64_tr_b16 a[88:89], v9 offset:26112\n" \
"v_mfma_f32_16x16x32_bf16 v[168:171], a[40:43], a[4:7], v[168:171]\n" \
"ds_read_b64_tr_b16 a[90:91], v10 offset:26112\n" \
"v_mfma_f32_16x16x32_bf16 v[172:175], a[44:47], a[4:7], v[172:175]\n" \
"ds_read_b64_tr_b16 a[92:93], v11 offset:26112\n" \
"v_mfma_f32_16x16x32_bf16 v[176:179], a[40:43], a[8:11], v[176:179]\n" \
"s_mov_b32 s47, 3407872\n" \
"s_add_u32 m0, s51, 8192\n" \
"ds_read_b64_tr_b16 a[94:95], v12 offset:26112\n" \
"v_mfma_f32_16x16x32_bf16 v[180:183], a[44:47], a[8:11], v[180:183]\n" \
"buffer_load_dwordx4 v21, s[32:35], s47 offen offset:0 lds\n" \
"v_mfma_f32_16x16x32_bf16 v[184:187], a[40:43], a[12:15], v[184:187]\n" \
"ds_read_b64_tr_b16 a[64:65], v9 offset:24576\n" \
"v_mfma_f32_16x16x32_bf16 v[188:191], a[44:47], a[12:15], v[188:191]\n" \
"v_mfma_f32_16x16x32_bf16 v[64:67], a[16:19], a[0:3], v[64:67]\n" \
"ds_read_b64_tr_b16 a[66:67], v10 offset:24576\n" \
"v_mfma_f32_16x16x32_bf16 v[68:71], a[20:23], a[0:3], v[68:71]\n" \
"s_add_u32 m0, s51, 9088\n" \
"ds_read_b64_tr_b16 a[68:69], v11 offset:24576\n" \
"v_mfma_f32_16x16x32_bf16 v[72:75], a[16:19], a[4:7], v[72:75]\n" \
"buffer_load_dwordx4 v21, s[32:35], s47 offen offset:128 lds\n" \
"v_mfma_f32_16x16x32_bf16 v[76:79], a[20:23], a[4:7], v[76:79]\n" \
"ds_read_b64_tr_b16 a[70:71], v12 offset:24576\n" \
"v_mfma_f32_16x16x32_bf16 v[80:83], a[16:19], a[8:11], v[80:83]\n" \
"buffer_store_dwordx4 v[236:239], v240, s[36:39], s60 offen offset:128 sc1\n" \
"v_mfma_f32_16x16x32_bf16 v[84:87], a[20:23], a[8:11], v[84:87]\n" \
"v_mfma_f32_16x16x32_bf16 v[88:91], a[16:19], a[12:15], v[88:91]\n" \
"s_waitcnt vmcnt(19) lgkmcnt(0)\n" \
"v_mfma_f32_16x16x32_bf16 v[92:95], a[20:23], a[12:15], v[92:95]\n" \
"s_barrier\n" \
"v_mfma_f32_16x16x32_bf16 v[96:99], a[72:75], a[48:51], v[96:99]\n" \
"s_mov_b32 s46, 768\n" \
"s_add_u32 m0, s50, 65536\n" \
"ds_read_b64_tr_b16 a[24:25], v9 offset:33280\n" \
"v_mfma_f32_16x16x32_bf16 v[100:103], a[76:79], a[48:51], v[100:103]\n" \
"buffer_load_dwordx4 v13, s[28:31], s46 offen lds\n" \
"v_mfma_f32_16x16x32_bf16 v[104:107], a[72:75], a[52:55], v[104:107]\n" \
"ds_read_b64_tr_b16 a[26:27], v10 offset:33280\n" \
"v_mfma_f32_16x16x32_bf16 v[108:111], a[76:79], a[52:55], v[108:111]\n" \
"ds_read_b64_tr_b16 a[28:29], v11 offset:33280\n" \
"v_mfma_f32_16x16x32_bf16 v[112:115], a[72:75], a[56:59], v[112:115]\n" \
"ds_read_b64_tr_b16 a[30:31], v12 offset:33280\n" \
"v_mfma_f32_16x16x32_bf16 v[116:119], a[76:79], a[56:59], v[116:119]\n" \
"s_add_u32 m0, s50, 69632\n" \
"ds_read_b128 a[0:3], v5 offset:0\n" \
"v_mfma_f32_16x16x32_bf16 v[120:123], a[72:75], a[60:63], v[120:123]\n" \
"buffer_load_dwordx4 v14, s[28:31], s46 offen lds\n" \
"v_mfma_f32_16x16x32_bf16 v[124:127], a[76:79], a[60:63], v[124:127]\n" \
"ds_read_b128 a[4:7], v5 offset:2048\n" \
"v_mfma_f32_16x16x32_bf16 v[128:131], a[80:83], a[48:51], v[128:131]\n" \
"v_mfma_f32_16x16x32_bf16 v[132:135], a[84:87], a[48:51], v[132:135]\n" \
"ds_read_b128 a[8:11], v5 offset:4096\n" \
"v_mfma_f32_16x16x32_bf16 v[136:139], a[80:83], a[52:55], v[136:139]\n" \
"s_add_u32 m0, s50, 73728\n" \
"ds_read_b128 a[12:15], v5 offset:6144\n" \
"v_mfma_f32_16x16x32_bf16 v[140:143], a[84:87], a[52:55], v[140:143]\n" \
"buffer_load_dwordx4 v15, s[28:31], s46 offen lds\n" \
"v_mfma_f32_16x16x32_bf16 v[144:147], a[80:83], a[56:59], v[144:147]\n" \
"ds_read_b64_tr_b16 a[32:33], v9 offset:33792\n" \
"v_mfma_f32_16x16x32_bf16 v[148:151], a[84:87], a[56:59], v[148:151]\n" \
"ds_read_b64_tr_b16 a[34:35], v10 offset:33792\n" \
"v_mfma_f32_16x16x32_bf16 v[152:155], a[80:83], a[60:63], v[152:155]\n" \
"ds_read_b64_tr_b16 a[36:37], v11 offset:33792\n" \
"v_mfma_f32_16x16x32_bf16 v[156:159], a[84:87], a[60:63], v[156:159]\n" \
"s_add_u32 m0, s50, 77824\n" \
"ds_read_b64_tr_b16 a[38:39], v12 offset:33792\n" \
"v_mfma_f32_16x16x32_bf16 v[160:163], a[88:91], a[48:51], v[160:163]\n" \
"buffer_load_dwordx4 v16, s[28:31], s46 offen lds\n" \
"v_mfma_f32_16x16x32_bf16 v[164:167], a[92:95], a[48:51], v[164:167]\n" \
"ds_read_b64_tr_b16 a[40:41], v9 offset:34304\n" \
"v_mfma_f32_16x16x32_bf16 v[168:171], a[88:91], a[52:55], v[168:171]\n" \
"ds_read_b64_tr_b16 a[42:43], v10 offset:34304\n" \
"v_mfma_f32_16x16x32_bf16 v[172:175], a[92:95], a[52:55], v[172:175]\n" \
"ds_read_b64_tr_b16 a[44:45], v11 offset:34304\n" \
"v_mfma_f32_16x16x32_bf16 v[176:179], a[88:91], a[56:59], v[176:179]\n" \
"s_mov_b32 s46, 768\n" \
"s_add_u32 m0, s50, 81920\n" \
"ds_read_b64_tr_b16 a[46:47], v12 offset:34304\n" \
"v_mfma_f32_16x16x32_bf16 v[180:183], a[92:95], a[56:59], v[180:183]\n" \
"buffer_load_dwordx4 v17, s[28:31], s46 offen lds\n" \
"v_mfma_f32_16x16x32_bf16 v[184:187], a[88:91], a[60:63], v[184:187]\n" \
"ds_read_b64_tr_b16 a[16:17], v9 offset:32768\n" \
"v_mfma_f32_16x16x32_bf16 v[188:191], a[92:95], a[60:63], v[188:191]\n" \
"v_mfma_f32_16x16x32_bf16 v[64:67], a[64:67], a[48:51], v[64:67]\n" \
"ds_read_b64_tr_b16 a[18:19], v10 offset:32768\n" \
"v_mfma_f32_16x16x32_bf16 v[68:71], a[68:71], a[48:51], v[68:71]\n" \
"s_add_u32 m0, s50, 86016\n" \
"ds_read_b64_tr_b16 a[20:21], v11 offset:32768\n" \
"v_mfma_f32_16x16x32_bf16 v[72:75], a[64:67], a[52:55], v[72:75]\n" \
"buffer_load_dwordx4 v18, s[28:31], s46 offen lds\n" \
"v_mfma_f32_16x16x32_bf16 v[76:79], a[68:71], a[52:55], v[76:79]\n" \
"ds_read_b64_tr_b16 a[22:23], v12 offset:32768\n" \
"v_mfma_f32_16x16x32_bf16 v[80:83], a[64:67], a[56:59], v[80:83]\n" \
"buffer_store_dwordx4 v[34:37], v4, s[36:39], s97 offen offset:0 sc1\n" \
"v_mfma_f32_16x16x32_bf16 v[84:87], a[68:71], a[56:59], v[84:87]\n" \
"s_add_u32 s96, s96, s92\n" \
"v_cvt_pk_bf16_f32 v34, v46, v47\n" \
"v_cvt_pk_bf16_f32 v35, v48, v49\n" \
"v_mfma_f32_16x16x32_bf16 v[88:91], a[64:67], a[60:63], v[88:91]\n" \
"v_accvgpr_read_b32 v50, a172\n" \
"v_accvgpr_read_b32 v51, a173\n" \
"v_mfma_f32_16x16x32_bf16 v[92:95], a[68:71], a[60:63], v[92:95]\n" \
"s_waitcnt lgkmcnt(0)\n" \
"v_accvgpr_read_b32 v52, a174\n" \
"v_mfma_f32_16x16x32_bf16 v[96:99], a[24:27], a[0:3], v[96:99]\n" \
"s_add_u32 m0, s50, 90112\n" \
"ds_read_b64_tr_b16 a[72:73], v9 offset:41472\n" \
"v_accvgpr_read_b32 v53, a175\n" \
"v_mfma_f32_16x16x32_bf16 v[100:103], a[28:31], a[0:3], v[100:103]\n" \
"buffer_load_dwordx4 v19, s[28:31], s46 offen lds\n" \
"v_mfma_f32_16x16x32_bf16 v[104:107], a[24:27], a[4:7], v[104:107]\n" \
"s_mov_b64 s[40:41], s[4:5]\n" \
"ds_read_b64_tr_b16 a[74:75], v10 offset:41472\n" \
"v_mul_f32 v50, s20, v50\n" \
"v_mfma_f32_16x16x32_bf16 v[108:111], a[28:31], a[4:7], v[108:111]\n" \
"s_and_b32 s41, s41, 0xffff\n" \
"ds_read_b64_tr_b16 a[76:77], v11 offset:41472\n" \
"v_mul_f32 v51, s20, v51\n" \
"v_mfma_f32_16x16x32_bf16 v[112:115], a[24:27], a[8:11], v[112:115]\n" \
"s_lshl_b32 s62, s6, 20\n" \
"ds_read_b64_tr_b16 a[78:79], v12 offset:41472\n" \
"v_mul_f32 v52, s20, v52\n" \
"v_mfma_f32_16x16x32_bf16 v[116:119], a[28:31], a[8:11], v[116:119]\n" \
"s_add_u32 m0, s50, 94208\n" \
"ds_read_b128 a[48:51], v6 offset:0\n" \
"v_mul_f32 v53, s20, v53\n" \
"v_mfma_f32_16x16x32_bf16 v[120:123], a[24:27], a[12:15], v[120:123]\n" \
"buffer_load_dwordx4 v20, s[28:31], s46 offen lds\n" \
"v_mfma_f32_16x16x32_bf16 v[124:127], a[28:31], a[12:15], v[124:127]\n" \
"s_add_u32 s16, s62, 33554432\n" \
"ds_read_b128 a[52:55], v6 offset:2048\n" \
"v_cvt_pk_bf16_f32 v36, v50, v51\n" \
"v_mfma_f32_16x16x32_bf16 v[128:131], a[32:35], a[0:3], v[128:131]\n" \
"s_lshl_b32 s63, s7, 16\n" \
"v_cvt_pk_bf16_f32 v37, v52, v53\n" \
"v_accvgpr_read_b32 v46, a176\n" \
"v_mfma_f32_16x16x32_bf16 v[132:135], a[36:39], a[0:3], v[132:135]\n" \
"s_sub_u32 s63, 1048576, s63\n" \
"ds_read_b128 a[56:59], v6 offset:4096\n" \
"v_accvgpr_read_b32 v47, a177\n" \
"v_mfma_f32_16x16x32_bf16 v[136:139], a[32:35], a[4:7], v[136:139]\n" \
"s_mov_b32 s47, 3670016\n" \
"s_add_u32 m0, s51, 16384\n" \
"ds_read_b128 a[60:63], v6 offset:6144\n" \
"v_mfma_f32_16x16x32_bf16 v[140:143], a[36:39], a[4:7], v[140:143]\n" \
"buffer_load_dwordx4 v21, s[32:35], s47 offen offset:0 lds\n" \
"v_mfma_f32_16x16x32_bf16 v[144:147], a[32:35], a[8:11], v[144:147]\n" \
"s_add_u32 s42, s16, s63\n" \
"ds_read_b64_tr_b16 a[80:81], v9 offset:41984\n" \
"v_accvgpr_read_b32 v48, a178\n" \
"v_mfma_f32_16x16x32_bf16 v[148:151], a[36:39], a[8:11], v[148:151]\n" \
"ds_read_b64_tr_b16 a[82:83], v10 offset:41984\n" \
"v_accvgpr_read_b32 v49, a179\n" \
"v_mfma_f32_16x16x32_bf16 v[152:155], a[32:35], a[12:15], v[152:155]\n" \
"ds_read_b64_tr_b16 a[84:85], v11 offset:41984\n" \
"v_mul_f32 v46, s20, v46\n" \
"v_mfma_f32_16x16x32_bf16 v[156:159], a[36:39], a[12:15], v[156:159]\n" \
"s_add_u32 m0, s51, 17280\n" \
"ds_read_b64_tr_b16 a[86:87], v12 offset:41984\n" \
"v_mul_f32 v47, s20, v47\n" \
"v_mfma_f32_16x16x32_bf16 v[160:163], a[40:43], a[0:3], v[160:163]\n" \
"buffer_load_dwordx4 v21, s[32:35], s47 offen offset:128 lds\n" \
"v_mfma_f32_16x16x32_bf16 v[164:167], a[44:47], a[0:3], v[164:167]\n" \
"ds_read_b64_tr_b16 a[88:89], v9 offset:42496\n" \
"v_mul_f32 v48, s20, v48\n" \
"v_mfma_f32_16x16x32_bf16 v[168:171], a[40:43], a[4:7], v[168:171]\n" \
"ds_read_b64_tr_b16 a[90:91], v10 offset:42496\n" \
"v_mul_f32 v49, s20, v49\n" \
"v_mfma_f32_16x16x32_bf16 v[172:175], a[44:47], a[4:7], v[172:175]\n" \
"s_add_u32 s100, s100, s64\ns_addc_u32 s101, s101, 0\n" \
"ds_read_b64_tr_b16 a[92:93], v11 offset:42496\n" \
"v_mfma_f32_16x16x32_bf16 v[176:179], a[40:43], a[8:11], v[176:179]\n" \
"s_mov_b32 s47, 3932160\n" \
"s_add_u32 m0, s51, 24576\n" \
"ds_read_b64_tr_b16 a[94:95], v12 offset:42496\n" \
"v_mfma_f32_16x16x32_bf16 v[180:183], a[44:47], a[8:11], v[180:183]\n" \
"buffer_load_dwordx4 v21, s[32:35], s47 offen offset:0 lds\n" \
"v_mfma_f32_16x16x32_bf16 v[184:187], a[40:43], a[12:15], v[184:187]\n" \
"ds_read_b64_tr_b16 a[64:65], v9 offset:40960\n" \
"v_mfma_f32_16x16x32_bf16 v[188:191], a[44:47], a[12:15], v[188:191]\n" \
"s_cmp_lt_u32 %2, 2\ns_cselect_b32 s62, 1064960, 0\ns_cselect_b32 s9, -1, 1\n" \
"v_mfma_f32_16x16x32_bf16 v[64:67], a[16:19], a[0:3], v[64:67]\n" \
"s_add_u32 s16, s16, s62\n" \
"ds_read_b64_tr_b16 a[66:67], v10 offset:40960\n" \
"s_add_u32 s17, s16, 8192\n" \
"v_mfma_f32_16x16x32_bf16 v[68:71], a[20:23], a[0:3], v[68:71]\n" \
"s_add_u32 m0, s51, 25472\n" \
"ds_read_b64_tr_b16 a[68:69], v11 offset:40960\n" \
"s_lshl_b32 s9, s9, 14\n" \
"v_mfma_f32_16x16x32_bf16 v[72:75], a[16:19], a[4:7], v[72:75]\n" \
"buffer_load_dwordx4 v21, s[32:35], s47 offen offset:128 lds\n" \
"v_mfma_f32_16x16x32_bf16 v[76:79], a[20:23], a[4:7], v[76:79]\n" \
"s_mov_b32 s19, s9\n" \
"ds_read_b64_tr_b16 a[70:71], v12 offset:40960\n" \
"s_and_b32 s63, %2, 1\n" \
"v_mfma_f32_16x16x32_bf16 v[80:83], a[16:19], a[8:11], v[80:83]\n" \
"buffer_store_dwordx4 v[38:41], v4, s[36:39], s96 offen offset:0 sc1\n" \
"v_mfma_f32_16x16x32_bf16 v[84:87], a[20:23], a[8:11], v[84:87]\n" \
"s_add_u32 s97, s96, s98\n" \
"v_cvt_pk_bf16_f32 v38, v46, v47\n" \
"v_cvt_pk_bf16_f32 v39, v48, v49\n" \
"v_mfma_f32_16x16x32_bf16 v[88:91], a[16:19], a[12:15], v[88:91]\n" \
"s_waitcnt vmcnt(19) lgkmcnt(0)\n" \
"v_accvgpr_read_b32 v50, a180\n" \
"v_accvgpr_read_b32 v51, a181\n" \
"v_mfma_f32_16x16x32_bf16 v[92:95], a[20:23], a[12:15], v[92:95]\n" \
"s_barrier\n" \
"v_accvgpr_read_b32 v52, a182\n" \
"s_lshl_b32 s63, s63, 12\n" \
"v_mfma_f32_16x16x32_bf16 v[96:99], a[72:75], a[48:51], v[96:99]\n" \
"s_mov_b32 s46, 896\n" \
"s_add_u32 m0, s50, 0\n" \
"ds_read_b64_tr_b16 a[24:25], v9 offset:49664\n" \
"v_mfma_f32_16x16x32_bf16 v[100:103], a[76:79], a[48:51], v[100:103]\n" \
"buffer_load_dwordx4 v13, s[28:31], s46 offen lds\n" \
"v_mfma_f32_16x16x32_bf16 v[104:107], a[72:75], a[52:55], v[104:107]\n" \
"ds_read_b64_tr_b16 a[26:27], v10 offset:49664\n" \
"v_accvgpr_read_b32 v53, a183\n" \
"v_mfma_f32_16x16x32_bf16 v[108:111], a[76:79], a[52:55], v[108:111]\n" \
"ds_read_b64_tr_b16 a[28:29], v11 offset:49664\n" \
"v_mul_f32 v50, s20, v50\n" \
"v_mfma_f32_16x16x32_bf16 v[112:115], a[72:75], a[56:59], v[112:115]\n" \
"ds_read_b64_tr_b16 a[30:31], v12 offset:49664\n" \
"v_mul_f32 v51, s20, v51\n" \
"v_mfma_f32_16x16x32_bf16 v[116:119], a[76:79], a[56:59], v[116:119]\n" \
"s_add_u32 m0, s50, 4096\n" \
"ds_read_b128 a[0:3], v5 offset:32768\n" \
"v_mul_f32 v52, s20, v52\n" \
"v_mfma_f32_16x16x32_bf16 v[120:123], a[72:75], a[60:63], v[120:123]\n" \
"buffer_load_dwordx4 v14, s[28:31], s46 offen lds\n" \
"v_mfma_f32_16x16x32_bf16 v[124:127], a[76:79], a[60:63], v[124:127]\n" \
"ds_read_b128 a[4:7], v5 offset:34816\n" \
"v_mul_f32 v53, s20, v53\n" \
"v_mfma_f32_16x16x32_bf16 v[128:131], a[80:83], a[48:51], v[128:131]\n" \
"v_cvt_pk_bf16_f32 v40, v50, v51\n" \
"v_cvt_pk_bf16_f32 v41, v52, v53\n" \
"v_mfma_f32_16x16x32_bf16 v[132:135], a[84:87], a[48:51], v[132:135]\n" \
"ds_read_b128 a[8:11], v5 offset:36864\n" \
"v_accvgpr_read_b32 v46, a184\n" \
"v_mfma_f32_16x16x32_bf16 v[136:139], a[80:83], a[52:55], v[136:139]\n" \
"s_add_u32 m0, s50, 8192\n" \
"ds_read_b128 a[12:15], v5 offset:38912\n" \
"v_accvgpr_read_b32 v47, a185\n" \
"v_mfma_f32_16x16x32_bf16 v[140:143], a[84:87], a[52:55], v[140:143]\n" \
"buffer_load_dwordx4 v15, s[28:31], s46 offen lds\n" \
"v_mfma_f32_16x16x32_bf16 v[144:147], a[80:83], a[56:59], v[144:147]\n" \
"ds_read_b64_tr_b16 a[32:33], v9 offset:50176\n" \
"v_accvgpr_read_b32 v48, a186\n" \
"v_mfma_f32_16x16x32_bf16 v[148:151], a[84:87], a[56:59], v[148:151]\n" \
"ds_read_b64_tr_b16 a[34:35], v10 offset:50176\n" \
"v_accvgpr_read_b32 v49, a187\n" \
"v_mfma_f32_16x16x32_bf16 v[152:155], a[80:83], a[60:63], v[152:155]\n" \
"ds_read_b64_tr_b16 a[36:37], v11 offset:50176\n" \
"v_mul_f32 v46, s20, v46\n" \
"v_mfma_f32_16x16x32_bf16 v[156:159], a[84:87], a[60:63], v[156:159]\n" \
"s_add_u32 m0, s50, 12288\n" \
"ds_read_b64_tr_b16 a[38:39], v12 offset:50176\n" \
"v_mul_f32 v47, s20, v47\n" \
"v_mfma_f32_16x16x32_bf16 v[160:163], a[88:91], a[48:51], v[160:163]\n" \
"buffer_load_dwordx4 v16, s[28:31], s46 offen lds\n" \
"v_mfma_f32_16x16x32_bf16 v[164:167], a[92:95], a[48:51], v[164:167]\n" \
"ds_read_b64_tr_b16 a[40:41], v9 offset:50688\n" \
"v_mul_f32 v48, s20, v48\n" \
"v_mfma_f32_16x16x32_bf16 v[168:171], a[88:91], a[52:55], v[168:171]\n" \
"ds_read_b64_tr_b16 a[42:43], v10 offset:50688\n" \
"v_mul_f32 v49, s20, v49\n" \
"v_mfma_f32_16x16x32_bf16 v[172:175], a[92:95], a[52:55], v[172:175]\n" \
"s_lshr_b32 s62, s6, 1\n" \
"ds_read_b64_tr_b16 a[44:45], v11 offset:50688\n" \
"v_add_u32 v60, s63, v4\n" \
"v_mfma_f32_16x16x32_bf16 v[176:179], a[88:91], a[56:59], v[176:179]\n" \
"s_mov_b32 s46, 896\n" \
"s_add_u32 m0, s50, 16384\n" \
"ds_read_b64_tr_b16 a[46:47], v12 offset:50688\n" \
"v_mfma_f32_16x16x32_bf16 v[180:183], a[92:95], a[56:59], v[180:183]\n" \
"buffer_load_dwordx4 v17, s[28:31], s46 offen lds\n" \
"v_mfma_f32_16x16x32_bf16 v[184:187], a[88:91], a[60:63], v[184:187]\n" \
"s_lshl_b32 s62, s62, 7\n" \
"ds_read_b64_tr_b16 a[16:17], v9 offset:49152\n" \
"s_add_u32 s62, s62, 94375936\n" \
"v_mfma_f32_16x16x32_bf16 v[188:191], a[92:95], a[60:63], v[188:191]\n" \
"v_bfe_u32 v57, v4, 4, 4\n" \
"v_lshl_add_u32 v57, v57, 3, s62\n" \
"v_mfma_f32_16x16x32_bf16 v[64:67], a[64:67], a[48:51], v[64:67]\n" \
"ds_read_b64_tr_b16 a[18:19], v10 offset:49152\n" \
"v_mfma_f32_16x16x32_bf16 v[68:71], a[68:71], a[48:51], v[68:71]\n" \
"s_add_u32 m0, s50, 20480\n" \
"ds_read_b64_tr_b16 a[20:21], v11 offset:49152\n" \
"v_mfma_f32_16x16x32_bf16 v[72:75], a[64:67], a[52:55], v[72:75]\n" \
"buffer_load_dwordx4 v18, s[28:31], s46 offen lds\n" \
"v_mfma_f32_16x16x32_bf16 v[76:79], a[68:71], a[52:55], v[76:79]\n" \
"ds_read_b64_tr_b16 a[22:23], v12 offset:49152\n" \
"v_mfma_f32_16x16x32_bf16 v[80:83], a[64:67], a[56:59], v[80:83]\n" \
"buffer_store_dwordx4 v[42:45], v4, s[36:39], s97 offen offset:0 sc1\n" \
"v_mfma_f32_16x16x32_bf16 v[84:87], a[68:71], a[56:59], v[84:87]\n" \
"s_add_u32 s96, s96, s92\n" \
"v_cvt_pk_bf16_f32 v42, v46, v47\n" \
"v_cvt_pk_bf16_f32 v43, v48, v49\n" \
"v_mfma_f32_16x16x32_bf16 v[88:91], a[64:67], a[60:63], v[88:91]\n" \
"v_accvgpr_read_b32 v50, a188\n" \
"v_accvgpr_read_b32 v51, a189\n" \
"v_mfma_f32_16x16x32_bf16 v[92:95], a[68:71], a[60:63], v[92:95]\n" \
"s_waitcnt lgkmcnt(0)\n" \
"v_accvgpr_read_b32 v52, a190\n" \
"v_mfma_f32_16x16x32_bf16 v[96:99], a[24:27], a[0:3], v[96:99]\n" \
"s_add_u32 m0, s50, 24576\n" \
"ds_read_b64_tr_b16 a[72:73], v9 offset:57856\n" \
"v_accvgpr_read_b32 v53, a191\n" \
"v_mfma_f32_16x16x32_bf16 v[100:103], a[28:31], a[0:3], v[100:103]\n" \
"LH11_%=:\ns_cbranch_execz LHR0_%=\n" \
"buffer_load_dwordx4 v19, s[28:31], s46 offen lds\n" \
"v_mfma_f32_16x16x32_bf16 v[104:107], a[24:27], a[4:7], v[104:107]\n" \
"ds_read_b64_tr_b16 a[74:75], v10 offset:57856\n" \
"v_mul_f32 v50, s20, v50\n" \
"v_mfma_f32_16x16x32_bf16 v[108:111], a[28:31], a[4:7], v[108:111]\n" \
"ds_read_b64_tr_b16 a[76:77], v11 offset:57856\n" \
"v_mul_f32 v51, s20, v51\n" \
"v_mfma_f32_16x16x32_bf16 v[112:115], a[24:27], a[8:11], v[112:115]\n" \
"ds_read_b64_tr_b16 a[78:79], v12 offset:57856\n" \
"v_mul_f32 v52, s20, v52\n" \
"v_mfma_f32_16x16x32_bf16 v[116:119], a[28:31], a[8:11], v[116:119]\n" \
"s_add_u32 m0, s50, 28672\n" \
"ds_read_b128 a[48:51], v6 offset:32768\n" \
"v_mul_f32 v53, s20, v53\n" \
"v_mfma_f32_16x16x32_bf16 v[120:123], a[24:27], a[12:15], v[120:123]\n" \
"buffer_load_dwordx4 v20, s[28:31], s46 offen lds\n" \
"v_mfma_f32_16x16x32_bf16 v[124:127], a[28:31], a[12:15], v[124:127]\n" \
"ds_read_b128 a[52:55], v6 offset:34816\n" \
"v_cvt_pk_bf16_f32 v44, v50, v51\n" \
"v_mfma_f32_16x16x32_bf16 v[128:131], a[32:35], a[0:3], v[128:131]\n" \
"v_cvt_pk_bf16_f32 v45, v52, v53\n" \
"v_mfma_f32_16x16x32_bf16 v[132:135], a[36:39], a[0:3], v[132:135]\n" \
"ds_read_b128 a[56:59], v6 offset:36864\n" \
"v_mfma_f32_16x16x32_bf16 v[136:139], a[32:35], a[4:7], v[136:139]\n" \
"s_mov_b32 s47, 4194304\n" \
"s_add_u32 m0, s51, 32768\n" \
"ds_read_b128 a[60:63], v6 offset:38912\n" \
"v_mfma_f32_16x16x32_bf16 v[140:143], a[36:39], a[4:7], v[140:143]\n" \
"buffer_load_dwordx4 v21, s[32:35], s47 offen offset:0 lds\n" \
"v_mfma_f32_16x16x32_bf16 v[144:147], a[32:35], a[8:11], v[144:147]\n" \
"ds_read_b64_tr_b16 a[80:81], v9 offset:58368\n" \
"v_mfma_f32_16x16x32_bf16 v[148:151], a[36:39], a[8:11], v[148:151]\n" \
"ds_read_b64_tr_b16 a[82:83], v10 offset:58368\n" \
"v_mfma_f32_16x16x32_bf16 v[152:155], a[32:35], a[12:15], v[152:155]\n" \
"ds_read_b64_tr_b16 a[84:85], v11 offset:58368\n" \
"v_mfma_f32_16x16x32_bf16 v[156:159], a[36:39], a[12:15], v[156:159]\n" \
"s_add_u32 m0, s51, 33664\n" \
"ds_read_b64_tr_b16 a[86:87], v12 offset:58368\n" \
"v_mfma_f32_16x16x32_bf16 v[160:163], a[40:43], a[0:3], v[160:163]\n" \
"buffer_load_dwordx4 v21, s[32:35], s47 offen offset:128 lds\n" \
"v_mfma_f32_16x16x32_bf16 v[164:167], a[44:47], a[0:3], v[164:167]\n" \
"ds_read_b64_tr_b16 a[88:89], v9 offset:58880\n" \
"v_mfma_f32_16x16x32_bf16 v[168:171], a[40:43], a[4:7], v[168:171]\n" \
"ds_read_b64_tr_b16 a[90:91], v10 offset:58880\n" \
"v_mfma_f32_16x16x32_bf16 v[172:175], a[44:47], a[4:7], v[172:175]\n" \
"ds_read_b64_tr_b16 a[92:93], v11 offset:58880\n" \
"v_mfma_f32_16x16x32_bf16 v[176:179], a[40:43], a[8:11], v[176:179]\n" \
"s_mov_b32 s47, 4456448\n" \
"s_add_u32 m0, s51, 40960\n" \
"ds_read_b64_tr_b16 a[94:95], v12 offset:58880\n" \
"v_mfma_f32_16x16x32_bf16 v[180:183], a[44:47], a[8:11], v[180:183]\n" \
"buffer_load_dwordx4 v21, s[32:35], s47 offen offset:0 lds\n" \
"v_mfma_f32_16x16x32_bf16 v[184:187], a[40:43], a[12:15], v[184:187]\n" \
"ds_read_b64_tr_b16 a[64:65], v9 offset:57344\n" \
"v_mfma_f32_16x16x32_bf16 v[188:191], a[44:47], a[12:15], v[188:191]\n" \
"v_mfma_f32_16x16x32_bf16 v[64:67], a[16:19], a[0:3], v[64:67]\n" \
"ds_read_b64_tr_b16 a[66:67], v10 offset:57344\n" \
"v_mfma_f32_16x16x32_bf16 v[68:71], a[20:23], a[0:3], v[68:71]\n" \
"s_add_u32 m0, s51, 41856\n" \
"ds_read_b64_tr_b16 a[68:69], v11 offset:57344\n" \
"v_mfma_f32_16x16x32_bf16 v[72:75], a[16:19], a[4:7], v[72:75]\n" \
"buffer_load_dwordx4 v21, s[32:35], s47 offen offset:128 lds\n" \
"v_mfma_f32_16x16x32_bf16 v[76:79], a[20:23], a[4:7], v[76:79]\n" \
"ds_read_b64_tr_b16 a[70:71], v12 offset:57344\n" \
"v_mfma_f32_16x16x32_bf16 v[80:83], a[16:19], a[8:11], v[80:83]\n" \
"buffer_store_dwordx4 v[30:33], v4, s[36:39], s96 offen offset:0 sc1\n" \
"v_mfma_f32_16x16x32_bf16 v[84:87], a[20:23], a[8:11], v[84:87]\n" \
"s_add_u32 s97, s96, s98\n" \
"v_mfma_f32_16x16x32_bf16 v[88:91], a[16:19], a[12:15], v[88:91]\n" \
"s_waitcnt vmcnt(19) lgkmcnt(0)\n" \
"v_mfma_f32_16x16x32_bf16 v[92:95], a[20:23], a[12:15], v[92:95]\n" \
"s_barrier\n" \
"v_mfma_f32_16x16x32_bf16 v[96:99], a[72:75], a[48:51], v[96:99]\n" \
"s_mov_b32 s46, 1024\n" \
"s_add_u32 m0, s50, 32768\n" \
"ds_read_b64_tr_b16 a[24:25], v9 offset:512\n" \
"v_mfma_f32_16x16x32_bf16 v[100:103], a[76:79], a[48:51], v[100:103]\n" \
"buffer_load_dwordx4 v13, s[28:31], s46 offen lds\n" \
"v_mfma_f32_16x16x32_bf16 v[104:107], a[72:75], a[52:55], v[104:107]\n" \
"ds_read_b64_tr_b16 a[26:27], v10 offset:512\n" \
"v_mfma_f32_16x16x32_bf16 v[108:111], a[76:79], a[52:55], v[108:111]\n" \
"ds_read_b64_tr_b16 a[28:29], v11 offset:512\n" \
"v_mfma_f32_16x16x32_bf16 v[112:115], a[72:75], a[56:59], v[112:115]\n" \
"ds_read_b64_tr_b16 a[30:31], v12 offset:512\n" \
"v_mfma_f32_16x16x32_bf16 v[116:119], a[76:79], a[56:59], v[116:119]\n" \
"s_add_u32 m0, s50, 36864\n" \
"ds_read_b128 a[0:3], v7 offset:0\n" \
"v_mfma_f32_16x16x32_bf16 v[120:123], a[72:75], a[60:63], v[120:123]\n" \
"buffer_load_dwordx4 v14, s[28:31], s46 offen lds\n" \
"v_mfma_f32_16x16x32_bf16 v[124:127], a[76:79], a[60:63], v[124:127]\n" \
"ds_read_b128 a[4:7], v7 offset:2048\n" \
"v_mfma_f32_16x16x32_bf16 v[128:131], a[80:83], a[48:51], v[128:131]\n" \
"v_mfma_f32_16x16x32_bf16 v[132:135], a[84:87], a[48:51], v[132:135]\n" \
"ds_read_b128 a[8:11], v7 offset:4096\n" \
"v_mfma_f32_16x16x32_bf16 v[136:139], a[80:83], a[52:55], v[136:139]\n" \
"s_add_u32 m0, s50, 40960\n" \
"ds_read_b128 a[12:15], v7 offset:6144\n" \
"v_mfma_f32_16x16x32_bf16 v[140:143], a[84:87], a[52:55], v[140:143]\n" \
"buffer_load_dwordx4 v15, s[28:31], s46 offen lds\n" \
"v_mfma_f32_16x16x32_bf16 v[144:147], a[80:83], a[56:59], v[144:147]\n" \
"ds_read_b64_tr_b16 a[32:33], v9 offset:1024\n" \
"v_mfma_f32_16x16x32_bf16 v[148:151], a[84:87], a[56:59], v[148:151]\n" \
"ds_read_b64_tr_b16 a[34:35], v10 offset:1024\n" \
"v_mfma_f32_16x16x32_bf16 v[152:155], a[80:83], a[60:63], v[152:155]\n" \
"ds_read_b64_tr_b16 a[36:37], v11 offset:1024\n" \
"v_mfma_f32_16x16x32_bf16 v[156:159], a[84:87], a[60:63], v[156:159]\n" \
"s_add_u32 m0, s50, 45056\n" \
"ds_read_b64_tr_b16 a[38:39], v12 offset:1024\n" \
"v_mfma_f32_16x16x32_bf16 v[160:163], a[88:91], a[48:51], v[160:163]\n" \
"buffer_load_dwordx4 v16, s[28:31], s46 offen lds\n" \
"v_mfma_f32_16x16x32_bf16 v[164:167], a[92:95], a[48:51], v[164:167]\n" \
"ds_read_b64_tr_b16 a[40:41], v9 offset:1536\n" \
"v_mfma_f32_16x16x32_bf16 v[168:171], a[88:91], a[52:55], v[168:171]\n" \
"ds_read_b64_tr_b16 a[42:43], v10 offset:1536\n" \
"v_mfma_f32_16x16x32_bf16 v[172:175], a[92:95], a[52:55], v[172:175]\n" \
"ds_read_b64_tr_b16 a[44:45], v11 offset:1536\n" \
"v_mfma_f32_16x16x32_bf16 v[176:179], a[88:91], a[56:59], v[176:179]\n" \
"s_mov_b32 s46, 1024\n" \
"s_add_u32 m0, s50, 49152\n" \
"ds_read_b64_tr_b16 a[46:47], v12 offset:1536\n" \
"v_mfma_f32_16x16x32_bf16 v[180:183], a[92:95], a[56:59], v[180:183]\n" \
"buffer_load_dwordx4 v17, s[28:31], s46 offen lds\n" \
"v_mfma_f32_16x16x32_bf16 v[184:187], a[88:91], a[60:63], v[184:187]\n" \
"ds_read_b64_tr_b16 a[16:17], v9 offset:0\n" \
"v_mfma_f32_16x16x32_bf16 v[188:191], a[92:95], a[60:63], v[188:191]\n" \
"v_mfma_f32_16x16x32_bf16 v[64:67], a[64:67], a[48:51], v[64:67]\n" \
"ds_read_b64_tr_b16 a[18:19], v10 offset:0\n" \
"v_mfma_f32_16x16x32_bf16 v[68:71], a[68:71], a[48:51], v[68:71]\n" \
"s_add_u32 m0, s50, 53248\n" \
"ds_read_b64_tr_b16 a[20:21], v11 offset:0\n" \
"v_mfma_f32_16x16x32_bf16 v[72:75], a[64:67], a[52:55], v[72:75]\n" \
"buffer_load_dwordx4 v18, s[28:31], s46 offen lds\n" \
"v_mfma_f32_16x16x32_bf16 v[76:79], a[68:71], a[52:55], v[76:79]\n" \
"ds_read_b64_tr_b16 a[22:23], v12 offset:0\n" \
"v_mfma_f32_16x16x32_bf16 v[80:83], a[64:67], a[56:59], v[80:83]\n" \
"buffer_store_dwordx4 v[34:37], v4, s[36:39], s97 offen offset:0 sc1\n" \
"v_mfma_f32_16x16x32_bf16 v[84:87], a[68:71], a[56:59], v[84:87]\n" \
"s_add_u32 s96, s96, s92\n" \
"v_mfma_f32_16x16x32_bf16 v[88:91], a[64:67], a[60:63], v[88:91]\n" \
"v_mfma_f32_16x16x32_bf16 v[92:95], a[68:71], a[60:63], v[92:95]\n" \
"s_waitcnt lgkmcnt(0)\n" \
"v_mfma_f32_16x16x32_bf16 v[96:99], a[24:27], a[0:3], v[96:99]\n" \
"s_add_u32 m0, s50, 57344\n" \
"ds_read_b64_tr_b16 a[72:73], v9 offset:8704\n" \
"v_mfma_f32_16x16x32_bf16 v[100:103], a[28:31], a[0:3], v[100:103]\n" \
"buffer_load_dwordx4 v19, s[28:31], s46 offen lds\n" \
"v_mfma_f32_16x16x32_bf16 v[104:107], a[24:27], a[4:7], v[104:107]\n" \
"ds_read_b64_tr_b16 a[74:75], v10 offset:8704\n" \
"v_mfma_f32_16x16x32_bf16 v[108:111], a[28:31], a[4:7], v[108:111]\n" \
"ds_read_b64_tr_b16 a[76:77], v11 offset:8704\n" \
"v_mfma_f32_16x16x32_bf16 v[112:115], a[24:27], a[8:11], v[112:115]\n" \
"ds_read_b64_tr_b16 a[78:79], v12 offset:8704\n" \
"v_mfma_f32_16x16x32_bf16 v[116:119], a[28:31], a[8:11], v[116:119]\n" \
"s_add_u32 m0, s50, 61440\n" \
"ds_read_b128 a[48:51], v8 offset:0\n" \
"v_mfma_f32_16x16x32_bf16 v[120:123], a[24:27], a[12:15], v[120:123]\n" \
"buffer_load_dwordx4 v20, s[28:31], s46 offen lds\n" \
"v_mfma_f32_16x16x32_bf16 v[124:127], a[28:31], a[12:15], v[124:127]\n" \
"ds_read_b128 a[52:55], v8 offset:2048\n" \
"v_mfma_f32_16x16x32_bf16 v[128:131], a[32:35], a[0:3], v[128:131]\n" \
"v_mfma_f32_16x16x32_bf16 v[132:135], a[36:39], a[0:3], v[132:135]\n" \
"ds_read_b128 a[56:59], v8 offset:4096\n" \
"v_mfma_f32_16x16x32_bf16 v[136:139], a[32:35], a[4:7], v[136:139]\n" \
"s_mov_b32 s47, 4718592\n" \
"s_add_u32 m0, s51, 49152\n" \
"ds_read_b128 a[60:63], v8 offset:6144\n" \
"v_mfma_f32_16x16x32_bf16 v[140:143], a[36:39], a[4:7], v[140:143]\n" \
"buffer_load_dwordx4 v21, s[32:35], s47 offen offset:0 lds\n" \
"v_mfma_f32_16x16x32_bf16 v[144:147], a[32:35], a[8:11], v[144:147]\n" \
"ds_read_b64_tr_b16 a[80:81], v9 offset:9216\n" \
"v_mfma_f32_16x16x32_bf16 v[148:151], a[36:39], a[8:11], v[148:151]\n" \
"ds_read_b64_tr_b16 a[82:83], v10 offset:9216\n" \
"v_mfma_f32_16x16x32_bf16 v[152:155], a[32:35], a[12:15], v[152:155]\n" \
"ds_read_b64_tr_b16 a[84:85], v11 offset:9216\n" \
"v_mfma_f32_16x16x32_bf16 v[156:159], a[36:39], a[12:15], v[156:159]\n" \
"s_add_u32 m0, s51, 50048\n" \
"ds_read_b64_tr_b16 a[86:87], v12 offset:9216\n" \
"v_mfma_f32_16x16x32_bf16 v[160:163], a[40:43], a[0:3], v[160:163]\n" \
"buffer_load_dwordx4 v21, s[32:35], s47 offen offset:128 lds\n" \
"v_mfma_f32_16x16x32_bf16 v[164:167], a[44:47], a[0:3], v[164:167]\n" \
"ds_read_b64_tr_b16 a[88:89], v9 offset:9728\n" \
"v_mfma_f32_16x16x32_bf16 v[168:171], a[40:43], a[4:7], v[168:171]\n" \
"ds_read_b64_tr_b16 a[90:91], v10 offset:9728\n" \
"v_mfma_f32_16x16x32_bf16 v[172:175], a[44:47], a[4:7], v[172:175]\n" \
"ds_read_b64_tr_b16 a[92:93], v11 offset:9728\n" \
"v_mfma_f32_16x16x32_bf16 v[176:179], a[40:43], a[8:11], v[176:179]\n" \
"s_mov_b32 s47, 4980736\n" \
"s_add_u32 m0, s51, 57344\n" \
"ds_read_b64_tr_b16 a[94:95], v12 offset:9728\n" \
"v_mfma_f32_16x16x32_bf16 v[180:183], a[44:47], a[8:11], v[180:183]\n" \
"buffer_load_dwordx4 v21, s[32:35], s47 offen offset:0 lds\n" \
"v_mfma_f32_16x16x32_bf16 v[184:187], a[40:43], a[12:15], v[184:187]\n" \
"ds_read_b64_tr_b16 a[64:65], v9 offset:8192\n" \
"v_mfma_f32_16x16x32_bf16 v[188:191], a[44:47], a[12:15], v[188:191]\n" \
"v_mfma_f32_16x16x32_bf16 v[64:67], a[16:19], a[0:3], v[64:67]\n" \
"ds_read_b64_tr_b16 a[66:67], v10 offset:8192\n" \
"v_mfma_f32_16x16x32_bf16 v[68:71], a[20:23], a[0:3], v[68:71]\n" \
"s_add_u32 m0, s51, 58240\n" \
"ds_read_b64_tr_b16 a[68:69], v11 offset:8192\n" \
"v_mfma_f32_16x16x32_bf16 v[72:75], a[16:19], a[4:7], v[72:75]\n" \
"buffer_load_dwordx4 v21, s[32:35], s47 offen offset:128 lds\n" \
"v_mfma_f32_16x16x32_bf16 v[76:79], a[20:23], a[4:7], v[76:79]\n" \
"ds_read_b64_tr_b16 a[70:71], v12 offset:8192\n" \
"v_mfma_f32_16x16x32_bf16 v[80:83], a[16:19], a[8:11], v[80:83]\n" \
"buffer_store_dwordx4 v[38:41], v4, s[36:39], s96 offen offset:0 sc1\n" \
"v_mfma_f32_16x16x32_bf16 v[84:87], a[20:23], a[8:11], v[84:87]\n" \
"s_add_u32 s97, s96, s98\n" \
"v_mfma_f32_16x16x32_bf16 v[88:91], a[16:19], a[12:15], v[88:91]\n" \
"s_waitcnt vmcnt(19) lgkmcnt(0)\n" \
"v_mfma_f32_16x16x32_bf16 v[92:95], a[20:23], a[12:15], v[92:95]\n" \
"s_barrier\n" \
"v_mfma_f32_16x16x32_bf16 v[96:99], a[72:75], a[48:51], v[96:99]\n" \
"s_mov_b32 s46, 1152\n" \
"s_add_u32 m0, s50, 65536\n" \
"ds_read_b64_tr_b16 a[24:25], v9 offset:16896\n" \
"v_mfma_f32_16x16x32_bf16 v[100:103], a[76:79], a[48:51], v[100:103]\n" \
"buffer_load_dwordx4 v13, s[28:31], s46 offen lds\n" \
"v_mfma_f32_16x16x32_bf16 v[104:107], a[72:75], a[52:55], v[104:107]\n" \
"ds_read_b64_tr_b16 a[26:27], v10 offset:16896\n" \
"v_mfma_f32_16x16x32_bf16 v[108:111], a[76:79], a[52:55], v[108:111]\n" \
"ds_read_b64_tr_b16 a[28:29], v11 offset:16896\n" \
"v_mfma_f32_16x16x32_bf16 v[112:115], a[72:75], a[56:59], v[112:115]\n" \
"ds_read_b64_tr_b16 a[30:31], v12 offset:16896\n" \
"v_mfma_f32_16x16x32_bf16 v[116:119], a[76:79], a[56:59], v[116:119]\n" \
"s_add_u32 m0, s50, 69632\n" \
"ds_read_b128 a[0:3], v5 offset:0\n" \
"v_mfma_f32_16x16x32_bf16 v[120:123], a[72:75], a[60:63], v[120:123]\n" \
"buffer_load_dwordx4 v14, s[28:31], s46 offen lds\n" \
"v_mfma_f32_16x16x32_bf16 v[124:127], a[76:79], a[60:63], v[124:127]\n" \
"ds_read_b128 a[4:7], v5 offset:2048\n" \
"v_mfma_f32_16x16x32_bf16 v[128:131], a[80:83], a[48:51], v[128:131]\n" \
"v_mfma_f32_16x16x32_bf16 v[132:135], a[84:87], a[48:51], v[132:135]\n" \
"ds_read_b128 a[8:11], v5 offset:4096\n" \
"v_mfma_f32_16x16x32_bf16 v[136:139], a[80:83], a[52:55], v[136:139]\n" \
"s_add_u32 m0, s50, 73728\n" \
"ds_read_b128 a[12:15], v5 offset:6144\n" \
"v_mfma_f32_16x16x32_bf16 v[140:143], a[84:87], a[52:55], v[140:143]\n" \
"buffer_load_dwordx4 v15, s[28:31], s46 offen lds\n" \
"v_mfma_f32_16x16x32_bf16 v[144:147], a[80:83], a[56:59], v[144:147]\n" \
"ds_read_b64_tr_b16 a[32:33], v9 offset:17408\n" \
"v_mfma_f32_16x16x32_bf16 v[148:151], a[84:87], a[56:59], v[148:151]\n" \
"ds_read_b64_tr_b16 a[34:35], v10 offset:17408\n" \
"v_mfma_f32_16x16x32_bf16 v[152:155], a[80:83], a[60:63], v[152:155]\n" \
"ds_read_b64_tr_b16 a[36:37], v11 offset:17408\n" \
"v_mfma_f32_16x16x32_bf16 v[156:159], a[84:87], a[60:63], v[156:159]\n" \
"s_add_u32 m0, s50, 77824\n" \
"ds_read_b64_tr_b16 a[38:39], v12 offset:17408\n" \
"v_mfma_f32_16x16x32_bf16 v[160:163], a[88:91], a[48:51], v[160:163]\n" \
"buffer_load_dwordx4 v16, s[28:31], s46 offen lds\n" \
"v_mfma_f32_16x16x32_bf16 v[164:167], a[92:95], a[48:51], v[164:167]\n" \
"ds_read_b64_tr_b16 a[40:41], v9 offset:17920\n" \
"v_mfma_f32_16x16x32_bf16 v[168:171], a[88:91], a[52:55], v[168:171]\n" \
"ds_read_b64_tr_b16 a[42:43], v10 offset:17920\n" \
"v_mfma_f32_16x16x32_bf16 v[172:175], a[92:95], a[52:55], v[172:175]\n" \
"ds_read_b64_tr_b16 a[44:45], v11 offset:17920\n" \
"v_mfma_f32_16x16x32_bf16 v[176:179], a[88:91], a[56:59], v[176:179]\n" \
"s_mov_b32 s46, 1152\n" \
"s_add_u32 m0, s50, 81920\n" \
"ds_read_b64_tr_b16 a[46:47], v12 offset:17920\n" \
"v_mfma_f32_16x16x32_bf16 v[180:183], a[92:95], a[56:59], v[180:183]\n" \
"buffer_load_dwordx4 v17, s[28:31], s46 offen lds\n" \
"v_mfma_f32_16x16x32_bf16 v[184:187], a[88:91], a[60:63], v[184:187]\n" \
"ds_read_b64_tr_b16 a[16:17], v9 offset:16384\n" \
"v_mfma_f32_16x16x32_bf16 v[188:191], a[92:95], a[60:63], v[188:191]\n" \
"v_mfma_f32_16x16x32_bf16 v[64:67], a[64:67], a[48:51], v[64:67]\n" \
"ds_read_b64_tr_b16 a[18:19], v10 offset:16384\n" \
"v_mfma_f32_16x16x32_bf16 v[68:71], a[68:71], a[48:51], v[68:71]\n" \
"s_add_u32 m0, s50, 86016\n" \
"ds_read_b64_tr_b16 a[20:21], v11 offset:16384\n" \
"v_mfma_f32_16x16x32_bf16 v[72:75], a[64:67], a[52:55], v[72:75]\n" \
"buffer_load_dwordx4 v18, s[28:31], s46 offen lds\n" \
"v_mfma_f32_16x16x32_bf16 v[76:79], a[68:71], a[52:55], v[76:79]\n" \
"ds_read_b64_tr_b16 a[22:23], v12 offset:16384\n" \
"v_mfma_f32_16x16x32_bf16 v[80:83], a[64:67], a[56:59], v[80:83]\n" \
"buffer_store_dwordx4 v[42:45], v4, s[36:39], s97 offen offset:0 sc1\n" \
"v_mfma_f32_16x16x32_bf16 v[84:87], a[68:71], a[56:59], v[84:87]\n" \
"v_mfma_f32_16x16x32_bf16 v[88:91], a[64:67], a[60:63], v[88:91]\n" \
"v_mfma_f32_16x16x32_bf16 v[92:95], a[68:71], a[60:63], v[92:95]\n" \
"s_waitcnt lgkmcnt(0)\n" \
"v_mfma_f32_16x16x32_bf16 v[96:99], a[24:27], a[0:3], v[96:99]\n" \
"s_add_u32 m0, s50, 90112\n" \
"ds_read_b64_tr_b16 a[72:73], v9 offset:25088\n" \
"v_mfma_f32_16x16x32_bf16 v[100:103], a[28:31], a[0:3], v[100:103]\n" \
"buffer_load_dwordx4 v19, s[28:31], s46 offen lds\n" \
"v_mfma_f32_16x16x32_bf16 v[104:107], a[24:27], a[4:7], v[104:107]\n" \
"ds_read_b64_tr_b16 a[74:75], v10 offset:25088\n" \
"v_mfma_f32_16x16x32_bf16 v[108:111], a[28:31], a[4:7], v[108:111]\n" \
"ds_read_b64_tr_b16 a[76:77], v11 offset:25088\n" \
"v_mfma_f32_16x16x32_bf16 v[112:115], a[24:27], a[8:11], v[112:115]\n" \
"ds_read_b64_tr_b16 a[78:79], v12 offset:25088\n" \
"v_mfma_f32_16x16x32_bf16 v[116:119], a[28:31], a[8:11], v[116:119]\n" \
"s_add_u32 m0, s50, 94208\n" \
"ds_read_b128 a[48:51], v6 offset:0\n" \
"v_mfma_f32_16x16x32_bf16 v[120:123], a[24:27], a[12:15], v[120:123]\n" \
"buffer_load_dwordx4 v20, s[28:31], s46 offen lds\n" \
"v_mfma_f32_16x16x32_bf16 v[124:127], a[28:31], a[12:15], v[124:127]\n" \
"ds_read_b128 a[52:55], v6 offset:2048\n" \
"v_mfma_f32_16x16x32_bf16 v[128:131], a[32:35], a[0:3], v[128:131]\n" \
"v_mfma_f32_16x16x32_bf16 v[132:135], a[36:39], a[0:3], v[132:135]\n" \
"ds_read_b128 a[56:59], v6 offset:4096\n" \
"v_mfma_f32_16x16x32_bf16 v[136:139], a[32:35], a[4:7], v[136:139]\n" \
"s_mov_b32 s47, 5242880\n" \
"s_add_u32 m0, s51, 0\n" \
"ds_read_b128 a[60:63], v6 offset:6144\n" \
"v_mfma_f32_16x16x32_bf16 v[140:143], a[36:39], a[4:7], v[140:143]\n" \
"buffer_load_dwordx4 v21, s[32:35], s47 offen offset:0 lds\n" \
"v_mfma_f32_16x16x32_bf16 v[144:147], a[32:35], a[8:11], v[144:147]\n" \
"ds_read_b64_tr_b16 a[80:81], v9 offset:25600\n" \
"v_mfma_f32_16x16x32_bf16 v[148:151], a[36:39], a[8:11], v[148:151]\n" \
"ds_read_b64_tr_b16 a[82:83], v10 offset:25600\n" \
"v_mfma_f32_16x16x32_bf16 v[152:155], a[32:35], a[12:15], v[152:155]\n" \
"ds_read_b64_tr_b16 a[84:85], v11 offset:25600\n" \
"v_mfma_f32_16x16x32_bf16 v[156:159], a[36:39], a[12:15], v[156:159]\n" \
"s_add_u32 m0, s51, 896\n" \
"ds_read_b64_tr_b16 a[86:87], v12 offset:25600\n" \
"v_mfma_f32_16x16x32_bf16 v[160:163], a[40:43], a[0:3], v[160:163]\n" \
"buffer_load_dwordx4 v21, s[32:35], s47 offen offset:128 lds\n" \
"v_mfma_f32_16x16x32_bf16 v[164:167], a[44:47], a[0:3], v[164:167]\n" \
"ds_read_b64_tr_b16 a[88:89], v9 offset:26112\n" \
"v_mfma_f32_16x16x32_bf16 v[168:171], a[40:43], a[4:7], v[168:171]\n" \
"ds_read_b64_tr_b16 a[90:91], v10 offset:26112\n" \
"v_mfma_f32_16x16x32_bf16 v[172:175], a[44:47], a[4:7], v[172:175]\n" \
"ds_read_b64_tr_b16 a[92:93], v11 offset:26112\n" \
"v_mfma_f32_16x16x32_bf16 v[176:179], a[40:43], a[8:11], v[176:179]\n" \
"s_mov_b32 s47, 5505024\n" \
"s_add_u32 m0, s51, 8192\n" \
"ds_read_b64_tr_b16 a[94:95], v12 offset:26112\n" \
"v_mfma_f32_16x16x32_bf16 v[180:183], a[44:47], a[8:11], v[180:183]\n" \
"buffer_load_dwordx4 v21, s[32:35], s47 offen offset:0 lds\n" \
"v_mfma_f32_16x16x32_bf16 v[184:187], a[40:43], a[12:15], v[184:187]\n" \
"ds_read_b64_tr_b16 a[64:65], v9 offset:24576\n" \
"v_mfma_f32_16x16x32_bf16 v[188:191], a[44:47], a[12:15], v[188:191]\n" \
"v_mfma_f32_16x16x32_bf16 v[64:67], a[16:19], a[0:3], v[64:67]\n" \
"ds_read_b64_tr_b16 a[66:67], v10 offset:24576\n" \
"v_mfma_f32_16x16x32_bf16 v[68:71], a[20:23], a[0:3], v[68:71]\n" \
"s_add_u32 m0, s51, 9088\n" \
"ds_read_b64_tr_b16 a[68:69], v11 offset:24576\n" \
"v_mfma_f32_16x16x32_bf16 v[72:75], a[16:19], a[4:7], v[72:75]\n" \
"buffer_load_dwordx4 v21, s[32:35], s47 offen offset:128 lds\n" \
"v_mfma_f32_16x16x32_bf16 v[76:79], a[20:23], a[4:7], v[76:79]\n" \
"ds_read_b64_tr_b16 a[70:71], v12 offset:24576\n" \
"v_mfma_f32_16x16x32_bf16 v[80:83], a[16:19], a[8:11], v[80:83]\n" \
"v_mfma_f32_16x16x32_bf16 v[84:87], a[20:23], a[8:11], v[84:87]\n" \
"v_mfma_f32_16x16x32_bf16 v[88:91], a[16:19], a[12:15], v[88:91]\n" \
"s_waitcnt vmcnt(18) lgkmcnt(0)\n" \
"v_mfma_f32_16x16x32_bf16 v[92:95], a[20:23], a[12:15], v[92:95]\n" \
"s_barrier\n" \
"v_mfma_f32_16x16x32_bf16 v[96:99], a[72:75], a[48:51], v[96:99]\n" \
"s_mov_b32 s46, 1280\n" \
"s_add_u32 m0, s50, 0\n" \
"ds_read_b64_tr_b16 a[24:25], v9 offset:33280\n" \
"v_mfma_f32_16x16x32_bf16 v[100:103], a[76:79], a[48:51], v[100:103]\n" \
"buffer_load_dwordx4 v13, s[28:31], s46 offen lds\n" \
"v_mfma_f32_16x16x32_bf16 v[104:107], a[72:75], a[52:55], v[104:107]\n" \
"ds_read_b64_tr_b16 a[26:27], v10 offset:33280\n" \
"v_mfma_f32_16x16x32_bf16 v[108:111], a[76:79], a[52:55], v[108:111]\n" \
"ds_read_b64_tr_b16 a[28:29], v11 offset:33280\n" \
"v_mfma_f32_16x16x32_bf16 v[112:115], a[72:75], a[56:59], v[112:115]\n" \
"ds_read_b64_tr_b16 a[30:31], v12 offset:33280\n" \
"v_mfma_f32_16x16x32_bf16 v[116:119], a[76:79], a[56:59], v[116:119]\n" \
"s_add_u32 m0, s50, 4096\n" \
"ds_read_b128 a[0:3], v5 offset:32768\n" \
"v_mfma_f32_16x16x32_bf16 v[120:123], a[72:75], a[60:63], v[120:123]\n" \
"buffer_load_dwordx4 v14, s[28:31], s46 offen lds\n" \
"v_mfma_f32_16x16x32_bf16 v[124:127], a[76:79], a[60:63], v[124:127]\n" \
"ds_read_b128 a[4:7], v5 offset:34816\n" \
"v_mfma_f32_16x16x32_bf16 v[128:131], a[80:83], a[48:51], v[128:131]\n" \
"v_mfma_f32_16x16x32_bf16 v[132:135], a[84:87], a[48:51], v[132:135]\n" \
"ds_read_b128 a[8:11], v5 offset:36864\n" \
"v_mfma_f32_16x16x32_bf16 v[136:139], a[80:83], a[52:55], v[136:139]\n" \
"s_add_u32 m0, s50, 8192\n" \
"ds_read_b128 a[12:15], v5 offset:38912\n" \
"v_mfma_f32_16x16x32_bf16 v[140:143], a[84:87], a[52:55], v[140:143]\n" \
"buffer_load_dwordx4 v15, s[28:31], s46 offen lds\n" \
"v_mfma_f32_16x16x32_bf16 v[144:147], a[80:83], a[56:59], v[144:147]\n" \
"ds_read_b64_tr_b16 a[32:33], v9 offset:33792\n" \
"v_mfma_f32_16x16x32_bf16 v[148:151], a[84:87], a[56:59], v[148:151]\n" \
"ds_read_b64_tr_b16 a[34:35], v10 offset:33792\n" \
"v_mfma_f32_16x16x32_bf16 v[152:155], a[80:83], a[60:63], v[152:155]\n" \
"ds_read_b64_tr_b16 a[36:37], v11 offset:33792\n" \
"v_mfma_f32_16x16x32_bf16 v[156:159], a[84:87], a[60:63], v[156:159]\n" \
"s_add_u32 m0, s50, 12288\n" \
"ds_read_b64_tr_b16 a[38:39], v12 offset:33792\n" \
"v_mfma_f32_16x16x32_bf16 v[160:163], a[88:91], a[48:51], v[160:163]\n" \
"buffer_load_dwordx4 v16, s[28:31], s46 offen lds\n" \
"v_mfma_f32_16x16x32_bf16 v[164:167], a[92:95], a[48:51], v[164:167]\n" \
"ds_read_b64_tr_b16 a[40:41], v9 offset:34304\n" \
"v_mfma_f32_16x16x32_bf16 v[168:171], a[88:91], a[52:55], v[168:171]\n" \
"ds_read_b64_tr_b16 a[42:43], v10 offset:34304\n" \
"v_mfma_f32_16x16x32_bf16 v[172:175], a[92:95], a[52:55], v[172:175]\n" \
"ds_read_b64_tr_b16 a[44:45], v11 offset:34304\n" \
"v_mfma_f32_16x16x32_bf16 v[176:179], a[88:91], a[56:59], v[176:179]\n" \
"s_mov_b32 s46, 1280\n" \
"s_add_u32 m0, s50, 16384\n" \
"ds_read_b64_tr_b16 a[46:47], v12 offset:34304\n" \
"v_mfma_f32_16x16x32_bf16 v[180:183], a[92:95], a[56:59], v[180:183]\n" \
"buffer_load_dwordx4 v17, s[28:31], s46 offen lds\n" \
"v_mfma_f32_16x16x32_bf16 v[184:187], a[88:91], a[60:63], v[184:187]\n" \
"ds_read_b64_tr_b16 a[16:17], v9 offset:32768\n" \
"v_mfma_f32_16x16x32_bf16 v[188:191], a[92:95], a[60:63], v[188:191]\n" \
"v_mfma_f32_16x16x32_bf16 v[64:67], a[64:67], a[48:51], v[64:67]\n" \
"ds_read_b64_tr_b16 a[18:19], v10 offset:32768\n" \
"v_mfma_f32_16x16x32_bf16 v[68:71], a[68:71], a[48:51], v[68:71]\n" \
"s_add_u32 m0, s50, 20480\n" \
"ds_read_b64_tr_b16 a[20:21], v11 offset:32768\n" \
"v_mfma_f32_16x16x32_bf16 v[72:75], a[64:67], a[52:55], v[72:75]\n" \
"buffer_load_dwordx4 v18, s[28:31], s46 offen lds\n" \
"v_mfma_f32_16x16x32_bf16 v[76:79], a[68:71], a[52:55], v[76:79]\n" \
"ds_read_b64_tr_b16 a[22:23], v12 offset:32768\n" \
"v_mfma_f32_16x16x32_bf16 v[80:83], a[64:67], a[56:59], v[80:83]\n" \
"v_mfma_f32_16x16x32_bf16 v[84:87], a[68:71], a[56:59], v[84:87]\n" \
"v_mfma_f32_16x16x32_bf16 v[88:91], a[64:67], a[60:63], v[88:91]\n" \
"v_mfma_f32_16x16x32_bf16 v[92:95], a[68:71], a[60:63], v[92:95]\n" \
"s_waitcnt lgkmcnt(0)\n" \
"v_mfma_f32_16x16x32_bf16 v[96:99], a[24:27], a[0:3], v[96:99]\n" \
"s_add_u32 m0, s50, 24576\n" \
"ds_read_b64_tr_b16 a[72:73], v9 offset:41472\n" \
"v_mfma_f32_16x16x32_bf16 v[100:103], a[28:31], a[0:3], v[100:103]\n" \
"buffer_load_dwordx4 v19, s[28:31], s46 offen lds\n" \
"v_mfma_f32_16x16x32_bf16 v[104:107], a[24:27], a[4:7], v[104:107]\n" \
"ds_read_b64_tr_b16 a[74:75], v10 offset:41472\n" \
"v_mfma_f32_16x16x32_bf16 v[108:111], a[28:31], a[4:7], v[108:111]\n" \
"ds_read_b64_tr_b16 a[76:77], v11 offset:41472\n" \
"v_mfma_f32_16x16x32_bf16 v[112:115], a[24:27], a[8:11], v[112:115]\n" \
"ds_read_b64_tr_b16 a[78:79], v12 offset:41472\n" \
"v_mfma_f32_16x16x32_bf16 v[116:119], a[28:31], a[8:11], v[116:119]\n" \
"s_add_u32 m0, s50, 28672\n" \
"ds_read_b128 a[48:51], v6 offset:32768\n" \
"v_mfma_f32_16x16x32_bf16 v[120:123], a[24:27], a[12:15], v[120:123]\n" \
"buffer_load_dwordx4 v20, s[28:31], s46 offen lds\n" \
"v_mfma_f32_16x16x32_bf16 v[124:127], a[28:31], a[12:15], v[124:127]\n" \
"ds_read_b128 a[52:55], v6 offset:34816\n" \
"v_mfma_f32_16x16x32_bf16 v[128:131], a[32:35], a[0:3], v[128:131]\n" \
"v_mfma_f32_16x16x32_bf16 v[132:135], a[36:39], a[0:3], v[132:135]\n" \
"ds_read_b128 a[56:59], v6 offset:36864\n" \
"v_mfma_f32_16x16x32_bf16 v[136:139], a[32:35], a[4:7], v[136:139]\n" \
"s_mov_b32 s47, 5767168\n" \
"s_add_u32 m0, s51, 16384\n" \
"ds_read_b128 a[60:63], v6 offset:38912\n" \
"v_mfma_f32_16x16x32_bf16 v[140:143], a[36:39], a[4:7], v[140:143]\n" \
"buffer_load_dwordx4 v21, s[32:35], s47 offen offset:0 lds\n" \
"v_mfma_f32_16x16x32_bf16 v[144:147], a[32:35], a[8:11], v[144:147]\n" \
"ds_read_b64_tr_b16 a[80:81], v9 offset:41984\n" \
"v_mfma_f32_16x16x32_bf16 v[148:151], a[36:39], a[8:11], v[148:151]\n" \
"ds_read_b64_tr_b16 a[82:83], v10 offset:41984\n" \
"v_mfma_f32_16x16x32_bf16 v[152:155], a[32:35], a[12:15], v[152:155]\n" \
"ds_read_b64_tr_b16 a[84:85], v11 offset:41984\n" \
"v_mfma_f32_16x16x32_bf16 v[156:159], a[36:39], a[12:15], v[156:159]\n" \
"s_add_u32 m0, s51, 17280\n" \
"ds_read_b64_tr_b16 a[86:87], v12 offset:41984\n" \
"v_mfma_f32_16x16x32_bf16 v[160:163], a[40:43], a[0:3], v[160:163]\n" \
"buffer_load_dwordx4 v21, s[32:35], s47 offen offset:128 lds\n" \
"v_mfma_f32_16x16x32_bf16 v[164:167], a[44:47], a[0:3], v[164:167]\n" \
"ds_read_b64_tr_b16 a[88:89], v9 offset:42496\n" \
"v_mfma_f32_16x16x32_bf16 v[168:171], a[40:43], a[4:7], v[168:171]\n" \
"ds_read_b64_tr_b16 a[90:91], v10 offset:42496\n" \
"v_mfma_f32_16x16x32_bf16 v[172:175], a[44:47], a[4:7], v[172:175]\n" \
"ds_read_b64_tr_b16 a[92:93], v11 offset:42496\n" \
"v_mfma_f32_16x16x32_bf16 v[176:179], a[40:43], a[8:11], v[176:179]\n" \
"s_mov_b32 s47, 6029312\n" \
"s_add_u32 m0, s51, 24576\n" \
"ds_read_b64_tr_b16 a[94:95], v12 offset:42496\n" \
"v_mfma_f32_16x16x32_bf16 v[180:183], a[44:47], a[8:11], v[180:183]\n" \
"buffer_load_dwordx4 v21, s[32:35], s47 offen offset:0 lds\n" \
"v_mfma_f32_16x16x32_bf16 v[184:187], a[40:43], a[12:15], v[184:187]\n" \
"ds_read_b64_tr_b16 a[64:65], v9 offset:40960\n" \
"v_mfma_f32_16x16x32_bf16 v[188:191], a[44:47], a[12:15], v[188:191]\n" \
"v_mfma_f32_16x16x32_bf16 v[64:67], a[16:19], a[0:3], v[64:67]\n" \
"ds_read_b64_tr_b16 a[66:67], v10 offset:40960\n" \
"v_mfma_f32_16x16x32_bf16 v[68:71], a[20:23], a[0:3], v[68:71]\n" \
"s_add_u32 m0, s51, 25472\n" \
"ds_read_b64_tr_b16 a[68:69], v11 offset:40960\n" \
"v_mfma_f32_16x16x32_bf16 v[72:75], a[16:19], a[4:7], v[72:75]\n" \
"buffer_load_dwordx4 v21, s[32:35], s47 offen offset:128 lds\n" \
"v_mfma_f32_16x16x32_bf16 v[76:79], a[20:23], a[4:7], v[76:79]\n" \
"ds_read_b64_tr_b16 a[70:71], v12 offset:40960\n" \
"v_mfma_f32_16x16x32_bf16 v[80:83], a[16:19], a[8:11], v[80:83]\n" \
"v_mfma_f32_16x16x32_bf16 v[84:87], a[20:23], a[8:11], v[84:87]\n" \
"v_mfma_f32_16x16x32_bf16 v[88:91], a[16:19], a[12:15], v[88:91]\n" \
"s_waitcnt vmcnt(16) lgkmcnt(0)\n" \
"v_mfma_f32_16x16x32_bf16 v[92:95], a[20:23], a[12:15], v[92:95]\n" \
"s_barrier\n" \
"v_mfma_f32_16x16x32_bf16 v[96:99], a[72:75], a[48:51], v[96:99]\n" \
"s_mov_b32 s46, 1408\n" \
"s_add_u32 m0, s50, 32768\n" \
"ds_read_b64_tr_b16 a[24:25], v9 offset:49664\n" \
"v_mfma_f32_16x16x32_bf16 v[100:103], a[76:79], a[48:51], v[100:103]\n" \
"buffer_load_dwordx4 v13, s[28:31], s46 offen lds\n" \
"v_mfma_f32_16x16x32_bf16 v[104:107], a[72:75], a[52:55], v[104:107]\n" \
"ds_read_b64_tr_b16 a[26:27], v10 offset:49664\n" \
"v_mfma_f32_16x16x32_bf16 v[108:111], a[76:79], a[52:55], v[108:111]\n" \
"ds_read_b64_tr_b16 a[28:29], v11 offset:49664\n" \
"v_mfma_f32_16x16x32_bf16 v[112:115], a[72:75], a[56:59], v[112:115]\n" \
"ds_read_b64_tr_b16 a[30:31], v12 offset:49664\n" \
"v_mfma_f32_16x16x32_bf16 v[116:119], a[76:79], a[56:59], v[116:119]\n" \
"s_add_u32 m0, s50, 36864\n" \
"ds_read_b128 a[0:3], v7 offset:0\n" \
"v_mfma_f32_16x16x32_bf16 v[120:123], a[72:75], a[60:63], v[120:123]\n" \
"buffer_load_dwordx4 v14, s[28:31], s46 offen lds\n" \
"v_mfma_f32_16x16x32_bf16 v[124:127], a[76:79], a[60:63], v[124:127]\n" \
"ds_read_b128 a[4:7], v7 offset:2048\n" \
"v_mfma_f32_16x16x32_bf16 v[128:131], a[80:83], a[48:51], v[128:131]\n" \
"v_mfma_f32_16x16x32_bf16 v[132:135], a[84:87], a[48:51], v[132:135]\n" \
"ds_read_b128 a[8:11], v7 offset:4096\n" \
"v_mfma_f32_16x16x32_bf16 v[136:139], a[80:83], a[52:55], v[136:139]\n" \
"s_add_u32 m0, s50, 40960\n" \
"ds_read_b128 a[12:15], v7 offset:6144\n" \
"v_mfma_f32_16x16x32_bf16 v[140:143], a[84:87], a[52:55], v[140:143]\n" \
"buffer_load_dwordx4 v15, s[28:31], s46 offen lds\n" \
"v_mfma_f32_16x16x32_bf16 v[144:147], a[80:83], a[56:59], v[144:147]\n" \
"ds_read_b64_tr_b16 a[32:33], v9 offset:50176\n" \
"v_mfma_f32_16x16x32_bf16 v[148:151], a[84:87], a[56:59], v[148:151]\n" \
"ds_read_b64_tr_b16 a[34:35], v10 offset:50176\n" \
"v_mfma_f32_16x16x32_bf16 v[152:155], a[80:83], a[60:63], v[152:155]\n" \
"ds_read_b64_tr_b16 a[36:37], v11 offset:50176\n" \
"v_mfma_f32_16x16x32_bf16 v[156:159], a[84:87], a[60:63], v[156:159]\n" \
"s_add_u32 m0, s50, 45056\n" \
"ds_read_b64_tr_b16 a[38:39], v12 offset:50176\n" \
"v_mfma_f32_16x16x32_bf16 v[160:163], a[88:91], a[48:51], v[160:163]\n" \
"LH12_%=:\ns_cbranch_execz LHR0_%=\n" \
"buffer_load_dwordx4 v16, s[28:31], s46 offen lds\n" \
"v_mfma_f32_16x16x32_bf16 v[164:167], a[92:95], a[48:51], v[164:167]\n" \
"ds_read_b64_tr_b16 a[40:41], v9 offset:50688\n" \
"v_mfma_f32_16x16x32_bf16 v[168:171], a[88:91], a[52:55], v[168:171]\n" \
"ds_read_b64_tr_b16 a[42:43], v10 offset:50688\n" \
"v_mfma_f32_16x16x32_bf16 v[172:175], a[92:95], a[52:55], v[172:175]\n" \
"ds_read_b64_tr_b16 a[44:45], v11 offset:50688\n" \
"v_mfma_f32_16x16x32_bf16 v[176:179], a[88:91], a[56:59], v[176:179]\n" \
"s_mov_b32 s46, 1408\n" \
"s_add_u32 m0, s50, 49152\n" \
"ds_read_b64_tr_b16 a[46:47], v12 offset:50688\n" \
"v_mfma_f32_16x16x32_bf16 v[180:183], a[92:95], a[56:59], v[180:183]\n" \
"buffer_load_dwordx4 v17, s[28:31], s46 offen lds\n" \
"v_mfma_f32_16x16x32_bf16 v[184:187], a[88:91], a[60:63], v[184:187]\n" \
"ds_read_b64_tr_b16 a[16:17], v9 offset:49152\n" \
"v_mfma_f32_16x16x32_bf16 v[188:191], a[92:95], a[60:63], v[188:191]\n" \
"v_mfma_f32_16x16x32_bf16 v[64:67], a[64:67], a[48:51], v[64:67]\n" \
"ds_read_b64_tr_b16 a[18:19], v10 offset:49152\n" \
"v_mfma_f32_16x16x32_bf16 v[68:71], a[68:71], a[48:51], v[68:71]\n" \
"s_add_u32 m0, s50, 53248\n" \
"ds_read_b64_tr_b16 a[20:21], v11 offset:49152\n" \
"v_mfma_f32_16x16x32_bf16 v[72:75], a[64:67], a[52:55], v[72:75]\n" \
"buffer_load_dwordx4 v18, s[28:31], s46 offen lds\n" \
"v_mfma_f32_16x16x32_bf16 v[76:79], a[68:71], a[52:55], v[76:79]\n" \
"ds_read_b64_tr_b16 a[22:23], v12 offset:49152\n" \
"v_mfma_f32_16x16x32_bf16 v[80:83], a[64:67], a[56:59], v[80:83]\n" \
"v_mfma_f32_16x16x32_bf16 v[84:87], a[68:71], a[56:59], v[84:87]\n" \
"v_mfma_f32_16x16x32_bf16 v[88:91], a[64:67], a[60:63], v[88:91]\n" \
"v_mfma_f32_16x16x32_bf16 v[92:95], a[68:71], a[60:63], v[92:95]\n" \
"s_waitcnt lgkmcnt(0)\n" \
"v_mfma_f32_16x16x32_bf16 v[96:99], a[24:27], a[0:3], v[96:99]\n" \
"s_add_u32 m0, s50, 57344\n" \
"ds_read_b64_tr_b16 a[72:73], v9 offset:57856\n" \
"s_bfe_u32 s89, %1, 0x20001\n" \
"v_mfma_f32_16x16x32_bf16 v[100:103], a[28:31], a[0:3], v[100:103]\n" \
"buffer_load_dwordx4 v19, s[28:31], s46 offen lds\n" \
"v_mfma_f32_16x16x32_bf16 v[104:107], a[24:27], a[4:7], v[104:107]\n" \
"s_and_b32 s90, %1, 1\n" \
"ds_read_b64_tr_b16 a[74:75], v10 offset:57856\n" \
"s_lshl_b32 s90, s90, 2\n" \
"v_mfma_f32_16x16x32_bf16 v[108:111], a[28:31], a[4:7], v[108:111]\n" \
"s_bfe_u32 s92, %1, 0x20003\n" \
"ds_read_b64_tr_b16 a[76:77], v11 offset:57856\n" \
"s_add_u32 s90, s90, s92\n" \
"v_mfma_f32_16x16x32_bf16 v[112:115], a[24:27], a[8:11], v[112:115]\n" \
"s_lshl_b32 s92, %1, 2\n" \
"ds_read_b64_tr_b16 a[78:79], v12 offset:57856\n" \
"s_add_u32 s92, s92, %2\n" \
"v_mfma_f32_16x16x32_bf16 v[116:119], a[28:31], a[8:11], v[116:119]\n" \
"s_add_u32 m0, s50, 61440\n" \
"ds_read_b128 a[48:51], v8 offset:0\n" \
"s_mul_i32 s93, s92, 12288\n" \
"v_mfma_f32_16x16x32_bf16 v[120:123], a[24:27], a[12:15], v[120:123]\n" \
"buffer_load_dwordx4 v20, s[28:31], s46 offen lds\n" \
"v_mfma_f32_16x16x32_bf16 v[124:127], a[28:31], a[12:15], v[124:127]\n" \
"s_add_u32 s78, s93, 109051904\n" \
"ds_read_b128 a[52:55], v8 offset:2048\n" \
"s_lshl_b32 s88, s92, 3\n" \
"v_mfma_f32_16x16x32_bf16 v[128:131], a[32:35], a[0:3], v[128:131]\n" \
"s_mov_b64 exec, 0xffff\nglobal_load_dword v1, v2, s[100:101]\ns_mov_b64 exec, -1\n" \
"v_mfma_f32_16x16x32_bf16 v[132:135], a[36:39], a[0:3], v[132:135]\n" \
"s_add_u32 s88, s88, 94404608\n" \
"ds_read_b128 a[56:59], v8 offset:4096\n" \
"v_mfma_f32_16x16x32_bf16 v[136:139], a[32:35], a[4:7], v[136:139]\n" \
"s_mov_b32 s47, 6291456\n" \
"s_add_u32 m0, s51, 32768\n" \
"ds_read_b128 a[60:63], v8 offset:6144\n" \
"v_mfma_f32_16x16x32_bf16 v[140:143], a[36:39], a[4:7], v[140:143]\n" \
"buffer_load_dwordx4 v21, s[32:35], s47 offen offset:0 lds\n" \
"v_mfma_f32_16x16x32_bf16 v[144:147], a[32:35], a[8:11], v[144:147]\n" \
"s_cmp_eq_u32 s92, 0\ns_cselect_b32 s12, 94371840, -1\n" \
"ds_read_b64_tr_b16 a[80:81], v9 offset:58368\n" \
"v_mfma_f32_16x16x32_bf16 v[148:151], a[36:39], a[8:11], v[148:151]\n" \
"s_mul_i32 s80, s90, 12\n" \
"ds_read_b64_tr_b16 a[82:83], v10 offset:58368\n" \
"s_add_u32 s80, s80, s89\n" \
"v_mfma_f32_16x16x32_bf16 v[152:155], a[32:35], a[12:15], v[152:155]\n" \
"s_lshl_b32 s80, s80, 16\n" \
"ds_read_b64_tr_b16 a[84:85], v11 offset:58368\n" \
"s_add_u32 s92, %2, 0\n" \
"v_mfma_f32_16x16x32_bf16 v[156:159], a[36:39], a[12:15], v[156:159]\n" \
"s_add_u32 m0, s51, 33664\n" \
"ds_read_b64_tr_b16 a[86:87], v12 offset:58368\n" \
"s_mul_i32 s93, s92, 43\n" \
"v_mfma_f32_16x16x32_bf16 v[160:163], a[40:43], a[0:3], v[160:163]\n" \
"buffer_load_dwordx4 v21, s[32:35], s47 offen offset:128 lds\n" \
"v_mfma_f32_16x16x32_bf16 v[164:167], a[44:47], a[0:3], v[164:167]\n" \
"s_lshr_b32 s93, s93, 7\n" \
"ds_read_b64_tr_b16 a[88:89], v9 offset:58880\n" \
"s_lshl_b32 s93, s93, 12\n" \
"v_mfma_f32_16x16x32_bf16 v[168:171], a[40:43], a[4:7], v[168:171]\n" \
"s_add_u32 s64, s80, s93\n" \
"ds_read_b64_tr_b16 a[90:91], v10 offset:58880\n" \
"s_add_u32 s92, s92, s89\n" \
"v_mfma_f32_16x16x32_bf16 v[172:175], a[44:47], a[4:7], v[172:175]\n" \
"s_add_u32 s92, s92, 1\n" \
"ds_read_b64_tr_b16 a[92:93], v11 offset:58880\n" \
"s_mul_i32 s93, s92, 43\n" \
"v_mfma_f32_16x16x32_bf16 v[176:179], a[40:43], a[8:11], v[176:179]\n" \
"s_mov_b32 s47, 6553600\n" \
"s_add_u32 m0, s51, 40960\n" \
"ds_read_b64_tr_b16 a[94:95], v12 offset:58880\n" \
"v_mfma_f32_16x16x32_bf16 v[180:183], a[44:47], a[8:11], v[180:183]\n" \
"buffer_load_dwordx4 v21, s[32:35], s47 offen offset:0 lds\n" \
"v_mfma_f32_16x16x32_bf16 v[184:187], a[40:43], a[12:15], v[184:187]\n" \
"s_lshr_b32 s93, s93, 7\n" \
"ds_read_b64_tr_b16 a[64:65], v9 offset:57344\n" \
"s_mul_i32 s93, s93, 3\n" \
"v_mfma_f32_16x16x32_bf16 v[188:191], a[44:47], a[12:15], v[188:191]\n" \
"s_sub_u32 s93, s92, s93\n" \
"s_lshl_b32 s93, s93, 18\n" \
"s_add_u32 s64, s64, s93\n" \
"v_mfma_f32_16x16x32_bf16 v[64:67], a[16:19], a[0:3], v[64:67]\n" \
"s_add_u32 s92, %2, 4\n" \
"ds_read_b64_tr_b16 a[66:67], v10 offset:57344\n" \
"s_mul_i32 s93, s92, 43\n" \
"v_mfma_f32_16x16x32_bf16 v[68:71], a[20:23], a[0:3], v[68:71]\n" \
"s_add_u32 m0, s51, 41856\n" \
"ds_read_b64_tr_b16 a[68:69], v11 offset:57344\n" \
"s_lshr_b32 s93, s93, 7\n" \
"v_mfma_f32_16x16x32_bf16 v[72:75], a[16:19], a[4:7], v[72:75]\n" \
"buffer_load_dwordx4 v21, s[32:35], s47 offen offset:128 lds\n" \
"v_mfma_f32_16x16x32_bf16 v[76:79], a[20:23], a[4:7], v[76:79]\n" \
"s_lshl_b32 s93, s93, 12\n" \
"ds_read_b64_tr_b16 a[70:71], v12 offset:57344\n" \
"s_add_u32 s65, s80, s93\n" \
"v_mfma_f32_16x16x32_bf16 v[80:83], a[16:19], a[8:11], v[80:83]\n" \
"s_add_u32 s92, s92, s89\n" \
"s_add_u32 s92, s92, 1\n" \
"s_mul_i32 s93, s92, 43\n" \
"v_mfma_f32_16x16x32_bf16 v[84:87], a[20:23], a[8:11], v[84:87]\n" \
"s_lshr_b32 s93, s93, 7\n" \
"s_mul_i32 s93, s93, 3\n" \
"s_sub_u32 s93, s92, s93\n" \
"v_mfma_f32_16x16x32_bf16 v[88:91], a[16:19], a[12:15], v[88:91]\n" \
"s_waitcnt vmcnt(17) lgkmcnt(0)\n" \
"s_lshl_b32 s93, s93, 18\n" \
"s_add_u32 s65, s65, s93\n" \
"v_mfma_f32_16x16x32_bf16 v[92:95], a[20:23], a[12:15], v[92:95]\n" \
"s_barrier\n" \
"s_add_u32 s92, %2, 8\n" \
"s_mul_i32 s93, s92, 43\n" \
"v_mfma_f32_16x16x32_bf16 v[96:99], a[72:75], a[48:51], v[96:99]\n" \
"s_mov_b32 s46, 1536\n" \
"s_add_u32 m0, s50, 65536\n" \
"ds_read_b64_tr_b16 a[24:25], v9 offset:512\n" \
"v_mfma_f32_16x16x32_bf16 v[100:103], a[76:79], a[48:51], v[100:103]\n" \
"buffer_load_dwordx4 v13, s[28:31], s46 offen lds\n" \
"v_mfma_f32_16x16x32_bf16 v[104:107], a[72:75], a[52:55], v[104:107]\n" \
"s_lshr_b32 s93, s93, 7\n" \
"ds_read_b64_tr_b16 a[26:27], v10 offset:512\n" \
"v_mov_b32 v58, s14\n" \
"v_mfma_f32_16x16x32_bf16 v[108:111], a[76:79], a[52:55], v[108:111]\n" \
"s_lshl_b32 s93, s93, 12\n" \
"ds_read_b64_tr_b16 a[28:29], v11 offset:512\n" \
"v_mov_b32 v59, s15\n" \
"v_mfma_f32_16x16x32_bf16 v[112:115], a[72:75], a[56:59], v[112:115]\n" \
"s_add_u32 s66, s80, s93\n" \
"ds_read_b64_tr_b16 a[30:31], v12 offset:512\n" \
"s_add_u32 s92, s92, s89\n" \
"v_mfma_f32_16x16x32_bf16 v[116:119], a[76:79], a[56:59], v[116:119]\n" \
"s_add_u32 m0, s50, 69632\n" \
"ds_read_b128 a[0:3], v5 offset:0\n" \
"s_add_u32 s92, s92, 1\n" \
"v_mfma_f32_16x16x32_bf16 v[120:123], a[72:75], a[60:63], v[120:123]\n" \
"buffer_load_dwordx4 v14, s[28:31], s46 offen lds\n" \
"v_mfma_f32_16x16x32_bf16 v[124:127], a[76:79], a[60:63], v[124:127]\n" \
"s_mul_i32 s93, s92, 43\n" \
"ds_read_b128 a[4:7], v5 offset:2048\n" \
"s_lshr_b32 s93, s93, 7\n" \
"v_mfma_f32_16x16x32_bf16 v[128:131], a[80:83], a[48:51], v[128:131]\n" \
"s_mul_i32 s93, s93, 3\n" \
"s_sub_u32 s93, s92, s93\n" \
"s_lshl_b32 s93, s93, 18\n" \
"v_mfma_f32_16x16x32_bf16 v[132:135], a[84:87], a[48:51], v[132:135]\n" \
"s_add_u32 s66, s66, s93\n" \
"ds_read_b128 a[8:11], v5 offset:4096\n" \
"s_and_b32 s92, %2, 1\n" \
"v_mfma_f32_16x16x32_bf16 v[136:139], a[80:83], a[52:55], v[136:139]\n" \
"s_add_u32 m0, s50, 73728\n" \
"ds_read_b128 a[12:15], v5 offset:6144\n" \
"s_lshl_b32 s92, s92, 12\n" \
"v_mfma_f32_16x16x32_bf16 v[140:143], a[84:87], a[52:55], v[140:143]\n" \
"buffer_load_dwordx4 v15, s[28:31], s46 offen lds\n" \
"v_mfma_f32_16x16x32_bf16 v[144:147], a[80:83], a[56:59], v[144:147]\n" \
"s_sub_u32 s57, s66, s64\n" \
"ds_read_b64_tr_b16 a[32:33], v9 offset:1024\n" \
"s_sub_u32 s56, s64, s57\n" \
"v_mfma_f32_16x16x32_bf16 v[148:151], a[84:87], a[56:59], v[148:151]\n" \
"s_sub_u32 s56, s56, s92\n" \
"ds_read_b64_tr_b16 a[34:35], v10 offset:1024\n" \
"s_sub_u32 s59, s64, s65\n" \
"v_mfma_f32_16x16x32_bf16 v[152:155], a[80:83], a[60:63], v[152:155]\n" \
"s_add_u32 s59, s59, 16384\n" \
"ds_read_b64_tr_b16 a[36:37], v11 offset:1024\n" \
"s_sub_u32 s58, s65, s59\n" \
"v_mfma_f32_16x16x32_bf16 v[156:159], a[84:87], a[60:63], v[156:159]\n" \
"s_add_u32 m0, s50, 77824\n" \
"ds_read_b64_tr_b16 a[38:39], v12 offset:1024\n" \
"s_sub_u32 s58, s58, s92\n" \
"v_mfma_f32_16x16x32_bf16 v[160:163], a[88:91], a[48:51], v[160:163]\n" \
"buffer_load_dwordx4 v16, s[28:31], s46 offen lds\n" \
"v_mfma_f32_16x16x32_bf16 v[164:167], a[92:95], a[48:51], v[164:167]\n" \
"s_lshl_b32 s92, s90, 18\n" \
"ds_read_b64_tr_b16 a[40:41], v9 offset:1536\n" \
"s_lshl_b32 s93, s89, 16\n" \
"v_mfma_f32_16x16x32_bf16 v[168:171], a[88:91], a[52:55], v[168:171]\n" \
"s_add_u32 s67, s92, s93\n" \
"ds_read_b64_tr_b16 a[42:43], v10 offset:1536\n" \
"s_add_u32 s67, s67, 6291456\n" \
"v_mfma_f32_16x16x32_bf16 v[172:175], a[92:95], a[52:55], v[172:175]\n" \
"s_lshl_b32 s92, s90, 20\n" \
"ds_read_b64_tr_b16 a[44:45], v11 offset:1536\n" \
"s_add_u32 s94, s92, s93\n" \
"v_mfma_f32_16x16x32_bf16 v[176:179], a[88:91], a[56:59], v[176:179]\n" \
"s_mov_b32 s46, 1536\n" \
"s_add_u32 m0, s50, 81920\n" \
"ds_read_b64_tr_b16 a[46:47], v12 offset:1536\n" \
"v_mfma_f32_16x16x32_bf16 v[180:183], a[92:95], a[56:59], v[180:183]\n" \
"buffer_load_dwordx4 v17, s[28:31], s46 offen lds\n" \
"v_mfma_f32_16x16x32_bf16 v[184:187], a[88:91], a[60:63], v[184:187]\n" \
"s_add_u32 s94, s94, 8388608\n" \
"ds_read_b64_tr_b16 a[16:17], v9 offset:0\n" \
"s_add_u32 s95, s89, 1\n" \
"v_mfma_f32_16x16x32_bf16 v[188:191], a[92:95], a[60:63], v[188:191]\n" \
"buffer_store_dwordx2 v[58:59], v56, s[36:39], s24 offen sc1\n" \
"v_mfma_f32_16x16x32_bf16 v[64:67], a[64:67], a[48:51], v[64:67]\n" \
"s_and_b32 s95, s95, 3\n" \
"ds_read_b64_tr_b16 a[18:19], v10 offset:0\n" \
"s_lshl_b32 s92, s95, 18\n" \
"v_mfma_f32_16x16x32_bf16 v[68:71], a[68:71], a[48:51], v[68:71]\n" \
"s_add_u32 m0, s50, 86016\n" \
"ds_read_b64_tr_b16 a[20:21], v11 offset:0\n" \
"s_add_u32 s68, s94, s92\n" \
"v_mfma_f32_16x16x32_bf16 v[72:75], a[64:67], a[52:55], v[72:75]\n" \
"buffer_load_dwordx4 v18, s[28:31], s46 offen lds\n" \
"v_mfma_f32_16x16x32_bf16 v[76:79], a[68:71], a[52:55], v[76:79]\n" \
"s_sub_u32 s92, s95, s89\n" \
"ds_read_b64_tr_b16 a[22:23], v12 offset:0\n" \
"s_mul_i32 s93, s92, 98304\n" \
"v_mfma_f32_16x16x32_bf16 v[80:83], a[64:67], a[56:59], v[80:83]\n" \
"s_add_u32 s75, s78, s93\n" \
"s_add_u32 s75, s75, 6144\n" \
"s_add_u32 s95, s89, 2\n" \
"v_mfma_f32_16x16x32_bf16 v[84:87], a[68:71], a[56:59], v[84:87]\n" \
"s_and_b32 s95, s95, 3\n" \
"s_lshl_b32 s92, s95, 18\n" \
"s_add_u32 s69, s94, s92\n" \
"v_mfma_f32_16x16x32_bf16 v[88:91], a[64:67], a[60:63], v[88:91]\n" \
"s_sub_u32 s92, s95, s89\n" \
"s_mul_i32 s93, s92, 98304\n" \
"s_add_u32 s76, s78, s93\n" \
"v_mfma_f32_16x16x32_bf16 v[92:95], a[68:71], a[60:63], v[92:95]\n" \
"s_waitcnt lgkmcnt(0)\n" \
"s_add_u32 s76, s76, 8192\n" \
"s_add_u32 s95, s89, 3\n" \
"v_mfma_f32_16x16x32_bf16 v[96:99], a[24:27], a[0:3], v[96:99]\n" \
"s_add_u32 m0, s50, 90112\n" \
"ds_read_b64_tr_b16 a[72:73], v9 offset:8704\n" \
"s_and_b32 s95, s95, 3\n" \
"v_mfma_f32_16x16x32_bf16 v[100:103], a[28:31], a[0:3], v[100:103]\n" \
"buffer_load_dwordx4 v19, s[28:31], s46 offen lds\n" \
"v_mfma_f32_16x16x32_bf16 v[104:107], a[24:27], a[4:7], v[104:107]\n" \
"s_lshl_b32 s92, s95, 18\n" \
"ds_read_b64_tr_b16 a[74:75], v10 offset:8704\n" \
"s_add_u32 s70, s94, s92\n" \
"v_mfma_f32_16x16x32_bf16 v[108:111], a[28:31], a[4:7], v[108:111]\n" \
"s_sub_u32 s92, s95, s89\n" \
"ds_read_b64_tr_b16 a[76:77], v11 offset:8704\n" \
"s_mul_i32 s93, s92, 98304\n" \
"v_mfma_f32_16x16x32_bf16 v[112:115], a[24:27], a[8:11], v[112:115]\n" \
"s_add_u32 s77, s78, s93\n" \
"ds_read_b64_tr_b16 a[78:79], v12 offset:8704\n" \
"s_add_u32 s77, s77, 10240\n" \
"v_mfma_f32_16x16x32_bf16 v[116:119], a[28:31], a[8:11], v[116:119]\n" \
"s_add_u32 m0, s50, 94208\n" \
"ds_read_b128 a[48:51], v6 offset:0\n" \
"s_add_u32 s95, s89, 4\n" \
"v_mfma_f32_16x16x32_bf16 v[120:123], a[24:27], a[12:15], v[120:123]\n" \
"buffer_load_dwordx4 v20, s[28:31], s46 offen lds\n" \
"v_mfma_f32_16x16x32_bf16 v[124:127], a[28:31], a[12:15], v[124:127]\n" \
"s_and_b32 s95, s95, 3\n" \
"ds_read_b128 a[52:55], v6 offset:2048\n" \
"s_lshl_b32 s92, s95, 18\n" \
"v_mfma_f32_16x16x32_bf16 v[128:131], a[32:35], a[0:3], v[128:131]\n" \
"s_add_u32 s71, s94, s92\n" \
"s_add_u32 s92, s89, 1\n" \
"s_mul_i32 s95, s92, 43\n" \
"v_mfma_f32_16x16x32_bf16 v[132:135], a[36:39], a[0:3], v[132:135]\n" \
"s_lshr_b32 s95, s95, 7\n" \
"ds_read_b128 a[56:59], v6 offset:4096\n" \
"s_mul_i32 s95, s95, 3\n" \
"v_mfma_f32_16x16x32_bf16 v[136:139], a[32:35], a[4:7], v[136:139]\n" \
"s_mov_b32 s47, 6815744\n" \
"s_add_u32 m0, s51, 49152\n" \
"ds_read_b128 a[60:63], v6 offset:6144\n" \
"v_mfma_f32_16x16x32_bf16 v[140:143], a[36:39], a[4:7], v[140:143]\n" \
"buffer_load_dwordx4 v21, s[32:35], s47 offen offset:0 lds\n" \
"v_mfma_f32_16x16x32_bf16 v[144:147], a[32:35], a[8:11], v[144:147]\n" \
"s_sub_u32 s95, s92, s95\n" \
"ds_read_b64_tr_b16 a[80:81], v9 offset:9216\n" \
"s_sub_u32 s92, s95, s89\n" \
"v_mfma_f32_16x16x32_bf16 v[148:151], a[36:39], a[8:11], v[148:151]\n" \
"s_mul_i32 s93, s92, 98304\n" \
"ds_read_b64_tr_b16 a[82:83], v10 offset:9216\n" \
"s_add_u32 s72, s78, s93\n" \
"v_mfma_f32_16x16x32_bf16 v[152:155], a[32:35], a[12:15], v[152:155]\n" \
"s_sub_u32 s92, s92, 1\n" \
"ds_read_b64_tr_b16 a[84:85], v11 offset:9216\n" \
"s_and_b32 s92, s92, 3\n" \
"v_mfma_f32_16x16x32_bf16 v[156:159], a[36:39], a[12:15], v[156:159]\n" \
"s_add_u32 m0, s51, 50048\n" \
"ds_read_b64_tr_b16 a[86:87], v12 offset:9216\n" \
"s_lshl_b32 s92, s92, 11\n" \
"v_mfma_f32_16x16x32_bf16 v[160:163], a[40:43], a[0:3], v[160:163]\n" \
"buffer_load_dwordx4 v21, s[32:35], s47 offen offset:128 lds\n" \
"v_mfma_f32_16x16x32_bf16 v[164:167], a[44:47], a[0:3], v[164:167]\n" \
"s_add_u32 s72, s72, s92\n" \
"ds_read_b64_tr_b16 a[88:89], v9 offset:9728\n" \
"s_add_u32 s92, s89, 2\n" \
"v_mfma_f32_16x16x32_bf16 v[168:171], a[40:43], a[4:7], v[168:171]\n" \
"s_mul_i32 s95, s92, 43\n" \
"ds_read_b64_tr_b16 a[90:91], v10 offset:9728\n" \
"s_lshr_b32 s95, s95, 7\n" \
"v_mfma_f32_16x16x32_bf16 v[172:175], a[44:47], a[4:7], v[172:175]\n" \
"s_mul_i32 s95, s95, 3\n" \
"ds_read_b64_tr_b16 a[92:93], v11 offset:9728\n" \
"s_sub_u32 s95, s92, s95\n" \
"v_mfma_f32_16x16x32_bf16 v[176:179], a[40:43], a[8:11], v[176:179]\n" \
"s_mov_b32 s47, 7077888\n" \
"s_add_u32 m0, s51, 57344\n" \
"ds_read_b64_tr_b16 a[94:95], v12 offset:9728\n" \
"v_mfma_f32_16x16x32_bf16 v[180:183], a[44:47], a[8:11], v[180:183]\n" \
"buffer_load_dwordx4 v21, s[32:35], s47 offen offset:0 lds\n" \
"v_mfma_f32_16x16x32_bf16 v[184:187], a[40:43], a[12:15], v[184:187]\n" \
"s_sub_u32 s92, s95, s89\n" \
"ds_read_b64_tr_b16 a[64:65], v9 offset:8192\n" \
"s_mul_i32 s93, s92, 98304\n" \
"v_mfma_f32_16x16x32_bf16 v[188:191], a[44:47], a[12:15], v[188:191]\n" \
"s_add_u32 s73, s78, s93\n" \
"s_sub_u32 s92, s92, 1\n" \
"s_and_b32 s92, s92, 3\n" \
"v_mfma_f32_16x16x32_bf16 v[64:67], a[16:19], a[0:3], v[64:67]\n" \
"s_lshl_b32 s92, s92, 11\n" \
"ds_read_b64_tr_b16 a[66:67], v10 offset:8192\n" \
"s_add_u32 s73, s73, s92\n" \
"v_mfma_f32_16x16x32_bf16 v[68:71], a[20:23], a[0:3], v[68:71]\n" \
"s_add_u32 m0, s51, 58240\n" \
"ds_read_b64_tr_b16 a[68:69], v11 offset:8192\n" \
"v_mfma_f32_16x16x32_bf16 v[72:75], a[16:19], a[4:7], v[72:75]\n" \
"buffer_load_dwordx4 v21, s[32:35], s47 offen offset:128 lds\n" \
"v_mfma_f32_16x16x32_bf16 v[76:79], a[20:23], a[4:7], v[76:79]\n" \
"s_cmp_lt_u32 s89, 3\ns_cselect_b32 s95, 3, 0\n" \
"ds_read_b64_tr_b16 a[70:71], v12 offset:8192\n" \
"v_mfma_f32_16x16x32_bf16 v[80:83], a[16:19], a[8:11], v[80:83]\n" \
"s_sub_u32 s92, s95, s89\n" \
"s_mul_i32 s93, s92, 98304\n" \
"s_add_u32 s74, s78, s93\n" \
"v_mfma_f32_16x16x32_bf16 v[84:87], a[20:23], a[8:11], v[84:87]\n" \
"s_sub_u32 s92, s92, 1\n" \
"s_and_b32 s92, s92, 3\n" \
"s_lshl_b32 s92, s92, 11\n" \
"v_mfma_f32_16x16x32_bf16 v[88:91], a[16:19], a[12:15], v[88:91]\n" \
"s_waitcnt vmcnt(18) lgkmcnt(0)\n" \
"s_add_u32 s74, s74, s92\n" \
"s_mul_i32 s96, s90, 3\n" \
"v_mfma_f32_16x16x32_bf16 v[92:95], a[20:23], a[12:15], v[92:95]\n" \
"s_barrier\n" \
"s_add_u32 s96, s96, s89\n" \
"s_add_u32 s92, s90, 24\n" \
"v_mfma_f32_16x16x32_bf16 v[96:99], a[72:75], a[48:51], v[96:99]\n" \
"s_mov_b32 s46, 1664\n" \
"s_add_u32 m0, s50, 0\n" \
"ds_read_b64_tr_b16 a[24:25], v9 offset:16896\n" \
"v_mfma_f32_16x16x32_bf16 v[100:103], a[76:79], a[48:51], v[100:103]\n" \
"buffer_load_dwordx4 v13, s[28:31], s46 offen lds\n" \
"v_mfma_f32_16x16x32_bf16 v[104:107], a[72:75], a[52:55], v[104:107]\n" \
"s_cmp_lt_u32 s89, 3\ns_cselect_b32 s96, s96, s92\n" \
"ds_read_b64_tr_b16 a[26:27], v10 offset:16896\n" \
"v_mfma_f32_16x16x32_bf16 v[108:111], a[76:79], a[52:55], v[108:111]\n" \
"s_lshl_b32 s96, s96, 5\n" \
"ds_read_b64_tr_b16 a[28:29], v11 offset:16896\n" \
"s_lshl_b32 s97, s90, 2\n" \
"v_mfma_f32_16x16x32_bf16 v[112:115], a[72:75], a[56:59], v[112:115]\n" \
"s_add_u32 s97, s97, s89\n" \
"ds_read_b64_tr_b16 a[30:31], v12 offset:16896\n" \
"s_add_u32 s97, s97, 32\n" \
"v_mfma_f32_16x16x32_bf16 v[116:119], a[76:79], a[56:59], v[116:119]\n" \
"s_add_u32 m0, s50, 4096\n" \
"ds_read_b128 a[0:3], v5 offset:32768\n" \
"s_lshl_b32 s97, s97, 5\n" \
"v_mfma_f32_16x16x32_bf16 v[120:123], a[72:75], a[60:63], v[120:123]\n" \
"buffer_load_dwordx4 v14, s[28:31], s46 offen lds\n" \
"v_mfma_f32_16x16x32_bf16 v[124:127], a[76:79], a[60:63], v[124:127]\n" \
"s_lshr_b32 s92, %1, 5\n" \
"ds_read_b128 a[4:7], v5 offset:34816\n" \
"s_lshl_b32 s92, s92, 2\n" \
"v_mfma_f32_16x16x32_bf16 v[128:131], a[80:83], a[48:51], v[128:131]\n" \
"s_add_u32 s92, s92, %2\n" \
"s_lshl_b32 s93, s92, 6\n" \
"s_lshl_b32 s92, s92, 17\n" \
"v_mfma_f32_16x16x32_bf16 v[132:135], a[84:87], a[48:51], v[132:135]\n" \
"s_add_u32 s92, s92, 67108864\n" \
"ds_read_b128 a[8:11], v5 offset:36864\n" \
"s_lshl_b32 s82, s96, 11\n" \
"v_mfma_f32_16x16x32_bf16 v[136:139], a[80:83], a[52:55], v[136:139]\n" \
"s_add_u32 m0, s50, 8192\n" \
"ds_read_b128 a[12:15], v5 offset:38912\n" \
"s_add_u32 s82, s82, s93\n" \
"v_mfma_f32_16x16x32_bf16 v[140:143], a[84:87], a[52:55], v[140:143]\n" \
"buffer_load_dwordx4 v15, s[28:31], s46 offen lds\n" \
"v_mfma_f32_16x16x32_bf16 v[144:147], a[80:83], a[56:59], v[144:147]\n" \
"s_lshl_b32 s86, s96, 2\n" \
"ds_read_b64_tr_b16 a[32:33], v9 offset:17408\n" \
"s_add_u32 s86, s86, 71303168\n" \
"v_mfma_f32_16x16x32_bf16 v[148:151], a[84:87], a[56:59], v[148:151]\n" \
"s_lshl_b32 s84, s96, 3\n" \
"ds_read_b64_tr_b16 a[34:35], v10 offset:17408\n" \
"s_add_u32 s84, s84, s92\n" \
"v_mfma_f32_16x16x32_bf16 v[152:155], a[80:83], a[60:63], v[152:155]\n" \
"s_lshl_b32 s83, s97, 11\n" \
"ds_read_b64_tr_b16 a[36:37], v11 offset:17408\n" \
"s_add_u32 s83, s83, s93\n" \
"v_mfma_f32_16x16x32_bf16 v[156:159], a[84:87], a[60:63], v[156:159]\n" \
"s_add_u32 m0, s50, 12288\n" \
"ds_read_b64_tr_b16 a[38:39], v12 offset:17408\n" \
"s_lshl_b32 s87, s97, 2\n" \
"v_mfma_f32_16x16x32_bf16 v[160:163], a[88:91], a[48:51], v[160:163]\n" \
"buffer_load_dwordx4 v16, s[28:31], s46 offen lds\n" \
"v_mfma_f32_16x16x32_bf16 v[164:167], a[92:95], a[48:51], v[164:167]\n" \
"s_add_u32 s87, s87, 71303168\n" \
"ds_read_b64_tr_b16 a[40:41], v9 offset:17920\n" \
"s_lshl_b32 s85, s97, 3\n" \
"v_mfma_f32_16x16x32_bf16 v[168:171], a[88:91], a[52:55], v[168:171]\n" \
"s_add_u32 s85, s85, s92\n" \
"ds_read_b64_tr_b16 a[42:43], v10 offset:17920\n" \
"s_lshl_b32 s93, s89, 6\n" \
"v_mfma_f32_16x16x32_bf16 v[172:175], a[92:95], a[52:55], v[172:175]\n" \
"s_add_u32 s93, s93, 94388224\n" \
"ds_read_b64_tr_b16 a[44:45], v11 offset:17920\n" \
"s_lshl_b32 s92, s90, 8\n" \
"v_mfma_f32_16x16x32_bf16 v[176:179], a[88:91], a[56:59], v[176:179]\n" \
"s_mov_b32 s46, 1664\n" \
"s_add_u32 m0, s50, 16384\n" \
"ds_read_b64_tr_b16 a[46:47], v12 offset:17920\n" \
"v_mfma_f32_16x16x32_bf16 v[180:183], a[92:95], a[56:59], v[180:183]\n" \
"buffer_load_dwordx4 v17, s[28:31], s46 offen lds\n" \
"v_mfma_f32_16x16x32_bf16 v[184:187], a[88:91], a[60:63], v[184:187]\n" \
"s_add_u32 s10, s93, s92\n" \
"ds_read_b64_tr_b16 a[16:17], v9 offset:16384\n" \
"s_add_u32 s10, s10, 6144\n" \
"v_mfma_f32_16x16x32_bf16 v[188:191], a[92:95], a[60:63], v[188:191]\n" \
"s_lshl_b32 s92, s90, 10\n" \
"s_add_u32 s11, s93, s92\n" \
"s_add_u32 s11, s11, 8192\n" \
"v_mfma_f32_16x16x32_bf16 v[64:67], a[64:67], a[48:51], v[64:67]\n" \
"s_mul_i32 s92, s90, 768\n" \
"ds_read_b64_tr_b16 a[18:19], v10 offset:16384\n" \
"s_add_u32 s98, s93, s92\n" \
"v_mfma_f32_16x16x32_bf16 v[68:71], a[68:71], a[48:51], v[68:71]\n" \
"s_add_u32 m0, s50, 20480\n" \
"ds_read_b64_tr_b16 a[20:21], v11 offset:16384\n" \
"v_mfma_f32_16x16x32_bf16 v[72:75], a[64:67], a[52:55], v[72:75]\n" \
"buffer_load_dwordx4 v18, s[28:31], s46 offen lds\n" \
"v_mfma_f32_16x16x32_bf16 v[76:79], a[68:71], a[52:55], v[76:79]\n" \
"ds_read_b64_tr_b16 a[22:23], v12 offset:16384\n" \
"v_mfma_f32_16x16x32_bf16 v[80:83], a[64:67], a[56:59], v[80:83]\n" \
"v_mfma_f32_16x16x32_bf16 v[84:87], a[68:71], a[56:59], v[84:87]\n" \
"v_mfma_f32_16x16x32_bf16 v[88:91], a[64:67], a[60:63], v[88:91]\n" \
"v_mfma_f32_16x16x32_bf16 v[92:95], a[68:71], a[60:63], v[92:95]\n" \
"s_waitcnt lgkmcnt(0)\n" \
"v_mfma_f32_16x16x32_bf16 v[96:99], a[24:27], a[0:3], v[96:99]\n" \
"s_add_u32 m0, s50, 24576\n" \
"ds_read_b64_tr_b16 a[72:73], v9 offset:25088\n" \
"v_mfma_f32_16x16x32_bf16 v[100:103], a[28:31], a[0:3], v[100:103]\n" \
"buffer_load_dwordx4 v19, s[28:31], s46 offen lds\n" \
"v_mfma_f32_16x16x32_bf16 v[104:107], a[24:27], a[4:7], v[104:107]\n" \
"s_add_u32 s100, s100, 262144\ns_addc_u32 s101, s101, 0\n" \
"ds_read_b64_tr_b16 a[74:75], v10 offset:25088\n" \
"v_mfma_f32_16x16x32_bf16 v[108:111], a[28:31], a[4:7], v[108:111]\n" \
"ds_read_b64_tr_b16 a[76:77], v11 offset:25088\n" \
"v_mfma_f32_16x16x32_bf16 v[112:115], a[24:27], a[8:11], v[112:115]\n" \
"ds_read_b64_tr_b16 a[78:79], v12 offset:25088\n" \
"v_mfma_f32_16x16x32_bf16 v[116:119], a[28:31], a[8:11], v[116:119]\n" \
"s_add_u32 m0, s50, 28672\n" \
"ds_read_b128 a[48:51], v6 offset:32768\n" \
"v_mfma_f32_16x16x32_bf16 v[120:123], a[24:27], a[12:15], v[120:123]\n" \
"buffer_load_dwordx4 v20, s[28:31], s46 offen lds\n" \
"v_mfma_f32_16x16x32_bf16 v[124:127], a[28:31], a[12:15], v[124:127]\n" \
"ds_read_b128 a[52:55], v6 offset:34816\n" \
"v_mfma_f32_16x16x32_bf16 v[128:131], a[32:35], a[0:3], v[128:131]\n" \
"s_mov_b64 exec, 0xffff\nglobal_load_dword v1, v2, s[100:101]\ns_mov_b64 exec, -1\n" \
"v_mfma_f32_16x16x32_bf16 v[132:135], a[36:39], a[0:3], v[132:135]\n" \
"ds_read_b128 a[56:59], v6 offset:36864\n" \
"v_mfma_f32_16x16x32_bf16 v[136:139], a[32:35], a[4:7], v[136:139]\n" \
"s_mov_b32 s47, 7340032\n" \
"s_add_u32 m0, s51, 0\n" \
"ds_read_b128 a[60:63], v6 offset:38912\n" \
"v_mfma_f32_16x16x32_bf16 v[140:143], a[36:39], a[4:7], v[140:143]\n" \
"buffer_load_dwordx4 v21, s[32:35], s47 offen offset:0 lds\n" \
"v_mfma_f32_16x16x32_bf16 v[144:147], a[32:35], a[8:11], v[144:147]\n" \
"ds_read_b64_tr_b16 a[80:81], v9 offset:25600\n" \
"v_mfma_f32_16x16x32_bf16 v[148:151], a[36:39], a[8:11], v[148:151]\n" \
"ds_read_b64_tr_b16 a[82:83], v10 offset:25600\n" \
"v_mfma_f32_16x16x32_bf16 v[152:155], a[32:35], a[12:15], v[152:155]\n" \
"ds_read_b64_tr_b16 a[84:85], v11 offset:25600\n" \
"v_mfma_f32_16x16x32_bf16 v[156:159], a[36:39], a[12:15], v[156:159]\n" \
"s_add_u32 m0, s51, 896\n" \
"ds_read_b64_tr_b16 a[86:87], v12 offset:25600\n" \
"v_mfma_f32_16x16x32_bf16 v[160:163], a[40:43], a[0:3], v[160:163]\n" \
"buffer_load_dwordx4 v21, s[32:35], s47 offen offset:128 lds\n" \
"v_mfma_f32_16x16x32_bf16 v[164:167], a[44:47], a[0:3], v[164:167]\n" \
"ds_read_b64_tr_b16 a[88:89], v9 offset:26112\n" \
"v_mfma_f32_16x16x32_bf16 v[168:171], a[40:43], a[4:7], v[168:171]\n" \
"ds_read_b64_tr_b16 a[90:91], v10 offset:26112\n" \
"v_mfma_f32_16x16x32_bf16 v[172:175], a[44:47], a[4:7], v[172:175]\n" \
"ds_read_b64_tr_b16 a[92:93], v11 offset:26112\n" \
"v_mfma_f32_16x16x32_bf16 v[176:179], a[40:43], a[8:11], v[176:179]\n" \
"s_mov_b32 s47, 7602176\n" \
"s_add_u32 m0, s51, 8192\n" \
"ds_read_b64_tr_b16 a[94:95], v12 offset:26112\n" \
"v_mfma_f32_16x16x32_bf16 v[180:183], a[44:47], a[8:11], v[180:183]\n" \
"buffer_load_dwordx4 v21, s[32:35], s47 offen offset:0 lds\n" \
"v_mfma_f32_16x16x32_bf16 v[184:187], a[40:43], a[12:15], v[184:187]\n" \
"ds_read_b64_tr_b16 a[64:65], v9 offset:24576\n" \
"v_mfma_f32_16x16x32_bf16 v[188:191], a[44:47], a[12:15], v[188:191]\n" \
"v_mfma_f32_16x16x32_bf16 v[64:67], a[16:19], a[0:3], v[64:67]\n" \
"ds_read_b64_tr_b16 a[66:67], v10 offset:24576\n" \
"v_mfma_f32_16x16x32_bf16 v[68:71], a[20:23], a[0:3], v[68:71]\n" \
"s_add_u32 m0, s51, 9088\n" \
"ds_read_b64_tr_b16 a[68:69], v11 offset:24576\n" \
"v_mfma_f32_16x16x32_bf16 v[72:75], a[16:19], a[4:7], v[72:75]\n" \
"buffer_load_dwordx4 v21, s[32:35], s47 offen offset:128 lds\n" \
"v_mfma_f32_16x16x32_bf16 v[76:79], a[20:23], a[4:7], v[76:79]\n" \
"ds_read_b64_tr_b16 a[70:71], v12 offset:24576\n" \
"v_mfma_f32_16x16x32_bf16 v[80:83], a[16:19], a[8:11], v[80:83]\n" \
"v_mfma_f32_16x16x32_bf16 v[84:87], a[20:23], a[8:11], v[84:87]\n" \
"v_mfma_f32_16x16x32_bf16 v[88:91], a[16:19], a[12:15], v[88:91]\n" \
"s_waitcnt vmcnt(17) lgkmcnt(0)\n" \
"v_mfma_f32_16x16x32_bf16 v[92:95], a[20:23], a[12:15], v[92:95]\n" \
"s_barrier\n" \
"v_mfma_f32_16x16x32_bf16 v[96:99], a[72:75], a[48:51], v[96:99]\n" \
"s_mov_b32 s46, 1792\n" \
"s_add_u32 m0, s50, 32768\n" \
"ds_read_b64_tr_b16 a[24:25], v9 offset:33280\n" \
"v_mfma_f32_16x16x32_bf16 v[100:103], a[76:79], a[48:51], v[100:103]\n" \
"buffer_load_dwordx4 v13, s[28:31], s46 offen lds\n" \
"v_mfma_f32_16x16x32_bf16 v[104:107], a[72:75], a[52:55], v[104:107]\n" \
"ds_read_b64_tr_b16 a[26:27], v10 offset:33280\n" \
"v_mfma_f32_16x16x32_bf16 v[108:111], a[76:79], a[52:55], v[108:111]\n" \
"ds_read_b64_tr_b16 a[28:29], v11 offset:33280\n" \
"v_mfma_f32_16x16x32_bf16 v[112:115], a[72:75], a[56:59], v[112:115]\n" \
"ds_read_b64_tr_b16 a[30:31], v12 offset:33280\n" \
"v_mfma_f32_16x16x32_bf16 v[116:119], a[76:79], a[56:59], v[116:119]\n" \
"s_add_u32 m0, s50, 36864\n" \
"ds_read_b128 a[0:3], v7 offset:0\n" \
"v_mfma_f32_16x16x32_bf16 v[120:123], a[72:75], a[60:63], v[120:123]\n" \
"buffer_load_dwordx4 v14, s[28:31], s46 offen lds\n" \
"v_mfma_f32_16x16x32_bf16 v[124:127], a[76:79], a[60:63], v[124:127]\n" \
"ds_read_b128 a[4:7], v7 offset:2048\n" \
"v_mfma_f32_16x16x32_bf16 v[128:131], a[80:83], a[48:51], v[128:131]\n" \
"v_mfma_f32_16x16x32_bf16 v[132:135], a[84:87], a[48:51], v[132:135]\n" \
"ds_read_b128 a[8:11], v7 offset:4096\n" \
"v_mfma_f32_16x16x32_bf16 v[136:139], a[80:83], a[52:55], v[136:139]\n" \
"s_add_u32 m0, s50, 40960\n" \
"ds_read_b128 a[12:15], v7 offset:6144\n" \
"v_mfma_f32_16x16x32_bf16 v[140:143], a[84:87], a[52:55], v[140:143]\n" \
"buffer_load_dwordx4 v15, s[28:31], s46 offen lds\n" \
"v_mfma_f32_16x16x32_bf16 v[144:147], a[80:83], a[56:59], v[144:147]\n" \
"ds_read_b64_tr_b16 a[32:33], v9 offset:33792\n" \
"v_mfma_f32_16x16x32_bf16 v[148:151], a[84:87], a[56:59], v[148:151]\n" \
"ds_read_b64_tr_b16 a[34:35], v10 offset:33792\n" \
"v_mfma_f32_16x16x32_bf16 v[152:155], a[80:83], a[60:63], v[152:155]\n" \
"ds_read_b64_tr_b16 a[36:37], v11 offset:33792\n" \
"v_mfma_f32_16x16x32_bf16 v[156:159], a[84:87], a[60:63], v[156:159]\n" \
"s_add_u32 m0, s50, 45056\n" \
"ds_read_b64_tr_b16 a[38:39], v12 offset:33792\n" \
"v_mfma_f32_16x16x32_bf16 v[160:163], a[88:91], a[48:51], v[160:163]\n" \
"buffer_load_dwordx4 v16, s[28:31], s46 offen lds\n" \
"v_mfma_f32_16x16x32_bf16 v[164:167], a[92:95], a[48:51], v[164:167]\n" \
"ds_read_b64_tr_b16 a[40:41], v9 offset:34304\n" \
"v_mfma_f32_16x16x32_bf16 v[168:171], a[88:91], a[52:55], v[168:171]\n" \
"ds_read_b64_tr_b16 a[42:43], v10 offset:34304\n" \
"v_mfma_f32_16x16x32_bf16 v[172:175], a[92:95], a[52:55], v[172:175]\n" \
"LH13_%=:\ns_cbranch_execz LHR0_%=\n" \
"ds_read_b64_tr_b16 a[44:45], v11 offset:34304\n" \
"v_mfma_f32_16x16x32_bf16 v[176:179], a[88:91], a[56:59], v[176:179]\n" \
"s_mov_b32 s46, 1792\n" \
"s_add_u32 m0, s50, 49152\n" \
"ds_read_b64_tr_b16 a[46:47], v12 offset:34304\n" \
"v_mfma_f32_16x16x32_bf16 v[180:183], a[92:95], a[56:59], v[180:183]\n" \
"buffer_load_dwordx4 v17, s[28:31], s46 offen lds\n" \
"v_mfma_f32_16x16x32_bf16 v[184:187], a[88:91], a[60:63], v[184:187]\n" \
"ds_read_b64_tr_b16 a[16:17], v9 offset:32768\n" \
"v_mfma_f32_16x16x32_bf16 v[188:191], a[92:95], a[60:63], v[188:191]\n" \
"v_mfma_f32_16x16x32_bf16 v[64:67], a[64:67], a[48:51], v[64:67]\n" \
"ds_read_b64_tr_b16 a[18:19], v10 offset:32768\n" \
"v_mfma_f32_16x16x32_bf16 v[68:71], a[68:71], a[48:51], v[68:71]\n" \
"s_add_u32 m0, s50, 53248\n" \
"ds_read_b64_tr_b16 a[20:21], v11 offset:32768\n" \
"v_mfma_f32_16x16x32_bf16 v[72:75], a[64:67], a[52:55], v[72:75]\n" \
"buffer_load_dwordx4 v18, s[28:31], s46 offen lds\n" \
"v_mfma_f32_16x16x32_bf16 v[76:79], a[68:71], a[52:55], v[76:79]\n" \
"ds_read_b64_tr_b16 a[22:23], v12 offset:32768\n" \
"v_mfma_f32_16x16x32_bf16 v[80:83], a[64:67], a[56:59], v[80:83]\n" \
"v_mfma_f32_16x16x32_bf16 v[84:87], a[68:71], a[56:59], v[84:87]\n" \
"v_mfma_f32_16x16x32_bf16 v[88:91], a[64:67], a[60:63], v[88:91]\n" \
"v_mfma_f32_16x16x32_bf16 v[92:95], a[68:71], a[60:63], v[92:95]\n" \
"s_waitcnt lgkmcnt(0)\n" \
"v_mfma_f32_16x16x32_bf16 v[96:99], a[24:27], a[0:3], v[96:99]\n" \
"s_add_u32 m0, s50, 57344\n" \
"ds_read_b64_tr_b16 a[72:73], v9 offset:41472\n" \
"v_mfma_f32_16x16x32_bf16 v[100:103], a[28:31], a[0:3], v[100:103]\n" \
"buffer_load_dwordx4 v19, s[28:31], s46 offen lds\n" \
"v_mfma_f32_16x16x32_bf16 v[104:107], a[24:27], a[4:7], v[104:107]\n" \
"s_add_u32 s100, s100, 262144\ns_addc_u32 s101, s101, 0\n" \
"ds_read_b64_tr_b16 a[74:75], v10 offset:41472\n" \
"v_mfma_f32_16x16x32_bf16 v[108:111], a[28:31], a[4:7], v[108:111]\n" \
"ds_read_b64_tr_b16 a[76:77], v11 offset:41472\n" \
"v_mfma_f32_16x16x32_bf16 v[112:115], a[24:27], a[8:11], v[112:115]\n" \
"ds_read_b64_tr_b16 a[78:79], v12 offset:41472\n" \
"v_mfma_f32_16x16x32_bf16 v[116:119], a[28:31], a[8:11], v[116:119]\n" \
"s_add_u32 m0, s50, 61440\n" \
"ds_read_b128 a[48:51], v8 offset:0\n" \
"v_mfma_f32_16x16x32_bf16 v[120:123], a[24:27], a[12:15], v[120:123]\n" \
"buffer_load_dwordx4 v20, s[28:31], s46 offen lds\n" \
"v_mfma_f32_16x16x32_bf16 v[124:127], a[28:31], a[12:15], v[124:127]\n" \
"ds_read_b128 a[52:55], v8 offset:2048\n" \
"v_mfma_f32_16x16x32_bf16 v[128:131], a[32:35], a[0:3], v[128:131]\n" \
"s_mov_b64 exec, 0xffff\nglobal_load_dword v1, v2, s[100:101]\ns_mov_b64 exec, -1\n" \
"v_mfma_f32_16x16x32_bf16 v[132:135], a[36:39], a[0:3], v[132:135]\n" \
"ds_read_b128 a[56:59], v8 offset:4096\n" \
"v_mfma_f32_16x16x32_bf16 v[136:139], a[32:35], a[4:7], v[136:139]\n" \
"s_mov_b32 s47, 7864320\n" \
"s_add_u32 m0, s51, 16384\n" \
"ds_read_b128 a[60:63], v8 offset:6144\n" \
"v_mfma_f32_16x16x32_bf16 v[140:143], a[36:39], a[4:7], v[140:143]\n" \
"buffer_load_dwordx4 v21, s[32:35], s47 offen offset:0 lds\n" \
"v_mfma_f32_16x16x32_bf16 v[144:147], a[32:35], a[8:11], v[144:147]\n" \
"ds_read_b64_tr_b16 a[80:81], v9 offset:41984\n" \
"v_mfma_f32_16x16x32_bf16 v[148:151], a[36:39], a[8:11], v[148:151]\n" \
"ds_read_b64_tr_b16 a[82:83], v10 offset:41984\n" \
"v_mfma_f32_16x16x32_bf16 v[152:155], a[32:35], a[12:15], v[152:155]\n" \
"ds_read_b64_tr_b16 a[84:85], v11 offset:41984\n" \
"v_mfma_f32_16x16x32_bf16 v[156:159], a[36:39], a[12:15], v[156:159]\n" \
"s_add_u32 m0, s51, 17280\n" \
"ds_read_b64_tr_b16 a[86:87], v12 offset:41984\n" \
"v_mfma_f32_16x16x32_bf16 v[160:163], a[40:43], a[0:3], v[160:163]\n" \
"buffer_load_dwordx4 v21, s[32:35], s47 offen offset:128 lds\n" \
"v_mfma_f32_16x16x32_bf16 v[164:167], a[44:47], a[0:3], v[164:167]\n" \
"ds_read_b64_tr_b16 a[88:89], v9 offset:42496\n" \
"v_mfma_f32_16x16x32_bf16 v[168:171], a[40:43], a[4:7], v[168:171]\n" \
"ds_read_b64_tr_b16 a[90:91], v10 offset:42496\n" \
"v_mfma_f32_16x16x32_bf16 v[172:175], a[44:47], a[4:7], v[172:175]\n" \
"ds_read_b64_tr_b16 a[92:93], v11 offset:42496\n" \
"v_mfma_f32_16x16x32_bf16 v[176:179], a[40:43], a[8:11], v[176:179]\n" \
"s_mov_b32 s47, 8126464\n" \
"s_add_u32 m0, s51, 24576\n" \
"ds_read_b64_tr_b16 a[94:95], v12 offset:42496\n" \
"v_mfma_f32_16x16x32_bf16 v[180:183], a[44:47], a[8:11], v[180:183]\n" \
"buffer_load_dwordx4 v21, s[32:35], s47 offen offset:0 lds\n" \
"v_mfma_f32_16x16x32_bf16 v[184:187], a[40:43], a[12:15], v[184:187]\n" \
"ds_read_b64_tr_b16 a[64:65], v9 offset:40960\n" \
"v_mfma_f32_16x16x32_bf16 v[188:191], a[44:47], a[12:15], v[188:191]\n" \
"v_mfma_f32_16x16x32_bf16 v[64:67], a[16:19], a[0:3], v[64:67]\n" \
"ds_read_b64_tr_b16 a[66:67], v10 offset:40960\n" \
"v_mfma_f32_16x16x32_bf16 v[68:71], a[20:23], a[0:3], v[68:71]\n" \
"s_add_u32 m0, s51, 25472\n" \
"ds_read_b64_tr_b16 a[68:69], v11 offset:40960\n" \
"v_mfma_f32_16x16x32_bf16 v[72:75], a[16:19], a[4:7], v[72:75]\n" \
"buffer_load_dwordx4 v21, s[32:35], s47 offen offset:128 lds\n" \
"v_mfma_f32_16x16x32_bf16 v[76:79], a[20:23], a[4:7], v[76:79]\n" \
"ds_read_b64_tr_b16 a[70:71], v12 offset:40960\n" \
"v_mfma_f32_16x16x32_bf16 v[80:83], a[16:19], a[8:11], v[80:83]\n" \
"v_mfma_f32_16x16x32_bf16 v[84:87], a[20:23], a[8:11], v[84:87]\n" \
"v_mfma_f32_16x16x32_bf16 v[88:91], a[16:19], a[12:15], v[88:91]\n" \
"s_waitcnt vmcnt(18) lgkmcnt(0)\n" \
"v_mfma_f32_16x16x32_bf16 v[92:95], a[20:23], a[12:15], v[92:95]\n" \
"s_barrier\n" \
"v_mfma_f32_16x16x32_bf16 v[96:99], a[72:75], a[48:51], v[96:99]\n" \
"s_mov_b32 s46, 1920\n" \
"s_add_u32 m0, s50, 65536\n" \
"ds_read_b64_tr_b16 a[24:25], v9 offset:49664\n" \
"v_mfma_f32_16x16x32_bf16 v[100:103], a[76:79], a[48:51], v[100:103]\n" \
"buffer_load_dwordx4 v13, s[28:31], s46 offen lds\n" \
"v_mfma_f32_16x16x32_bf16 v[104:107], a[72:75], a[52:55], v[104:107]\n" \
"ds_read_b64_tr_b16 a[26:27], v10 offset:49664\n" \
"v_mfma_f32_16x16x32_bf16 v[108:111], a[76:79], a[52:55], v[108:111]\n" \
"ds_read_b64_tr_b16 a[28:29], v11 offset:49664\n" \
"v_mfma_f32_16x16x32_bf16 v[112:115], a[72:75], a[56:59], v[112:115]\n" \
"ds_read_b64_tr_b16 a[30:31], v12 offset:49664\n" \
"v_mfma_f32_16x16x32_bf16 v[116:119], a[76:79], a[56:59], v[116:119]\n" \
"s_add_u32 m0, s50, 69632\n" \
"ds_read_b128 a[0:3], v5 offset:0\n" \
"v_mfma_f32_16x16x32_bf16 v[120:123], a[72:75], a[60:63], v[120:123]\n" \
"buffer_load_dwordx4 v14, s[28:31], s46 offen lds\n" \
"v_mfma_f32_16x16x32_bf16 v[124:127], a[76:79], a[60:63], v[124:127]\n" \
"ds_read_b128 a[4:7], v5 offset:2048\n" \
"v_mfma_f32_16x16x32_bf16 v[128:131], a[80:83], a[48:51], v[128:131]\n" \
"v_mfma_f32_16x16x32_bf16 v[132:135], a[84:87], a[48:51], v[132:135]\n" \
"ds_read_b128 a[8:11], v5 offset:4096\n" \
"v_mfma_f32_16x16x32_bf16 v[136:139], a[80:83], a[52:55], v[136:139]\n" \
"s_add_u32 m0, s50, 73728\n" \
"ds_read_b128 a[12:15], v5 offset:6144\n" \
"v_mfma_f32_16x16x32_bf16 v[140:143], a[84:87], a[52:55], v[140:143]\n" \
"buffer_load_dwordx4 v15, s[28:31], s46 offen lds\n" \
"v_mfma_f32_16x16x32_bf16 v[144:147], a[80:83], a[56:59], v[144:147]\n" \
"ds_read_b64_tr_b16 a[32:33], v9 offset:50176\n" \
"v_mfma_f32_16x16x32_bf16 v[148:151], a[84:87], a[56:59], v[148:151]\n" \
"ds_read_b64_tr_b16 a[34:35], v10 offset:50176\n" \
"v_mfma_f32_16x16x32_bf16 v[152:155], a[80:83], a[60:63], v[152:155]\n" \
"ds_read_b64_tr_b16 a[36:37], v11 offset:50176\n" \
"v_mfma_f32_16x16x32_bf16 v[156:159], a[84:87], a[60:63], v[156:159]\n" \
"s_add_u32 m0, s50, 77824\n" \
"ds_read_b64_tr_b16 a[38:39], v12 offset:50176\n" \
"v_mfma_f32_16x16x32_bf16 v[160:163], a[88:91], a[48:51], v[160:163]\n" \
"buffer_load_dwordx4 v16, s[28:31], s46 offen lds\n" \
"v_mfma_f32_16x16x32_bf16 v[164:167], a[92:95], a[48:51], v[164:167]\n" \
"ds_read_b64_tr_b16 a[40:41], v9 offset:50688\n" \
"v_mfma_f32_16x16x32_bf16 v[168:171], a[88:91], a[52:55], v[168:171]\n" \
"ds_read_b64_tr_b16 a[42:43], v10 offset:50688\n" \
"v_mfma_f32_16x16x32_bf16 v[172:175], a[92:95], a[52:55], v[172:175]\n" \
"ds_read_b64_tr_b16 a[44:45], v11 offset:50688\n" \
"v_mfma_f32_16x16x32_bf16 v[176:179], a[88:91], a[56:59], v[176:179]\n" \
"s_mov_b32 s46, 1920\n" \
"s_add_u32 m0, s50, 81920\n" \
"ds_read_b64_tr_b16 a[46:47], v12 offset:50688\n" \
"v_mfma_f32_16x16x32_bf16 v[180:183], a[92:95], a[56:59], v[180:183]\n" \
"buffer_load_dwordx4 v17, s[28:31], s46 offen lds\n" \
"v_mfma_f32_16x16x32_bf16 v[184:187], a[88:91], a[60:63], v[184:187]\n" \
"ds_read_b64_tr_b16 a[16:17], v9 offset:49152\n" \
"v_mfma_f32_16x16x32_bf16 v[188:191], a[92:95], a[60:63], v[188:191]\n" \
"v_mfma_f32_16x16x32_bf16 v[64:67], a[64:67], a[48:51], v[64:67]\n" \
"ds_read_b64_tr_b16 a[18:19], v10 offset:49152\n" \
"v_mfma_f32_16x16x32_bf16 v[68:71], a[68:71], a[48:51], v[68:71]\n" \
"s_add_u32 m0, s50, 86016\n" \
"ds_read_b64_tr_b16 a[20:21], v11 offset:49152\n" \
"v_mfma_f32_16x16x32_bf16 v[72:75], a[64:67], a[52:55], v[72:75]\n" \
"buffer_load_dwordx4 v18, s[28:31], s46 offen lds\n" \
"v_mfma_f32_16x16x32_bf16 v[76:79], a[68:71], a[52:55], v[76:79]\n" \
"ds_read_b64_tr_b16 a[22:23], v12 offset:49152\n" \
"v_mfma_f32_16x16x32_bf16 v[80:83], a[64:67], a[56:59], v[80:83]\n" \
"v_mfma_f32_16x16x32_bf16 v[84:87], a[68:71], a[56:59], v[84:87]\n" \
"v_mfma_f32_16x16x32_bf16 v[88:91], a[64:67], a[60:63], v[88:91]\n" \
"v_mfma_f32_16x16x32_bf16 v[92:95], a[68:71], a[60:63], v[92:95]\n" \
"s_waitcnt lgkmcnt(0)\n" \
"v_mfma_f32_16x16x32_bf16 v[96:99], a[24:27], a[0:3], v[96:99]\n" \
"ds_read_b64_tr_b16 a[72:73], v9 offset:57856\n" \
"v_mfma_f32_16x16x32_bf16 v[100:103], a[28:31], a[0:3], v[100:103]\n" \
"buffer_load_dwordx2 v[58:59], v57, s[36:39], 0 offen sc1\n" \
"v_mfma_f32_16x16x32_bf16 v[104:107], a[24:27], a[4:7], v[104:107]\n" \
"ds_read_b64_tr_b16 a[74:75], v10 offset:57856\n" \
"v_mfma_f32_16x16x32_bf16 v[108:111], a[28:31], a[4:7], v[108:111]\n" \
"ds_read_b64_tr_b16 a[76:77], v11 offset:57856\n" \
"v_mfma_f32_16x16x32_bf16 v[112:115], a[24:27], a[8:11], v[112:115]\n" \
"s_add_u32 m0, s50, 90112\n" \
"ds_read_b64_tr_b16 a[78:79], v12 offset:57856\n" \
"v_mfma_f32_16x16x32_bf16 v[116:119], a[28:31], a[8:11], v[116:119]\n" \
"buffer_load_dwordx4 v19, s[28:31], s46 offen lds\n" \
"v_mfma_f32_16x16x32_bf16 v[120:123], a[24:27], a[12:15], v[120:123]\n" \
"ds_read_b128 a[48:51], v6 offset:0\n" \
"v_mfma_f32_16x16x32_bf16 v[124:127], a[28:31], a[12:15], v[124:127]\n" \
"ds_read_b128 a[52:55], v6 offset:2048\n" \
"v_mfma_f32_16x16x32_bf16 v[128:131], a[32:35], a[0:3], v[128:131]\n" \
"s_add_u32 m0, s50, 94208\n" \
"ds_read_b128 a[56:59], v6 offset:4096\n" \
"v_mfma_f32_16x16x32_bf16 v[132:135], a[36:39], a[0:3], v[132:135]\n" \
"buffer_load_dwordx4 v20, s[28:31], s46 offen lds\n" \
"v_mfma_f32_16x16x32_bf16 v[136:139], a[32:35], a[4:7], v[136:139]\n" \
"ds_read_b128 a[60:63], v6 offset:6144\n" \
"v_mfma_f32_16x16x32_bf16 v[140:143], a[36:39], a[4:7], v[140:143]\n" \
"ds_read_b64_tr_b16 a[80:81], v9 offset:58368\n" \
"v_mfma_f32_16x16x32_bf16 v[144:147], a[32:35], a[8:11], v[144:147]\n" \
"s_mov_b32 s44, 0\n" \
"ds_read_b64_tr_b16 a[82:83], v10 offset:58368\n" \
"s_mov_b64 s[54:55], 0\n" \
"v_mfma_f32_16x16x32_bf16 v[148:151], a[36:39], a[8:11], v[148:151]\n" \
"s_mov_b32 s49, s22\n" \
"ds_read_b64_tr_b16 a[84:85], v11 offset:58368\n" \
"s_add_u32 s48, s22, 0x10000\n" \
"v_mfma_f32_16x16x32_bf16 v[152:155], a[32:35], a[12:15], v[152:155]\n" \
"s_lshl_b32 s60, s7, 1\n" \
"ds_read_b64_tr_b16 a[86:87], v12 offset:58368\n" \
"s_add_u32 s60, s60, %2\n" \
"v_mfma_f32_16x16x32_bf16 v[156:159], a[36:39], a[12:15], v[156:159]\n" \
"s_sub_u32 s46, s60, 1\n" \
"ds_read_b64_tr_b16 a[88:89], v9 offset:58880\n" \
"s_xor_b32 s62, %2, 3\n" \
"v_mfma_f32_16x16x32_bf16 v[160:163], a[40:43], a[0:3], v[160:163]\n" \
"ds_read_b64_tr_b16 a[90:91], v10 offset:58880\n" \
"v_mfma_f32_16x16x32_bf16 v[164:167], a[44:47], a[0:3], v[164:167]\n" \
"ds_read_b64_tr_b16 a[92:93], v11 offset:58880\n" \
"v_mfma_f32_16x16x32_bf16 v[168:171], a[40:43], a[4:7], v[168:171]\n" \
"ds_read_b64_tr_b16 a[94:95], v12 offset:58880\n" \
"v_mfma_f32_16x16x32_bf16 v[172:175], a[44:47], a[4:7], v[172:175]\n" \
"ds_read_b64_tr_b16 a[64:65], v9 offset:57344\n" \
"v_mfma_f32_16x16x32_bf16 v[176:179], a[40:43], a[8:11], v[176:179]\n" \
"ds_read_b64_tr_b16 a[66:67], v10 offset:57344\n" \
"v_mfma_f32_16x16x32_bf16 v[180:183], a[44:47], a[8:11], v[180:183]\n" \
"ds_read_b64_tr_b16 a[68:69], v11 offset:57344\n" \
"v_mfma_f32_16x16x32_bf16 v[184:187], a[40:43], a[12:15], v[184:187]\n" \
"ds_read_b64_tr_b16 a[70:71], v12 offset:57344\n" \
"v_mfma_f32_16x16x32_bf16 v[188:191], a[44:47], a[12:15], v[188:191]\n" \
"s_cmp_lt_u32 %2, 2\ns_cselect_b32 s46, s46, 14\ns_cselect_b32 s62, s62, %2\ns_cselect_b32 s61, 56, 57\n" \
"v_mfma_f32_16x16x32_bf16 v[64:67], a[16:19], a[0:3], v[64:67]\n" \
"s_cmp_lt_u32 s7, 3\ns_cselect_b32 s45, 8, 0\n" \
"v_mfma_f32_16x16x32_bf16 v[68:71], a[20:23], a[0:3], v[68:71]\n" \
"s_cmp_lt_i32 s46, s45\ns_cselect_b32 s23, 99, s46\ns_cselect_b32 s24, s45, 99\n" \
"v_mfma_f32_16x16x32_bf16 v[72:75], a[16:19], a[4:7], v[72:75]\n" \
"s_min_i32 s46, s46, s45\n" \
"s_lshl_b32 s60, s60, 6\n" \
"s_add_u32 s60, s60, s6\n" \
"v_mfma_f32_16x16x32_bf16 v[76:79], a[20:23], a[4:7], v[76:79]\n" \
"s_lshl_b32 s50, s60, 13\n" \
"s_lshl_b32 s52, s60, 3\n" \
"s_add_u32 s52, s52, 94388224\n" \
"v_mfma_f32_16x16x32_bf16 v[80:83], a[16:19], a[8:11], v[80:83]\n" \
"s_waitcnt vmcnt(2)\n" \
"s_lshr_b32 s60, %2, 1\n" \
"s_lshl_b32 s60, s60, 28\n" \
"v_mfma_f32_16x16x32_bf16 v[84:87], a[20:23], a[8:11], v[84:87]\n" \
"s_add_u32 s52, s52, s60\n" \
"v_cmp_ne_u64 vcc, s[14:15], v[58:59]\n" \
"s_lshl_b32 s62, s62, 1\n" \
"v_mfma_f32_16x16x32_bf16 v[88:91], a[16:19], a[12:15], v[88:91]\n" \
"s_waitcnt lgkmcnt(0)\n" \
"s_add_u32 s61, s61, s62\n" \
"s_lshl_b32 s60, s7, 2\n" \
"v_mfma_f32_16x16x32_bf16 v[92:95], a[20:23], a[12:15], v[92:95]\n" \
"s_cbranch_vccnz LPK_%=\nLPKR_%=:\n" \
"s_barrier\n" \
"s_sub_u32 s61, s61, s60\n" \
"v_mfma_f32_16x16x32_bf16 v[96:99], a[72:75], a[48:51], v[96:99]\n" \
"s_add_u32 m0, s22, 32768\n" \
"ds_read_b64_tr_b16 a[24:25], v9 offset:512\n" \
"s_lshl_b32 s61, s61, 5\n" \
"v_mfma_f32_16x16x32_bf16 v[100:103], a[76:79], a[48:51], v[100:103]\n" \
"buffer_load_dwordx4 v60, s[40:43], s16 offen offset:0 sc1 lds\n" \
"v_mfma_f32_16x16x32_bf16 v[104:107], a[72:75], a[52:55], v[104:107]\n" \
"s_add_u32 s61, s61, s6\n" \
"ds_read_b64_tr_b16 a[26:27], v10 offset:512\n" \
"s_lshl_b32 s51, s61, 13\n" \
"v_mfma_f32_16x16x32_bf16 v[108:111], a[76:79], a[52:55], v[108:111]\n" \
"s_lshl_b32 s53, s61, 3\n" \
"ds_read_b64_tr_b16 a[28:29], v11 offset:512\n" \
"s_add_u32 s53, s53, 94388224\n" \
"v_mfma_f32_16x16x32_bf16 v[112:115], a[72:75], a[56:59], v[112:115]\n" \
"s_and_b32 s62, s62, 2\n" \
"ds_read_b64_tr_b16 a[30:31], v12 offset:512\n" \
"s_lshl_b32 s62, s62, 12\n" \
"v_mfma_f32_16x16x32_bf16 v[116:119], a[76:79], a[56:59], v[116:119]\n" \
"buffer_load_dwordx4 v60, s[40:43], s16 offen offset:1024 sc1 lds\n" \
"v_mfma_f32_16x16x32_bf16 v[120:123], a[72:75], a[60:63], v[120:123]\n" \
"s_add_u32 s62, s62, 131072\n" \
"ds_read_b128 a[0:3], v5 offset:32768\n" \
"v_add_u32 v21, s62, v4\n" \
"v_mfma_f32_16x16x32_bf16 v[124:127], a[76:79], a[60:63], v[124:127]\n" \
"s_or_b32 s60, s7, %2\n" \
"ds_read_b128 a[4:7], v5 offset:34816\n" \
"v_mfma_f32_16x16x32_bf16 v[128:131], a[80:83], a[48:51], v[128:131]\n" \
"s_cmp_eq_u32 s60, 0\ns_cselect_b32 s60, 0xff800000, 0\n" \
"ds_read_b128 a[8:11], v5 offset:36864\n" \
"v_mfma_f32_16x16x32_bf16 v[132:135], a[84:87], a[48:51], v[132:135]\n" \
"buffer_load_dwordx4 v60, s[40:43], s16 offen offset:2048 sc1 lds\n" \
"v_mfma_f32_16x16x32_bf16 v[136:139], a[80:83], a[52:55], v[136:139]\n" \
"ds_read_b128 a[12:15], v5 offset:38912\n" \
"v_lshrrev_b32 v22, 6, v4\n" \
"v_mfma_f32_16x16x32_bf16 v[140:143], a[84:87], a[52:55], v[140:143]\n" \
"ds_read_b64_tr_b16 a[32:33], v9 offset:1024\n" \
"v_and_b32 v22, 12, v22\n" \
"v_mfma_f32_16x16x32_bf16 v[144:147], a[80:83], a[56:59], v[144:147]\n" \
"ds_read_b64_tr_b16 a[34:35], v10 offset:1024\n" \
"v_bfe_u32 v23, v4, 4, 4\n" \
"v_mfma_f32_16x16x32_bf16 v[148:151], a[84:87], a[56:59], v[148:151]\n" \
"buffer_load_dwordx4 v60, s[40:43], s16 offen offset:3072 sc1 lds\n" \
"v_mfma_f32_16x16x32_bf16 v[152:155], a[80:83], a[60:63], v[152:155]\n" \
"ds_read_b64_tr_b16 a[36:37], v11 offset:1024\n" \
"v_sub_u32 v22, v23, v22\n" \
"v_mfma_f32_16x16x32_bf16 v[156:159], a[84:87], a[60:63], v[156:159]\n" \
"ds_read_b64_tr_b16 a[38:39], v12 offset:1024\n" \
"v_subrev_u32 v240, 0, v22\n" \
"v_mfma_f32_16x16x32_bf16 v[160:163], a[88:91], a[48:51], v[160:163]\n" \
"s_add_u32 s16, s16, s9\n" \
"s_add_u32 m0, s22, 65536\n" \
"ds_read_b64_tr_b16 a[40:41], v9 offset:1536\n" \
"v_mfma_f32_16x16x32_bf16 v[164:167], a[92:95], a[48:51], v[164:167]\n" \
"buffer_load_dwordx4 v60, s[40:43], s16 offen offset:0 sc1 lds\n" \
"v_mfma_f32_16x16x32_bf16 v[168:171], a[88:91], a[52:55], v[168:171]\n" \
"ds_read_b64_tr_b16 a[42:43], v10 offset:1536\n" \
"v_ashrrev_i32 v240, 31, v240\n" \
"v_mfma_f32_16x16x32_bf16 v[172:175], a[92:95], a[52:55], v[172:175]\n" \
"ds_read_b64_tr_b16 a[44:45], v11 offset:1536\n" \
"v_and_b32 v240, 0xff800000, v240\n" \
"v_mfma_f32_16x16x32_bf16 v[176:179], a[88:91], a[56:59], v[176:179]\n" \
"ds_read_b64_tr_b16 a[46:47], v12 offset:1536\n" \
"v_and_b32 v24, s60, v240\n" \
"v_mfma_f32_16x16x32_bf16 v[180:183], a[92:95], a[56:59], v[180:183]\n" \
"buffer_load_dwordx4 v60, s[40:43], s16 offen offset:1024 sc1 lds\n" \
"v_mfma_f32_16x16x32_bf16 v[184:187], a[88:91], a[60:63], v[184:187]\n" \
"ds_read_b64_tr_b16 a[16:17], v9 offset:0\n" \
"v_mov_b32 v244, s60\n" \
"v_mfma_f32_16x16x32_bf16 v[188:191], a[92:95], a[60:63], v[188:191]\n" \
"ds_read_b64_tr_b16 a[18:19], v10 offset:0\n" \
"v_subrev_u32 v241, 1, v22\n" \
"v_mfma_f32_16x16x32_bf16 v[64:67], a[64:67], a[48:51], v[64:67]\n" \
"ds_read_b64_tr_b16 a[20:21], v11 offset:0\n" \
"v_ashrrev_i32 v241, 31, v241\n" \
"v_mfma_f32_16x16x32_bf16 v[68:71], a[68:71], a[48:51], v[68:71]\n" \
"buffer_load_dwordx4 v60, s[40:43], s16 offen offset:2048 sc1 lds\n" \
"v_mfma_f32_16x16x32_bf16 v[72:75], a[64:67], a[52:55], v[72:75]\n" \
"ds_read_b64_tr_b16 a[22:23], v12 offset:0\n" \
"v_and_b32 v241, 0xff800000, v241\n" \
"v_mfma_f32_16x16x32_bf16 v[76:79], a[68:71], a[52:55], v[76:79]\n" \
"v_and_b32 v25, s60, v241\n" \
"v_mov_b32 v245, s60\n" \
"v_mfma_f32_16x16x32_bf16 v[80:83], a[64:67], a[56:59], v[80:83]\n" \
"v_subrev_u32 v242, 2, v22\n" \
"v_ashrrev_i32 v242, 31, v242\n" \
"v_mfma_f32_16x16x32_bf16 v[84:87], a[68:71], a[56:59], v[84:87]\n" \
"buffer_load_dwordx4 v60, s[40:43], s16 offen offset:3072 sc1 lds\n" \
"v_mfma_f32_16x16x32_bf16 v[88:91], a[64:67], a[60:63], v[88:91]\n" \
"v_and_b32 v242, 0xff800000, v242\n" \
"v_and_b32 v26, s60, v242\n" \
"v_mfma_f32_16x16x32_bf16 v[92:95], a[68:71], a[60:63], v[92:95]\n" \
"s_waitcnt lgkmcnt(0)\n" \
"v_mov_b32 v246, s60\n" \
"v_subrev_u32 v243, 3, v22\n" \
"v_mfma_f32_16x16x32_bf16 v[96:99], a[24:27], a[0:3], v[96:99]\n" \
"s_add_u32 m0, s22, 81920\n" \
"ds_read_b64_tr_b16 a[72:73], v9 offset:8704\n" \
"v_ashrrev_i32 v243, 31, v243\n" \
"v_mfma_f32_16x16x32_bf16 v[100:103], a[28:31], a[0:3], v[100:103]\n" \
"buffer_load_dwordx4 v60, s[40:43], s17 offen offset:0 sc1 lds\n" \
"v_mfma_f32_16x16x32_bf16 v[104:107], a[24:27], a[4:7], v[104:107]\n" \
"ds_read_b64_tr_b16 a[74:75], v10 offset:8704\n" \
"v_and_b32 v243, 0xff800000, v243\n" \
"v_mfma_f32_16x16x32_bf16 v[108:111], a[28:31], a[4:7], v[108:111]\n" \
"ds_read_b64_tr_b16 a[76:77], v11 offset:8704\n" \
"v_and_b32 v27, s60, v243\n" \
"v_mfma_f32_16x16x32_bf16 v[112:115], a[24:27], a[8:11], v[112:115]\n" \
"ds_read_b64_tr_b16 a[78:79], v12 offset:8704\n" \
"v_mov_b32 v247, s60\n" \
"v_mfma_f32_16x16x32_bf16 v[116:119], a[28:31], a[8:11], v[116:119]\n" \
"buffer_load_dwordx4 v60, s[40:43], s17 offen offset:1024 sc1 lds\n" \
"v_mfma_f32_16x16x32_bf16 v[120:123], a[24:27], a[12:15], v[120:123]\n" \
"ds_read_b128 a[48:51], v6 offset:32768\n" \
"s_load_dwordx2 s[28:29], %0, 0x50\n" \
"v_mfma_f32_16x16x32_bf16 v[124:127], a[28:31], a[12:15], v[124:127]\n" \
"ds_read_b128 a[52:55], v6 offset:34816\n" \
"s_load_dwordx2 s[32:33], %0, 0x60\n" \
"v_mfma_f32_16x16x32_bf16 v[128:131], a[32:35], a[0:3], v[128:131]\n" \
"ds_read_b128 a[56:59], v6 offset:36864\n" \
"s_load_dwordx2 s[60:61], %0, 0x0\n" \
"v_mfma_f32_16x16x32_bf16 v[132:135], a[36:39], a[0:3], v[132:135]\n" \
"buffer_load_dwordx4 v60, s[40:43], s17 offen offset:2048 sc1 lds\n" \
"v_mfma_f32_16x16x32_bf16 v[136:139], a[32:35], a[4:7], v[136:139]\n" \
"s_lshl_b32 s79, %2, 11\n" \
"ds_read_b128 a[60:63], v6 offset:38912\n" \
"s_lshl_b32 s20, s89, 21\n" \
"v_mfma_f32_16x16x32_bf16 v[140:143], a[36:39], a[4:7], v[140:143]\n" \
"s_lshr_b32 s21, %1, 5\n" \
"ds_read_b64_tr_b16 a[80:81], v9 offset:9216\n" \
"s_lshl_b32 s21, s21, 8\n" \
"v_mfma_f32_16x16x32_bf16 v[144:147], a[32:35], a[8:11], v[144:147]\n" \
"s_add_u32 s18, s20, s21\n" \
"ds_read_b64_tr_b16 a[82:83], v10 offset:9216\n" \
"s_sub_u32 s30, 8388608, s18\n" \
"v_mfma_f32_16x16x32_bf16 v[148:151], a[36:39], a[8:11], v[148:151]\n" \
"buffer_load_dwordx4 v60, s[40:43], s17 offen offset:3072 sc1 lds\n" \
"v_mfma_f32_16x16x32_bf16 v[152:155], a[32:35], a[12:15], v[152:155]\n" \
"s_lshl_b32 s20, %2, 2\n" \
"ds_read_b64_tr_b16 a[84:85], v11 offset:9216\n" \
"s_lshl_b32 s21, %2, 1\n" \
"v_mfma_f32_16x16x32_bf16 v[156:159], a[36:39], a[12:15], v[156:159]\n" \
"ds_read_b64_tr_b16 a[86:87], v12 offset:9216\n" \
"v_bfe_u32 v22, v4, 6, 2\n" \
"v_mfma_f32_16x16x32_bf16 v[160:163], a[40:43], a[0:3], v[160:163]\n" \
"ds_read_b64_tr_b16 a[88:89], v9 offset:9728\n" \
"v_add_u32 v22, s20, v22\n" \
"v_mfma_f32_16x16x32_bf16 v[164:167], a[44:47], a[0:3], v[164:167]\n" \
"ds_read_b64_tr_b16 a[90:91], v10 offset:9728\n" \
"v_bfe_u32 v23, v4, 8, 1\n" \
"v_mfma_f32_16x16x32_bf16 v[168:171], a[40:43], a[4:7], v[168:171]\n" \
"ds_read_b64_tr_b16 a[92:93], v11 offset:9728\n" \
"v_lshl_add_u32 v22, v23, 4, v22\n" \
"v_mfma_f32_16x16x32_bf16 v[172:175], a[44:47], a[4:7], v[172:175]\n" \
"ds_read_b64_tr_b16 a[94:95], v12 offset:9728\n" \
"v_lshlrev_b32 v22, 11, v22\n" \
"v_mfma_f32_16x16x32_bf16 v[176:179], a[40:43], a[8:11], v[176:179]\n" \
"ds_read_b64_tr_b16 a[64:65], v9 offset:8192\n" \
"v_add_u32 v23, s21, v23\n" \
"v_mfma_f32_16x16x32_bf16 v[180:183], a[44:47], a[8:11], v[180:183]\n" \
"ds_read_b64_tr_b16 a[66:67], v10 offset:8192\n" \
"v_lshrrev_b32 v28, 4, v4\n" \
"v_mfma_f32_16x16x32_bf16 v[184:187], a[40:43], a[12:15], v[184:187]\n" \
"ds_read_b64_tr_b16 a[68:69], v11 offset:8192\n" \
"v_xor_b32 v23, v23, v28\n" \
"v_mfma_f32_16x16x32_bf16 v[188:191], a[44:47], a[12:15], v[188:191]\n" \
"ds_read_b64_tr_b16 a[70:71], v12 offset:8192\n" \
"v_and_b32 v23, 3, v23\n" \
"v_mfma_f32_16x16x32_bf16 v[64:67], a[16:19], a[0:3], v[64:67]\n" \
"v_lshrrev_b32 v28, 9, v4\n" \
"v_lshl_add_u32 v23, v28, 2, v23\n" \
"v_mfma_f32_16x16x32_bf16 v[68:71], a[20:23], a[0:3], v[68:71]\n" \
"s_lshl_b32 s20, %2, 9\n" \
"v_lshl_add_u32 v30, v23, 4, v22\n" \
"s_add_u32 s20, s20, 0x20000\n" \
"v_mfma_f32_16x16x32_bf16 v[72:75], a[16:19], a[4:7], v[72:75]\n" \
"v_add_u32 v13, s20, v9\n" \
"v_add_u32 v14, s20, v10\n" \
"v_mfma_f32_16x16x32_bf16 v[76:79], a[20:23], a[4:7], v[76:79]\n" \
"v_add_u32 v15, s20, v11\n" \
"v_add_u32 v16, s20, v12\n" \
"v_mfma_f32_16x16x32_bf16 v[80:83], a[16:19], a[8:11], v[80:83]\n" \
"v_lshlrev_b32 v20, 23, v4\n" \
"v_mfma_f32_16x16x32_bf16 v[84:87], a[20:23], a[8:11], v[84:87]\n" \
"v_mfma_f32_16x16x32_bf16 v[88:91], a[16:19], a[12:15], v[88:91]\n" \
"s_waitcnt vmcnt(12) lgkmcnt(0)\n" \
"v_mfma_f32_16x16x32_bf16 v[92:95], a[20:23], a[12:15], v[92:95]\n" \
"s_barrier\n" \
"v_mfma_f32_16x16x32_bf16 v[96:99], a[72:75], a[48:51], v[96:99]\n" \
"s_add_u32 s16, s16, s9\n" \
"s_add_u32 m0, s22, 98304\n" \
"ds_read_b64_tr_b16 a[24:25], v9 offset:16896\n" \
"v_mfma_f32_16x16x32_bf16 v[100:103], a[76:79], a[48:51], v[100:103]\n" \
"buffer_load_dwordx4 v60, s[40:43], s16 offen offset:0 sc1 lds\n" \
"v_mfma_f32_16x16x32_bf16 v[104:107], a[72:75], a[52:55], v[104:107]\n" \
"s_add_u32 s28, s28, s18\ns_addc_u32 s29, s29, 0\n" \
"ds_read_b64_tr_b16 a[26:27], v10 offset:16896\n" \
"v_mfma_f32_16x16x32_bf16 v[108:111], a[76:79], a[52:55], v[108:111]\n" \
"s_and_b32 s29, s29, 0xffff\n" \
"ds_read_b64_tr_b16 a[28:29], v11 offset:16896\n" \
"s_and_b32 s33, s33, 0xffff\n" \
"v_mfma_f32_16x16x32_bf16 v[112:115], a[72:75], a[56:59], v[112:115]\n" \
"s_mov_b32 s34, 4194304\n" \
"ds_read_b64_tr_b16 a[30:31], v12 offset:16896\n" \
"s_mov_b32 s35, 0x00020000\n" \
"v_mfma_f32_16x16x32_bf16 v[116:119], a[76:79], a[56:59], v[116:119]\n" \
"buffer_load_dwordx4 v60, s[40:43], s16 offen offset:1024 sc1 lds\n" \
"v_mfma_f32_16x16x32_bf16 v[120:123], a[72:75], a[60:63], v[120:123]\n" \
"ds_read_b128 a[0:3], v7 offset:0\n" \
"v_mfma_f32_16x16x32_bf16 v[124:127], a[76:79], a[60:63], v[124:127]\n" \
"ds_read_b128 a[4:7], v7 offset:2048\n" \
"v_mfma_f32_16x16x32_bf16 v[128:131], a[80:83], a[48:51], v[128:131]\n" \
"ds_read_b128 a[8:11], v7 offset:4096\n" \
"v_mfma_f32_16x16x32_bf16 v[132:135], a[84:87], a[48:51], v[132:135]\n" \
"buffer_load_dwordx4 v60, s[40:43], s16 offen offset:2048 sc1 lds\n" \
"v_mfma_f32_16x16x32_bf16 v[136:139], a[80:83], a[52:55], v[136:139]\n" \
"ds_read_b128 a[12:15], v7 offset:6144\n" \
"v_mfma_f32_16x16x32_bf16 v[140:143], a[84:87], a[52:55], v[140:143]\n" \
"ds_read_b64_tr_b16 a[32:33], v9 offset:17408\n" \
"v_mfma_f32_16x16x32_bf16 v[144:147], a[80:83], a[56:59], v[144:147]\n" \
"ds_read_b64_tr_b16 a[34:35], v10 offset:17408\n" \
"v_mfma_f32_16x16x32_bf16 v[148:151], a[84:87], a[56:59], v[148:151]\n" \
"buffer_load_dwordx4 v60, s[40:43], s16 offen offset:3072 sc1 lds\n" \
"v_mfma_f32_16x16x32_bf16 v[152:155], a[80:83], a[60:63], v[152:155]\n" \
"ds_read_b64_tr_b16 a[36:37], v11 offset:17408\n" \
"v_mfma_f32_16x16x32_bf16 v[156:159], a[84:87], a[60:63], v[156:159]\n" \
"ds_read_b64_tr_b16 a[38:39], v12 offset:17408\n" \
"v_mfma_f32_16x16x32_bf16 v[160:163], a[88:91], a[48:51], v[160:163]\n" \
"s_add_u32 s17, s17, s19\n" \
"s_add_u32 m0, s22, 114688\n" \
"ds_read_b64_tr_b16 a[40:41], v9 offset:17920\n" \
"v_mfma_f32_16x16x32_bf16 v[164:167], a[92:95], a[48:51], v[164:167]\n" \
"buffer_load_dwordx4 v60, s[40:43], s17 offen offset:0 sc1 lds\n" \
"v_mfma_f32_16x16x32_bf16 v[168:171], a[88:91], a[52:55], v[168:171]\n" \
"ds_read_b64_tr_b16 a[42:43], v10 offset:17920\n" \
"v_mfma_f32_16x16x32_bf16 v[172:175], a[92:95], a[52:55], v[172:175]\n" \
"ds_read_b64_tr_b16 a[44:45], v11 offset:17920\n" \
"v_mfma_f32_16x16x32_bf16 v[176:179], a[88:91], a[56:59], v[176:179]\n" \
"ds_read_b64_tr_b16 a[46:47], v12 offset:17920\n" \
"v_mfma_f32_16x16x32_bf16 v[180:183], a[92:95], a[56:59], v[180:183]\n" \
"buffer_load_dwordx4 v60, s[40:43], s17 offen offset:1024 sc1 lds\n" \
"v_mfma_f32_16x16x32_bf16 v[184:187], a[88:91], a[60:63], v[184:187]\n" \
"ds_read_b64_tr_b16 a[16:17], v9 offset:16384\n" \
"v_mfma_f32_16x16x32_bf16 v[188:191], a[92:95], a[60:63], v[188:191]\n" \
"ds_read_b64_tr_b16 a[18:19], v10 offset:16384\n" \
"v_mfma_f32_16x16x32_bf16 v[64:67], a[64:67], a[48:51], v[64:67]\n" \
"ds_read_b64_tr_b16 a[20:21], v11 offset:16384\n" \
"v_mfma_f32_16x16x32_bf16 v[68:71], a[68:71], a[48:51], v[68:71]\n" \
"buffer_load_dwordx4 v60, s[40:43], s17 offen offset:2048 sc1 lds\n" \
"v_mfma_f32_16x16x32_bf16 v[72:75], a[64:67], a[52:55], v[72:75]\n" \
"ds_read_b64_tr_b16 a[22:23], v12 offset:16384\n" \
"v_mfma_f32_16x16x32_bf16 v[76:79], a[68:71], a[52:55], v[76:79]\n" \
"v_mfma_f32_16x16x32_bf16 v[80:83], a[64:67], a[56:59], v[80:83]\n" \
"v_mfma_f32_16x16x32_bf16 v[84:87], a[68:71], a[56:59], v[84:87]\n" \
"buffer_load_dwordx4 v60, s[40:43], s17 offen offset:3072 sc1 lds\n" \
"v_mfma_f32_16x16x32_bf16 v[88:91], a[64:67], a[60:63], v[88:91]\n" \
"v_mfma_f32_16x16x32_bf16 v[92:95], a[68:71], a[60:63], v[92:95]\n" \
"s_waitcnt lgkmcnt(0)\n" \
"s_mov_b64 exec, 0\ns_cmp_eq_u32 %2, 0\ns_cbranch_scc1 LH15_%=\ns_cmp_eq_u32 %2, 1\ns_cbranch_scc1 LH16_%=\ns_cmp_eq_u32 %2, 2\ns_cbranch_scc1 LH17_%=\ns_cmp_eq_u32 %2, 3\ns_cbranch_scc1 LH18_%=\nLHR1_%=:\ns_mov_b64 exec, -1\n" \
"v_mfma_f32_16x16x32_bf16 v[64:67], a[16:19], a[0:3], v[64:67]\n" \
"ds_read_b128 a[48:51], v8 offset:0\n" \
"v_mfma_f32_16x16x32_bf16 v[68:71], a[20:23], a[0:3], v[68:71]\n" \
"ds_read_b128 a[52:55], v8 offset:2048\n" \
"v_mfma_f32_16x16x32_bf16 v[72:75], a[16:19], a[4:7], v[72:75]\n" \
"ds_read_b128 a[56:59], v8 offset:4096\n" \
"v_mfma_f32_16x16x32_bf16 v[76:79], a[20:23], a[4:7], v[76:79]\n" \
"ds_read_b128 a[60:63], v8 offset:6144\n" \
"v_mfma_f32_16x16x32_bf16 v[80:83], a[16:19], a[8:11], v[80:83]\n" \
"ds_read_b64_tr_b16 a[64:65], v9 offset:24576\n" \
"v_mfma_f32_16x16x32_bf16 v[84:87], a[20:23], a[8:11], v[84:87]\n" \
"ds_read_b64_tr_b16 a[66:67], v10 offset:24576\n" \
"v_mfma_f32_16x16x32_bf16 v[88:91], a[16:19], a[12:15], v[88:91]\n" \
"ds_read_b64_tr_b16 a[68:69], v11 offset:24576\n" \
"v_mfma_f32_16x16x32_bf16 v[92:95], a[20:23], a[12:15], v[92:95]\n" \
"ds_read_b64_tr_b16 a[70:71], v12 offset:24576\n" \
"v_mfma_f32_16x16x32_bf16 v[96:99], a[24:27], a[0:3], v[96:99]\n" \
"ds_read_b64_tr_b16 a[72:73], v9 offset:25088\n" \
"v_mfma_f32_16x16x32_bf16 v[100:103], a[28:31], a[0:3], v[100:103]\n" \
"ds_read_b64_tr_b16 a[74:75], v10 offset:25088\n" \
"v_mfma_f32_16x16x32_bf16 v[104:107], a[24:27], a[4:7], v[104:107]\n" \
"ds_read_b64_tr_b16 a[76:77], v11 offset:25088\n" \
"v_mfma_f32_16x16x32_bf16 v[108:111], a[28:31], a[4:7], v[108:111]\n" \
"ds_read_b64_tr_b16 a[78:79], v12 offset:25088\n" \
"v_mfma_f32_16x16x32_bf16 v[112:115], a[24:27], a[8:11], v[112:115]\n" \
"ds_read_b64_tr_b16 a[80:81], v9 offset:25600\n" \
"v_mfma_f32_16x16x32_bf16 v[116:119], a[28:31], a[8:11], v[116:119]\n" \
"ds_read_b64_tr_b16 a[82:83], v10 offset:25600\n" \
"v_mfma_f32_16x16x32_bf16 v[120:123], a[24:27], a[12:15], v[120:123]\n" \
"ds_read_b64_tr_b16 a[84:85], v11 offset:25600\n" \
"v_mfma_f32_16x16x32_bf16 v[124:127], a[28:31], a[12:15], v[124:127]\n" \
"ds_read_b64_tr_b16 a[86:87], v12 offset:25600\n" \
"s_waitcnt lgkmcnt(8)\n" \
"v_mfma_f32_16x16x32_bf16 v[64:67], a[64:67], a[48:51], v[64:67]\n" \
"s_lshl_b32 s25, s45, 2\n" \
"ds_read_b64_tr_b16 a[88:89], v9 offset:26112\n" \
"s_and_b32 s25, s25, 32\n" \
"v_mfma_f32_16x16x32_bf16 v[68:71], a[68:71], a[48:51], v[68:71]\n" \
"LH14_%=:\ns_cbranch_execz LHR0_%=\n" \
"s_add_u32 s25, s25, %2\n" \
"ds_read_b64_tr_b16 a[90:91], v10 offset:26112\n" \
"v_mfma_f32_16x16x32_bf16 v[72:75], a[64:67], a[52:55], v[72:75]\n" \
"s_cmp_ge_u32 s25, 34\ns_cselect_b64 vcc, -1, 0\n" \
"ds_read_b64_tr_b16 a[92:93], v11 offset:26112\n" \
"v_mfma_f32_16x16x32_bf16 v[76:79], a[68:71], a[52:55], v[76:79]\n" \
"ds_read_b64_tr_b16 a[94:95], v12 offset:26112\n" \
"v_mfma_f32_16x16x32_bf16 v[80:83], a[64:67], a[56:59], v[80:83]\n" \
"v_cvt_pk_bf16_f32 v32, v64, v65\n" \
"v_cvt_pk_bf16_f32 v33, v66, v67\n" \
"v_mfma_f32_16x16x32_bf16 v[84:87], a[68:71], a[56:59], v[84:87]\n" \
"v_cvt_pk_bf16_f32 v34, v68, v69\n" \
"v_cvt_pk_bf16_f32 v35, v70, v71\n" \
"v_mfma_f32_16x16x32_bf16 v[88:91], a[64:67], a[60:63], v[88:91]\n" \
"v_cvt_pk_bf16_f32 v48, v72, v73\n" \
"v_mfma_f32_16x16x32_bf16 v[92:95], a[68:71], a[60:63], v[92:95]\n" \
"s_waitcnt vmcnt(16) lgkmcnt(0)\n" \
"v_cvt_pk_bf16_f32 v49, v74, v75\n" \
"v_cvt_pk_bf16_f32 v50, v76, v77\n" \
"v_mfma_f32_16x16x32_bf16 v[96:99], a[72:75], a[48:51], v[96:99]\n" \
"s_barrier\n" \
"v_cvt_pk_bf16_f32 v51, v78, v79\n" \
"v_cvt_pk_bf16_f32 v192, v80, v81\n" \
"v_mfma_f32_16x16x32_bf16 v[100:103], a[76:79], a[48:51], v[100:103]\n" \
"v_add_u32 v5, 0x2000, v4\n" \
"v_mov_b32 v6, v60\n" \
"v_mfma_f32_16x16x32_bf16 v[104:107], a[72:75], a[52:55], v[104:107]\n" \
"s_add_u32 s16, s16, s9\n" \
"v_add_u32 v7, 0x1000, v4\n" \
"s_add_u32 m0, s49, 0\n" \
"v_mfma_f32_16x16x32_bf16 v[108:111], a[76:79], a[52:55], v[108:111]\n" \
"buffer_load_dwordx4 v6, s[40:43], s16 offen offset:0 sc1 lds\n" \
"v_mfma_f32_16x16x32_bf16 v[112:115], a[72:75], a[56:59], v[112:115]\n" \
"ds_read_b128 a[128:131], v5 offset:32768\n" \
"v_cvt_pk_bf16_f32 v193, v82, v83\n" \
"v_mfma_f32_16x16x32_bf16 v[116:119], a[76:79], a[56:59], v[116:119]\n" \
"s_cbranch_vccz LQW1_%=\nds_write_b128 v21, v[32:35] offset:16384\nLQW1_%=:\n" \
"v_mfma_f32_16x16x32_bf16 v[120:123], a[72:75], a[60:63], v[120:123]\n" \
"s_cbranch_vccz LQW2_%=\nds_write_b128 v21, v[48:51] offset:20480\nLQW2_%=:\n" \
"v_mfma_f32_16x16x32_bf16 v[124:127], a[76:79], a[60:63], v[124:127]\n" \
"buffer_load_dwordx4 v6, s[40:43], s16 offen offset:1024 sc1 lds\n" \
"v_mfma_f32_16x16x32_bf16 v[128:131], a[32:35], a[0:3], v[128:131]\n" \
"ds_read_b128 a[132:135], v5 offset:33792\n" \
"v_cvt_pk_bf16_f32 v194, v84, v85\n" \
"v_mfma_f32_16x16x32_bf16 v[132:135], a[36:39], a[0:3], v[132:135]\n" \
"v_cvt_pk_bf16_f32 v195, v86, v87\n" \
"v_cvt_pk_bf16_f32 v208, v88, v89\n" \
"v_mfma_f32_16x16x32_bf16 v[136:139], a[32:35], a[4:7], v[136:139]\n" \
"s_cbranch_vccz LQW3_%=\nds_write_b128 v21, v[192:195] offset:0\nLQW3_%=:\n" \
"v_mfma_f32_16x16x32_bf16 v[140:143], a[36:39], a[4:7], v[140:143]\n" \
"buffer_load_dwordx4 v6, s[40:43], s16 offen offset:2048 sc1 lds\n" \
"v_mfma_f32_16x16x32_bf16 v[144:147], a[32:35], a[8:11], v[144:147]\n" \
"ds_read_b128 a[136:139], v5 offset:34816\n" \
"v_cvt_pk_bf16_f32 v209, v90, v91\n" \
"v_mfma_f32_16x16x32_bf16 v[148:151], a[36:39], a[8:11], v[148:151]\n" \
"v_cvt_pk_bf16_f32 v210, v92, v93\n" \
"v_cvt_pk_bf16_f32 v211, v94, v95\n" \
"v_mfma_f32_16x16x32_bf16 v[152:155], a[32:35], a[12:15], v[152:155]\n" \
"s_cbranch_vccz LQW4_%=\nds_write_b128 v21, v[208:211] offset:4096\nLQW4_%=:\n" \
"v_mfma_f32_16x16x32_bf16 v[156:159], a[36:39], a[12:15], v[156:159]\n" \
"buffer_load_dwordx4 v6, s[40:43], s16 offen offset:3072 sc1 lds\n" \
"v_mfma_f32_16x16x32_bf16 v[128:131], a[80:83], a[48:51], v[128:131]\n" \
"s_add_u32 s17, s17, s19\n" \
"ds_read_b128 a[144:147], v5 offset:36864\n" \
"s_add_u32 m0, s49, 16384\n" \
"v_mfma_f32_16x16x32_bf16 v[132:135], a[84:87], a[48:51], v[132:135]\n" \
"v_cvt_pk_bf16_f32 v36, v96, v97\n" \
"v_cvt_pk_bf16_f32 v37, v98, v99\n" \
"v_mfma_f32_16x16x32_bf16 v[136:139], a[80:83], a[52:55], v[136:139]\n" \
"v_cvt_pk_bf16_f32 v38, v100, v101\n" \
"v_cvt_pk_bf16_f32 v39, v102, v103\n" \
"v_mfma_f32_16x16x32_bf16 v[140:143], a[84:87], a[52:55], v[140:143]\n" \
"buffer_load_dwordx4 v6, s[40:43], s17 offen offset:0 sc1 lds\n" \
"v_mfma_f32_16x16x32_bf16 v[144:147], a[80:83], a[56:59], v[144:147]\n" \
"ds_read_b128 a[140:143], v5 offset:35840\n" \
"v_cvt_pk_bf16_f32 v52, v104, v105\n" \
"v_mfma_f32_16x16x32_bf16 v[148:151], a[84:87], a[56:59], v[148:151]\n" \
"s_cbranch_vccz LQW5_%=\nds_write_b128 v21, v[36:39] offset:17408\nLQW5_%=:\n" \
"v_mfma_f32_16x16x32_bf16 v[152:155], a[80:83], a[60:63], v[152:155]\n" \
"v_cvt_pk_bf16_f32 v53, v106, v107\n" \
"v_cvt_pk_bf16_f32 v54, v108, v109\n" \
"v_mfma_f32_16x16x32_bf16 v[156:159], a[84:87], a[60:63], v[156:159]\n" \
"buffer_load_dwordx4 v6, s[40:43], s17 offen offset:1024 sc1 lds\n" \
"v_mfma_f32_16x16x32_bf16 v[160:163], a[40:43], a[0:3], v[160:163]\n" \
"ds_read_b128 a[148:151], v5 offset:37888\n" \
"v_cvt_pk_bf16_f32 v55, v110, v111\n" \
"v_mfma_f32_16x16x32_bf16 v[164:167], a[44:47], a[0:3], v[164:167]\n" \
"s_cbranch_vccz LQW6_%=\nds_write_b128 v21, v[52:55] offset:21504\nLQW6_%=:\n" \
"v_mfma_f32_16x16x32_bf16 v[168:171], a[40:43], a[4:7], v[168:171]\n" \
"v_cvt_pk_bf16_f32 v196, v112, v113\n" \
"v_cvt_pk_bf16_f32 v197, v114, v115\n" \
"v_mfma_f32_16x16x32_bf16 v[172:175], a[44:47], a[4:7], v[172:175]\n" \
"buffer_load_dwordx4 v6, s[40:43], s17 offen offset:2048 sc1 lds\n" \
"v_mfma_f32_16x16x32_bf16 v[176:179], a[40:43], a[8:11], v[176:179]\n" \
"ds_read_b128 a[152:155], v5 offset:38912\n" \
"v_cvt_pk_bf16_f32 v198, v116, v117\n" \
"v_mfma_f32_16x16x32_bf16 v[180:183], a[44:47], a[8:11], v[180:183]\n" \
"v_cvt_pk_bf16_f32 v199, v118, v119\n" \
"v_cvt_pk_bf16_f32 v212, v120, v121\n" \
"v_mfma_f32_16x16x32_bf16 v[184:187], a[40:43], a[12:15], v[184:187]\n" \
"s_cbranch_vccz LQW7_%=\nds_write_b128 v21, v[196:199] offset:1024\nLQW7_%=:\n" \
"v_mfma_f32_16x16x32_bf16 v[188:191], a[44:47], a[12:15], v[188:191]\n" \
"buffer_load_dwordx4 v6, s[40:43], s17 offen offset:3072 sc1 lds\n" \
"v_mfma_f32_16x16x32_bf16 v[160:163], a[88:91], a[48:51], v[160:163]\n" \
"ds_read_b128 a[156:159], v5 offset:39936\n" \
"v_cvt_pk_bf16_f32 v213, v122, v123\n" \
"v_mfma_f32_16x16x32_bf16 v[164:167], a[92:95], a[48:51], v[164:167]\n" \
"v_cvt_pk_bf16_f32 v214, v124, v125\n" \
"v_cvt_pk_bf16_f32 v215, v126, v127\n" \
"v_mfma_f32_16x16x32_bf16 v[168:171], a[88:91], a[52:55], v[168:171]\n" \
"s_cbranch_vccz LQW8_%=\nds_write_b128 v21, v[212:215] offset:5120\nLQW8_%=:\n" \
"v_mfma_f32_16x16x32_bf16 v[172:175], a[92:95], a[52:55], v[172:175]\n" \
"v_cvt_pk_bf16_f32 v40, v128, v129\n" \
"v_cvt_pk_bf16_f32 v41, v130, v131\n" \
"v_mfma_f32_16x16x32_bf16 v[176:179], a[88:91], a[56:59], v[176:179]\n" \
"v_cvt_pk_bf16_f32 v42, v132, v133\n" \
"v_cvt_pk_bf16_f32 v43, v134, v135\n" \
"v_mfma_f32_16x16x32_bf16 v[180:183], a[92:95], a[56:59], v[180:183]\n" \
"s_cbranch_vccz LQW9_%=\nds_write_b128 v21, v[40:43] offset:18432\nLQW9_%=:\n" \
"v_mfma_f32_16x16x32_bf16 v[184:187], a[88:91], a[60:63], v[184:187]\n" \
"v_xor_b32 v5, 0x10000, v5\n" \
"v_cvt_pk_bf16_f32 v56, v136, v137\n" \
"v_mfma_f32_16x16x32_bf16 v[188:191], a[92:95], a[60:63], v[188:191]\n" \
"s_waitcnt lgkmcnt(0)\n" \
"v_cvt_pk_bf16_f32 v57, v138, v139\n" \
"v_cvt_pk_bf16_f32 v58, v140, v141\n" \
"v_mfma_f32_16x16x32_bf16 v[64:67], a[128:131], v[32:35], v[24:27]\n" \
"v_cvt_pk_bf16_f32 v59, v142, v143\n" \
"v_mfma_f32_16x16x32_bf16 v[72:75], a[128:131], v[48:51], 0\n" \
"s_waitcnt vmcnt(16)\n" \
"s_cbranch_vccz LQW10_%=\nds_write_b128 v21, v[56:59] offset:22528\nLQW10_%=:\n" \
"v_mfma_f32_16x16x32_bf16 v[80:83], a[128:131], v[192:195], 0\n" \
"s_barrier\n" \
"v_cvt_pk_bf16_f32 v200, v144, v145\n" \
"v_cvt_pk_bf16_f32 v201, v146, v147\n" \
"v_mfma_f32_16x16x32_bf16 v[88:91], a[128:131], v[208:211], 0\n" \
"v_cvt_pk_bf16_f32 v202, v148, v149\n" \
"v_cvt_pk_bf16_f32 v203, v150, v151\n" \
"v_mfma_f32_16x16x32_bf16 v[64:67], a[132:135], v[36:39], v[64:67]\n" \
"s_cbranch_vccz LQW11_%=\nds_write_b128 v21, v[200:203] offset:2048\nLQW11_%=:\n" \
"v_mfma_f32_16x16x32_bf16 v[72:75], a[132:135], v[52:55], v[72:75]\n" \
"ds_read_b128 a[128:131], v5 offset:0\n" \
"v_cvt_pk_bf16_f32 v216, v152, v153\n" \
"v_mfma_f32_16x16x32_bf16 v[80:83], a[132:135], v[196:199], v[80:83]\n" \
"v_cvt_pk_bf16_f32 v217, v154, v155\n" \
"v_cvt_pk_bf16_f32 v218, v156, v157\n" \
"v_mfma_f32_16x16x32_bf16 v[88:91], a[132:135], v[212:215], v[88:91]\n" \
"v_cvt_pk_bf16_f32 v219, v158, v159\n" \
"v_cvt_pk_bf16_f32 v44, v160, v161\n" \
"v_mfma_f32_16x16x32_bf16 v[64:67], a[136:139], v[40:43], v[64:67]\n" \
"s_cbranch_vccz LQW12_%=\nds_write_b128 v21, v[216:219] offset:6144\nLQW12_%=:\n" \
"v_mfma_f32_16x16x32_bf16 v[72:75], a[136:139], v[56:59], v[72:75]\n" \
"ds_read_b128 a[132:135], v5 offset:1024\n" \
"v_cvt_pk_bf16_f32 v45, v162, v163\n" \
"v_mfma_f32_16x16x32_bf16 v[80:83], a[136:139], v[200:203], v[80:83]\n" \
"v_cvt_pk_bf16_f32 v46, v164, v165\n" \
"v_cvt_pk_bf16_f32 v47, v166, v167\n" \
"v_mfma_f32_16x16x32_bf16 v[88:91], a[136:139], v[216:219], v[88:91]\n" \
"s_cbranch_vccz LQW13_%=\nds_write_b128 v21, v[44:47] offset:19456\nLQW13_%=:\n" \
"v_mfma_f32_16x16x32_bf16 v[68:71], a[144:147], v[32:35], v[244:247]\n" \
"v_cvt_pk_bf16_f32 v60, v168, v169\n" \
"v_cvt_pk_bf16_f32 v61, v170, v171\n" \
"v_mfma_f32_16x16x32_bf16 v[76:79], a[144:147], v[48:51], v[24:27]\n" \
"ds_read_b128 a[136:139], v5 offset:2048\n" \
"v_cvt_pk_bf16_f32 v62, v172, v173\n" \
"v_mfma_f32_16x16x32_bf16 v[84:87], a[144:147], v[192:195], 0\n" \
"v_cvt_pk_bf16_f32 v63, v174, v175\n" \
"v_cvt_pk_bf16_f32 v204, v176, v177\n" \
"v_mfma_f32_16x16x32_bf16 v[92:95], a[144:147], v[208:211], 0\n" \
"s_cbranch_vccz LQW14_%=\nds_write_b128 v21, v[60:63] offset:23552\nLQW14_%=:\n" \
"v_cvt_pk_bf16_f32 v205, v178, v179\n" \
"v_cvt_pk_bf16_f32 v206, v180, v181\n" \
"v_cvt_pk_bf16_f32 v207, v182, v183\n" \
"v_mfma_f32_16x16x32_bf16 v[64:67], a[140:143], v[44:47], v[64:67]\n" \
"s_cbranch_vccz LQW15_%=\nds_write_b128 v21, v[204:207] offset:3072\nLQW15_%=:\n" \
"v_cvt_pk_bf16_f32 v220, v184, v185\n" \
"v_cvt_pk_bf16_f32 v221, v186, v187\n" \
"v_cvt_pk_bf16_f32 v222, v188, v189\n" \
"v_cvt_pk_bf16_f32 v223, v190, v191\n" \
"v_mfma_f32_16x16x32_bf16 v[72:75], a[140:143], v[60:63], v[72:75]\n" \
"ds_read_b128 a[144:147], v5 offset:4096\n" \
"v_accvgpr_write_b32 a64, v192\n" \
"v_mfma_f32_16x16x32_bf16 v[80:83], a[140:143], v[204:207], v[80:83]\n" \
"s_cbranch_vccz LQW16_%=\nds_write_b128 v21, v[220:223] offset:7168\nLQW16_%=:\n" \
"v_mfma_f32_16x16x32_bf16 v[88:91], a[140:143], v[220:223], v[88:91]\n" \
"v_exp_f32 v64, v64\n" \
"v_mfma_f32_16x16x32_bf16 v[68:71], a[148:151], v[36:39], v[68:71]\n" \
"v_exp_f32 v65, v65\n" \
"v_mfma_f32_16x16x32_bf16 v[76:79], a[148:151], v[52:55], v[76:79]\n" \
"ds_read_b128 a[140:143], v5 offset:3072\n" \
"v_accvgpr_write_b32 a65, v193\n" \
"v_mfma_f32_16x16x32_bf16 v[84:87], a[148:151], v[196:199], v[84:87]\n" \
"v_exp_f32 v66, v66\n" \
"v_mfma_f32_16x16x32_bf16 v[92:95], a[148:151], v[212:215], v[92:95]\n" \
"v_exp_f32 v67, v67\n" \
"v_mfma_f32_16x16x32_bf16 v[68:71], a[152:155], v[40:43], v[68:71]\n" \
"v_exp_f32 v72, v72\n" \
"v_mfma_f32_16x16x32_bf16 v[76:79], a[152:155], v[56:59], v[76:79]\n" \
"ds_read_b128 a[148:151], v5 offset:5120\n" \
"v_accvgpr_write_b32 a66, v194\n" \
"v_mfma_f32_16x16x32_bf16 v[84:87], a[152:155], v[200:203], v[84:87]\n" \
"v_exp_f32 v73, v73\n" \
"v_mfma_f32_16x16x32_bf16 v[92:95], a[152:155], v[216:219], v[92:95]\n" \
"v_exp_f32 v74, v74\n" \
"v_mfma_f32_16x16x32_bf16 v[68:71], a[156:159], v[44:47], v[68:71]\n" \
"v_exp_f32 v75, v75\n" \
"v_mfma_f32_16x16x32_bf16 v[76:79], a[156:159], v[60:63], v[76:79]\n" \
"v_exp_f32 v80, v80\n" \
"v_mfma_f32_16x16x32_bf16 v[84:87], a[156:159], v[204:207], v[84:87]\n" \
"v_exp_f32 v81, v81\n" \
"v_mfma_f32_16x16x32_bf16 v[92:95], a[156:159], v[220:223], v[92:95]\n" \
"s_waitcnt lgkmcnt(0)\n" \
"v_exp_f32 v82, v82\n" \
"v_mfma_f32_16x16x32_bf16 v[96:99], a[128:131], v[32:35], v[244:247]\n" \
"v_exp_f32 v83, v83\n" \
"v_mfma_f32_16x16x32_bf16 v[104:107], a[128:131], v[48:51], v[244:247]\n" \
"s_add_u32 s16, s16, s9\n" \
"v_exp_f32 v88, v88\n" \
"v_mfma_f32_16x16x32_bf16 v[112:115], a[128:131], v[192:195], v[24:27]\n" \
"ds_read_b128 a[152:155], v5 offset:6144\n" \
"v_cvt_pk_bf16_f32 v128, v64, v65\n" \
"v_mfma_f32_16x16x32_bf16 v[120:123], a[128:131], v[208:211], 0\n" \
"s_add_u32 m0, s49, 32768\n" \
"v_exp_f32 v89, v89\n" \
"v_mfma_f32_16x16x32_bf16 v[96:99], a[132:135], v[36:39], v[96:99]\n" \
"buffer_load_dwordx4 v6, s[40:43], s16 offen offset:0 sc1 lds\n" \
"v_mfma_f32_16x16x32_bf16 v[104:107], a[132:135], v[52:55], v[104:107]\n" \
"v_exp_f32 v90, v90\n" \
"v_mfma_f32_16x16x32_bf16 v[112:115], a[132:135], v[196:199], v[112:115]\n" \
"ds_read_b128 a[156:159], v5 offset:7168\n" \
"v_cvt_pk_bf16_f32 v129, v66, v67\n" \
"v_mfma_f32_16x16x32_bf16 v[120:123], a[132:135], v[212:215], v[120:123]\n" \
"s_waitcnt lgkmcnt(4)\n" \
"v_exp_f32 v91, v91\n" \
"v_mfma_f32_16x16x32_bf16 v[96:99], a[136:139], v[40:43], v[96:99]\n" \
"v_exp_f32 v68, v68\n" \
"v_mfma_f32_16x16x32_bf16 v[104:107], a[136:139], v[56:59], v[104:107]\n" \
"v_exp_f32 v69, v69\n" \
"v_mfma_f32_16x16x32_bf16 v[112:115], a[136:139], v[200:203], v[112:115]\n" \
"ds_read_b128 a[128:131], v5 offset:16384\n" \
"v_cvt_pk_bf16_f32 v132, v72, v73\n" \
"v_mfma_f32_16x16x32_bf16 v[120:123], a[136:139], v[216:219], v[120:123]\n" \
"v_exp_f32 v70, v70\n" \
"v_mfma_f32_16x16x32_bf16 v[96:99], a[140:143], v[44:47], v[96:99]\n" \
"buffer_load_dwordx4 v6, s[40:43], s16 offen offset:1024 sc1 lds\n" \
"v_mfma_f32_16x16x32_bf16 v[104:107], a[140:143], v[60:63], v[104:107]\n" \
"v_exp_f32 v71, v71\n" \
"v_mfma_f32_16x16x32_bf16 v[112:115], a[140:143], v[204:207], v[112:115]\n" \
"ds_read_b128 a[132:135], v5 offset:17408\n" \
"v_cvt_pk_bf16_f32 v133, v74, v75\n" \
"v_mfma_f32_16x16x32_bf16 v[120:123], a[140:143], v[220:223], v[120:123]\n" \
"s_waitcnt lgkmcnt(4)\n" \
"v_exp_f32 v76, v76\n" \
"v_mfma_f32_16x16x32_bf16 v[100:103], a[144:147], v[32:35], v[244:247]\n" \
"v_exp_f32 v77, v77\n" \
"v_mfma_f32_16x16x32_bf16 v[108:111], a[144:147], v[48:51], v[244:247]\n" \
"v_exp_f32 v78, v78\n" \
"v_mfma_f32_16x16x32_bf16 v[116:119], a[144:147], v[192:195], v[244:247]\n" \
"ds_read_b128 a[136:139], v5 offset:18432\n" \
"v_cvt_pk_bf16_f32 v136, v80, v81\n" \
"v_mfma_f32_16x16x32_bf16 v[124:127], a[144:147], v[208:211], v[24:27]\n" \
"v_exp_f32 v79, v79\n" \
"v_accvgpr_write_b32 a67, v195\n" \
"v_mfma_f32_16x16x32_bf16 v[100:103], a[148:151], v[36:39], v[100:103]\n" \
"buffer_load_dwordx4 v6, s[40:43], s16 offen offset:2048 sc1 lds\n" \
"v_accvgpr_write_b32 a80, v208\n" \
"v_accvgpr_write_b32 a81, v209\n" \
"v_mfma_f32_16x16x32_bf16 v[108:111], a[148:151], v[52:55], v[108:111]\n" \
"v_exp_f32 v84, v84\n" \
"v_accvgpr_write_b32 a82, v210\n" \
"v_accvgpr_write_b32 a83, v211\n" \
"v_mfma_f32_16x16x32_bf16 v[116:119], a[148:151], v[196:199], v[116:119]\n" \
"ds_read_b128 a[140:143], v5 offset:19456\n" \
"v_cvt_pk_bf16_f32 v137, v82, v83\n" \
"v_accvgpr_write_b32 a68, v196\n" \
"v_accvgpr_write_b32 a69, v197\n" \
"v_mfma_f32_16x16x32_bf16 v[124:127], a[148:151], v[212:215], v[124:127]\n" \
"s_waitcnt lgkmcnt(4)\n" \
"v_exp_f32 v85, v85\n" \
"v_accvgpr_write_b32 a70, v198\n" \
"v_accvgpr_write_b32 a71, v199\n" \
"v_mfma_f32_16x16x32_bf16 v[100:103], a[152:155], v[40:43], v[100:103]\n" \
"v_exp_f32 v86, v86\n" \
"v_accvgpr_write_b32 a84, v212\n" \
"v_accvgpr_write_b32 a85, v213\n" \
"v_mfma_f32_16x16x32_bf16 v[108:111], a[152:155], v[56:59], v[108:111]\n" \
"v_exp_f32 v87, v87\n" \
"v_accvgpr_write_b32 a86, v214\n" \
"v_accvgpr_write_b32 a87, v215\n" \
"v_mfma_f32_16x16x32_bf16 v[116:119], a[152:155], v[200:203], v[116:119]\n" \
"ds_read_b128 a[144:147], v5 offset:20480\n" \
"v_cvt_pk_bf16_f32 v140, v88, v89\n" \
"v_accvgpr_write_b32 a72, v200\n" \
"v_accvgpr_write_b32 a73, v201\n" \
"v_mfma_f32_16x16x32_bf16 v[124:127], a[152:155], v[216:219], v[124:127]\n" \
"v_exp_f32 v92, v92\n" \
"v_accvgpr_write_b32 a74, v202\n" \
"v_accvgpr_write_b32 a75, v203\n" \
"v_mov_b32 v180, 0x3f803f80\n" \
"v_mov_b32 v181, 0x3f803f80\n" \
"v_mov_b32 v182, 0x3f803f80\n" \
"v_mov_b32 v183, 0x3f803f80\n" \
"v_mfma_f32_16x16x32_bf16 v[100:103], a[156:159], v[44:47], v[100:103]\n" \
"buffer_load_dwordx4 v6, s[40:43], s16 offen offset:3072 sc1 lds\n" \
"v_accvgpr_write_b32 a88, v216\n" \
"v_accvgpr_write_b32 a89, v217\n" \
"v_mfma_f32_16x16x32_bf16 v[108:111], a[156:159], v[60:63], v[108:111]\n" \
"v_exp_f32 v93, v93\n" \
"v_accvgpr_write_b32 a90, v218\n" \
"v_accvgpr_write_b32 a91, v219\n" \
"v_mov_b32 v176, v240\n" \
"v_mov_b32 v177, v241\n" \
"v_mov_b32 v178, v242\n" \
"v_mov_b32 v179, v243\n" \
"v_mfma_f32_16x16x32_bf16 v[116:119], a[156:159], v[204:207], v[116:119]\n" \
"ds_read_b128 a[148:151], v5 offset:21504\n" \
"v_cvt_pk_bf16_f32 v141, v90, v91\n" \
"v_cvt_pk_bf16_f32 v130, v68, v69\n" \
"v_cvt_pk_bf16_f32 v131, v70, v71\n" \
"v_accvgpr_write_b32 a76, v204\n" \
"v_accvgpr_write_b32 a77, v205\n" \
"v_mfma_f32_16x16x32_bf16 v[124:127], a[156:159], v[220:223], v[124:127]\n" \
"s_waitcnt lgkmcnt(4)\n" \
"v_exp_f32 v94, v94\n" \
"v_cvt_pk_bf16_f32 v134, v76, v77\n" \
"v_cvt_pk_bf16_f32 v135, v78, v79\n" \
"v_accvgpr_write_b32 a78, v206\n" \
"v_accvgpr_write_b32 a79, v207\n" \
"v_mfma_f32_16x16x32_bf16 v[160:163], v[180:183], v[128:131], 0\n" \
"v_exp_f32 v95, v95\n" \
"v_cvt_pk_bf16_f32 v138, v84, v85\n" \
"v_cvt_pk_bf16_f32 v139, v86, v87\n" \
"v_accvgpr_write_b32 a92, v220\n" \
"v_accvgpr_write_b32 a93, v221\n" \
"v_mfma_f32_16x16x32_bf16 v[164:167], v[180:183], v[132:135], 0\n" \
"v_cvt_pk_bf16_f32 v142, v92, v93\n" \
"v_cvt_pk_bf16_f32 v143, v94, v95\n" \
"v_accvgpr_write_b32 a94, v222\n" \
"v_accvgpr_write_b32 a95, v223\n" \
"v_mfma_f32_16x16x32_bf16 v[168:171], v[180:183], v[136:139], 0\n" \
"v_exp_f32 v96, v96\n" \
"v_mfma_f32_16x16x32_bf16 v[172:175], v[180:183], v[140:143], 0\n" \
"v_lshrrev_b32 v9, 5, v4\n" \
"v_lshlrev_b32 v9, 27, v9\n" \
"v_mfma_f32_16x16x32_bf16 v[192:195], a[128:131], v[128:131], 0\n" \
"v_exp_f32 v97, v97\n" \
"v_mfma_f32_16x16x32_bf16 v[224:227], a[128:131], v[132:135], 0\n" \
"v_cvt_pk_bf16_f32 v144, v96, v97\n" \
"v_lshl_add_u32 v9, v4, 4, v9\n" \
"v_mfma_f32_16x16x32_bf16 a[0:3], a[128:131], v[136:139], 0\n" \
"ds_read_b128 a[152:155], v5 offset:22528\n" \
"v_mov_b32 v10, s14\n" \
"v_mfma_f32_16x16x32_bf16 a[32:35], a[128:131], v[140:143], 0\n" \
"v_exp_f32 v98, v98\n" \
"v_mfma_f32_16x16x32_bf16 v[196:199], a[132:135], v[128:131], 0\n" \
"v_exp_f32 v99, v99\n" \
"v_mfma_f32_16x16x32_bf16 v[228:231], a[132:135], v[132:135], 0\n" \
"v_cvt_pk_bf16_f32 v145, v98, v99\n" \
"v_mov_b32 v11, s15\n" \
"v_mfma_f32_16x16x32_bf16 a[4:7], a[132:135], v[136:139], 0\n" \
"ds_read_b128 a[156:159], v5 offset:23552\n" \
"s_waitcnt lgkmcnt(12)\n" \
"v_mfma_f32_16x16x32_bf16 a[36:39], a[132:135], v[140:143], 0\n" \
"s_waitcnt vmcnt(12) lgkmcnt(4)\n" \
"v_exp_f32 v100, v100\n" \
"v_mfma_f32_16x16x32_bf16 v[200:203], a[136:139], v[128:131], 0\n" \
"s_barrier\n" \
"v_exp_f32 v101, v101\n" \
"v_mfma_f32_16x16x32_bf16 v[232:235], a[136:139], v[132:135], 0\n" \
"s_add_u32 s17, s17, s19\n" \
"v_cvt_pk_bf16_f32 v146, v100, v101\n" \
"v_mfma_f32_16x16x32_bf16 a[8:11], a[136:139], v[136:139], 0\n" \
"ds_read_b128 a[128:131], v5 offset:32768\n" \
"v_mfma_f32_16x16x32_bf16 a[40:43], a[136:139], v[140:143], 0\n" \
"s_add_u32 m0, s49, 49152\n" \
"v_exp_f32 v102, v102\n" \
"v_mfma_f32_16x16x32_bf16 v[204:207], a[140:143], v[128:131], 0\n" \
"buffer_load_dwordx4 v6, s[40:43], s17 offen offset:0 sc1 lds\n" \
"v_mfma_f32_16x16x32_bf16 v[236:239], a[140:143], v[132:135], 0\n" \
"v_exp_f32 v103, v103\n" \
"v_mfma_f32_16x16x32_bf16 a[12:15], a[140:143], v[136:139], 0\n" \
"ds_read_b128 a[132:135], v5 offset:33792\n" \
"v_cvt_pk_bf16_f32 v147, v102, v103\n" \
"v_mfma_f32_16x16x32_bf16 a[44:47], a[140:143], v[140:143], 0\n" \
"s_waitcnt lgkmcnt(4)\n" \
"v_exp_f32 v104, v104\n" \
"v_mfma_f32_16x16x32_bf16 v[208:211], a[144:147], v[128:131], 0\n" \
"v_exp_f32 v105, v105\n" \
"v_mfma_f32_16x16x32_bf16 v[240:243], a[144:147], v[132:135], 0\n" \
"v_cvt_pk_bf16_f32 v148, v104, v105\n" \
"v_mfma_f32_16x16x32_bf16 a[16:19], a[144:147], v[136:139], 0\n" \
"ds_read_b128 a[136:139], v5 offset:34816\n" \
"v_mfma_f32_16x16x32_bf16 a[48:51], a[144:147], v[140:143], 0\n" \
"v_exp_f32 v106, v106\n" \
"v_mfma_f32_16x16x32_bf16 v[212:215], a[148:151], v[128:131], 0\n" \
"buffer_load_dwordx4 v6, s[40:43], s17 offen offset:1024 sc1 lds\n" \
"v_mfma_f32_16x16x32_bf16 v[244:247], a[148:151], v[132:135], 0\n" \
"v_exp_f32 v107, v107\n" \
"v_mfma_f32_16x16x32_bf16 a[20:23], a[148:151], v[136:139], 0\n" \
"ds_read_b128 a[140:143], v5 offset:35840\n" \
"v_cvt_pk_bf16_f32 v149, v106, v107\n" \
"v_mfma_f32_16x16x32_bf16 a[52:55], a[148:151], v[140:143], 0\n" \
"s_waitcnt lgkmcnt(4)\n" \
"v_exp_f32 v108, v108\n" \
"v_mfma_f32_16x16x32_bf16 v[216:219], a[152:155], v[128:131], 0\n" \
"buffer_load_dwordx4 v6, s[40:43], s17 offen offset:2048 sc1 lds\n" \
"v_mfma_f32_16x16x32_bf16 v[248:251], a[152:155], v[132:135], 0\n" \
"v_exp_f32 v109, v109\n" \
"v_mfma_f32_16x16x32_bf16 a[24:27], a[152:155], v[136:139], 0\n" \
"ds_read_b128 a[144:147], v5 offset:36864\n" \
"v_cvt_pk_bf16_f32 v150, v108, v109\n" \
"v_mfma_f32_16x16x32_bf16 a[56:59], a[152:155], v[140:143], 0\n" \
"v_exp_f32 v110, v110\n" \
"v_mfma_f32_16x16x32_bf16 v[220:223], a[156:159], v[128:131], 0\n" \
"buffer_load_dwordx4 v6, s[40:43], s17 offen offset:3072 sc1 lds\n" \
"v_mfma_f32_16x16x32_bf16 v[252:255], a[156:159], v[132:135], 0\n" \
"v_exp_f32 v111, v111\n" \
"v_mfma_f32_16x16x32_bf16 a[28:31], a[156:159], v[136:139], 0\n" \
"ds_read_b128 a[148:151], v5 offset:37888\n" \
"v_cvt_pk_bf16_f32 v151, v110, v111\n" \
"v_mfma_f32_16x16x32_bf16 a[60:63], a[156:159], v[140:143], 0\n" \
"s_waitcnt lgkmcnt(4)\n" \
"s_cmp_lt_i32 s46, 0\n" \
"s_cbranch_scc1 LAD2_%=\n" \
"LCHK_%=:\n" \
"s_cmp_eq_u32 s44, s46\n" \
"s_cbranch_scc1 LMID_%=\n" \
".p2align 6\n" \
"LODD_%=:\n" \
"v_mfma_f32_16x16x32_bf16 v[64:67], a[128:131], v[32:35], 0\n" \
"v_exp_f32 v112, v112\n" \
"v_mfma_f32_16x16x32_bf16 v[72:75], a[128:131], v[48:51], 0\n" \
"s_add_u32 s16, s16, s9\n" \
"v_mfma_f32_16x16x32_bf16 v[80:83], a[128:131], a[64:67], 0\n" \
"ds_read_b128 a[152:155], v5 offset:38912\n" \
"v_mfma_f32_16x16x32_bf16 v[88:91], a[128:131], a[80:83], 0\n" \
"s_add_u32 m0, s48, 0\n" \
"v_exp_f32 v113, v113\n" \
"v_mfma_f32_16x16x32_bf16 v[64:67], a[132:135], v[36:39], v[64:67]\n" \
"buffer_load_dwordx4 v6, s[40:43], s16 offen offset:0 sc1 lds\n" \
"v_mfma_f32_16x16x32_bf16 v[72:75], a[132:135], v[52:55], v[72:75]\n" \
"v_exp_f32 v114, v114\n" \
"v_mfma_f32_16x16x32_bf16 v[80:83], a[132:135], a[68:71], v[80:83]\n" \
"ds_read_b128 a[156:159], v5 offset:39936\n" \
"v_cvt_pk_bf16_f32 v152, v112, v113\n" \
"v_mfma_f32_16x16x32_bf16 v[88:91], a[132:135], a[84:87], v[88:91]\n" \
"s_waitcnt lgkmcnt(4)\n" \
"v_exp_f32 v115, v115\n" \
"v_mfma_f32_16x16x32_bf16 v[64:67], a[136:139], v[40:43], v[64:67]\n" \
"v_exp_f32 v116, v116\n" \
"v_mfma_f32_16x16x32_bf16 v[72:75], a[136:139], v[56:59], v[72:75]\n" \
"s_xor_b32 s49, s49, 0x10000\n" \
"v_cvt_pk_bf16_f32 v153, v114, v115\n" \
"v_mfma_f32_16x16x32_bf16 v[80:83], a[136:139], a[72:75], v[80:83]\n" \
"ds_read_b128 a[128:131], v5 offset:49152\n" \
"v_mfma_f32_16x16x32_bf16 v[88:91], a[136:139], a[88:91], v[88:91]\n" \
"v_exp_f32 v117, v117\n" \
"v_mfma_f32_16x16x32_bf16 v[64:67], a[140:143], v[44:47], v[64:67]\n" \
"buffer_load_dwordx4 v6, s[40:43], s16 offen offset:1024 sc1 lds\n" \
"v_mfma_f32_16x16x32_bf16 v[72:75], a[140:143], v[60:63], v[72:75]\n" \
"v_exp_f32 v118, v118\n" \
"v_mfma_f32_16x16x32_bf16 v[80:83], a[140:143], a[76:79], v[80:83]\n" \
"ds_read_b128 a[132:135], v5 offset:50176\n" \
"v_cvt_pk_bf16_f32 v154, v116, v117\n" \
"v_mfma_f32_16x16x32_bf16 v[88:91], a[140:143], a[92:95], v[88:91]\n" \
"s_waitcnt lgkmcnt(4)\n" \
"v_exp_f32 v119, v119\n" \
"v_mfma_f32_16x16x32_bf16 v[68:71], a[144:147], v[32:35], 0\n" \
"v_exp_f32 v120, v120\n" \
"v_mfma_f32_16x16x32_bf16 v[76:79], a[144:147], v[48:51], 0\n" \
"v_cvt_pk_bf16_f32 v155, v118, v119\n" \
"v_mfma_f32_16x16x32_bf16 v[84:87], a[144:147], a[64:67], 0\n" \
"ds_read_b128 a[136:139], v5 offset:51200\n" \
"v_mfma_f32_16x16x32_bf16 v[92:95], a[144:147], a[80:83], 0\n" \
"v_exp_f32 v121, v121\n" \
"v_mfma_f32_16x16x32_bf16 v[68:71], a[148:151], v[36:39], v[68:71]\n" \
"buffer_load_dwordx4 v6, s[40:43], s16 offen offset:2048 sc1 lds\n" \
"v_mfma_f32_16x16x32_bf16 v[76:79], a[148:151], v[52:55], v[76:79]\n" \
"v_exp_f32 v122, v122\n" \
"v_mfma_f32_16x16x32_bf16 v[84:87], a[148:151], a[68:71], v[84:87]\n" \
"ds_read_b128 a[140:143], v5 offset:52224\n" \
"v_cvt_pk_bf16_f32 v156, v120, v121\n" \
"v_mfma_f32_16x16x32_bf16 v[92:95], a[148:151], a[84:87], v[92:95]\n" \
"s_waitcnt lgkmcnt(4)\n" \
"v_exp_f32 v123, v123\n" \
"v_mfma_f32_16x16x32_bf16 v[68:71], a[152:155], v[40:43], v[68:71]\n" \
"v_exp_f32 v124, v124\n" \
"v_mfma_f32_16x16x32_bf16 v[76:79], a[152:155], v[56:59], v[76:79]\n" \
"v_cvt_pk_bf16_f32 v157, v122, v123\n" \
"v_mfma_f32_16x16x32_bf16 v[84:87], a[152:155], a[72:75], v[84:87]\n" \
"ds_read_b128 a[144:147], v5 offset:53248\n" \
"v_mfma_f32_16x16x32_bf16 v[92:95], a[152:155], a[88:91], v[92:95]\n" \
"v_exp_f32 v125, v125\n" \
"v_mfma_f32_16x16x32_bf16 v[68:71], a[156:159], v[44:47], v[68:71]\n" \
"buffer_load_dwordx4 v6, s[40:43], s16 offen offset:3072 sc1 lds\n" \
"v_mfma_f32_16x16x32_bf16 v[76:79], a[156:159], v[60:63], v[76:79]\n" \
"v_exp_f32 v126, v126\n" \
"v_mfma_f32_16x16x32_bf16 v[84:87], a[156:159], a[76:79], v[84:87]\n" \
"ds_read_b128 a[148:151], v5 offset:54272\n" \
"v_cvt_pk_bf16_f32 v158, v124, v125\n" \
"v_mfma_f32_16x16x32_bf16 v[92:95], a[156:159], a[92:95], v[92:95]\n" \
"s_waitcnt lgkmcnt(4)\n" \
"v_exp_f32 v127, v127\n" \
"v_mfma_f32_16x16x32_bf16 v[160:163], v[180:183], v[144:147], v[160:163]\n" \
"v_cvt_pk_bf16_f32 v159, v126, v127\n" \
"v_mfma_f32_16x16x32_bf16 v[164:167], v[180:183], v[148:151], v[164:167]\n" \
"v_mfma_f32_16x16x32_bf16 v[168:171], v[180:183], v[152:155], v[168:171]\n" \
"v_exp_f32 v64, v64\n" \
"v_mfma_f32_16x16x32_bf16 v[172:175], v[180:183], v[156:159], v[172:175]\n" \
"v_mfma_f32_16x16x32_bf16 v[192:195], a[128:131], v[144:147], v[192:195]\n" \
"v_exp_f32 v65, v65\n" \
"v_mfma_f32_16x16x32_bf16 v[224:227], a[128:131], v[148:151], v[224:227]\n" \
"v_cvt_pk_bf16_f32 v128, v64, v65\n" \
"v_mfma_f32_16x16x32_bf16 a[0:3], a[128:131], v[152:155], a[0:3]\n" \
"ds_read_b128 a[152:155], v5 offset:55296\n" \
"v_mfma_f32_16x16x32_bf16 a[32:35], a[128:131], v[156:159], a[32:35]\n" \
"v_exp_f32 v66, v66\n" \
"v_mfma_f32_16x16x32_bf16 v[196:199], a[132:135], v[144:147], v[196:199]\n" \
"v_exp_f32 v67, v67\n" \
"v_mfma_f32_16x16x32_bf16 v[228:231], a[132:135], v[148:151], v[228:231]\n" \
"v_cvt_pk_bf16_f32 v129, v66, v67\n" \
"v_mfma_f32_16x16x32_bf16 a[4:7], a[132:135], v[152:155], a[4:7]\n" \
"ds_read_b128 a[156:159], v5 offset:56320\n" \
"v_mfma_f32_16x16x32_bf16 a[36:39], a[132:135], v[156:159], a[36:39]\n" \
"s_waitcnt vmcnt(12) lgkmcnt(4)\n" \
"v_exp_f32 v68, v68\n" \
"v_mfma_f32_16x16x32_bf16 v[200:203], a[136:139], v[144:147], v[200:203]\n" \
"s_barrier\n" \
"v_xor_b32 v5, 0x10000, v5\n" \
"v_mfma_f32_16x16x32_bf16 v[232:235], a[136:139], v[148:151], v[232:235]\n" \
"s_add_u32 s17, s17, s19\n" \
"v_exp_f32 v69, v69\n" \
"v_mfma_f32_16x16x32_bf16 a[8:11], a[136:139], v[152:155], a[8:11]\n" \
"ds_read_b128 a[128:131], v5 offset:0\n" \
"v_cvt_pk_bf16_f32 v130, v68, v69\n" \
"v_mfma_f32_16x16x32_bf16 a[40:43], a[136:139], v[156:159], a[40:43]\n" \
"s_add_u32 m0, s48, 16384\n" \
"v_exp_f32 v70, v70\n" \
"v_mfma_f32_16x16x32_bf16 v[204:207], a[140:143], v[144:147], v[204:207]\n" \
"buffer_load_dwordx4 v6, s[40:43], s17 offen offset:0 sc1 lds\n" \
"v_mfma_f32_16x16x32_bf16 v[236:239], a[140:143], v[148:151], v[236:239]\n" \
"v_exp_f32 v71, v71\n" \
"v_mfma_f32_16x16x32_bf16 a[12:15], a[140:143], v[152:155], a[12:15]\n" \
"ds_read_b128 a[132:135], v5 offset:1024\n" \
"v_cvt_pk_bf16_f32 v131, v70, v71\n" \
"v_mfma_f32_16x16x32_bf16 a[44:47], a[140:143], v[156:159], a[44:47]\n" \
"s_waitcnt lgkmcnt(4)\n" \
"v_exp_f32 v72, v72\n" \
"v_mfma_f32_16x16x32_bf16 v[208:211], a[144:147], v[144:147], v[208:211]\n" \
"v_exp_f32 v73, v73\n" \
"v_mfma_f32_16x16x32_bf16 v[240:243], a[144:147], v[148:151], v[240:243]\n" \
"v_cvt_pk_bf16_f32 v132, v72, v73\n" \
"v_mfma_f32_16x16x32_bf16 a[16:19], a[144:147], v[152:155], a[16:19]\n" \
"ds_read_b128 a[136:139], v5 offset:2048\n" \
"v_mfma_f32_16x16x32_bf16 a[48:51], a[144:147], v[156:159], a[48:51]\n" \
"v_exp_f32 v74, v74\n" \
"v_mfma_f32_16x16x32_bf16 v[212:215], a[148:151], v[144:147], v[212:215]\n" \
"buffer_load_dwordx4 v6, s[40:43], s17 offen offset:1024 sc1 lds\n" \
"v_mfma_f32_16x16x32_bf16 v[244:247], a[148:151], v[148:151], v[244:247]\n" \
"v_exp_f32 v75, v75\n" \
"v_mfma_f32_16x16x32_bf16 a[20:23], a[148:151], v[152:155], a[20:23]\n" \
"LH15_%=:\ns_cbranch_execz LH19_%=\n" \
"ds_read_b128 a[140:143], v5 offset:3072\n" \
"v_cvt_pk_bf16_f32 v133, v74, v75\n" \
"v_mfma_f32_16x16x32_bf16 a[52:55], a[148:151], v[156:159], a[52:55]\n" \
"s_waitcnt lgkmcnt(4)\n" \
"v_exp_f32 v76, v76\n" \
"v_mfma_f32_16x16x32_bf16 v[216:219], a[152:155], v[144:147], v[216:219]\n" \
"buffer_load_dwordx4 v6, s[40:43], s17 offen offset:2048 sc1 lds\n" \
"v_mfma_f32_16x16x32_bf16 v[248:251], a[152:155], v[148:151], v[248:251]\n" \
"s_add_u32 s44, s44, 1\n" \
"v_exp_f32 v77, v77\n" \
"v_mfma_f32_16x16x32_bf16 a[24:27], a[152:155], v[152:155], a[24:27]\n" \
"ds_read_b128 a[144:147], v5 offset:4096\n" \
"v_cvt_pk_bf16_f32 v134, v76, v77\n" \
"v_mfma_f32_16x16x32_bf16 a[56:59], a[152:155], v[156:159], a[56:59]\n" \
"v_exp_f32 v78, v78\n" \
"v_mfma_f32_16x16x32_bf16 v[220:223], a[156:159], v[144:147], v[220:223]\n" \
"buffer_load_dwordx4 v6, s[40:43], s17 offen offset:3072 sc1 lds\n" \
"v_mfma_f32_16x16x32_bf16 v[252:255], a[156:159], v[148:151], v[252:255]\n" \
"s_xor_b32 s48, s48, 0x10000\n" \
"v_exp_f32 v79, v79\n" \
"v_mfma_f32_16x16x32_bf16 a[28:31], a[156:159], v[152:155], a[28:31]\n" \
"ds_read_b128 a[148:151], v5 offset:5120\n" \
"v_cvt_pk_bf16_f32 v135, v78, v79\n" \
"v_mfma_f32_16x16x32_bf16 a[60:63], a[156:159], v[156:159], a[60:63]\n" \
"s_waitcnt lgkmcnt(4)\n" \
"LBOT_%=:\n" \
"s_cmp_ge_u32 s44, 14\n" \
"s_cbranch_scc1 LDISP_%=\n" \
"LEVEN_%=:\n" \
"v_mfma_f32_16x16x32_bf16 v[96:99], a[128:131], v[32:35], 0\n" \
"v_exp_f32 v80, v80\n" \
"v_mfma_f32_16x16x32_bf16 v[104:107], a[128:131], v[48:51], 0\n" \
"s_add_u32 s16, s16, s9\n" \
"v_mfma_f32_16x16x32_bf16 v[112:115], a[128:131], a[64:67], 0\n" \
"ds_read_b128 a[152:155], v5 offset:6144\n" \
"v_mfma_f32_16x16x32_bf16 v[120:123], a[128:131], a[80:83], 0\n" \
"s_add_u32 m0, s49, 32768\n" \
"v_exp_f32 v81, v81\n" \
"v_mfma_f32_16x16x32_bf16 v[96:99], a[132:135], v[36:39], v[96:99]\n" \
"buffer_load_dwordx4 v6, s[40:43], s16 offen offset:0 sc1 lds\n" \
"v_mfma_f32_16x16x32_bf16 v[104:107], a[132:135], v[52:55], v[104:107]\n" \
"v_exp_f32 v82, v82\n" \
"v_mfma_f32_16x16x32_bf16 v[112:115], a[132:135], a[68:71], v[112:115]\n" \
"ds_read_b128 a[156:159], v5 offset:7168\n" \
"v_cvt_pk_bf16_f32 v136, v80, v81\n" \
"v_mfma_f32_16x16x32_bf16 v[120:123], a[132:135], a[84:87], v[120:123]\n" \
"s_waitcnt lgkmcnt(4)\n" \
"v_exp_f32 v83, v83\n" \
"v_mfma_f32_16x16x32_bf16 v[96:99], a[136:139], v[40:43], v[96:99]\n" \
"v_exp_f32 v84, v84\n" \
"v_mfma_f32_16x16x32_bf16 v[104:107], a[136:139], v[56:59], v[104:107]\n" \
"v_cvt_pk_bf16_f32 v137, v82, v83\n" \
"v_mfma_f32_16x16x32_bf16 v[112:115], a[136:139], a[72:75], v[112:115]\n" \
"ds_read_b128 a[128:131], v5 offset:16384\n" \
"v_mfma_f32_16x16x32_bf16 v[120:123], a[136:139], a[88:91], v[120:123]\n" \
"v_exp_f32 v85, v85\n" \
"v_mfma_f32_16x16x32_bf16 v[96:99], a[140:143], v[44:47], v[96:99]\n" \
"buffer_load_dwordx4 v6, s[40:43], s16 offen offset:1024 sc1 lds\n" \
"v_mfma_f32_16x16x32_bf16 v[104:107], a[140:143], v[60:63], v[104:107]\n" \
"v_exp_f32 v86, v86\n" \
"v_mfma_f32_16x16x32_bf16 v[112:115], a[140:143], a[76:79], v[112:115]\n" \
"ds_read_b128 a[132:135], v5 offset:17408\n" \
"v_cvt_pk_bf16_f32 v138, v84, v85\n" \
"v_mfma_f32_16x16x32_bf16 v[120:123], a[140:143], a[92:95], v[120:123]\n" \
"s_waitcnt lgkmcnt(4)\n" \
"v_exp_f32 v87, v87\n" \
"v_mfma_f32_16x16x32_bf16 v[100:103], a[144:147], v[32:35], 0\n" \
"v_exp_f32 v88, v88\n" \
"v_mfma_f32_16x16x32_bf16 v[108:111], a[144:147], v[48:51], 0\n" \
"v_cvt_pk_bf16_f32 v139, v86, v87\n" \
"v_mfma_f32_16x16x32_bf16 v[116:119], a[144:147], a[64:67], 0\n" \
"ds_read_b128 a[136:139], v5 offset:18432\n" \
"v_mfma_f32_16x16x32_bf16 v[124:127], a[144:147], a[80:83], 0\n" \
"v_exp_f32 v89, v89\n" \
"v_mfma_f32_16x16x32_bf16 v[100:103], a[148:151], v[36:39], v[100:103]\n" \
"buffer_load_dwordx4 v6, s[40:43], s16 offen offset:2048 sc1 lds\n" \
"v_mfma_f32_16x16x32_bf16 v[108:111], a[148:151], v[52:55], v[108:111]\n" \
"v_exp_f32 v90, v90\n" \
"v_mfma_f32_16x16x32_bf16 v[116:119], a[148:151], a[68:71], v[116:119]\n" \
"ds_read_b128 a[140:143], v5 offset:19456\n" \
"v_cvt_pk_bf16_f32 v140, v88, v89\n" \
"v_mfma_f32_16x16x32_bf16 v[124:127], a[148:151], a[84:87], v[124:127]\n" \
"s_waitcnt lgkmcnt(4)\n" \
"v_exp_f32 v91, v91\n" \
"v_mfma_f32_16x16x32_bf16 v[100:103], a[152:155], v[40:43], v[100:103]\n" \
"v_exp_f32 v92, v92\n" \
"v_mfma_f32_16x16x32_bf16 v[108:111], a[152:155], v[56:59], v[108:111]\n" \
"v_cvt_pk_bf16_f32 v141, v90, v91\n" \
"v_mfma_f32_16x16x32_bf16 v[116:119], a[152:155], a[72:75], v[116:119]\n" \
"ds_read_b128 a[144:147], v5 offset:20480\n" \
"v_mfma_f32_16x16x32_bf16 v[124:127], a[152:155], a[88:91], v[124:127]\n" \
"v_exp_f32 v93, v93\n" \
"v_mfma_f32_16x16x32_bf16 v[100:103], a[156:159], v[44:47], v[100:103]\n" \
"buffer_load_dwordx4 v6, s[40:43], s16 offen offset:3072 sc1 lds\n" \
"v_mfma_f32_16x16x32_bf16 v[108:111], a[156:159], v[60:63], v[108:111]\n" \
"v_exp_f32 v94, v94\n" \
"v_mfma_f32_16x16x32_bf16 v[116:119], a[156:159], a[76:79], v[116:119]\n" \
"ds_read_b128 a[148:151], v5 offset:21504\n" \
"v_cvt_pk_bf16_f32 v142, v92, v93\n" \
"v_mfma_f32_16x16x32_bf16 v[124:127], a[156:159], a[92:95], v[124:127]\n" \
"s_waitcnt lgkmcnt(4)\n" \
"v_exp_f32 v95, v95\n" \
"v_mfma_f32_16x16x32_bf16 v[160:163], v[180:183], v[128:131], v[160:163]\n" \
"v_cvt_pk_bf16_f32 v143, v94, v95\n" \
"v_mfma_f32_16x16x32_bf16 v[164:167], v[180:183], v[132:135], v[164:167]\n" \
"v_mfma_f32_16x16x32_bf16 v[168:171], v[180:183], v[136:139], v[168:171]\n" \
"v_exp_f32 v96, v96\n" \
"v_mfma_f32_16x16x32_bf16 v[172:175], v[180:183], v[140:143], v[172:175]\n" \
"v_mfma_f32_16x16x32_bf16 v[192:195], a[128:131], v[128:131], v[192:195]\n" \
"v_exp_f32 v97, v97\n" \
"v_mfma_f32_16x16x32_bf16 v[224:227], a[128:131], v[132:135], v[224:227]\n" \
"v_cvt_pk_bf16_f32 v144, v96, v97\n" \
"v_mfma_f32_16x16x32_bf16 a[0:3], a[128:131], v[136:139], a[0:3]\n" \
"ds_read_b128 a[152:155], v5 offset:22528\n" \
"v_mfma_f32_16x16x32_bf16 a[32:35], a[128:131], v[140:143], a[32:35]\n" \
"v_exp_f32 v98, v98\n" \
"v_mfma_f32_16x16x32_bf16 v[196:199], a[132:135], v[128:131], v[196:199]\n" \
"v_exp_f32 v99, v99\n" \
"v_mfma_f32_16x16x32_bf16 v[228:231], a[132:135], v[132:135], v[228:231]\n" \
"v_cvt_pk_bf16_f32 v145, v98, v99\n" \
"v_mfma_f32_16x16x32_bf16 a[4:7], a[132:135], v[136:139], a[4:7]\n" \
"ds_read_b128 a[156:159], v5 offset:23552\n" \
"v_mfma_f32_16x16x32_bf16 a[36:39], a[132:135], v[140:143], a[36:39]\n" \
"s_waitcnt vmcnt(12) lgkmcnt(4)\n" \
"v_exp_f32 v100, v100\n" \
"v_mfma_f32_16x16x32_bf16 v[200:203], a[136:139], v[128:131], v[200:203]\n" \
"s_barrier\n" \
"v_exp_f32 v101, v101\n" \
"v_mfma_f32_16x16x32_bf16 v[232:235], a[136:139], v[132:135], v[232:235]\n" \
"s_add_u32 s17, s17, s19\n" \
"v_cvt_pk_bf16_f32 v146, v100, v101\n" \
"v_mfma_f32_16x16x32_bf16 a[8:11], a[136:139], v[136:139], a[8:11]\n" \
"ds_read_b128 a[128:131], v5 offset:32768\n" \
"v_mfma_f32_16x16x32_bf16 a[40:43], a[136:139], v[140:143], a[40:43]\n" \
"s_add_u32 m0, s49, 49152\n" \
"v_exp_f32 v102, v102\n" \
"v_mfma_f32_16x16x32_bf16 v[204:207], a[140:143], v[128:131], v[204:207]\n" \
"buffer_load_dwordx4 v6, s[40:43], s17 offen offset:0 sc1 lds\n" \
"v_mfma_f32_16x16x32_bf16 v[236:239], a[140:143], v[132:135], v[236:239]\n" \
"v_exp_f32 v103, v103\n" \
"v_mfma_f32_16x16x32_bf16 a[12:15], a[140:143], v[136:139], a[12:15]\n" \
"ds_read_b128 a[132:135], v5 offset:33792\n" \
"v_cvt_pk_bf16_f32 v147, v102, v103\n" \
"v_mfma_f32_16x16x32_bf16 a[44:47], a[140:143], v[140:143], a[44:47]\n" \
"s_waitcnt lgkmcnt(4)\n" \
"v_exp_f32 v104, v104\n" \
"v_mfma_f32_16x16x32_bf16 v[208:211], a[144:147], v[128:131], v[208:211]\n" \
"v_exp_f32 v105, v105\n" \
"v_mfma_f32_16x16x32_bf16 v[240:243], a[144:147], v[132:135], v[240:243]\n" \
"v_cvt_pk_bf16_f32 v148, v104, v105\n" \
"v_mfma_f32_16x16x32_bf16 a[16:19], a[144:147], v[136:139], a[16:19]\n" \
"ds_read_b128 a[136:139], v5 offset:34816\n" \
"v_mfma_f32_16x16x32_bf16 a[48:51], a[144:147], v[140:143], a[48:51]\n" \
"v_exp_f32 v106, v106\n" \
"v_mfma_f32_16x16x32_bf16 v[212:215], a[148:151], v[128:131], v[212:215]\n" \
"buffer_load_dwordx4 v6, s[40:43], s17 offen offset:1024 sc1 lds\n" \
"v_mfma_f32_16x16x32_bf16 v[244:247], a[148:151], v[132:135], v[244:247]\n" \
"v_exp_f32 v107, v107\n" \
"v_mfma_f32_16x16x32_bf16 a[20:23], a[148:151], v[136:139], a[20:23]\n" \
"ds_read_b128 a[140:143], v5 offset:35840\n" \
"v_cvt_pk_bf16_f32 v149, v106, v107\n" \
"v_mfma_f32_16x16x32_bf16 a[52:55], a[148:151], v[140:143], a[52:55]\n" \
"s_waitcnt lgkmcnt(4)\n" \
"v_exp_f32 v108, v108\n" \
"v_mfma_f32_16x16x32_bf16 v[216:219], a[152:155], v[128:131], v[216:219]\n" \
"buffer_load_dwordx4 v6, s[40:43], s17 offen offset:2048 sc1 lds\n" \
"v_mfma_f32_16x16x32_bf16 v[248:251], a[152:155], v[132:135], v[248:251]\n" \
"v_exp_f32 v109, v109\n" \
"v_mfma_f32_16x16x32_bf16 a[24:27], a[152:155], v[136:139], a[24:27]\n" \
"ds_read_b128 a[144:147], v5 offset:36864\n" \
"v_cvt_pk_bf16_f32 v150, v108, v109\n" \
"v_mfma_f32_16x16x32_bf16 a[56:59], a[152:155], v[140:143], a[56:59]\n" \
"v_exp_f32 v110, v110\n" \
"v_mfma_f32_16x16x32_bf16 v[220:223], a[156:159], v[128:131], v[220:223]\n" \
"buffer_load_dwordx4 v6, s[40:43], s17 offen offset:3072 sc1 lds\n" \
"v_mfma_f32_16x16x32_bf16 v[252:255], a[156:159], v[132:135], v[252:255]\n" \
"v_exp_f32 v111, v111\n" \
"v_mfma_f32_16x16x32_bf16 a[28:31], a[156:159], v[136:139], a[28:31]\n" \
"ds_read_b128 a[148:151], v5 offset:37888\n" \
"v_cvt_pk_bf16_f32 v151, v110, v111\n" \
"v_mfma_f32_16x16x32_bf16 a[60:63], a[156:159], v[140:143], a[60:63]\n" \
"s_waitcnt lgkmcnt(4)\n" \
"s_cmp_lg_u32 s44, s46\n" \
"s_cbranch_scc1 LODD_%=\n" \
"LMID_%=:\n" \
"s_cmp_eq_u32 s44, s45\n" \
"s_cbranch_scc1 LWS_%=\n" \
"s_cmp_lt_u32 %2, 2\n" \
"s_cbranch_scc1 LS1_%=\n" \
"s_cmp_eq_u32 s44, 15\n" \
"s_cbranch_scc1 LS1_%=\n" \
"s_lshl_b32 s46, s7, 2\n" \
"s_add_u32 s46, s46, %2\n" \
"s_waitcnt vmcnt(8)\n" \
"v_cmp_ne_u64 vcc, s[14:15], v[18:19]\n" \
"s_cmp_eq_u32 s46, 30\ns_cselect_b32 s46, 15, 99\n" \
"s_cbranch_vccnz LPE0_%=\nLPE0R_%=:\n" \
"s_branch LODD_%=\n" \
"LWS_%=:\n" \
"v_mfma_f32_16x16x32_bf16 v[64:67], a[128:131], v[32:35], 0\n" \
"s_mov_b32 s81, 0\n" \
"v_exp_f32 v112, v112\n" \
"v_mfma_f32_16x16x32_bf16 v[72:75], a[128:131], v[48:51], 0\n" \
"s_add_u32 s16, s16, s9\n" \
"v_mov_b32 v12, v30\n" \
"v_mov_b32 v28, -1\n" \
"v_mfma_f32_16x16x32_bf16 v[80:83], a[128:131], a[64:67], 0\n" \
"ds_read_b128 a[152:155], v5 offset:38912\n" \
"v_mov_b32 v29, -1\n" \
"v_mfma_f32_16x16x32_bf16 v[88:91], a[128:131], a[80:83], 0\n" \
"s_add_u32 m0, s48, 0\n" \
"v_exp_f32 v113, v113\n" \
"v_mfma_f32_16x16x32_bf16 v[64:67], a[132:135], v[36:39], v[64:67]\n" \
"buffer_load_dwordx4 v6, s[40:43], s16 offen offset:0 sc1 lds\n" \
"v_mfma_f32_16x16x32_bf16 v[72:75], a[132:135], v[52:55], v[72:75]\n" \
"v_exp_f32 v114, v114\n" \
"v_mfma_f32_16x16x32_bf16 v[80:83], a[132:135], a[68:71], v[80:83]\n" \
"ds_read_b128 a[156:159], v5 offset:39936\n" \
"v_cvt_pk_bf16_f32 v152, v112, v113\n" \
"v_mfma_f32_16x16x32_bf16 v[88:91], a[132:135], a[84:87], v[88:91]\n" \
"s_waitcnt lgkmcnt(4)\n" \
"v_exp_f32 v115, v115\n" \
"s_add_u32 m0, s79, 131072\n" \
"v_mfma_f32_16x16x32_bf16 v[64:67], a[136:139], v[40:43], v[64:67]\n" \
"buffer_load_dwordx4 v12, s[28:31], s81 offen offset:0 lds\n" \
"v_mfma_f32_16x16x32_bf16 v[72:75], a[136:139], v[56:59], v[72:75]\n" \
"s_xor_b32 s49, s49, 0x10000\n" \
"v_exp_f32 v116, v116\n" \
"v_mfma_f32_16x16x32_bf16 v[80:83], a[136:139], a[72:75], v[80:83]\n" \
"ds_read_b128 a[128:131], v5 offset:49152\n" \
"v_cvt_pk_bf16_f32 v153, v114, v115\n" \
"v_mfma_f32_16x16x32_bf16 v[88:91], a[136:139], a[88:91], v[88:91]\n" \
"s_add_u32 m0, s48, 0\n" \
"v_exp_f32 v117, v117\n" \
"v_mfma_f32_16x16x32_bf16 v[64:67], a[140:143], v[44:47], v[64:67]\n" \
"buffer_load_dwordx4 v6, s[40:43], s16 offen offset:1024 sc1 lds\n" \
"v_mfma_f32_16x16x32_bf16 v[72:75], a[140:143], v[60:63], v[72:75]\n" \
"v_exp_f32 v118, v118\n" \
"v_mfma_f32_16x16x32_bf16 v[80:83], a[140:143], a[76:79], v[80:83]\n" \
"ds_read_b128 a[132:135], v5 offset:50176\n" \
"v_cvt_pk_bf16_f32 v154, v116, v117\n" \
"v_mfma_f32_16x16x32_bf16 v[88:91], a[140:143], a[92:95], v[88:91]\n" \
"s_waitcnt lgkmcnt(4)\n" \
"v_exp_f32 v119, v119\n" \
"s_add_u32 m0, s79, 131968\n" \
"v_mfma_f32_16x16x32_bf16 v[68:71], a[144:147], v[32:35], 0\n" \
"buffer_load_dwordx4 v12, s[28:31], s81 offen offset:128 lds\n" \
"v_mfma_f32_16x16x32_bf16 v[76:79], a[144:147], v[48:51], 0\n" \
"s_mov_b32 s81, 65536\n" \
"v_exp_f32 v120, v120\n" \
"v_mfma_f32_16x16x32_bf16 v[84:87], a[144:147], a[64:67], 0\n" \
"ds_read_b128 a[136:139], v5 offset:51200\n" \
"v_cvt_pk_bf16_f32 v155, v118, v119\n" \
"v_mfma_f32_16x16x32_bf16 v[92:95], a[144:147], a[80:83], 0\n" \
"s_add_u32 m0, s48, 0\n" \
"v_exp_f32 v121, v121\n" \
"v_mfma_f32_16x16x32_bf16 v[68:71], a[148:151], v[36:39], v[68:71]\n" \
"buffer_load_dwordx4 v6, s[40:43], s16 offen offset:2048 sc1 lds\n" \
"v_mfma_f32_16x16x32_bf16 v[76:79], a[148:151], v[52:55], v[76:79]\n" \
"v_exp_f32 v122, v122\n" \
"v_mfma_f32_16x16x32_bf16 v[84:87], a[148:151], a[68:71], v[84:87]\n" \
"ds_read_b128 a[140:143], v5 offset:52224\n" \
"v_cvt_pk_bf16_f32 v156, v120, v121\n" \
"v_mfma_f32_16x16x32_bf16 v[92:95], a[148:151], a[84:87], v[92:95]\n" \
"s_waitcnt lgkmcnt(4)\n" \
"v_exp_f32 v123, v123\n" \
"s_add_u32 m0, s79, 139264\n" \
"v_mfma_f32_16x16x32_bf16 v[68:71], a[152:155], v[40:43], v[68:71]\n" \
"buffer_load_dwordx4 v12, s[28:31], s81 offen offset:0 lds\n" \
"v_mfma_f32_16x16x32_bf16 v[76:79], a[152:155], v[56:59], v[76:79]\n" \
"v_exp_f32 v124, v124\n" \
"v_mfma_f32_16x16x32_bf16 v[84:87], a[152:155], a[72:75], v[84:87]\n" \
"ds_read_b128 a[144:147], v5 offset:53248\n" \
"v_cvt_pk_bf16_f32 v157, v122, v123\n" \
"v_mfma_f32_16x16x32_bf16 v[92:95], a[152:155], a[88:91], v[92:95]\n" \
"s_add_u32 m0, s48, 0\n" \
"v_exp_f32 v125, v125\n" \
"v_mfma_f32_16x16x32_bf16 v[68:71], a[156:159], v[44:47], v[68:71]\n" \
"buffer_load_dwordx4 v6, s[40:43], s16 offen offset:3072 sc1 lds\n" \
"v_mfma_f32_16x16x32_bf16 v[76:79], a[156:159], v[60:63], v[76:79]\n" \
"v_exp_f32 v126, v126\n" \
"v_mfma_f32_16x16x32_bf16 v[84:87], a[156:159], a[76:79], v[84:87]\n" \
"ds_read_b128 a[148:151], v5 offset:54272\n" \
"v_cvt_pk_bf16_f32 v158, v124, v125\n" \
"v_mfma_f32_16x16x32_bf16 v[92:95], a[156:159], a[92:95], v[92:95]\n" \
"s_waitcnt lgkmcnt(4)\n" \
"v_exp_f32 v127, v127\n" \
"s_add_u32 m0, s79, 140160\n" \
"v_mfma_f32_16x16x32_bf16 v[160:163], v[180:183], v[144:147], v[160:163]\n" \
"buffer_load_dwordx4 v12, s[28:31], s81 offen offset:128 lds\n" \
"v_mfma_f32_16x16x32_bf16 v[164:167], v[180:183], v[148:151], v[164:167]\n" \
"s_mov_b32 s81, 131072\n" \
"v_cvt_pk_bf16_f32 v159, v126, v127\n" \
"v_mov_b32 v30, -1\n" \
"v_mfma_f32_16x16x32_bf16 v[168:171], v[180:183], v[152:155], v[168:171]\n" \
"v_exp_f32 v64, v64\n" \
"v_mfma_f32_16x16x32_bf16 v[172:175], v[180:183], v[156:159], v[172:175]\n" \
"s_add_u32 s80, s78, 4096\n" \
"v_mov_b32 v31, -1\n" \
"v_bfe_u32 v189, v4, 7, 1\n" \
"v_mfma_f32_16x16x32_bf16 v[192:195], a[128:131], v[144:147], v[192:195]\n" \
"buffer_store_dwordx4 v[28:31], v4, s[36:39], s80 offen offset:2048 sc1\n" \
"v_mfma_f32_16x16x32_bf16 v[224:227], a[128:131], v[148:151], v[224:227]\n" \
"v_bfe_u32 v190, v4, 4, 3\n" \
"v_lshl_add_u32 v189, v190, 1, v189\n" \
"v_mfma_f32_16x16x32_bf16 a[0:3], a[128:131], v[152:155], a[0:3]\n" \
"ds_read_b128 a[152:155], v5 offset:55296\n" \
"v_lshrrev_b32 v190, 8, v4\n" \
"v_mfma_f32_16x16x32_bf16 a[32:35], a[128:131], v[156:159], a[32:35]\n" \
"v_exp_f32 v65, v65\n" \
"v_mfma_f32_16x16x32_bf16 v[196:199], a[132:135], v[144:147], v[196:199]\n" \
"v_exp_f32 v66, v66\n" \
"v_mfma_f32_16x16x32_bf16 v[228:231], a[132:135], v[148:151], v[228:231]\n" \
"v_exp_f32 v67, v67\n" \
"v_mfma_f32_16x16x32_bf16 a[4:7], a[132:135], v[152:155], a[4:7]\n" \
"ds_read_b128 a[156:159], v5 offset:56320\n" \
"v_lshlrev_b32 v188, 11, v189\n" \
"v_mfma_f32_16x16x32_bf16 a[36:39], a[132:135], v[156:159], a[36:39]\n" \
"s_waitcnt vmcnt(17) lgkmcnt(4)\n" \
"v_exp_f32 v68, v68\n" \
"v_mfma_f32_16x16x32_bf16 v[200:203], a[136:139], v[144:147], v[200:203]\n" \
"s_barrier\n" \
"v_xor_b32 v5, 0x10000, v5\n" \
"v_and_b32 v189, 1, v190\n" \
"v_mfma_f32_16x16x32_bf16 v[232:235], a[136:139], v[148:151], v[232:235]\n" \
"s_add_u32 s17, s17, s19\n" \
"v_exp_f32 v69, v69\n" \
"v_mfma_f32_16x16x32_bf16 a[8:11], a[136:139], v[152:155], a[8:11]\n" \
"ds_read_b128 a[128:131], v5 offset:0\n" \
"v_lshl_add_u32 v188, v189, 5, v188\n" \
"v_mfma_f32_16x16x32_bf16 a[40:43], a[136:139], v[156:159], a[40:43]\n" \
"s_add_u32 m0, s48, 16384\n" \
"v_exp_f32 v70, v70\n" \
"v_mfma_f32_16x16x32_bf16 v[204:207], a[140:143], v[144:147], v[204:207]\n" \
"buffer_load_dwordx4 v6, s[40:43], s17 offen offset:0 sc1 lds\n" \
"v_mfma_f32_16x16x32_bf16 v[236:239], a[140:143], v[148:151], v[236:239]\n" \
"v_exp_f32 v71, v71\n" \
"v_mfma_f32_16x16x32_bf16 a[12:15], a[140:143], v[152:155], a[12:15]\n" \
"ds_read_b128 a[132:135], v5 offset:1024\n" \
"v_lshrrev_b32 v189, 1, v190\n" \
"v_mfma_f32_16x16x32_bf16 a[44:47], a[140:143], v[156:159], a[44:47]\n" \
"s_waitcnt lgkmcnt(4)\n" \
"v_exp_f32 v72, v72\n" \
"v_mfma_f32_16x16x32_bf16 v[208:211], a[144:147], v[144:147], v[208:211]\n" \
"v_exp_f32 v73, v73\n" \
"v_mfma_f32_16x16x32_bf16 v[240:243], a[144:147], v[148:151], v[240:243]\n" \
"v_mfma_f32_16x16x32_bf16 a[16:19], a[144:147], v[152:155], a[16:19]\n" \
"ds_read_b128 a[136:139], v5 offset:2048\n" \
"v_lshl_add_u32 v188, v189, 4, v188\n" \
"v_mfma_f32_16x16x32_bf16 a[48:51], a[144:147], v[156:159], a[48:51]\n" \
"s_add_u32 m0, s48, 16384\n" \
"v_exp_f32 v74, v74\n" \
"v_mfma_f32_16x16x32_bf16 v[212:215], a[148:151], v[144:147], v[212:215]\n" \
"buffer_load_dwordx4 v6, s[40:43], s17 offen offset:1024 sc1 lds\n" \
"v_mfma_f32_16x16x32_bf16 v[244:247], a[148:151], v[148:151], v[244:247]\n" \
"v_exp_f32 v75, v75\n" \
"v_mfma_f32_16x16x32_bf16 a[20:23], a[148:151], v[152:155], a[20:23]\n" \
"ds_read_b128 a[140:143], v5 offset:3072\n" \
"v_add_u32 v190, s83, v188\n" \
"v_mfma_f32_16x16x32_bf16 a[52:55], a[148:151], v[156:159], a[52:55]\n" \
"s_waitcnt lgkmcnt(4)\n" \
"s_add_u32 m0, s48, 16384\n" \
"v_add_u32 v188, s82, v188\n" \
"v_mfma_f32_16x16x32_bf16 v[216:219], a[152:155], v[144:147], v[216:219]\n" \
"buffer_load_dwordx4 v6, s[40:43], s17 offen offset:2048 sc1 lds\n" \
"v_mfma_f32_16x16x32_bf16 v[248:251], a[152:155], v[148:151], v[248:251]\n" \
"s_add_u32 s44, s44, 1\n" \
"v_exp_f32 v76, v76\n" \
"v_mfma_f32_16x16x32_bf16 a[24:27], a[152:155], v[152:155], a[24:27]\n" \
"ds_read_b128 a[144:147], v5 offset:4096\n" \
"v_add_u32 v189, 0x8000, v188\n" \
"v_mfma_f32_16x16x32_bf16 a[56:59], a[152:155], v[156:159], a[56:59]\n" \
"s_add_u32 m0, s48, 16384\n" \
"v_exp_f32 v77, v77\n" \
"v_mfma_f32_16x16x32_bf16 v[220:223], a[156:159], v[144:147], v[220:223]\n" \
"buffer_load_dwordx4 v6, s[40:43], s17 offen offset:3072 sc1 lds\n" \
"v_mfma_f32_16x16x32_bf16 v[252:255], a[156:159], v[148:151], v[252:255]\n" \
"s_xor_b32 s48, s48, 0x10000\n" \
"v_exp_f32 v78, v78\n" \
"v_mfma_f32_16x16x32_bf16 a[28:31], a[156:159], v[152:155], a[28:31]\n" \
"ds_read_b128 a[148:151], v5 offset:5120\n" \
"v_add_u32 v191, 0x8000, v190\n" \
"v_exp_f32 v79, v79\n" \
"v_mfma_f32_16x16x32_bf16 a[60:63], a[156:159], v[156:159], a[60:63]\n" \
"s_waitcnt lgkmcnt(4)\n" \
"v_cvt_pk_bf16_f32 v128, v64, v65\n" \
"v_cvt_pk_bf16_f32 v129, v66, v67\n" \
"v_cvt_pk_bf16_f32 v130, v68, v69\n" \
"v_cvt_pk_bf16_f32 v131, v70, v71\n" \
"v_cvt_pk_bf16_f32 v132, v72, v73\n" \
"v_cvt_pk_bf16_f32 v133, v74, v75\n" \
"v_cvt_pk_bf16_f32 v134, v76, v77\n" \
"v_cvt_pk_bf16_f32 v135, v78, v79\n" \
"v_mfma_f32_16x16x32_bf16 v[96:99], a[128:131], v[32:35], 0\n" \
"global_load_dwordx4 a[96:99], v188, s[60:61]\n" \
"v_mfma_f32_16x16x32_bf16 v[104:107], a[128:131], v[48:51], 0\n" \
"s_add_u32 s16, s16, s9\n" \
"v_exp_f32 v80, v80\n" \
"v_mfma_f32_16x16x32_bf16 v[112:115], a[128:131], a[64:67], 0\n" \
"ds_read_b128 a[152:155], v5 offset:6144\n" \
"v_mfma_f32_16x16x32_bf16 v[120:123], a[128:131], a[80:83], 0\n" \
"s_add_u32 m0, s49, 32768\n" \
"v_exp_f32 v81, v81\n" \
"v_mfma_f32_16x16x32_bf16 v[96:99], a[132:135], v[36:39], v[96:99]\n" \
"buffer_load_dwordx4 v6, s[40:43], s16 offen offset:0 sc1 lds\n" \
"v_mfma_f32_16x16x32_bf16 v[104:107], a[132:135], v[52:55], v[104:107]\n" \
"v_exp_f32 v82, v82\n" \
"v_mfma_f32_16x16x32_bf16 v[112:115], a[132:135], a[68:71], v[112:115]\n" \
"ds_read_b128 a[156:159], v5 offset:7168\n" \
"v_cvt_pk_bf16_f32 v136, v80, v81\n" \
"v_mfma_f32_16x16x32_bf16 v[120:123], a[132:135], a[84:87], v[120:123]\n" \
"s_waitcnt lgkmcnt(4)\n" \
"v_exp_f32 v83, v83\n" \
"s_add_u32 m0, s79, 147456\n" \
"v_mfma_f32_16x16x32_bf16 v[96:99], a[136:139], v[40:43], v[96:99]\n" \
"buffer_load_dwordx4 v12, s[28:31], s81 offen offset:0 lds\n" \
"v_mfma_f32_16x16x32_bf16 v[104:107], a[136:139], v[56:59], v[104:107]\n" \
"v_exp_f32 v84, v84\n" \
"v_mfma_f32_16x16x32_bf16 v[112:115], a[136:139], a[72:75], v[112:115]\n" \
"ds_read_b128 a[128:131], v5 offset:16384\n" \
"v_cvt_pk_bf16_f32 v137, v82, v83\n" \
"v_mfma_f32_16x16x32_bf16 v[120:123], a[136:139], a[88:91], v[120:123]\n" \
"s_add_u32 m0, s49, 32768\n" \
"v_exp_f32 v85, v85\n" \
"v_mfma_f32_16x16x32_bf16 v[96:99], a[140:143], v[44:47], v[96:99]\n" \
"buffer_load_dwordx4 v6, s[40:43], s16 offen offset:1024 sc1 lds\n" \
"v_mfma_f32_16x16x32_bf16 v[104:107], a[140:143], v[60:63], v[104:107]\n" \
"v_exp_f32 v86, v86\n" \
"v_mfma_f32_16x16x32_bf16 v[112:115], a[140:143], a[76:79], v[112:115]\n" \
"ds_read_b128 a[132:135], v5 offset:17408\n" \
"v_cvt_pk_bf16_f32 v138, v84, v85\n" \
"v_mfma_f32_16x16x32_bf16 v[120:123], a[140:143], a[92:95], v[120:123]\n" \
"s_waitcnt lgkmcnt(4)\n" \
"v_exp_f32 v87, v87\n" \
"s_add_u32 m0, s79, 148352\n" \
"v_mfma_f32_16x16x32_bf16 v[100:103], a[144:147], v[32:35], 0\n" \
"buffer_load_dwordx4 v12, s[28:31], s81 offen offset:128 lds\n" \
"v_mfma_f32_16x16x32_bf16 v[108:111], a[144:147], v[48:51], 0\n" \
"s_mov_b32 s81, 196608\n" \
"v_exp_f32 v88, v88\n" \
"v_mfma_f32_16x16x32_bf16 v[116:119], a[144:147], a[64:67], 0\n" \
"ds_read_b128 a[136:139], v5 offset:18432\n" \
"v_cvt_pk_bf16_f32 v139, v86, v87\n" \
"v_mfma_f32_16x16x32_bf16 v[124:127], a[144:147], a[80:83], 0\n" \
"s_add_u32 m0, s49, 32768\n" \
"v_exp_f32 v89, v89\n" \
"v_mfma_f32_16x16x32_bf16 v[100:103], a[148:151], v[36:39], v[100:103]\n" \
"buffer_load_dwordx4 v6, s[40:43], s16 offen offset:2048 sc1 lds\n" \
"v_mfma_f32_16x16x32_bf16 v[108:111], a[148:151], v[52:55], v[108:111]\n" \
"v_exp_f32 v90, v90\n" \
"v_mfma_f32_16x16x32_bf16 v[116:119], a[148:151], a[68:71], v[116:119]\n" \
"ds_read_b128 a[140:143], v5 offset:19456\n" \
"v_cvt_pk_bf16_f32 v140, v88, v89\n" \
"v_mfma_f32_16x16x32_bf16 v[124:127], a[148:151], a[84:87], v[124:127]\n" \
"s_waitcnt lgkmcnt(4)\n" \
"v_exp_f32 v91, v91\n" \
"s_add_u32 m0, s79, 155648\n" \
"v_mfma_f32_16x16x32_bf16 v[100:103], a[152:155], v[40:43], v[100:103]\n" \
"buffer_load_dwordx4 v12, s[28:31], s81 offen offset:0 lds\n" \
"v_mfma_f32_16x16x32_bf16 v[108:111], a[152:155], v[56:59], v[108:111]\n" \
"v_exp_f32 v92, v92\n" \
"v_mfma_f32_16x16x32_bf16 v[116:119], a[152:155], a[72:75], v[116:119]\n" \
"ds_read_b128 a[144:147], v5 offset:20480\n" \
"v_cvt_pk_bf16_f32 v141, v90, v91\n" \
"v_mfma_f32_16x16x32_bf16 v[124:127], a[152:155], a[88:91], v[124:127]\n" \
"s_add_u32 m0, s49, 32768\n" \
"v_exp_f32 v93, v93\n" \
"v_mfma_f32_16x16x32_bf16 v[100:103], a[156:159], v[44:47], v[100:103]\n" \
"buffer_load_dwordx4 v6, s[40:43], s16 offen offset:3072 sc1 lds\n" \
"v_mfma_f32_16x16x32_bf16 v[108:111], a[156:159], v[60:63], v[108:111]\n" \
"v_exp_f32 v94, v94\n" \
"v_mfma_f32_16x16x32_bf16 v[116:119], a[156:159], a[76:79], v[116:119]\n" \
"ds_read_b128 a[148:151], v5 offset:21504\n" \
"v_cvt_pk_bf16_f32 v142, v92, v93\n" \
"v_mfma_f32_16x16x32_bf16 v[124:127], a[156:159], a[92:95], v[124:127]\n" \
"s_waitcnt lgkmcnt(4)\n" \
"v_exp_f32 v95, v95\n" \
"s_add_u32 m0, s79, 156544\n" \
"v_mfma_f32_16x16x32_bf16 v[160:163], v[180:183], v[128:131], v[160:163]\n" \
"buffer_load_dwordx4 v12, s[28:31], s81 offen offset:128 lds\n" \
"v_mfma_f32_16x16x32_bf16 v[164:167], v[180:183], v[132:135], v[164:167]\n" \
"s_mov_b32 s81, 262144\n" \
"v_cvt_pk_bf16_f32 v143, v94, v95\n" \
"v_mfma_f32_16x16x32_bf16 v[168:171], v[180:183], v[136:139], v[168:171]\n" \
"buffer_store_dwordx4 v[28:31], v4, s[36:39], s80 offen offset:3072 sc1\n" \
"v_mfma_f32_16x16x32_bf16 v[172:175], v[180:183], v[140:143], v[172:175]\n" \
"s_add_u32 s80, s78, 8192\n" \
"v_exp_f32 v96, v96\n" \
"v_mfma_f32_16x16x32_bf16 v[192:195], a[128:131], v[128:131], v[192:195]\n" \
"global_load_dwordx4 a[100:103], v189, s[60:61]\n" \
"v_mfma_f32_16x16x32_bf16 v[224:227], a[128:131], v[132:135], v[224:227]\n" \
"v_exp_f32 v97, v97\n" \
"v_mfma_f32_16x16x32_bf16 a[0:3], a[128:131], v[136:139], a[0:3]\n" \
"ds_read_b128 a[152:155], v5 offset:22528\n" \
"v_cvt_pk_bf16_f32 v144, v96, v97\n" \
"v_mfma_f32_16x16x32_bf16 a[32:35], a[128:131], v[140:143], a[32:35]\n" \
"buffer_store_dwordx4 v[28:31], v4, s[36:39], s80 offen offset:0 sc1\n" \
"v_mfma_f32_16x16x32_bf16 v[196:199], a[132:135], v[128:131], v[196:199]\n" \
"v_exp_f32 v98, v98\n" \
"v_mfma_f32_16x16x32_bf16 v[228:231], a[132:135], v[132:135], v[228:231]\n" \
"global_load_dwordx4 a[104:107], v190, s[60:61]\n" \
"v_mfma_f32_16x16x32_bf16 a[4:7], a[132:135], v[136:139], a[4:7]\n" \
"ds_read_b128 a[156:159], v5 offset:23552\n" \
"v_mfma_f32_16x16x32_bf16 a[36:39], a[132:135], v[140:143], a[36:39]\n" \
"s_waitcnt vmcnt(18) lgkmcnt(4)\n" \
"v_exp_f32 v99, v99\n" \
"v_mfma_f32_16x16x32_bf16 v[200:203], a[136:139], v[128:131], v[200:203]\n" \
"s_barrier\n" \
"v_exp_f32 v100, v100\n" \
"v_mfma_f32_16x16x32_bf16 v[232:235], a[136:139], v[132:135], v[232:235]\n" \
"s_add_u32 s17, s17, s19\n" \
"v_exp_f32 v101, v101\n" \
"v_mfma_f32_16x16x32_bf16 a[8:11], a[136:139], v[136:139], a[8:11]\n" \
"ds_read_b128 a[128:131], v5 offset:32768\n" \
"v_cvt_pk_bf16_f32 v145, v98, v99\n" \
"v_mfma_f32_16x16x32_bf16 a[40:43], a[136:139], v[140:143], a[40:43]\n" \
"s_add_u32 m0, s49, 49152\n" \
"v_exp_f32 v102, v102\n" \
"v_mfma_f32_16x16x32_bf16 v[204:207], a[140:143], v[128:131], v[204:207]\n" \
"buffer_load_dwordx4 v6, s[40:43], s17 offen offset:0 sc1 lds\n" \
"v_mfma_f32_16x16x32_bf16 v[236:239], a[140:143], v[132:135], v[236:239]\n" \
"v_exp_f32 v103, v103\n" \
"v_mfma_f32_16x16x32_bf16 a[12:15], a[140:143], v[136:139], a[12:15]\n" \
"ds_read_b128 a[132:135], v5 offset:33792\n" \
"v_cvt_pk_bf16_f32 v146, v100, v101\n" \
"v_mfma_f32_16x16x32_bf16 a[44:47], a[140:143], v[140:143], a[44:47]\n" \
"s_waitcnt lgkmcnt(4)\n" \
"v_exp_f32 v104, v104\n" \
"v_mfma_f32_16x16x32_bf16 v[208:211], a[144:147], v[128:131], v[208:211]\n" \
"v_exp_f32 v105, v105\n" \
"v_mfma_f32_16x16x32_bf16 v[240:243], a[144:147], v[132:135], v[240:243]\n" \
"v_mfma_f32_16x16x32_bf16 a[16:19], a[144:147], v[136:139], a[16:19]\n" \
"ds_read_b128 a[136:139], v5 offset:34816\n" \
"v_cvt_pk_bf16_f32 v147, v102, v103\n" \
"v_mfma_f32_16x16x32_bf16 a[48:51], a[144:147], v[140:143], a[48:51]\n" \
"s_add_u32 m0, s49, 49152\n" \
"v_exp_f32 v106, v106\n" \
"v_mfma_f32_16x16x32_bf16 v[212:215], a[148:151], v[128:131], v[212:215]\n" \
"buffer_load_dwordx4 v6, s[40:43], s17 offen offset:1024 sc1 lds\n" \
"v_mfma_f32_16x16x32_bf16 v[244:247], a[148:151], v[132:135], v[244:247]\n" \
"v_exp_f32 v107, v107\n" \
"v_mfma_f32_16x16x32_bf16 a[20:23], a[148:151], v[136:139], a[20:23]\n" \
"ds_read_b128 a[140:143], v5 offset:35840\n" \
"v_cvt_pk_bf16_f32 v148, v104, v105\n" \
"v_mfma_f32_16x16x32_bf16 a[52:55], a[148:151], v[140:143], a[52:55]\n" \
"s_waitcnt lgkmcnt(4)\n" \
"s_add_u32 m0, s49, 49152\n" \
"ds_read_b64_tr_b16 a[160:161], v13 offset:0\n" \
"v_mfma_f32_16x16x32_bf16 v[216:219], a[152:155], v[128:131], v[216:219]\n" \
"buffer_load_dwordx4 v6, s[40:43], s17 offen offset:2048 sc1 lds\n" \
"v_mfma_f32_16x16x32_bf16 v[248:251], a[152:155], v[132:135], v[248:251]\n" \
"v_exp_f32 v108, v108\n" \
"v_mfma_f32_16x16x32_bf16 a[24:27], a[152:155], v[136:139], a[24:27]\n" \
"ds_read_b128 a[144:147], v5 offset:36864\n" \
"v_cvt_pk_bf16_f32 v149, v106, v107\n" \
"v_mfma_f32_16x16x32_bf16 a[56:59], a[152:155], v[140:143], a[56:59]\n" \
"s_add_u32 m0, s49, 49152\n" \
"v_exp_f32 v109, v109\n" \
"v_mfma_f32_16x16x32_bf16 v[220:223], a[156:159], v[128:131], v[220:223]\n" \
"buffer_load_dwordx4 v6, s[40:43], s17 offen offset:3072 sc1 lds\n" \
"v_mfma_f32_16x16x32_bf16 v[252:255], a[156:159], v[132:135], v[252:255]\n" \
"v_exp_f32 v110, v110\n" \
"v_mfma_f32_16x16x32_bf16 a[28:31], a[156:159], v[136:139], a[28:31]\n" \
"ds_read_b128 a[148:151], v5 offset:37888\n" \
"v_exp_f32 v111, v111\n" \
"v_mfma_f32_16x16x32_bf16 a[60:63], a[156:159], v[140:143], a[60:63]\n" \
"s_waitcnt lgkmcnt(4)\n" \
"ds_read_b64_tr_b16 a[162:163], v14 offset:0\n" \
"v_cvt_pk_bf16_f32 v150, v108, v109\n" \
"v_cvt_pk_bf16_f32 v151, v110, v111\n" \
"v_mfma_f32_16x16x32_bf16 v[64:67], a[128:131], v[32:35], 0\n" \
"global_load_dwordx4 a[108:111], v191, s[60:61]\n" \
"v_mfma_f32_16x16x32_bf16 v[72:75], a[128:131], v[48:51], 0\n" \
"s_add_u32 s16, s16, s9\n" \
"ds_read_b64_tr_b16 a[164:165], v15 offset:0\n" \
"s_lshl_b32 s92, %2, 9\n" \
"v_mfma_f32_16x16x32_bf16 v[80:83], a[128:131], a[64:67], 0\n" \
"s_add_u32 s92, s92, 94375936\n" \
"ds_read_b128 a[152:155], v5 offset:38912\n" \
"v_lshrrev_b32 v17, 1, v4\n" \
"v_mfma_f32_16x16x32_bf16 v[88:91], a[128:131], a[80:83], 0\n" \
"s_add_u32 m0, s48, 0\n" \
"ds_read_b64_tr_b16 a[166:167], v16 offset:0\n" \
"v_add_u32 v17, s92, v17\n" \
"v_mfma_f32_16x16x32_bf16 v[64:67], a[132:135], v[36:39], v[64:67]\n" \
"buffer_load_dwordx4 v6, s[40:43], s16 offen offset:0 sc1 lds\n" \
"v_mfma_f32_16x16x32_bf16 v[72:75], a[132:135], v[52:55], v[72:75]\n" \
"s_mov_b64 s[4:5], 0\n" \
"ds_read_b64_tr_b16 a[168:169], v13 offset:8192\n" \
"v_mfma_f32_16x16x32_bf16 v[80:83], a[132:135], a[68:71], v[80:83]\n" \
"s_add_u32 s100, s100, 262144\ns_addc_u32 s101, s101, 0\n" \
"ds_read_b128 a[156:159], v5 offset:39936\n" \
"v_mfma_f32_16x16x32_bf16 v[88:91], a[132:135], a[84:87], v[88:91]\n" \
"s_waitcnt lgkmcnt(4)\n" \
"v_exp_f32 v112, v112\n" \
"v_mfma_f32_16x16x32_bf16 v[64:67], a[136:139], v[40:43], v[64:67]\n" \
"s_mov_b64 exec, 0xffff\nglobal_load_dword v1, v2, s[100:101]\ns_mov_b64 exec, -1\n" \
"v_mfma_f32_16x16x32_bf16 v[72:75], a[136:139], v[56:59], v[72:75]\n" \
"s_xor_b32 s49, s49, 0x10000\n" \
"v_exp_f32 v113, v113\n" \
"v_mfma_f32_16x16x32_bf16 v[80:83], a[136:139], a[72:75], v[80:83]\n" \
"LH16_%=:\ns_cbranch_execz LH20_%=\n" \
"s_add_u32 s100, s100, 262144\ns_addc_u32 s101, s101, 0\n" \
"ds_read_b128 a[128:131], v5 offset:49152\n" \
"v_mfma_f32_16x16x32_bf16 v[88:91], a[136:139], a[88:91], v[88:91]\n" \
"s_add_u32 m0, s48, 0\n" \
"v_exp_f32 v114, v114\n" \
"v_mfma_f32_16x16x32_bf16 v[64:67], a[140:143], v[44:47], v[64:67]\n" \
"buffer_load_dwordx4 v6, s[40:43], s16 offen offset:1024 sc1 lds\n" \
"v_mfma_f32_16x16x32_bf16 v[72:75], a[140:143], v[60:63], v[72:75]\n" \
"v_exp_f32 v115, v115\n" \
"v_mfma_f32_16x16x32_bf16 v[80:83], a[140:143], a[76:79], v[80:83]\n" \
"ds_read_b128 a[132:135], v5 offset:50176\n" \
"v_cvt_pk_bf16_f32 v152, v112, v113\n" \
"v_mfma_f32_16x16x32_bf16 v[88:91], a[140:143], a[92:95], v[88:91]\n" \
"s_waitcnt lgkmcnt(4)\n" \
"v_exp_f32 v116, v116\n" \
"v_mfma_f32_16x16x32_bf16 v[68:71], a[144:147], v[32:35], 0\n" \
"v_exp_f32 v117, v117\n" \
"v_mfma_f32_16x16x32_bf16 v[76:79], a[144:147], v[48:51], 0\n" \
"v_exp_f32 v118, v118\n" \
"v_mfma_f32_16x16x32_bf16 v[84:87], a[144:147], a[64:67], 0\n" \
"ds_read_b128 a[136:139], v5 offset:51200\n" \
"v_cvt_pk_bf16_f32 v153, v114, v115\n" \
"v_mfma_f32_16x16x32_bf16 v[92:95], a[144:147], a[80:83], 0\n" \
"s_add_u32 m0, s48, 0\n" \
"v_exp_f32 v119, v119\n" \
"v_mfma_f32_16x16x32_bf16 v[68:71], a[148:151], v[36:39], v[68:71]\n" \
"buffer_load_dwordx4 v6, s[40:43], s16 offen offset:2048 sc1 lds\n" \
"v_mfma_f32_16x16x32_bf16 v[76:79], a[148:151], v[52:55], v[76:79]\n" \
"v_exp_f32 v120, v120\n" \
"v_mfma_f32_16x16x32_bf16 v[84:87], a[148:151], a[68:71], v[84:87]\n" \
"ds_read_b128 a[140:143], v5 offset:52224\n" \
"v_cvt_pk_bf16_f32 v154, v116, v117\n" \
"v_mfma_f32_16x16x32_bf16 v[92:95], a[148:151], a[84:87], v[92:95]\n" \
"s_waitcnt lgkmcnt(4)\n" \
"v_exp_f32 v121, v121\n" \
"v_mfma_f32_16x16x32_bf16 v[68:71], a[152:155], v[40:43], v[68:71]\n" \
"v_exp_f32 v122, v122\n" \
"v_mfma_f32_16x16x32_bf16 v[76:79], a[152:155], v[56:59], v[76:79]\n" \
"v_exp_f32 v123, v123\n" \
"v_mfma_f32_16x16x32_bf16 v[84:87], a[152:155], a[72:75], v[84:87]\n" \
"ds_read_b128 a[144:147], v5 offset:53248\n" \
"v_cvt_pk_bf16_f32 v155, v118, v119\n" \
"v_mfma_f32_16x16x32_bf16 v[92:95], a[152:155], a[88:91], v[92:95]\n" \
"s_add_u32 m0, s48, 0\n" \
"v_exp_f32 v124, v124\n" \
"v_mfma_f32_16x16x32_bf16 v[68:71], a[156:159], v[44:47], v[68:71]\n" \
"buffer_load_dwordx4 v6, s[40:43], s16 offen offset:3072 sc1 lds\n" \
"v_mfma_f32_16x16x32_bf16 v[76:79], a[156:159], v[60:63], v[76:79]\n" \
"v_exp_f32 v125, v125\n" \
"v_mfma_f32_16x16x32_bf16 v[84:87], a[156:159], a[76:79], v[84:87]\n" \
"ds_read_b128 a[148:151], v5 offset:54272\n" \
"v_cvt_pk_bf16_f32 v156, v120, v121\n" \
"v_mfma_f32_16x16x32_bf16 v[92:95], a[156:159], a[92:95], v[92:95]\n" \
"s_waitcnt lgkmcnt(4)\n" \
"v_exp_f32 v126, v126\n" \
"v_mfma_f32_16x16x32_bf16 v[160:163], v[180:183], v[144:147], v[160:163]\n" \
"v_exp_f32 v127, v127\n" \
"v_mfma_f32_16x16x32_bf16 v[164:167], v[180:183], v[148:151], v[164:167]\n" \
"s_mov_b64 exec, 0xffff\nglobal_load_dword v1, v2, s[100:101]\ns_mov_b64 exec, -1\n" \
"v_cvt_pk_bf16_f32 v157, v122, v123\n" \
"v_cvt_pk_bf16_f32 v158, v124, v125\n" \
"v_cvt_pk_bf16_f32 v159, v126, v127\n" \
"v_mfma_f32_16x16x32_bf16 v[168:171], v[180:183], v[152:155], v[168:171]\n" \
"ds_read_b64_tr_b16 a[170:171], v14 offset:8192\n" \
"v_mfma_f32_16x16x32_bf16 v[172:175], v[180:183], v[156:159], v[172:175]\n" \
"ds_read_b64_tr_b16 a[172:173], v15 offset:8192\n" \
"v_mfma_f32_16x16x32_bf16 v[192:195], a[128:131], v[144:147], v[192:195]\n" \
"ds_read_b64_tr_b16 a[174:175], v16 offset:8192\n" \
"v_mfma_f32_16x16x32_bf16 v[224:227], a[128:131], v[148:151], v[224:227]\n" \
"v_exp_f32 v64, v64\n" \
"v_mfma_f32_16x16x32_bf16 a[0:3], a[128:131], v[152:155], a[0:3]\n" \
"ds_read_b128 a[152:155], v5 offset:55296\n" \
"v_mfma_f32_16x16x32_bf16 a[32:35], a[128:131], v[156:159], a[32:35]\n" \
"v_exp_f32 v65, v65\n" \
"v_mfma_f32_16x16x32_bf16 v[196:199], a[132:135], v[144:147], v[196:199]\n" \
"v_exp_f32 v66, v66\n" \
"v_mfma_f32_16x16x32_bf16 v[228:231], a[132:135], v[148:151], v[228:231]\n" \
"s_waitcnt lgkmcnt(1)\n" \
"v_exp_f32 v67, v67\n" \
"v_mfma_f32_16x16x32_bf16 a[4:7], a[132:135], v[152:155], a[4:7]\n" \
"ds_read_b128 a[156:159], v5 offset:56320\n" \
"v_cvt_pk_bf16_f32 v128, v64, v65\n" \
"v_mfma_f32_16x16x32_bf16 a[36:39], a[132:135], v[156:159], a[36:39]\n" \
"s_waitcnt vmcnt(15) lgkmcnt(4)\n" \
"v_exp_f32 v68, v68\n" \
"v_mfma_f32_16x16x32_bf16 v[200:203], a[136:139], v[144:147], v[200:203]\n" \
"s_barrier\n" \
"v_xor_b32 v5, 0x10000, v5\n" \
"v_cvt_pk_bf16_f32 v129, v66, v67\n" \
"v_mfma_f32_16x16x32_bf16 v[232:235], a[136:139], v[148:151], v[232:235]\n" \
"s_add_u32 s17, s17, s19\n" \
"v_exp_f32 v69, v69\n" \
"v_mfma_f32_16x16x32_bf16 a[8:11], a[136:139], v[152:155], a[8:11]\n" \
"ds_read_b128 a[128:131], v5 offset:0\n" \
"v_cvt_pk_bf16_f32 v130, v68, v69\n" \
"v_mfma_f32_16x16x32_bf16 a[40:43], a[136:139], v[156:159], a[40:43]\n" \
"s_add_u32 m0, s48, 16384\n" \
"v_exp_f32 v70, v70\n" \
"v_mfma_f32_16x16x32_bf16 v[204:207], a[140:143], v[144:147], v[204:207]\n" \
"buffer_load_dwordx4 v6, s[40:43], s17 offen offset:0 sc1 lds\n" \
"v_mfma_f32_16x16x32_bf16 v[236:239], a[140:143], v[148:151], v[236:239]\n" \
"v_exp_f32 v71, v71\n" \
"v_mfma_f32_16x16x32_bf16 a[12:15], a[140:143], v[152:155], a[12:15]\n" \
"ds_read_b128 a[132:135], v5 offset:1024\n" \
"v_cvt_pk_bf16_f32 v131, v70, v71\n" \
"v_mfma_f32_16x16x32_bf16 a[44:47], a[140:143], v[156:159], a[44:47]\n" \
"s_waitcnt lgkmcnt(4)\n" \
"v_exp_f32 v72, v72\n" \
"v_mfma_f32_16x16x32_bf16 v[208:211], a[144:147], v[144:147], v[208:211]\n" \
"v_exp_f32 v73, v73\n" \
"v_mfma_f32_16x16x32_bf16 v[240:243], a[144:147], v[148:151], v[240:243]\n" \
"v_mfma_f32_16x16x32_bf16 a[16:19], a[144:147], v[152:155], a[16:19]\n" \
"ds_read_b128 a[136:139], v5 offset:2048\n" \
"v_cvt_pk_bf16_f32 v132, v72, v73\n" \
"v_mfma_f32_16x16x32_bf16 a[48:51], a[144:147], v[156:159], a[48:51]\n" \
"s_add_u32 m0, s48, 16384\n" \
"v_exp_f32 v74, v74\n" \
"v_mfma_f32_16x16x32_bf16 v[212:215], a[148:151], v[144:147], v[212:215]\n" \
"buffer_load_dwordx4 v6, s[40:43], s17 offen offset:1024 sc1 lds\n" \
"v_mfma_f32_16x16x32_bf16 v[244:247], a[148:151], v[148:151], v[244:247]\n" \
"v_exp_f32 v75, v75\n" \
"v_mfma_f32_16x16x32_bf16 a[20:23], a[148:151], v[152:155], a[20:23]\n" \
"ds_read_b128 a[140:143], v5 offset:3072\n" \
"v_cvt_pk_bf16_f32 v133, v74, v75\n" \
"v_mfma_f32_16x16x32_bf16 a[52:55], a[148:151], v[156:159], a[52:55]\n" \
"s_waitcnt lgkmcnt(4)\n" \
"s_add_u32 m0, s48, 16384\n" \
"ds_read_b64_tr_b16 a[176:177], v13 offset:16384\n" \
"v_mfma_f32_16x16x32_bf16 v[216:219], a[152:155], v[144:147], v[216:219]\n" \
"buffer_load_dwordx4 v6, s[40:43], s17 offen offset:2048 sc1 lds\n" \
"v_mfma_f32_16x16x32_bf16 v[248:251], a[152:155], v[148:151], v[248:251]\n" \
"s_add_u32 s44, s44, 1\n" \
"v_exp_f32 v76, v76\n" \
"v_mfma_f32_16x16x32_bf16 a[24:27], a[152:155], v[152:155], a[24:27]\n" \
"ds_read_b128 a[144:147], v5 offset:4096\n" \
"v_mfma_f32_16x16x32_bf16 a[56:59], a[152:155], v[156:159], a[56:59]\n" \
"s_add_u32 m0, s48, 16384\n" \
"v_exp_f32 v77, v77\n" \
"v_mfma_f32_16x16x32_bf16 v[220:223], a[156:159], v[144:147], v[220:223]\n" \
"buffer_load_dwordx4 v6, s[40:43], s17 offen offset:3072 sc1 lds\n" \
"v_mfma_f32_16x16x32_bf16 v[252:255], a[156:159], v[148:151], v[252:255]\n" \
"s_xor_b32 s48, s48, 0x10000\n" \
"v_exp_f32 v78, v78\n" \
"v_mfma_f32_16x16x32_bf16 a[28:31], a[156:159], v[152:155], a[28:31]\n" \
"ds_read_b128 a[148:151], v5 offset:5120\n" \
"v_exp_f32 v79, v79\n" \
"v_mfma_f32_16x16x32_bf16 a[60:63], a[156:159], v[156:159], a[60:63]\n" \
"s_waitcnt lgkmcnt(4)\n" \
"ds_read_b64_tr_b16 a[178:179], v14 offset:16384\n" \
"v_cvt_pk_bf16_f32 v134, v76, v77\n" \
"v_cvt_pk_bf16_f32 v135, v78, v79\n" \
"v_mfma_f32_16x16x32_bf16 v[96:99], a[128:131], v[32:35], 0\n" \
"ds_read_b64_tr_b16 a[180:181], v15 offset:16384\n" \
"v_mfma_f32_16x16x32_bf16 v[104:107], a[128:131], v[48:51], 0\n" \
"s_add_u32 s16, s16, s9\n" \
"ds_read_b64_tr_b16 a[182:183], v16 offset:16384\n" \
"v_mfma_f32_16x16x32_bf16 v[112:115], a[128:131], a[64:67], 0\n" \
"ds_read_b128 a[152:155], v5 offset:6144\n" \
"v_mfma_f32_16x16x32_bf16 v[120:123], a[128:131], a[80:83], 0\n" \
"s_add_u32 m0, s49, 32768\n" \
"v_exp_f32 v80, v80\n" \
"v_mfma_f32_16x16x32_bf16 v[96:99], a[132:135], v[36:39], v[96:99]\n" \
"buffer_load_dwordx4 v6, s[40:43], s16 offen offset:0 sc1 lds\n" \
"v_mfma_f32_16x16x32_bf16 v[104:107], a[132:135], v[52:55], v[104:107]\n" \
"ds_read_b64_tr_b16 a[184:185], v13 offset:24576\n" \
"v_mfma_f32_16x16x32_bf16 v[112:115], a[132:135], a[68:71], v[112:115]\n" \
"ds_read_b128 a[156:159], v5 offset:7168\n" \
"v_mfma_f32_16x16x32_bf16 v[120:123], a[132:135], a[84:87], v[120:123]\n" \
"s_waitcnt lgkmcnt(4)\n" \
"v_exp_f32 v81, v81\n" \
"s_add_u32 m0, s79, 131072\n" \
"v_mfma_f32_16x16x32_bf16 v[96:99], a[136:139], v[40:43], v[96:99]\n" \
"buffer_load_dwordx4 v12, s[28:31], s81 offen offset:0 lds\n" \
"v_mfma_f32_16x16x32_bf16 v[104:107], a[136:139], v[56:59], v[104:107]\n" \
"v_exp_f32 v82, v82\n" \
"v_mfma_f32_16x16x32_bf16 v[112:115], a[136:139], a[72:75], v[112:115]\n" \
"ds_read_b128 a[128:131], v5 offset:16384\n" \
"v_cvt_pk_bf16_f32 v136, v80, v81\n" \
"v_mfma_f32_16x16x32_bf16 v[120:123], a[136:139], a[88:91], v[120:123]\n" \
"s_add_u32 m0, s49, 32768\n" \
"v_exp_f32 v83, v83\n" \
"v_mfma_f32_16x16x32_bf16 v[96:99], a[140:143], v[44:47], v[96:99]\n" \
"buffer_load_dwordx4 v6, s[40:43], s16 offen offset:1024 sc1 lds\n" \
"v_mfma_f32_16x16x32_bf16 v[104:107], a[140:143], v[60:63], v[104:107]\n" \
"ds_read_b64_tr_b16 a[186:187], v14 offset:24576\n" \
"v_cvt_pk_bf16_f32 v137, v82, v83\n" \
"v_mfma_f32_16x16x32_bf16 v[112:115], a[140:143], a[76:79], v[112:115]\n" \
"ds_read_b128 a[132:135], v5 offset:17408\n" \
"v_mfma_f32_16x16x32_bf16 v[120:123], a[140:143], a[92:95], v[120:123]\n" \
"s_waitcnt lgkmcnt(4)\n" \
"v_exp_f32 v84, v84\n" \
"v_mfma_f32_16x16x32_bf16 v[100:103], a[144:147], v[32:35], 0\n" \
"v_exp_f32 v85, v85\n" \
"v_mfma_f32_16x16x32_bf16 v[108:111], a[144:147], v[48:51], 0\n" \
"v_exp_f32 v86, v86\n" \
"v_mfma_f32_16x16x32_bf16 v[116:119], a[144:147], a[64:67], 0\n" \
"ds_read_b128 a[136:139], v5 offset:18432\n" \
"v_cvt_pk_bf16_f32 v138, v84, v85\n" \
"v_mfma_f32_16x16x32_bf16 v[124:127], a[144:147], a[80:83], 0\n" \
"s_add_u32 m0, s49, 32768\n" \
"v_exp_f32 v87, v87\n" \
"v_mfma_f32_16x16x32_bf16 v[100:103], a[148:151], v[36:39], v[100:103]\n" \
"buffer_load_dwordx4 v6, s[40:43], s16 offen offset:2048 sc1 lds\n" \
"v_mfma_f32_16x16x32_bf16 v[108:111], a[148:151], v[52:55], v[108:111]\n" \
"v_exp_f32 v88, v88\n" \
"v_mfma_f32_16x16x32_bf16 v[116:119], a[148:151], a[68:71], v[116:119]\n" \
"ds_read_b128 a[140:143], v5 offset:19456\n" \
"v_cvt_pk_bf16_f32 v139, v86, v87\n" \
"v_mfma_f32_16x16x32_bf16 v[124:127], a[148:151], a[84:87], v[124:127]\n" \
"s_waitcnt lgkmcnt(4)\n" \
"v_exp_f32 v89, v89\n" \
"v_mfma_f32_16x16x32_bf16 v[100:103], a[152:155], v[40:43], v[100:103]\n" \
"v_exp_f32 v90, v90\n" \
"v_mfma_f32_16x16x32_bf16 v[108:111], a[152:155], v[56:59], v[108:111]\n" \
"v_exp_f32 v91, v91\n" \
"v_mfma_f32_16x16x32_bf16 v[116:119], a[152:155], a[72:75], v[116:119]\n" \
"ds_read_b128 a[144:147], v5 offset:20480\n" \
"v_cvt_pk_bf16_f32 v140, v88, v89\n" \
"v_mfma_f32_16x16x32_bf16 v[124:127], a[152:155], a[88:91], v[124:127]\n" \
"s_add_u32 m0, s49, 32768\n" \
"v_exp_f32 v92, v92\n" \
"v_mfma_f32_16x16x32_bf16 v[100:103], a[156:159], v[44:47], v[100:103]\n" \
"buffer_load_dwordx4 v6, s[40:43], s16 offen offset:3072 sc1 lds\n" \
"v_mfma_f32_16x16x32_bf16 v[108:111], a[156:159], v[60:63], v[108:111]\n" \
"v_exp_f32 v93, v93\n" \
"v_mfma_f32_16x16x32_bf16 v[116:119], a[156:159], a[76:79], v[116:119]\n" \
"ds_read_b128 a[148:151], v5 offset:21504\n" \
"v_cvt_pk_bf16_f32 v141, v90, v91\n" \
"v_mfma_f32_16x16x32_bf16 v[124:127], a[156:159], a[92:95], v[124:127]\n" \
"s_waitcnt lgkmcnt(4)\n" \
"v_exp_f32 v94, v94\n" \
"v_mfma_f32_16x16x32_bf16 v[160:163], v[180:183], v[128:131], v[160:163]\n" \
"s_add_u32 m0, s79, 131968\n" \
"v_exp_f32 v95, v95\n" \
"v_mfma_f32_16x16x32_bf16 v[164:167], v[180:183], v[132:135], v[164:167]\n" \
"buffer_load_dwordx4 v12, s[28:31], s81 offen offset:128 lds\n" \
"v_cvt_pk_bf16_f32 v142, v92, v93\n" \
"v_cvt_pk_bf16_f32 v143, v94, v95\n" \
"v_mfma_f32_16x16x32_bf16 v[168:171], v[180:183], v[136:139], v[168:171]\n" \
"s_mov_b32 s81, 327680\n" \
"ds_read_b64_tr_b16 a[188:189], v15 offset:24576\n" \
"v_mfma_f32_16x16x32_bf16 v[172:175], v[180:183], v[140:143], v[172:175]\n" \
"ds_read_b64_tr_b16 a[190:191], v16 offset:24576\n" \
"v_mfma_f32_16x16x32_bf16 v[192:195], a[128:131], v[128:131], v[192:195]\n" \
"s_add_u32 m0, s79, 139264\n" \
"v_exp_f32 v96, v96\n" \
"v_mfma_f32_16x16x32_bf16 v[224:227], a[128:131], v[132:135], v[224:227]\n" \
"buffer_load_dwordx4 v12, s[28:31], s81 offen offset:0 lds\n" \
"v_mfma_f32_16x16x32_bf16 a[0:3], a[128:131], v[136:139], a[0:3]\n" \
"ds_read_b128 a[152:155], v5 offset:22528\n" \
"v_mfma_f32_16x16x32_bf16 a[32:35], a[128:131], v[140:143], a[32:35]\n" \
"v_exp_f32 v97, v97\n" \
"v_mfma_f32_16x16x32_bf16 v[196:199], a[132:135], v[128:131], v[196:199]\n" \
"v_exp_f32 v98, v98\n" \
"v_mfma_f32_16x16x32_bf16 v[228:231], a[132:135], v[132:135], v[228:231]\n" \
"s_waitcnt lgkmcnt(1)\n" \
"v_exp_f32 v99, v99\n" \
"v_mfma_f32_16x16x32_bf16 a[4:7], a[132:135], v[136:139], a[4:7]\n" \
"ds_read_b128 a[156:159], v5 offset:23552\n" \
"v_cvt_pk_bf16_f32 v144, v96, v97\n" \
"v_mfma_f32_16x16x32_bf16 a[36:39], a[132:135], v[140:143], a[36:39]\n" \
"s_waitcnt vmcnt(18) lgkmcnt(4)\n" \
"v_exp_f32 v100, v100\n" \
"v_mfma_f32_16x16x32_bf16 v[200:203], a[136:139], v[128:131], v[200:203]\n" \
"s_barrier\n" \
"v_exp_f32 v101, v101\n" \
"v_mfma_f32_16x16x32_bf16 v[232:235], a[136:139], v[132:135], v[232:235]\n" \
"s_add_u32 s17, s17, s19\n" \
"v_cvt_pk_bf16_f32 v145, v98, v99\n" \
"v_cvt_pk_bf16_f32 v146, v100, v101\n" \
"v_mfma_f32_16x16x32_bf16 a[8:11], a[136:139], v[136:139], a[8:11]\n" \
"ds_read_b128 a[128:131], v5 offset:32768\n" \
"v_mfma_f32_16x16x32_bf16 a[40:43], a[136:139], v[140:143], a[40:43]\n" \
"s_add_u32 m0, s49, 49152\n" \
"v_exp_f32 v102, v102\n" \
"v_mfma_f32_16x16x32_bf16 v[204:207], a[140:143], v[128:131], v[204:207]\n" \
"buffer_load_dwordx4 v6, s[40:43], s17 offen offset:0 sc1 lds\n" \
"v_mfma_f32_16x16x32_bf16 v[236:239], a[140:143], v[132:135], v[236:239]\n" \
"v_exp_f32 v103, v103\n" \
"v_mfma_f32_16x16x32_bf16 a[12:15], a[140:143], v[136:139], a[12:15]\n" \
"ds_read_b128 a[132:135], v5 offset:33792\n" \
"v_cvt_pk_bf16_f32 v147, v102, v103\n" \
"v_mfma_f32_16x16x32_bf16 a[44:47], a[140:143], v[140:143], a[44:47]\n" \
"s_waitcnt lgkmcnt(4)\n" \
"v_exp_f32 v104, v104\n" \
"v_mfma_f32_16x16x32_bf16 v[208:211], a[144:147], v[128:131], v[208:211]\n" \
"v_exp_f32 v105, v105\n" \
"v_mfma_f32_16x16x32_bf16 v[240:243], a[144:147], v[132:135], v[240:243]\n" \
"v_mfma_f32_16x16x32_bf16 a[16:19], a[144:147], v[136:139], a[16:19]\n" \
"ds_read_b128 a[136:139], v5 offset:34816\n" \
"v_cvt_pk_bf16_f32 v148, v104, v105\n" \
"v_mfma_f32_16x16x32_bf16 a[48:51], a[144:147], v[140:143], a[48:51]\n" \
"s_add_u32 m0, s49, 49152\n" \
"v_exp_f32 v106, v106\n" \
"v_mfma_f32_16x16x32_bf16 v[212:215], a[148:151], v[128:131], v[212:215]\n" \
"buffer_load_dwordx4 v6, s[40:43], s17 offen offset:1024 sc1 lds\n" \
"v_mfma_f32_16x16x32_bf16 v[244:247], a[148:151], v[132:135], v[244:247]\n" \
"v_exp_f32 v107, v107\n" \
"v_mfma_f32_16x16x32_bf16 a[20:23], a[148:151], v[136:139], a[20:23]\n" \
"ds_read_b128 a[140:143], v5 offset:35840\n" \
"v_cvt_pk_bf16_f32 v149, v106, v107\n" \
"v_mfma_f32_16x16x32_bf16 a[52:55], a[148:151], v[140:143], a[52:55]\n" \
"s_waitcnt lgkmcnt(4)\n" \
"s_add_u32 m0, s49, 49152\n" \
"v_mfma_f32_16x16x32_bf16 v[216:219], a[152:155], v[128:131], v[216:219]\n" \
"buffer_load_dwordx4 v6, s[40:43], s17 offen offset:2048 sc1 lds\n" \
"v_mfma_f32_16x16x32_bf16 v[248:251], a[152:155], v[132:135], v[248:251]\n" \
"v_exp_f32 v108, v108\n" \
"v_mfma_f32_16x16x32_bf16 a[24:27], a[152:155], v[136:139], a[24:27]\n" \
"ds_read_b128 a[144:147], v5 offset:36864\n" \
"v_mfma_f32_16x16x32_bf16 a[56:59], a[152:155], v[140:143], a[56:59]\n" \
"s_add_u32 m0, s49, 49152\n" \
"v_exp_f32 v109, v109\n" \
"v_mfma_f32_16x16x32_bf16 v[220:223], a[156:159], v[128:131], v[220:223]\n" \
"buffer_load_dwordx4 v6, s[40:43], s17 offen offset:3072 sc1 lds\n" \
"v_mfma_f32_16x16x32_bf16 v[252:255], a[156:159], v[132:135], v[252:255]\n" \
"v_exp_f32 v110, v110\n" \
"v_mfma_f32_16x16x32_bf16 a[28:31], a[156:159], v[136:139], a[28:31]\n" \
"ds_read_b128 a[148:151], v5 offset:37888\n" \
"v_exp_f32 v111, v111\n" \
"v_mfma_f32_16x16x32_bf16 a[60:63], a[156:159], v[140:143], a[60:63]\n" \
"s_waitcnt lgkmcnt(4)\n" \
"v_cvt_pk_bf16_f32 v150, v108, v109\n" \
"v_cvt_pk_bf16_f32 v151, v110, v111\n" \
"v_mfma_f32_16x16x32_bf16 v[64:67], a[128:131], v[32:35], 0\n" \
"buffer_load_dwordx2 v[18:19], v17, s[36:39], 0 offen sc1\n" \
"v_mfma_f32_16x16x32_bf16 v[72:75], a[128:131], v[48:51], 0\n" \
"s_add_u32 s16, s16, s9\n" \
"v_exp_f32 v112, v112\n" \
"v_mfma_f32_16x16x32_bf16 v[80:83], a[128:131], a[64:67], 0\n" \
"ds_read_b128 a[152:155], v5 offset:38912\n" \
"v_mfma_f32_16x16x32_bf16 v[88:91], a[128:131], a[80:83], 0\n" \
"s_add_u32 m0, s48, 0\n" \
"v_exp_f32 v113, v113\n" \
"v_mfma_f32_16x16x32_bf16 v[64:67], a[132:135], v[36:39], v[64:67]\n" \
"buffer_load_dwordx4 v6, s[40:43], s16 offen offset:0 sc1 lds\n" \
"v_mfma_f32_16x16x32_bf16 v[72:75], a[132:135], v[52:55], v[72:75]\n" \
"v_exp_f32 v114, v114\n" \
"v_mfma_f32_16x16x32_bf16 v[80:83], a[132:135], a[68:71], v[80:83]\n" \
"ds_read_b128 a[156:159], v5 offset:39936\n" \
"v_cvt_pk_bf16_f32 v152, v112, v113\n" \
"v_mfma_f32_16x16x32_bf16 v[88:91], a[132:135], a[84:87], v[88:91]\n" \
"s_waitcnt lgkmcnt(4)\n" \
"v_exp_f32 v115, v115\n" \
"s_add_u32 m0, s79, 140160\n" \
"v_mfma_f32_16x16x32_bf16 v[64:67], a[136:139], v[40:43], v[64:67]\n" \
"buffer_load_dwordx4 v12, s[28:31], s81 offen offset:128 lds\n" \
"v_mfma_f32_16x16x32_bf16 v[72:75], a[136:139], v[56:59], v[72:75]\n" \
"s_xor_b32 s49, s49, 0x10000\n" \
"s_mov_b32 s81, 393216\n" \
"v_cvt_pk_bf16_f32 v153, v114, v115\n" \
"v_mfma_f32_16x16x32_bf16 v[80:83], a[136:139], a[72:75], v[80:83]\n" \
"ds_read_b128 a[128:131], v5 offset:49152\n" \
"v_mfma_f32_16x16x32_bf16 v[88:91], a[136:139], a[88:91], v[88:91]\n" \
"s_add_u32 m0, s48, 0\n" \
"v_exp_f32 v116, v116\n" \
"v_mfma_f32_16x16x32_bf16 v[64:67], a[140:143], v[44:47], v[64:67]\n" \
"buffer_load_dwordx4 v6, s[40:43], s16 offen offset:1024 sc1 lds\n" \
"v_mfma_f32_16x16x32_bf16 v[72:75], a[140:143], v[60:63], v[72:75]\n" \
"v_exp_f32 v117, v117\n" \
"v_mfma_f32_16x16x32_bf16 v[80:83], a[140:143], a[76:79], v[80:83]\n" \
"ds_read_b128 a[132:135], v5 offset:50176\n" \
"v_cvt_pk_bf16_f32 v154, v116, v117\n" \
"v_mfma_f32_16x16x32_bf16 v[88:91], a[140:143], a[92:95], v[88:91]\n" \
"s_waitcnt lgkmcnt(4)\n" \
"v_exp_f32 v118, v118\n" \
"s_add_u32 m0, s79, 147456\n" \
"v_mfma_f32_16x16x32_bf16 v[68:71], a[144:147], v[32:35], 0\n" \
"buffer_load_dwordx4 v12, s[28:31], s81 offen offset:0 lds\n" \
"v_mfma_f32_16x16x32_bf16 v[76:79], a[144:147], v[48:51], 0\n" \
"v_exp_f32 v119, v119\n" \
"v_mfma_f32_16x16x32_bf16 v[84:87], a[144:147], a[64:67], 0\n" \
"ds_read_b128 a[136:139], v5 offset:51200\n" \
"v_cvt_pk_bf16_f32 v155, v118, v119\n" \
"v_mfma_f32_16x16x32_bf16 v[92:95], a[144:147], a[80:83], 0\n" \
"s_add_u32 m0, s48, 0\n" \
"v_exp_f32 v120, v120\n" \
"v_mfma_f32_16x16x32_bf16 v[68:71], a[148:151], v[36:39], v[68:71]\n" \
"buffer_load_dwordx4 v6, s[40:43], s16 offen offset:2048 sc1 lds\n" \
"v_mfma_f32_16x16x32_bf16 v[76:79], a[148:151], v[52:55], v[76:79]\n" \
"v_exp_f32 v121, v121\n" \
"v_mfma_f32_16x16x32_bf16 v[84:87], a[148:151], a[68:71], v[84:87]\n" \
"ds_read_b128 a[140:143], v5 offset:52224\n" \
"v_cvt_pk_bf16_f32 v156, v120, v121\n" \
"v_mfma_f32_16x16x32_bf16 v[92:95], a[148:151], a[84:87], v[92:95]\n" \
"s_waitcnt lgkmcnt(4)\n" \
"v_exp_f32 v122, v122\n" \
"s_add_u32 m0, s79, 148352\n" \
"v_mfma_f32_16x16x32_bf16 v[68:71], a[152:155], v[40:43], v[68:71]\n" \
"buffer_load_dwordx4 v12, s[28:31], s81 offen offset:128 lds\n" \
"v_mfma_f32_16x16x32_bf16 v[76:79], a[152:155], v[56:59], v[76:79]\n" \
"s_mov_b32 s81, 458752\n" \
"v_exp_f32 v123, v123\n" \
"v_mfma_f32_16x16x32_bf16 v[84:87], a[152:155], a[72:75], v[84:87]\n" \
"ds_read_b128 a[144:147], v5 offset:53248\n" \
"v_cvt_pk_bf16_f32 v157, v122, v123\n" \
"v_mfma_f32_16x16x32_bf16 v[92:95], a[152:155], a[88:91], v[92:95]\n" \
"s_add_u32 m0, s48, 0\n" \
"v_exp_f32 v124, v124\n" \
"v_mfma_f32_16x16x32_bf16 v[68:71], a[156:159], v[44:47], v[68:71]\n" \
"buffer_load_dwordx4 v6, s[40:43], s16 offen offset:3072 sc1 lds\n" \
"v_mfma_f32_16x16x32_bf16 v[76:79], a[156:159], v[60:63], v[76:79]\n" \
"v_exp_f32 v125, v125\n" \
"v_mfma_f32_16x16x32_bf16 v[84:87], a[156:159], a[76:79], v[84:87]\n" \
"ds_read_b128 a[148:151], v5 offset:54272\n" \
"v_cvt_pk_bf16_f32 v158, v124, v125\n" \
"v_mfma_f32_16x16x32_bf16 v[92:95], a[156:159], a[92:95], v[92:95]\n" \
"s_waitcnt lgkmcnt(4)\n" \
"v_exp_f32 v126, v126\n" \
"v_mfma_f32_16x16x32_bf16 v[160:163], v[180:183], v[144:147], v[160:163]\n" \
"s_add_u32 m0, s79, 155648\n" \
"v_exp_f32 v127, v127\n" \
"v_mfma_f32_16x16x32_bf16 v[164:167], v[180:183], v[148:151], v[164:167]\n" \
"buffer_load_dwordx4 v12, s[28:31], s81 offen offset:0 lds\n" \
"v_cvt_pk_bf16_f32 v159, v126, v127\n" \
"v_mfma_f32_16x16x32_bf16 v[168:171], v[180:183], v[152:155], v[168:171]\n" \
"v_exp_f32 v64, v64\n" \
"v_mfma_f32_16x16x32_bf16 v[172:175], v[180:183], v[156:159], v[172:175]\n" \
"buffer_store_dwordx4 v[28:31], v4, s[36:39], s80 offen offset:1024 sc1\n" \
"v_mfma_f32_16x16x32_bf16 v[192:195], a[128:131], v[144:147], v[192:195]\n" \
"s_add_u32 m0, s79, 156544\n" \
"v_exp_f32 v65, v65\n" \
"v_mfma_f32_16x16x32_bf16 v[224:227], a[128:131], v[148:151], v[224:227]\n" \
"buffer_load_dwordx4 v12, s[28:31], s81 offen offset:128 lds\n" \
"v_mfma_f32_16x16x32_bf16 a[0:3], a[128:131], v[152:155], a[0:3]\n" \
"s_mov_b32 s81, 524288\n" \
"ds_read_b128 a[152:155], v5 offset:55296\n" \
"v_cvt_pk_bf16_f32 v128, v64, v65\n" \
"v_mfma_f32_16x16x32_bf16 a[32:35], a[128:131], v[156:159], a[32:35]\n" \
"buffer_store_dwordx4 v[28:31], v4, s[36:39], s80 offen offset:2048 sc1\n" \
"v_mfma_f32_16x16x32_bf16 v[196:199], a[132:135], v[144:147], v[196:199]\n" \
"v_exp_f32 v66, v66\n" \
"v_mfma_f32_16x16x32_bf16 v[228:231], a[132:135], v[148:151], v[228:231]\n" \
"v_exp_f32 v67, v67\n" \
"v_mfma_f32_16x16x32_bf16 a[4:7], a[132:135], v[152:155], a[4:7]\n" \
"ds_read_b128 a[156:159], v5 offset:56320\n" \
"v_cvt_pk_bf16_f32 v129, v66, v67\n" \
"v_mfma_f32_16x16x32_bf16 a[36:39], a[132:135], v[156:159], a[36:39]\n" \
"s_waitcnt vmcnt(9) lgkmcnt(4)\n" \
"v_exp_f32 v68, v68\n" \
"v_mfma_f32_16x16x32_bf16 v[200:203], a[136:139], v[144:147], v[200:203]\n" \
"s_barrier\n" \
"v_xor_b32 v5, 0x10000, v5\n" \
"v_mfma_f32_16x16x32_bf16 v[232:235], a[136:139], v[148:151], v[232:235]\n" \
"s_add_u32 s17, s17, s19\n" \
"v_exp_f32 v69, v69\n" \
"v_mfma_f32_16x16x32_bf16 a[8:11], a[136:139], v[152:155], a[8:11]\n" \
"ds_read_b128 a[128:131], v5 offset:0\n" \
"v_cvt_pk_bf16_f32 v130, v68, v69\n" \
"v_mfma_f32_16x16x32_bf16 a[40:43], a[136:139], v[156:159], a[40:43]\n" \
"s_add_u32 m0, s48, 16384\n" \
"v_exp_f32 v70, v70\n" \
"v_mfma_f32_16x16x32_bf16 v[204:207], a[140:143], v[144:147], v[204:207]\n" \
"buffer_load_dwordx4 v6, s[40:43], s17 offen offset:0 sc1 lds\n" \
"v_mfma_f32_16x16x32_bf16 v[236:239], a[140:143], v[148:151], v[236:239]\n" \
"v_exp_f32 v71, v71\n" \
"v_mfma_f32_16x16x32_bf16 a[12:15], a[140:143], v[152:155], a[12:15]\n" \
"ds_read_b128 a[132:135], v5 offset:1024\n" \
"v_cvt_pk_bf16_f32 v131, v70, v71\n" \
"v_mfma_f32_16x16x32_bf16 a[44:47], a[140:143], v[156:159], a[44:47]\n" \
"s_waitcnt lgkmcnt(4)\n" \
"v_exp_f32 v72, v72\n" \
"v_mfma_f32_16x16x32_bf16 v[208:211], a[144:147], v[144:147], v[208:211]\n" \
"v_exp_f32 v73, v73\n" \
"v_mfma_f32_16x16x32_bf16 v[240:243], a[144:147], v[148:151], v[240:243]\n" \
"v_mfma_f32_16x16x32_bf16 a[16:19], a[144:147], v[152:155], a[16:19]\n" \
"ds_read_b128 a[136:139], v5 offset:2048\n" \
"v_cvt_pk_bf16_f32 v132, v72, v73\n" \
"v_mfma_f32_16x16x32_bf16 a[48:51], a[144:147], v[156:159], a[48:51]\n" \
"s_add_u32 m0, s48, 16384\n" \
"v_exp_f32 v74, v74\n" \
"v_mfma_f32_16x16x32_bf16 v[212:215], a[148:151], v[144:147], v[212:215]\n" \
"buffer_load_dwordx4 v6, s[40:43], s17 offen offset:1024 sc1 lds\n" \
"v_mfma_f32_16x16x32_bf16 v[244:247], a[148:151], v[148:151], v[244:247]\n" \
"v_exp_f32 v75, v75\n" \
"v_mfma_f32_16x16x32_bf16 a[20:23], a[148:151], v[152:155], a[20:23]\n" \
"ds_read_b128 a[140:143], v5 offset:3072\n" \
"v_cvt_pk_bf16_f32 v133, v74, v75\n" \
"v_mfma_f32_16x16x32_bf16 a[52:55], a[148:151], v[156:159], a[52:55]\n" \
"s_waitcnt lgkmcnt(4)\n" \
"s_add_u32 m0, s48, 16384\n" \
"ds_read_b64_tr_b16 a[192:193], v13 offset:0\n" \
"v_mfma_f32_16x16x32_bf16 v[216:219], a[152:155], v[144:147], v[216:219]\n" \
"buffer_load_dwordx4 v6, s[40:43], s17 offen offset:2048 sc1 lds\n" \
"v_mfma_f32_16x16x32_bf16 v[248:251], a[152:155], v[148:151], v[248:251]\n" \
"s_add_u32 s44, s44, 1\n" \
"v_exp_f32 v76, v76\n" \
"v_mfma_f32_16x16x32_bf16 a[24:27], a[152:155], v[152:155], a[24:27]\n" \
"ds_read_b128 a[144:147], v5 offset:4096\n" \
"v_mfma_f32_16x16x32_bf16 a[56:59], a[152:155], v[156:159], a[56:59]\n" \
"s_add_u32 m0, s48, 16384\n" \
"v_exp_f32 v77, v77\n" \
"v_mfma_f32_16x16x32_bf16 v[220:223], a[156:159], v[144:147], v[220:223]\n" \
"buffer_load_dwordx4 v6, s[40:43], s17 offen offset:3072 sc1 lds\n" \
"v_mfma_f32_16x16x32_bf16 v[252:255], a[156:159], v[148:151], v[252:255]\n" \
"s_xor_b32 s48, s48, 0x10000\n" \
"v_exp_f32 v78, v78\n" \
"v_mfma_f32_16x16x32_bf16 a[28:31], a[156:159], v[152:155], a[28:31]\n" \
"ds_read_b128 a[148:151], v5 offset:5120\n" \
"v_exp_f32 v79, v79\n" \
"v_mfma_f32_16x16x32_bf16 a[60:63], a[156:159], v[156:159], a[60:63]\n" \
"s_waitcnt lgkmcnt(4)\n" \
"ds_read_b64_tr_b16 a[194:195], v14 offset:0\n" \
"v_cvt_pk_bf16_f32 v134, v76, v77\n" \
"v_cvt_pk_bf16_f32 v135, v78, v79\n" \
"v_mfma_f32_16x16x32_bf16 v[96:99], a[128:131], v[32:35], 0\n" \
"s_add_u32 s100, s100, 262144\ns_addc_u32 s101, s101, 0\n" \
"ds_read_b64_tr_b16 a[196:197], v15 offset:0\n" \
"v_mfma_f32_16x16x32_bf16 v[104:107], a[128:131], v[48:51], 0\n" \
"s_add_u32 s16, s16, s9\n" \
"ds_read_b64_tr_b16 a[198:199], v16 offset:0\n" \
"v_mfma_f32_16x16x32_bf16 v[112:115], a[128:131], a[64:67], 0\n" \
"ds_read_b128 a[152:155], v5 offset:6144\n" \
"v_mfma_f32_16x16x32_bf16 v[120:123], a[128:131], a[80:83], 0\n" \
"s_add_u32 m0, s49, 32768\n" \
"v_exp_f32 v80, v80\n" \
"v_mfma_f32_16x16x32_bf16 v[96:99], a[132:135], v[36:39], v[96:99]\n" \
"buffer_load_dwordx4 v6, s[40:43], s16 offen offset:0 sc1 lds\n" \
"v_mfma_f32_16x16x32_bf16 v[104:107], a[132:135], v[52:55], v[104:107]\n" \
"ds_read_b64_tr_b16 a[200:201], v13 offset:8192\n" \
"v_mfma_f32_16x16x32_bf16 v[112:115], a[132:135], a[68:71], v[112:115]\n" \
"ds_read_b128 a[156:159], v5 offset:7168\n" \
"v_mfma_f32_16x16x32_bf16 v[120:123], a[132:135], a[84:87], v[120:123]\n" \
"s_waitcnt lgkmcnt(4)\n" \
"v_exp_f32 v81, v81\n" \
"v_mfma_f32_16x16x32_bf16 v[96:99], a[136:139], v[40:43], v[96:99]\n" \
"s_mov_b64 exec, 0xffff\nglobal_load_dword v1, v2, s[100:101]\ns_mov_b64 exec, -1\n" \
"v_mfma_f32_16x16x32_bf16 v[104:107], a[136:139], v[56:59], v[104:107]\n" \
"s_add_u32 s100, s100, 262144\ns_addc_u32 s101, s101, 0\n" \
"v_cvt_pk_bf16_f32 v136, v80, v81\n" \
"v_mfma_f32_16x16x32_bf16 v[112:115], a[136:139], a[72:75], v[112:115]\n" \
"ds_read_b128 a[128:131], v5 offset:16384\n" \
"v_mfma_f32_16x16x32_bf16 v[120:123], a[136:139], a[88:91], v[120:123]\n" \
"s_add_u32 m0, s49, 32768\n" \
"v_exp_f32 v82, v82\n" \
"v_mfma_f32_16x16x32_bf16 v[96:99], a[140:143], v[44:47], v[96:99]\n" \
"buffer_load_dwordx4 v6, s[40:43], s16 offen offset:1024 sc1 lds\n" \
"v_mfma_f32_16x16x32_bf16 v[104:107], a[140:143], v[60:63], v[104:107]\n" \
"v_exp_f32 v83, v83\n" \
"v_mfma_f32_16x16x32_bf16 v[112:115], a[140:143], a[76:79], v[112:115]\n" \
"ds_read_b128 a[132:135], v5 offset:17408\n" \
"v_cvt_pk_bf16_f32 v137, v82, v83\n" \
"v_mfma_f32_16x16x32_bf16 v[120:123], a[140:143], a[92:95], v[120:123]\n" \
"s_waitcnt lgkmcnt(4)\n" \
"v_exp_f32 v84, v84\n" \
"v_mfma_f32_16x16x32_bf16 v[100:103], a[144:147], v[32:35], 0\n" \
"v_exp_f32 v85, v85\n" \
"v_mfma_f32_16x16x32_bf16 v[108:111], a[144:147], v[48:51], 0\n" \
"v_exp_f32 v86, v86\n" \
"v_mfma_f32_16x16x32_bf16 v[116:119], a[144:147], a[64:67], 0\n" \
"ds_read_b128 a[136:139], v5 offset:18432\n" \
"v_cvt_pk_bf16_f32 v138, v84, v85\n" \
"v_mfma_f32_16x16x32_bf16 v[124:127], a[144:147], a[80:83], 0\n" \
"s_add_u32 m0, s49, 32768\n" \
"v_exp_f32 v87, v87\n" \
"v_mfma_f32_16x16x32_bf16 v[100:103], a[148:151], v[36:39], v[100:103]\n" \
"buffer_load_dwordx4 v6, s[40:43], s16 offen offset:2048 sc1 lds\n" \
"v_mfma_f32_16x16x32_bf16 v[108:111], a[148:151], v[52:55], v[108:111]\n" \
"v_exp_f32 v88, v88\n" \
"v_mfma_f32_16x16x32_bf16 v[116:119], a[148:151], a[68:71], v[116:119]\n" \
"ds_read_b128 a[140:143], v5 offset:19456\n" \
"v_cvt_pk_bf16_f32 v139, v86, v87\n" \
"v_mfma_f32_16x16x32_bf16 v[124:127], a[148:151], a[84:87], v[124:127]\n" \
"s_waitcnt lgkmcnt(4)\n" \
"v_exp_f32 v89, v89\n" \
"v_mfma_f32_16x16x32_bf16 v[100:103], a[152:155], v[40:43], v[100:103]\n" \
"v_exp_f32 v90, v90\n" \
"v_mfma_f32_16x16x32_bf16 v[108:111], a[152:155], v[56:59], v[108:111]\n" \
"v_exp_f32 v91, v91\n" \
"v_mfma_f32_16x16x32_bf16 v[116:119], a[152:155], a[72:75], v[116:119]\n" \
"ds_read_b128 a[144:147], v5 offset:20480\n" \
"v_cvt_pk_bf16_f32 v140, v88, v89\n" \
"v_mfma_f32_16x16x32_bf16 v[124:127], a[152:155], a[88:91], v[124:127]\n" \
"s_add_u32 m0, s49, 32768\n" \
"v_exp_f32 v92, v92\n" \
"v_mfma_f32_16x16x32_bf16 v[100:103], a[156:159], v[44:47], v[100:103]\n" \
"buffer_load_dwordx4 v6, s[40:43], s16 offen offset:3072 sc1 lds\n" \
"v_mfma_f32_16x16x32_bf16 v[108:111], a[156:159], v[60:63], v[108:111]\n" \
"v_exp_f32 v93, v93\n" \
"v_mfma_f32_16x16x32_bf16 v[116:119], a[156:159], a[76:79], v[116:119]\n" \
"ds_read_b128 a[148:151], v5 offset:21504\n" \
"v_cvt_pk_bf16_f32 v141, v90, v91\n" \
"v_mfma_f32_16x16x32_bf16 v[124:127], a[156:159], a[92:95], v[124:127]\n" \
"s_waitcnt lgkmcnt(4)\n" \
"v_exp_f32 v94, v94\n" \
"v_mfma_f32_16x16x32_bf16 v[160:163], v[180:183], v[128:131], v[160:163]\n" \
"LH17_%=:\ns_cbranch_execz LHR1_%=\n" \
"s_waitcnt vmcnt(20)\n" \
"v_exp_f32 v95, v95\n" \
"v_mfma_f32_16x16x32_bf16 v[164:167], v[180:183], v[132:135], v[164:167]\n" \
"s_mov_b64 exec, 0xffff\nglobal_load_dword v1, v2, s[100:101]\ns_mov_b64 exec, -1\n" \
"v_cvt_pk_bf16_f32 v142, v92, v93\n" \
"v_cvt_pk_bf16_f32 v143, v94, v95\n" \
"v_mfma_f32_16x16x32_bf16 v[168:171], v[180:183], v[136:139], v[168:171]\n" \
"ds_read_b64_tr_b16 a[202:203], v14 offset:8192\n" \
"v_cmp_ne_u64 vcc, s[14:15], v[18:19]\n" \
"v_mfma_f32_16x16x32_bf16 v[172:175], v[180:183], v[140:143], v[172:175]\n" \
"ds_read_b64_tr_b16 a[204:205], v15 offset:8192\n" \
"s_nop 0\n" \
"v_mfma_f32_16x16x32_bf16 v[192:195], a[128:131], v[128:131], v[192:195]\n" \
"ds_read_b64_tr_b16 a[206:207], v16 offset:8192\n" \
"s_cbranch_vccnz LPZ_%=\nLPZR_%=:\n" \
"v_mfma_f32_16x16x32_bf16 v[224:227], a[128:131], v[132:135], v[224:227]\n" \
"v_and_b32 v188, 0xf0, v4\n" \
"v_lshrrev_b32 v189, 8, v4\n" \
"v_mfma_f32_16x16x32_bf16 a[0:3], a[128:131], v[136:139], a[0:3]\n" \
"ds_read_b128 a[152:155], v5 offset:22528\n" \
"v_lshl_add_u32 v188, v189, 14, v188\n" \
"v_mfma_f32_16x16x32_bf16 a[32:35], a[128:131], v[140:143], a[32:35]\n" \
"v_exp_f32 v96, v96\n" \
"v_mfma_f32_16x16x32_bf16 v[196:199], a[132:135], v[128:131], v[196:199]\n" \
"v_exp_f32 v97, v97\n" \
"v_mfma_f32_16x16x32_bf16 v[228:231], a[132:135], v[132:135], v[228:231]\n" \
"s_waitcnt lgkmcnt(1)\n" \
"v_exp_f32 v98, v98\n" \
"v_mfma_f32_16x16x32_bf16 a[4:7], a[132:135], v[136:139], a[4:7]\n" \
"ds_read_b128 a[156:159], v5 offset:23552\n" \
"v_add_u32 v189, 0x10000, v188\n" \
"v_mfma_f32_16x16x32_bf16 a[36:39], a[132:135], v[140:143], a[36:39]\n" \
"s_waitcnt vmcnt(11) lgkmcnt(4)\n" \
"v_exp_f32 v99, v99\n" \
"v_mfma_f32_16x16x32_bf16 v[200:203], a[136:139], v[128:131], v[200:203]\n" \
"s_barrier\n" \
"v_exp_f32 v100, v100\n" \
"v_mfma_f32_16x16x32_bf16 v[232:235], a[136:139], v[132:135], v[232:235]\n" \
"s_add_u32 s17, s17, s19\n" \
"v_exp_f32 v101, v101\n" \
"v_mfma_f32_16x16x32_bf16 a[8:11], a[136:139], v[136:139], a[8:11]\n" \
"ds_read_b128 a[128:131], v5 offset:32768\n" \
"v_cvt_pk_bf16_f32 v144, v96, v97\n" \
"v_mfma_f32_16x16x32_bf16 a[40:43], a[136:139], v[140:143], a[40:43]\n" \
"s_add_u32 m0, s49, 49152\n" \
"v_exp_f32 v102, v102\n" \
"v_mfma_f32_16x16x32_bf16 v[204:207], a[140:143], v[128:131], v[204:207]\n" \
"buffer_load_dwordx4 v6, s[40:43], s17 offen offset:0 sc1 lds\n" \
"v_mfma_f32_16x16x32_bf16 v[236:239], a[140:143], v[132:135], v[236:239]\n" \
"v_exp_f32 v103, v103\n" \
"v_mfma_f32_16x16x32_bf16 a[12:15], a[140:143], v[136:139], a[12:15]\n" \
"ds_read_b128 a[132:135], v5 offset:33792\n" \
"v_cvt_pk_bf16_f32 v145, v98, v99\n" \
"v_mfma_f32_16x16x32_bf16 a[44:47], a[140:143], v[140:143], a[44:47]\n" \
"s_waitcnt lgkmcnt(4)\n" \
"v_exp_f32 v104, v104\n" \
"v_mfma_f32_16x16x32_bf16 v[208:211], a[144:147], v[128:131], v[208:211]\n" \
"v_exp_f32 v105, v105\n" \
"v_mfma_f32_16x16x32_bf16 v[240:243], a[144:147], v[132:135], v[240:243]\n" \
"v_mfma_f32_16x16x32_bf16 a[16:19], a[144:147], v[136:139], a[16:19]\n" \
"ds_read_b128 a[136:139], v5 offset:34816\n" \
"v_cvt_pk_bf16_f32 v146, v100, v101\n" \
"v_mfma_f32_16x16x32_bf16 a[48:51], a[144:147], v[140:143], a[48:51]\n" \
"s_add_u32 m0, s49, 49152\n" \
"v_exp_f32 v106, v106\n" \
"v_mfma_f32_16x16x32_bf16 v[212:215], a[148:151], v[128:131], v[212:215]\n" \
"buffer_load_dwordx4 v6, s[40:43], s17 offen offset:1024 sc1 lds\n" \
"v_mfma_f32_16x16x32_bf16 v[244:247], a[148:151], v[132:135], v[244:247]\n" \
"v_exp_f32 v107, v107\n" \
"v_mfma_f32_16x16x32_bf16 a[20:23], a[148:151], v[136:139], a[20:23]\n" \
"ds_read_b128 a[140:143], v5 offset:35840\n" \
"v_cvt_pk_bf16_f32 v147, v102, v103\n" \
"v_mfma_f32_16x16x32_bf16 a[52:55], a[148:151], v[140:143], a[52:55]\n" \
"s_waitcnt lgkmcnt(4)\n" \
"s_add_u32 m0, s49, 49152\n" \
"ds_read_b64_tr_b16 a[208:209], v13 offset:16384\n" \
"v_mfma_f32_16x16x32_bf16 v[216:219], a[152:155], v[128:131], v[216:219]\n" \
"buffer_load_dwordx4 v6, s[40:43], s17 offen offset:2048 sc1 lds\n" \
"v_mfma_f32_16x16x32_bf16 v[248:251], a[152:155], v[132:135], v[248:251]\n" \
"v_exp_f32 v108, v108\n" \
"v_mfma_f32_16x16x32_bf16 a[24:27], a[152:155], v[136:139], a[24:27]\n" \
"ds_read_b128 a[144:147], v5 offset:36864\n" \
"v_cvt_pk_bf16_f32 v148, v104, v105\n" \
"v_mfma_f32_16x16x32_bf16 a[56:59], a[152:155], v[140:143], a[56:59]\n" \
"s_add_u32 m0, s49, 49152\n" \
"v_exp_f32 v109, v109\n" \
"v_mfma_f32_16x16x32_bf16 v[220:223], a[156:159], v[128:131], v[220:223]\n" \
"buffer_load_dwordx4 v6, s[40:43], s17 offen offset:3072 sc1 lds\n" \
"v_mfma_f32_16x16x32_bf16 v[252:255], a[156:159], v[132:135], v[252:255]\n" \
"v_exp_f32 v110, v110\n" \
"v_mfma_f32_16x16x32_bf16 a[28:31], a[156:159], v[136:139], a[28:31]\n" \
"ds_read_b128 a[148:151], v5 offset:37888\n" \
"v_exp_f32 v111, v111\n" \
"v_mfma_f32_16x16x32_bf16 a[60:63], a[156:159], v[140:143], a[60:63]\n" \
"s_waitcnt lgkmcnt(4)\n" \
"ds_read_b64_tr_b16 a[210:211], v14 offset:16384\n" \
"v_cvt_pk_bf16_f32 v149, v106, v107\n" \
"v_cvt_pk_bf16_f32 v150, v108, v109\n" \
"v_cvt_pk_bf16_f32 v151, v110, v111\n" \
"v_mfma_f32_16x16x32_bf16 v[64:67], a[128:131], v[32:35], 0\n" \
"ds_read_b64_tr_b16 a[212:213], v15 offset:16384\n" \
"v_mfma_f32_16x16x32_bf16 v[72:75], a[128:131], v[48:51], 0\n" \
"s_add_u32 s16, s16, s9\n" \
"ds_read_b64_tr_b16 a[214:215], v16 offset:16384\n" \
"v_mfma_f32_16x16x32_bf16 v[80:83], a[128:131], a[64:67], 0\n" \
"ds_read_b128 a[152:155], v5 offset:38912\n" \
"v_mfma_f32_16x16x32_bf16 v[88:91], a[128:131], a[80:83], 0\n" \
"s_add_u32 m0, s48, 0\n" \
"v_exp_f32 v112, v112\n" \
"v_mfma_f32_16x16x32_bf16 v[64:67], a[132:135], v[36:39], v[64:67]\n" \
"buffer_load_dwordx4 v6, s[40:43], s16 offen offset:0 sc1 lds\n" \
"v_mfma_f32_16x16x32_bf16 v[72:75], a[132:135], v[52:55], v[72:75]\n" \
"ds_read_b64_tr_b16 a[216:217], v13 offset:24576\n" \
"v_mfma_f32_16x16x32_bf16 v[80:83], a[132:135], a[68:71], v[80:83]\n" \
"ds_read_b128 a[156:159], v5 offset:39936\n" \
"v_mfma_f32_16x16x32_bf16 v[88:91], a[132:135], a[84:87], v[88:91]\n" \
"s_waitcnt lgkmcnt(4)\n" \
"v_exp_f32 v113, v113\n" \
"s_add_u32 m0, s79, 131072\n" \
"v_mfma_f32_16x16x32_bf16 v[64:67], a[136:139], v[40:43], v[64:67]\n" \
"buffer_load_dwordx4 v12, s[28:31], s81 offen offset:0 lds\n" \
"v_mfma_f32_16x16x32_bf16 v[72:75], a[136:139], v[56:59], v[72:75]\n" \
"s_xor_b32 s49, s49, 0x10000\n" \
"v_exp_f32 v114, v114\n" \
"v_mfma_f32_16x16x32_bf16 v[80:83], a[136:139], a[72:75], v[80:83]\n" \
"ds_read_b128 a[128:131], v5 offset:49152\n" \
"v_cvt_pk_bf16_f32 v152, v112, v113\n" \
"v_mfma_f32_16x16x32_bf16 v[88:91], a[136:139], a[88:91], v[88:91]\n" \
"s_add_u32 m0, s48, 0\n" \
"v_exp_f32 v115, v115\n" \
"v_mfma_f32_16x16x32_bf16 v[64:67], a[140:143], v[44:47], v[64:67]\n" \
"buffer_load_dwordx4 v6, s[40:43], s16 offen offset:1024 sc1 lds\n" \
"v_mfma_f32_16x16x32_bf16 v[72:75], a[140:143], v[60:63], v[72:75]\n" \
"ds_read_b64_tr_b16 a[218:219], v14 offset:24576\n" \
"v_cvt_pk_bf16_f32 v153, v114, v115\n" \
"v_mfma_f32_16x16x32_bf16 v[80:83], a[140:143], a[76:79], v[80:83]\n" \
"ds_read_b128 a[132:135], v5 offset:50176\n" \
"v_mfma_f32_16x16x32_bf16 v[88:91], a[140:143], a[92:95], v[88:91]\n" \
"s_waitcnt lgkmcnt(4)\n" \
"v_exp_f32 v116, v116\n" \
"v_mfma_f32_16x16x32_bf16 v[68:71], a[144:147], v[32:35], 0\n" \
"v_exp_f32 v117, v117\n" \
"v_mfma_f32_16x16x32_bf16 v[76:79], a[144:147], v[48:51], 0\n" \
"v_exp_f32 v118, v118\n" \
"v_mfma_f32_16x16x32_bf16 v[84:87], a[144:147], a[64:67], 0\n" \
"ds_read_b128 a[136:139], v5 offset:51200\n" \
"v_cvt_pk_bf16_f32 v154, v116, v117\n" \
"v_mfma_f32_16x16x32_bf16 v[92:95], a[144:147], a[80:83], 0\n" \
"s_add_u32 m0, s48, 0\n" \
"v_exp_f32 v119, v119\n" \
"v_mfma_f32_16x16x32_bf16 v[68:71], a[148:151], v[36:39], v[68:71]\n" \
"buffer_load_dwordx4 v6, s[40:43], s16 offen offset:2048 sc1 lds\n" \
"v_mfma_f32_16x16x32_bf16 v[76:79], a[148:151], v[52:55], v[76:79]\n" \
"v_exp_f32 v120, v120\n" \
"v_mfma_f32_16x16x32_bf16 v[84:87], a[148:151], a[68:71], v[84:87]\n" \
"ds_read_b128 a[140:143], v5 offset:52224\n" \
"v_cvt_pk_bf16_f32 v155, v118, v119\n" \
"v_mfma_f32_16x16x32_bf16 v[92:95], a[148:151], a[84:87], v[92:95]\n" \
"s_waitcnt lgkmcnt(4)\n" \
"v_exp_f32 v121, v121\n" \
"v_mfma_f32_16x16x32_bf16 v[68:71], a[152:155], v[40:43], v[68:71]\n" \
"v_exp_f32 v122, v122\n" \
"v_mfma_f32_16x16x32_bf16 v[76:79], a[152:155], v[56:59], v[76:79]\n" \
"v_exp_f32 v123, v123\n" \
"v_mfma_f32_16x16x32_bf16 v[84:87], a[152:155], a[72:75], v[84:87]\n" \
"ds_read_b128 a[144:147], v5 offset:53248\n" \
"v_cvt_pk_bf16_f32 v156, v120, v121\n" \
"v_mfma_f32_16x16x32_bf16 v[92:95], a[152:155], a[88:91], v[92:95]\n" \
"s_add_u32 m0, s48, 0\n" \
"v_exp_f32 v124, v124\n" \
"v_mfma_f32_16x16x32_bf16 v[68:71], a[156:159], v[44:47], v[68:71]\n" \
"buffer_load_dwordx4 v6, s[40:43], s16 offen offset:3072 sc1 lds\n" \
"v_mfma_f32_16x16x32_bf16 v[76:79], a[156:159], v[60:63], v[76:79]\n" \
"v_exp_f32 v125, v125\n" \
"v_mfma_f32_16x16x32_bf16 v[84:87], a[156:159], a[76:79], v[84:87]\n" \
"ds_read_b128 a[148:151], v5 offset:54272\n" \
"v_cvt_pk_bf16_f32 v157, v122, v123\n" \
"v_mfma_f32_16x16x32_bf16 v[92:95], a[156:159], a[92:95], v[92:95]\n" \
"s_waitcnt lgkmcnt(4)\n" \
"v_exp_f32 v126, v126\n" \
"v_mfma_f32_16x16x32_bf16 v[160:163], v[180:183], v[144:147], v[160:163]\n" \
"s_add_u32 m0, s79, 131968\n" \
"v_exp_f32 v127, v127\n" \
"v_mfma_f32_16x16x32_bf16 v[164:167], v[180:183], v[148:151], v[164:167]\n" \
"buffer_load_dwordx4 v12, s[28:31], s81 offen offset:128 lds\n" \
"v_cvt_pk_bf16_f32 v158, v124, v125\n" \
"v_cvt_pk_bf16_f32 v159, v126, v127\n" \
"v_mfma_f32_16x16x32_bf16 v[168:171], v[180:183], v[152:155], v[168:171]\n" \
"s_mov_b32 s81, 589824\n" \
"ds_read_b64_tr_b16 a[220:221], v15 offset:24576\n" \
"v_mfma_f32_16x16x32_bf16 v[172:175], v[180:183], v[156:159], v[172:175]\n" \
"ds_read_b64_tr_b16 a[222:223], v16 offset:24576\n" \
"v_mfma_f32_16x16x32_bf16 v[192:195], a[128:131], v[144:147], v[192:195]\n" \
"s_add_u32 m0, s79, 139264\n" \
"v_exp_f32 v64, v64\n" \
"v_mfma_f32_16x16x32_bf16 v[224:227], a[128:131], v[148:151], v[224:227]\n" \
"buffer_load_dwordx4 v12, s[28:31], s81 offen offset:0 lds\n" \
"v_mfma_f32_16x16x32_bf16 a[0:3], a[128:131], v[152:155], a[0:3]\n" \
"ds_read_b128 a[152:155], v5 offset:55296\n" \
"v_mfma_f32_16x16x32_bf16 a[32:35], a[128:131], v[156:159], a[32:35]\n" \
"v_exp_f32 v65, v65\n" \
"v_mfma_f32_16x16x32_bf16 v[196:199], a[132:135], v[144:147], v[196:199]\n" \
"v_exp_f32 v66, v66\n" \
"v_mfma_f32_16x16x32_bf16 v[228:231], a[132:135], v[148:151], v[228:231]\n" \
"s_waitcnt lgkmcnt(1)\n" \
"v_exp_f32 v67, v67\n" \
"v_mfma_f32_16x16x32_bf16 a[4:7], a[132:135], v[152:155], a[4:7]\n" \
"ds_read_b128 a[156:159], v5 offset:56320\n" \
"v_cvt_pk_bf16_f32 v128, v64, v65\n" \
"v_mfma_f32_16x16x32_bf16 a[36:39], a[132:135], v[156:159], a[36:39]\n" \
"s_waitcnt vmcnt(17) lgkmcnt(4)\n" \
"v_exp_f32 v68, v68\n" \
"v_mfma_f32_16x16x32_bf16 v[200:203], a[136:139], v[144:147], v[200:203]\n" \
"s_barrier\n" \
"v_xor_b32 v5, 0x10000, v5\n" \
"v_cvt_pk_bf16_f32 v129, v66, v67\n" \
"v_mfma_f32_16x16x32_bf16 v[232:235], a[136:139], v[148:151], v[232:235]\n" \
"s_add_u32 s17, s17, s19\n" \
"v_exp_f32 v69, v69\n" \
"v_mfma_f32_16x16x32_bf16 a[8:11], a[136:139], v[152:155], a[8:11]\n" \
"ds_read_b128 a[128:131], v5 offset:0\n" \
"v_cvt_pk_bf16_f32 v130, v68, v69\n" \
"v_mfma_f32_16x16x32_bf16 a[40:43], a[136:139], v[156:159], a[40:43]\n" \
"s_add_u32 m0, s48, 16384\n" \
"v_exp_f32 v70, v70\n" \
"v_mfma_f32_16x16x32_bf16 v[204:207], a[140:143], v[144:147], v[204:207]\n" \
"buffer_load_dwordx4 v6, s[40:43], s17 offen offset:0 sc1 lds\n" \
"v_mfma_f32_16x16x32_bf16 v[236:239], a[140:143], v[148:151], v[236:239]\n" \
"v_exp_f32 v71, v71\n" \
"v_mfma_f32_16x16x32_bf16 a[12:15], a[140:143], v[152:155], a[12:15]\n" \
"ds_read_b128 a[132:135], v5 offset:1024\n" \
"v_cvt_pk_bf16_f32 v131, v70, v71\n" \
"v_mfma_f32_16x16x32_bf16 a[44:47], a[140:143], v[156:159], a[44:47]\n" \
"s_waitcnt lgkmcnt(4)\n" \
"v_exp_f32 v72, v72\n" \
"v_mfma_f32_16x16x32_bf16 v[208:211], a[144:147], v[144:147], v[208:211]\n" \
"v_exp_f32 v73, v73\n" \
"v_mfma_f32_16x16x32_bf16 v[240:243], a[144:147], v[148:151], v[240:243]\n" \
"v_mfma_f32_16x16x32_bf16 a[16:19], a[144:147], v[152:155], a[16:19]\n" \
"ds_read_b128 a[136:139], v5 offset:2048\n" \
"v_cvt_pk_bf16_f32 v132, v72, v73\n" \
"v_mfma_f32_16x16x32_bf16 a[48:51], a[144:147], v[156:159], a[48:51]\n" \
"s_add_u32 m0, s48, 16384\n" \
"v_exp_f32 v74, v74\n" \
"v_mfma_f32_16x16x32_bf16 v[212:215], a[148:151], v[144:147], v[212:215]\n" \
"buffer_load_dwordx4 v6, s[40:43], s17 offen offset:1024 sc1 lds\n" \
"v_mfma_f32_16x16x32_bf16 v[244:247], a[148:151], v[148:151], v[244:247]\n" \
"v_exp_f32 v75, v75\n" \
"v_mfma_f32_16x16x32_bf16 a[20:23], a[148:151], v[152:155], a[20:23]\n" \
"ds_read_b128 a[140:143], v5 offset:3072\n" \
"v_cvt_pk_bf16_f32 v133, v74, v75\n" \
"v_mfma_f32_16x16x32_bf16 a[52:55], a[148:151], v[156:159], a[52:55]\n" \
"s_waitcnt lgkmcnt(4)\n" \
"s_add_u32 m0, s48, 16384\n" \
"v_mfma_f32_16x16x32_bf16 v[216:219], a[152:155], v[144:147], v[216:219]\n" \
"buffer_load_dwordx4 v6, s[40:43], s17 offen offset:2048 sc1 lds\n" \
"v_mfma_f32_16x16x32_bf16 v[248:251], a[152:155], v[148:151], v[248:251]\n" \
"s_add_u32 s44, s44, 1\n" \
"v_exp_f32 v76, v76\n" \
"v_mfma_f32_16x16x32_bf16 a[24:27], a[152:155], v[152:155], a[24:27]\n" \
"ds_read_b128 a[144:147], v5 offset:4096\n" \
"v_mfma_f32_16x16x32_bf16 a[56:59], a[152:155], v[156:159], a[56:59]\n" \
"s_add_u32 m0, s48, 16384\n" \
"v_exp_f32 v77, v77\n" \
"v_mfma_f32_16x16x32_bf16 v[220:223], a[156:159], v[144:147], v[220:223]\n" \
"buffer_load_dwordx4 v6, s[40:43], s17 offen offset:3072 sc1 lds\n" \
"v_mfma_f32_16x16x32_bf16 v[252:255], a[156:159], v[148:151], v[252:255]\n" \
"s_xor_b32 s48, s48, 0x10000\n" \
"v_exp_f32 v78, v78\n" \
"v_mfma_f32_16x16x32_bf16 a[28:31], a[156:159], v[152:155], a[28:31]\n" \
"ds_read_b128 a[148:151], v5 offset:5120\n" \
"v_exp_f32 v79, v79\n" \
"v_mfma_f32_16x16x32_bf16 a[60:63], a[156:159], v[156:159], a[60:63]\n" \
"s_waitcnt lgkmcnt(4)\n" \
"v_cvt_pk_bf16_f32 v134, v76, v77\n" \
"v_cvt_pk_bf16_f32 v135, v78, v79\n" \
"v_mfma_f32_16x16x32_bf16 v[96:99], a[128:131], v[32:35], 0\n" \
"buffer_load_dwordx4 a[112:115], v188, s[36:39], s84 offen sc1\n" \
"v_mfma_f32_16x16x32_bf16 v[104:107], a[128:131], v[48:51], 0\n" \
"s_add_u32 s16, s16, s9\n" \
"v_exp_f32 v80, v80\n" \
"v_mfma_f32_16x16x32_bf16 v[112:115], a[128:131], a[64:67], 0\n" \
"ds_read_b128 a[152:155], v5 offset:6144\n" \
"v_mfma_f32_16x16x32_bf16 v[120:123], a[128:131], a[80:83], 0\n" \
"s_add_u32 m0, s49, 32768\n" \
"v_exp_f32 v81, v81\n" \
"v_mfma_f32_16x16x32_bf16 v[96:99], a[132:135], v[36:39], v[96:99]\n" \
"buffer_load_dwordx4 v6, s[40:43], s16 offen offset:0 sc1 lds\n" \
"v_mfma_f32_16x16x32_bf16 v[104:107], a[132:135], v[52:55], v[104:107]\n" \
"v_exp_f32 v82, v82\n" \
"v_mfma_f32_16x16x32_bf16 v[112:115], a[132:135], a[68:71], v[112:115]\n" \
"ds_read_b128 a[156:159], v5 offset:7168\n" \
"v_cvt_pk_bf16_f32 v136, v80, v81\n" \
"v_mfma_f32_16x16x32_bf16 v[120:123], a[132:135], a[84:87], v[120:123]\n" \
"s_waitcnt lgkmcnt(4)\n" \
"v_exp_f32 v83, v83\n" \
"s_add_u32 m0, s79, 140160\n" \
"v_mfma_f32_16x16x32_bf16 v[96:99], a[136:139], v[40:43], v[96:99]\n" \
"buffer_load_dwordx4 v12, s[28:31], s81 offen offset:128 lds\n" \
"v_mfma_f32_16x16x32_bf16 v[104:107], a[136:139], v[56:59], v[104:107]\n" \
"s_mov_b32 s81, 655360\n" \
"v_exp_f32 v84, v84\n" \
"v_mfma_f32_16x16x32_bf16 v[112:115], a[136:139], a[72:75], v[112:115]\n" \
"ds_read_b128 a[128:131], v5 offset:16384\n" \
"v_cvt_pk_bf16_f32 v137, v82, v83\n" \
"v_mfma_f32_16x16x32_bf16 v[120:123], a[136:139], a[88:91], v[120:123]\n" \
"s_add_u32 m0, s49, 32768\n" \
"v_exp_f32 v85, v85\n" \
"v_mfma_f32_16x16x32_bf16 v[96:99], a[140:143], v[44:47], v[96:99]\n" \
"buffer_load_dwordx4 v6, s[40:43], s16 offen offset:1024 sc1 lds\n" \
"v_mfma_f32_16x16x32_bf16 v[104:107], a[140:143], v[60:63], v[104:107]\n" \
"v_exp_f32 v86, v86\n" \
"v_mfma_f32_16x16x32_bf16 v[112:115], a[140:143], a[76:79], v[112:115]\n" \
"ds_read_b128 a[132:135], v5 offset:17408\n" \
"v_cvt_pk_bf16_f32 v138, v84, v85\n" \
"v_mfma_f32_16x16x32_bf16 v[120:123], a[140:143], a[92:95], v[120:123]\n" \
"s_waitcnt lgkmcnt(4)\n" \
"v_exp_f32 v87, v87\n" \
"s_add_u32 m0, s79, 147456\n" \
"v_mfma_f32_16x16x32_bf16 v[100:103], a[144:147], v[32:35], 0\n" \
"buffer_load_dwordx4 v12, s[28:31], s81 offen offset:0 lds\n" \
"v_mfma_f32_16x16x32_bf16 v[108:111], a[144:147], v[48:51], 0\n" \
"v_exp_f32 v88, v88\n" \
"v_mfma_f32_16x16x32_bf16 v[116:119], a[144:147], a[64:67], 0\n" \
"ds_read_b128 a[136:139], v5 offset:18432\n" \
"v_cvt_pk_bf16_f32 v139, v86, v87\n" \
"v_mfma_f32_16x16x32_bf16 v[124:127], a[144:147], a[80:83], 0\n" \
"s_add_u32 m0, s49, 32768\n" \
"v_exp_f32 v89, v89\n" \
"v_mfma_f32_16x16x32_bf16 v[100:103], a[148:151], v[36:39], v[100:103]\n" \
"buffer_load_dwordx4 v6, s[40:43], s16 offen offset:2048 sc1 lds\n" \
"v_mfma_f32_16x16x32_bf16 v[108:111], a[148:151], v[52:55], v[108:111]\n" \
"v_exp_f32 v90, v90\n" \
"v_mfma_f32_16x16x32_bf16 v[116:119], a[148:151], a[68:71], v[116:119]\n" \
"ds_read_b128 a[140:143], v5 offset:19456\n" \
"v_cvt_pk_bf16_f32 v140, v88, v89\n" \
"v_mfma_f32_16x16x32_bf16 v[124:127], a[148:151], a[84:87], v[124:127]\n" \
"s_waitcnt lgkmcnt(4)\n" \
"v_exp_f32 v91, v91\n" \
"s_add_u32 m0, s79, 148352\n" \
"v_mfma_f32_16x16x32_bf16 v[100:103], a[152:155], v[40:43], v[100:103]\n" \
"buffer_load_dwordx4 v12, s[28:31], s81 offen offset:128 lds\n" \
"v_mfma_f32_16x16x32_bf16 v[108:111], a[152:155], v[56:59], v[108:111]\n" \
"s_mov_b32 s81, 720896\n" \
"v_exp_f32 v92, v92\n" \
"v_mfma_f32_16x16x32_bf16 v[116:119], a[152:155], a[72:75], v[116:119]\n" \
"ds_read_b128 a[144:147], v5 offset:20480\n" \
"v_cvt_pk_bf16_f32 v141, v90, v91\n" \
"v_mfma_f32_16x16x32_bf16 v[124:127], a[152:155], a[88:91], v[124:127]\n" \
"s_add_u32 m0, s49, 32768\n" \
"v_exp_f32 v93, v93\n" \
"v_mfma_f32_16x16x32_bf16 v[100:103], a[156:159], v[44:47], v[100:103]\n" \
"buffer_load_dwordx4 v6, s[40:43], s16 offen offset:3072 sc1 lds\n" \
"v_mfma_f32_16x16x32_bf16 v[108:111], a[156:159], v[60:63], v[108:111]\n" \
"v_exp_f32 v94, v94\n" \
"v_mfma_f32_16x16x32_bf16 v[116:119], a[156:159], a[76:79], v[116:119]\n" \
"ds_read_b128 a[148:151], v5 offset:21504\n" \
"v_cvt_pk_bf16_f32 v142, v92, v93\n" \
"v_mfma_f32_16x16x32_bf16 v[124:127], a[156:159], a[92:95], v[124:127]\n" \
"s_waitcnt lgkmcnt(4)\n" \
"v_exp_f32 v95, v95\n" \
"s_add_u32 m0, s79, 155648\n" \
"v_mfma_f32_16x16x32_bf16 v[160:163], v[180:183], v[128:131], v[160:163]\n" \
"buffer_load_dwordx4 v12, s[28:31], s81 offen offset:0 lds\n" \
"v_mfma_f32_16x16x32_bf16 v[164:167], v[180:183], v[132:135], v[164:167]\n" \
"v_cvt_pk_bf16_f32 v143, v94, v95\n" \
"v_mfma_f32_16x16x32_bf16 v[168:171], v[180:183], v[136:139], v[168:171]\n" \
"buffer_store_dwordx4 v[28:31], v4, s[36:39], s80 offen offset:3072 sc1\n" \
"v_mfma_f32_16x16x32_bf16 v[172:175], v[180:183], v[140:143], v[172:175]\n" \
"s_add_u32 m0, s79, 156544\n" \
"v_exp_f32 v96, v96\n" \
"v_mfma_f32_16x16x32_bf16 v[192:195], a[128:131], v[128:131], v[192:195]\n" \
"buffer_load_dwordx4 v12, s[28:31], s81 offen offset:128 lds\n" \
"v_mfma_f32_16x16x32_bf16 v[224:227], a[128:131], v[132:135], v[224:227]\n" \
"v_bfe_u32 v28, v4, 7, 1\n" \
"v_bfe_u32 v29, v4, 4, 3\n" \
"v_mfma_f32_16x16x32_bf16 a[0:3], a[128:131], v[136:139], a[0:3]\n" \
"ds_read_b128 a[152:155], v5 offset:22528\n" \
"v_lshl_add_u32 v28, v29, 1, v28\n" \
"v_mfma_f32_16x16x32_bf16 a[32:35], a[128:131], v[140:143], a[32:35]\n" \
"v_lshrrev_b32 v29, 8, v4\n" \
"v_lshl_add_u32 v28, v29, 4, v28\n" \
"v_mfma_f32_16x16x32_bf16 v[196:199], a[132:135], v[128:131], v[196:199]\n" \
"v_exp_f32 v97, v97\n" \
"v_mfma_f32_16x16x32_bf16 v[228:231], a[132:135], v[132:135], v[228:231]\n" \
"v_exp_f32 v98, v98\n" \
"v_mfma_f32_16x16x32_bf16 a[4:7], a[132:135], v[136:139], a[4:7]\n" \
"ds_read_b128 a[156:159], v5 offset:23552\n" \
"v_lshlrev_b32 v28, 4, v28\n" \
"v_mfma_f32_16x16x32_bf16 a[36:39], a[132:135], v[140:143], a[36:39]\n" \
"s_waitcnt vmcnt(8) lgkmcnt(4)\n" \
"v_exp_f32 v99, v99\n" \
"v_mfma_f32_16x16x32_bf16 v[200:203], a[136:139], v[128:131], v[200:203]\n" \
"s_barrier\n" \
"v_exp_f32 v100, v100\n" \
"v_mfma_f32_16x16x32_bf16 v[232:235], a[136:139], v[132:135], v[232:235]\n" \
"s_add_u32 s17, s17, s19\n" \
"v_exp_f32 v101, v101\n" \
"v_mfma_f32_16x16x32_bf16 a[8:11], a[136:139], v[136:139], a[8:11]\n" \
"ds_read_b128 a[128:131], v5 offset:32768\n" \
"v_add_u32 v29, 0x10000, v28\n" \
"v_mfma_f32_16x16x32_bf16 a[40:43], a[136:139], v[140:143], a[40:43]\n" \
"s_add_u32 m0, s49, 49152\n" \
"v_exp_f32 v102, v102\n" \
"v_mfma_f32_16x16x32_bf16 v[204:207], a[140:143], v[128:131], v[204:207]\n" \
"buffer_load_dwordx4 v6, s[40:43], s17 offen offset:0 sc1 lds\n" \
"v_mfma_f32_16x16x32_bf16 v[236:239], a[140:143], v[132:135], v[236:239]\n" \
"v_exp_f32 v103, v103\n" \
"v_mfma_f32_16x16x32_bf16 a[12:15], a[140:143], v[136:139], a[12:15]\n" \
"ds_read_b128 a[132:135], v5 offset:33792\n" \
"v_add_u32 v30, 0x20000, v28\n" \
"v_mfma_f32_16x16x32_bf16 a[44:47], a[140:143], v[140:143], a[44:47]\n" \
"s_waitcnt lgkmcnt(4)\n" \
"v_exp_f32 v104, v104\n" \
"v_mfma_f32_16x16x32_bf16 v[208:211], a[144:147], v[128:131], v[208:211]\n" \
"v_exp_f32 v105, v105\n" \
"v_mfma_f32_16x16x32_bf16 v[240:243], a[144:147], v[132:135], v[240:243]\n" \
"v_mfma_f32_16x16x32_bf16 a[16:19], a[144:147], v[136:139], a[16:19]\n" \
"ds_read_b128 a[136:139], v5 offset:34816\n" \
"v_add_u32 v31, s22, v4\n" \
"v_mfma_f32_16x16x32_bf16 a[48:51], a[144:147], v[140:143], a[48:51]\n" \
"s_add_u32 m0, s49, 49152\n" \
"v_exp_f32 v106, v106\n" \
"v_mfma_f32_16x16x32_bf16 v[212:215], a[148:151], v[128:131], v[212:215]\n" \
"buffer_load_dwordx4 v6, s[40:43], s17 offen offset:1024 sc1 lds\n" \
"v_mfma_f32_16x16x32_bf16 v[244:247], a[148:151], v[132:135], v[244:247]\n" \
"v_exp_f32 v107, v107\n" \
"v_mfma_f32_16x16x32_bf16 a[20:23], a[148:151], v[136:139], a[20:23]\n" \
"ds_read_b128 a[140:143], v5 offset:35840\n" \
"v_cvt_pk_bf16_f32 v144, v96, v97\n" \
"v_mfma_f32_16x16x32_bf16 a[52:55], a[148:151], v[140:143], a[52:55]\n" \
"s_waitcnt lgkmcnt(4)\n" \
"s_add_u32 m0, s49, 49152\n" \
"ds_read_b64_tr_b16 a[224:225], v13 offset:0\n" \
"v_mfma_f32_16x16x32_bf16 v[216:219], a[152:155], v[128:131], v[216:219]\n" \
"buffer_load_dwordx4 v6, s[40:43], s17 offen offset:2048 sc1 lds\n" \
"v_mfma_f32_16x16x32_bf16 v[248:251], a[152:155], v[132:135], v[248:251]\n" \
"v_exp_f32 v108, v108\n" \
"v_mfma_f32_16x16x32_bf16 a[24:27], a[152:155], v[136:139], a[24:27]\n" \
"ds_read_b128 a[144:147], v5 offset:36864\n" \
"v_cvt_pk_bf16_f32 v145, v98, v99\n" \
"v_mfma_f32_16x16x32_bf16 a[56:59], a[152:155], v[140:143], a[56:59]\n" \
"s_add_u32 m0, s49, 49152\n" \
"v_exp_f32 v109, v109\n" \
"v_mfma_f32_16x16x32_bf16 v[220:223], a[156:159], v[128:131], v[220:223]\n" \
"buffer_load_dwordx4 v6, s[40:43], s17 offen offset:3072 sc1 lds\n" \
"v_mfma_f32_16x16x32_bf16 v[252:255], a[156:159], v[132:135], v[252:255]\n" \
"v_exp_f32 v110, v110\n" \
"v_mfma_f32_16x16x32_bf16 a[28:31], a[156:159], v[136:139], a[28:31]\n" \
"ds_read_b128 a[148:151], v5 offset:37888\n" \
"v_exp_f32 v111, v111\n" \
"v_mfma_f32_16x16x32_bf16 a[60:63], a[156:159], v[140:143], a[60:63]\n" \
"s_waitcnt lgkmcnt(4)\n" \
"ds_read_b64_tr_b16 a[226:227], v14 offset:0\n" \
"v_cvt_pk_bf16_f32 v146, v100, v101\n" \
"v_cvt_pk_bf16_f32 v147, v102, v103\n" \
"v_cvt_pk_bf16_f32 v148, v104, v105\n" \
"v_cvt_pk_bf16_f32 v149, v106, v107\n" \
"v_cvt_pk_bf16_f32 v150, v108, v109\n" \
"v_cvt_pk_bf16_f32 v151, v110, v111\n" \
"v_mfma_f32_16x16x32_bf16 v[64:67], a[128:131], v[32:35], 0\n" \
"buffer_load_dwordx4 a[116:119], v189, s[36:39], s84 offen sc1\n" \
"v_mfma_f32_16x16x32_bf16 v[72:75], a[128:131], v[48:51], 0\n" \
"s_add_u32 s16, s16, s9\n" \
"ds_read_b64_tr_b16 a[228:229], v15 offset:0\n" \
"v_mfma_f32_16x16x32_bf16 v[80:83], a[128:131], a[64:67], 0\n" \
"s_add_u32 s100, s100, 262144\ns_addc_u32 s101, s101, 0\n" \
"ds_read_b128 a[152:155], v5 offset:38912\n" \
"v_mfma_f32_16x16x32_bf16 v[88:91], a[128:131], a[80:83], 0\n" \
"s_add_u32 m0, s48, 0\n" \
"ds_read_b64_tr_b16 a[230:231], v16 offset:0\n" \
"v_mfma_f32_16x16x32_bf16 v[64:67], a[132:135], v[36:39], v[64:67]\n" \
"buffer_load_dwordx4 v6, s[40:43], s16 offen offset:0 sc1 lds\n" \
"v_mfma_f32_16x16x32_bf16 v[72:75], a[132:135], v[52:55], v[72:75]\n" \
"ds_read_b64_tr_b16 a[232:233], v13 offset:8192\n" \
"v_mfma_f32_16x16x32_bf16 v[80:83], a[132:135], a[68:71], v[80:83]\n" \
"ds_read_b128 a[156:159], v5 offset:39936\n" \
"v_mfma_f32_16x16x32_bf16 v[88:91], a[132:135], a[84:87], v[88:91]\n" \
"s_waitcnt lgkmcnt(4)\n" \
"v_exp_f32 v112, v112\n" \
"v_mfma_f32_16x16x32_bf16 v[64:67], a[136:139], v[40:43], v[64:67]\n" \
"buffer_load_dwordx4 a[120:123], v188, s[36:39], s85 offen sc1\n" \
"v_mfma_f32_16x16x32_bf16 v[72:75], a[136:139], v[56:59], v[72:75]\n" \
"s_xor_b32 s49, s49, 0x10000\n" \
"v_exp_f32 v113, v113\n" \
"v_mfma_f32_16x16x32_bf16 v[80:83], a[136:139], a[72:75], v[80:83]\n" \
"ds_read_b128 a[128:131], v5 offset:49152\n" \
"v_cvt_pk_bf16_f32 v152, v112, v113\n" \
"v_mfma_f32_16x16x32_bf16 v[88:91], a[136:139], a[88:91], v[88:91]\n" \
"s_add_u32 m0, s48, 0\n" \
"v_exp_f32 v114, v114\n" \
"v_mfma_f32_16x16x32_bf16 v[64:67], a[140:143], v[44:47], v[64:67]\n" \
"buffer_load_dwordx4 v6, s[40:43], s16 offen offset:1024 sc1 lds\n" \
"v_mfma_f32_16x16x32_bf16 v[72:75], a[140:143], v[60:63], v[72:75]\n" \
"v_exp_f32 v115, v115\n" \
"v_mfma_f32_16x16x32_bf16 v[80:83], a[140:143], a[76:79], v[80:83]\n" \
"ds_read_b128 a[132:135], v5 offset:50176\n" \
"v_cvt_pk_bf16_f32 v153, v114, v115\n" \
"v_mfma_f32_16x16x32_bf16 v[88:91], a[140:143], a[92:95], v[88:91]\n" \
"s_waitcnt lgkmcnt(4)\n" \
"v_exp_f32 v116, v116\n" \
"v_mfma_f32_16x16x32_bf16 v[68:71], a[144:147], v[32:35], 0\n" \
"v_exp_f32 v117, v117\n" \
"v_mfma_f32_16x16x32_bf16 v[76:79], a[144:147], v[48:51], 0\n" \
"v_exp_f32 v118, v118\n" \
"v_mfma_f32_16x16x32_bf16 v[84:87], a[144:147], a[64:67], 0\n" \
"ds_read_b128 a[136:139], v5 offset:51200\n" \
"v_cvt_pk_bf16_f32 v154, v116, v117\n" \
"v_mfma_f32_16x16x32_bf16 v[92:95], a[144:147], a[80:83], 0\n" \
"s_add_u32 m0, s48, 0\n" \
"v_exp_f32 v119, v119\n" \
"v_mfma_f32_16x16x32_bf16 v[68:71], a[148:151], v[36:39], v[68:71]\n" \
"buffer_load_dwordx4 v6, s[40:43], s16 offen offset:2048 sc1 lds\n" \
"v_mfma_f32_16x16x32_bf16 v[76:79], a[148:151], v[52:55], v[76:79]\n" \
"v_exp_f32 v120, v120\n" \
"v_mfma_f32_16x16x32_bf16 v[84:87], a[148:151], a[68:71], v[84:87]\n" \
"ds_read_b128 a[140:143], v5 offset:52224\n" \
"v_cvt_pk_bf16_f32 v155, v118, v119\n" \
"v_mfma_f32_16x16x32_bf16 v[92:95], a[148:151], a[84:87], v[92:95]\n" \
"s_waitcnt lgkmcnt(4)\n" \
"v_exp_f32 v121, v121\n" \
"v_mfma_f32_16x16x32_bf16 v[68:71], a[152:155], v[40:43], v[68:71]\n" \
"v_exp_f32 v122, v122\n" \
"v_mfma_f32_16x16x32_bf16 v[76:79], a[152:155], v[56:59], v[76:79]\n" \
"v_exp_f32 v123, v123\n" \
"v_mfma_f32_16x16x32_bf16 v[84:87], a[152:155], a[72:75], v[84:87]\n" \
"ds_read_b128 a[144:147], v5 offset:53248\n" \
"v_cvt_pk_bf16_f32 v156, v120, v121\n" \
"v_mfma_f32_16x16x32_bf16 v[92:95], a[152:155], a[88:91], v[92:95]\n" \
"s_add_u32 m0, s48, 0\n" \
"v_exp_f32 v124, v124\n" \
"v_mfma_f32_16x16x32_bf16 v[68:71], a[156:159], v[44:47], v[68:71]\n" \
"buffer_load_dwordx4 v6, s[40:43], s16 offen offset:3072 sc1 lds\n" \
"v_mfma_f32_16x16x32_bf16 v[76:79], a[156:159], v[60:63], v[76:79]\n" \
"v_exp_f32 v125, v125\n" \
"v_mfma_f32_16x16x32_bf16 v[84:87], a[156:159], a[76:79], v[84:87]\n" \
"ds_read_b128 a[148:151], v5 offset:54272\n" \
"v_cvt_pk_bf16_f32 v157, v122, v123\n" \
"v_mfma_f32_16x16x32_bf16 v[92:95], a[156:159], a[92:95], v[92:95]\n" \
"s_waitcnt lgkmcnt(4)\n" \
"v_exp_f32 v126, v126\n" \
"v_mfma_f32_16x16x32_bf16 v[160:163], v[180:183], v[144:147], v[160:163]\n" \
"s_lshl_b32 s25, s45, 2\n" \
"v_exp_f32 v127, v127\n" \
"v_mfma_f32_16x16x32_bf16 v[164:167], v[180:183], v[148:151], v[164:167]\n" \
"buffer_load_dwordx4 a[124:127], v189, s[36:39], s85 offen sc1\n" \
"v_cvt_pk_bf16_f32 v158, v124, v125\n" \
"v_cvt_pk_bf16_f32 v159, v126, v127\n" \
"v_mfma_f32_16x16x32_bf16 v[168:171], v[180:183], v[152:155], v[168:171]\n" \
"s_andn2_b32 s25, 32, s25\n" \
"ds_read_b64_tr_b16 a[234:235], v14 offset:8192\n" \
"s_add_u32 s25, s25, %2\n" \
"v_mfma_f32_16x16x32_bf16 v[172:175], v[180:183], v[156:159], v[172:175]\n" \
"buffer_store_dwordx2 v[10:11], v20, s[36:39], s12 offen sc1\n" \
"v_mfma_f32_16x16x32_bf16 v[192:195], a[128:131], v[144:147], v[192:195]\n" \
"s_cmp_ge_u32 s25, 34\ns_cselect_b64 vcc, -1, 0\n" \
"ds_read_b64_tr_b16 a[236:237], v15 offset:8192\n" \
"v_mfma_f32_16x16x32_bf16 v[224:227], a[128:131], v[148:151], v[224:227]\n" \
"ds_read_b64_tr_b16 a[238:239], v16 offset:8192\n" \
"v_bfe_u32 v190, v4, 7, 2\n" \
"v_mfma_f32_16x16x32_bf16 a[0:3], a[128:131], v[152:155], a[0:3]\n" \
"ds_read_b128 a[152:155], v5 offset:55296\n" \
"v_min_u32 v190, 2, v190\n" \
"v_mfma_f32_16x16x32_bf16 a[32:35], a[128:131], v[156:159], a[32:35]\n" \
"v_exp_f32 v64, v64\n" \
"v_mfma_f32_16x16x32_bf16 v[196:199], a[132:135], v[144:147], v[196:199]\n" \
"v_exp_f32 v65, v65\n" \
"v_mfma_f32_16x16x32_bf16 v[228:231], a[132:135], v[148:151], v[228:231]\n" \
"s_waitcnt lgkmcnt(1)\n" \
"v_exp_f32 v66, v66\n" \
"v_mfma_f32_16x16x32_bf16 a[4:7], a[132:135], v[152:155], a[4:7]\n" \
"ds_read_b128 a[156:159], v5 offset:56320\n" \
"v_lshl_add_u32 v17, v190, 8, s98\n" \
"v_mfma_f32_16x16x32_bf16 a[36:39], a[132:135], v[156:159], a[36:39]\n" \
"s_waitcnt vmcnt(12) lgkmcnt(4)\n" \
"v_exp_f32 v67, v67\n" \
"v_mfma_f32_16x16x32_bf16 v[200:203], a[136:139], v[144:147], v[200:203]\n" \
"s_barrier\n" \
"v_xor_b32 v5, 0x10000, v5\n" \
"v_bfe_u32 v190, v4, 4, 3\n" \
"v_mfma_f32_16x16x32_bf16 v[232:235], a[136:139], v[148:151], v[232:235]\n" \
"s_add_u32 s17, s17, s19\n" \
"v_exp_f32 v68, v68\n" \
"v_mfma_f32_16x16x32_bf16 a[8:11], a[136:139], v[152:155], a[8:11]\n" \
"s_lshr_b32 s92, %1, 5\n" \
"ds_read_b128 a[128:131], v5 offset:0\n" \
"v_lshl_add_u32 v17, v190, 3, v17\n" \
"v_mfma_f32_16x16x32_bf16 a[40:43], a[136:139], v[156:159], a[40:43]\n" \
"s_add_u32 m0, s48, 16384\n" \
"v_exp_f32 v69, v69\n" \
"v_mfma_f32_16x16x32_bf16 v[204:207], a[140:143], v[144:147], v[204:207]\n" \
"LH18_%=:\ns_cbranch_execz LHR1_%=\n" \
"buffer_load_dwordx4 v6, s[40:43], s17 offen offset:0 sc1 lds\n" \
"v_mfma_f32_16x16x32_bf16 v[236:239], a[140:143], v[148:151], v[236:239]\n" \
"s_lshl_b32 s92, s92, 2\n" \
"v_exp_f32 v70, v70\n" \
"v_mfma_f32_16x16x32_bf16 a[12:15], a[140:143], v[152:155], a[12:15]\n" \
"s_add_u32 s92, s92, %2\n" \
"ds_read_b128 a[132:135], v5 offset:1024\n" \
"s_lshl_b32 s92, s92, 11\n" \
"v_mfma_f32_16x16x32_bf16 a[44:47], a[140:143], v[156:159], a[44:47]\n" \
"s_waitcnt lgkmcnt(4)\n" \
"v_exp_f32 v71, v71\n" \
"v_mfma_f32_16x16x32_bf16 v[208:211], a[144:147], v[144:147], v[208:211]\n" \
"s_waitcnt vmcnt(14)\n" \
"v_exp_f32 v72, v72\n" \
"v_mfma_f32_16x16x32_bf16 v[240:243], a[144:147], v[148:151], v[240:243]\n" \
"v_exp_f32 v73, v73\n" \
"v_mfma_f32_16x16x32_bf16 a[16:19], a[144:147], v[152:155], a[16:19]\n" \
"ds_read_b128 a[136:139], v5 offset:2048\n" \
"v_and_b32 v188, 0xf0, v4\n" \
"v_mfma_f32_16x16x32_bf16 a[48:51], a[144:147], v[156:159], a[48:51]\n" \
"s_add_u32 m0, s48, 16384\n" \
"v_exp_f32 v74, v74\n" \
"v_mfma_f32_16x16x32_bf16 v[212:215], a[148:151], v[144:147], v[212:215]\n" \
"buffer_load_dwordx4 v6, s[40:43], s17 offen offset:1024 sc1 lds\n" \
"v_mfma_f32_16x16x32_bf16 v[244:247], a[148:151], v[148:151], v[244:247]\n" \
"v_exp_f32 v75, v75\n" \
"v_mfma_f32_16x16x32_bf16 a[20:23], a[148:151], v[152:155], a[20:23]\n" \
"ds_read_b128 a[140:143], v5 offset:3072\n" \
"v_lshl_add_u32 v188, v188, 3, s92\n" \
"v_mfma_f32_16x16x32_bf16 a[52:55], a[148:151], v[156:159], a[52:55]\n" \
"s_waitcnt lgkmcnt(4)\n" \
"s_add_u32 m0, s48, 16384\n" \
"ds_read_b64_tr_b16 a[240:241], v13 offset:16384\n" \
"v_mfma_f32_16x16x32_bf16 v[216:219], a[152:155], v[144:147], v[216:219]\n" \
"buffer_load_dwordx4 v6, s[40:43], s17 offen offset:2048 sc1 lds\n" \
"v_mfma_f32_16x16x32_bf16 v[248:251], a[152:155], v[148:151], v[248:251]\n" \
"s_add_u32 s44, s44, 1\n" \
"v_exp_f32 v76, v76\n" \
"v_mfma_f32_16x16x32_bf16 a[24:27], a[152:155], v[152:155], a[24:27]\n" \
"ds_read_b128 a[144:147], v5 offset:4096\n" \
"v_lshrrev_b32 v189, 8, v4\n" \
"v_mfma_f32_16x16x32_bf16 a[56:59], a[152:155], v[156:159], a[56:59]\n" \
"s_add_u32 m0, s48, 16384\n" \
"v_exp_f32 v77, v77\n" \
"v_mfma_f32_16x16x32_bf16 v[220:223], a[156:159], v[144:147], v[220:223]\n" \
"buffer_load_dwordx4 v6, s[40:43], s17 offen offset:3072 sc1 lds\n" \
"v_mfma_f32_16x16x32_bf16 v[252:255], a[156:159], v[148:151], v[252:255]\n" \
"s_xor_b32 s48, s48, 0x10000\n" \
"v_exp_f32 v78, v78\n" \
"v_mfma_f32_16x16x32_bf16 a[28:31], a[156:159], v[152:155], a[28:31]\n" \
"ds_read_b128 a[148:151], v5 offset:5120\n" \
"v_lshl_add_u32 v190, v189, 18, v188\n" \
"v_exp_f32 v79, v79\n" \
"v_mfma_f32_16x16x32_bf16 a[60:63], a[156:159], v[156:159], a[60:63]\n" \
"s_waitcnt lgkmcnt(4)\n" \
"ds_read_b64_tr_b16 a[242:243], v14 offset:16384\n" \
"v_min_u32 v189, 2, v189\n" \
"v_cvt_pk_bf16_f32 v128, v64, v65\n" \
"v_cvt_pk_bf16_f32 v129, v66, v67\n" \
"v_cvt_pk_bf16_f32 v130, v68, v69\n" \
"v_cvt_pk_bf16_f32 v131, v70, v71\n" \
"v_cvt_pk_bf16_f32 v132, v72, v73\n" \
"v_cvt_pk_bf16_f32 v133, v74, v75\n" \
"v_cvt_pk_bf16_f32 v134, v76, v77\n" \
"v_cvt_pk_bf16_f32 v135, v78, v79\n" \
"v_mfma_f32_16x16x32_bf16 v[96:99], a[128:131], v[32:35], 0\n" \
"buffer_store_dwordx2 v[10:11], v20, s[36:39], s88 offen sc1\n" \
"v_mfma_f32_16x16x32_bf16 v[104:107], a[128:131], v[48:51], 0\n" \
"s_add_u32 s16, s16, s9\n" \
"ds_read_b64_tr_b16 a[244:245], v15 offset:16384\n" \
"s_mul_i32 s92, s90, 12\n" \
"v_mfma_f32_16x16x32_bf16 v[112:115], a[128:131], a[64:67], 0\n" \
"s_add_u32 s92, s92, s89\n" \
"ds_read_b128 a[152:155], v5 offset:6144\n" \
"s_lshl_b32 s92, s92, 16\n" \
"v_mfma_f32_16x16x32_bf16 v[120:123], a[128:131], a[80:83], 0\n" \
"s_add_u32 m0, s49, 32768\n" \
"ds_read_b64_tr_b16 a[246:247], v16 offset:16384\n" \
"v_lshl_add_u32 v191, v189, 18, v188\n" \
"v_mfma_f32_16x16x32_bf16 v[96:99], a[132:135], v[36:39], v[96:99]\n" \
"buffer_load_dwordx4 v6, s[40:43], s16 offen offset:0 sc1 lds\n" \
"v_mfma_f32_16x16x32_bf16 v[104:107], a[132:135], v[52:55], v[104:107]\n" \
"s_mov_b32 s46, s23\n" \
"ds_read_b64_tr_b16 a[248:249], v13 offset:24576\n" \
"v_add_u32 v191, s92, v191\n" \
"v_mfma_f32_16x16x32_bf16 v[112:115], a[132:135], a[68:71], v[112:115]\n" \
"s_lshl_b32 s92, %2, 13\n" \
"ds_read_b128 a[156:159], v5 offset:7168\n" \
"s_add_u32 s92, s92, 131072\n" \
"v_mfma_f32_16x16x32_bf16 v[120:123], a[132:135], a[84:87], v[120:123]\n" \
"s_waitcnt lgkmcnt(4)\n" \
"v_add_u32 v186, s92, v4\n" \
"v_xor_b32 v187, 0x6000, v186\n" \
"v_mfma_f32_16x16x32_bf16 v[96:99], a[136:139], v[40:43], v[96:99]\n" \
"s_lshl_b32 s92, %2, 9\n" \
"v_exp_f32 v80, v80\n" \
"v_mfma_f32_16x16x32_bf16 v[104:107], a[136:139], v[56:59], v[104:107]\n" \
"s_add_u32 s92, s92, 65536\n" \
"v_exp_f32 v81, v81\n" \
"v_mfma_f32_16x16x32_bf16 v[112:115], a[136:139], a[72:75], v[112:115]\n" \
"ds_read_b128 a[128:131], v5 offset:16384\n" \
"v_lshrrev_b32 v184, 2, v4\n" \
"v_mfma_f32_16x16x32_bf16 v[120:123], a[136:139], a[88:91], v[120:123]\n" \
"s_add_u32 m0, s49, 32768\n" \
"v_exp_f32 v82, v82\n" \
"v_mfma_f32_16x16x32_bf16 v[96:99], a[140:143], v[44:47], v[96:99]\n" \
"buffer_load_dwordx4 v6, s[40:43], s16 offen offset:1024 sc1 lds\n" \
"v_mfma_f32_16x16x32_bf16 v[104:107], a[140:143], v[60:63], v[104:107]\n" \
"v_exp_f32 v83, v83\n" \
"v_mfma_f32_16x16x32_bf16 v[112:115], a[140:143], a[76:79], v[112:115]\n" \
"ds_read_b128 a[132:135], v5 offset:17408\n" \
"v_add_u32 v184, s92, v184\n" \
"v_mfma_f32_16x16x32_bf16 v[120:123], a[140:143], a[92:95], v[120:123]\n" \
"s_waitcnt lgkmcnt(4)\n" \
"v_exp_f32 v84, v84\n" \
"v_mfma_f32_16x16x32_bf16 v[100:103], a[144:147], v[32:35], 0\n" \
"v_exp_f32 v85, v85\n" \
"v_mfma_f32_16x16x32_bf16 v[108:111], a[144:147], v[48:51], 0\n" \
"v_exp_f32 v86, v86\n" \
"v_mfma_f32_16x16x32_bf16 v[116:119], a[144:147], a[64:67], 0\n" \
"ds_read_b128 a[136:139], v5 offset:18432\n" \
"v_xor_b32 v185, 0x600, v184\n" \
"v_mfma_f32_16x16x32_bf16 v[124:127], a[144:147], a[80:83], 0\n" \
"s_add_u32 m0, s49, 32768\n" \
"v_exp_f32 v87, v87\n" \
"v_mfma_f32_16x16x32_bf16 v[100:103], a[148:151], v[36:39], v[100:103]\n" \
"buffer_load_dwordx4 v6, s[40:43], s16 offen offset:2048 sc1 lds\n" \
"v_mfma_f32_16x16x32_bf16 v[108:111], a[148:151], v[52:55], v[108:111]\n" \
"v_exp_f32 v88, v88\n" \
"v_mfma_f32_16x16x32_bf16 v[116:119], a[148:151], a[68:71], v[116:119]\n" \
"ds_read_b128 a[140:143], v5 offset:19456\n" \
"v_cvt_pk_bf16_f32 v136, v80, v81\n" \
"v_mfma_f32_16x16x32_bf16 v[124:127], a[148:151], a[84:87], v[124:127]\n" \
"s_waitcnt lgkmcnt(4)\n" \
"v_exp_f32 v89, v89\n" \
"v_mfma_f32_16x16x32_bf16 v[100:103], a[152:155], v[40:43], v[100:103]\n" \
"v_exp_f32 v90, v90\n" \
"v_mfma_f32_16x16x32_bf16 v[108:111], a[152:155], v[56:59], v[108:111]\n" \
"v_exp_f32 v91, v91\n" \
"v_mfma_f32_16x16x32_bf16 v[116:119], a[152:155], a[72:75], v[116:119]\n" \
"ds_read_b128 a[144:147], v5 offset:20480\n" \
"v_cvt_pk_bf16_f32 v137, v82, v83\n" \
"v_mfma_f32_16x16x32_bf16 v[124:127], a[152:155], a[88:91], v[124:127]\n" \
"s_add_u32 m0, s49, 32768\n" \
"v_exp_f32 v92, v92\n" \
"v_mfma_f32_16x16x32_bf16 v[100:103], a[156:159], v[44:47], v[100:103]\n" \
"buffer_load_dwordx4 v6, s[40:43], s16 offen offset:3072 sc1 lds\n" \
"v_mfma_f32_16x16x32_bf16 v[108:111], a[156:159], v[60:63], v[108:111]\n" \
"v_exp_f32 v93, v93\n" \
"v_mfma_f32_16x16x32_bf16 v[116:119], a[156:159], a[76:79], v[116:119]\n" \
"ds_read_b128 a[148:151], v5 offset:21504\n" \
"v_cvt_pk_bf16_f32 v138, v84, v85\n" \
"v_mfma_f32_16x16x32_bf16 v[124:127], a[156:159], a[92:95], v[124:127]\n" \
"s_waitcnt lgkmcnt(4)\n" \
"v_exp_f32 v94, v94\n" \
"v_mfma_f32_16x16x32_bf16 v[160:163], v[180:183], v[128:131], v[160:163]\n" \
"v_exp_f32 v95, v95\n" \
"v_cvt_pk_bf16_f32 v139, v86, v87\n" \
"v_mfma_f32_16x16x32_bf16 v[164:167], v[180:183], v[132:135], v[164:167]\n" \
"s_mov_b64 exec, 0xffff\nglobal_load_dword v1, v2, s[100:101]\ns_mov_b64 exec, -1\n" \
"v_cvt_pk_bf16_f32 v140, v88, v89\n" \
"v_cvt_pk_bf16_f32 v141, v90, v91\n" \
"v_cvt_pk_bf16_f32 v142, v92, v93\n" \
"v_cvt_pk_bf16_f32 v143, v94, v95\n" \
"v_mfma_f32_16x16x32_bf16 v[168:171], v[180:183], v[136:139], v[168:171]\n" \
"ds_read_b64_tr_b16 a[250:251], v14 offset:24576\n" \
"v_mov_b32 v2, v190\n" \
"v_mfma_f32_16x16x32_bf16 v[172:175], v[180:183], v[140:143], v[172:175]\n" \
"ds_read_b64_tr_b16 a[252:253], v15 offset:24576\n" \
"v_mfma_f32_16x16x32_bf16 v[192:195], a[128:131], v[128:131], v[192:195]\n" \
"ds_read_b64_tr_b16 a[254:255], v16 offset:24576\n" \
"v_mfma_f32_16x16x32_bf16 v[224:227], a[128:131], v[132:135], v[224:227]\n" \
"v_exp_f32 v96, v96\n" \
"v_mfma_f32_16x16x32_bf16 a[0:3], a[128:131], v[136:139], a[0:3]\n" \
"ds_read_b128 a[152:155], v5 offset:22528\n" \
"v_mfma_f32_16x16x32_bf16 a[32:35], a[128:131], v[140:143], a[32:35]\n" \
"v_exp_f32 v97, v97\n" \
"v_mfma_f32_16x16x32_bf16 v[196:199], a[132:135], v[128:131], v[196:199]\n" \
"v_exp_f32 v98, v98\n" \
"v_mfma_f32_16x16x32_bf16 v[228:231], a[132:135], v[132:135], v[228:231]\n" \
"s_waitcnt lgkmcnt(1)\n" \
"v_exp_f32 v99, v99\n" \
"v_mfma_f32_16x16x32_bf16 a[4:7], a[132:135], v[136:139], a[4:7]\n" \
"ds_read_b128 a[156:159], v5 offset:23552\n" \
"v_cvt_pk_bf16_f32 v144, v96, v97\n" \
"v_mfma_f32_16x16x32_bf16 a[36:39], a[132:135], v[140:143], a[36:39]\n" \
"s_waitcnt vmcnt(18) lgkmcnt(4)\n" \
"v_exp_f32 v100, v100\n" \
"v_mfma_f32_16x16x32_bf16 v[200:203], a[136:139], v[128:131], v[200:203]\n" \
"s_barrier\n" \
"v_exp_f32 v101, v101\n" \
"v_mfma_f32_16x16x32_bf16 v[232:235], a[136:139], v[132:135], v[232:235]\n" \
"s_add_u32 s17, s17, s19\n" \
"v_cvt_pk_bf16_f32 v145, v98, v99\n" \
"v_cvt_pk_bf16_f32 v146, v100, v101\n" \
"v_mfma_f32_16x16x32_bf16 a[8:11], a[136:139], v[136:139], a[8:11]\n" \
"ds_read_b128 a[128:131], v5 offset:32768\n" \
"v_mfma_f32_16x16x32_bf16 a[40:43], a[136:139], v[140:143], a[40:43]\n" \
"s_add_u32 m0, s49, 49152\n" \
"v_exp_f32 v102, v102\n" \
"v_mfma_f32_16x16x32_bf16 v[204:207], a[140:143], v[128:131], v[204:207]\n" \
"buffer_load_dwordx4 v6, s[40:43], s17 offen offset:0 sc1 lds\n" \
"v_mfma_f32_16x16x32_bf16 v[236:239], a[140:143], v[132:135], v[236:239]\n" \
"v_exp_f32 v103, v103\n" \
"v_mfma_f32_16x16x32_bf16 a[12:15], a[140:143], v[136:139], a[12:15]\n" \
"ds_read_b128 a[132:135], v5 offset:33792\n" \
"v_cvt_pk_bf16_f32 v147, v102, v103\n" \
"v_mfma_f32_16x16x32_bf16 a[44:47], a[140:143], v[140:143], a[44:47]\n" \
"s_waitcnt lgkmcnt(4)\n" \
"v_exp_f32 v104, v104\n" \
"v_mfma_f32_16x16x32_bf16 v[208:211], a[144:147], v[128:131], v[208:211]\n" \
"v_exp_f32 v105, v105\n" \
"v_mfma_f32_16x16x32_bf16 v[240:243], a[144:147], v[132:135], v[240:243]\n" \
"v_mfma_f32_16x16x32_bf16 a[16:19], a[144:147], v[136:139], a[16:19]\n" \
"ds_read_b128 a[136:139], v5 offset:34816\n" \
"v_cvt_pk_bf16_f32 v148, v104, v105\n" \
"v_mfma_f32_16x16x32_bf16 a[48:51], a[144:147], v[140:143], a[48:51]\n" \
"s_add_u32 m0, s49, 49152\n" \
"v_exp_f32 v106, v106\n" \
"v_mfma_f32_16x16x32_bf16 v[212:215], a[148:151], v[128:131], v[212:215]\n" \
"buffer_load_dwordx4 v6, s[40:43], s17 offen offset:1024 sc1 lds\n" \
"v_mfma_f32_16x16x32_bf16 v[244:247], a[148:151], v[132:135], v[244:247]\n" \
"v_exp_f32 v107, v107\n" \
"v_mfma_f32_16x16x32_bf16 a[20:23], a[148:151], v[136:139], a[20:23]\n" \
"ds_read_b128 a[140:143], v5 offset:35840\n" \
"v_cvt_pk_bf16_f32 v149, v106, v107\n" \
"v_mfma_f32_16x16x32_bf16 a[52:55], a[148:151], v[140:143], a[52:55]\n" \
"s_waitcnt lgkmcnt(4)\n" \
"s_add_u32 m0, s49, 49152\n" \
"v_mfma_f32_16x16x32_bf16 v[216:219], a[152:155], v[128:131], v[216:219]\n" \
"buffer_load_dwordx4 v6, s[40:43], s17 offen offset:2048 sc1 lds\n" \
"v_mfma_f32_16x16x32_bf16 v[248:251], a[152:155], v[132:135], v[248:251]\n" \
"v_exp_f32 v108, v108\n" \
"v_mfma_f32_16x16x32_bf16 a[24:27], a[152:155], v[136:139], a[24:27]\n" \
"ds_read_b128 a[144:147], v5 offset:36864\n" \
"v_mfma_f32_16x16x32_bf16 a[56:59], a[152:155], v[140:143], a[56:59]\n" \
"s_add_u32 m0, s49, 49152\n" \
"v_exp_f32 v109, v109\n" \
"v_mfma_f32_16x16x32_bf16 v[220:223], a[156:159], v[128:131], v[220:223]\n" \
"buffer_load_dwordx4 v6, s[40:43], s17 offen offset:3072 sc1 lds\n" \
"v_mfma_f32_16x16x32_bf16 v[252:255], a[156:159], v[132:135], v[252:255]\n" \
"v_exp_f32 v110, v110\n" \
"v_mfma_f32_16x16x32_bf16 a[28:31], a[156:159], v[136:139], a[28:31]\n" \
"ds_read_b128 a[148:151], v5 offset:37888\n" \
"v_exp_f32 v111, v111\n" \
"v_mfma_f32_16x16x32_bf16 a[60:63], a[156:159], v[140:143], a[60:63]\n" \
"s_waitcnt lgkmcnt(4)\n" \
"v_cvt_pk_bf16_f32 v150, v108, v109\n" \
"v_cvt_pk_bf16_f32 v151, v110, v111\n" \
"s_cbranch_vccz LQWB_%=\nds_write_b128 v21, v[32:35] offset:16384\n" \
"ds_write_b128 v21, v[36:39] offset:17408\n" \
"ds_write_b128 v21, v[40:43] offset:18432\n" \
"ds_write_b128 v21, v[44:47] offset:19456\n" \
"ds_write_b128 v21, v[48:51] offset:20480\n" \
"ds_write_b128 v21, v[52:55] offset:21504\n" \
"ds_write_b128 v21, v[56:59] offset:22528\n" \
"ds_write_b128 v21, v[60:63] offset:23552\n" \
"ds_write_b128 v21, a[64:67] offset:0\n" \
"ds_write_b128 v21, a[68:71] offset:1024\n" \
"ds_write_b128 v21, a[72:75] offset:2048\n" \
"ds_write_b128 v21, a[76:79] offset:3072\n" \
"ds_write_b128 v21, a[80:83] offset:4096\n" \
"ds_write_b128 v21, a[84:87] offset:5120\n" \
"ds_write_b128 v21, a[88:91] offset:6144\n" \
"ds_write_b128 v21, a[92:95] offset:7168\nLQWB_%=:\n" \
"s_mov_b64 exec, 0\ns_cmp_eq_u32 %2, 0\ns_cbranch_scc1 LH21_%=\ns_cmp_eq_u32 %2, 1\ns_cbranch_scc1 LH22_%=\ns_cmp_eq_u32 %2, 2\ns_cbranch_scc1 LH23_%=\ns_cmp_eq_u32 %2, 3\ns_cbranch_scc1 LH24_%=\nLHR2_%=:\ns_mov_b64 exec, -1\n" \
"s_branch LCHK_%=\n" \
"LAD2_%=:\n" \
"v_xor_b32 v8, 0x2000, v5\n" \
"ds_read_b128 v[32:35], v21 offset:0\n" \
"ds_read_b128 v[36:39], v21 offset:1024\n" \
"ds_read_b128 v[40:43], v21 offset:2048\n" \
"ds_read_b128 v[44:47], v21 offset:3072\n" \
"ds_read_b128 v[48:51], v21 offset:4096\n" \
"ds_read_b128 v[52:55], v21 offset:5120\n" \
"ds_read_b128 v[56:59], v21 offset:6144\n" \
"ds_read_b128 v[60:63], v21 offset:7168\n" \
"ds_read_b128 a[64:67], v21 offset:16384\n" \
"ds_read_b128 a[68:71], v21 offset:17408\n" \
"ds_read_b128 a[72:75], v21 offset:18432\n" \
"ds_read_b128 a[76:79], v21 offset:19456\n" \
"ds_read_b128 a[128:131], v5 offset:49152\n" \
"ds_read_b128 a[80:83], v21 offset:20480\n" \
"ds_read_b128 a[84:87], v21 offset:21504\n" \
"ds_read_b128 a[88:91], v21 offset:22528\n" \
"ds_read_b128 a[92:95], v21 offset:23552\n" \
"ds_read_b128 a[132:135], v5 offset:50176\n" \
"ds_read_b128 a[136:139], v5 offset:51200\n" \
"ds_read_b128 a[140:143], v5 offset:52224\n" \
"ds_read_b128 a[144:147], v5 offset:53248\n" \
"ds_read_b128 a[148:151], v5 offset:54272\n" \
"v_exp_f32 v112, v112\n" \
"s_nop 0\n" \
"v_exp_f32 v113, v113\n" \
"s_nop 0\n" \
"v_exp_f32 v114, v114\n" \
"s_nop 0\n" \
"v_exp_f32 v115, v115\n" \
"s_nop 0\n" \
"v_exp_f32 v120, v120\n" \
"s_nop 0\n" \
"v_exp_f32 v121, v121\n" \
"s_nop 0\n" \
"v_exp_f32 v122, v122\n" \
"s_nop 0\n" \
"v_exp_f32 v123, v123\n" \
"s_nop 0\n" \
"v_exp_f32 v124, v124\n" \
"s_nop 0\n" \
"v_exp_f32 v125, v125\n" \
"s_nop 0\n" \
"v_exp_f32 v126, v126\n" \
"s_nop 0\n" \
"v_exp_f32 v127, v127\n" \
"s_nop 0\n" \
"v_cvt_pk_bf16_f32 v152, v112, v113\n" \
"v_cvt_pk_bf16_f32 v153, v114, v115\n" \
"v_mov_b32 v154, 0\n" \
"v_mov_b32 v155, 0\n" \
"v_cvt_pk_bf16_f32 v156, v120, v121\n" \
"v_cvt_pk_bf16_f32 v157, v122, v123\n" \
"v_cvt_pk_bf16_f32 v158, v124, v125\n" \
"v_cvt_pk_bf16_f32 v159, v126, v127\n" \
"v_subrev_u32 v23, 0x1f800000, v160\n" \
"v_subrev_u32 v27, 0x1f800000, v164\n" \
"v_max_u32 v23, v23, v27\n" \
"v_cmp_le_u32 vcc, 0x40000000, v23\n" \
"s_or_b64 s[54:55], s[54:55], vcc\n" \
"s_nop 0\n" \
"v_rcp_f32 v22, v160\n" \
"s_nop 0\n" \
"v_rcp_f32 v26, v164\n" \
"s_nop 0\n" \
"s_waitcnt lgkmcnt(0)\n" \
"s_branch LS3_%=\n" \
"LS1_%=:\n" \
"v_mfma_f32_16x16x32_bf16 v[64:67], a[128:131], v[32:35], v[176:179]\n" \
"v_exp_f32 v112, v112\n" \
"v_mfma_f32_16x16x32_bf16 v[72:75], a[128:131], v[48:51], 0\n" \
"s_add_u32 s16, s16, s9\n" \
"v_mfma_f32_16x16x32_bf16 v[80:83], a[128:131], a[64:67], 0\n" \
"ds_read_b128 a[152:155], v5 offset:38912\n" \
"v_mfma_f32_16x16x32_bf16 v[88:91], a[128:131], a[80:83], 0\n" \
"s_add_u32 m0, s48, 0\n" \
"v_exp_f32 v113, v113\n" \
"v_mfma_f32_16x16x32_bf16 v[64:67], a[132:135], v[36:39], v[64:67]\n" \
"buffer_load_dwordx4 v6, s[40:43], s16 offen offset:0 sc1 lds\n" \
"v_mfma_f32_16x16x32_bf16 v[72:75], a[132:135], v[52:55], v[72:75]\n" \
"v_exp_f32 v114, v114\n" \
"v_mfma_f32_16x16x32_bf16 v[80:83], a[132:135], a[68:71], v[80:83]\n" \
"ds_read_b128 a[156:159], v5 offset:39936\n" \
"v_cvt_pk_bf16_f32 v152, v112, v113\n" \
"v_mfma_f32_16x16x32_bf16 v[88:91], a[132:135], a[84:87], v[88:91]\n" \
"s_waitcnt lgkmcnt(4)\n" \
"v_exp_f32 v115, v115\n" \
"v_mfma_f32_16x16x32_bf16 v[64:67], a[136:139], v[40:43], v[64:67]\n" \
"v_exp_f32 v116, v116\n" \
"v_mfma_f32_16x16x32_bf16 v[72:75], a[136:139], v[56:59], v[72:75]\n" \
"s_xor_b32 s49, s49, 0x10000\n" \
"v_cvt_pk_bf16_f32 v153, v114, v115\n" \
"v_mfma_f32_16x16x32_bf16 v[80:83], a[136:139], a[72:75], v[80:83]\n" \
"ds_read_b128 a[128:131], v5 offset:49152\n" \
"v_mfma_f32_16x16x32_bf16 v[88:91], a[136:139], a[88:91], v[88:91]\n" \
"v_exp_f32 v117, v117\n" \
"v_mfma_f32_16x16x32_bf16 v[64:67], a[140:143], v[44:47], v[64:67]\n" \
"buffer_load_dwordx4 v6, s[40:43], s16 offen offset:1024 sc1 lds\n" \
"v_mfma_f32_16x16x32_bf16 v[72:75], a[140:143], v[60:63], v[72:75]\n" \
"v_exp_f32 v118, v118\n" \
"v_mfma_f32_16x16x32_bf16 v[80:83], a[140:143], a[76:79], v[80:83]\n" \
"ds_read_b128 a[132:135], v5 offset:50176\n" \
"v_cvt_pk_bf16_f32 v154, v116, v117\n" \
"v_mfma_f32_16x16x32_bf16 v[88:91], a[140:143], a[92:95], v[88:91]\n" \
"s_waitcnt lgkmcnt(4)\n" \
"v_exp_f32 v119, v119\n" \
"v_mov_b32 v68, 0xff800000\n" \
"v_mov_b32 v69, 0xff800000\n" \
"v_exp_f32 v120, v120\n" \
"v_mfma_f32_16x16x32_bf16 v[76:79], a[144:147], v[48:51], v[176:179]\n" \
"v_cvt_pk_bf16_f32 v155, v118, v119\n" \
"v_mfma_f32_16x16x32_bf16 v[84:87], a[144:147], a[64:67], 0\n" \
"ds_read_b128 a[136:139], v5 offset:51200\n" \
"v_mfma_f32_16x16x32_bf16 v[92:95], a[144:147], a[80:83], 0\n" \
"v_exp_f32 v121, v121\n" \
"buffer_load_dwordx4 v6, s[40:43], s16 offen offset:2048 sc1 lds\n" \
"v_exp_f32 v122, v122\n" \
"v_mfma_f32_16x16x32_bf16 v[76:79], a[148:151], v[52:55], v[76:79]\n" \
"v_cvt_pk_bf16_f32 v156, v120, v121\n" \
"v_mfma_f32_16x16x32_bf16 v[84:87], a[148:151], a[68:71], v[84:87]\n" \
"ds_read_b128 a[140:143], v5 offset:52224\n" \
"v_mfma_f32_16x16x32_bf16 v[92:95], a[148:151], a[84:87], v[92:95]\n" \
"s_waitcnt lgkmcnt(4)\n" \
"v_exp_f32 v123, v123\n" \
"v_mov_b32 v70, 0xff800000\n" \
"v_mov_b32 v71, 0xff800000\n" \
"v_exp_f32 v124, v124\n" \
"v_mfma_f32_16x16x32_bf16 v[76:79], a[152:155], v[56:59], v[76:79]\n" \
"v_cvt_pk_bf16_f32 v157, v122, v123\n" \
"v_mfma_f32_16x16x32_bf16 v[84:87], a[152:155], a[72:75], v[84:87]\n" \
"ds_read_b128 a[144:147], v5 offset:53248\n" \
"v_mfma_f32_16x16x32_bf16 v[92:95], a[152:155], a[88:91], v[92:95]\n" \
"v_exp_f32 v125, v125\n" \
"buffer_load_dwordx4 v6, s[40:43], s16 offen offset:3072 sc1 lds\n" \
"v_exp_f32 v126, v126\n" \
"v_mfma_f32_16x16x32_bf16 v[76:79], a[156:159], v[60:63], v[76:79]\n" \
"v_cvt_pk_bf16_f32 v158, v124, v125\n" \
"v_mfma_f32_16x16x32_bf16 v[84:87], a[156:159], a[76:79], v[84:87]\n" \
"ds_read_b128 a[148:151], v5 offset:54272\n" \
"v_mfma_f32_16x16x32_bf16 v[92:95], a[156:159], a[92:95], v[92:95]\n" \
"s_waitcnt lgkmcnt(4)\n" \
"v_exp_f32 v127, v127\n" \
"v_mfma_f32_16x16x32_bf16 v[160:163], v[180:183], v[144:147], v[160:163]\n" \
"v_cvt_pk_bf16_f32 v159, v126, v127\n" \
"v_mfma_f32_16x16x32_bf16 v[164:167], v[180:183], v[148:151], v[164:167]\n" \
"v_mfma_f32_16x16x32_bf16 v[168:171], v[180:183], v[152:155], v[168:171]\n" \
"v_exp_f32 v64, v64\n" \
"v_mfma_f32_16x16x32_bf16 v[172:175], v[180:183], v[156:159], v[172:175]\n" \
"v_mfma_f32_16x16x32_bf16 v[192:195], a[128:131], v[144:147], v[192:195]\n" \
"v_exp_f32 v65, v65\n" \
"v_mfma_f32_16x16x32_bf16 v[224:227], a[128:131], v[148:151], v[224:227]\n" \
"v_cvt_pk_bf16_f32 v128, v64, v65\n" \
"v_mfma_f32_16x16x32_bf16 a[0:3], a[128:131], v[152:155], a[0:3]\n" \
"ds_read_b128 a[152:155], v5 offset:55296\n" \
"v_mfma_f32_16x16x32_bf16 a[32:35], a[128:131], v[156:159], a[32:35]\n" \
"v_exp_f32 v66, v66\n" \
"v_mfma_f32_16x16x32_bf16 v[196:199], a[132:135], v[144:147], v[196:199]\n" \
"v_exp_f32 v67, v67\n" \
"v_mfma_f32_16x16x32_bf16 v[228:231], a[132:135], v[148:151], v[228:231]\n" \
"v_cvt_pk_bf16_f32 v129, v66, v67\n" \
"v_mfma_f32_16x16x32_bf16 a[4:7], a[132:135], v[152:155], a[4:7]\n" \
"ds_read_b128 a[156:159], v5 offset:56320\n" \
"v_mfma_f32_16x16x32_bf16 a[36:39], a[132:135], v[156:159], a[36:39]\n" \
"s_waitcnt vmcnt(12) lgkmcnt(4)\n" \
"v_exp_f32 v68, v68\n" \
"v_mfma_f32_16x16x32_bf16 v[200:203], a[136:139], v[144:147], v[200:203]\n" \
"s_barrier\n" \
"v_xor_b32 v5, 0x10000, v5\n" \
"v_mfma_f32_16x16x32_bf16 v[232:235], a[136:139], v[148:151], v[232:235]\n" \
"s_add_u32 s17, s17, s19\n" \
"v_exp_f32 v69, v69\n" \
"v_mfma_f32_16x16x32_bf16 a[8:11], a[136:139], v[152:155], a[8:11]\n" \
"ds_read_b128 a[128:131], v5 offset:0\n" \
"v_cvt_pk_bf16_f32 v130, v68, v69\n" \
"v_mfma_f32_16x16x32_bf16 a[40:43], a[136:139], v[156:159], a[40:43]\n" \
"s_add_u32 m0, s48, 16384\n" \
"v_exp_f32 v70, v70\n" \
"v_mfma_f32_16x16x32_bf16 v[204:207], a[140:143], v[144:147], v[204:207]\n" \
"buffer_load_dwordx4 v6, s[40:43], s17 offen offset:0 sc1 lds\n" \
"v_mfma_f32_16x16x32_bf16 v[236:239], a[140:143], v[148:151], v[236:239]\n" \
"v_exp_f32 v71, v71\n" \
"v_mfma_f32_16x16x32_bf16 a[12:15], a[140:143], v[152:155], a[12:15]\n" \
"ds_read_b128 a[132:135], v5 offset:1024\n" \
"v_cvt_pk_bf16_f32 v131, v70, v71\n" \
"v_mfma_f32_16x16x32_bf16 a[44:47], a[140:143], v[156:159], a[44:47]\n" \
"s_waitcnt lgkmcnt(4)\n" \
"v_exp_f32 v72, v72\n" \
"v_mfma_f32_16x16x32_bf16 v[208:211], a[144:147], v[144:147], v[208:211]\n" \
"v_exp_f32 v73, v73\n" \
"v_mfma_f32_16x16x32_bf16 v[240:243], a[144:147], v[148:151], v[240:243]\n" \
"v_cvt_pk_bf16_f32 v132, v72, v73\n" \
"v_mfma_f32_16x16x32_bf16 a[16:19], a[144:147], v[152:155], a[16:19]\n" \
"ds_read_b128 a[136:139], v5 offset:2048\n" \
"v_mfma_f32_16x16x32_bf16 a[48:51], a[144:147], v[156:159], a[48:51]\n" \
"v_exp_f32 v74, v74\n" \
"v_mfma_f32_16x16x32_bf16 v[212:215], a[148:151], v[144:147], v[212:215]\n" \
"buffer_load_dwordx4 v6, s[40:43], s17 offen offset:1024 sc1 lds\n" \
"v_mfma_f32_16x16x32_bf16 v[244:247], a[148:151], v[148:151], v[244:247]\n" \
"v_exp_f32 v75, v75\n" \
"v_mfma_f32_16x16x32_bf16 a[20:23], a[148:151], v[152:155], a[20:23]\n" \
"ds_read_b128 a[140:143], v5 offset:3072\n" \
"v_cvt_pk_bf16_f32 v133, v74, v75\n" \
"v_mfma_f32_16x16x32_bf16 a[52:55], a[148:151], v[156:159], a[52:55]\n" \
"s_waitcnt lgkmcnt(4)\n" \
"v_exp_f32 v76, v76\n" \
"v_mfma_f32_16x16x32_bf16 v[216:219], a[152:155], v[144:147], v[216:219]\n" \
"buffer_load_dwordx4 v6, s[40:43], s17 offen offset:2048 sc1 lds\n" \
"v_mfma_f32_16x16x32_bf16 v[248:251], a[152:155], v[148:151], v[248:251]\n" \
"s_add_u32 s44, s44, 1\n" \
"v_exp_f32 v77, v77\n" \
"v_mfma_f32_16x16x32_bf16 a[24:27], a[152:155], v[152:155], a[24:27]\n" \
"ds_read_b128 a[144:147], v5 offset:4096\n" \
"v_cvt_pk_bf16_f32 v134, v76, v77\n" \
"v_mfma_f32_16x16x32_bf16 a[56:59], a[152:155], v[156:159], a[56:59]\n" \
"v_exp_f32 v78, v78\n" \
"v_mfma_f32_16x16x32_bf16 v[220:223], a[156:159], v[144:147], v[220:223]\n" \
"buffer_load_dwordx4 v6, s[40:43], s17 offen offset:3072 sc1 lds\n" \
"v_mfma_f32_16x16x32_bf16 v[252:255], a[156:159], v[148:151], v[252:255]\n" \
"s_xor_b32 s48, s48, 0x10000\n" \
"v_exp_f32 v79, v79\n" \
"v_mfma_f32_16x16x32_bf16 a[28:31], a[156:159], v[152:155], a[28:31]\n" \
"ds_read_b128 a[148:151], v5 offset:5120\n" \
"v_cvt_pk_bf16_f32 v135, v78, v79\n" \
"v_mfma_f32_16x16x32_bf16 a[60:63], a[156:159], v[156:159], a[60:63]\n" \
"s_waitcnt lgkmcnt(4)\n" \
"s_cmp_ge_u32 %2, 2\n" \
"s_cbranch_scc1 LFIN_%=\n" \
"s_cmp_eq_u32 s44, 15\ns_cselect_b32 s16, s56, s16\ns_cselect_b32 s9, s57, s9\ns_cselect_b32 s17, s58, s17\ns_cselect_b32 s19, s59, s19\n" \
"v_mfma_f32_16x16x32_bf16 v[112:115], a[128:131], a[64:67], v[176:179]\n" \
"v_xor_b32 v8, 0x2000, v5\n" \
"ds_read_b128 v[32:35], v21 offset:0\n" \
"v_mfma_f32_16x16x32_bf16 v[120:123], a[128:131], a[80:83], 0\n" \
"s_waitcnt lgkmcnt(5)\n" \
"ds_read_b128 a[152:155], v5 offset:6144\n" \
"s_add_u32 s16, s16, s9\n" \
"v_mfma_f32_16x16x32_bf16 v[112:115], a[132:135], a[68:71], v[112:115]\n" \
"s_add_u32 m0, s49, 32768\n" \
"ds_read_b128 a[156:159], v5 offset:7168\n" \
"v_mfma_f32_16x16x32_bf16 v[120:123], a[132:135], a[84:87], v[120:123]\n" \
"s_waitcnt lgkmcnt(6)\n" \
"ds_read_b128 a[128:131], v5 offset:16384\n" \
"v_mfma_f32_16x16x32_bf16 v[112:115], a[136:139], a[72:75], v[112:115]\n" \
"buffer_load_dwordx4 v6, s[40:43], s16 offen offset:0 sc1 lds\n" \
"v_mfma_f32_16x16x32_bf16 v[120:123], a[136:139], a[88:91], v[120:123]\n" \
"s_waitcnt lgkmcnt(6)\n" \
"ds_read_b128 a[132:135], v5 offset:17408\n" \
"v_exp_f32 v80, v80\n" \
"v_mfma_f32_16x16x32_bf16 v[112:115], a[140:143], a[76:79], v[112:115]\n" \
"ds_read_b128 a[136:139], v5 offset:18432\n" \
"v_exp_f32 v81, v81\n" \
"v_mfma_f32_16x16x32_bf16 v[120:123], a[140:143], a[92:95], v[120:123]\n" \
"s_waitcnt lgkmcnt(7)\n" \
"v_exp_f32 v82, v82\n" \
"v_mfma_f32_16x16x32_bf16 v[124:127], a[144:147], a[80:83], v[176:179]\n" \
"s_waitcnt lgkmcnt(6)\n" \
"ds_read_b128 a[140:143], v5 offset:19456\n" \
"v_exp_f32 v83, v83\n" \
"v_mfma_f32_16x16x32_bf16 v[124:127], a[148:151], a[84:87], v[124:127]\n" \
"s_waitcnt lgkmcnt(5)\n" \
"ds_read_b128 a[144:147], v5 offset:20480\n" \
"v_exp_f32 v84, v84\n" \
"v_mfma_f32_16x16x32_bf16 v[124:127], a[152:155], a[88:91], v[124:127]\n" \
"s_waitcnt lgkmcnt(5)\n" \
"ds_read_b128 a[148:151], v5 offset:21504\n" \
"v_exp_f32 v85, v85\n" \
"v_mfma_f32_16x16x32_bf16 v[124:127], a[156:159], a[92:95], v[124:127]\n" \
"buffer_load_dwordx4 v6, s[40:43], s16 offen offset:1024 sc1 lds\n" \
"v_exp_f32 v86, v86\n" \
"ds_read_b128 a[152:155], v5 offset:22528\n" \
"v_exp_f32 v87, v87\n" \
"v_cvt_pk_bf16_f32 v136, v80, v81\n" \
"buffer_load_dwordx4 v6, s[40:43], s16 offen offset:2048 sc1 lds\n" \
"v_exp_f32 v88, v88\n" \
"ds_read_b128 a[156:159], v5 offset:23552\n" \
"v_exp_f32 v89, v89\n" \
"v_cvt_pk_bf16_f32 v137, v82, v83\n" \
"v_exp_f32 v90, v90\n" \
"v_exp_f32 v91, v91\n" \
"v_exp_f32 v92, v92\n" \
"v_exp_f32 v93, v93\n" \
"v_exp_f32 v94, v94\n" \
"v_exp_f32 v95, v95\n" \
"v_cvt_pk_bf16_f32 v138, v84, v85\n" \
"v_cvt_pk_bf16_f32 v139, v86, v87\n" \
"v_cvt_pk_bf16_f32 v140, v88, v89\n" \
"v_cvt_pk_bf16_f32 v141, v90, v91\n" \
"buffer_load_dwordx4 v6, s[40:43], s16 offen offset:3072 sc1 lds\n" \
"v_cvt_pk_bf16_f32 v142, v92, v93\n" \
"v_cvt_pk_bf16_f32 v143, v94, v95\n" \
"v_mfma_f32_16x16x32_bf16 v[160:163], v[180:183], v[128:131], v[160:163]\n" \
"v_exp_f32 v112, v112\n" \
"v_mfma_f32_16x16x32_bf16 v[164:167], v[180:183], v[132:135], v[164:167]\n" \
"v_exp_f32 v113, v113\n" \
"v_mfma_f32_16x16x32_bf16 v[168:171], v[180:183], v[136:139], v[168:171]\n" \
"v_exp_f32 v114, v114\n" \
"v_mfma_f32_16x16x32_bf16 v[172:175], v[180:183], v[140:143], v[172:175]\n" \
"s_waitcnt lgkmcnt(6)\n" \
"v_exp_f32 v115, v115\n" \
"v_mfma_f32_16x16x32_bf16 v[192:195], a[128:131], v[128:131], v[192:195]\n" \
"v_exp_f32 v120, v120\n" \
"v_mfma_f32_16x16x32_bf16 v[224:227], a[128:131], v[132:135], v[224:227]\n" \
"v_exp_f32 v121, v121\n" \
"v_mfma_f32_16x16x32_bf16 a[0:3], a[128:131], v[136:139], a[0:3]\n" \
"v_exp_f32 v122, v122\n" \
"v_mfma_f32_16x16x32_bf16 a[32:35], a[128:131], v[140:143], a[32:35]\n" \
"v_exp_f32 v123, v123\n" \
"v_mfma_f32_16x16x32_bf16 v[196:199], a[132:135], v[128:131], v[196:199]\n" \
"v_exp_f32 v124, v124\n" \
"v_mfma_f32_16x16x32_bf16 v[228:231], a[132:135], v[132:135], v[228:231]\n" \
"v_exp_f32 v125, v125\n" \
"v_mfma_f32_16x16x32_bf16 a[4:7], a[132:135], v[136:139], a[4:7]\n" \
"v_exp_f32 v126, v126\n" \
"v_mfma_f32_16x16x32_bf16 a[36:39], a[132:135], v[140:143], a[36:39]\n" \
"s_waitcnt lgkmcnt(4)\n" \
"v_exp_f32 v127, v127\n" \
"v_mfma_f32_16x16x32_bf16 v[200:203], a[136:139], v[128:131], v[200:203]\n" \
"v_cvt_pk_bf16_f32 v152, v112, v113\n" \
"v_cvt_pk_bf16_f32 v153, v114, v115\n" \
"v_mfma_f32_16x16x32_bf16 v[232:235], a[136:139], v[132:135], v[232:235]\n" \
"v_mov_b32 v154, 0\n" \
"v_mov_b32 v155, 0\n" \
"v_mfma_f32_16x16x32_bf16 a[8:11], a[136:139], v[136:139], a[8:11]\n" \
"v_cvt_pk_bf16_f32 v156, v120, v121\n" \
"ds_read_b128 v[36:39], v21 offset:1024\n" \
"v_mfma_f32_16x16x32_bf16 a[40:43], a[136:139], v[140:143], a[40:43]\n" \
"v_cvt_pk_bf16_f32 v157, v122, v123\n" \
"v_cvt_pk_bf16_f32 v158, v124, v125\n" \
"v_mfma_f32_16x16x32_bf16 v[204:207], a[140:143], v[128:131], v[204:207]\n" \
"v_cvt_pk_bf16_f32 v159, v126, v127\n" \
"ds_read_b128 v[40:43], v21 offset:2048\n" \
"v_mfma_f32_16x16x32_bf16 v[236:239], a[140:143], v[132:135], v[236:239]\n" \
"ds_read_b128 v[44:47], v21 offset:3072\n" \
"v_subrev_u32 v23, 0x1f800000, v160\n" \
"v_mfma_f32_16x16x32_bf16 a[12:15], a[140:143], v[136:139], a[12:15]\n" \
"ds_read_b128 v[48:51], v21 offset:4096\n" \
"v_subrev_u32 v27, 0x1f800000, v164\n" \
"v_mfma_f32_16x16x32_bf16 a[44:47], a[140:143], v[140:143], a[44:47]\n" \
"LH19_%=:\ns_cbranch_execz LHR1_%=\n" \
"s_waitcnt lgkmcnt(6)\n" \
"ds_read_b128 v[52:55], v21 offset:5120\n" \
"v_max_u32 v23, v23, v27\n" \
"v_mfma_f32_16x16x32_bf16 v[208:211], a[144:147], v[128:131], v[208:211]\n" \
"ds_read_b128 v[56:59], v21 offset:6144\n" \
"v_cmp_le_u32 vcc, 0x40000000, v23\n" \
"v_mfma_f32_16x16x32_bf16 v[240:243], a[144:147], v[132:135], v[240:243]\n" \
"s_or_b64 s[54:55], s[54:55], vcc\n" \
"ds_read_b128 v[60:63], v21 offset:7168\n" \
"s_nop 0\n" \
"v_mfma_f32_16x16x32_bf16 a[16:19], a[144:147], v[136:139], a[16:19]\n" \
"ds_read_b128 a[64:67], v21 offset:16384\n" \
"v_mfma_f32_16x16x32_bf16 a[48:51], a[144:147], v[140:143], a[48:51]\n" \
"s_add_u32 s17, s17, s19\n" \
"s_add_u32 m0, s49, 49152\n" \
"ds_read_b128 a[68:71], v21 offset:17408\n" \
"v_mfma_f32_16x16x32_bf16 v[212:215], a[148:151], v[128:131], v[212:215]\n" \
"s_waitcnt vmcnt(12) lgkmcnt(15)\n" \
"ds_read_b128 a[72:75], v21 offset:18432\n" \
"v_mfma_f32_16x16x32_bf16 v[244:247], a[148:151], v[132:135], v[244:247]\n" \
"s_barrier\n" \
"ds_read_b128 a[76:79], v21 offset:19456\n" \
"v_mfma_f32_16x16x32_bf16 a[20:23], a[148:151], v[136:139], a[20:23]\n" \
"buffer_load_dwordx4 v6, s[40:43], s17 offen offset:0 sc1 lds\n" \
"v_mfma_f32_16x16x32_bf16 a[52:55], a[148:151], v[140:143], a[52:55]\n" \
"s_waitcnt lgkmcnt(11)\n" \
"ds_read_b128 a[128:131], v5 offset:49152\n" \
"ds_read_b128 a[80:83], v21 offset:20480\n" \
"ds_read_b128 a[84:87], v21 offset:21504\n" \
"ds_read_b128 a[88:91], v21 offset:22528\n" \
"ds_read_b128 a[92:95], v21 offset:23552\n" \
"v_mfma_f32_16x16x32_bf16 v[216:219], a[152:155], v[128:131], v[216:219]\n" \
"buffer_load_dwordx4 v6, s[40:43], s17 offen offset:1024 sc1 lds\n" \
"v_mfma_f32_16x16x32_bf16 v[248:251], a[152:155], v[132:135], v[248:251]\n" \
"ds_read_b128 a[132:135], v5 offset:50176\n" \
"v_mfma_f32_16x16x32_bf16 a[24:27], a[152:155], v[136:139], a[24:27]\n" \
"buffer_load_dwordx4 v6, s[40:43], s17 offen offset:2048 sc1 lds\n" \
"v_mfma_f32_16x16x32_bf16 a[56:59], a[152:155], v[140:143], a[56:59]\n" \
"ds_read_b128 a[136:139], v5 offset:51200\n" \
"v_mfma_f32_16x16x32_bf16 v[220:223], a[156:159], v[128:131], v[220:223]\n" \
"buffer_load_dwordx4 v6, s[40:43], s17 offen offset:3072 sc1 lds\n" \
"v_mfma_f32_16x16x32_bf16 v[252:255], a[156:159], v[132:135], v[252:255]\n" \
"ds_read_b128 a[140:143], v5 offset:52224\n" \
"v_mfma_f32_16x16x32_bf16 a[28:31], a[156:159], v[136:139], a[28:31]\n" \
"ds_read_b128 a[144:147], v5 offset:53248\n" \
"v_mfma_f32_16x16x32_bf16 a[60:63], a[156:159], v[140:143], a[60:63]\n" \
"ds_read_b128 a[148:151], v5 offset:54272\n" \
"v_rcp_f32 v22, v160\n" \
"v_rcp_f32 v26, v164\n" \
"LS3_%=:\n" \
"v_mfma_f32_16x16x32_bf16 v[168:171], v[180:183], v[152:155], v[168:171]\n" \
"s_add_u32 s16, s16, s9\n" \
"ds_read_b128 a[152:155], v5 offset:55296\n" \
"s_add_u32 m0, s48, 0\n" \
"v_mfma_f32_16x16x32_bf16 v[172:175], v[180:183], v[156:159], v[172:175]\n" \
"s_waitcnt lgkmcnt(10)\n" \
"ds_read_b128 a[156:159], v5 offset:56320\n" \
"v_mfma_f32_16x16x32_bf16 a[0:3], a[128:131], v[152:155], a[0:3]\n" \
"buffer_load_dwordx4 v6, s[40:43], s16 offen offset:0 sc1 lds\n" \
"v_mfma_f32_16x16x32_bf16 a[32:35], a[128:131], v[156:159], a[32:35]\n" \
"s_waitcnt lgkmcnt(6)\n" \
"v_mul_f32 v192, v22, v192\nv_mul_f32 v193, v22, v193\n" \
"v_mfma_f32_16x16x32_bf16 a[4:7], a[132:135], v[152:155], a[4:7]\n" \
"ds_read_b128 a[128:131], v8 offset:32768\n" \
"v_mfma_f32_16x16x32_bf16 a[36:39], a[132:135], v[156:159], a[36:39]\n" \
"s_waitcnt lgkmcnt(6)\n" \
"v_mul_f32 v194, v22, v194\nv_mul_f32 v195, v22, v195\n" \
"v_mfma_f32_16x16x32_bf16 a[8:11], a[136:139], v[152:155], a[8:11]\n" \
"buffer_load_dwordx4 v6, s[40:43], s16 offen offset:1024 sc1 lds\n" \
"v_mfma_f32_16x16x32_bf16 a[40:43], a[136:139], v[156:159], a[40:43]\n" \
"s_waitcnt lgkmcnt(5)\n" \
"ds_read_b128 a[132:135], v8 offset:33792\n" \
"v_mfma_f32_16x16x32_bf16 a[12:15], a[140:143], v[152:155], a[12:15]\n" \
"ds_read_b128 a[136:139], v8 offset:34816\n" \
"v_accvgpr_read_b32 v80, a0\n" \
"v_mfma_f32_16x16x32_bf16 a[44:47], a[140:143], v[156:159], a[44:47]\n" \
"s_waitcnt lgkmcnt(6)\n" \
"v_mul_f32 v196, v22, v196\nv_mul_f32 v197, v22, v197\n" \
"v_mfma_f32_16x16x32_bf16 a[16:19], a[144:147], v[152:155], a[16:19]\n" \
"ds_read_b128 a[140:143], v8 offset:35840\n" \
"v_accvgpr_read_b32 v81, a1\n" \
"v_mfma_f32_16x16x32_bf16 a[48:51], a[144:147], v[156:159], a[48:51]\n" \
"s_waitcnt lgkmcnt(6)\n" \
"v_mul_f32 v198, v22, v198\nv_mul_f32 v199, v22, v199\n" \
"v_mfma_f32_16x16x32_bf16 a[20:23], a[148:151], v[152:155], a[20:23]\n" \
"buffer_load_dwordx4 v6, s[40:43], s16 offen offset:2048 sc1 lds\n" \
"v_mfma_f32_16x16x32_bf16 a[52:55], a[148:151], v[156:159], a[52:55]\n" \
"s_waitcnt lgkmcnt(5)\n" \
"ds_read_b128 a[144:147], v8 offset:36864\n" \
"v_cvt_pk_bf16_f32 v192, v192, v193\n" \
"v_mfma_f32_16x16x32_bf16 a[24:27], a[152:155], v[152:155], a[24:27]\n" \
"ds_read_b128 a[148:151], v8 offset:37888\n" \
"v_cvt_pk_bf16_f32 v193, v194, v195\n" \
"v_mfma_f32_16x16x32_bf16 a[56:59], a[152:155], v[156:159], a[56:59]\n" \
"s_waitcnt lgkmcnt(6)\n" \
"v_cvt_pk_bf16_f32 v194, v196, v197\n" \
"v_cvt_pk_bf16_f32 v195, v198, v199\n" \
"v_mfma_f32_16x16x32_bf16 a[28:31], a[156:159], v[152:155], a[28:31]\n" \
"buffer_store_dwordx4 v[192:195], v4, s[36:39], s50 offen offset:0 sc1\n" \
"v_mfma_f32_16x16x32_bf16 a[60:63], a[156:159], v[156:159], a[60:63]\n" \
"ds_read_b128 a[152:155], v8 offset:38912\n" \
"v_accvgpr_read_b32 v82, a2\n" \
"buffer_load_dwordx4 v6, s[40:43], s16 offen offset:3072 sc1 lds\n" \
"v_mul_f32 v224, v26, v224\nv_mul_f32 v225, v26, v225\n" \
"ds_read_b128 a[156:159], v8 offset:39936\n" \
"v_pk_mul_f32 v[226:227], v[226:227], v[26:27] op_sel_hi:[1,0]\n" \
"v_pk_mul_f32 v[228:229], v[228:229], v[26:27] op_sel_hi:[1,0]\n" \
"v_pk_mul_f32 v[230:231], v[230:231], v[26:27] op_sel_hi:[1,0]\n" \
"v_cvt_pk_bf16_f32 v224, v224, v225\n" \
"v_cvt_pk_bf16_f32 v225, v226, v227\n" \
"v_cvt_pk_bf16_f32 v226, v228, v229\n" \
"v_cvt_pk_bf16_f32 v227, v230, v231\n" \
"buffer_store_dwordx4 v[224:227], v4, s[36:39], s50 offen offset:1024 sc1\n" \
"v_pk_mul_f32 v[200:201], v[200:201], v[22:23] op_sel_hi:[1,0]\n" \
"v_pk_mul_f32 v[202:203], v[202:203], v[22:23] op_sel_hi:[1,0]\n" \
"v_pk_mul_f32 v[204:205], v[204:205], v[22:23] op_sel_hi:[1,0]\n" \
"v_pk_mul_f32 v[206:207], v[206:207], v[22:23] op_sel_hi:[1,0]\n" \
"v_cvt_pk_bf16_f32 v200, v200, v201\n" \
"v_cvt_pk_bf16_f32 v201, v202, v203\n" \
"v_cvt_pk_bf16_f32 v202, v204, v205\n" \
"v_cvt_pk_bf16_f32 v203, v206, v207\n" \
"v_pk_mul_f32 v[232:233], v[232:233], v[26:27] op_sel_hi:[1,0]\n" \
"v_pk_mul_f32 v[234:235], v[234:235], v[26:27] op_sel_hi:[1,0]\n" \
"v_pk_mul_f32 v[236:237], v[236:237], v[26:27] op_sel_hi:[1,0]\n" \
"v_pk_mul_f32 v[238:239], v[238:239], v[26:27] op_sel_hi:[1,0]\n" \
"v_cvt_pk_bf16_f32 v232, v232, v233\n" \
"v_cvt_pk_bf16_f32 v233, v234, v235\n" \
"buffer_store_dwordx4 v[200:203], v4, s[36:39], s50 offen offset:2048 sc1\n" \
"v_cvt_pk_bf16_f32 v234, v236, v237\n" \
"v_cvt_pk_bf16_f32 v235, v238, v239\n" \
"v_pk_mul_f32 v[208:209], v[208:209], v[22:23] op_sel_hi:[1,0]\n" \
"v_pk_mul_f32 v[210:211], v[210:211], v[22:23] op_sel_hi:[1,0]\n" \
"v_pk_mul_f32 v[212:213], v[212:213], v[22:23] op_sel_hi:[1,0]\n" \
"v_pk_mul_f32 v[214:215], v[214:215], v[22:23] op_sel_hi:[1,0]\n" \
"v_cvt_pk_bf16_f32 v208, v208, v209\n" \
"v_cvt_pk_bf16_f32 v209, v210, v211\n" \
"v_cvt_pk_bf16_f32 v210, v212, v213\n" \
"v_cvt_pk_bf16_f32 v211, v214, v215\n" \
"v_pk_mul_f32 v[240:241], v[240:241], v[26:27] op_sel_hi:[1,0]\n" \
"v_pk_mul_f32 v[242:243], v[242:243], v[26:27] op_sel_hi:[1,0]\n" \
"v_pk_mul_f32 v[244:245], v[244:245], v[26:27] op_sel_hi:[1,0]\n" \
"v_pk_mul_f32 v[246:247], v[246:247], v[26:27] op_sel_hi:[1,0]\n" \
"buffer_store_dwordx4 v[232:235], v4, s[36:39], s50 offen offset:3072 sc1\n" \
"v_cvt_pk_bf16_f32 v240, v240, v241\n" \
"v_cvt_pk_bf16_f32 v241, v242, v243\n" \
"v_cvt_pk_bf16_f32 v242, v244, v245\n" \
"v_cvt_pk_bf16_f32 v243, v246, v247\n" \
"v_pk_mul_f32 v[216:217], v[216:217], v[22:23] op_sel_hi:[1,0]\n" \
"v_pk_mul_f32 v[218:219], v[218:219], v[22:23] op_sel_hi:[1,0]\n" \
"v_pk_mul_f32 v[220:221], v[220:221], v[22:23] op_sel_hi:[1,0]\n" \
"v_pk_mul_f32 v[222:223], v[222:223], v[22:23] op_sel_hi:[1,0]\n" \
"v_cvt_pk_bf16_f32 v216, v216, v217\n" \
"v_cvt_pk_bf16_f32 v217, v218, v219\n" \
"v_cvt_pk_bf16_f32 v218, v220, v221\n" \
"v_cvt_pk_bf16_f32 v219, v222, v223\n" \
"v_pk_mul_f32 v[248:249], v[248:249], v[26:27] op_sel_hi:[1,0]\n" \
"v_pk_mul_f32 v[250:251], v[250:251], v[26:27] op_sel_hi:[1,0]\n" \
"s_waitcnt lgkmcnt(6)\n" \
"buffer_store_dwordx4 v[208:211], v7, s[36:39], s50 offen offset:0 sc1\n" \
"v_pk_mul_f32 v[252:253], v[252:253], v[26:27] op_sel_hi:[1,0]\n" \
"v_mfma_f32_16x16x32_bf16 v[64:67], a[128:131], v[32:35], v[176:179]\n" \
"v_mul_f32 v254, v26, v254\nv_mul_f32 v255, v26, v255\n" \
"v_mfma_f32_16x16x32_bf16 v[72:75], a[128:131], v[48:51], 0\n" \
"v_cvt_pk_bf16_f32 v248, v248, v249\n" \
"v_cvt_pk_bf16_f32 v249, v250, v251\n" \
"v_mfma_f32_16x16x32_bf16 v[64:67], a[132:135], v[36:39], v[64:67]\n" \
"s_add_u32 s63, s50, 262144\n" \
"v_cvt_pk_bf16_f32 v250, v252, v253\n" \
"v_cvt_pk_bf16_f32 v251, v254, v255\n" \
"v_mfma_f32_16x16x32_bf16 v[72:75], a[132:135], v[52:55], v[72:75]\n" \
"s_waitcnt lgkmcnt(4)\n" \
"v_subrev_u32 v23, 0x1f800000, v168\n" \
"v_mfma_f32_16x16x32_bf16 v[64:67], a[136:139], v[40:43], v[64:67]\n" \
"buffer_store_dwordx4 v[240:243], v7, s[36:39], s50 offen offset:1024 sc1\n" \
"v_mfma_f32_16x16x32_bf16 v[72:75], a[136:139], v[56:59], v[72:75]\n" \
"v_subrev_u32 v27, 0x1f800000, v172\n" \
"v_max_u32 v23, v23, v27\n" \
"v_mfma_f32_16x16x32_bf16 v[64:67], a[140:143], v[44:47], v[64:67]\n" \
"v_cmp_le_u32 vcc, 0x40000000, v23\n" \
"s_nop 0\n" \
"v_mfma_f32_16x16x32_bf16 v[72:75], a[140:143], v[60:63], v[72:75]\n" \
"s_waitcnt lgkmcnt(2)\n" \
"s_or_b64 s[54:55], s[54:55], vcc\n" \
"v_accvgpr_read_b32 v83, a3\n" \
"v_mfma_f32_16x16x32_bf16 v[76:79], a[144:147], v[48:51], v[176:179]\n" \
"buffer_store_dwordx4 v[216:219], v7, s[36:39], s50 offen offset:2048 sc1\n" \
"v_mfma_f32_16x16x32_bf16 v[76:79], a[148:151], v[52:55], v[76:79]\n" \
"s_waitcnt lgkmcnt(0)\n" \
"v_rcp_f32 v188, v168\n" \
"v_mfma_f32_16x16x32_bf16 v[76:79], a[152:155], v[56:59], v[76:79]\n" \
"v_rcp_f32 v24, v172\n" \
"v_mfma_f32_16x16x32_bf16 v[76:79], a[156:159], v[60:63], v[76:79]\n" \
"v_accvgpr_read_b32 v84, a4\n" \
"v_accvgpr_read_b32 v85, a5\n" \
"buffer_store_dwordx4 v[248:251], v7, s[36:39], s50 offen offset:3072 sc1\n" \
"v_accvgpr_read_b32 v86, a6\n" \
"v_accvgpr_read_b32 v87, a7\n" \
"v_pk_mul_f32 v[80:81], v[80:81], v[188:189] op_sel_hi:[1,0]\n" \
"v_pk_mul_f32 v[82:83], v[82:83], v[188:189] op_sel_hi:[1,0]\n" \
"v_pk_mul_f32 v[84:85], v[84:85], v[188:189] op_sel_hi:[1,0]\n" \
"v_pk_mul_f32 v[86:87], v[86:87], v[188:189] op_sel_hi:[1,0]\n" \
"v_exp_f32 v64, v64\n" \
"v_exp_f32 v65, v65\n" \
"v_exp_f32 v66, v66\n" \
"v_exp_f32 v67, v67\n" \
"v_exp_f32 v72, v72\n" \
"v_exp_f32 v73, v73\n" \
"v_exp_f32 v74, v74\n" \
"v_exp_f32 v75, v75\n" \
"v_exp_f32 v76, v76\n" \
"v_exp_f32 v77, v77\n" \
"v_exp_f32 v78, v78\n" \
"v_exp_f32 v79, v79\n" \
"v_cvt_pk_bf16_f32 v128, v64, v65\n" \
"v_cvt_pk_bf16_f32 v129, v66, v67\n" \
"v_mov_b32 v130, 0\n" \
"v_mov_b32 v131, 0\n" \
"s_add_u32 s17, s17, s19\n" \
"s_add_u32 m0, s48, 16384\n" \
"v_cvt_pk_bf16_f32 v132, v72, v73\n" \
"v_cvt_pk_bf16_f32 v133, v74, v75\n" \
"s_waitcnt vmcnt(20) lgkmcnt(0)\n" \
"v_cvt_pk_bf16_f32 v134, v76, v77\n" \
"v_cvt_pk_bf16_f32 v135, v78, v79\n" \
"v_cvt_pk_bf16_f32 v80, v80, v81\n" \
"s_barrier\n" \
"v_xor_b32 v5, 0x12000, v5\n" \
"v_cvt_pk_bf16_f32 v81, v82, v83\n" \
"v_cvt_pk_bf16_f32 v82, v84, v85\n" \
"buffer_load_dwordx4 v6, s[40:43], s17 offen offset:0 sc1 lds\n" \
"v_cvt_pk_bf16_f32 v83, v86, v87\n" \
"v_accvgpr_read_b32 v88, a32\n" \
"ds_read_b128 a[128:131], v5 offset:0\n" \
"v_accvgpr_read_b32 v89, a33\n" \
"v_accvgpr_read_b32 v90, a34\n" \
"v_accvgpr_read_b32 v91, a35\n" \
"buffer_load_dwordx4 v6, s[40:43], s17 offen offset:1024 sc1 lds\n" \
"v_accvgpr_read_b32 v92, a36\n" \
"v_accvgpr_read_b32 v93, a37\n" \
"ds_read_b128 a[132:135], v5 offset:1024\n" \
"v_accvgpr_read_b32 v94, a38\n" \
"v_accvgpr_read_b32 v95, a39\n" \
"v_accvgpr_read_b32 v136, a8\n" \
"buffer_load_dwordx4 v6, s[40:43], s17 offen offset:2048 sc1 lds\n" \
"v_pk_mul_f32 v[88:89], v[88:89], v[24:25] op_sel_hi:[1,0]\n" \
"v_pk_mul_f32 v[90:91], v[90:91], v[24:25] op_sel_hi:[1,0]\n" \
"ds_read_b128 a[136:139], v5 offset:2048\n" \
"v_pk_mul_f32 v[92:93], v[92:93], v[24:25] op_sel_hi:[1,0]\n" \
"v_pk_mul_f32 v[94:95], v[94:95], v[24:25] op_sel_hi:[1,0]\n" \
"v_cvt_pk_bf16_f32 v88, v88, v89\n" \
"s_xor_b32 s48, s48, 0x10000\n" \
"buffer_load_dwordx4 v6, s[40:43], s17 offen offset:3072 sc1 lds\n" \
"s_xor_b32 s49, s49, 0x10000\n" \
"s_add_u32 s44, s44, 1\n" \
"ds_read_b128 a[140:143], v5 offset:3072\n" \
"v_cvt_pk_bf16_f32 v89, v90, v91\n" \
"v_cvt_pk_bf16_f32 v90, v92, v93\n" \
"buffer_store_dwordx4 v[80:83], v4, s[36:39], s63 offen offset:0 sc1\n" \
"v_cvt_pk_bf16_f32 v91, v94, v95\n" \
"v_accvgpr_read_b32 v137, a9\n" \
"ds_read_b128 a[144:147], v5 offset:4096\n" \
"v_accvgpr_read_b32 v138, a10\n" \
"v_accvgpr_read_b32 v139, a11\n" \
"v_accvgpr_read_b32 v140, a12\n" \
"ds_read_b128 a[148:151], v5 offset:5120\n" \
"v_accvgpr_read_b32 v141, a13\n" \
"v_accvgpr_read_b32 v142, a14\n" \
"v_accvgpr_read_b32 v143, a15\n" \
"ds_read_b128 a[152:155], v5 offset:6144\n" \
"v_pk_mul_f32 v[136:137], v[136:137], v[188:189] op_sel_hi:[1,0]\n" \
"v_pk_mul_f32 v[138:139], v[138:139], v[188:189] op_sel_hi:[1,0]\n" \
"v_pk_mul_f32 v[140:141], v[140:141], v[188:189] op_sel_hi:[1,0]\n" \
"s_cmp_eq_u32 s44, 16\n" \
"s_cbranch_scc1 LAD32_%=\n" \
"s_cmp_eq_u32 s44, 15\ns_cselect_b32 s16, s56, s16\ns_cselect_b32 s9, s57, s9\ns_cselect_b32 s17, s58, s17\ns_cselect_b32 s19, s59, s19\n" \
"buffer_store_dwordx4 v[88:91], v4, s[36:39], s63 offen offset:1024 sc1\n" \
"v_mul_f32 v142, v188, v142\nv_mul_f32 v143, v188, v143\n" \
"s_waitcnt lgkmcnt(5)\n" \
"ds_read_b128 a[156:159], v5 offset:7168\n" \
"s_add_u32 s16, s16, s9\n" \
"s_add_u32 m0, s49, 32768\n" \
"v_mfma_f32_16x16x32_bf16 v[96:99], a[128:131], v[32:35], 0\n" \
"buffer_load_dwordx4 v6, s[40:43], s16 offen offset:0 sc1 lds\n" \
"v_mfma_f32_16x16x32_bf16 v[104:107], a[128:131], v[48:51], 0\n" \
"v_cvt_pk_bf16_f32 v136, v136, v137\n" \
"v_cvt_pk_bf16_f32 v137, v138, v139\n" \
"v_mfma_f32_16x16x32_bf16 v[112:115], a[128:131], a[64:67], v[176:179]\n" \
"v_cvt_pk_bf16_f32 v138, v140, v141\n" \
"v_cvt_pk_bf16_f32 v139, v142, v143\n" \
"v_mfma_f32_16x16x32_bf16 v[120:123], a[128:131], a[80:83], 0\n" \
"buffer_store_dwordx4 v[136:139], v4, s[36:39], s63 offen offset:2048 sc1\n" \
"v_mfma_f32_16x16x32_bf16 v[96:99], a[132:135], v[36:39], v[96:99]\n" \
"ds_read_b128 a[128:131], v5 offset:16384\n" \
"v_accvgpr_read_b32 v80, a40\n" \
"v_mfma_f32_16x16x32_bf16 v[104:107], a[132:135], v[52:55], v[104:107]\n" \
"buffer_load_dwordx4 v6, s[40:43], s16 offen offset:1024 sc1 lds\n" \
"v_mfma_f32_16x16x32_bf16 v[112:115], a[132:135], a[68:71], v[112:115]\n" \
"v_accvgpr_read_b32 v81, a41\n" \
"v_accvgpr_read_b32 v82, a42\n" \
"v_mfma_f32_16x16x32_bf16 v[120:123], a[132:135], a[84:87], v[120:123]\n" \
"s_waitcnt lgkmcnt(5)\n" \
"v_accvgpr_read_b32 v83, a43\n" \
"v_accvgpr_read_b32 v84, a44\n" \
"v_mfma_f32_16x16x32_bf16 v[96:99], a[136:139], v[40:43], v[96:99]\n" \
"ds_read_b128 a[132:135], v5 offset:17408\n" \
"v_accvgpr_read_b32 v85, a45\n" \
"v_mfma_f32_16x16x32_bf16 v[104:107], a[136:139], v[56:59], v[104:107]\n" \
"v_accvgpr_read_b32 v86, a46\n" \
"v_accvgpr_read_b32 v87, a47\n" \
"v_mfma_f32_16x16x32_bf16 v[112:115], a[136:139], a[72:75], v[112:115]\n" \
"buffer_load_dwordx4 v6, s[40:43], s16 offen offset:2048 sc1 lds\n" \
"v_mfma_f32_16x16x32_bf16 v[120:123], a[136:139], a[88:91], v[120:123]\n" \
"v_mul_f32 v80, v24, v80\nv_mul_f32 v81, v24, v81\n" \
"v_mfma_f32_16x16x32_bf16 v[96:99], a[140:143], v[44:47], v[96:99]\n" \
"ds_read_b128 a[136:139], v5 offset:18432\n" \
"v_accvgpr_read_b32 v88, a16\n" \
"v_mfma_f32_16x16x32_bf16 v[104:107], a[140:143], v[60:63], v[104:107]\n" \
"v_mul_f32 v82, v24, v82\nv_mul_f32 v83, v24, v83\n" \
"v_mfma_f32_16x16x32_bf16 v[112:115], a[140:143], a[76:79], v[112:115]\n" \
"v_mul_f32 v84, v24, v84\nv_mul_f32 v85, v24, v85\n" \
"v_mfma_f32_16x16x32_bf16 v[120:123], a[140:143], a[92:95], v[120:123]\n" \
"s_waitcnt lgkmcnt(5)\n" \
"v_mul_f32 v86, v24, v86\nv_mul_f32 v87, v24, v87\n" \
"v_mfma_f32_16x16x32_bf16 v[100:103], a[144:147], v[32:35], 0\n" \
"buffer_load_dwordx4 v6, s[40:43], s16 offen offset:3072 sc1 lds\n" \
"v_mfma_f32_16x16x32_bf16 v[108:111], a[144:147], v[48:51], 0\n" \
"ds_read_b128 a[140:143], v5 offset:19456\n" \
"v_cvt_pk_bf16_f32 v80, v80, v81\n" \
"v_mfma_f32_16x16x32_bf16 v[124:127], a[144:147], a[80:83], v[176:179]\n" \
"v_cvt_pk_bf16_f32 v81, v82, v83\n" \
"v_cvt_pk_bf16_f32 v82, v84, v85\n" \
"v_mfma_f32_16x16x32_bf16 v[100:103], a[148:151], v[36:39], v[100:103]\n" \
"ds_read_b128 a[144:147], v5 offset:20480\n" \
"v_cvt_pk_bf16_f32 v83, v86, v87\n" \
"v_mfma_f32_16x16x32_bf16 v[108:111], a[148:151], v[52:55], v[108:111]\n" \
"buffer_store_dwordx4 v[80:83], v4, s[36:39], s63 offen offset:3072 sc1\n" \
"v_mfma_f32_16x16x32_bf16 v[124:127], a[148:151], a[84:87], v[124:127]\n" \
"s_waitcnt lgkmcnt(5)\n" \
"v_accvgpr_read_b32 v89, a17\n" \
"v_accvgpr_read_b32 v90, a18\n" \
"v_mfma_f32_16x16x32_bf16 v[100:103], a[152:155], v[40:43], v[100:103]\n" \
"ds_read_b128 a[148:151], v5 offset:21504\n" \
"v_accvgpr_read_b32 v91, a19\n" \
"v_mfma_f32_16x16x32_bf16 v[108:111], a[152:155], v[56:59], v[108:111]\n" \
"v_accvgpr_read_b32 v92, a20\n" \
"v_accvgpr_read_b32 v93, a21\n" \
"v_mfma_f32_16x16x32_bf16 v[124:127], a[152:155], a[88:91], v[124:127]\n" \
"v_accvgpr_read_b32 v94, a22\n" \
"v_accvgpr_read_b32 v95, a23\n" \
"v_mfma_f32_16x16x32_bf16 v[100:103], a[156:159], v[44:47], v[100:103]\n" \
"ds_read_b128 a[152:155], v5 offset:22528\n" \
"v_accvgpr_read_b32 v136, a48\n" \
"v_mfma_f32_16x16x32_bf16 v[108:111], a[156:159], v[60:63], v[108:111]\n" \
"v_mul_f32 v88, v188, v88\nv_mul_f32 v89, v188, v89\n" \
"v_mfma_f32_16x16x32_bf16 v[124:127], a[156:159], a[92:95], v[124:127]\n" \
"v_mul_f32 v90, v188, v90\nv_mul_f32 v91, v188, v91\n" \
"v_mfma_f32_16x16x32_bf16 v[160:163], v[180:183], v[128:131], 0\n" \
"ds_read_b128 a[156:159], v5 offset:23552\n" \
"v_accvgpr_read_b32 v137, a49\n" \
"v_mfma_f32_16x16x32_bf16 v[164:167], v[180:183], v[132:135], 0\n" \
"s_waitcnt lgkmcnt(7)\n" \
"v_mul_f32 v92, v188, v92\nv_mul_f32 v93, v188, v93\n" \
"v_mfma_f32_16x16x32_bf16 v[192:195], a[128:131], v[128:131], 0\n" \
"v_mul_f32 v94, v188, v94\nv_mul_f32 v95, v188, v95\n" \
"v_mfma_f32_16x16x32_bf16 v[224:227], a[128:131], v[132:135], 0\n" \
"s_waitcnt lgkmcnt(6)\n" \
"v_exp_f32 v96, v96\n" \
"v_mfma_f32_16x16x32_bf16 v[196:199], a[132:135], v[128:131], 0\n" \
"v_exp_f32 v97, v97\n" \
"v_mfma_f32_16x16x32_bf16 v[228:231], a[132:135], v[132:135], 0\n" \
"s_waitcnt lgkmcnt(5)\n" \
"v_exp_f32 v98, v98\n" \
"v_mfma_f32_16x16x32_bf16 v[200:203], a[136:139], v[128:131], 0\n" \
"v_exp_f32 v99, v99\n" \
"v_mfma_f32_16x16x32_bf16 v[232:235], a[136:139], v[132:135], 0\n" \
"s_waitcnt lgkmcnt(4)\n" \
"v_exp_f32 v100, v100\n" \
"v_mfma_f32_16x16x32_bf16 v[204:207], a[140:143], v[128:131], 0\n" \
"v_exp_f32 v101, v101\n" \
"v_mfma_f32_16x16x32_bf16 v[236:239], a[140:143], v[132:135], 0\n" \
"s_waitcnt lgkmcnt(3)\n" \
"v_exp_f32 v102, v102\n" \
"v_mfma_f32_16x16x32_bf16 v[208:211], a[144:147], v[128:131], 0\n" \
"v_exp_f32 v103, v103\n" \
"v_mfma_f32_16x16x32_bf16 v[240:243], a[144:147], v[132:135], 0\n" \
"s_waitcnt lgkmcnt(2)\n" \
"v_exp_f32 v104, v104\n" \
"v_mfma_f32_16x16x32_bf16 v[212:215], a[148:151], v[128:131], 0\n" \
"v_exp_f32 v105, v105\n" \
"v_mfma_f32_16x16x32_bf16 v[244:247], a[148:151], v[132:135], 0\n" \
"s_waitcnt lgkmcnt(1)\n" \
"v_exp_f32 v106, v106\n" \
"v_mfma_f32_16x16x32_bf16 v[216:219], a[152:155], v[128:131], 0\n" \
"v_exp_f32 v107, v107\n" \
"v_mfma_f32_16x16x32_bf16 v[248:251], a[152:155], v[132:135], 0\n" \
"s_waitcnt lgkmcnt(0)\n" \
"v_exp_f32 v108, v108\n" \
"v_mfma_f32_16x16x32_bf16 v[220:223], a[156:159], v[128:131], 0\n" \
"v_exp_f32 v109, v109\n" \
"v_mfma_f32_16x16x32_bf16 v[252:255], a[156:159], v[132:135], 0\n" \
"v_exp_f32 v110, v110\n" \
"v_exp_f32 v111, v111\n" \
"v_cvt_pk_bf16_f32 v144, v96, v97\n" \
"v_cvt_pk_bf16_f32 v145, v98, v99\n" \
"v_cvt_pk_bf16_f32 v146, v100, v101\n" \
"v_cvt_pk_bf16_f32 v147, v102, v103\n" \
"v_cvt_pk_bf16_f32 v148, v104, v105\n" \
"v_cvt_pk_bf16_f32 v149, v106, v107\n" \
"v_cvt_pk_bf16_f32 v150, v108, v109\n" \
"v_cvt_pk_bf16_f32 v151, v110, v111\n" \
"v_cvt_pk_bf16_f32 v88, v88, v89\n" \
"v_cvt_pk_bf16_f32 v89, v90, v91\n" \
"v_cvt_pk_bf16_f32 v90, v92, v93\n" \
"v_cvt_pk_bf16_f32 v91, v94, v95\n" \
"v_accvgpr_read_b32 v138, a50\n" \
"v_accvgpr_read_b32 v139, a51\n" \
"buffer_store_dwordx4 v[88:91], v7, s[36:39], s63 offen offset:0 sc1\n" \
"v_accvgpr_read_b32 v140, a52\n" \
"v_accvgpr_read_b32 v141, a53\n" \
"v_accvgpr_read_b32 v142, a54\n" \
"v_accvgpr_read_b32 v143, a55\n" \
"v_accvgpr_read_b32 v80, a24\n" \
"v_accvgpr_read_b32 v81, a25\n" \
"v_pk_mul_f32 v[136:137], v[136:137], v[24:25] op_sel_hi:[1,0]\n" \
"v_pk_mul_f32 v[138:139], v[138:139], v[24:25] op_sel_hi:[1,0]\n" \
"v_pk_mul_f32 v[140:141], v[140:141], v[24:25] op_sel_hi:[1,0]\n" \
"v_pk_mul_f32 v[142:143], v[142:143], v[24:25] op_sel_hi:[1,0]\n" \
"s_add_u32 s17, s17, s19\n" \
"s_add_u32 m0, s49, 49152\n" \
"v_cvt_pk_bf16_f32 v136, v136, v137\n" \
"v_cvt_pk_bf16_f32 v137, v138, v139\n" \
"s_waitcnt vmcnt(25) lgkmcnt(8)\n" \
"v_cvt_pk_bf16_f32 v138, v140, v141\n" \
"v_cvt_pk_bf16_f32 v139, v142, v143\n" \
"v_accvgpr_read_b32 v82, a26\n" \
"s_barrier\n" \
"buffer_store_dwordx4 v[136:139], v7, s[36:39], s63 offen offset:1024 sc1\n" \
"v_accvgpr_read_b32 v83, a27\n" \
"ds_read_b128 a[128:131], v5 offset:32768\n" \
"v_accvgpr_read_b32 v84, a28\n" \
"v_accvgpr_read_b32 v85, a29\n" \
"v_accvgpr_read_b32 v86, a30\n" \
"buffer_load_dwordx4 v6, s[40:43], s17 offen offset:0 sc1 lds\n" \
"v_accvgpr_read_b32 v87, a31\n" \
"v_accvgpr_read_b32 v88, a56\n" \
"ds_read_b128 a[132:135], v5 offset:33792\n" \
"v_pk_mul_f32 v[80:81], v[80:81], v[188:189] op_sel_hi:[1,0]\n" \
"v_pk_mul_f32 v[82:83], v[82:83], v[188:189] op_sel_hi:[1,0]\n" \
"v_pk_mul_f32 v[84:85], v[84:85], v[188:189] op_sel_hi:[1,0]\n" \
"buffer_load_dwordx4 v6, s[40:43], s17 offen offset:1024 sc1 lds\n" \
"v_pk_mul_f32 v[86:87], v[86:87], v[188:189] op_sel_hi:[1,0]\n" \
"v_cvt_pk_bf16_f32 v80, v80, v81\n" \
"ds_read_b128 a[136:139], v5 offset:34816\n" \
"v_cvt_pk_bf16_f32 v81, v82, v83\n" \
"v_cvt_pk_bf16_f32 v82, v84, v85\n" \
"v_cvt_pk_bf16_f32 v83, v86, v87\n" \
"buffer_load_dwordx4 v6, s[40:43], s17 offen offset:2048 sc1 lds\n" \
"v_accvgpr_read_b32 v89, a57\n" \
"v_accvgpr_read_b32 v90, a58\n" \
"ds_read_b128 a[140:143], v5 offset:35840\n" \
"v_accvgpr_read_b32 v91, a59\n" \
"v_accvgpr_read_b32 v92, a60\n" \
"v_accvgpr_read_b32 v93, a61\n" \
"buffer_load_dwordx4 v6, s[40:43], s17 offen offset:3072 sc1 lds\n" \
"v_accvgpr_read_b32 v94, a62\n" \
"v_accvgpr_read_b32 v95, a63\n" \
"ds_read_b128 a[144:147], v5 offset:36864\n" \
"v_pk_mul_f32 v[88:89], v[88:89], v[24:25] op_sel_hi:[1,0]\n" \
"v_pk_mul_f32 v[90:91], v[90:91], v[24:25] op_sel_hi:[1,0]\n" \
"v_pk_mul_f32 v[92:93], v[92:93], v[24:25] op_sel_hi:[1,0]\n" \
"buffer_store_dwordx4 v[80:83], v7, s[36:39], s63 offen offset:2048 sc1\n" \
"v_pk_mul_f32 v[94:95], v[94:95], v[24:25] op_sel_hi:[1,0]\n" \
"v_cvt_pk_bf16_f32 v88, v88, v89\n" \
"ds_read_b128 a[148:151], v5 offset:37888\n" \
"v_cvt_pk_bf16_f32 v89, v90, v91\n" \
"v_cvt_pk_bf16_f32 v90, v92, v93\n" \
"v_cvt_pk_bf16_f32 v91, v94, v95\n" \
"v_mov_b32 v116, 0xff800000\n" \
"v_mov_b32 v117, 0xff800000\n" \
"v_mov_b32 v118, 0xff800000\n" \
"v_mov_b32 v119, 0xff800000\n" \
"buffer_store_dwordx4 v[88:91], v7, s[36:39], s63 offen offset:3072 sc1\n" \
"s_waitcnt lgkmcnt(4)\n" \
"v_mfma_f32_16x16x32_bf16 v[64:67], a[128:131], v[32:35], 0\n" \
"v_exp_f32 v112, v112\n" \
"v_mfma_f32_16x16x32_bf16 v[72:75], a[128:131], v[48:51], 0\n" \
"s_add_u32 s16, s16, s9\n" \
"v_mfma_f32_16x16x32_bf16 v[80:83], a[128:131], a[64:67], 0\n" \
"ds_read_b128 a[152:155], v5 offset:38912\n" \
"v_mfma_f32_16x16x32_bf16 v[88:91], a[128:131], a[80:83], 0\n" \
"s_add_u32 m0, s48, 0\n" \
"v_exp_f32 v113, v113\n" \
"v_mfma_f32_16x16x32_bf16 v[64:67], a[132:135], v[36:39], v[64:67]\n" \
"buffer_load_dwordx4 v6, s[40:43], s16 offen offset:0 sc1 lds\n" \
"v_mfma_f32_16x16x32_bf16 v[72:75], a[132:135], v[52:55], v[72:75]\n" \
"v_exp_f32 v114, v114\n" \
"v_mfma_f32_16x16x32_bf16 v[80:83], a[132:135], a[68:71], v[80:83]\n" \
"ds_read_b128 a[156:159], v5 offset:39936\n" \
"v_cvt_pk_bf16_f32 v152, v112, v113\n" \
"v_mfma_f32_16x16x32_bf16 v[88:91], a[132:135], a[84:87], v[88:91]\n" \
"s_waitcnt lgkmcnt(4)\n" \
"v_exp_f32 v115, v115\n" \
"v_mfma_f32_16x16x32_bf16 v[64:67], a[136:139], v[40:43], v[64:67]\n" \
"v_exp_f32 v116, v116\n" \
"v_mfma_f32_16x16x32_bf16 v[72:75], a[136:139], v[56:59], v[72:75]\n" \
"s_xor_b32 s49, s49, 0x10000\n" \
"v_cvt_pk_bf16_f32 v153, v114, v115\n" \
"v_mfma_f32_16x16x32_bf16 v[80:83], a[136:139], a[72:75], v[80:83]\n" \
"ds_read_b128 a[128:131], v5 offset:49152\n" \
"v_mfma_f32_16x16x32_bf16 v[88:91], a[136:139], a[88:91], v[88:91]\n" \
"v_exp_f32 v117, v117\n" \
"v_mfma_f32_16x16x32_bf16 v[64:67], a[140:143], v[44:47], v[64:67]\n" \
"buffer_load_dwordx4 v6, s[40:43], s16 offen offset:1024 sc1 lds\n" \
"v_mfma_f32_16x16x32_bf16 v[72:75], a[140:143], v[60:63], v[72:75]\n" \
"v_exp_f32 v118, v118\n" \
"v_mfma_f32_16x16x32_bf16 v[80:83], a[140:143], a[76:79], v[80:83]\n" \
"ds_read_b128 a[132:135], v5 offset:50176\n" \
"v_cvt_pk_bf16_f32 v154, v116, v117\n" \
"v_mfma_f32_16x16x32_bf16 v[88:91], a[140:143], a[92:95], v[88:91]\n" \
"s_waitcnt lgkmcnt(4)\n" \
"v_exp_f32 v119, v119\n" \
"v_mfma_f32_16x16x32_bf16 v[68:71], a[144:147], v[32:35], 0\n" \
"v_exp_f32 v120, v120\n" \
"v_mfma_f32_16x16x32_bf16 v[76:79], a[144:147], v[48:51], 0\n" \
"v_cvt_pk_bf16_f32 v155, v118, v119\n" \
"v_mfma_f32_16x16x32_bf16 v[84:87], a[144:147], a[64:67], 0\n" \
"ds_read_b128 a[136:139], v5 offset:51200\n" \
"v_mfma_f32_16x16x32_bf16 v[92:95], a[144:147], a[80:83], 0\n" \
"v_exp_f32 v121, v121\n" \
"v_mfma_f32_16x16x32_bf16 v[68:71], a[148:151], v[36:39], v[68:71]\n" \
"buffer_load_dwordx4 v6, s[40:43], s16 offen offset:2048 sc1 lds\n" \
"v_mfma_f32_16x16x32_bf16 v[76:79], a[148:151], v[52:55], v[76:79]\n" \
"v_exp_f32 v122, v122\n" \
"v_mfma_f32_16x16x32_bf16 v[84:87], a[148:151], a[68:71], v[84:87]\n" \
"ds_read_b128 a[140:143], v5 offset:52224\n" \
"v_cvt_pk_bf16_f32 v156, v120, v121\n" \
"v_mfma_f32_16x16x32_bf16 v[92:95], a[148:151], a[84:87], v[92:95]\n" \
"s_waitcnt lgkmcnt(4)\n" \
"v_exp_f32 v123, v123\n" \
"v_mfma_f32_16x16x32_bf16 v[68:71], a[152:155], v[40:43], v[68:71]\n" \
"v_exp_f32 v124, v124\n" \
"v_mfma_f32_16x16x32_bf16 v[76:79], a[152:155], v[56:59], v[76:79]\n" \
"v_cvt_pk_bf16_f32 v157, v122, v123\n" \
"v_mfma_f32_16x16x32_bf16 v[84:87], a[152:155], a[72:75], v[84:87]\n" \
"ds_read_b128 a[144:147], v5 offset:53248\n" \
"v_mfma_f32_16x16x32_bf16 v[92:95], a[152:155], a[88:91], v[92:95]\n" \
"v_exp_f32 v125, v125\n" \
"v_mfma_f32_16x16x32_bf16 v[68:71], a[156:159], v[44:47], v[68:71]\n" \
"buffer_load_dwordx4 v6, s[40:43], s16 offen offset:3072 sc1 lds\n" \
"v_mfma_f32_16x16x32_bf16 v[76:79], a[156:159], v[60:63], v[76:79]\n" \
"v_exp_f32 v126, v126\n" \
"v_mfma_f32_16x16x32_bf16 v[84:87], a[156:159], a[76:79], v[84:87]\n" \
"ds_read_b128 a[148:151], v5 offset:54272\n" \
"v_cvt_pk_bf16_f32 v158, v124, v125\n" \
"v_mfma_f32_16x16x32_bf16 v[92:95], a[156:159], a[92:95], v[92:95]\n" \
"s_waitcnt lgkmcnt(4)\n" \
"v_exp_f32 v127, v127\n" \
"v_mfma_f32_16x16x32_bf16 v[160:163], v[180:183], v[144:147], v[160:163]\n" \
"v_cvt_pk_bf16_f32 v159, v126, v127\n" \
"v_mfma_f32_16x16x32_bf16 v[164:167], v[180:183], v[148:151], v[164:167]\n" \
"v_mfma_f32_16x16x32_bf16 v[168:171], v[180:183], v[152:155], 0\n" \
"v_exp_f32 v64, v64\n" \
"v_mfma_f32_16x16x32_bf16 v[172:175], v[180:183], v[156:159], 0\n" \
"v_mfma_f32_16x16x32_bf16 v[192:195], a[128:131], v[144:147], v[192:195]\n" \
"v_exp_f32 v65, v65\n" \
"v_mfma_f32_16x16x32_bf16 v[224:227], a[128:131], v[148:151], v[224:227]\n" \
"LH20_%=:\ns_cbranch_execz LHR1_%=\n" \
"v_cvt_pk_bf16_f32 v128, v64, v65\n" \
"v_mfma_f32_16x16x32_bf16 a[0:3], a[128:131], v[152:155], 0\n" \
"ds_read_b128 a[152:155], v5 offset:55296\n" \
"v_mfma_f32_16x16x32_bf16 a[32:35], a[128:131], v[156:159], 0\n" \
"v_exp_f32 v66, v66\n" \
"v_mfma_f32_16x16x32_bf16 v[196:199], a[132:135], v[144:147], v[196:199]\n" \
"v_exp_f32 v67, v67\n" \
"v_mfma_f32_16x16x32_bf16 v[228:231], a[132:135], v[148:151], v[228:231]\n" \
"v_cvt_pk_bf16_f32 v129, v66, v67\n" \
"v_mfma_f32_16x16x32_bf16 a[4:7], a[132:135], v[152:155], 0\n" \
"ds_read_b128 a[156:159], v5 offset:56320\n" \
"v_mfma_f32_16x16x32_bf16 a[36:39], a[132:135], v[156:159], 0\n" \
"s_waitcnt vmcnt(20) lgkmcnt(4)\n" \
"v_exp_f32 v68, v68\n" \
"v_mfma_f32_16x16x32_bf16 v[200:203], a[136:139], v[144:147], v[200:203]\n" \
"s_barrier\n" \
"v_xor_b32 v5, 0x10000, v5\n" \
"v_mfma_f32_16x16x32_bf16 v[232:235], a[136:139], v[148:151], v[232:235]\n" \
"s_add_u32 s17, s17, s19\n" \
"v_exp_f32 v69, v69\n" \
"v_mfma_f32_16x16x32_bf16 a[8:11], a[136:139], v[152:155], 0\n" \
"ds_read_b128 a[128:131], v5 offset:0\n" \
"v_cvt_pk_bf16_f32 v130, v68, v69\n" \
"v_mfma_f32_16x16x32_bf16 a[40:43], a[136:139], v[156:159], 0\n" \
"s_add_u32 m0, s48, 16384\n" \
"v_exp_f32 v70, v70\n" \
"v_mfma_f32_16x16x32_bf16 v[204:207], a[140:143], v[144:147], v[204:207]\n" \
"buffer_load_dwordx4 v6, s[40:43], s17 offen offset:0 sc1 lds\n" \
"v_mfma_f32_16x16x32_bf16 v[236:239], a[140:143], v[148:151], v[236:239]\n" \
"s_waitcnt vmcnt(5)\n" \
"s_cmp_lg_u64 s[54:55], 0\ns_cbranch_scc1 LFBA_%=\nLFBAR_%=:\n" \
"v_mfma_f32_16x16x32_bf16 a[12:15], a[140:143], v[152:155], 0\n" \
"ds_read_b128 a[132:135], v5 offset:1024\n" \
"v_mfma_f32_16x16x32_bf16 a[44:47], a[140:143], v[156:159], 0\n" \
"s_waitcnt lgkmcnt(4)\n" \
"v_exp_f32 v71, v71\n" \
"v_mfma_f32_16x16x32_bf16 v[208:211], a[144:147], v[144:147], v[208:211]\n" \
"v_exp_f32 v72, v72\n" \
"v_mfma_f32_16x16x32_bf16 v[240:243], a[144:147], v[148:151], v[240:243]\n" \
"v_exp_f32 v73, v73\n" \
"v_mfma_f32_16x16x32_bf16 a[16:19], a[144:147], v[152:155], 0\n" \
"ds_read_b128 a[136:139], v5 offset:2048\n" \
"v_cvt_pk_bf16_f32 v131, v70, v71\n" \
"v_mfma_f32_16x16x32_bf16 a[48:51], a[144:147], v[156:159], 0\n" \
"v_exp_f32 v74, v74\n" \
"v_mfma_f32_16x16x32_bf16 v[212:215], a[148:151], v[144:147], v[212:215]\n" \
"buffer_load_dwordx4 v6, s[40:43], s17 offen offset:1024 sc1 lds\n" \
"v_mfma_f32_16x16x32_bf16 v[244:247], a[148:151], v[148:151], v[244:247]\n" \
"v_exp_f32 v75, v75\n" \
"v_mfma_f32_16x16x32_bf16 a[20:23], a[148:151], v[152:155], 0\n" \
"ds_read_b128 a[140:143], v5 offset:3072\n" \
"v_cvt_pk_bf16_f32 v132, v72, v73\n" \
"v_mfma_f32_16x16x32_bf16 a[52:55], a[148:151], v[156:159], 0\n" \
"s_waitcnt lgkmcnt(4)\n" \
"v_exp_f32 v76, v76\n" \
"v_mfma_f32_16x16x32_bf16 v[216:219], a[152:155], v[144:147], v[216:219]\n" \
"buffer_load_dwordx4 v6, s[40:43], s17 offen offset:2048 sc1 lds\n" \
"v_mfma_f32_16x16x32_bf16 v[248:251], a[152:155], v[148:151], v[248:251]\n" \
"s_add_u32 s44, s44, 1\n" \
"v_exp_f32 v77, v77\n" \
"v_mfma_f32_16x16x32_bf16 a[24:27], a[152:155], v[152:155], 0\n" \
"ds_read_b128 a[144:147], v5 offset:4096\n" \
"v_cvt_pk_bf16_f32 v133, v74, v75\n" \
"v_mfma_f32_16x16x32_bf16 a[56:59], a[152:155], v[156:159], 0\n" \
"v_exp_f32 v78, v78\n" \
"v_mfma_f32_16x16x32_bf16 v[220:223], a[156:159], v[144:147], v[220:223]\n" \
"buffer_load_dwordx4 v6, s[40:43], s17 offen offset:3072 sc1 lds\n" \
"v_mfma_f32_16x16x32_bf16 v[252:255], a[156:159], v[148:151], v[252:255]\n" \
"s_xor_b32 s48, s48, 0x10000\n" \
"v_exp_f32 v79, v79\n" \
"v_mfma_f32_16x16x32_bf16 a[28:31], a[156:159], v[152:155], 0\n" \
"ds_read_b128 a[148:151], v5 offset:5120\n" \
"v_cvt_pk_bf16_f32 v134, v76, v77\n" \
"v_mfma_f32_16x16x32_bf16 a[60:63], a[156:159], v[156:159], 0\n" \
"s_waitcnt lgkmcnt(4)\n" \
"v_cvt_pk_bf16_f32 v135, v78, v79\n" \
"buffer_store_dwordx2 v[10:11], v9, s[36:39], s52 offen sc1\n" \
"s_mov_b32 s46, s24\n" \
"s_branch LBOT_%=\n" \
"LAD32_%=:\n" \
"v_mov_b32 v96, 0\n" \
"v_mov_b32 v97, 0\n" \
"v_mov_b32 v98, 0\n" \
"v_mov_b32 v99, 0\n" \
"buffer_store_dwordx4 v[88:91], v4, s[36:39], s63 offen offset:1024 sc1\n" \
"v_pk_mul_f32 v[142:143], v[142:143], v[188:189] op_sel_hi:[1,0]\n" \
"v_cvt_pk_bf16_f32 v136, v136, v137\n" \
"v_cvt_pk_bf16_f32 v137, v138, v139\n" \
"v_cvt_pk_bf16_f32 v138, v140, v141\n" \
"v_cvt_pk_bf16_f32 v139, v142, v143\n" \
"v_accvgpr_read_b32 v80, a40\n" \
"buffer_store_dwordx4 v[136:139], v4, s[36:39], s63 offen offset:2048 sc1\n" \
"v_accvgpr_read_b32 v81, a41\n" \
"v_accvgpr_read_b32 v82, a42\n" \
"v_accvgpr_read_b32 v83, a43\n" \
"v_accvgpr_read_b32 v84, a44\n" \
"v_accvgpr_read_b32 v85, a45\n" \
"v_accvgpr_read_b32 v86, a46\n" \
"v_accvgpr_read_b32 v87, a47\n" \
"v_accvgpr_read_b32 v88, a16\n" \
"v_accvgpr_read_b32 v89, a17\n" \
"v_accvgpr_read_b32 v90, a18\n" \
"v_pk_mul_f32 v[80:81], v[80:81], v[24:25] op_sel_hi:[1,0]\n" \
"v_pk_mul_f32 v[82:83], v[82:83], v[24:25] op_sel_hi:[1,0]\n" \
"v_pk_mul_f32 v[84:85], v[84:85], v[24:25] op_sel_hi:[1,0]\n" \
"v_pk_mul_f32 v[86:87], v[86:87], v[24:25] op_sel_hi:[1,0]\n" \
"v_cvt_pk_bf16_f32 v80, v80, v81\n" \
"v_cvt_pk_bf16_f32 v81, v82, v83\n" \
"v_cvt_pk_bf16_f32 v82, v84, v85\n" \
"v_cvt_pk_bf16_f32 v83, v86, v87\n" \
"buffer_store_dwordx4 v[80:83], v4, s[36:39], s63 offen offset:3072 sc1\n" \
"v_accvgpr_read_b32 v91, a19\n" \
"v_accvgpr_read_b32 v92, a20\n" \
"v_accvgpr_read_b32 v93, a21\n" \
"v_accvgpr_read_b32 v94, a22\n" \
"v_accvgpr_read_b32 v95, a23\n" \
"v_accvgpr_read_b32 v136, a48\n" \
"v_pk_mul_f32 v[88:89], v[88:89], v[188:189] op_sel_hi:[1,0]\n" \
"v_pk_mul_f32 v[90:91], v[90:91], v[188:189] op_sel_hi:[1,0]\n" \
"v_pk_mul_f32 v[92:93], v[92:93], v[188:189] op_sel_hi:[1,0]\n" \
"v_pk_mul_f32 v[94:95], v[94:95], v[188:189] op_sel_hi:[1,0]\n" \
"v_cvt_pk_bf16_f32 v88, v88, v89\n" \
"v_cvt_pk_bf16_f32 v89, v90, v91\n" \
"v_cvt_pk_bf16_f32 v90, v92, v93\n" \
"v_cvt_pk_bf16_f32 v91, v94, v95\n" \
"buffer_store_dwordx4 v[88:91], v7, s[36:39], s63 offen offset:0 sc1\n" \
"v_accvgpr_read_b32 v137, a49\n" \
"v_accvgpr_read_b32 v138, a50\n" \
"v_accvgpr_read_b32 v139, a51\n" \
"v_accvgpr_read_b32 v140, a52\n" \
"v_accvgpr_read_b32 v141, a53\n" \
"v_accvgpr_read_b32 v142, a54\n" \
"v_accvgpr_read_b32 v143, a55\n" \
"v_accvgpr_read_b32 v80, a24\n" \
"v_accvgpr_read_b32 v81, a25\n" \
"v_accvgpr_read_b32 v82, a26\n" \
"v_pk_mul_f32 v[136:137], v[136:137], v[24:25] op_sel_hi:[1,0]\n" \
"v_pk_mul_f32 v[138:139], v[138:139], v[24:25] op_sel_hi:[1,0]\n" \
"v_pk_mul_f32 v[140:141], v[140:141], v[24:25] op_sel_hi:[1,0]\n" \
"v_pk_mul_f32 v[142:143], v[142:143], v[24:25] op_sel_hi:[1,0]\n" \
"v_cvt_pk_bf16_f32 v136, v136, v137\n" \
"v_cvt_pk_bf16_f32 v137, v138, v139\n" \
"v_cvt_pk_bf16_f32 v138, v140, v141\n" \
"v_cvt_pk_bf16_f32 v139, v142, v143\n" \
"buffer_store_dwordx4 v[136:139], v7, s[36:39], s63 offen offset:1024 sc1\n" \
"v_accvgpr_read_b32 v83, a27\n" \
"v_accvgpr_read_b32 v84, a28\n" \
"v_accvgpr_read_b32 v85, a29\n" \
"v_accvgpr_read_b32 v86, a30\n" \
"v_accvgpr_read_b32 v87, a31\n" \
"v_accvgpr_read_b32 v88, a56\n" \
"v_pk_mul_f32 v[80:81], v[80:81], v[188:189] op_sel_hi:[1,0]\n" \
"v_pk_mul_f32 v[82:83], v[82:83], v[188:189] op_sel_hi:[1,0]\n" \
"v_pk_mul_f32 v[84:85], v[84:85], v[188:189] op_sel_hi:[1,0]\n" \
"v_pk_mul_f32 v[86:87], v[86:87], v[188:189] op_sel_hi:[1,0]\n" \
"v_cvt_pk_bf16_f32 v80, v80, v81\n" \
"v_cvt_pk_bf16_f32 v81, v82, v83\n" \
"v_cvt_pk_bf16_f32 v82, v84, v85\n" \
"v_cvt_pk_bf16_f32 v83, v86, v87\n" \
"buffer_store_dwordx4 v[80:83], v7, s[36:39], s63 offen offset:2048 sc1\n" \
"v_accvgpr_read_b32 v89, a57\n" \
"v_accvgpr_read_b32 v90, a58\n" \
"v_accvgpr_read_b32 v91, a59\n" \
"v_accvgpr_read_b32 v92, a60\n" \
"v_accvgpr_read_b32 v93, a61\n" \
"v_accvgpr_read_b32 v94, a62\n" \
"v_accvgpr_read_b32 v95, a63\n" \
"v_pk_mul_f32 v[88:89], v[88:89], v[24:25] op_sel_hi:[1,0]\n" \
"v_pk_mul_f32 v[90:91], v[90:91], v[24:25] op_sel_hi:[1,0]\n" \
"v_pk_mul_f32 v[92:93], v[92:93], v[24:25] op_sel_hi:[1,0]\n" \
"v_pk_mul_f32 v[94:95], v[94:95], v[24:25] op_sel_hi:[1,0]\n" \
"v_cvt_pk_bf16_f32 v88, v88, v89\n" \
"v_cvt_pk_bf16_f32 v89, v90, v91\n" \
"v_cvt_pk_bf16_f32 v90, v92, v93\n" \
"v_cvt_pk_bf16_f32 v91, v94, v95\n" \
"buffer_store_dwordx4 v[88:91], v7, s[36:39], s63 offen offset:3072 sc1\n" \
"v_mfma_f32_32x32x16_bf16 v[192:207], v[96:99], v[96:99], 0\n" \
"v_mfma_f32_32x32x16_bf16 v[208:223], v[96:99], v[96:99], 0\n" \
"v_mfma_f32_32x32x16_bf16 v[224:239], v[96:99], v[96:99], 0\n" \
"v_mfma_f32_32x32x16_bf16 v[240:255], v[96:99], v[96:99], 0\n" \
"v_mfma_f32_32x32x16_bf16 a[0:15], v[96:99], v[96:99], 0\n" \
"v_mfma_f32_32x32x16_bf16 a[16:31], v[96:99], v[96:99], 0\n" \
"v_mfma_f32_32x32x16_bf16 a[32:47], v[96:99], v[96:99], 0\n" \
"v_mfma_f32_32x32x16_bf16 a[48:63], v[96:99], v[96:99], 0\n" \
"v_mfma_f32_32x32x16_bf16 v[160:175], v[96:99], v[96:99], 0\n" \
"v_mov_b32 v80, 0xff800000\n" \
"v_mov_b32 v81, 0xff800000\n" \
"v_mov_b32 v82, 0xff800000\n" \
"v_mov_b32 v83, 0xff800000\n" \
"v_mov_b32 v84, 0xff800000\n" \
"v_mov_b32 v85, 0xff800000\n" \
"v_mov_b32 v86, 0xff800000\n" \
"v_mov_b32 v87, 0xff800000\n" \
"v_mov_b32 v88, 0xff800000\n" \
"v_mov_b32 v89, 0xff800000\n" \
"v_mov_b32 v90, 0xff800000\n" \
"v_mov_b32 v91, 0xff800000\n" \
"v_mov_b32 v92, 0xff800000\n" \
"v_mov_b32 v93, 0xff800000\n" \
"v_mov_b32 v94, 0xff800000\n" \
"v_mov_b32 v95, 0xff800000\n" \
"s_branch LFIN_%=\n" \
"LDISP_%=:\n" \
"s_cmp_eq_u32 s44, 16\n" \
"s_cbranch_scc1 LFIN_%=\n" \
"s_cmp_eq_u32 s44, 15\ns_cselect_b32 s16, s56, s16\ns_cselect_b32 s9, s57, s9\ns_cselect_b32 s17, s58, s17\ns_cselect_b32 s19, s59, s19\n" \
"s_cbranch_scc0 LD14_%=\n" \
"buffer_load_dword v1, v191, s[36:39], 0 offen sc1\n" \
"s_branch LEVEN_%=\n" \
"LD14_%=:\n" \
"buffer_load_dwordx2 v[18:19], v17, s[36:39], 0 offen sc1\n" \
"s_branch LEVEN_%=\n" \
"LFIN_%=:\n" \
"s_waitcnt lgkmcnt(6)\n" \
"ds_read_b128 a[128:131], v5 offset:16384\n" \
"ds_read_b128 a[132:135], v5 offset:17408\n" \
"s_add_u32 s80, s65, 16384\n" \
"ds_read_b128 a[136:139], v5 offset:18432\n" \
"ds_read_b128 a[140:143], v5 offset:19456\n" \
"s_waitcnt vmcnt(8)\n" \
"s_barrier\n" \
"ds_read_b128 a[144:147], v5 offset:20480\n" \
"ds_read_b128 a[148:151], v5 offset:21504\n" \
"v_exp_f32 v80, v80\n" \
"ds_read_b128 a[152:155], v5 offset:22528\n" \
"ds_read_b128 a[156:159], v5 offset:23552\n" \
"v_exp_f32 v81, v81\n" \
"ds_read_b128 v[96:99], v29 offset:32768\n" \
"v_exp_f32 v82, v82\n" \
"v_cvt_pk_bf16_f32 v136, v80, v81\n" \
"v_mfma_f32_16x16x32_bf16 v[160:163], v[180:183], v[128:131], v[160:163]\n" \
"s_add_u32 m0, s22, 32768\n" \
"ds_read_b128 v[100:103], v29 offset:33792\n" \
"v_exp_f32 v83, v83\n" \
"v_mfma_f32_16x16x32_bf16 v[164:167], v[180:183], v[132:135], v[164:167]\n" \
"ds_read_b128 v[104:107], v29 offset:36864\n" \
"s_waitcnt lgkmcnt(9)\n" \
"v_cvt_pk_bf16_f32 v137, v82, v83\n" \
"v_mfma_f32_16x16x32_bf16 v[192:195], a[128:131], v[128:131], v[192:195]\n" \
"ds_read_b128 v[108:111], v29 offset:37888\n" \
"v_exp_f32 v84, v84\n" \
"v_mfma_f32_16x16x32_bf16 v[224:227], a[128:131], v[132:135], v[224:227]\n" \
"ds_read_b128 v[112:115], v29 offset:40960\n" \
"v_exp_f32 v85, v85\n" \
"v_mfma_f32_16x16x32_bf16 v[196:199], a[132:135], v[128:131], v[196:199]\n" \
"ds_read_b128 v[116:119], v29 offset:41984\n" \
"v_exp_f32 v86, v86\n" \
"v_mfma_f32_16x16x32_bf16 v[228:231], a[132:135], v[132:135], v[228:231]\n" \
"ds_read_b128 v[120:123], v29 offset:34816\n" \
"s_waitcnt lgkmcnt(7)\n" \
"v_cvt_pk_bf16_f32 v138, v84, v85\n" \
"v_mfma_f32_16x16x32_bf16 v[200:203], a[136:139], v[128:131], v[200:203]\n" \
"ds_read_b128 v[124:127], v29 offset:35840\n" \
"v_exp_f32 v87, v87\n" \
"v_mfma_f32_16x16x32_bf16 v[232:235], a[136:139], v[132:135], v[232:235]\n" \
"ds_write2_b32 v184, v160, v164 offset1:64\n" \
"v_exp_f32 v88, v88\n" \
"v_mfma_f32_16x16x32_bf16 v[204:207], a[140:143], v[128:131], v[204:207]\n" \
"buffer_load_dwordx4 v4, s[36:39], s80 offen offset:0 sc1 lds\n" \
"v_mfma_f32_16x16x32_bf16 v[236:239], a[140:143], v[132:135], v[236:239]\n" \
"v_exp_f32 v89, v89\n" \
"v_cvt_pk_bf16_f32 v139, v86, v87\n" \
"v_mfma_f32_16x16x32_bf16 v[208:211], a[144:147], v[128:131], v[208:211]\n" \
"v_exp_f32 v90, v90\n" \
"v_cvt_pk_bf16_f32 v140, v88, v89\n" \
"v_mfma_f32_16x16x32_bf16 v[240:243], a[144:147], v[132:135], v[240:243]\n" \
"v_exp_f32 v91, v91\n" \
"v_cvt_pk_bf16_f32 v192, v192, v193\n" \
"v_mfma_f32_16x16x32_bf16 v[212:215], a[148:151], v[128:131], v[212:215]\n" \
"v_exp_f32 v92, v92\n" \
"v_cvt_pk_bf16_f32 v141, v90, v91\n" \
"v_mfma_f32_16x16x32_bf16 v[244:247], a[148:151], v[132:135], v[244:247]\n" \
"v_exp_f32 v93, v93\n" \
"v_cvt_pk_bf16_f32 v193, v194, v195\n" \
"v_mfma_f32_16x16x32_bf16 v[216:219], a[152:155], v[128:131], v[216:219]\n" \
"v_exp_f32 v94, v94\n" \
"v_cvt_pk_bf16_f32 v142, v92, v93\n" \
"v_mfma_f32_16x16x32_bf16 v[248:251], a[152:155], v[132:135], v[248:251]\n" \
"v_exp_f32 v95, v95\n" \
"v_cvt_pk_bf16_f32 v194, v196, v197\n" \
"v_mfma_f32_16x16x32_bf16 v[220:223], a[156:159], v[128:131], v[220:223]\n" \
"v_cvt_pk_bf16_f32 v143, v94, v95\n" \
"v_cvt_pk_bf16_f32 v195, v198, v199\n" \
"v_cvt_pk_bf16_f32 v224, v224, v225\n" \
"v_mfma_f32_16x16x32_bf16 v[252:255], a[156:159], v[132:135], v[252:255]\n" \
"buffer_load_dwordx4 v4, s[36:39], s80 offen offset:1024 sc1 lds\n" \
"v_mfma_f32_16x16x32_bf16 v[168:171], v[180:183], v[136:139], v[168:171]\n" \
"ds_write_b128 v186, v[192:195] offset:0\n" \
"v_cvt_pk_bf16_f32 v225, v226, v227\n" \
"v_cvt_pk_bf16_f32 v226, v228, v229\n" \
"v_mfma_f32_16x16x32_bf16 v[172:175], v[180:183], v[140:143], v[172:175]\n" \
"v_cvt_pk_bf16_f32 v227, v230, v231\n" \
"v_cvt_pk_bf16_f32 v200, v200, v201\n" \
"v_cvt_pk_bf16_f32 v201, v202, v203\n" \
"v_mfma_f32_16x16x32_bf16 a[0:3], a[128:131], v[136:139], a[0:3]\n" \
"ds_write_b128 v186, v[224:227] offset:4096\n" \
"v_cvt_pk_bf16_f32 v202, v204, v205\n" \
"v_cvt_pk_bf16_f32 v203, v206, v207\n" \
"v_mfma_f32_16x16x32_bf16 a[32:35], a[128:131], v[140:143], a[32:35]\n" \
"buffer_load_dwordx4 v4, s[36:39], s80 offen offset:2048 sc1 lds\n" \
"v_mfma_f32_16x16x32_bf16 a[4:7], a[132:135], v[136:139], a[4:7]\n" \
"ds_write_b128 v186, v[200:203] offset:1024\n" \
"v_cvt_pk_bf16_f32 v232, v232, v233\n" \
"v_cvt_pk_bf16_f32 v233, v234, v235\n" \
"v_mfma_f32_16x16x32_bf16 a[36:39], a[132:135], v[140:143], a[36:39]\n" \
"v_cvt_pk_bf16_f32 v234, v236, v237\n" \
"v_cvt_pk_bf16_f32 v235, v238, v239\n" \
"v_cvt_pk_bf16_f32 v208, v208, v209\n" \
"v_mfma_f32_16x16x32_bf16 a[8:11], a[136:139], v[136:139], a[8:11]\n" \
"ds_write_b128 v186, v[232:235] offset:5120\n" \
"v_cvt_pk_bf16_f32 v209, v210, v211\n" \
"v_cvt_pk_bf16_f32 v210, v212, v213\n" \
"v_mfma_f32_16x16x32_bf16 a[40:43], a[136:139], v[140:143], a[40:43]\n" \
"buffer_load_dwordx4 v4, s[36:39], s80 offen offset:3072 sc1 lds\n" \
"v_mfma_f32_16x16x32_bf16 a[12:15], a[140:143], v[136:139], a[12:15]\n" \
"s_mov_b32 s81, 786432\n" \
"v_cvt_pk_bf16_f32 v211, v214, v215\n" \
"v_cvt_pk_bf16_f32 v240, v240, v241\n" \
"v_cvt_pk_bf16_f32 v241, v242, v243\n" \
"v_mfma_f32_16x16x32_bf16 a[44:47], a[140:143], v[140:143], a[44:47]\n" \
"s_add_u32 m0, s79, 49152\n" \
"ds_write_b128 v186, v[208:211] offset:2048\n" \
"v_cvt_pk_bf16_f32 v242, v244, v245\n" \
"v_cvt_pk_bf16_f32 v243, v246, v247\n" \
"v_mfma_f32_16x16x32_bf16 a[16:19], a[144:147], v[136:139], a[16:19]\n" \
"ds_write_b128 v186, v[240:243] offset:6144\n" \
"v_cvt_pk_bf16_f32 v216, v216, v217\n" \
"v_cvt_pk_bf16_f32 v217, v218, v219\n" \
"v_mfma_f32_16x16x32_bf16 a[48:51], a[144:147], v[140:143], a[48:51]\n" \
"buffer_load_dwordx4 v12, s[28:31], s81 offen offset:0 lds\n" \
"v_mfma_f32_16x16x32_bf16 a[20:23], a[148:151], v[136:139], a[20:23]\n" \
"s_add_u32 m0, s79, 50048\n" \
"v_cvt_pk_bf16_f32 v218, v220, v221\n" \
"v_cvt_pk_bf16_f32 v219, v222, v223\n" \
"v_cvt_pk_bf16_f32 v248, v248, v249\n" \
"v_mfma_f32_16x16x32_bf16 a[52:55], a[148:151], v[140:143], a[52:55]\n" \
"ds_write_b128 v186, v[216:219] offset:3072\n" \
"v_cvt_pk_bf16_f32 v249, v250, v251\n" \
"v_cvt_pk_bf16_f32 v250, v252, v253\n" \
"v_mfma_f32_16x16x32_bf16 a[24:27], a[152:155], v[136:139], a[24:27]\n" \
"v_cvt_pk_bf16_f32 v251, v254, v255\n" \
"v_and_b32 v188, 16, v4\n" \
"v_mov_b32 v189, 0x3f80\n" \
"v_mfma_f32_16x16x32_bf16 a[56:59], a[152:155], v[140:143], a[56:59]\n" \
"buffer_load_dwordx4 v12, s[28:31], s81 offen offset:128 lds\n" \
"v_mfma_f32_16x16x32_bf16 a[28:31], a[156:159], v[136:139], a[28:31]\n" \
"s_waitcnt lgkmcnt(14)\n" \
"ds_write_b128 v186, v[248:251] offset:7168\n" \
"s_mov_b32 s81, 851968\n" \
"s_add_u32 m0, s79, 57344\n" \
"v_mfma_f32_16x16x32_bf16 a[60:63], a[156:159], v[140:143], a[60:63]\n" \
"v_lshlrev_b32 v188, v188, v189\n" \
"v_mov_b32 v22, 0\n" \
"v_mov_b32 v23, 0\n" \
"v_mfma_f32_16x16x32_bf16 v[32:35], a[160:163], v[96:99], 0\n" \
"s_mov_b32 s94, 0x300003\n" \
"v_mov_b32 v26, 0\n" \
"v_mov_b32 v27, 0\n" \
"v_mfma_f32_16x16x32_bf16 v[36:39], a[164:167], v[96:99], 0\n" \
"s_waitcnt lgkmcnt(0)\n" \
"s_mov_b32 s95, 0x30000300\n" \
"v_cndmask_b32 v24, 0, v188, s[94:95]\n" \
"v_mfma_f32_16x16x32_bf16 v[40:43], a[160:163], v[100:103], 0\n" \
"s_waitcnt lgkmcnt(13)\n" \
"ds_read_b128 v[96:99], v29 offset:38912\n" \
"s_barrier\n" \
"ds_read2_b32 v[190:191], v185 offset1:64\n" \
"v_mfma_f32_16x16x32_bf16 v[44:47], a[164:167], v[100:103], 0\n" \
"buffer_load_dwordx4 v12, s[28:31], s81 offen offset:0 lds\n" \
"v_mfma_f32_16x16x32_bf16 v[48:51], a[160:163], v[104:107], 0\n" \
"s_add_u32 m0, s79, 58240\n" \
"ds_read_b128 v[100:103], v29 offset:39936\n" \
"s_mov_b32 s94, 0xc0000c\n" \
"v_mfma_f32_16x16x32_bf16 v[52:55], a[164:167], v[104:107], 0\n" \
"s_mov_b32 s95, 0xc0000c00\n" \
"ds_read_b128 v[128:131], v187 offset:0\n" \
"v_cndmask_b32 v25, 0, v188, s[94:95]\n" \
"v_mfma_f32_16x16x32_bf16 v[56:59], a[160:163], v[108:111], 0\n" \
"s_waitcnt lgkmcnt(15)\n" \
"ds_read_b128 v[104:107], v29 offset:43008\n" \
"v_add_u32 v176, 0xfffe0000, v13\n" \
"v_mfma_f32_16x16x32_bf16 v[60:63], a[164:167], v[108:111], 0\n" \
"buffer_load_dwordx4 v12, s[28:31], s81 offen offset:128 lds\n" \
"v_mfma_f32_16x16x32_bf16 v[64:67], a[160:163], v[112:115], 0\n" \
"s_mov_b32 s81, 917504\n" \
"ds_read_b128 v[108:111], v29 offset:44032\n" \
"s_add_u32 m0, s79, 81920\n" \
"v_mfma_f32_16x16x32_bf16 v[68:71], a[164:167], v[112:115], 0\n" \
"ds_read_b128 v[132:135], v187 offset:1024\n" \
"v_add_u32 v177, 0xfffe0000, v14\n" \
"v_mfma_f32_16x16x32_bf16 v[72:75], a[160:163], v[116:119], 0\n" \
"s_waitcnt lgkmcnt(15)\n" \
"ds_read_b128 v[112:115], v29 offset:45056\n" \
"s_waitcnt vmcnt(8)\n" \
"v_mfma_f32_16x16x32_bf16 v[76:79], a[164:167], v[116:119], 0\n" \
"buffer_load_dwordx4 v12, s[28:31], s81 offen offset:0 lds\n" \
"v_mfma_f32_16x16x32_bf16 v[32:35], a[168:171], v[120:123], v[32:35]\n" \
"s_add_u32 m0, s79, 82816\n" \
"ds_read_b128 v[116:119], v29 offset:46080\n" \
"v_add_u32 v178, 0xfffe0000, v15\n" \
"v_mfma_f32_16x16x32_bf16 v[36:39], a[172:175], v[120:123], v[36:39]\n" \
"ds_read_b128 v[136:139], v187 offset:2048\n" \
"v_add_u32 v179, 0xfffe0000, v16\n" \
"v_mfma_f32_16x16x32_bf16 v[40:43], a[168:171], v[124:127], v[40:43]\n" \
"ds_read_b128 v[120:123], v29 offset:49152\n" \
"v_add_u32 v180, 0xffff0000, v13\n" \
"v_mfma_f32_16x16x32_bf16 v[44:47], a[172:175], v[124:127], v[44:47]\n" \
"s_waitcnt lgkmcnt(8)\n" \
"s_barrier\n" \
"ds_read_b128 v[140:143], v187 offset:3072\n" \
"v_mfma_f32_16x16x32_bf16 v[48:51], a[168:171], v[96:99], v[48:51]\n" \
"ds_read_b128 v[124:127], v29 offset:50176\n" \
"v_add_u32 v181, 0xffff0000, v14\n" \
"v_mfma_f32_16x16x32_bf16 v[52:55], a[172:175], v[96:99], v[52:55]\n" \
"buffer_load_dwordx4 v12, s[28:31], s81 offen offset:128 lds\n" \
"v_mfma_f32_16x16x32_bf16 v[56:59], a[168:171], v[100:103], v[56:59]\n" \
"s_waitcnt lgkmcnt(7)\n" \
"ds_read_b128 v[96:99], v29 offset:53248\n" \
"s_mov_b32 s81, 983040\n" \
"v_mfma_f32_16x16x32_bf16 v[60:63], a[172:175], v[100:103], v[60:63]\n" \
"s_add_u32 m0, s79, 90112\n" \
"ds_read_b128 v[144:147], v187 offset:4096\n" \
"v_add_u32 v182, 0xffff0000, v15\n" \
"v_mfma_f32_16x16x32_bf16 v[64:67], a[168:171], v[104:107], v[64:67]\n" \
"ds_read_b128 v[100:103], v29 offset:54272\n" \
"v_add_u32 v183, 0xffff0000, v16\n" \
"v_mfma_f32_16x16x32_bf16 v[68:71], a[172:175], v[104:107], v[68:71]\n" \
"buffer_load_dwordx4 v12, s[28:31], s81 offen offset:0 lds\n" \
"v_mfma_f32_16x16x32_bf16 v[72:75], a[168:171], v[108:111], v[72:75]\n" \
"s_waitcnt lgkmcnt(7)\n" \
"ds_read_b128 v[104:107], v29 offset:47104\n" \
"s_add_u32 m0, s79, 91008\n" \
"v_mfma_f32_16x16x32_bf16 v[76:79], a[172:175], v[108:111], v[76:79]\n" \
"ds_read_b128 v[148:151], v187 offset:5120\n" \
"s_waitcnt lgkmcnt(15)\n" \
"v_mfma_f32_16x16x32_bf16 v[32:35], a[176:179], v[112:115], v[32:35]\n" \
"ds_read_b128 v[108:111], v29 offset:48128\n" \
"v_add_f32 v190, v168, v190\n" \
"v_accvgpr_read_b32 v192, a0\n" \
"v_accvgpr_read_b32 v193, a1\n" \
"v_accvgpr_read_b32 v194, a2\n" \
"v_accvgpr_read_b32 v195, a3\n" \
"v_accvgpr_read_b32 v196, a4\n" \
"v_accvgpr_read_b32 v197, a5\n" \
"v_accvgpr_read_b32 v198, a6\n" \
"v_accvgpr_read_b32 v199, a7\n" \
"v_mfma_f32_16x16x32_bf16 v[36:39], a[180:183], v[112:115], v[36:39]\n" \
"s_waitcnt lgkmcnt(15)\n" \
"ds_read_b128 v[152:155], v187 offset:6144\n" \
"v_add_f32 v191, v172, v191\n" \
"v_mfma_f32_16x16x32_bf16 v[192:195], v[24:27], v[128:131], v[192:195]\n" \
"buffer_load_dwordx4 v12, s[28:31], s81 offen offset:128 lds\n" \
"v_mfma_f32_16x16x32_bf16 v[196:199], v[22:25], v[128:131], v[196:199]\n" \
"s_add_u32 s80, s66, 16384\n" \
"s_add_u32 m0, s22, 65536\n" \
"ds_read_b128 v[156:159], v187 offset:7168\n" \
"v_mfma_f32_16x16x32_bf16 v[40:43], a[176:179], v[116:119], v[40:43]\n" \
"s_waitcnt lgkmcnt(8)\n" \
"ds_read_b128 v[112:115], v29 offset:51200\n" \
"v_subrev_u32 v188, 0x1f800000, v190\n" \
"v_mfma_f32_16x16x32_bf16 v[44:47], a[180:183], v[116:119], v[44:47]\n" \
"v_subrev_u32 v189, 0x1f800000, v191\n" \
"v_max_u32 v188, v188, v189\n" \
"v_mfma_f32_16x16x32_bf16 v[48:51], a[176:179], v[120:123], v[48:51]\n" \
"ds_read_b128 v[116:119], v29 offset:52224\n" \
"v_cmp_le_u32 vcc, 0x40000000, v188\n" \
"v_mfma_f32_16x16x32_bf16 v[52:55], a[180:183], v[120:123], v[52:55]\n" \
"buffer_load_dwordx4 v4, s[36:39], s80 offen offset:0 sc1 lds\n" \
"v_mfma_f32_16x16x32_bf16 v[56:59], a[176:179], v[124:127], v[56:59]\n" \
"s_or_b64 s[4:5], s[4:5], vcc\n" \
"ds_read_b128 v[120:123], v29 offset:55296\n" \
"s_nop 0\n" \
"v_mfma_f32_16x16x32_bf16 v[60:63], a[180:183], v[124:127], v[60:63]\n" \
"s_waitcnt vmcnt(17) lgkmcnt(3)\n" \
"s_barrier\n" \
"v_accvgpr_read_b32 v200, a8\n" \
"v_mfma_f32_16x16x32_bf16 v[64:67], a[176:179], v[96:99], v[64:67]\n" \
"ds_read_b128 v[124:127], v29 offset:56320\n" \
"v_accvgpr_read_b32 v201, a9\n" \
"v_mfma_f32_16x16x32_bf16 v[68:71], a[180:183], v[96:99], v[68:71]\n" \
"buffer_load_dwordx4 v4, s[36:39], s80 offen offset:1024 sc1 lds\n" \
"v_mfma_f32_16x16x32_bf16 v[72:75], a[176:179], v[100:103], v[72:75]\n" \
"s_waitcnt lgkmcnt(6)\n" \
"ds_read_b128 v[96:99], v29 offset:57344\n" \
"v_accvgpr_read_b32 v202, a10\n" \
"v_mfma_f32_16x16x32_bf16 v[76:79], a[180:183], v[100:103], v[76:79]\n" \
"v_rcp_f32 v186, v190\n" \
"v_mfma_f32_16x16x32_bf16 v[32:35], a[184:187], v[104:107], v[32:35]\n" \
"ds_read_b128 v[100:103], v29 offset:58368\n" \
"v_accvgpr_read_b32 v203, a11\n" \
"v_accvgpr_read_b32 v204, a12\n" \
"v_accvgpr_read_b32 v205, a13\n" \
"v_accvgpr_read_b32 v206, a14\n" \
"v_accvgpr_read_b32 v207, a15\n" \
"v_mfma_f32_16x16x32_bf16 v[36:39], a[188:191], v[104:107], v[36:39]\n" \
"s_waitcnt lgkmcnt(15)\n" \
"v_rcp_f32 v187, v191\n" \
"v_mfma_f32_16x16x32_bf16 v[200:203], v[24:27], v[132:135], v[200:203]\n" \
"buffer_load_dwordx4 v4, s[36:39], s80 offen offset:2048 sc1 lds\n" \
"v_mfma_f32_16x16x32_bf16 v[204:207], v[22:25], v[132:135], v[204:207]\n" \
"s_cmp_lg_u64 s[54:55], 0\ns_cbranch_scc1 LFBC_%=\nLFBCR_%=:\n" \
"v_bfe_u32 v188, v4, 7, 2\n" \
"v_mfma_f32_16x16x32_bf16 v[40:43], a[184:187], v[108:111], v[40:43]\n" \
"s_waitcnt lgkmcnt(4)\n" \
"ds_read_b128 v[104:107], v29 offset:61440\n" \
"v_min_u32 v188, 0, v188\n" \
"v_mfma_f32_16x16x32_bf16 v[44:47], a[188:191], v[108:111], v[44:47]\n" \
"buffer_store_dwordx2 v[10:11], v9, s[36:39], s52 offen sc1\n" \
"v_mfma_f32_16x16x32_bf16 v[48:51], a[184:187], v[112:115], v[48:51]\n" \
"ds_read_b128 v[108:111], v29 offset:62464\n" \
"v_lshl_add_u32 v17, v188, 8, s10\n" \
"v_mfma_f32_16x16x32_bf16 v[52:55], a[188:191], v[112:115], v[52:55]\n" \
"buffer_load_dwordx4 v4, s[36:39], s80 offen offset:3072 sc1 lds\n" \
"v_mfma_f32_16x16x32_bf16 v[56:59], a[184:187], v[116:119], v[56:59]\n" \
"s_waitcnt lgkmcnt(4)\n" \
"ds_read_b128 v[112:115], v28 offset:0\n" \
"s_mov_b32 s81, 1048576\n" \
"v_mfma_f32_16x16x32_bf16 v[60:63], a[188:191], v[116:119], v[60:63]\n" \
"s_add_u32 m0, s79, 131072\n" \
"v_bfe_u32 v188, v4, 4, 3\n" \
"v_lshl_add_u32 v17, v188, 3, v17\n" \
"v_mfma_f32_16x16x32_bf16 v[64:67], a[184:187], v[120:123], v[64:67]\n" \
"s_andn2_b32 s94, s88, 0xc0\n" \
"ds_read_b128 v[116:119], v28 offset:1024\n" \
"v_and_b32 v188, 0x30, v4\n" \
"v_mfma_f32_16x16x32_bf16 v[68:71], a[188:191], v[120:123], v[68:71]\n" \
"buffer_load_dwordx4 v12, s[28:31], s81 offen offset:0 lds\n" \
"v_mfma_f32_16x16x32_bf16 v[72:75], a[184:187], v[124:127], v[72:75]\n" \
"s_add_u32 m0, s79, 131968\n" \
"ds_read_b128 v[120:123], v29 offset:59392\n" \
"v_mul_f32 v192, v186, v192\n" \
"v_mfma_f32_16x16x32_bf16 v[76:79], a[188:191], v[124:127], v[76:79]\n" \
"s_waitcnt vmcnt(10) lgkmcnt(5)\n" \
"s_barrier\n" \
"v_mul_f32 v193, v186, v193\n" \
"v_mfma_f32_16x16x32_bf16 v[32:35], a[192:195], v[96:99], v[32:35]\n" \
"ds_read_b128 v[124:127], v29 offset:60416\n" \
"v_mul_f32 v194, v186, v194\n" \
"v_accvgpr_read_b32 v208, a16\n" \
"v_accvgpr_read_b32 v209, a17\n" \
"v_accvgpr_read_b32 v210, a18\n" \
"v_accvgpr_read_b32 v211, a19\n" \
"v_accvgpr_read_b32 v212, a20\n" \
"v_accvgpr_read_b32 v213, a21\n" \
"v_accvgpr_read_b32 v214, a22\n" \
"v_accvgpr_read_b32 v215, a23\n" \
"v_mfma_f32_16x16x32_bf16 v[36:39], a[196:199], v[96:99], v[36:39]\n" \
"s_waitcnt lgkmcnt(15)\n" \
"ds_read_b64_tr_b16 a[64:65], v176 offset:49152\n" \
"v_mul_f32 v195, v186, v195\n" \
"v_mfma_f32_16x16x32_bf16 v[208:211], v[24:27], v[136:139], v[208:211]\n" \
"buffer_load_dwordx4 v12, s[28:31], s81 offen offset:128 lds\n" \
"v_mfma_f32_16x16x32_bf16 v[212:215], v[22:25], v[136:139], v[212:215]\n" \
"s_mov_b32 s81, 1114112\n" \
"s_add_u32 m0, s79, 139264\n" \
"ds_read_b64_tr_b16 a[66:67], v177 offset:49152\n" \
"v_mfma_f32_16x16x32_bf16 v[40:43], a[192:195], v[100:103], v[40:43]\n" \
"s_waitcnt lgkmcnt(6)\n" \
"ds_read_b128 v[96:99], v29 offset:63488\n" \
"v_mul_f32 v196, v186, v196\n" \
"v_mfma_f32_16x16x32_bf16 v[44:47], a[196:199], v[100:103], v[44:47]\n" \
"ds_read_b64_tr_b16 a[68:69], v178 offset:49152\n" \
"v_mul_f32 v197, v186, v197\n" \
"v_mfma_f32_16x16x32_bf16 v[48:51], a[192:195], v[104:107], v[48:51]\n" \
"LH21_%=:\ns_cbranch_execz LH25_%=\n" \
"ds_read_b128 v[100:103], v29 offset:64512\n" \
"v_mul_f32 v198, v186, v198\n" \
"v_mfma_f32_16x16x32_bf16 v[52:55], a[196:199], v[104:107], v[52:55]\n" \
"buffer_load_dwordx4 v12, s[28:31], s81 offen offset:0 lds\n" \
"v_mfma_f32_16x16x32_bf16 v[56:59], a[192:195], v[108:111], v[56:59]\n" \
"s_waitcnt lgkmcnt(7)\n" \
"ds_read_b128 v[104:107], v28 offset:2048\n" \
"s_add_u32 m0, s79, 140160\n" \
"v_mfma_f32_16x16x32_bf16 v[60:63], a[196:199], v[108:111], v[60:63]\n" \
"ds_read_b64_tr_b16 a[70:71], v179 offset:49152\n" \
"v_mul_f32 v199, v186, v199\n" \
"v_mfma_f32_16x16x32_bf16 v[64:67], a[192:195], v[112:115], v[64:67]\n" \
"ds_read_b128 v[108:111], v28 offset:3072\n" \
"v_cvt_pk_bf16_f32 v192, v192, v193\n" \
"v_mfma_f32_16x16x32_bf16 v[68:71], a[196:199], v[112:115], v[68:71]\n" \
"buffer_load_dwordx4 v12, s[28:31], s81 offen offset:128 lds\n" \
"v_mfma_f32_16x16x32_bf16 v[72:75], a[192:195], v[116:119], v[72:75]\n" \
"s_waitcnt lgkmcnt(8)\n" \
"ds_read_b128 v[112:115], v28 offset:4096\n" \
"s_add_u32 s80, s64, 32768\n" \
"v_mfma_f32_16x16x32_bf16 v[76:79], a[196:199], v[116:119], v[76:79]\n" \
"s_add_u32 m0, s22, 147456\n" \
"ds_read_b64_tr_b16 a[72:73], v176 offset:57344\n" \
"v_cvt_pk_bf16_f32 v193, v194, v195\n" \
"v_mfma_f32_16x16x32_bf16 v[32:35], a[200:203], v[120:123], v[32:35]\n" \
"ds_read_b128 v[116:119], v28 offset:5120\n" \
"v_cvt_pk_bf16_f32 v194, v196, v197\n" \
"v_accvgpr_read_b32 v216, a24\n" \
"v_accvgpr_read_b32 v217, a25\n" \
"v_accvgpr_read_b32 v218, a26\n" \
"v_accvgpr_read_b32 v219, a27\n" \
"v_accvgpr_read_b32 v220, a28\n" \
"v_accvgpr_read_b32 v221, a29\n" \
"v_accvgpr_read_b32 v222, a30\n" \
"v_accvgpr_read_b32 v223, a31\n" \
"v_mfma_f32_16x16x32_bf16 v[36:39], a[204:207], v[120:123], v[36:39]\n" \
"s_waitcnt lgkmcnt(15)\n" \
"ds_read_b64_tr_b16 a[74:75], v177 offset:57344\n" \
"v_cvt_pk_bf16_f32 v195, v198, v199\n" \
"v_mfma_f32_16x16x32_bf16 v[216:219], v[24:27], v[140:143], v[216:219]\n" \
"buffer_store_dwordx4 v[192:195], v4, s[36:39], s51 offen offset:0 sc1\n" \
"v_mfma_f32_16x16x32_bf16 v[220:223], v[22:25], v[140:143], v[220:223]\n" \
"ds_read_b64_tr_b16 a[76:77], v178 offset:57344\n" \
"v_mul_f32 v200, v186, v200\n" \
"v_mfma_f32_16x16x32_bf16 v[40:43], a[200:203], v[124:127], v[40:43]\n" \
"ds_read_b128 v[120:123], v28 offset:8192\n" \
"v_mul_f32 v201, v186, v201\n" \
"v_mfma_f32_16x16x32_bf16 v[44:47], a[204:207], v[124:127], v[44:47]\n" \
"s_waitcnt lgkmcnt(9)\n" \
"s_barrier\n" \
"ds_read_b64_tr_b16 a[78:79], v179 offset:57344\n" \
"v_mfma_f32_16x16x32_bf16 v[48:51], a[200:203], v[96:99], v[48:51]\n" \
"ds_read_b128 v[124:127], v28 offset:9216\n" \
"v_mul_f32 v202, v186, v202\n" \
"v_mfma_f32_16x16x32_bf16 v[52:55], a[204:207], v[96:99], v[52:55]\n" \
"buffer_load_dwordx4 v4, s[36:39], s80 offen offset:0 sc1 lds\n" \
"v_mfma_f32_16x16x32_bf16 v[56:59], a[200:203], v[100:103], v[56:59]\n" \
"s_waitcnt lgkmcnt(8)\n" \
"ds_read_b128 v[96:99], v28 offset:12288\n" \
"v_mul_f32 v203, v186, v203\n" \
"v_mfma_f32_16x16x32_bf16 v[60:63], a[204:207], v[100:103], v[60:63]\n" \
"s_mov_b32 exec_lo, 0\nv_lshl_add_u32 v17, v188, 2, s94\ns_mov_b32 exec_lo, -1\n" \
"v_mfma_f32_16x16x32_bf16 v[64:67], a[200:203], v[104:107], v[64:67]\n" \
"s_sub_u32 s96, s72, 109051904\n" \
"ds_read_b128 v[100:103], v28 offset:13312\n" \
"s_lshr_b32 s96, s96, 11\n" \
"v_mfma_f32_16x16x32_bf16 v[68:71], a[204:207], v[104:107], v[68:71]\n" \
"buffer_load_dwordx4 v4, s[36:39], s80 offen offset:1024 sc1 lds\n" \
"v_mfma_f32_16x16x32_bf16 v[72:75], a[200:203], v[108:111], v[72:75]\n" \
"s_waitcnt lgkmcnt(7)\n" \
"ds_read_b128 v[104:107], v28 offset:6144\n" \
"s_mul_i32 s97, s96, 10923\n" \
"v_mfma_f32_16x16x32_bf16 v[76:79], a[204:207], v[108:111], v[76:79]\n" \
"s_lshr_b32 s97, s97, 16\n" \
"s_lshl_b32 s97, s97, 1\n" \
"s_sub_u32 s96, s96, s97\n" \
"v_mfma_f32_16x16x32_bf16 v[32:35], a[208:211], v[112:115], v[32:35]\n" \
"s_lshl_b32 s96, s96, 3\n" \
"ds_read_b128 v[108:111], v28 offset:7168\n" \
"s_add_u32 s23, s96, 94437376\n" \
"v_accvgpr_read_b32 v224, a32\n" \
"v_accvgpr_read_b32 v225, a33\n" \
"v_accvgpr_read_b32 v226, a34\n" \
"v_accvgpr_read_b32 v227, a35\n" \
"v_accvgpr_read_b32 v228, a36\n" \
"v_accvgpr_read_b32 v229, a37\n" \
"v_accvgpr_read_b32 v230, a38\n" \
"v_accvgpr_read_b32 v231, a39\n" \
"v_mfma_f32_16x16x32_bf16 v[36:39], a[212:215], v[112:115], v[36:39]\n" \
"s_waitcnt lgkmcnt(15)\n" \
"s_sub_u32 s96, s73, 109051904\n" \
"s_lshr_b32 s96, s96, 11\n" \
"v_mfma_f32_16x16x32_bf16 v[224:227], v[24:27], v[144:147], v[224:227]\n" \
"buffer_load_dwordx4 v4, s[36:39], s80 offen offset:2048 sc1 lds\n" \
"v_mfma_f32_16x16x32_bf16 v[228:231], v[22:25], v[144:147], v[228:231]\n" \
"s_mul_i32 s97, s96, 10923\n" \
"s_lshr_b32 s97, s97, 16\n" \
"s_lshl_b32 s97, s97, 1\n" \
"v_mfma_f32_16x16x32_bf16 v[40:43], a[208:211], v[116:119], v[40:43]\n" \
"s_waitcnt lgkmcnt(4)\n" \
"ds_read_b128 v[112:115], v28 offset:10240\n" \
"s_sub_u32 s96, s96, s97\n" \
"v_mfma_f32_16x16x32_bf16 v[44:47], a[212:215], v[116:119], v[44:47]\n" \
"s_lshl_b32 s96, s96, 3\n" \
"s_add_u32 s24, s96, 94437376\n" \
"s_sub_u32 s96, s74, 109051904\n" \
"v_mfma_f32_16x16x32_bf16 v[48:51], a[208:211], v[120:123], v[48:51]\n" \
"s_lshr_b32 s96, s96, 11\n" \
"ds_read_b128 v[116:119], v28 offset:11264\n" \
"s_mul_i32 s97, s96, 10923\n" \
"v_mfma_f32_16x16x32_bf16 v[52:55], a[212:215], v[120:123], v[52:55]\n" \
"buffer_load_dwordx4 v4, s[36:39], s80 offen offset:3072 sc1 lds\n" \
"v_mfma_f32_16x16x32_bf16 v[56:59], a[208:211], v[124:127], v[56:59]\n" \
"s_mov_b32 s81, 1179648\n" \
"ds_read_b128 v[120:123], v28 offset:14336\n" \
"s_add_u32 m0, s79, 98304\n" \
"v_mfma_f32_16x16x32_bf16 v[60:63], a[212:215], v[124:127], v[60:63]\n" \
"s_waitcnt vmcnt(14) lgkmcnt(5)\n" \
"s_barrier\n" \
"s_lshr_b32 s97, s97, 16\n" \
"v_mfma_f32_16x16x32_bf16 v[64:67], a[208:211], v[96:99], v[64:67]\n" \
"s_lshl_b32 s97, s97, 1\n" \
"ds_read_b128 v[124:127], v28 offset:15360\n" \
"s_sub_u32 s96, s96, s97\n" \
"v_mfma_f32_16x16x32_bf16 v[68:71], a[212:215], v[96:99], v[68:71]\n" \
"buffer_load_dwordx4 v12, s[28:31], s81 offen offset:0 lds\n" \
"v_mfma_f32_16x16x32_bf16 v[72:75], a[208:211], v[100:103], v[72:75]\n" \
"s_waitcnt lgkmcnt(4)\n" \
"ds_read_b128 v[96:99], v28 offset:16384\n" \
"s_add_u32 m0, s79, 99200\n" \
"v_mfma_f32_16x16x32_bf16 v[76:79], a[212:215], v[100:103], v[76:79]\n" \
"s_lshl_b32 s96, s96, 3\n" \
"ds_read_b64_tr_b16 a[80:81], v180 offset:16384\n" \
"s_add_u32 s45, s96, 94437376\n" \
"v_mfma_f32_16x16x32_bf16 v[32:35], a[216:219], v[104:107], v[32:35]\n" \
"ds_read_b128 v[100:103], v28 offset:17408\n" \
"v_mul_f32 v204, v186, v204\n" \
"v_accvgpr_read_b32 v232, a40\n" \
"v_accvgpr_read_b32 v233, a41\n" \
"v_accvgpr_read_b32 v234, a42\n" \
"v_accvgpr_read_b32 v235, a43\n" \
"v_accvgpr_read_b32 v236, a44\n" \
"v_accvgpr_read_b32 v237, a45\n" \
"v_accvgpr_read_b32 v238, a46\n" \
"v_accvgpr_read_b32 v239, a47\n" \
"v_mfma_f32_16x16x32_bf16 v[36:39], a[220:223], v[104:107], v[36:39]\n" \
"s_waitcnt lgkmcnt(15)\n" \
"ds_read_b64_tr_b16 a[82:83], v181 offset:16384\n" \
"v_mul_f32 v205, v186, v205\n" \
"v_mfma_f32_16x16x32_bf16 v[232:235], v[24:27], v[148:151], v[232:235]\n" \
"buffer_load_dwordx2 v[18:19], v17, s[36:39], 0 offen sc1\n" \
"v_mfma_f32_16x16x32_bf16 v[236:239], v[22:25], v[148:151], v[236:239]\n" \
"ds_read_b64_tr_b16 a[84:85], v182 offset:16384\n" \
"v_mul_f32 v206, v186, v206\n" \
"v_mfma_f32_16x16x32_bf16 v[40:43], a[216:219], v[108:111], v[40:43]\n" \
"s_waitcnt lgkmcnt(7)\n" \
"ds_read_b128 v[104:107], v28 offset:20480\n" \
"v_mul_f32 v207, v186, v207\n" \
"v_mfma_f32_16x16x32_bf16 v[44:47], a[220:223], v[108:111], v[44:47]\n" \
"ds_read_b64_tr_b16 a[86:87], v183 offset:16384\n" \
"v_cvt_pk_bf16_f32 v200, v200, v201\n" \
"v_mfma_f32_16x16x32_bf16 v[48:51], a[216:219], v[112:115], v[48:51]\n" \
"ds_read_b128 v[108:111], v28 offset:21504\n" \
"v_cvt_pk_bf16_f32 v201, v202, v203\n" \
"v_mfma_f32_16x16x32_bf16 v[52:55], a[220:223], v[112:115], v[52:55]\n" \
"buffer_load_dwordx4 v12, s[28:31], s81 offen offset:128 lds\n" \
"v_mfma_f32_16x16x32_bf16 v[56:59], a[216:219], v[116:119], v[56:59]\n" \
"s_waitcnt lgkmcnt(8)\n" \
"ds_read_b128 v[112:115], v28 offset:24576\n" \
"s_mov_b32 s81, 1245184\n" \
"v_mfma_f32_16x16x32_bf16 v[60:63], a[220:223], v[116:119], v[60:63]\n" \
"s_add_u32 m0, s79, 106496\n" \
"ds_read_b64_tr_b16 a[88:89], v180 offset:24576\n" \
"v_cvt_pk_bf16_f32 v202, v204, v205\n" \
"v_mfma_f32_16x16x32_bf16 v[64:67], a[216:219], v[120:123], v[64:67]\n" \
"ds_read_b128 v[116:119], v28 offset:25600\n" \
"v_cvt_pk_bf16_f32 v203, v206, v207\n" \
"v_mfma_f32_16x16x32_bf16 v[68:71], a[220:223], v[120:123], v[68:71]\n" \
"buffer_store_dwordx4 v[200:203], v4, s[36:39], s51 offen offset:2048 sc1\n" \
"v_mfma_f32_16x16x32_bf16 v[72:75], a[216:219], v[124:127], v[72:75]\n" \
"ds_read_b128 v[120:123], v28 offset:18432\n" \
"v_mul_f32 v208, v186, v208\n" \
"v_mfma_f32_16x16x32_bf16 v[76:79], a[220:223], v[124:127], v[76:79]\n" \
"s_waitcnt vmcnt(26) lgkmcnt(9)\n" \
"s_barrier\n" \
"ds_read_b64_tr_b16 a[90:91], v181 offset:24576\n" \
"v_mfma_f32_16x16x32_bf16 v[32:35], a[224:227], v[96:99], v[32:35]\n" \
"ds_read_b128 v[124:127], v28 offset:19456\n" \
"v_mul_f32 v209, v186, v209\n" \
"v_accvgpr_read_b32 v240, a48\n" \
"v_accvgpr_read_b32 v241, a49\n" \
"v_accvgpr_read_b32 v242, a50\n" \
"v_accvgpr_read_b32 v243, a51\n" \
"v_accvgpr_read_b32 v244, a52\n" \
"v_accvgpr_read_b32 v245, a53\n" \
"v_accvgpr_read_b32 v246, a54\n" \
"v_accvgpr_read_b32 v247, a55\n" \
"v_mfma_f32_16x16x32_bf16 v[36:39], a[228:231], v[96:99], v[36:39]\n" \
"s_waitcnt lgkmcnt(15)\n" \
"ds_read_b64_tr_b16 a[92:93], v182 offset:24576\n" \
"v_mul_f32 v210, v186, v210\n" \
"v_mfma_f32_16x16x32_bf16 v[240:243], v[24:27], v[152:155], v[240:243]\n" \
"buffer_load_dwordx4 v12, s[28:31], s81 offen offset:0 lds\n" \
"v_mfma_f32_16x16x32_bf16 v[244:247], v[22:25], v[152:155], v[244:247]\n" \
"s_add_u32 m0, s79, 107392\n" \
"ds_read_b64_tr_b16 a[94:95], v183 offset:24576\n" \
"v_mul_f32 v211, v186, v211\n" \
"v_mfma_f32_16x16x32_bf16 v[40:43], a[224:227], v[100:103], v[40:43]\n" \
"s_waitcnt lgkmcnt(8)\n" \
"ds_read_b128 v[96:99], v28 offset:22528\n" \
"v_mul_f32 v212, v186, v212\n" \
"v_mfma_f32_16x16x32_bf16 v[44:47], a[228:231], v[100:103], v[44:47]\n" \
"v_mul_f32 v213, v186, v213\n" \
"v_mul_f32 v214, v186, v214\n" \
"v_mfma_f32_16x16x32_bf16 v[48:51], a[224:227], v[104:107], v[48:51]\n" \
"ds_read_b128 v[100:103], v28 offset:23552\n" \
"v_mul_f32 v215, v186, v215\n" \
"v_mfma_f32_16x16x32_bf16 v[52:55], a[228:231], v[104:107], v[52:55]\n" \
"buffer_load_dwordx4 v12, s[28:31], s81 offen offset:128 lds\n" \
"v_mfma_f32_16x16x32_bf16 v[56:59], a[224:227], v[108:111], v[56:59]\n" \
"s_waitcnt lgkmcnt(7)\n" \
"ds_read_b128 v[104:107], v28 offset:26624\n" \
"s_add_u32 s80, s65, 32768\n" \
"v_mfma_f32_16x16x32_bf16 v[60:63], a[228:231], v[108:111], v[60:63]\n" \
"s_add_u32 m0, s22, 114688\n" \
"v_cvt_pk_bf16_f32 v208, v208, v209\n" \
"v_cvt_pk_bf16_f32 v209, v210, v211\n" \
"v_mfma_f32_16x16x32_bf16 v[64:67], a[224:227], v[112:115], v[64:67]\n" \
"ds_read_b128 v[108:111], v28 offset:27648\n" \
"v_cvt_pk_bf16_f32 v210, v212, v213\n" \
"v_mfma_f32_16x16x32_bf16 v[68:71], a[228:231], v[112:115], v[68:71]\n" \
"buffer_load_dwordx4 v4, s[36:39], s80 offen offset:0 sc1 lds\n" \
"v_mfma_f32_16x16x32_bf16 v[72:75], a[224:227], v[116:119], v[72:75]\n" \
"s_waitcnt lgkmcnt(6)\n" \
"ds_read_b128 v[112:115], v28 offset:28672\n" \
"v_cvt_pk_bf16_f32 v211, v214, v215\n" \
"v_mfma_f32_16x16x32_bf16 v[76:79], a[228:231], v[116:119], v[76:79]\n" \
"buffer_store_dwordx4 v[208:211], v7, s[36:39], s51 offen offset:0 sc1\n" \
"v_mfma_f32_16x16x32_bf16 v[32:35], a[232:235], v[120:123], v[32:35]\n" \
"ds_read_b128 v[116:119], v28 offset:29696\n" \
"v_mul_f32 v216, v186, v216\n" \
"v_accvgpr_read_b32 v248, a56\n" \
"v_accvgpr_read_b32 v249, a57\n" \
"v_accvgpr_read_b32 v250, a58\n" \
"v_accvgpr_read_b32 v251, a59\n" \
"v_accvgpr_read_b32 v252, a60\n" \
"v_accvgpr_read_b32 v253, a61\n" \
"v_accvgpr_read_b32 v254, a62\n" \
"v_accvgpr_read_b32 v255, a63\n" \
"v_mfma_f32_16x16x32_bf16 v[36:39], a[236:239], v[120:123], v[36:39]\n" \
"s_waitcnt lgkmcnt(15)\n" \
"v_mul_f32 v217, v186, v217\n" \
"v_mul_f32 v218, v186, v218\n" \
"v_mfma_f32_16x16x32_bf16 v[248:251], v[24:27], v[156:159], v[248:251]\n" \
"v_mul_f32 v219, v186, v219\n" \
"v_mfma_f32_16x16x32_bf16 v[252:255], v[22:25], v[156:159], v[252:255]\n" \
"v_mul_f32 v220, v186, v220\n" \
"v_mul_f32 v221, v186, v221\n" \
"v_mfma_f32_16x16x32_bf16 v[40:43], a[232:235], v[124:127], v[40:43]\n" \
"ds_read_b128 v[120:123], v28 offset:32768\n" \
"s_waitcnt vmcnt(6)\n" \
"v_mfma_f32_16x16x32_bf16 v[44:47], a[236:239], v[124:127], v[44:47]\n" \
"s_waitcnt vmcnt(13) lgkmcnt(5)\n" \
"s_barrier\n" \
"v_cmp_ne_u64 vcc, s[14:15], v[18:19]\n" \
"v_mfma_f32_16x16x32_bf16 v[48:51], a[232:235], v[96:99], v[48:51]\n" \
"ds_read_b128 v[124:127], v28 offset:33792\n" \
"s_nop 0\n" \
"v_mfma_f32_16x16x32_bf16 v[52:55], a[236:239], v[96:99], v[52:55]\n" \
"buffer_load_dwordx4 v4, s[36:39], s80 offen offset:1024 sc1 lds\n" \
"v_mfma_f32_16x16x32_bf16 v[56:59], a[232:235], v[100:103], v[56:59]\n" \
"s_waitcnt lgkmcnt(4)\n" \
"ds_read_b128 v[96:99], v28 offset:36864\n" \
"s_cbranch_vccnz LPS_%=\nLPSR_%=:\n" \
"v_mfma_f32_16x16x32_bf16 v[60:63], a[236:239], v[100:103], v[60:63]\n" \
"ds_read_b64_tr_b16 a[128:129], v13 offset:0\n" \
"v_mul_f32 v222, v186, v222\n" \
"v_mfma_f32_16x16x32_bf16 v[64:67], a[232:235], v[104:107], v[64:67]\n" \
"ds_read_b128 v[100:103], v28 offset:37888\n" \
"v_mul_f32 v223, v186, v223\n" \
"v_mfma_f32_16x16x32_bf16 v[68:71], a[236:239], v[104:107], v[68:71]\n" \
"s_mov_b64 exec, 0xffff\nbuffer_load_dword v1, v2, s[36:39], s67 offen sc1\ns_mov_b64 exec, -1\n" \
"v_mfma_f32_16x16x32_bf16 v[72:75], a[232:235], v[108:111], v[72:75]\n" \
"s_waitcnt lgkmcnt(5)\n" \
"ds_read_b128 v[104:107], v28 offset:30720\n" \
"v_cvt_pk_bf16_f32 v216, v216, v217\n" \
"v_mfma_f32_16x16x32_bf16 v[76:79], a[236:239], v[108:111], v[76:79]\n" \
"ds_read_b64_tr_b16 a[130:131], v14 offset:0\n" \
"v_cvt_pk_bf16_f32 v217, v218, v219\n" \
"v_mfma_f32_16x16x32_bf16 v[32:35], a[240:243], v[112:115], v[32:35]\n" \
"ds_read_b128 v[108:111], v28 offset:31744\n" \
"v_cvt_pk_bf16_f32 v218, v220, v221\n" \
"v_mfma_f32_16x16x32_bf16 v[36:39], a[244:247], v[112:115], v[36:39]\n" \
"buffer_load_dwordx4 v4, s[36:39], s80 offen offset:2048 sc1 lds\n" \
"v_mfma_f32_16x16x32_bf16 v[40:43], a[240:243], v[116:119], v[40:43]\n" \
"s_waitcnt lgkmcnt(6)\n" \
"ds_read_b128 v[112:115], v28 offset:34816\n" \
"v_cvt_pk_bf16_f32 v219, v222, v223\n" \
"v_mfma_f32_16x16x32_bf16 v[44:47], a[244:247], v[116:119], v[44:47]\n" \
"buffer_store_dwordx4 v[216:219], v7, s[36:39], s51 offen offset:2048 sc1\n" \
"v_mfma_f32_16x16x32_bf16 v[48:51], a[240:243], v[120:123], v[48:51]\n" \
"ds_read_b128 v[116:119], v28 offset:35840\n" \
"v_mul_f32 v224, v187, v224\n" \
"v_mfma_f32_16x16x32_bf16 v[52:55], a[244:247], v[120:123], v[52:55]\n" \
"ds_read_b64_tr_b16 a[132:133], v15 offset:0\n" \
"v_mul_f32 v225, v187, v225\n" \
"v_mfma_f32_16x16x32_bf16 v[56:59], a[240:243], v[124:127], v[56:59]\n" \
"ds_read_b128 v[120:123], v28 offset:38912\n" \
"v_mul_f32 v226, v187, v226\n" \
"v_mfma_f32_16x16x32_bf16 v[60:63], a[244:247], v[124:127], v[60:63]\n" \
"s_waitcnt vmcnt(21) lgkmcnt(7)\n" \
"s_barrier\n" \
"ds_read_b64_tr_b16 a[134:135], v16 offset:0\n" \
"v_mfma_f32_16x16x32_bf16 v[64:67], a[240:243], v[96:99], v[64:67]\n" \
"ds_read_b128 v[124:127], v28 offset:39936\n" \
"v_mul_f32 v227, v187, v227\n" \
"v_mfma_f32_16x16x32_bf16 v[68:71], a[244:247], v[96:99], v[68:71]\n" \
"ds_read_b64_tr_b16 a[136:137], v13 offset:8192\n" \
"v_mul_f32 v228, v187, v228\n" \
"v_mfma_f32_16x16x32_bf16 v[72:75], a[240:243], v[100:103], v[72:75]\n" \
"s_waitcnt lgkmcnt(7)\n" \
"ds_read_b128 v[96:99], v28 offset:40960\n" \
"v_mul_f32 v229, v187, v229\n" \
"v_mfma_f32_16x16x32_bf16 v[76:79], a[244:247], v[100:103], v[76:79]\n" \
"buffer_load_dwordx4 v4, s[36:39], s80 offen offset:3072 sc1 lds\n" \
"v_mfma_f32_16x16x32_bf16 v[32:35], a[248:251], v[104:107], v[32:35]\n" \
"s_add_u32 s80, s67, 0\n" \
"ds_read_b128 v[100:103], v28 offset:41984\n" \
"s_add_u32 m0, s22, 49152\n" \
"v_mfma_f32_16x16x32_bf16 v[36:39], a[252:255], v[104:107], v[36:39]\n" \
"ds_read_b64_tr_b16 a[138:139], v14 offset:8192\n" \
"v_mul_f32 v230, v187, v230\n" \
"v_mfma_f32_16x16x32_bf16 v[40:43], a[248:251], v[108:111], v[40:43]\n" \
"s_waitcnt lgkmcnt(8)\n" \
"ds_read_b128 v[104:107], v28 offset:45056\n" \
"v_mul_f32 v231, v187, v231\n" \
"v_mfma_f32_16x16x32_bf16 v[44:47], a[252:255], v[108:111], v[44:47]\n" \
"buffer_load_dwordx4 v31, s[36:39], s80 offen offset:0 sc1 lds\n" \
"v_mfma_f32_16x16x32_bf16 v[48:51], a[248:251], v[112:115], v[48:51]\n" \
"ds_read_b128 v[108:111], v28 offset:46080\n" \
"v_cvt_pk_bf16_f32 v224, v224, v225\n" \
"v_mfma_f32_16x16x32_bf16 v[52:55], a[252:255], v[112:115], v[52:55]\n" \
"ds_read_b64_tr_b16 a[140:141], v15 offset:8192\n" \
"v_cvt_pk_bf16_f32 v225, v226, v227\n" \
"v_mfma_f32_16x16x32_bf16 v[56:59], a[248:251], v[116:119], v[56:59]\n" \
"s_waitcnt lgkmcnt(7)\n" \
"ds_read_b128 v[112:115], v29 offset:0\n" \
"ds_read_b64_tr_b16 a[142:143], v16 offset:8192\n" \
"v_mfma_f32_16x16x32_bf16 v[60:63], a[252:255], v[116:119], v[60:63]\n" \
"buffer_load_dwordx4 v31, s[36:39], s80 offen offset:1024 sc1 lds\n" \
"v_mfma_f32_16x16x32_bf16 v[64:67], a[248:251], v[120:123], v[64:67]\n" \
"ds_read_b128 v[116:119], v29 offset:1024\n" \
"v_cvt_pk_bf16_f32 v226, v228, v229\n" \
"v_mfma_f32_16x16x32_bf16 v[68:71], a[252:255], v[120:123], v[68:71]\n" \
"v_cvt_pk_bf16_f32 v227, v230, v231\n" \
"v_mul_f32 v232, v187, v232\n" \
"v_mfma_f32_16x16x32_bf16 v[72:75], a[248:251], v[124:127], v[72:75]\n" \
"ds_read_b128 v[120:123], v28 offset:43008\n" \
"v_mul_f32 v233, v187, v233\n" \
"v_mfma_f32_16x16x32_bf16 v[76:79], a[252:255], v[124:127], v[76:79]\n" \
"s_waitcnt lgkmcnt(2)\n" \
"s_barrier\n" \
"v_mul_f32 v234, v187, v234\n" \
"v_mfma_f32_16x16x32_bf16 v[32:35], a[64:67], v[96:99], v[32:35]\n" \
"ds_read_b128 v[124:127], v28 offset:44032\n" \
"v_mul_f32 v235, v187, v235\n" \
"v_mfma_f32_16x16x32_bf16 v[36:39], a[68:71], v[96:99], v[36:39]\n" \
"buffer_store_dwordx4 v[224:227], v4, s[36:39], s51 offen offset:1024 sc1\n" \
"v_mfma_f32_16x16x32_bf16 v[40:43], a[64:67], v[100:103], v[40:43]\n" \
"s_waitcnt lgkmcnt(6)\n" \
"ds_read_b128 v[96:99], v28 offset:47104\n" \
"v_mul_f32 v236, v187, v236\n" \
"v_mfma_f32_16x16x32_bf16 v[44:47], a[68:71], v[100:103], v[44:47]\n" \
"v_mul_f32 v237, v187, v237\n" \
"v_mul_f32 v238, v187, v238\n" \
"v_mfma_f32_16x16x32_bf16 v[48:51], a[64:67], v[104:107], v[48:51]\n" \
"ds_read_b128 v[100:103], v28 offset:48128\n" \
"v_mul_f32 v239, v187, v239\n" \
"v_mfma_f32_16x16x32_bf16 v[52:55], a[68:71], v[104:107], v[52:55]\n" \
"v_cvt_pk_bf16_f32 v232, v232, v233\n" \
"v_cvt_pk_bf16_f32 v233, v234, v235\n" \
"v_mfma_f32_16x16x32_bf16 v[56:59], a[64:67], v[108:111], v[56:59]\n" \
"s_waitcnt lgkmcnt(4)\n" \
"ds_read_b128 v[104:107], v29 offset:2048\n" \
"v_cvt_pk_bf16_f32 v234, v236, v237\n" \
"v_mfma_f32_16x16x32_bf16 v[60:63], a[68:71], v[108:111], v[60:63]\n" \
"buffer_load_dwordx4 v31, s[36:39], s80 offen offset:2048 sc1 lds\n" \
"v_mfma_f32_16x16x32_bf16 v[64:67], a[64:67], v[112:115], v[64:67]\n" \
"ds_read_b128 v[108:111], v29 offset:3072\n" \
"v_cvt_pk_bf16_f32 v235, v238, v239\n" \
"v_mfma_f32_16x16x32_bf16 v[68:71], a[68:71], v[112:115], v[68:71]\n" \
"v_mul_f32 v240, v187, v240\n" \
"v_mul_f32 v241, v187, v241\n" \
"v_mfma_f32_16x16x32_bf16 v[72:75], a[64:67], v[116:119], v[72:75]\n" \
"s_waitcnt lgkmcnt(4)\n" \
"ds_read_b128 v[112:115], v29 offset:4096\n" \
"v_mul_f32 v242, v187, v242\n" \
"v_mfma_f32_16x16x32_bf16 v[76:79], a[68:71], v[116:119], v[76:79]\n" \
"buffer_store_dwordx4 v[232:235], v4, s[36:39], s51 offen offset:3072 sc1\n" \
"v_mfma_f32_16x16x32_bf16 v[32:35], a[72:75], v[120:123], v[32:35]\n" \
"ds_read_b128 v[116:119], v29 offset:5120\n" \
"v_mul_f32 v243, v187, v243\n" \
"v_mfma_f32_16x16x32_bf16 v[36:39], a[76:79], v[120:123], v[36:39]\n" \
"v_mul_f32 v244, v187, v244\n" \
"v_mul_f32 v245, v187, v245\n" \
"v_mfma_f32_16x16x32_bf16 v[40:43], a[72:75], v[124:127], v[40:43]\n" \
"ds_read_b128 v[120:123], v29 offset:8192\n" \
"v_mul_f32 v246, v187, v246\n" \
"v_mfma_f32_16x16x32_bf16 v[44:47], a[76:79], v[124:127], v[44:47]\n" \
"s_waitcnt vmcnt(12) lgkmcnt(5)\n" \
"s_barrier\n" \
"v_mul_f32 v247, v187, v247\n" \
"v_mfma_f32_16x16x32_bf16 v[48:51], a[72:75], v[96:99], v[48:51]\n" \
"ds_read_b128 v[124:127], v29 offset:9216\n" \
"buffer_load_dwordx4 v31, s[36:39], s80 offen offset:3072 sc1 lds\n" \
"v_mfma_f32_16x16x32_bf16 v[52:55], a[76:79], v[96:99], v[52:55]\n" \
"s_add_u32 s80, s67, 16384\n" \
"s_add_u32 m0, s22, 0\n" \
"ds_read_b64_tr_b16 a[144:145], v180 offset:32768\n" \
"v_mfma_f32_16x16x32_bf16 v[56:59], a[72:75], v[100:103], v[56:59]\n" \
"s_waitcnt lgkmcnt(5)\n" \
"ds_read_b128 v[96:99], v29 offset:12288\n" \
"v_cvt_pk_bf16_f32 v240, v240, v241\n" \
"v_mfma_f32_16x16x32_bf16 v[60:63], a[76:79], v[100:103], v[60:63]\n" \
"ds_read_b64_tr_b16 a[146:147], v181 offset:32768\n" \
"v_cvt_pk_bf16_f32 v241, v242, v243\n" \
"v_mfma_f32_16x16x32_bf16 v[64:67], a[72:75], v[104:107], v[64:67]\n" \
"ds_read_b128 v[100:103], v29 offset:13312\n" \
"v_cvt_pk_bf16_f32 v242, v244, v245\n" \
"v_mfma_f32_16x16x32_bf16 v[68:71], a[76:79], v[104:107], v[68:71]\n" \
"ds_read_b64_tr_b16 a[148:149], v182 offset:32768\n" \
"v_cvt_pk_bf16_f32 v243, v246, v247\n" \
"v_mfma_f32_16x16x32_bf16 v[72:75], a[72:75], v[108:111], v[72:75]\n" \
"s_waitcnt lgkmcnt(7)\n" \
"ds_read_b128 v[104:107], v29 offset:6144\n" \
"v_mul_f32 v248, v187, v248\n" \
"v_mfma_f32_16x16x32_bf16 v[76:79], a[76:79], v[108:111], v[76:79]\n" \
"buffer_store_dwordx4 v[240:243], v7, s[36:39], s51 offen offset:1024 sc1\n" \
"v_mfma_f32_16x16x32_bf16 v[32:35], a[80:83], v[112:115], v[32:35]\n" \
"ds_read_b128 v[108:111], v29 offset:7168\n" \
"v_mul_f32 v249, v187, v249\n" \
"v_mfma_f32_16x16x32_bf16 v[36:39], a[84:87], v[112:115], v[36:39]\n" \
"ds_read_b64_tr_b16 a[150:151], v183 offset:32768\n" \
"v_mul_f32 v250, v187, v250\n" \
"v_mfma_f32_16x16x32_bf16 v[40:43], a[80:83], v[116:119], v[40:43]\n" \
"s_waitcnt lgkmcnt(8)\n" \
"ds_read_b128 v[112:115], v29 offset:10240\n" \
"v_mul_f32 v251, v187, v251\n" \
"v_mfma_f32_16x16x32_bf16 v[44:47], a[84:87], v[116:119], v[44:47]\n" \
"ds_read_b64_tr_b16 a[152:153], v180 offset:40960\n" \
"v_mul_f32 v252, v187, v252\n" \
"v_mfma_f32_16x16x32_bf16 v[48:51], a[80:83], v[120:123], v[48:51]\n" \
"ds_read_b128 v[116:119], v29 offset:11264\n" \
"v_mul_f32 v253, v187, v253\n" \
"v_mfma_f32_16x16x32_bf16 v[52:55], a[84:87], v[120:123], v[52:55]\n" \
"ds_read_b64_tr_b16 a[154:155], v181 offset:40960\n" \
"v_mul_f32 v254, v187, v254\n" \
"v_mfma_f32_16x16x32_bf16 v[56:59], a[80:83], v[124:127], v[56:59]\n" \
"ds_read_b128 v[120:123], v29 offset:14336\n" \
"v_mul_f32 v255, v187, v255\n" \
"v_mfma_f32_16x16x32_bf16 v[60:63], a[84:87], v[124:127], v[60:63]\n" \
"s_waitcnt vmcnt(20) lgkmcnt(9)\n" \
"s_barrier\n" \
"ds_read_b64_tr_b16 a[156:157], v182 offset:40960\n" \
"v_mfma_f32_16x16x32_bf16 v[64:67], a[80:83], v[96:99], v[64:67]\n" \
"ds_read_b128 v[124:127], v29 offset:15360\n" \
"v_cvt_pk_bf16_f32 v248, v248, v249\n" \
"v_mfma_f32_16x16x32_bf16 v[68:71], a[84:87], v[96:99], v[68:71]\n" \
"buffer_load_dwordx4 v31, s[36:39], s80 offen offset:0 sc1 lds\n" \
"v_mfma_f32_16x16x32_bf16 v[72:75], a[80:83], v[100:103], v[72:75]\n" \
"s_waitcnt lgkmcnt(8)\n" \
"ds_read_b128 v[96:99], v30 offset:16384\n" \
"v_cvt_pk_bf16_f32 v249, v250, v251\n" \
"v_mfma_f32_16x16x32_bf16 v[76:79], a[84:87], v[100:103], v[76:79]\n" \
"ds_read_b64_tr_b16 a[158:159], v183 offset:40960\n" \
"v_cvt_pk_bf16_f32 v250, v252, v253\n" \
"v_mfma_f32_16x16x32_bf16 v[32:35], a[88:91], v[104:107], v[32:35]\n" \
"ds_read_b128 v[100:103], v30 offset:17408\n" \
"v_cvt_pk_bf16_f32 v251, v254, v255\n" \
"v_mfma_f32_16x16x32_bf16 v[36:39], a[92:95], v[104:107], v[36:39]\n" \
"buffer_store_dwordx4 v[248:251], v7, s[36:39], s51 offen offset:3072 sc1\n" \
"v_mfma_f32_16x16x32_bf16 v[40:43], a[88:91], v[108:111], v[40:43]\n" \
"s_waitcnt lgkmcnt(7)\n" \
"ds_read_b128 v[104:107], v30 offset:20480\n" \
"v_bfe_u32 v188, v4, 7, 1\n" \
"v_mfma_f32_16x16x32_bf16 v[44:47], a[92:95], v[108:111], v[44:47]\n" \
"v_bfe_u32 v189, v4, 4, 3\n" \
"v_lshl_add_u32 v188, v189, 1, v188\n" \
"v_mfma_f32_16x16x32_bf16 v[48:51], a[88:91], v[112:115], v[48:51]\n" \
"ds_read_b128 v[108:111], v30 offset:21504\n" \
"v_lshrrev_b32 v189, 8, v4\n" \
"v_mfma_f32_16x16x32_bf16 v[52:55], a[92:95], v[112:115], v[52:55]\n" \
"v_lshlrev_b32 v184, 11, v188\n" \
"v_and_b32 v188, 1, v189\n" \
"v_mfma_f32_16x16x32_bf16 v[56:59], a[88:91], v[116:119], v[56:59]\n" \
"s_waitcnt lgkmcnt(5)\n" \
"ds_read_b128 v[112:115], v30 offset:24576\n" \
"v_lshl_add_u32 v184, v188, 5, v184\n" \
"v_mfma_f32_16x16x32_bf16 v[60:63], a[92:95], v[116:119], v[60:63]\n" \
"buffer_load_dwordx4 v31, s[36:39], s80 offen offset:1024 sc1 lds\n" \
"v_mfma_f32_16x16x32_bf16 v[64:67], a[88:91], v[120:123], v[64:67]\n" \
"ds_read_b128 v[116:119], v30 offset:25600\n" \
"v_lshrrev_b32 v188, 1, v189\n" \
"v_mfma_f32_16x16x32_bf16 v[68:71], a[92:95], v[120:123], v[68:71]\n" \
"v_lshl_add_u32 v184, v188, 4, v184\n" \
"v_bfe_u32 v188, v4, 7, 1\n" \
"v_mfma_f32_16x16x32_bf16 v[72:75], a[88:91], v[124:127], v[72:75]\n" \
"ds_read_b128 v[120:123], v30 offset:18432\n" \
"v_bfe_u32 v189, v4, 4, 3\n" \
"v_mfma_f32_16x16x32_bf16 v[76:79], a[92:95], v[124:127], v[76:79]\n" \
"s_waitcnt vmcnt(4) lgkmcnt(5)\n" \
"s_barrier\n" \
"v_lshl_add_u32 v188, v189, 1, v188\n" \
"v_mfma_f32_16x16x32_bf16 v[32:35], a[128:131], v[96:99], v[32:35]\n" \
"ds_read_b128 v[124:127], v30 offset:19456\n" \
"buffer_load_dwordx4 v31, s[36:39], s80 offen offset:2048 sc1 lds\n" \
"v_mfma_f32_16x16x32_bf16 v[36:39], a[132:135], v[96:99], v[36:39]\n" \
"buffer_load_dwordx4 v31, s[36:39], s80 offen offset:3072 sc1 lds\n" \
"v_mfma_f32_16x16x32_bf16 v[40:43], a[128:131], v[100:103], v[40:43]\n" \
"s_waitcnt lgkmcnt(4)\n" \
"ds_read_b128 v[96:99], v30 offset:22528\n" \
"s_add_u32 s80, s67, 32768\n" \
"v_mfma_f32_16x16x32_bf16 v[44:47], a[132:135], v[100:103], v[44:47]\n" \
"s_add_u32 m0, s22, 81920\n" \
"v_lshlrev_b32 v185, 3, v188\n" \
"v_accvgpr_read_b32 v144, a96\n" \
"v_mfma_f32_16x16x32_bf16 v[48:51], a[128:131], v[104:107], v[48:51]\n" \
"ds_read_b128 v[100:103], v30 offset:23552\n" \
"v_accvgpr_read_b32 v145, a97\n" \
"v_mfma_f32_16x16x32_bf16 v[52:55], a[132:135], v[104:107], v[52:55]\n" \
"v_accvgpr_read_b32 v146, a98\n" \
"v_accvgpr_read_b32 v147, a99\n" \
"v_mfma_f32_16x16x32_bf16 v[56:59], a[128:131], v[108:111], v[56:59]\n" \
"s_waitcnt lgkmcnt(4)\n" \
"ds_read_b128 v[104:107], v30 offset:26624\n" \
"v_accvgpr_read_b32 v148, a100\n" \
"v_mfma_f32_16x16x32_bf16 v[60:63], a[132:135], v[108:111], v[60:63]\n" \
"buffer_load_dwordx4 v31, s[36:39], s80 offen offset:0 sc1 lds\n" \
"v_mfma_f32_16x16x32_bf16 v[64:67], a[128:131], v[112:115], v[64:67]\n" \
"ds_read_b128 v[108:111], v30 offset:27648\n" \
"v_accvgpr_read_b32 v149, a101\n" \
"v_mfma_f32_16x16x32_bf16 v[68:71], a[132:135], v[112:115], v[68:71]\n" \
"v_accvgpr_read_b32 v150, a102\n" \
"v_accvgpr_read_b32 v151, a103\n" \
"v_mfma_f32_16x16x32_bf16 v[72:75], a[128:131], v[116:119], v[72:75]\n" \
"s_waitcnt lgkmcnt(4)\n" \
"ds_read_b128 v[112:115], v28 offset:49152\n" \
"v_accvgpr_read_b32 v152, a104\n" \
"v_mfma_f32_16x16x32_bf16 v[76:79], a[132:135], v[116:119], v[76:79]\n" \
"buffer_load_dwordx4 v31, s[36:39], s80 offen offset:1024 sc1 lds\n" \
"v_mfma_f32_16x16x32_bf16 v[32:35], a[136:139], v[120:123], v[32:35]\n" \
"ds_read_b128 v[116:119], v28 offset:50176\n" \
"v_accvgpr_read_b32 v153, a105\n" \
"v_mfma_f32_16x16x32_bf16 v[36:39], a[140:143], v[120:123], v[36:39]\n" \
"v_accvgpr_read_b32 v154, a106\n" \
"v_accvgpr_read_b32 v155, a107\n" \
"v_mfma_f32_16x16x32_bf16 v[40:43], a[136:139], v[124:127], v[40:43]\n" \
"ds_read_b128 v[120:123], v28 offset:51200\n" \
"v_accvgpr_read_b32 v156, a108\n" \
"v_mfma_f32_16x16x32_bf16 v[44:47], a[140:143], v[124:127], v[44:47]\n" \
"s_waitcnt lgkmcnt(5)\n" \
"s_barrier\n" \
"v_accvgpr_read_b32 v157, a109\n" \
"v_mfma_f32_16x16x32_bf16 v[48:51], a[136:139], v[96:99], v[48:51]\n" \
"ds_read_b128 v[124:127], v28 offset:52224\n" \
"v_accvgpr_read_b32 v158, a110\n" \
"v_mfma_f32_16x16x32_bf16 v[52:55], a[140:143], v[96:99], v[52:55]\n" \
"buffer_load_dwordx4 v31, s[36:39], s80 offen offset:2048 sc1 lds\n" \
"v_mfma_f32_16x16x32_bf16 v[56:59], a[136:139], v[100:103], v[56:59]\n" \
"s_waitcnt lgkmcnt(4)\n" \
"ds_read_b128 v[96:99], v28 offset:53248\n" \
"v_accvgpr_read_b32 v159, a111\n" \
"v_mfma_f32_16x16x32_bf16 v[60:63], a[140:143], v[100:103], v[60:63]\n" \
"v_accvgpr_read_b32 v128, a112\n" \
"v_accvgpr_read_b32 v129, a113\n" \
"v_mfma_f32_16x16x32_bf16 v[64:67], a[136:139], v[104:107], v[64:67]\n" \
"ds_read_b128 v[100:103], v28 offset:54272\n" \
"v_accvgpr_read_b32 v130, a114\n" \
"v_mfma_f32_16x16x32_bf16 v[68:71], a[140:143], v[104:107], v[68:71]\n" \
"LH22_%=:\ns_cbranch_execz LHR2_%=\n" \
"buffer_load_dwordx4 v31, s[36:39], s80 offen offset:3072 sc1 lds\n" \
"v_mfma_f32_16x16x32_bf16 v[72:75], a[136:139], v[108:111], v[72:75]\n" \
"s_waitcnt lgkmcnt(4)\n" \
"ds_read_b128 v[104:107], v28 offset:55296\n" \
"s_mov_b32 s81, 1310720\n" \
"v_mfma_f32_16x16x32_bf16 v[76:79], a[140:143], v[108:111], v[76:79]\n" \
"s_add_u32 m0, s79, 16384\n" \
"v_accvgpr_read_b32 v131, a115\n" \
"v_accvgpr_read_b32 v132, a116\n" \
"v_mfma_f32_16x16x32_bf16 v[80:83], a[160:163], v[112:115], 0\n" \
"ds_read_b128 v[108:111], v28 offset:56320\n" \
"v_accvgpr_read_b32 v133, a117\n" \
"v_mfma_f32_16x16x32_bf16 v[84:87], a[164:167], v[112:115], 0\n" \
"buffer_load_dwordx4 v12, s[28:31], s81 offen offset:0 lds\n" \
"v_mfma_f32_16x16x32_bf16 v[88:91], a[160:163], v[116:119], 0\n" \
"s_waitcnt lgkmcnt(4)\n" \
"ds_read_b128 v[112:115], v28 offset:57344\n" \
"s_add_u32 m0, s79, 17280\n" \
"v_mfma_f32_16x16x32_bf16 v[92:95], a[164:167], v[116:119], 0\n" \
"v_accvgpr_read_b32 v134, a118\n" \
"v_accvgpr_read_b32 v135, a119\n" \
"v_mfma_f32_16x16x32_bf16 v[80:83], a[168:171], v[120:123], v[80:83]\n" \
"ds_read_b128 v[116:119], v28 offset:58368\n" \
"v_accvgpr_read_b32 v136, a120\n" \
"v_mfma_f32_16x16x32_bf16 v[84:87], a[172:175], v[120:123], v[84:87]\n" \
"buffer_load_dwordx4 v12, s[28:31], s81 offen offset:128 lds\n" \
"v_mfma_f32_16x16x32_bf16 v[88:91], a[168:171], v[124:127], v[88:91]\n" \
"s_mov_b32 s81, 1376256\n" \
"ds_read_b128 v[120:123], v28 offset:59392\n" \
"s_add_u32 m0, s79, 24576\n" \
"v_mfma_f32_16x16x32_bf16 v[92:95], a[172:175], v[124:127], v[92:95]\n" \
"s_waitcnt vmcnt(6) lgkmcnt(5)\n" \
"s_barrier\n" \
"v_accvgpr_read_b32 v137, a121\n" \
"v_mfma_f32_16x16x32_bf16 v[80:83], a[176:179], v[96:99], v[80:83]\n" \
"ds_read_b128 v[124:127], v28 offset:60416\n" \
"v_accvgpr_read_b32 v138, a122\n" \
"v_mfma_f32_16x16x32_bf16 v[84:87], a[180:183], v[96:99], v[84:87]\n" \
"buffer_load_dwordx4 v12, s[28:31], s81 offen offset:0 lds\n" \
"v_mfma_f32_16x16x32_bf16 v[88:91], a[176:179], v[100:103], v[88:91]\n" \
"s_waitcnt lgkmcnt(4)\n" \
"ds_read_b128 v[96:99], v28 offset:61440\n" \
"s_add_u32 m0, s79, 25472\n" \
"v_mfma_f32_16x16x32_bf16 v[92:95], a[180:183], v[100:103], v[92:95]\n" \
"v_accvgpr_read_b32 v139, a123\n" \
"v_accvgpr_read_b32 v140, a124\n" \
"v_mfma_f32_16x16x32_bf16 v[80:83], a[184:187], v[104:107], v[80:83]\n" \
"ds_read_b128 v[100:103], v28 offset:62464\n" \
"v_accvgpr_read_b32 v141, a125\n" \
"v_mfma_f32_16x16x32_bf16 v[84:87], a[188:191], v[104:107], v[84:87]\n" \
"buffer_load_dwordx4 v12, s[28:31], s81 offen offset:128 lds\n" \
"v_mfma_f32_16x16x32_bf16 v[88:91], a[184:187], v[108:111], v[88:91]\n" \
"s_waitcnt lgkmcnt(4)\n" \
"ds_read_b128 v[104:107], v28 offset:63488\n" \
"s_add_u32 s80, s66, 32768\n" \
"v_mfma_f32_16x16x32_bf16 v[92:95], a[188:191], v[108:111], v[92:95]\n" \
"s_add_u32 m0, s22, 131072\n" \
"v_accvgpr_read_b32 v142, a126\n" \
"v_accvgpr_read_b32 v143, a127\n" \
"v_mfma_f32_16x16x32_bf16 v[80:83], a[192:195], v[112:115], v[80:83]\n" \
"ds_read_b128 v[108:111], v28 offset:64512\n" \
"v_mfma_f32_16x16x32_bf16 v[84:87], a[196:199], v[112:115], v[84:87]\n" \
"buffer_load_dwordx4 v4, s[36:39], s80 offen offset:0 sc1 lds\n" \
"v_mfma_f32_16x16x32_bf16 v[88:91], a[192:195], v[116:119], v[88:91]\n" \
"s_waitcnt lgkmcnt(4)\n" \
"ds_read_b128 v[112:115], v28 offset:0\n" \
"v_mfma_f32_16x16x32_bf16 v[92:95], a[196:199], v[116:119], v[92:95]\n" \
"v_mfma_f32_16x16x32_bf16 v[80:83], a[200:203], v[120:123], v[80:83]\n" \
"ds_read_b128 v[116:119], v28 offset:1024\n" \
"v_mfma_f32_16x16x32_bf16 v[84:87], a[204:207], v[120:123], v[84:87]\n" \
"buffer_load_dwordx4 v4, s[36:39], s80 offen offset:1024 sc1 lds\n" \
"v_mfma_f32_16x16x32_bf16 v[88:91], a[200:203], v[124:127], v[88:91]\n" \
"ds_read_b128 v[120:123], v28 offset:2048\n" \
"v_mfma_f32_16x16x32_bf16 v[92:95], a[204:207], v[124:127], v[92:95]\n" \
"s_waitcnt lgkmcnt(5)\n" \
"s_barrier\n" \
"v_mfma_f32_16x16x32_bf16 v[80:83], a[208:211], v[96:99], v[80:83]\n" \
"ds_read_b128 v[124:127], v28 offset:3072\n" \
"v_mfma_f32_16x16x32_bf16 v[84:87], a[212:215], v[96:99], v[84:87]\n" \
"buffer_load_dwordx4 v4, s[36:39], s80 offen offset:2048 sc1 lds\n" \
"v_mfma_f32_16x16x32_bf16 v[88:91], a[208:211], v[100:103], v[88:91]\n" \
"s_waitcnt lgkmcnt(4)\n" \
"ds_read_b128 v[96:99], v28 offset:4096\n" \
"v_mfma_f32_16x16x32_bf16 v[92:95], a[212:215], v[100:103], v[92:95]\n" \
"v_mfma_f32_16x16x32_bf16 v[80:83], a[216:219], v[104:107], v[80:83]\n" \
"ds_read_b128 v[100:103], v28 offset:5120\n" \
"v_mfma_f32_16x16x32_bf16 v[84:87], a[220:223], v[104:107], v[84:87]\n" \
"buffer_load_dwordx4 v4, s[36:39], s80 offen offset:3072 sc1 lds\n" \
"v_mfma_f32_16x16x32_bf16 v[88:91], a[216:219], v[108:111], v[88:91]\n" \
"s_waitcnt lgkmcnt(4)\n" \
"ds_read_b128 v[104:107], v28 offset:6144\n" \
"s_mov_b32 s81, 1441792\n" \
"v_mfma_f32_16x16x32_bf16 v[92:95], a[220:223], v[108:111], v[92:95]\n" \
"s_add_u32 m0, s79, 32768\n" \
"v_mfma_f32_16x16x32_bf16 v[80:83], a[224:227], v[112:115], v[80:83]\n" \
"ds_read_b128 v[108:111], v28 offset:7168\n" \
"v_mfma_f32_16x16x32_bf16 v[84:87], a[228:231], v[112:115], v[84:87]\n" \
"buffer_load_dwordx4 v12, s[28:31], s81 offen offset:0 lds\n" \
"v_mfma_f32_16x16x32_bf16 v[88:91], a[224:227], v[116:119], v[88:91]\n" \
"s_waitcnt lgkmcnt(4)\n" \
"ds_read_b128 v[112:115], v28 offset:8192\n" \
"s_add_u32 m0, s79, 33664\n" \
"v_mfma_f32_16x16x32_bf16 v[92:95], a[228:231], v[116:119], v[92:95]\n" \
"s_waitcnt vmcnt(16)\n" \
"s_cmp_lg_u64 s[4:5], 0\ns_cbranch_scc1 LFBB_%=\nLFBBR_%=:\n" \
"v_mfma_f32_16x16x32_bf16 v[80:83], a[232:235], v[120:123], v[80:83]\n" \
"ds_read_b128 v[116:119], v28 offset:9216\n" \
"v_mfma_f32_16x16x32_bf16 v[84:87], a[236:239], v[120:123], v[84:87]\n" \
"buffer_store_dwordx2 v[10:11], v20, s[36:39], s53 offen sc1\n" \
"v_mfma_f32_16x16x32_bf16 v[88:91], a[232:235], v[124:127], v[88:91]\n" \
"ds_read_b128 v[120:123], v28 offset:10240\n" \
"v_mfma_f32_16x16x32_bf16 v[92:95], a[236:239], v[124:127], v[92:95]\n" \
"s_waitcnt vmcnt(10) lgkmcnt(5)\n" \
"s_barrier\n" \
"v_mfma_f32_16x16x32_bf16 v[80:83], a[240:243], v[96:99], v[80:83]\n" \
"ds_read_b128 v[124:127], v28 offset:11264\n" \
"v_mfma_f32_16x16x32_bf16 v[84:87], a[244:247], v[96:99], v[84:87]\n" \
"buffer_load_dwordx4 v12, s[28:31], s81 offen offset:128 lds\n" \
"v_mfma_f32_16x16x32_bf16 v[88:91], a[240:243], v[100:103], v[88:91]\n" \
"s_waitcnt lgkmcnt(4)\n" \
"ds_read_b128 v[96:99], v28 offset:12288\n" \
"s_mov_b32 s81, 1507328\n" \
"v_mfma_f32_16x16x32_bf16 v[92:95], a[244:247], v[100:103], v[92:95]\n" \
"s_add_u32 m0, s79, 40960\n" \
"v_mfma_f32_16x16x32_bf16 v[80:83], a[248:251], v[104:107], v[80:83]\n" \
"ds_read_b128 v[100:103], v28 offset:13312\n" \
"v_mfma_f32_16x16x32_bf16 v[84:87], a[252:255], v[104:107], v[84:87]\n" \
"buffer_load_dwordx4 v12, s[28:31], s81 offen offset:0 lds\n" \
"v_mfma_f32_16x16x32_bf16 v[88:91], a[248:251], v[108:111], v[88:91]\n" \
"s_waitcnt lgkmcnt(4)\n" \
"ds_read_b128 v[104:107], v28 offset:14336\n" \
"s_add_u32 m0, s79, 41856\n" \
"v_mfma_f32_16x16x32_bf16 v[92:95], a[252:255], v[108:111], v[92:95]\n" \
"v_mfma_f32_16x16x32_bf16 v[80:83], a[64:67], v[112:115], v[80:83]\n" \
"ds_read_b128 v[108:111], v28 offset:15360\n" \
"v_mfma_f32_16x16x32_bf16 v[84:87], a[68:71], v[112:115], v[84:87]\n" \
"buffer_load_dwordx4 v12, s[28:31], s81 offen offset:128 lds\n" \
"v_mfma_f32_16x16x32_bf16 v[88:91], a[64:67], v[116:119], v[88:91]\n" \
"s_waitcnt lgkmcnt(4)\n" \
"ds_read_b128 v[112:115], v29 offset:16384\n" \
"s_mov_b32 s81, 1572864\n" \
"v_mfma_f32_16x16x32_bf16 v[92:95], a[68:71], v[116:119], v[92:95]\n" \
"s_add_u32 m0, s79, 65536\n" \
"v_mfma_f32_16x16x32_bf16 v[80:83], a[72:75], v[120:123], v[80:83]\n" \
"ds_read_b128 v[116:119], v29 offset:17408\n" \
"v_mfma_f32_16x16x32_bf16 v[84:87], a[76:79], v[120:123], v[84:87]\n" \
"buffer_load_dwordx4 v12, s[28:31], s81 offen offset:0 lds\n" \
"v_mfma_f32_16x16x32_bf16 v[88:91], a[72:75], v[124:127], v[88:91]\n" \
"s_add_u32 m0, s79, 66432\n" \
"ds_read_b128 v[120:123], v29 offset:18432\n" \
"v_mfma_f32_16x16x32_bf16 v[92:95], a[76:79], v[124:127], v[92:95]\n" \
"s_waitcnt vmcnt(30) lgkmcnt(5)\n" \
"s_barrier\n" \
"v_mfma_f32_16x16x32_bf16 v[80:83], a[80:83], v[96:99], v[80:83]\n" \
"ds_read_b128 v[124:127], v29 offset:19456\n" \
"v_mfma_f32_16x16x32_bf16 v[84:87], a[84:87], v[96:99], v[84:87]\n" \
"buffer_load_dwordx4 v12, s[28:31], s81 offen offset:128 lds\n" \
"v_mfma_f32_16x16x32_bf16 v[88:91], a[80:83], v[100:103], v[88:91]\n" \
"s_waitcnt lgkmcnt(4)\n" \
"ds_read_b128 v[96:99], v30 offset:28672\n" \
"s_mov_b32 s81, 1638400\n" \
"v_mfma_f32_16x16x32_bf16 v[92:95], a[84:87], v[100:103], v[92:95]\n" \
"s_add_u32 m0, s79, 73728\n" \
"v_mfma_f32_16x16x32_bf16 v[80:83], a[88:91], v[104:107], v[80:83]\n" \
"ds_read_b128 v[100:103], v30 offset:29696\n" \
"v_mfma_f32_16x16x32_bf16 v[84:87], a[92:95], v[104:107], v[84:87]\n" \
"buffer_load_dwordx4 v12, s[28:31], s81 offen offset:0 lds\n" \
"v_mfma_f32_16x16x32_bf16 v[88:91], a[88:91], v[108:111], v[88:91]\n" \
"s_waitcnt lgkmcnt(4)\n" \
"ds_read_b128 v[104:107], v29 offset:49152\n" \
"s_add_u32 m0, s79, 74624\n" \
"v_mfma_f32_16x16x32_bf16 v[92:95], a[92:95], v[108:111], v[92:95]\n" \
"v_mfma_f32_16x16x32_bf16 v[80:83], a[128:131], v[112:115], v[80:83]\n" \
"ds_read_b128 v[108:111], v29 offset:50176\n" \
"v_mfma_f32_16x16x32_bf16 v[84:87], a[132:135], v[112:115], v[84:87]\n" \
"buffer_load_dwordx4 v12, s[28:31], s81 offen offset:128 lds\n" \
"v_mfma_f32_16x16x32_bf16 v[88:91], a[128:131], v[116:119], v[88:91]\n" \
"s_waitcnt lgkmcnt(4)\n" \
"ds_read_b128 v[112:115], v29 offset:53248\n" \
"s_add_u32 s80, s67, 49152\n" \
"v_mfma_f32_16x16x32_bf16 v[92:95], a[132:135], v[116:119], v[92:95]\n" \
"s_add_u32 m0, s22, 98304\n" \
"v_mfma_f32_16x16x32_bf16 v[80:83], a[136:139], v[120:123], v[80:83]\n" \
"ds_read_b128 v[116:119], v29 offset:54272\n" \
"v_mfma_f32_16x16x32_bf16 v[84:87], a[140:143], v[120:123], v[84:87]\n" \
"buffer_load_dwordx4 v31, s[36:39], s80 offen offset:0 sc1 lds\n" \
"v_mfma_f32_16x16x32_bf16 v[88:91], a[136:139], v[124:127], v[88:91]\n" \
"ds_read_b128 v[120:123], v29 offset:20480\n" \
"v_mfma_f32_16x16x32_bf16 v[92:95], a[140:143], v[124:127], v[92:95]\n" \
"s_waitcnt vmcnt(14) lgkmcnt(5)\n" \
"s_barrier\n" \
"v_mfma_f32_16x16x32_bf16 v[32:35], a[144:147], v[96:99], v[32:35]\n" \
"ds_read_b128 v[124:127], v29 offset:21504\n" \
"v_mfma_f32_16x16x32_bf16 v[36:39], a[148:151], v[96:99], v[36:39]\n" \
"buffer_load_dwordx4 v31, s[36:39], s80 offen offset:1024 sc1 lds\n" \
"v_mfma_f32_16x16x32_bf16 v[40:43], a[144:147], v[100:103], v[40:43]\n" \
"s_waitcnt lgkmcnt(4)\n" \
"ds_read_b128 v[96:99], v30 offset:30720\n" \
"v_mfma_f32_16x16x32_bf16 v[44:47], a[148:151], v[100:103], v[44:47]\n" \
"ds_read_b64_tr_b16 a[0:1], v176 offset:16384\n" \
"v_mfma_f32_16x16x32_bf16 v[48:51], a[144:147], v[104:107], v[48:51]\n" \
"ds_read_b128 v[100:103], v30 offset:31744\n" \
"v_mfma_f32_16x16x32_bf16 v[52:55], a[148:151], v[104:107], v[52:55]\n" \
"buffer_load_dwordx4 v31, s[36:39], s80 offen offset:2048 sc1 lds\n" \
"v_mfma_f32_16x16x32_bf16 v[56:59], a[144:147], v[108:111], v[56:59]\n" \
"s_waitcnt lgkmcnt(5)\n" \
"ds_read_b128 v[104:107], v29 offset:51200\n" \
"v_mfma_f32_16x16x32_bf16 v[60:63], a[148:151], v[108:111], v[60:63]\n" \
"ds_read_b64_tr_b16 a[2:3], v177 offset:16384\n" \
"v_mfma_f32_16x16x32_bf16 v[64:67], a[144:147], v[112:115], v[64:67]\n" \
"ds_read_b128 v[108:111], v29 offset:52224\n" \
"v_mfma_f32_16x16x32_bf16 v[68:71], a[148:151], v[112:115], v[68:71]\n" \
"buffer_load_dwordx4 v31, s[36:39], s80 offen offset:3072 sc1 lds\n" \
"v_mfma_f32_16x16x32_bf16 v[72:75], a[144:147], v[116:119], v[72:75]\n" \
"s_waitcnt lgkmcnt(6)\n" \
"ds_read_b128 v[112:115], v29 offset:55296\n" \
"s_add_u32 s80, s64, 49152\n" \
"v_mfma_f32_16x16x32_bf16 v[76:79], a[148:151], v[116:119], v[76:79]\n" \
"s_add_u32 m0, s22, 49152\n" \
"ds_read_b64_tr_b16 a[4:5], v178 offset:16384\n" \
"v_mfma_f32_16x16x32_bf16 v[80:83], a[144:147], v[120:123], v[80:83]\n" \
"ds_read_b128 v[116:119], v29 offset:56320\n" \
"v_mfma_f32_16x16x32_bf16 v[84:87], a[148:151], v[120:123], v[84:87]\n" \
"buffer_load_dwordx4 v4, s[36:39], s80 offen offset:0 sc1 lds\n" \
"v_mfma_f32_16x16x32_bf16 v[88:91], a[144:147], v[124:127], v[88:91]\n" \
"ds_read_b128 v[120:123], v29 offset:22528\n" \
"v_mfma_f32_16x16x32_bf16 v[92:95], a[148:151], v[124:127], v[92:95]\n" \
"s_waitcnt vmcnt(14) lgkmcnt(7)\n" \
"s_barrier\n" \
"ds_read_b64_tr_b16 a[6:7], v179 offset:16384\n" \
"v_mfma_f32_16x16x32_bf16 v[32:35], a[152:155], v[96:99], v[32:35]\n" \
"ds_read_b128 v[124:127], v29 offset:23552\n" \
"v_mfma_f32_16x16x32_bf16 v[36:39], a[156:159], v[96:99], v[36:39]\n" \
"buffer_load_dwordx4 v4, s[36:39], s80 offen offset:1024 sc1 lds\n" \
"v_mfma_f32_16x16x32_bf16 v[40:43], a[152:155], v[100:103], v[40:43]\n" \
"s_waitcnt lgkmcnt(6)\n" \
"ds_read_b128 v[96:99], v29 offset:57344\n" \
"v_mfma_f32_16x16x32_bf16 v[44:47], a[156:159], v[100:103], v[44:47]\n" \
"ds_read_b64_tr_b16 a[8:9], v176 offset:24576\n" \
"v_mfma_f32_16x16x32_bf16 v[48:51], a[152:155], v[104:107], v[48:51]\n" \
"ds_read_b128 v[100:103], v29 offset:58368\n" \
"v_mfma_f32_16x16x32_bf16 v[52:55], a[156:159], v[104:107], v[52:55]\n" \
"buffer_load_dwordx4 v4, s[36:39], s80 offen offset:2048 sc1 lds\n" \
"v_mfma_f32_16x16x32_bf16 v[56:59], a[152:155], v[108:111], v[56:59]\n" \
"s_waitcnt lgkmcnt(6)\n" \
"ds_read_b128 v[104:107], v29 offset:61440\n" \
"v_mfma_f32_16x16x32_bf16 v[60:63], a[156:159], v[108:111], v[60:63]\n" \
"ds_read_b64_tr_b16 a[10:11], v177 offset:24576\n" \
"v_mfma_f32_16x16x32_bf16 v[64:67], a[152:155], v[112:115], v[64:67]\n" \
"ds_read_b128 v[108:111], v29 offset:62464\n" \
"v_mfma_f32_16x16x32_bf16 v[68:71], a[156:159], v[112:115], v[68:71]\n" \
"buffer_load_dwordx4 v4, s[36:39], s80 offen offset:3072 sc1 lds\n" \
"v_mfma_f32_16x16x32_bf16 v[72:75], a[152:155], v[116:119], v[72:75]\n" \
"s_waitcnt lgkmcnt(6)\n" \
"ds_read_b128 v[112:115], v30 offset:0\n" \
"s_mov_b32 s81, 1703936\n" \
"ds_read_b64_tr_b16 a[12:13], v178 offset:24576\n" \
"ds_read_b64_tr_b16 a[14:15], v179 offset:24576\n" \
"v_mfma_f32_16x16x32_bf16 v[76:79], a[156:159], v[116:119], v[76:79]\n" \
"s_add_u32 m0, s79, 0\n" \
"v_mfma_f32_16x16x32_bf16 v[80:83], a[152:155], v[120:123], v[80:83]\n" \
"ds_read_b128 v[116:119], v30 offset:1024\n" \
"v_mfma_f32_16x16x32_bf16 v[84:87], a[156:159], v[120:123], v[84:87]\n" \
"buffer_load_dwordx4 v12, s[28:31], s81 offen offset:0 lds\n" \
"v_mfma_f32_16x16x32_bf16 v[88:91], a[152:155], v[124:127], v[88:91]\n" \
"s_add_u32 m0, s79, 896\n" \
"ds_read_b128 v[120:123], v29 offset:24576\n" \
"v_mfma_f32_16x16x32_bf16 v[92:95], a[156:159], v[124:127], v[92:95]\n" \
"s_waitcnt vmcnt(13) lgkmcnt(2)\n" \
"s_barrier\n" \
"v_mfma_f32_16x16x32_bf16 v[32:35], a[0:3], v[96:99], v[32:35]\n" \
"ds_read_b128 v[124:127], v29 offset:25600\n" \
"v_mfma_f32_16x16x32_bf16 v[36:39], a[4:7], v[96:99], v[36:39]\n" \
"buffer_load_dwordx4 v12, s[28:31], s81 offen offset:128 lds\n" \
"v_mfma_f32_16x16x32_bf16 v[40:43], a[0:3], v[100:103], v[40:43]\n" \
"s_waitcnt lgkmcnt(6)\n" \
"ds_read_b128 v[96:99], v29 offset:59392\n" \
"s_mov_b32 s81, 1769472\n" \
"v_mfma_f32_16x16x32_bf16 v[44:47], a[4:7], v[100:103], v[44:47]\n" \
"s_add_u32 m0, s79, 8192\n" \
"ds_read_b64_tr_b16 a[16:17], v176 offset:32768\n" \
"v_mfma_f32_16x16x32_bf16 v[48:51], a[0:3], v[104:107], v[48:51]\n" \
"ds_read_b128 v[100:103], v29 offset:60416\n" \
"v_mfma_f32_16x16x32_bf16 v[52:55], a[4:7], v[104:107], v[52:55]\n" \
"buffer_load_dwordx4 v12, s[28:31], s81 offen offset:0 lds\n" \
"v_mfma_f32_16x16x32_bf16 v[56:59], a[0:3], v[108:111], v[56:59]\n" \
"s_waitcnt lgkmcnt(5)\n" \
"ds_read_b128 v[104:107], v29 offset:63488\n" \
"s_add_u32 m0, s79, 9088\n" \
"v_mfma_f32_16x16x32_bf16 v[60:63], a[4:7], v[108:111], v[60:63]\n" \
"ds_read_b64_tr_b16 a[18:19], v177 offset:32768\n" \
"v_mfma_f32_16x16x32_bf16 v[64:67], a[0:3], v[112:115], v[64:67]\n" \
"ds_read_b128 v[108:111], v29 offset:64512\n" \
"v_mfma_f32_16x16x32_bf16 v[68:71], a[4:7], v[112:115], v[68:71]\n" \
"buffer_load_dwordx4 v12, s[28:31], s81 offen offset:128 lds\n" \
"v_mfma_f32_16x16x32_bf16 v[72:75], a[0:3], v[116:119], v[72:75]\n" \
"s_waitcnt lgkmcnt(6)\n" \
"ds_read_b128 v[112:115], v30 offset:2048\n" \
"s_add_u32 s80, s65, 49152\n" \
"v_mfma_f32_16x16x32_bf16 v[76:79], a[4:7], v[116:119], v[76:79]\n" \
"s_add_u32 m0, s22, 147456\n" \
"ds_read_b64_tr_b16 a[20:21], v178 offset:32768\n" \
"v_mfma_f32_16x16x32_bf16 v[80:83], a[0:3], v[120:123], v[80:83]\n" \
"ds_read_b128 v[116:119], v30 offset:3072\n" \
"v_mfma_f32_16x16x32_bf16 v[84:87], a[4:7], v[120:123], v[84:87]\n" \
"buffer_load_dwordx4 v4, s[36:39], s80 offen offset:0 sc1 lds\n" \
"v_mfma_f32_16x16x32_bf16 v[88:91], a[0:3], v[124:127], v[88:91]\n" \
"ds_read_b128 v[120:123], v29 offset:26624\n" \
"v_mfma_f32_16x16x32_bf16 v[92:95], a[4:7], v[124:127], v[92:95]\n" \
"s_waitcnt lgkmcnt(7)\n" \
"s_barrier\n" \
"ds_read_b64_tr_b16 a[22:23], v179 offset:32768\n" \
"v_mfma_f32_16x16x32_bf16 v[32:35], a[8:11], v[96:99], v[32:35]\n" \
"ds_read_b128 v[124:127], v29 offset:27648\n" \
"v_mfma_f32_16x16x32_bf16 v[36:39], a[12:15], v[96:99], v[36:39]\n" \
"buffer_load_dwordx4 v4, s[36:39], s80 offen offset:1024 sc1 lds\n" \
"v_mfma_f32_16x16x32_bf16 v[40:43], a[8:11], v[100:103], v[40:43]\n" \
"s_waitcnt lgkmcnt(6)\n" \
"ds_read_b128 v[96:99], v30 offset:4096\n" \
"v_mfma_f32_16x16x32_bf16 v[44:47], a[12:15], v[100:103], v[44:47]\n" \
"ds_read_b64_tr_b16 a[24:25], v176 offset:40960\n" \
"v_mfma_f32_16x16x32_bf16 v[48:51], a[8:11], v[104:107], v[48:51]\n" \
"ds_read_b128 v[100:103], v30 offset:5120\n" \
"v_mfma_f32_16x16x32_bf16 v[52:55], a[12:15], v[104:107], v[52:55]\n" \
"buffer_load_dwordx4 v4, s[36:39], s80 offen offset:2048 sc1 lds\n" \
"v_mfma_f32_16x16x32_bf16 v[56:59], a[8:11], v[108:111], v[56:59]\n" \
"s_waitcnt lgkmcnt(6)\n" \
"ds_read_b128 v[104:107], v30 offset:8192\n" \
"v_mfma_f32_16x16x32_bf16 v[60:63], a[12:15], v[108:111], v[60:63]\n" \
"ds_read_b64_tr_b16 a[26:27], v177 offset:40960\n" \
"v_mfma_f32_16x16x32_bf16 v[64:67], a[8:11], v[112:115], v[64:67]\n" \
"ds_read_b128 v[108:111], v30 offset:9216\n" \
"v_mfma_f32_16x16x32_bf16 v[68:71], a[12:15], v[112:115], v[68:71]\n" \
"buffer_load_dwordx4 v4, s[36:39], s80 offen offset:3072 sc1 lds\n" \
"v_mfma_f32_16x16x32_bf16 v[72:75], a[8:11], v[116:119], v[72:75]\n" \
"s_waitcnt lgkmcnt(6)\n" \
"ds_read_b128 v[112:115], v30 offset:12288\n" \
"v_bfe_u32 v186, v4, 7, 2\n" \
"ds_read_b64_tr_b16 a[28:29], v178 offset:40960\n" \
"ds_read_b64_tr_b16 a[30:31], v179 offset:40960\n" \
"v_mfma_f32_16x16x32_bf16 v[76:79], a[12:15], v[116:119], v[76:79]\n" \
"s_mov_b32 s81, 1835008\n" \
"v_min_u32 v186, 3, v186\n" \
"v_lshl_add_u32 v17, v186, 8, s11\n" \
"v_mfma_f32_16x16x32_bf16 v[80:83], a[8:11], v[120:123], v[80:83]\n" \
"s_add_u32 m0, s79, 16384\n" \
"ds_read_b128 v[116:119], v30 offset:13312\n" \
"v_bfe_u32 v186, v4, 4, 3\n" \
"v_mfma_f32_16x16x32_bf16 v[84:87], a[12:15], v[120:123], v[84:87]\n" \
"buffer_load_dwordx4 v12, s[28:31], s81 offen offset:0 lds\n" \
"v_mfma_f32_16x16x32_bf16 v[88:91], a[8:11], v[124:127], v[88:91]\n" \
"s_add_u32 m0, s79, 17280\n" \
"ds_read_b128 v[120:123], v29 offset:28672\n" \
"v_lshl_add_u32 v17, v186, 3, v17\n" \
"v_mfma_f32_16x16x32_bf16 v[92:95], a[12:15], v[124:127], v[92:95]\n" \
"s_waitcnt vmcnt(17) lgkmcnt(2)\n" \
"s_barrier\n" \
"v_mfma_f32_16x16x32_bf16 v[32:35], a[16:19], v[96:99], v[32:35]\n" \
"ds_read_b128 v[124:127], v29 offset:29696\n" \
"v_mfma_f32_16x16x32_bf16 v[36:39], a[20:23], v[96:99], v[36:39]\n" \
"buffer_load_dwordx4 v12, s[28:31], s81 offen offset:128 lds\n" \
"v_mfma_f32_16x16x32_bf16 v[40:43], a[16:19], v[100:103], v[40:43]\n" \
"s_waitcnt lgkmcnt(6)\n" \
"ds_read_b128 v[96:99], v30 offset:6144\n" \
"s_mov_b32 s81, 1900544\n" \
"v_mfma_f32_16x16x32_bf16 v[44:47], a[20:23], v[100:103], v[44:47]\n" \
"s_add_u32 m0, s79, 24576\n" \
"ds_read_b64_tr_b16 a[32:33], v180 offset:0\n" \
"v_mfma_f32_16x16x32_bf16 v[48:51], a[16:19], v[104:107], v[48:51]\n" \
"ds_read_b128 v[100:103], v30 offset:7168\n" \
"v_mfma_f32_16x16x32_bf16 v[52:55], a[20:23], v[104:107], v[52:55]\n" \
"buffer_load_dwordx4 v12, s[28:31], s81 offen offset:0 lds\n" \
"v_mfma_f32_16x16x32_bf16 v[56:59], a[16:19], v[108:111], v[56:59]\n" \
"s_waitcnt lgkmcnt(5)\n" \
"ds_read_b128 v[104:107], v30 offset:10240\n" \
"s_add_u32 m0, s79, 25472\n" \
"v_mfma_f32_16x16x32_bf16 v[60:63], a[20:23], v[108:111], v[60:63]\n" \
"ds_read_b64_tr_b16 a[34:35], v181 offset:0\n" \
"v_mfma_f32_16x16x32_bf16 v[64:67], a[16:19], v[112:115], v[64:67]\n" \
"ds_read_b128 v[108:111], v30 offset:11264\n" \
"v_mfma_f32_16x16x32_bf16 v[68:71], a[20:23], v[112:115], v[68:71]\n" \
"buffer_load_dwordx2 v[18:19], v17, s[36:39], 0 offen sc1\n" \
"v_mfma_f32_16x16x32_bf16 v[72:75], a[16:19], v[116:119], v[72:75]\n" \
"s_waitcnt lgkmcnt(6)\n" \
"ds_read_b128 v[112:115], v30 offset:14336\n" \
"v_mfma_f32_16x16x32_bf16 v[76:79], a[20:23], v[116:119], v[76:79]\n" \
"ds_read_b64_tr_b16 a[36:37], v182 offset:0\n" \
"v_mfma_f32_16x16x32_bf16 v[80:83], a[16:19], v[120:123], v[80:83]\n" \
"ds_read_b128 v[116:119], v30 offset:15360\n" \
"v_mfma_f32_16x16x32_bf16 v[84:87], a[20:23], v[120:123], v[84:87]\n" \
"buffer_load_dwordx4 v12, s[28:31], s81 offen offset:128 lds\n" \
"v_mfma_f32_16x16x32_bf16 v[88:91], a[16:19], v[124:127], v[88:91]\n" \
"s_add_u32 s80, s66, 49152\n" \
"ds_read_b128 v[120:123], v29 offset:30720\n" \
"s_add_u32 m0, s22, 32768\n" \
"v_mfma_f32_16x16x32_bf16 v[92:95], a[20:23], v[124:127], v[92:95]\n" \
"s_waitcnt vmcnt(13) lgkmcnt(7)\n" \
"s_barrier\n" \
"ds_read_b64_tr_b16 a[38:39], v183 offset:0\n" \
"v_mfma_f32_16x16x32_bf16 v[32:35], a[24:27], v[96:99], v[32:35]\n" \
"ds_read_b128 v[124:127], v29 offset:31744\n" \
"v_mfma_f32_16x16x32_bf16 v[36:39], a[28:31], v[96:99], v[36:39]\n" \
"buffer_load_dwordx4 v4, s[36:39], s80 offen offset:0 sc1 lds\n" \
"v_mfma_f32_16x16x32_bf16 v[40:43], a[24:27], v[100:103], v[40:43]\n" \
"s_waitcnt lgkmcnt(6)\n" \
"ds_read_b128 v[96:99], v28 offset:49152\n" \
"v_mfma_f32_16x16x32_bf16 v[44:47], a[28:31], v[100:103], v[44:47]\n" \
"ds_read_b64_tr_b16 a[40:41], v180 offset:8192\n" \
"v_mfma_f32_16x16x32_bf16 v[48:51], a[24:27], v[104:107], v[48:51]\n" \
"ds_read_b128 v[100:103], v28 offset:50176\n" \
"v_mfma_f32_16x16x32_bf16 v[52:55], a[28:31], v[104:107], v[52:55]\n" \
"buffer_load_dwordx4 v4, s[36:39], s80 offen offset:1024 sc1 lds\n" \
"v_mfma_f32_16x16x32_bf16 v[56:59], a[24:27], v[108:111], v[56:59]\n" \
"s_waitcnt lgkmcnt(6)\n" \
"ds_read_b128 v[104:107], v28 offset:53248\n" \
"v_mfma_f32_16x16x32_bf16 v[60:63], a[28:31], v[108:111], v[60:63]\n" \
"ds_read_b64_tr_b16 a[42:43], v181 offset:8192\n" \
"v_mfma_f32_16x16x32_bf16 v[64:67], a[24:27], v[112:115], v[64:67]\n" \
"ds_read_b128 v[108:111], v28 offset:54272\n" \
"v_mfma_f32_16x16x32_bf16 v[68:71], a[28:31], v[112:115], v[68:71]\n" \
"buffer_load_dwordx4 v4, s[36:39], s80 offen offset:2048 sc1 lds\n" \
"v_mfma_f32_16x16x32_bf16 v[72:75], a[24:27], v[116:119], v[72:75]\n" \
"s_waitcnt lgkmcnt(6)\n" \
"ds_read_b128 v[112:115], v28 offset:57344\n" \
"ds_read_b64_tr_b16 a[44:45], v182 offset:8192\n" \
"ds_read_b64_tr_b16 a[46:47], v183 offset:8192\n" \
"v_mfma_f32_16x16x32_bf16 v[76:79], a[28:31], v[116:119], v[76:79]\n" \
"v_mfma_f32_16x16x32_bf16 v[80:83], a[24:27], v[120:123], v[80:83]\n" \
"ds_read_b128 v[116:119], v28 offset:58368\n" \
"v_mfma_f32_16x16x32_bf16 v[84:87], a[28:31], v[120:123], v[84:87]\n" \
"buffer_load_dwordx4 v4, s[36:39], s80 offen offset:3072 sc1 lds\n" \
"v_mfma_f32_16x16x32_bf16 v[88:91], a[24:27], v[124:127], v[88:91]\n" \
"s_mov_b32 s81, 1966080\n" \
"ds_read_b128 v[120:123], v29 offset:32768\n" \
"s_add_u32 m0, s79, 114688\n" \
"v_mfma_f32_16x16x32_bf16 v[92:95], a[28:31], v[124:127], v[92:95]\n" \
"s_waitcnt vmcnt(13) lgkmcnt(2)\n" \
"s_barrier\n" \
"v_mfma_f32_16x16x32_bf16 v[32:35], a[32:35], v[96:99], v[32:35]\n" \
"ds_read_b128 v[124:127], v29 offset:33792\n" \
"v_mfma_f32_16x16x32_bf16 v[36:39], a[36:39], v[96:99], v[36:39]\n" \
"buffer_load_dwordx4 v12, s[28:31], s81 offen offset:0 lds\n" \
"v_mfma_f32_16x16x32_bf16 v[40:43], a[32:35], v[100:103], v[40:43]\n" \
"s_waitcnt lgkmcnt(6)\n" \
"ds_read_b128 v[96:99], v28 offset:51200\n" \
"s_add_u32 m0, s79, 115584\n" \
"v_mfma_f32_16x16x32_bf16 v[44:47], a[36:39], v[100:103], v[44:47]\n" \
"ds_read_b64_tr_b16 a[48:49], v176 offset:0\n" \
"v_mfma_f32_16x16x32_bf16 v[48:51], a[32:35], v[104:107], v[48:51]\n" \
"ds_read_b128 v[100:103], v28 offset:52224\n" \
"v_mfma_f32_16x16x32_bf16 v[52:55], a[36:39], v[104:107], v[52:55]\n" \
"buffer_load_dwordx4 v12, s[28:31], s81 offen offset:128 lds\n" \
"v_mfma_f32_16x16x32_bf16 v[56:59], a[32:35], v[108:111], v[56:59]\n" \
"s_waitcnt lgkmcnt(5)\n" \
"ds_read_b128 v[104:107], v28 offset:55296\n" \
"s_mov_b32 s81, 2031616\n" \
"v_mfma_f32_16x16x32_bf16 v[60:63], a[36:39], v[108:111], v[60:63]\n" \
"s_add_u32 m0, s79, 122880\n" \
"ds_read_b64_tr_b16 a[50:51], v177 offset:0\n" \
"v_mfma_f32_16x16x32_bf16 v[64:67], a[32:35], v[112:115], v[64:67]\n" \
"ds_read_b128 v[108:111], v28 offset:56320\n" \
"v_mfma_f32_16x16x32_bf16 v[68:71], a[36:39], v[112:115], v[68:71]\n" \
"buffer_load_dwordx4 v12, s[28:31], s81 offen offset:0 lds\n" \
"v_mfma_f32_16x16x32_bf16 v[72:75], a[32:35], v[116:119], v[72:75]\n" \
"s_waitcnt lgkmcnt(6)\n" \
"ds_read_b128 v[112:115], v28 offset:59392\n" \
"s_add_u32 m0, s79, 123776\n" \
"v_mfma_f32_16x16x32_bf16 v[76:79], a[36:39], v[116:119], v[76:79]\n" \
"ds_read_b64_tr_b16 a[52:53], v178 offset:0\n" \
"v_mfma_f32_16x16x32_bf16 v[80:83], a[32:35], v[120:123], v[80:83]\n" \
"ds_read_b128 v[116:119], v28 offset:60416\n" \
"v_mfma_f32_16x16x32_bf16 v[84:87], a[36:39], v[120:123], v[84:87]\n" \
"buffer_load_dwordx4 v12, s[28:31], s81 offen offset:128 lds\n" \
"v_mfma_f32_16x16x32_bf16 v[88:91], a[32:35], v[124:127], v[88:91]\n" \
"ds_read_b128 v[120:123], v29 offset:34816\n" \
"v_mfma_f32_16x16x32_bf16 v[92:95], a[36:39], v[124:127], v[92:95]\n" \
"s_waitcnt vmcnt(13) lgkmcnt(7)\n" \
"s_barrier\n" \
"s_add_u32 s80, s68, 0\n" \
"v_mfma_f32_16x16x32_bf16 v[32:35], a[40:43], v[96:99], v[32:35]\n" \
"ds_read_b128 v[124:127], v29 offset:35840\n" \
"s_waitcnt vmcnt(9)\n" \
"v_mfma_f32_16x16x32_bf16 v[36:39], a[44:47], v[96:99], v[36:39]\n" \
"s_add_u32 m0, s22, 65536\n" \
"v_cmp_ne_u64 vcc, s[14:15], v[18:19]\n" \
"s_nop 0\n" \
"v_mfma_f32_16x16x32_bf16 v[40:43], a[40:43], v[100:103], v[40:43]\n" \
"s_waitcnt lgkmcnt(5)\n" \
"ds_read_b128 v[96:99], v28 offset:61440\n" \
"s_cbranch_vccnz LPL_%=\nLPLR_%=:\n" \
"v_mfma_f32_16x16x32_bf16 v[44:47], a[44:47], v[100:103], v[44:47]\n" \
"ds_read_b64_tr_b16 a[54:55], v179 offset:0\n" \
"v_mfma_f32_16x16x32_bf16 v[48:51], a[40:43], v[104:107], v[48:51]\n" \
"ds_read_b128 v[100:103], v28 offset:62464\n" \
"v_mfma_f32_16x16x32_bf16 v[52:55], a[44:47], v[104:107], v[52:55]\n" \
"ds_read_b64_tr_b16 a[56:57], v176 offset:8192\n" \
"v_mfma_f32_16x16x32_bf16 v[56:59], a[40:43], v[108:111], v[56:59]\n" \
"s_waitcnt lgkmcnt(6)\n" \
"ds_read_b128 v[104:107], v30 offset:16384\n" \
"v_mfma_f32_16x16x32_bf16 v[60:63], a[44:47], v[108:111], v[60:63]\n" \
"buffer_load_dwordx4 v31, s[36:39], s80 offen offset:0 sc1 lds\n" \
"v_mfma_f32_16x16x32_bf16 v[64:67], a[40:43], v[112:115], v[64:67]\n" \
"ds_read_b128 v[108:111], v30 offset:17408\n" \
"v_mfma_f32_16x16x32_bf16 v[68:71], a[44:47], v[112:115], v[68:71]\n" \
"ds_read_b64_tr_b16 a[58:59], v177 offset:8192\n" \
"v_mfma_f32_16x16x32_bf16 v[72:75], a[40:43], v[116:119], v[72:75]\n" \
"s_waitcnt lgkmcnt(7)\n" \
"ds_read_b128 v[112:115], v30 offset:20480\n" \
"ds_read_b64_tr_b16 a[60:61], v178 offset:8192\n" \
"ds_read_b64_tr_b16 a[62:63], v179 offset:8192\n" \
"v_mfma_f32_16x16x32_bf16 v[76:79], a[44:47], v[116:119], v[76:79]\n" \
"buffer_load_dwordx4 v31, s[36:39], s80 offen offset:1024 sc1 lds\n" \
"v_mfma_f32_16x16x32_bf16 v[80:83], a[40:43], v[120:123], v[80:83]\n" \
"ds_read_b128 v[116:119], v30 offset:21504\n" \
"v_mfma_f32_16x16x32_bf16 v[84:87], a[44:47], v[120:123], v[84:87]\n" \
"v_mfma_f32_16x16x32_bf16 v[88:91], a[40:43], v[124:127], v[88:91]\n" \
"ds_read_b128 v[120:123], v29 offset:36864\n" \
"v_mfma_f32_16x16x32_bf16 v[92:95], a[44:47], v[124:127], v[92:95]\n" \
"s_waitcnt vmcnt(10) lgkmcnt(2)\n" \
"s_barrier\n" \
"v_mfma_f32_16x16x32_bf16 v[32:35], a[48:51], v[96:99], v[32:35]\n" \
"ds_read_b128 v[124:127], v29 offset:37888\n" \
"v_mfma_f32_16x16x32_bf16 v[36:39], a[52:55], v[96:99], v[36:39]\n" \
"buffer_load_dwordx4 v31, s[36:39], s80 offen offset:2048 sc1 lds\n" \
"v_mfma_f32_16x16x32_bf16 v[40:43], a[48:51], v[100:103], v[40:43]\n" \
"s_waitcnt lgkmcnt(7)\n" \
"ds_read_b128 v[96:99], v28 offset:63488\n" \
"v_mfma_f32_16x16x32_bf16 v[44:47], a[52:55], v[100:103], v[44:47]\n" \
"ds_read_b64_tr_b16 a[96:97], v176 offset:16384\n" \
"v_mfma_f32_16x16x32_bf16 v[48:51], a[48:51], v[104:107], v[48:51]\n" \
"ds_read_b128 v[100:103], v28 offset:64512\n" \
"v_mfma_f32_16x16x32_bf16 v[52:55], a[52:55], v[104:107], v[52:55]\n" \
"buffer_load_dwordx4 v31, s[36:39], s80 offen offset:3072 sc1 lds\n" \
"v_mfma_f32_16x16x32_bf16 v[56:59], a[48:51], v[108:111], v[56:59]\n" \
"s_waitcnt lgkmcnt(5)\n" \
"ds_read_b128 v[104:107], v30 offset:18432\n" \
"s_add_u32 s80, s68, 16384\n" \
"v_mfma_f32_16x16x32_bf16 v[60:63], a[52:55], v[108:111], v[60:63]\n" \
"s_add_u32 m0, s22, 81920\n" \
"ds_read_b64_tr_b16 a[98:99], v177 offset:16384\n" \
"v_mfma_f32_16x16x32_bf16 v[64:67], a[48:51], v[112:115], v[64:67]\n" \
"ds_read_b128 v[108:111], v30 offset:19456\n" \
"v_mfma_f32_16x16x32_bf16 v[68:71], a[52:55], v[112:115], v[68:71]\n" \
"buffer_load_dwordx4 v31, s[36:39], s80 offen offset:0 sc1 lds\n" \
"v_mfma_f32_16x16x32_bf16 v[72:75], a[48:51], v[116:119], v[72:75]\n" \
"s_waitcnt lgkmcnt(6)\n" \
"ds_read_b128 v[112:115], v30 offset:22528\n" \
"v_mfma_f32_16x16x32_bf16 v[76:79], a[52:55], v[116:119], v[76:79]\n" \
"LH23_%=:\ns_cbranch_execz LHR2_%=\n" \
"ds_read_b64_tr_b16 a[100:101], v178 offset:16384\n" \
"v_mfma_f32_16x16x32_bf16 v[80:83], a[48:51], v[120:123], v[80:83]\n" \
"ds_read_b128 v[116:119], v30 offset:23552\n" \
"v_mfma_f32_16x16x32_bf16 v[84:87], a[52:55], v[120:123], v[84:87]\n" \
"buffer_load_dwordx4 v31, s[36:39], s80 offen offset:1024 sc1 lds\n" \
"v_mfma_f32_16x16x32_bf16 v[88:91], a[48:51], v[124:127], v[88:91]\n" \
"ds_read_b128 v[120:123], v29 offset:38912\n" \
"v_mfma_f32_16x16x32_bf16 v[92:95], a[52:55], v[124:127], v[92:95]\n" \
"s_waitcnt vmcnt(10) lgkmcnt(7)\n" \
"s_barrier\n" \
"ds_read_b64_tr_b16 a[102:103], v179 offset:16384\n" \
"v_mfma_f32_16x16x32_bf16 v[32:35], a[56:59], v[96:99], v[32:35]\n" \
"ds_read_b128 v[124:127], v29 offset:39936\n" \
"v_mfma_f32_16x16x32_bf16 v[36:39], a[60:63], v[96:99], v[36:39]\n" \
"buffer_load_dwordx4 v31, s[36:39], s80 offen offset:2048 sc1 lds\n" \
"v_mfma_f32_16x16x32_bf16 v[40:43], a[56:59], v[100:103], v[40:43]\n" \
"s_waitcnt lgkmcnt(6)\n" \
"ds_read_b128 v[96:99], v30 offset:24576\n" \
"v_mfma_f32_16x16x32_bf16 v[44:47], a[60:63], v[100:103], v[44:47]\n" \
"ds_read_b64_tr_b16 a[104:105], v176 offset:24576\n" \
"v_mfma_f32_16x16x32_bf16 v[48:51], a[56:59], v[104:107], v[48:51]\n" \
"ds_read_b128 v[100:103], v30 offset:25600\n" \
"v_mfma_f32_16x16x32_bf16 v[52:55], a[60:63], v[104:107], v[52:55]\n" \
"buffer_load_dwordx4 v31, s[36:39], s80 offen offset:3072 sc1 lds\n" \
"v_mfma_f32_16x16x32_bf16 v[56:59], a[56:59], v[108:111], v[56:59]\n" \
"s_waitcnt lgkmcnt(6)\n" \
"ds_read_b128 v[104:107], v30 offset:28672\n" \
"s_add_u32 s80, s68, 32768\n" \
"v_mfma_f32_16x16x32_bf16 v[60:63], a[60:63], v[108:111], v[60:63]\n" \
"s_add_u32 m0, s22, 131072\n" \
"ds_read_b64_tr_b16 a[106:107], v177 offset:24576\n" \
"v_mfma_f32_16x16x32_bf16 v[64:67], a[56:59], v[112:115], v[64:67]\n" \
"ds_read_b128 v[108:111], v30 offset:29696\n" \
"v_mfma_f32_16x16x32_bf16 v[68:71], a[60:63], v[112:115], v[68:71]\n" \
"buffer_load_dwordx4 v31, s[36:39], s80 offen offset:0 sc1 lds\n" \
"v_mfma_f32_16x16x32_bf16 v[72:75], a[56:59], v[116:119], v[72:75]\n" \
"s_waitcnt lgkmcnt(6)\n" \
"ds_read_b128 v[112:115], v28 offset:32768\n" \
"ds_read_b64_tr_b16 a[108:109], v178 offset:24576\n" \
"ds_read_b64_tr_b16 a[110:111], v179 offset:24576\n" \
"v_mfma_f32_16x16x32_bf16 v[76:79], a[60:63], v[116:119], v[76:79]\n" \
"v_mfma_f32_16x16x32_bf16 v[80:83], a[56:59], v[120:123], v[80:83]\n" \
"ds_read_b128 v[116:119], v28 offset:33792\n" \
"v_mfma_f32_16x16x32_bf16 v[84:87], a[60:63], v[120:123], v[84:87]\n" \
"buffer_load_dwordx4 v31, s[36:39], s80 offen offset:1024 sc1 lds\n" \
"v_mfma_f32_16x16x32_bf16 v[88:91], a[56:59], v[124:127], v[88:91]\n" \
"ds_read_b128 v[120:123], v29 offset:40960\n" \
"v_mfma_f32_16x16x32_bf16 v[92:95], a[60:63], v[124:127], v[92:95]\n" \
"s_waitcnt vmcnt(10) lgkmcnt(2)\n" \
"s_barrier\n" \
"v_mfma_f32_16x16x32_bf16 v[32:35], a[96:99], v[96:99], v[32:35]\n" \
"ds_read_b128 v[124:127], v29 offset:41984\n" \
"v_mfma_f32_16x16x32_bf16 v[36:39], a[100:103], v[96:99], v[36:39]\n" \
"buffer_load_dwordx4 v31, s[36:39], s80 offen offset:2048 sc1 lds\n" \
"v_mfma_f32_16x16x32_bf16 v[40:43], a[96:99], v[100:103], v[40:43]\n" \
"s_waitcnt lgkmcnt(6)\n" \
"ds_read_b128 v[96:99], v30 offset:26624\n" \
"v_mfma_f32_16x16x32_bf16 v[44:47], a[100:103], v[100:103], v[44:47]\n" \
"ds_read_b64_tr_b16 a[112:113], v180 offset:49152\n" \
"v_mfma_f32_16x16x32_bf16 v[48:51], a[96:99], v[104:107], v[48:51]\n" \
"ds_read_b128 v[100:103], v30 offset:27648\n" \
"v_mfma_f32_16x16x32_bf16 v[52:55], a[100:103], v[104:107], v[52:55]\n" \
"buffer_load_dwordx4 v31, s[36:39], s80 offen offset:3072 sc1 lds\n" \
"v_mfma_f32_16x16x32_bf16 v[56:59], a[96:99], v[108:111], v[56:59]\n" \
"s_waitcnt lgkmcnt(5)\n" \
"ds_read_b128 v[104:107], v30 offset:30720\n" \
"s_add_u32 s80, s68, 49152\n" \
"v_mfma_f32_16x16x32_bf16 v[60:63], a[100:103], v[108:111], v[60:63]\n" \
"s_add_u32 m0, s22, 0\n" \
"ds_read_b64_tr_b16 a[114:115], v181 offset:49152\n" \
"v_mfma_f32_16x16x32_bf16 v[64:67], a[96:99], v[112:115], v[64:67]\n" \
"ds_read_b128 v[108:111], v30 offset:31744\n" \
"v_mfma_f32_16x16x32_bf16 v[68:71], a[100:103], v[112:115], v[68:71]\n" \
"buffer_load_dwordx4 v31, s[36:39], s80 offen offset:0 sc1 lds\n" \
"v_mfma_f32_16x16x32_bf16 v[72:75], a[96:99], v[116:119], v[72:75]\n" \
"s_waitcnt lgkmcnt(6)\n" \
"ds_read_b128 v[112:115], v28 offset:34816\n" \
"v_mfma_f32_16x16x32_bf16 v[76:79], a[100:103], v[116:119], v[76:79]\n" \
"ds_read_b64_tr_b16 a[116:117], v182 offset:49152\n" \
"v_mfma_f32_16x16x32_bf16 v[80:83], a[96:99], v[120:123], v[80:83]\n" \
"ds_read_b128 v[116:119], v28 offset:35840\n" \
"v_mfma_f32_16x16x32_bf16 v[84:87], a[100:103], v[120:123], v[84:87]\n" \
"buffer_load_dwordx4 v31, s[36:39], s80 offen offset:1024 sc1 lds\n" \
"v_mfma_f32_16x16x32_bf16 v[88:91], a[96:99], v[124:127], v[88:91]\n" \
"ds_read_b128 v[120:123], v29 offset:43008\n" \
"v_mfma_f32_16x16x32_bf16 v[92:95], a[100:103], v[124:127], v[92:95]\n" \
"s_waitcnt lgkmcnt(7)\n" \
"s_barrier\n" \
"ds_read_b64_tr_b16 a[118:119], v183 offset:49152\n" \
"v_mfma_f32_16x16x32_bf16 v[32:35], a[104:107], v[96:99], v[32:35]\n" \
"ds_read_b128 v[124:127], v29 offset:44032\n" \
"v_mfma_f32_16x16x32_bf16 v[36:39], a[108:111], v[96:99], v[36:39]\n" \
"buffer_load_dwordx4 v31, s[36:39], s80 offen offset:2048 sc1 lds\n" \
"v_mfma_f32_16x16x32_bf16 v[40:43], a[104:107], v[100:103], v[40:43]\n" \
"s_waitcnt lgkmcnt(6)\n" \
"ds_read_b128 v[96:99], v28 offset:36864\n" \
"v_mfma_f32_16x16x32_bf16 v[44:47], a[108:111], v[100:103], v[44:47]\n" \
"ds_read_b64_tr_b16 a[120:121], v180 offset:57344\n" \
"v_mfma_f32_16x16x32_bf16 v[48:51], a[104:107], v[104:107], v[48:51]\n" \
"ds_read_b128 v[100:103], v28 offset:37888\n" \
"v_mfma_f32_16x16x32_bf16 v[52:55], a[108:111], v[104:107], v[52:55]\n" \
"buffer_load_dwordx4 v31, s[36:39], s80 offen offset:3072 sc1 lds\n" \
"v_mfma_f32_16x16x32_bf16 v[56:59], a[104:107], v[108:111], v[56:59]\n" \
"s_waitcnt lgkmcnt(6)\n" \
"ds_read_b128 v[104:107], v28 offset:40960\n" \
"s_add_u32 s80, s69, 0\n" \
"v_mfma_f32_16x16x32_bf16 v[60:63], a[108:111], v[108:111], v[60:63]\n" \
"s_add_u32 m0, s22, 49152\n" \
"ds_read_b64_tr_b16 a[122:123], v181 offset:57344\n" \
"v_mfma_f32_16x16x32_bf16 v[64:67], a[104:107], v[112:115], v[64:67]\n" \
"ds_read_b128 v[108:111], v28 offset:41984\n" \
"v_mfma_f32_16x16x32_bf16 v[68:71], a[108:111], v[112:115], v[68:71]\n" \
"buffer_load_dwordx4 v31, s[36:39], s80 offen offset:0 sc1 lds\n" \
"v_mfma_f32_16x16x32_bf16 v[72:75], a[104:107], v[116:119], v[72:75]\n" \
"s_waitcnt lgkmcnt(6)\n" \
"ds_read_b128 v[112:115], v28 offset:45056\n" \
"ds_read_b64_tr_b16 a[124:125], v182 offset:57344\n" \
"ds_read_b64_tr_b16 a[126:127], v183 offset:57344\n" \
"v_mfma_f32_16x16x32_bf16 v[76:79], a[108:111], v[116:119], v[76:79]\n" \
"v_mfma_f32_16x16x32_bf16 v[80:83], a[104:107], v[120:123], v[80:83]\n" \
"ds_read_b128 v[116:119], v28 offset:46080\n" \
"v_mfma_f32_16x16x32_bf16 v[84:87], a[108:111], v[120:123], v[84:87]\n" \
"buffer_load_dwordx4 v31, s[36:39], s80 offen offset:1024 sc1 lds\n" \
"v_mfma_f32_16x16x32_bf16 v[88:91], a[104:107], v[124:127], v[88:91]\n" \
"ds_read_b128 v[120:123], v29 offset:45056\n" \
"v_mfma_f32_16x16x32_bf16 v[92:95], a[108:111], v[124:127], v[92:95]\n" \
"s_waitcnt lgkmcnt(2)\n" \
"s_barrier\n" \
"v_mfma_f32_16x16x32_bf16 v[32:35], a[112:115], v[96:99], v[32:35]\n" \
"ds_read_b128 v[124:127], v29 offset:46080\n" \
"v_mfma_f32_16x16x32_bf16 v[36:39], a[116:119], v[96:99], v[36:39]\n" \
"buffer_load_dwordx2 v[176:177], v185, s[36:39], s86 offen offset:0 sc1\n" \
"v_mfma_f32_16x16x32_bf16 v[40:43], a[112:115], v[100:103], v[40:43]\n" \
"s_waitcnt lgkmcnt(6)\n" \
"ds_read_b128 v[96:99], v28 offset:38912\n" \
"v_mfma_f32_16x16x32_bf16 v[44:47], a[116:119], v[100:103], v[44:47]\n" \
"v_mfma_f32_16x16x32_bf16 v[48:51], a[112:115], v[104:107], v[48:51]\n" \
"ds_read_b128 v[100:103], v28 offset:39936\n" \
"v_mfma_f32_16x16x32_bf16 v[52:55], a[116:119], v[104:107], v[52:55]\n" \
"buffer_load_dwordx2 v[178:179], v185, s[36:39], s87 offen offset:0 sc1\n" \
"v_mfma_f32_16x16x32_bf16 v[56:59], a[112:115], v[108:111], v[56:59]\n" \
"s_waitcnt lgkmcnt(4)\n" \
"ds_read_b128 v[104:107], v28 offset:43008\n" \
"v_mfma_f32_16x16x32_bf16 v[60:63], a[116:119], v[108:111], v[60:63]\n" \
"v_mfma_f32_16x16x32_bf16 v[64:67], a[112:115], v[112:115], v[64:67]\n" \
"ds_read_b128 v[108:111], v28 offset:44032\n" \
"v_mfma_f32_16x16x32_bf16 v[68:71], a[116:119], v[112:115], v[68:71]\n" \
"buffer_load_dwordx4 v31, s[36:39], s80 offen offset:2048 sc1 lds\n" \
"v_mfma_f32_16x16x32_bf16 v[72:75], a[112:115], v[116:119], v[72:75]\n" \
"s_waitcnt lgkmcnt(4)\n" \
"ds_read_b128 v[112:115], v28 offset:47104\n" \
"v_mfma_f32_16x16x32_bf16 v[76:79], a[116:119], v[116:119], v[76:79]\n" \
"v_mfma_f32_16x16x32_bf16 v[80:83], a[112:115], v[120:123], v[80:83]\n" \
"ds_read_b128 v[116:119], v28 offset:48128\n" \
"v_mfma_f32_16x16x32_bf16 v[84:87], a[116:119], v[120:123], v[84:87]\n" \
"buffer_load_dwordx4 v31, s[36:39], s80 offen offset:3072 sc1 lds\n" \
"v_mfma_f32_16x16x32_bf16 v[88:91], a[112:115], v[124:127], v[88:91]\n" \
"s_add_u32 s80, s69, 16384\n" \
"ds_read_b128 v[120:123], v29 offset:47104\n" \
"s_add_u32 m0, s22, 16384\n" \
"v_mfma_f32_16x16x32_bf16 v[92:95], a[116:119], v[124:127], v[92:95]\n" \
"s_waitcnt vmcnt(18) lgkmcnt(5)\n" \
"s_barrier\n" \
"v_mfma_f32_16x16x32_bf16 v[32:35], a[120:123], v[96:99], v[32:35]\n" \
"ds_read_b128 v[124:127], v29 offset:48128\n" \
"v_mfma_f32_16x16x32_bf16 v[36:39], a[124:127], v[96:99], v[36:39]\n" \
"buffer_load_dwordx4 v31, s[36:39], s80 offen offset:0 sc1 lds\n" \
"v_mfma_f32_16x16x32_bf16 v[40:43], a[120:123], v[100:103], v[40:43]\n" \
"s_waitcnt lgkmcnt(4)\n" \
"ds_read_b128 v[96:99], v29 offset:0\n" \
"v_mfma_f32_16x16x32_bf16 v[44:47], a[124:127], v[100:103], v[44:47]\n" \
"v_mfma_f32_16x16x32_bf16 v[48:51], a[120:123], v[104:107], v[48:51]\n" \
"ds_read_b128 v[100:103], v29 offset:1024\n" \
"v_mfma_f32_16x16x32_bf16 v[52:55], a[124:127], v[104:107], v[52:55]\n" \
"buffer_load_dwordx4 v31, s[36:39], s80 offen offset:1024 sc1 lds\n" \
"v_mfma_f32_16x16x32_bf16 v[56:59], a[120:123], v[108:111], v[56:59]\n" \
"s_waitcnt lgkmcnt(4)\n" \
"ds_read_b128 v[104:107], v29 offset:2048\n" \
"v_mfma_f32_16x16x32_bf16 v[60:63], a[124:127], v[108:111], v[60:63]\n" \
"v_mfma_f32_16x16x32_bf16 v[64:67], a[120:123], v[112:115], v[64:67]\n" \
"ds_read_b128 v[108:111], v29 offset:3072\n" \
"v_mfma_f32_16x16x32_bf16 v[68:71], a[124:127], v[112:115], v[68:71]\n" \
"buffer_load_dwordx4 v31, s[36:39], s80 offen offset:2048 sc1 lds\n" \
"v_mfma_f32_16x16x32_bf16 v[72:75], a[120:123], v[116:119], v[72:75]\n" \
"s_waitcnt lgkmcnt(4)\n" \
"ds_read_b128 v[112:115], v29 offset:4096\n" \
"v_mfma_f32_16x16x32_bf16 v[76:79], a[124:127], v[116:119], v[76:79]\n" \
"v_mfma_f32_16x16x32_bf16 v[80:83], a[120:123], v[120:123], v[80:83]\n" \
"ds_read_b128 v[116:119], v29 offset:5120\n" \
"v_mfma_f32_16x16x32_bf16 v[84:87], a[124:127], v[120:123], v[84:87]\n" \
"buffer_load_dwordx4 v31, s[36:39], s80 offen offset:3072 sc1 lds\n" \
"v_mfma_f32_16x16x32_bf16 v[88:91], a[120:123], v[124:127], v[88:91]\n" \
"s_add_u32 s80, s69, 32768\n" \
"ds_read_b128 v[120:123], v29 offset:6144\n" \
"s_add_u32 m0, s22, 114688\n" \
"v_mfma_f32_16x16x32_bf16 v[92:95], a[124:127], v[124:127], v[92:95]\n" \
"s_waitcnt lgkmcnt(5)\n" \
"s_barrier\n" \
"v_mfma_f32_16x16x32_bf16 v[192:195], a[160:163], v[96:99], 0\n" \
"ds_read_b128 v[124:127], v29 offset:7168\n" \
"v_mfma_f32_16x16x32_bf16 v[196:199], a[164:167], v[96:99], 0\n" \
"buffer_load_dwordx4 v31, s[36:39], s80 offen offset:0 sc1 lds\n" \
"v_mfma_f32_16x16x32_bf16 v[200:203], a[160:163], v[100:103], 0\n" \
"s_waitcnt lgkmcnt(4)\n" \
"ds_read_b128 v[96:99], v29 offset:8192\n" \
"v_cvt_pk_bf16_f32 v32, v32, v33\n" \
"v_mfma_f32_16x16x32_bf16 v[204:207], a[164:167], v[100:103], 0\n" \
"v_cvt_pk_bf16_f32 v33, v34, v35\n" \
"v_cvt_pk_bf16_f32 v34, v36, v37\n" \
"v_mfma_f32_16x16x32_bf16 v[192:195], a[168:171], v[104:107], v[192:195]\n" \
"ds_read_b128 v[100:103], v29 offset:9216\n" \
"v_cvt_pk_bf16_f32 v35, v38, v39\n" \
"v_mfma_f32_16x16x32_bf16 v[196:199], a[172:175], v[104:107], v[196:199]\n" \
"buffer_store_dwordx4 v[32:35], v4, s[36:39], s72 offen offset:0 sc1\n" \
"v_mfma_f32_16x16x32_bf16 v[200:203], a[168:171], v[108:111], v[200:203]\n" \
"s_waitcnt lgkmcnt(4)\n" \
"ds_read_b128 v[104:107], v29 offset:10240\n" \
"v_cvt_pk_bf16_f32 v40, v40, v41\n" \
"v_mfma_f32_16x16x32_bf16 v[204:207], a[172:175], v[108:111], v[204:207]\n" \
"v_cvt_pk_bf16_f32 v41, v42, v43\n" \
"v_cvt_pk_bf16_f32 v42, v44, v45\n" \
"v_mfma_f32_16x16x32_bf16 v[192:195], a[176:179], v[112:115], v[192:195]\n" \
"ds_read_b128 v[108:111], v29 offset:11264\n" \
"v_cvt_pk_bf16_f32 v43, v46, v47\n" \
"v_mfma_f32_16x16x32_bf16 v[196:199], a[180:183], v[112:115], v[196:199]\n" \
"buffer_load_dwordx4 v31, s[36:39], s80 offen offset:1024 sc1 lds\n" \
"v_mfma_f32_16x16x32_bf16 v[200:203], a[176:179], v[116:119], v[200:203]\n" \
"s_waitcnt lgkmcnt(4)\n" \
"ds_read_b128 v[112:115], v29 offset:12288\n" \
"v_mfma_f32_16x16x32_bf16 v[204:207], a[180:183], v[116:119], v[204:207]\n" \
"v_mfma_f32_16x16x32_bf16 v[192:195], a[184:187], v[120:123], v[192:195]\n" \
"ds_read_b128 v[116:119], v29 offset:13312\n" \
"v_mfma_f32_16x16x32_bf16 v[196:199], a[188:191], v[120:123], v[196:199]\n" \
"buffer_store_dwordx4 v[40:43], v4, s[36:39], s72 offen offset:1024 sc1\n" \
"v_mfma_f32_16x16x32_bf16 v[200:203], a[184:187], v[124:127], v[200:203]\n" \
"ds_read_b128 v[120:123], v29 offset:14336\n" \
"v_mfma_f32_16x16x32_bf16 v[204:207], a[188:191], v[124:127], v[204:207]\n" \
"s_waitcnt vmcnt(22) lgkmcnt(5)\n" \
"s_barrier\n" \
"v_mfma_f32_16x16x32_bf16 v[192:195], a[192:195], v[96:99], v[192:195]\n" \
"ds_read_b128 v[124:127], v29 offset:15360\n" \
"v_mfma_f32_16x16x32_bf16 v[196:199], a[196:199], v[96:99], v[196:199]\n" \
"v_mfma_f32_16x16x32_bf16 v[200:203], a[192:195], v[100:103], v[200:203]\n" \
"s_waitcnt lgkmcnt(4)\n" \
"ds_read_b128 v[96:99], v29 offset:16384\n" \
"v_mfma_f32_16x16x32_bf16 v[204:207], a[196:199], v[100:103], v[204:207]\n" \
"v_mfma_f32_16x16x32_bf16 v[192:195], a[200:203], v[104:107], v[192:195]\n" \
"ds_read_b128 v[100:103], v29 offset:17408\n" \
"v_mfma_f32_16x16x32_bf16 v[196:199], a[204:207], v[104:107], v[196:199]\n" \
"buffer_load_dwordx4 v31, s[36:39], s80 offen offset:2048 sc1 lds\n" \
"v_mfma_f32_16x16x32_bf16 v[200:203], a[200:203], v[108:111], v[200:203]\n" \
"s_waitcnt lgkmcnt(4)\n" \
"ds_read_b128 v[104:107], v29 offset:18432\n" \
"v_mfma_f32_16x16x32_bf16 v[204:207], a[204:207], v[108:111], v[204:207]\n" \
"s_waitcnt vmcnt(11)\n" \
"v_mfma_f32_16x16x32_bf16 v[192:195], a[208:211], v[112:115], v[192:195]\n" \
"ds_read_b128 v[108:111], v29 offset:19456\n" \
"v_mfma_f32_16x16x32_bf16 v[196:199], a[212:215], v[112:115], v[196:199]\n" \
"buffer_load_dwordx4 v31, s[36:39], s80 offen offset:3072 sc1 lds\n" \
"v_mfma_f32_16x16x32_bf16 v[200:203], a[208:211], v[116:119], v[200:203]\n" \
"s_waitcnt lgkmcnt(4)\n" \
"ds_read_b128 v[112:115], v29 offset:20480\n" \
"s_add_u32 s80, s69, 49152\n" \
"v_mfma_f32_16x16x32_bf16 v[204:207], a[212:215], v[116:119], v[204:207]\n" \
"s_add_u32 m0, s22, 147456\n" \
"v_mfma_f32_16x16x32_bf16 v[192:195], a[216:219], v[120:123], v[192:195]\n" \
"ds_read_b128 v[116:119], v29 offset:21504\n" \
"v_mfma_f32_16x16x32_bf16 v[196:199], a[220:223], v[120:123], v[196:199]\n" \
"buffer_load_dwordx4 v31, s[36:39], s80 offen offset:0 sc1 lds\n" \
"v_mfma_f32_16x16x32_bf16 v[200:203], a[216:219], v[124:127], v[200:203]\n" \
"s_waitcnt lgkmcnt(4)\n" \
"ds_read_b128 v[120:123], v29 offset:22528\n" \
"v_mfma_f32_16x16x32_bf16 v[204:207], a[220:223], v[124:127], v[204:207]\n" \
"v_mfma_f32_16x16x32_bf16 v[192:195], a[224:227], v[96:99], v[192:195]\n" \
"ds_read_b128 v[124:127], v29 offset:23552\n" \
"v_mfma_f32_16x16x32_bf16 v[196:199], a[228:231], v[96:99], v[196:199]\n" \
"buffer_load_dwordx4 v31, s[36:39], s80 offen offset:1024 sc1 lds\n" \
"v_mfma_f32_16x16x32_bf16 v[200:203], a[224:227], v[100:103], v[200:203]\n" \
"s_waitcnt lgkmcnt(4)\n" \
"ds_read_b128 v[96:99], v29 offset:24576\n" \
"v_mfma_f32_16x16x32_bf16 v[204:207], a[228:231], v[100:103], v[204:207]\n" \
"v_mfma_f32_16x16x32_bf16 v[192:195], a[232:235], v[104:107], v[192:195]\n" \
"ds_read_b128 v[100:103], v29 offset:25600\n" \
"v_mfma_f32_16x16x32_bf16 v[196:199], a[236:239], v[104:107], v[196:199]\n" \
"buffer_load_dwordx4 v31, s[36:39], s80 offen offset:2048 sc1 lds\n" \
"v_mfma_f32_16x16x32_bf16 v[200:203], a[232:235], v[108:111], v[200:203]\n" \
"s_waitcnt lgkmcnt(4)\n" \
"ds_read_b128 v[104:107], v29 offset:26624\n" \
"v_cvt_pk_bf16_f32 v48, v48, v49\n" \
"v_mfma_f32_16x16x32_bf16 v[204:207], a[236:239], v[108:111], v[204:207]\n" \
"v_cvt_pk_bf16_f32 v49, v50, v51\n" \
"v_cvt_pk_bf16_f32 v50, v52, v53\n" \
"v_mfma_f32_16x16x32_bf16 v[192:195], a[240:243], v[112:115], v[192:195]\n" \
"ds_read_b128 v[108:111], v29 offset:27648\n" \
"v_cvt_pk_bf16_f32 v51, v54, v55\n" \
"v_mfma_f32_16x16x32_bf16 v[196:199], a[244:247], v[112:115], v[196:199]\n" \
"buffer_store_dwordx4 v[48:51], v4, s[36:39], s73 offen offset:0 sc1\n" \
"v_mfma_f32_16x16x32_bf16 v[200:203], a[240:243], v[116:119], v[200:203]\n" \
"s_waitcnt lgkmcnt(4)\n" \
"ds_read_b128 v[112:115], v29 offset:28672\n" \
"v_cvt_pk_bf16_f32 v56, v56, v57\n" \
"v_mfma_f32_16x16x32_bf16 v[204:207], a[244:247], v[116:119], v[204:207]\n" \
"v_cvt_pk_bf16_f32 v57, v58, v59\n" \
"v_cvt_pk_bf16_f32 v58, v60, v61\n" \
"v_mfma_f32_16x16x32_bf16 v[192:195], a[248:251], v[120:123], v[192:195]\n" \
"ds_read_b128 v[116:119], v29 offset:29696\n" \
"v_cvt_pk_bf16_f32 v59, v62, v63\n" \
"v_mfma_f32_16x16x32_bf16 v[196:199], a[252:255], v[120:123], v[196:199]\n" \
"v_mfma_f32_16x16x32_bf16 v[200:203], a[248:251], v[124:127], v[200:203]\n" \
"ds_read_b128 v[120:123], v29 offset:30720\n" \
"v_mfma_f32_16x16x32_bf16 v[204:207], a[252:255], v[124:127], v[204:207]\n" \
"s_waitcnt vmcnt(24) lgkmcnt(5)\n" \
"s_barrier\n" \
"v_mfma_f32_16x16x32_bf16 v[192:195], a[64:67], v[96:99], v[192:195]\n" \
"ds_read_b128 v[124:127], v29 offset:31744\n" \
"v_mfma_f32_16x16x32_bf16 v[196:199], a[68:71], v[96:99], v[196:199]\n" \
"buffer_store_dwordx4 v[56:59], v4, s[36:39], s73 offen offset:1024 sc1\n" \
"v_mfma_f32_16x16x32_bf16 v[200:203], a[64:67], v[100:103], v[200:203]\n" \
"s_waitcnt lgkmcnt(4)\n" \
"ds_read_b128 v[96:99], v30 offset:0\n" \
"v_permlane16_swap_b32 v144, v146\n" \
"v_mfma_f32_16x16x32_bf16 v[204:207], a[68:71], v[100:103], v[204:207]\n" \
"v_permlane16_swap_b32 v145, v147\n" \
"v_sub_f32 v5, 1.0, v176\n" \
"v_mfma_f32_16x16x32_bf16 v[192:195], a[72:75], v[104:107], v[192:195]\n" \
"ds_read_b128 v[100:103], v30 offset:1024\n" \
"v_cvt_pk_bf16_f32 v5, v5, v5\n" \
"v_mfma_f32_16x16x32_bf16 v[196:199], a[76:79], v[104:107], v[196:199]\n" \
"v_and_b32 v5, 0xffff0000, v5\n" \
"v_lshlrev_b32 v32, 16, v144\n" \
"v_mfma_f32_16x16x32_bf16 v[200:203], a[72:75], v[108:111], v[200:203]\n" \
"s_waitcnt lgkmcnt(4)\n" \
"ds_read_b128 v[104:107], v30 offset:2048\n" \
"v_and_b32 v33, 0xffff0000, v144\n" \
"v_mfma_f32_16x16x32_bf16 v[204:207], a[76:79], v[108:111], v[204:207]\n" \
"v_lshlrev_b32 v188, 16, v128\n" \
"v_and_b32 v189, 0xffff0000, v128\n" \
"v_mfma_f32_16x16x32_bf16 v[192:195], a[80:83], v[112:115], v[192:195]\n" \
"ds_read_b128 v[108:111], v30 offset:3072\n" \
"v_mul_f32 v32, v176, v32\n" \
"v_mfma_f32_16x16x32_bf16 v[196:199], a[84:87], v[112:115], v[196:199]\n" \
"buffer_load_dwordx4 v31, s[36:39], s80 offen offset:3072 sc1 lds\n" \
"v_mfma_f32_16x16x32_bf16 v[200:203], a[80:83], v[116:119], v[200:203]\n" \
"s_waitcnt lgkmcnt(4)\n" \
"ds_read_b128 v[112:115], v30 offset:4096\n" \
"s_add_u32 s80, s70, 0\n" \
"v_mfma_f32_16x16x32_bf16 v[204:207], a[84:87], v[116:119], v[204:207]\n" \
"s_add_u32 m0, s22, 32768\n" \
"v_mul_f32 v33, v176, v33\n" \
"v_mul_f32 v32, v188, v32\n" \
"v_mfma_f32_16x16x32_bf16 v[192:195], a[88:91], v[120:123], v[192:195]\n" \
"ds_read_b128 v[116:119], v30 offset:5120\n" \
"v_mul_f32 v33, v189, v33\n" \
"v_mfma_f32_16x16x32_bf16 v[196:199], a[92:95], v[120:123], v[196:199]\n" \
"buffer_load_dwordx4 v31, s[36:39], s80 offen offset:0 sc1 lds\n" \
"v_mfma_f32_16x16x32_bf16 v[200:203], a[88:91], v[124:127], v[200:203]\n" \
"s_waitcnt lgkmcnt(4)\n" \
"ds_read_b128 v[120:123], v30 offset:6144\n" \
"v_lshlrev_b32 v34, 16, v145\n" \
"v_mfma_f32_16x16x32_bf16 v[204:207], a[92:95], v[124:127], v[204:207]\n" \
"v_and_b32 v35, 0xffff0000, v145\n" \
"v_lshlrev_b32 v188, 16, v129\n" \
"v_mfma_f32_16x16x32_bf16 v[192:195], a[128:131], v[96:99], v[192:195]\n" \
"ds_read_b128 v[124:127], v30 offset:7168\n" \
"v_and_b32 v189, 0xffff0000, v129\n" \
"v_mfma_f32_16x16x32_bf16 v[196:199], a[132:135], v[96:99], v[196:199]\n" \
"buffer_load_dwordx4 v31, s[36:39], s80 offen offset:1024 sc1 lds\n" \
"v_mfma_f32_16x16x32_bf16 v[200:203], a[128:131], v[100:103], v[200:203]\n" \
"s_waitcnt lgkmcnt(4)\n" \
"ds_read_b128 v[96:99], v30 offset:8192\n" \
"v_mul_f32 v34, v176, v34\n" \
"v_mfma_f32_16x16x32_bf16 v[204:207], a[132:135], v[100:103], v[204:207]\n" \
"v_mul_f32 v35, v176, v35\n" \
"v_mul_f32 v34, v188, v34\n" \
"v_mfma_f32_16x16x32_bf16 v[192:195], a[136:139], v[104:107], v[192:195]\n" \
"ds_read_b128 v[100:103], v30 offset:9216\n" \
"v_mul_f32 v35, v189, v35\n" \
"v_mfma_f32_16x16x32_bf16 v[196:199], a[140:143], v[104:107], v[196:199]\n" \
"buffer_load_dwordx4 v31, s[36:39], s80 offen offset:2048 sc1 lds\n" \
"v_mfma_f32_16x16x32_bf16 v[200:203], a[136:139], v[108:111], v[200:203]\n" \
"s_waitcnt lgkmcnt(4)\n" \
"ds_read_b128 v[104:107], v30 offset:10240\n" \
"v_lshlrev_b32 v36, 16, v146\n" \
"v_mfma_f32_16x16x32_bf16 v[204:207], a[140:143], v[108:111], v[204:207]\n" \
"v_and_b32 v37, 0xffff0000, v146\n" \
"v_lshlrev_b32 v188, 16, v132\n" \
"v_mfma_f32_16x16x32_bf16 v[192:195], a[144:147], v[112:115], v[192:195]\n" \
"ds_read_b128 v[108:111], v30 offset:11264\n" \
"v_and_b32 v189, 0xffff0000, v132\n" \
"v_mfma_f32_16x16x32_bf16 v[196:199], a[148:151], v[112:115], v[196:199]\n" \
"buffer_load_dwordx4 v31, s[36:39], s80 offen offset:3072 sc1 lds\n" \
"v_mfma_f32_16x16x32_bf16 v[200:203], a[144:147], v[116:119], v[200:203]\n" \
"s_waitcnt lgkmcnt(4)\n" \
"ds_read_b128 v[112:115], v30 offset:12288\n" \
"s_add_u32 s80, s70, 16384\n" \
"v_mfma_f32_16x16x32_bf16 v[204:207], a[148:151], v[116:119], v[204:207]\n" \
"s_add_u32 m0, s22, 98304\n" \
"s_cmp_eq_u32 s89, 3\ns_cbranch_scc1 LSW_%=\nLSWR_%=:\n" \
"v_mfma_f32_16x16x32_bf16 v[192:195], a[152:155], v[120:123], v[192:195]\n" \
"ds_read_b128 v[116:119], v30 offset:13312\n" \
"v_cvt_pk_bf16_f32 v80, v80, v81\n" \
"v_mfma_f32_16x16x32_bf16 v[196:199], a[156:159], v[120:123], v[196:199]\n" \
"buffer_load_dwordx4 v31, s[36:39], s80 offen offset:0 sc1 lds\n" \
"v_mfma_f32_16x16x32_bf16 v[200:203], a[152:155], v[124:127], v[200:203]\n" \
"ds_read_b128 v[120:123], v30 offset:14336\n" \
"v_cvt_pk_bf16_f32 v81, v82, v83\n" \
"v_mfma_f32_16x16x32_bf16 v[204:207], a[156:159], v[124:127], v[204:207]\n" \
"s_waitcnt vmcnt(27) lgkmcnt(5)\n" \
"s_barrier\n" \
"v_cvt_pk_bf16_f32 v82, v84, v85\n" \
"v_mfma_f32_16x16x32_bf16 v[192:195], a[0:3], v[96:99], v[192:195]\n" \
"ds_read_b128 v[124:127], v30 offset:15360\n" \
"v_cvt_pk_bf16_f32 v83, v86, v87\n" \
"v_mfma_f32_16x16x32_bf16 v[196:199], a[4:7], v[96:99], v[196:199]\n" \
"buffer_store_dwordx4 v[80:83], v4, s[36:39], s74 offen offset:0 sc1\n" \
"v_mfma_f32_16x16x32_bf16 v[200:203], a[0:3], v[100:103], v[200:203]\n" \
"s_waitcnt lgkmcnt(4)\n" \
"ds_read_b128 v[96:99], v28 offset:0\n" \
"v_cvt_pk_bf16_f32 v88, v88, v89\n" \
"v_mfma_f32_16x16x32_bf16 v[204:207], a[4:7], v[100:103], v[204:207]\n" \
"s_waitcnt vmcnt(14)\n" \
"v_cvt_pk_bf16_f32 v89, v90, v91\n" \
"v_cvt_pk_bf16_f32 v90, v92, v93\n" \
"v_mfma_f32_16x16x32_bf16 v[192:195], a[8:11], v[104:107], v[192:195]\n" \
"ds_read_b128 v[100:103], v28 offset:1024\n" \
"v_cvt_pk_bf16_f32 v91, v94, v95\n" \
"v_mfma_f32_16x16x32_bf16 v[196:199], a[12:15], v[104:107], v[196:199]\n" \
"buffer_store_dwordx2 v[10:11], v20, s[36:39], s23 offen sc1\n" \
"v_mfma_f32_16x16x32_bf16 v[200:203], a[8:11], v[108:111], v[200:203]\n" \
"s_waitcnt lgkmcnt(4)\n" \
"ds_read_b128 v[104:107], v28 offset:2048\n" \
"v_mul_f32 v36, v176, v36\n" \
"v_mfma_f32_16x16x32_bf16 v[204:207], a[12:15], v[108:111], v[204:207]\n" \
"v_mul_f32 v37, v176, v37\n" \
"v_mul_f32 v36, v188, v36\n" \
"v_mfma_f32_16x16x32_bf16 v[192:195], a[16:19], v[112:115], v[192:195]\n" \
"ds_read_b128 v[108:111], v28 offset:3072\n" \
"v_mul_f32 v37, v189, v37\n" \
"v_mfma_f32_16x16x32_bf16 v[196:199], a[20:23], v[112:115], v[196:199]\n" \
"v_lshlrev_b32 v38, 16, v147\n" \
"v_and_b32 v39, 0xffff0000, v147\n" \
"v_mfma_f32_16x16x32_bf16 v[200:203], a[16:19], v[116:119], v[200:203]\n" \
"s_waitcnt lgkmcnt(4)\n" \
"ds_read_b128 v[112:115], v28 offset:4096\n" \
"v_lshlrev_b32 v188, 16, v133\n" \
"v_mfma_f32_16x16x32_bf16 v[204:207], a[20:23], v[116:119], v[204:207]\n" \
"buffer_store_dwordx4 v[88:91], v4, s[36:39], s74 offen offset:1024 sc1\n" \
"v_mfma_f32_16x16x32_bf16 v[192:195], a[24:27], v[120:123], v[192:195]\n" \
"ds_read_b128 v[116:119], v28 offset:5120\n" \
"v_and_b32 v189, 0xffff0000, v133\n" \
"v_mfma_f32_16x16x32_bf16 v[196:199], a[28:31], v[120:123], v[196:199]\n" \
"v_mul_f32 v38, v176, v38\n" \
"v_mul_f32 v39, v176, v39\n" \
"v_mfma_f32_16x16x32_bf16 v[200:203], a[24:27], v[124:127], v[200:203]\n" \
"s_waitcnt lgkmcnt(4)\n" \
"ds_read_b128 v[120:123], v28 offset:6144\n" \
"v_mul_f32 v38, v188, v38\n" \
"v_mfma_f32_16x16x32_bf16 v[204:207], a[28:31], v[124:127], v[204:207]\n" \
"v_mul_f32 v39, v189, v39\n" \
"v_permlane16_swap_b32 v148, v150\n" \
"v_mfma_f32_16x16x32_bf16 v[192:195], a[32:35], v[96:99], v[192:195]\n" \
"ds_read_b128 v[124:127], v28 offset:7168\n" \
"v_permlane16_swap_b32 v149, v151\n" \
"v_mfma_f32_16x16x32_bf16 v[196:199], a[36:39], v[96:99], v[196:199]\n" \
"v_sub_f32 v21, 1.0, v177\n" \
"v_cvt_pk_bf16_f32 v21, v21, v21\n" \
"v_mfma_f32_16x16x32_bf16 v[200:203], a[32:35], v[100:103], v[200:203]\n" \
"s_waitcnt lgkmcnt(4)\n" \
"ds_read_b128 v[96:99], v28 offset:8192\n" \
"v_and_b32 v21, 0xffff0000, v21\n" \
"v_mfma_f32_16x16x32_bf16 v[204:207], a[36:39], v[100:103], v[204:207]\n" \
"buffer_load_dwordx4 v31, s[36:39], s80 offen offset:1024 sc1 lds\n" \
"v_mfma_f32_16x16x32_bf16 v[192:195], a[40:43], v[104:107], v[192:195]\n" \
"ds_read_b128 v[100:103], v28 offset:9216\n" \
"v_lshlrev_b32 v40, 16, v148\n" \
"v_mfma_f32_16x16x32_bf16 v[196:199], a[44:47], v[104:107], v[196:199]\n" \
"v_and_b32 v41, 0xffff0000, v148\n" \
"v_lshlrev_b32 v188, 16, v130\n" \
"v_mfma_f32_16x16x32_bf16 v[200:203], a[40:43], v[108:111], v[200:203]\n" \
"s_waitcnt lgkmcnt(4)\n" \
"ds_read_b128 v[104:107], v28 offset:10240\n" \
"v_and_b32 v189, 0xffff0000, v130\n" \
"v_mfma_f32_16x16x32_bf16 v[204:207], a[44:47], v[108:111], v[204:207]\n" \
"buffer_load_dwordx4 v31, s[36:39], s80 offen offset:2048 sc1 lds\n" \
"v_mfma_f32_16x16x32_bf16 v[192:195], a[48:51], v[112:115], v[192:195]\n" \
"ds_read_b128 v[108:111], v28 offset:11264\n" \
"v_mul_f32 v40, v177, v40\n" \
"v_mfma_f32_16x16x32_bf16 v[196:199], a[52:55], v[112:115], v[196:199]\n" \
"v_mul_f32 v41, v177, v41\n" \
"v_mul_f32 v40, v188, v40\n" \
"v_mfma_f32_16x16x32_bf16 v[200:203], a[48:51], v[116:119], v[200:203]\n" \
"s_waitcnt lgkmcnt(4)\n" \
"ds_read_b128 v[112:115], v28 offset:12288\n" \
"v_mul_f32 v41, v189, v41\n" \
"v_mfma_f32_16x16x32_bf16 v[204:207], a[52:55], v[116:119], v[204:207]\n" \
"buffer_load_dwordx4 v31, s[36:39], s80 offen offset:3072 sc1 lds\n" \
"v_mfma_f32_16x16x32_bf16 v[192:195], a[56:59], v[120:123], v[192:195]\n" \
"s_add_u32 s80, s70, 32768\n" \
"ds_read_b128 v[116:119], v28 offset:13312\n" \
"s_add_u32 m0, s22, 65536\n" \
"v_mfma_f32_16x16x32_bf16 v[196:199], a[60:63], v[120:123], v[196:199]\n" \
"v_lshlrev_b32 v42, 16, v149\n" \
"v_and_b32 v43, 0xffff0000, v149\n" \
"v_mfma_f32_16x16x32_bf16 v[200:203], a[56:59], v[124:127], v[200:203]\n" \
"ds_read_b128 v[120:123], v28 offset:14336\n" \
"v_lshlrev_b32 v188, 16, v131\n" \
"v_mfma_f32_16x16x32_bf16 v[204:207], a[60:63], v[124:127], v[204:207]\n" \
"s_waitcnt vmcnt(27) lgkmcnt(5)\n" \
"s_barrier\n" \
"v_and_b32 v189, 0xffff0000, v131\n" \
"v_mfma_f32_16x16x32_bf16 v[192:195], a[96:99], v[96:99], v[192:195]\n" \
"ds_read_b128 v[124:127], v28 offset:15360\n" \
"v_mul_f32 v42, v177, v42\n" \
"v_mfma_f32_16x16x32_bf16 v[196:199], a[100:103], v[96:99], v[196:199]\n" \
"buffer_load_dwordx4 v31, s[36:39], s80 offen offset:0 sc1 lds\n" \
"v_mfma_f32_16x16x32_bf16 v[200:203], a[96:99], v[100:103], v[200:203]\n" \
"s_waitcnt lgkmcnt(4)\n" \
"ds_read_b128 v[96:99], v28 offset:49152\n" \
"v_mul_f32 v43, v177, v43\n" \
"v_mfma_f32_16x16x32_bf16 v[204:207], a[100:103], v[100:103], v[204:207]\n" \
"v_mul_f32 v42, v188, v42\n" \
"v_mul_f32 v43, v189, v43\n" \
"v_mfma_f32_16x16x32_bf16 v[192:195], a[104:107], v[104:107], v[192:195]\n" \
"ds_read_b128 v[100:103], v28 offset:50176\n" \
"v_lshlrev_b32 v44, 16, v150\n" \
"v_mfma_f32_16x16x32_bf16 v[196:199], a[108:111], v[104:107], v[196:199]\n" \
"buffer_load_dwordx4 v31, s[36:39], s80 offen offset:1024 sc1 lds\n" \
"v_mfma_f32_16x16x32_bf16 v[200:203], a[104:107], v[108:111], v[200:203]\n" \
"s_waitcnt lgkmcnt(4)\n" \
"ds_read_b128 v[104:107], v28 offset:51200\n" \
"v_and_b32 v45, 0xffff0000, v150\n" \
"v_mfma_f32_16x16x32_bf16 v[204:207], a[108:111], v[108:111], v[204:207]\n" \
"s_waitcnt vmcnt(14)\n" \
"v_lshlrev_b32 v188, 16, v134\n" \
"v_and_b32 v189, 0xffff0000, v134\n" \
"v_mfma_f32_16x16x32_bf16 v[192:195], a[112:115], v[112:115], v[192:195]\n" \
"ds_read_b128 v[108:111], v28 offset:52224\n" \
"v_mul_f32 v44, v177, v44\n" \
"v_mfma_f32_16x16x32_bf16 v[196:199], a[116:119], v[112:115], v[196:199]\n" \
"buffer_store_dwordx2 v[10:11], v20, s[36:39], s24 offen sc1\n" \
"v_mfma_f32_16x16x32_bf16 v[200:203], a[112:115], v[116:119], v[200:203]\n" \
"s_waitcnt lgkmcnt(4)\n" \
"ds_read_b128 v[112:115], v28 offset:53248\n" \
"v_mul_f32 v45, v177, v45\n" \
"v_mfma_f32_16x16x32_bf16 v[204:207], a[116:119], v[116:119], v[204:207]\n" \
"buffer_load_dwordx4 v31, s[36:39], s80 offen offset:2048 sc1 lds\n" \
"v_mfma_f32_16x16x32_bf16 v[192:195], a[120:123], v[120:123], v[192:195]\n" \
"ds_read_b128 v[116:119], v28 offset:54272\n" \
"v_mul_f32 v44, v188, v44\n" \
"v_mfma_f32_16x16x32_bf16 v[196:199], a[124:127], v[120:123], v[196:199]\n" \
"v_mul_f32 v45, v189, v45\n" \
"v_lshlrev_b32 v46, 16, v151\n" \
"v_mfma_f32_16x16x32_bf16 v[200:203], a[120:123], v[124:127], v[200:203]\n" \
"s_waitcnt lgkmcnt(4)\n" \
"ds_read_b128 v[120:123], v28 offset:55296\n" \
"v_and_b32 v47, 0xffff0000, v151\n" \
"v_mfma_f32_16x16x32_bf16 v[204:207], a[124:127], v[124:127], v[204:207]\n" \
"buffer_load_dwordx4 v31, s[36:39], s80 offen offset:3072 sc1 lds\n" \
"v_mfma_f32_16x16x32_bf16 v[208:211], a[160:163], v[96:99], 0\n" \
"s_add_u32 s80, s70, 49152\n" \
"ds_read_b128 v[124:127], v28 offset:56320\n" \
"s_add_u32 m0, s22, 81920\n" \
"v_mfma_f32_16x16x32_bf16 v[212:215], a[164:167], v[96:99], 0\n" \
"v_lshlrev_b32 v188, 16, v135\n" \
"v_and_b32 v189, 0xffff0000, v135\n" \
"v_mfma_f32_16x16x32_bf16 v[216:219], a[160:163], v[100:103], 0\n" \
"s_waitcnt lgkmcnt(4)\n" \
"ds_read_b128 v[96:99], v28 offset:57344\n" \
"v_cvt_pk_bf16_f32 v192, v192, v193\n" \
"v_mfma_f32_16x16x32_bf16 v[220:223], a[164:167], v[100:103], 0\n" \
"v_cvt_pk_bf16_f32 v193, v194, v195\n" \
"v_cvt_pk_bf16_f32 v194, v196, v197\n" \
"v_mfma_f32_16x16x32_bf16 v[208:211], a[168:171], v[104:107], v[208:211]\n" \
"LH24_%=:\ns_cbranch_execz LHR2_%=\n" \
"ds_read_b128 v[100:103], v28 offset:58368\n" \
"v_cvt_pk_bf16_f32 v195, v198, v199\n" \
"v_mfma_f32_16x16x32_bf16 v[212:215], a[172:175], v[104:107], v[212:215]\n" \
"buffer_store_dwordx4 v[192:195], v4, s[36:39], s75 offen offset:0 sc1\n" \
"v_mfma_f32_16x16x32_bf16 v[216:219], a[168:171], v[108:111], v[216:219]\n" \
"s_waitcnt lgkmcnt(4)\n" \
"ds_read_b128 v[104:107], v28 offset:59392\n" \
"v_cvt_pk_bf16_f32 v200, v200, v201\n" \
"v_mfma_f32_16x16x32_bf16 v[220:223], a[172:175], v[108:111], v[220:223]\n" \
"v_cvt_pk_bf16_f32 v201, v202, v203\n" \
"v_cvt_pk_bf16_f32 v202, v204, v205\n" \
"v_mfma_f32_16x16x32_bf16 v[208:211], a[176:179], v[112:115], v[208:211]\n" \
"ds_read_b128 v[108:111], v28 offset:60416\n" \
"v_cvt_pk_bf16_f32 v203, v206, v207\n" \
"v_mfma_f32_16x16x32_bf16 v[212:215], a[180:183], v[112:115], v[212:215]\n" \
"buffer_load_dwordx4 v31, s[36:39], s80 offen offset:0 sc1 lds\n" \
"v_mfma_f32_16x16x32_bf16 v[216:219], a[176:179], v[116:119], v[216:219]\n" \
"s_waitcnt lgkmcnt(4)\n" \
"ds_read_b128 v[112:115], v28 offset:61440\n" \
"v_mul_f32 v46, v177, v46\n" \
"v_mfma_f32_16x16x32_bf16 v[220:223], a[180:183], v[116:119], v[220:223]\n" \
"v_mul_f32 v47, v177, v47\n" \
"v_mul_f32 v46, v188, v46\n" \
"v_mfma_f32_16x16x32_bf16 v[208:211], a[184:187], v[120:123], v[208:211]\n" \
"ds_read_b128 v[116:119], v28 offset:62464\n" \
"v_mul_f32 v47, v189, v47\n" \
"v_mfma_f32_16x16x32_bf16 v[212:215], a[188:191], v[120:123], v[212:215]\n" \
"buffer_store_dwordx4 v[200:203], v4, s[36:39], s75 offen offset:1024 sc1\n" \
"v_mfma_f32_16x16x32_bf16 v[216:219], a[184:187], v[124:127], v[216:219]\n" \
"ds_read_b128 v[120:123], v28 offset:63488\n" \
"v_permlane16_swap_b32 v152, v154\n" \
"v_mfma_f32_16x16x32_bf16 v[220:223], a[188:191], v[124:127], v[220:223]\n" \
"s_waitcnt vmcnt(31) lgkmcnt(5)\n" \
"s_barrier\n" \
"v_permlane16_swap_b32 v153, v155\n" \
"v_mfma_f32_16x16x32_bf16 v[208:211], a[192:195], v[96:99], v[208:211]\n" \
"ds_read_b128 v[124:127], v28 offset:64512\n" \
"v_sub_f32 v6, 1.0, v178\n" \
"v_mfma_f32_16x16x32_bf16 v[212:215], a[196:199], v[96:99], v[212:215]\n" \
"v_cvt_pk_bf16_f32 v6, v6, v6\n" \
"v_and_b32 v6, 0xffff0000, v6\n" \
"v_mfma_f32_16x16x32_bf16 v[216:219], a[192:195], v[100:103], v[216:219]\n" \
"s_waitcnt lgkmcnt(4)\n" \
"ds_read_b128 v[96:99], v28 offset:16384\n" \
"v_lshlrev_b32 v48, 16, v152\n" \
"v_mfma_f32_16x16x32_bf16 v[220:223], a[196:199], v[100:103], v[220:223]\n" \
"v_and_b32 v49, 0xffff0000, v152\n" \
"v_lshlrev_b32 v188, 16, v136\n" \
"v_mfma_f32_16x16x32_bf16 v[208:211], a[200:203], v[104:107], v[208:211]\n" \
"ds_read_b128 v[100:103], v28 offset:17408\n" \
"v_and_b32 v189, 0xffff0000, v136\n" \
"v_mfma_f32_16x16x32_bf16 v[212:215], a[204:207], v[104:107], v[212:215]\n" \
"buffer_load_dwordx4 v31, s[36:39], s80 offen offset:1024 sc1 lds\n" \
"v_mfma_f32_16x16x32_bf16 v[216:219], a[200:203], v[108:111], v[216:219]\n" \
"s_waitcnt lgkmcnt(4)\n" \
"ds_read_b128 v[104:107], v28 offset:18432\n" \
"v_mul_f32 v48, v178, v48\n" \
"v_mfma_f32_16x16x32_bf16 v[220:223], a[204:207], v[108:111], v[220:223]\n" \
"v_mul_f32 v49, v178, v49\n" \
"v_mul_f32 v48, v188, v48\n" \
"v_mfma_f32_16x16x32_bf16 v[208:211], a[208:211], v[112:115], v[208:211]\n" \
"ds_read_b128 v[108:111], v28 offset:19456\n" \
"v_mul_f32 v49, v189, v49\n" \
"v_mfma_f32_16x16x32_bf16 v[212:215], a[212:215], v[112:115], v[212:215]\n" \
"buffer_load_dwordx4 v31, s[36:39], s80 offen offset:2048 sc1 lds\n" \
"v_mfma_f32_16x16x32_bf16 v[216:219], a[208:211], v[116:119], v[216:219]\n" \
"s_waitcnt lgkmcnt(4)\n" \
"ds_read_b128 v[112:115], v28 offset:20480\n" \
"v_lshlrev_b32 v50, 16, v153\n" \
"v_mfma_f32_16x16x32_bf16 v[220:223], a[212:215], v[116:119], v[220:223]\n" \
"v_and_b32 v51, 0xffff0000, v153\n" \
"v_lshlrev_b32 v188, 16, v137\n" \
"v_mfma_f32_16x16x32_bf16 v[208:211], a[216:219], v[120:123], v[208:211]\n" \
"ds_read_b128 v[116:119], v28 offset:21504\n" \
"v_and_b32 v189, 0xffff0000, v137\n" \
"v_mfma_f32_16x16x32_bf16 v[212:215], a[220:223], v[120:123], v[212:215]\n" \
"buffer_load_dwordx4 v31, s[36:39], s80 offen offset:3072 sc1 lds\n" \
"v_mfma_f32_16x16x32_bf16 v[216:219], a[216:219], v[124:127], v[216:219]\n" \
"s_waitcnt lgkmcnt(4)\n" \
"ds_read_b128 v[120:123], v28 offset:22528\n" \
"v_mul_f32 v50, v178, v50\n" \
"v_mfma_f32_16x16x32_bf16 v[220:223], a[220:223], v[124:127], v[220:223]\n" \
"s_add_u32 s80, s71, 0\n" \
"s_add_u32 m0, s22, 131072\n" \
"v_mul_f32 v51, v178, v51\n" \
"v_mfma_f32_16x16x32_bf16 v[208:211], a[224:227], v[96:99], v[208:211]\n" \
"ds_read_b128 v[124:127], v28 offset:23552\n" \
"v_mul_f32 v50, v188, v50\n" \
"v_mfma_f32_16x16x32_bf16 v[212:215], a[228:231], v[96:99], v[212:215]\n" \
"s_waitcnt vmcnt(14)\n" \
"v_mul_f32 v51, v189, v51\n" \
"v_lshlrev_b32 v52, 16, v154\n" \
"v_mfma_f32_16x16x32_bf16 v[216:219], a[224:227], v[100:103], v[216:219]\n" \
"s_waitcnt lgkmcnt(4)\n" \
"ds_read_b128 v[96:99], v28 offset:24576\n" \
"v_and_b32 v53, 0xffff0000, v154\n" \
"v_mfma_f32_16x16x32_bf16 v[220:223], a[228:231], v[100:103], v[220:223]\n" \
"buffer_store_dwordx2 v[10:11], v20, s[36:39], s45 offen sc1\n" \
"v_mfma_f32_16x16x32_bf16 v[208:211], a[232:235], v[104:107], v[208:211]\n" \
"ds_read_b128 v[100:103], v28 offset:25600\n" \
"v_lshlrev_b32 v188, 16, v140\n" \
"v_mfma_f32_16x16x32_bf16 v[212:215], a[236:239], v[104:107], v[212:215]\n" \
"v_and_b32 v189, 0xffff0000, v140\n" \
"v_mul_f32 v52, v178, v52\n" \
"v_mfma_f32_16x16x32_bf16 v[216:219], a[232:235], v[108:111], v[216:219]\n" \
"s_waitcnt lgkmcnt(4)\n" \
"ds_read_b128 v[104:107], v28 offset:26624\n" \
"v_mul_f32 v53, v178, v53\n" \
"v_mfma_f32_16x16x32_bf16 v[220:223], a[236:239], v[108:111], v[220:223]\n" \
"buffer_load_dwordx4 v31, s[36:39], s80 offen offset:0 sc1 lds\n" \
"v_mfma_f32_16x16x32_bf16 v[208:211], a[240:243], v[112:115], v[208:211]\n" \
"ds_read_b128 v[108:111], v28 offset:27648\n" \
"v_mul_f32 v52, v188, v52\n" \
"v_mfma_f32_16x16x32_bf16 v[212:215], a[244:247], v[112:115], v[212:215]\n" \
"v_mul_f32 v53, v189, v53\n" \
"v_lshlrev_b32 v54, 16, v155\n" \
"v_mfma_f32_16x16x32_bf16 v[216:219], a[240:243], v[116:119], v[216:219]\n" \
"s_waitcnt lgkmcnt(4)\n" \
"ds_read_b128 v[112:115], v28 offset:28672\n" \
"v_and_b32 v55, 0xffff0000, v155\n" \
"v_mfma_f32_16x16x32_bf16 v[220:223], a[244:247], v[116:119], v[220:223]\n" \
"buffer_load_dwordx4 v31, s[36:39], s80 offen offset:1024 sc1 lds\n" \
"v_mfma_f32_16x16x32_bf16 v[208:211], a[248:251], v[120:123], v[208:211]\n" \
"ds_read_b128 v[116:119], v28 offset:29696\n" \
"v_lshlrev_b32 v188, 16, v141\n" \
"v_mfma_f32_16x16x32_bf16 v[212:215], a[252:255], v[120:123], v[212:215]\n" \
"v_and_b32 v189, 0xffff0000, v141\n" \
"v_mul_f32 v54, v178, v54\n" \
"v_mfma_f32_16x16x32_bf16 v[216:219], a[248:251], v[124:127], v[216:219]\n" \
"ds_read_b128 v[120:123], v28 offset:30720\n" \
"v_mul_f32 v55, v178, v55\n" \
"v_mfma_f32_16x16x32_bf16 v[220:223], a[252:255], v[124:127], v[220:223]\n" \
"s_waitcnt vmcnt(31) lgkmcnt(5)\n" \
"s_barrier\n" \
"v_mul_f32 v54, v188, v54\n" \
"v_mfma_f32_16x16x32_bf16 v[208:211], a[64:67], v[96:99], v[208:211]\n" \
"ds_read_b128 v[124:127], v28 offset:31744\n" \
"v_mul_f32 v55, v189, v55\n" \
"v_mfma_f32_16x16x32_bf16 v[212:215], a[68:71], v[96:99], v[212:215]\n" \
"buffer_load_dwordx4 v31, s[36:39], s80 offen offset:2048 sc1 lds\n" \
"v_mfma_f32_16x16x32_bf16 v[216:219], a[64:67], v[100:103], v[216:219]\n" \
"s_waitcnt lgkmcnt(4)\n" \
"ds_read_b128 v[96:99], v29 offset:49152\n" \
"v_permlane16_swap_b32 v156, v158\n" \
"v_mfma_f32_16x16x32_bf16 v[220:223], a[68:71], v[100:103], v[220:223]\n" \
"v_permlane16_swap_b32 v157, v159\n" \
"v_sub_f32 v8, 1.0, v179\n" \
"v_mfma_f32_16x16x32_bf16 v[208:211], a[72:75], v[104:107], v[208:211]\n" \
"ds_read_b128 v[100:103], v29 offset:50176\n" \
"v_cvt_pk_bf16_f32 v8, v8, v8\n" \
"v_mfma_f32_16x16x32_bf16 v[212:215], a[76:79], v[104:107], v[212:215]\n" \
"buffer_load_dwordx4 v31, s[36:39], s80 offen offset:3072 sc1 lds\n" \
"v_mfma_f32_16x16x32_bf16 v[216:219], a[72:75], v[108:111], v[216:219]\n" \
"s_waitcnt lgkmcnt(4)\n" \
"ds_read_b128 v[104:107], v29 offset:51200\n" \
"v_and_b32 v8, 0xffff0000, v8\n" \
"v_mfma_f32_16x16x32_bf16 v[220:223], a[76:79], v[108:111], v[220:223]\n" \
"v_lshlrev_b32 v56, 16, v156\n" \
"v_and_b32 v57, 0xffff0000, v156\n" \
"v_mfma_f32_16x16x32_bf16 v[208:211], a[80:83], v[112:115], v[208:211]\n" \
"ds_read_b128 v[108:111], v29 offset:52224\n" \
"v_lshlrev_b32 v188, 16, v138\n" \
"v_mfma_f32_16x16x32_bf16 v[212:215], a[84:87], v[112:115], v[212:215]\n" \
"v_and_b32 v189, 0xffff0000, v138\n" \
"v_mul_f32 v56, v179, v56\n" \
"v_mfma_f32_16x16x32_bf16 v[216:219], a[80:83], v[116:119], v[216:219]\n" \
"s_waitcnt lgkmcnt(4)\n" \
"ds_read_b128 v[112:115], v29 offset:53248\n" \
"v_mul_f32 v57, v179, v57\n" \
"v_mfma_f32_16x16x32_bf16 v[220:223], a[84:87], v[116:119], v[220:223]\n" \
"v_mul_f32 v56, v188, v56\n" \
"v_mul_f32 v57, v189, v57\n" \
"v_mfma_f32_16x16x32_bf16 v[208:211], a[88:91], v[120:123], v[208:211]\n" \
"ds_read_b128 v[116:119], v29 offset:54272\n" \
"v_lshlrev_b32 v58, 16, v157\n" \
"v_mfma_f32_16x16x32_bf16 v[212:215], a[92:95], v[120:123], v[212:215]\n" \
"v_and_b32 v59, 0xffff0000, v157\n" \
"v_lshlrev_b32 v188, 16, v139\n" \
"v_mfma_f32_16x16x32_bf16 v[216:219], a[88:91], v[124:127], v[216:219]\n" \
"s_waitcnt lgkmcnt(4)\n" \
"ds_read_b128 v[120:123], v29 offset:55296\n" \
"v_and_b32 v189, 0xffff0000, v139\n" \
"v_mfma_f32_16x16x32_bf16 v[220:223], a[92:95], v[124:127], v[220:223]\n" \
"v_mul_f32 v58, v179, v58\n" \
"v_mul_f32 v59, v179, v59\n" \
"v_mfma_f32_16x16x32_bf16 v[208:211], a[128:131], v[96:99], v[208:211]\n" \
"ds_read_b128 v[124:127], v29 offset:56320\n" \
"v_mul_f32 v58, v188, v58\n" \
"v_mfma_f32_16x16x32_bf16 v[212:215], a[132:135], v[96:99], v[212:215]\n" \
"v_mul_f32 v59, v189, v59\n" \
"v_lshlrev_b32 v60, 16, v158\n" \
"v_mfma_f32_16x16x32_bf16 v[216:219], a[128:131], v[100:103], v[216:219]\n" \
"s_waitcnt lgkmcnt(4)\n" \
"ds_read_b128 v[96:99], v29 offset:57344\n" \
"v_and_b32 v61, 0xffff0000, v158\n" \
"v_mfma_f32_16x16x32_bf16 v[220:223], a[132:135], v[100:103], v[220:223]\n" \
"v_lshlrev_b32 v188, 16, v142\n" \
"v_and_b32 v189, 0xffff0000, v142\n" \
"v_mfma_f32_16x16x32_bf16 v[208:211], a[136:139], v[104:107], v[208:211]\n" \
"ds_read_b128 v[100:103], v29 offset:58368\n" \
"v_mul_f32 v60, v179, v60\n" \
"v_mfma_f32_16x16x32_bf16 v[212:215], a[140:143], v[104:107], v[212:215]\n" \
"v_mul_f32 v61, v179, v61\n" \
"v_mul_f32 v60, v188, v60\n" \
"v_mfma_f32_16x16x32_bf16 v[216:219], a[136:139], v[108:111], v[216:219]\n" \
"s_waitcnt lgkmcnt(4)\n" \
"ds_read_b128 v[104:107], v29 offset:59392\n" \
"v_mul_f32 v61, v189, v61\n" \
"v_mfma_f32_16x16x32_bf16 v[220:223], a[140:143], v[108:111], v[220:223]\n" \
"v_lshlrev_b32 v62, 16, v159\n" \
"v_and_b32 v63, 0xffff0000, v159\n" \
"v_mfma_f32_16x16x32_bf16 v[208:211], a[144:147], v[112:115], v[208:211]\n" \
"ds_read_b128 v[108:111], v29 offset:60416\n" \
"v_lshlrev_b32 v188, 16, v143\n" \
"v_mfma_f32_16x16x32_bf16 v[212:215], a[148:151], v[112:115], v[212:215]\n" \
"v_and_b32 v189, 0xffff0000, v143\n" \
"v_mul_f32 v62, v179, v62\n" \
"v_mfma_f32_16x16x32_bf16 v[216:219], a[144:147], v[116:119], v[216:219]\n" \
"s_waitcnt lgkmcnt(4)\n" \
"ds_read_b128 v[112:115], v29 offset:61440\n" \
"v_mul_f32 v63, v179, v63\n" \
"v_mfma_f32_16x16x32_bf16 v[220:223], a[148:151], v[116:119], v[220:223]\n" \
"s_add_u32 s80, s71, 16384\n" \
"s_add_u32 m0, s22, 0\n" \
"v_mul_f32 v62, v188, v62\n" \
"v_mfma_f32_16x16x32_bf16 v[208:211], a[152:155], v[120:123], v[208:211]\n" \
"ds_read_b128 v[116:119], v29 offset:62464\n" \
"v_mul_f32 v63, v189, v63\n" \
"v_mfma_f32_16x16x32_bf16 v[212:215], a[156:159], v[120:123], v[212:215]\n" \
"v_mfma_f32_16x16x32_bf16 v[216:219], a[152:155], v[124:127], v[216:219]\n" \
"ds_read_b128 v[120:123], v29 offset:63488\n" \
"v_mfma_f32_16x16x32_bf16 v[220:223], a[156:159], v[124:127], v[220:223]\n" \
"s_waitcnt vmcnt(27) lgkmcnt(5)\n" \
"s_barrier\n" \
"v_mfma_f32_16x16x32_bf16 v[208:211], a[0:3], v[96:99], v[208:211]\n" \
"ds_read_b128 v[124:127], v29 offset:64512\n" \
"v_mfma_f32_16x16x32_bf16 v[212:215], a[4:7], v[96:99], v[212:215]\n" \
"v_mfma_f32_16x16x32_bf16 v[216:219], a[0:3], v[100:103], v[216:219]\n" \
"s_waitcnt lgkmcnt(4)\n" \
"ds_read_b128 v[96:99], v30 offset:16384\n" \
"v_mfma_f32_16x16x32_bf16 v[220:223], a[4:7], v[100:103], v[220:223]\n" \
"buffer_load_dwordx4 v31, s[36:39], s80 offen offset:0 sc1 lds\n" \
"v_mfma_f32_16x16x32_bf16 v[208:211], a[8:11], v[104:107], v[208:211]\n" \
"ds_read_b128 v[100:103], v30 offset:17408\n" \
"v_mfma_f32_16x16x32_bf16 v[212:215], a[12:15], v[104:107], v[212:215]\n" \
"v_mfma_f32_16x16x32_bf16 v[216:219], a[8:11], v[108:111], v[216:219]\n" \
"s_waitcnt lgkmcnt(4)\n" \
"ds_read_b128 v[104:107], v30 offset:18432\n" \
"v_mfma_f32_16x16x32_bf16 v[220:223], a[12:15], v[108:111], v[220:223]\n" \
"buffer_load_dwordx4 v31, s[36:39], s80 offen offset:1024 sc1 lds\n" \
"v_mfma_f32_16x16x32_bf16 v[208:211], a[16:19], v[112:115], v[208:211]\n" \
"ds_read_b128 v[108:111], v30 offset:19456\n" \
"v_mfma_f32_16x16x32_bf16 v[212:215], a[20:23], v[112:115], v[212:215]\n" \
"v_mfma_f32_16x16x32_bf16 v[216:219], a[16:19], v[116:119], v[216:219]\n" \
"s_waitcnt lgkmcnt(4)\n" \
"ds_read_b128 v[112:115], v30 offset:20480\n" \
"v_mfma_f32_16x16x32_bf16 v[220:223], a[20:23], v[116:119], v[220:223]\n" \
"buffer_load_dwordx4 v31, s[36:39], s80 offen offset:2048 sc1 lds\n" \
"v_mfma_f32_16x16x32_bf16 v[208:211], a[24:27], v[120:123], v[208:211]\n" \
"ds_read_b128 v[116:119], v30 offset:21504\n" \
"v_mfma_f32_16x16x32_bf16 v[212:215], a[28:31], v[120:123], v[212:215]\n" \
"v_mfma_f32_16x16x32_bf16 v[216:219], a[24:27], v[124:127], v[216:219]\n" \
"s_waitcnt lgkmcnt(4)\n" \
"ds_read_b128 v[120:123], v30 offset:22528\n" \
"v_mfma_f32_16x16x32_bf16 v[220:223], a[28:31], v[124:127], v[220:223]\n" \
"buffer_load_dwordx4 v31, s[36:39], s80 offen offset:3072 sc1 lds\n" \
"v_mfma_f32_16x16x32_bf16 v[208:211], a[32:35], v[96:99], v[208:211]\n" \
"ds_read_b128 v[124:127], v30 offset:23552\n" \
"v_mfma_f32_16x16x32_bf16 v[212:215], a[36:39], v[96:99], v[212:215]\n" \
"v_mfma_f32_16x16x32_bf16 v[216:219], a[32:35], v[100:103], v[216:219]\n" \
"s_waitcnt lgkmcnt(4)\n" \
"ds_read_b128 v[96:99], v30 offset:24576\n" \
"v_mfma_f32_16x16x32_bf16 v[220:223], a[36:39], v[100:103], v[220:223]\n" \
"v_mfma_f32_16x16x32_bf16 v[208:211], a[40:43], v[104:107], v[208:211]\n" \
"ds_read_b128 v[100:103], v30 offset:25600\n" \
"v_mfma_f32_16x16x32_bf16 v[212:215], a[44:47], v[104:107], v[212:215]\n" \
"v_mfma_f32_16x16x32_bf16 v[216:219], a[40:43], v[108:111], v[216:219]\n" \
"s_waitcnt lgkmcnt(4)\n" \
"ds_read_b128 v[104:107], v30 offset:26624\n" \
"v_mfma_f32_16x16x32_bf16 v[220:223], a[44:47], v[108:111], v[220:223]\n" \
"v_mfma_f32_16x16x32_bf16 v[208:211], a[48:51], v[112:115], v[208:211]\n" \
"ds_read_b128 v[108:111], v30 offset:27648\n" \
"v_mfma_f32_16x16x32_bf16 v[212:215], a[52:55], v[112:115], v[212:215]\n" \
"v_mfma_f32_16x16x32_bf16 v[216:219], a[48:51], v[116:119], v[216:219]\n" \
"s_waitcnt lgkmcnt(4)\n" \
"ds_read_b128 v[112:115], v30 offset:28672\n" \
"v_mfma_f32_16x16x32_bf16 v[220:223], a[52:55], v[116:119], v[220:223]\n" \
"s_add_u32 s80, s71, 32768\n" \
"s_add_u32 m0, s22, 49152\n" \
"v_mfma_f32_16x16x32_bf16 v[208:211], a[56:59], v[120:123], v[208:211]\n" \
"s_sub_u32 s96, s88, 94404608\n" \
"ds_read_b128 v[116:119], v30 offset:29696\n" \
"s_lshl_b32 s96, s96, 2\n" \
"v_mfma_f32_16x16x32_bf16 v[212:215], a[60:63], v[120:123], v[212:215]\n" \
"s_add_u32 s96, s96, 94437376\n" \
"v_bfe_u32 v17, v4, 4, 2\n" \
"v_min_u32 v17, 2, v17\n" \
"v_mfma_f32_16x16x32_bf16 v[216:219], a[56:59], v[124:127], v[216:219]\n" \
"ds_read_b128 v[120:123], v30 offset:30720\n" \
"v_lshl_add_u32 v17, v17, 3, s96\n" \
"v_mfma_f32_16x16x32_bf16 v[220:223], a[60:63], v[124:127], v[220:223]\n" \
"s_waitcnt vmcnt(27) lgkmcnt(5)\n" \
"s_barrier\n" \
"v_mfma_f32_16x16x32_bf16 v[208:211], a[96:99], v[96:99], v[208:211]\n" \
"ds_read_b128 v[124:127], v30 offset:31744\n" \
"v_mfma_f32_16x16x32_bf16 v[212:215], a[100:103], v[96:99], v[212:215]\n" \
"v_mfma_f32_16x16x32_bf16 v[216:219], a[96:99], v[100:103], v[216:219]\n" \
"s_waitcnt lgkmcnt(4)\n" \
"ds_read_b128 v[96:99], v28 offset:32768\n" \
"v_mfma_f32_16x16x32_bf16 v[220:223], a[100:103], v[100:103], v[220:223]\n" \
"buffer_load_dwordx4 v31, s[36:39], s80 offen offset:0 sc1 lds\n" \
"v_mfma_f32_16x16x32_bf16 v[208:211], a[104:107], v[104:107], v[208:211]\n" \
"ds_read_b128 v[100:103], v28 offset:33792\n" \
"v_mfma_f32_16x16x32_bf16 v[212:215], a[108:111], v[104:107], v[212:215]\n" \
"v_mfma_f32_16x16x32_bf16 v[216:219], a[104:107], v[108:111], v[216:219]\n" \
"s_waitcnt lgkmcnt(4)\n" \
"ds_read_b128 v[104:107], v28 offset:34816\n" \
"v_mfma_f32_16x16x32_bf16 v[220:223], a[108:111], v[108:111], v[220:223]\n" \
"buffer_load_dwordx4 v31, s[36:39], s80 offen offset:1024 sc1 lds\n" \
"v_mfma_f32_16x16x32_bf16 v[208:211], a[112:115], v[112:115], v[208:211]\n" \
"ds_read_b128 v[108:111], v28 offset:35840\n" \
"v_mfma_f32_16x16x32_bf16 v[212:215], a[116:119], v[112:115], v[212:215]\n" \
"v_mfma_f32_16x16x32_bf16 v[216:219], a[112:115], v[116:119], v[216:219]\n" \
"s_waitcnt lgkmcnt(4)\n" \
"ds_read_b128 v[112:115], v28 offset:36864\n" \
"v_mfma_f32_16x16x32_bf16 v[220:223], a[116:119], v[116:119], v[220:223]\n" \
"buffer_load_dwordx2 v[18:19], v17, s[36:39], 0 offen sc1\n" \
"v_mfma_f32_16x16x32_bf16 v[208:211], a[120:123], v[120:123], v[208:211]\n" \
"ds_read_b128 v[116:119], v28 offset:37888\n" \
"v_mfma_f32_16x16x32_bf16 v[212:215], a[124:127], v[120:123], v[212:215]\n" \
"v_mfma_f32_16x16x32_bf16 v[216:219], a[120:123], v[124:127], v[216:219]\n" \
"s_waitcnt lgkmcnt(4)\n" \
"ds_read_b128 v[120:123], v28 offset:38912\n" \
"v_mfma_f32_16x16x32_bf16 v[220:223], a[124:127], v[124:127], v[220:223]\n" \
"buffer_load_dwordx4 v31, s[36:39], s80 offen offset:2048 sc1 lds\n" \
"v_mfma_f32_16x16x32_bf16 v[192:195], a[160:163], v[96:99], 0\n" \
"ds_read_b128 v[124:127], v28 offset:39936\n" \
"v_mfma_f32_16x16x32_bf16 v[196:199], a[164:167], v[96:99], 0\n" \
"v_mfma_f32_16x16x32_bf16 v[200:203], a[160:163], v[100:103], 0\n" \
"s_waitcnt lgkmcnt(4)\n" \
"ds_read_b128 v[96:99], v28 offset:40960\n" \
"v_cvt_pk_bf16_f32 v208, v208, v209\n" \
"v_mfma_f32_16x16x32_bf16 v[204:207], a[164:167], v[100:103], 0\n" \
"buffer_load_dwordx4 v31, s[36:39], s80 offen offset:3072 sc1 lds\n" \
"v_mfma_f32_16x16x32_bf16 v[192:195], a[168:171], v[104:107], v[192:195]\n" \
"ds_read_b128 v[100:103], v28 offset:41984\n" \
"v_cvt_pk_bf16_f32 v209, v210, v211\n" \
"v_mfma_f32_16x16x32_bf16 v[196:199], a[172:175], v[104:107], v[196:199]\n" \
"v_cvt_pk_bf16_f32 v210, v212, v213\n" \
"v_cvt_pk_bf16_f32 v211, v214, v215\n" \
"v_mfma_f32_16x16x32_bf16 v[200:203], a[168:171], v[108:111], v[200:203]\n" \
"s_waitcnt lgkmcnt(4)\n" \
"ds_read_b128 v[104:107], v28 offset:43008\n" \
"v_cvt_pk_bf16_f32 v216, v216, v217\n" \
"v_mfma_f32_16x16x32_bf16 v[204:207], a[172:175], v[108:111], v[204:207]\n" \
"buffer_store_dwordx4 v[208:211], v4, s[36:39], s76 offen offset:0 sc1\n" \
"v_mfma_f32_16x16x32_bf16 v[192:195], a[176:179], v[112:115], v[192:195]\n" \
"ds_read_b128 v[108:111], v28 offset:44032\n" \
"v_cvt_pk_bf16_f32 v217, v218, v219\n" \
"v_mfma_f32_16x16x32_bf16 v[196:199], a[180:183], v[112:115], v[196:199]\n" \
"v_cvt_pk_bf16_f32 v218, v220, v221\n" \
"v_cvt_pk_bf16_f32 v219, v222, v223\n" \
"v_mfma_f32_16x16x32_bf16 v[200:203], a[176:179], v[116:119], v[200:203]\n" \
"s_waitcnt lgkmcnt(4)\n" \
"ds_read_b128 v[112:115], v28 offset:45056\n" \
"v_mfma_f32_16x16x32_bf16 v[204:207], a[180:183], v[116:119], v[204:207]\n" \
"s_add_u32 s80, s71, 49152\n" \
"s_add_u32 m0, s22, 16384\n" \
"v_mfma_f32_16x16x32_bf16 v[192:195], a[184:187], v[120:123], v[192:195]\n" \
"ds_read_b128 v[116:119], v28 offset:46080\n" \
"v_mfma_f32_16x16x32_bf16 v[196:199], a[188:191], v[120:123], v[196:199]\n" \
"v_mfma_f32_16x16x32_bf16 v[200:203], a[184:187], v[124:127], v[200:203]\n" \
"ds_read_b128 v[120:123], v28 offset:47104\n" \
"v_mfma_f32_16x16x32_bf16 v[204:207], a[188:191], v[124:127], v[204:207]\n" \
"s_waitcnt vmcnt(26) lgkmcnt(5)\n" \
"s_barrier\n" \
"v_mfma_f32_16x16x32_bf16 v[192:195], a[192:195], v[96:99], v[192:195]\n" \
"ds_read_b128 v[124:127], v28 offset:48128\n" \
"v_mfma_f32_16x16x32_bf16 v[196:199], a[196:199], v[96:99], v[196:199]\n" \
"buffer_store_dwordx4 v[216:219], v4, s[36:39], s76 offen offset:1024 sc1\n" \
"v_mfma_f32_16x16x32_bf16 v[200:203], a[192:195], v[100:103], v[200:203]\n" \
"s_waitcnt lgkmcnt(4)\n" \
"ds_read_b128 v[96:99], v29 offset:32768\n" \
"v_mfma_f32_16x16x32_bf16 v[204:207], a[196:199], v[100:103], v[204:207]\n" \
"v_mfma_f32_16x16x32_bf16 v[192:195], a[200:203], v[104:107], v[192:195]\n" \
"ds_read_b128 v[100:103], v29 offset:33792\n" \
"v_mfma_f32_16x16x32_bf16 v[196:199], a[204:207], v[104:107], v[196:199]\n" \
"buffer_load_dwordx4 v31, s[36:39], s80 offen offset:0 sc1 lds\n" \
"v_mfma_f32_16x16x32_bf16 v[200:203], a[200:203], v[108:111], v[200:203]\n" \
"s_waitcnt lgkmcnt(4)\n" \
"ds_read_b128 v[104:107], v29 offset:34816\n" \
"v_mfma_f32_16x16x32_bf16 v[204:207], a[204:207], v[108:111], v[204:207]\n" \
"v_mfma_f32_16x16x32_bf16 v[192:195], a[208:211], v[112:115], v[192:195]\n" \
"ds_read_b128 v[108:111], v29 offset:35840\n" \
"v_mfma_f32_16x16x32_bf16 v[196:199], a[212:215], v[112:115], v[196:199]\n" \
"buffer_load_dwordx4 v31, s[36:39], s80 offen offset:1024 sc1 lds\n" \
"v_mfma_f32_16x16x32_bf16 v[200:203], a[208:211], v[116:119], v[200:203]\n" \
"s_waitcnt lgkmcnt(4)\n" \
"ds_read_b128 v[112:115], v29 offset:36864\n" \
"v_mfma_f32_16x16x32_bf16 v[204:207], a[212:215], v[116:119], v[204:207]\n" \
"v_mfma_f32_16x16x32_bf16 v[192:195], a[216:219], v[120:123], v[192:195]\n" \
"ds_read_b128 v[116:119], v29 offset:37888\n" \
"v_mfma_f32_16x16x32_bf16 v[196:199], a[220:223], v[120:123], v[196:199]\n" \
"buffer_load_dwordx4 v31, s[36:39], s80 offen offset:2048 sc1 lds\n" \
"v_mfma_f32_16x16x32_bf16 v[200:203], a[216:219], v[124:127], v[200:203]\n" \
"s_waitcnt lgkmcnt(4)\n" \
"ds_read_b128 v[120:123], v29 offset:38912\n" \
"v_mfma_f32_16x16x32_bf16 v[204:207], a[220:223], v[124:127], v[204:207]\n" \
"v_mfma_f32_16x16x32_bf16 v[192:195], a[224:227], v[96:99], v[192:195]\n" \
"ds_read_b128 v[124:127], v29 offset:39936\n" \
"v_mfma_f32_16x16x32_bf16 v[196:199], a[228:231], v[96:99], v[196:199]\n" \
"buffer_load_dwordx4 v31, s[36:39], s80 offen offset:3072 sc1 lds\n" \
"v_mfma_f32_16x16x32_bf16 v[200:203], a[224:227], v[100:103], v[200:203]\n" \
"s_waitcnt lgkmcnt(4)\n" \
"ds_read_b128 v[96:99], v29 offset:40960\n" \
"v_mfma_f32_16x16x32_bf16 v[204:207], a[228:231], v[100:103], v[204:207]\n" \
"v_mfma_f32_16x16x32_bf16 v[192:195], a[232:235], v[104:107], v[192:195]\n" \
"ds_read_b128 v[100:103], v29 offset:41984\n" \
"v_mfma_f32_16x16x32_bf16 v[196:199], a[236:239], v[104:107], v[196:199]\n" \
"v_mfma_f32_16x16x32_bf16 v[200:203], a[232:235], v[108:111], v[200:203]\n" \
"s_waitcnt lgkmcnt(4)\n" \
"ds_read_b128 v[104:107], v29 offset:43008\n" \
"s_waitcnt vmcnt(8)\n" \
"v_mfma_f32_16x16x32_bf16 v[204:207], a[236:239], v[108:111], v[204:207]\n" \
"v_cmp_ne_u64 vcc, s[14:15], v[18:19]\n" \
"s_nop 0\n" \
"v_mfma_f32_16x16x32_bf16 v[192:195], a[240:243], v[112:115], v[192:195]\n" \
"ds_read_b128 v[108:111], v29 offset:44032\n" \
"s_cbranch_vccnz LPE_%=\nLPER_%=:\n" \
"v_mfma_f32_16x16x32_bf16 v[196:199], a[244:247], v[112:115], v[196:199]\n" \
"s_add_u32 s97, s78, 0\n" \
"v_mfma_f32_16x16x32_bf16 v[200:203], a[240:243], v[116:119], v[200:203]\n" \
"s_waitcnt lgkmcnt(4)\n" \
"ds_read_b128 v[112:115], v29 offset:45056\n" \
"v_mfma_f32_16x16x32_bf16 v[204:207], a[244:247], v[116:119], v[204:207]\n" \
"buffer_load_dwordx4 v[128:131], v4, s[36:39], s97 offen offset:0 sc1\n" \
"v_mfma_f32_16x16x32_bf16 v[192:195], a[248:251], v[120:123], v[192:195]\n" \
"ds_read_b128 v[116:119], v29 offset:46080\n" \
"v_mfma_f32_16x16x32_bf16 v[196:199], a[252:255], v[120:123], v[196:199]\n" \
"v_mfma_f32_16x16x32_bf16 v[200:203], a[248:251], v[124:127], v[200:203]\n" \
"ds_read_b128 v[120:123], v29 offset:47104\n" \
"v_mfma_f32_16x16x32_bf16 v[204:207], a[252:255], v[124:127], v[204:207]\n" \
"s_waitcnt vmcnt(27) lgkmcnt(5)\n" \
"s_barrier\n" \
"v_mfma_f32_16x16x32_bf16 v[192:195], a[64:67], v[96:99], v[192:195]\n" \
"ds_read_b128 v[124:127], v29 offset:48128\n" \
"v_mfma_f32_16x16x32_bf16 v[196:199], a[68:71], v[96:99], v[196:199]\n" \
"buffer_load_dwordx4 v[132:135], v4, s[36:39], s97 offen offset:1024 sc1\n" \
"v_mfma_f32_16x16x32_bf16 v[200:203], a[64:67], v[100:103], v[200:203]\n" \
"s_waitcnt lgkmcnt(4)\n" \
"ds_read_b128 v[96:99], v29 offset:0\n" \
"v_mfma_f32_16x16x32_bf16 v[204:207], a[68:71], v[100:103], v[204:207]\n" \
"v_mfma_f32_16x16x32_bf16 v[192:195], a[72:75], v[104:107], v[192:195]\n" \
"ds_read_b128 v[100:103], v29 offset:1024\n" \
"v_mfma_f32_16x16x32_bf16 v[196:199], a[76:79], v[104:107], v[196:199]\n" \
"buffer_load_dwordx4 v[136:139], v4, s[36:39], s97 offen offset:2048 sc1\n" \
"v_mfma_f32_16x16x32_bf16 v[200:203], a[72:75], v[108:111], v[200:203]\n" \
"s_waitcnt lgkmcnt(4)\n" \
"ds_read_b128 v[104:107], v29 offset:2048\n" \
"v_mfma_f32_16x16x32_bf16 v[204:207], a[76:79], v[108:111], v[204:207]\n" \
"v_mfma_f32_16x16x32_bf16 v[192:195], a[80:83], v[112:115], v[192:195]\n" \
"ds_read_b128 v[108:111], v29 offset:3072\n" \
"v_mfma_f32_16x16x32_bf16 v[196:199], a[84:87], v[112:115], v[196:199]\n" \
"buffer_load_dwordx4 v[140:143], v4, s[36:39], s97 offen offset:3072 sc1\n" \
"v_mfma_f32_16x16x32_bf16 v[200:203], a[80:83], v[116:119], v[200:203]\n" \
"s_waitcnt lgkmcnt(4)\n" \
"ds_read_b128 v[112:115], v29 offset:4096\n" \
"s_add_u32 s97, s78, 4096\n" \
"v_mfma_f32_16x16x32_bf16 v[204:207], a[84:87], v[116:119], v[204:207]\n" \
"v_mfma_f32_16x16x32_bf16 v[192:195], a[88:91], v[120:123], v[192:195]\n" \
"ds_read_b128 v[116:119], v29 offset:5120\n" \
"v_mfma_f32_16x16x32_bf16 v[196:199], a[92:95], v[120:123], v[196:199]\n" \
"buffer_load_dwordx4 v[160:163], v4, s[36:39], s97 offen offset:0 sc1\n" \
"v_mfma_f32_16x16x32_bf16 v[200:203], a[88:91], v[124:127], v[200:203]\n" \
"s_waitcnt lgkmcnt(4)\n" \
"ds_read_b128 v[120:123], v29 offset:6144\n" \
"v_mfma_f32_16x16x32_bf16 v[204:207], a[92:95], v[124:127], v[204:207]\n" \
"v_mfma_f32_16x16x32_bf16 v[192:195], a[128:131], v[96:99], v[192:195]\n" \
"ds_read_b128 v[124:127], v29 offset:7168\n" \
"v_mfma_f32_16x16x32_bf16 v[196:199], a[132:135], v[96:99], v[196:199]\n" \
"buffer_load_dwordx4 v[164:167], v4, s[36:39], s97 offen offset:1024 sc1\n" \
"v_mfma_f32_16x16x32_bf16 v[200:203], a[128:131], v[100:103], v[200:203]\n" \
"s_waitcnt lgkmcnt(4)\n" \
"ds_read_b128 v[96:99], v29 offset:8192\n" \
"v_mfma_f32_16x16x32_bf16 v[204:207], a[132:135], v[100:103], v[204:207]\n" \
"v_mfma_f32_16x16x32_bf16 v[192:195], a[136:139], v[104:107], v[192:195]\n" \
"ds_read_b128 v[100:103], v29 offset:9216\n" \
"v_mfma_f32_16x16x32_bf16 v[196:199], a[140:143], v[104:107], v[196:199]\n" \
"v_mfma_f32_16x16x32_bf16 v[200:203], a[136:139], v[108:111], v[200:203]\n" \
"s_waitcnt lgkmcnt(4)\n" \
"ds_read_b128 v[104:107], v29 offset:10240\n" \
"v_mfma_f32_16x16x32_bf16 v[204:207], a[140:143], v[108:111], v[204:207]\n" \
"v_mfma_f32_16x16x32_bf16 v[192:195], a[144:147], v[112:115], v[192:195]\n" \
"ds_read_b128 v[108:111], v29 offset:11264\n" \
"v_mfma_f32_16x16x32_bf16 v[196:199], a[148:151], v[112:115], v[196:199]\n" \
"v_mfma_f32_16x16x32_bf16 v[200:203], a[144:147], v[116:119], v[200:203]\n" \
"s_waitcnt lgkmcnt(4)\n" \
"ds_read_b128 v[112:115], v29 offset:12288\n" \
"v_mfma_f32_16x16x32_bf16 v[204:207], a[148:151], v[116:119], v[204:207]\n" \
"v_mfma_f32_16x16x32_bf16 v[192:195], a[152:155], v[120:123], v[192:195]\n" \
"ds_read_b128 v[116:119], v29 offset:13312\n" \
"v_mfma_f32_16x16x32_bf16 v[196:199], a[156:159], v[120:123], v[196:199]\n" \
"v_mfma_f32_16x16x32_bf16 v[200:203], a[152:155], v[124:127], v[200:203]\n" \
"ds_read_b128 v[120:123], v29 offset:14336\n" \
"v_mfma_f32_16x16x32_bf16 v[204:207], a[156:159], v[124:127], v[204:207]\n" \
"s_waitcnt vmcnt(26) lgkmcnt(5)\n" \
"s_barrier\n" \
"v_mfma_f32_16x16x32_bf16 v[192:195], a[0:3], v[96:99], v[192:195]\n" \
"ds_read_b128 v[124:127], v29 offset:15360\n" \
"v_mfma_f32_16x16x32_bf16 v[196:199], a[4:7], v[96:99], v[196:199]\n" \
"v_mfma_f32_16x16x32_bf16 v[200:203], a[0:3], v[100:103], v[200:203]\n" \
"s_waitcnt lgkmcnt(4)\n" \
"ds_read_b128 v[96:99], v29 offset:16384\n" \
"v_mfma_f32_16x16x32_bf16 v[204:207], a[4:7], v[100:103], v[204:207]\n" \
"v_mfma_f32_16x16x32_bf16 v[192:195], a[8:11], v[104:107], v[192:195]\n" \
"ds_read_b128 v[100:103], v29 offset:17408\n" \
"v_mfma_f32_16x16x32_bf16 v[196:199], a[12:15], v[104:107], v[196:199]\n" \
"v_mfma_f32_16x16x32_bf16 v[200:203], a[8:11], v[108:111], v[200:203]\n" \
"s_waitcnt lgkmcnt(4)\n" \
"ds_read_b128 v[104:107], v29 offset:18432\n" \
"v_mfma_f32_16x16x32_bf16 v[204:207], a[12:15], v[108:111], v[204:207]\n" \
"v_mfma_f32_16x16x32_bf16 v[192:195], a[16:19], v[112:115], v[192:195]\n" \
"ds_read_b128 v[108:111], v29 offset:19456\n" \
"v_mfma_f32_16x16x32_bf16 v[196:199], a[20:23], v[112:115], v[196:199]\n" \
"v_mfma_f32_16x16x32_bf16 v[200:203], a[16:19], v[116:119], v[200:203]\n" \
"s_waitcnt lgkmcnt(4)\n" \
"ds_read_b128 v[112:115], v29 offset:20480\n" \
"v_mfma_f32_16x16x32_bf16 v[204:207], a[20:23], v[116:119], v[204:207]\n" \
"v_mfma_f32_16x16x32_bf16 v[192:195], a[24:27], v[120:123], v[192:195]\n" \
"ds_read_b128 v[116:119], v29 offset:21504\n" \
"v_mfma_f32_16x16x32_bf16 v[196:199], a[28:31], v[120:123], v[196:199]\n" \
"v_mfma_f32_16x16x32_bf16 v[200:203], a[24:27], v[124:127], v[200:203]\n" \
"s_waitcnt lgkmcnt(4)\n" \
"ds_read_b128 v[120:123], v29 offset:22528\n" \
"v_mfma_f32_16x16x32_bf16 v[204:207], a[28:31], v[124:127], v[204:207]\n" \
"v_mfma_f32_16x16x32_bf16 v[192:195], a[32:35], v[96:99], v[192:195]\n" \
"ds_read_b128 v[124:127], v29 offset:23552\n" \
"v_mfma_f32_16x16x32_bf16 v[196:199], a[36:39], v[96:99], v[196:199]\n" \
"v_mfma_f32_16x16x32_bf16 v[200:203], a[32:35], v[100:103], v[200:203]\n" \
"s_waitcnt lgkmcnt(4)\n" \
"ds_read_b128 v[96:99], v29 offset:24576\n" \
"v_mfma_f32_16x16x32_bf16 v[204:207], a[36:39], v[100:103], v[204:207]\n" \
"v_mfma_f32_16x16x32_bf16 v[192:195], a[40:43], v[104:107], v[192:195]\n" \
"ds_read_b128 v[100:103], v29 offset:25600\n" \
"v_mfma_f32_16x16x32_bf16 v[196:199], a[44:47], v[104:107], v[196:199]\n" \
"v_mfma_f32_16x16x32_bf16 v[200:203], a[40:43], v[108:111], v[200:203]\n" \
"s_waitcnt lgkmcnt(4)\n" \
"ds_read_b128 v[104:107], v29 offset:26624\n" \
"v_mfma_f32_16x16x32_bf16 v[204:207], a[44:47], v[108:111], v[204:207]\n" \
"v_mfma_f32_16x16x32_bf16 v[192:195], a[48:51], v[112:115], v[192:195]\n" \
"ds_read_b128 v[108:111], v29 offset:27648\n" \
"v_mfma_f32_16x16x32_bf16 v[196:199], a[52:55], v[112:115], v[196:199]\n" \
"v_mfma_f32_16x16x32_bf16 v[200:203], a[48:51], v[116:119], v[200:203]\n" \
"s_waitcnt lgkmcnt(4)\n" \
"ds_read_b128 v[112:115], v29 offset:28672\n" \
"v_mfma_f32_16x16x32_bf16 v[204:207], a[52:55], v[116:119], v[204:207]\n" \
"v_mfma_f32_16x16x32_bf16 v[192:195], a[56:59], v[120:123], v[192:195]\n" \
"ds_read_b128 v[116:119], v29 offset:29696\n" \
"v_mfma_f32_16x16x32_bf16 v[196:199], a[60:63], v[120:123], v[196:199]\n" \
"v_mfma_f32_16x16x32_bf16 v[200:203], a[56:59], v[124:127], v[200:203]\n" \
"ds_read_b128 v[120:123], v29 offset:30720\n" \
"v_mfma_f32_16x16x32_bf16 v[204:207], a[60:63], v[124:127], v[204:207]\n" \
"s_waitcnt vmcnt(21) lgkmcnt(5)\n" \
"s_barrier\n" \
"v_mfma_f32_16x16x32_bf16 v[192:195], a[96:99], v[96:99], v[192:195]\n" \
"ds_read_b128 v[124:127], v29 offset:31744\n" \
"v_mfma_f32_16x16x32_bf16 v[196:199], a[100:103], v[96:99], v[196:199]\n" \
"v_mfma_f32_16x16x32_bf16 v[200:203], a[96:99], v[100:103], v[200:203]\n" \
"s_waitcnt lgkmcnt(4)\n" \
"ds_read_b128 v[96:99], v30 offset:0\n" \
"v_mfma_f32_16x16x32_bf16 v[204:207], a[100:103], v[100:103], v[204:207]\n" \
"v_mfma_f32_16x16x32_bf16 v[192:195], a[104:107], v[104:107], v[192:195]\n" \
"ds_read_b128 v[100:103], v30 offset:1024\n" \
"v_mfma_f32_16x16x32_bf16 v[196:199], a[108:111], v[104:107], v[196:199]\n" \
"v_mfma_f32_16x16x32_bf16 v[200:203], a[104:107], v[108:111], v[200:203]\n" \
"s_waitcnt lgkmcnt(4)\n" \
"ds_read_b128 v[104:107], v30 offset:2048\n" \
"v_mfma_f32_16x16x32_bf16 v[204:207], a[108:111], v[108:111], v[204:207]\n" \
"v_mfma_f32_16x16x32_bf16 v[192:195], a[112:115], v[112:115], v[192:195]\n" \
"LH25_%=:\ns_cbranch_execz LHR2_%=\n" \
"ds_read_b128 v[108:111], v30 offset:3072\n" \
"v_mfma_f32_16x16x32_bf16 v[196:199], a[116:119], v[112:115], v[196:199]\n" \
"v_mfma_f32_16x16x32_bf16 v[200:203], a[112:115], v[116:119], v[200:203]\n" \
"s_waitcnt lgkmcnt(4)\n" \
"ds_read_b128 v[112:115], v30 offset:4096\n" \
"v_mfma_f32_16x16x32_bf16 v[204:207], a[116:119], v[116:119], v[204:207]\n" \
"v_mfma_f32_16x16x32_bf16 v[192:195], a[120:123], v[120:123], v[192:195]\n" \
"ds_read_b128 v[116:119], v30 offset:5120\n" \
"v_mfma_f32_16x16x32_bf16 v[196:199], a[124:127], v[120:123], v[196:199]\n" \
"v_mfma_f32_16x16x32_bf16 v[200:203], a[120:123], v[124:127], v[200:203]\n" \
"ds_read_b128 v[120:123], v30 offset:6144\n" \
"v_mfma_f32_16x16x32_bf16 v[204:207], a[124:127], v[124:127], v[204:207]\n" \
"s_waitcnt vmcnt(0)\n" \
"v_mfma_f32_16x16x32_bf16 v[64:67], v[24:27], v[128:131], v[64:67]\n" \
"v_mfma_f32_16x16x32_bf16 v[68:71], v[22:25], v[128:131], v[68:71]\n" \
"v_mfma_f32_16x16x32_bf16 v[72:75], v[24:27], v[132:135], v[72:75]\n" \
"v_cvt_pk_bf16_f32 v192, v192, v193\n" \
"v_cvt_pk_bf16_f32 v193, v194, v195\n" \
"v_mfma_f32_16x16x32_bf16 v[76:79], v[22:25], v[132:135], v[76:79]\n" \
"v_cvt_pk_bf16_f32 v194, v196, v197\n" \
"v_cvt_pk_bf16_f32 v195, v198, v199\n" \
"v_mfma_f32_16x16x32_bf16 v[64:67], v[24:27], v[136:139], v[64:67]\n" \
"buffer_store_dwordx4 v[192:195], v4, s[36:39], s77 offen offset:0 sc1\n" \
"v_mfma_f32_16x16x32_bf16 v[68:71], v[22:25], v[136:139], v[68:71]\n" \
"v_cvt_pk_bf16_f32 v200, v200, v201\n" \
"v_cvt_pk_bf16_f32 v201, v202, v203\n" \
"v_mfma_f32_16x16x32_bf16 v[72:75], v[24:27], v[140:143], v[72:75]\n" \
"v_cvt_pk_bf16_f32 v202, v204, v205\n" \
"v_cvt_pk_bf16_f32 v203, v206, v207\n" \
"v_mfma_f32_16x16x32_bf16 v[76:79], v[22:25], v[140:143], v[76:79]\n" \
"v_mfma_f32_16x16x32_bf16 v[64:67], v[24:27], v[160:163], v[64:67]\n" \
"v_mfma_f32_16x16x32_bf16 v[68:71], v[22:25], v[160:163], v[68:71]\n" \
"v_mfma_f32_16x16x32_bf16 v[72:75], v[24:27], v[164:167], v[72:75]\n" \
"s_waitcnt lgkmcnt(5)\n" \
"v_mfma_f32_16x16x32_bf16 v[76:79], v[22:25], v[164:167], v[76:79]\n" \
"v_mfma_f32_16x16x32_bf16 v[240:243], a[160:163], v[96:99], 0\n" \
"ds_read_b128 v[124:127], v30 offset:7168\n" \
"v_mfma_f32_16x16x32_bf16 v[244:247], a[164:167], v[96:99], 0\n" \
"buffer_store_dwordx4 v[200:203], v4, s[36:39], s77 offen offset:1024 sc1\n" \
"v_mfma_f32_16x16x32_bf16 v[248:251], a[160:163], v[100:103], 0\n" \
"s_waitcnt lgkmcnt(4)\n" \
"ds_read_b128 v[96:99], v30 offset:8192\n" \
"v_fmac_f32 v32, v5, v64\n" \
"v_mfma_f32_16x16x32_bf16 v[252:255], a[164:167], v[100:103], 0\n" \
"v_fmac_f32 v33, v5, v65\n" \
"v_cvt_pk_bf16_f32 v80, v32, v33\n" \
"v_mfma_f32_16x16x32_bf16 v[240:243], a[168:171], v[104:107], v[240:243]\n" \
"ds_read_b128 v[100:103], v30 offset:9216\n" \
"v_fmac_f32 v34, v5, v66\n" \
"v_mfma_f32_16x16x32_bf16 v[244:247], a[172:175], v[104:107], v[244:247]\n" \
"v_fmac_f32 v35, v5, v67\n" \
"v_cvt_pk_bf16_f32 v81, v34, v35\n" \
"v_mfma_f32_16x16x32_bf16 v[248:251], a[168:171], v[108:111], v[248:251]\n" \
"s_waitcnt lgkmcnt(4)\n" \
"ds_read_b128 v[104:107], v30 offset:10240\n" \
"v_fmac_f32 v36, v5, v68\n" \
"v_mfma_f32_16x16x32_bf16 v[252:255], a[172:175], v[108:111], v[252:255]\n" \
"v_fmac_f32 v37, v5, v69\n" \
"v_cvt_pk_bf16_f32 v82, v36, v37\n" \
"v_mfma_f32_16x16x32_bf16 v[240:243], a[176:179], v[112:115], v[240:243]\n" \
"ds_read_b128 v[108:111], v30 offset:11264\n" \
"v_fmac_f32 v38, v5, v70\n" \
"v_mfma_f32_16x16x32_bf16 v[244:247], a[180:183], v[112:115], v[244:247]\n" \
"s_add_u32 s92, s82, 0\n" \
"v_fmac_f32 v39, v5, v71\n" \
"v_cvt_pk_bf16_f32 v83, v38, v39\n" \
"v_mfma_f32_16x16x32_bf16 v[248:251], a[176:179], v[116:119], v[248:251]\n" \
"s_waitcnt lgkmcnt(4)\n" \
"ds_read_b128 v[112:115], v30 offset:12288\n" \
"s_nop 0\n" \
"v_mfma_f32_16x16x32_bf16 v[252:255], a[180:183], v[116:119], v[252:255]\n" \
"v_permlane16_swap_b32 v80, v82\n" \
"v_permlane16_swap_b32 v81, v83\n" \
"v_mfma_f32_16x16x32_bf16 v[240:243], a[184:187], v[120:123], v[240:243]\n" \
"ds_read_b128 v[116:119], v30 offset:13312\n" \
"v_fmac_f32 v40, v21, v72\n" \
"v_mfma_f32_16x16x32_bf16 v[244:247], a[188:191], v[120:123], v[244:247]\n" \
"v_fmac_f32 v41, v21, v73\n" \
"v_cvt_pk_bf16_f32 v84, v40, v41\n" \
"v_mfma_f32_16x16x32_bf16 v[248:251], a[184:187], v[124:127], v[248:251]\n" \
"ds_read_b128 v[120:123], v30 offset:14336\n" \
"v_fmac_f32 v42, v21, v74\n" \
"v_mfma_f32_16x16x32_bf16 v[252:255], a[188:191], v[124:127], v[252:255]\n" \
"s_waitcnt vmcnt(19) lgkmcnt(5)\n" \
"s_barrier\n" \
"v_fmac_f32 v43, v21, v75\n" \
"v_mfma_f32_16x16x32_bf16 v[240:243], a[192:195], v[96:99], v[240:243]\n" \
"ds_read_b128 v[124:127], v30 offset:15360\n" \
"v_cvt_pk_bf16_f32 v85, v42, v43\n" \
"v_mfma_f32_16x16x32_bf16 v[244:247], a[196:199], v[96:99], v[244:247]\n" \
"v_fmac_f32 v44, v21, v76\n" \
"v_fmac_f32 v45, v21, v77\n" \
"v_mfma_f32_16x16x32_bf16 v[248:251], a[192:195], v[100:103], v[248:251]\n" \
"s_waitcnt lgkmcnt(4)\n" \
"ds_read_b128 v[96:99], v28 offset:0\n" \
"v_cvt_pk_bf16_f32 v86, v44, v45\n" \
"v_mfma_f32_16x16x32_bf16 v[252:255], a[196:199], v[100:103], v[252:255]\n" \
"v_fmac_f32 v46, v21, v78\n" \
"v_fmac_f32 v47, v21, v79\n" \
"v_mfma_f32_16x16x32_bf16 v[240:243], a[200:203], v[104:107], v[240:243]\n" \
"s_add_u32 s97, s78, 4096\n" \
"ds_read_b128 v[100:103], v28 offset:1024\n" \
"v_cvt_pk_bf16_f32 v87, v46, v47\n" \
"v_mfma_f32_16x16x32_bf16 v[244:247], a[204:207], v[104:107], v[244:247]\n" \
"buffer_load_dwordx4 v[168:171], v4, s[36:39], s97 offen offset:2048 sc1\n" \
"v_mfma_f32_16x16x32_bf16 v[248:251], a[200:203], v[108:111], v[248:251]\n" \
"s_waitcnt lgkmcnt(4)\n" \
"ds_read_b128 v[104:107], v28 offset:2048\n" \
"s_add_u32 s93, s82, 32768\n" \
"v_mfma_f32_16x16x32_bf16 v[252:255], a[204:207], v[108:111], v[252:255]\n" \
"s_nop 0\n" \
"v_permlane16_swap_b32 v84, v86\n" \
"v_mfma_f32_16x16x32_bf16 v[240:243], a[208:211], v[112:115], v[240:243]\n" \
"ds_read_b128 v[108:111], v28 offset:3072\n" \
"v_permlane16_swap_b32 v85, v87\n" \
"v_mfma_f32_16x16x32_bf16 v[244:247], a[212:215], v[112:115], v[244:247]\n" \
"buffer_load_dwordx4 v[172:175], v4, s[36:39], s97 offen offset:3072 sc1\n" \
"v_mfma_f32_16x16x32_bf16 v[248:251], a[208:211], v[116:119], v[248:251]\n" \
"s_waitcnt lgkmcnt(4)\n" \
"ds_read_b128 v[112:115], v28 offset:4096\n" \
"s_add_u32 s97, s78, 8192\n" \
"v_mfma_f32_16x16x32_bf16 v[252:255], a[212:215], v[116:119], v[252:255]\n" \
"v_mfma_f32_16x16x32_bf16 v[240:243], a[216:219], v[120:123], v[240:243]\n" \
"ds_read_b128 v[116:119], v28 offset:5120\n" \
"v_mfma_f32_16x16x32_bf16 v[244:247], a[220:223], v[120:123], v[244:247]\n" \
"buffer_load_dwordx4 v[224:227], v4, s[36:39], s97 offen offset:0 sc1\n" \
"v_mfma_f32_16x16x32_bf16 v[248:251], a[216:219], v[124:127], v[248:251]\n" \
"s_waitcnt lgkmcnt(4)\n" \
"ds_read_b128 v[120:123], v28 offset:6144\n" \
"v_mfma_f32_16x16x32_bf16 v[252:255], a[220:223], v[124:127], v[252:255]\n" \
"v_mfma_f32_16x16x32_bf16 v[240:243], a[224:227], v[96:99], v[240:243]\n" \
"ds_read_b128 v[124:127], v28 offset:7168\n" \
"v_mfma_f32_16x16x32_bf16 v[244:247], a[228:231], v[96:99], v[244:247]\n" \
"buffer_load_dwordx4 v[228:231], v4, s[36:39], s97 offen offset:1024 sc1\n" \
"v_mfma_f32_16x16x32_bf16 v[248:251], a[224:227], v[100:103], v[248:251]\n" \
"s_waitcnt lgkmcnt(4)\n" \
"ds_read_b128 v[96:99], v28 offset:8192\n" \
"v_mfma_f32_16x16x32_bf16 v[252:255], a[228:231], v[100:103], v[252:255]\n" \
"v_mfma_f32_16x16x32_bf16 v[240:243], a[232:235], v[104:107], v[240:243]\n" \
"ds_read_b128 v[100:103], v28 offset:9216\n" \
"v_mfma_f32_16x16x32_bf16 v[244:247], a[236:239], v[104:107], v[244:247]\n" \
"v_mfma_f32_16x16x32_bf16 v[248:251], a[232:235], v[108:111], v[248:251]\n" \
"s_waitcnt lgkmcnt(4)\n" \
"ds_read_b128 v[104:107], v28 offset:10240\n" \
"v_mfma_f32_16x16x32_bf16 v[252:255], a[236:239], v[108:111], v[252:255]\n" \
"v_mfma_f32_16x16x32_bf16 v[240:243], a[240:243], v[112:115], v[240:243]\n" \
"ds_read_b128 v[108:111], v28 offset:11264\n" \
"v_mfma_f32_16x16x32_bf16 v[244:247], a[244:247], v[112:115], v[244:247]\n" \
"v_mfma_f32_16x16x32_bf16 v[248:251], a[240:243], v[116:119], v[248:251]\n" \
"s_waitcnt lgkmcnt(4)\n" \
"ds_read_b128 v[112:115], v28 offset:12288\n" \
"v_mfma_f32_16x16x32_bf16 v[252:255], a[244:247], v[116:119], v[252:255]\n" \
"v_mfma_f32_16x16x32_bf16 v[240:243], a[248:251], v[120:123], v[240:243]\n" \
"ds_read_b128 v[116:119], v28 offset:13312\n" \
"v_mfma_f32_16x16x32_bf16 v[244:247], a[252:255], v[120:123], v[244:247]\n" \
"v_mfma_f32_16x16x32_bf16 v[248:251], a[248:251], v[124:127], v[248:251]\n" \
"ds_read_b128 v[120:123], v28 offset:14336\n" \
"v_mfma_f32_16x16x32_bf16 v[252:255], a[252:255], v[124:127], v[252:255]\n" \
"s_waitcnt vmcnt(18) lgkmcnt(5)\n" \
"s_barrier\n" \
"v_mfma_f32_16x16x32_bf16 v[240:243], a[64:67], v[96:99], v[240:243]\n" \
"ds_read_b128 v[124:127], v28 offset:15360\n" \
"v_mfma_f32_16x16x32_bf16 v[244:247], a[68:71], v[96:99], v[244:247]\n" \
"v_mfma_f32_16x16x32_bf16 v[248:251], a[64:67], v[100:103], v[248:251]\n" \
"s_waitcnt lgkmcnt(4)\n" \
"ds_read_b128 v[96:99], v28 offset:49152\n" \
"v_mfma_f32_16x16x32_bf16 v[252:255], a[68:71], v[100:103], v[252:255]\n" \
"v_mfma_f32_16x16x32_bf16 v[240:243], a[72:75], v[104:107], v[240:243]\n" \
"ds_read_b128 v[100:103], v28 offset:50176\n" \
"v_mfma_f32_16x16x32_bf16 v[244:247], a[76:79], v[104:107], v[244:247]\n" \
"v_mfma_f32_16x16x32_bf16 v[248:251], a[72:75], v[108:111], v[248:251]\n" \
"s_waitcnt lgkmcnt(4)\n" \
"ds_read_b128 v[104:107], v28 offset:51200\n" \
"v_mfma_f32_16x16x32_bf16 v[252:255], a[76:79], v[108:111], v[252:255]\n" \
"v_mfma_f32_16x16x32_bf16 v[240:243], a[80:83], v[112:115], v[240:243]\n" \
"ds_read_b128 v[108:111], v28 offset:52224\n" \
"v_mfma_f32_16x16x32_bf16 v[244:247], a[84:87], v[112:115], v[244:247]\n" \
"v_mfma_f32_16x16x32_bf16 v[248:251], a[80:83], v[116:119], v[248:251]\n" \
"s_waitcnt lgkmcnt(4)\n" \
"ds_read_b128 v[112:115], v28 offset:53248\n" \
"v_mfma_f32_16x16x32_bf16 v[252:255], a[84:87], v[116:119], v[252:255]\n" \
"v_mfma_f32_16x16x32_bf16 v[240:243], a[88:91], v[120:123], v[240:243]\n" \
"ds_read_b128 v[116:119], v28 offset:54272\n" \
"v_mfma_f32_16x16x32_bf16 v[244:247], a[92:95], v[120:123], v[244:247]\n" \
"v_mfma_f32_16x16x32_bf16 v[248:251], a[88:91], v[124:127], v[248:251]\n" \
"s_waitcnt lgkmcnt(4)\n" \
"ds_read_b128 v[120:123], v28 offset:55296\n" \
"v_mfma_f32_16x16x32_bf16 v[252:255], a[92:95], v[124:127], v[252:255]\n" \
"v_mfma_f32_16x16x32_bf16 v[240:243], a[128:131], v[96:99], v[240:243]\n" \
"ds_read_b128 v[124:127], v28 offset:56320\n" \
"v_mfma_f32_16x16x32_bf16 v[244:247], a[132:135], v[96:99], v[244:247]\n" \
"v_mfma_f32_16x16x32_bf16 v[248:251], a[128:131], v[100:103], v[248:251]\n" \
"s_waitcnt lgkmcnt(4)\n" \
"ds_read_b128 v[96:99], v28 offset:57344\n" \
"v_mfma_f32_16x16x32_bf16 v[252:255], a[132:135], v[100:103], v[252:255]\n" \
"v_mfma_f32_16x16x32_bf16 v[240:243], a[136:139], v[104:107], v[240:243]\n" \
"ds_read_b128 v[100:103], v28 offset:58368\n" \
"v_mfma_f32_16x16x32_bf16 v[244:247], a[140:143], v[104:107], v[244:247]\n" \
"v_mfma_f32_16x16x32_bf16 v[248:251], a[136:139], v[108:111], v[248:251]\n" \
"s_waitcnt lgkmcnt(4)\n" \
"ds_read_b128 v[104:107], v28 offset:59392\n" \
"v_mfma_f32_16x16x32_bf16 v[252:255], a[140:143], v[108:111], v[252:255]\n" \
"v_mfma_f32_16x16x32_bf16 v[240:243], a[144:147], v[112:115], v[240:243]\n" \
"ds_read_b128 v[108:111], v28 offset:60416\n" \
"v_mfma_f32_16x16x32_bf16 v[244:247], a[148:151], v[112:115], v[244:247]\n" \
"v_mfma_f32_16x16x32_bf16 v[248:251], a[144:147], v[116:119], v[248:251]\n" \
"s_waitcnt lgkmcnt(4)\n" \
"ds_read_b128 v[112:115], v28 offset:61440\n" \
"v_mfma_f32_16x16x32_bf16 v[252:255], a[148:151], v[116:119], v[252:255]\n" \
"v_mfma_f32_16x16x32_bf16 v[240:243], a[152:155], v[120:123], v[240:243]\n" \
"ds_read_b128 v[116:119], v28 offset:62464\n" \
"v_mfma_f32_16x16x32_bf16 v[244:247], a[156:159], v[120:123], v[244:247]\n" \
"v_mfma_f32_16x16x32_bf16 v[248:251], a[152:155], v[124:127], v[248:251]\n" \
"ds_read_b128 v[120:123], v28 offset:63488\n" \
"v_mfma_f32_16x16x32_bf16 v[252:255], a[156:159], v[124:127], v[252:255]\n" \
"s_waitcnt vmcnt(12) lgkmcnt(5)\n" \
"s_barrier\n" \
"v_mfma_f32_16x16x32_bf16 v[240:243], a[0:3], v[96:99], v[240:243]\n" \
"ds_read_b128 v[124:127], v28 offset:64512\n" \
"v_mfma_f32_16x16x32_bf16 v[244:247], a[4:7], v[96:99], v[244:247]\n" \
"v_mfma_f32_16x16x32_bf16 v[248:251], a[0:3], v[100:103], v[248:251]\n" \
"s_waitcnt lgkmcnt(4)\n" \
"ds_read_b128 v[96:99], v28 offset:16384\n" \
"v_mfma_f32_16x16x32_bf16 v[252:255], a[4:7], v[100:103], v[252:255]\n" \
"v_mfma_f32_16x16x32_bf16 v[240:243], a[8:11], v[104:107], v[240:243]\n" \
"ds_read_b128 v[100:103], v28 offset:17408\n" \
"v_mfma_f32_16x16x32_bf16 v[244:247], a[12:15], v[104:107], v[244:247]\n" \
"v_mfma_f32_16x16x32_bf16 v[248:251], a[8:11], v[108:111], v[248:251]\n" \
"s_waitcnt lgkmcnt(4)\n" \
"ds_read_b128 v[104:107], v28 offset:18432\n" \
"v_mfma_f32_16x16x32_bf16 v[252:255], a[12:15], v[108:111], v[252:255]\n" \
"v_mfma_f32_16x16x32_bf16 v[240:243], a[16:19], v[112:115], v[240:243]\n" \
"ds_read_b128 v[108:111], v28 offset:19456\n" \
"v_mfma_f32_16x16x32_bf16 v[244:247], a[20:23], v[112:115], v[244:247]\n" \
"v_mfma_f32_16x16x32_bf16 v[248:251], a[16:19], v[116:119], v[248:251]\n" \
"s_waitcnt lgkmcnt(4)\n" \
"ds_read_b128 v[112:115], v28 offset:20480\n" \
"s_add_u32 s97, s78, 8192\n" \
"v_mfma_f32_16x16x32_bf16 v[252:255], a[20:23], v[116:119], v[252:255]\n" \
"buffer_load_dwordx4 v[232:235], v4, s[36:39], s97 offen offset:2048 sc1\n" \
"v_mfma_f32_16x16x32_bf16 v[240:243], a[24:27], v[120:123], v[240:243]\n" \
"ds_read_b128 v[116:119], v28 offset:21504\n" \
"s_waitcnt vmcnt(1)\n" \
"v_mfma_f32_16x16x32_bf16 v[244:247], a[28:31], v[120:123], v[244:247]\n" \
"v_max_u32 v18, v168, v169\n" \
"v_max3_u32 v18, v18, v170, v171\n" \
"v_mfma_f32_16x16x32_bf16 v[248:251], a[24:27], v[124:127], v[248:251]\n" \
"s_waitcnt lgkmcnt(4)\n" \
"ds_read_b128 v[120:123], v28 offset:22528\n" \
"v_max3_u32 v18, v18, v172, v173\n" \
"v_mfma_f32_16x16x32_bf16 v[252:255], a[28:31], v[124:127], v[252:255]\n" \
"buffer_load_dwordx4 v[236:239], v4, s[36:39], s97 offen offset:3072 sc1\n" \
"v_mfma_f32_16x16x32_bf16 v[240:243], a[32:35], v[96:99], v[240:243]\n" \
"ds_read_b128 v[124:127], v28 offset:23552\n" \
"v_max3_u32 v18, v18, v174, v175\n" \
"v_mfma_f32_16x16x32_bf16 v[244:247], a[36:39], v[96:99], v[244:247]\n" \
"buffer_store_dwordx4 v[80:83], v184, s[32:35], s92 offen\n" \
"v_mfma_f32_16x16x32_bf16 v[248:251], a[32:35], v[100:103], v[248:251]\n" \
"s_waitcnt lgkmcnt(4)\n" \
"ds_read_b128 v[96:99], v28 offset:24576\n" \
"v_max3_u32 v18, v18, v224, v225\n" \
"v_mfma_f32_16x16x32_bf16 v[252:255], a[36:39], v[100:103], v[252:255]\n" \
"v_max3_u32 v18, v18, v226, v227\n" \
"v_max3_u32 v18, v18, v228, v229\n" \
"v_mfma_f32_16x16x32_bf16 v[240:243], a[40:43], v[104:107], v[240:243]\n" \
"ds_read_b128 v[100:103], v28 offset:25600\n" \
"v_max3_u32 v18, v18, v230, v231\n" \
"v_mfma_f32_16x16x32_bf16 v[244:247], a[44:47], v[104:107], v[244:247]\n" \
"v_cmp_eq_u32 vcc, -1, v18\n" \
"s_cbranch_vccnz LIM_%=\nLIMR_%=:\n" \
"v_mfma_f32_16x16x32_bf16 v[248:251], a[40:43], v[108:111], v[248:251]\n" \
"s_waitcnt lgkmcnt(4)\n" \
"ds_read_b128 v[104:107], v28 offset:26624\n" \
"v_mfma_f32_16x16x32_bf16 v[252:255], a[44:47], v[108:111], v[252:255]\n" \
"v_mfma_f32_16x16x32_bf16 v[240:243], a[48:51], v[112:115], v[240:243]\n" \
"ds_read_b128 v[108:111], v28 offset:27648\n" \
"v_mfma_f32_16x16x32_bf16 v[244:247], a[52:55], v[112:115], v[244:247]\n" \
"buffer_store_dwordx4 v[84:87], v184, s[32:35], s93 offen\n" \
"v_mfma_f32_16x16x32_bf16 v[248:251], a[48:51], v[116:119], v[248:251]\n" \
"s_waitcnt lgkmcnt(4)\n" \
"ds_read_b128 v[112:115], v28 offset:28672\n" \
"v_mfma_f32_16x16x32_bf16 v[252:255], a[52:55], v[116:119], v[252:255]\n" \
"v_mfma_f32_16x16x32_bf16 v[240:243], a[56:59], v[120:123], v[240:243]\n" \
"ds_read_b128 v[116:119], v28 offset:29696\n" \
"v_mfma_f32_16x16x32_bf16 v[244:247], a[60:63], v[120:123], v[244:247]\n" \
"v_mfma_f32_16x16x32_bf16 v[248:251], a[56:59], v[124:127], v[248:251]\n" \
"ds_read_b128 v[120:123], v28 offset:30720\n" \
"v_mfma_f32_16x16x32_bf16 v[252:255], a[60:63], v[124:127], v[252:255]\n" \
"s_waitcnt lgkmcnt(5)\n" \
"s_barrier\n" \
"v_mfma_f32_16x16x32_bf16 v[240:243], a[96:99], v[96:99], v[240:243]\n" \
"ds_read_b128 v[124:127], v28 offset:31744\n" \
"v_mfma_f32_16x16x32_bf16 v[244:247], a[100:103], v[96:99], v[244:247]\n" \
"v_mfma_f32_16x16x32_bf16 v[248:251], a[96:99], v[100:103], v[248:251]\n" \
"s_waitcnt lgkmcnt(4)\n" \
"v_mfma_f32_16x16x32_bf16 v[252:255], a[100:103], v[100:103], v[252:255]\n" \
"v_mfma_f32_16x16x32_bf16 v[240:243], a[104:107], v[104:107], v[240:243]\n" \
"v_mfma_f32_16x16x32_bf16 v[244:247], a[108:111], v[104:107], v[244:247]\n" \
"v_mfma_f32_16x16x32_bf16 v[248:251], a[104:107], v[108:111], v[248:251]\n" \
"s_waitcnt lgkmcnt(2)\n" \
"v_mfma_f32_16x16x32_bf16 v[252:255], a[108:111], v[108:111], v[252:255]\n" \
"v_mfma_f32_16x16x32_bf16 v[240:243], a[112:115], v[112:115], v[240:243]\n" \
"v_mfma_f32_16x16x32_bf16 v[244:247], a[116:119], v[112:115], v[244:247]\n" \
"v_mfma_f32_16x16x32_bf16 v[248:251], a[112:115], v[116:119], v[248:251]\n" \
"s_waitcnt lgkmcnt(0)\n" \
"v_mfma_f32_16x16x32_bf16 v[252:255], a[116:119], v[116:119], v[252:255]\n" \
"v_mfma_f32_16x16x32_bf16 v[240:243], a[120:123], v[120:123], v[240:243]\n" \
"v_mfma_f32_16x16x32_bf16 v[244:247], a[124:127], v[120:123], v[244:247]\n" \
"v_mfma_f32_16x16x32_bf16 v[248:251], a[120:123], v[124:127], v[248:251]\n" \
"v_mfma_f32_16x16x32_bf16 v[252:255], a[124:127], v[124:127], v[252:255]\n" \
"v_mfma_f32_16x16x32_bf16 v[240:243], v[24:27], v[168:171], v[240:243]\n" \
"v_mfma_f32_16x16x32_bf16 v[244:247], v[22:25], v[168:171], v[244:247]\n" \
"v_mfma_f32_16x16x32_bf16 v[248:251], v[24:27], v[172:175], v[248:251]\n" \
"v_mfma_f32_16x16x32_bf16 v[252:255], v[22:25], v[172:175], v[252:255]\n" \
"v_mfma_f32_16x16x32_bf16 v[240:243], v[24:27], v[224:227], v[240:243]\n" \
"v_mfma_f32_16x16x32_bf16 v[244:247], v[22:25], v[224:227], v[244:247]\n" \
"v_mfma_f32_16x16x32_bf16 v[248:251], v[24:27], v[228:231], v[248:251]\n" \
"v_mfma_f32_16x16x32_bf16 v[252:255], v[22:25], v[228:231], v[252:255]\n" \
"s_mov_b32 s92, s83\n" \
"s_add_u32 s93, s83, 32768\n" \
"s_waitcnt vmcnt(2)\n" \
"v_max_u32 v18, v232, v233\n" \
"v_max3_u32 v18, v18, v234, v235\n" \
"v_max3_u32 v18, v18, v236, v237\n" \
"v_max3_u32 v18, v18, v238, v239\n" \
"v_cmp_eq_u32 vcc, -1, v18\n" \
"s_cbranch_vccnz LIN_%=\nLINR_%=:\n" \
"v_mfma_f32_16x16x32_bf16 v[240:243], v[24:27], v[232:235], v[240:243]\n" \
"v_mfma_f32_16x16x32_bf16 v[244:247], v[22:25], v[232:235], v[244:247]\n" \
"v_mfma_f32_16x16x32_bf16 v[248:251], v[24:27], v[236:239], v[248:251]\n" \
"v_mfma_f32_16x16x32_bf16 v[252:255], v[22:25], v[236:239], v[252:255]\n" \
"s_nop 3\n" \
"v_pk_fma_f32 v[240:241], v[240:241], v[6:7], v[48:49] op_sel_hi:[1,0,1]\n" \
"v_pk_fma_f32 v[242:243], v[242:243], v[6:7], v[50:51] op_sel_hi:[1,0,1]\n" \
"v_pk_fma_f32 v[244:245], v[244:245], v[6:7], v[52:53] op_sel_hi:[1,0,1]\n" \
"v_pk_fma_f32 v[246:247], v[246:247], v[6:7], v[54:55] op_sel_hi:[1,0,1]\n" \
"v_cvt_pk_bf16_f32 v80, v240, v241\n" \
"v_cvt_pk_bf16_f32 v81, v242, v243\n" \
"v_cvt_pk_bf16_f32 v82, v244, v245\n" \
"v_cvt_pk_bf16_f32 v83, v246, v247\n" \
"s_nop 1\n" \
"v_permlane16_swap_b32 v80, v82\n" \
"v_permlane16_swap_b32 v81, v83\n" \
"buffer_store_dwordx4 v[80:83], v184, s[32:35], s92 offen\n" \
"v_pk_fma_f32 v[248:249], v[248:249], v[8:9], v[56:57] op_sel_hi:[1,0,1]\n" \
"v_pk_fma_f32 v[250:251], v[250:251], v[8:9], v[58:59] op_sel_hi:[1,0,1]\n" \
"v_pk_fma_f32 v[252:253], v[252:253], v[8:9], v[60:61] op_sel_hi:[1,0,1]\n" \
"v_pk_fma_f32 v[254:255], v[254:255], v[8:9], v[62:63] op_sel_hi:[1,0,1]\n" \
"v_cvt_pk_bf16_f32 v84, v248, v249\n" \
"v_cvt_pk_bf16_f32 v85, v250, v251\n" \
"v_cvt_pk_bf16_f32 v86, v252, v253\n" \
"v_cvt_pk_bf16_f32 v87, v254, v255\n" \
"s_nop 1\n" \
"v_permlane16_swap_b32 v84, v86\n" \
"v_permlane16_swap_b32 v85, v87\n" \
"buffer_store_dwordx4 v[84:87], v184, s[32:35], s93 offen\n" \
"s_branch LEND_%=\n" \
"LPUK_%=:\n" \
"s_mov_b32 s25, 0\n" \
"LPUKL_%=:\n" \
"s_sleep 2\n" \
"buffer_load_dwordx2 v[58:59], v57, s[36:39], 0 offen sc1\n" \
"s_add_u32 s25, s25, 1\n" \
"s_waitcnt vmcnt(0)\n" \
"v_cmp_ne_u64 vcc, s[14:15], v[58:59]\n" \
"s_cmp_gt_u32 s25, 262144\n" \
"s_cbranch_scc1 LPUKR_%=\n" \
"s_cbranch_vccnz LPUKL_%=\n" \
"s_branch LPUKR_%=\n" \
"LPUV_%=:\n" \
"s_mov_b32 s25, 0\n" \
"LPUVL_%=:\n" \
"s_sleep 2\n" \
"buffer_load_dwordx2 v[58:59], v57, s[36:39], 0 offen sc1\n" \
"s_add_u32 s25, s25, 1\n" \
"s_waitcnt vmcnt(0)\n" \
"v_cmp_ne_u64 vcc, s[14:15], v[58:59]\n" \
"s_cmp_gt_u32 s25, 262144\n" \
"s_cbranch_scc1 LPUVR_%=\n" \
"s_cbranch_vccnz LPUVL_%=\n" \
"s_branch LPUVR_%=\n" \
"LPK_%=:\n" \
"s_mov_b32 s25, 0\n" \
"LPKL_%=:\n" \
"s_sleep 2\n" \
"buffer_load_dwordx2 v[58:59], v57, s[36:39], 0 offen sc1\n" \
"s_add_u32 s25, s25, 1\n" \
"s_waitcnt vmcnt(0)\n" \
"v_cmp_ne_u64 vcc, s[14:15], v[58:59]\n" \
"s_cmp_gt_u32 s25, 262144\n" \
"s_cbranch_scc1 LPKR_%=\n" \
"s_cbranch_vccnz LPKL_%=\n" \
"s_branch LPKR_%=\n" \
"LSW_%=:\n" \
"v_swap_b32 v64, v80\n" \
"v_swap_b32 v65, v81\n" \
"v_swap_b32 v66, v82\n" \
"v_swap_b32 v67, v83\n" \
"v_swap_b32 v68, v84\n" \
"v_swap_b32 v69, v85\n" \
"v_swap_b32 v70, v86\n" \
"v_swap_b32 v71, v87\n" \
"v_swap_b32 v72, v88\n" \
"v_swap_b32 v73, v89\n" \
"v_swap_b32 v74, v90\n" \
"v_swap_b32 v75, v91\n" \
"v_swap_b32 v76, v92\n" \
"v_swap_b32 v77, v93\n" \
"v_swap_b32 v78, v94\n" \
"v_swap_b32 v79, v95\n" \
"s_branch LSWR_%=\n" \
"LIM_%=:\n" \
"s_mov_b32 s25, 0\n" \
"LIML_%=:\n" \
"s_sleep 1\n" \
"s_add_u32 s98, s78, 4096\n" \
"buffer_load_dwordx4 v[168:171], v4, s[36:39], s98 offen offset:2048 sc1\n" \
"buffer_load_dwordx4 v[172:175], v4, s[36:39], s98 offen offset:3072 sc1\n" \
"s_add_u32 s98, s78, 8192\n" \
"buffer_load_dwordx4 v[224:227], v4, s[36:39], s98 offen offset:0 sc1\n" \
"buffer_load_dwordx4 v[228:231], v4, s[36:39], s98 offen offset:1024 sc1\n" \
"s_add_u32 s25, s25, 1\n" \
"s_waitcnt vmcnt(0)\n" \
"v_max_u32 v18, v168, v169\n" \
"v_max3_u32 v18, v18, v170, v171\n" \
"v_max3_u32 v18, v18, v172, v173\n" \
"v_max3_u32 v18, v18, v174, v175\n" \
"v_max3_u32 v18, v18, v224, v225\n" \
"v_max3_u32 v18, v18, v226, v227\n" \
"v_max3_u32 v18, v18, v228, v229\n" \
"v_max3_u32 v18, v18, v230, v231\n" \
"v_cmp_eq_u32 vcc, -1, v18\n" \
"s_cmp_gt_u32 s25, 262144\n" \
"s_cbranch_scc1 LIMR_%=\n" \
"s_cbranch_vccnz LIML_%=\n" \
"s_branch LIMR_%=\n" \
"LIN_%=:\n" \
"s_mov_b32 s25, 0\n" \
"LINL_%=:\n" \
"s_sleep 1\n" \
"s_add_u32 s98, s78, 8192\n" \
"buffer_load_dwordx4 v[232:235], v4, s[36:39], s98 offen offset:2048 sc1\n" \
"buffer_load_dwordx4 v[236:239], v4, s[36:39], s98 offen offset:3072 sc1\n" \
"s_add_u32 s25, s25, 1\n" \
"s_waitcnt vmcnt(0)\n" \
"v_max_u32 v18, v232, v233\n" \
"v_max3_u32 v18, v18, v234, v235\n" \
"v_max3_u32 v18, v18, v236, v237\n" \
"v_max3_u32 v18, v18, v238, v239\n" \
"v_cmp_eq_u32 vcc, -1, v18\n" \
"s_cmp_gt_u32 s25, 262144\n" \
"s_cbranch_scc1 LINR_%=\n" \
"s_cbranch_vccnz LINL_%=\n" \
"s_branch LINR_%=\n" \
"LPZ_%=:\n" \
"s_mov_b32 s25, 0\n" \
"LPZL_%=:\n" \
"s_sleep 2\n" \
"buffer_load_dwordx2 v[18:19], v17, s[36:39], 0 offen sc1\n" \
"s_add_u32 s25, s25, 1\n" \
"s_waitcnt vmcnt(0)\n" \
"v_cmp_ne_u64 vcc, s[14:15], v[18:19]\n" \
"s_cmp_gt_u32 s25, 262144\n" \
"s_cbranch_scc1 LPZR_%=\n" \
"s_cbranch_vccnz LPZL_%=\n" \
"s_branch LPZR_%=\n" \
"LPS_%=:\n" \
"s_mov_b32 s25, 0\n" \
"LPSL_%=:\n" \
"s_sleep 2\n" \
"buffer_load_dwordx2 v[18:19], v17, s[36:39], 0 offen sc1\n" \
"s_add_u32 s25, s25, 1\n" \
"s_waitcnt vmcnt(0)\n" \
"v_cmp_ne_u64 vcc, s[14:15], v[18:19]\n" \
"s_cmp_gt_u32 s25, 262144\n" \
"s_cbranch_scc1 LPSR_%=\n" \
"s_cbranch_vccnz LPSL_%=\n" \
"s_branch LPSR_%=\n" \
"LPL_%=:\n" \
"s_mov_b32 s25, 0\n" \
"LPLL_%=:\n" \
"s_sleep 2\n" \
"buffer_load_dwordx2 v[18:19], v17, s[36:39], 0 offen sc1\n" \
"s_add_u32 s25, s25, 1\n" \
"s_waitcnt vmcnt(0)\n" \
"v_cmp_ne_u64 vcc, s[14:15], v[18:19]\n" \
"s_cmp_gt_u32 s25, 262144\n" \
"s_cbranch_scc1 LPLR_%=\n" \
"s_cbranch_vccnz LPLL_%=\n" \
"s_branch LPLR_%=\n" \
"LPE_%=:\n" \
"s_mov_b32 s25, 0\n" \
"LPEL_%=:\n" \
"s_sleep 2\n" \
"buffer_load_dwordx2 v[18:19], v17, s[36:39], 0 offen sc1\n" \
"s_add_u32 s25, s25, 1\n" \
"s_waitcnt vmcnt(0)\n" \
"v_cmp_ne_u64 vcc, s[14:15], v[18:19]\n" \
"s_cmp_gt_u32 s25, 262144\n" \
"s_cbranch_scc1 LPER_%=\n" \
"s_cbranch_vccnz LPEL_%=\n" \
"s_branch LPER_%=\n" \
"LPE0_%=:\n" \
"s_mov_b32 s25, 0\n" \
"LPE0L_%=:\n" \
"s_sleep 2\n" \
"buffer_load_dwordx2 v[18:19], v17, s[36:39], 0 offen sc1\n" \
"s_add_u32 s25, s25, 1\n" \
"s_waitcnt vmcnt(0)\n" \
"v_cmp_ne_u64 vcc, s[14:15], v[18:19]\n" \
"s_cmp_gt_u32 s25, 262144\n" \
"s_cbranch_scc1 LPE0R_%=\n" \
"s_cbranch_vccnz LPE0L_%=\n" \
"s_branch LPE0R_%=\n" \
"LFBA_%=:\n" \
"s_mov_b32 s99, 0\n" \
"s_branch LFB_%=\n" \
"LFBC_%=:\n" \
"s_mov_b32 s99, 2\n" \
"s_branch LFB_%=\n" \
"LFBB_%=:\n" \
"s_mov_b32 s99, 1\n" \
"LFB_%=:\n" \
"s_waitcnt vmcnt(0) lgkmcnt(0)\n" \
"s_nop 15\n" \
"s_lshl_b32 s25, %1, 2\n" \
"s_add_u32 s25, s25, %2\n" \
"s_lshl_b32 s25, s25, 13\n" \
"s_add_u32 s25, s25, 16777216\n" \
"buffer_store_dwordx4 v[64:67], v4, s[36:39], s25 offen offset:0\n" \
"buffer_store_dwordx4 v[68:71], v4, s[36:39], s25 offen offset:1024\n" \
"buffer_store_dwordx4 v[72:75], v4, s[36:39], s25 offen offset:2048\n" \
"buffer_store_dwordx4 v[76:79], v4, s[36:39], s25 offen offset:3072\n" \
"buffer_store_dwordx4 v[80:83], v7, s[36:39], s25 offen offset:0\n" \
"buffer_store_dwordx4 v[84:87], v7, s[36:39], s25 offen offset:1024\n" \
"buffer_store_dwordx4 v[88:91], v7, s[36:39], s25 offen offset:2048\n" \
"buffer_store_dwordx4 v[92:95], v7, s[36:39], s25 offen offset:3072\n" \
"s_add_u32 s25, s25, 8388608\n" \
"buffer_store_dwordx4 v[96:99], v4, s[36:39], s25 offen offset:0\n" \
"buffer_store_dwordx4 v[100:103], v4, s[36:39], s25 offen offset:1024\n" \
"buffer_store_dwordx4 v[104:107], v4, s[36:39], s25 offen offset:2048\n" \
"buffer_store_dwordx4 v[108:111], v4, s[36:39], s25 offen offset:3072\n" \
"buffer_store_dwordx4 v[112:115], v7, s[36:39], s25 offen offset:0\n" \
"buffer_store_dwordx4 v[116:119], v7, s[36:39], s25 offen offset:1024\n" \
"buffer_store_dwordx4 v[120:123], v7, s[36:39], s25 offen offset:2048\n" \
"buffer_store_dwordx4 v[124:127], v7, s[36:39], s25 offen offset:3072\n" \
"s_add_u32 s25, s25, 58720256\n" \
"buffer_store_dwordx4 v[128:131], v4, s[36:39], s25 offen offset:0\n" \
"buffer_store_dwordx4 v[132:135], v4, s[36:39], s25 offen offset:1024\n" \
"buffer_store_dwordx4 v[136:139], v4, s[36:39], s25 offen offset:2048\n" \
"buffer_store_dwordx4 v[140:143], v4, s[36:39], s25 offen offset:3072\n" \
"buffer_store_dwordx4 v[144:147], v7, s[36:39], s25 offen offset:0\n" \
"buffer_store_dwordx4 v[148:151], v7, s[36:39], s25 offen offset:1024\n" \
"buffer_store_dwordx4 v[152:155], v7, s[36:39], s25 offen offset:2048\n" \
"buffer_store_dwordx4 v[156:159], v7, s[36:39], s25 offen offset:3072\n" \
"s_add_u32 s25, s25, 37748736\n" \
"buffer_store_dwordx4 v[160:163], v4, s[36:39], s25 offen offset:0\n" \
"buffer_store_dwordx4 v[164:167], v4, s[36:39], s25 offen offset:1024\n" \
"buffer_store_dwordx4 v[168:171], v4, s[36:39], s25 offen offset:2048\n" \
"buffer_store_dwordx4 v[172:175], v4, s[36:39], s25 offen offset:3072\n" \
"s_waitcnt vmcnt(0)\n" \
"v_writelane_b32 v172, vcc_lo, 0\n" \
"v_writelane_b32 v172, vcc_hi, 1\n" \
"v_writelane_b32 v172, s64, 2\n" \
"v_writelane_b32 v172, s65, 3\n" \
"v_writelane_b32 v172, s66, 4\n" \
"v_writelane_b32 v172, s67, 5\n" \
"v_writelane_b32 v172, s68, 6\n" \
"v_writelane_b32 v172, s69, 7\n" \
"v_writelane_b32 v172, s70, 8\n" \
"v_writelane_b32 v172, s71, 9\n" \
"v_writelane_b32 v172, s72, 10\n" \
"v_writelane_b32 v172, s73, 11\n" \
"s_cmp_eq_u32 s99, 1\n" \
"s_cselect_b32 s64, s51, s50\n" \
"s_cselect_b32 s65, 2, 4\n" \
"s_cselect_b64 s[54:55], s[54:55], 0\n" \
"s_lshr_b32 s64, s64, 18\n" \
"s_lshl_b32 s64, s64, 5\n" \
"v_mov_b32 v160, 0x3f803f80\n" \
"v_mov_b32 v161, 0x3f803f80\n" \
"v_mov_b32 v162, 0x3f803f80\n" \
"v_mov_b32 v163, 0x3f803f80\n" \
"LFG_%=:\n" \
"s_mov_b32 s69, 0\n" \
"v_bfe_u32 v167, v4, 4, 4\n" \
"v_lshrrev_b32 v168, 8, v4\n" \
"v_add_u32 v165, s64, v167\n" \
"s_lshl_b32 s67, s6, 8\n" \
"v_lshl_add_u32 v128, v167, 1, s67\n" \
"v_lshl_add_u32 v128, v168, 16, v128\n" \
"v_add_u32 v129, 0x2000, v128\n" \
"v_add_u32 v130, 0x2000, v129\n" \
"v_add_u32 v131, 0x2000, v130\n" \
"v_add_u32 v132, 0x2000, v131\n" \
"v_add_u32 v133, 0x2000, v132\n" \
"v_add_u32 v134, 0x2000, v133\n" \
"v_add_u32 v135, 0x2000, v134\n" \
"LFQ_%=:\n" \
"s_load_dwordx2 s[70:71], %0, 0x0\n" \
"s_load_dwordx2 s[72:73], %0, 0x38\n" \
"v_lshlrev_b32 v166, 11, v165\n" \
"v_lshl_add_u32 v166, v168, 4, v166\n" \
"s_mov_b32 s66, 0\n" \
"v_mov_b32 v136, 0\n" \
"v_mov_b32 v137, 0\n" \
"v_mov_b32 v138, 0\n" \
"v_mov_b32 v139, 0\n" \
"v_mov_b32 v140, 0\n" \
"v_mov_b32 v141, 0\n" \
"v_mov_b32 v142, 0\n" \
"v_mov_b32 v143, 0\n" \
"s_waitcnt lgkmcnt(0)\n" \
"LFK_%=:\n" \
"global_load_dwordx4 v[112:115], v166, s[70:71]\n" \
"global_load_ushort v96, v128, s[72:73] offset:0\n" \
"global_load_ushort v97, v129, s[72:73] offset:0\n" \
"global_load_ushort v98, v130, s[72:73] offset:0\n" \
"global_load_ushort v99, v131, s[72:73] offset:0\n" \
"global_load_ushort v100, v132, s[72:73] offset:0\n" \
"global_load_ushort v101, v133, s[72:73] offset:0\n" \
"global_load_ushort v102, v134, s[72:73] offset:0\n" \
"global_load_ushort v103, v135, s[72:73] offset:0\n" \
"global_load_ushort v104, v128, s[72:73] offset:32\n" \
"global_load_ushort v105, v129, s[72:73] offset:32\n" \
"global_load_ushort v106, v130, s[72:73] offset:32\n" \
"global_load_ushort v107, v131, s[72:73] offset:32\n" \
"global_load_ushort v108, v132, s[72:73] offset:32\n" \
"global_load_ushort v109, v133, s[72:73] offset:32\n" \
"global_load_ushort v110, v134, s[72:73] offset:32\n" \
"global_load_ushort v111, v135, s[72:73] offset:32\n" \
"v_add_u32 v166, 64, v166\n" \
"s_add_u32 s72, s72, 262144\ns_addc_u32 s73, s73, 0\n" \
"s_add_u32 s66, s66, 1\n" \
"s_waitcnt vmcnt(0)\n" \
"v_lshl_or_b32 v116, v97, 16, v96\n" \
"v_lshl_or_b32 v117, v99, 16, v98\n" \
"v_lshl_or_b32 v118, v101, 16, v100\n" \
"v_lshl_or_b32 v119, v103, 16, v102\n" \
"v_lshl_or_b32 v120, v105, 16, v104\n" \
"v_lshl_or_b32 v121, v107, 16, v106\n" \
"v_lshl_or_b32 v122, v109, 16, v108\n" \
"v_lshl_or_b32 v123, v111, 16, v110\n" \
"s_nop 1\n" \
"v_mfma_f32_16x16x32_bf16 v[136:139], v[116:119], v[112:115], v[136:139]\n" \
"v_mfma_f32_16x16x32_bf16 v[140:143], v[120:123], v[112:115], v[140:143]\n" \
"s_cmp_lt_u32 s66, 32\n" \
"s_cbranch_scc1 LFK_%=\n" \
"s_nop 7\n" \
"v_mov_b32 v144, v148\n" \
"v_mov_b32 v145, v149\n" \
"v_mov_b32 v146, v150\n" \
"v_mov_b32 v147, v151\n" \
"v_mov_b32 v148, v152\n" \
"v_mov_b32 v149, v153\n" \
"v_mov_b32 v150, v154\n" \
"v_mov_b32 v151, v155\n" \
"v_mov_b32 v152, v156\n" \
"v_mov_b32 v153, v157\n" \
"v_mov_b32 v154, v158\n" \
"v_mov_b32 v155, v159\n" \
"v_cvt_pk_bf16_f32 v156, v136, v137\n" \
"v_cvt_pk_bf16_f32 v157, v138, v139\n" \
"v_cvt_pk_bf16_f32 v158, v140, v141\n" \
"v_cvt_pk_bf16_f32 v159, v142, v143\n" \
"v_add_u32 v128, 64, v128\n" \
"v_add_u32 v129, 64, v129\n" \
"v_add_u32 v130, 64, v130\n" \
"v_add_u32 v131, 64, v131\n" \
"v_add_u32 v132, 64, v132\n" \
"v_add_u32 v133, 64, v133\n" \
"v_add_u32 v134, 64, v134\n" \
"v_add_u32 v135, 64, v135\n" \
"s_add_u32 s69, s69, 1\n" \
"s_cmp_lt_u32 s69, 4\n" \
"s_cbranch_scc1 LFQ_%=\n" \
"v_mov_b32 v64, 0\n" \
"v_mov_b32 v65, 0\n" \
"v_mov_b32 v66, 0\n" \
"v_mov_b32 v67, 0\n" \
"v_mov_b32 v68, 0\n" \
"v_mov_b32 v69, 0\n" \
"v_mov_b32 v70, 0\n" \
"v_mov_b32 v71, 0\n" \
"v_mov_b32 v72, 0\n" \
"v_mov_b32 v73, 0\n" \
"v_mov_b32 v74, 0\n" \
"v_mov_b32 v75, 0\n" \
"v_mov_b32 v76, 0\n" \
"v_mov_b32 v77, 0\n" \
"v_mov_b32 v78, 0\n" \
"v_mov_b32 v79, 0\n" \
"v_mov_b32 v80, 0\n" \
"v_mov_b32 v81, 0\n" \
"v_mov_b32 v82, 0\n" \
"v_mov_b32 v83, 0\n" \
"v_mov_b32 v84, 0\n" \
"v_mov_b32 v85, 0\n" \
"v_mov_b32 v86, 0\n" \
"v_mov_b32 v87, 0\n" \
"v_mov_b32 v88, 0\n" \
"v_mov_b32 v89, 0\n" \
"v_mov_b32 v90, 0\n" \
"v_mov_b32 v91, 0\n" \
"v_mov_b32 v92, 0\n" \
"v_mov_b32 v93, 0\n" \
"v_mov_b32 v94, 0\n" \
"v_mov_b32 v95, 0\n" \
"v_mov_b32 v132, 0\n" \
"v_mov_b32 v133, 0\n" \
"v_mov_b32 v134, 0\n" \
"v_mov_b32 v135, 0\n" \
"v_mov_b32 v164, 0xff800000\n" \
"v_mov_b32 v169, 0xff800000\n" \
"s_mov_b32 s69, 0\n" \
"s_lshr_b32 s68, s64, 5\n" \
"LFP_%=:\n" \
"s_mov_b32 s66, 0\n" \
"s_lshl_b32 s67, s6, 20\n" \
"s_add_u32 s67, s67, 33554432\n" \
"v_lshlrev_b32 v166, 2, v168\n" \
"v_sub_u32 v166, v165, v166\n" \
"LFT_%=:\n" \
"buffer_load_dwordx4 v[96:99], v4, s[36:39], s67 offen offset:0 sc1\n" \
"buffer_load_dwordx4 v[100:103], v4, s[36:39], s67 offen offset:1024 sc1\n" \
"buffer_load_dwordx4 v[104:107], v4, s[36:39], s67 offen offset:2048 sc1\n" \
"buffer_load_dwordx4 v[108:111], v4, s[36:39], s67 offen offset:3072 sc1\n" \
"buffer_load_dwordx4 v[112:115], v7, s[36:39], s67 offen offset:0 sc1\n" \
"buffer_load_dwordx4 v[116:119], v7, s[36:39], s67 offen offset:1024 sc1\n" \
"buffer_load_dwordx4 v[120:123], v7, s[36:39], s67 offen offset:2048 sc1\n" \
"buffer_load_dwordx4 v[124:127], v7, s[36:39], s67 offen offset:3072 sc1\n" \
"s_add_u32 s67, s67, 8192\n" \
"s_waitcnt vmcnt(0)\n" \
"v_mfma_f32_16x16x32_bf16 v[136:139], v[96:99], v[144:147], 0\n" \
"v_mfma_f32_16x16x32_bf16 v[136:139], v[100:103], v[148:151], v[136:139]\n" \
"v_mfma_f32_16x16x32_bf16 v[136:139], v[104:107], v[152:155], v[136:139]\n" \
"v_mfma_f32_16x16x32_bf16 v[136:139], v[108:111], v[156:159], v[136:139]\n" \
"v_mfma_f32_16x16x32_bf16 v[140:143], v[112:115], v[144:147], 0\n" \
"v_mfma_f32_16x16x32_bf16 v[140:143], v[116:119], v[148:151], v[140:143]\n" \
"v_mfma_f32_16x16x32_bf16 v[140:143], v[120:123], v[152:155], v[140:143]\n" \
"v_mfma_f32_16x16x32_bf16 v[140:143], v[124:127], v[156:159], v[140:143]\n" \
"s_nop 7\n" \
"v_cmp_gt_i32 vcc, 0, v166\n" \
"s_nop 1\n" \
"v_cndmask_b32 v136, v136, v169, vcc\n" \
"v_cmp_gt_i32 vcc, 1, v166\n" \
"s_nop 1\n" \
"v_cndmask_b32 v137, v137, v169, vcc\n" \
"v_cmp_gt_i32 vcc, 2, v166\n" \
"s_nop 1\n" \
"v_cndmask_b32 v138, v138, v169, vcc\n" \
"v_cmp_gt_i32 vcc, 3, v166\n" \
"s_nop 1\n" \
"v_cndmask_b32 v139, v139, v169, vcc\n" \
"v_cmp_gt_i32 vcc, 16, v166\n" \
"s_nop 1\n" \
"v_cndmask_b32 v140, v140, v169, vcc\n" \
"v_cmp_gt_i32 vcc, 17, v166\n" \
"s_nop 1\n" \
"v_cndmask_b32 v141, v141, v169, vcc\n" \
"v_cmp_gt_i32 vcc, 18, v166\n" \
"s_nop 1\n" \
"v_cndmask_b32 v142, v142, v169, vcc\n" \
"v_cmp_gt_i32 vcc, 19, v166\n" \
"s_nop 1\n" \
"v_cndmask_b32 v143, v143, v169, vcc\n" \
"s_cmp_lg_u32 s69, 0\n" \
"s_cbranch_scc1 LF2_%=\n" \
"v_max3_f32 v164, v164, v136, v137\n" \
"v_max3_f32 v164, v164, v138, v139\n" \
"v_max3_f32 v164, v164, v140, v141\n" \
"v_max3_f32 v164, v164, v142, v143\n" \
"s_branch LFN_%=\n" \
"LF2_%=:\n" \
"buffer_load_dwordx4 v[96:99], v4, s[36:39], s67 offen offset:0 sc1\n" \
"buffer_load_dwordx4 v[100:103], v4, s[36:39], s67 offen offset:1024 sc1\n" \
"buffer_load_dwordx4 v[104:107], v4, s[36:39], s67 offen offset:2048 sc1\n" \
"buffer_load_dwordx4 v[108:111], v4, s[36:39], s67 offen offset:3072 sc1\n" \
"buffer_load_dwordx4 v[112:115], v7, s[36:39], s67 offen offset:0 sc1\n" \
"buffer_load_dwordx4 v[116:119], v7, s[36:39], s67 offen offset:1024 sc1\n" \
"buffer_load_dwordx4 v[120:123], v7, s[36:39], s67 offen offset:2048 sc1\n" \
"buffer_load_dwordx4 v[124:127], v7, s[36:39], s67 offen offset:3072 sc1\n" \
"v_sub_f32 v136, v136, v164\n" \
"v_sub_f32 v137, v137, v164\n" \
"v_sub_f32 v138, v138, v164\n" \
"v_sub_f32 v139, v139, v164\n" \
"v_sub_f32 v140, v140, v164\n" \
"v_sub_f32 v141, v141, v164\n" \
"v_sub_f32 v142, v142, v164\n" \
"v_sub_f32 v143, v143, v164\n" \
"v_exp_f32 v136, v136\n" \
"v_exp_f32 v137, v137\n" \
"v_exp_f32 v138, v138\n" \
"v_exp_f32 v139, v139\n" \
"v_exp_f32 v140, v140\n" \
"v_exp_f32 v141, v141\n" \
"v_exp_f32 v142, v142\n" \
"v_exp_f32 v143, v143\n" \
"s_nop 0\n" \
"v_cvt_pk_bf16_f32 v128, v136, v137\n" \
"v_cvt_pk_bf16_f32 v129, v138, v139\n" \
"v_cvt_pk_bf16_f32 v130, v140, v141\n" \
"v_cvt_pk_bf16_f32 v131, v142, v143\n" \
"s_waitcnt vmcnt(0)\n" \
"s_nop 1\n" \
"v_mfma_f32_16x16x32_bf16 v[132:135], v[160:163], v[128:131], v[132:135]\n" \
"v_mfma_f32_16x16x32_bf16 v[64:67], v[96:99], v[128:131], v[64:67]\n" \
"v_mfma_f32_16x16x32_bf16 v[68:71], v[100:103], v[128:131], v[68:71]\n" \
"v_mfma_f32_16x16x32_bf16 v[72:75], v[104:107], v[128:131], v[72:75]\n" \
"v_mfma_f32_16x16x32_bf16 v[76:79], v[108:111], v[128:131], v[76:79]\n" \
"v_mfma_f32_16x16x32_bf16 v[80:83], v[112:115], v[128:131], v[80:83]\n" \
"v_mfma_f32_16x16x32_bf16 v[84:87], v[116:119], v[128:131], v[84:87]\n" \
"v_mfma_f32_16x16x32_bf16 v[88:91], v[120:123], v[128:131], v[88:91]\n" \
"v_mfma_f32_16x16x32_bf16 v[92:95], v[124:127], v[128:131], v[92:95]\n" \
"LFN_%=:\n" \
"s_add_u32 s67, s67, 8192\n" \
"v_subrev_u32 v166, 32, v166\n" \
"s_add_u32 s66, s66, 1\n" \
"s_cmp_le_u32 s66, s68\n" \
"s_cbranch_scc1 LFT_%=\n" \
"v_mov_b32 v170, v164\n" \
"s_nop 1\n" \
"v_permlane32_swap_b32 v164, v170\n" \
"s_nop 0\n" \
"v_max_f32 v164, v164, v170\n" \
"v_mov_b32 v170, v164\n" \
"s_nop 1\n" \
"v_permlane16_swap_b32 v164, v170\n" \
"s_nop 0\n" \
"v_max_f32 v164, v164, v170\n" \
"s_add_u32 s69, s69, 1\n" \
"s_cmp_lt_u32 s69, 2\n" \
"s_cbranch_scc1 LFP_%=\n" \
"s_nop 7\n" \
"v_rcp_f32 v170, v132\n" \
"s_lshl_b32 s67, s68, 5\n" \
"s_add_u32 s67, s67, s6\n" \
"s_lshl_b32 s67, s67, 13\n" \
"s_and_b32 s66, s64, 16\n" \
"s_lshl_b32 s66, s66, 6\n" \
"s_add_u32 s67, s67, s66\n" \
"v_mul_f32 v64, v170, v64\n" \
"v_mul_f32 v65, v170, v65\n" \
"v_mul_f32 v66, v170, v66\n" \
"v_mul_f32 v67, v170, v67\n" \
"v_mul_f32 v68, v170, v68\n" \
"v_mul_f32 v69, v170, v69\n" \
"v_mul_f32 v70, v170, v70\n" \
"v_mul_f32 v71, v170, v71\n" \
"v_mul_f32 v72, v170, v72\n" \
"v_mul_f32 v73, v170, v73\n" \
"v_mul_f32 v74, v170, v74\n" \
"v_mul_f32 v75, v170, v75\n" \
"v_mul_f32 v76, v170, v76\n" \
"v_mul_f32 v77, v170, v77\n" \
"v_mul_f32 v78, v170, v78\n" \
"v_mul_f32 v79, v170, v79\n" \
"v_mul_f32 v80, v170, v80\n" \
"v_mul_f32 v81, v170, v81\n" \
"v_mul_f32 v82, v170, v82\n" \
"v_mul_f32 v83, v170, v83\n" \
"v_mul_f32 v84, v170, v84\n" \
"v_mul_f32 v85, v170, v85\n" \
"v_mul_f32 v86, v170, v86\n" \
"v_mul_f32 v87, v170, v87\n" \
"v_mul_f32 v88, v170, v88\n" \
"v_mul_f32 v89, v170, v89\n" \
"v_mul_f32 v90, v170, v90\n" \
"v_mul_f32 v91, v170, v91\n" \
"v_mul_f32 v92, v170, v92\n" \
"v_mul_f32 v93, v170, v93\n" \
"v_mul_f32 v94, v170, v94\n" \
"v_mul_f32 v95, v170, v95\n" \
"v_cvt_pk_bf16_f32 v64, v64, v65\n" \
"v_cvt_pk_bf16_f32 v65, v66, v67\n" \
"v_cvt_pk_bf16_f32 v66, v68, v69\n" \
"v_cvt_pk_bf16_f32 v67, v70, v71\n" \
"v_cvt_pk_bf16_f32 v68, v72, v73\n" \
"v_cvt_pk_bf16_f32 v69, v74, v75\n" \
"v_cvt_pk_bf16_f32 v70, v76, v77\n" \
"v_cvt_pk_bf16_f32 v71, v78, v79\n" \
"v_cvt_pk_bf16_f32 v72, v80, v81\n" \
"v_cvt_pk_bf16_f32 v73, v82, v83\n" \
"v_cvt_pk_bf16_f32 v74, v84, v85\n" \
"v_cvt_pk_bf16_f32 v75, v86, v87\n" \
"v_cvt_pk_bf16_f32 v76, v88, v89\n" \
"v_cvt_pk_bf16_f32 v77, v90, v91\n" \
"v_cvt_pk_bf16_f32 v78, v92, v93\n" \
"v_cvt_pk_bf16_f32 v79, v94, v95\n" \
"buffer_store_dwordx4 v[64:67], v4, s[36:39], s67 offen offset:0 sc1\n" \
"buffer_store_dwordx4 v[68:71], v4, s[36:39], s67 offen offset:2048 sc1\n" \
"buffer_store_dwordx4 v[72:75], v7, s[36:39], s67 offen offset:0 sc1\n" \
"buffer_store_dwordx4 v[76:79], v7, s[36:39], s67 offen offset:2048 sc1\n" \
"s_waitcnt vmcnt(0)\n" \
"s_add_u32 s64, s64, 16\n" \
"s_sub_u32 s65, s65, 1\n" \
"s_cmp_lg_u32 s65, 0\n" \
"s_cbranch_scc1 LFG_%=\n" \
"v_readlane_b32 vcc_lo, v172, 0\n" \
"v_readlane_b32 vcc_hi, v172, 1\n" \
"v_readlane_b32 s64, v172, 2\n" \
"v_readlane_b32 s65, v172, 3\n" \
"v_readlane_b32 s66, v172, 4\n" \
"v_readlane_b32 s67, v172, 5\n" \
"v_readlane_b32 s68, v172, 6\n" \
"v_readlane_b32 s69, v172, 7\n" \
"v_readlane_b32 s70, v172, 8\n" \
"v_readlane_b32 s71, v172, 9\n" \
"v_readlane_b32 s72, v172, 10\n" \
"v_readlane_b32 s73, v172, 11\n" \
"s_lshl_b32 s25, %1, 2\n" \
"s_add_u32 s25, s25, %2\n" \
"s_lshl_b32 s25, s25, 13\n" \
"s_add_u32 s25, s25, 16777216\n" \
"buffer_load_dwordx4 v[64:67], v4, s[36:39], s25 offen offset:0\n" \
"buffer_load_dwordx4 v[68:71], v4, s[36:39], s25 offen offset:1024\n" \
"buffer_load_dwordx4 v[72:75], v4, s[36:39], s25 offen offset:2048\n" \
"buffer_load_dwordx4 v[76:79], v4, s[36:39], s25 offen offset:3072\n" \
"buffer_load_dwordx4 v[80:83], v7, s[36:39], s25 offen offset:0\n" \
"buffer_load_dwordx4 v[84:87], v7, s[36:39], s25 offen offset:1024\n" \
"buffer_load_dwordx4 v[88:91], v7, s[36:39], s25 offen offset:2048\n" \
"buffer_load_dwordx4 v[92:95], v7, s[36:39], s25 offen offset:3072\n" \
"s_add_u32 s25, s25, 8388608\n" \
"buffer_load_dwordx4 v[96:99], v4, s[36:39], s25 offen offset:0\n" \
"buffer_load_dwordx4 v[100:103], v4, s[36:39], s25 offen offset:1024\n" \
"buffer_load_dwordx4 v[104:107], v4, s[36:39], s25 offen offset:2048\n" \
"buffer_load_dwordx4 v[108:111], v4, s[36:39], s25 offen offset:3072\n" \
"buffer_load_dwordx4 v[112:115], v7, s[36:39], s25 offen offset:0\n" \
"buffer_load_dwordx4 v[116:119], v7, s[36:39], s25 offen offset:1024\n" \
"buffer_load_dwordx4 v[120:123], v7, s[36:39], s25 offen offset:2048\n" \
"buffer_load_dwordx4 v[124:127], v7, s[36:39], s25 offen offset:3072\n" \
"s_add_u32 s25, s25, 58720256\n" \
"buffer_load_dwordx4 v[128:131], v4, s[36:39], s25 offen offset:0\n" \
"buffer_load_dwordx4 v[132:135], v4, s[36:39], s25 offen offset:1024\n" \
"buffer_load_dwordx4 v[136:139], v4, s[36:39], s25 offen offset:2048\n" \
"buffer_load_dwordx4 v[140:143], v4, s[36:39], s25 offen offset:3072\n" \
"buffer_load_dwordx4 v[144:147], v7, s[36:39], s25 offen offset:0\n" \
"buffer_load_dwordx4 v[148:151], v7, s[36:39], s25 offen offset:1024\n" \
"buffer_load_dwordx4 v[152:155], v7, s[36:39], s25 offen offset:2048\n" \
"buffer_load_dwordx4 v[156:159], v7, s[36:39], s25 offen offset:3072\n" \
"s_add_u32 s25, s25, 37748736\n" \
"buffer_load_dwordx4 v[160:163], v4, s[36:39], s25 offen offset:0\n" \
"buffer_load_dwordx4 v[164:167], v4, s[36:39], s25 offen offset:1024\n" \
"buffer_load_dwordx4 v[168:171], v4, s[36:39], s25 offen offset:2048\n" \
"buffer_load_dwordx4 v[172:175], v4, s[36:39], s25 offen offset:3072\n" \
"s_waitcnt vmcnt(0)\n" \
"s_cmp_eq_u32 s99, 0\n" \
"s_cbranch_scc1 LFBAR_%=\n" \
"s_cmp_eq_u32 s99, 1\n" \
"s_cbranch_scc1 LFBBR_%=\n" \
"s_branch LFBCR_%=\n" \
"LEND_%=:\n" \
"s_waitcnt vmcnt(0) lgkmcnt(0)\n" \
""
#define QKV_ASM_CLOBBERS "v1", "v2", "v3", "v4", "v5", "v6", "v7", "v8", "v9", "v10", "v11", "v12", "v13", "v14", "v15", "v16", "v17", "v18", "v19", "v20", "v21", "v22", "v23", "v24", "v25", "v26", "v27", "v28", "v29", "v30", "v31", "v32", "v33", "v34", "v35", "v36", "v37", "v38", "v39", "v40", "v41", "v42", "v43", "v44", "v45", "v46", "v47", "v48", "v49", "v50", "v51", "v52", "v53", "v54", "v55", "v56", "v57", "v58", "v59", "v60", "v61", "v62", "v63", "v64", "v65", "v66", "v67", "v68", "v69", "v70", "v71", "v72", "v73", "v74", "v75", "v76", "v77", "v78", "v79", "v80", "v81", "v82", "v83", "v84", "v85", "v86", "v87", "v88", "v89", "v90", "v91", "v92", "v93", "v94", "v95", "v96", "v97", "v98", "v99", "v100", "v101", "v102", "v103", "v104", "v105", "v106", "v107", "v108", "v109", "v110", "v111", "v112", "v113", "v114", "v115", "v116", "v117", "v118", "v119", "v120", "v121", "v122", "v123", "v124", "v125", "v126", "v127", "v128", "v129", "v130", "v131", "v132", "v133", "v134", "v135", "v136", "v137", "v138", "v139", "v140", "v141", "v142", "v143", "v144", "v145", "v146", "v147", "v148", "v149", "v150", "v151", "v152", "v153", "v154", "v155", "v156", "v157", "v158", "v159", "v160", "v161", "v162", "v163", "v164", "v165", "v166", "v167", "v168", "v169", "v170", "v171", "v172", "v173", "v174", "v175", "v176", "v177", "v178", "v179", "v180", "v181", "v182", "v183", "v184", "v185", "v186", "v187", "v188", "v189", "v190", "v191", "v192", "v193", "v194", "v195", "v196", "v197", "v198", "v199", "v200", "v201", "v202", "v203", "v204", "v205", "v206", "v207", "v208", "v209", "v210", "v211", "v212", "v213", "v214", "v215", "v216", "v217", "v218", "v219", "v220", "v221", "v222", "v223", "v224", "v225", "v226", "v227", "v228", "v229", "v230", "v231", "v232", "v233", "v234", "v235", "v236", "v237", "v238", "v239", "v240", "v241", "v242", "v243", "v244", "v245", "v246", "v247", "v248", "v249", "v250", "v251", "v252", "v253", "v254", "v255", "a0", "a1", "a2", "a3", "a4", "a5", "a6", "a7", "a8", "a9", "a10", "a11", "a12", "a13", "a14", "a15", "a16", "a17", "a18", "a19", "a20", "a21", "a22", "a23", "a24", "a25", "a26", "a27", "a28", "a29", "a30", "a31", "a32", "a33", "a34", "a35", "a36", "a37", "a38", "a39", "a40", "a41", "a42", "a43", "a44", "a45", "a46", "a47", "a48", "a49", "a50", "a51", "a52", "a53", "a54", "a55", "a56", "a57", "a58", "a59", "a60", "a61", "a62", "a63", "a64", "a65", "a66", "a67", "a68", "a69", "a70", "a71", "a72", "a73", "a74", "a75", "a76", "a77", "a78", "a79", "a80", "a81", "a82", "a83", "a84", "a85", "a86", "a87", "a88", "a89", "a90", "a91", "a92", "a93", "a94", "a95", "a96", "a97", "a98", "a99", "a100", "a101", "a102", "a103", "a104", "a105", "a106", "a107", "a108", "a109", "a110", "a111", "a112", "a113", "a114", "a115", "a116", "a117", "a118", "a119", "a120", "a121", "a122", "a123", "a124", "a125", "a126", "a127", "a128", "a129", "a130", "a131", "a132", "a133", "a134", "a135", "a136", "a137", "a138", "a139", "a140", "a141", "a142", "a143", "a144", "a145", "a146", "a147", "a148", "a149", "a150", "a151", "a152", "a153", "a154", "a155", "a156", "a157", "a158", "a159", "a160", "a161", "a162", "a163", "a164", "a165", "a166", "a167", "a168", "a169", "a170", "a171", "a172", "a173", "a174", "a175", "a176", "a177", "a178", "a179", "a180", "a181", "a182", "a183", "a184", "a185", "a186", "a187", "a188", "a189", "a190", "a191", "a192", "a193", "a194", "a195", "a196", "a197", "a198", "a199", "a200", "a201", "a202", "a203", "a204", "a205", "a206", "a207", "a208", "a209", "a210", "a211", "a212", "a213", "a214", "a215", "a216", "a217", "a218", "a219", "a220", "a221", "a222", "a223", "a224", "a225", "a226", "a227", "a228", "a229", "a230", "a231", "a232", "a233", "a234", "a235", "a236", "a237", "a238", "a239", "a240", "a241", "a242", "a243", "a244", "a245", "a246", "a247", "a248", "a249", "a250", "a251", "a252", "a253", "a254", "a255", "s4", "s5", "s6", "s7", "s8", "s9", "s10", "s11", "s12", "s13", "s14", "s15", "s16", "s17", "s18", "s19", "s20", "s21", "s22", "s23", "s24", "s25", "s26", "s27", "s28", "s29", "s30", "s31", "s32", "s33", "s34", "s35", "s36", "s37", "s38", "s39", "s40", "s41", "s42", "s43", "s44", "s45", "s46", "s47", "s48", "s49", "s50", "s51", "s52", "s53", "s54", "s55", "s56", "s57", "s58", "s59", "s60", "s61", "s62", "s63", "s64", "s65", "s66", "s67", "s68", "s69", "s70", "s71", "s72", "s73", "s74", "s75", "s76", "s77", "s78", "s79", "s80", "s81", "s82", "s83", "s84", "s85", "s86", "s87", "s88", "s89", "s90", "s91", "s92", "s93", "s94", "s95", "s96", "s97", "s98", "s99", "s100", "s101", "vcc", "scc", "memory"
constexpr int Q_LDS = 163840;
__global__ __launch_bounds__(256, 1) void fused_kernel(const void* x, const void* k, const void* v, const void* shk, const void* ssmk, const void* ssmD, const void* ckv,
                                                       const void* wq, const void* wuk, const void* wuv, const void* wo, const void* gw, void* out, void* ws) {
    const int w = __builtin_amdgcn_readfirstlane(threadIdx.x >> 6);
    const unsigned long long ka = (unsigned long long)__builtin_amdgcn_kernarg_segment_ptr();
    asm volatile(QKV_ASM_TEXT :: "s"(ka), "s"(blockIdx.x), "s"(w) : QKV_ASM_CLOBBERS);
}

extern "C" void kernel_launch(void* const* d_in, const int* in_sizes, int n_in, void* d_out, int out_size, void* d_ws, size_t ws_size,
                              hipStream_t stream) {
    (void)hipFuncSetAttribute((const void*)fused_kernel, hipFuncAttributeMaxDynamicSharedMemorySize, Q_LDS);
    fused_kernel<<<256, 256, Q_LDS, stream>>>(d_in[0], d_in[1], d_in[2], d_in[3], d_in[4], d_in[5], d_in[7], d_in[8], d_in[9], d_in[10], d_in[11], d_in[12], d_out, d_ws);
}
```

```cpp
#include <hip/hip_runtime.h>
#include <stdint.h>
#define QKV_ASM_TEXT \
"s_nop 0\n" \
"s_load_dwordx16 s[64:79], %0, 0x0\n" \
"s_load_dwordx8 s[80:87], %0, 0x40\n" \
"s_load_dwordx4 s[88:91], %0, 0x60\n" \
"v_mbcnt_lo_u32_b32 v23, -1, 0\n" \
"v_mbcnt_hi_u32_b32 v23, -1, v23\n" \
"s_bfe_u32 s58, %1, 0x30003\n" \
"s_and_b32 s59, %1, 7\n" \
"s_lshl_b32 s59, s59, 1\n" \
"s_bfe_u32 s60, %1, 0x10006\n" \
"s_add_u32 s59, s59, s60\n" \
"v_lshlrev_b32 v4, 4, v23\n" \
"s_getpc_b64 s[40:41]\nLCTBEG_%=:\n" \
"s_add_u32 s40, s40, LBEG_%=-LCTBEG_%=\ns_addc_u32 s41, s41, 0\n" \
"s_and_b32 s41, s41, 0xffff\n" \
"s_mov_b32 s42, LEND_%=-LBEG_%=\n" \
"s_mov_b32 s43, 0x00020000\n" \
"s_lshr_b32 s62, %1, 3\n" \
"s_lshl_b32 s62, s62, 2\n" \
"s_add_u32 s62, s62, %2\n" \
"s_and_b32 s62, s62, 31\n" \
"s_lshl_b32 s62, s62, 13\n" \
"v_lshlrev_b32 v64, 3, v4\n" \
"buffer_load_dword v64, v64, s[40:43], s62 offen\n" \
"s_lshl_b32 s60, %2, 2\n" \
"v_lshrrev_b32 v24, 4, v23\n" \
"v_add_u32 v25, s60, v24\n" \
"v_xor_b32 v25, v25, v23\n" \
"v_and_b32 v25, 7, v25\n" \
"s_lshl_b32 s61, %2, 3\n" \
"v_lshrrev_b32 v26, 3, v23\n" \
"v_add_u32 v26, s61, v26\n" \
"v_lshlrev_b32 v26, 10, v26\n" \
"v_lshl_add_u32 v13, v25, 4, v26\n" \
"v_add_u32 v63, v13, v26\n" \
"v_add_u32 v14, 0x8000, v13\n" \
"v_add_u32 v15, 0x8000, v14\n" \
"v_add_u32 v16, 0x8000, v15\n" \
"v_add_u32 v17, 0x8000, v16\n" \
"v_add_u32 v18, 0x8000, v17\n" \
"v_add_u32 v19, 0x8000, v18\n" \
"v_add_u32 v20, 0x8000, v19\n" \
"v_bfe_u32 v25, v23, 2, 3\n" \
"v_add_u32 v25, s61, v25\n" \
"v_lshlrev_b32 v25, 13, v25\n" \
"s_lshl_b32 s61, %2, 1\n" \
"v_and_b32 v26, 1, v24\n" \
"v_add_u32 v26, s61, v26\n" \
"v_xor_b32 v26, v26, v23\n" \
"v_and_b32 v26, 3, v26\n" \
"v_lshrrev_b32 v27, 5, v23\n" \
"v_lshl_add_u32 v26, v27, 2, v26\n" \
"v_lshl_add_u32 v21, v26, 4, v25\n" \
"s_lshl_b32 s50, %2, 10\n" \
"s_add_u32 s50, s50, 65536\n" \
"s_lshl_b32 s22, %2, 12\n" \
"s_lshl_b32 s51, %2, 11\n" \
"s_mov_b32 s31, 0x00020000\n" \
"s_mov_b32 s35, 0x00020000\n" \
"s_mov_b32 s30, 262144\n" \
"s_lshl_b32 s60, s58, 18\n" \
"s_lshl_b32 s61, s59, 9\n" \
"s_sub_u32 s34, 4194304, s61\n" \
"s_mov_b32 s46, s60\n" \
"s_mov_b32 s47, s61\n" \
"s_lshl_b32 s57, s59, 1\n" \
"s_and_b32 s60, %2, 1\n" \
"s_add_u32 s57, s57, s60\n" \
"s_lshl_b32 s57, s57, 20\n" \
"s_lshl_b32 s60, s58, 17\n" \
"s_add_u32 s57, s57, s60\n" \
"s_lshr_b32 s60, %2, 1\n" \
"s_lshl_b32 s60, s60, 16\n" \
"s_add_u32 s57, s57, s60\n" \
"s_add_u32 s57, s57, 33554432\n" \
"s_lshl_b32 s24, s59, 7\n" \
"s_lshl_b32 s60, s58, 3\n" \
"s_add_u32 s24, s24, s60\n" \
"s_lshl_b32 s60, %2, 27\n" \
"s_add_u32 s24, s24, s60\n" \
"s_and_b32 s23, %1, 7\n" \
"s_lshl_b32 s23, s23, 8\n" \
"s_add_u32 s23, s23, s60\n" \
"s_and_b32 s60, %1, 0xf8\n" \
"s_add_u32 s23, s23, s60\n" \
"s_add_u32 s23, s23, 94380032\n" \
"s_cmp_lt_u32 %1, 128\n" \
"s_cselect_b32 s60, 0, 8192\n" \
"s_cselect_b32 s61, 0, 64\n" \
"s_cselect_b32 s62, 10, 14\n" \
"s_cselect_b32 s63, 14, 11\n" \
"s_cselect_b32 s98, 12, 10\n" \
"s_cselect_b32 s20, 0x3e0293ee, 1.0\n" \
"s_add_u32 s57, s57, s60\n" \
"s_add_u32 s24, s24, s61\n" \
"s_add_u32 s24, s24, 94375936\n" \
"s_lshl_b32 s92, 1, s62\n" \
"s_lshl_b32 s93, 1, s63\n" \
"s_lshl_b32 s98, 1, s98\n" \
"s_and_b32 s6, %1, 7\n" \
"s_lshl_b32 s8, s6, 5\n" \
"s_lshl_b32 s6, s6, 2\n" \
"s_lshr_b32 s58, %1, 6\n" \
"s_add_u32 s6, s6, s58\n" \
"s_lshr_b32 s58, %1, 3\n" \
"s_add_u32 s8, s8, s58\n" \
"s_and_b32 s7, s58, 7\n" \
"s_lshl_b32 s58, s6, 8\n" \
"s_mov_b32 s94, s58\n" \
"s_sub_u32 s49, 8388608, s58\n" \
"s_waitcnt lgkmcnt(0)\n" \
"s_cmp_lt_u32 %1, 128\n" \
"s_cselect_b64 s[32:33], s[80:81], s[82:83]\n" \
"s_add_u32 s28, s76, s46\ns_addc_u32 s29, s77, 0\n" \
"s_and_b32 s29, s29, 0xffff\n" \
"s_add_u32 s32, s32, s47\ns_addc_u32 s33, s33, 0\n" \
"s_and_b32 s33, s33, 0xffff\n" \
"s_mov_b32 s99, 94371840\n" \
"s_load_dwordx2 s[14:15], s[90:91], s99\n" \
"s_mov_b32 s46, 0\n" \
"s_add_u32 m0, s50, 65536\n" \
"s_mov_b64 s[36:37], s[90:91]\n" \
"buffer_load_dwordx4 v13, s[28:31], s46 offen lds\n" \
"s_and_b32 s37, s37, 0xffff\n" \
"s_mov_b32 s38, 134217728\n" \
"s_mov_b32 s39, 0x00020000\n" \
"v_lshrrev_b32 v23, 4, v4\n" \
"s_add_u32 m0, s50, 69632\n" \
"v_and_b32 v24, 15, v23\n" \
"buffer_load_dwordx4 v14, s[28:31], s46 offen lds\n" \
"v_lshrrev_b32 v25, 4, v23\n" \
"s_lshr_b32 s58, %2, 1\n" \
"s_lshl_b32 s58, s58, 14\n" \
"s_and_b32 s54, %2, 1\n" \
"s_add_u32 m0, s50, 73728\n" \
"s_lshl_b32 s54, s54, 13\n" \
"buffer_load_dwordx4 v15, s[28:31], s46 offen lds\n" \
"s_add_u32 s58, s58, 65536\n" \
"v_lshrrev_b32 v26, 1, v24\n" \
"v_xor_b32 v26, v26, v25\n" \
"v_lshlrev_b32 v26, 4, v26\n" \
"s_add_u32 m0, s50, 77824\n" \
"v_lshl_add_u32 v26, v24, 7, v26\n" \
"buffer_load_dwordx4 v16, s[28:31], s46 offen lds\n" \
"v_add_u32 v5, s58, v26\n" \
"v_xor_b32 v6, 64, v5\n" \
"v_add_u32 v7, 0x10000, v5\n" \
"v_add_u32 v8, 0x10000, v6\n" \
"s_mov_b32 s47, 0\n" \
"s_add_u32 m0, s22, 0\n" \
"s_and_b32 s58, %2, 1\n" \
"buffer_load_dwordx4 v21, s[32:35], s47 offen offset:0 nt lds\n" \
"s_lshl_b32 s58, s58, 11\n" \
"v_lshrrev_b32 v26, 2, v24\n" \
"v_lshlrev_b32 v26, 6, v26\n" \
"v_and_b32 v27, 1, v23\n" \
"s_mov_b32 s46, 0\n" \
"s_add_u32 m0, s50, 81920\n" \
"v_lshl_add_u32 v26, v27, 3, v26\n" \
"buffer_load_dwordx4 v17, s[28:31], s46 offen lds\n" \
"v_bfe_u32 v27, v23, 1, 1\n" \
"v_lshlrev_b32 v28, 1, v25\n" \
"v_xor_b32 v27, v27, v28\n" \
"v_and_b32 v27, 3, v27\n" \
"s_add_u32 m0, s22, 896\n" \
"v_lshl_add_u32 v26, v27, 4, v26\n" \
"buffer_load_dwordx4 v21, s[32:35], s47 offen offset:128 nt lds\n" \
"v_lshl_add_u32 v9, v25, 12, v26\n" \
"v_add_u32 v9, s58, v9\n" \
"v_lshl_add_u32 v62, v25, 11, s58\n" \
"v_xor_b32 v10, 0x110, v9\n" \
"s_add_u32 m0, s50, 86016\n" \
"v_xor_b32 v11, 32, v9\n" \
"buffer_load_dwordx4 v18, s[28:31], s46 offen lds\n" \
"v_xor_b32 v12, 0x130, v9\n" \
"s_and_b32 s62, %1, 7\n" \
"s_lshl_b32 s62, s62, 7\n" \
"s_lshl_b32 s63, %2, 6\n" \
"s_add_u32 m0, s22, 1792\n" \
"s_and_b32 s63, s63, 127\n" \
"buffer_load_dwordx4 v21, s[32:35], s47 offen offset:256 nt lds\n" \
"s_add_u32 s62, s62, s63\n" \
"v_bfe_u32 v24, v4, 4, 4\n" \
"v_lshl_add_u32 v24, v24, 2, s62\n" \
"s_lshr_b32 s62, %2, 1\n" \
"s_add_u32 m0, s50, 90112\n" \
"s_lshl_b32 s62, s62, 2\n" \
"buffer_load_dwordx4 v19, s[28:31], s46 offen lds\n" \
"s_and_b32 s63, %1, 0xf8\n" \
"s_add_u32 s63, s63, s62\n" \
"v_lshrrev_b32 v25, 8, v4\n" \
"v_lshrrev_b32 v23, 7, v4\n" \
"s_add_u32 m0, s22, 2688\n" \
"v_add_u32 v25, s63, v25\n" \
"buffer_load_dwordx4 v21, s[32:35], s47 offen offset:384 nt lds\n" \
"v_lshlrev_b32 v26, 14, v25\n" \
"v_lshl_add_u32 v59, v24, 1, v26\n" \
"v_add_u32 v54, 0xffffe800, v59\n" \
"v_add_u32 v55, 0xfffff800, v59\n" \
"s_add_u32 m0, s50, 94208\n" \
"v_lshlrev_b32 v60, 12, v24\n" \
"buffer_load_dwordx4 v20, s[28:31], s46 offen lds\n" \
"v_lshl_add_u32 v60, v25, 4, v60\n" \
"v_lshlrev_b32 v27, 3, v24\n" \
"s_lshl_b32 s62, %1, 2\n" \
"s_add_u32 s62, s62, %2\n" \
"s_mov_b32 s47, 262144\n" \
"s_add_u32 m0, s22, 16384\n" \
"s_lshl_b32 s53, s62, 12\n" \
"buffer_load_dwordx4 v21, s[32:35], s47 offen offset:0 nt lds\n" \
"s_and_b32 s63, s62, 7\n" \
"s_lshl_b32 s63, s63, 4\n" \
"s_add_u32 s63, s63, 71303168\n" \
"v_and_b32 v61, 4, v23\n" \
"s_add_u32 m0, s22, 17280\n" \
"v_lshl_add_u32 v61, v61, 1, s63\n" \
"buffer_load_dwordx4 v21, s[32:35], s47 offen offset:128 nt lds\n" \
"s_bfe_u32 s63, s62, 0x10003\n" \
"s_lshl_b32 s63, s63, 2\n" \
"s_lshr_b32 s62, s62, 4\n" \
"s_lshl_b32 s62, s62, 7\n" \
"s_add_u32 m0, s22, 18176\n" \
"s_add_u32 s62, s62, s63\n" \
"buffer_load_dwordx4 v21, s[32:35], s47 offen offset:256 nt lds\n" \
"v_add_u32 v61, s62, v61\n" \
"v_lshlrev_b32 v2, 3, v4\n" \
"v_and_b32 v1, 0x380, v2\n" \
"v_lshrrev_b32 v23, 7, v4\n" \
"s_add_u32 m0, s22, 19072\n" \
"v_lshl_add_u32 v1, v23, 13, v1\n" \
"buffer_load_dwordx4 v21, s[32:35], s47 offen offset:384 nt lds\n" \
"s_lshr_b32 s58, %1, 3\n" \
"s_lshl_b32 s58, s58, 2\n" \
"s_add_u32 s58, s58, %2\n" \
"s_and_b32 s60, %1, 7\n" \
"s_mov_b32 s47, 524288\n" \
"s_add_u32 m0, s22, 32768\n" \
"s_lshl_b32 s60, s60, 10\n" \
"buffer_load_dwordx4 v21, s[32:35], s47 offen offset:0 nt lds\n" \
"s_lshl_b32 s59, s58, 14\n" \
"s_add_u32 s59, s59, s60\n" \
"s_mov_b64 s[4:5], s[90:91]\n" \
"s_lshl_b32 s95, s58, 11\n" \
"s_add_u32 m0, s22, 33664\n" \
"s_lshl_b32 s96, s60, 8\n" \
"buffer_load_dwordx4 v21, s[32:35], s47 offen offset:128 nt lds\n" \
"s_add_u32 s95, s95, s96\n" \
"s_add_u32 s96, s76, s95\ns_addc_u32 s97, s77, 0\n" \
"s_add_u32 s76, s78, s59\ns_addc_u32 s77, s79, 0\n" \
"s_and_b32 s59, s59, 1048575\n" \
"s_add_u32 m0, s22, 34560\n" \
"s_cmp_lt_u32 s58, 64\ns_cselect_b64 s[84:85], s[80:81], s[82:83]\n" \
"buffer_load_dwordx4 v21, s[32:35], s47 offen offset:256 nt lds\n" \
"s_add_u32 s84, s84, s59\ns_addc_u32 s85, s85, 0\n" \
"s_lshl_b32 s59, s8, 2\n" \
"s_add_u32 s59, s59, %2\n" \
"s_lshl_b32 s59, s59, 12\n" \
"s_add_u32 m0, s22, 35456\n" \
"s_add_u32 s88, s72, s59\ns_addc_u32 s89, s73, 0\n" \
"buffer_load_dwordx4 v21, s[32:35], s47 offen offset:384 nt lds\n" \
"s_mov_b64 s[4:5], s[90:91]\n" \
"s_mov_b64 s[10:11], s[72:73]\n" \
"s_mov_b64 s[12:13], s[74:75]\n" \
"s_add_u32 s44, s78, s94\ns_addc_u32 s45, s79, 0\n" \
"s_mov_b32 s46, 128\n" \
"s_add_u32 m0, s50, 0\n" \
"s_mov_b64 s[40:41], s[66:67]\n" \
"buffer_load_dwordx4 v13, s[28:31], s46 offen lds\n" \
"s_mov_b64 s[80:81], s[68:69]\n" \
"s_and_b32 s81, s81, 0xffff\n" \
"s_mov_b32 s82, 4194304\n" \
"s_mov_b32 s83, 0x00020000\n" \
"s_add_u32 m0, s50, 4096\n" \
"s_and_b32 s45, s45, 0xffff\n" \
"buffer_load_dwordx4 v14, s[28:31], s46 offen lds\n" \
"s_and_b32 s41, s41, 0xffff\n" \
"s_mov_b32 s42, 4194304\n" \
"s_mov_b32 s43, 0x00020000\n" \
"s_mov_b64 exec, 0xffff\nglobal_load_dword v22, v1, s[84:85]\ns_mov_b64 exec, -1\n" \
"s_mov_b64 exec, 0xffff\nglobal_load_dword v22, v2, s[96:97]\ns_mov_b64 exec, -1\n" \
"s_cmp_lt_u32 %1, 128\ns_cbranch_scc0 LHV_%=\ns_mov_b64 exec, 0\ns_cmp_eq_u32 %2, 0\ns_cbranch_scc1 LH0_%=\ns_cmp_eq_u32 %2, 1\ns_cbranch_scc1 LH1_%=\ns_cmp_eq_u32 %2, 2\ns_cbranch_scc1 LH2_%=\ns_cmp_eq_u32 %2, 3\ns_cbranch_scc1 LH3_%=\nLHV_%=:\ns_mov_b64 exec, 0\ns_cmp_eq_u32 %2, 0\ns_cbranch_scc1 LH4_%=\ns_cmp_eq_u32 %2, 1\ns_cbranch_scc1 LH5_%=\ns_cmp_eq_u32 %2, 2\ns_cbranch_scc1 LH6_%=\ns_cmp_eq_u32 %2, 3\ns_cbranch_scc1 LH7_%=\nLHR0_%=:\ns_mov_b64 exec, -1\n" \
"s_waitcnt vmcnt(8)\n" \
"s_barrier\n" \
"s_add_u32 m0, s50, 8192\n" \
"ds_read_b128 a[0:3], v7 offset:0\n" \
"ds_read_b64_tr_b16 a[32:33], v9 offset:0\n" \
"ds_read_b64_tr_b16 a[34:35], v10 offset:0\n" \
"ds_read_b128 a[4:7], v7 offset:2048\n" \
"buffer_load_dwordx4 v15, s[28:31], s46 offen lds\n" \
"s_add_u32 m0, s50, 12288\n" \
"ds_read_b64_tr_b16 a[36:37], v11 offset:0\n" \
"ds_read_b64_tr_b16 a[38:39], v12 offset:0\n" \
"ds_read_b128 a[8:11], v7 offset:4096\n" \
"ds_read_b64_tr_b16 a[40:41], v9 offset:512\n" \
"buffer_load_dwordx4 v16, s[28:31], s46 offen lds\n" \
"s_mov_b32 s46, 128\n" \
"s_add_u32 m0, s50, 16384\n" \
"ds_read_b64_tr_b16 a[42:43], v10 offset:512\n" \
"ds_read_b128 a[12:15], v7 offset:6144\n" \
"ds_read_b64_tr_b16 a[44:45], v11 offset:512\n" \
"ds_read_b64_tr_b16 a[46:47], v12 offset:512\n" \
"buffer_load_dwordx4 v17, s[28:31], s46 offen lds\n" \
"s_add_u32 m0, s50, 20480\n" \
"ds_read_b128 a[16:19], v7 offset:8192\n" \
"ds_read_b64_tr_b16 a[48:49], v9 offset:1024\n" \
"ds_read_b64_tr_b16 a[50:51], v10 offset:1024\n" \
"ds_read_b128 a[20:23], v7 offset:10240\n" \
"buffer_load_dwordx4 v18, s[28:31], s46 offen lds\n" \
"s_add_u32 m0, s50, 24576\n" \
"ds_read_b64_tr_b16 a[52:53], v11 offset:1024\n" \
"ds_read_b64_tr_b16 a[54:55], v12 offset:1024\n" \
"ds_read_b128 a[24:27], v7 offset:12288\n" \
"ds_read_b64_tr_b16 a[56:57], v9 offset:1536\n" \
"buffer_load_dwordx4 v19, s[28:31], s46 offen lds\n" \
"s_add_u32 m0, s50, 28672\n" \
"ds_read_b64_tr_b16 a[58:59], v10 offset:1536\n" \
"ds_read_b128 a[28:31], v7 offset:14336\n" \
"ds_read_b64_tr_b16 a[60:61], v11 offset:1536\n" \
"ds_read_b64_tr_b16 a[62:63], v12 offset:1536\n" \
"buffer_load_dwordx4 v20, s[28:31], s46 offen lds\n" \
"s_waitcnt lgkmcnt(0)\n" \
"s_branch LBEG_%=\n" \
".p2align 12\n" \
"LBEG_%=:\n" \
"s_cmp_lt_u32 %1, 128\n" \
"s_cbranch_scc0 LAV_%=\n" \
"v_mfma_f32_16x16x32_bf16 v[64:67], a[32:35], a[0:3], 0\n" \
"s_add_u32 s14, s14, 1\ns_addc_u32 s15, s15, 0\n" \
"ds_read_b128 a[64:67], v8 offset:0\n" \
"v_mfma_f32_16x16x32_bf16 v[68:71], a[36:39], a[0:3], 0\n" \
"v_mfma_f32_16x16x32_bf16 v[72:75], a[32:35], a[4:7], 0\n" \
"s_mov_b32 s47, 786432\n" \
"s_add_u32 m0, s22, 49152\n" \
"ds_read_b64_tr_b16 a[96:97], v9 offset:16384\n" \
"v_mfma_f32_16x16x32_bf16 v[76:79], a[36:39], a[4:7], 0\n" \
"buffer_load_dwordx4 v21, s[32:35], s47 offen offset:0 nt lds\n" \
"v_mfma_f32_16x16x32_bf16 v[80:83], a[40:43], a[0:3], 0\n" \
"ds_read_b64_tr_b16 a[98:99], v10 offset:16384\n" \
"v_mfma_f32_16x16x32_bf16 v[84:87], a[44:47], a[0:3], 0\n" \
"v_mfma_f32_16x16x32_bf16 v[88:91], a[40:43], a[4:7], 0\n" \
"s_add_u32 m0, s22, 50048\n" \
"ds_read_b128 a[68:71], v8 offset:2048\n" \
"v_mfma_f32_16x16x32_bf16 v[92:95], a[44:47], a[4:7], 0\n" \
"buffer_load_dwordx4 v21, s[32:35], s47 offen offset:128 nt lds\n" \
"v_mfma_f32_16x16x32_bf16 v[96:99], a[48:51], a[0:3], 0\n" \
"ds_read_b64_tr_b16 a[100:101], v11 offset:16384\n" \
"v_mfma_f32_16x16x32_bf16 v[100:103], a[52:55], a[0:3], 0\n" \
"v_mfma_f32_16x16x32_bf16 v[104:107], a[48:51], a[4:7], 0\n" \
"s_add_u32 m0, s22, 50944\n" \
"ds_read_b64_tr_b16 a[102:103], v12 offset:16384\n" \
"v_mfma_f32_16x16x32_bf16 v[108:111], a[52:55], a[4:7], 0\n" \
"buffer_load_dwordx4 v21, s[32:35], s47 offen offset:256 nt lds\n" \
"v_mfma_f32_16x16x32_bf16 v[112:115], a[56:59], a[0:3], 0\n" \
"ds_read_b128 a[72:75], v8 offset:4096\n" \
"v_mfma_f32_16x16x32_bf16 v[116:119], a[60:63], a[0:3], 0\n" \
"v_mfma_f32_16x16x32_bf16 v[120:123], a[56:59], a[4:7], 0\n" \
"s_add_u32 m0, s22, 51840\n" \
"ds_read_b64_tr_b16 a[104:105], v9 offset:16896\n" \
"v_mfma_f32_16x16x32_bf16 v[124:127], a[60:63], a[4:7], 0\n" \
"buffer_load_dwordx4 v21, s[32:35], s47 offen offset:384 nt lds\n" \
"v_mfma_f32_16x16x32_bf16 v[128:131], a[32:35], a[8:11], 0\n" \
"ds_read_b64_tr_b16 a[106:107], v10 offset:16896\n" \
"v_mfma_f32_16x16x32_bf16 v[132:135], a[36:39], a[8:11], 0\n" \
"v_mfma_f32_16x16x32_bf16 v[136:139], a[32:35], a[12:15], 0\n" \
"s_mov_b32 s46, 256\n" \
"s_add_u32 m0, s50, 32768\n" \
"ds_read_b128 a[76:79], v8 offset:6144\n" \
"v_mfma_f32_16x16x32_bf16 v[140:143], a[36:39], a[12:15], 0\n" \
"buffer_load_dwordx4 v13, s[28:31], s46 offen lds\n" \
"v_mfma_f32_16x16x32_bf16 v[144:147], a[40:43], a[8:11], 0\n" \
"ds_read_b64_tr_b16 a[108:109], v11 offset:16896\n" \
"v_mfma_f32_16x16x32_bf16 v[148:151], a[44:47], a[8:11], 0\n" \
"v_mfma_f32_16x16x32_bf16 v[152:155], a[40:43], a[12:15], 0\n" \
"s_add_u32 m0, s50, 36864\n" \
"ds_read_b64_tr_b16 a[110:111], v12 offset:16896\n" \
"v_mfma_f32_16x16x32_bf16 v[156:159], a[44:47], a[12:15], 0\n" \
"buffer_load_dwordx4 v14, s[28:31], s46 offen lds\n" \
"v_mfma_f32_16x16x32_bf16 v[160:163], a[48:51], a[8:11], 0\n" \
"ds_read_b128 a[80:83], v8 offset:8192\n" \
"v_mfma_f32_16x16x32_bf16 v[164:167], a[52:55], a[8:11], 0\n" \
"v_mfma_f32_16x16x32_bf16 v[168:171], a[48:51], a[12:15], 0\n" \
"s_add_u32 m0, s50, 40960\n" \
"ds_read_b64_tr_b16 a[112:113], v9 offset:17408\n" \
"v_mfma_f32_16x16x32_bf16 v[172:175], a[52:55], a[12:15], 0\n" \
"buffer_load_dwordx4 v15, s[28:31], s46 offen lds\n" \
"v_mfma_f32_16x16x32_bf16 v[176:179], a[56:59], a[8:11], 0\n" \
"ds_read_b64_tr_b16 a[114:115], v10 offset:17408\n" \
"v_mfma_f32_16x16x32_bf16 v[180:183], a[60:63], a[8:11], 0\n" \
"v_mfma_f32_16x16x32_bf16 v[184:187], a[56:59], a[12:15], 0\n" \
"s_add_u32 m0, s50, 45056\n" \
"ds_read_b128 a[84:87], v8 offset:10240\n" \
"v_mfma_f32_16x16x32_bf16 v[188:191], a[60:63], a[12:15], 0\n" \
"buffer_load_dwordx4 v16, s[28:31], s46 offen lds\n" \
"v_mfma_f32_16x16x32_bf16 v[192:195], a[32:35], a[16:19], 0\n" \
"ds_read_b64_tr_b16 a[116:117], v11 offset:17408\n" \
"v_mfma_f32_16x16x32_bf16 v[196:199], a[36:39], a[16:19], 0\n" \
"v_mfma_f32_16x16x32_bf16 v[200:203], a[32:35], a[20:23], 0\n" \
"s_mov_b32 s46, 256\n" \
"s_add_u32 m0, s50, 49152\n" \
"ds_read_b64_tr_b16 a[118:119], v12 offset:17408\n" \
"v_mfma_f32_16x16x32_bf16 v[204:207], a[36:39], a[20:23], 0\n" \
"buffer_load_dwordx4 v17, s[28:31], s46 offen lds\n" \
"v_mfma_f32_16x16x32_bf16 v[208:211], a[40:43], a[16:19], 0\n" \
"ds_read_b128 a[88:91], v8 offset:12288\n" \
"v_mfma_f32_16x16x32_bf16 v[212:215], a[44:47], a[16:19], 0\n" \
"v_mfma_f32_16x16x32_bf16 v[216:219], a[40:43], a[20:23], 0\n" \
"s_add_u32 m0, s50, 53248\n" \
"ds_read_b64_tr_b16 a[120:121], v9 offset:17920\n" \
"v_mfma_f32_16x16x32_bf16 v[220:223], a[44:47], a[20:23], 0\n" \
"buffer_load_dwordx4 v18, s[28:31], s46 offen lds\n" \
"v_mfma_f32_16x16x32_bf16 v[224:227], a[48:51], a[16:19], 0\n" \
"ds_read_b64_tr_b16 a[122:123], v10 offset:17920\n" \
"v_mfma_f32_16x16x32_bf16 v[228:231], a[52:55], a[16:19], 0\n" \
"v_mfma_f32_16x16x32_bf16 v[232:235], a[48:51], a[20:23], 0\n" \
"s_add_u32 m0, s50, 57344\n" \
"ds_read_b128 a[92:95], v8 offset:14336\n" \
"v_mfma_f32_16x16x32_bf16 v[236:239], a[52:55], a[20:23], 0\n" \
"buffer_load_dwordx4 v19, s[28:31], s46 offen lds\n" \
"v_mfma_f32_16x16x32_bf16 v[240:243], a[56:59], a[16:19], 0\n" \
"ds_read_b64_tr_b16 a[124:125], v11 offset:17920\n" \
"v_mfma_f32_16x16x32_bf16 v[244:247], a[60:63], a[16:19], 0\n" \
"v_mfma_f32_16x16x32_bf16 v[248:251], a[56:59], a[20:23], 0\n" \
"s_add_u32 m0, s50, 61440\n" \
"ds_read_b64_tr_b16 a[126:127], v12 offset:17920\n" \
"v_mfma_f32_16x16x32_bf16 v[252:255], a[60:63], a[20:23], 0\n" \
"buffer_load_dwordx4 v20, s[28:31], s46 offen lds\n" \
"v_mfma_f32_16x16x32_bf16 a[128:131], a[32:35], a[24:27], 0\n" \
"v_mfma_f32_16x16x32_bf16 a[132:135], a[36:39], a[24:27], 0\n" \
"v_mfma_f32_16x16x32_bf16 a[136:139], a[32:35], a[28:31], 0\n" \
"v_mfma_f32_16x16x32_bf16 a[140:143], a[36:39], a[28:31], 0\n" \
"v_mfma_f32_16x16x32_bf16 a[144:147], a[40:43], a[24:27], 0\n" \
"global_load_dwordx4 a[230:233], v27, s[70:71] offset:0\n" \
"v_mfma_f32_16x16x32_bf16 a[148:151], a[44:47], a[24:27], 0\n" \
"v_mfma_f32_16x16x32_bf16 a[152:155], a[40:43], a[28:31], 0\n" \
"v_mfma_f32_16x16x32_bf16 a[156:159], a[44:47], a[28:31], 0\n" \
"s_waitcnt vmcnt(13) lgkmcnt(0)\n" \
"v_mfma_f32_16x16x32_bf16 a[160:163], a[48:51], a[24:27], 0\n" \
"s_barrier\n" \
"v_mfma_f32_16x16x32_bf16 a[164:167], a[52:55], a[24:27], 0\n" \
"v_mfma_f32_16x16x32_bf16 a[168:171], a[48:51], a[28:31], 0\n" \
"v_mfma_f32_16x16x32_bf16 a[172:175], a[52:55], a[28:31], 0\n" \
"v_mfma_f32_16x16x32_bf16 a[176:179], a[56:59], a[24:27], 0\n" \
"v_mfma_f32_16x16x32_bf16 a[180:183], a[60:63], a[24:27], 0\n" \
"v_mfma_f32_16x16x32_bf16 a[184:187], a[56:59], a[28:31], 0\n" \
"v_mfma_f32_16x16x32_bf16 a[188:191], a[60:63], a[28:31], 0\n" \
"global_load_dwordx4 a[234:237], v27, s[70:71] offset:16\n" \
"v_mfma_f32_16x16x32_bf16 v[64:67], a[96:99], a[64:67], v[64:67]\n" \
"ds_read_b128 a[0:3], v5 offset:0\n" \
"v_mfma_f32_16x16x32_bf16 v[68:71], a[100:103], a[64:67], v[68:71]\n" \
"v_mfma_f32_16x16x32_bf16 v[72:75], a[96:99], a[68:71], v[72:75]\n" \
"s_mov_b32 s47, 1048576\n" \
"s_add_u32 m0, s22, 0\n" \
"ds_read_b64_tr_b16 a[32:33], v9 offset:32768\n" \
"v_mfma_f32_16x16x32_bf16 v[76:79], a[100:103], a[68:71], v[76:79]\n" \
"buffer_load_dwordx4 v21, s[32:35], s47 offen offset:0 lds\n" \
"v_mfma_f32_16x16x32_bf16 v[80:83], a[104:107], a[64:67], v[80:83]\n" \
"ds_read_b64_tr_b16 a[34:35], v10 offset:32768\n" \
"v_mfma_f32_16x16x32_bf16 v[84:87], a[108:111], a[64:67], v[84:87]\n" \
"v_mfma_f32_16x16x32_bf16 v[88:91], a[104:107], a[68:71], v[88:91]\n" \
"s_add_u32 m0, s22, 896\n" \
"ds_read_b128 a[4:7], v5 offset:2048\n" \
"v_mfma_f32_16x16x32_bf16 v[92:95], a[108:111], a[68:71], v[92:95]\n" \
"buffer_load_dwordx4 v21, s[32:35], s47 offen offset:128 lds\n" \
"v_mfma_f32_16x16x32_bf16 v[96:99], a[112:115], a[64:67], v[96:99]\n" \
"ds_read_b64_tr_b16 a[36:37], v11 offset:32768\n" \
"v_mfma_f32_16x16x32_bf16 v[100:103], a[116:119], a[64:67], v[100:103]\n" \
"v_mfma_f32_16x16x32_bf16 v[104:107], a[112:115], a[68:71], v[104:107]\n" \
"s_add_u32 m0, s22, 1792\n" \
"ds_read_b64_tr_b16 a[38:39], v12 offset:32768\n" \
"v_mfma_f32_16x16x32_bf16 v[108:111], a[116:119], a[68:71], v[108:111]\n" \
"buffer_load_dwordx4 v21, s[32:35], s47 offen offset:256 lds\n" \
"v_mfma_f32_16x16x32_bf16 v[112:115], a[120:123], a[64:67], v[112:115]\n" \
"ds_read_b128 a[8:11], v5 offset:4096\n" \
"v_mfma_f32_16x16x32_bf16 v[116:119], a[124:127], a[64:67], v[116:119]\n" \
"v_mfma_f32_16x16x32_bf16 v[120:123], a[120:123], a[68:71], v[120:123]\n" \
"s_add_u32 m0, s22, 2688\n" \
"ds_read_b64_tr_b16 a[40:41], v9 offset:33280\n" \
"v_mfma_f32_16x16x32_bf16 v[124:127], a[124:127], a[68:71], v[124:127]\n" \
"buffer_load_dwordx4 v21, s[32:35], s47 offen offset:384 lds\n" \
"v_mfma_f32_16x16x32_bf16 v[128:131], a[96:99], a[72:75], v[128:131]\n" \
"ds_read_b64_tr_b16 a[42:43], v10 offset:33280\n" \
"v_mfma_f32_16x16x32_bf16 v[132:135], a[100:103], a[72:75], v[132:135]\n" \
"v_mfma_f32_16x16x32_bf16 v[136:139], a[96:99], a[76:79], v[136:139]\n" \
"s_mov_b32 s47, 1310720\n" \
"s_add_u32 m0, s22, 16384\n" \
"ds_read_b128 a[12:15], v5 offset:6144\n" \
"v_mfma_f32_16x16x32_bf16 v[140:143], a[100:103], a[76:79], v[140:143]\n" \
"buffer_load_dwordx4 v21, s[32:35], s47 offen offset:0 lds\n" \
"v_mfma_f32_16x16x32_bf16 v[144:147], a[104:107], a[72:75], v[144:147]\n" \
"ds_read_b64_tr_b16 a[44:45], v11 offset:33280\n" \
"v_mfma_f32_16x16x32_bf16 v[148:151], a[108:111], a[72:75], v[148:151]\n" \
"v_mfma_f32_16x16x32_bf16 v[152:155], a[104:107], a[76:79], v[152:155]\n" \
"s_mov_b32 s46, 384\n" \
"s_add_u32 m0, s50, 65536\n" \
"ds_read_b64_tr_b16 a[46:47], v12 offset:33280\n" \
"v_mfma_f32_16x16x32_bf16 v[156:159], a[108:111], a[76:79], v[156:159]\n" \
"buffer_load_dwordx4 v13, s[28:31], s46 offen lds\n" \
"v_mfma_f32_16x16x32_bf16 v[160:163], a[112:115], a[72:75], v[160:163]\n" \
"ds_read_b128 a[16:19], v5 offset:8192\n" \
"v_mfma_f32_16x16x32_bf16 v[164:167], a[116:119], a[72:75], v[164:167]\n" \
"v_mfma_f32_16x16x32_bf16 v[168:171], a[112:115], a[76:79], v[168:171]\n" \
"s_add_u32 m0, s22, 17280\n" \
"ds_read_b64_tr_b16 a[48:49], v9 offset:33792\n" \
"v_mfma_f32_16x16x32_bf16 v[172:175], a[116:119], a[76:79], v[172:175]\n" \
"buffer_load_dwordx4 v21, s[32:35], s47 offen offset:128 lds\n" \
"v_mfma_f32_16x16x32_bf16 v[176:179], a[120:123], a[72:75], v[176:179]\n" \
"ds_read_b64_tr_b16 a[50:51], v10 offset:33792\n" \
"v_mfma_f32_16x16x32_bf16 v[180:183], a[124:127], a[72:75], v[180:183]\n" \
"v_mfma_f32_16x16x32_bf16 v[184:187], a[120:123], a[76:79], v[184:187]\n" \
"s_add_u32 m0, s50, 69632\n" \
"ds_read_b128 a[20:23], v5 offset:10240\n" \
"v_mfma_f32_16x16x32_bf16 v[188:191], a[124:127], a[76:79], v[188:191]\n" \
"buffer_load_dwordx4 v14, s[28:31], s46 offen lds\n" \
"v_mfma_f32_16x16x32_bf16 v[192:195], a[96:99], a[80:83], v[192:195]\n" \
"ds_read_b64_tr_b16 a[52:53], v11 offset:33792\n" \
"v_mfma_f32_16x16x32_bf16 v[196:199], a[100:103], a[80:83], v[196:199]\n" \
"v_mfma_f32_16x16x32_bf16 v[200:203], a[96:99], a[84:87], v[200:203]\n" \
"s_add_u32 m0, s22, 18176\n" \
"ds_read_b64_tr_b16 a[54:55], v12 offset:33792\n" \
"v_mfma_f32_16x16x32_bf16 v[204:207], a[100:103], a[84:87], v[204:207]\n" \
"buffer_load_dwordx4 v21, s[32:35], s47 offen offset:256 lds\n" \
"v_mfma_f32_16x16x32_bf16 v[208:211], a[104:107], a[80:83], v[208:211]\n" \
"ds_read_b128 a[24:27], v5 offset:12288\n" \
"v_mfma_f32_16x16x32_bf16 v[212:215], a[108:111], a[80:83], v[212:215]\n" \
"v_mfma_f32_16x16x32_bf16 v[216:219], a[104:107], a[84:87], v[216:219]\n" \
"s_add_u32 m0, s50, 73728\n" \
"ds_read_b64_tr_b16 a[56:57], v9 offset:34304\n" \
"v_mfma_f32_16x16x32_bf16 v[220:223], a[108:111], a[84:87], v[220:223]\n" \
"buffer_load_dwordx4 v15, s[28:31], s46 offen lds\n" \
"v_mfma_f32_16x16x32_bf16 v[224:227], a[112:115], a[80:83], v[224:227]\n" \
"ds_read_b64_tr_b16 a[58:59], v10 offset:34304\n" \
"v_mfma_f32_16x16x32_bf16 v[228:231], a[116:119], a[80:83], v[228:231]\n" \
"v_mfma_f32_16x16x32_bf16 v[232:235], a[112:115], a[84:87], v[232:235]\n" \
"s_add_u32 m0, s22, 19072\n" \
"ds_read_b128 a[28:31], v5 offset:14336\n" \
"v_mfma_f32_16x16x32_bf16 v[236:239], a[116:119], a[84:87], v[236:239]\n" \
"buffer_load_dwordx4 v21, s[32:35], s47 offen offset:384 lds\n" \
"v_mfma_f32_16x16x32_bf16 v[240:243], a[120:123], a[80:83], v[240:243]\n" \
"ds_read_b64_tr_b16 a[60:61], v11 offset:34304\n" \
"v_mfma_f32_16x16x32_bf16 v[244:247], a[124:127], a[80:83], v[244:247]\n" \
"v_mfma_f32_16x16x32_bf16 v[248:251], a[120:123], a[84:87], v[248:251]\n" \
"s_add_u32 m0, s50, 77824\n" \
"ds_read_b64_tr_b16 a[62:63], v12 offset:34304\n" \
"v_mfma_f32_16x16x32_bf16 v[252:255], a[124:127], a[84:87], v[252:255]\n" \
"buffer_load_dwordx4 v16, s[28:31], s46 offen lds\n" \
"v_mfma_f32_16x16x32_bf16 a[128:131], a[96:99], a[88:91], a[128:131]\n" \
"v_mfma_f32_16x16x32_bf16 a[132:135], a[100:103], a[88:91], a[132:135]\n" \
"v_mfma_f32_16x16x32_bf16 a[136:139], a[96:99], a[92:95], a[136:139]\n" \
"s_add_u32 s84, s84, 1048576\ns_addc_u32 s85, s85, 0\n" \
"v_mfma_f32_16x16x32_bf16 a[140:143], a[100:103], a[92:95], a[140:143]\n" \
"buffer_load_dwordx2 a[192:193], v54, s[40:43], 0 offen offset:0 nt\n" \
"v_mfma_f32_16x16x32_bf16 a[144:147], a[104:107], a[88:91], a[144:147]\n" \
"v_mfma_f32_16x16x32_bf16 a[148:151], a[108:111], a[88:91], a[148:151]\n" \
"v_mfma_f32_16x16x32_bf16 a[152:155], a[104:107], a[92:95], a[152:155]\n" \
"v_mfma_f32_16x16x32_bf16 a[156:159], a[108:111], a[92:95], a[156:159]\n" \
"s_waitcnt vmcnt(23) lgkmcnt(0)\n" \
"v_mfma_f32_16x16x32_bf16 a[160:163], a[112:115], a[88:91], a[160:163]\n" \
"s_barrier\n" \
"v_mfma_f32_16x16x32_bf16 a[164:167], a[116:119], a[88:91], a[164:167]\n" \
"buffer_load_dwordx2 a[194:195], v54, s[40:43], 0 offen offset:2048 nt\n" \
"v_mfma_f32_16x16x32_bf16 a[168:171], a[112:115], a[92:95], a[168:171]\n" \
"v_mfma_f32_16x16x32_bf16 a[172:175], a[116:119], a[92:95], a[172:175]\n" \
"v_mfma_f32_16x16x32_bf16 a[176:179], a[120:123], a[88:91], a[176:179]\n" \
"v_mfma_f32_16x16x32_bf16 a[180:183], a[124:127], a[88:91], a[180:183]\n" \
"s_mov_b64 exec, 0xffff\nglobal_load_dword v22, v1, s[84:85]\ns_mov_b64 exec, -1\n" \
"v_mfma_f32_16x16x32_bf16 a[184:187], a[120:123], a[92:95], a[184:187]\n" \
"v_mfma_f32_16x16x32_bf16 a[188:191], a[124:127], a[92:95], a[188:191]\n" \
"v_mfma_f32_16x16x32_bf16 v[64:67], a[32:35], a[0:3], v[64:67]\n" \
"ds_read_b128 a[64:67], v6 offset:0\n" \
"v_mfma_f32_16x16x32_bf16 v[68:71], a[36:39], a[0:3], v[68:71]\n" \
"v_mfma_f32_16x16x32_bf16 v[72:75], a[32:35], a[4:7], v[72:75]\n" \
"s_mov_b32 s47, 1572864\n" \
"s_add_u32 m0, s22, 32768\n" \
"ds_read_b64_tr_b16 a[96:97], v9 offset:49152\n" \
"v_mfma_f32_16x16x32_bf16 v[76:79], a[36:39], a[4:7], v[76:79]\n" \
"buffer_load_dwordx4 v21, s[32:35], s47 offen offset:0 lds\n" \
"v_mfma_f32_16x16x32_bf16 v[80:83], a[40:43], a[0:3], v[80:83]\n" \
"ds_read_b64_tr_b16 a[98:99], v10 offset:49152\n" \
"v_mfma_f32_16x16x32_bf16 v[84:87], a[44:47], a[0:3], v[84:87]\n" \
"v_mfma_f32_16x16x32_bf16 v[88:91], a[40:43], a[4:7], v[88:91]\n" \
"ds_read_b128 a[68:71], v6 offset:2048\n" \
"v_mfma_f32_16x16x32_bf16 v[92:95], a[44:47], a[4:7], v[92:95]\n" \
"buffer_load_dwordx2 a[196:197], v55, s[40:43], 0 offen offset:0 nt\n" \
"v_mfma_f32_16x16x32_bf16 v[96:99], a[48:51], a[0:3], v[96:99]\n" \
"s_mov_b32 s52, 0\n" \
"ds_read_b64_tr_b16 a[100:101], v11 offset:49152\n" \
"v_mfma_f32_16x16x32_bf16 v[100:103], a[52:55], a[0:3], v[100:103]\n" \
"v_mfma_f32_16x16x32_bf16 v[104:107], a[48:51], a[4:7], v[104:107]\n" \
"s_mov_b32 s46, 384\n" \
"s_add_u32 m0, s50, 81920\n" \
"ds_read_b64_tr_b16 a[102:103], v12 offset:49152\n" \
"v_mfma_f32_16x16x32_bf16 v[108:111], a[52:55], a[4:7], v[108:111]\n" \
"buffer_load_dwordx4 v17, s[28:31], s46 offen lds\n" \
"v_mfma_f32_16x16x32_bf16 v[112:115], a[56:59], a[0:3], v[112:115]\n" \
"ds_read_b128 a[72:75], v6 offset:4096\n" \
"v_mfma_f32_16x16x32_bf16 v[116:119], a[60:63], a[0:3], v[116:119]\n" \
"v_mfma_f32_16x16x32_bf16 v[120:123], a[56:59], a[4:7], v[120:123]\n" \
"ds_read_b64_tr_b16 a[104:105], v9 offset:49664\n" \
"v_mfma_f32_16x16x32_bf16 v[124:127], a[60:63], a[4:7], v[124:127]\n" \
"buffer_load_dwordx2 a[198:199], v59, s[40:43], s52 offen offset:0 nt\n" \
"v_mfma_f32_16x16x32_bf16 v[128:131], a[32:35], a[8:11], v[128:131]\n" \
"ds_read_b64_tr_b16 a[106:107], v10 offset:49664\n" \
"v_mfma_f32_16x16x32_bf16 v[132:135], a[36:39], a[8:11], v[132:135]\n" \
"v_mfma_f32_16x16x32_bf16 v[136:139], a[32:35], a[12:15], v[136:139]\n" \
"s_add_u32 m0, s22, 33664\n" \
"ds_read_b128 a[76:79], v6 offset:6144\n" \
"v_mfma_f32_16x16x32_bf16 v[140:143], a[36:39], a[12:15], v[140:143]\n" \
"buffer_load_dwordx4 v21, s[32:35], s47 offen offset:128 lds\n" \
"v_mfma_f32_16x16x32_bf16 v[144:147], a[40:43], a[8:11], v[144:147]\n" \
"ds_read_b64_tr_b16 a[108:109], v11 offset:49664\n" \
"v_mfma_f32_16x16x32_bf16 v[148:151], a[44:47], a[8:11], v[148:151]\n" \
"v_mfma_f32_16x16x32_bf16 v[152:155], a[40:43], a[12:15], v[152:155]\n" \
"ds_read_b64_tr_b16 a[110:111], v12 offset:49664\n" \
"v_mfma_f32_16x16x32_bf16 v[156:159], a[44:47], a[12:15], v[156:159]\n" \
"buffer_load_dwordx2 a[214:215], v59, s[80:83], s52 offen offset:0 nt\n" \
"v_mfma_f32_16x16x32_bf16 v[160:163], a[48:51], a[8:11], v[160:163]\n" \
"ds_read_b128 a[80:83], v6 offset:8192\n" \
"v_mfma_f32_16x16x32_bf16 v[164:167], a[52:55], a[8:11], v[164:167]\n" \
"v_mfma_f32_16x16x32_bf16 v[168:171], a[48:51], a[12:15], v[168:171]\n" \
"s_add_u32 m0, s50, 86016\n" \
"ds_read_b64_tr_b16 a[112:113], v9 offset:50176\n" \
"v_mfma_f32_16x16x32_bf16 v[172:175], a[52:55], a[12:15], v[172:175]\n" \
"buffer_load_dwordx4 v18, s[28:31], s46 offen lds\n" \
"v_mfma_f32_16x16x32_bf16 v[176:179], a[56:59], a[8:11], v[176:179]\n" \
"ds_read_b64_tr_b16 a[114:115], v10 offset:50176\n" \
"v_mfma_f32_16x16x32_bf16 v[180:183], a[60:63], a[8:11], v[180:183]\n" \
"v_mfma_f32_16x16x32_bf16 v[184:187], a[56:59], a[12:15], v[184:187]\n" \
"ds_read_b128 a[84:87], v6 offset:10240\n" \
"v_mfma_f32_16x16x32_bf16 v[188:191], a[60:63], a[12:15], v[188:191]\n" \
"buffer_load_dwordx2 a[200:201], v59, s[40:43], s52 offen offset:2048 nt\n" \
"v_mfma_f32_16x16x32_bf16 v[192:195], a[32:35], a[16:19], v[192:195]\n" \
"ds_read_b64_tr_b16 a[116:117], v11 offset:50176\n" \
"v_mfma_f32_16x16x32_bf16 v[196:199], a[36:39], a[16:19], v[196:199]\n" \
"v_mfma_f32_16x16x32_bf16 v[200:203], a[32:35], a[20:23], v[200:203]\n" \
"s_add_u32 m0, s22, 34560\n" \
"ds_read_b64_tr_b16 a[118:119], v12 offset:50176\n" \
"v_mfma_f32_16x16x32_bf16 v[204:207], a[36:39], a[20:23], v[204:207]\n" \
"buffer_load_dwordx4 v21, s[32:35], s47 offen offset:256 lds\n" \
"v_mfma_f32_16x16x32_bf16 v[208:211], a[40:43], a[16:19], v[208:211]\n" \
"ds_read_b128 a[88:91], v6 offset:12288\n" \
"v_mfma_f32_16x16x32_bf16 v[212:215], a[44:47], a[16:19], v[212:215]\n" \
"v_mfma_f32_16x16x32_bf16 v[216:219], a[40:43], a[20:23], v[216:219]\n" \
"ds_read_b64_tr_b16 a[120:121], v9 offset:50688\n" \
"v_mfma_f32_16x16x32_bf16 v[220:223], a[44:47], a[20:23], v[220:223]\n" \
"buffer_load_dwordx2 a[216:217], v59, s[80:83], s52 offen offset:2048 nt\n" \
"v_mfma_f32_16x16x32_bf16 v[224:227], a[48:51], a[16:19], v[224:227]\n" \
"s_mov_b32 s52, 4096\n" \
"ds_read_b64_tr_b16 a[122:123], v10 offset:50688\n" \
"v_mfma_f32_16x16x32_bf16 v[228:231], a[52:55], a[16:19], v[228:231]\n" \
"v_mfma_f32_16x16x32_bf16 v[232:235], a[48:51], a[20:23], v[232:235]\n" \
"s_add_u32 m0, s50, 90112\n" \
"ds_read_b128 a[92:95], v6 offset:14336\n" \
"v_mfma_f32_16x16x32_bf16 v[236:239], a[52:55], a[20:23], v[236:239]\n" \
"buffer_load_dwordx4 v19, s[28:31], s46 offen lds\n" \
"v_mfma_f32_16x16x32_bf16 v[240:243], a[56:59], a[16:19], v[240:243]\n" \
"ds_read_b64_tr_b16 a[124:125], v11 offset:50688\n" \
"v_mfma_f32_16x16x32_bf16 v[244:247], a[60:63], a[16:19], v[244:247]\n" \
"v_mfma_f32_16x16x32_bf16 v[248:251], a[56:59], a[20:23], v[248:251]\n" \
"ds_read_b64_tr_b16 a[126:127], v12 offset:50688\n" \
"v_mfma_f32_16x16x32_bf16 v[252:255], a[60:63], a[20:23], v[252:255]\n" \
"buffer_load_dwordx2 a[202:203], v59, s[40:43], s52 offen offset:0 nt\n" \
"v_mfma_f32_16x16x32_bf16 a[128:131], a[32:35], a[24:27], a[128:131]\n" \
"v_mfma_f32_16x16x32_bf16 a[132:135], a[36:39], a[24:27], a[132:135]\n" \
"v_mfma_f32_16x16x32_bf16 a[136:139], a[32:35], a[28:31], a[136:139]\n" \
"s_add_u32 m0, s22, 35456\n" \
"v_mfma_f32_16x16x32_bf16 a[140:143], a[36:39], a[28:31], a[140:143]\n" \
"buffer_load_dwordx4 v21, s[32:35], s47 offen offset:384 lds\n" \
"v_mfma_f32_16x16x32_bf16 a[144:147], a[40:43], a[24:27], a[144:147]\n" \
"v_mfma_f32_16x16x32_bf16 a[148:151], a[44:47], a[24:27], a[148:151]\n" \
"v_mfma_f32_16x16x32_bf16 a[152:155], a[40:43], a[28:31], a[152:155]\n" \
"v_mfma_f32_16x16x32_bf16 a[156:159], a[44:47], a[28:31], a[156:159]\n" \
"s_waitcnt vmcnt(24) lgkmcnt(0)\n" \
"v_mfma_f32_16x16x32_bf16 a[160:163], a[48:51], a[24:27], a[160:163]\n" \
"s_barrier\n" \
"v_mfma_f32_16x16x32_bf16 a[164:167], a[52:55], a[24:27], a[164:167]\n" \
"v_mfma_f32_16x16x32_bf16 a[168:171], a[48:51], a[28:31], a[168:171]\n" \
"s_add_u32 m0, s50, 94208\n" \
"v_mfma_f32_16x16x32_bf16 a[172:175], a[52:55], a[28:31], a[172:175]\n" \
"buffer_load_dwordx4 v20, s[28:31], s46 offen lds\n" \
"v_mfma_f32_16x16x32_bf16 a[176:179], a[56:59], a[24:27], a[176:179]\n" \
"v_mfma_f32_16x16x32_bf16 a[180:183], a[60:63], a[24:27], a[180:183]\n" \
"v_mfma_f32_16x16x32_bf16 a[184:187], a[56:59], a[28:31], a[184:187]\n" \
"v_mfma_f32_16x16x32_bf16 a[188:191], a[60:63], a[28:31], a[188:191]\n" \
"buffer_load_dwordx2 a[218:219], v59, s[80:83], s52 offen offset:0 nt\n" \
"v_mfma_f32_16x16x32_bf16 v[64:67], a[96:99], a[64:67], v[64:67]\n" \
"ds_read_b128 a[0:3], v5 offset:32768\n" \
"v_mfma_f32_16x16x32_bf16 v[68:71], a[100:103], a[64:67], v[68:71]\n" \
"v_mfma_f32_16x16x32_bf16 v[72:75], a[96:99], a[68:71], v[72:75]\n" \
"s_mov_b32 s47, 1835008\n" \
"s_add_u32 m0, s22, 49152\n" \
"ds_read_b64_tr_b16 a[32:33], v9 offset:0\n" \
"v_mfma_f32_16x16x32_bf16 v[76:79], a[100:103], a[68:71], v[76:79]\n" \
"buffer_load_dwordx4 v21, s[32:35], s47 offen offset:0 lds\n" \
"v_mfma_f32_16x16x32_bf16 v[80:83], a[104:107], a[64:67], v[80:83]\n" \
"ds_read_b64_tr_b16 a[34:35], v10 offset:0\n" \
"v_mfma_f32_16x16x32_bf16 v[84:87], a[108:111], a[64:67], v[84:87]\n" \
"v_mfma_f32_16x16x32_bf16 v[88:91], a[104:107], a[68:71], v[88:91]\n" \
"ds_read_b128 a[4:7], v5 offset:34816\n" \
"v_mfma_f32_16x16x32_bf16 v[92:95], a[108:111], a[68:71], v[92:95]\n" \
"buffer_load_dwordx2 a[204:205], v59, s[40:43], s52 offen offset:2048 nt\n" \
"v_mfma_f32_16x16x32_bf16 v[96:99], a[112:115], a[64:67], v[96:99]\n" \
"ds_read_b64_tr_b16 a[36:37], v11 offset:0\n" \
"v_mfma_f32_16x16x32_bf16 v[100:103], a[116:119], a[64:67], v[100:103]\n" \
"v_mfma_f32_16x16x32_bf16 v[104:107], a[112:115], a[68:71], v[104:107]\n" \
"s_mov_b32 s46, 512\n" \
"s_add_u32 m0, s50, 0\n" \
"ds_read_b64_tr_b16 a[38:39], v12 offset:0\n" \
"v_mfma_f32_16x16x32_bf16 v[108:111], a[116:119], a[68:71], v[108:111]\n" \
"buffer_load_dwordx4 v13, s[28:31], s46 offen lds\n" \
"v_mfma_f32_16x16x32_bf16 v[112:115], a[120:123], a[64:67], v[112:115]\n" \
"ds_read_b128 a[8:11], v5 offset:36864\n" \
"v_mfma_f32_16x16x32_bf16 v[116:119], a[124:127], a[64:67], v[116:119]\n" \
"v_mfma_f32_16x16x32_bf16 v[120:123], a[120:123], a[68:71], v[120:123]\n" \
"ds_read_b64_tr_b16 a[40:41], v9 offset:512\n" \
"v_mfma_f32_16x16x32_bf16 v[124:127], a[124:127], a[68:71], v[124:127]\n" \
"buffer_load_dwordx2 a[220:221], v59, s[80:83], s52 offen offset:2048 nt\n" \
"v_mfma_f32_16x16x32_bf16 v[128:131], a[96:99], a[72:75], v[128:131]\n" \
"s_mov_b32 s52, 8192\n" \
"ds_read_b64_tr_b16 a[42:43], v10 offset:512\n" \
"v_mfma_f32_16x16x32_bf16 v[132:135], a[100:103], a[72:75], v[132:135]\n" \
"v_mfma_f32_16x16x32_bf16 v[136:139], a[96:99], a[76:79], v[136:139]\n" \
"s_add_u32 m0, s22, 50048\n" \
"ds_read_b128 a[12:15], v5 offset:38912\n" \
"v_mfma_f32_16x16x32_bf16 v[140:143], a[100:103], a[76:79], v[140:143]\n" \
"buffer_load_dwordx4 v21, s[32:35], s47 offen offset:128 lds\n" \
"v_mfma_f32_16x16x32_bf16 v[144:147], a[104:107], a[72:75], v[144:147]\n" \
"ds_read_b64_tr_b16 a[44:45], v11 offset:512\n" \
"v_mfma_f32_16x16x32_bf16 v[148:151], a[108:111], a[72:75], v[148:151]\n" \
"v_mfma_f32_16x16x32_bf16 v[152:155], a[104:107], a[76:79], v[152:155]\n" \
"ds_read_b64_tr_b16 a[46:47], v12 offset:512\n" \
"v_mfma_f32_16x16x32_bf16 v[156:159], a[108:111], a[76:79], v[156:159]\n" \
"buffer_load_dwordx2 a[206:207], v59, s[40:43], s52 offen offset:0 nt\n" \
"v_mfma_f32_16x16x32_bf16 v[160:163], a[112:115], a[72:75], v[160:163]\n" \
"ds_read_b128 a[16:19], v5 offset:40960\n" \
"v_mfma_f32_16x16x32_bf16 v[164:167], a[116:119], a[72:75], v[164:167]\n" \
"v_mfma_f32_16x16x32_bf16 v[168:171], a[112:115], a[76:79], v[168:171]\n" \
"s_add_u32 m0, s50, 4096\n" \
"ds_read_b64_tr_b16 a[48:49], v9 offset:1024\n" \
"v_mfma_f32_16x16x32_bf16 v[172:175], a[116:119], a[76:79], v[172:175]\n" \
"buffer_load_dwordx4 v14, s[28:31], s46 offen lds\n" \
"v_mfma_f32_16x16x32_bf16 v[176:179], a[120:123], a[72:75], v[176:179]\n" \
"ds_read_b64_tr_b16 a[50:51], v10 offset:1024\n" \
"v_mfma_f32_16x16x32_bf16 v[180:183], a[124:127], a[72:75], v[180:183]\n" \
"v_mfma_f32_16x16x32_bf16 v[184:187], a[120:123], a[76:79], v[184:187]\n" \
"ds_read_b128 a[20:23], v5 offset:43008\n" \
"v_mfma_f32_16x16x32_bf16 v[188:191], a[124:127], a[76:79], v[188:191]\n" \
"buffer_load_dwordx2 a[222:223], v59, s[80:83], s52 offen offset:0 nt\n" \
"v_mfma_f32_16x16x32_bf16 v[192:195], a[96:99], a[80:83], v[192:195]\n" \
"ds_read_b64_tr_b16 a[52:53], v11 offset:1024\n" \
"v_mfma_f32_16x16x32_bf16 v[196:199], a[100:103], a[80:83], v[196:199]\n" \
"v_mfma_f32_16x16x32_bf16 v[200:203], a[96:99], a[84:87], v[200:203]\n" \
"s_add_u32 m0, s22, 50944\n" \
"ds_read_b64_tr_b16 a[54:55], v12 offset:1024\n" \
"v_mfma_f32_16x16x32_bf16 v[204:207], a[100:103], a[84:87], v[204:207]\n" \
"buffer_load_dwordx4 v21, s[32:35], s47 offen offset:256 lds\n" \
"v_mfma_f32_16x16x32_bf16 v[208:211], a[104:107], a[80:83], v[208:211]\n" \
"ds_read_b128 a[24:27], v5 offset:45056\n" \
"v_mfma_f32_16x16x32_bf16 v[212:215], a[108:111], a[80:83], v[212:215]\n" \
"v_mfma_f32_16x16x32_bf16 v[216:219], a[104:107], a[84:87], v[216:219]\n" \
"ds_read_b64_tr_b16 a[56:57], v9 offset:1536\n" \
"v_mfma_f32_16x16x32_bf16 v[220:223], a[108:111], a[84:87], v[220:223]\n" \
"buffer_load_dwordx2 a[208:209], v59, s[40:43], s52 offen offset:2048 nt\n" \
"v_mfma_f32_16x16x32_bf16 v[224:227], a[112:115], a[80:83], v[224:227]\n" \
"ds_read_b64_tr_b16 a[58:59], v10 offset:1536\n" \
"v_mfma_f32_16x16x32_bf16 v[228:231], a[116:119], a[80:83], v[228:231]\n" \
"v_mfma_f32_16x16x32_bf16 v[232:235], a[112:115], a[84:87], v[232:235]\n" \
"s_add_u32 m0, s50, 8192\n" \
"ds_read_b128 a[28:31], v5 offset:47104\n" \
"v_mfma_f32_16x16x32_bf16 v[236:239], a[116:119], a[84:87], v[236:239]\n" \
"buffer_load_dwordx4 v15, s[28:31], s46 offen lds\n" \
"v_mfma_f32_16x16x32_bf16 v[240:243], a[120:123], a[80:83], v[240:243]\n" \
"ds_read_b64_tr_b16 a[60:61], v11 offset:1536\n" \
"v_mfma_f32_16x16x32_bf16 v[244:247], a[124:127], a[80:83], v[244:247]\n" \
"v_mfma_f32_16x16x32_bf16 v[248:251], a[120:123], a[84:87], v[248:251]\n" \
"ds_read_b64_tr_b16 a[62:63], v12 offset:1536\n" \
"v_mfma_f32_16x16x32_bf16 v[252:255], a[124:127], a[84:87], v[252:255]\n" \
"buffer_load_dwordx2 a[224:225], v59, s[80:83], s52 offen offset:2048 nt\n" \
"v_mfma_f32_16x16x32_bf16 a[128:131], a[96:99], a[88:91], a[128:131]\n" \
"s_mov_b32 s52, 12288\n" \
"v_mfma_f32_16x16x32_bf16 a[132:135], a[100:103], a[88:91], a[132:135]\n" \
"v_mfma_f32_16x16x32_bf16 a[136:139], a[96:99], a[92:95], a[136:139]\n" \
"s_add_u32 m0, s22, 51840\n" \
"v_mfma_f32_16x16x32_bf16 a[140:143], a[100:103], a[92:95], a[140:143]\n" \
"buffer_load_dwordx4 v21, s[32:35], s47 offen offset:384 lds\n" \
"v_mfma_f32_16x16x32_bf16 a[144:147], a[104:107], a[88:91], a[144:147]\n" \
"v_mfma_f32_16x16x32_bf16 a[148:151], a[108:111], a[88:91], a[148:151]\n" \
"v_mfma_f32_16x16x32_bf16 a[152:155], a[104:107], a[92:95], a[152:155]\n" \
"v_mfma_f32_16x16x32_bf16 a[156:159], a[108:111], a[92:95], a[156:159]\n" \
"s_waitcnt vmcnt(32) lgkmcnt(0)\n" \
"v_mfma_f32_16x16x32_bf16 a[160:163], a[112:115], a[88:91], a[160:163]\n" \
"s_barrier\n" \
"v_mfma_f32_16x16x32_bf16 a[164:167], a[116:119], a[88:91], a[164:167]\n" \
"v_mfma_f32_16x16x32_bf16 a[168:171], a[112:115], a[92:95], a[168:171]\n" \
"s_add_u32 m0, s50, 12288\n" \
"v_mfma_f32_16x16x32_bf16 a[172:175], a[116:119], a[92:95], a[172:175]\n" \
"buffer_load_dwordx4 v16, s[28:31], s46 offen lds\n" \
"v_mfma_f32_16x16x32_bf16 a[176:179], a[120:123], a[88:91], a[176:179]\n" \
"v_mfma_f32_16x16x32_bf16 a[180:183], a[124:127], a[88:91], a[180:183]\n" \
"v_mfma_f32_16x16x32_bf16 a[184:187], a[120:123], a[92:95], a[184:187]\n" \
"v_mfma_f32_16x16x32_bf16 a[188:191], a[124:127], a[92:95], a[188:191]\n" \
"buffer_load_dwordx2 a[210:211], v59, s[40:43], s52 offen offset:0 nt\n" \
"v_mfma_f32_16x16x32_bf16 v[64:67], a[32:35], a[0:3], v[64:67]\n" \
"ds_read_b128 a[64:67], v6 offset:32768\n" \
"v_mfma_f32_16x16x32_bf16 v[68:71], a[36:39], a[0:3], v[68:71]\n" \
"s_waitcnt vmcnt(11)\n" \
"v_accvgpr_read_b32 v43, a230\n" \
"v_lshlrev_b32 v42, 16, v43\n" \
"v_mfma_f32_16x16x32_bf16 v[72:75], a[32:35], a[4:7], v[72:75]\n" \
"s_mov_b32 s47, 2097152\n" \
"s_add_u32 m0, s22, 0\n" \
"ds_read_b64_tr_b16 a[96:97], v9 offset:16384\n" \
"v_mfma_f32_16x16x32_bf16 v[76:79], a[36:39], a[4:7], v[76:79]\n" \
"buffer_load_dwordx4 v21, s[32:35], s47 offen offset:0 lds\n" \
"v_mfma_f32_16x16x32_bf16 v[80:83], a[40:43], a[0:3], v[80:83]\n" \
"ds_read_b64_tr_b16 a[98:99], v10 offset:16384\n" \
"v_and_b32 v43, 0xffff0000, v43\n" \
"v_mfma_f32_16x16x32_bf16 v[84:87], a[44:47], a[0:3], v[84:87]\n" \
"v_accvgpr_read_b32 v45, a231\n" \
"v_lshlrev_b32 v44, 16, v45\n" \
"v_mfma_f32_16x16x32_bf16 v[88:91], a[40:43], a[4:7], v[88:91]\n" \
"ds_read_b128 a[68:71], v6 offset:34816\n" \
"v_and_b32 v45, 0xffff0000, v45\n" \
"v_mfma_f32_16x16x32_bf16 v[92:95], a[44:47], a[4:7], v[92:95]\n" \
"buffer_load_dwordx2 a[226:227], v59, s[80:83], s52 offen offset:0 nt\n" \
"v_mfma_f32_16x16x32_bf16 v[96:99], a[48:51], a[0:3], v[96:99]\n" \
"ds_read_b64_tr_b16 a[100:101], v11 offset:16384\n" \
"v_accvgpr_read_b32 v47, a232\n" \
"v_mfma_f32_16x16x32_bf16 v[100:103], a[52:55], a[0:3], v[100:103]\n" \
"v_lshlrev_b32 v46, 16, v47\n" \
"v_and_b32 v47, 0xffff0000, v47\n" \
"v_mfma_f32_16x16x32_bf16 v[104:107], a[48:51], a[4:7], v[104:107]\n" \
"s_mov_b32 s46, 512\n" \
"s_add_u32 m0, s50, 16384\n" \
"ds_read_b64_tr_b16 a[102:103], v12 offset:16384\n" \
"v_mfma_f32_16x16x32_bf16 v[108:111], a[52:55], a[4:7], v[108:111]\n" \
"buffer_load_dwordx4 v17, s[28:31], s46 offen lds\n" \
"v_mfma_f32_16x16x32_bf16 v[112:115], a[56:59], a[0:3], v[112:115]\n" \
"ds_read_b128 a[72:75], v6 offset:36864\n" \
"v_accvgpr_read_b32 v49, a233\n" \
"v_mfma_f32_16x16x32_bf16 v[116:119], a[60:63], a[0:3], v[116:119]\n" \
"v_lshlrev_b32 v48, 16, v49\n" \
"v_and_b32 v49, 0xffff0000, v49\n" \
"v_mfma_f32_16x16x32_bf16 v[120:123], a[56:59], a[4:7], v[120:123]\n" \
"LH0_%=:\ns_cbranch_execz LH8_%=\n" \
"ds_read_b64_tr_b16 a[104:105], v9 offset:16896\n" \
"v_accvgpr_read_b32 v36, a192\n" \
"v_mfma_f32_16x16x32_bf16 v[124:127], a[60:63], a[4:7], v[124:127]\n" \
"buffer_load_dwordx2 a[212:213], v59, s[40:43], s52 offen offset:2048 nt\n" \
"v_mfma_f32_16x16x32_bf16 v[128:131], a[32:35], a[8:11], v[128:131]\n" \
"ds_read_b64_tr_b16 a[106:107], v10 offset:16896\n" \
"v_lshlrev_b32 v30, 16, v36\n" \
"v_mfma_f32_16x16x32_bf16 v[132:135], a[36:39], a[8:11], v[132:135]\n" \
"v_and_b32 v36, 0xffff0000, v36\n" \
"v_accvgpr_read_b32 v37, a194\n" \
"v_mfma_f32_16x16x32_bf16 v[136:139], a[32:35], a[12:15], v[136:139]\n" \
"s_add_u32 m0, s22, 896\n" \
"ds_read_b128 a[76:79], v6 offset:38912\n" \
"v_lshlrev_b32 v31, 16, v37\n" \
"v_mfma_f32_16x16x32_bf16 v[140:143], a[36:39], a[12:15], v[140:143]\n" \
"buffer_load_dwordx4 v21, s[32:35], s47 offen offset:128 lds\n" \
"v_mfma_f32_16x16x32_bf16 v[144:147], a[40:43], a[8:11], v[144:147]\n" \
"ds_read_b64_tr_b16 a[108:109], v11 offset:16896\n" \
"v_and_b32 v37, 0xffff0000, v37\n" \
"v_mfma_f32_16x16x32_bf16 v[148:151], a[44:47], a[8:11], v[148:151]\n" \
"v_accvgpr_read_b32 v38, a196\n" \
"v_lshlrev_b32 v32, 16, v38\n" \
"v_mfma_f32_16x16x32_bf16 v[152:155], a[40:43], a[12:15], v[152:155]\n" \
"ds_read_b64_tr_b16 a[110:111], v12 offset:16896\n" \
"v_and_b32 v38, 0xffff0000, v38\n" \
"v_mfma_f32_16x16x32_bf16 v[156:159], a[44:47], a[12:15], v[156:159]\n" \
"buffer_load_dwordx2 a[228:229], v59, s[80:83], s52 offen offset:2048 nt\n" \
"v_mfma_f32_16x16x32_bf16 v[160:163], a[48:51], a[8:11], v[160:163]\n" \
"ds_read_b128 a[80:83], v6 offset:40960\n" \
"v_accvgpr_read_b32 v39, a198\n" \
"v_mfma_f32_16x16x32_bf16 v[164:167], a[52:55], a[8:11], v[164:167]\n" \
"v_lshlrev_b32 v33, 16, v39\n" \
"v_and_b32 v39, 0xffff0000, v39\n" \
"v_mfma_f32_16x16x32_bf16 v[168:171], a[48:51], a[12:15], v[168:171]\n" \
"s_add_u32 m0, s50, 20480\n" \
"ds_read_b64_tr_b16 a[112:113], v9 offset:17408\n" \
"v_mul_f32 v25, v30, v45\n" \
"v_mfma_f32_16x16x32_bf16 v[172:175], a[52:55], a[12:15], v[172:175]\n" \
"buffer_load_dwordx4 v18, s[28:31], s46 offen lds\n" \
"v_mfma_f32_16x16x32_bf16 v[176:179], a[56:59], a[8:11], v[176:179]\n" \
"ds_read_b64_tr_b16 a[114:115], v10 offset:17408\n" \
"v_mul_f32 v26, v36, v49\n" \
"v_mfma_f32_16x16x32_bf16 v[180:183], a[60:63], a[8:11], v[180:183]\n" \
"v_fmac_f32 v25, v31, v44\n" \
"v_fmac_f32 v26, v37, v48\n" \
"v_mfma_f32_16x16x32_bf16 v[184:187], a[56:59], a[12:15], v[184:187]\n" \
"ds_read_b128 a[84:87], v6 offset:43008\n" \
"v_fmac_f32 v25, v32, v43\n" \
"v_mfma_f32_16x16x32_bf16 v[188:191], a[60:63], a[12:15], v[188:191]\n" \
"v_fmac_f32 v26, v38, v47\n" \
"v_fmac_f32 v25, v33, v42\n" \
"v_mfma_f32_16x16x32_bf16 v[192:195], a[32:35], a[16:19], v[192:195]\n" \
"ds_read_b64_tr_b16 a[116:117], v11 offset:17408\n" \
"v_fmac_f32 v26, v39, v46\n" \
"v_mfma_f32_16x16x32_bf16 v[196:199], a[36:39], a[16:19], v[196:199]\n" \
"v_accvgpr_read_b32 v28, a214\n" \
"v_lshlrev_b32 v27, 16, v28\n" \
"v_mfma_f32_16x16x32_bf16 v[200:203], a[32:35], a[20:23], v[200:203]\n" \
"s_add_u32 m0, s22, 1792\n" \
"ds_read_b64_tr_b16 a[118:119], v12 offset:17408\n" \
"v_and_b32 v28, 0xffff0000, v28\n" \
"v_mfma_f32_16x16x32_bf16 v[204:207], a[36:39], a[20:23], v[204:207]\n" \
"buffer_load_dwordx4 v21, s[32:35], s47 offen offset:256 lds\n" \
"v_mfma_f32_16x16x32_bf16 v[208:211], a[40:43], a[16:19], v[208:211]\n" \
"ds_read_b128 a[88:91], v6 offset:45056\n" \
"v_mul_f32 v29, v25, v27\n" \
"v_mfma_f32_16x16x32_bf16 v[212:215], a[44:47], a[16:19], v[212:215]\n" \
"v_mul_f32 v58, v26, v28\n" \
"v_accvgpr_read_b32 v40, a200\n" \
"v_mfma_f32_16x16x32_bf16 v[216:219], a[40:43], a[20:23], v[216:219]\n" \
"ds_read_b64_tr_b16 a[120:121], v9 offset:17920\n" \
"v_lshlrev_b32 v34, 16, v40\n" \
"v_mfma_f32_16x16x32_bf16 v[220:223], a[44:47], a[20:23], v[220:223]\n" \
"v_and_b32 v40, 0xffff0000, v40\n" \
"v_mul_f32 v25, v31, v45\n" \
"v_mfma_f32_16x16x32_bf16 v[224:227], a[48:51], a[16:19], v[224:227]\n" \
"ds_read_b64_tr_b16 a[122:123], v10 offset:17920\n" \
"v_mul_f32 v26, v37, v49\n" \
"v_mfma_f32_16x16x32_bf16 v[228:231], a[52:55], a[16:19], v[228:231]\n" \
"v_fmac_f32 v25, v32, v44\n" \
"v_fmac_f32 v26, v38, v48\n" \
"v_mfma_f32_16x16x32_bf16 v[232:235], a[48:51], a[20:23], v[232:235]\n" \
"s_add_u32 m0, s50, 24576\n" \
"ds_read_b128 a[92:95], v6 offset:47104\n" \
"v_fmac_f32 v25, v33, v43\n" \
"v_mfma_f32_16x16x32_bf16 v[236:239], a[52:55], a[20:23], v[236:239]\n" \
"buffer_load_dwordx4 v19, s[28:31], s46 offen lds\n" \
"v_mfma_f32_16x16x32_bf16 v[240:243], a[56:59], a[16:19], v[240:243]\n" \
"ds_read_b64_tr_b16 a[124:125], v11 offset:17920\n" \
"v_fmac_f32 v26, v39, v47\n" \
"v_mfma_f32_16x16x32_bf16 v[244:247], a[60:63], a[16:19], v[244:247]\n" \
"v_fmac_f32 v25, v34, v42\n" \
"v_fmac_f32 v26, v40, v46\n" \
"v_mfma_f32_16x16x32_bf16 v[248:251], a[56:59], a[20:23], v[248:251]\n" \
"ds_read_b64_tr_b16 a[126:127], v12 offset:17920\n" \
"v_accvgpr_read_b32 v28, a216\n" \
"v_mfma_f32_16x16x32_bf16 v[252:255], a[60:63], a[20:23], v[252:255]\n" \
"v_lshlrev_b32 v27, 16, v28\n" \
"v_and_b32 v28, 0xffff0000, v28\n" \
"v_mfma_f32_16x16x32_bf16 a[128:131], a[32:35], a[24:27], a[128:131]\n" \
"v_mul_f32 v25, v25, v27\n" \
"v_mul_f32 v26, v26, v28\n" \
"v_mfma_f32_16x16x32_bf16 a[132:135], a[36:39], a[24:27], a[132:135]\n" \
"v_cvt_pk_bf16_f32 v53, v25, v29\n" \
"v_mfma_f32_16x16x32_bf16 a[136:139], a[32:35], a[28:31], a[136:139]\n" \
"s_add_u32 m0, s22, 2688\n" \
"s_add_u32 s84, s84, 1048576\ns_addc_u32 s85, s85, 0\n" \
"v_mfma_f32_16x16x32_bf16 a[140:143], a[36:39], a[28:31], a[140:143]\n" \
"buffer_load_dwordx4 v21, s[32:35], s47 offen offset:384 lds\n" \
"v_mfma_f32_16x16x32_bf16 a[144:147], a[40:43], a[24:27], a[144:147]\n" \
"v_cvt_pk_bf16_f32 v57, v26, v58\n" \
"v_accvgpr_read_b32 v41, a202\n" \
"v_mfma_f32_16x16x32_bf16 a[148:151], a[44:47], a[24:27], a[148:151]\n" \
"v_lshlrev_b32 v35, 16, v41\n" \
"v_and_b32 v41, 0xffff0000, v41\n" \
"v_mfma_f32_16x16x32_bf16 a[152:155], a[40:43], a[28:31], a[152:155]\n" \
"v_mul_f32 v25, v32, v45\n" \
"v_mfma_f32_16x16x32_bf16 a[156:159], a[44:47], a[28:31], a[156:159]\n" \
"s_waitcnt vmcnt(26) lgkmcnt(0)\n" \
"v_mul_f32 v26, v38, v49\n" \
"v_fmac_f32 v25, v33, v44\n" \
"v_mfma_f32_16x16x32_bf16 a[160:163], a[48:51], a[24:27], a[160:163]\n" \
"s_barrier\n" \
"v_fmac_f32 v26, v39, v48\n" \
"v_fmac_f32 v25, v34, v43\n" \
"v_mfma_f32_16x16x32_bf16 a[164:167], a[52:55], a[24:27], a[164:167]\n" \
"v_fmac_f32 v26, v40, v47\n" \
"v_mfma_f32_16x16x32_bf16 a[168:171], a[48:51], a[28:31], a[168:171]\n" \
"s_add_u32 m0, s50, 28672\n" \
"v_fmac_f32 v25, v35, v42\n" \
"v_fmac_f32 v26, v41, v46\n" \
"v_mfma_f32_16x16x32_bf16 a[172:175], a[52:55], a[28:31], a[172:175]\n" \
"buffer_load_dwordx4 v20, s[28:31], s46 offen lds\n" \
"v_mfma_f32_16x16x32_bf16 a[176:179], a[56:59], a[24:27], a[176:179]\n" \
"v_accvgpr_read_b32 v28, a218\n" \
"v_lshlrev_b32 v27, 16, v28\n" \
"v_mfma_f32_16x16x32_bf16 a[180:183], a[60:63], a[24:27], a[180:183]\n" \
"v_and_b32 v28, 0xffff0000, v28\n" \
"v_mul_f32 v29, v25, v27\n" \
"v_mfma_f32_16x16x32_bf16 a[184:187], a[56:59], a[28:31], a[184:187]\n" \
"v_mul_f32 v58, v26, v28\n" \
"v_mfma_f32_16x16x32_bf16 a[188:191], a[60:63], a[28:31], a[188:191]\n" \
"s_mov_b64 exec, 0xffff\nglobal_load_dword v22, v1, s[84:85]\ns_mov_b64 exec, -1\n" \
"v_mfma_f32_16x16x32_bf16 v[64:67], a[96:99], a[64:67], v[64:67]\n" \
"ds_read_b128 a[0:3], v7 offset:0\n" \
"v_accvgpr_read_b32 v36, a204\n" \
"v_mfma_f32_16x16x32_bf16 v[68:71], a[100:103], a[64:67], v[68:71]\n" \
"v_lshlrev_b32 v30, 16, v36\n" \
"v_and_b32 v36, 0xffff0000, v36\n" \
"v_mfma_f32_16x16x32_bf16 v[72:75], a[96:99], a[68:71], v[72:75]\n" \
"s_mov_b32 s47, 2359296\n" \
"s_add_u32 m0, s22, 16384\n" \
"ds_read_b64_tr_b16 a[32:33], v9 offset:32768\n" \
"v_mfma_f32_16x16x32_bf16 v[76:79], a[100:103], a[68:71], v[76:79]\n" \
"buffer_load_dwordx4 v21, s[32:35], s47 offen offset:0 lds\n" \
"v_mfma_f32_16x16x32_bf16 v[80:83], a[104:107], a[64:67], v[80:83]\n" \
"ds_read_b64_tr_b16 a[34:35], v10 offset:32768\n" \
"v_mul_f32 v25, v33, v45\n" \
"v_mfma_f32_16x16x32_bf16 v[84:87], a[108:111], a[64:67], v[84:87]\n" \
"v_mul_f32 v26, v39, v49\n" \
"v_fmac_f32 v25, v34, v44\n" \
"v_mfma_f32_16x16x32_bf16 v[88:91], a[104:107], a[68:71], v[88:91]\n" \
"ds_read_b128 a[4:7], v7 offset:2048\n" \
"v_fmac_f32 v26, v40, v48\n" \
"v_mfma_f32_16x16x32_bf16 v[92:95], a[108:111], a[68:71], v[92:95]\n" \
"v_fmac_f32 v25, v35, v43\n" \
"v_fmac_f32 v26, v41, v47\n" \
"v_mfma_f32_16x16x32_bf16 v[96:99], a[112:115], a[64:67], v[96:99]\n" \
"ds_read_b64_tr_b16 a[36:37], v11 offset:32768\n" \
"v_fmac_f32 v25, v30, v42\n" \
"v_mfma_f32_16x16x32_bf16 v[100:103], a[116:119], a[64:67], v[100:103]\n" \
"v_fmac_f32 v26, v36, v46\n" \
"v_accvgpr_read_b32 v28, a220\n" \
"v_mfma_f32_16x16x32_bf16 v[104:107], a[112:115], a[68:71], v[104:107]\n" \
"s_mov_b32 s46, 640\n" \
"s_add_u32 m0, s50, 32768\n" \
"ds_read_b64_tr_b16 a[38:39], v12 offset:32768\n" \
"v_mfma_f32_16x16x32_bf16 v[108:111], a[116:119], a[68:71], v[108:111]\n" \
"buffer_load_dwordx4 v13, s[28:31], s46 offen lds\n" \
"v_mfma_f32_16x16x32_bf16 v[112:115], a[120:123], a[64:67], v[112:115]\n" \
"ds_read_b128 a[8:11], v7 offset:4096\n" \
"v_lshlrev_b32 v27, 16, v28\n" \
"v_mfma_f32_16x16x32_bf16 v[116:119], a[124:127], a[64:67], v[116:119]\n" \
"v_and_b32 v28, 0xffff0000, v28\n" \
"v_mul_f32 v25, v25, v27\n" \
"v_mfma_f32_16x16x32_bf16 v[120:123], a[120:123], a[68:71], v[120:123]\n" \
"ds_read_b64_tr_b16 a[40:41], v9 offset:33280\n" \
"v_mul_f32 v26, v26, v28\n" \
"v_mfma_f32_16x16x32_bf16 v[124:127], a[124:127], a[68:71], v[124:127]\n" \
"s_waitcnt vmcnt(8)\n" \
"v_cvt_pk_bf16_f32 v52, v25, v29\n" \
"v_cvt_pk_bf16_f32 v56, v26, v58\n" \
"v_mfma_f32_16x16x32_bf16 v[128:131], a[96:99], a[72:75], v[128:131]\n" \
"ds_read_b64_tr_b16 a[42:43], v10 offset:33280\n" \
"v_accvgpr_read_b32 v37, a206\n" \
"v_mfma_f32_16x16x32_bf16 v[132:135], a[100:103], a[72:75], v[132:135]\n" \
"v_lshlrev_b32 v31, 16, v37\n" \
"v_and_b32 v37, 0xffff0000, v37\n" \
"v_mfma_f32_16x16x32_bf16 v[136:139], a[96:99], a[76:79], v[136:139]\n" \
"s_add_u32 m0, s22, 17280\n" \
"ds_read_b128 a[12:15], v7 offset:6144\n" \
"v_mul_f32 v25, v34, v45\n" \
"v_mfma_f32_16x16x32_bf16 v[140:143], a[100:103], a[76:79], v[140:143]\n" \
"buffer_load_dwordx4 v21, s[32:35], s47 offen offset:128 lds\n" \
"v_mfma_f32_16x16x32_bf16 v[144:147], a[104:107], a[72:75], v[144:147]\n" \
"ds_read_b64_tr_b16 a[44:45], v11 offset:33280\n" \
"v_mul_f32 v26, v40, v49\n" \
"v_mfma_f32_16x16x32_bf16 v[148:151], a[108:111], a[72:75], v[148:151]\n" \
"v_fmac_f32 v25, v35, v44\n" \
"v_fmac_f32 v26, v41, v48\n" \
"v_mfma_f32_16x16x32_bf16 v[152:155], a[104:107], a[76:79], v[152:155]\n" \
"ds_read_b64_tr_b16 a[46:47], v12 offset:33280\n" \
"v_fmac_f32 v25, v30, v43\n" \
"v_mfma_f32_16x16x32_bf16 v[156:159], a[108:111], a[76:79], v[156:159]\n" \
"v_fmac_f32 v26, v36, v47\n" \
"v_fmac_f32 v25, v31, v42\n" \
"v_mfma_f32_16x16x32_bf16 v[160:163], a[112:115], a[72:75], v[160:163]\n" \
"ds_read_b128 a[16:19], v7 offset:8192\n" \
"v_fmac_f32 v26, v37, v46\n" \
"v_mfma_f32_16x16x32_bf16 v[164:167], a[116:119], a[72:75], v[164:167]\n" \
"v_accvgpr_read_b32 v28, a222\n" \
"v_lshlrev_b32 v27, 16, v28\n" \
"v_mfma_f32_16x16x32_bf16 v[168:171], a[112:115], a[76:79], v[168:171]\n" \
"s_add_u32 m0, s50, 36864\n" \
"ds_read_b64_tr_b16 a[48:49], v9 offset:33792\n" \
"v_and_b32 v28, 0xffff0000, v28\n" \
"v_mfma_f32_16x16x32_bf16 v[172:175], a[116:119], a[76:79], v[172:175]\n" \
"buffer_load_dwordx4 v14, s[28:31], s46 offen lds\n" \
"v_mfma_f32_16x16x32_bf16 v[176:179], a[120:123], a[72:75], v[176:179]\n" \
"ds_read_b64_tr_b16 a[50:51], v10 offset:33792\n" \
"v_mul_f32 v29, v25, v27\n" \
"v_mfma_f32_16x16x32_bf16 v[180:183], a[124:127], a[72:75], v[180:183]\n" \
"v_mul_f32 v58, v26, v28\n" \
"v_accvgpr_read_b32 v38, a208\n" \
"v_mfma_f32_16x16x32_bf16 v[184:187], a[120:123], a[76:79], v[184:187]\n" \
"ds_read_b128 a[20:23], v7 offset:10240\n" \
"v_lshlrev_b32 v32, 16, v38\n" \
"v_mfma_f32_16x16x32_bf16 v[188:191], a[124:127], a[76:79], v[188:191]\n" \
"v_and_b32 v38, 0xffff0000, v38\n" \
"v_mul_f32 v25, v35, v45\n" \
"v_mfma_f32_16x16x32_bf16 v[192:195], a[96:99], a[80:83], v[192:195]\n" \
"ds_read_b64_tr_b16 a[52:53], v11 offset:33792\n" \
"v_mul_f32 v26, v41, v49\n" \
"v_mfma_f32_16x16x32_bf16 v[196:199], a[100:103], a[80:83], v[196:199]\n" \
"v_fmac_f32 v25, v30, v44\n" \
"v_fmac_f32 v26, v36, v48\n" \
"v_mfma_f32_16x16x32_bf16 v[200:203], a[96:99], a[84:87], v[200:203]\n" \
"s_add_u32 m0, s22, 18176\n" \
"ds_read_b64_tr_b16 a[54:55], v12 offset:33792\n" \
"v_fmac_f32 v25, v31, v43\n" \
"v_mfma_f32_16x16x32_bf16 v[204:207], a[100:103], a[84:87], v[204:207]\n" \
"buffer_load_dwordx4 v21, s[32:35], s47 offen offset:256 lds\n" \
"v_mfma_f32_16x16x32_bf16 v[208:211], a[104:107], a[80:83], v[208:211]\n" \
"ds_read_b128 a[24:27], v7 offset:12288\n" \
"v_fmac_f32 v26, v37, v47\n" \
"v_mfma_f32_16x16x32_bf16 v[212:215], a[108:111], a[80:83], v[212:215]\n" \
"v_fmac_f32 v25, v32, v42\n" \
"v_fmac_f32 v26, v38, v46\n" \
"v_mfma_f32_16x16x32_bf16 v[216:219], a[104:107], a[84:87], v[216:219]\n" \
"ds_read_b64_tr_b16 a[56:57], v9 offset:34304\n" \
"v_accvgpr_read_b32 v28, a224\n" \
"v_mfma_f32_16x16x32_bf16 v[220:223], a[108:111], a[84:87], v[220:223]\n" \
"v_lshlrev_b32 v27, 16, v28\n" \
"v_and_b32 v28, 0xffff0000, v28\n" \
"v_mfma_f32_16x16x32_bf16 v[224:227], a[112:115], a[80:83], v[224:227]\n" \
"ds_read_b64_tr_b16 a[58:59], v10 offset:34304\n" \
"v_mul_f32 v25, v25, v27\n" \
"v_mfma_f32_16x16x32_bf16 v[228:231], a[116:119], a[80:83], v[228:231]\n" \
"v_mul_f32 v26, v26, v28\n" \
"v_cvt_pk_bf16_f32 v51, v25, v29\n" \
"v_mfma_f32_16x16x32_bf16 v[232:235], a[112:115], a[84:87], v[232:235]\n" \
"s_add_u32 m0, s50, 40960\n" \
"ds_read_b128 a[28:31], v7 offset:14336\n" \
"v_cvt_pk_bf16_f32 v55, v26, v58\n" \
"v_mfma_f32_16x16x32_bf16 v[236:239], a[116:119], a[84:87], v[236:239]\n" \
"buffer_load_dwordx4 v15, s[28:31], s46 offen lds\n" \
"v_mfma_f32_16x16x32_bf16 v[240:243], a[120:123], a[80:83], v[240:243]\n" \
"ds_read_b64_tr_b16 a[60:61], v11 offset:34304\n" \
"v_accvgpr_read_b32 v39, a210\n" \
"v_mfma_f32_16x16x32_bf16 v[244:247], a[124:127], a[80:83], v[244:247]\n" \
"v_lshlrev_b32 v33, 16, v39\n" \
"v_and_b32 v39, 0xffff0000, v39\n" \
"v_mfma_f32_16x16x32_bf16 v[248:251], a[120:123], a[84:87], v[248:251]\n" \
"ds_read_b64_tr_b16 a[62:63], v12 offset:34304\n" \
"v_mul_f32 v25, v30, v45\n" \
"v_mfma_f32_16x16x32_bf16 v[252:255], a[124:127], a[84:87], v[252:255]\n" \
"v_mul_f32 v26, v36, v49\n" \
"v_fmac_f32 v25, v31, v44\n" \
"v_mfma_f32_16x16x32_bf16 a[128:131], a[96:99], a[88:91], a[128:131]\n" \
"v_fmac_f32 v26, v37, v48\n" \
"v_fmac_f32 v25, v32, v43\n" \
"v_mfma_f32_16x16x32_bf16 a[132:135], a[100:103], a[88:91], a[132:135]\n" \
"v_fmac_f32 v26, v38, v47\n" \
"v_mfma_f32_16x16x32_bf16 a[136:139], a[96:99], a[92:95], a[136:139]\n" \
"s_add_u32 m0, s22, 19072\n" \
"v_fmac_f32 v25, v33, v42\n" \
"v_fmac_f32 v26, v39, v46\n" \
"v_mfma_f32_16x16x32_bf16 a[140:143], a[100:103], a[92:95], a[140:143]\n" \
"buffer_load_dwordx4 v21, s[32:35], s47 offen offset:384 lds\n" \
"v_mfma_f32_16x16x32_bf16 a[144:147], a[104:107], a[88:91], a[144:147]\n" \
"v_accvgpr_read_b32 v28, a226\n" \
"v_lshlrev_b32 v27, 16, v28\n" \
"v_mfma_f32_16x16x32_bf16 a[148:151], a[108:111], a[88:91], a[148:151]\n" \
"v_and_b32 v28, 0xffff0000, v28\n" \
"v_mul_f32 v29, v25, v27\n" \
"v_mfma_f32_16x16x32_bf16 a[152:155], a[104:107], a[92:95], a[152:155]\n" \
"v_mul_f32 v58, v26, v28\n" \
"v_mfma_f32_16x16x32_bf16 a[156:159], a[108:111], a[92:95], a[156:159]\n" \
"s_waitcnt vmcnt(21) lgkmcnt(0)\n" \
"v_accvgpr_read_b32 v40, a212\n" \
"v_lshlrev_b32 v34, 16, v40\n" \
"v_mfma_f32_16x16x32_bf16 a[160:163], a[112:115], a[88:91], a[160:163]\n" \
"s_barrier\n" \
"v_and_b32 v40, 0xffff0000, v40\n" \
"v_mul_f32 v25, v31, v45\n" \
"v_mfma_f32_16x16x32_bf16 a[164:167], a[116:119], a[88:91], a[164:167]\n" \
"v_mul_f32 v26, v37, v49\n" \
"v_mfma_f32_16x16x32_bf16 a[168:171], a[112:115], a[92:95], a[168:171]\n" \
"s_add_u32 m0, s50, 45056\n" \
"v_fmac_f32 v25, v32, v44\n" \
"v_fmac_f32 v26, v38, v48\n" \
"v_mfma_f32_16x16x32_bf16 a[172:175], a[116:119], a[92:95], a[172:175]\n" \
"buffer_load_dwordx4 v16, s[28:31], s46 offen lds\n" \
"v_mfma_f32_16x16x32_bf16 a[176:179], a[120:123], a[88:91], a[176:179]\n" \
"v_fmac_f32 v25, v33, v43\n" \
"v_fmac_f32 v26, v39, v47\n" \
"v_mfma_f32_16x16x32_bf16 a[180:183], a[124:127], a[88:91], a[180:183]\n" \
"v_fmac_f32 v25, v34, v42\n" \
"v_fmac_f32 v26, v40, v46\n" \
"v_mfma_f32_16x16x32_bf16 a[184:187], a[120:123], a[92:95], a[184:187]\n" \
"v_accvgpr_read_b32 v28, a228\n" \
"v_mfma_f32_16x16x32_bf16 a[188:191], a[124:127], a[92:95], a[188:191]\n" \
"v_lshlrev_b32 v27, 16, v28\n" \
"v_and_b32 v28, 0xffff0000, v28\n" \
"v_mfma_f32_16x16x32_bf16 v[64:67], a[32:35], a[0:3], v[64:67]\n" \
"s_mov_b32 s47, 2621440\n" \
"s_add_u32 m0, s22, 32768\n" \
"ds_read_b128 a[64:67], v8 offset:0\n" \
"v_mfma_f32_16x16x32_bf16 v[68:71], a[36:39], a[0:3], v[68:71]\n" \
"buffer_load_dwordx4 v21, s[32:35], s47 offen offset:0 lds\n" \
"v_mfma_f32_16x16x32_bf16 v[72:75], a[32:35], a[4:7], v[72:75]\n" \
"ds_read_b64_tr_b16 a[96:97], v9 offset:49152\n" \
"v_mul_f32 v25, v25, v27\n" \
"v_mfma_f32_16x16x32_bf16 v[76:79], a[36:39], a[4:7], v[76:79]\n" \
"v_mul_f32 v26, v26, v28\n" \
"v_cvt_pk_bf16_f32 v50, v25, v29\n" \
"v_mfma_f32_16x16x32_bf16 v[80:83], a[40:43], a[0:3], v[80:83]\n" \
"s_mov_b32 s46, 640\n" \
"s_add_u32 m0, s50, 49152\n" \
"ds_read_b64_tr_b16 a[98:99], v10 offset:49152\n" \
"v_mfma_f32_16x16x32_bf16 v[84:87], a[44:47], a[0:3], v[84:87]\n" \
"buffer_load_dwordx4 v17, s[28:31], s46 offen lds\n" \
"v_mfma_f32_16x16x32_bf16 v[88:91], a[40:43], a[4:7], v[88:91]\n" \
"s_mov_b32 s94, 79691776\n" \
"ds_read_b128 a[68:71], v8 offset:2048\n" \
"v_cvt_pk_bf16_f32 v54, v26, v58\n" \
"v_mfma_f32_16x16x32_bf16 v[92:95], a[44:47], a[4:7], v[92:95]\n" \
"buffer_store_dwordx4 v[50:53], v60, s[36:39], s94 offen offset:0 sc1\n" \
"v_mfma_f32_16x16x32_bf16 v[96:99], a[48:51], a[0:3], v[96:99]\n" \
"s_add_u32 m0, s22, 33664\n" \
"ds_read_b64_tr_b16 a[100:101], v11 offset:49152\n" \
"s_mov_b32 s95, 79695872\n" \
"v_mfma_f32_16x16x32_bf16 v[100:103], a[52:55], a[0:3], v[100:103]\n" \
"buffer_load_dwordx4 v21, s[32:35], s47 offen offset:128 lds\n" \
"v_mfma_f32_16x16x32_bf16 v[104:107], a[48:51], a[4:7], v[104:107]\n" \
"ds_read_b64_tr_b16 a[102:103], v12 offset:49152\n" \
"v_accvgpr_read_b32 v43, a234\n" \
"v_mfma_f32_16x16x32_bf16 v[108:111], a[52:55], a[4:7], v[108:111]\n" \
"v_lshlrev_b32 v42, 16, v43\n" \
"v_and_b32 v43, 0xffff0000, v43\n" \
"v_mfma_f32_16x16x32_bf16 v[112:115], a[56:59], a[0:3], v[112:115]\n" \
"s_add_u32 m0, s50, 53248\n" \
"ds_read_b128 a[72:75], v8 offset:4096\n" \
"v_accvgpr_read_b32 v45, a235\n" \
"v_mfma_f32_16x16x32_bf16 v[116:119], a[60:63], a[0:3], v[116:119]\n" \
"buffer_load_dwordx4 v18, s[28:31], s46 offen lds\n" \
"v_mfma_f32_16x16x32_bf16 v[120:123], a[56:59], a[4:7], v[120:123]\n" \
"ds_read_b64_tr_b16 a[104:105], v9 offset:49664\n" \
"v_lshlrev_b32 v44, 16, v45\n" \
"v_mfma_f32_16x16x32_bf16 v[124:127], a[60:63], a[4:7], v[124:127]\n" \
"v_and_b32 v45, 0xffff0000, v45\n" \
"v_accvgpr_read_b32 v47, a236\n" \
"v_mfma_f32_16x16x32_bf16 v[128:131], a[32:35], a[8:11], v[128:131]\n" \
"s_add_u32 m0, s22, 34560\n" \
"ds_read_b64_tr_b16 a[106:107], v10 offset:49664\n" \
"v_lshlrev_b32 v46, 16, v47\n" \
"v_mfma_f32_16x16x32_bf16 v[132:135], a[36:39], a[8:11], v[132:135]\n" \
"buffer_load_dwordx4 v21, s[32:35], s47 offen offset:256 lds\n" \
"v_mfma_f32_16x16x32_bf16 v[136:139], a[32:35], a[12:15], v[136:139]\n" \
"ds_read_b128 a[76:79], v8 offset:6144\n" \
"v_and_b32 v47, 0xffff0000, v47\n" \
"v_mfma_f32_16x16x32_bf16 v[140:143], a[36:39], a[12:15], v[140:143]\n" \
"v_accvgpr_read_b32 v49, a237\n" \
"v_lshlrev_b32 v48, 16, v49\n" \
"v_mfma_f32_16x16x32_bf16 v[144:147], a[40:43], a[8:11], v[144:147]\n" \
"s_add_u32 m0, s50, 57344\n" \
"ds_read_b64_tr_b16 a[108:109], v11 offset:49664\n" \
"v_and_b32 v49, 0xffff0000, v49\n" \
"v_mfma_f32_16x16x32_bf16 v[148:151], a[44:47], a[8:11], v[148:151]\n" \
"buffer_load_dwordx4 v19, s[28:31], s46 offen lds\n" \
"v_mfma_f32_16x16x32_bf16 v[152:155], a[40:43], a[12:15], v[152:155]\n" \
"ds_read_b64_tr_b16 a[110:111], v12 offset:49664\n" \
"v_accvgpr_read_b32 v36, a193\n" \
"v_mfma_f32_16x16x32_bf16 v[156:159], a[44:47], a[12:15], v[156:159]\n" \
"buffer_store_dwordx4 v[54:57], v60, s[36:39], s95 offen offset:0 sc1\n" \
"v_mfma_f32_16x16x32_bf16 v[160:163], a[48:51], a[8:11], v[160:163]\n" \
"s_add_u32 m0, s22, 35456\n" \
"ds_read_b128 a[80:83], v8 offset:8192\n" \
"v_lshlrev_b32 v30, 16, v36\n" \
"v_mfma_f32_16x16x32_bf16 v[164:167], a[52:55], a[8:11], v[164:167]\n" \
"buffer_load_dwordx4 v21, s[32:35], s47 offen offset:384 lds\n" \
"v_mfma_f32_16x16x32_bf16 v[168:171], a[48:51], a[12:15], v[168:171]\n" \
"s_add_u32 s84, s84, 1048576\ns_addc_u32 s85, s85, 0\n" \
"ds_read_b64_tr_b16 a[112:113], v9 offset:50176\n" \
"v_mfma_f32_16x16x32_bf16 v[172:175], a[52:55], a[12:15], v[172:175]\n" \
"v_and_b32 v36, 0xffff0000, v36\n" \
"v_accvgpr_read_b32 v37, a195\n" \
"v_mfma_f32_16x16x32_bf16 v[176:179], a[56:59], a[8:11], v[176:179]\n" \
"s_add_u32 m0, s50, 61440\n" \
"ds_read_b64_tr_b16 a[114:115], v10 offset:50176\n" \
"v_lshlrev_b32 v31, 16, v37\n" \
"v_mfma_f32_16x16x32_bf16 v[180:183], a[60:63], a[8:11], v[180:183]\n" \
"buffer_load_dwordx4 v20, s[28:31], s46 offen lds\n" \
"v_mfma_f32_16x16x32_bf16 v[184:187], a[56:59], a[12:15], v[184:187]\n" \
"ds_read_b128 a[84:87], v8 offset:10240\n" \
"v_and_b32 v37, 0xffff0000, v37\n" \
"v_mfma_f32_16x16x32_bf16 v[188:191], a[60:63], a[12:15], v[188:191]\n" \
"v_accvgpr_read_b32 v38, a197\n" \
"v_lshlrev_b32 v32, 16, v38\n" \
"v_mfma_f32_16x16x32_bf16 v[192:195], a[32:35], a[16:19], v[192:195]\n" \
"ds_read_b64_tr_b16 a[116:117], v11 offset:50176\n" \
"v_and_b32 v38, 0xffff0000, v38\n" \
"v_mfma_f32_16x16x32_bf16 v[196:199], a[36:39], a[16:19], v[196:199]\n" \
"s_mov_b64 exec, 0xffff\nglobal_load_dword v22, v1, s[84:85]\ns_mov_b64 exec, -1\n" \
"v_mfma_f32_16x16x32_bf16 v[200:203], a[32:35], a[20:23], v[200:203]\n" \
"ds_read_b64_tr_b16 a[118:119], v12 offset:50176\n" \
"v_accvgpr_read_b32 v39, a199\n" \
"v_mfma_f32_16x16x32_bf16 v[204:207], a[36:39], a[20:23], v[204:207]\n" \
"v_lshlrev_b32 v33, 16, v39\n" \
"v_and_b32 v39, 0xffff0000, v39\n" \
"v_mfma_f32_16x16x32_bf16 v[208:211], a[40:43], a[16:19], v[208:211]\n" \
"ds_read_b128 a[88:91], v8 offset:12288\n" \
"v_mul_f32 v25, v30, v45\n" \
"v_mfma_f32_16x16x32_bf16 v[212:215], a[44:47], a[16:19], v[212:215]\n" \
"s_mov_b64 exec, 0xffff\nglobal_load_dword v22, v1, s[76:77]\ns_mov_b64 exec, -1\n" \
"v_mfma_f32_16x16x32_bf16 v[216:219], a[40:43], a[20:23], v[216:219]\n" \
"ds_read_b64_tr_b16 a[120:121], v9 offset:50688\n" \
"v_mul_f32 v26, v36, v49\n" \
"v_mfma_f32_16x16x32_bf16 v[220:223], a[44:47], a[20:23], v[220:223]\n" \
"v_fmac_f32 v25, v31, v44\n" \
"v_fmac_f32 v26, v37, v48\n" \
"v_mfma_f32_16x16x32_bf16 v[224:227], a[48:51], a[16:19], v[224:227]\n" \
"ds_read_b64_tr_b16 a[122:123], v10 offset:50688\n" \
"v_fmac_f32 v25, v32, v43\n" \
"v_mfma_f32_16x16x32_bf16 v[228:231], a[52:55], a[16:19], v[228:231]\n" \
"v_fmac_f32 v26, v38, v47\n" \
"v_fmac_f32 v25, v33, v42\n" \
"v_mfma_f32_16x16x32_bf16 v[232:235], a[48:51], a[20:23], v[232:235]\n" \
"ds_read_b128 a[92:95], v8 offset:14336\n" \
"v_fmac_f32 v26, v39, v46\n" \
"v_mfma_f32_16x16x32_bf16 v[236:239], a[52:55], a[20:23], v[236:239]\n" \
"v_accvgpr_read_b32 v28, a215\n" \
"v_lshlrev_b32 v27, 16, v28\n" \
"v_mfma_f32_16x16x32_bf16 v[240:243], a[56:59], a[16:19], v[240:243]\n" \
"ds_read_b64_tr_b16 a[124:125], v11 offset:50688\n" \
"v_and_b32 v28, 0xffff0000, v28\n" \
"v_mfma_f32_16x16x32_bf16 v[244:247], a[60:63], a[16:19], v[244:247]\n" \
"v_mul_f32 v29, v25, v27\n" \
"v_mul_f32 v58, v26, v28\n" \
"v_mfma_f32_16x16x32_bf16 v[248:251], a[56:59], a[20:23], v[248:251]\n" \
"ds_read_b64_tr_b16 a[126:127], v12 offset:50688\n" \
"v_accvgpr_read_b32 v40, a201\n" \
"v_mfma_f32_16x16x32_bf16 v[252:255], a[60:63], a[20:23], v[252:255]\n" \
"v_lshlrev_b32 v34, 16, v40\n" \
"v_and_b32 v40, 0xffff0000, v40\n" \
"v_mfma_f32_16x16x32_bf16 a[128:131], a[32:35], a[24:27], a[128:131]\n" \
"v_mul_f32 v25, v31, v45\n" \
"v_mul_f32 v26, v37, v49\n" \
"v_mfma_f32_16x16x32_bf16 a[132:135], a[36:39], a[24:27], a[132:135]\n" \
"v_fmac_f32 v25, v32, v44\n" \
"v_mfma_f32_16x16x32_bf16 a[136:139], a[32:35], a[28:31], a[136:139]\n" \
"v_fmac_f32 v26, v38, v48\n" \
"v_fmac_f32 v25, v33, v43\n" \
"v_mfma_f32_16x16x32_bf16 a[140:143], a[36:39], a[28:31], a[140:143]\n" \
"v_fmac_f32 v26, v39, v47\n" \
"v_fmac_f32 v25, v34, v42\n" \
"v_mfma_f32_16x16x32_bf16 a[144:147], a[40:43], a[24:27], a[144:147]\n" \
"v_fmac_f32 v26, v40, v46\n" \
"v_mfma_f32_16x16x32_bf16 a[148:151], a[44:47], a[24:27], a[148:151]\n" \
"v_accvgpr_read_b32 v28, a217\n" \
"v_lshlrev_b32 v27, 16, v28\n" \
"v_mfma_f32_16x16x32_bf16 a[152:155], a[40:43], a[28:31], a[152:155]\n" \
"v_and_b32 v28, 0xffff0000, v28\n" \
"v_mul_f32 v25, v25, v27\n" \
"v_mfma_f32_16x16x32_bf16 a[156:159], a[44:47], a[28:31], a[156:159]\n" \
"s_waitcnt vmcnt(21) lgkmcnt(0)\n" \
"v_mul_f32 v26, v26, v28\n" \
"v_mfma_f32_16x16x32_bf16 a[160:163], a[48:51], a[24:27], a[160:163]\n" \
"s_barrier\n" \
"v_cvt_pk_bf16_f32 v53, v25, v29\n" \
"v_cvt_pk_bf16_f32 v57, v26, v58\n" \
"v_mfma_f32_16x16x32_bf16 a[164:167], a[52:55], a[24:27], a[164:167]\n" \
"v_accvgpr_read_b32 v41, a203\n" \
"v_lshlrev_b32 v35, 16, v41\n" \
"v_mfma_f32_16x16x32_bf16 a[168:171], a[48:51], a[28:31], a[168:171]\n" \
"v_and_b32 v41, 0xffff0000, v41\n" \
"v_mfma_f32_16x16x32_bf16 a[172:175], a[52:55], a[28:31], a[172:175]\n" \
"v_mul_f32 v25, v32, v45\n" \
"v_mul_f32 v26, v38, v49\n" \
"v_mfma_f32_16x16x32_bf16 a[176:179], a[56:59], a[24:27], a[176:179]\n" \
"v_fmac_f32 v25, v33, v44\n" \
"v_fmac_f32 v26, v39, v48\n" \
"v_mfma_f32_16x16x32_bf16 a[180:183], a[60:63], a[24:27], a[180:183]\n" \
"v_fmac_f32 v25, v34, v43\n" \
"v_mfma_f32_16x16x32_bf16 a[184:187], a[56:59], a[28:31], a[184:187]\n" \
"v_fmac_f32 v26, v40, v47\n" \
"v_fmac_f32 v25, v35, v42\n" \
"v_mfma_f32_16x16x32_bf16 a[188:191], a[60:63], a[28:31], a[188:191]\n" \
"v_fmac_f32 v26, v41, v46\n" \
"v_accvgpr_read_b32 v28, a219\n" \
"v_mfma_f32_16x16x32_bf16 v[64:67], a[96:99], a[64:67], v[64:67]\n" \
"ds_read_b128 a[0:3], v5 offset:0\n" \
"v_lshlrev_b32 v27, 16, v28\n" \
"v_mfma_f32_16x16x32_bf16 v[68:71], a[100:103], a[64:67], v[68:71]\n" \
"v_and_b32 v28, 0xffff0000, v28\n" \
"v_mul_f32 v29, v25, v27\n" \
"v_mfma_f32_16x16x32_bf16 v[72:75], a[96:99], a[68:71], v[72:75]\n" \
"ds_read_b64_tr_b16 a[32:33], v9 offset:0\n" \
"v_mul_f32 v58, v26, v28\n" \
"v_mfma_f32_16x16x32_bf16 v[76:79], a[100:103], a[68:71], v[76:79]\n" \
"v_accvgpr_read_b32 v36, a205\n" \
"v_lshlrev_b32 v30, 16, v36\n" \
"v_mfma_f32_16x16x32_bf16 v[80:83], a[104:107], a[64:67], v[80:83]\n" \
"ds_read_b64_tr_b16 a[34:35], v10 offset:0\n" \
"v_and_b32 v36, 0xffff0000, v36\n" \
"v_mfma_f32_16x16x32_bf16 v[84:87], a[108:111], a[64:67], v[84:87]\n" \
"v_mul_f32 v25, v33, v45\n" \
"v_mul_f32 v26, v39, v49\n" \
"v_mfma_f32_16x16x32_bf16 v[88:91], a[104:107], a[68:71], v[88:91]\n" \
"ds_read_b128 a[4:7], v5 offset:2048\n" \
"v_fmac_f32 v25, v34, v44\n" \
"v_mfma_f32_16x16x32_bf16 v[92:95], a[108:111], a[68:71], v[92:95]\n" \
"v_fmac_f32 v26, v40, v48\n" \
"v_fmac_f32 v25, v35, v43\n" \
"v_mfma_f32_16x16x32_bf16 v[96:99], a[112:115], a[64:67], v[96:99]\n" \
"ds_read_b64_tr_b16 a[36:37], v11 offset:0\n" \
"v_fmac_f32 v26, v41, v47\n" \
"v_mfma_f32_16x16x32_bf16 v[100:103], a[116:119], a[64:67], v[100:103]\n" \
"v_fmac_f32 v25, v30, v42\n" \
"v_fmac_f32 v26, v36, v46\n" \
"v_mfma_f32_16x16x32_bf16 v[104:107], a[112:115], a[68:71], v[104:107]\n" \
"ds_read_b64_tr_b16 a[38:39], v12 offset:0\n" \
"v_accvgpr_read_b32 v28, a221\n" \
"v_mfma_f32_16x16x32_bf16 v[108:111], a[116:119], a[68:71], v[108:111]\n" \
"v_lshlrev_b32 v27, 16, v28\n" \
"v_and_b32 v28, 0xffff0000, v28\n" \
"v_mfma_f32_16x16x32_bf16 v[112:115], a[120:123], a[64:67], v[112:115]\n" \
"ds_read_b128 a[8:11], v5 offset:4096\n" \
"v_mul_f32 v25, v25, v27\n" \
"v_mfma_f32_16x16x32_bf16 v[116:119], a[124:127], a[64:67], v[116:119]\n" \
"v_mul_f32 v26, v26, v28\n" \
"v_cvt_pk_bf16_f32 v52, v25, v29\n" \
"v_mfma_f32_16x16x32_bf16 v[120:123], a[120:123], a[68:71], v[120:123]\n" \
"ds_read_b64_tr_b16 a[40:41], v9 offset:512\n" \
"v_cvt_pk_bf16_f32 v56, v26, v58\n" \
"v_mfma_f32_16x16x32_bf16 v[124:127], a[124:127], a[68:71], v[124:127]\n" \
"v_accvgpr_read_b32 v37, a207\n" \
"v_lshlrev_b32 v31, 16, v37\n" \
"v_mfma_f32_16x16x32_bf16 v[128:131], a[96:99], a[72:75], v[128:131]\n" \
"ds_read_b64_tr_b16 a[42:43], v10 offset:512\n" \
"v_and_b32 v37, 0xffff0000, v37\n" \
"v_mfma_f32_16x16x32_bf16 v[132:135], a[100:103], a[72:75], v[132:135]\n" \
"v_mul_f32 v25, v34, v45\n" \
"v_mul_f32 v26, v40, v49\n" \
"v_mfma_f32_16x16x32_bf16 v[136:139], a[96:99], a[76:79], v[136:139]\n" \
"ds_read_b128 a[12:15], v5 offset:6144\n" \
"v_fmac_f32 v25, v35, v44\n" \
"v_mfma_f32_16x16x32_bf16 v[140:143], a[100:103], a[76:79], v[140:143]\n" \
"v_fmac_f32 v26, v41, v48\n" \
"v_fmac_f32 v25, v30, v43\n" \
"v_mfma_f32_16x16x32_bf16 v[144:147], a[104:107], a[72:75], v[144:147]\n" \
"ds_read_b64_tr_b16 a[44:45], v11 offset:512\n" \
"v_fmac_f32 v26, v36, v47\n" \
"v_mfma_f32_16x16x32_bf16 v[148:151], a[108:111], a[72:75], v[148:151]\n" \
"v_fmac_f32 v25, v31, v42\n" \
"v_fmac_f32 v26, v37, v46\n" \
"v_mfma_f32_16x16x32_bf16 v[152:155], a[104:107], a[76:79], v[152:155]\n" \
"ds_read_b64_tr_b16 a[46:47], v12 offset:512\n" \
"v_accvgpr_read_b32 v28, a223\n" \
"v_mfma_f32_16x16x32_bf16 v[156:159], a[108:111], a[76:79], v[156:159]\n" \
"v_lshlrev_b32 v27, 16, v28\n" \
"v_and_b32 v28, 0xffff0000, v28\n" \
"v_mfma_f32_16x16x32_bf16 v[160:163], a[112:115], a[72:75], v[160:163]\n" \
"LH1_%=:\ns_cbranch_execz LH9_%=\n" \
"ds_read_b128 a[16:19], v5 offset:8192\n" \
"v_mul_f32 v29, v25, v27\n" \
"v_mfma_f32_16x16x32_bf16 v[164:167], a[116:119], a[72:75], v[164:167]\n" \
"v_mul_f32 v58, v26, v28\n" \
"v_accvgpr_read_b32 v38, a209\n" \
"v_mfma_f32_16x16x32_bf16 v[168:171], a[112:115], a[76:79], v[168:171]\n" \
"ds_read_b64_tr_b16 a[48:49], v9 offset:1024\n" \
"v_lshlrev_b32 v32, 16, v38\n" \
"v_mfma_f32_16x16x32_bf16 v[172:175], a[116:119], a[76:79], v[172:175]\n" \
"v_and_b32 v38, 0xffff0000, v38\n" \
"v_mul_f32 v25, v35, v45\n" \
"v_mfma_f32_16x16x32_bf16 v[176:179], a[120:123], a[72:75], v[176:179]\n" \
"ds_read_b64_tr_b16 a[50:51], v10 offset:1024\n" \
"v_mul_f32 v26, v41, v49\n" \
"v_mfma_f32_16x16x32_bf16 v[180:183], a[124:127], a[72:75], v[180:183]\n" \
"v_fmac_f32 v25, v30, v44\n" \
"v_fmac_f32 v26, v36, v48\n" \
"v_mfma_f32_16x16x32_bf16 v[184:187], a[120:123], a[76:79], v[184:187]\n" \
"ds_read_b128 a[20:23], v5 offset:10240\n" \
"v_fmac_f32 v25, v31, v43\n" \
"v_mfma_f32_16x16x32_bf16 v[188:191], a[124:127], a[76:79], v[188:191]\n" \
"v_fmac_f32 v26, v37, v47\n" \
"v_fmac_f32 v25, v32, v42\n" \
"v_mfma_f32_16x16x32_bf16 v[192:195], a[96:99], a[80:83], v[192:195]\n" \
"ds_read_b64_tr_b16 a[52:53], v11 offset:1024\n" \
"v_fmac_f32 v26, v38, v46\n" \
"v_mfma_f32_16x16x32_bf16 v[196:199], a[100:103], a[80:83], v[196:199]\n" \
"v_accvgpr_read_b32 v28, a225\n" \
"v_lshlrev_b32 v27, 16, v28\n" \
"v_mfma_f32_16x16x32_bf16 v[200:203], a[96:99], a[84:87], v[200:203]\n" \
"ds_read_b64_tr_b16 a[54:55], v12 offset:1024\n" \
"v_and_b32 v28, 0xffff0000, v28\n" \
"v_mfma_f32_16x16x32_bf16 v[204:207], a[100:103], a[84:87], v[204:207]\n" \
"v_mul_f32 v25, v25, v27\n" \
"v_mul_f32 v26, v26, v28\n" \
"v_mfma_f32_16x16x32_bf16 v[208:211], a[104:107], a[80:83], v[208:211]\n" \
"ds_read_b128 a[24:27], v5 offset:12288\n" \
"v_cvt_pk_bf16_f32 v51, v25, v29\n" \
"v_mfma_f32_16x16x32_bf16 v[212:215], a[108:111], a[80:83], v[212:215]\n" \
"v_cvt_pk_bf16_f32 v55, v26, v58\n" \
"v_accvgpr_read_b32 v39, a211\n" \
"v_mfma_f32_16x16x32_bf16 v[216:219], a[104:107], a[84:87], v[216:219]\n" \
"ds_read_b64_tr_b16 a[56:57], v9 offset:1536\n" \
"v_lshlrev_b32 v33, 16, v39\n" \
"v_mfma_f32_16x16x32_bf16 v[220:223], a[108:111], a[84:87], v[220:223]\n" \
"v_and_b32 v39, 0xffff0000, v39\n" \
"v_mul_f32 v25, v30, v45\n" \
"v_mfma_f32_16x16x32_bf16 v[224:227], a[112:115], a[80:83], v[224:227]\n" \
"ds_read_b64_tr_b16 a[58:59], v10 offset:1536\n" \
"v_mul_f32 v26, v36, v49\n" \
"v_mfma_f32_16x16x32_bf16 v[228:231], a[116:119], a[80:83], v[228:231]\n" \
"v_fmac_f32 v25, v31, v44\n" \
"v_fmac_f32 v26, v37, v48\n" \
"v_mfma_f32_16x16x32_bf16 v[232:235], a[112:115], a[84:87], v[232:235]\n" \
"ds_read_b128 a[28:31], v5 offset:14336\n" \
"v_fmac_f32 v25, v32, v43\n" \
"v_mfma_f32_16x16x32_bf16 v[236:239], a[116:119], a[84:87], v[236:239]\n" \
"v_fmac_f32 v26, v38, v47\n" \
"v_fmac_f32 v25, v33, v42\n" \
"v_mfma_f32_16x16x32_bf16 v[240:243], a[120:123], a[80:83], v[240:243]\n" \
"ds_read_b64_tr_b16 a[60:61], v11 offset:1536\n" \
"v_fmac_f32 v26, v39, v46\n" \
"v_mfma_f32_16x16x32_bf16 v[244:247], a[124:127], a[80:83], v[244:247]\n" \
"v_accvgpr_read_b32 v28, a227\n" \
"v_lshlrev_b32 v27, 16, v28\n" \
"v_mfma_f32_16x16x32_bf16 v[248:251], a[120:123], a[84:87], v[248:251]\n" \
"ds_read_b64_tr_b16 a[62:63], v12 offset:1536\n" \
"v_and_b32 v28, 0xffff0000, v28\n" \
"v_mfma_f32_16x16x32_bf16 v[252:255], a[124:127], a[84:87], v[252:255]\n" \
"v_mul_f32 v29, v25, v27\n" \
"v_mul_f32 v58, v26, v28\n" \
"v_mfma_f32_16x16x32_bf16 a[128:131], a[96:99], a[88:91], a[128:131]\n" \
"v_accvgpr_read_b32 v40, a213\n" \
"v_lshlrev_b32 v34, 16, v40\n" \
"v_mfma_f32_16x16x32_bf16 a[132:135], a[100:103], a[88:91], a[132:135]\n" \
"v_and_b32 v40, 0xffff0000, v40\n" \
"v_mfma_f32_16x16x32_bf16 a[136:139], a[96:99], a[92:95], a[136:139]\n" \
"v_mul_f32 v25, v31, v45\n" \
"v_mul_f32 v26, v37, v49\n" \
"v_mfma_f32_16x16x32_bf16 a[140:143], a[100:103], a[92:95], a[140:143]\n" \
"v_fmac_f32 v25, v32, v44\n" \
"v_fmac_f32 v26, v38, v48\n" \
"v_mfma_f32_16x16x32_bf16 a[144:147], a[104:107], a[88:91], a[144:147]\n" \
"v_fmac_f32 v25, v33, v43\n" \
"v_mfma_f32_16x16x32_bf16 a[148:151], a[108:111], a[88:91], a[148:151]\n" \
"v_fmac_f32 v26, v39, v47\n" \
"v_fmac_f32 v25, v34, v42\n" \
"v_mfma_f32_16x16x32_bf16 a[152:155], a[104:107], a[92:95], a[152:155]\n" \
"v_fmac_f32 v26, v40, v46\n" \
"v_accvgpr_read_b32 v28, a229\n" \
"v_mfma_f32_16x16x32_bf16 a[156:159], a[108:111], a[92:95], a[156:159]\n" \
"s_waitcnt vmcnt(13) lgkmcnt(0)\n" \
"v_lshlrev_b32 v27, 16, v28\n" \
"v_mfma_f32_16x16x32_bf16 a[160:163], a[112:115], a[88:91], a[160:163]\n" \
"s_barrier\n" \
"v_and_b32 v28, 0xffff0000, v28\n" \
"v_mul_f32 v25, v25, v27\n" \
"v_mfma_f32_16x16x32_bf16 a[164:167], a[116:119], a[88:91], a[164:167]\n" \
"v_mul_f32 v26, v26, v28\n" \
"v_cvt_pk_bf16_f32 v50, v25, v29\n" \
"v_mfma_f32_16x16x32_bf16 a[168:171], a[112:115], a[92:95], a[168:171]\n" \
"s_mov_b32 s94, 79699968\n" \
"v_cvt_pk_bf16_f32 v54, v26, v58\n" \
"v_mfma_f32_16x16x32_bf16 a[172:175], a[116:119], a[92:95], a[172:175]\n" \
"buffer_store_dwordx4 v[50:53], v60, s[36:39], s94 offen offset:0 sc1\n" \
"v_mfma_f32_16x16x32_bf16 a[176:179], a[120:123], a[88:91], a[176:179]\n" \
"s_mov_b32 s95, 79704064\n" \
"v_mfma_f32_16x16x32_bf16 a[180:183], a[124:127], a[88:91], a[180:183]\n" \
"v_mfma_f32_16x16x32_bf16 a[184:187], a[120:123], a[92:95], a[184:187]\n" \
"v_mfma_f32_16x16x32_bf16 a[188:191], a[124:127], a[92:95], a[188:191]\n" \
"v_mfma_f32_16x16x32_bf16 v[64:67], a[32:35], a[0:3], v[64:67]\n" \
"s_mov_b32 s47, 2883584\n" \
"s_add_u32 m0, s22, 49152\n" \
"ds_read_b128 a[64:67], v6 offset:0\n" \
"v_mfma_f32_16x16x32_bf16 v[68:71], a[36:39], a[0:3], v[68:71]\n" \
"buffer_load_dwordx4 v21, s[32:35], s47 offen offset:0 lds\n" \
"v_mfma_f32_16x16x32_bf16 v[72:75], a[32:35], a[4:7], v[72:75]\n" \
"ds_read_b64_tr_b16 a[96:97], v9 offset:16384\n" \
"v_mfma_f32_16x16x32_bf16 v[76:79], a[36:39], a[4:7], v[76:79]\n" \
"v_mfma_f32_16x16x32_bf16 v[80:83], a[40:43], a[0:3], v[80:83]\n" \
"s_add_u32 m0, s22, 50048\n" \
"ds_read_b64_tr_b16 a[98:99], v10 offset:16384\n" \
"v_mfma_f32_16x16x32_bf16 v[84:87], a[44:47], a[0:3], v[84:87]\n" \
"buffer_load_dwordx4 v21, s[32:35], s47 offen offset:128 lds\n" \
"v_mfma_f32_16x16x32_bf16 v[88:91], a[40:43], a[4:7], v[88:91]\n" \
"ds_read_b128 a[68:71], v6 offset:2048\n" \
"v_mfma_f32_16x16x32_bf16 v[92:95], a[44:47], a[4:7], v[92:95]\n" \
"v_mfma_f32_16x16x32_bf16 v[96:99], a[48:51], a[0:3], v[96:99]\n" \
"s_add_u32 m0, s22, 50944\n" \
"ds_read_b64_tr_b16 a[100:101], v11 offset:16384\n" \
"v_mfma_f32_16x16x32_bf16 v[100:103], a[52:55], a[0:3], v[100:103]\n" \
"buffer_load_dwordx4 v21, s[32:35], s47 offen offset:256 lds\n" \
"v_mfma_f32_16x16x32_bf16 v[104:107], a[48:51], a[4:7], v[104:107]\n" \
"ds_read_b64_tr_b16 a[102:103], v12 offset:16384\n" \
"v_mfma_f32_16x16x32_bf16 v[108:111], a[52:55], a[4:7], v[108:111]\n" \
"buffer_store_dwordx4 v[54:57], v60, s[36:39], s95 offen offset:0 sc1\n" \
"v_mfma_f32_16x16x32_bf16 v[112:115], a[56:59], a[0:3], v[112:115]\n" \
"s_add_u32 m0, s22, 51840\n" \
"ds_read_b128 a[72:75], v6 offset:4096\n" \
"s_lshl_b32 s62, s8, 2\n" \
"v_mfma_f32_16x16x32_bf16 v[116:119], a[60:63], a[0:3], v[116:119]\n" \
"buffer_load_dwordx4 v21, s[32:35], s47 offen offset:384 lds\n" \
"v_mfma_f32_16x16x32_bf16 v[120:123], a[56:59], a[4:7], v[120:123]\n" \
"s_add_u32 s62, s62, %2\n" \
"ds_read_b64_tr_b16 a[104:105], v9 offset:16896\n" \
"s_lshl_b32 s63, s62, 12\n" \
"v_mfma_f32_16x16x32_bf16 v[124:127], a[60:63], a[4:7], v[124:127]\n" \
"s_add_u32 s68, s10, s63\ns_addc_u32 s69, s11, 0\n" \
"s_and_b32 s69, s69, 0xffff\n" \
"v_mfma_f32_16x16x32_bf16 v[128:131], a[32:35], a[8:11], v[128:131]\n" \
"s_mov_b32 s46, 768\n" \
"s_add_u32 m0, s50, 65536\n" \
"ds_read_b64_tr_b16 a[106:107], v10 offset:16896\n" \
"v_mfma_f32_16x16x32_bf16 v[132:135], a[36:39], a[8:11], v[132:135]\n" \
"buffer_load_dwordx4 v13, s[28:31], s46 offen lds\n" \
"v_mfma_f32_16x16x32_bf16 v[136:139], a[32:35], a[12:15], v[136:139]\n" \
"s_mov_b32 s70, 4096\n" \
"ds_read_b128 a[76:79], v6 offset:6144\n" \
"s_mov_b32 s71, 0x00020000\n" \
"v_mfma_f32_16x16x32_bf16 v[140:143], a[36:39], a[12:15], v[140:143]\n" \
"s_add_u32 s63, s63, 79691776\n" \
"s_add_u32 s72, s4, s63\ns_addc_u32 s73, s5, 0\n" \
"v_mfma_f32_16x16x32_bf16 v[144:147], a[40:43], a[8:11], v[144:147]\n" \
"s_add_u32 m0, s50, 69632\n" \
"ds_read_b64_tr_b16 a[108:109], v11 offset:16896\n" \
"s_and_b32 s73, s73, 0xffff\n" \
"v_mfma_f32_16x16x32_bf16 v[148:151], a[44:47], a[8:11], v[148:151]\n" \
"buffer_load_dwordx4 v14, s[28:31], s46 offen lds\n" \
"v_mfma_f32_16x16x32_bf16 v[152:155], a[40:43], a[12:15], v[152:155]\n" \
"s_mov_b32 s74, 4096\n" \
"ds_read_b64_tr_b16 a[110:111], v12 offset:16896\n" \
"s_mov_b32 s75, 0x00020000\n" \
"v_mfma_f32_16x16x32_bf16 v[156:159], a[44:47], a[12:15], v[156:159]\n" \
"v_add_u32 v23, -32, v4\n" \
"v_add_u32 v24, 992, v4\n" \
"v_mfma_f32_16x16x32_bf16 v[160:163], a[48:51], a[8:11], v[160:163]\n" \
"s_add_u32 m0, s50, 73728\n" \
"ds_read_b128 a[80:83], v6 offset:8192\n" \
"v_add_u32 v25, -16, v4\n" \
"v_mfma_f32_16x16x32_bf16 v[164:167], a[52:55], a[8:11], v[164:167]\n" \
"buffer_load_dwordx4 v15, s[28:31], s46 offen lds\n" \
"v_mfma_f32_16x16x32_bf16 v[168:171], a[48:51], a[12:15], v[168:171]\n" \
"ds_read_b64_tr_b16 a[112:113], v9 offset:17408\n" \
"v_add_u32 v26, -512, v4\n" \
"v_mfma_f32_16x16x32_bf16 v[172:175], a[52:55], a[12:15], v[172:175]\n" \
"s_lshr_b32 s63, %2, 1\n" \
"v_add_u32 v27, 512, v4\n" \
"s_lshl_b32 s63, s63, 16\n" \
"v_mfma_f32_16x16x32_bf16 v[176:179], a[56:59], a[8:11], v[176:179]\n" \
"s_add_u32 m0, s50, 77824\n" \
"ds_read_b64_tr_b16 a[114:115], v10 offset:17408\n" \
"s_lshl_b32 s56, %2, 14\n" \
"v_mfma_f32_16x16x32_bf16 v[180:183], a[60:63], a[8:11], v[180:183]\n" \
"buffer_load_dwordx4 v16, s[28:31], s46 offen lds\n" \
"v_mfma_f32_16x16x32_bf16 v[184:187], a[56:59], a[12:15], v[184:187]\n" \
"s_add_u32 s56, s56, s63\n" \
"ds_read_b128 a[84:87], v6 offset:10240\n" \
"v_add_u32 v55, s56, v4\n" \
"v_mfma_f32_16x16x32_bf16 v[188:191], a[60:63], a[12:15], v[188:191]\n" \
"s_lshl_b32 s62, s62, 1\n" \
"v_mov_b32 v28, 0\n" \
"v_mov_b32 v29, s62\n" \
"v_mfma_f32_16x16x32_bf16 v[192:195], a[32:35], a[16:19], v[192:195]\n" \
"s_mov_b32 s46, 768\n" \
"s_add_u32 m0, s50, 81920\n" \
"ds_read_b64_tr_b16 a[116:117], v11 offset:17408\n" \
"v_mfma_f32_16x16x32_bf16 v[196:199], a[36:39], a[16:19], v[196:199]\n" \
"buffer_load_dwordx4 v17, s[28:31], s46 offen lds\n" \
"v_mfma_f32_16x16x32_bf16 v[200:203], a[32:35], a[20:23], v[200:203]\n" \
"s_and_b32 s62, %1, 7\n" \
"ds_read_b64_tr_b16 a[118:119], v12 offset:17408\n" \
"v_lshlrev_b32 v56, 23, v4\n" \
"v_mfma_f32_16x16x32_bf16 v[204:207], a[36:39], a[20:23], v[204:207]\n" \
"s_lshl_b32 s62, s62, 8\n" \
"s_add_u32 s62, s62, 94380032\n" \
"v_bfe_u32 v57, v4, 4, 5\n" \
"v_mfma_f32_16x16x32_bf16 v[208:211], a[40:43], a[16:19], v[208:211]\n" \
"s_add_u32 m0, s50, 86016\n" \
"ds_read_b128 a[88:91], v6 offset:12288\n" \
"v_lshl_add_u32 v57, v57, 3, s62\n" \
"v_mfma_f32_16x16x32_bf16 v[212:215], a[44:47], a[16:19], v[212:215]\n" \
"buffer_load_dwordx4 v18, s[28:31], s46 offen lds\n" \
"v_mfma_f32_16x16x32_bf16 v[216:219], a[40:43], a[20:23], v[216:219]\n" \
"ds_read_b64_tr_b16 a[120:121], v9 offset:17920\n" \
"v_mfma_f32_16x16x32_bf16 v[220:223], a[44:47], a[20:23], v[220:223]\n" \
"v_mfma_f32_16x16x32_bf16 v[224:227], a[48:51], a[16:19], v[224:227]\n" \
"s_add_u32 m0, s50, 90112\n" \
"ds_read_b64_tr_b16 a[122:123], v10 offset:17920\n" \
"v_mfma_f32_16x16x32_bf16 v[228:231], a[52:55], a[16:19], v[228:231]\n" \
"buffer_load_dwordx4 v19, s[28:31], s46 offen lds\n" \
"v_mfma_f32_16x16x32_bf16 v[232:235], a[48:51], a[20:23], v[232:235]\n" \
"ds_read_b128 a[92:95], v6 offset:14336\n" \
"v_mfma_f32_16x16x32_bf16 v[236:239], a[52:55], a[20:23], v[236:239]\n" \
"v_mfma_f32_16x16x32_bf16 v[240:243], a[56:59], a[16:19], v[240:243]\n" \
"s_add_u32 m0, s50, 94208\n" \
"ds_read_b64_tr_b16 a[124:125], v11 offset:17920\n" \
"v_mfma_f32_16x16x32_bf16 v[244:247], a[60:63], a[16:19], v[244:247]\n" \
"buffer_load_dwordx4 v20, s[28:31], s46 offen lds\n" \
"v_mfma_f32_16x16x32_bf16 v[248:251], a[56:59], a[20:23], v[248:251]\n" \
"ds_read_b64_tr_b16 a[126:127], v12 offset:17920\n" \
"v_mfma_f32_16x16x32_bf16 v[252:255], a[60:63], a[20:23], v[252:255]\n" \
"v_mfma_f32_16x16x32_bf16 a[128:131], a[32:35], a[24:27], a[128:131]\n" \
"s_mov_b32 s47, 3145728\n" \
"s_add_u32 m0, s22, 0\n" \
"v_mfma_f32_16x16x32_bf16 a[132:135], a[36:39], a[24:27], a[132:135]\n" \
"buffer_load_dwordx4 v21, s[32:35], s47 offen offset:0 lds\n" \
"v_mfma_f32_16x16x32_bf16 a[136:139], a[32:35], a[28:31], a[136:139]\n" \
"v_mfma_f32_16x16x32_bf16 a[140:143], a[36:39], a[28:31], a[140:143]\n" \
"v_mfma_f32_16x16x32_bf16 a[144:147], a[40:43], a[24:27], a[144:147]\n" \
"s_add_u32 m0, s22, 896\n" \
"v_mfma_f32_16x16x32_bf16 a[148:151], a[44:47], a[24:27], a[148:151]\n" \
"buffer_load_dwordx4 v21, s[32:35], s47 offen offset:128 lds\n" \
"v_mfma_f32_16x16x32_bf16 a[152:155], a[40:43], a[28:31], a[152:155]\n" \
"v_mfma_f32_16x16x32_bf16 a[156:159], a[44:47], a[28:31], a[156:159]\n" \
"s_waitcnt vmcnt(18) lgkmcnt(0)\n" \
"v_mfma_f32_16x16x32_bf16 a[160:163], a[48:51], a[24:27], a[160:163]\n" \
"s_add_u32 m0, s22, 1792\n" \
"s_barrier\n" \
"v_mfma_f32_16x16x32_bf16 a[164:167], a[52:55], a[24:27], a[164:167]\n" \
"buffer_load_dwordx4 v21, s[32:35], s47 offen offset:256 lds\n" \
"v_mfma_f32_16x16x32_bf16 a[168:171], a[48:51], a[28:31], a[168:171]\n" \
"s_add_u32 s90, s64, s53\ns_addc_u32 s91, s65, 0\n" \
"v_mfma_f32_16x16x32_bf16 a[172:175], a[52:55], a[28:31], a[172:175]\n" \
"v_mfma_f32_16x16x32_bf16 a[176:179], a[56:59], a[24:27], a[176:179]\n" \
"s_add_u32 m0, s22, 2688\n" \
"v_mfma_f32_16x16x32_bf16 a[180:183], a[60:63], a[24:27], a[180:183]\n" \
"buffer_load_dwordx4 v21, s[32:35], s47 offen offset:384 lds\n" \
"v_mfma_f32_16x16x32_bf16 a[184:187], a[56:59], a[28:31], a[184:187]\n" \
"v_mfma_f32_16x16x32_bf16 a[188:191], a[60:63], a[28:31], a[188:191]\n" \
"v_mfma_f32_16x16x32_bf16 v[64:67], a[96:99], a[64:67], v[64:67]\n" \
"ds_read_b128 a[0:3], v5 offset:32768\n" \
"v_mfma_f32_16x16x32_bf16 v[68:71], a[100:103], a[64:67], v[68:71]\n" \
"global_load_dwordx4 a[240:243], v4, s[90:91] offset:0\n" \
"v_mfma_f32_16x16x32_bf16 v[72:75], a[96:99], a[68:71], v[72:75]\n" \
"ds_read_b64_tr_b16 a[32:33], v9 offset:32768\n" \
"v_mfma_f32_16x16x32_bf16 v[76:79], a[100:103], a[68:71], v[76:79]\n" \
"v_mfma_f32_16x16x32_bf16 v[80:83], a[104:107], a[64:67], v[80:83]\n" \
"ds_read_b64_tr_b16 a[34:35], v10 offset:32768\n" \
"v_mfma_f32_16x16x32_bf16 v[84:87], a[108:111], a[64:67], v[84:87]\n" \
"global_load_dwordx4 a[244:247], v4, s[90:91] offset:1024\n" \
"v_mfma_f32_16x16x32_bf16 v[88:91], a[104:107], a[68:71], v[88:91]\n" \
"ds_read_b128 a[4:7], v5 offset:34816\n" \
"v_mfma_f32_16x16x32_bf16 v[92:95], a[108:111], a[68:71], v[92:95]\n" \
"v_mfma_f32_16x16x32_bf16 v[96:99], a[112:115], a[64:67], v[96:99]\n" \
"ds_read_b64_tr_b16 a[36:37], v11 offset:32768\n" \
"v_mfma_f32_16x16x32_bf16 v[100:103], a[116:119], a[64:67], v[100:103]\n" \
"global_load_dwordx4 a[248:251], v4, s[90:91] offset:2048\n" \
"v_mfma_f32_16x16x32_bf16 v[104:107], a[112:115], a[68:71], v[104:107]\n" \
"ds_read_b64_tr_b16 a[38:39], v12 offset:32768\n" \
"v_mfma_f32_16x16x32_bf16 v[108:111], a[116:119], a[68:71], v[108:111]\n" \
"v_mfma_f32_16x16x32_bf16 v[112:115], a[120:123], a[64:67], v[112:115]\n" \
"ds_read_b128 a[8:11], v5 offset:36864\n" \
"v_mfma_f32_16x16x32_bf16 v[116:119], a[124:127], a[64:67], v[116:119]\n" \
"global_load_dwordx4 a[252:255], v4, s[90:91] offset:3072\n" \
"v_mfma_f32_16x16x32_bf16 v[120:123], a[120:123], a[68:71], v[120:123]\n" \
"ds_read_b64_tr_b16 a[40:41], v9 offset:33280\n" \
"v_mfma_f32_16x16x32_bf16 v[124:127], a[124:127], a[68:71], v[124:127]\n" \
"v_mfma_f32_16x16x32_bf16 v[128:131], a[96:99], a[72:75], v[128:131]\n" \
"ds_read_b64_tr_b16 a[42:43], v10 offset:33280\n" \
"v_mfma_f32_16x16x32_bf16 v[132:135], a[100:103], a[72:75], v[132:135]\n" \
"v_mfma_f32_16x16x32_bf16 v[136:139], a[96:99], a[76:79], v[136:139]\n" \
"ds_read_b128 a[12:15], v5 offset:38912\n" \
"v_mfma_f32_16x16x32_bf16 v[140:143], a[100:103], a[76:79], v[140:143]\n" \
"v_mfma_f32_16x16x32_bf16 v[144:147], a[104:107], a[72:75], v[144:147]\n" \
"ds_read_b64_tr_b16 a[44:45], v11 offset:33280\n" \
"v_mfma_f32_16x16x32_bf16 v[148:151], a[108:111], a[72:75], v[148:151]\n" \
"v_mfma_f32_16x16x32_bf16 v[152:155], a[104:107], a[76:79], v[152:155]\n" \
"ds_read_b64_tr_b16 a[46:47], v12 offset:33280\n" \
"v_mfma_f32_16x16x32_bf16 v[156:159], a[108:111], a[76:79], v[156:159]\n" \
"v_mfma_f32_16x16x32_bf16 v[160:163], a[112:115], a[72:75], v[160:163]\n" \
"ds_read_b128 a[16:19], v5 offset:40960\n" \
"v_mfma_f32_16x16x32_bf16 v[164:167], a[116:119], a[72:75], v[164:167]\n" \
"v_mfma_f32_16x16x32_bf16 v[168:171], a[112:115], a[76:79], v[168:171]\n" \
"ds_read_b64_tr_b16 a[48:49], v9 offset:33792\n" \
"v_mfma_f32_16x16x32_bf16 v[172:175], a[116:119], a[76:79], v[172:175]\n" \
"v_mfma_f32_16x16x32_bf16 v[176:179], a[120:123], a[72:75], v[176:179]\n" \
"ds_read_b64_tr_b16 a[50:51], v10 offset:33792\n" \
"v_mfma_f32_16x16x32_bf16 v[180:183], a[124:127], a[72:75], v[180:183]\n" \
"v_mfma_f32_16x16x32_bf16 v[184:187], a[120:123], a[76:79], v[184:187]\n" \
"ds_read_b128 a[20:23], v5 offset:43008\n" \
"v_mfma_f32_16x16x32_bf16 v[188:191], a[124:127], a[76:79], v[188:191]\n" \
"v_mfma_f32_16x16x32_bf16 v[192:195], a[96:99], a[80:83], v[192:195]\n" \
"ds_read_b64_tr_b16 a[52:53], v11 offset:33792\n" \
"v_mfma_f32_16x16x32_bf16 v[196:199], a[100:103], a[80:83], v[196:199]\n" \
"v_mfma_f32_16x16x32_bf16 v[200:203], a[96:99], a[84:87], v[200:203]\n" \
"ds_read_b64_tr_b16 a[54:55], v12 offset:33792\n" \
"v_mfma_f32_16x16x32_bf16 v[204:207], a[100:103], a[84:87], v[204:207]\n" \
"v_mfma_f32_16x16x32_bf16 v[208:211], a[104:107], a[80:83], v[208:211]\n" \
"ds_read_b128 a[24:27], v5 offset:45056\n" \
"v_mfma_f32_16x16x32_bf16 v[212:215], a[108:111], a[80:83], v[212:215]\n" \
"v_mfma_f32_16x16x32_bf16 v[216:219], a[104:107], a[84:87], v[216:219]\n" \
"ds_read_b64_tr_b16 a[56:57], v9 offset:34304\n" \
"v_mfma_f32_16x16x32_bf16 v[220:223], a[108:111], a[84:87], v[220:223]\n" \
"global_load_dwordx4 a[192:195], v4, s[86:87] offset:0\n" \
"v_mfma_f32_16x16x32_bf16 v[224:227], a[112:115], a[80:83], v[224:227]\n" \
"ds_read_b64_tr_b16 a[58:59], v10 offset:34304\n" \
"v_mfma_f32_16x16x32_bf16 v[228:231], a[116:119], a[80:83], v[228:231]\n" \
"v_mfma_f32_16x16x32_bf16 v[232:235], a[112:115], a[84:87], v[232:235]\n" \
"ds_read_b128 a[28:31], v5 offset:47104\n" \
"v_mfma_f32_16x16x32_bf16 v[236:239], a[116:119], a[84:87], v[236:239]\n" \
"global_load_dwordx4 a[196:199], v4, s[86:87] offset:1024\n" \
"v_mfma_f32_16x16x32_bf16 v[240:243], a[120:123], a[80:83], v[240:243]\n" \
"ds_read_b64_tr_b16 a[60:61], v11 offset:34304\n" \
"v_mfma_f32_16x16x32_bf16 v[244:247], a[124:127], a[80:83], v[244:247]\n" \
"v_mfma_f32_16x16x32_bf16 v[248:251], a[120:123], a[84:87], v[248:251]\n" \
"ds_read_b64_tr_b16 a[62:63], v12 offset:34304\n" \
"v_mfma_f32_16x16x32_bf16 v[252:255], a[124:127], a[84:87], v[252:255]\n" \
"v_mfma_f32_16x16x32_bf16 a[128:131], a[96:99], a[88:91], a[128:131]\n" \
"v_mfma_f32_16x16x32_bf16 a[132:135], a[100:103], a[88:91], a[132:135]\n" \
"v_mfma_f32_16x16x32_bf16 a[136:139], a[96:99], a[92:95], a[136:139]\n" \
"v_mfma_f32_16x16x32_bf16 a[140:143], a[100:103], a[92:95], a[140:143]\n" \
"v_mfma_f32_16x16x32_bf16 a[144:147], a[104:107], a[88:91], a[144:147]\n" \
"v_mov_b32 v58, s14\n" \
"v_mfma_f32_16x16x32_bf16 a[148:151], a[108:111], a[88:91], a[148:151]\n" \
"v_mov_b32 v59, s15\n" \
"v_mfma_f32_16x16x32_bf16 a[152:155], a[104:107], a[92:95], a[152:155]\n" \
"v_mfma_f32_16x16x32_bf16 a[156:159], a[108:111], a[92:95], a[156:159]\n" \
"s_waitcnt vmcnt(18) lgkmcnt(0)\n" \
"v_mfma_f32_16x16x32_bf16 a[160:163], a[112:115], a[88:91], a[160:163]\n" \
"s_barrier\n" \
"v_mfma_f32_16x16x32_bf16 a[164:167], a[116:119], a[88:91], a[164:167]\n" \
"v_mfma_f32_16x16x32_bf16 a[168:171], a[112:115], a[92:95], a[168:171]\n" \
"v_mfma_f32_16x16x32_bf16 a[172:175], a[116:119], a[92:95], a[172:175]\n" \
"v_mfma_f32_16x16x32_bf16 a[176:179], a[120:123], a[88:91], a[176:179]\n" \
"v_mfma_f32_16x16x32_bf16 a[180:183], a[124:127], a[88:91], a[180:183]\n" \
"v_mfma_f32_16x16x32_bf16 a[184:187], a[120:123], a[92:95], a[184:187]\n" \
"v_mfma_f32_16x16x32_bf16 a[188:191], a[124:127], a[92:95], a[188:191]\n" \
"buffer_store_dwordx2 v[58:59], v56, s[36:39], s23 offen sc1\n" \
"v_mfma_f32_16x16x32_bf16 v[64:67], a[32:35], a[0:3], v[64:67]\n" \
"s_mov_b32 s47, 3407872\n" \
"s_add_u32 m0, s22, 16384\n" \
"ds_read_b128 a[64:67], v6 offset:32768\n" \
"v_mfma_f32_16x16x32_bf16 v[68:71], a[36:39], a[0:3], v[68:71]\n" \
"buffer_load_dwordx4 v21, s[32:35], s47 offen offset:0 lds\n" \
"v_mfma_f32_16x16x32_bf16 v[72:75], a[32:35], a[4:7], v[72:75]\n" \
"ds_read_b64_tr_b16 a[96:97], v9 offset:49152\n" \
"v_mfma_f32_16x16x32_bf16 v[76:79], a[36:39], a[4:7], v[76:79]\n" \
"v_mfma_f32_16x16x32_bf16 v[80:83], a[40:43], a[0:3], v[80:83]\n" \
"s_add_u32 m0, s22, 17280\n" \
"ds_read_b64_tr_b16 a[98:99], v10 offset:49152\n" \
"v_mfma_f32_16x16x32_bf16 v[84:87], a[44:47], a[0:3], v[84:87]\n" \
"buffer_load_dwordx4 v21, s[32:35], s47 offen offset:128 lds\n" \
"v_mfma_f32_16x16x32_bf16 v[88:91], a[40:43], a[4:7], v[88:91]\n" \
"ds_read_b128 a[68:71], v6 offset:34816\n" \
"v_mfma_f32_16x16x32_bf16 v[92:95], a[44:47], a[4:7], v[92:95]\n" \
"v_mfma_f32_16x16x32_bf16 v[96:99], a[48:51], a[0:3], v[96:99]\n" \
"s_add_u32 m0, s22, 18176\n" \
"ds_read_b64_tr_b16 a[100:101], v11 offset:49152\n" \
"v_mfma_f32_16x16x32_bf16 v[100:103], a[52:55], a[0:3], v[100:103]\n" \
"buffer_load_dwordx4 v21, s[32:35], s47 offen offset:256 lds\n" \
"v_mfma_f32_16x16x32_bf16 v[104:107], a[48:51], a[4:7], v[104:107]\n" \
"ds_read_b64_tr_b16 a[102:103], v12 offset:49152\n" \
"v_mfma_f32_16x16x32_bf16 v[108:111], a[52:55], a[4:7], v[108:111]\n" \
"v_mfma_f32_16x16x32_bf16 v[112:115], a[56:59], a[0:3], v[112:115]\n" \
"s_add_u32 m0, s22, 19072\n" \
"ds_read_b128 a[72:75], v6 offset:36864\n" \
"v_mfma_f32_16x16x32_bf16 v[116:119], a[60:63], a[0:3], v[116:119]\n" \
"buffer_load_dwordx4 v21, s[32:35], s47 offen offset:384 lds\n" \
"v_mfma_f32_16x16x32_bf16 v[120:123], a[56:59], a[4:7], v[120:123]\n" \
"ds_read_b64_tr_b16 a[104:105], v9 offset:49664\n" \
"v_mfma_f32_16x16x32_bf16 v[124:127], a[60:63], a[4:7], v[124:127]\n" \
"v_mfma_f32_16x16x32_bf16 v[128:131], a[32:35], a[8:11], v[128:131]\n" \
"s_mov_b32 s46, 896\n" \
"s_add_u32 m0, s50, 0\n" \
"ds_read_b64_tr_b16 a[106:107], v10 offset:49664\n" \
"v_mfma_f32_16x16x32_bf16 v[132:135], a[36:39], a[8:11], v[132:135]\n" \
"buffer_load_dwordx4 v13, s[28:31], s46 offen lds\n" \
"v_mfma_f32_16x16x32_bf16 v[136:139], a[32:35], a[12:15], v[136:139]\n" \
"ds_read_b128 a[76:79], v6 offset:38912\n" \
"v_mfma_f32_16x16x32_bf16 v[140:143], a[36:39], a[12:15], v[140:143]\n" \
"v_mfma_f32_16x16x32_bf16 v[144:147], a[40:43], a[8:11], v[144:147]\n" \
"s_add_u32 m0, s50, 4096\n" \
"ds_read_b64_tr_b16 a[108:109], v11 offset:49664\n" \
"v_mfma_f32_16x16x32_bf16 v[148:151], a[44:47], a[8:11], v[148:151]\n" \
"buffer_load_dwordx4 v14, s[28:31], s46 offen lds\n" \
"v_mfma_f32_16x16x32_bf16 v[152:155], a[40:43], a[12:15], v[152:155]\n" \
"ds_read_b64_tr_b16 a[110:111], v12 offset:49664\n" \
"v_mfma_f32_16x16x32_bf16 v[156:159], a[44:47], a[12:15], v[156:159]\n" \
"v_mfma_f32_16x16x32_bf16 v[160:163], a[48:51], a[8:11], v[160:163]\n" \
"s_add_u32 m0, s50, 8192\n" \
"ds_read_b128 a[80:83], v6 offset:40960\n" \
"v_mfma_f32_16x16x32_bf16 v[164:167], a[52:55], a[8:11], v[164:167]\n" \
"buffer_load_dwordx4 v15, s[28:31], s46 offen lds\n" \
"v_mfma_f32_16x16x32_bf16 v[168:171], a[48:51], a[12:15], v[168:171]\n" \
"ds_read_b64_tr_b16 a[112:113], v9 offset:50176\n" \
"v_mfma_f32_16x16x32_bf16 v[172:175], a[52:55], a[12:15], v[172:175]\n" \
"v_mfma_f32_16x16x32_bf16 v[176:179], a[56:59], a[8:11], v[176:179]\n" \
"s_add_u32 m0, s50, 12288\n" \
"ds_read_b64_tr_b16 a[114:115], v10 offset:50176\n" \
"v_mfma_f32_16x16x32_bf16 v[180:183], a[60:63], a[8:11], v[180:183]\n" \
"buffer_load_dwordx4 v16, s[28:31], s46 offen lds\n" \
"v_mfma_f32_16x16x32_bf16 v[184:187], a[56:59], a[12:15], v[184:187]\n" \
"ds_read_b128 a[84:87], v6 offset:43008\n" \
"v_mfma_f32_16x16x32_bf16 v[188:191], a[60:63], a[12:15], v[188:191]\n" \
"v_mfma_f32_16x16x32_bf16 v[192:195], a[32:35], a[16:19], v[192:195]\n" \
"s_mov_b32 s46, 896\n" \
"s_add_u32 m0, s50, 16384\n" \
"ds_read_b64_tr_b16 a[116:117], v11 offset:50176\n" \
"v_mfma_f32_16x16x32_bf16 v[196:199], a[36:39], a[16:19], v[196:199]\n" \
"buffer_load_dwordx4 v17, s[28:31], s46 offen lds\n" \
"v_mfma_f32_16x16x32_bf16 v[200:203], a[32:35], a[20:23], v[200:203]\n" \
"ds_read_b64_tr_b16 a[118:119], v12 offset:50176\n" \
"v_mfma_f32_16x16x32_bf16 v[204:207], a[36:39], a[20:23], v[204:207]\n" \
"v_mfma_f32_16x16x32_bf16 v[208:211], a[40:43], a[16:19], v[208:211]\n" \
"s_add_u32 m0, s50, 20480\n" \
"ds_read_b128 a[88:91], v6 offset:45056\n" \
"v_mfma_f32_16x16x32_bf16 v[212:215], a[44:47], a[16:19], v[212:215]\n" \
"buffer_load_dwordx4 v18, s[28:31], s46 offen lds\n" \
"v_mfma_f32_16x16x32_bf16 v[216:219], a[40:43], a[20:23], v[216:219]\n" \
"ds_read_b64_tr_b16 a[120:121], v9 offset:50688\n" \
"v_mfma_f32_16x16x32_bf16 v[220:223], a[44:47], a[20:23], v[220:223]\n" \
"v_mfma_f32_16x16x32_bf16 v[224:227], a[48:51], a[16:19], v[224:227]\n" \
"s_add_u32 m0, s50, 24576\n" \
"ds_read_b64_tr_b16 a[122:123], v10 offset:50688\n" \
"v_mfma_f32_16x16x32_bf16 v[228:231], a[52:55], a[16:19], v[228:231]\n" \
"buffer_load_dwordx4 v19, s[28:31], s46 offen lds\n" \
"v_mfma_f32_16x16x32_bf16 v[232:235], a[48:51], a[20:23], v[232:235]\n" \
"ds_read_b128 a[92:95], v6 offset:47104\n" \
"v_mfma_f32_16x16x32_bf16 v[236:239], a[52:55], a[20:23], v[236:239]\n" \
"v_mfma_f32_16x16x32_bf16 v[240:243], a[56:59], a[16:19], v[240:243]\n" \
"s_add_u32 m0, s50, 28672\n" \
"ds_read_b64_tr_b16 a[124:125], v11 offset:50688\n" \
"v_mfma_f32_16x16x32_bf16 v[244:247], a[60:63], a[16:19], v[244:247]\n" \
"buffer_load_dwordx4 v20, s[28:31], s46 offen lds\n" \
"v_mfma_f32_16x16x32_bf16 v[248:251], a[56:59], a[20:23], v[248:251]\n" \
"ds_read_b64_tr_b16 a[126:127], v12 offset:50688\n" \
"v_mfma_f32_16x16x32_bf16 v[252:255], a[60:63], a[20:23], v[252:255]\n" \
"v_mfma_f32_16x16x32_bf16 a[128:131], a[32:35], a[24:27], a[128:131]\n" \
"s_mov_b32 s47, 3670016\n" \
"s_add_u32 m0, s22, 32768\n" \
"v_mfma_f32_16x16x32_bf16 a[132:135], a[36:39], a[24:27], a[132:135]\n" \
"buffer_load_dwordx4 v21, s[32:35], s47 offen offset:0 lds\n" \
"v_mfma_f32_16x16x32_bf16 a[136:139], a[32:35], a[28:31], a[136:139]\n" \
"v_mfma_f32_16x16x32_bf16 a[140:143], a[36:39], a[28:31], a[140:143]\n" \
"v_mfma_f32_16x16x32_bf16 a[144:147], a[40:43], a[24:27], a[144:147]\n" \
"s_add_u32 m0, s22, 33664\n" \
"v_mfma_f32_16x16x32_bf16 a[148:151], a[44:47], a[24:27], a[148:151]\n" \
"buffer_load_dwordx4 v21, s[32:35], s47 offen offset:128 lds\n" \
"v_mfma_f32_16x16x32_bf16 a[152:155], a[40:43], a[28:31], a[152:155]\n" \
"v_mfma_f32_16x16x32_bf16 a[156:159], a[44:47], a[28:31], a[156:159]\n" \
"s_waitcnt vmcnt(21) lgkmcnt(0)\n" \
"v_mfma_f32_16x16x32_bf16 a[160:163], a[48:51], a[24:27], a[160:163]\n" \
"s_add_u32 m0, s22, 34560\n" \
"s_barrier\n" \
"v_mfma_f32_16x16x32_bf16 a[164:167], a[52:55], a[24:27], a[164:167]\n" \
"buffer_load_dwordx4 v21, s[32:35], s47 offen offset:256 lds\n" \
"v_mfma_f32_16x16x32_bf16 a[168:171], a[48:51], a[28:31], a[168:171]\n" \
"v_mfma_f32_16x16x32_bf16 a[172:175], a[52:55], a[28:31], a[172:175]\n" \
"v_mfma_f32_16x16x32_bf16 a[176:179], a[56:59], a[24:27], a[176:179]\n" \
"s_add_u32 m0, s22, 35456\n" \
"v_mfma_f32_16x16x32_bf16 a[180:183], a[60:63], a[24:27], a[180:183]\n" \
"buffer_load_dwordx4 v21, s[32:35], s47 offen offset:384 lds\n" \
"v_mfma_f32_16x16x32_bf16 a[184:187], a[56:59], a[28:31], a[184:187]\n" \
"v_mfma_f32_16x16x32_bf16 a[188:191], a[60:63], a[28:31], a[188:191]\n" \
"v_mfma_f32_16x16x32_bf16 v[64:67], a[96:99], a[64:67], v[64:67]\n" \
"ds_read_b128 a[0:3], v7 offset:0\n" \
"v_mfma_f32_16x16x32_bf16 v[68:71], a[100:103], a[64:67], v[68:71]\n" \
"buffer_load_dwordx4 v[30:33], v23, s[68:71], 0 offen offset:0 nt\n" \
"v_mfma_f32_16x16x32_bf16 v[72:75], a[96:99], a[68:71], v[72:75]\n" \
"s_mov_b64 s[28:29], s[64:65]\n" \
"ds_read_b64_tr_b16 a[32:33], v9 offset:0\n" \
"v_mfma_f32_16x16x32_bf16 v[76:79], a[100:103], a[68:71], v[76:79]\n" \
"v_mfma_f32_16x16x32_bf16 v[80:83], a[104:107], a[64:67], v[80:83]\n" \
"ds_read_b64_tr_b16 a[34:35], v10 offset:0\n" \
"v_mfma_f32_16x16x32_bf16 v[84:87], a[108:111], a[64:67], v[84:87]\n" \
"buffer_load_dwordx4 v[34:37], v24, s[68:71], 0 offen offset:0 nt\n" \
"v_mfma_f32_16x16x32_bf16 v[88:91], a[104:107], a[68:71], v[88:91]\n" \
"s_and_b32 s29, s29, 0xffff\n" \
"ds_read_b128 a[4:7], v7 offset:2048\n" \
"v_mfma_f32_16x16x32_bf16 v[92:95], a[108:111], a[68:71], v[92:95]\n" \
"v_mfma_f32_16x16x32_bf16 v[96:99], a[112:115], a[64:67], v[96:99]\n" \
"ds_read_b64_tr_b16 a[36:37], v11 offset:0\n" \
"v_mfma_f32_16x16x32_bf16 v[100:103], a[116:119], a[64:67], v[100:103]\n" \
"buffer_load_dwordx4 v[38:41], v24, s[68:71], 0 offen offset:1024 nt\n" \
"v_mfma_f32_16x16x32_bf16 v[104:107], a[112:115], a[68:71], v[104:107]\n" \
"s_mov_b32 s30, 4194304\n" \
"ds_read_b64_tr_b16 a[38:39], v12 offset:0\n" \
"v_mfma_f32_16x16x32_bf16 v[108:111], a[116:119], a[68:71], v[108:111]\n" \
"v_mfma_f32_16x16x32_bf16 v[112:115], a[120:123], a[64:67], v[112:115]\n" \
"ds_read_b128 a[8:11], v7 offset:4096\n" \
"v_mfma_f32_16x16x32_bf16 v[116:119], a[124:127], a[64:67], v[116:119]\n" \
"buffer_load_dwordx4 v[42:45], v24, s[68:71], 0 offen offset:2048 nt\n" \
"v_mfma_f32_16x16x32_bf16 v[120:123], a[120:123], a[68:71], v[120:123]\n" \
"s_lshl_b32 s60, s7, 18\n" \
"ds_read_b64_tr_b16 a[40:41], v9 offset:512\n" \
"v_mfma_f32_16x16x32_bf16 v[124:127], a[124:127], a[68:71], v[124:127]\n" \
"v_mfma_f32_16x16x32_bf16 v[128:131], a[96:99], a[72:75], v[128:131]\n" \
"ds_read_b64_tr_b16 a[42:43], v10 offset:512\n" \
"v_mfma_f32_16x16x32_bf16 v[132:135], a[100:103], a[72:75], v[132:135]\n" \
"buffer_load_dwordx4 v[46:49], v24, s[68:71], 0 offen offset:3072 nt\n" \
"v_mfma_f32_16x16x32_bf16 v[136:139], a[96:99], a[76:79], v[136:139]\n" \
"s_sub_u32 s61, 3932160, s60\n" \
"ds_read_b128 a[12:15], v7 offset:6144\n" \
"v_mfma_f32_16x16x32_bf16 v[140:143], a[100:103], a[76:79], v[140:143]\n" \
"v_mfma_f32_16x16x32_bf16 v[144:147], a[104:107], a[72:75], v[144:147]\n" \
"ds_read_b64_tr_b16 a[44:45], v11 offset:512\n" \
"v_mfma_f32_16x16x32_bf16 v[148:151], a[108:111], a[72:75], v[148:151]\n" \
"buffer_load_dword v50, v25, s[68:71], 0 offen offset:0\n" \
"v_mfma_f32_16x16x32_bf16 v[152:155], a[104:107], a[76:79], v[152:155]\n" \
"ds_read_b64_tr_b16 a[46:47], v12 offset:512\n" \
"v_add_u32 v13, s60, v63\n" \
"v_mfma_f32_16x16x32_bf16 v[156:159], a[108:111], a[76:79], v[156:159]\n" \
"v_mfma_f32_16x16x32_bf16 v[160:163], a[112:115], a[72:75], v[160:163]\n" \
"ds_read_b128 a[16:19], v7 offset:8192\n" \
"v_mfma_f32_16x16x32_bf16 v[164:167], a[116:119], a[72:75], v[164:167]\n" \
"LH2_%=:\ns_cbranch_execz LH10_%=\n" \
"buffer_load_dword v51, v24, s[68:71], 0 offen offset:16\n" \
"v_mfma_f32_16x16x32_bf16 v[168:171], a[112:115], a[76:79], v[168:171]\n" \
"ds_read_b64_tr_b16 a[48:49], v9 offset:1024\n" \
"v_add_u32 v17, s61, v63\n" \
"v_mfma_f32_16x16x32_bf16 v[172:175], a[116:119], a[76:79], v[172:175]\n" \
"v_mfma_f32_16x16x32_bf16 v[176:179], a[120:123], a[72:75], v[176:179]\n" \
"ds_read_b64_tr_b16 a[50:51], v10 offset:1024\n" \
"v_mfma_f32_16x16x32_bf16 v[180:183], a[124:127], a[72:75], v[180:183]\n" \
"buffer_load_dword v52, v24, s[68:71], 0 offen offset:1040\n" \
"v_mfma_f32_16x16x32_bf16 v[184:187], a[120:123], a[76:79], v[184:187]\n" \
"ds_read_b128 a[20:23], v7 offset:10240\n" \
"v_add_u32 v14, 0x10000, v13\n" \
"v_mfma_f32_16x16x32_bf16 v[188:191], a[124:127], a[76:79], v[188:191]\n" \
"v_mfma_f32_16x16x32_bf16 v[192:195], a[96:99], a[80:83], v[192:195]\n" \
"ds_read_b64_tr_b16 a[52:53], v11 offset:1024\n" \
"v_mfma_f32_16x16x32_bf16 v[196:199], a[100:103], a[80:83], v[196:199]\n" \
"buffer_load_dword v53, v24, s[68:71], 0 offen offset:2064\n" \
"v_mfma_f32_16x16x32_bf16 v[200:203], a[96:99], a[84:87], v[200:203]\n" \
"ds_read_b64_tr_b16 a[54:55], v12 offset:1024\n" \
"v_add_u32 v18, 0x10000, v17\n" \
"v_mfma_f32_16x16x32_bf16 v[204:207], a[100:103], a[84:87], v[204:207]\n" \
"v_mfma_f32_16x16x32_bf16 v[208:211], a[104:107], a[80:83], v[208:211]\n" \
"ds_read_b128 a[24:27], v7 offset:12288\n" \
"v_mfma_f32_16x16x32_bf16 v[212:215], a[108:111], a[80:83], v[212:215]\n" \
"buffer_load_dword v54, v24, s[68:71], 0 offen offset:3088\n" \
"v_mfma_f32_16x16x32_bf16 v[216:219], a[104:107], a[84:87], v[216:219]\n" \
"ds_read_b64_tr_b16 a[56:57], v9 offset:1536\n" \
"v_add_u32 v15, 0x10000, v14\n" \
"v_mfma_f32_16x16x32_bf16 v[220:223], a[108:111], a[84:87], v[220:223]\n" \
"v_mfma_f32_16x16x32_bf16 v[224:227], a[112:115], a[80:83], v[224:227]\n" \
"ds_read_b64_tr_b16 a[58:59], v10 offset:1536\n" \
"v_mfma_f32_16x16x32_bf16 v[228:231], a[116:119], a[80:83], v[228:231]\n" \
"global_load_short_d16_hi v28, v29, s[12:13]\n" \
"v_mfma_f32_16x16x32_bf16 v[232:235], a[112:115], a[84:87], v[232:235]\n" \
"ds_read_b128 a[28:31], v7 offset:14336\n" \
"v_add_u32 v19, 0x10000, v18\n" \
"v_mfma_f32_16x16x32_bf16 v[236:239], a[116:119], a[84:87], v[236:239]\n" \
"v_mfma_f32_16x16x32_bf16 v[240:243], a[120:123], a[80:83], v[240:243]\n" \
"ds_read_b64_tr_b16 a[60:61], v11 offset:1536\n" \
"v_mfma_f32_16x16x32_bf16 v[244:247], a[124:127], a[80:83], v[244:247]\n" \
"v_mfma_f32_16x16x32_bf16 v[248:251], a[120:123], a[84:87], v[248:251]\n" \
"ds_read_b64_tr_b16 a[62:63], v12 offset:1536\n" \
"v_add_u32 v16, 0x10000, v15\n" \
"v_mfma_f32_16x16x32_bf16 v[252:255], a[124:127], a[84:87], v[252:255]\n" \
"v_mfma_f32_16x16x32_bf16 a[128:131], a[96:99], a[88:91], a[128:131]\n" \
"v_mfma_f32_16x16x32_bf16 a[132:135], a[100:103], a[88:91], a[132:135]\n" \
"v_mfma_f32_16x16x32_bf16 a[136:139], a[96:99], a[92:95], a[136:139]\n" \
"v_add_u32 v20, 0x10000, v19\n" \
"v_mfma_f32_16x16x32_bf16 a[140:143], a[100:103], a[92:95], a[140:143]\n" \
"v_mfma_f32_16x16x32_bf16 a[144:147], a[104:107], a[88:91], a[144:147]\n" \
"v_mfma_f32_16x16x32_bf16 a[148:151], a[108:111], a[88:91], a[148:151]\n" \
"v_mfma_f32_16x16x32_bf16 a[152:155], a[104:107], a[92:95], a[152:155]\n" \
"v_mfma_f32_16x16x32_bf16 a[156:159], a[108:111], a[92:95], a[156:159]\n" \
"s_waitcnt vmcnt(23) lgkmcnt(0)\n" \
"v_mfma_f32_16x16x32_bf16 a[160:163], a[112:115], a[88:91], a[160:163]\n" \
"s_barrier\n" \
"v_mfma_f32_16x16x32_bf16 a[164:167], a[116:119], a[88:91], a[164:167]\n" \
"v_mfma_f32_16x16x32_bf16 a[168:171], a[112:115], a[92:95], a[168:171]\n" \
"v_mfma_f32_16x16x32_bf16 a[172:175], a[116:119], a[92:95], a[172:175]\n" \
"v_mfma_f32_16x16x32_bf16 a[176:179], a[120:123], a[88:91], a[176:179]\n" \
"v_mfma_f32_16x16x32_bf16 a[180:183], a[124:127], a[88:91], a[180:183]\n" \
"v_mfma_f32_16x16x32_bf16 a[184:187], a[120:123], a[92:95], a[184:187]\n" \
"v_mfma_f32_16x16x32_bf16 a[188:191], a[124:127], a[92:95], a[188:191]\n" \
"v_mfma_f32_16x16x32_bf16 v[64:67], a[32:35], a[0:3], v[64:67]\n" \
"s_mov_b32 s47, 3932160\n" \
"s_add_u32 m0, s22, 49152\n" \
"ds_read_b128 a[64:67], v8 offset:0\n" \
"v_mfma_f32_16x16x32_bf16 v[68:71], a[36:39], a[0:3], v[68:71]\n" \
"buffer_load_dwordx4 v21, s[32:35], s47 offen offset:0 lds\n" \
"v_mfma_f32_16x16x32_bf16 v[72:75], a[32:35], a[4:7], v[72:75]\n" \
"ds_read_b64_tr_b16 a[96:97], v9 offset:16384\n" \
"v_mfma_f32_16x16x32_bf16 v[76:79], a[36:39], a[4:7], v[76:79]\n" \
"v_mfma_f32_16x16x32_bf16 v[80:83], a[40:43], a[0:3], v[80:83]\n" \
"s_add_u32 m0, s22, 50048\n" \
"ds_read_b64_tr_b16 a[98:99], v10 offset:16384\n" \
"v_mfma_f32_16x16x32_bf16 v[84:87], a[44:47], a[0:3], v[84:87]\n" \
"buffer_load_dwordx4 v21, s[32:35], s47 offen offset:128 lds\n" \
"v_mfma_f32_16x16x32_bf16 v[88:91], a[40:43], a[4:7], v[88:91]\n" \
"ds_read_b128 a[68:71], v8 offset:2048\n" \
"v_mfma_f32_16x16x32_bf16 v[92:95], a[44:47], a[4:7], v[92:95]\n" \
"v_mfma_f32_16x16x32_bf16 v[96:99], a[48:51], a[0:3], v[96:99]\n" \
"s_add_u32 m0, s22, 50944\n" \
"ds_read_b64_tr_b16 a[100:101], v11 offset:16384\n" \
"v_mfma_f32_16x16x32_bf16 v[100:103], a[52:55], a[0:3], v[100:103]\n" \
"buffer_load_dwordx4 v21, s[32:35], s47 offen offset:256 lds\n" \
"v_mfma_f32_16x16x32_bf16 v[104:107], a[48:51], a[4:7], v[104:107]\n" \
"ds_read_b64_tr_b16 a[102:103], v12 offset:16384\n" \
"v_mfma_f32_16x16x32_bf16 v[108:111], a[52:55], a[4:7], v[108:111]\n" \
"v_mfma_f32_16x16x32_bf16 v[112:115], a[56:59], a[0:3], v[112:115]\n" \
"s_add_u32 m0, s22, 51840\n" \
"ds_read_b128 a[72:75], v8 offset:4096\n" \
"v_mfma_f32_16x16x32_bf16 v[116:119], a[60:63], a[0:3], v[116:119]\n" \
"buffer_load_dwordx4 v21, s[32:35], s47 offen offset:384 lds\n" \
"v_mfma_f32_16x16x32_bf16 v[120:123], a[56:59], a[4:7], v[120:123]\n" \
"ds_read_b64_tr_b16 a[104:105], v9 offset:16896\n" \
"v_mfma_f32_16x16x32_bf16 v[124:127], a[60:63], a[4:7], v[124:127]\n" \
"v_mfma_f32_16x16x32_bf16 v[128:131], a[32:35], a[8:11], v[128:131]\n" \
"ds_read_b64_tr_b16 a[106:107], v10 offset:16896\n" \
"v_mfma_f32_16x16x32_bf16 v[132:135], a[36:39], a[8:11], v[132:135]\n" \
"buffer_load_dwordx2 v[58:59], v57, s[36:39], 0 offen sc1\n" \
"v_mfma_f32_16x16x32_bf16 v[136:139], a[32:35], a[12:15], v[136:139]\n" \
"s_add_u32 s76, s76, 2097152\ns_addc_u32 s77, s77, 0\n" \
"ds_read_b128 a[76:79], v8 offset:6144\n" \
"v_mfma_f32_16x16x32_bf16 v[140:143], a[36:39], a[12:15], v[140:143]\n" \
"v_mfma_f32_16x16x32_bf16 v[144:147], a[40:43], a[8:11], v[144:147]\n" \
"ds_read_b64_tr_b16 a[108:109], v11 offset:16896\n" \
"v_mfma_f32_16x16x32_bf16 v[148:151], a[44:47], a[8:11], v[148:151]\n" \
"v_mfma_f32_16x16x32_bf16 v[152:155], a[40:43], a[12:15], v[152:155]\n" \
"ds_read_b64_tr_b16 a[110:111], v12 offset:16896\n" \
"v_mfma_f32_16x16x32_bf16 v[156:159], a[44:47], a[12:15], v[156:159]\n" \
"v_mfma_f32_16x16x32_bf16 v[160:163], a[48:51], a[8:11], v[160:163]\n" \
"ds_read_b128 a[80:83], v8 offset:8192\n" \
"v_mfma_f32_16x16x32_bf16 v[164:167], a[52:55], a[8:11], v[164:167]\n" \
"v_mfma_f32_16x16x32_bf16 v[168:171], a[48:51], a[12:15], v[168:171]\n" \
"ds_read_b64_tr_b16 a[112:113], v9 offset:17408\n" \
"v_mfma_f32_16x16x32_bf16 v[172:175], a[52:55], a[12:15], v[172:175]\n" \
"s_mov_b64 exec, 0xffff\nglobal_load_dword v22, v1, s[76:77]\ns_mov_b64 exec, -1\n" \
"v_mfma_f32_16x16x32_bf16 v[176:179], a[56:59], a[8:11], v[176:179]\n" \
"s_add_u32 s76, s76, 2097152\ns_addc_u32 s77, s77, 0\n" \
"ds_read_b64_tr_b16 a[114:115], v10 offset:17408\n" \
"v_mfma_f32_16x16x32_bf16 v[180:183], a[60:63], a[8:11], v[180:183]\n" \
"v_mfma_f32_16x16x32_bf16 v[184:187], a[56:59], a[12:15], v[184:187]\n" \
"ds_read_b128 a[84:87], v8 offset:10240\n" \
"v_mfma_f32_16x16x32_bf16 v[188:191], a[60:63], a[12:15], v[188:191]\n" \
"s_mov_b64 exec, 0xffff\nglobal_load_dword v22, v1, s[76:77]\ns_mov_b64 exec, -1\n" \
"v_mfma_f32_16x16x32_bf16 v[192:195], a[32:35], a[16:19], v[192:195]\n" \
"ds_read_b64_tr_b16 a[116:117], v11 offset:17408\n" \
"v_mfma_f32_16x16x32_bf16 v[196:199], a[36:39], a[16:19], v[196:199]\n" \
"v_mfma_f32_16x16x32_bf16 v[200:203], a[32:35], a[20:23], v[200:203]\n" \
"ds_read_b64_tr_b16 a[118:119], v12 offset:17408\n" \
"v_mfma_f32_16x16x32_bf16 v[204:207], a[36:39], a[20:23], v[204:207]\n" \
"v_mfma_f32_16x16x32_bf16 v[208:211], a[40:43], a[16:19], v[208:211]\n" \
"ds_read_b128 a[88:91], v8 offset:12288\n" \
"v_mfma_f32_16x16x32_bf16 v[212:215], a[44:47], a[16:19], v[212:215]\n" \
"v_mfma_f32_16x16x32_bf16 v[216:219], a[40:43], a[20:23], v[216:219]\n" \
"ds_read_b64_tr_b16 a[120:121], v9 offset:17920\n" \
"v_mfma_f32_16x16x32_bf16 v[220:223], a[44:47], a[20:23], v[220:223]\n" \
"v_mfma_f32_16x16x32_bf16 v[224:227], a[48:51], a[16:19], v[224:227]\n" \
"ds_read_b64_tr_b16 a[122:123], v10 offset:17920\n" \
"v_mfma_f32_16x16x32_bf16 v[228:231], a[52:55], a[16:19], v[228:231]\n" \
"v_mfma_f32_16x16x32_bf16 v[232:235], a[48:51], a[20:23], v[232:235]\n" \
"ds_read_b128 a[92:95], v8 offset:14336\n" \
"v_mfma_f32_16x16x32_bf16 v[236:239], a[52:55], a[20:23], v[236:239]\n" \
"v_mfma_f32_16x16x32_bf16 v[240:243], a[56:59], a[16:19], v[240:243]\n" \
"ds_read_b64_tr_b16 a[124:125], v11 offset:17920\n" \
"v_mfma_f32_16x16x32_bf16 v[244:247], a[60:63], a[16:19], v[244:247]\n" \
"v_mfma_f32_16x16x32_bf16 v[248:251], a[56:59], a[20:23], v[248:251]\n" \
"ds_read_b64_tr_b16 a[126:127], v12 offset:17920\n" \
"v_mfma_f32_16x16x32_bf16 v[252:255], a[60:63], a[20:23], v[252:255]\n" \
"v_mfma_f32_16x16x32_bf16 a[128:131], a[32:35], a[24:27], a[128:131]\n" \
"v_mfma_f32_16x16x32_bf16 a[132:135], a[36:39], a[24:27], a[132:135]\n" \
"v_mfma_f32_16x16x32_bf16 a[136:139], a[32:35], a[28:31], a[136:139]\n" \
"v_mfma_f32_16x16x32_bf16 a[140:143], a[36:39], a[28:31], a[140:143]\n" \
"v_mfma_f32_16x16x32_bf16 a[144:147], a[40:43], a[24:27], a[144:147]\n" \
"v_mfma_f32_16x16x32_bf16 a[148:151], a[44:47], a[24:27], a[148:151]\n" \
"v_mfma_f32_16x16x32_bf16 a[152:155], a[40:43], a[28:31], a[152:155]\n" \
"v_mfma_f32_16x16x32_bf16 a[156:159], a[44:47], a[28:31], a[156:159]\n" \
"s_waitcnt vmcnt(2) lgkmcnt(0)\n" \
"v_mfma_f32_16x16x32_bf16 a[160:163], a[48:51], a[24:27], a[160:163]\n" \
"s_barrier\n" \
"v_mfma_f32_16x16x32_bf16 a[164:167], a[52:55], a[24:27], a[164:167]\n" \
"v_cmp_ne_u64 vcc, s[14:15], v[58:59]\n" \
"v_mfma_f32_16x16x32_bf16 a[168:171], a[48:51], a[28:31], a[168:171]\n" \
"v_mfma_f32_16x16x32_bf16 a[172:175], a[52:55], a[28:31], a[172:175]\n" \
"s_cbranch_vccnz LPUK_%=\nLPUKR_%=:\n" \
"v_mfma_f32_16x16x32_bf16 a[176:179], a[56:59], a[24:27], a[176:179]\n" \
"v_mfma_f32_16x16x32_bf16 a[180:183], a[60:63], a[24:27], a[180:183]\n" \
"v_mfma_f32_16x16x32_bf16 a[184:187], a[56:59], a[28:31], a[184:187]\n" \
"v_mfma_f32_16x16x32_bf16 a[188:191], a[60:63], a[28:31], a[188:191]\n" \
"v_mfma_f32_16x16x32_bf16 v[64:67], a[96:99], a[64:67], v[64:67]\n" \
"s_mov_b64 s[32:33], s[44:45]\n" \
"ds_read_b128 a[0:3], v5 offset:0\n" \
"v_mfma_f32_16x16x32_bf16 v[68:71], a[100:103], a[64:67], v[68:71]\n" \
"v_mfma_f32_16x16x32_bf16 v[72:75], a[96:99], a[68:71], v[72:75]\n" \
"s_add_u32 m0, s56, 10304\n" \
"ds_read_b64_tr_b16 a[32:33], v9 offset:32768\n" \
"v_mfma_f32_16x16x32_bf16 v[76:79], a[100:103], a[68:71], v[76:79]\n" \
"buffer_load_dwordx4 v26, s[72:75], 0 offen offset:0 sc1 lds\n" \
"v_mfma_f32_16x16x32_bf16 v[80:83], a[104:107], a[64:67], v[80:83]\n" \
"s_mov_b32 s34, s49\n" \
"ds_read_b64_tr_b16 a[34:35], v10 offset:32768\n" \
"s_waitcnt vmcnt(8)\n" \
"v_mfma_f32_16x16x32_bf16 v[84:87], a[108:111], a[64:67], v[84:87]\n" \
"v_alignbyte_b32 v30, v31, v30, 2\n" \
"v_alignbyte_b32 v31, v32, v31, 2\n" \
"v_mfma_f32_16x16x32_bf16 v[88:91], a[104:107], a[68:71], v[88:91]\n" \
"s_add_u32 m0, s56, 11328\n" \
"ds_read_b128 a[4:7], v5 offset:2048\n" \
"v_alignbyte_b32 v32, v33, v32, 2\n" \
"v_mfma_f32_16x16x32_bf16 v[92:95], a[108:111], a[68:71], v[92:95]\n" \
"buffer_load_dwordx4 v27, s[72:75], 0 offen offset:0 sc1 lds\n" \
"v_mfma_f32_16x16x32_bf16 v[96:99], a[112:115], a[64:67], v[96:99]\n" \
"ds_read_b64_tr_b16 a[36:37], v11 offset:32768\n" \
"v_alignbyte_b32 v33, v50, v33, 2\n" \
"v_mfma_f32_16x16x32_bf16 v[100:103], a[116:119], a[64:67], v[100:103]\n" \
"ds_write_b128 v55, v[30:33] offset:5184\n" \
"v_alignbyte_b32 v34, v35, v34, 2\n" \
"v_mfma_f32_16x16x32_bf16 v[104:107], a[112:115], a[68:71], v[104:107]\n" \
"s_add_u32 m0, s56, 11328\n" \
"ds_read_b64_tr_b16 a[38:39], v12 offset:32768\n" \
"v_alignbyte_b32 v35, v36, v35, 2\n" \
"v_mfma_f32_16x16x32_bf16 v[108:111], a[116:119], a[68:71], v[108:111]\n" \
"buffer_load_dwordx4 v27, s[72:75], 0 offen offset:1024 sc1 lds\n" \
"v_mfma_f32_16x16x32_bf16 v[112:115], a[120:123], a[64:67], v[112:115]\n" \
"ds_read_b128 a[8:11], v5 offset:4096\n" \
"v_alignbyte_b32 v36, v37, v36, 2\n" \
"v_mfma_f32_16x16x32_bf16 v[116:119], a[124:127], a[64:67], v[116:119]\n" \
"v_alignbyte_b32 v37, v51, v37, 2\n" \
"ds_write_b128 v55, v[34:37] offset:6208\n" \
"v_mfma_f32_16x16x32_bf16 v[120:123], a[120:123], a[68:71], v[120:123]\n" \
"s_add_u32 m0, s56, 11328\n" \
"ds_read_b64_tr_b16 a[40:41], v9 offset:33280\n" \
"v_alignbyte_b32 v38, v39, v38, 2\n" \
"v_mfma_f32_16x16x32_bf16 v[124:127], a[124:127], a[68:71], v[124:127]\n" \
"buffer_load_dwordx4 v27, s[72:75], 0 offen offset:2048 sc1 lds\n" \
"v_mfma_f32_16x16x32_bf16 v[128:131], a[96:99], a[72:75], v[128:131]\n" \
"ds_read_b64_tr_b16 a[42:43], v10 offset:33280\n" \
"v_alignbyte_b32 v39, v40, v39, 2\n" \
"v_mfma_f32_16x16x32_bf16 v[132:135], a[100:103], a[72:75], v[132:135]\n" \
"v_alignbyte_b32 v40, v41, v40, 2\n" \
"v_alignbyte_b32 v41, v52, v41, 2\n" \
"v_mfma_f32_16x16x32_bf16 v[136:139], a[96:99], a[76:79], v[136:139]\n" \
"s_add_u32 m0, s56, 11328\n" \
"ds_read_b128 a[12:15], v5 offset:6144\n" \
"v_mfma_f32_16x16x32_bf16 v[140:143], a[100:103], a[76:79], v[140:143]\n" \
"buffer_load_dwordx4 v27, s[72:75], 0 offen offset:3072 sc1 lds\n" \
"v_mfma_f32_16x16x32_bf16 v[144:147], a[104:107], a[72:75], v[144:147]\n" \
"ds_read_b64_tr_b16 a[44:45], v11 offset:33280\n" \
"v_mfma_f32_16x16x32_bf16 v[148:151], a[108:111], a[72:75], v[148:151]\n" \
"ds_write_b128 v55, v[38:41] offset:7232\n" \
"v_alignbyte_b32 v42, v43, v42, 2\n" \
"v_mfma_f32_16x16x32_bf16 v[152:155], a[104:107], a[76:79], v[152:155]\n" \
"s_add_u32 m0, s56, 0\n" \
"ds_read_b64_tr_b16 a[46:47], v12 offset:33280\n" \
"v_alignbyte_b32 v43, v44, v43, 2\n" \
"v_mfma_f32_16x16x32_bf16 v[156:159], a[108:111], a[76:79], v[156:159]\n" \
"buffer_load_dwordx4 v23, s[68:71], 0 offen offset:0 lds\n" \
"v_mfma_f32_16x16x32_bf16 v[160:163], a[112:115], a[72:75], v[160:163]\n" \
"ds_read_b128 a[16:19], v5 offset:8192\n" \
"v_alignbyte_b32 v44, v45, v44, 2\n" \
"v_mfma_f32_16x16x32_bf16 v[164:167], a[116:119], a[72:75], v[164:167]\n" \
"v_alignbyte_b32 v45, v53, v45, 2\n" \
"ds_write_b128 v55, v[42:45] offset:8256\n" \
"v_mfma_f32_16x16x32_bf16 v[168:171], a[112:115], a[76:79], v[168:171]\n" \
"s_add_u32 m0, s56, 1024\n" \
"ds_read_b64_tr_b16 a[48:49], v9 offset:33792\n" \
"v_alignbyte_b32 v46, v47, v46, 2\n" \
"v_mfma_f32_16x16x32_bf16 v[172:175], a[116:119], a[76:79], v[172:175]\n" \
"buffer_load_dwordx4 v24, s[68:71], 0 offen offset:0 lds\n" \
"v_mfma_f32_16x16x32_bf16 v[176:179], a[120:123], a[72:75], v[176:179]\n" \
"ds_read_b64_tr_b16 a[50:51], v10 offset:33792\n" \
"v_alignbyte_b32 v47, v48, v47, 2\n" \
"v_mfma_f32_16x16x32_bf16 v[180:183], a[124:127], a[72:75], v[180:183]\n" \
"v_alignbyte_b32 v48, v49, v48, 2\n" \
"v_alignbyte_b32 v49, v54, v49, 2\n" \
"v_mfma_f32_16x16x32_bf16 v[184:187], a[120:123], a[76:79], v[184:187]\n" \
"s_add_u32 m0, s56, 1024\n" \
"ds_read_b128 a[20:23], v5 offset:10240\n" \
"v_mfma_f32_16x16x32_bf16 v[188:191], a[124:127], a[76:79], v[188:191]\n" \
"buffer_load_dwordx4 v24, s[68:71], 0 offen offset:1024 lds\n" \
"v_mfma_f32_16x16x32_bf16 v[192:195], a[96:99], a[80:83], v[192:195]\n" \
"ds_read_b64_tr_b16 a[52:53], v11 offset:33792\n" \
"v_mfma_f32_16x16x32_bf16 v[196:199], a[100:103], a[80:83], v[196:199]\n" \
"ds_write_b128 v55, v[46:49] offset:9280\n" \
"v_lshrrev_b32 v30, 4, v4\n" \
"v_mfma_f32_16x16x32_bf16 v[200:203], a[96:99], a[84:87], v[200:203]\n" \
"s_add_u32 m0, s56, 1024\n" \
"ds_read_b64_tr_b16 a[54:55], v12 offset:33792\n" \
"v_and_b32 v31, 15, v30\n" \
"v_mfma_f32_16x16x32_bf16 v[204:207], a[100:103], a[84:87], v[204:207]\n" \
"buffer_load_dwordx4 v24, s[68:71], 0 offen offset:2048 lds\n" \
"v_mfma_f32_16x16x32_bf16 v[208:211], a[104:107], a[80:83], v[208:211]\n" \
"ds_read_b128 a[24:27], v5 offset:12288\n" \
"v_lshrrev_b32 v32, 4, v30\n" \
"v_mfma_f32_16x16x32_bf16 v[212:215], a[108:111], a[80:83], v[212:215]\n" \
"v_add_u32 v33, 1, v31\n" \
"v_and_b32 v34, 1, v33\n" \
"v_mfma_f32_16x16x32_bf16 v[216:219], a[104:107], a[84:87], v[216:219]\n" \
"s_add_u32 m0, s56, 1024\n" \
"ds_read_b64_tr_b16 a[56:57], v9 offset:34304\n" \
"v_and_b32 v33, 30, v33\n" \
"v_mfma_f32_16x16x32_bf16 v[220:223], a[108:111], a[84:87], v[220:223]\n" \
"buffer_load_dwordx4 v24, s[68:71], 0 offen offset:3072 lds\n" \
"v_mfma_f32_16x16x32_bf16 v[224:227], a[112:115], a[80:83], v[224:227]\n" \
"ds_read_b64_tr_b16 a[58:59], v10 offset:34304\n" \
"v_mul_u32_u24 v34, 5184, v34\n" \
"v_mfma_f32_16x16x32_bf16 v[228:231], a[116:119], a[80:83], v[228:231]\n" \
"v_lshl_add_u32 v33, v33, 1, v34\n" \
"v_lshl_add_u32 v33, v32, 4, v33\n" \
"v_mfma_f32_16x16x32_bf16 v[232:235], a[112:115], a[84:87], v[232:235]\n" \
"s_mov_b32 s46, 0\n" \
"s_add_u32 m0, s50, 65536\n" \
"ds_read_b128 a[28:31], v5 offset:14336\n" \
"v_mfma_f32_16x16x32_bf16 v[236:239], a[116:119], a[84:87], v[236:239]\n" \
"buffer_load_dwordx4 v13, s[28:31], s46 offen lds\n" \
"v_mfma_f32_16x16x32_bf16 v[240:243], a[120:123], a[80:83], v[240:243]\n" \
"ds_read_b64_tr_b16 a[60:61], v11 offset:34304\n" \
"v_add_u32 v46, s56, v33\n" \
"v_mfma_f32_16x16x32_bf16 v[244:247], a[124:127], a[80:83], v[244:247]\n" \
"v_add_u32 v47, 1024, v46\n" \
"v_add_u32 v48, 2048, v46\n" \
"v_mfma_f32_16x16x32_bf16 v[248:251], a[120:123], a[84:87], v[248:251]\n" \
"s_add_u32 m0, s50, 69632\n" \
"ds_read_b64_tr_b16 a[62:63], v12 offset:34304\n" \
"v_add_u32 v49, 3072, v46\n" \
"v_mfma_f32_16x16x32_bf16 v[252:255], a[124:127], a[84:87], v[252:255]\n" \
"buffer_load_dwordx4 v14, s[28:31], s46 offen lds\n" \
"v_mfma_f32_16x16x32_bf16 a[128:131], a[96:99], a[88:91], a[128:131]\n" \
"v_lshlrev_b32 v33, 1, v31\n" \
"v_sub_u32 v33, v33, v32\n" \
"v_mfma_f32_16x16x32_bf16 a[132:135], a[100:103], a[88:91], a[132:135]\n" \
"v_lshlrev_b32 v33, 4, v33\n" \
"v_add_u32 v33, 10320, v33\n" \
"v_mfma_f32_16x16x32_bf16 a[136:139], a[96:99], a[92:95], a[136:139]\n" \
"s_add_u32 m0, s50, 73728\n" \
"v_add_u32 v50, s56, v33\n" \
"v_mfma_f32_16x16x32_bf16 a[140:143], a[100:103], a[92:95], a[140:143]\n" \
"buffer_load_dwordx4 v15, s[28:31], s46 offen lds\n" \
"v_mfma_f32_16x16x32_bf16 a[144:147], a[104:107], a[88:91], a[144:147]\n" \
"v_lshlrev_b32 v33, 5, v31\n" \
"v_lshl_add_u32 v55, v32, 3, v33\n" \
"v_mfma_f32_16x16x32_bf16 a[148:151], a[108:111], a[88:91], a[148:151]\n" \
"v_add_u32 v55, s22, v55\n" \
"v_add_u32 v33, s56, v33\n" \
"v_mfma_f32_16x16x32_bf16 a[152:155], a[104:107], a[92:95], a[152:155]\n" \
"v_xor_b32 v32, 1, v32\n" \
"v_mfma_f32_16x16x32_bf16 a[156:159], a[108:111], a[92:95], a[156:159]\n" \
"s_waitcnt vmcnt(16) lgkmcnt(0)\n" \
"v_lshl_add_u32 v51, v32, 3, v33\n" \
"v_mfma_f32_16x16x32_bf16 a[160:163], a[112:115], a[88:91], a[160:163]\n" \
"s_barrier\n" \
"v_mfma_f32_16x16x32_bf16 a[164:167], a[116:119], a[88:91], a[164:167]\n" \
"v_mfma_f32_16x16x32_bf16 a[168:171], a[112:115], a[92:95], a[168:171]\n" \
"s_add_u32 m0, s50, 77824\n" \
"v_mfma_f32_16x16x32_bf16 a[172:175], a[116:119], a[92:95], a[172:175]\n" \
"buffer_load_dwordx4 v16, s[28:31], s46 offen lds\n" \
"v_mfma_f32_16x16x32_bf16 a[176:179], a[120:123], a[88:91], a[176:179]\n" \
"v_mfma_f32_16x16x32_bf16 a[180:183], a[124:127], a[88:91], a[180:183]\n" \
"v_mfma_f32_16x16x32_bf16 a[184:187], a[120:123], a[92:95], a[184:187]\n" \
"v_mfma_f32_16x16x32_bf16 a[188:191], a[124:127], a[92:95], a[188:191]\n" \
"v_mfma_f32_16x16x32_bf16 v[64:67], a[32:35], a[0:3], v[64:67]\n" \
"ds_read_b128 a[64:67], v6 offset:0\n" \
"v_mfma_f32_16x16x32_bf16 v[68:71], a[36:39], a[0:3], v[68:71]\n" \
"v_mfma_f32_16x16x32_bf16 v[72:75], a[32:35], a[4:7], v[72:75]\n" \
"s_mov_b32 s46, 0\n" \
"s_add_u32 m0, s50, 81920\n" \
"ds_read_b64_tr_b16 a[96:97], v9 offset:49152\n" \
"v_mfma_f32_16x16x32_bf16 v[76:79], a[36:39], a[4:7], v[76:79]\n" \
"buffer_load_dwordx4 v17, s[28:31], s46 offen lds\n" \
"v_mfma_f32_16x16x32_bf16 v[80:83], a[40:43], a[0:3], v[80:83]\n" \
"ds_read_b64_tr_b16 a[98:99], v10 offset:49152\n" \
"v_mfma_f32_16x16x32_bf16 v[84:87], a[44:47], a[0:3], v[84:87]\n" \
"v_mfma_f32_16x16x32_bf16 v[88:91], a[40:43], a[4:7], v[88:91]\n" \
"ds_read_b128 a[68:71], v6 offset:2048\n" \
"v_mfma_f32_16x16x32_bf16 v[92:95], a[44:47], a[4:7], v[92:95]\n" \
"v_mfma_f32_16x16x32_bf16 v[96:99], a[48:51], a[0:3], v[96:99]\n" \
"ds_read_b64_tr_b16 a[100:101], v11 offset:49152\n" \
"v_mfma_f32_16x16x32_bf16 v[100:103], a[52:55], a[0:3], v[100:103]\n" \
"v_mfma_f32_16x16x32_bf16 v[104:107], a[48:51], a[4:7], v[104:107]\n" \
"s_add_u32 m0, s50, 86016\n" \
"ds_read_b64_tr_b16 a[102:103], v12 offset:49152\n" \
"v_mfma_f32_16x16x32_bf16 v[108:111], a[52:55], a[4:7], v[108:111]\n" \
"buffer_load_dwordx4 v18, s[28:31], s46 offen lds\n" \
"v_mfma_f32_16x16x32_bf16 v[112:115], a[56:59], a[0:3], v[112:115]\n" \
"ds_read_b128 a[72:75], v6 offset:4096\n" \
"v_mfma_f32_16x16x32_bf16 v[116:119], a[60:63], a[0:3], v[116:119]\n" \
"v_mfma_f32_16x16x32_bf16 v[120:123], a[56:59], a[4:7], v[120:123]\n" \
"ds_read_b64_tr_b16 a[104:105], v9 offset:49664\n" \
"v_mfma_f32_16x16x32_bf16 v[124:127], a[60:63], a[4:7], v[124:127]\n" \
"v_mfma_f32_16x16x32_bf16 v[128:131], a[32:35], a[8:11], v[128:131]\n" \
"ds_read_b64_tr_b16 a[106:107], v10 offset:49664\n" \
"v_mfma_f32_16x16x32_bf16 v[132:135], a[36:39], a[8:11], v[132:135]\n" \
"v_mfma_f32_16x16x32_bf16 v[136:139], a[32:35], a[12:15], v[136:139]\n" \
"s_add_u32 m0, s50, 90112\n" \
"ds_read_b128 a[76:79], v6 offset:6144\n" \
"v_mfma_f32_16x16x32_bf16 v[140:143], a[36:39], a[12:15], v[140:143]\n" \
"buffer_load_dwordx4 v19, s[28:31], s46 offen lds\n" \
"v_mfma_f32_16x16x32_bf16 v[144:147], a[40:43], a[8:11], v[144:147]\n" \
"ds_read_b64_tr_b16 a[108:109], v11 offset:49664\n" \
"v_mfma_f32_16x16x32_bf16 v[148:151], a[44:47], a[8:11], v[148:151]\n" \
"v_mfma_f32_16x16x32_bf16 v[152:155], a[40:43], a[12:15], v[152:155]\n" \
"ds_read_b64_tr_b16 a[110:111], v12 offset:49664\n" \
"v_mfma_f32_16x16x32_bf16 v[156:159], a[44:47], a[12:15], v[156:159]\n" \
"v_mfma_f32_16x16x32_bf16 v[160:163], a[48:51], a[8:11], v[160:163]\n" \
"s_add_u32 s76, s76, 2097152\ns_addc_u32 s77, s77, 0\n" \
"ds_read_b128 a[80:83], v6 offset:8192\n" \
"v_mfma_f32_16x16x32_bf16 v[164:167], a[52:55], a[8:11], v[164:167]\n" \
"v_mfma_f32_16x16x32_bf16 v[168:171], a[48:51], a[12:15], v[168:171]\n" \
"s_add_u32 m0, s50, 94208\n" \
"ds_read_b64_tr_b16 a[112:113], v9 offset:50176\n" \
"v_mfma_f32_16x16x32_bf16 v[172:175], a[52:55], a[12:15], v[172:175]\n" \
"buffer_load_dwordx4 v20, s[28:31], s46 offen lds\n" \
"v_mfma_f32_16x16x32_bf16 v[176:179], a[56:59], a[8:11], v[176:179]\n" \
"ds_read_b64_tr_b16 a[114:115], v10 offset:50176\n" \
"v_mfma_f32_16x16x32_bf16 v[180:183], a[60:63], a[8:11], v[180:183]\n" \
"v_mfma_f32_16x16x32_bf16 v[184:187], a[56:59], a[12:15], v[184:187]\n" \
"ds_read_b128 a[84:87], v6 offset:10240\n" \
"v_mfma_f32_16x16x32_bf16 v[188:191], a[60:63], a[12:15], v[188:191]\n" \
"v_mfma_f32_16x16x32_bf16 v[192:195], a[32:35], a[16:19], v[192:195]\n" \
"ds_read_b64_tr_b16 a[116:117], v11 offset:50176\n" \
"v_mfma_f32_16x16x32_bf16 v[196:199], a[36:39], a[16:19], v[196:199]\n" \
"v_mfma_f32_16x16x32_bf16 v[200:203], a[32:35], a[20:23], v[200:203]\n" \
"s_mov_b32 s47, 0\n" \
"s_add_u32 m0, s51, 32768\n" \
"ds_read_b64_tr_b16 a[118:119], v12 offset:50176\n" \
"v_mfma_f32_16x16x32_bf16 v[204:207], a[36:39], a[20:23], v[204:207]\n" \
"buffer_load_dwordx4 v21, s[32:35], s47 offen offset:0 lds\n" \
"v_mfma_f32_16x16x32_bf16 v[208:211], a[40:43], a[16:19], v[208:211]\n" \
"ds_read_b128 a[88:91], v6 offset:12288\n" \
"v_mfma_f32_16x16x32_bf16 v[212:215], a[44:47], a[16:19], v[212:215]\n" \
"v_mfma_f32_16x16x32_bf16 v[216:219], a[40:43], a[20:23], v[216:219]\n" \
"ds_read_b64_tr_b16 a[120:121], v9 offset:50688\n" \
"v_mfma_f32_16x16x32_bf16 v[220:223], a[44:47], a[20:23], v[220:223]\n" \
"s_mov_b64 exec, 0xffff\nglobal_load_dword v22, v1, s[76:77]\ns_mov_b64 exec, -1\n" \
"v_mfma_f32_16x16x32_bf16 v[224:227], a[48:51], a[16:19], v[224:227]\n" \
"ds_read_b64_tr_b16 a[122:123], v10 offset:50688\n" \
"v_mfma_f32_16x16x32_bf16 v[228:231], a[52:55], a[16:19], v[228:231]\n" \
"v_mfma_f32_16x16x32_bf16 v[232:235], a[48:51], a[20:23], v[232:235]\n" \
"s_add_u32 m0, s51, 33664\n" \
"ds_read_b128 a[92:95], v6 offset:14336\n" \
"v_mfma_f32_16x16x32_bf16 v[236:239], a[52:55], a[20:23], v[236:239]\n" \
"buffer_load_dwordx4 v21, s[32:35], s47 offen offset:128 lds\n" \
"v_mfma_f32_16x16x32_bf16 v[240:243], a[56:59], a[16:19], v[240:243]\n" \
"ds_read_b64_tr_b16 a[124:125], v11 offset:50688\n" \
"v_mfma_f32_16x16x32_bf16 v[244:247], a[60:63], a[16:19], v[244:247]\n" \
"v_mfma_f32_16x16x32_bf16 v[248:251], a[56:59], a[20:23], v[248:251]\n" \
"ds_read_b64_tr_b16 a[126:127], v12 offset:50688\n" \
"v_mfma_f32_16x16x32_bf16 v[252:255], a[60:63], a[20:23], v[252:255]\n" \
"v_add_u32 v5, s54, v5\n" \
"v_mfma_f32_16x16x32_bf16 a[128:131], a[32:35], a[24:27], a[128:131]\n" \
"v_add_u32 v6, s54, v6\n" \
"v_mfma_f32_16x16x32_bf16 a[132:135], a[36:39], a[24:27], a[132:135]\n" \
"v_add_u32 v7, s54, v7\n" \
"v_mfma_f32_16x16x32_bf16 a[136:139], a[32:35], a[28:31], a[136:139]\n" \
"s_mov_b32 s47, 262144\n" \
"s_add_u32 m0, s51, 40960\n" \
"v_mfma_f32_16x16x32_bf16 a[140:143], a[36:39], a[28:31], a[140:143]\n" \
"buffer_load_dwordx4 v21, s[32:35], s47 offen offset:0 lds\n" \
"v_mfma_f32_16x16x32_bf16 a[144:147], a[40:43], a[24:27], a[144:147]\n" \
"v_add_u32 v8, s54, v8\n" \
"v_mfma_f32_16x16x32_bf16 a[148:151], a[44:47], a[24:27], a[148:151]\n" \
"v_sub_u32 v9, v9, v62\n" \
"v_mfma_f32_16x16x32_bf16 a[152:155], a[40:43], a[28:31], a[152:155]\n" \
"v_sub_u32 v10, v10, v62\n" \
"v_mfma_f32_16x16x32_bf16 a[156:159], a[44:47], a[28:31], a[156:159]\n" \
"s_waitcnt lgkmcnt(0)\n" \
"v_mfma_f32_16x16x32_bf16 a[160:163], a[48:51], a[24:27], a[160:163]\n" \
"s_barrier\n" \
"v_mfma_f32_16x16x32_bf16 a[164:167], a[52:55], a[24:27], a[164:167]\n" \
"v_sub_u32 v11, v11, v62\n" \
"v_mfma_f32_16x16x32_bf16 a[168:171], a[48:51], a[28:31], a[168:171]\n" \
"s_add_u32 m0, s51, 41856\n" \
"v_mfma_f32_16x16x32_bf16 a[172:175], a[52:55], a[28:31], a[172:175]\n" \
"buffer_load_dwordx4 v21, s[32:35], s47 offen offset:128 lds\n" \
"v_mfma_f32_16x16x32_bf16 a[176:179], a[56:59], a[24:27], a[176:179]\n" \
"v_sub_u32 v12, v12, v62\n" \
"v_mfma_f32_16x16x32_bf16 a[180:183], a[60:63], a[24:27], a[180:183]\n" \
"v_mfma_f32_16x16x32_bf16 a[184:187], a[56:59], a[28:31], a[184:187]\n" \
"v_mfma_f32_16x16x32_bf16 a[188:191], a[60:63], a[28:31], a[188:191]\n" \
"v_mfma_f32_16x16x32_bf16 v[64:67], a[96:99], a[64:67], v[64:67]\n" \
"s_waitcnt vmcnt(13) lgkmcnt(0)\n" \
"v_mfma_f32_16x16x32_bf16 v[68:71], a[100:103], a[64:67], v[68:71]\n" \
"v_mfma_f32_16x16x32_bf16 v[72:75], a[96:99], a[68:71], v[72:75]\n" \
"s_mov_b32 s46, 128\n" \
"s_add_u32 m0, s50, 0\n" \
"ds_read_b128 a[32:35], v50 offset:512\n" \
"v_mfma_f32_16x16x32_bf16 v[76:79], a[100:103], a[68:71], v[76:79]\n" \
"buffer_load_dwordx4 v13, s[28:31], s46 offen lds\n" \
"v_mfma_f32_16x16x32_bf16 v[80:83], a[104:107], a[64:67], v[80:83]\n" \
"s_mov_b32 s96, s57\n" \
"ds_read2_b32 a[0:1], v46 offset0:0 offset1:1\n" \
"v_mul_f32 v64, s20, v64\n" \
"v_mfma_f32_16x16x32_bf16 v[84:87], a[108:111], a[64:67], v[84:87]\n" \
"v_mul_f32 v65, s20, v65\n" \
"v_mul_f32 v66, s20, v66\n" \
"v_mfma_f32_16x16x32_bf16 v[88:91], a[104:107], a[68:71], v[88:91]\n" \
"ds_read2_b32 a[2:3], v46 offset0:2 offset1:3\n" \
"v_mul_f32 v67, s20, v67\n" \
"v_mfma_f32_16x16x32_bf16 v[92:95], a[108:111], a[68:71], v[92:95]\n" \
"v_cvt_pk_bf16_f32 v30, v64, v65\n" \
"v_cvt_pk_bf16_f32 v31, v66, v67\n" \
"v_mfma_f32_16x16x32_bf16 v[96:99], a[112:115], a[64:67], v[96:99]\n" \
"ds_read_b128 a[36:39], v50 offset:1024\n" \
"v_mul_f32 v68, s20, v68\n" \
"v_mfma_f32_16x16x32_bf16 v[100:103], a[116:119], a[64:67], v[100:103]\n" \
"v_mul_f32 v69, s20, v69\n" \
"v_mul_f32 v70, s20, v70\n" \
"v_mfma_f32_16x16x32_bf16 v[104:107], a[112:115], a[68:71], v[104:107]\n" \
"s_add_u32 m0, s50, 4096\n" \
"ds_read2_b32 a[4:5], v46 offset0:128 offset1:129\n" \
"v_mul_f32 v71, s20, v71\n" \
"v_mfma_f32_16x16x32_bf16 v[108:111], a[116:119], a[68:71], v[108:111]\n" \
"buffer_load_dwordx4 v14, s[28:31], s46 offen lds\n" \
"v_mfma_f32_16x16x32_bf16 v[112:115], a[120:123], a[64:67], v[112:115]\n" \
"ds_read2_b32 a[6:7], v46 offset0:130 offset1:131\n" \
"v_cvt_pk_bf16_f32 v32, v68, v69\n" \
"v_mfma_f32_16x16x32_bf16 v[116:119], a[124:127], a[64:67], v[116:119]\n" \
"v_cvt_pk_bf16_f32 v33, v70, v71\n" \
"v_mul_f32 v72, s20, v72\n" \
"v_mfma_f32_16x16x32_bf16 v[120:123], a[120:123], a[68:71], v[120:123]\n" \
"ds_read_b128 a[40:43], v50 offset:1536\n" \
"v_mul_f32 v73, s20, v73\n" \
"v_mfma_f32_16x16x32_bf16 v[124:127], a[124:127], a[68:71], v[124:127]\n" \
"buffer_store_dwordx4 v[30:33], v4, s[36:39], s96 offen offset:0 sc1\n" \
"v_mfma_f32_16x16x32_bf16 v[128:131], a[96:99], a[72:75], v[128:131]\n" \
"s_add_u32 s97, s96, s98\n" \
"ds_read2_b32 a[8:9], v47 offset0:0 offset1:1\n" \
"v_mul_f32 v74, s20, v74\n" \
"v_mfma_f32_16x16x32_bf16 v[132:135], a[100:103], a[72:75], v[132:135]\n" \
"v_mul_f32 v75, s20, v75\n" \
"v_cvt_pk_bf16_f32 v34, v72, v73\n" \
"v_mfma_f32_16x16x32_bf16 v[136:139], a[96:99], a[76:79], v[136:139]\n" \
"s_add_u32 m0, s50, 8192\n" \
"ds_read2_b32 a[10:11], v47 offset0:2 offset1:3\n" \
"v_cvt_pk_bf16_f32 v35, v74, v75\n" \
"v_mfma_f32_16x16x32_bf16 v[140:143], a[100:103], a[76:79], v[140:143]\n" \
"buffer_load_dwordx4 v15, s[28:31], s46 offen lds\n" \
"v_mfma_f32_16x16x32_bf16 v[144:147], a[104:107], a[72:75], v[144:147]\n" \
"ds_read_b128 a[44:47], v50 offset:2048\n" \
"v_mul_f32 v76, s20, v76\n" \
"v_mfma_f32_16x16x32_bf16 v[148:151], a[108:111], a[72:75], v[148:151]\n" \
"v_mul_f32 v77, s20, v77\n" \
"v_mul_f32 v78, s20, v78\n" \
"v_mfma_f32_16x16x32_bf16 v[152:155], a[104:107], a[76:79], v[152:155]\n" \
"ds_read2_b32 a[12:13], v47 offset0:128 offset1:129\n" \
"v_mul_f32 v79, s20, v79\n" \
"v_mfma_f32_16x16x32_bf16 v[156:159], a[108:111], a[76:79], v[156:159]\n" \
"v_cvt_pk_bf16_f32 v36, v76, v77\n" \
"v_cvt_pk_bf16_f32 v37, v78, v79\n" \
"v_mfma_f32_16x16x32_bf16 v[160:163], a[112:115], a[72:75], v[160:163]\n" \
"ds_read2_b32 a[14:15], v47 offset0:130 offset1:131\n" \
"v_mul_f32 v80, s20, v80\n" \
"v_mfma_f32_16x16x32_bf16 v[164:167], a[116:119], a[72:75], v[164:167]\n" \
"v_mul_f32 v81, s20, v81\n" \
"v_mul_f32 v82, s20, v82\n" \
"v_mfma_f32_16x16x32_bf16 v[168:171], a[112:115], a[76:79], v[168:171]\n" \
"s_add_u32 m0, s50, 12288\n" \
"ds_read_b128 a[48:51], v50 offset:2560\n" \
"v_mul_f32 v83, s20, v83\n" \
"v_mfma_f32_16x16x32_bf16 v[172:175], a[116:119], a[76:79], v[172:175]\n" \
"LH3_%=:\ns_cbranch_execz LH11_%=\n" \
"buffer_load_dwordx4 v16, s[28:31], s46 offen lds\n" \
"v_mfma_f32_16x16x32_bf16 v[176:179], a[120:123], a[72:75], v[176:179]\n" \
"ds_read2_b32 a[16:17], v48 offset0:0 offset1:1\n" \
"v_cvt_pk_bf16_f32 v38, v80, v81\n" \
"v_mfma_f32_16x16x32_bf16 v[180:183], a[124:127], a[72:75], v[180:183]\n" \
"buffer_store_dwordx4 v[34:37], v4, s[36:39], s97 offen offset:0 sc1\n" \
"v_mfma_f32_16x16x32_bf16 v[184:187], a[120:123], a[76:79], v[184:187]\n" \
"s_add_u32 s96, s96, s92\n" \
"ds_read2_b32 a[18:19], v48 offset0:2 offset1:3\n" \
"v_cvt_pk_bf16_f32 v39, v82, v83\n" \
"v_mfma_f32_16x16x32_bf16 v[188:191], a[124:127], a[76:79], v[188:191]\n" \
"v_mul_f32 v84, s20, v84\n" \
"v_mul_f32 v85, s20, v85\n" \
"v_mfma_f32_16x16x32_bf16 v[192:195], a[96:99], a[80:83], v[192:195]\n" \
"ds_read_b128 a[52:55], v50 offset:3072\n" \
"v_mul_f32 v86, s20, v86\n" \
"v_mfma_f32_16x16x32_bf16 v[196:199], a[100:103], a[80:83], v[196:199]\n" \
"v_mul_f32 v87, s20, v87\n" \
"v_cvt_pk_bf16_f32 v40, v84, v85\n" \
"v_mfma_f32_16x16x32_bf16 v[200:203], a[96:99], a[84:87], v[200:203]\n" \
"s_mov_b32 s46, 128\n" \
"s_add_u32 m0, s50, 16384\n" \
"ds_read2_b32 a[20:21], v48 offset0:128 offset1:129\n" \
"v_mfma_f32_16x16x32_bf16 v[204:207], a[100:103], a[84:87], v[204:207]\n" \
"buffer_load_dwordx4 v17, s[28:31], s46 offen lds\n" \
"v_mfma_f32_16x16x32_bf16 v[208:211], a[104:107], a[80:83], v[208:211]\n" \
"ds_read2_b32 a[22:23], v48 offset0:130 offset1:131\n" \
"v_cvt_pk_bf16_f32 v41, v86, v87\n" \
"v_mfma_f32_16x16x32_bf16 v[212:215], a[108:111], a[80:83], v[212:215]\n" \
"v_mul_f32 v88, s20, v88\n" \
"v_mul_f32 v89, s20, v89\n" \
"v_mfma_f32_16x16x32_bf16 v[216:219], a[104:107], a[84:87], v[216:219]\n" \
"ds_read_b128 a[56:59], v50 offset:3584\n" \
"v_mul_f32 v90, s20, v90\n" \
"v_mfma_f32_16x16x32_bf16 v[220:223], a[108:111], a[84:87], v[220:223]\n" \
"v_mul_f32 v91, s20, v91\n" \
"v_cvt_pk_bf16_f32 v42, v88, v89\n" \
"v_mfma_f32_16x16x32_bf16 v[224:227], a[112:115], a[80:83], v[224:227]\n" \
"ds_read2_b32 a[24:25], v49 offset0:0 offset1:1\n" \
"v_cvt_pk_bf16_f32 v43, v90, v91\n" \
"v_mfma_f32_16x16x32_bf16 v[228:231], a[116:119], a[80:83], v[228:231]\n" \
"v_mul_f32 v92, s20, v92\n" \
"v_mul_f32 v93, s20, v93\n" \
"v_mfma_f32_16x16x32_bf16 v[232:235], a[112:115], a[84:87], v[232:235]\n" \
"s_add_u32 m0, s50, 20480\n" \
"ds_read2_b32 a[26:27], v49 offset0:2 offset1:3\n" \
"v_mul_f32 v94, s20, v94\n" \
"v_mfma_f32_16x16x32_bf16 v[236:239], a[116:119], a[84:87], v[236:239]\n" \
"buffer_load_dwordx4 v18, s[28:31], s46 offen lds\n" \
"v_mfma_f32_16x16x32_bf16 v[240:243], a[120:123], a[80:83], v[240:243]\n" \
"ds_read_b128 a[60:63], v50 offset:4096\n" \
"v_mul_f32 v95, s20, v95\n" \
"v_mfma_f32_16x16x32_bf16 v[244:247], a[124:127], a[80:83], v[244:247]\n" \
"buffer_store_dwordx4 v[38:41], v4, s[36:39], s96 offen offset:0 sc1\n" \
"v_mfma_f32_16x16x32_bf16 v[248:251], a[120:123], a[84:87], v[248:251]\n" \
"s_add_u32 s97, s96, s98\n" \
"ds_read2_b32 a[28:29], v49 offset0:128 offset1:129\n" \
"v_cvt_pk_bf16_f32 v44, v92, v93\n" \
"v_mfma_f32_16x16x32_bf16 v[252:255], a[124:127], a[84:87], v[252:255]\n" \
"v_cvt_pk_bf16_f32 v45, v94, v95\n" \
"v_mul_f32 v96, s20, v96\n" \
"v_mfma_f32_16x16x32_bf16 a[128:131], a[96:99], a[88:91], a[128:131]\n" \
"ds_read2_b32 a[30:31], v49 offset0:130 offset1:131\n" \
"v_mul_f32 v97, s20, v97\n" \
"v_mfma_f32_16x16x32_bf16 a[132:135], a[100:103], a[88:91], a[132:135]\n" \
"v_mul_f32 v98, s20, v98\n" \
"v_mul_f32 v99, s20, v99\n" \
"v_mfma_f32_16x16x32_bf16 a[136:139], a[96:99], a[92:95], a[136:139]\n" \
"s_add_u32 m0, s50, 24576\n" \
"v_cvt_pk_bf16_f32 v30, v96, v97\n" \
"v_cvt_pk_bf16_f32 v31, v98, v99\n" \
"v_mfma_f32_16x16x32_bf16 a[140:143], a[100:103], a[92:95], a[140:143]\n" \
"buffer_load_dwordx4 v19, s[28:31], s46 offen lds\n" \
"v_mfma_f32_16x16x32_bf16 a[144:147], a[104:107], a[88:91], a[144:147]\n" \
"v_mul_f32 v100, s20, v100\n" \
"v_mul_f32 v101, s20, v101\n" \
"v_mfma_f32_16x16x32_bf16 a[148:151], a[108:111], a[88:91], a[148:151]\n" \
"v_mul_f32 v102, s20, v102\n" \
"v_mul_f32 v103, s20, v103\n" \
"v_mfma_f32_16x16x32_bf16 a[152:155], a[104:107], a[92:95], a[152:155]\n" \
"v_cvt_pk_bf16_f32 v32, v100, v101\n" \
"v_mfma_f32_16x16x32_bf16 a[156:159], a[108:111], a[92:95], a[156:159]\n" \
"v_cvt_pk_bf16_f32 v33, v102, v103\n" \
"v_mul_f32 v104, s20, v104\n" \
"v_mfma_f32_16x16x32_bf16 a[160:163], a[112:115], a[88:91], a[160:163]\n" \
"v_mul_f32 v105, s20, v105\n" \
"v_mul_f32 v106, s20, v106\n" \
"v_mfma_f32_16x16x32_bf16 a[164:167], a[116:119], a[88:91], a[164:167]\n" \
"v_mul_f32 v107, s20, v107\n" \
"v_mfma_f32_16x16x32_bf16 a[168:171], a[112:115], a[92:95], a[168:171]\n" \
"s_add_u32 m0, s50, 28672\n" \
"v_cvt_pk_bf16_f32 v34, v104, v105\n" \
"v_cvt_pk_bf16_f32 v35, v106, v107\n" \
"v_mfma_f32_16x16x32_bf16 a[172:175], a[116:119], a[92:95], a[172:175]\n" \
"buffer_load_dwordx4 v20, s[28:31], s46 offen lds\n" \
"v_mfma_f32_16x16x32_bf16 a[176:179], a[120:123], a[88:91], a[176:179]\n" \
"v_mul_f32 v108, s20, v108\n" \
"v_mul_f32 v109, s20, v109\n" \
"v_mfma_f32_16x16x32_bf16 a[180:183], a[124:127], a[88:91], a[180:183]\n" \
"buffer_store_dwordx4 v[42:45], v4, s[36:39], s97 offen offset:0 sc1\n" \
"v_mfma_f32_16x16x32_bf16 a[184:187], a[120:123], a[92:95], a[184:187]\n" \
"s_waitcnt lgkmcnt(0)\n" \
"s_add_u32 s96, s96, s92\n" \
"v_mul_f32 v110, s20, v110\n" \
"v_mfma_f32_16x16x32_bf16 a[188:191], a[124:127], a[92:95], a[188:191]\n" \
"v_mul_f32 v111, s20, v111\n" \
"v_cvt_pk_bf16_f32 v36, v108, v109\n" \
"s_branch LC_%=\n" \
"LAV_%=:\n" \
"v_mfma_f32_16x16x32_bf16 v[64:67], a[0:3], a[32:35], 0\n" \
"s_add_u32 s14, s14, 1\ns_addc_u32 s15, s15, 0\n" \
"ds_read_b128 a[64:67], v8 offset:0\n" \
"v_mfma_f32_16x16x32_bf16 v[68:71], a[4:7], a[32:35], 0\n" \
"v_mfma_f32_16x16x32_bf16 v[72:75], a[0:3], a[36:39], 0\n" \
"s_mov_b32 s47, 786432\n" \
"s_add_u32 m0, s22, 49152\n" \
"ds_read_b64_tr_b16 a[96:97], v9 offset:16384\n" \
"v_mfma_f32_16x16x32_bf16 v[76:79], a[4:7], a[36:39], 0\n" \
"buffer_load_dwordx4 v21, s[32:35], s47 offen offset:0 nt lds\n" \
"v_mfma_f32_16x16x32_bf16 v[80:83], a[8:11], a[32:35], 0\n" \
"ds_read_b64_tr_b16 a[98:99], v10 offset:16384\n" \
"v_mfma_f32_16x16x32_bf16 v[84:87], a[12:15], a[32:35], 0\n" \
"v_mfma_f32_16x16x32_bf16 v[88:91], a[8:11], a[36:39], 0\n" \
"s_add_u32 m0, s22, 50048\n" \
"ds_read_b128 a[68:71], v8 offset:2048\n" \
"v_mfma_f32_16x16x32_bf16 v[92:95], a[12:15], a[36:39], 0\n" \
"buffer_load_dwordx4 v21, s[32:35], s47 offen offset:128 nt lds\n" \
"v_mfma_f32_16x16x32_bf16 v[96:99], a[16:19], a[32:35], 0\n" \
"ds_read_b64_tr_b16 a[100:101], v11 offset:16384\n" \
"v_mfma_f32_16x16x32_bf16 v[100:103], a[20:23], a[32:35], 0\n" \
"v_mfma_f32_16x16x32_bf16 v[104:107], a[16:19], a[36:39], 0\n" \
"s_add_u32 m0, s22, 50944\n" \
"ds_read_b64_tr_b16 a[102:103], v12 offset:16384\n" \
"v_mfma_f32_16x16x32_bf16 v[108:111], a[20:23], a[36:39], 0\n" \
"buffer_load_dwordx4 v21, s[32:35], s47 offen offset:256 nt lds\n" \
"v_mfma_f32_16x16x32_bf16 v[112:115], a[24:27], a[32:35], 0\n" \
"ds_read_b128 a[72:75], v8 offset:4096\n" \
"v_mfma_f32_16x16x32_bf16 v[116:119], a[28:31], a[32:35], 0\n" \
"v_mfma_f32_16x16x32_bf16 v[120:123], a[24:27], a[36:39], 0\n" \
"s_add_u32 m0, s22, 51840\n" \
"ds_read_b64_tr_b16 a[104:105], v9 offset:16896\n" \
"v_mfma_f32_16x16x32_bf16 v[124:127], a[28:31], a[36:39], 0\n" \
"buffer_load_dwordx4 v21, s[32:35], s47 offen offset:384 nt lds\n" \
"v_mfma_f32_16x16x32_bf16 v[128:131], a[0:3], a[40:43], 0\n" \
"ds_read_b64_tr_b16 a[106:107], v10 offset:16896\n" \
"v_mfma_f32_16x16x32_bf16 v[132:135], a[4:7], a[40:43], 0\n" \
"v_mfma_f32_16x16x32_bf16 v[136:139], a[0:3], a[44:47], 0\n" \
"s_mov_b32 s46, 256\n" \
"s_add_u32 m0, s50, 32768\n" \
"ds_read_b128 a[76:79], v8 offset:6144\n" \
"v_mfma_f32_16x16x32_bf16 v[140:143], a[4:7], a[44:47], 0\n" \
"buffer_load_dwordx4 v13, s[28:31], s46 offen lds\n" \
"v_mfma_f32_16x16x32_bf16 v[144:147], a[8:11], a[40:43], 0\n" \
"ds_read_b64_tr_b16 a[108:109], v11 offset:16896\n" \
"v_mfma_f32_16x16x32_bf16 v[148:151], a[12:15], a[40:43], 0\n" \
"v_mfma_f32_16x16x32_bf16 v[152:155], a[8:11], a[44:47], 0\n" \
"s_add_u32 m0, s50, 36864\n" \
"ds_read_b64_tr_b16 a[110:111], v12 offset:16896\n" \
"v_mfma_f32_16x16x32_bf16 v[156:159], a[12:15], a[44:47], 0\n" \
"buffer_load_dwordx4 v14, s[28:31], s46 offen lds\n" \
"v_mfma_f32_16x16x32_bf16 v[160:163], a[16:19], a[40:43], 0\n" \
"ds_read_b128 a[80:83], v8 offset:8192\n" \
"v_mfma_f32_16x16x32_bf16 v[164:167], a[20:23], a[40:43], 0\n" \
"v_mfma_f32_16x16x32_bf16 v[168:171], a[16:19], a[44:47], 0\n" \
"s_add_u32 m0, s50, 40960\n" \
"ds_read_b64_tr_b16 a[112:113], v9 offset:17408\n" \
"v_mfma_f32_16x16x32_bf16 v[172:175], a[20:23], a[44:47], 0\n" \
"buffer_load_dwordx4 v15, s[28:31], s46 offen lds\n" \
"v_mfma_f32_16x16x32_bf16 v[176:179], a[24:27], a[40:43], 0\n" \
"ds_read_b64_tr_b16 a[114:115], v10 offset:17408\n" \
"v_mfma_f32_16x16x32_bf16 v[180:183], a[28:31], a[40:43], 0\n" \
"v_mfma_f32_16x16x32_bf16 v[184:187], a[24:27], a[44:47], 0\n" \
"s_add_u32 m0, s50, 45056\n" \
"ds_read_b128 a[84:87], v8 offset:10240\n" \
"v_mfma_f32_16x16x32_bf16 v[188:191], a[28:31], a[44:47], 0\n" \
"buffer_load_dwordx4 v16, s[28:31], s46 offen lds\n" \
"v_mfma_f32_16x16x32_bf16 v[192:195], a[0:3], a[48:51], 0\n" \
"ds_read_b64_tr_b16 a[116:117], v11 offset:17408\n" \
"v_mfma_f32_16x16x32_bf16 v[196:199], a[4:7], a[48:51], 0\n" \
"v_mfma_f32_16x16x32_bf16 v[200:203], a[0:3], a[52:55], 0\n" \
"s_mov_b32 s46, 256\n" \
"s_add_u32 m0, s50, 49152\n" \
"ds_read_b64_tr_b16 a[118:119], v12 offset:17408\n" \
"v_mfma_f32_16x16x32_bf16 v[204:207], a[4:7], a[52:55], 0\n" \
"buffer_load_dwordx4 v17, s[28:31], s46 offen lds\n" \
"v_mfma_f32_16x16x32_bf16 v[208:211], a[8:11], a[48:51], 0\n" \
"ds_read_b128 a[88:91], v8 offset:12288\n" \
"v_mfma_f32_16x16x32_bf16 v[212:215], a[12:15], a[48:51], 0\n" \
"v_mfma_f32_16x16x32_bf16 v[216:219], a[8:11], a[52:55], 0\n" \
"s_add_u32 m0, s50, 53248\n" \
"ds_read_b64_tr_b16 a[120:121], v9 offset:17920\n" \
"v_mfma_f32_16x16x32_bf16 v[220:223], a[12:15], a[52:55], 0\n" \
"buffer_load_dwordx4 v18, s[28:31], s46 offen lds\n" \
"v_mfma_f32_16x16x32_bf16 v[224:227], a[16:19], a[48:51], 0\n" \
"ds_read_b64_tr_b16 a[122:123], v10 offset:17920\n" \
"v_mfma_f32_16x16x32_bf16 v[228:231], a[20:23], a[48:51], 0\n" \
"v_mfma_f32_16x16x32_bf16 v[232:235], a[16:19], a[52:55], 0\n" \
"s_add_u32 m0, s50, 57344\n" \
"ds_read_b128 a[92:95], v8 offset:14336\n" \
"v_mfma_f32_16x16x32_bf16 v[236:239], a[20:23], a[52:55], 0\n" \
"buffer_load_dwordx4 v19, s[28:31], s46 offen lds\n" \
"v_mfma_f32_16x16x32_bf16 v[240:243], a[24:27], a[48:51], 0\n" \
"ds_read_b64_tr_b16 a[124:125], v11 offset:17920\n" \
"v_mfma_f32_16x16x32_bf16 v[244:247], a[28:31], a[48:51], 0\n" \
"v_mfma_f32_16x16x32_bf16 v[248:251], a[24:27], a[52:55], 0\n" \
"s_add_u32 m0, s50, 61440\n" \
"ds_read_b64_tr_b16 a[126:127], v12 offset:17920\n" \
"v_mfma_f32_16x16x32_bf16 v[252:255], a[28:31], a[52:55], 0\n" \
"buffer_load_dwordx4 v20, s[28:31], s46 offen lds\n" \
"v_mfma_f32_16x16x32_bf16 a[128:131], a[0:3], a[56:59], 0\n" \
"v_mfma_f32_16x16x32_bf16 a[132:135], a[4:7], a[56:59], 0\n" \
"v_mfma_f32_16x16x32_bf16 a[136:139], a[0:3], a[60:63], 0\n" \
"v_mfma_f32_16x16x32_bf16 a[140:143], a[4:7], a[60:63], 0\n" \
"v_mfma_f32_16x16x32_bf16 a[144:147], a[8:11], a[56:59], 0\n" \
"global_load_dwordx4 a[230:233], v27, s[70:71] offset:0\n" \
"v_mfma_f32_16x16x32_bf16 a[148:151], a[12:15], a[56:59], 0\n" \
"v_mfma_f32_16x16x32_bf16 a[152:155], a[8:11], a[60:63], 0\n" \
"v_mfma_f32_16x16x32_bf16 a[156:159], a[12:15], a[60:63], 0\n" \
"s_waitcnt vmcnt(13) lgkmcnt(0)\n" \
"v_mfma_f32_16x16x32_bf16 a[160:163], a[16:19], a[56:59], 0\n" \
"s_barrier\n" \
"v_mfma_f32_16x16x32_bf16 a[164:167], a[20:23], a[56:59], 0\n" \
"v_mfma_f32_16x16x32_bf16 a[168:171], a[16:19], a[60:63], 0\n" \
"v_mfma_f32_16x16x32_bf16 a[172:175], a[20:23], a[60:63], 0\n" \
"v_mfma_f32_16x16x32_bf16 a[176:179], a[24:27], a[56:59], 0\n" \
"v_mfma_f32_16x16x32_bf16 a[180:183], a[28:31], a[56:59], 0\n" \
"v_mfma_f32_16x16x32_bf16 a[184:187], a[24:27], a[60:63], 0\n" \
"v_mfma_f32_16x16x32_bf16 a[188:191], a[28:31], a[60:63], 0\n" \
"global_load_dwordx4 a[234:237], v27, s[70:71] offset:16\n" \
"v_mfma_f32_16x16x32_bf16 v[64:67], a[64:67], a[96:99], v[64:67]\n" \
"ds_read_b128 a[0:3], v5 offset:0\n" \
"v_mfma_f32_16x16x32_bf16 v[68:71], a[68:71], a[96:99], v[68:71]\n" \
"v_mfma_f32_16x16x32_bf16 v[72:75], a[64:67], a[100:103], v[72:75]\n" \
"s_mov_b32 s47, 1048576\n" \
"s_add_u32 m0, s22, 0\n" \
"ds_read_b64_tr_b16 a[32:33], v9 offset:32768\n" \
"v_mfma_f32_16x16x32_bf16 v[76:79], a[68:71], a[100:103], v[76:79]\n" \
"buffer_load_dwordx4 v21, s[32:35], s47 offen offset:0 lds\n" \
"v_mfma_f32_16x16x32_bf16 v[80:83], a[72:75], a[96:99], v[80:83]\n" \
"ds_read_b64_tr_b16 a[34:35], v10 offset:32768\n" \
"v_mfma_f32_16x16x32_bf16 v[84:87], a[76:79], a[96:99], v[84:87]\n" \
"v_mfma_f32_16x16x32_bf16 v[88:91], a[72:75], a[100:103], v[88:91]\n" \
"s_add_u32 m0, s22, 896\n" \
"ds_read_b128 a[4:7], v5 offset:2048\n" \
"v_mfma_f32_16x16x32_bf16 v[92:95], a[76:79], a[100:103], v[92:95]\n" \
"buffer_load_dwordx4 v21, s[32:35], s47 offen offset:128 lds\n" \
"v_mfma_f32_16x16x32_bf16 v[96:99], a[80:83], a[96:99], v[96:99]\n" \
"ds_read_b64_tr_b16 a[36:37], v11 offset:32768\n" \
"v_mfma_f32_16x16x32_bf16 v[100:103], a[84:87], a[96:99], v[100:103]\n" \
"v_mfma_f32_16x16x32_bf16 v[104:107], a[80:83], a[100:103], v[104:107]\n" \
"s_add_u32 m0, s22, 1792\n" \
"ds_read_b64_tr_b16 a[38:39], v12 offset:32768\n" \
"v_mfma_f32_16x16x32_bf16 v[108:111], a[84:87], a[100:103], v[108:111]\n" \
"buffer_load_dwordx4 v21, s[32:35], s47 offen offset:256 lds\n" \
"v_mfma_f32_16x16x32_bf16 v[112:115], a[88:91], a[96:99], v[112:115]\n" \
"ds_read_b128 a[8:11], v5 offset:4096\n" \
"v_mfma_f32_16x16x32_bf16 v[116:119], a[92:95], a[96:99], v[116:119]\n" \
"v_mfma_f32_16x16x32_bf16 v[120:123], a[88:91], a[100:103], v[120:123]\n" \
"s_add_u32 m0, s22, 2688\n" \
"ds_read_b64_tr_b16 a[40:41], v9 offset:33280\n" \
"v_mfma_f32_16x16x32_bf16 v[124:127], a[92:95], a[100:103], v[124:127]\n" \
"buffer_load_dwordx4 v21, s[32:35], s47 offen offset:384 lds\n" \
"v_mfma_f32_16x16x32_bf16 v[128:131], a[64:67], a[104:107], v[128:131]\n" \
"ds_read_b64_tr_b16 a[42:43], v10 offset:33280\n" \
"v_mfma_f32_16x16x32_bf16 v[132:135], a[68:71], a[104:107], v[132:135]\n" \
"v_mfma_f32_16x16x32_bf16 v[136:139], a[64:67], a[108:111], v[136:139]\n" \
"s_mov_b32 s47, 1310720\n" \
"s_add_u32 m0, s22, 16384\n" \
"ds_read_b128 a[12:15], v5 offset:6144\n" \
"v_mfma_f32_16x16x32_bf16 v[140:143], a[68:71], a[108:111], v[140:143]\n" \
"buffer_load_dwordx4 v21, s[32:35], s47 offen offset:0 lds\n" \
"v_mfma_f32_16x16x32_bf16 v[144:147], a[72:75], a[104:107], v[144:147]\n" \
"ds_read_b64_tr_b16 a[44:45], v11 offset:33280\n" \
"v_mfma_f32_16x16x32_bf16 v[148:151], a[76:79], a[104:107], v[148:151]\n" \
"v_mfma_f32_16x16x32_bf16 v[152:155], a[72:75], a[108:111], v[152:155]\n" \
"s_mov_b32 s46, 384\n" \
"s_add_u32 m0, s50, 65536\n" \
"ds_read_b64_tr_b16 a[46:47], v12 offset:33280\n" \
"v_mfma_f32_16x16x32_bf16 v[156:159], a[76:79], a[108:111], v[156:159]\n" \
"buffer_load_dwordx4 v13, s[28:31], s46 offen lds\n" \
"v_mfma_f32_16x16x32_bf16 v[160:163], a[80:83], a[104:107], v[160:163]\n" \
"ds_read_b128 a[16:19], v5 offset:8192\n" \
"v_mfma_f32_16x16x32_bf16 v[164:167], a[84:87], a[104:107], v[164:167]\n" \
"v_mfma_f32_16x16x32_bf16 v[168:171], a[80:83], a[108:111], v[168:171]\n" \
"s_add_u32 m0, s22, 17280\n" \
"ds_read_b64_tr_b16 a[48:49], v9 offset:33792\n" \
"v_mfma_f32_16x16x32_bf16 v[172:175], a[84:87], a[108:111], v[172:175]\n" \
"buffer_load_dwordx4 v21, s[32:35], s47 offen offset:128 lds\n" \
"v_mfma_f32_16x16x32_bf16 v[176:179], a[88:91], a[104:107], v[176:179]\n" \
"ds_read_b64_tr_b16 a[50:51], v10 offset:33792\n" \
"v_mfma_f32_16x16x32_bf16 v[180:183], a[92:95], a[104:107], v[180:183]\n" \
"v_mfma_f32_16x16x32_bf16 v[184:187], a[88:91], a[108:111], v[184:187]\n" \
"s_add_u32 m0, s50, 69632\n" \
"ds_read_b128 a[20:23], v5 offset:10240\n" \
"v_mfma_f32_16x16x32_bf16 v[188:191], a[92:95], a[108:111], v[188:191]\n" \
"buffer_load_dwordx4 v14, s[28:31], s46 offen lds\n" \
"v_mfma_f32_16x16x32_bf16 v[192:195], a[64:67], a[112:115], v[192:195]\n" \
"ds_read_b64_tr_b16 a[52:53], v11 offset:33792\n" \
"v_mfma_f32_16x16x32_bf16 v[196:199], a[68:71], a[112:115], v[196:199]\n" \
"v_mfma_f32_16x16x32_bf16 v[200:203], a[64:67], a[116:119], v[200:203]\n" \
"s_add_u32 m0, s22, 18176\n" \
"ds_read_b64_tr_b16 a[54:55], v12 offset:33792\n" \
"v_mfma_f32_16x16x32_bf16 v[204:207], a[68:71], a[116:119], v[204:207]\n" \
"buffer_load_dwordx4 v21, s[32:35], s47 offen offset:256 lds\n" \
"v_mfma_f32_16x16x32_bf16 v[208:211], a[72:75], a[112:115], v[208:211]\n" \
"ds_read_b128 a[24:27], v5 offset:12288\n" \
"v_mfma_f32_16x16x32_bf16 v[212:215], a[76:79], a[112:115], v[212:215]\n" \
"v_mfma_f32_16x16x32_bf16 v[216:219], a[72:75], a[116:119], v[216:219]\n" \
"s_add_u32 m0, s50, 73728\n" \
"ds_read_b64_tr_b16 a[56:57], v9 offset:34304\n" \
"v_mfma_f32_16x16x32_bf16 v[220:223], a[76:79], a[116:119], v[220:223]\n" \
"buffer_load_dwordx4 v15, s[28:31], s46 offen lds\n" \
"v_mfma_f32_16x16x32_bf16 v[224:227], a[80:83], a[112:115], v[224:227]\n" \
"ds_read_b64_tr_b16 a[58:59], v10 offset:34304\n" \
"v_mfma_f32_16x16x32_bf16 v[228:231], a[84:87], a[112:115], v[228:231]\n" \
"v_mfma_f32_16x16x32_bf16 v[232:235], a[80:83], a[116:119], v[232:235]\n" \
"s_add_u32 m0, s22, 19072\n" \
"ds_read_b128 a[28:31], v5 offset:14336\n" \
"v_mfma_f32_16x16x32_bf16 v[236:239], a[84:87], a[116:119], v[236:239]\n" \
"buffer_load_dwordx4 v21, s[32:35], s47 offen offset:384 lds\n" \
"v_mfma_f32_16x16x32_bf16 v[240:243], a[88:91], a[112:115], v[240:243]\n" \
"ds_read_b64_tr_b16 a[60:61], v11 offset:34304\n" \
"v_mfma_f32_16x16x32_bf16 v[244:247], a[92:95], a[112:115], v[244:247]\n" \
"v_mfma_f32_16x16x32_bf16 v[248:251], a[88:91], a[116:119], v[248:251]\n" \
"s_add_u32 m0, s50, 77824\n" \
"ds_read_b64_tr_b16 a[62:63], v12 offset:34304\n" \
"v_mfma_f32_16x16x32_bf16 v[252:255], a[92:95], a[116:119], v[252:255]\n" \
"buffer_load_dwordx4 v16, s[28:31], s46 offen lds\n" \
"v_mfma_f32_16x16x32_bf16 a[128:131], a[64:67], a[120:123], a[128:131]\n" \
"v_mfma_f32_16x16x32_bf16 a[132:135], a[68:71], a[120:123], a[132:135]\n" \
"v_mfma_f32_16x16x32_bf16 a[136:139], a[64:67], a[124:127], a[136:139]\n" \
"s_add_u32 s84, s84, 1048576\ns_addc_u32 s85, s85, 0\n" \
"v_mfma_f32_16x16x32_bf16 a[140:143], a[68:71], a[124:127], a[140:143]\n" \
"buffer_load_dwordx2 a[192:193], v54, s[40:43], 0 offen offset:0 nt\n" \
"v_mfma_f32_16x16x32_bf16 a[144:147], a[72:75], a[120:123], a[144:147]\n" \
"v_mfma_f32_16x16x32_bf16 a[148:151], a[76:79], a[120:123], a[148:151]\n" \
"v_mfma_f32_16x16x32_bf16 a[152:155], a[72:75], a[124:127], a[152:155]\n" \
"v_mfma_f32_16x16x32_bf16 a[156:159], a[76:79], a[124:127], a[156:159]\n" \
"s_waitcnt vmcnt(23) lgkmcnt(0)\n" \
"v_mfma_f32_16x16x32_bf16 a[160:163], a[80:83], a[120:123], a[160:163]\n" \
"s_barrier\n" \
"v_mfma_f32_16x16x32_bf16 a[164:167], a[84:87], a[120:123], a[164:167]\n" \
"buffer_load_dwordx2 a[194:195], v54, s[40:43], 0 offen offset:2048 nt\n" \
"v_mfma_f32_16x16x32_bf16 a[168:171], a[80:83], a[124:127], a[168:171]\n" \
"v_mfma_f32_16x16x32_bf16 a[172:175], a[84:87], a[124:127], a[172:175]\n" \
"v_mfma_f32_16x16x32_bf16 a[176:179], a[88:91], a[120:123], a[176:179]\n" \
"v_mfma_f32_16x16x32_bf16 a[180:183], a[92:95], a[120:123], a[180:183]\n" \
"s_mov_b64 exec, 0xffff\nglobal_load_dword v22, v1, s[84:85]\ns_mov_b64 exec, -1\n" \
"v_mfma_f32_16x16x32_bf16 a[184:187], a[88:91], a[124:127], a[184:187]\n" \
"v_mfma_f32_16x16x32_bf16 a[188:191], a[92:95], a[124:127], a[188:191]\n" \
"v_mfma_f32_16x16x32_bf16 v[64:67], a[0:3], a[32:35], v[64:67]\n" \
"ds_read_b128 a[64:67], v6 offset:0\n" \
"v_mfma_f32_16x16x32_bf16 v[68:71], a[4:7], a[32:35], v[68:71]\n" \
"v_mfma_f32_16x16x32_bf16 v[72:75], a[0:3], a[36:39], v[72:75]\n" \
"s_mov_b32 s47, 1572864\n" \
"s_add_u32 m0, s22, 32768\n" \
"ds_read_b64_tr_b16 a[96:97], v9 offset:49152\n" \
"v_mfma_f32_16x16x32_bf16 v[76:79], a[4:7], a[36:39], v[76:79]\n" \
"buffer_load_dwordx4 v21, s[32:35], s47 offen offset:0 lds\n" \
"v_mfma_f32_16x16x32_bf16 v[80:83], a[8:11], a[32:35], v[80:83]\n" \
"ds_read_b64_tr_b16 a[98:99], v10 offset:49152\n" \
"v_mfma_f32_16x16x32_bf16 v[84:87], a[12:15], a[32:35], v[84:87]\n" \
"v_mfma_f32_16x16x32_bf16 v[88:91], a[8:11], a[36:39], v[88:91]\n" \
"ds_read_b128 a[68:71], v6 offset:2048\n" \
"v_mfma_f32_16x16x32_bf16 v[92:95], a[12:15], a[36:39], v[92:95]\n" \
"buffer_load_dwordx2 a[196:197], v55, s[40:43], 0 offen offset:0 nt\n" \
"v_mfma_f32_16x16x32_bf16 v[96:99], a[16:19], a[32:35], v[96:99]\n" \
"s_mov_b32 s52, 0\n" \
"ds_read_b64_tr_b16 a[100:101], v11 offset:49152\n" \
"v_mfma_f32_16x16x32_bf16 v[100:103], a[20:23], a[32:35], v[100:103]\n" \
"v_mfma_f32_16x16x32_bf16 v[104:107], a[16:19], a[36:39], v[104:107]\n" \
"s_mov_b32 s46, 384\n" \
"s_add_u32 m0, s50, 81920\n" \
"ds_read_b64_tr_b16 a[102:103], v12 offset:49152\n" \
"v_mfma_f32_16x16x32_bf16 v[108:111], a[20:23], a[36:39], v[108:111]\n" \
"buffer_load_dwordx4 v17, s[28:31], s46 offen lds\n" \
"v_mfma_f32_16x16x32_bf16 v[112:115], a[24:27], a[32:35], v[112:115]\n" \
"ds_read_b128 a[72:75], v6 offset:4096\n" \
"v_mfma_f32_16x16x32_bf16 v[116:119], a[28:31], a[32:35], v[116:119]\n" \
"v_mfma_f32_16x16x32_bf16 v[120:123], a[24:27], a[36:39], v[120:123]\n" \
"ds_read_b64_tr_b16 a[104:105], v9 offset:49664\n" \
"v_mfma_f32_16x16x32_bf16 v[124:127], a[28:31], a[36:39], v[124:127]\n" \
"buffer_load_dwordx2 a[198:199], v59, s[40:43], s52 offen offset:0 nt\n" \
"v_mfma_f32_16x16x32_bf16 v[128:131], a[0:3], a[40:43], v[128:131]\n" \
"ds_read_b64_tr_b16 a[106:107], v10 offset:49664\n" \
"v_mfma_f32_16x16x32_bf16 v[132:135], a[4:7], a[40:43], v[132:135]\n" \
"v_mfma_f32_16x16x32_bf16 v[136:139], a[0:3], a[44:47], v[136:139]\n" \
"s_add_u32 m0, s22, 33664\n" \
"ds_read_b128 a[76:79], v6 offset:6144\n" \
"v_mfma_f32_16x16x32_bf16 v[140:143], a[4:7], a[44:47], v[140:143]\n" \
"buffer_load_dwordx4 v21, s[32:35], s47 offen offset:128 lds\n" \
"v_mfma_f32_16x16x32_bf16 v[144:147], a[8:11], a[40:43], v[144:147]\n" \
"ds_read_b64_tr_b16 a[108:109], v11 offset:49664\n" \
"v_mfma_f32_16x16x32_bf16 v[148:151], a[12:15], a[40:43], v[148:151]\n" \
"v_mfma_f32_16x16x32_bf16 v[152:155], a[8:11], a[44:47], v[152:155]\n" \
"ds_read_b64_tr_b16 a[110:111], v12 offset:49664\n" \
"v_mfma_f32_16x16x32_bf16 v[156:159], a[12:15], a[44:47], v[156:159]\n" \
"buffer_load_dwordx2 a[214:215], v59, s[80:83], s52 offen offset:0 nt\n" \
"v_mfma_f32_16x16x32_bf16 v[160:163], a[16:19], a[40:43], v[160:163]\n" \
"ds_read_b128 a[80:83], v6 offset:8192\n" \
"v_mfma_f32_16x16x32_bf16 v[164:167], a[20:23], a[40:43], v[164:167]\n" \
"v_mfma_f32_16x16x32_bf16 v[168:171], a[16:19], a[44:47], v[168:171]\n" \
"s_add_u32 m0, s50, 86016\n" \
"ds_read_b64_tr_b16 a[112:113], v9 offset:50176\n" \
"v_mfma_f32_16x16x32_bf16 v[172:175], a[20:23], a[44:47], v[172:175]\n" \
"buffer_load_dwordx4 v18, s[28:31], s46 offen lds\n" \
"v_mfma_f32_16x16x32_bf16 v[176:179], a[24:27], a[40:43], v[176:179]\n" \
"ds_read_b64_tr_b16 a[114:115], v10 offset:50176\n" \
"v_mfma_f32_16x16x32_bf16 v[180:183], a[28:31], a[40:43], v[180:183]\n" \
"v_mfma_f32_16x16x32_bf16 v[184:187], a[24:27], a[44:47], v[184:187]\n" \
"ds_read_b128 a[84:87], v6 offset:10240\n" \
"v_mfma_f32_16x16x32_bf16 v[188:191], a[28:31], a[44:47], v[188:191]\n" \
"buffer_load_dwordx2 a[200:201], v59, s[40:43], s52 offen offset:2048 nt\n" \
"v_mfma_f32_16x16x32_bf16 v[192:195], a[0:3], a[48:51], v[192:195]\n" \
"ds_read_b64_tr_b16 a[116:117], v11 offset:50176\n" \
"v_mfma_f32_16x16x32_bf16 v[196:199], a[4:7], a[48:51], v[196:199]\n" \
"v_mfma_f32_16x16x32_bf16 v[200:203], a[0:3], a[52:55], v[200:203]\n" \
"s_add_u32 m0, s22, 34560\n" \
"ds_read_b64_tr_b16 a[118:119], v12 offset:50176\n" \
"v_mfma_f32_16x16x32_bf16 v[204:207], a[4:7], a[52:55], v[204:207]\n" \
"buffer_load_dwordx4 v21, s[32:35], s47 offen offset:256 lds\n" \
"v_mfma_f32_16x16x32_bf16 v[208:211], a[8:11], a[48:51], v[208:211]\n" \
"ds_read_b128 a[88:91], v6 offset:12288\n" \
"v_mfma_f32_16x16x32_bf16 v[212:215], a[12:15], a[48:51], v[212:215]\n" \
"v_mfma_f32_16x16x32_bf16 v[216:219], a[8:11], a[52:55], v[216:219]\n" \
"ds_read_b64_tr_b16 a[120:121], v9 offset:50688\n" \
"v_mfma_f32_16x16x32_bf16 v[220:223], a[12:15], a[52:55], v[220:223]\n" \
"buffer_load_dwordx2 a[216:217], v59, s[80:83], s52 offen offset:2048 nt\n" \
"v_mfma_f32_16x16x32_bf16 v[224:227], a[16:19], a[48:51], v[224:227]\n" \
"s_mov_b32 s52, 4096\n" \
"ds_read_b64_tr_b16 a[122:123], v10 offset:50688\n" \
"v_mfma_f32_16x16x32_bf16 v[228:231], a[20:23], a[48:51], v[228:231]\n" \
"v_mfma_f32_16x16x32_bf16 v[232:235], a[16:19], a[52:55], v[232:235]\n" \
"s_add_u32 m0, s50, 90112\n" \
"ds_read_b128 a[92:95], v6 offset:14336\n" \
"v_mfma_f32_16x16x32_bf16 v[236:239], a[20:23], a[52:55], v[236:239]\n" \
"buffer_load_dwordx4 v19, s[28:31], s46 offen lds\n" \
"v_mfma_f32_16x16x32_bf16 v[240:243], a[24:27], a[48:51], v[240:243]\n" \
"ds_read_b64_tr_b16 a[124:125], v11 offset:50688\n" \
"v_mfma_f32_16x16x32_bf16 v[244:247], a[28:31], a[48:51], v[244:247]\n" \
"v_mfma_f32_16x16x32_bf16 v[248:251], a[24:27], a[52:55], v[248:251]\n" \
"ds_read_b64_tr_b16 a[126:127], v12 offset:50688\n" \
"v_mfma_f32_16x16x32_bf16 v[252:255], a[28:31], a[52:55], v[252:255]\n" \
"buffer_load_dwordx2 a[202:203], v59, s[40:43], s52 offen offset:0 nt\n" \
"v_mfma_f32_16x16x32_bf16 a[128:131], a[0:3], a[56:59], a[128:131]\n" \
"v_mfma_f32_16x16x32_bf16 a[132:135], a[4:7], a[56:59], a[132:135]\n" \
"v_mfma_f32_16x16x32_bf16 a[136:139], a[0:3], a[60:63], a[136:139]\n" \
"s_add_u32 m0, s22, 35456\n" \
"v_mfma_f32_16x16x32_bf16 a[140:143], a[4:7], a[60:63], a[140:143]\n" \
"buffer_load_dwordx4 v21, s[32:35], s47 offen offset:384 lds\n" \
"v_mfma_f32_16x16x32_bf16 a[144:147], a[8:11], a[56:59], a[144:147]\n" \
"v_mfma_f32_16x16x32_bf16 a[148:151], a[12:15], a[56:59], a[148:151]\n" \
"v_mfma_f32_16x16x32_bf16 a[152:155], a[8:11], a[60:63], a[152:155]\n" \
"v_mfma_f32_16x16x32_bf16 a[156:159], a[12:15], a[60:63], a[156:159]\n" \
"s_waitcnt vmcnt(24) lgkmcnt(0)\n" \
"v_mfma_f32_16x16x32_bf16 a[160:163], a[16:19], a[56:59], a[160:163]\n" \
"s_barrier\n" \
"v_mfma_f32_16x16x32_bf16 a[164:167], a[20:23], a[56:59], a[164:167]\n" \
"v_mfma_f32_16x16x32_bf16 a[168:171], a[16:19], a[60:63], a[168:171]\n" \
"s_add_u32 m0, s50, 94208\n" \
"v_mfma_f32_16x16x32_bf16 a[172:175], a[20:23], a[60:63], a[172:175]\n" \
"buffer_load_dwordx4 v20, s[28:31], s46 offen lds\n" \
"v_mfma_f32_16x16x32_bf16 a[176:179], a[24:27], a[56:59], a[176:179]\n" \
"v_mfma_f32_16x16x32_bf16 a[180:183], a[28:31], a[56:59], a[180:183]\n" \
"v_mfma_f32_16x16x32_bf16 a[184:187], a[24:27], a[60:63], a[184:187]\n" \
"v_mfma_f32_16x16x32_bf16 a[188:191], a[28:31], a[60:63], a[188:191]\n" \
"buffer_load_dwordx2 a[218:219], v59, s[80:83], s52 offen offset:0 nt\n" \
"v_mfma_f32_16x16x32_bf16 v[64:67], a[64:67], a[96:99], v[64:67]\n" \
"ds_read_b128 a[0:3], v5 offset:32768\n" \
"v_mfma_f32_16x16x32_bf16 v[68:71], a[68:71], a[96:99], v[68:71]\n" \
"v_mfma_f32_16x16x32_bf16 v[72:75], a[64:67], a[100:103], v[72:75]\n" \
"s_mov_b32 s47, 1835008\n" \
"s_add_u32 m0, s22, 49152\n" \
"ds_read_b64_tr_b16 a[32:33], v9 offset:0\n" \
"v_mfma_f32_16x16x32_bf16 v[76:79], a[68:71], a[100:103], v[76:79]\n" \
"buffer_load_dwordx4 v21, s[32:35], s47 offen offset:0 lds\n" \
"v_mfma_f32_16x16x32_bf16 v[80:83], a[72:75], a[96:99], v[80:83]\n" \
"ds_read_b64_tr_b16 a[34:35], v10 offset:0\n" \
"v_mfma_f32_16x16x32_bf16 v[84:87], a[76:79], a[96:99], v[84:87]\n" \
"v_mfma_f32_16x16x32_bf16 v[88:91], a[72:75], a[100:103], v[88:91]\n" \
"ds_read_b128 a[4:7], v5 offset:34816\n" \
"v_mfma_f32_16x16x32_bf16 v[92:95], a[76:79], a[100:103], v[92:95]\n" \
"buffer_load_dwordx2 a[204:205], v59, s[40:43], s52 offen offset:2048 nt\n" \
"v_mfma_f32_16x16x32_bf16 v[96:99], a[80:83], a[96:99], v[96:99]\n" \
"ds_read_b64_tr_b16 a[36:37], v11 offset:0\n" \
"v_mfma_f32_16x16x32_bf16 v[100:103], a[84:87], a[96:99], v[100:103]\n" \
"v_mfma_f32_16x16x32_bf16 v[104:107], a[80:83], a[100:103], v[104:107]\n" \
"s_mov_b32 s46, 512\n" \
"s_add_u32 m0, s50, 0\n" \
"ds_read_b64_tr_b16 a[38:39], v12 offset:0\n" \
"v_mfma_f32_16x16x32_bf16 v[108:111], a[84:87], a[100:103], v[108:111]\n" \
"buffer_load_dwordx4 v13, s[28:31], s46 offen lds\n" \
"v_mfma_f32_16x16x32_bf16 v[112:115], a[88:91], a[96:99], v[112:115]\n" \
"ds_read_b128 a[8:11], v5 offset:36864\n" \
"v_mfma_f32_16x16x32_bf16 v[116:119], a[92:95], a[96:99], v[116:119]\n" \
"v_mfma_f32_16x16x32_bf16 v[120:123], a[88:91], a[100:103], v[120:123]\n" \
"ds_read_b64_tr_b16 a[40:41], v9 offset:512\n" \
"v_mfma_f32_16x16x32_bf16 v[124:127], a[92:95], a[100:103], v[124:127]\n" \
"buffer_load_dwordx2 a[220:221], v59, s[80:83], s52 offen offset:2048 nt\n" \
"v_mfma_f32_16x16x32_bf16 v[128:131], a[64:67], a[104:107], v[128:131]\n" \
"s_mov_b32 s52, 8192\n" \
"ds_read_b64_tr_b16 a[42:43], v10 offset:512\n" \
"v_mfma_f32_16x16x32_bf16 v[132:135], a[68:71], a[104:107], v[132:135]\n" \
"v_mfma_f32_16x16x32_bf16 v[136:139], a[64:67], a[108:111], v[136:139]\n" \
"s_add_u32 m0, s22, 50048\n" \
"ds_read_b128 a[12:15], v5 offset:38912\n" \
"v_mfma_f32_16x16x32_bf16 v[140:143], a[68:71], a[108:111], v[140:143]\n" \
"buffer_load_dwordx4 v21, s[32:35], s47 offen offset:128 lds\n" \
"v_mfma_f32_16x16x32_bf16 v[144:147], a[72:75], a[104:107], v[144:147]\n" \
"ds_read_b64_tr_b16 a[44:45], v11 offset:512\n" \
"v_mfma_f32_16x16x32_bf16 v[148:151], a[76:79], a[104:107], v[148:151]\n" \
"v_mfma_f32_16x16x32_bf16 v[152:155], a[72:75], a[108:111], v[152:155]\n" \
"ds_read_b64_tr_b16 a[46:47], v12 offset:512\n" \
"v_mfma_f32_16x16x32_bf16 v[156:159], a[76:79], a[108:111], v[156:159]\n" \
"buffer_load_dwordx2 a[206:207], v59, s[40:43], s52 offen offset:0 nt\n" \
"v_mfma_f32_16x16x32_bf16 v[160:163], a[80:83], a[104:107], v[160:163]\n" \
"ds_read_b128 a[16:19], v5 offset:40960\n" \
"v_mfma_f32_16x16x32_bf16 v[164:167], a[84:87], a[104:107], v[164:167]\n" \
"v_mfma_f32_16x16x32_bf16 v[168:171], a[80:83], a[108:111], v[168:171]\n" \
"s_add_u32 m0, s50, 4096\n" \
"ds_read_b64_tr_b16 a[48:49], v9 offset:1024\n" \
"v_mfma_f32_16x16x32_bf16 v[172:175], a[84:87], a[108:111], v[172:175]\n" \
"buffer_load_dwordx4 v14, s[28:31], s46 offen lds\n" \
"v_mfma_f32_16x16x32_bf16 v[176:179], a[88:91], a[104:107], v[176:179]\n" \
"ds_read_b64_tr_b16 a[50:51], v10 offset:1024\n" \
"v_mfma_f32_16x16x32_bf16 v[180:183], a[92:95], a[104:107], v[180:183]\n" \
"v_mfma_f32_16x16x32_bf16 v[184:187], a[88:91], a[108:111], v[184:187]\n" \
"ds_read_b128 a[20:23], v5 offset:43008\n" \
"v_mfma_f32_16x16x32_bf16 v[188:191], a[92:95], a[108:111], v[188:191]\n" \
"LH4_%=:\ns_cbranch_execz LH8_%=\n" \
"buffer_load_dwordx2 a[222:223], v59, s[80:83], s52 offen offset:0 nt\n" \
"v_mfma_f32_16x16x32_bf16 v[192:195], a[64:67], a[112:115], v[192:195]\n" \
"ds_read_b64_tr_b16 a[52:53], v11 offset:1024\n" \
"v_mfma_f32_16x16x32_bf16 v[196:199], a[68:71], a[112:115], v[196:199]\n" \
"v_mfma_f32_16x16x32_bf16 v[200:203], a[64:67], a[116:119], v[200:203]\n" \
"s_add_u32 m0, s22, 50944\n" \
"ds_read_b64_tr_b16 a[54:55], v12 offset:1024\n" \
"v_mfma_f32_16x16x32_bf16 v[204:207], a[68:71], a[116:119], v[204:207]\n" \
"buffer_load_dwordx4 v21, s[32:35], s47 offen offset:256 lds\n" \
"v_mfma_f32_16x16x32_bf16 v[208:211], a[72:75], a[112:115], v[208:211]\n" \
"ds_read_b128 a[24:27], v5 offset:45056\n" \
"v_mfma_f32_16x16x32_bf16 v[212:215], a[76:79], a[112:115], v[212:215]\n" \
"v_mfma_f32_16x16x32_bf16 v[216:219], a[72:75], a[116:119], v[216:219]\n" \
"ds_read_b64_tr_b16 a[56:57], v9 offset:1536\n" \
"v_mfma_f32_16x16x32_bf16 v[220:223], a[76:79], a[116:119], v[220:223]\n" \
"buffer_load_dwordx2 a[208:209], v59, s[40:43], s52 offen offset:2048 nt\n" \
"v_mfma_f32_16x16x32_bf16 v[224:227], a[80:83], a[112:115], v[224:227]\n" \
"ds_read_b64_tr_b16 a[58:59], v10 offset:1536\n" \
"v_mfma_f32_16x16x32_bf16 v[228:231], a[84:87], a[112:115], v[228:231]\n" \
"v_mfma_f32_16x16x32_bf16 v[232:235], a[80:83], a[116:119], v[232:235]\n" \
"s_add_u32 m0, s50, 8192\n" \
"ds_read_b128 a[28:31], v5 offset:47104\n" \
"v_mfma_f32_16x16x32_bf16 v[236:239], a[84:87], a[116:119], v[236:239]\n" \
"buffer_load_dwordx4 v15, s[28:31], s46 offen lds\n" \
"v_mfma_f32_16x16x32_bf16 v[240:243], a[88:91], a[112:115], v[240:243]\n" \
"ds_read_b64_tr_b16 a[60:61], v11 offset:1536\n" \
"v_mfma_f32_16x16x32_bf16 v[244:247], a[92:95], a[112:115], v[244:247]\n" \
"v_mfma_f32_16x16x32_bf16 v[248:251], a[88:91], a[116:119], v[248:251]\n" \
"ds_read_b64_tr_b16 a[62:63], v12 offset:1536\n" \
"v_mfma_f32_16x16x32_bf16 v[252:255], a[92:95], a[116:119], v[252:255]\n" \
"buffer_load_dwordx2 a[224:225], v59, s[80:83], s52 offen offset:2048 nt\n" \
"v_mfma_f32_16x16x32_bf16 a[128:131], a[64:67], a[120:123], a[128:131]\n" \
"s_mov_b32 s52, 12288\n" \
"v_mfma_f32_16x16x32_bf16 a[132:135], a[68:71], a[120:123], a[132:135]\n" \
"v_mfma_f32_16x16x32_bf16 a[136:139], a[64:67], a[124:127], a[136:139]\n" \
"s_add_u32 m0, s22, 51840\n" \
"v_mfma_f32_16x16x32_bf16 a[140:143], a[68:71], a[124:127], a[140:143]\n" \
"buffer_load_dwordx4 v21, s[32:35], s47 offen offset:384 lds\n" \
"v_mfma_f32_16x16x32_bf16 a[144:147], a[72:75], a[120:123], a[144:147]\n" \
"v_mfma_f32_16x16x32_bf16 a[148:151], a[76:79], a[120:123], a[148:151]\n" \
"v_mfma_f32_16x16x32_bf16 a[152:155], a[72:75], a[124:127], a[152:155]\n" \
"v_mfma_f32_16x16x32_bf16 a[156:159], a[76:79], a[124:127], a[156:159]\n" \
"s_waitcnt vmcnt(32) lgkmcnt(0)\n" \
"v_mfma_f32_16x16x32_bf16 a[160:163], a[80:83], a[120:123], a[160:163]\n" \
"s_barrier\n" \
"v_mfma_f32_16x16x32_bf16 a[164:167], a[84:87], a[120:123], a[164:167]\n" \
"v_mfma_f32_16x16x32_bf16 a[168:171], a[80:83], a[124:127], a[168:171]\n" \
"s_add_u32 m0, s50, 12288\n" \
"v_mfma_f32_16x16x32_bf16 a[172:175], a[84:87], a[124:127], a[172:175]\n" \
"buffer_load_dwordx4 v16, s[28:31], s46 offen lds\n" \
"v_mfma_f32_16x16x32_bf16 a[176:179], a[88:91], a[120:123], a[176:179]\n" \
"v_mfma_f32_16x16x32_bf16 a[180:183], a[92:95], a[120:123], a[180:183]\n" \
"v_mfma_f32_16x16x32_bf16 a[184:187], a[88:91], a[124:127], a[184:187]\n" \
"v_mfma_f32_16x16x32_bf16 a[188:191], a[92:95], a[124:127], a[188:191]\n" \
"buffer_load_dwordx2 a[210:211], v59, s[40:43], s52 offen offset:0 nt\n" \
"v_mfma_f32_16x16x32_bf16 v[64:67], a[0:3], a[32:35], v[64:67]\n" \
"ds_read_b128 a[64:67], v6 offset:32768\n" \
"v_mfma_f32_16x16x32_bf16 v[68:71], a[4:7], a[32:35], v[68:71]\n" \
"s_waitcnt vmcnt(11)\n" \
"v_accvgpr_read_b32 v43, a230\n" \
"v_lshlrev_b32 v42, 16, v43\n" \
"v_mfma_f32_16x16x32_bf16 v[72:75], a[0:3], a[36:39], v[72:75]\n" \
"s_mov_b32 s47, 2097152\n" \
"s_add_u32 m0, s22, 0\n" \
"ds_read_b64_tr_b16 a[96:97], v9 offset:16384\n" \
"v_mfma_f32_16x16x32_bf16 v[76:79], a[4:7], a[36:39], v[76:79]\n" \
"buffer_load_dwordx4 v21, s[32:35], s47 offen offset:0 lds\n" \
"v_mfma_f32_16x16x32_bf16 v[80:83], a[8:11], a[32:35], v[80:83]\n" \
"ds_read_b64_tr_b16 a[98:99], v10 offset:16384\n" \
"v_and_b32 v43, 0xffff0000, v43\n" \
"v_mfma_f32_16x16x32_bf16 v[84:87], a[12:15], a[32:35], v[84:87]\n" \
"v_accvgpr_read_b32 v45, a231\n" \
"v_lshlrev_b32 v44, 16, v45\n" \
"v_mfma_f32_16x16x32_bf16 v[88:91], a[8:11], a[36:39], v[88:91]\n" \
"ds_read_b128 a[68:71], v6 offset:34816\n" \
"v_and_b32 v45, 0xffff0000, v45\n" \
"v_mfma_f32_16x16x32_bf16 v[92:95], a[12:15], a[36:39], v[92:95]\n" \
"buffer_load_dwordx2 a[226:227], v59, s[80:83], s52 offen offset:0 nt\n" \
"v_mfma_f32_16x16x32_bf16 v[96:99], a[16:19], a[32:35], v[96:99]\n" \
"ds_read_b64_tr_b16 a[100:101], v11 offset:16384\n" \
"v_accvgpr_read_b32 v47, a232\n" \
"v_mfma_f32_16x16x32_bf16 v[100:103], a[20:23], a[32:35], v[100:103]\n" \
"v_lshlrev_b32 v46, 16, v47\n" \
"v_and_b32 v47, 0xffff0000, v47\n" \
"v_mfma_f32_16x16x32_bf16 v[104:107], a[16:19], a[36:39], v[104:107]\n" \
"s_mov_b32 s46, 512\n" \
"s_add_u32 m0, s50, 16384\n" \
"ds_read_b64_tr_b16 a[102:103], v12 offset:16384\n" \
"v_mfma_f32_16x16x32_bf16 v[108:111], a[20:23], a[36:39], v[108:111]\n" \
"buffer_load_dwordx4 v17, s[28:31], s46 offen lds\n" \
"v_mfma_f32_16x16x32_bf16 v[112:115], a[24:27], a[32:35], v[112:115]\n" \
"ds_read_b128 a[72:75], v6 offset:36864\n" \
"v_accvgpr_read_b32 v49, a233\n" \
"v_mfma_f32_16x16x32_bf16 v[116:119], a[28:31], a[32:35], v[116:119]\n" \
"v_lshlrev_b32 v48, 16, v49\n" \
"v_and_b32 v49, 0xffff0000, v49\n" \
"v_mfma_f32_16x16x32_bf16 v[120:123], a[24:27], a[36:39], v[120:123]\n" \
"ds_read_b64_tr_b16 a[104:105], v9 offset:16896\n" \
"v_accvgpr_read_b32 v36, a192\n" \
"v_mfma_f32_16x16x32_bf16 v[124:127], a[28:31], a[36:39], v[124:127]\n" \
"buffer_load_dwordx2 a[212:213], v59, s[40:43], s52 offen offset:2048 nt\n" \
"v_mfma_f32_16x16x32_bf16 v[128:131], a[0:3], a[40:43], v[128:131]\n" \
"ds_read_b64_tr_b16 a[106:107], v10 offset:16896\n" \
"v_lshlrev_b32 v30, 16, v36\n" \
"v_mfma_f32_16x16x32_bf16 v[132:135], a[4:7], a[40:43], v[132:135]\n" \
"v_and_b32 v36, 0xffff0000, v36\n" \
"v_accvgpr_read_b32 v37, a194\n" \
"v_mfma_f32_16x16x32_bf16 v[136:139], a[0:3], a[44:47], v[136:139]\n" \
"s_add_u32 m0, s22, 896\n" \
"ds_read_b128 a[76:79], v6 offset:38912\n" \
"v_lshlrev_b32 v31, 16, v37\n" \
"v_mfma_f32_16x16x32_bf16 v[140:143], a[4:7], a[44:47], v[140:143]\n" \
"buffer_load_dwordx4 v21, s[32:35], s47 offen offset:128 lds\n" \
"v_mfma_f32_16x16x32_bf16 v[144:147], a[8:11], a[40:43], v[144:147]\n" \
"ds_read_b64_tr_b16 a[108:109], v11 offset:16896\n" \
"v_and_b32 v37, 0xffff0000, v37\n" \
"v_mfma_f32_16x16x32_bf16 v[148:151], a[12:15], a[40:43], v[148:151]\n" \
"v_accvgpr_read_b32 v38, a196\n" \
"v_lshlrev_b32 v32, 16, v38\n" \
"v_mfma_f32_16x16x32_bf16 v[152:155], a[8:11], a[44:47], v[152:155]\n" \
"ds_read_b64_tr_b16 a[110:111], v12 offset:16896\n" \
"v_and_b32 v38, 0xffff0000, v38\n" \
"v_mfma_f32_16x16x32_bf16 v[156:159], a[12:15], a[44:47], v[156:159]\n" \
"buffer_load_dwordx2 a[228:229], v59, s[80:83], s52 offen offset:2048 nt\n" \
"v_mfma_f32_16x16x32_bf16 v[160:163], a[16:19], a[40:43], v[160:163]\n" \
"ds_read_b128 a[80:83], v6 offset:40960\n" \
"v_accvgpr_read_b32 v39, a198\n" \
"v_mfma_f32_16x16x32_bf16 v[164:167], a[20:23], a[40:43], v[164:167]\n" \
"v_lshlrev_b32 v33, 16, v39\n" \
"v_and_b32 v39, 0xffff0000, v39\n" \
"v_mfma_f32_16x16x32_bf16 v[168:171], a[16:19], a[44:47], v[168:171]\n" \
"s_add_u32 m0, s50, 20480\n" \
"ds_read_b64_tr_b16 a[112:113], v9 offset:17408\n" \
"v_mul_f32 v25, v30, v45\n" \
"v_mfma_f32_16x16x32_bf16 v[172:175], a[20:23], a[44:47], v[172:175]\n" \
"buffer_load_dwordx4 v18, s[28:31], s46 offen lds\n" \
"v_mfma_f32_16x16x32_bf16 v[176:179], a[24:27], a[40:43], v[176:179]\n" \
"ds_read_b64_tr_b16 a[114:115], v10 offset:17408\n" \
"v_mul_f32 v26, v36, v49\n" \
"v_mfma_f32_16x16x32_bf16 v[180:183], a[28:31], a[40:43], v[180:183]\n" \
"v_fmac_f32 v25, v31, v44\n" \
"v_fmac_f32 v26, v37, v48\n" \
"v_mfma_f32_16x16x32_bf16 v[184:187], a[24:27], a[44:47], v[184:187]\n" \
"ds_read_b128 a[84:87], v6 offset:43008\n" \
"v_fmac_f32 v25, v32, v43\n" \
"v_mfma_f32_16x16x32_bf16 v[188:191], a[28:31], a[44:47], v[188:191]\n" \
"v_fmac_f32 v26, v38, v47\n" \
"v_fmac_f32 v25, v33, v42\n" \
"v_mfma_f32_16x16x32_bf16 v[192:195], a[0:3], a[48:51], v[192:195]\n" \
"ds_read_b64_tr_b16 a[116:117], v11 offset:17408\n" \
"v_fmac_f32 v26, v39, v46\n" \
"v_mfma_f32_16x16x32_bf16 v[196:199], a[4:7], a[48:51], v[196:199]\n" \
"v_accvgpr_read_b32 v28, a214\n" \
"v_lshlrev_b32 v27, 16, v28\n" \
"v_mfma_f32_16x16x32_bf16 v[200:203], a[0:3], a[52:55], v[200:203]\n" \
"s_add_u32 m0, s22, 1792\n" \
"ds_read_b64_tr_b16 a[118:119], v12 offset:17408\n" \
"v_and_b32 v28, 0xffff0000, v28\n" \
"v_mfma_f32_16x16x32_bf16 v[204:207], a[4:7], a[52:55], v[204:207]\n" \
"buffer_load_dwordx4 v21, s[32:35], s47 offen offset:256 lds\n" \
"v_mfma_f32_16x16x32_bf16 v[208:211], a[8:11], a[48:51], v[208:211]\n" \
"ds_read_b128 a[88:91], v6 offset:45056\n" \
"v_mul_f32 v29, v25, v27\n" \
"v_mfma_f32_16x16x32_bf16 v[212:215], a[12:15], a[48:51], v[212:215]\n" \
"v_mul_f32 v58, v26, v28\n" \
"v_accvgpr_read_b32 v40, a200\n" \
"v_mfma_f32_16x16x32_bf16 v[216:219], a[8:11], a[52:55], v[216:219]\n" \
"ds_read_b64_tr_b16 a[120:121], v9 offset:17920\n" \
"v_lshlrev_b32 v34, 16, v40\n" \
"v_mfma_f32_16x16x32_bf16 v[220:223], a[12:15], a[52:55], v[220:223]\n" \
"v_and_b32 v40, 0xffff0000, v40\n" \
"v_mul_f32 v25, v31, v45\n" \
"v_mfma_f32_16x16x32_bf16 v[224:227], a[16:19], a[48:51], v[224:227]\n" \
"ds_read_b64_tr_b16 a[122:123], v10 offset:17920\n" \
"v_mul_f32 v26, v37, v49\n" \
"v_mfma_f32_16x16x32_bf16 v[228:231], a[20:23], a[48:51], v[228:231]\n" \
"v_fmac_f32 v25, v32, v44\n" \
"v_fmac_f32 v26, v38, v48\n" \
"v_mfma_f32_16x16x32_bf16 v[232:235], a[16:19], a[52:55], v[232:235]\n" \
"s_add_u32 m0, s50, 24576\n" \
"ds_read_b128 a[92:95], v6 offset:47104\n" \
"v_fmac_f32 v25, v33, v43\n" \
"v_mfma_f32_16x16x32_bf16 v[236:239], a[20:23], a[52:55], v[236:239]\n" \
"buffer_load_dwordx4 v19, s[28:31], s46 offen lds\n" \
"v_mfma_f32_16x16x32_bf16 v[240:243], a[24:27], a[48:51], v[240:243]\n" \
"ds_read_b64_tr_b16 a[124:125], v11 offset:17920\n" \
"v_fmac_f32 v26, v39, v47\n" \
"v_mfma_f32_16x16x32_bf16 v[244:247], a[28:31], a[48:51], v[244:247]\n" \
"v_fmac_f32 v25, v34, v42\n" \
"v_fmac_f32 v26, v40, v46\n" \
"v_mfma_f32_16x16x32_bf16 v[248:251], a[24:27], a[52:55], v[248:251]\n" \
"ds_read_b64_tr_b16 a[126:127], v12 offset:17920\n" \
"v_accvgpr_read_b32 v28, a216\n" \
"v_mfma_f32_16x16x32_bf16 v[252:255], a[28:31], a[52:55], v[252:255]\n" \
"v_lshlrev_b32 v27, 16, v28\n" \
"v_and_b32 v28, 0xffff0000, v28\n" \
"v_mfma_f32_16x16x32_bf16 a[128:131], a[0:3], a[56:59], a[128:131]\n" \
"v_mul_f32 v25, v25, v27\n" \
"v_mul_f32 v26, v26, v28\n" \
"v_mfma_f32_16x16x32_bf16 a[132:135], a[4:7], a[56:59], a[132:135]\n" \
"v_cvt_pk_bf16_f32 v53, v25, v29\n" \
"v_mfma_f32_16x16x32_bf16 a[136:139], a[0:3], a[60:63], a[136:139]\n" \
"s_add_u32 m0, s22, 2688\n" \
"s_add_u32 s84, s84, 1048576\ns_addc_u32 s85, s85, 0\n" \
"v_mfma_f32_16x16x32_bf16 a[140:143], a[4:7], a[60:63], a[140:143]\n" \
"buffer_load_dwordx4 v21, s[32:35], s47 offen offset:384 lds\n" \
"v_mfma_f32_16x16x32_bf16 a[144:147], a[8:11], a[56:59], a[144:147]\n" \
"v_cvt_pk_bf16_f32 v57, v26, v58\n" \
"v_accvgpr_read_b32 v41, a202\n" \
"v_mfma_f32_16x16x32_bf16 a[148:151], a[12:15], a[56:59], a[148:151]\n" \
"v_lshlrev_b32 v35, 16, v41\n" \
"v_and_b32 v41, 0xffff0000, v41\n" \
"v_mfma_f32_16x16x32_bf16 a[152:155], a[8:11], a[60:63], a[152:155]\n" \
"v_mul_f32 v25, v32, v45\n" \
"v_mfma_f32_16x16x32_bf16 a[156:159], a[12:15], a[60:63], a[156:159]\n" \
"s_waitcnt vmcnt(26) lgkmcnt(0)\n" \
"v_mul_f32 v26, v38, v49\n" \
"v_fmac_f32 v25, v33, v44\n" \
"v_mfma_f32_16x16x32_bf16 a[160:163], a[16:19], a[56:59], a[160:163]\n" \
"s_barrier\n" \
"v_fmac_f32 v26, v39, v48\n" \
"v_fmac_f32 v25, v34, v43\n" \
"v_mfma_f32_16x16x32_bf16 a[164:167], a[20:23], a[56:59], a[164:167]\n" \
"v_fmac_f32 v26, v40, v47\n" \
"v_mfma_f32_16x16x32_bf16 a[168:171], a[16:19], a[60:63], a[168:171]\n" \
"s_add_u32 m0, s50, 28672\n" \
"v_fmac_f32 v25, v35, v42\n" \
"v_fmac_f32 v26, v41, v46\n" \
"v_mfma_f32_16x16x32_bf16 a[172:175], a[20:23], a[60:63], a[172:175]\n" \
"buffer_load_dwordx4 v20, s[28:31], s46 offen lds\n" \
"v_mfma_f32_16x16x32_bf16 a[176:179], a[24:27], a[56:59], a[176:179]\n" \
"v_accvgpr_read_b32 v28, a218\n" \
"v_lshlrev_b32 v27, 16, v28\n" \
"v_mfma_f32_16x16x32_bf16 a[180:183], a[28:31], a[56:59], a[180:183]\n" \
"v_and_b32 v28, 0xffff0000, v28\n" \
"v_mul_f32 v29, v25, v27\n" \
"v_mfma_f32_16x16x32_bf16 a[184:187], a[24:27], a[60:63], a[184:187]\n" \
"v_mul_f32 v58, v26, v28\n" \
"v_mfma_f32_16x16x32_bf16 a[188:191], a[28:31], a[60:63], a[188:191]\n" \
"s_mov_b64 exec, 0xffff\nglobal_load_dword v22, v1, s[84:85]\ns_mov_b64 exec, -1\n" \
"v_mfma_f32_16x16x32_bf16 v[64:67], a[64:67], a[96:99], v[64:67]\n" \
"ds_read_b128 a[0:3], v7 offset:0\n" \
"v_accvgpr_read_b32 v36, a204\n" \
"v_mfma_f32_16x16x32_bf16 v[68:71], a[68:71], a[96:99], v[68:71]\n" \
"v_lshlrev_b32 v30, 16, v36\n" \
"v_and_b32 v36, 0xffff0000, v36\n" \
"v_mfma_f32_16x16x32_bf16 v[72:75], a[64:67], a[100:103], v[72:75]\n" \
"s_mov_b32 s47, 2359296\n" \
"s_add_u32 m0, s22, 16384\n" \
"ds_read_b64_tr_b16 a[32:33], v9 offset:32768\n" \
"v_mfma_f32_16x16x32_bf16 v[76:79], a[68:71], a[100:103], v[76:79]\n" \
"buffer_load_dwordx4 v21, s[32:35], s47 offen offset:0 lds\n" \
"v_mfma_f32_16x16x32_bf16 v[80:83], a[72:75], a[96:99], v[80:83]\n" \
"ds_read_b64_tr_b16 a[34:35], v10 offset:32768\n" \
"v_mul_f32 v25, v33, v45\n" \
"v_mfma_f32_16x16x32_bf16 v[84:87], a[76:79], a[96:99], v[84:87]\n" \
"v_mul_f32 v26, v39, v49\n" \
"v_fmac_f32 v25, v34, v44\n" \
"v_mfma_f32_16x16x32_bf16 v[88:91], a[72:75], a[100:103], v[88:91]\n" \
"ds_read_b128 a[4:7], v7 offset:2048\n" \
"v_fmac_f32 v26, v40, v48\n" \
"v_mfma_f32_16x16x32_bf16 v[92:95], a[76:79], a[100:103], v[92:95]\n" \
"v_fmac_f32 v25, v35, v43\n" \
"v_fmac_f32 v26, v41, v47\n" \
"v_mfma_f32_16x16x32_bf16 v[96:99], a[80:83], a[96:99], v[96:99]\n" \
"ds_read_b64_tr_b16 a[36:37], v11 offset:32768\n" \
"v_fmac_f32 v25, v30, v42\n" \
"v_mfma_f32_16x16x32_bf16 v[100:103], a[84:87], a[96:99], v[100:103]\n" \
"v_fmac_f32 v26, v36, v46\n" \
"v_accvgpr_read_b32 v28, a220\n" \
"v_mfma_f32_16x16x32_bf16 v[104:107], a[80:83], a[100:103], v[104:107]\n" \
"s_mov_b32 s46, 640\n" \
"s_add_u32 m0, s50, 32768\n" \
"ds_read_b64_tr_b16 a[38:39], v12 offset:32768\n" \
"v_mfma_f32_16x16x32_bf16 v[108:111], a[84:87], a[100:103], v[108:111]\n" \
"buffer_load_dwordx4 v13, s[28:31], s46 offen lds\n" \
"v_mfma_f32_16x16x32_bf16 v[112:115], a[88:91], a[96:99], v[112:115]\n" \
"ds_read_b128 a[8:11], v7 offset:4096\n" \
"v_lshlrev_b32 v27, 16, v28\n" \
"v_mfma_f32_16x16x32_bf16 v[116:119], a[92:95], a[96:99], v[116:119]\n" \
"v_and_b32 v28, 0xffff0000, v28\n" \
"v_mul_f32 v25, v25, v27\n" \
"v_mfma_f32_16x16x32_bf16 v[120:123], a[88:91], a[100:103], v[120:123]\n" \
"ds_read_b64_tr_b16 a[40:41], v9 offset:33280\n" \
"v_mul_f32 v26, v26, v28\n" \
"v_mfma_f32_16x16x32_bf16 v[124:127], a[92:95], a[100:103], v[124:127]\n" \
"s_waitcnt vmcnt(8)\n" \
"v_cvt_pk_bf16_f32 v52, v25, v29\n" \
"v_cvt_pk_bf16_f32 v56, v26, v58\n" \
"v_mfma_f32_16x16x32_bf16 v[128:131], a[64:67], a[104:107], v[128:131]\n" \
"ds_read_b64_tr_b16 a[42:43], v10 offset:33280\n" \
"v_accvgpr_read_b32 v37, a206\n" \
"v_mfma_f32_16x16x32_bf16 v[132:135], a[68:71], a[104:107], v[132:135]\n" \
"v_lshlrev_b32 v31, 16, v37\n" \
"v_and_b32 v37, 0xffff0000, v37\n" \
"v_mfma_f32_16x16x32_bf16 v[136:139], a[64:67], a[108:111], v[136:139]\n" \
"s_add_u32 m0, s22, 17280\n" \
"ds_read_b128 a[12:15], v7 offset:6144\n" \
"v_mul_f32 v25, v34, v45\n" \
"v_mfma_f32_16x16x32_bf16 v[140:143], a[68:71], a[108:111], v[140:143]\n" \
"buffer_load_dwordx4 v21, s[32:35], s47 offen offset:128 lds\n" \
"v_mfma_f32_16x16x32_bf16 v[144:147], a[72:75], a[104:107], v[144:147]\n" \
"ds_read_b64_tr_b16 a[44:45], v11 offset:33280\n" \
"v_mul_f32 v26, v40, v49\n" \
"v_mfma_f32_16x16x32_bf16 v[148:151], a[76:79], a[104:107], v[148:151]\n" \
"v_fmac_f32 v25, v35, v44\n" \
"v_fmac_f32 v26, v41, v48\n" \
"v_mfma_f32_16x16x32_bf16 v[152:155], a[72:75], a[108:111], v[152:155]\n" \
"ds_read_b64_tr_b16 a[46:47], v12 offset:33280\n" \
"v_fmac_f32 v25, v30, v43\n" \
"v_mfma_f32_16x16x32_bf16 v[156:159], a[76:79], a[108:111], v[156:159]\n" \
"v_fmac_f32 v26, v36, v47\n" \
"v_fmac_f32 v25, v31, v42\n" \
"v_mfma_f32_16x16x32_bf16 v[160:163], a[80:83], a[104:107], v[160:163]\n" \
"ds_read_b128 a[16:19], v7 offset:8192\n" \
"v_fmac_f32 v26, v37, v46\n" \
"v_mfma_f32_16x16x32_bf16 v[164:167], a[84:87], a[104:107], v[164:167]\n" \
"v_accvgpr_read_b32 v28, a222\n" \
"v_lshlrev_b32 v27, 16, v28\n" \
"v_mfma_f32_16x16x32_bf16 v[168:171], a[80:83], a[108:111], v[168:171]\n" \
"s_add_u32 m0, s50, 36864\n" \
"ds_read_b64_tr_b16 a[48:49], v9 offset:33792\n" \
"v_and_b32 v28, 0xffff0000, v28\n" \
"v_mfma_f32_16x16x32_bf16 v[172:175], a[84:87], a[108:111], v[172:175]\n" \
"buffer_load_dwordx4 v14, s[28:31], s46 offen lds\n" \
"v_mfma_f32_16x16x32_bf16 v[176:179], a[88:91], a[104:107], v[176:179]\n" \
"ds_read_b64_tr_b16 a[50:51], v10 offset:33792\n" \
"v_mul_f32 v29, v25, v27\n" \
"v_mfma_f32_16x16x32_bf16 v[180:183], a[92:95], a[104:107], v[180:183]\n" \
"v_mul_f32 v58, v26, v28\n" \
"v_accvgpr_read_b32 v38, a208\n" \
"v_mfma_f32_16x16x32_bf16 v[184:187], a[88:91], a[108:111], v[184:187]\n" \
"ds_read_b128 a[20:23], v7 offset:10240\n" \
"v_lshlrev_b32 v32, 16, v38\n" \
"v_mfma_f32_16x16x32_bf16 v[188:191], a[92:95], a[108:111], v[188:191]\n" \
"v_and_b32 v38, 0xffff0000, v38\n" \
"v_mul_f32 v25, v35, v45\n" \
"v_mfma_f32_16x16x32_bf16 v[192:195], a[64:67], a[112:115], v[192:195]\n" \
"ds_read_b64_tr_b16 a[52:53], v11 offset:33792\n" \
"v_mul_f32 v26, v41, v49\n" \
"v_mfma_f32_16x16x32_bf16 v[196:199], a[68:71], a[112:115], v[196:199]\n" \
"v_fmac_f32 v25, v30, v44\n" \
"v_fmac_f32 v26, v36, v48\n" \
"v_mfma_f32_16x16x32_bf16 v[200:203], a[64:67], a[116:119], v[200:203]\n" \
"s_add_u32 m0, s22, 18176\n" \
"ds_read_b64_tr_b16 a[54:55], v12 offset:33792\n" \
"v_fmac_f32 v25, v31, v43\n" \
"v_mfma_f32_16x16x32_bf16 v[204:207], a[68:71], a[116:119], v[204:207]\n" \
"buffer_load_dwordx4 v21, s[32:35], s47 offen offset:256 lds\n" \
"v_mfma_f32_16x16x32_bf16 v[208:211], a[72:75], a[112:115], v[208:211]\n" \
"ds_read_b128 a[24:27], v7 offset:12288\n" \
"v_fmac_f32 v26, v37, v47\n" \
"v_mfma_f32_16x16x32_bf16 v[212:215], a[76:79], a[112:115], v[212:215]\n" \
"v_fmac_f32 v25, v32, v42\n" \
"v_fmac_f32 v26, v38, v46\n" \
"v_mfma_f32_16x16x32_bf16 v[216:219], a[72:75], a[116:119], v[216:219]\n" \
"ds_read_b64_tr_b16 a[56:57], v9 offset:34304\n" \
"v_accvgpr_read_b32 v28, a224\n" \
"v_mfma_f32_16x16x32_bf16 v[220:223], a[76:79], a[116:119], v[220:223]\n" \
"v_lshlrev_b32 v27, 16, v28\n" \
"v_and_b32 v28, 0xffff0000, v28\n" \
"v_mfma_f32_16x16x32_bf16 v[224:227], a[80:83], a[112:115], v[224:227]\n" \
"ds_read_b64_tr_b16 a[58:59], v10 offset:34304\n" \
"v_mul_f32 v25, v25, v27\n" \
"v_mfma_f32_16x16x32_bf16 v[228:231], a[84:87], a[112:115], v[228:231]\n" \
"v_mul_f32 v26, v26, v28\n" \
"v_cvt_pk_bf16_f32 v51, v25, v29\n" \
"v_mfma_f32_16x16x32_bf16 v[232:235], a[80:83], a[116:119], v[232:235]\n" \
"s_add_u32 m0, s50, 40960\n" \
"ds_read_b128 a[28:31], v7 offset:14336\n" \
"v_cvt_pk_bf16_f32 v55, v26, v58\n" \
"v_mfma_f32_16x16x32_bf16 v[236:239], a[84:87], a[116:119], v[236:239]\n" \
"buffer_load_dwordx4 v15, s[28:31], s46 offen lds\n" \
"v_mfma_f32_16x16x32_bf16 v[240:243], a[88:91], a[112:115], v[240:243]\n" \
"ds_read_b64_tr_b16 a[60:61], v11 offset:34304\n" \
"v_accvgpr_read_b32 v39, a210\n" \
"v_mfma_f32_16x16x32_bf16 v[244:247], a[92:95], a[112:115], v[244:247]\n" \
"v_lshlrev_b32 v33, 16, v39\n" \
"v_and_b32 v39, 0xffff0000, v39\n" \
"v_mfma_f32_16x16x32_bf16 v[248:251], a[88:91], a[116:119], v[248:251]\n" \
"ds_read_b64_tr_b16 a[62:63], v12 offset:34304\n" \
"v_mul_f32 v25, v30, v45\n" \
"v_mfma_f32_16x16x32_bf16 v[252:255], a[92:95], a[116:119], v[252:255]\n" \
"v_mul_f32 v26, v36, v49\n" \
"v_fmac_f32 v25, v31, v44\n" \
"v_mfma_f32_16x16x32_bf16 a[128:131], a[64:67], a[120:123], a[128:131]\n" \
"v_fmac_f32 v26, v37, v48\n" \
"v_fmac_f32 v25, v32, v43\n" \
"v_mfma_f32_16x16x32_bf16 a[132:135], a[68:71], a[120:123], a[132:135]\n" \
"v_fmac_f32 v26, v38, v47\n" \
"v_mfma_f32_16x16x32_bf16 a[136:139], a[64:67], a[124:127], a[136:139]\n" \
"s_add_u32 m0, s22, 19072\n" \
"v_fmac_f32 v25, v33, v42\n" \
"v_fmac_f32 v26, v39, v46\n" \
"v_mfma_f32_16x16x32_bf16 a[140:143], a[68:71], a[124:127], a[140:143]\n" \
"buffer_load_dwordx4 v21, s[32:35], s47 offen offset:384 lds\n" \
"v_mfma_f32_16x16x32_bf16 a[144:147], a[72:75], a[120:123], a[144:147]\n" \
"v_accvgpr_read_b32 v28, a226\n" \
"v_lshlrev_b32 v27, 16, v28\n" \
"v_mfma_f32_16x16x32_bf16 a[148:151], a[76:79], a[120:123], a[148:151]\n" \
"v_and_b32 v28, 0xffff0000, v28\n" \
"v_mul_f32 v29, v25, v27\n" \
"v_mfma_f32_16x16x32_bf16 a[152:155], a[72:75], a[124:127], a[152:155]\n" \
"v_mul_f32 v58, v26, v28\n" \
"v_mfma_f32_16x16x32_bf16 a[156:159], a[76:79], a[124:127], a[156:159]\n" \
"s_waitcnt vmcnt(21) lgkmcnt(0)\n" \
"v_accvgpr_read_b32 v40, a212\n" \
"v_lshlrev_b32 v34, 16, v40\n" \
"v_mfma_f32_16x16x32_bf16 a[160:163], a[80:83], a[120:123], a[160:163]\n" \
"s_barrier\n" \
"v_and_b32 v40, 0xffff0000, v40\n" \
"v_mul_f32 v25, v31, v45\n" \
"v_mfma_f32_16x16x32_bf16 a[164:167], a[84:87], a[120:123], a[164:167]\n" \
"v_mul_f32 v26, v37, v49\n" \
"v_mfma_f32_16x16x32_bf16 a[168:171], a[80:83], a[124:127], a[168:171]\n" \
"s_add_u32 m0, s50, 45056\n" \
"v_fmac_f32 v25, v32, v44\n" \
"v_fmac_f32 v26, v38, v48\n" \
"v_mfma_f32_16x16x32_bf16 a[172:175], a[84:87], a[124:127], a[172:175]\n" \
"buffer_load_dwordx4 v16, s[28:31], s46 offen lds\n" \
"v_mfma_f32_16x16x32_bf16 a[176:179], a[88:91], a[120:123], a[176:179]\n" \
"v_fmac_f32 v25, v33, v43\n" \
"v_fmac_f32 v26, v39, v47\n" \
"v_mfma_f32_16x16x32_bf16 a[180:183], a[92:95], a[120:123], a[180:183]\n" \
"v_fmac_f32 v25, v34, v42\n" \
"v_fmac_f32 v26, v40, v46\n" \
"v_mfma_f32_16x16x32_bf16 a[184:187], a[88:91], a[124:127], a[184:187]\n" \
"v_accvgpr_read_b32 v28, a228\n" \
"v_mfma_f32_16x16x32_bf16 a[188:191], a[92:95], a[124:127], a[188:191]\n" \
"v_lshlrev_b32 v27, 16, v28\n" \
"v_and_b32 v28, 0xffff0000, v28\n" \
"v_mfma_f32_16x16x32_bf16 v[64:67], a[0:3], a[32:35], v[64:67]\n" \
"s_mov_b32 s47, 2621440\n" \
"s_add_u32 m0, s22, 32768\n" \
"ds_read_b128 a[64:67], v8 offset:0\n" \
"v_mfma_f32_16x16x32_bf16 v[68:71], a[4:7], a[32:35], v[68:71]\n" \
"buffer_load_dwordx4 v21, s[32:35], s47 offen offset:0 lds\n" \
"v_mfma_f32_16x16x32_bf16 v[72:75], a[0:3], a[36:39], v[72:75]\n" \
"ds_read_b64_tr_b16 a[96:97], v9 offset:49152\n" \
"v_mul_f32 v25, v25, v27\n" \
"v_mfma_f32_16x16x32_bf16 v[76:79], a[4:7], a[36:39], v[76:79]\n" \
"v_mul_f32 v26, v26, v28\n" \
"v_cvt_pk_bf16_f32 v50, v25, v29\n" \
"v_mfma_f32_16x16x32_bf16 v[80:83], a[8:11], a[32:35], v[80:83]\n" \
"s_mov_b32 s46, 640\n" \
"s_add_u32 m0, s50, 49152\n" \
"ds_read_b64_tr_b16 a[98:99], v10 offset:49152\n" \
"v_mfma_f32_16x16x32_bf16 v[84:87], a[12:15], a[32:35], v[84:87]\n" \
"buffer_load_dwordx4 v17, s[28:31], s46 offen lds\n" \
"v_mfma_f32_16x16x32_bf16 v[88:91], a[8:11], a[36:39], v[88:91]\n" \
"s_mov_b32 s94, 79691776\n" \
"ds_read_b128 a[68:71], v8 offset:2048\n" \
"v_cvt_pk_bf16_f32 v54, v26, v58\n" \
"v_mfma_f32_16x16x32_bf16 v[92:95], a[12:15], a[36:39], v[92:95]\n" \
"buffer_store_dwordx4 v[50:53], v60, s[36:39], s94 offen offset:0 sc1\n" \
"v_mfma_f32_16x16x32_bf16 v[96:99], a[16:19], a[32:35], v[96:99]\n" \
"s_add_u32 m0, s22, 33664\n" \
"ds_read_b64_tr_b16 a[100:101], v11 offset:49152\n" \
"s_mov_b32 s95, 79695872\n" \
"v_mfma_f32_16x16x32_bf16 v[100:103], a[20:23], a[32:35], v[100:103]\n" \
"buffer_load_dwordx4 v21, s[32:35], s47 offen offset:128 lds\n" \
"v_mfma_f32_16x16x32_bf16 v[104:107], a[16:19], a[36:39], v[104:107]\n" \
"ds_read_b64_tr_b16 a[102:103], v12 offset:49152\n" \
"v_accvgpr_read_b32 v43, a234\n" \
"v_mfma_f32_16x16x32_bf16 v[108:111], a[20:23], a[36:39], v[108:111]\n" \
"v_lshlrev_b32 v42, 16, v43\n" \
"v_and_b32 v43, 0xffff0000, v43\n" \
"v_mfma_f32_16x16x32_bf16 v[112:115], a[24:27], a[32:35], v[112:115]\n" \
"s_add_u32 m0, s50, 53248\n" \
"ds_read_b128 a[72:75], v8 offset:4096\n" \
"v_accvgpr_read_b32 v45, a235\n" \
"v_mfma_f32_16x16x32_bf16 v[116:119], a[28:31], a[32:35], v[116:119]\n" \
"buffer_load_dwordx4 v18, s[28:31], s46 offen lds\n" \
"v_mfma_f32_16x16x32_bf16 v[120:123], a[24:27], a[36:39], v[120:123]\n" \
"ds_read_b64_tr_b16 a[104:105], v9 offset:49664\n" \
"v_lshlrev_b32 v44, 16, v45\n" \
"v_mfma_f32_16x16x32_bf16 v[124:127], a[28:31], a[36:39], v[124:127]\n" \
"v_and_b32 v45, 0xffff0000, v45\n" \
"v_accvgpr_read_b32 v47, a236\n" \
"v_mfma_f32_16x16x32_bf16 v[128:131], a[0:3], a[40:43], v[128:131]\n" \
"s_add_u32 m0, s22, 34560\n" \
"ds_read_b64_tr_b16 a[106:107], v10 offset:49664\n" \
"v_lshlrev_b32 v46, 16, v47\n" \
"v_mfma_f32_16x16x32_bf16 v[132:135], a[4:7], a[40:43], v[132:135]\n" \
"buffer_load_dwordx4 v21, s[32:35], s47 offen offset:256 lds\n" \
"v_mfma_f32_16x16x32_bf16 v[136:139], a[0:3], a[44:47], v[136:139]\n" \
"ds_read_b128 a[76:79], v8 offset:6144\n" \
"v_and_b32 v47, 0xffff0000, v47\n" \
"v_mfma_f32_16x16x32_bf16 v[140:143], a[4:7], a[44:47], v[140:143]\n" \
"v_accvgpr_read_b32 v49, a237\n" \
"v_lshlrev_b32 v48, 16, v49\n" \
"v_mfma_f32_16x16x32_bf16 v[144:147], a[8:11], a[40:43], v[144:147]\n" \
"s_add_u32 m0, s50, 57344\n" \
"ds_read_b64_tr_b16 a[108:109], v11 offset:49664\n" \
"v_and_b32 v49, 0xffff0000, v49\n" \
"v_mfma_f32_16x16x32_bf16 v[148:151], a[12:15], a[40:43], v[148:151]\n" \
"buffer_load_dwordx4 v19, s[28:31], s46 offen lds\n" \
"v_mfma_f32_16x16x32_bf16 v[152:155], a[8:11], a[44:47], v[152:155]\n" \
"ds_read_b64_tr_b16 a[110:111], v12 offset:49664\n" \
"v_accvgpr_read_b32 v36, a193\n" \
"v_mfma_f32_16x16x32_bf16 v[156:159], a[12:15], a[44:47], v[156:159]\n" \
"buffer_store_dwordx4 v[54:57], v60, s[36:39], s95 offen offset:0 sc1\n" \
"v_mfma_f32_16x16x32_bf16 v[160:163], a[16:19], a[40:43], v[160:163]\n" \
"s_add_u32 m0, s22, 35456\n" \
"ds_read_b128 a[80:83], v8 offset:8192\n" \
"v_lshlrev_b32 v30, 16, v36\n" \
"v_mfma_f32_16x16x32_bf16 v[164:167], a[20:23], a[40:43], v[164:167]\n" \
"buffer_load_dwordx4 v21, s[32:35], s47 offen offset:384 lds\n" \
"v_mfma_f32_16x16x32_bf16 v[168:171], a[16:19], a[44:47], v[168:171]\n" \
"s_add_u32 s84, s84, 1048576\ns_addc_u32 s85, s85, 0\n" \
"ds_read_b64_tr_b16 a[112:113], v9 offset:50176\n" \
"v_mfma_f32_16x16x32_bf16 v[172:175], a[20:23], a[44:47], v[172:175]\n" \
"v_and_b32 v36, 0xffff0000, v36\n" \
"v_accvgpr_read_b32 v37, a195\n" \
"v_mfma_f32_16x16x32_bf16 v[176:179], a[24:27], a[40:43], v[176:179]\n" \
"s_add_u32 m0, s50, 61440\n" \
"ds_read_b64_tr_b16 a[114:115], v10 offset:50176\n" \
"v_lshlrev_b32 v31, 16, v37\n" \
"v_mfma_f32_16x16x32_bf16 v[180:183], a[28:31], a[40:43], v[180:183]\n" \
"buffer_load_dwordx4 v20, s[28:31], s46 offen lds\n" \
"v_mfma_f32_16x16x32_bf16 v[184:187], a[24:27], a[44:47], v[184:187]\n" \
"ds_read_b128 a[84:87], v8 offset:10240\n" \
"v_and_b32 v37, 0xffff0000, v37\n" \
"v_mfma_f32_16x16x32_bf16 v[188:191], a[28:31], a[44:47], v[188:191]\n" \
"v_accvgpr_read_b32 v38, a197\n" \
"v_lshlrev_b32 v32, 16, v38\n" \
"v_mfma_f32_16x16x32_bf16 v[192:195], a[0:3], a[48:51], v[192:195]\n" \
"ds_read_b64_tr_b16 a[116:117], v11 offset:50176\n" \
"v_and_b32 v38, 0xffff0000, v38\n" \
"v_mfma_f32_16x16x32_bf16 v[196:199], a[4:7], a[48:51], v[196:199]\n" \
"s_mov_b64 exec, 0xffff\nglobal_load_dword v22, v1, s[84:85]\ns_mov_b64 exec, -1\n" \
"v_mfma_f32_16x16x32_bf16 v[200:203], a[0:3], a[52:55], v[200:203]\n" \
"ds_read_b64_tr_b16 a[118:119], v12 offset:50176\n" \
"v_accvgpr_read_b32 v39, a199\n" \
"v_mfma_f32_16x16x32_bf16 v[204:207], a[4:7], a[52:55], v[204:207]\n" \
"v_lshlrev_b32 v33, 16, v39\n" \
"v_and_b32 v39, 0xffff0000, v39\n" \
"v_mfma_f32_16x16x32_bf16 v[208:211], a[8:11], a[48:51], v[208:211]\n" \
"ds_read_b128 a[88:91], v8 offset:12288\n" \
"v_mul_f32 v25, v30, v45\n" \
"v_mfma_f32_16x16x32_bf16 v[212:215], a[12:15], a[48:51], v[212:215]\n" \
"s_mov_b64 exec, 0xffff\nglobal_load_dword v22, v1, s[76:77]\ns_mov_b64 exec, -1\n" \
"v_mfma_f32_16x16x32_bf16 v[216:219], a[8:11], a[52:55], v[216:219]\n" \
"ds_read_b64_tr_b16 a[120:121], v9 offset:50688\n" \
"v_mul_f32 v26, v36, v49\n" \
"v_mfma_f32_16x16x32_bf16 v[220:223], a[12:15], a[52:55], v[220:223]\n" \
"v_fmac_f32 v25, v31, v44\n" \
"v_fmac_f32 v26, v37, v48\n" \
"v_mfma_f32_16x16x32_bf16 v[224:227], a[16:19], a[48:51], v[224:227]\n" \
"ds_read_b64_tr_b16 a[122:123], v10 offset:50688\n" \
"v_fmac_f32 v25, v32, v43\n" \
"v_mfma_f32_16x16x32_bf16 v[228:231], a[20:23], a[48:51], v[228:231]\n" \
"v_fmac_f32 v26, v38, v47\n" \
"v_fmac_f32 v25, v33, v42\n" \
"v_mfma_f32_16x16x32_bf16 v[232:235], a[16:19], a[52:55], v[232:235]\n" \
"ds_read_b128 a[92:95], v8 offset:14336\n" \
"v_fmac_f32 v26, v39, v46\n" \
"v_mfma_f32_16x16x32_bf16 v[236:239], a[20:23], a[52:55], v[236:239]\n" \
"v_accvgpr_read_b32 v28, a215\n" \
"v_lshlrev_b32 v27, 16, v28\n" \
"v_mfma_f32_16x16x32_bf16 v[240:243], a[24:27], a[48:51], v[240:243]\n" \
"ds_read_b64_tr_b16 a[124:125], v11 offset:50688\n" \
"v_and_b32 v28, 0xffff0000, v28\n" \
"v_mfma_f32_16x16x32_bf16 v[244:247], a[28:31], a[48:51], v[244:247]\n" \
"v_mul_f32 v29, v25, v27\n" \
"v_mul_f32 v58, v26, v28\n" \
"v_mfma_f32_16x16x32_bf16 v[248:251], a[24:27], a[52:55], v[248:251]\n" \
"ds_read_b64_tr_b16 a[126:127], v12 offset:50688\n" \
"v_accvgpr_read_b32 v40, a201\n" \
"v_mfma_f32_16x16x32_bf16 v[252:255], a[28:31], a[52:55], v[252:255]\n" \
"v_lshlrev_b32 v34, 16, v40\n" \
"v_and_b32 v40, 0xffff0000, v40\n" \
"v_mfma_f32_16x16x32_bf16 a[128:131], a[0:3], a[56:59], a[128:131]\n" \
"LH5_%=:\ns_cbranch_execz LH9_%=\n" \
"v_mul_f32 v25, v31, v45\n" \
"v_mul_f32 v26, v37, v49\n" \
"v_mfma_f32_16x16x32_bf16 a[132:135], a[4:7], a[56:59], a[132:135]\n" \
"v_fmac_f32 v25, v32, v44\n" \
"v_mfma_f32_16x16x32_bf16 a[136:139], a[0:3], a[60:63], a[136:139]\n" \
"v_fmac_f32 v26, v38, v48\n" \
"v_fmac_f32 v25, v33, v43\n" \
"v_mfma_f32_16x16x32_bf16 a[140:143], a[4:7], a[60:63], a[140:143]\n" \
"v_fmac_f32 v26, v39, v47\n" \
"v_fmac_f32 v25, v34, v42\n" \
"v_mfma_f32_16x16x32_bf16 a[144:147], a[8:11], a[56:59], a[144:147]\n" \
"v_fmac_f32 v26, v40, v46\n" \
"v_mfma_f32_16x16x32_bf16 a[148:151], a[12:15], a[56:59], a[148:151]\n" \
"v_accvgpr_read_b32 v28, a217\n" \
"v_lshlrev_b32 v27, 16, v28\n" \
"v_mfma_f32_16x16x32_bf16 a[152:155], a[8:11], a[60:63], a[152:155]\n" \
"v_and_b32 v28, 0xffff0000, v28\n" \
"v_mul_f32 v25, v25, v27\n" \
"v_mfma_f32_16x16x32_bf16 a[156:159], a[12:15], a[60:63], a[156:159]\n" \
"s_waitcnt vmcnt(21) lgkmcnt(0)\n" \
"v_mul_f32 v26, v26, v28\n" \
"v_mfma_f32_16x16x32_bf16 a[160:163], a[16:19], a[56:59], a[160:163]\n" \
"s_barrier\n" \
"v_cvt_pk_bf16_f32 v53, v25, v29\n" \
"v_cvt_pk_bf16_f32 v57, v26, v58\n" \
"v_mfma_f32_16x16x32_bf16 a[164:167], a[20:23], a[56:59], a[164:167]\n" \
"v_accvgpr_read_b32 v41, a203\n" \
"v_lshlrev_b32 v35, 16, v41\n" \
"v_mfma_f32_16x16x32_bf16 a[168:171], a[16:19], a[60:63], a[168:171]\n" \
"v_and_b32 v41, 0xffff0000, v41\n" \
"v_mfma_f32_16x16x32_bf16 a[172:175], a[20:23], a[60:63], a[172:175]\n" \
"v_mul_f32 v25, v32, v45\n" \
"v_mul_f32 v26, v38, v49\n" \
"v_mfma_f32_16x16x32_bf16 a[176:179], a[24:27], a[56:59], a[176:179]\n" \
"v_fmac_f32 v25, v33, v44\n" \
"v_fmac_f32 v26, v39, v48\n" \
"v_mfma_f32_16x16x32_bf16 a[180:183], a[28:31], a[56:59], a[180:183]\n" \
"v_fmac_f32 v25, v34, v43\n" \
"v_mfma_f32_16x16x32_bf16 a[184:187], a[24:27], a[60:63], a[184:187]\n" \
"v_fmac_f32 v26, v40, v47\n" \
"v_fmac_f32 v25, v35, v42\n" \
"v_mfma_f32_16x16x32_bf16 a[188:191], a[28:31], a[60:63], a[188:191]\n" \
"v_fmac_f32 v26, v41, v46\n" \
"v_accvgpr_read_b32 v28, a219\n" \
"v_mfma_f32_16x16x32_bf16 v[64:67], a[64:67], a[96:99], v[64:67]\n" \
"ds_read_b128 a[0:3], v5 offset:0\n" \
"v_lshlrev_b32 v27, 16, v28\n" \
"v_mfma_f32_16x16x32_bf16 v[68:71], a[68:71], a[96:99], v[68:71]\n" \
"v_and_b32 v28, 0xffff0000, v28\n" \
"v_mul_f32 v29, v25, v27\n" \
"v_mfma_f32_16x16x32_bf16 v[72:75], a[64:67], a[100:103], v[72:75]\n" \
"ds_read_b64_tr_b16 a[32:33], v9 offset:0\n" \
"v_mul_f32 v58, v26, v28\n" \
"v_mfma_f32_16x16x32_bf16 v[76:79], a[68:71], a[100:103], v[76:79]\n" \
"v_accvgpr_read_b32 v36, a205\n" \
"v_lshlrev_b32 v30, 16, v36\n" \
"v_mfma_f32_16x16x32_bf16 v[80:83], a[72:75], a[96:99], v[80:83]\n" \
"ds_read_b64_tr_b16 a[34:35], v10 offset:0\n" \
"v_and_b32 v36, 0xffff0000, v36\n" \
"v_mfma_f32_16x16x32_bf16 v[84:87], a[76:79], a[96:99], v[84:87]\n" \
"v_mul_f32 v25, v33, v45\n" \
"v_mul_f32 v26, v39, v49\n" \
"v_mfma_f32_16x16x32_bf16 v[88:91], a[72:75], a[100:103], v[88:91]\n" \
"ds_read_b128 a[4:7], v5 offset:2048\n" \
"v_fmac_f32 v25, v34, v44\n" \
"v_mfma_f32_16x16x32_bf16 v[92:95], a[76:79], a[100:103], v[92:95]\n" \
"v_fmac_f32 v26, v40, v48\n" \
"v_fmac_f32 v25, v35, v43\n" \
"v_mfma_f32_16x16x32_bf16 v[96:99], a[80:83], a[96:99], v[96:99]\n" \
"ds_read_b64_tr_b16 a[36:37], v11 offset:0\n" \
"v_fmac_f32 v26, v41, v47\n" \
"v_mfma_f32_16x16x32_bf16 v[100:103], a[84:87], a[96:99], v[100:103]\n" \
"v_fmac_f32 v25, v30, v42\n" \
"v_fmac_f32 v26, v36, v46\n" \
"v_mfma_f32_16x16x32_bf16 v[104:107], a[80:83], a[100:103], v[104:107]\n" \
"ds_read_b64_tr_b16 a[38:39], v12 offset:0\n" \
"v_accvgpr_read_b32 v28, a221\n" \
"v_mfma_f32_16x16x32_bf16 v[108:111], a[84:87], a[100:103], v[108:111]\n" \
"v_lshlrev_b32 v27, 16, v28\n" \
"v_and_b32 v28, 0xffff0000, v28\n" \
"v_mfma_f32_16x16x32_bf16 v[112:115], a[88:91], a[96:99], v[112:115]\n" \
"ds_read_b128 a[8:11], v5 offset:4096\n" \
"v_mul_f32 v25, v25, v27\n" \
"v_mfma_f32_16x16x32_bf16 v[116:119], a[92:95], a[96:99], v[116:119]\n" \
"v_mul_f32 v26, v26, v28\n" \
"v_cvt_pk_bf16_f32 v52, v25, v29\n" \
"v_mfma_f32_16x16x32_bf16 v[120:123], a[88:91], a[100:103], v[120:123]\n" \
"ds_read_b64_tr_b16 a[40:41], v9 offset:512\n" \
"v_cvt_pk_bf16_f32 v56, v26, v58\n" \
"v_mfma_f32_16x16x32_bf16 v[124:127], a[92:95], a[100:103], v[124:127]\n" \
"v_accvgpr_read_b32 v37, a207\n" \
"v_lshlrev_b32 v31, 16, v37\n" \
"v_mfma_f32_16x16x32_bf16 v[128:131], a[64:67], a[104:107], v[128:131]\n" \
"ds_read_b64_tr_b16 a[42:43], v10 offset:512\n" \
"v_and_b32 v37, 0xffff0000, v37\n" \
"v_mfma_f32_16x16x32_bf16 v[132:135], a[68:71], a[104:107], v[132:135]\n" \
"v_mul_f32 v25, v34, v45\n" \
"v_mul_f32 v26, v40, v49\n" \
"v_mfma_f32_16x16x32_bf16 v[136:139], a[64:67], a[108:111], v[136:139]\n" \
"ds_read_b128 a[12:15], v5 offset:6144\n" \
"v_fmac_f32 v25, v35, v44\n" \
"v_mfma_f32_16x16x32_bf16 v[140:143], a[68:71], a[108:111], v[140:143]\n" \
"v_fmac_f32 v26, v41, v48\n" \
"v_fmac_f32 v25, v30, v43\n" \
"v_mfma_f32_16x16x32_bf16 v[144:147], a[72:75], a[104:107], v[144:147]\n" \
"ds_read_b64_tr_b16 a[44:45], v11 offset:512\n" \
"v_fmac_f32 v26, v36, v47\n" \
"v_mfma_f32_16x16x32_bf16 v[148:151], a[76:79], a[104:107], v[148:151]\n" \
"v_fmac_f32 v25, v31, v42\n" \
"v_fmac_f32 v26, v37, v46\n" \
"v_mfma_f32_16x16x32_bf16 v[152:155], a[72:75], a[108:111], v[152:155]\n" \
"ds_read_b64_tr_b16 a[46:47], v12 offset:512\n" \
"v_accvgpr_read_b32 v28, a223\n" \
"v_mfma_f32_16x16x32_bf16 v[156:159], a[76:79], a[108:111], v[156:159]\n" \
"v_lshlrev_b32 v27, 16, v28\n" \
"v_and_b32 v28, 0xffff0000, v28\n" \
"v_mfma_f32_16x16x32_bf16 v[160:163], a[80:83], a[104:107], v[160:163]\n" \
"ds_read_b128 a[16:19], v5 offset:8192\n" \
"v_mul_f32 v29, v25, v27\n" \
"v_mfma_f32_16x16x32_bf16 v[164:167], a[84:87], a[104:107], v[164:167]\n" \
"v_mul_f32 v58, v26, v28\n" \
"v_accvgpr_read_b32 v38, a209\n" \
"v_mfma_f32_16x16x32_bf16 v[168:171], a[80:83], a[108:111], v[168:171]\n" \
"ds_read_b64_tr_b16 a[48:49], v9 offset:1024\n" \
"v_lshlrev_b32 v32, 16, v38\n" \
"v_mfma_f32_16x16x32_bf16 v[172:175], a[84:87], a[108:111], v[172:175]\n" \
"v_and_b32 v38, 0xffff0000, v38\n" \
"v_mul_f32 v25, v35, v45\n" \
"v_mfma_f32_16x16x32_bf16 v[176:179], a[88:91], a[104:107], v[176:179]\n" \
"ds_read_b64_tr_b16 a[50:51], v10 offset:1024\n" \
"v_mul_f32 v26, v41, v49\n" \
"v_mfma_f32_16x16x32_bf16 v[180:183], a[92:95], a[104:107], v[180:183]\n" \
"v_fmac_f32 v25, v30, v44\n" \
"v_fmac_f32 v26, v36, v48\n" \
"v_mfma_f32_16x16x32_bf16 v[184:187], a[88:91], a[108:111], v[184:187]\n" \
"ds_read_b128 a[20:23], v5 offset:10240\n" \
"v_fmac_f32 v25, v31, v43\n" \
"v_mfma_f32_16x16x32_bf16 v[188:191], a[92:95], a[108:111], v[188:191]\n" \
"v_fmac_f32 v26, v37, v47\n" \
"v_fmac_f32 v25, v32, v42\n" \
"v_mfma_f32_16x16x32_bf16 v[192:195], a[64:67], a[112:115], v[192:195]\n" \
"ds_read_b64_tr_b16 a[52:53], v11 offset:1024\n" \
"v_fmac_f32 v26, v38, v46\n" \
"v_mfma_f32_16x16x32_bf16 v[196:199], a[68:71], a[112:115], v[196:199]\n" \
"v_accvgpr_read_b32 v28, a225\n" \
"v_lshlrev_b32 v27, 16, v28\n" \
"v_mfma_f32_16x16x32_bf16 v[200:203], a[64:67], a[116:119], v[200:203]\n" \
"ds_read_b64_tr_b16 a[54:55], v12 offset:1024\n" \
"v_and_b32 v28, 0xffff0000, v28\n" \
"v_mfma_f32_16x16x32_bf16 v[204:207], a[68:71], a[116:119], v[204:207]\n" \
"v_mul_f32 v25, v25, v27\n" \
"v_mul_f32 v26, v26, v28\n" \
"v_mfma_f32_16x16x32_bf16 v[208:211], a[72:75], a[112:115], v[208:211]\n" \
"ds_read_b128 a[24:27], v5 offset:12288\n" \
"v_cvt_pk_bf16_f32 v51, v25, v29\n" \
"v_mfma_f32_16x16x32_bf16 v[212:215], a[76:79], a[112:115], v[212:215]\n" \
"v_cvt_pk_bf16_f32 v55, v26, v58\n" \
"v_accvgpr_read_b32 v39, a211\n" \
"v_mfma_f32_16x16x32_bf16 v[216:219], a[72:75], a[116:119], v[216:219]\n" \
"ds_read_b64_tr_b16 a[56:57], v9 offset:1536\n" \
"v_lshlrev_b32 v33, 16, v39\n" \
"v_mfma_f32_16x16x32_bf16 v[220:223], a[76:79], a[116:119], v[220:223]\n" \
"v_and_b32 v39, 0xffff0000, v39\n" \
"v_mul_f32 v25, v30, v45\n" \
"v_mfma_f32_16x16x32_bf16 v[224:227], a[80:83], a[112:115], v[224:227]\n" \
"ds_read_b64_tr_b16 a[58:59], v10 offset:1536\n" \
"v_mul_f32 v26, v36, v49\n" \
"v_mfma_f32_16x16x32_bf16 v[228:231], a[84:87], a[112:115], v[228:231]\n" \
"v_fmac_f32 v25, v31, v44\n" \
"v_fmac_f32 v26, v37, v48\n" \
"v_mfma_f32_16x16x32_bf16 v[232:235], a[80:83], a[116:119], v[232:235]\n" \
"ds_read_b128 a[28:31], v5 offset:14336\n" \
"v_fmac_f32 v25, v32, v43\n" \
"v_mfma_f32_16x16x32_bf16 v[236:239], a[84:87], a[116:119], v[236:239]\n" \
"v_fmac_f32 v26, v38, v47\n" \
"v_fmac_f32 v25, v33, v42\n" \
"v_mfma_f32_16x16x32_bf16 v[240:243], a[88:91], a[112:115], v[240:243]\n" \
"ds_read_b64_tr_b16 a[60:61], v11 offset:1536\n" \
"v_fmac_f32 v26, v39, v46\n" \
"v_mfma_f32_16x16x32_bf16 v[244:247], a[92:95], a[112:115], v[244:247]\n" \
"v_accvgpr_read_b32 v28, a227\n" \
"v_lshlrev_b32 v27, 16, v28\n" \
"v_mfma_f32_16x16x32_bf16 v[248:251], a[88:91], a[116:119], v[248:251]\n" \
"ds_read_b64_tr_b16 a[62:63], v12 offset:1536\n" \
"v_and_b32 v28, 0xffff0000, v28\n" \
"v_mfma_f32_16x16x32_bf16 v[252:255], a[92:95], a[116:119], v[252:255]\n" \
"v_mul_f32 v29, v25, v27\n" \
"v_mul_f32 v58, v26, v28\n" \
"v_mfma_f32_16x16x32_bf16 a[128:131], a[64:67], a[120:123], a[128:131]\n" \
"v_accvgpr_read_b32 v40, a213\n" \
"v_lshlrev_b32 v34, 16, v40\n" \
"v_mfma_f32_16x16x32_bf16 a[132:135], a[68:71], a[120:123], a[132:135]\n" \
"v_and_b32 v40, 0xffff0000, v40\n" \
"v_mfma_f32_16x16x32_bf16 a[136:139], a[64:67], a[124:127], a[136:139]\n" \
"v_mul_f32 v25, v31, v45\n" \
"v_mul_f32 v26, v37, v49\n" \
"v_mfma_f32_16x16x32_bf16 a[140:143], a[68:71], a[124:127], a[140:143]\n" \
"v_fmac_f32 v25, v32, v44\n" \
"v_fmac_f32 v26, v38, v48\n" \
"v_mfma_f32_16x16x32_bf16 a[144:147], a[72:75], a[120:123], a[144:147]\n" \
"v_fmac_f32 v25, v33, v43\n" \
"v_mfma_f32_16x16x32_bf16 a[148:151], a[76:79], a[120:123], a[148:151]\n" \
"v_fmac_f32 v26, v39, v47\n" \
"v_fmac_f32 v25, v34, v42\n" \
"v_mfma_f32_16x16x32_bf16 a[152:155], a[72:75], a[124:127], a[152:155]\n" \
"v_fmac_f32 v26, v40, v46\n" \
"v_accvgpr_read_b32 v28, a229\n" \
"v_mfma_f32_16x16x32_bf16 a[156:159], a[76:79], a[124:127], a[156:159]\n" \
"s_waitcnt vmcnt(13) lgkmcnt(0)\n" \
"v_lshlrev_b32 v27, 16, v28\n" \
"v_mfma_f32_16x16x32_bf16 a[160:163], a[80:83], a[120:123], a[160:163]\n" \
"s_barrier\n" \
"v_and_b32 v28, 0xffff0000, v28\n" \
"v_mul_f32 v25, v25, v27\n" \
"v_mfma_f32_16x16x32_bf16 a[164:167], a[84:87], a[120:123], a[164:167]\n" \
"v_mul_f32 v26, v26, v28\n" \
"v_cvt_pk_bf16_f32 v50, v25, v29\n" \
"v_mfma_f32_16x16x32_bf16 a[168:171], a[80:83], a[124:127], a[168:171]\n" \
"s_mov_b32 s94, 79699968\n" \
"v_cvt_pk_bf16_f32 v54, v26, v58\n" \
"v_mfma_f32_16x16x32_bf16 a[172:175], a[84:87], a[124:127], a[172:175]\n" \
"buffer_store_dwordx4 v[50:53], v60, s[36:39], s94 offen offset:0 sc1\n" \
"v_mfma_f32_16x16x32_bf16 a[176:179], a[88:91], a[120:123], a[176:179]\n" \
"s_mov_b32 s95, 79704064\n" \
"v_mfma_f32_16x16x32_bf16 a[180:183], a[92:95], a[120:123], a[180:183]\n" \
"v_mfma_f32_16x16x32_bf16 a[184:187], a[88:91], a[124:127], a[184:187]\n" \
"v_mfma_f32_16x16x32_bf16 a[188:191], a[92:95], a[124:127], a[188:191]\n" \
"v_mfma_f32_16x16x32_bf16 v[64:67], a[0:3], a[32:35], v[64:67]\n" \
"s_mov_b32 s47, 2883584\n" \
"s_add_u32 m0, s22, 49152\n" \
"ds_read_b128 a[64:67], v6 offset:0\n" \
"v_mfma_f32_16x16x32_bf16 v[68:71], a[4:7], a[32:35], v[68:71]\n" \
"buffer_load_dwordx4 v21, s[32:35], s47 offen offset:0 lds\n" \
"v_mfma_f32_16x16x32_bf16 v[72:75], a[0:3], a[36:39], v[72:75]\n" \
"ds_read_b64_tr_b16 a[96:97], v9 offset:16384\n" \
"v_mfma_f32_16x16x32_bf16 v[76:79], a[4:7], a[36:39], v[76:79]\n" \
"v_mfma_f32_16x16x32_bf16 v[80:83], a[8:11], a[32:35], v[80:83]\n" \
"s_add_u32 m0, s22, 50048\n" \
"ds_read_b64_tr_b16 a[98:99], v10 offset:16384\n" \
"v_mfma_f32_16x16x32_bf16 v[84:87], a[12:15], a[32:35], v[84:87]\n" \
"buffer_load_dwordx4 v21, s[32:35], s47 offen offset:128 lds\n" \
"v_mfma_f32_16x16x32_bf16 v[88:91], a[8:11], a[36:39], v[88:91]\n" \
"ds_read_b128 a[68:71], v6 offset:2048\n" \
"v_mfma_f32_16x16x32_bf16 v[92:95], a[12:15], a[36:39], v[92:95]\n" \
"v_mfma_f32_16x16x32_bf16 v[96:99], a[16:19], a[32:35], v[96:99]\n" \
"s_add_u32 m0, s22, 50944\n" \
"ds_read_b64_tr_b16 a[100:101], v11 offset:16384\n" \
"v_mfma_f32_16x16x32_bf16 v[100:103], a[20:23], a[32:35], v[100:103]\n" \
"buffer_load_dwordx4 v21, s[32:35], s47 offen offset:256 lds\n" \
"v_mfma_f32_16x16x32_bf16 v[104:107], a[16:19], a[36:39], v[104:107]\n" \
"ds_read_b64_tr_b16 a[102:103], v12 offset:16384\n" \
"v_mfma_f32_16x16x32_bf16 v[108:111], a[20:23], a[36:39], v[108:111]\n" \
"buffer_store_dwordx4 v[54:57], v60, s[36:39], s95 offen offset:0 sc1\n" \
"v_mfma_f32_16x16x32_bf16 v[112:115], a[24:27], a[32:35], v[112:115]\n" \
"s_add_u32 m0, s22, 51840\n" \
"ds_read_b128 a[72:75], v6 offset:4096\n" \
"s_lshl_b32 s62, s8, 2\n" \
"v_mfma_f32_16x16x32_bf16 v[116:119], a[28:31], a[32:35], v[116:119]\n" \
"buffer_load_dwordx4 v21, s[32:35], s47 offen offset:384 lds\n" \
"v_mfma_f32_16x16x32_bf16 v[120:123], a[24:27], a[36:39], v[120:123]\n" \
"s_add_u32 s62, s62, %2\n" \
"ds_read_b64_tr_b16 a[104:105], v9 offset:16896\n" \
"s_lshl_b32 s63, s62, 12\n" \
"v_mfma_f32_16x16x32_bf16 v[124:127], a[28:31], a[36:39], v[124:127]\n" \
"s_add_u32 s68, s10, s63\ns_addc_u32 s69, s11, 0\n" \
"s_and_b32 s69, s69, 0xffff\n" \
"v_mfma_f32_16x16x32_bf16 v[128:131], a[0:3], a[40:43], v[128:131]\n" \
"s_mov_b32 s46, 768\n" \
"s_add_u32 m0, s50, 65536\n" \
"ds_read_b64_tr_b16 a[106:107], v10 offset:16896\n" \
"v_mfma_f32_16x16x32_bf16 v[132:135], a[4:7], a[40:43], v[132:135]\n" \
"buffer_load_dwordx4 v13, s[28:31], s46 offen lds\n" \
"v_mfma_f32_16x16x32_bf16 v[136:139], a[0:3], a[44:47], v[136:139]\n" \
"s_mov_b32 s70, 4096\n" \
"ds_read_b128 a[76:79], v6 offset:6144\n" \
"s_mov_b32 s71, 0x00020000\n" \
"v_mfma_f32_16x16x32_bf16 v[140:143], a[4:7], a[44:47], v[140:143]\n" \
"s_add_u32 s63, s63, 79691776\n" \
"s_add_u32 s72, s4, s63\ns_addc_u32 s73, s5, 0\n" \
"v_mfma_f32_16x16x32_bf16 v[144:147], a[8:11], a[40:43], v[144:147]\n" \
"s_add_u32 m0, s50, 69632\n" \
"ds_read_b64_tr_b16 a[108:109], v11 offset:16896\n" \
"s_and_b32 s73, s73, 0xffff\n" \
"v_mfma_f32_16x16x32_bf16 v[148:151], a[12:15], a[40:43], v[148:151]\n" \
"buffer_load_dwordx4 v14, s[28:31], s46 offen lds\n" \
"v_mfma_f32_16x16x32_bf16 v[152:155], a[8:11], a[44:47], v[152:155]\n" \
"s_mov_b32 s74, 4096\n" \
"ds_read_b64_tr_b16 a[110:111], v12 offset:16896\n" \
"s_mov_b32 s75, 0x00020000\n" \
"v_mfma_f32_16x16x32_bf16 v[156:159], a[12:15], a[44:47], v[156:159]\n" \
"v_add_u32 v23, -32, v4\n" \
"v_add_u32 v24, 992, v4\n" \
"v_mfma_f32_16x16x32_bf16 v[160:163], a[16:19], a[40:43], v[160:163]\n" \
"s_add_u32 m0, s50, 73728\n" \
"ds_read_b128 a[80:83], v6 offset:8192\n" \
"v_add_u32 v25, -16, v4\n" \
"v_mfma_f32_16x16x32_bf16 v[164:167], a[20:23], a[40:43], v[164:167]\n" \
"buffer_load_dwordx4 v15, s[28:31], s46 offen lds\n" \
"v_mfma_f32_16x16x32_bf16 v[168:171], a[16:19], a[44:47], v[168:171]\n" \
"ds_read_b64_tr_b16 a[112:113], v9 offset:17408\n" \
"v_add_u32 v26, -512, v4\n" \
"v_mfma_f32_16x16x32_bf16 v[172:175], a[20:23], a[44:47], v[172:175]\n" \
"s_lshr_b32 s63, %2, 1\n" \
"v_add_u32 v27, 512, v4\n" \
"s_lshl_b32 s63, s63, 16\n" \
"v_mfma_f32_16x16x32_bf16 v[176:179], a[24:27], a[40:43], v[176:179]\n" \
"s_add_u32 m0, s50, 77824\n" \
"ds_read_b64_tr_b16 a[114:115], v10 offset:17408\n" \
"s_lshl_b32 s56, %2, 14\n" \
"v_mfma_f32_16x16x32_bf16 v[180:183], a[28:31], a[40:43], v[180:183]\n" \
"buffer_load_dwordx4 v16, s[28:31], s46 offen lds\n" \
"v_mfma_f32_16x16x32_bf16 v[184:187], a[24:27], a[44:47], v[184:187]\n" \
"s_add_u32 s56, s56, s63\n" \
"ds_read_b128 a[84:87], v6 offset:10240\n" \
"v_add_u32 v55, s56, v4\n" \
"v_mfma_f32_16x16x32_bf16 v[188:191], a[28:31], a[44:47], v[188:191]\n" \
"s_lshl_b32 s62, s62, 1\n" \
"v_mov_b32 v28, 0\n" \
"v_mov_b32 v29, s62\n" \
"v_mfma_f32_16x16x32_bf16 v[192:195], a[0:3], a[48:51], v[192:195]\n" \
"s_mov_b32 s46, 768\n" \
"s_add_u32 m0, s50, 81920\n" \
"ds_read_b64_tr_b16 a[116:117], v11 offset:17408\n" \
"v_mfma_f32_16x16x32_bf16 v[196:199], a[4:7], a[48:51], v[196:199]\n" \
"buffer_load_dwordx4 v17, s[28:31], s46 offen lds\n" \
"v_mfma_f32_16x16x32_bf16 v[200:203], a[0:3], a[52:55], v[200:203]\n" \
"s_and_b32 s62, %1, 7\n" \
"ds_read_b64_tr_b16 a[118:119], v12 offset:17408\n" \
"v_lshlrev_b32 v56, 23, v4\n" \
"v_mfma_f32_16x16x32_bf16 v[204:207], a[4:7], a[52:55], v[204:207]\n" \
"s_lshl_b32 s62, s62, 8\n" \
"s_add_u32 s62, s62, 94380032\n" \
"v_bfe_u32 v57, v4, 4, 5\n" \
"v_mfma_f32_16x16x32_bf16 v[208:211], a[8:11], a[48:51], v[208:211]\n" \
"s_add_u32 m0, s50, 86016\n" \
"ds_read_b128 a[88:91], v6 offset:12288\n" \
"v_lshl_add_u32 v57, v57, 3, s62\n" \
"v_mfma_f32_16x16x32_bf16 v[212:215], a[12:15], a[48:51], v[212:215]\n" \
"buffer_load_dwordx4 v18, s[28:31], s46 offen lds\n" \
"v_mfma_f32_16x16x32_bf16 v[216:219], a[8:11], a[52:55], v[216:219]\n" \
"ds_read_b64_tr_b16 a[120:121], v9 offset:17920\n" \
"v_mfma_f32_16x16x32_bf16 v[220:223], a[12:15], a[52:55], v[220:223]\n" \
"v_mfma_f32_16x16x32_bf16 v[224:227], a[16:19], a[48:51], v[224:227]\n" \
"s_add_u32 m0, s50, 90112\n" \
"ds_read_b64_tr_b16 a[122:123], v10 offset:17920\n" \
"v_mfma_f32_16x16x32_bf16 v[228:231], a[20:23], a[48:51], v[228:231]\n" \
"buffer_load_dwordx4 v19, s[28:31], s46 offen lds\n" \
"v_mfma_f32_16x16x32_bf16 v[232:235], a[16:19], a[52:55], v[232:235]\n" \
"ds_read_b128 a[92:95], v6 offset:14336\n" \
"v_mfma_f32_16x16x32_bf16 v[236:239], a[20:23], a[52:55], v[236:239]\n" \
"v_mfma_f32_16x16x32_bf16 v[240:243], a[24:27], a[48:51], v[240:243]\n" \
"s_add_u32 m0, s50, 94208\n" \
"ds_read_b64_tr_b16 a[124:125], v11 offset:17920\n" \
"v_mfma_f32_16x16x32_bf16 v[244:247], a[28:31], a[48:51], v[244:247]\n" \
"buffer_load_dwordx4 v20, s[28:31], s46 offen lds\n" \
"v_mfma_f32_16x16x32_bf16 v[248:251], a[24:27], a[52:55], v[248:251]\n" \
"ds_read_b64_tr_b16 a[126:127], v12 offset:17920\n" \
"v_mfma_f32_16x16x32_bf16 v[252:255], a[28:31], a[52:55], v[252:255]\n" \
"v_mfma_f32_16x16x32_bf16 a[128:131], a[0:3], a[56:59], a[128:131]\n" \
"s_mov_b32 s47, 3145728\n" \
"s_add_u32 m0, s22, 0\n" \
"v_mfma_f32_16x16x32_bf16 a[132:135], a[4:7], a[56:59], a[132:135]\n" \
"buffer_load_dwordx4 v21, s[32:35], s47 offen offset:0 lds\n" \
"v_mfma_f32_16x16x32_bf16 a[136:139], a[0:3], a[60:63], a[136:139]\n" \
"v_mfma_f32_16x16x32_bf16 a[140:143], a[4:7], a[60:63], a[140:143]\n" \
"v_mfma_f32_16x16x32_bf16 a[144:147], a[8:11], a[56:59], a[144:147]\n" \
"s_add_u32 m0, s22, 896\n" \
"v_mfma_f32_16x16x32_bf16 a[148:151], a[12:15], a[56:59], a[148:151]\n" \
"buffer_load_dwordx4 v21, s[32:35], s47 offen offset:128 lds\n" \
"v_mfma_f32_16x16x32_bf16 a[152:155], a[8:11], a[60:63], a[152:155]\n" \
"v_mfma_f32_16x16x32_bf16 a[156:159], a[12:15], a[60:63], a[156:159]\n" \
"s_waitcnt vmcnt(18) lgkmcnt(0)\n" \
"v_mfma_f32_16x16x32_bf16 a[160:163], a[16:19], a[56:59], a[160:163]\n" \
"s_add_u32 m0, s22, 1792\n" \
"s_barrier\n" \
"v_mfma_f32_16x16x32_bf16 a[164:167], a[20:23], a[56:59], a[164:167]\n" \
"buffer_load_dwordx4 v21, s[32:35], s47 offen offset:256 lds\n" \
"v_mfma_f32_16x16x32_bf16 a[168:171], a[16:19], a[60:63], a[168:171]\n" \
"s_add_u32 s90, s64, s53\ns_addc_u32 s91, s65, 0\n" \
"v_mfma_f32_16x16x32_bf16 a[172:175], a[20:23], a[60:63], a[172:175]\n" \
"v_mfma_f32_16x16x32_bf16 a[176:179], a[24:27], a[56:59], a[176:179]\n" \
"s_add_u32 m0, s22, 2688\n" \
"v_mfma_f32_16x16x32_bf16 a[180:183], a[28:31], a[56:59], a[180:183]\n" \
"buffer_load_dwordx4 v21, s[32:35], s47 offen offset:384 lds\n" \
"v_mfma_f32_16x16x32_bf16 a[184:187], a[24:27], a[60:63], a[184:187]\n" \
"v_mfma_f32_16x16x32_bf16 a[188:191], a[28:31], a[60:63], a[188:191]\n" \
"v_mfma_f32_16x16x32_bf16 v[64:67], a[64:67], a[96:99], v[64:67]\n" \
"ds_read_b128 a[0:3], v5 offset:32768\n" \
"v_mfma_f32_16x16x32_bf16 v[68:71], a[68:71], a[96:99], v[68:71]\n" \
"global_load_dwordx4 a[240:243], v4, s[90:91] offset:0\n" \
"v_mfma_f32_16x16x32_bf16 v[72:75], a[64:67], a[100:103], v[72:75]\n" \
"ds_read_b64_tr_b16 a[32:33], v9 offset:32768\n" \
"v_mfma_f32_16x16x32_bf16 v[76:79], a[68:71], a[100:103], v[76:79]\n" \
"v_mfma_f32_16x16x32_bf16 v[80:83], a[72:75], a[96:99], v[80:83]\n" \
"ds_read_b64_tr_b16 a[34:35], v10 offset:32768\n" \
"v_mfma_f32_16x16x32_bf16 v[84:87], a[76:79], a[96:99], v[84:87]\n" \
"global_load_dwordx4 a[244:247], v4, s[90:91] offset:1024\n" \
"v_mfma_f32_16x16x32_bf16 v[88:91], a[72:75], a[100:103], v[88:91]\n" \
"ds_read_b128 a[4:7], v5 offset:34816\n" \
"v_mfma_f32_16x16x32_bf16 v[92:95], a[76:79], a[100:103], v[92:95]\n" \
"v_mfma_f32_16x16x32_bf16 v[96:99], a[80:83], a[96:99], v[96:99]\n" \
"ds_read_b64_tr_b16 a[36:37], v11 offset:32768\n" \
"v_mfma_f32_16x16x32_bf16 v[100:103], a[84:87], a[96:99], v[100:103]\n" \
"global_load_dwordx4 a[248:251], v4, s[90:91] offset:2048\n" \
"v_mfma_f32_16x16x32_bf16 v[104:107], a[80:83], a[100:103], v[104:107]\n" \
"ds_read_b64_tr_b16 a[38:39], v12 offset:32768\n" \
"v_mfma_f32_16x16x32_bf16 v[108:111], a[84:87], a[100:103], v[108:111]\n" \
"v_mfma_f32_16x16x32_bf16 v[112:115], a[88:91], a[96:99], v[112:115]\n" \
"ds_read_b128 a[8:11], v5 offset:36864\n" \
"v_mfma_f32_16x16x32_bf16 v[116:119], a[92:95], a[96:99], v[116:119]\n" \
"global_load_dwordx4 a[252:255], v4, s[90:91] offset:3072\n" \
"v_mfma_f32_16x16x32_bf16 v[120:123], a[88:91], a[100:103], v[120:123]\n" \
"ds_read_b64_tr_b16 a[40:41], v9 offset:33280\n" \
"v_mfma_f32_16x16x32_bf16 v[124:127], a[92:95], a[100:103], v[124:127]\n" \
"v_mfma_f32_16x16x32_bf16 v[128:131], a[64:67], a[104:107], v[128:131]\n" \
"ds_read_b64_tr_b16 a[42:43], v10 offset:33280\n" \
"v_mfma_f32_16x16x32_bf16 v[132:135], a[68:71], a[104:107], v[132:135]\n" \
"v_mfma_f32_16x16x32_bf16 v[136:139], a[64:67], a[108:111], v[136:139]\n" \
"ds_read_b128 a[12:15], v5 offset:38912\n" \
"v_mfma_f32_16x16x32_bf16 v[140:143], a[68:71], a[108:111], v[140:143]\n" \
"v_mfma_f32_16x16x32_bf16 v[144:147], a[72:75], a[104:107], v[144:147]\n" \
"ds_read_b64_tr_b16 a[44:45], v11 offset:33280\n" \
"v_mfma_f32_16x16x32_bf16 v[148:151], a[76:79], a[104:107], v[148:151]\n" \
"v_mfma_f32_16x16x32_bf16 v[152:155], a[72:75], a[108:111], v[152:155]\n" \
"ds_read_b64_tr_b16 a[46:47], v12 offset:33280\n" \
"v_mfma_f32_16x16x32_bf16 v[156:159], a[76:79], a[108:111], v[156:159]\n" \
"v_mfma_f32_16x16x32_bf16 v[160:163], a[80:83], a[104:107], v[160:163]\n" \
"ds_read_b128 a[16:19], v5 offset:40960\n" \
"v_mfma_f32_16x16x32_bf16 v[164:167], a[84:87], a[104:107], v[164:167]\n" \
"v_mfma_f32_16x16x32_bf16 v[168:171], a[80:83], a[108:111], v[168:171]\n" \
"ds_read_b64_tr_b16 a[48:49], v9 offset:33792\n" \
"v_mfma_f32_16x16x32_bf16 v[172:175], a[84:87], a[108:111], v[172:175]\n" \
"v_mfma_f32_16x16x32_bf16 v[176:179], a[88:91], a[104:107], v[176:179]\n" \
"ds_read_b64_tr_b16 a[50:51], v10 offset:33792\n" \
"v_mfma_f32_16x16x32_bf16 v[180:183], a[92:95], a[104:107], v[180:183]\n" \
"v_mfma_f32_16x16x32_bf16 v[184:187], a[88:91], a[108:111], v[184:187]\n" \
"ds_read_b128 a[20:23], v5 offset:43008\n" \
"v_mfma_f32_16x16x32_bf16 v[188:191], a[92:95], a[108:111], v[188:191]\n" \
"v_mfma_f32_16x16x32_bf16 v[192:195], a[64:67], a[112:115], v[192:195]\n" \
"ds_read_b64_tr_b16 a[52:53], v11 offset:33792\n" \
"v_mfma_f32_16x16x32_bf16 v[196:199], a[68:71], a[112:115], v[196:199]\n" \
"v_mfma_f32_16x16x32_bf16 v[200:203], a[64:67], a[116:119], v[200:203]\n" \
"ds_read_b64_tr_b16 a[54:55], v12 offset:33792\n" \
"v_mfma_f32_16x16x32_bf16 v[204:207], a[68:71], a[116:119], v[204:207]\n" \
"v_mfma_f32_16x16x32_bf16 v[208:211], a[72:75], a[112:115], v[208:211]\n" \
"ds_read_b128 a[24:27], v5 offset:45056\n" \
"v_mfma_f32_16x16x32_bf16 v[212:215], a[76:79], a[112:115], v[212:215]\n" \
"v_mfma_f32_16x16x32_bf16 v[216:219], a[72:75], a[116:119], v[216:219]\n" \
"ds_read_b64_tr_b16 a[56:57], v9 offset:34304\n" \
"v_mfma_f32_16x16x32_bf16 v[220:223], a[76:79], a[116:119], v[220:223]\n" \
"global_load_dwordx4 a[192:195], v4, s[86:87] offset:0\n" \
"v_mfma_f32_16x16x32_bf16 v[224:227], a[80:83], a[112:115], v[224:227]\n" \
"ds_read_b64_tr_b16 a[58:59], v10 offset:34304\n" \
"v_mfma_f32_16x16x32_bf16 v[228:231], a[84:87], a[112:115], v[228:231]\n" \
"v_mfma_f32_16x16x32_bf16 v[232:235], a[80:83], a[116:119], v[232:235]\n" \
"ds_read_b128 a[28:31], v5 offset:47104\n" \
"v_mfma_f32_16x16x32_bf16 v[236:239], a[84:87], a[116:119], v[236:239]\n" \
"global_load_dwordx4 a[196:199], v4, s[86:87] offset:1024\n" \
"v_mfma_f32_16x16x32_bf16 v[240:243], a[88:91], a[112:115], v[240:243]\n" \
"ds_read_b64_tr_b16 a[60:61], v11 offset:34304\n" \
"v_mfma_f32_16x16x32_bf16 v[244:247], a[92:95], a[112:115], v[244:247]\n" \
"v_mfma_f32_16x16x32_bf16 v[248:251], a[88:91], a[116:119], v[248:251]\n" \
"ds_read_b64_tr_b16 a[62:63], v12 offset:34304\n" \
"v_mfma_f32_16x16x32_bf16 v[252:255], a[92:95], a[116:119], v[252:255]\n" \
"v_mfma_f32_16x16x32_bf16 a[128:131], a[64:67], a[120:123], a[128:131]\n" \
"v_mfma_f32_16x16x32_bf16 a[132:135], a[68:71], a[120:123], a[132:135]\n" \
"v_mfma_f32_16x16x32_bf16 a[136:139], a[64:67], a[124:127], a[136:139]\n" \
"v_mfma_f32_16x16x32_bf16 a[140:143], a[68:71], a[124:127], a[140:143]\n" \
"v_mfma_f32_16x16x32_bf16 a[144:147], a[72:75], a[120:123], a[144:147]\n" \
"v_mov_b32 v58, s14\n" \
"v_mfma_f32_16x16x32_bf16 a[148:151], a[76:79], a[120:123], a[148:151]\n" \
"v_mov_b32 v59, s15\n" \
"v_mfma_f32_16x16x32_bf16 a[152:155], a[72:75], a[124:127], a[152:155]\n" \
"v_mfma_f32_16x16x32_bf16 a[156:159], a[76:79], a[124:127], a[156:159]\n" \
"s_waitcnt vmcnt(18) lgkmcnt(0)\n" \
"v_mfma_f32_16x16x32_bf16 a[160:163], a[80:83], a[120:123], a[160:163]\n" \
"s_barrier\n" \
"v_mfma_f32_16x16x32_bf16 a[164:167], a[84:87], a[120:123], a[164:167]\n" \
"v_mfma_f32_16x16x32_bf16 a[168:171], a[80:83], a[124:127], a[168:171]\n" \
"v_mfma_f32_16x16x32_bf16 a[172:175], a[84:87], a[124:127], a[172:175]\n" \
"v_mfma_f32_16x16x32_bf16 a[176:179], a[88:91], a[120:123], a[176:179]\n" \
"v_mfma_f32_16x16x32_bf16 a[180:183], a[92:95], a[120:123], a[180:183]\n" \
"v_mfma_f32_16x16x32_bf16 a[184:187], a[88:91], a[124:127], a[184:187]\n" \
"v_mfma_f32_16x16x32_bf16 a[188:191], a[92:95], a[124:127], a[188:191]\n" \
"buffer_store_dwordx2 v[58:59], v56, s[36:39], s23 offen sc1\n" \
"v_mfma_f32_16x16x32_bf16 v[64:67], a[0:3], a[32:35], v[64:67]\n" \
"s_mov_b32 s47, 3407872\n" \
"s_add_u32 m0, s22, 16384\n" \
"ds_read_b128 a[64:67], v6 offset:32768\n" \
"v_mfma_f32_16x16x32_bf16 v[68:71], a[4:7], a[32:35], v[68:71]\n" \
"buffer_load_dwordx4 v21, s[32:35], s47 offen offset:0 lds\n" \
"v_mfma_f32_16x16x32_bf16 v[72:75], a[0:3], a[36:39], v[72:75]\n" \
"ds_read_b64_tr_b16 a[96:97], v9 offset:49152\n" \
"v_mfma_f32_16x16x32_bf16 v[76:79], a[4:7], a[36:39], v[76:79]\n" \
"v_mfma_f32_16x16x32_bf16 v[80:83], a[8:11], a[32:35], v[80:83]\n" \
"s_add_u32 m0, s22, 17280\n" \
"ds_read_b64_tr_b16 a[98:99], v10 offset:49152\n" \
"v_mfma_f32_16x16x32_bf16 v[84:87], a[12:15], a[32:35], v[84:87]\n" \
"buffer_load_dwordx4 v21, s[32:35], s47 offen offset:128 lds\n" \
"v_mfma_f32_16x16x32_bf16 v[88:91], a[8:11], a[36:39], v[88:91]\n" \
"ds_read_b128 a[68:71], v6 offset:34816\n" \
"v_mfma_f32_16x16x32_bf16 v[92:95], a[12:15], a[36:39], v[92:95]\n" \
"v_mfma_f32_16x16x32_bf16 v[96:99], a[16:19], a[32:35], v[96:99]\n" \
"s_add_u32 m0, s22, 18176\n" \
"ds_read_b64_tr_b16 a[100:101], v11 offset:49152\n" \
"v_mfma_f32_16x16x32_bf16 v[100:103], a[20:23], a[32:35], v[100:103]\n" \
"buffer_load_dwordx4 v21, s[32:35], s47 offen offset:256 lds\n" \
"v_mfma_f32_16x16x32_bf16 v[104:107], a[16:19], a[36:39], v[104:107]\n" \
"ds_read_b64_tr_b16 a[102:103], v12 offset:49152\n" \
"v_mfma_f32_16x16x32_bf16 v[108:111], a[20:23], a[36:39], v[108:111]\n" \
"v_mfma_f32_16x16x32_bf16 v[112:115], a[24:27], a[32:35], v[112:115]\n" \
"s_add_u32 m0, s22, 19072\n" \
"ds_read_b128 a[72:75], v6 offset:36864\n" \
"v_mfma_f32_16x16x32_bf16 v[116:119], a[28:31], a[32:35], v[116:119]\n" \
"buffer_load_dwordx4 v21, s[32:35], s47 offen offset:384 lds\n" \
"v_mfma_f32_16x16x32_bf16 v[120:123], a[24:27], a[36:39], v[120:123]\n" \
"ds_read_b64_tr_b16 a[104:105], v9 offset:49664\n" \
"v_mfma_f32_16x16x32_bf16 v[124:127], a[28:31], a[36:39], v[124:127]\n" \
"v_mfma_f32_16x16x32_bf16 v[128:131], a[0:3], a[40:43], v[128:131]\n" \
"s_mov_b32 s46, 896\n" \
"s_add_u32 m0, s50, 0\n" \
"ds_read_b64_tr_b16 a[106:107], v10 offset:49664\n" \
"v_mfma_f32_16x16x32_bf16 v[132:135], a[4:7], a[40:43], v[132:135]\n" \
"buffer_load_dwordx4 v13, s[28:31], s46 offen lds\n" \
"v_mfma_f32_16x16x32_bf16 v[136:139], a[0:3], a[44:47], v[136:139]\n" \
"ds_read_b128 a[76:79], v6 offset:38912\n" \
"v_mfma_f32_16x16x32_bf16 v[140:143], a[4:7], a[44:47], v[140:143]\n" \
"v_mfma_f32_16x16x32_bf16 v[144:147], a[8:11], a[40:43], v[144:147]\n" \
"s_add_u32 m0, s50, 4096\n" \
"ds_read_b64_tr_b16 a[108:109], v11 offset:49664\n" \
"v_mfma_f32_16x16x32_bf16 v[148:151], a[12:15], a[40:43], v[148:151]\n" \
"buffer_load_dwordx4 v14, s[28:31], s46 offen lds\n" \
"v_mfma_f32_16x16x32_bf16 v[152:155], a[8:11], a[44:47], v[152:155]\n" \
"ds_read_b64_tr_b16 a[110:111], v12 offset:49664\n" \
"v_mfma_f32_16x16x32_bf16 v[156:159], a[12:15], a[44:47], v[156:159]\n" \
"v_mfma_f32_16x16x32_bf16 v[160:163], a[16:19], a[40:43], v[160:163]\n" \
"s_add_u32 m0, s50, 8192\n" \
"ds_read_b128 a[80:83], v6 offset:40960\n" \
"v_mfma_f32_16x16x32_bf16 v[164:167], a[20:23], a[40:43], v[164:167]\n" \
"buffer_load_dwordx4 v15, s[28:31], s46 offen lds\n" \
"v_mfma_f32_16x16x32_bf16 v[168:171], a[16:19], a[44:47], v[168:171]\n" \
"ds_read_b64_tr_b16 a[112:113], v9 offset:50176\n" \
"v_mfma_f32_16x16x32_bf16 v[172:175], a[20:23], a[44:47], v[172:175]\n" \
"v_mfma_f32_16x16x32_bf16 v[176:179], a[24:27], a[40:43], v[176:179]\n" \
"s_add_u32 m0, s50, 12288\n" \
"ds_read_b64_tr_b16 a[114:115], v10 offset:50176\n" \
"v_mfma_f32_16x16x32_bf16 v[180:183], a[28:31], a[40:43], v[180:183]\n" \
"buffer_load_dwordx4 v16, s[28:31], s46 offen lds\n" \
"v_mfma_f32_16x16x32_bf16 v[184:187], a[24:27], a[44:47], v[184:187]\n" \
"ds_read_b128 a[84:87], v6 offset:43008\n" \
"v_mfma_f32_16x16x32_bf16 v[188:191], a[28:31], a[44:47], v[188:191]\n" \
"v_mfma_f32_16x16x32_bf16 v[192:195], a[0:3], a[48:51], v[192:195]\n" \
"s_mov_b32 s46, 896\n" \
"s_add_u32 m0, s50, 16384\n" \
"ds_read_b64_tr_b16 a[116:117], v11 offset:50176\n" \
"v_mfma_f32_16x16x32_bf16 v[196:199], a[4:7], a[48:51], v[196:199]\n" \
"buffer_load_dwordx4 v17, s[28:31], s46 offen lds\n" \
"v_mfma_f32_16x16x32_bf16 v[200:203], a[0:3], a[52:55], v[200:203]\n" \
"ds_read_b64_tr_b16 a[118:119], v12 offset:50176\n" \
"v_mfma_f32_16x16x32_bf16 v[204:207], a[4:7], a[52:55], v[204:207]\n" \
"v_mfma_f32_16x16x32_bf16 v[208:211], a[8:11], a[48:51], v[208:211]\n" \
"s_add_u32 m0, s50, 20480\n" \
"ds_read_b128 a[88:91], v6 offset:45056\n" \
"v_mfma_f32_16x16x32_bf16 v[212:215], a[12:15], a[48:51], v[212:215]\n" \
"buffer_load_dwordx4 v18, s[28:31], s46 offen lds\n" \
"v_mfma_f32_16x16x32_bf16 v[216:219], a[8:11], a[52:55], v[216:219]\n" \
"LH6_%=:\ns_cbranch_execz LH10_%=\n" \
"ds_read_b64_tr_b16 a[120:121], v9 offset:50688\n" \
"v_mfma_f32_16x16x32_bf16 v[220:223], a[12:15], a[52:55], v[220:223]\n" \
"v_mfma_f32_16x16x32_bf16 v[224:227], a[16:19], a[48:51], v[224:227]\n" \
"s_add_u32 m0, s50, 24576\n" \
"ds_read_b64_tr_b16 a[122:123], v10 offset:50688\n" \
"v_mfma_f32_16x16x32_bf16 v[228:231], a[20:23], a[48:51], v[228:231]\n" \
"buffer_load_dwordx4 v19, s[28:31], s46 offen lds\n" \
"v_mfma_f32_16x16x32_bf16 v[232:235], a[16:19], a[52:55], v[232:235]\n" \
"ds_read_b128 a[92:95], v6 offset:47104\n" \
"v_mfma_f32_16x16x32_bf16 v[236:239], a[20:23], a[52:55], v[236:239]\n" \
"v_mfma_f32_16x16x32_bf16 v[240:243], a[24:27], a[48:51], v[240:243]\n" \
"s_add_u32 m0, s50, 28672\n" \
"ds_read_b64_tr_b16 a[124:125], v11 offset:50688\n" \
"v_mfma_f32_16x16x32_bf16 v[244:247], a[28:31], a[48:51], v[244:247]\n" \
"buffer_load_dwordx4 v20, s[28:31], s46 offen lds\n" \
"v_mfma_f32_16x16x32_bf16 v[248:251], a[24:27], a[52:55], v[248:251]\n" \
"ds_read_b64_tr_b16 a[126:127], v12 offset:50688\n" \
"v_mfma_f32_16x16x32_bf16 v[252:255], a[28:31], a[52:55], v[252:255]\n" \
"v_mfma_f32_16x16x32_bf16 a[128:131], a[0:3], a[56:59], a[128:131]\n" \
"s_mov_b32 s47, 3670016\n" \
"s_add_u32 m0, s22, 32768\n" \
"v_mfma_f32_16x16x32_bf16 a[132:135], a[4:7], a[56:59], a[132:135]\n" \
"buffer_load_dwordx4 v21, s[32:35], s47 offen offset:0 lds\n" \
"v_mfma_f32_16x16x32_bf16 a[136:139], a[0:3], a[60:63], a[136:139]\n" \
"v_mfma_f32_16x16x32_bf16 a[140:143], a[4:7], a[60:63], a[140:143]\n" \
"v_mfma_f32_16x16x32_bf16 a[144:147], a[8:11], a[56:59], a[144:147]\n" \
"s_add_u32 m0, s22, 33664\n" \
"v_mfma_f32_16x16x32_bf16 a[148:151], a[12:15], a[56:59], a[148:151]\n" \
"buffer_load_dwordx4 v21, s[32:35], s47 offen offset:128 lds\n" \
"v_mfma_f32_16x16x32_bf16 a[152:155], a[8:11], a[60:63], a[152:155]\n" \
"v_mfma_f32_16x16x32_bf16 a[156:159], a[12:15], a[60:63], a[156:159]\n" \
"s_waitcnt vmcnt(21) lgkmcnt(0)\n" \
"v_mfma_f32_16x16x32_bf16 a[160:163], a[16:19], a[56:59], a[160:163]\n" \
"s_add_u32 m0, s22, 34560\n" \
"s_barrier\n" \
"v_mfma_f32_16x16x32_bf16 a[164:167], a[20:23], a[56:59], a[164:167]\n" \
"buffer_load_dwordx4 v21, s[32:35], s47 offen offset:256 lds\n" \
"v_mfma_f32_16x16x32_bf16 a[168:171], a[16:19], a[60:63], a[168:171]\n" \
"v_mfma_f32_16x16x32_bf16 a[172:175], a[20:23], a[60:63], a[172:175]\n" \
"v_mfma_f32_16x16x32_bf16 a[176:179], a[24:27], a[56:59], a[176:179]\n" \
"s_add_u32 m0, s22, 35456\n" \
"v_mfma_f32_16x16x32_bf16 a[180:183], a[28:31], a[56:59], a[180:183]\n" \
"buffer_load_dwordx4 v21, s[32:35], s47 offen offset:384 lds\n" \
"v_mfma_f32_16x16x32_bf16 a[184:187], a[24:27], a[60:63], a[184:187]\n" \
"v_mfma_f32_16x16x32_bf16 a[188:191], a[28:31], a[60:63], a[188:191]\n" \
"v_mfma_f32_16x16x32_bf16 v[64:67], a[64:67], a[96:99], v[64:67]\n" \
"ds_read_b128 a[0:3], v7 offset:0\n" \
"v_mfma_f32_16x16x32_bf16 v[68:71], a[68:71], a[96:99], v[68:71]\n" \
"buffer_load_dwordx4 v[30:33], v23, s[68:71], 0 offen offset:0 nt\n" \
"v_mfma_f32_16x16x32_bf16 v[72:75], a[64:67], a[100:103], v[72:75]\n" \
"s_mov_b64 s[28:29], s[64:65]\n" \
"ds_read_b64_tr_b16 a[32:33], v9 offset:0\n" \
"v_mfma_f32_16x16x32_bf16 v[76:79], a[68:71], a[100:103], v[76:79]\n" \
"v_mfma_f32_16x16x32_bf16 v[80:83], a[72:75], a[96:99], v[80:83]\n" \
"ds_read_b64_tr_b16 a[34:35], v10 offset:0\n" \
"v_mfma_f32_16x16x32_bf16 v[84:87], a[76:79], a[96:99], v[84:87]\n" \
"buffer_load_dwordx4 v[34:37], v24, s[68:71], 0 offen offset:0 nt\n" \
"v_mfma_f32_16x16x32_bf16 v[88:91], a[72:75], a[100:103], v[88:91]\n" \
"s_and_b32 s29, s29, 0xffff\n" \
"ds_read_b128 a[4:7], v7 offset:2048\n" \
"v_mfma_f32_16x16x32_bf16 v[92:95], a[76:79], a[100:103], v[92:95]\n" \
"v_mfma_f32_16x16x32_bf16 v[96:99], a[80:83], a[96:99], v[96:99]\n" \
"ds_read_b64_tr_b16 a[36:37], v11 offset:0\n" \
"v_mfma_f32_16x16x32_bf16 v[100:103], a[84:87], a[96:99], v[100:103]\n" \
"buffer_load_dwordx4 v[38:41], v24, s[68:71], 0 offen offset:1024 nt\n" \
"v_mfma_f32_16x16x32_bf16 v[104:107], a[80:83], a[100:103], v[104:107]\n" \
"s_mov_b32 s30, 4194304\n" \
"ds_read_b64_tr_b16 a[38:39], v12 offset:0\n" \
"v_mfma_f32_16x16x32_bf16 v[108:111], a[84:87], a[100:103], v[108:111]\n" \
"v_mfma_f32_16x16x32_bf16 v[112:115], a[88:91], a[96:99], v[112:115]\n" \
"ds_read_b128 a[8:11], v7 offset:4096\n" \
"v_mfma_f32_16x16x32_bf16 v[116:119], a[92:95], a[96:99], v[116:119]\n" \
"buffer_load_dwordx4 v[42:45], v24, s[68:71], 0 offen offset:2048 nt\n" \
"v_mfma_f32_16x16x32_bf16 v[120:123], a[88:91], a[100:103], v[120:123]\n" \
"s_lshl_b32 s60, s7, 18\n" \
"ds_read_b64_tr_b16 a[40:41], v9 offset:512\n" \
"v_mfma_f32_16x16x32_bf16 v[124:127], a[92:95], a[100:103], v[124:127]\n" \
"v_mfma_f32_16x16x32_bf16 v[128:131], a[64:67], a[104:107], v[128:131]\n" \
"ds_read_b64_tr_b16 a[42:43], v10 offset:512\n" \
"v_mfma_f32_16x16x32_bf16 v[132:135], a[68:71], a[104:107], v[132:135]\n" \
"buffer_load_dwordx4 v[46:49], v24, s[68:71], 0 offen offset:3072 nt\n" \
"v_mfma_f32_16x16x32_bf16 v[136:139], a[64:67], a[108:111], v[136:139]\n" \
"s_sub_u32 s61, 3932160, s60\n" \
"ds_read_b128 a[12:15], v7 offset:6144\n" \
"v_mfma_f32_16x16x32_bf16 v[140:143], a[68:71], a[108:111], v[140:143]\n" \
"v_mfma_f32_16x16x32_bf16 v[144:147], a[72:75], a[104:107], v[144:147]\n" \
"ds_read_b64_tr_b16 a[44:45], v11 offset:512\n" \
"v_mfma_f32_16x16x32_bf16 v[148:151], a[76:79], a[104:107], v[148:151]\n" \
"buffer_load_dword v50, v25, s[68:71], 0 offen offset:0\n" \
"v_mfma_f32_16x16x32_bf16 v[152:155], a[72:75], a[108:111], v[152:155]\n" \
"ds_read_b64_tr_b16 a[46:47], v12 offset:512\n" \
"v_add_u32 v13, s60, v63\n" \
"v_mfma_f32_16x16x32_bf16 v[156:159], a[76:79], a[108:111], v[156:159]\n" \
"v_mfma_f32_16x16x32_bf16 v[160:163], a[80:83], a[104:107], v[160:163]\n" \
"ds_read_b128 a[16:19], v7 offset:8192\n" \
"v_mfma_f32_16x16x32_bf16 v[164:167], a[84:87], a[104:107], v[164:167]\n" \
"buffer_load_dword v51, v24, s[68:71], 0 offen offset:16\n" \
"v_mfma_f32_16x16x32_bf16 v[168:171], a[80:83], a[108:111], v[168:171]\n" \
"ds_read_b64_tr_b16 a[48:49], v9 offset:1024\n" \
"v_add_u32 v17, s61, v63\n" \
"v_mfma_f32_16x16x32_bf16 v[172:175], a[84:87], a[108:111], v[172:175]\n" \
"v_mfma_f32_16x16x32_bf16 v[176:179], a[88:91], a[104:107], v[176:179]\n" \
"ds_read_b64_tr_b16 a[50:51], v10 offset:1024\n" \
"v_mfma_f32_16x16x32_bf16 v[180:183], a[92:95], a[104:107], v[180:183]\n" \
"buffer_load_dword v52, v24, s[68:71], 0 offen offset:1040\n" \
"v_mfma_f32_16x16x32_bf16 v[184:187], a[88:91], a[108:111], v[184:187]\n" \
"ds_read_b128 a[20:23], v7 offset:10240\n" \
"v_add_u32 v14, 0x10000, v13\n" \
"v_mfma_f32_16x16x32_bf16 v[188:191], a[92:95], a[108:111], v[188:191]\n" \
"v_mfma_f32_16x16x32_bf16 v[192:195], a[64:67], a[112:115], v[192:195]\n" \
"ds_read_b64_tr_b16 a[52:53], v11 offset:1024\n" \
"v_mfma_f32_16x16x32_bf16 v[196:199], a[68:71], a[112:115], v[196:199]\n" \
"buffer_load_dword v53, v24, s[68:71], 0 offen offset:2064\n" \
"v_mfma_f32_16x16x32_bf16 v[200:203], a[64:67], a[116:119], v[200:203]\n" \
"ds_read_b64_tr_b16 a[54:55], v12 offset:1024\n" \
"v_add_u32 v18, 0x10000, v17\n" \
"v_mfma_f32_16x16x32_bf16 v[204:207], a[68:71], a[116:119], v[204:207]\n" \
"v_mfma_f32_16x16x32_bf16 v[208:211], a[72:75], a[112:115], v[208:211]\n" \
"ds_read_b128 a[24:27], v7 offset:12288\n" \
"v_mfma_f32_16x16x32_bf16 v[212:215], a[76:79], a[112:115], v[212:215]\n" \
"buffer_load_dword v54, v24, s[68:71], 0 offen offset:3088\n" \
"v_mfma_f32_16x16x32_bf16 v[216:219], a[72:75], a[116:119], v[216:219]\n" \
"ds_read_b64_tr_b16 a[56:57], v9 offset:1536\n" \
"v_add_u32 v15, 0x10000, v14\n" \
"v_mfma_f32_16x16x32_bf16 v[220:223], a[76:79], a[116:119], v[220:223]\n" \
"v_mfma_f32_16x16x32_bf16 v[224:227], a[80:83], a[112:115], v[224:227]\n" \
"ds_read_b64_tr_b16 a[58:59], v10 offset:1536\n" \
"v_mfma_f32_16x16x32_bf16 v[228:231], a[84:87], a[112:115], v[228:231]\n" \
"global_load_short_d16_hi v28, v29, s[12:13]\n" \
"v_mfma_f32_16x16x32_bf16 v[232:235], a[80:83], a[116:119], v[232:235]\n" \
"ds_read_b128 a[28:31], v7 offset:14336\n" \
"v_add_u32 v19, 0x10000, v18\n" \
"v_mfma_f32_16x16x32_bf16 v[236:239], a[84:87], a[116:119], v[236:239]\n" \
"v_mfma_f32_16x16x32_bf16 v[240:243], a[88:91], a[112:115], v[240:243]\n" \
"ds_read_b64_tr_b16 a[60:61], v11 offset:1536\n" \
"v_mfma_f32_16x16x32_bf16 v[244:247], a[92:95], a[112:115], v[244:247]\n" \
"v_mfma_f32_16x16x32_bf16 v[248:251], a[88:91], a[116:119], v[248:251]\n" \
"ds_read_b64_tr_b16 a[62:63], v12 offset:1536\n" \
"v_add_u32 v16, 0x10000, v15\n" \
"v_mfma_f32_16x16x32_bf16 v[252:255], a[92:95], a[116:119], v[252:255]\n" \
"v_mfma_f32_16x16x32_bf16 a[128:131], a[64:67], a[120:123], a[128:131]\n" \
"v_mfma_f32_16x16x32_bf16 a[132:135], a[68:71], a[120:123], a[132:135]\n" \
"v_mfma_f32_16x16x32_bf16 a[136:139], a[64:67], a[124:127], a[136:139]\n" \
"v_add_u32 v20, 0x10000, v19\n" \
"v_mfma_f32_16x16x32_bf16 a[140:143], a[68:71], a[124:127], a[140:143]\n" \
"v_mfma_f32_16x16x32_bf16 a[144:147], a[72:75], a[120:123], a[144:147]\n" \
"v_mfma_f32_16x16x32_bf16 a[148:151], a[76:79], a[120:123], a[148:151]\n" \
"v_mfma_f32_16x16x32_bf16 a[152:155], a[72:75], a[124:127], a[152:155]\n" \
"v_mfma_f32_16x16x32_bf16 a[156:159], a[76:79], a[124:127], a[156:159]\n" \
"s_waitcnt vmcnt(23) lgkmcnt(0)\n" \
"v_mfma_f32_16x16x32_bf16 a[160:163], a[80:83], a[120:123], a[160:163]\n" \
"s_barrier\n" \
"v_mfma_f32_16x16x32_bf16 a[164:167], a[84:87], a[120:123], a[164:167]\n" \
"v_mfma_f32_16x16x32_bf16 a[168:171], a[80:83], a[124:127], a[168:171]\n" \
"v_mfma_f32_16x16x32_bf16 a[172:175], a[84:87], a[124:127], a[172:175]\n" \
"v_mfma_f32_16x16x32_bf16 a[176:179], a[88:91], a[120:123], a[176:179]\n" \
"v_mfma_f32_16x16x32_bf16 a[180:183], a[92:95], a[120:123], a[180:183]\n" \
"v_mfma_f32_16x16x32_bf16 a[184:187], a[88:91], a[124:127], a[184:187]\n" \
"v_mfma_f32_16x16x32_bf16 a[188:191], a[92:95], a[124:127], a[188:191]\n" \
"v_mfma_f32_16x16x32_bf16 v[64:67], a[0:3], a[32:35], v[64:67]\n" \
"s_mov_b32 s47, 3932160\n" \
"s_add_u32 m0, s22, 49152\n" \
"ds_read_b128 a[64:67], v8 offset:0\n" \
"v_mfma_f32_16x16x32_bf16 v[68:71], a[4:7], a[32:35], v[68:71]\n" \
"buffer_load_dwordx4 v21, s[32:35], s47 offen offset:0 lds\n" \
"v_mfma_f32_16x16x32_bf16 v[72:75], a[0:3], a[36:39], v[72:75]\n" \
"ds_read_b64_tr_b16 a[96:97], v9 offset:16384\n" \
"v_mfma_f32_16x16x32_bf16 v[76:79], a[4:7], a[36:39], v[76:79]\n" \
"v_mfma_f32_16x16x32_bf16 v[80:83], a[8:11], a[32:35], v[80:83]\n" \
"s_add_u32 m0, s22, 50048\n" \
"ds_read_b64_tr_b16 a[98:99], v10 offset:16384\n" \
"v_mfma_f32_16x16x32_bf16 v[84:87], a[12:15], a[32:35], v[84:87]\n" \
"buffer_load_dwordx4 v21, s[32:35], s47 offen offset:128 lds\n" \
"v_mfma_f32_16x16x32_bf16 v[88:91], a[8:11], a[36:39], v[88:91]\n" \
"ds_read_b128 a[68:71], v8 offset:2048\n" \
"v_mfma_f32_16x16x32_bf16 v[92:95], a[12:15], a[36:39], v[92:95]\n" \
"v_mfma_f32_16x16x32_bf16 v[96:99], a[16:19], a[32:35], v[96:99]\n" \
"s_add_u32 m0, s22, 50944\n" \
"ds_read_b64_tr_b16 a[100:101], v11 offset:16384\n" \
"v_mfma_f32_16x16x32_bf16 v[100:103], a[20:23], a[32:35], v[100:103]\n" \
"buffer_load_dwordx4 v21, s[32:35], s47 offen offset:256 lds\n" \
"v_mfma_f32_16x16x32_bf16 v[104:107], a[16:19], a[36:39], v[104:107]\n" \
"ds_read_b64_tr_b16 a[102:103], v12 offset:16384\n" \
"v_mfma_f32_16x16x32_bf16 v[108:111], a[20:23], a[36:39], v[108:111]\n" \
"v_mfma_f32_16x16x32_bf16 v[112:115], a[24:27], a[32:35], v[112:115]\n" \
"s_add_u32 m0, s22, 51840\n" \
"ds_read_b128 a[72:75], v8 offset:4096\n" \
"v_mfma_f32_16x16x32_bf16 v[116:119], a[28:31], a[32:35], v[116:119]\n" \
"buffer_load_dwordx4 v21, s[32:35], s47 offen offset:384 lds\n" \
"v_mfma_f32_16x16x32_bf16 v[120:123], a[24:27], a[36:39], v[120:123]\n" \
"ds_read_b64_tr_b16 a[104:105], v9 offset:16896\n" \
"v_mfma_f32_16x16x32_bf16 v[124:127], a[28:31], a[36:39], v[124:127]\n" \
"v_mfma_f32_16x16x32_bf16 v[128:131], a[0:3], a[40:43], v[128:131]\n" \
"ds_read_b64_tr_b16 a[106:107], v10 offset:16896\n" \
"v_mfma_f32_16x16x32_bf16 v[132:135], a[4:7], a[40:43], v[132:135]\n" \
"buffer_load_dwordx2 v[58:59], v57, s[36:39], 0 offen sc1\n" \
"v_mfma_f32_16x16x32_bf16 v[136:139], a[0:3], a[44:47], v[136:139]\n" \
"s_add_u32 s76, s76, 2097152\ns_addc_u32 s77, s77, 0\n" \
"ds_read_b128 a[76:79], v8 offset:6144\n" \
"v_mfma_f32_16x16x32_bf16 v[140:143], a[4:7], a[44:47], v[140:143]\n" \
"v_mfma_f32_16x16x32_bf16 v[144:147], a[8:11], a[40:43], v[144:147]\n" \
"ds_read_b64_tr_b16 a[108:109], v11 offset:16896\n" \
"v_mfma_f32_16x16x32_bf16 v[148:151], a[12:15], a[40:43], v[148:151]\n" \
"v_mfma_f32_16x16x32_bf16 v[152:155], a[8:11], a[44:47], v[152:155]\n" \
"ds_read_b64_tr_b16 a[110:111], v12 offset:16896\n" \
"v_mfma_f32_16x16x32_bf16 v[156:159], a[12:15], a[44:47], v[156:159]\n" \
"v_mfma_f32_16x16x32_bf16 v[160:163], a[16:19], a[40:43], v[160:163]\n" \
"ds_read_b128 a[80:83], v8 offset:8192\n" \
"v_mfma_f32_16x16x32_bf16 v[164:167], a[20:23], a[40:43], v[164:167]\n" \
"v_mfma_f32_16x16x32_bf16 v[168:171], a[16:19], a[44:47], v[168:171]\n" \
"ds_read_b64_tr_b16 a[112:113], v9 offset:17408\n" \
"v_mfma_f32_16x16x32_bf16 v[172:175], a[20:23], a[44:47], v[172:175]\n" \
"s_mov_b64 exec, 0xffff\nglobal_load_dword v22, v1, s[76:77]\ns_mov_b64 exec, -1\n" \
"v_mfma_f32_16x16x32_bf16 v[176:179], a[24:27], a[40:43], v[176:179]\n" \
"s_add_u32 s76, s76, 2097152\ns_addc_u32 s77, s77, 0\n" \
"ds_read_b64_tr_b16 a[114:115], v10 offset:17408\n" \
"v_mfma_f32_16x16x32_bf16 v[180:183], a[28:31], a[40:43], v[180:183]\n" \
"v_mfma_f32_16x16x32_bf16 v[184:187], a[24:27], a[44:47], v[184:187]\n" \
"ds_read_b128 a[84:87], v8 offset:10240\n" \
"v_mfma_f32_16x16x32_bf16 v[188:191], a[28:31], a[44:47], v[188:191]\n" \
"s_mov_b64 exec, 0xffff\nglobal_load_dword v22, v1, s[76:77]\ns_mov_b64 exec, -1\n" \
"v_mfma_f32_16x16x32_bf16 v[192:195], a[0:3], a[48:51], v[192:195]\n" \
"ds_read_b64_tr_b16 a[116:117], v11 offset:17408\n" \
"v_mfma_f32_16x16x32_bf16 v[196:199], a[4:7], a[48:51], v[196:199]\n" \
"v_mfma_f32_16x16x32_bf16 v[200:203], a[0:3], a[52:55], v[200:203]\n" \
"ds_read_b64_tr_b16 a[118:119], v12 offset:17408\n" \
"v_mfma_f32_16x16x32_bf16 v[204:207], a[4:7], a[52:55], v[204:207]\n" \
"v_mfma_f32_16x16x32_bf16 v[208:211], a[8:11], a[48:51], v[208:211]\n" \
"ds_read_b128 a[88:91], v8 offset:12288\n" \
"v_mfma_f32_16x16x32_bf16 v[212:215], a[12:15], a[48:51], v[212:215]\n" \
"v_mfma_f32_16x16x32_bf16 v[216:219], a[8:11], a[52:55], v[216:219]\n" \
"ds_read_b64_tr_b16 a[120:121], v9 offset:17920\n" \
"v_mfma_f32_16x16x32_bf16 v[220:223], a[12:15], a[52:55], v[220:223]\n" \
"v_mfma_f32_16x16x32_bf16 v[224:227], a[16:19], a[48:51], v[224:227]\n" \
"ds_read_b64_tr_b16 a[122:123], v10 offset:17920\n" \
"v_mfma_f32_16x16x32_bf16 v[228:231], a[20:23], a[48:51], v[228:231]\n" \
"v_mfma_f32_16x16x32_bf16 v[232:235], a[16:19], a[52:55], v[232:235]\n" \
"ds_read_b128 a[92:95], v8 offset:14336\n" \
"v_mfma_f32_16x16x32_bf16 v[236:239], a[20:23], a[52:55], v[236:239]\n" \
"v_mfma_f32_16x16x32_bf16 v[240:243], a[24:27], a[48:51], v[240:243]\n" \
"ds_read_b64_tr_b16 a[124:125], v11 offset:17920\n" \
"v_mfma_f32_16x16x32_bf16 v[244:247], a[28:31], a[48:51], v[244:247]\n" \
"v_mfma_f32_16x16x32_bf16 v[248:251], a[24:27], a[52:55], v[248:251]\n" \
"ds_read_b64_tr_b16 a[126:127], v12 offset:17920\n" \
"v_mfma_f32_16x16x32_bf16 v[252:255], a[28:31], a[52:55], v[252:255]\n" \
"v_mfma_f32_16x16x32_bf16 a[128:131], a[0:3], a[56:59], a[128:131]\n" \
"v_mfma_f32_16x16x32_bf16 a[132:135], a[4:7], a[56:59], a[132:135]\n" \
"v_mfma_f32_16x16x32_bf16 a[136:139], a[0:3], a[60:63], a[136:139]\n" \
"v_mfma_f32_16x16x32_bf16 a[140:143], a[4:7], a[60:63], a[140:143]\n" \
"v_mfma_f32_16x16x32_bf16 a[144:147], a[8:11], a[56:59], a[144:147]\n" \
"v_mfma_f32_16x16x32_bf16 a[148:151], a[12:15], a[56:59], a[148:151]\n" \
"v_mfma_f32_16x16x32_bf16 a[152:155], a[8:11], a[60:63], a[152:155]\n" \
"v_mfma_f32_16x16x32_bf16 a[156:159], a[12:15], a[60:63], a[156:159]\n" \
"s_waitcnt vmcnt(2) lgkmcnt(0)\n" \
"v_mfma_f32_16x16x32_bf16 a[160:163], a[16:19], a[56:59], a[160:163]\n" \
"s_barrier\n" \
"v_mfma_f32_16x16x32_bf16 a[164:167], a[20:23], a[56:59], a[164:167]\n" \
"v_cmp_ne_u64 vcc, s[14:15], v[58:59]\n" \
"v_mfma_f32_16x16x32_bf16 a[168:171], a[16:19], a[60:63], a[168:171]\n" \
"v_mfma_f32_16x16x32_bf16 a[172:175], a[20:23], a[60:63], a[172:175]\n" \
"s_cbranch_vccnz LPUV_%=\nLPUVR_%=:\n" \
"v_mfma_f32_16x16x32_bf16 a[176:179], a[24:27], a[56:59], a[176:179]\n" \
"v_mfma_f32_16x16x32_bf16 a[180:183], a[28:31], a[56:59], a[180:183]\n" \
"v_mfma_f32_16x16x32_bf16 a[184:187], a[24:27], a[60:63], a[184:187]\n" \
"v_mfma_f32_16x16x32_bf16 a[188:191], a[28:31], a[60:63], a[188:191]\n" \
"v_mfma_f32_16x16x32_bf16 v[64:67], a[64:67], a[96:99], v[64:67]\n" \
"s_mov_b64 s[32:33], s[44:45]\n" \
"ds_read_b128 a[0:3], v5 offset:0\n" \
"v_mfma_f32_16x16x32_bf16 v[68:71], a[68:71], a[96:99], v[68:71]\n" \
"v_mfma_f32_16x16x32_bf16 v[72:75], a[64:67], a[100:103], v[72:75]\n" \
"s_add_u32 m0, s56, 10304\n" \
"ds_read_b64_tr_b16 a[32:33], v9 offset:32768\n" \
"v_mfma_f32_16x16x32_bf16 v[76:79], a[68:71], a[100:103], v[76:79]\n" \
"buffer_load_dwordx4 v26, s[72:75], 0 offen offset:0 sc1 lds\n" \
"v_mfma_f32_16x16x32_bf16 v[80:83], a[72:75], a[96:99], v[80:83]\n" \
"s_mov_b32 s34, s49\n" \
"ds_read_b64_tr_b16 a[34:35], v10 offset:32768\n" \
"s_waitcnt vmcnt(8)\n" \
"v_mfma_f32_16x16x32_bf16 v[84:87], a[76:79], a[96:99], v[84:87]\n" \
"v_alignbyte_b32 v30, v31, v30, 2\n" \
"v_alignbyte_b32 v31, v32, v31, 2\n" \
"v_mfma_f32_16x16x32_bf16 v[88:91], a[72:75], a[100:103], v[88:91]\n" \
"s_add_u32 m0, s56, 11328\n" \
"ds_read_b128 a[4:7], v5 offset:2048\n" \
"v_alignbyte_b32 v32, v33, v32, 2\n" \
"v_mfma_f32_16x16x32_bf16 v[92:95], a[76:79], a[100:103], v[92:95]\n" \
"buffer_load_dwordx4 v27, s[72:75], 0 offen offset:0 sc1 lds\n" \
"v_mfma_f32_16x16x32_bf16 v[96:99], a[80:83], a[96:99], v[96:99]\n" \
"ds_read_b64_tr_b16 a[36:37], v11 offset:32768\n" \
"v_alignbyte_b32 v33, v50, v33, 2\n" \
"v_mfma_f32_16x16x32_bf16 v[100:103], a[84:87], a[96:99], v[100:103]\n" \
"ds_write_b128 v55, v[30:33] offset:5184\n" \
"v_alignbyte_b32 v34, v35, v34, 2\n" \
"v_mfma_f32_16x16x32_bf16 v[104:107], a[80:83], a[100:103], v[104:107]\n" \
"s_add_u32 m0, s56, 11328\n" \
"ds_read_b64_tr_b16 a[38:39], v12 offset:32768\n" \
"v_alignbyte_b32 v35, v36, v35, 2\n" \
"v_mfma_f32_16x16x32_bf16 v[108:111], a[84:87], a[100:103], v[108:111]\n" \
"buffer_load_dwordx4 v27, s[72:75], 0 offen offset:1024 sc1 lds\n" \
"v_mfma_f32_16x16x32_bf16 v[112:115], a[88:91], a[96:99], v[112:115]\n" \
"ds_read_b128 a[8:11], v5 offset:4096\n" \
"v_alignbyte_b32 v36, v37, v36, 2\n" \
"v_mfma_f32_16x16x32_bf16 v[116:119], a[92:95], a[96:99], v[116:119]\n" \
"v_alignbyte_b32 v37, v51, v37, 2\n" \
"ds_write_b128 v55, v[34:37] offset:6208\n" \
"v_mfma_f32_16x16x32_bf16 v[120:123], a[88:91], a[100:103], v[120:123]\n" \
"s_add_u32 m0, s56, 11328\n" \
"ds_read_b64_tr_b16 a[40:41], v9 offset:33280\n" \
"v_alignbyte_b32 v38, v39, v38, 2\n" \
"v_mfma_f32_16x16x32_bf16 v[124:127], a[92:95], a[100:103], v[124:127]\n" \
"buffer_load_dwordx4 v27, s[72:75], 0 offen offset:2048 sc1 lds\n" \
"v_mfma_f32_16x16x32_bf16 v[128:131], a[64:67], a[104:107], v[128:131]\n" \
"ds_read_b64_tr_b16 a[42:43], v10 offset:33280\n" \
"v_alignbyte_b32 v39, v40, v39, 2\n" \
"v_mfma_f32_16x16x32_bf16 v[132:135], a[68:71], a[104:107], v[132:135]\n" \
"v_alignbyte_b32 v40, v41, v40, 2\n" \
"v_alignbyte_b32 v41, v52, v41, 2\n" \
"v_mfma_f32_16x16x32_bf16 v[136:139], a[64:67], a[108:111], v[136:139]\n" \
"s_add_u32 m0, s56, 11328\n" \
"ds_read_b128 a[12:15], v5 offset:6144\n" \
"v_mfma_f32_16x16x32_bf16 v[140:143], a[68:71], a[108:111], v[140:143]\n" \
"buffer_load_dwordx4 v27, s[72:75], 0 offen offset:3072 sc1 lds\n" \
"v_mfma_f32_16x16x32_bf16 v[144:147], a[72:75], a[104:107], v[144:147]\n" \
"ds_read_b64_tr_b16 a[44:45], v11 offset:33280\n" \
"v_mfma_f32_16x16x32_bf16 v[148:151], a[76:79], a[104:107], v[148:151]\n" \
"ds_write_b128 v55, v[38:41] offset:7232\n" \
"v_alignbyte_b32 v42, v43, v42, 2\n" \
"v_mfma_f32_16x16x32_bf16 v[152:155], a[72:75], a[108:111], v[152:155]\n" \
"s_add_u32 m0, s56, 0\n" \
"ds_read_b64_tr_b16 a[46:47], v12 offset:33280\n" \
"v_alignbyte_b32 v43, v44, v43, 2\n" \
"v_mfma_f32_16x16x32_bf16 v[156:159], a[76:79], a[108:111], v[156:159]\n" \
"buffer_load_dwordx4 v23, s[68:71], 0 offen offset:0 lds\n" \
"v_mfma_f32_16x16x32_bf16 v[160:163], a[80:83], a[104:107], v[160:163]\n" \
"ds_read_b128 a[16:19], v5 offset:8192\n" \
"v_alignbyte_b32 v44, v45, v44, 2\n" \
"v_mfma_f32_16x16x32_bf16 v[164:167], a[84:87], a[104:107], v[164:167]\n" \
"v_alignbyte_b32 v45, v53, v45, 2\n" \
"ds_write_b128 v55, v[42:45] offset:8256\n" \
"v_mfma_f32_16x16x32_bf16 v[168:171], a[80:83], a[108:111], v[168:171]\n" \
"s_add_u32 m0, s56, 1024\n" \
"ds_read_b64_tr_b16 a[48:49], v9 offset:33792\n" \
"v_alignbyte_b32 v46, v47, v46, 2\n" \
"v_mfma_f32_16x16x32_bf16 v[172:175], a[84:87], a[108:111], v[172:175]\n" \
"buffer_load_dwordx4 v24, s[68:71], 0 offen offset:0 lds\n" \
"v_mfma_f32_16x16x32_bf16 v[176:179], a[88:91], a[104:107], v[176:179]\n" \
"ds_read_b64_tr_b16 a[50:51], v10 offset:33792\n" \
"v_alignbyte_b32 v47, v48, v47, 2\n" \
"v_mfma_f32_16x16x32_bf16 v[180:183], a[92:95], a[104:107], v[180:183]\n" \
"v_alignbyte_b32 v48, v49, v48, 2\n" \
"v_alignbyte_b32 v49, v54, v49, 2\n" \
"v_mfma_f32_16x16x32_bf16 v[184:187], a[88:91], a[108:111], v[184:187]\n" \
"s_add_u32 m0, s56, 1024\n" \
"ds_read_b128 a[20:23], v5 offset:10240\n" \
"v_mfma_f32_16x16x32_bf16 v[188:191], a[92:95], a[108:111], v[188:191]\n" \
"buffer_load_dwordx4 v24, s[68:71], 0 offen offset:1024 lds\n" \
"v_mfma_f32_16x16x32_bf16 v[192:195], a[64:67], a[112:115], v[192:195]\n" \
"ds_read_b64_tr_b16 a[52:53], v11 offset:33792\n" \
"v_mfma_f32_16x16x32_bf16 v[196:199], a[68:71], a[112:115], v[196:199]\n" \
"ds_write_b128 v55, v[46:49] offset:9280\n" \
"v_lshrrev_b32 v30, 4, v4\n" \
"v_mfma_f32_16x16x32_bf16 v[200:203], a[64:67], a[116:119], v[200:203]\n" \
"s_add_u32 m0, s56, 1024\n" \
"ds_read_b64_tr_b16 a[54:55], v12 offset:33792\n" \
"v_and_b32 v31, 15, v30\n" \
"v_mfma_f32_16x16x32_bf16 v[204:207], a[68:71], a[116:119], v[204:207]\n" \
"buffer_load_dwordx4 v24, s[68:71], 0 offen offset:2048 lds\n" \
"v_mfma_f32_16x16x32_bf16 v[208:211], a[72:75], a[112:115], v[208:211]\n" \
"ds_read_b128 a[24:27], v5 offset:12288\n" \
"v_lshrrev_b32 v32, 4, v30\n" \
"v_mfma_f32_16x16x32_bf16 v[212:215], a[76:79], a[112:115], v[212:215]\n" \
"v_add_u32 v33, 1, v31\n" \
"v_and_b32 v34, 1, v33\n" \
"v_mfma_f32_16x16x32_bf16 v[216:219], a[72:75], a[116:119], v[216:219]\n" \
"s_add_u32 m0, s56, 1024\n" \
"ds_read_b64_tr_b16 a[56:57], v9 offset:34304\n" \
"v_and_b32 v33, 30, v33\n" \
"v_mfma_f32_16x16x32_bf16 v[220:223], a[76:79], a[116:119], v[220:223]\n" \
"buffer_load_dwordx4 v24, s[68:71], 0 offen offset:3072 lds\n" \
"v_mfma_f32_16x16x32_bf16 v[224:227], a[80:83], a[112:115], v[224:227]\n" \
"ds_read_b64_tr_b16 a[58:59], v10 offset:34304\n" \
"v_mul_u32_u24 v34, 5184, v34\n" \
"v_mfma_f32_16x16x32_bf16 v[228:231], a[84:87], a[112:115], v[228:231]\n" \
"v_lshl_add_u32 v33, v33, 1, v34\n" \
"v_lshl_add_u32 v33, v32, 4, v33\n" \
"v_mfma_f32_16x16x32_bf16 v[232:235], a[80:83], a[116:119], v[232:235]\n" \
"s_mov_b32 s46, 0\n" \
"s_add_u32 m0, s50, 65536\n" \
"ds_read_b128 a[28:31], v5 offset:14336\n" \
"v_mfma_f32_16x16x32_bf16 v[236:239], a[84:87], a[116:119], v[236:239]\n" \
"buffer_load_dwordx4 v13, s[28:31], s46 offen lds\n" \
"v_mfma_f32_16x16x32_bf16 v[240:243], a[88:91], a[112:115], v[240:243]\n" \
"ds_read_b64_tr_b16 a[60:61], v11 offset:34304\n" \
"v_add_u32 v46, s56, v33\n" \
"v_mfma_f32_16x16x32_bf16 v[244:247], a[92:95], a[112:115], v[244:247]\n" \
"v_add_u32 v47, 1024, v46\n" \
"v_add_u32 v48, 2048, v46\n" \
"v_mfma_f32_16x16x32_bf16 v[248:251], a[88:91], a[116:119], v[248:251]\n" \
"s_add_u32 m0, s50, 69632\n" \
"ds_read_b64_tr_b16 a[62:63], v12 offset:34304\n" \
"v_add_u32 v49, 3072, v46\n" \
"v_mfma_f32_16x16x32_bf16 v[252:255], a[92:95], a[116:119], v[252:255]\n" \
"buffer_load_dwordx4 v14, s[28:31], s46 offen lds\n" \
"v_mfma_f32_16x16x32_bf16 a[128:131], a[64:67], a[120:123], a[128:131]\n" \
"v_lshlrev_b32 v33, 1, v31\n" \
"v_sub_u32 v33, v33, v32\n" \
"v_mfma_f32_16x16x32_bf16 a[132:135], a[68:71], a[120:123], a[132:135]\n" \
"v_lshlrev_b32 v33, 4, v33\n" \
"v_add_u32 v33, 10320, v33\n" \
"v_mfma_f32_16x16x32_bf16 a[136:139], a[64:67], a[124:127], a[136:139]\n" \
"s_add_u32 m0, s50, 73728\n" \
"v_add_u32 v50, s56, v33\n" \
"v_mfma_f32_16x16x32_bf16 a[140:143], a[68:71], a[124:127], a[140:143]\n" \
"buffer_load_dwordx4 v15, s[28:31], s46 offen lds\n" \
"v_mfma_f32_16x16x32_bf16 a[144:147], a[72:75], a[120:123], a[144:147]\n" \
"v_lshlrev_b32 v33, 5, v31\n" \
"v_lshl_add_u32 v55, v32, 3, v33\n" \
"v_mfma_f32_16x16x32_bf16 a[148:151], a[76:79], a[120:123], a[148:151]\n" \
"v_add_u32 v55, s22, v55\n" \
"v_add_u32 v33, s56, v33\n" \
"v_mfma_f32_16x16x32_bf16 a[152:155], a[72:75], a[124:127], a[152:155]\n" \
"v_xor_b32 v32, 1, v32\n" \
"v_mfma_f32_16x16x32_bf16 a[156:159], a[76:79], a[124:127], a[156:159]\n" \
"s_waitcnt vmcnt(16) lgkmcnt(0)\n" \
"v_lshl_add_u32 v51, v32, 3, v33\n" \
"v_mfma_f32_16x16x32_bf16 a[160:163], a[80:83], a[120:123], a[160:163]\n" \
"s_barrier\n" \
"v_mfma_f32_16x16x32_bf16 a[164:167], a[84:87], a[120:123], a[164:167]\n" \
"v_mfma_f32_16x16x32_bf16 a[168:171], a[80:83], a[124:127], a[168:171]\n" \
"s_add_u32 m0, s50, 77824\n" \
"v_mfma_f32_16x16x32_bf16 a[172:175], a[84:87], a[124:127], a[172:175]\n" \
"buffer_load_dwordx4 v16, s[28:31], s46 offen lds\n" \
"v_mfma_f32_16x16x32_bf16 a[176:179], a[88:91], a[120:123], a[176:179]\n" \
"v_mfma_f32_16x16x32_bf16 a[180:183], a[92:95], a[120:123], a[180:183]\n" \
"v_mfma_f32_16x16x32_bf16 a[184:187], a[88:91], a[124:127], a[184:187]\n" \
"v_mfma_f32_16x16x32_bf16 a[188:191], a[92:95], a[124:127], a[188:191]\n" \
"v_mfma_f32_16x16x32_bf16 v[64:67], a[0:3], a[32:35], v[64:67]\n" \
"ds_read_b128 a[64:67], v6 offset:0\n" \
"v_mfma_f32_16x16x32_bf16 v[68:71], a[4:7], a[32:35], v[68:71]\n" \
"v_mfma_f32_16x16x32_bf16 v[72:75], a[0:3], a[36:39], v[72:75]\n" \
"s_mov_b32 s46, 0\n" \
"s_add_u32 m0, s50, 81920\n" \
"ds_read_b64_tr_b16 a[96:97], v9 offset:49152\n" \
"v_mfma_f32_16x16x32_bf16 v[76:79], a[4:7], a[36:39], v[76:79]\n" \
"buffer_load_dwordx4 v17, s[28:31], s46 offen lds\n" \
"v_mfma_f32_16x16x32_bf16 v[80:83], a[8:11], a[32:35], v[80:83]\n" \
"ds_read_b64_tr_b16 a[98:99], v10 offset:49152\n" \
"v_mfma_f32_16x16x32_bf16 v[84:87], a[12:15], a[32:35], v[84:87]\n" \
"v_mfma_f32_16x16x32_bf16 v[88:91], a[8:11], a[36:39], v[88:91]\n" \
"ds_read_b128 a[68:71], v6 offset:2048\n" \
"v_mfma_f32_16x16x32_bf16 v[92:95], a[12:15], a[36:39], v[92:95]\n" \
"v_mfma_f32_16x16x32_bf16 v[96:99], a[16:19], a[32:35], v[96:99]\n" \
"ds_read_b64_tr_b16 a[100:101], v11 offset:49152\n" \
"v_mfma_f32_16x16x32_bf16 v[100:103], a[20:23], a[32:35], v[100:103]\n" \
"v_mfma_f32_16x16x32_bf16 v[104:107], a[16:19], a[36:39], v[104:107]\n" \
"s_add_u32 m0, s50, 86016\n" \
"ds_read_b64_tr_b16 a[102:103], v12 offset:49152\n" \
"v_mfma_f32_16x16x32_bf16 v[108:111], a[20:23], a[36:39], v[108:111]\n" \
"buffer_load_dwordx4 v18, s[28:31], s46 offen lds\n" \
"v_mfma_f32_16x16x32_bf16 v[112:115], a[24:27], a[32:35], v[112:115]\n" \
"ds_read_b128 a[72:75], v6 offset:4096\n" \
"v_mfma_f32_16x16x32_bf16 v[116:119], a[28:31], a[32:35], v[116:119]\n" \
"v_mfma_f32_16x16x32_bf16 v[120:123], a[24:27], a[36:39], v[120:123]\n" \
"ds_read_b64_tr_b16 a[104:105], v9 offset:49664\n" \
"v_mfma_f32_16x16x32_bf16 v[124:127], a[28:31], a[36:39], v[124:127]\n" \
"v_mfma_f32_16x16x32_bf16 v[128:131], a[0:3], a[40:43], v[128:131]\n" \
"ds_read_b64_tr_b16 a[106:107], v10 offset:49664\n" \
"v_mfma_f32_16x16x32_bf16 v[132:135], a[4:7], a[40:43], v[132:135]\n" \
"v_mfma_f32_16x16x32_bf16 v[136:139], a[0:3], a[44:47], v[136:139]\n" \
"s_add_u32 m0, s50, 90112\n" \
"ds_read_b128 a[76:79], v6 offset:6144\n" \
"v_mfma_f32_16x16x32_bf16 v[140:143], a[4:7], a[44:47], v[140:143]\n" \
"buffer_load_dwordx4 v19, s[28:31], s46 offen lds\n" \
"v_mfma_f32_16x16x32_bf16 v[144:147], a[8:11], a[40:43], v[144:147]\n" \
"ds_read_b64_tr_b16 a[108:109], v11 offset:49664\n" \
"v_mfma_f32_16x16x32_bf16 v[148:151], a[12:15], a[40:43], v[148:151]\n" \
"v_mfma_f32_16x16x32_bf16 v[152:155], a[8:11], a[44:47], v[152:155]\n" \
"ds_read_b64_tr_b16 a[110:111], v12 offset:49664\n" \
"v_mfma_f32_16x16x32_bf16 v[156:159], a[12:15], a[44:47], v[156:159]\n" \
"v_mfma_f32_16x16x32_bf16 v[160:163], a[16:19], a[40:43], v[160:163]\n" \
"s_add_u32 s76, s76, 2097152\ns_addc_u32 s77, s77, 0\n" \
"ds_read_b128 a[80:83], v6 offset:8192\n" \
"v_mfma_f32_16x16x32_bf16 v[164:167], a[20:23], a[40:43], v[164:167]\n" \
"v_mfma_f32_16x16x32_bf16 v[168:171], a[16:19], a[44:47], v[168:171]\n" \
"s_add_u32 m0, s50, 94208\n" \
"ds_read_b64_tr_b16 a[112:113], v9 offset:50176\n" \
"v_mfma_f32_16x16x32_bf16 v[172:175], a[20:23], a[44:47], v[172:175]\n" \
"buffer_load_dwordx4 v20, s[28:31], s46 offen lds\n" \
"v_mfma_f32_16x16x32_bf16 v[176:179], a[24:27], a[40:43], v[176:179]\n" \
"ds_read_b64_tr_b16 a[114:115], v10 offset:50176\n" \
"v_mfma_f32_16x16x32_bf16 v[180:183], a[28:31], a[40:43], v[180:183]\n" \
"v_mfma_f32_16x16x32_bf16 v[184:187], a[24:27], a[44:47], v[184:187]\n" \
"ds_read_b128 a[84:87], v6 offset:10240\n" \
"v_mfma_f32_16x16x32_bf16 v[188:191], a[28:31], a[44:47], v[188:191]\n" \
"v_mfma_f32_16x16x32_bf16 v[192:195], a[0:3], a[48:51], v[192:195]\n" \
"ds_read_b64_tr_b16 a[116:117], v11 offset:50176\n" \
"v_mfma_f32_16x16x32_bf16 v[196:199], a[4:7], a[48:51], v[196:199]\n" \
"v_mfma_f32_16x16x32_bf16 v[200:203], a[0:3], a[52:55], v[200:203]\n" \
"s_mov_b32 s47, 0\n" \
"s_add_u32 m0, s51, 32768\n" \
"ds_read_b64_tr_b16 a[118:119], v12 offset:50176\n" \
"v_mfma_f32_16x16x32_bf16 v[204:207], a[4:7], a[52:55], v[204:207]\n" \
"buffer_load_dwordx4 v21, s[32:35], s47 offen offset:0 lds\n" \
"v_mfma_f32_16x16x32_bf16 v[208:211], a[8:11], a[48:51], v[208:211]\n" \
"ds_read_b128 a[88:91], v6 offset:12288\n" \
"v_mfma_f32_16x16x32_bf16 v[212:215], a[12:15], a[48:51], v[212:215]\n" \
"v_mfma_f32_16x16x32_bf16 v[216:219], a[8:11], a[52:55], v[216:219]\n" \
"ds_read_b64_tr_b16 a[120:121], v9 offset:50688\n" \
"v_mfma_f32_16x16x32_bf16 v[220:223], a[12:15], a[52:55], v[220:223]\n" \
"s_mov_b64 exec, 0xffff\nglobal_load_dword v22, v1, s[76:77]\ns_mov_b64 exec, -1\n" \
"v_mfma_f32_16x16x32_bf16 v[224:227], a[16:19], a[48:51], v[224:227]\n" \
"ds_read_b64_tr_b16 a[122:123], v10 offset:50688\n" \
"v_mfma_f32_16x16x32_bf16 v[228:231], a[20:23], a[48:51], v[228:231]\n" \
"v_mfma_f32_16x16x32_bf16 v[232:235], a[16:19], a[52:55], v[232:235]\n" \
"s_add_u32 m0, s51, 33664\n" \
"ds_read_b128 a[92:95], v6 offset:14336\n" \
"v_mfma_f32_16x16x32_bf16 v[236:239], a[20:23], a[52:55], v[236:239]\n" \
"buffer_load_dwordx4 v21, s[32:35], s47 offen offset:128 lds\n" \
"v_mfma_f32_16x16x32_bf16 v[240:243], a[24:27], a[48:51], v[240:243]\n" \
"ds_read_b64_tr_b16 a[124:125], v11 offset:50688\n" \
"v_mfma_f32_16x16x32_bf16 v[244:247], a[28:31], a[48:51], v[244:247]\n" \
"v_mfma_f32_16x16x32_bf16 v[248:251], a[24:27], a[52:55], v[248:251]\n" \
"ds_read_b64_tr_b16 a[126:127], v12 offset:50688\n" \
"v_mfma_f32_16x16x32_bf16 v[252:255], a[28:31], a[52:55], v[252:255]\n" \
"LH7_%=:\ns_cbranch_execz LH11_%=\n" \
"v_add_u32 v5, s54, v5\n" \
"v_mfma_f32_16x16x32_bf16 a[128:131], a[0:3], a[56:59], a[128:131]\n" \
"v_add_u32 v6, s54, v6\n" \
"v_mfma_f32_16x16x32_bf16 a[132:135], a[4:7], a[56:59], a[132:135]\n" \
"v_add_u32 v7, s54, v7\n" \
"v_mfma_f32_16x16x32_bf16 a[136:139], a[0:3], a[60:63], a[136:139]\n" \
"s_mov_b32 s47, 262144\n" \
"s_add_u32 m0, s51, 40960\n" \
"v_mfma_f32_16x16x32_bf16 a[140:143], a[4:7], a[60:63], a[140:143]\n" \
"buffer_load_dwordx4 v21, s[32:35], s47 offen offset:0 lds\n" \
"v_mfma_f32_16x16x32_bf16 a[144:147], a[8:11], a[56:59], a[144:147]\n" \
"v_add_u32 v8, s54, v8\n" \
"v_mfma_f32_16x16x32_bf16 a[148:151], a[12:15], a[56:59], a[148:151]\n" \
"v_sub_u32 v9, v9, v62\n" \
"v_mfma_f32_16x16x32_bf16 a[152:155], a[8:11], a[60:63], a[152:155]\n" \
"v_sub_u32 v10, v10, v62\n" \
"v_mfma_f32_16x16x32_bf16 a[156:159], a[12:15], a[60:63], a[156:159]\n" \
"s_waitcnt lgkmcnt(0)\n" \
"v_mfma_f32_16x16x32_bf16 a[160:163], a[16:19], a[56:59], a[160:163]\n" \
"s_barrier\n" \
"v_mfma_f32_16x16x32_bf16 a[164:167], a[20:23], a[56:59], a[164:167]\n" \
"v_sub_u32 v11, v11, v62\n" \
"v_mfma_f32_16x16x32_bf16 a[168:171], a[16:19], a[60:63], a[168:171]\n" \
"s_add_u32 m0, s51, 41856\n" \
"v_mfma_f32_16x16x32_bf16 a[172:175], a[20:23], a[60:63], a[172:175]\n" \
"buffer_load_dwordx4 v21, s[32:35], s47 offen offset:128 lds\n" \
"v_mfma_f32_16x16x32_bf16 a[176:179], a[24:27], a[56:59], a[176:179]\n" \
"v_sub_u32 v12, v12, v62\n" \
"v_mfma_f32_16x16x32_bf16 a[180:183], a[28:31], a[56:59], a[180:183]\n" \
"v_mfma_f32_16x16x32_bf16 a[184:187], a[24:27], a[60:63], a[184:187]\n" \
"v_mfma_f32_16x16x32_bf16 a[188:191], a[28:31], a[60:63], a[188:191]\n" \
"v_mfma_f32_16x16x32_bf16 v[64:67], a[64:67], a[96:99], v[64:67]\n" \
"s_waitcnt vmcnt(13) lgkmcnt(0)\n" \
"v_mfma_f32_16x16x32_bf16 v[68:71], a[68:71], a[96:99], v[68:71]\n" \
"v_mfma_f32_16x16x32_bf16 v[72:75], a[64:67], a[100:103], v[72:75]\n" \
"s_mov_b32 s46, 128\n" \
"s_add_u32 m0, s50, 0\n" \
"ds_read_b128 a[32:35], v50 offset:512\n" \
"v_mfma_f32_16x16x32_bf16 v[76:79], a[68:71], a[100:103], v[76:79]\n" \
"buffer_load_dwordx4 v13, s[28:31], s46 offen lds\n" \
"v_mfma_f32_16x16x32_bf16 v[80:83], a[72:75], a[96:99], v[80:83]\n" \
"s_mov_b32 s96, s57\n" \
"ds_read2_b32 a[0:1], v46 offset0:0 offset1:1\n" \
"v_mul_f32 v64, s20, v64\n" \
"v_mfma_f32_16x16x32_bf16 v[84:87], a[76:79], a[96:99], v[84:87]\n" \
"v_mul_f32 v65, s20, v65\n" \
"v_mul_f32 v66, s20, v66\n" \
"v_mfma_f32_16x16x32_bf16 v[88:91], a[72:75], a[100:103], v[88:91]\n" \
"ds_read2_b32 a[2:3], v46 offset0:2 offset1:3\n" \
"v_mul_f32 v67, s20, v67\n" \
"v_mfma_f32_16x16x32_bf16 v[92:95], a[76:79], a[100:103], v[92:95]\n" \
"v_cvt_pk_bf16_f32 v30, v64, v65\n" \
"v_cvt_pk_bf16_f32 v31, v66, v67\n" \
"v_mfma_f32_16x16x32_bf16 v[96:99], a[80:83], a[96:99], v[96:99]\n" \
"ds_read_b128 a[36:39], v50 offset:1024\n" \
"v_mul_f32 v68, s20, v68\n" \
"v_mfma_f32_16x16x32_bf16 v[100:103], a[84:87], a[96:99], v[100:103]\n" \
"v_mul_f32 v69, s20, v69\n" \
"v_mul_f32 v70, s20, v70\n" \
"v_mfma_f32_16x16x32_bf16 v[104:107], a[80:83], a[100:103], v[104:107]\n" \
"s_add_u32 m0, s50, 4096\n" \
"ds_read2_b32 a[4:5], v46 offset0:128 offset1:129\n" \
"v_mul_f32 v71, s20, v71\n" \
"v_mfma_f32_16x16x32_bf16 v[108:111], a[84:87], a[100:103], v[108:111]\n" \
"buffer_load_dwordx4 v14, s[28:31], s46 offen lds\n" \
"v_mfma_f32_16x16x32_bf16 v[112:115], a[88:91], a[96:99], v[112:115]\n" \
"ds_read2_b32 a[6:7], v46 offset0:130 offset1:131\n" \
"v_cvt_pk_bf16_f32 v32, v68, v69\n" \
"v_mfma_f32_16x16x32_bf16 v[116:119], a[92:95], a[96:99], v[116:119]\n" \
"v_cvt_pk_bf16_f32 v33, v70, v71\n" \
"v_mul_f32 v72, s20, v72\n" \
"v_mfma_f32_16x16x32_bf16 v[120:123], a[88:91], a[100:103], v[120:123]\n" \
"ds_read_b128 a[40:43], v50 offset:1536\n" \
"v_mul_f32 v73, s20, v73\n" \
"v_mfma_f32_16x16x32_bf16 v[124:127], a[92:95], a[100:103], v[124:127]\n" \
"buffer_store_dwordx4 v[30:33], v4, s[36:39], s96 offen offset:0 sc1\n" \
"v_mfma_f32_16x16x32_bf16 v[128:131], a[64:67], a[104:107], v[128:131]\n" \
"s_add_u32 s97, s96, s98\n" \
"ds_read2_b32 a[8:9], v47 offset0:0 offset1:1\n" \
"v_mul_f32 v74, s20, v74\n" \
"v_mfma_f32_16x16x32_bf16 v[132:135], a[68:71], a[104:107], v[132:135]\n" \
"v_mul_f32 v75, s20, v75\n" \
"v_cvt_pk_bf16_f32 v34, v72, v73\n" \
"v_mfma_f32_16x16x32_bf16 v[136:139], a[64:67], a[108:111], v[136:139]\n" \
"s_add_u32 m0, s50, 8192\n" \
"ds_read2_b32 a[10:11], v47 offset0:2 offset1:3\n" \
"v_cvt_pk_bf16_f32 v35, v74, v75\n" \
"v_mfma_f32_16x16x32_bf16 v[140:143], a[68:71], a[108:111], v[140:143]\n" \
"buffer_load_dwordx4 v15, s[28:31], s46 offen lds\n" \
"v_mfma_f32_16x16x32_bf16 v[144:147], a[72:75], a[104:107], v[144:147]\n" \
"ds_read_b128 a[44:47], v50 offset:2048\n" \
"v_mul_f32 v76, s20, v76\n" \
"v_mfma_f32_16x16x32_bf16 v[148:151], a[76:79], a[104:107], v[148:151]\n" \
"v_mul_f32 v77, s20, v77\n" \
"v_mul_f32 v78, s20, v78\n" \
"v_mfma_f32_16x16x32_bf16 v[152:155], a[72:75], a[108:111], v[152:155]\n" \
"ds_read2_b32 a[12:13], v47 offset0:128 offset1:129\n" \
"v_mul_f32 v79, s20, v79\n" \
"v_mfma_f32_16x16x32_bf16 v[156:159], a[76:79], a[108:111], v[156:159]\n" \
"v_cvt_pk_bf16_f32 v36, v76, v77\n" \
"v_cvt_pk_bf16_f32 v37, v78, v79\n" \
"v_mfma_f32_16x16x32_bf16 v[160:163], a[80:83], a[104:107], v[160:163]\n" \
"ds_read2_b32 a[14:15], v47 offset0:130 offset1:131\n" \
"v_mul_f32 v80, s20, v80\n" \
"v_mfma_f32_16x16x32_bf16 v[164:167], a[84:87], a[104:107], v[164:167]\n" \
"v_mul_f32 v81, s20, v81\n" \
"v_mul_f32 v82, s20, v82\n" \
"v_mfma_f32_16x16x32_bf16 v[168:171], a[80:83], a[108:111], v[168:171]\n" \
"s_add_u32 m0, s50, 12288\n" \
"ds_read_b128 a[48:51], v50 offset:2560\n" \
"v_mul_f32 v83, s20, v83\n" \
"v_mfma_f32_16x16x32_bf16 v[172:175], a[84:87], a[108:111], v[172:175]\n" \
"buffer_load_dwordx4 v16, s[28:31], s46 offen lds\n" \
"v_mfma_f32_16x16x32_bf16 v[176:179], a[88:91], a[104:107], v[176:179]\n" \
"ds_read2_b32 a[16:17], v48 offset0:0 offset1:1\n" \
"v_cvt_pk_bf16_f32 v38, v80, v81\n" \
"v_mfma_f32_16x16x32_bf16 v[180:183], a[92:95], a[104:107], v[180:183]\n" \
"buffer_store_dwordx4 v[34:37], v4, s[36:39], s97 offen offset:0 sc1\n" \
"v_mfma_f32_16x16x32_bf16 v[184:187], a[88:91], a[108:111], v[184:187]\n" \
"s_add_u32 s96, s96, s92\n" \
"ds_read2_b32 a[18:19], v48 offset0:2 offset1:3\n" \
"v_cvt_pk_bf16_f32 v39, v82, v83\n" \
"v_mfma_f32_16x16x32_bf16 v[188:191], a[92:95], a[108:111], v[188:191]\n" \
"v_mul_f32 v84, s20, v84\n" \
"v_mul_f32 v85, s20, v85\n" \
"v_mfma_f32_16x16x32_bf16 v[192:195], a[64:67], a[112:115], v[192:195]\n" \
"ds_read_b128 a[52:55], v50 offset:3072\n" \
"v_mul_f32 v86, s20, v86\n" \
"v_mfma_f32_16x16x32_bf16 v[196:199], a[68:71], a[112:115], v[196:199]\n" \
"v_mul_f32 v87, s20, v87\n" \
"v_cvt_pk_bf16_f32 v40, v84, v85\n" \
"v_mfma_f32_16x16x32_bf16 v[200:203], a[64:67], a[116:119], v[200:203]\n" \
"s_mov_b32 s46, 128\n" \
"s_add_u32 m0, s50, 16384\n" \
"ds_read2_b32 a[20:21], v48 offset0:128 offset1:129\n" \
"v_mfma_f32_16x16x32_bf16 v[204:207], a[68:71], a[116:119], v[204:207]\n" \
"buffer_load_dwordx4 v17, s[28:31], s46 offen lds\n" \
"v_mfma_f32_16x16x32_bf16 v[208:211], a[72:75], a[112:115], v[208:211]\n" \
"ds_read2_b32 a[22:23], v48 offset0:130 offset1:131\n" \
"v_cvt_pk_bf16_f32 v41, v86, v87\n" \
"v_mfma_f32_16x16x32_bf16 v[212:215], a[76:79], a[112:115], v[212:215]\n" \
"v_mul_f32 v88, s20, v88\n" \
"v_mul_f32 v89, s20, v89\n" \
"v_mfma_f32_16x16x32_bf16 v[216:219], a[72:75], a[116:119], v[216:219]\n" \
"ds_read_b128 a[56:59], v50 offset:3584\n" \
"v_mul_f32 v90, s20, v90\n" \
"v_mfma_f32_16x16x32_bf16 v[220:223], a[76:79], a[116:119], v[220:223]\n" \
"v_mul_f32 v91, s20, v91\n" \
"v_cvt_pk_bf16_f32 v42, v88, v89\n" \
"v_mfma_f32_16x16x32_bf16 v[224:227], a[80:83], a[112:115], v[224:227]\n" \
"ds_read2_b32 a[24:25], v49 offset0:0 offset1:1\n" \
"v_cvt_pk_bf16_f32 v43, v90, v91\n" \
"v_mfma_f32_16x16x32_bf16 v[228:231], a[84:87], a[112:115], v[228:231]\n" \
"v_mul_f32 v92, s20, v92\n" \
"v_mul_f32 v93, s20, v93\n" \
"v_mfma_f32_16x16x32_bf16 v[232:235], a[80:83], a[116:119], v[232:235]\n" \
"s_add_u32 m0, s50, 20480\n" \
"ds_read2_b32 a[26:27], v49 offset0:2 offset1:3\n" \
"v_mul_f32 v94, s20, v94\n" \
"v_mfma_f32_16x16x32_bf16 v[236:239], a[84:87], a[116:119], v[236:239]\n" \
"buffer_load_dwordx4 v18, s[28:31], s46 offen lds\n" \
"v_mfma_f32_16x16x32_bf16 v[240:243], a[88:91], a[112:115], v[240:243]\n" \
"ds_read_b128 a[60:63], v50 offset:4096\n" \
"v_mul_f32 v95, s20, v95\n" \
"v_mfma_f32_16x16x32_bf16 v[244:247], a[92:95], a[112:115], v[244:247]\n" \
"buffer_store_dwordx4 v[38:41], v4, s[36:39], s96 offen offset:0 sc1\n" \
"v_mfma_f32_16x16x32_bf16 v[248:251], a[88:91], a[116:119], v[248:251]\n" \
"s_add_u32 s97, s96, s98\n" \
"ds_read2_b32 a[28:29], v49 offset0:128 offset1:129\n" \
"v_cvt_pk_bf16_f32 v44, v92, v93\n" \
"v_mfma_f32_16x16x32_bf16 v[252:255], a[92:95], a[116:119], v[252:255]\n" \
"v_cvt_pk_bf16_f32 v45, v94, v95\n" \
"v_mul_f32 v96, s20, v96\n" \
"v_mfma_f32_16x16x32_bf16 a[128:131], a[64:67], a[120:123], a[128:131]\n" \
"ds_read2_b32 a[30:31], v49 offset0:130 offset1:131\n" \
"v_mul_f32 v97, s20, v97\n" \
"v_mfma_f32_16x16x32_bf16 a[132:135], a[68:71], a[120:123], a[132:135]\n" \
"v_mul_f32 v98, s20, v98\n" \
"v_mul_f32 v99, s20, v99\n" \
"v_mfma_f32_16x16x32_bf16 a[136:139], a[64:67], a[124:127], a[136:139]\n" \
"s_add_u32 m0, s50, 24576\n" \
"v_cvt_pk_bf16_f32 v30, v96, v97\n" \
"v_cvt_pk_bf16_f32 v31, v98, v99\n" \
"v_mfma_f32_16x16x32_bf16 a[140:143], a[68:71], a[124:127], a[140:143]\n" \
"buffer_load_dwordx4 v19, s[28:31], s46 offen lds\n" \
"v_mfma_f32_16x16x32_bf16 a[144:147], a[72:75], a[120:123], a[144:147]\n" \
"v_mul_f32 v100, s20, v100\n" \
"v_mul_f32 v101, s20, v101\n" \
"v_mfma_f32_16x16x32_bf16 a[148:151], a[76:79], a[120:123], a[148:151]\n" \
"v_mul_f32 v102, s20, v102\n" \
"v_mul_f32 v103, s20, v103\n" \
"v_mfma_f32_16x16x32_bf16 a[152:155], a[72:75], a[124:127], a[152:155]\n" \
"v_cvt_pk_bf16_f32 v32, v100, v101\n" \
"v_mfma_f32_16x16x32_bf16 a[156:159], a[76:79], a[124:127], a[156:159]\n" \
"v_cvt_pk_bf16_f32 v33, v102, v103\n" \
"v_mul_f32 v104, s20, v104\n" \
"v_mfma_f32_16x16x32_bf16 a[160:163], a[80:83], a[120:123], a[160:163]\n" \
"v_mul_f32 v105, s20, v105\n" \
"v_mul_f32 v106, s20, v106\n" \
"v_mfma_f32_16x16x32_bf16 a[164:167], a[84:87], a[120:123], a[164:167]\n" \
"v_mul_f32 v107, s20, v107\n" \
"v_mfma_f32_16x16x32_bf16 a[168:171], a[80:83], a[124:127], a[168:171]\n" \
"s_add_u32 m0, s50, 28672\n" \
"v_cvt_pk_bf16_f32 v34, v104, v105\n" \
"v_cvt_pk_bf16_f32 v35, v106, v107\n" \
"v_mfma_f32_16x16x32_bf16 a[172:175], a[84:87], a[124:127], a[172:175]\n" \
"buffer_load_dwordx4 v20, s[28:31], s46 offen lds\n" \
"v_mfma_f32_16x16x32_bf16 a[176:179], a[88:91], a[120:123], a[176:179]\n" \
"v_mul_f32 v108, s20, v108\n" \
"v_mul_f32 v109, s20, v109\n" \
"v_mfma_f32_16x16x32_bf16 a[180:183], a[92:95], a[120:123], a[180:183]\n" \
"buffer_store_dwordx4 v[42:45], v4, s[36:39], s97 offen offset:0 sc1\n" \
"v_mfma_f32_16x16x32_bf16 a[184:187], a[88:91], a[124:127], a[184:187]\n" \
"s_waitcnt lgkmcnt(0)\n" \
"s_add_u32 s96, s96, s92\n" \
"v_mul_f32 v110, s20, v110\n" \
"v_mfma_f32_16x16x32_bf16 a[188:191], a[92:95], a[124:127], a[188:191]\n" \
"v_mul_f32 v111, s20, v111\n" \
"v_cvt_pk_bf16_f32 v36, v108, v109\n" \
"LC_%=:\n" \
"v_mfma_f32_16x16x32_bf16 v[64:67], a[0:3], a[32:35], 0\n" \
"ds_read_b128 a[96:99], v50 offset:448\n" \
"v_cvt_pk_bf16_f32 v37, v110, v111\n" \
"v_mfma_f32_16x16x32_bf16 v[68:71], a[0:3], a[36:39], 0\n" \
"v_mul_f32 v112, s20, v112\n" \
"v_mul_f32 v113, s20, v113\n" \
"v_mfma_f32_16x16x32_bf16 v[68:71], a[4:7], a[32:35], v[68:71]\n" \
"ds_read2_b32 a[64:65], v46 offset0:16 offset1:17\n" \
"v_mul_f32 v114, s20, v114\n" \
"v_mfma_f32_16x16x32_bf16 v[72:75], a[0:3], a[40:43], 0\n" \
"ds_read2_b32 a[66:67], v46 offset0:18 offset1:19\n" \
"v_mul_f32 v115, s20, v115\n" \
"v_mfma_f32_16x16x32_bf16 v[72:75], a[4:7], a[36:39], v[72:75]\n" \
"s_mov_b32 s47, 524288\n" \
"s_add_u32 m0, s51, 49152\n" \
"ds_read_b128 a[100:103], v50 offset:960\n" \
"v_mfma_f32_16x16x32_bf16 v[72:75], a[8:11], a[32:35], v[72:75]\n" \
"buffer_load_dwordx4 v21, s[32:35], s47 offen offset:0 lds\n" \
"v_mfma_f32_16x16x32_bf16 v[76:79], a[0:3], a[44:47], 0\n" \
"ds_read2_b32 a[68:69], v46 offset0:144 offset1:145\n" \
"v_cvt_pk_bf16_f32 v38, v112, v113\n" \
"v_mfma_f32_16x16x32_bf16 v[76:79], a[4:7], a[40:43], v[76:79]\n" \
"ds_read2_b32 a[70:71], v46 offset0:146 offset1:147\n" \
"v_cvt_pk_bf16_f32 v39, v114, v115\n" \
"v_mfma_f32_16x16x32_bf16 v[76:79], a[8:11], a[36:39], v[76:79]\n" \
"ds_read_b128 a[104:107], v50 offset:1472\n" \
"v_mul_f32 v116, s20, v116\n" \
"v_mfma_f32_16x16x32_bf16 v[76:79], a[12:15], a[32:35], v[76:79]\n" \
"buffer_store_dwordx4 v[30:33], v4, s[36:39], s96 offen offset:0 sc1\n" \
"v_mfma_f32_16x16x32_bf16 v[80:83], a[0:3], a[48:51], 0\n" \
"s_add_u32 s97, s96, s98\n" \
"ds_read2_b32 a[72:73], v47 offset0:16 offset1:17\n" \
"v_mul_f32 v117, s20, v117\n" \
"v_mfma_f32_16x16x32_bf16 v[80:83], a[4:7], a[44:47], v[80:83]\n" \
"ds_read2_b32 a[74:75], v47 offset0:18 offset1:19\n" \
"v_mul_f32 v118, s20, v118\n" \
"v_mfma_f32_16x16x32_bf16 v[80:83], a[8:11], a[40:43], v[80:83]\n" \
"s_add_u32 m0, s51, 50048\n" \
"ds_read_b128 a[108:111], v50 offset:1984\n" \
"v_mul_f32 v119, s20, v119\n" \
"v_mfma_f32_16x16x32_bf16 v[80:83], a[12:15], a[36:39], v[80:83]\n" \
"buffer_load_dwordx4 v21, s[32:35], s47 offen offset:128 lds\n" \
"v_mfma_f32_16x16x32_bf16 v[80:83], a[16:19], a[32:35], v[80:83]\n" \
"ds_read2_b32 a[76:77], v47 offset0:144 offset1:145\n" \
"v_cvt_pk_bf16_f32 v40, v116, v117\n" \
"v_mfma_f32_16x16x32_bf16 v[84:87], a[0:3], a[52:55], 0\n" \
"ds_read2_b32 a[78:79], v47 offset0:146 offset1:147\n" \
"v_cvt_pk_bf16_f32 v41, v118, v119\n" \
"v_mfma_f32_16x16x32_bf16 v[84:87], a[4:7], a[48:51], v[84:87]\n" \
"ds_read_b128 a[112:115], v50 offset:2496\n" \
"v_mul_f32 v120, s20, v120\n" \
"v_mfma_f32_16x16x32_bf16 v[84:87], a[8:11], a[44:47], v[84:87]\n" \
"v_mul_f32 v121, s20, v121\n" \
"v_mul_f32 v122, s20, v122\n" \
"v_mfma_f32_16x16x32_bf16 v[84:87], a[12:15], a[40:43], v[84:87]\n" \
"ds_read2_b32 a[80:81], v48 offset0:16 offset1:17\n" \
"v_mul_f32 v123, s20, v123\n" \
"v_mfma_f32_16x16x32_bf16 v[84:87], a[16:19], a[36:39], v[84:87]\n" \
"ds_read2_b32 a[82:83], v48 offset0:18 offset1:19\n" \
"v_cvt_pk_bf16_f32 v42, v120, v121\n" \
"v_mfma_f32_16x16x32_bf16 v[84:87], a[20:23], a[32:35], v[84:87]\n" \
"s_mov_b32 s47, 786432\n" \
"s_add_u32 m0, s51, 57344\n" \
"ds_read_b128 a[116:119], v50 offset:3008\n" \
"v_mfma_f32_16x16x32_bf16 v[88:91], a[0:3], a[56:59], 0\n" \
"buffer_load_dwordx4 v21, s[32:35], s47 offen offset:0 lds\n" \
"v_mfma_f32_16x16x32_bf16 v[88:91], a[4:7], a[52:55], v[88:91]\n" \
"ds_read2_b32 a[84:85], v48 offset0:144 offset1:145\n" \
"v_cvt_pk_bf16_f32 v43, v122, v123\n" \
"v_mfma_f32_16x16x32_bf16 v[88:91], a[8:11], a[48:51], v[88:91]\n" \
"ds_read2_b32 a[86:87], v48 offset0:146 offset1:147\n" \
"v_mul_f32 v124, s20, v124\n" \
"v_mfma_f32_16x16x32_bf16 v[88:91], a[12:15], a[44:47], v[88:91]\n" \
"ds_read_b128 a[120:123], v50 offset:3520\n" \
"v_mul_f32 v125, s20, v125\n" \
"v_mfma_f32_16x16x32_bf16 v[88:91], a[16:19], a[40:43], v[88:91]\n" \
"buffer_store_dwordx4 v[34:37], v4, s[36:39], s97 offen offset:0 sc1\n" \
"v_mfma_f32_16x16x32_bf16 v[88:91], a[20:23], a[36:39], v[88:91]\n" \
"s_add_u32 s96, s96, s92\n" \
"ds_read2_b32 a[88:89], v49 offset0:16 offset1:17\n" \
"v_mul_f32 v126, s20, v126\n" \
"v_mfma_f32_16x16x32_bf16 v[88:91], a[24:27], a[32:35], v[88:91]\n" \
"ds_read2_b32 a[90:91], v49 offset0:18 offset1:19\n" \
"v_mul_f32 v127, s20, v127\n" \
"v_mfma_f32_16x16x32_bf16 v[92:95], a[0:3], a[60:63], 0\n" \
"s_add_u32 m0, s51, 58240\n" \
"ds_read_b128 a[124:127], v50 offset:4032\n" \
"v_cvt_pk_bf16_f32 v44, v124, v125\n" \
"v_mfma_f32_16x16x32_bf16 v[92:95], a[4:7], a[56:59], v[92:95]\n" \
"buffer_load_dwordx4 v21, s[32:35], s47 offen offset:128 lds\n" \
"v_mfma_f32_16x16x32_bf16 v[92:95], a[8:11], a[52:55], v[92:95]\n" \
"ds_read2_b32 a[92:93], v49 offset0:144 offset1:145\n" \
"v_cvt_pk_bf16_f32 v45, v126, v127\n" \
"v_mfma_f32_16x16x32_bf16 v[92:95], a[12:15], a[48:51], v[92:95]\n" \
"ds_read2_b32 a[94:95], v49 offset0:146 offset1:147\n" \
"v_mul_f32 v128, s20, v128\n" \
"v_mfma_f32_16x16x32_bf16 v[92:95], a[16:19], a[44:47], v[92:95]\n" \
"v_mul_f32 v129, s20, v129\n" \
"v_mul_f32 v130, s20, v130\n" \
"v_mfma_f32_16x16x32_bf16 v[92:95], a[20:23], a[40:43], v[92:95]\n" \
"v_mul_f32 v131, s20, v131\n" \
"v_cvt_pk_bf16_f32 v30, v128, v129\n" \
"v_mfma_f32_16x16x32_bf16 v[92:95], a[24:27], a[36:39], v[92:95]\n" \
"v_cvt_pk_bf16_f32 v31, v130, v131\n" \
"v_mfma_f32_16x16x32_bf16 v[92:95], a[28:31], a[32:35], v[92:95]\n" \
"s_waitcnt lgkmcnt(0)\n" \
"v_mul_f32 v132, s20, v132\n" \
"v_mul_f32 v133, s20, v133\n" \
"v_mfma_f32_16x16x32_bf16 v[64:67], a[64:67], a[96:99], v[64:67]\n" \
"ds_read_b128 a[32:35], v50 offset:384\n" \
"v_mul_f32 v134, s20, v134\n" \
"v_mfma_f32_16x16x32_bf16 v[68:71], a[64:67], a[100:103], v[68:71]\n" \
"buffer_store_dwordx4 v[38:41], v4, s[36:39], s96 offen offset:0 sc1\n" \
"v_mfma_f32_16x16x32_bf16 v[68:71], a[68:71], a[96:99], v[68:71]\n" \
"s_add_u32 s97, s96, s98\n" \
"ds_read2_b32 a[0:1], v46 offset0:32 offset1:33\n" \
"v_mul_f32 v135, s20, v135\n" \
"v_mfma_f32_16x16x32_bf16 v[72:75], a[64:67], a[104:107], v[72:75]\n" \
"ds_read2_b32 a[2:3], v46 offset0:34 offset1:35\n" \
"v_cvt_pk_bf16_f32 v32, v132, v133\n" \
"v_mfma_f32_16x16x32_bf16 v[72:75], a[68:71], a[100:103], v[72:75]\n" \
"ds_read_b128 a[36:39], v50 offset:896\n" \
"v_cvt_pk_bf16_f32 v33, v134, v135\n" \
"v_mfma_f32_16x16x32_bf16 v[72:75], a[72:75], a[96:99], v[72:75]\n" \
"v_mul_f32 v136, s20, v136\n" \
"v_mul_f32 v137, s20, v137\n" \
"v_mfma_f32_16x16x32_bf16 v[76:79], a[64:67], a[108:111], v[76:79]\n" \
"ds_read2_b32 a[4:5], v46 offset0:160 offset1:161\n" \
"v_mul_f32 v138, s20, v138\n" \
"v_mfma_f32_16x16x32_bf16 v[76:79], a[68:71], a[104:107], v[76:79]\n" \
"ds_read2_b32 a[6:7], v46 offset0:162 offset1:163\n" \
"v_mul_f32 v139, s20, v139\n" \
"v_mfma_f32_16x16x32_bf16 v[76:79], a[72:75], a[100:103], v[76:79]\n" \
"ds_read_b128 a[40:43], v50 offset:1408\n" \
"v_cvt_pk_bf16_f32 v34, v136, v137\n" \
"v_mfma_f32_16x16x32_bf16 v[76:79], a[76:79], a[96:99], v[76:79]\n" \
"v_cvt_pk_bf16_f32 v35, v138, v139\n" \
"v_mul_f32 v140, s20, v140\n" \
"v_mfma_f32_16x16x32_bf16 v[80:83], a[64:67], a[112:115], v[80:83]\n" \
"ds_read2_b32 a[8:9], v47 offset0:32 offset1:33\n" \
"v_mul_f32 v141, s20, v141\n" \
"v_mfma_f32_16x16x32_bf16 v[80:83], a[68:71], a[108:111], v[80:83]\n" \
"ds_read2_b32 a[10:11], v47 offset0:34 offset1:35\n" \
"v_mul_f32 v142, s20, v142\n" \
"v_mfma_f32_16x16x32_bf16 v[80:83], a[72:75], a[104:107], v[80:83]\n" \
"ds_read_b128 a[44:47], v50 offset:1920\n" \
"v_mul_f32 v143, s20, v143\n" \
"v_mfma_f32_16x16x32_bf16 v[80:83], a[76:79], a[100:103], v[80:83]\n" \
"buffer_store_dwordx4 v[42:45], v4, s[36:39], s97 offen offset:0 sc1\n" \
"v_mfma_f32_16x16x32_bf16 v[80:83], a[80:83], a[96:99], v[80:83]\n" \
"s_add_u32 s57, s57, s93\n" \
"ds_read2_b32 a[12:13], v47 offset0:160 offset1:161\n" \
"s_mov_b32 s96, s57\n" \
"v_mfma_f32_16x16x32_bf16 v[84:87], a[64:67], a[116:119], v[84:87]\n" \
"ds_read2_b32 a[14:15], v47 offset0:162 offset1:163\n" \
"v_cvt_pk_bf16_f32 v36, v140, v141\n" \
"v_mfma_f32_16x16x32_bf16 v[84:87], a[68:71], a[112:115], v[84:87]\n" \
"ds_read_b128 a[48:51], v50 offset:2432\n" \
"v_cvt_pk_bf16_f32 v37, v142, v143\n" \
"v_mfma_f32_16x16x32_bf16 v[84:87], a[72:75], a[108:111], v[84:87]\n" \
"v_mul_f32 v144, s20, v144\n" \
"v_mul_f32 v145, s20, v145\n" \
"v_mfma_f32_16x16x32_bf16 v[84:87], a[76:79], a[104:107], v[84:87]\n" \
"ds_read2_b32 a[16:17], v48 offset0:32 offset1:33\n" \
"v_mul_f32 v146, s20, v146\n" \
"v_mfma_f32_16x16x32_bf16 v[84:87], a[80:83], a[100:103], v[84:87]\n" \
"ds_read2_b32 a[18:19], v48 offset0:34 offset1:35\n" \
"v_mul_f32 v147, s20, v147\n" \
"v_mfma_f32_16x16x32_bf16 v[84:87], a[84:87], a[96:99], v[84:87]\n" \
"ds_read_b128 a[52:55], v50 offset:2944\n" \
"v_cvt_pk_bf16_f32 v38, v144, v145\n" \
"v_mfma_f32_16x16x32_bf16 v[88:91], a[64:67], a[120:123], v[88:91]\n" \
"v_cvt_pk_bf16_f32 v39, v146, v147\n" \
"v_mul_f32 v148, s20, v148\n" \
"v_mfma_f32_16x16x32_bf16 v[88:91], a[68:71], a[116:119], v[88:91]\n" \
"ds_read2_b32 a[20:21], v48 offset0:160 offset1:161\n" \
"v_mul_f32 v149, s20, v149\n" \
"v_mfma_f32_16x16x32_bf16 v[88:91], a[72:75], a[112:115], v[88:91]\n" \
"ds_read2_b32 a[22:23], v48 offset0:162 offset1:163\n" \
"v_mul_f32 v150, s20, v150\n" \
"v_mfma_f32_16x16x32_bf16 v[88:91], a[76:79], a[108:111], v[88:91]\n" \
"ds_read_b128 a[56:59], v50 offset:3456\n" \
"v_mul_f32 v151, s20, v151\n" \
"v_mfma_f32_16x16x32_bf16 v[88:91], a[80:83], a[104:107], v[88:91]\n" \
"buffer_store_dwordx4 v[30:33], v4, s[36:39], s96 offen offset:0 sc1\n" \
"v_mfma_f32_16x16x32_bf16 v[88:91], a[84:87], a[100:103], v[88:91]\n" \
"s_add_u32 s97, s96, s98\n" \
"ds_read2_b32 a[24:25], v49 offset0:32 offset1:33\n" \
"v_cvt_pk_bf16_f32 v40, v148, v149\n" \
"v_mfma_f32_16x16x32_bf16 v[88:91], a[88:91], a[96:99], v[88:91]\n" \
"ds_read2_b32 a[26:27], v49 offset0:34 offset1:35\n" \
"v_cvt_pk_bf16_f32 v41, v150, v151\n" \
"v_mfma_f32_16x16x32_bf16 v[92:95], a[64:67], a[124:127], v[92:95]\n" \
"ds_read_b128 a[60:63], v50 offset:3968\n" \
"v_mul_f32 v152, s20, v152\n" \
"v_mfma_f32_16x16x32_bf16 v[92:95], a[68:71], a[120:123], v[92:95]\n" \
"v_mul_f32 v153, s20, v153\n" \
"v_mul_f32 v154, s20, v154\n" \
"v_mfma_f32_16x16x32_bf16 v[92:95], a[72:75], a[116:119], v[92:95]\n" \
"ds_read2_b32 a[28:29], v49 offset0:160 offset1:161\n" \
"v_mul_f32 v155, s20, v155\n" \
"v_mfma_f32_16x16x32_bf16 v[92:95], a[76:79], a[112:115], v[92:95]\n" \
"ds_read2_b32 a[30:31], v49 offset0:162 offset1:163\n" \
"v_cvt_pk_bf16_f32 v42, v152, v153\n" \
"v_mfma_f32_16x16x32_bf16 v[92:95], a[80:83], a[108:111], v[92:95]\n" \
"v_cvt_pk_bf16_f32 v43, v154, v155\n" \
"v_mul_f32 v156, s20, v156\n" \
"v_mfma_f32_16x16x32_bf16 v[92:95], a[84:87], a[104:107], v[92:95]\n" \
"v_mul_f32 v157, s20, v157\n" \
"v_mul_f32 v158, s20, v158\n" \
"v_mfma_f32_16x16x32_bf16 v[92:95], a[88:91], a[100:103], v[92:95]\n" \
"v_mul_f32 v159, s20, v159\n" \
"v_mfma_f32_16x16x32_bf16 v[92:95], a[92:95], a[96:99], v[92:95]\n" \
"s_waitcnt lgkmcnt(0)\n" \
"v_cvt_pk_bf16_f32 v44, v156, v157\n" \
"v_cvt_pk_bf16_f32 v45, v158, v159\n" \
"v_mfma_f32_16x16x32_bf16 v[64:67], a[0:3], a[32:35], v[64:67]\n" \
"ds_read_b128 a[96:99], v50 offset:320\n" \
"v_mul_f32 v160, s20, v160\n" \
"v_mfma_f32_16x16x32_bf16 v[68:71], a[0:3], a[36:39], v[68:71]\n" \
"buffer_store_dwordx4 v[34:37], v4, s[36:39], s97 offen offset:0 sc1\n" \
"v_mfma_f32_16x16x32_bf16 v[68:71], a[4:7], a[32:35], v[68:71]\n" \
"s_add_u32 s96, s96, s92\n" \
"ds_read2_b32 a[64:65], v46 offset0:48 offset1:49\n" \
"v_mul_f32 v161, s20, v161\n" \
"v_mfma_f32_16x16x32_bf16 v[72:75], a[0:3], a[40:43], v[72:75]\n" \
"ds_read2_b32 a[66:67], v46 offset0:50 offset1:51\n" \
"v_mul_f32 v162, s20, v162\n" \
"v_mfma_f32_16x16x32_bf16 v[72:75], a[4:7], a[36:39], v[72:75]\n" \
"ds_read_b128 a[100:103], v50 offset:832\n" \
"v_mul_f32 v163, s20, v163\n" \
"v_mfma_f32_16x16x32_bf16 v[72:75], a[8:11], a[32:35], v[72:75]\n" \
"v_cvt_pk_bf16_f32 v30, v160, v161\n" \
"v_cvt_pk_bf16_f32 v31, v162, v163\n" \
"v_mfma_f32_16x16x32_bf16 v[76:79], a[0:3], a[44:47], v[76:79]\n" \
"ds_read2_b32 a[68:69], v46 offset0:176 offset1:177\n" \
"v_mul_f32 v164, s20, v164\n" \
"v_mfma_f32_16x16x32_bf16 v[76:79], a[4:7], a[40:43], v[76:79]\n" \
"ds_read2_b32 a[70:71], v46 offset0:178 offset1:179\n" \
"v_mul_f32 v165, s20, v165\n" \
"v_mfma_f32_16x16x32_bf16 v[76:79], a[8:11], a[36:39], v[76:79]\n" \
"ds_read_b128 a[104:107], v50 offset:1344\n" \
"v_mul_f32 v166, s20, v166\n" \
"v_mfma_f32_16x16x32_bf16 v[76:79], a[12:15], a[32:35], v[76:79]\n" \
"v_mul_f32 v167, s20, v167\n" \
"v_cvt_pk_bf16_f32 v32, v164, v165\n" \
"v_mfma_f32_16x16x32_bf16 v[80:83], a[0:3], a[48:51], v[80:83]\n" \
"ds_read2_b32 a[72:73], v47 offset0:48 offset1:49\n" \
"v_cvt_pk_bf16_f32 v33, v166, v167\n" \
"v_mfma_f32_16x16x32_bf16 v[80:83], a[4:7], a[44:47], v[80:83]\n" \
"ds_read2_b32 a[74:75], v47 offset0:50 offset1:51\n" \
"v_mul_f32 v168, s20, v168\n" \
"v_mfma_f32_16x16x32_bf16 v[80:83], a[8:11], a[40:43], v[80:83]\n" \
"ds_read_b128 a[108:111], v50 offset:1856\n" \
"v_mul_f32 v169, s20, v169\n" \
"v_mfma_f32_16x16x32_bf16 v[80:83], a[12:15], a[36:39], v[80:83]\n" \
"buffer_store_dwordx4 v[38:41], v4, s[36:39], s96 offen offset:0 sc1\n" \
"v_mfma_f32_16x16x32_bf16 v[80:83], a[16:19], a[32:35], v[80:83]\n" \
"s_add_u32 s97, s96, s98\n" \
"ds_read2_b32 a[76:77], v47 offset0:176 offset1:177\n" \
"v_mul_f32 v170, s20, v170\n" \
"v_mfma_f32_16x16x32_bf16 v[84:87], a[0:3], a[52:55], v[84:87]\n" \
"ds_read2_b32 a[78:79], v47 offset0:178 offset1:179\n" \
"v_mul_f32 v171, s20, v171\n" \
"v_mfma_f32_16x16x32_bf16 v[84:87], a[4:7], a[48:51], v[84:87]\n" \
"ds_read_b128 a[112:115], v50 offset:2368\n" \
"v_cvt_pk_bf16_f32 v34, v168, v169\n" \
"v_mfma_f32_16x16x32_bf16 v[84:87], a[8:11], a[44:47], v[84:87]\n" \
"v_cvt_pk_bf16_f32 v35, v170, v171\n" \
"v_mul_f32 v172, s20, v172\n" \
"v_mfma_f32_16x16x32_bf16 v[84:87], a[12:15], a[40:43], v[84:87]\n" \
"ds_read2_b32 a[80:81], v48 offset0:48 offset1:49\n" \
"v_mul_f32 v173, s20, v173\n" \
"v_mfma_f32_16x16x32_bf16 v[84:87], a[16:19], a[36:39], v[84:87]\n" \
"ds_read2_b32 a[82:83], v48 offset0:50 offset1:51\n" \
"v_mul_f32 v174, s20, v174\n" \
"v_mfma_f32_16x16x32_bf16 v[84:87], a[20:23], a[32:35], v[84:87]\n" \
"ds_read_b128 a[116:119], v50 offset:2880\n" \
"v_mul_f32 v175, s20, v175\n" \
"v_mfma_f32_16x16x32_bf16 v[88:91], a[0:3], a[56:59], v[88:91]\n" \
"v_cvt_pk_bf16_f32 v36, v172, v173\n" \
"v_cvt_pk_bf16_f32 v37, v174, v175\n" \
"v_mfma_f32_16x16x32_bf16 v[88:91], a[4:7], a[52:55], v[88:91]\n" \
"ds_read2_b32 a[84:85], v48 offset0:176 offset1:177\n" \
"v_mul_f32 v176, s20, v176\n" \
"v_mfma_f32_16x16x32_bf16 v[88:91], a[8:11], a[48:51], v[88:91]\n" \
"ds_read2_b32 a[86:87], v48 offset0:178 offset1:179\n" \
"v_mul_f32 v177, s20, v177\n" \
"v_mfma_f32_16x16x32_bf16 v[88:91], a[12:15], a[44:47], v[88:91]\n" \
"ds_read_b128 a[120:123], v50 offset:3392\n" \
"v_mul_f32 v178, s20, v178\n" \
"v_mfma_f32_16x16x32_bf16 v[88:91], a[16:19], a[40:43], v[88:91]\n" \
"buffer_store_dwordx4 v[42:45], v4, s[36:39], s97 offen offset:0 sc1\n" \
"v_mfma_f32_16x16x32_bf16 v[88:91], a[20:23], a[36:39], v[88:91]\n" \
"s_add_u32 s96, s96, s92\n" \
"ds_read2_b32 a[88:89], v49 offset0:48 offset1:49\n" \
"v_mul_f32 v179, s20, v179\n" \
"v_mfma_f32_16x16x32_bf16 v[88:91], a[24:27], a[32:35], v[88:91]\n" \
"ds_read2_b32 a[90:91], v49 offset0:50 offset1:51\n" \
"v_cvt_pk_bf16_f32 v38, v176, v177\n" \
"v_mfma_f32_16x16x32_bf16 v[92:95], a[0:3], a[60:63], v[92:95]\n" \
"ds_read_b128 a[124:127], v50 offset:3904\n" \
"v_cvt_pk_bf16_f32 v39, v178, v179\n" \
"v_mfma_f32_16x16x32_bf16 v[92:95], a[4:7], a[56:59], v[92:95]\n" \
"v_mul_f32 v180, s20, v180\n" \
"v_mul_f32 v181, s20, v181\n" \
"v_mfma_f32_16x16x32_bf16 v[92:95], a[8:11], a[52:55], v[92:95]\n" \
"ds_read2_b32 a[92:93], v49 offset0:176 offset1:177\n" \
"v_mul_f32 v182, s20, v182\n" \
"v_mfma_f32_16x16x32_bf16 v[92:95], a[12:15], a[48:51], v[92:95]\n" \
"ds_read2_b32 a[94:95], v49 offset0:178 offset1:179\n" \
"v_mul_f32 v183, s20, v183\n" \
"v_mfma_f32_16x16x32_bf16 v[92:95], a[16:19], a[44:47], v[92:95]\n" \
"v_cvt_pk_bf16_f32 v40, v180, v181\n" \
"v_cvt_pk_bf16_f32 v41, v182, v183\n" \
"v_mfma_f32_16x16x32_bf16 v[92:95], a[20:23], a[40:43], v[92:95]\n" \
"v_mul_f32 v184, s20, v184\n" \
"v_mul_f32 v185, s20, v185\n" \
"v_mfma_f32_16x16x32_bf16 v[92:95], a[24:27], a[36:39], v[92:95]\n" \
"v_mul_f32 v186, s20, v186\n" \
"v_mfma_f32_16x16x32_bf16 v[92:95], a[28:31], a[32:35], v[92:95]\n" \
"s_waitcnt lgkmcnt(0)\n" \
"v_mul_f32 v187, s20, v187\n" \
"v_cvt_pk_bf16_f32 v42, v184, v185\n" \
"v_mfma_f32_16x16x32_bf16 v[64:67], a[64:67], a[96:99], v[64:67]\n" \
"ds_read_b128 a[32:35], v50 offset:256\n" \
"v_cvt_pk_bf16_f32 v43, v186, v187\n" \
"v_mfma_f32_16x16x32_bf16 v[68:71], a[64:67], a[100:103], v[68:71]\n" \
"buffer_store_dwordx4 v[30:33], v4, s[36:39], s96 offen offset:0 sc1\n" \
"v_mfma_f32_16x16x32_bf16 v[68:71], a[68:71], a[96:99], v[68:71]\n" \
"s_add_u32 s97, s96, s98\n" \
"ds_read2_b32 a[0:1], v46 offset0:64 offset1:65\n" \
"v_mul_f32 v188, s20, v188\n" \
"v_mfma_f32_16x16x32_bf16 v[72:75], a[64:67], a[104:107], v[72:75]\n" \
"ds_read2_b32 a[2:3], v46 offset0:66 offset1:67\n" \
"v_mul_f32 v189, s20, v189\n" \
"v_mfma_f32_16x16x32_bf16 v[72:75], a[68:71], a[100:103], v[72:75]\n" \
"ds_read_b128 a[36:39], v50 offset:768\n" \
"v_mul_f32 v190, s20, v190\n" \
"v_mfma_f32_16x16x32_bf16 v[72:75], a[72:75], a[96:99], v[72:75]\n" \
"v_mul_f32 v191, s20, v191\n" \
"v_cvt_pk_bf16_f32 v44, v188, v189\n" \
"v_mfma_f32_16x16x32_bf16 v[76:79], a[64:67], a[108:111], v[76:79]\n" \
"ds_read2_b32 a[4:5], v46 offset0:192 offset1:193\n" \
"v_cvt_pk_bf16_f32 v45, v190, v191\n" \
"v_mfma_f32_16x16x32_bf16 v[76:79], a[68:71], a[104:107], v[76:79]\n" \
"ds_read2_b32 a[6:7], v46 offset0:194 offset1:195\n" \
"v_mul_f32 v192, s20, v192\n" \
"v_mfma_f32_16x16x32_bf16 v[76:79], a[72:75], a[100:103], v[76:79]\n" \
"ds_read_b128 a[40:43], v50 offset:1280\n" \
"v_mul_f32 v193, s20, v193\n" \
"v_mfma_f32_16x16x32_bf16 v[76:79], a[76:79], a[96:99], v[76:79]\n" \
"v_mul_f32 v194, s20, v194\n" \
"v_mul_f32 v195, s20, v195\n" \
"v_mfma_f32_16x16x32_bf16 v[80:83], a[64:67], a[112:115], v[80:83]\n" \
"ds_read2_b32 a[8:9], v47 offset0:64 offset1:65\n" \
"v_cvt_pk_bf16_f32 v30, v192, v193\n" \
"v_mfma_f32_16x16x32_bf16 v[80:83], a[68:71], a[108:111], v[80:83]\n" \
"ds_read2_b32 a[10:11], v47 offset0:66 offset1:67\n" \
"v_cvt_pk_bf16_f32 v31, v194, v195\n" \
"v_mfma_f32_16x16x32_bf16 v[80:83], a[72:75], a[104:107], v[80:83]\n" \
"ds_read_b128 a[44:47], v50 offset:1792\n" \
"v_mul_f32 v196, s20, v196\n" \
"v_mfma_f32_16x16x32_bf16 v[80:83], a[76:79], a[100:103], v[80:83]\n" \
"buffer_store_dwordx4 v[34:37], v4, s[36:39], s97 offen offset:0 sc1\n" \
"v_mfma_f32_16x16x32_bf16 v[80:83], a[80:83], a[96:99], v[80:83]\n" \
"s_add_u32 s96, s96, s92\n" \
"ds_read2_b32 a[12:13], v47 offset0:192 offset1:193\n" \
"v_mul_f32 v197, s20, v197\n" \
"v_mfma_f32_16x16x32_bf16 v[84:87], a[64:67], a[116:119], v[84:87]\n" \
"ds_read2_b32 a[14:15], v47 offset0:194 offset1:195\n" \
"v_mul_f32 v198, s20, v198\n" \
"v_mfma_f32_16x16x32_bf16 v[84:87], a[68:71], a[112:115], v[84:87]\n" \
"ds_read_b128 a[48:51], v50 offset:2304\n" \
"v_mul_f32 v199, s20, v199\n" \
"v_mfma_f32_16x16x32_bf16 v[84:87], a[72:75], a[108:111], v[84:87]\n" \
"LH8_%=:\ns_cbranch_execz LH12_%=\n" \
"v_cvt_pk_bf16_f32 v32, v196, v197\n" \
"v_cvt_pk_bf16_f32 v33, v198, v199\n" \
"v_mfma_f32_16x16x32_bf16 v[84:87], a[76:79], a[104:107], v[84:87]\n" \
"ds_read2_b32 a[16:17], v48 offset0:64 offset1:65\n" \
"v_mul_f32 v200, s20, v200\n" \
"v_mfma_f32_16x16x32_bf16 v[84:87], a[80:83], a[100:103], v[84:87]\n" \
"ds_read2_b32 a[18:19], v48 offset0:66 offset1:67\n" \
"v_mul_f32 v201, s20, v201\n" \
"v_mfma_f32_16x16x32_bf16 v[84:87], a[84:87], a[96:99], v[84:87]\n" \
"ds_read_b128 a[52:55], v50 offset:2816\n" \
"v_mul_f32 v202, s20, v202\n" \
"v_mfma_f32_16x16x32_bf16 v[88:91], a[64:67], a[120:123], v[88:91]\n" \
"v_mul_f32 v203, s20, v203\n" \
"v_cvt_pk_bf16_f32 v34, v200, v201\n" \
"v_mfma_f32_16x16x32_bf16 v[88:91], a[68:71], a[116:119], v[88:91]\n" \
"ds_read2_b32 a[20:21], v48 offset0:192 offset1:193\n" \
"v_cvt_pk_bf16_f32 v35, v202, v203\n" \
"v_mfma_f32_16x16x32_bf16 v[88:91], a[72:75], a[112:115], v[88:91]\n" \
"ds_read2_b32 a[22:23], v48 offset0:194 offset1:195\n" \
"v_mul_f32 v204, s20, v204\n" \
"v_mfma_f32_16x16x32_bf16 v[88:91], a[76:79], a[108:111], v[88:91]\n" \
"ds_read_b128 a[56:59], v50 offset:3328\n" \
"v_mul_f32 v205, s20, v205\n" \
"v_mfma_f32_16x16x32_bf16 v[88:91], a[80:83], a[104:107], v[88:91]\n" \
"buffer_store_dwordx4 v[38:41], v4, s[36:39], s96 offen offset:0 sc1\n" \
"v_mfma_f32_16x16x32_bf16 v[88:91], a[84:87], a[100:103], v[88:91]\n" \
"s_add_u32 s97, s96, s98\n" \
"ds_read2_b32 a[24:25], v49 offset0:64 offset1:65\n" \
"v_mul_f32 v206, s20, v206\n" \
"v_mfma_f32_16x16x32_bf16 v[88:91], a[88:91], a[96:99], v[88:91]\n" \
"ds_read2_b32 a[26:27], v49 offset0:66 offset1:67\n" \
"v_mul_f32 v207, s20, v207\n" \
"v_mfma_f32_16x16x32_bf16 v[92:95], a[64:67], a[124:127], v[92:95]\n" \
"ds_read_b128 a[60:63], v50 offset:3840\n" \
"v_cvt_pk_bf16_f32 v36, v204, v205\n" \
"v_mfma_f32_16x16x32_bf16 v[92:95], a[68:71], a[120:123], v[92:95]\n" \
"v_cvt_pk_bf16_f32 v37, v206, v207\n" \
"v_mul_f32 v208, s20, v208\n" \
"v_mfma_f32_16x16x32_bf16 v[92:95], a[72:75], a[116:119], v[92:95]\n" \
"ds_read2_b32 a[28:29], v49 offset0:192 offset1:193\n" \
"v_mul_f32 v209, s20, v209\n" \
"v_mfma_f32_16x16x32_bf16 v[92:95], a[76:79], a[112:115], v[92:95]\n" \
"ds_read2_b32 a[30:31], v49 offset0:194 offset1:195\n" \
"v_mul_f32 v210, s20, v210\n" \
"v_mfma_f32_16x16x32_bf16 v[92:95], a[80:83], a[108:111], v[92:95]\n" \
"v_mul_f32 v211, s20, v211\n" \
"v_cvt_pk_bf16_f32 v38, v208, v209\n" \
"v_mfma_f32_16x16x32_bf16 v[92:95], a[84:87], a[104:107], v[92:95]\n" \
"v_cvt_pk_bf16_f32 v39, v210, v211\n" \
"v_mul_f32 v212, s20, v212\n" \
"v_mfma_f32_16x16x32_bf16 v[92:95], a[88:91], a[100:103], v[92:95]\n" \
"v_mul_f32 v213, s20, v213\n" \
"v_mfma_f32_16x16x32_bf16 v[92:95], a[92:95], a[96:99], v[92:95]\n" \
"s_waitcnt lgkmcnt(0)\n" \
"v_mul_f32 v214, s20, v214\n" \
"v_mul_f32 v215, s20, v215\n" \
"v_mfma_f32_16x16x32_bf16 v[64:67], a[0:3], a[32:35], v[64:67]\n" \
"ds_read_b128 a[96:99], v50 offset:192\n" \
"v_cvt_pk_bf16_f32 v40, v212, v213\n" \
"v_mfma_f32_16x16x32_bf16 v[68:71], a[0:3], a[36:39], v[68:71]\n" \
"buffer_store_dwordx4 v[42:45], v4, s[36:39], s97 offen offset:0 sc1\n" \
"v_mfma_f32_16x16x32_bf16 v[68:71], a[4:7], a[32:35], v[68:71]\n" \
"s_add_u32 s57, s57, s93\n" \
"ds_read2_b32 a[64:65], v46 offset0:80 offset1:81\n" \
"s_mov_b32 s96, s57\n" \
"v_mfma_f32_16x16x32_bf16 v[72:75], a[0:3], a[40:43], v[72:75]\n" \
"ds_read2_b32 a[66:67], v46 offset0:82 offset1:83\n" \
"v_cvt_pk_bf16_f32 v41, v214, v215\n" \
"v_mfma_f32_16x16x32_bf16 v[72:75], a[4:7], a[36:39], v[72:75]\n" \
"ds_read_b128 a[100:103], v50 offset:704\n" \
"v_mul_f32 v216, s20, v216\n" \
"v_mfma_f32_16x16x32_bf16 v[72:75], a[8:11], a[32:35], v[72:75]\n" \
"v_mul_f32 v217, s20, v217\n" \
"v_mul_f32 v218, s20, v218\n" \
"v_mfma_f32_16x16x32_bf16 v[76:79], a[0:3], a[44:47], v[76:79]\n" \
"ds_read2_b32 a[68:69], v46 offset0:208 offset1:209\n" \
"v_mul_f32 v219, s20, v219\n" \
"v_mfma_f32_16x16x32_bf16 v[76:79], a[4:7], a[40:43], v[76:79]\n" \
"ds_read2_b32 a[70:71], v46 offset0:210 offset1:211\n" \
"v_cvt_pk_bf16_f32 v42, v216, v217\n" \
"v_mfma_f32_16x16x32_bf16 v[76:79], a[8:11], a[36:39], v[76:79]\n" \
"ds_read_b128 a[104:107], v50 offset:1216\n" \
"v_cvt_pk_bf16_f32 v43, v218, v219\n" \
"v_mfma_f32_16x16x32_bf16 v[76:79], a[12:15], a[32:35], v[76:79]\n" \
"v_mul_f32 v220, s20, v220\n" \
"v_mul_f32 v221, s20, v221\n" \
"v_mfma_f32_16x16x32_bf16 v[80:83], a[0:3], a[48:51], v[80:83]\n" \
"ds_read2_b32 a[72:73], v47 offset0:80 offset1:81\n" \
"v_mul_f32 v222, s20, v222\n" \
"v_mfma_f32_16x16x32_bf16 v[80:83], a[4:7], a[44:47], v[80:83]\n" \
"ds_read2_b32 a[74:75], v47 offset0:82 offset1:83\n" \
"v_mul_f32 v223, s20, v223\n" \
"v_mfma_f32_16x16x32_bf16 v[80:83], a[8:11], a[40:43], v[80:83]\n" \
"ds_read_b128 a[108:111], v50 offset:1728\n" \
"v_cvt_pk_bf16_f32 v44, v220, v221\n" \
"v_mfma_f32_16x16x32_bf16 v[80:83], a[12:15], a[36:39], v[80:83]\n" \
"buffer_store_dwordx4 v[30:33], v4, s[36:39], s96 offen offset:0 sc1\n" \
"v_mfma_f32_16x16x32_bf16 v[80:83], a[16:19], a[32:35], v[80:83]\n" \
"s_add_u32 s97, s96, s98\n" \
"ds_read2_b32 a[76:77], v47 offset0:208 offset1:209\n" \
"v_cvt_pk_bf16_f32 v45, v222, v223\n" \
"v_mfma_f32_16x16x32_bf16 v[84:87], a[0:3], a[52:55], v[84:87]\n" \
"ds_read2_b32 a[78:79], v47 offset0:210 offset1:211\n" \
"v_mul_f32 v224, s20, v224\n" \
"v_mfma_f32_16x16x32_bf16 v[84:87], a[4:7], a[48:51], v[84:87]\n" \
"ds_read_b128 a[112:115], v50 offset:2240\n" \
"v_mul_f32 v225, s20, v225\n" \
"v_mfma_f32_16x16x32_bf16 v[84:87], a[8:11], a[44:47], v[84:87]\n" \
"v_mul_f32 v226, s20, v226\n" \
"v_mul_f32 v227, s20, v227\n" \
"v_mfma_f32_16x16x32_bf16 v[84:87], a[12:15], a[40:43], v[84:87]\n" \
"ds_read2_b32 a[80:81], v48 offset0:80 offset1:81\n" \
"v_cvt_pk_bf16_f32 v30, v224, v225\n" \
"v_mfma_f32_16x16x32_bf16 v[84:87], a[16:19], a[36:39], v[84:87]\n" \
"ds_read2_b32 a[82:83], v48 offset0:82 offset1:83\n" \
"v_cvt_pk_bf16_f32 v31, v226, v227\n" \
"v_mfma_f32_16x16x32_bf16 v[84:87], a[20:23], a[32:35], v[84:87]\n" \
"ds_read_b128 a[116:119], v50 offset:2752\n" \
"v_mul_f32 v228, s20, v228\n" \
"v_mfma_f32_16x16x32_bf16 v[88:91], a[0:3], a[56:59], v[88:91]\n" \
"v_mul_f32 v229, s20, v229\n" \
"v_mul_f32 v230, s20, v230\n" \
"v_mfma_f32_16x16x32_bf16 v[88:91], a[4:7], a[52:55], v[88:91]\n" \
"ds_read2_b32 a[84:85], v48 offset0:208 offset1:209\n" \
"v_mul_f32 v231, s20, v231\n" \
"v_mfma_f32_16x16x32_bf16 v[88:91], a[8:11], a[48:51], v[88:91]\n" \
"ds_read2_b32 a[86:87], v48 offset0:210 offset1:211\n" \
"v_cvt_pk_bf16_f32 v32, v228, v229\n" \
"v_mfma_f32_16x16x32_bf16 v[88:91], a[12:15], a[44:47], v[88:91]\n" \
"ds_read_b128 a[120:123], v50 offset:3264\n" \
"v_cvt_pk_bf16_f32 v33, v230, v231\n" \
"v_mfma_f32_16x16x32_bf16 v[88:91], a[16:19], a[40:43], v[88:91]\n" \
"buffer_store_dwordx4 v[34:37], v4, s[36:39], s97 offen offset:0 sc1\n" \
"v_mfma_f32_16x16x32_bf16 v[88:91], a[20:23], a[36:39], v[88:91]\n" \
"s_add_u32 s96, s96, s92\n" \
"ds_read2_b32 a[88:89], v49 offset0:80 offset1:81\n" \
"v_mul_f32 v232, s20, v232\n" \
"v_mfma_f32_16x16x32_bf16 v[88:91], a[24:27], a[32:35], v[88:91]\n" \
"ds_read2_b32 a[90:91], v49 offset0:82 offset1:83\n" \
"v_mul_f32 v233, s20, v233\n" \
"v_mfma_f32_16x16x32_bf16 v[92:95], a[0:3], a[60:63], v[92:95]\n" \
"ds_read_b128 a[124:127], v50 offset:3776\n" \
"v_mul_f32 v234, s20, v234\n" \
"v_mfma_f32_16x16x32_bf16 v[92:95], a[4:7], a[56:59], v[92:95]\n" \
"v_mul_f32 v235, s20, v235\n" \
"v_cvt_pk_bf16_f32 v34, v232, v233\n" \
"v_mfma_f32_16x16x32_bf16 v[92:95], a[8:11], a[52:55], v[92:95]\n" \
"ds_read2_b32 a[92:93], v49 offset0:208 offset1:209\n" \
"v_cvt_pk_bf16_f32 v35, v234, v235\n" \
"v_mfma_f32_16x16x32_bf16 v[92:95], a[12:15], a[48:51], v[92:95]\n" \
"ds_read2_b32 a[94:95], v49 offset0:210 offset1:211\n" \
"v_mul_f32 v236, s20, v236\n" \
"v_mfma_f32_16x16x32_bf16 v[92:95], a[16:19], a[44:47], v[92:95]\n" \
"v_mul_f32 v237, s20, v237\n" \
"v_mul_f32 v238, s20, v238\n" \
"v_mfma_f32_16x16x32_bf16 v[92:95], a[20:23], a[40:43], v[92:95]\n" \
"v_mul_f32 v239, s20, v239\n" \
"v_cvt_pk_bf16_f32 v36, v236, v237\n" \
"v_mfma_f32_16x16x32_bf16 v[92:95], a[24:27], a[36:39], v[92:95]\n" \
"v_cvt_pk_bf16_f32 v37, v238, v239\n" \
"v_mfma_f32_16x16x32_bf16 v[92:95], a[28:31], a[32:35], v[92:95]\n" \
"s_waitcnt lgkmcnt(0)\n" \
"v_mul_f32 v240, s20, v240\n" \
"v_mul_f32 v241, s20, v241\n" \
"v_mfma_f32_16x16x32_bf16 v[64:67], a[64:67], a[96:99], v[64:67]\n" \
"ds_read_b128 a[32:35], v50 offset:128\n" \
"v_mul_f32 v242, s20, v242\n" \
"v_mfma_f32_16x16x32_bf16 v[68:71], a[64:67], a[100:103], v[68:71]\n" \
"buffer_store_dwordx4 v[38:41], v4, s[36:39], s96 offen offset:0 sc1\n" \
"v_mfma_f32_16x16x32_bf16 v[68:71], a[68:71], a[96:99], v[68:71]\n" \
"s_add_u32 s97, s96, s98\n" \
"ds_read2_b32 a[0:1], v46 offset0:96 offset1:97\n" \
"v_mul_f32 v243, s20, v243\n" \
"v_mfma_f32_16x16x32_bf16 v[72:75], a[64:67], a[104:107], v[72:75]\n" \
"ds_read2_b32 a[2:3], v46 offset0:98 offset1:99\n" \
"v_cvt_pk_bf16_f32 v38, v240, v241\n" \
"v_mfma_f32_16x16x32_bf16 v[72:75], a[68:71], a[100:103], v[72:75]\n" \
"ds_read_b128 a[36:39], v50 offset:640\n" \
"v_cvt_pk_bf16_f32 v39, v242, v243\n" \
"v_mfma_f32_16x16x32_bf16 v[72:75], a[72:75], a[96:99], v[72:75]\n" \
"v_mul_f32 v244, s20, v244\n" \
"v_mul_f32 v245, s20, v245\n" \
"v_mfma_f32_16x16x32_bf16 v[76:79], a[64:67], a[108:111], v[76:79]\n" \
"ds_read2_b32 a[4:5], v46 offset0:224 offset1:225\n" \
"v_mul_f32 v246, s20, v246\n" \
"v_mfma_f32_16x16x32_bf16 v[76:79], a[68:71], a[104:107], v[76:79]\n" \
"ds_read2_b32 a[6:7], v46 offset0:226 offset1:227\n" \
"v_mul_f32 v247, s20, v247\n" \
"v_mfma_f32_16x16x32_bf16 v[76:79], a[72:75], a[100:103], v[76:79]\n" \
"ds_read_b128 a[40:43], v50 offset:1152\n" \
"v_cvt_pk_bf16_f32 v40, v244, v245\n" \
"v_mfma_f32_16x16x32_bf16 v[76:79], a[76:79], a[96:99], v[76:79]\n" \
"v_cvt_pk_bf16_f32 v41, v246, v247\n" \
"v_mul_f32 v248, s20, v248\n" \
"v_mfma_f32_16x16x32_bf16 v[80:83], a[64:67], a[112:115], v[80:83]\n" \
"ds_read2_b32 a[8:9], v47 offset0:96 offset1:97\n" \
"v_mul_f32 v249, s20, v249\n" \
"v_mfma_f32_16x16x32_bf16 v[80:83], a[68:71], a[108:111], v[80:83]\n" \
"ds_read2_b32 a[10:11], v47 offset0:98 offset1:99\n" \
"v_mul_f32 v250, s20, v250\n" \
"v_mfma_f32_16x16x32_bf16 v[80:83], a[72:75], a[104:107], v[80:83]\n" \
"ds_read_b128 a[44:47], v50 offset:1664\n" \
"v_mul_f32 v251, s20, v251\n" \
"v_mfma_f32_16x16x32_bf16 v[80:83], a[76:79], a[100:103], v[80:83]\n" \
"buffer_store_dwordx4 v[42:45], v4, s[36:39], s97 offen offset:0 sc1\n" \
"v_mfma_f32_16x16x32_bf16 v[80:83], a[80:83], a[96:99], v[80:83]\n" \
"s_add_u32 s96, s96, s92\n" \
"ds_read2_b32 a[12:13], v47 offset0:224 offset1:225\n" \
"v_cvt_pk_bf16_f32 v42, v248, v249\n" \
"v_mfma_f32_16x16x32_bf16 v[84:87], a[64:67], a[116:119], v[84:87]\n" \
"ds_read2_b32 a[14:15], v47 offset0:226 offset1:227\n" \
"v_cvt_pk_bf16_f32 v43, v250, v251\n" \
"v_mfma_f32_16x16x32_bf16 v[84:87], a[68:71], a[112:115], v[84:87]\n" \
"ds_read_b128 a[48:51], v50 offset:2176\n" \
"v_mul_f32 v252, s20, v252\n" \
"v_mfma_f32_16x16x32_bf16 v[84:87], a[72:75], a[108:111], v[84:87]\n" \
"v_mul_f32 v253, s20, v253\n" \
"v_mul_f32 v254, s20, v254\n" \
"v_mfma_f32_16x16x32_bf16 v[84:87], a[76:79], a[104:107], v[84:87]\n" \
"ds_read2_b32 a[16:17], v48 offset0:96 offset1:97\n" \
"v_mul_f32 v255, s20, v255\n" \
"v_mfma_f32_16x16x32_bf16 v[84:87], a[80:83], a[100:103], v[84:87]\n" \
"ds_read2_b32 a[18:19], v48 offset0:98 offset1:99\n" \
"v_cvt_pk_bf16_f32 v44, v252, v253\n" \
"v_mfma_f32_16x16x32_bf16 v[84:87], a[84:87], a[96:99], v[84:87]\n" \
"ds_read_b128 a[52:55], v50 offset:2688\n" \
"v_cvt_pk_bf16_f32 v45, v254, v255\n" \
"v_mfma_f32_16x16x32_bf16 v[88:91], a[64:67], a[120:123], v[88:91]\n" \
"v_mfma_f32_16x16x32_bf16 v[88:91], a[68:71], a[116:119], v[88:91]\n" \
"ds_read2_b32 a[20:21], v48 offset0:224 offset1:225\n" \
"v_mfma_f32_16x16x32_bf16 v[88:91], a[72:75], a[112:115], v[88:91]\n" \
"ds_read2_b32 a[22:23], v48 offset0:226 offset1:227\n" \
"v_mfma_f32_16x16x32_bf16 v[88:91], a[76:79], a[108:111], v[88:91]\n" \
"ds_read_b128 a[56:59], v50 offset:3200\n" \
"v_mfma_f32_16x16x32_bf16 v[88:91], a[80:83], a[104:107], v[88:91]\n" \
"buffer_store_dwordx4 v[30:33], v4, s[36:39], s96 offen offset:0 sc1\n" \
"v_mfma_f32_16x16x32_bf16 v[88:91], a[84:87], a[100:103], v[88:91]\n" \
"s_add_u32 s97, s96, s98\n" \
"ds_read2_b32 a[24:25], v49 offset0:96 offset1:97\n" \
"v_mfma_f32_16x16x32_bf16 v[88:91], a[88:91], a[96:99], v[88:91]\n" \
"ds_read2_b32 a[26:27], v49 offset0:98 offset1:99\n" \
"v_mfma_f32_16x16x32_bf16 v[92:95], a[64:67], a[124:127], v[92:95]\n" \
"ds_read_b128 a[60:63], v50 offset:3712\n" \
"v_mfma_f32_16x16x32_bf16 v[92:95], a[68:71], a[120:123], v[92:95]\n" \
"v_mfma_f32_16x16x32_bf16 v[92:95], a[72:75], a[116:119], v[92:95]\n" \
"ds_read2_b32 a[28:29], v49 offset0:224 offset1:225\n" \
"v_mfma_f32_16x16x32_bf16 v[92:95], a[76:79], a[112:115], v[92:95]\n" \
"ds_read2_b32 a[30:31], v49 offset0:226 offset1:227\n" \
"v_mfma_f32_16x16x32_bf16 v[92:95], a[80:83], a[108:111], v[92:95]\n" \
"v_mfma_f32_16x16x32_bf16 v[92:95], a[84:87], a[104:107], v[92:95]\n" \
"v_mfma_f32_16x16x32_bf16 v[92:95], a[88:91], a[100:103], v[92:95]\n" \
"v_mfma_f32_16x16x32_bf16 v[92:95], a[92:95], a[96:99], v[92:95]\n" \
"s_waitcnt lgkmcnt(0)\n" \
"v_mfma_f32_16x16x32_bf16 v[64:67], a[0:3], a[32:35], v[64:67]\n" \
"ds_read_b128 a[96:99], v50 offset:64\n" \
"v_mfma_f32_16x16x32_bf16 v[68:71], a[0:3], a[36:39], v[68:71]\n" \
"buffer_store_dwordx4 v[34:37], v4, s[36:39], s97 offen offset:0 sc1\n" \
"v_mfma_f32_16x16x32_bf16 v[68:71], a[4:7], a[32:35], v[68:71]\n" \
"s_add_u32 s96, s96, s92\n" \
"ds_read2_b32 a[64:65], v46 offset0:112 offset1:113\n" \
"v_mfma_f32_16x16x32_bf16 v[72:75], a[0:3], a[40:43], v[72:75]\n" \
"ds_read2_b32 a[66:67], v46 offset0:114 offset1:115\n" \
"v_mfma_f32_16x16x32_bf16 v[72:75], a[4:7], a[36:39], v[72:75]\n" \
"ds_read_b128 a[100:103], v50 offset:576\n" \
"v_mfma_f32_16x16x32_bf16 v[72:75], a[8:11], a[32:35], v[72:75]\n" \
"v_mfma_f32_16x16x32_bf16 v[76:79], a[0:3], a[44:47], v[76:79]\n" \
"ds_read2_b32 a[68:69], v46 offset0:240 offset1:241\n" \
"v_mfma_f32_16x16x32_bf16 v[76:79], a[4:7], a[40:43], v[76:79]\n" \
"ds_read2_b32 a[70:71], v46 offset0:242 offset1:243\n" \
"v_mfma_f32_16x16x32_bf16 v[76:79], a[8:11], a[36:39], v[76:79]\n" \
"ds_read_b128 a[104:107], v50 offset:1088\n" \
"v_mfma_f32_16x16x32_bf16 v[76:79], a[12:15], a[32:35], v[76:79]\n" \
"v_mfma_f32_16x16x32_bf16 v[80:83], a[0:3], a[48:51], v[80:83]\n" \
"ds_read2_b32 a[72:73], v47 offset0:112 offset1:113\n" \
"v_mfma_f32_16x16x32_bf16 v[80:83], a[4:7], a[44:47], v[80:83]\n" \
"ds_read2_b32 a[74:75], v47 offset0:114 offset1:115\n" \
"v_mfma_f32_16x16x32_bf16 v[80:83], a[8:11], a[40:43], v[80:83]\n" \
"ds_read_b128 a[108:111], v50 offset:1600\n" \
"v_mfma_f32_16x16x32_bf16 v[80:83], a[12:15], a[36:39], v[80:83]\n" \
"buffer_store_dwordx4 v[38:41], v4, s[36:39], s96 offen offset:0 sc1\n" \
"v_mfma_f32_16x16x32_bf16 v[80:83], a[16:19], a[32:35], v[80:83]\n" \
"s_add_u32 s97, s96, s98\n" \
"ds_read2_b32 a[76:77], v47 offset0:240 offset1:241\n" \
"v_mfma_f32_16x16x32_bf16 v[84:87], a[0:3], a[52:55], v[84:87]\n" \
"ds_read2_b32 a[78:79], v47 offset0:242 offset1:243\n" \
"v_mfma_f32_16x16x32_bf16 v[84:87], a[4:7], a[48:51], v[84:87]\n" \
"ds_read_b128 a[112:115], v50 offset:2112\n" \
"v_mfma_f32_16x16x32_bf16 v[84:87], a[8:11], a[44:47], v[84:87]\n" \
"v_mfma_f32_16x16x32_bf16 v[84:87], a[12:15], a[40:43], v[84:87]\n" \
"ds_read2_b32 a[80:81], v48 offset0:112 offset1:113\n" \
"v_mfma_f32_16x16x32_bf16 v[84:87], a[16:19], a[36:39], v[84:87]\n" \
"ds_read2_b32 a[82:83], v48 offset0:114 offset1:115\n" \
"v_mfma_f32_16x16x32_bf16 v[84:87], a[20:23], a[32:35], v[84:87]\n" \
"ds_read_b128 a[116:119], v50 offset:2624\n" \
"v_mfma_f32_16x16x32_bf16 v[88:91], a[0:3], a[56:59], v[88:91]\n" \
"v_mfma_f32_16x16x32_bf16 v[88:91], a[4:7], a[52:55], v[88:91]\n" \
"ds_read2_b32 a[84:85], v48 offset0:240 offset1:241\n" \
"v_mfma_f32_16x16x32_bf16 v[88:91], a[8:11], a[48:51], v[88:91]\n" \
"ds_read2_b32 a[86:87], v48 offset0:242 offset1:243\n" \
"v_mfma_f32_16x16x32_bf16 v[88:91], a[12:15], a[44:47], v[88:91]\n" \
"ds_read_b128 a[120:123], v50 offset:3136\n" \
"v_mfma_f32_16x16x32_bf16 v[88:91], a[16:19], a[40:43], v[88:91]\n" \
"buffer_store_dwordx4 v[42:45], v4, s[36:39], s97 offen offset:0 sc1\n" \
"v_mfma_f32_16x16x32_bf16 v[88:91], a[20:23], a[36:39], v[88:91]\n" \
"s_add_u32 s57, s57, s93\n" \
"ds_read2_b32 a[88:89], v49 offset0:112 offset1:113\n" \
"s_mov_b32 s96, s57\n" \
"v_mfma_f32_16x16x32_bf16 v[88:91], a[24:27], a[32:35], v[88:91]\n" \
"ds_read2_b32 a[90:91], v49 offset0:114 offset1:115\n" \
"v_mfma_f32_16x16x32_bf16 v[92:95], a[0:3], a[60:63], v[92:95]\n" \
"ds_read_b128 a[124:127], v50 offset:3648\n" \
"v_mfma_f32_16x16x32_bf16 v[92:95], a[4:7], a[56:59], v[92:95]\n" \
"v_mfma_f32_16x16x32_bf16 v[92:95], a[8:11], a[52:55], v[92:95]\n" \
"ds_read2_b32 a[92:93], v49 offset0:240 offset1:241\n" \
"v_mfma_f32_16x16x32_bf16 v[92:95], a[12:15], a[48:51], v[92:95]\n" \
"ds_read2_b32 a[94:95], v49 offset0:242 offset1:243\n" \
"v_mfma_f32_16x16x32_bf16 v[92:95], a[16:19], a[44:47], v[92:95]\n" \
"v_mfma_f32_16x16x32_bf16 v[92:95], a[20:23], a[40:43], v[92:95]\n" \
"v_mfma_f32_16x16x32_bf16 v[92:95], a[24:27], a[36:39], v[92:95]\n" \
"v_mfma_f32_16x16x32_bf16 v[92:95], a[28:31], a[32:35], v[92:95]\n" \
"s_waitcnt lgkmcnt(0)\n" \
"v_mfma_f32_16x16x32_bf16 v[64:67], a[64:67], a[96:99], v[64:67]\n" \
"s_lshl_b32 s59, %2, 10\n" \
"ds_read_b64 v[192:193], v51 offset:10816\n" \
"v_add_u32 v243, s59, v4\n" \
"v_mfma_f32_16x16x32_bf16 v[68:71], a[64:67], a[100:103], v[68:71]\n" \
"ds_read_b64 v[194:195], v51 offset:11328\n" \
"v_and_b32 v240, 0x70, v243\n" \
"v_mfma_f32_16x16x32_bf16 v[68:71], a[68:71], a[96:99], v[68:71]\n" \
"ds_read_b64 v[196:197], v51 offset:11840\n" \
"v_lshrrev_b32 v243, 7, v243\n" \
"v_mfma_f32_16x16x32_bf16 v[72:75], a[64:67], a[104:107], v[72:75]\n" \
"ds_read_b64 v[198:199], v51 offset:12352\n" \
"v_lshl_add_u32 v240, v243, 8, v240\n" \
"v_mfma_f32_16x16x32_bf16 v[72:75], a[68:71], a[100:103], v[72:75]\n" \
"ds_read_b64 v[200:201], v51 offset:12864\n" \
"v_lshrrev_b32 v241, 2, v240\n" \
"v_mfma_f32_16x16x32_bf16 v[72:75], a[72:75], a[96:99], v[72:75]\n" \
"s_lshl_b32 s59, s8, 14\n" \
"ds_read_b64 v[202:203], v51 offset:13376\n" \
"v_add_u32 v242, 32, v241\n" \
"v_mfma_f32_16x16x32_bf16 v[76:79], a[64:67], a[108:111], v[76:79]\n" \
"s_add_u32 s59, s59, 67108864\n" \
"ds_read_b64 v[204:205], v51 offset:13888\n" \
"s_add_u32 s60, s59, 8192\n" \
"v_mfma_f32_16x16x32_bf16 v[76:79], a[68:71], a[104:107], v[76:79]\n" \
"s_mov_b32 s94, 0x05040100\n" \
"ds_read_b64 v[206:207], v51 offset:14400\n" \
"s_mov_b32 s95, 0x07060302\n" \
"v_mfma_f32_16x16x32_bf16 v[76:79], a[72:75], a[100:103], v[76:79]\n" \
"v_mfma_f32_16x16x32_bf16 v[76:79], a[76:79], a[96:99], v[76:79]\n" \
"v_mfma_f32_16x16x32_bf16 v[80:83], a[64:67], a[112:115], v[80:83]\n" \
"v_mfma_f32_16x16x32_bf16 v[80:83], a[68:71], a[108:111], v[80:83]\n" \
"s_waitcnt vmcnt(36) lgkmcnt(0)\n" \
"v_mfma_f32_16x16x32_bf16 v[80:83], a[72:75], a[104:107], v[80:83]\n" \
"s_barrier\n" \
"ds_read_b64_tr_b16 a[24:25], v9 offset:33280\n" \
"v_and_b32 v208, 0xffff0000, v193\n" \
"v_mfma_f32_16x16x32_bf16 v[80:83], a[76:79], a[100:103], v[80:83]\n" \
"ds_read_b64_tr_b16 a[26:27], v10 offset:33280\n" \
"v_lshlrev_b32 v209, 16, v193\n" \
"v_mfma_f32_16x16x32_bf16 v[80:83], a[80:83], a[96:99], v[80:83]\n" \
"ds_read_b64_tr_b16 a[28:29], v11 offset:33280\n" \
"v_and_b32 v210, 0xffff0000, v192\n" \
"v_mfma_f32_16x16x32_bf16 v[84:87], a[64:67], a[116:119], v[84:87]\n" \
"ds_read_b64_tr_b16 a[30:31], v12 offset:33280\n" \
"v_lshlrev_b32 v211, 16, v192\n" \
"v_mfma_f32_16x16x32_bf16 v[84:87], a[68:71], a[112:115], v[84:87]\n" \
"s_mov_b32 s46, 256\n" \
"s_add_u32 m0, s50, 32768\n" \
"ds_read_b128 a[0:3], v7 offset:0\n" \
"v_mfma_f32_16x16x32_bf16 v[84:87], a[72:75], a[108:111], v[84:87]\n" \
"buffer_load_dwordx4 v13, s[28:31], s46 offen lds\n" \
"v_mfma_f32_16x16x32_bf16 v[84:87], a[76:79], a[104:107], v[84:87]\n" \
"ds_read_b128 a[4:7], v7 offset:2048\n" \
"v_fma_f32 v208, v208, v28, v64\n" \
"v_mfma_f32_16x16x32_bf16 v[84:87], a[80:83], a[100:103], v[84:87]\n" \
"ds_read_b128 a[8:11], v7 offset:4096\n" \
"v_fma_f32 v209, v209, v28, v65\n" \
"v_mfma_f32_16x16x32_bf16 v[84:87], a[84:87], a[96:99], v[84:87]\n" \
"ds_read_b128 a[12:15], v7 offset:6144\n" \
"v_fma_f32 v210, v210, v28, v66\n" \
"v_mfma_f32_16x16x32_bf16 v[88:91], a[64:67], a[120:123], v[88:91]\n" \
"ds_read_b64_tr_b16 a[32:33], v9 offset:33792\n" \
"v_fma_f32 v211, v211, v28, v67\n" \
"v_mfma_f32_16x16x32_bf16 v[88:91], a[68:71], a[116:119], v[88:91]\n" \
"s_add_u32 m0, s50, 36864\n" \
"ds_read_b64_tr_b16 a[34:35], v10 offset:33792\n" \
"v_and_b32 v212, 0xffff0000, v195\n" \
"v_mfma_f32_16x16x32_bf16 v[88:91], a[72:75], a[112:115], v[88:91]\n" \
"buffer_load_dwordx4 v14, s[28:31], s46 offen lds\n" \
"v_mfma_f32_16x16x32_bf16 v[88:91], a[76:79], a[108:111], v[88:91]\n" \
"ds_read_b64_tr_b16 a[36:37], v11 offset:33792\n" \
"v_lshlrev_b32 v213, 16, v195\n" \
"v_mfma_f32_16x16x32_bf16 v[88:91], a[80:83], a[104:107], v[88:91]\n" \
"ds_read_b64_tr_b16 a[38:39], v12 offset:33792\n" \
"v_and_b32 v214, 0xffff0000, v194\n" \
"v_mfma_f32_16x16x32_bf16 v[88:91], a[84:87], a[100:103], v[88:91]\n" \
"ds_read_b64_tr_b16 a[40:41], v9 offset:34304\n" \
"v_lshlrev_b32 v215, 16, v194\n" \
"v_mfma_f32_16x16x32_bf16 v[88:91], a[88:91], a[96:99], v[88:91]\n" \
"ds_read_b64_tr_b16 a[42:43], v10 offset:34304\n" \
"v_fma_f32 v212, v212, v28, v68\n" \
"v_mfma_f32_16x16x32_bf16 v[92:95], a[64:67], a[124:127], v[92:95]\n" \
"s_add_u32 m0, s50, 40960\n" \
"ds_read_b64_tr_b16 a[44:45], v11 offset:34304\n" \
"v_fma_f32 v213, v213, v28, v69\n" \
"v_mfma_f32_16x16x32_bf16 v[92:95], a[68:71], a[120:123], v[92:95]\n" \
"buffer_load_dwordx4 v15, s[28:31], s46 offen lds\n" \
"v_mfma_f32_16x16x32_bf16 v[92:95], a[72:75], a[116:119], v[92:95]\n" \
"ds_read_b64_tr_b16 a[46:47], v12 offset:34304\n" \
"v_fma_f32 v214, v214, v28, v70\n" \
"v_mfma_f32_16x16x32_bf16 v[92:95], a[76:79], a[112:115], v[92:95]\n" \
"ds_read_b64_tr_b16 a[16:17], v9 offset:32768\n" \
"v_fma_f32 v215, v215, v28, v71\n" \
"v_mfma_f32_16x16x32_bf16 v[92:95], a[80:83], a[108:111], v[92:95]\n" \
"ds_read_b64_tr_b16 a[18:19], v10 offset:32768\n" \
"v_and_b32 v216, 0xffff0000, v197\n" \
"v_mfma_f32_16x16x32_bf16 v[92:95], a[84:87], a[104:107], v[92:95]\n" \
"ds_read_b64_tr_b16 a[20:21], v11 offset:32768\n" \
"v_lshlrev_b32 v217, 16, v197\n" \
"v_mfma_f32_16x16x32_bf16 v[92:95], a[88:91], a[100:103], v[92:95]\n" \
"ds_read_b64_tr_b16 a[22:23], v12 offset:32768\n" \
"v_and_b32 v218, 0xffff0000, v196\n" \
"v_mfma_f32_16x16x32_bf16 v[92:95], a[92:95], a[96:99], v[92:95]\n" \
"s_waitcnt lgkmcnt(8)\n" \
"v_lshlrev_b32 v219, 16, v196\n" \
"v_fma_f32 v216, v216, v28, v72\n" \
"s_branch LHR3_%=\ns_nop 0\ns_nop 0\ns_nop 0\ns_nop 0\ns_nop 0\ns_nop 0\ns_nop 0\ns_nop 0\ns_nop 0\nLHR3_%=:\n" \
"v_mfma_f32_16x16x32_bf16 v[96:99], a[24:27], a[0:3], 0\n" \
"s_add_u32 m0, s50, 45056\n" \
"ds_read_b64_tr_b16 a[72:73], v9 offset:41472\n" \
"v_fma_f32 v217, v217, v28, v73\n" \
"v_mfma_f32_16x16x32_bf16 v[100:103], a[28:31], a[0:3], 0\n" \
"buffer_load_dwordx4 v16, s[28:31], s46 offen lds\n" \
"v_mfma_f32_16x16x32_bf16 v[104:107], a[24:27], a[4:7], 0\n" \
"ds_read_b64_tr_b16 a[74:75], v10 offset:41472\n" \
"v_fma_f32 v218, v218, v28, v74\n" \
"v_mfma_f32_16x16x32_bf16 v[108:111], a[28:31], a[4:7], 0\n" \
"ds_read_b64_tr_b16 a[76:77], v11 offset:41472\n" \
"v_fma_f32 v219, v219, v28, v75\n" \
"v_mfma_f32_16x16x32_bf16 v[112:115], a[24:27], a[8:11], 0\n" \
"s_mov_b32 s46, 256\n" \
"s_add_u32 m0, s50, 49152\n" \
"ds_read_b64_tr_b16 a[78:79], v12 offset:41472\n" \
"v_mfma_f32_16x16x32_bf16 v[116:119], a[28:31], a[8:11], 0\n" \
"buffer_load_dwordx4 v17, s[28:31], s46 offen lds\n" \
"v_mfma_f32_16x16x32_bf16 v[120:123], a[24:27], a[12:15], 0\n" \
"ds_read_b128 a[48:51], v8 offset:0\n" \
"v_and_b32 v220, 0xffff0000, v199\n" \
"v_mfma_f32_16x16x32_bf16 v[124:127], a[28:31], a[12:15], 0\n" \
"ds_read_b128 a[52:55], v8 offset:2048\n" \
"s_waitcnt lgkmcnt(6)\n" \
"v_mfma_f32_16x16x32_bf16 v[128:131], a[32:35], a[0:3], 0\n" \
"s_add_u32 m0, s50, 53248\n" \
"ds_read_b128 a[56:59], v8 offset:4096\n" \
"v_lshlrev_b32 v221, 16, v199\n" \
"v_mfma_f32_16x16x32_bf16 v[132:135], a[36:39], a[0:3], 0\n" \
"buffer_load_dwordx4 v18, s[28:31], s46 offen lds\n" \
"v_mfma_f32_16x16x32_bf16 v[136:139], a[32:35], a[4:7], 0\n" \
"ds_read_b128 a[60:63], v8 offset:6144\n" \
"v_and_b32 v222, 0xffff0000, v198\n" \
"v_mfma_f32_16x16x32_bf16 v[140:143], a[36:39], a[4:7], 0\n" \
"ds_read_b64_tr_b16 a[80:81], v9 offset:41984\n" \
"v_lshlrev_b32 v223, 16, v198\n" \
"v_mfma_f32_16x16x32_bf16 v[144:147], a[32:35], a[8:11], 0\n" \
"s_add_u32 m0, s50, 57344\n" \
"ds_read_b64_tr_b16 a[82:83], v10 offset:41984\n" \
"v_fma_f32 v220, v220, v28, v76\n" \
"v_mfma_f32_16x16x32_bf16 v[148:151], a[36:39], a[8:11], 0\n" \
"buffer_load_dwordx4 v19, s[28:31], s46 offen lds\n" \
"v_mfma_f32_16x16x32_bf16 v[152:155], a[32:35], a[12:15], 0\n" \
"ds_read_b64_tr_b16 a[84:85], v11 offset:41984\n" \
"v_fma_f32 v221, v221, v28, v77\n" \
"v_mfma_f32_16x16x32_bf16 v[156:159], a[36:39], a[12:15], 0\n" \
"ds_read_b64_tr_b16 a[86:87], v12 offset:41984\n" \
"v_fma_f32 v222, v222, v28, v78\n" \
"v_mfma_f32_16x16x32_bf16 v[160:163], a[40:43], a[0:3], 0\n" \
"s_add_u32 m0, s50, 61440\n" \
"ds_read_b64_tr_b16 a[88:89], v9 offset:42496\n" \
"v_fma_f32 v223, v223, v28, v79\n" \
"v_mfma_f32_16x16x32_bf16 v[164:167], a[44:47], a[0:3], 0\n" \
"buffer_load_dwordx4 v20, s[28:31], s46 offen lds\n" \
"v_mfma_f32_16x16x32_bf16 v[168:171], a[40:43], a[4:7], 0\n" \
"ds_read_b64_tr_b16 a[90:91], v10 offset:42496\n" \
"v_and_b32 v224, 0xffff0000, v201\n" \
"v_mfma_f32_16x16x32_bf16 v[172:175], a[44:47], a[4:7], 0\n" \
"ds_read_b64_tr_b16 a[92:93], v11 offset:42496\n" \
"v_lshlrev_b32 v225, 16, v201\n" \
"v_mfma_f32_16x16x32_bf16 v[176:179], a[40:43], a[8:11], 0\n" \
"s_mov_b32 s47, 1048576\n" \
"s_add_u32 m0, s51, 0\n" \
"ds_read_b64_tr_b16 a[94:95], v12 offset:42496\n" \
"v_mfma_f32_16x16x32_bf16 v[180:183], a[44:47], a[8:11], 0\n" \
"buffer_load_dwordx4 v21, s[32:35], s47 offen offset:0 lds\n" \
"v_mfma_f32_16x16x32_bf16 v[184:187], a[40:43], a[12:15], 0\n" \
"ds_read_b64_tr_b16 a[64:65], v9 offset:40960\n" \
"v_and_b32 v226, 0xffff0000, v200\n" \
"v_mfma_f32_16x16x32_bf16 v[188:191], a[44:47], a[12:15], 0\n" \
"ds_read_b64_tr_b16 a[66:67], v10 offset:40960\n" \
"v_lshlrev_b32 v227, 16, v200\n" \
"v_mfma_f32_16x16x32_bf16 v[64:67], a[16:19], a[0:3], 0\n" \
"s_add_u32 m0, s51, 896\n" \
"ds_read_b64_tr_b16 a[68:69], v11 offset:40960\n" \
"v_fma_f32 v224, v224, v28, v80\n" \
"v_mfma_f32_16x16x32_bf16 v[68:71], a[20:23], a[0:3], 0\n" \
"buffer_load_dwordx4 v21, s[32:35], s47 offen offset:128 lds\n" \
"v_mfma_f32_16x16x32_bf16 v[72:75], a[16:19], a[4:7], 0\n" \
"ds_read_b64_tr_b16 a[70:71], v12 offset:40960\n" \
"v_fma_f32 v225, v225, v28, v81\n" \
"v_fma_f32 v226, v226, v28, v82\n" \
"v_fma_f32 v227, v227, v28, v83\n" \
"v_mfma_f32_16x16x32_bf16 v[76:79], a[20:23], a[4:7], 0\n" \
"v_and_b32 v228, 0xffff0000, v203\n" \
"v_lshlrev_b32 v229, 16, v203\n" \
"v_and_b32 v230, 0xffff0000, v202\n" \
"v_lshlrev_b32 v231, 16, v202\n" \
"v_fma_f32 v228, v228, v28, v84\n" \
"v_fma_f32 v229, v229, v28, v85\n" \
"v_fma_f32 v230, v230, v28, v86\n" \
"v_fma_f32 v231, v231, v28, v87\n" \
"v_mfma_f32_16x16x32_bf16 v[80:83], a[16:19], a[8:11], 0\n" \
"v_and_b32 v232, 0xffff0000, v205\n" \
"v_lshlrev_b32 v233, 16, v205\n" \
"v_and_b32 v234, 0xffff0000, v204\n" \
"v_lshlrev_b32 v235, 16, v204\n" \
"v_fma_f32 v232, v232, v28, v88\n" \
"v_fma_f32 v233, v233, v28, v89\n" \
"v_fma_f32 v234, v234, v28, v90\n" \
"v_fma_f32 v235, v235, v28, v91\n" \
"v_mfma_f32_16x16x32_bf16 v[84:87], a[20:23], a[8:11], 0\n" \
"v_and_b32 v236, 0xffff0000, v207\n" \
"v_lshlrev_b32 v237, 16, v207\n" \
"v_and_b32 v238, 0xffff0000, v206\n" \
"v_lshlrev_b32 v239, 16, v206\n" \
"v_fma_f32 v236, v236, v28, v92\n" \
"v_fma_f32 v237, v237, v28, v93\n" \
"v_fma_f32 v238, v238, v28, v94\n" \
"v_fma_f32 v239, v239, v28, v95\n" \
"v_mfma_f32_16x16x32_bf16 v[88:91], a[16:19], a[12:15], 0\n" \
"s_waitcnt vmcnt(28) lgkmcnt(0)\n" \
"v_cvt_pk_bf16_f32 v192, v208, v209\n" \
"v_mfma_f32_16x16x32_bf16 v[92:95], a[20:23], a[12:15], 0\n" \
"s_barrier\n" \
"v_cvt_pk_bf16_f32 v193, v210, v211\n" \
"ds_write_b64 v55, v[192:193] offset:16384\n" \
"v_mfma_f32_16x16x32_bf16 v[96:99], a[72:75], a[48:51], v[96:99]\n" \
"s_mov_b32 s47, 1310720\n" \
"s_add_u32 m0, s51, 8192\n" \
"ds_read_b64_tr_b16 a[24:25], v9 offset:49664\n" \
"v_mfma_f32_16x16x32_bf16 v[100:103], a[76:79], a[48:51], v[100:103]\n" \
"buffer_load_dwordx4 v21, s[32:35], s47 offen offset:0 lds\n" \
"v_mfma_f32_16x16x32_bf16 v[104:107], a[72:75], a[52:55], v[104:107]\n" \
"ds_read_b64_tr_b16 a[26:27], v10 offset:49664\n" \
"v_cvt_pk_bf16_f32 v194, v212, v213\n" \
"v_mfma_f32_16x16x32_bf16 v[108:111], a[76:79], a[52:55], v[108:111]\n" \
"ds_read_b64_tr_b16 a[28:29], v11 offset:49664\n" \
"v_cvt_pk_bf16_f32 v195, v214, v215\n" \
"v_mfma_f32_16x16x32_bf16 v[112:115], a[72:75], a[56:59], v[112:115]\n" \
"LH9_%=:\ns_cbranch_execz LH13_%=\n" \
"s_add_u32 m0, s51, 9088\n" \
"ds_read_b64_tr_b16 a[30:31], v12 offset:49664\n" \
"v_accvgpr_read_b32 v46, a128\n" \
"v_mfma_f32_16x16x32_bf16 v[116:119], a[76:79], a[56:59], v[116:119]\n" \
"buffer_load_dwordx4 v21, s[32:35], s47 offen offset:128 lds\n" \
"v_mfma_f32_16x16x32_bf16 v[120:123], a[72:75], a[60:63], v[120:123]\n" \
"ds_read_b128 a[0:3], v5 offset:0\n" \
"v_accvgpr_read_b32 v47, a129\n" \
"v_mfma_f32_16x16x32_bf16 v[124:127], a[76:79], a[60:63], v[124:127]\n" \
"ds_read_b128 a[4:7], v5 offset:2048\n" \
"v_accvgpr_read_b32 v48, a130\n" \
"v_mfma_f32_16x16x32_bf16 v[128:131], a[80:83], a[48:51], v[128:131]\n" \
"s_mov_b32 s46, 384\n" \
"s_add_u32 m0, s50, 65536\n" \
"ds_read_b128 a[8:11], v5 offset:4096\n" \
"v_mfma_f32_16x16x32_bf16 v[132:135], a[84:87], a[48:51], v[132:135]\n" \
"buffer_load_dwordx4 v13, s[28:31], s46 offen lds\n" \
"v_mfma_f32_16x16x32_bf16 v[136:139], a[80:83], a[52:55], v[136:139]\n" \
"ds_read_b128 a[12:15], v5 offset:6144\n" \
"v_accvgpr_read_b32 v49, a131\n" \
"v_mfma_f32_16x16x32_bf16 v[140:143], a[84:87], a[52:55], v[140:143]\n" \
"ds_read_b64_tr_b16 a[32:33], v9 offset:50176\n" \
"v_mul_f32 v46, s20, v46\n" \
"v_mfma_f32_16x16x32_bf16 v[144:147], a[80:83], a[56:59], v[144:147]\n" \
"s_add_u32 m0, s50, 69632\n" \
"ds_read_b64_tr_b16 a[34:35], v10 offset:50176\n" \
"v_mul_f32 v47, s20, v47\n" \
"v_mfma_f32_16x16x32_bf16 v[148:151], a[84:87], a[56:59], v[148:151]\n" \
"buffer_load_dwordx4 v14, s[28:31], s46 offen lds\n" \
"v_mfma_f32_16x16x32_bf16 v[152:155], a[80:83], a[60:63], v[152:155]\n" \
"ds_read_b64_tr_b16 a[36:37], v11 offset:50176\n" \
"v_mul_f32 v48, s20, v48\n" \
"v_mfma_f32_16x16x32_bf16 v[156:159], a[84:87], a[60:63], v[156:159]\n" \
"ds_read_b64_tr_b16 a[38:39], v12 offset:50176\n" \
"v_mul_f32 v49, s20, v49\n" \
"v_mfma_f32_16x16x32_bf16 v[160:163], a[88:91], a[48:51], v[160:163]\n" \
"s_add_u32 m0, s50, 73728\n" \
"ds_read_b64_tr_b16 a[40:41], v9 offset:50688\n" \
"v_cvt_pk_bf16_f32 v30, v46, v47\n" \
"v_mfma_f32_16x16x32_bf16 v[164:167], a[92:95], a[48:51], v[164:167]\n" \
"buffer_load_dwordx4 v15, s[28:31], s46 offen lds\n" \
"v_mfma_f32_16x16x32_bf16 v[168:171], a[88:91], a[52:55], v[168:171]\n" \
"ds_read_b64_tr_b16 a[42:43], v10 offset:50688\n" \
"v_cvt_pk_bf16_f32 v31, v48, v49\n" \
"v_mfma_f32_16x16x32_bf16 v[172:175], a[92:95], a[52:55], v[172:175]\n" \
"ds_read_b64_tr_b16 a[44:45], v11 offset:50688\n" \
"v_accvgpr_read_b32 v50, a132\n" \
"v_mfma_f32_16x16x32_bf16 v[176:179], a[88:91], a[56:59], v[176:179]\n" \
"s_add_u32 m0, s50, 77824\n" \
"ds_read_b64_tr_b16 a[46:47], v12 offset:50688\n" \
"v_accvgpr_read_b32 v51, a133\n" \
"v_mfma_f32_16x16x32_bf16 v[180:183], a[92:95], a[56:59], v[180:183]\n" \
"buffer_load_dwordx4 v16, s[28:31], s46 offen lds\n" \
"v_mfma_f32_16x16x32_bf16 v[184:187], a[88:91], a[60:63], v[184:187]\n" \
"ds_read_b64_tr_b16 a[16:17], v9 offset:49152\n" \
"v_accvgpr_read_b32 v52, a134\n" \
"v_mfma_f32_16x16x32_bf16 v[188:191], a[92:95], a[60:63], v[188:191]\n" \
"ds_read_b64_tr_b16 a[18:19], v10 offset:49152\n" \
"v_accvgpr_read_b32 v53, a135\n" \
"v_mfma_f32_16x16x32_bf16 v[64:67], a[64:67], a[48:51], v[64:67]\n" \
"ds_read_b64_tr_b16 a[20:21], v11 offset:49152\n" \
"v_mul_f32 v50, s20, v50\n" \
"v_mfma_f32_16x16x32_bf16 v[68:71], a[68:71], a[48:51], v[68:71]\n" \
"ds_read_b64_tr_b16 a[22:23], v12 offset:49152\n" \
"v_mul_f32 v51, s20, v51\n" \
"v_mfma_f32_16x16x32_bf16 v[72:75], a[64:67], a[52:55], v[72:75]\n" \
"ds_write_b64 v55, v[194:195] offset:16896\n" \
"v_cvt_pk_bf16_f32 v196, v216, v217\n" \
"v_mfma_f32_16x16x32_bf16 v[76:79], a[68:71], a[52:55], v[76:79]\n" \
"v_cvt_pk_bf16_f32 v197, v218, v219\n" \
"ds_write_b64 v55, v[196:197] offset:17408\n" \
"v_cvt_pk_bf16_f32 v198, v220, v221\n" \
"v_cvt_pk_bf16_f32 v199, v222, v223\n" \
"ds_write_b64 v55, v[198:199] offset:17920\n" \
"v_cvt_pk_bf16_f32 v200, v224, v225\n" \
"v_cvt_pk_bf16_f32 v201, v226, v227\n" \
"ds_write_b64 v55, v[200:201] offset:18432\n" \
"v_cvt_pk_bf16_f32 v202, v228, v229\n" \
"v_cvt_pk_bf16_f32 v203, v230, v231\n" \
"ds_write_b64 v55, v[202:203] offset:18944\n" \
"v_cvt_pk_bf16_f32 v204, v232, v233\n" \
"v_cvt_pk_bf16_f32 v205, v234, v235\n" \
"ds_write_b64 v55, v[204:205] offset:19456\n" \
"v_cvt_pk_bf16_f32 v206, v236, v237\n" \
"v_cvt_pk_bf16_f32 v207, v238, v239\n" \
"ds_write_b64 v55, v[206:207] offset:19968\n" \
"v_mfma_f32_16x16x32_bf16 v[80:83], a[64:67], a[56:59], v[80:83]\n" \
"v_mul_f32 v52, s20, v52\n" \
"v_mul_f32 v53, s20, v53\n" \
"v_mfma_f32_16x16x32_bf16 v[84:87], a[68:71], a[56:59], v[84:87]\n" \
"v_cvt_pk_bf16_f32 v32, v50, v51\n" \
"v_mfma_f32_16x16x32_bf16 v[88:91], a[64:67], a[60:63], v[88:91]\n" \
"v_cvt_pk_bf16_f32 v33, v52, v53\n" \
"v_accvgpr_read_b32 v46, a136\n" \
"v_mfma_f32_16x16x32_bf16 v[92:95], a[68:71], a[60:63], v[92:95]\n" \
"s_waitcnt lgkmcnt(0)\n" \
"s_barrier\n" \
"v_accvgpr_read_b32 v47, a137\n" \
"v_mfma_f32_16x16x32_bf16 v[96:99], a[24:27], a[0:3], v[96:99]\n" \
"s_mov_b32 s46, 384\n" \
"s_add_u32 m0, s50, 81920\n" \
"ds_read_b64_tr_b16 a[72:73], v9 offset:57856\n" \
"v_mfma_f32_16x16x32_bf16 v[100:103], a[28:31], a[0:3], v[100:103]\n" \
"buffer_load_dwordx4 v17, s[28:31], s46 offen lds\n" \
"v_mfma_f32_16x16x32_bf16 v[104:107], a[24:27], a[4:7], v[104:107]\n" \
"ds_read_b64_tr_b16 a[74:75], v10 offset:57856\n" \
"v_accvgpr_read_b32 v48, a138\n" \
"v_mfma_f32_16x16x32_bf16 v[108:111], a[28:31], a[4:7], v[108:111]\n" \
"ds_read_b64_tr_b16 a[76:77], v11 offset:57856\n" \
"v_accvgpr_read_b32 v49, a139\n" \
"v_mfma_f32_16x16x32_bf16 v[112:115], a[24:27], a[8:11], v[112:115]\n" \
"s_add_u32 m0, s50, 86016\n" \
"ds_read_b64_tr_b16 a[78:79], v12 offset:57856\n" \
"v_mul_f32 v46, s20, v46\n" \
"v_mfma_f32_16x16x32_bf16 v[116:119], a[28:31], a[8:11], v[116:119]\n" \
"buffer_load_dwordx4 v18, s[28:31], s46 offen lds\n" \
"v_mfma_f32_16x16x32_bf16 v[120:123], a[24:27], a[12:15], v[120:123]\n" \
"ds_read_b128 a[48:51], v6 offset:0\n" \
"v_mul_f32 v47, s20, v47\n" \
"v_mfma_f32_16x16x32_bf16 v[124:127], a[28:31], a[12:15], v[124:127]\n" \
"ds_read_b128 a[52:55], v6 offset:2048\n" \
"v_mul_f32 v48, s20, v48\n" \
"v_mfma_f32_16x16x32_bf16 v[128:131], a[32:35], a[0:3], v[128:131]\n" \
"s_add_u32 m0, s50, 90112\n" \
"ds_read_b128 a[56:59], v6 offset:4096\n" \
"v_mul_f32 v49, s20, v49\n" \
"v_mfma_f32_16x16x32_bf16 v[132:135], a[36:39], a[0:3], v[132:135]\n" \
"buffer_load_dwordx4 v19, s[28:31], s46 offen lds\n" \
"v_mfma_f32_16x16x32_bf16 v[136:139], a[32:35], a[4:7], v[136:139]\n" \
"ds_read_b128 a[60:63], v6 offset:6144\n" \
"v_cvt_pk_bf16_f32 v34, v46, v47\n" \
"v_mfma_f32_16x16x32_bf16 v[140:143], a[36:39], a[4:7], v[140:143]\n" \
"ds_read_b64_tr_b16 a[80:81], v9 offset:58368\n" \
"v_cvt_pk_bf16_f32 v35, v48, v49\n" \
"v_mfma_f32_16x16x32_bf16 v[144:147], a[32:35], a[8:11], v[144:147]\n" \
"s_add_u32 m0, s50, 94208\n" \
"ds_read_b64_tr_b16 a[82:83], v10 offset:58368\n" \
"v_accvgpr_read_b32 v50, a140\n" \
"v_mfma_f32_16x16x32_bf16 v[148:151], a[36:39], a[8:11], v[148:151]\n" \
"buffer_load_dwordx4 v20, s[28:31], s46 offen lds\n" \
"v_mfma_f32_16x16x32_bf16 v[152:155], a[32:35], a[12:15], v[152:155]\n" \
"ds_read_b64_tr_b16 a[84:85], v11 offset:58368\n" \
"v_accvgpr_read_b32 v51, a141\n" \
"v_mfma_f32_16x16x32_bf16 v[156:159], a[36:39], a[12:15], v[156:159]\n" \
"ds_read_b64_tr_b16 a[86:87], v12 offset:58368\n" \
"v_accvgpr_read_b32 v52, a142\n" \
"v_mfma_f32_16x16x32_bf16 v[160:163], a[40:43], a[0:3], v[160:163]\n" \
"s_mov_b32 s47, 2097152\n" \
"s_add_u32 m0, s51, 32768\n" \
"ds_read_b64_tr_b16 a[88:89], v9 offset:58880\n" \
"v_mfma_f32_16x16x32_bf16 v[164:167], a[44:47], a[0:3], v[164:167]\n" \
"buffer_load_dwordx4 v21, s[32:35], s47 offen offset:0 lds\n" \
"v_mfma_f32_16x16x32_bf16 v[168:171], a[40:43], a[4:7], v[168:171]\n" \
"ds_read_b64_tr_b16 a[90:91], v10 offset:58880\n" \
"v_accvgpr_read_b32 v53, a143\n" \
"v_mfma_f32_16x16x32_bf16 v[172:175], a[44:47], a[4:7], v[172:175]\n" \
"ds_read_b64_tr_b16 a[92:93], v11 offset:58880\n" \
"v_mul_f32 v50, s20, v50\n" \
"v_mfma_f32_16x16x32_bf16 v[176:179], a[40:43], a[8:11], v[176:179]\n" \
"s_add_u32 m0, s51, 33664\n" \
"ds_read_b64_tr_b16 a[94:95], v12 offset:58880\n" \
"v_mul_f32 v51, s20, v51\n" \
"v_mfma_f32_16x16x32_bf16 v[180:183], a[44:47], a[8:11], v[180:183]\n" \
"buffer_load_dwordx4 v21, s[32:35], s47 offen offset:128 lds\n" \
"v_mfma_f32_16x16x32_bf16 v[184:187], a[40:43], a[12:15], v[184:187]\n" \
"ds_read_b64_tr_b16 a[64:65], v9 offset:57344\n" \
"v_mul_f32 v52, s20, v52\n" \
"v_mfma_f32_16x16x32_bf16 v[188:191], a[44:47], a[12:15], v[188:191]\n" \
"ds_read_b64_tr_b16 a[66:67], v10 offset:57344\n" \
"v_mul_f32 v53, s20, v53\n" \
"v_mfma_f32_16x16x32_bf16 v[64:67], a[16:19], a[0:3], v[64:67]\n" \
"ds_read_b64_tr_b16 a[68:69], v11 offset:57344\n" \
"v_cvt_pk_bf16_f32 v36, v50, v51\n" \
"v_mfma_f32_16x16x32_bf16 v[68:71], a[20:23], a[0:3], v[68:71]\n" \
"ds_read_b64_tr_b16 a[70:71], v12 offset:57344\n" \
"v_cvt_pk_bf16_f32 v37, v52, v53\n" \
"v_mfma_f32_16x16x32_bf16 v[72:75], a[16:19], a[4:7], v[72:75]\n" \
"buffer_store_dwordx4 v[30:33], v4, s[36:39], s96 offen offset:0 sc1\n" \
"v_mfma_f32_16x16x32_bf16 v[76:79], a[20:23], a[4:7], v[76:79]\n" \
"s_add_u32 s97, s96, s98\n" \
"ds_read2st64_b32 v[208:209], v241 offset0:64 offset1:80\n" \
"ds_read2st64_b32 v[210:211], v241 offset0:96 offset1:112\n" \
"ds_read2st64_b32 v[212:213], v242 offset0:64 offset1:80\n" \
"ds_read2st64_b32 v[214:215], v242 offset0:96 offset1:112\n" \
"ds_read2st64_b32 v[216:217], v241 offset0:72 offset1:88\n" \
"ds_read2st64_b32 v[218:219], v241 offset0:104 offset1:120\n" \
"ds_read2st64_b32 v[220:221], v242 offset0:72 offset1:88\n" \
"ds_read2st64_b32 v[222:223], v242 offset0:104 offset1:120\n" \
"v_mfma_f32_16x16x32_bf16 v[80:83], a[16:19], a[8:11], v[80:83]\n" \
"v_accvgpr_read_b32 v46, a144\n" \
"v_accvgpr_read_b32 v47, a145\n" \
"v_mfma_f32_16x16x32_bf16 v[84:87], a[20:23], a[8:11], v[84:87]\n" \
"v_accvgpr_read_b32 v48, a146\n" \
"v_accvgpr_read_b32 v49, a147\n" \
"v_mfma_f32_16x16x32_bf16 v[88:91], a[16:19], a[12:15], v[88:91]\n" \
"s_waitcnt vmcnt(11) lgkmcnt(0)\n" \
"v_mul_f32 v46, s20, v46\n" \
"v_mfma_f32_16x16x32_bf16 v[92:95], a[20:23], a[12:15], v[92:95]\n" \
"s_barrier\n" \
"v_mul_f32 v47, s20, v47\n" \
"v_mul_f32 v48, s20, v48\n" \
"v_mfma_f32_16x16x32_bf16 v[96:99], a[72:75], a[48:51], v[96:99]\n" \
"s_mov_b32 s47, 1572864\n" \
"s_add_u32 m0, s51, 16384\n" \
"ds_read_b64_tr_b16 a[24:25], v9 offset:512\n" \
"v_mfma_f32_16x16x32_bf16 v[100:103], a[76:79], a[48:51], v[100:103]\n" \
"buffer_load_dwordx4 v21, s[32:35], s47 offen offset:0 lds\n" \
"v_mfma_f32_16x16x32_bf16 v[104:107], a[72:75], a[52:55], v[104:107]\n" \
"ds_read_b64_tr_b16 a[26:27], v10 offset:512\n" \
"v_perm_b32 v224, v209, v208, s94\n" \
"v_mfma_f32_16x16x32_bf16 v[108:111], a[76:79], a[52:55], v[108:111]\n" \
"ds_read_b64_tr_b16 a[28:29], v11 offset:512\n" \
"v_perm_b32 v225, v211, v210, s94\n" \
"v_mfma_f32_16x16x32_bf16 v[112:115], a[72:75], a[56:59], v[112:115]\n" \
"s_add_u32 m0, s51, 17280\n" \
"ds_read_b64_tr_b16 a[30:31], v12 offset:512\n" \
"v_perm_b32 v226, v213, v212, s94\n" \
"v_mfma_f32_16x16x32_bf16 v[116:119], a[76:79], a[56:59], v[116:119]\n" \
"buffer_load_dwordx4 v21, s[32:35], s47 offen offset:128 lds\n" \
"v_mfma_f32_16x16x32_bf16 v[120:123], a[72:75], a[60:63], v[120:123]\n" \
"ds_read_b128 a[0:3], v5 offset:32768\n" \
"v_perm_b32 v227, v215, v214, s94\n" \
"v_mfma_f32_16x16x32_bf16 v[124:127], a[76:79], a[60:63], v[124:127]\n" \
"ds_read_b128 a[4:7], v5 offset:34816\n" \
"v_perm_b32 v228, v209, v208, s95\n" \
"v_mfma_f32_16x16x32_bf16 v[128:131], a[80:83], a[48:51], v[128:131]\n" \
"s_mov_b32 s47, 1835008\n" \
"s_add_u32 m0, s51, 24576\n" \
"ds_read_b128 a[8:11], v5 offset:36864\n" \
"v_mfma_f32_16x16x32_bf16 v[132:135], a[84:87], a[48:51], v[132:135]\n" \
"buffer_load_dwordx4 v21, s[32:35], s47 offen offset:0 lds\n" \
"v_mfma_f32_16x16x32_bf16 v[136:139], a[80:83], a[52:55], v[136:139]\n" \
"ds_read_b128 a[12:15], v5 offset:38912\n" \
"v_perm_b32 v229, v211, v210, s95\n" \
"v_mfma_f32_16x16x32_bf16 v[140:143], a[84:87], a[52:55], v[140:143]\n" \
"ds_read_b64_tr_b16 a[32:33], v9 offset:1024\n" \
"v_perm_b32 v230, v213, v212, s95\n" \
"v_mfma_f32_16x16x32_bf16 v[144:147], a[80:83], a[56:59], v[144:147]\n" \
"s_add_u32 m0, s51, 25472\n" \
"ds_read_b64_tr_b16 a[34:35], v10 offset:1024\n" \
"v_perm_b32 v231, v215, v214, s95\n" \
"v_mfma_f32_16x16x32_bf16 v[148:151], a[84:87], a[56:59], v[148:151]\n" \
"buffer_load_dwordx4 v21, s[32:35], s47 offen offset:128 lds\n" \
"v_mfma_f32_16x16x32_bf16 v[152:155], a[80:83], a[60:63], v[152:155]\n" \
"ds_read_b64_tr_b16 a[36:37], v11 offset:1024\n" \
"v_perm_b32 v232, v217, v216, s94\n" \
"v_mfma_f32_16x16x32_bf16 v[156:159], a[84:87], a[60:63], v[156:159]\n" \
"ds_read_b64_tr_b16 a[38:39], v12 offset:1024\n" \
"v_perm_b32 v233, v219, v218, s94\n" \
"v_mfma_f32_16x16x32_bf16 v[160:163], a[88:91], a[48:51], v[160:163]\n" \
"s_mov_b32 s46, 512\n" \
"s_add_u32 m0, s50, 0\n" \
"ds_read_b64_tr_b16 a[40:41], v9 offset:1536\n" \
"v_mfma_f32_16x16x32_bf16 v[164:167], a[92:95], a[48:51], v[164:167]\n" \
"buffer_load_dwordx4 v13, s[28:31], s46 offen lds\n" \
"v_mfma_f32_16x16x32_bf16 v[168:171], a[88:91], a[52:55], v[168:171]\n" \
"ds_read_b64_tr_b16 a[42:43], v10 offset:1536\n" \
"v_perm_b32 v234, v221, v220, s94\n" \
"v_mfma_f32_16x16x32_bf16 v[172:175], a[92:95], a[52:55], v[172:175]\n" \
"ds_read_b64_tr_b16 a[44:45], v11 offset:1536\n" \
"v_perm_b32 v235, v223, v222, s94\n" \
"v_mfma_f32_16x16x32_bf16 v[176:179], a[88:91], a[56:59], v[176:179]\n" \
"s_add_u32 m0, s50, 4096\n" \
"ds_read_b64_tr_b16 a[46:47], v12 offset:1536\n" \
"v_perm_b32 v236, v217, v216, s95\n" \
"v_mfma_f32_16x16x32_bf16 v[180:183], a[92:95], a[56:59], v[180:183]\n" \
"buffer_load_dwordx4 v14, s[28:31], s46 offen lds\n" \
"v_mfma_f32_16x16x32_bf16 v[184:187], a[88:91], a[60:63], v[184:187]\n" \
"ds_read_b64_tr_b16 a[16:17], v9 offset:0\n" \
"v_perm_b32 v237, v219, v218, s95\n" \
"v_mfma_f32_16x16x32_bf16 v[188:191], a[92:95], a[60:63], v[188:191]\n" \
"ds_read_b64_tr_b16 a[18:19], v10 offset:0\n" \
"v_perm_b32 v238, v221, v220, s95\n" \
"v_mfma_f32_16x16x32_bf16 v[64:67], a[64:67], a[48:51], v[64:67]\n" \
"s_add_u32 m0, s50, 8192\n" \
"ds_read_b64_tr_b16 a[20:21], v11 offset:0\n" \
"v_perm_b32 v239, v223, v222, s95\n" \
"v_mfma_f32_16x16x32_bf16 v[68:71], a[68:71], a[48:51], v[68:71]\n" \
"buffer_load_dwordx4 v15, s[28:31], s46 offen lds\n" \
"v_mfma_f32_16x16x32_bf16 v[72:75], a[64:67], a[52:55], v[72:75]\n" \
"ds_read_b64_tr_b16 a[22:23], v12 offset:0\n" \
"v_mul_f32 v49, s20, v49\n" \
"v_mfma_f32_16x16x32_bf16 v[76:79], a[68:71], a[52:55], v[76:79]\n" \
"buffer_store_dwordx4 v[224:227], v240, s[36:39], s59 offen offset:0 sc1\n" \
"v_mfma_f32_16x16x32_bf16 v[80:83], a[64:67], a[56:59], v[80:83]\n" \
"s_add_u32 m0, s50, 12288\n" \
"v_cvt_pk_bf16_f32 v38, v46, v47\n" \
"v_cvt_pk_bf16_f32 v39, v48, v49\n" \
"v_mfma_f32_16x16x32_bf16 v[84:87], a[68:71], a[56:59], v[84:87]\n" \
"buffer_load_dwordx4 v16, s[28:31], s46 offen lds\n" \
"v_mfma_f32_16x16x32_bf16 v[88:91], a[64:67], a[60:63], v[88:91]\n" \
"v_accvgpr_read_b32 v50, a148\n" \
"v_accvgpr_read_b32 v51, a149\n" \
"v_mfma_f32_16x16x32_bf16 v[92:95], a[68:71], a[60:63], v[92:95]\n" \
"s_waitcnt lgkmcnt(0)\n" \
"v_accvgpr_read_b32 v52, a150\n" \
"v_accvgpr_read_b32 v53, a151\n" \
"v_mfma_f32_16x16x32_bf16 v[96:99], a[24:27], a[0:3], v[96:99]\n" \
"s_mov_b32 s47, 2359296\n" \
"s_add_u32 m0, s51, 40960\n" \
"ds_read_b64_tr_b16 a[72:73], v9 offset:8704\n" \
"v_mfma_f32_16x16x32_bf16 v[100:103], a[28:31], a[0:3], v[100:103]\n" \
"buffer_load_dwordx4 v21, s[32:35], s47 offen offset:0 lds\n" \
"v_mfma_f32_16x16x32_bf16 v[104:107], a[24:27], a[4:7], v[104:107]\n" \
"ds_read_b64_tr_b16 a[74:75], v10 offset:8704\n" \
"v_mul_f32 v50, s20, v50\n" \
"v_mfma_f32_16x16x32_bf16 v[108:111], a[28:31], a[4:7], v[108:111]\n" \
"ds_read_b64_tr_b16 a[76:77], v11 offset:8704\n" \
"v_mul_f32 v51, s20, v51\n" \
"v_mfma_f32_16x16x32_bf16 v[112:115], a[24:27], a[8:11], v[112:115]\n" \
"s_add_u32 m0, s51, 41856\n" \
"ds_read_b64_tr_b16 a[78:79], v12 offset:8704\n" \
"v_mul_f32 v52, s20, v52\n" \
"v_mfma_f32_16x16x32_bf16 v[116:119], a[28:31], a[8:11], v[116:119]\n" \
"buffer_load_dwordx4 v21, s[32:35], s47 offen offset:128 lds\n" \
"v_mfma_f32_16x16x32_bf16 v[120:123], a[24:27], a[12:15], v[120:123]\n" \
"ds_read_b128 a[48:51], v6 offset:32768\n" \
"v_mul_f32 v53, s20, v53\n" \
"v_mfma_f32_16x16x32_bf16 v[124:127], a[28:31], a[12:15], v[124:127]\n" \
"ds_read_b128 a[52:55], v6 offset:34816\n" \
"v_cvt_pk_bf16_f32 v40, v50, v51\n" \
"v_mfma_f32_16x16x32_bf16 v[128:131], a[32:35], a[0:3], v[128:131]\n" \
"s_mov_b32 s46, 512\n" \
"s_add_u32 m0, s50, 16384\n" \
"ds_read_b128 a[56:59], v6 offset:36864\n" \
"v_mfma_f32_16x16x32_bf16 v[132:135], a[36:39], a[0:3], v[132:135]\n" \
"buffer_load_dwordx4 v17, s[28:31], s46 offen lds\n" \
"v_mfma_f32_16x16x32_bf16 v[136:139], a[32:35], a[4:7], v[136:139]\n" \
"ds_read_b128 a[60:63], v6 offset:38912\n" \
"v_cvt_pk_bf16_f32 v41, v52, v53\n" \
"v_mfma_f32_16x16x32_bf16 v[140:143], a[36:39], a[4:7], v[140:143]\n" \
"ds_read_b64_tr_b16 a[80:81], v9 offset:9216\n" \
"v_accvgpr_read_b32 v46, a152\n" \
"v_mfma_f32_16x16x32_bf16 v[144:147], a[32:35], a[8:11], v[144:147]\n" \
"s_add_u32 m0, s50, 20480\n" \
"ds_read_b64_tr_b16 a[82:83], v10 offset:9216\n" \
"v_accvgpr_read_b32 v47, a153\n" \
"v_mfma_f32_16x16x32_bf16 v[148:151], a[36:39], a[8:11], v[148:151]\n" \
"buffer_load_dwordx4 v18, s[28:31], s46 offen lds\n" \
"v_mfma_f32_16x16x32_bf16 v[152:155], a[32:35], a[12:15], v[152:155]\n" \
"ds_read_b64_tr_b16 a[84:85], v11 offset:9216\n" \
"v_accvgpr_read_b32 v48, a154\n" \
"v_mfma_f32_16x16x32_bf16 v[156:159], a[36:39], a[12:15], v[156:159]\n" \
"ds_read_b64_tr_b16 a[86:87], v12 offset:9216\n" \
"v_accvgpr_read_b32 v49, a155\n" \
"v_mfma_f32_16x16x32_bf16 v[160:163], a[40:43], a[0:3], v[160:163]\n" \
"s_add_u32 m0, s50, 24576\n" \
"ds_read_b64_tr_b16 a[88:89], v9 offset:9728\n" \
"v_mul_f32 v46, s20, v46\n" \
"v_mfma_f32_16x16x32_bf16 v[164:167], a[44:47], a[0:3], v[164:167]\n" \
"buffer_load_dwordx4 v19, s[28:31], s46 offen lds\n" \
"v_mfma_f32_16x16x32_bf16 v[168:171], a[40:43], a[4:7], v[168:171]\n" \
"ds_read_b64_tr_b16 a[90:91], v10 offset:9728\n" \
"v_mul_f32 v47, s20, v47\n" \
"v_mfma_f32_16x16x32_bf16 v[172:175], a[44:47], a[4:7], v[172:175]\n" \
"ds_read_b64_tr_b16 a[92:93], v11 offset:9728\n" \
"v_mul_f32 v48, s20, v48\n" \
"v_mfma_f32_16x16x32_bf16 v[176:179], a[40:43], a[8:11], v[176:179]\n" \
"s_add_u32 m0, s50, 28672\n" \
"ds_read_b64_tr_b16 a[94:95], v12 offset:9728\n" \
"v_mul_f32 v49, s20, v49\n" \
"v_mfma_f32_16x16x32_bf16 v[180:183], a[44:47], a[8:11], v[180:183]\n" \
"buffer_load_dwordx4 v20, s[28:31], s46 offen lds\n" \
"v_mfma_f32_16x16x32_bf16 v[184:187], a[40:43], a[12:15], v[184:187]\n" \
"ds_read_b64_tr_b16 a[64:65], v9 offset:8192\n" \
"v_cvt_pk_bf16_f32 v42, v46, v47\n" \
"v_mfma_f32_16x16x32_bf16 v[188:191], a[44:47], a[12:15], v[188:191]\n" \
"ds_read_b64_tr_b16 a[66:67], v10 offset:8192\n" \
"v_cvt_pk_bf16_f32 v43, v48, v49\n" \
"v_mfma_f32_16x16x32_bf16 v[64:67], a[16:19], a[0:3], v[64:67]\n" \
"s_mov_b32 s47, 2621440\n" \
"s_add_u32 m0, s51, 49152\n" \
"ds_read_b64_tr_b16 a[68:69], v11 offset:8192\n" \
"v_mfma_f32_16x16x32_bf16 v[68:71], a[20:23], a[0:3], v[68:71]\n" \
"buffer_load_dwordx4 v21, s[32:35], s47 offen offset:0 lds\n" \
"v_mfma_f32_16x16x32_bf16 v[72:75], a[16:19], a[4:7], v[72:75]\n" \
"ds_read_b64_tr_b16 a[70:71], v12 offset:8192\n" \
"v_accvgpr_read_b32 v50, a156\n" \
"v_mfma_f32_16x16x32_bf16 v[76:79], a[20:23], a[4:7], v[76:79]\n" \
"buffer_store_dwordx4 v[228:231], v240, s[36:39], s59 offen offset:128 sc1\n" \
"v_mfma_f32_16x16x32_bf16 v[80:83], a[16:19], a[8:11], v[80:83]\n" \
"s_add_u32 m0, s51, 50048\n" \
"v_accvgpr_read_b32 v51, a157\n" \
"v_accvgpr_read_b32 v52, a158\n" \
"v_mfma_f32_16x16x32_bf16 v[84:87], a[20:23], a[8:11], v[84:87]\n" \
"buffer_load_dwordx4 v21, s[32:35], s47 offen offset:128 lds\n" \
"v_mfma_f32_16x16x32_bf16 v[88:91], a[16:19], a[12:15], v[88:91]\n" \
"s_waitcnt vmcnt(14) lgkmcnt(0)\n" \
"v_accvgpr_read_b32 v53, a159\n" \
"v_mul_f32 v50, s20, v50\n" \
"v_mfma_f32_16x16x32_bf16 v[92:95], a[20:23], a[12:15], v[92:95]\n" \
"s_barrier\n" \
"v_mul_f32 v51, s20, v51\n" \
"v_mul_f32 v52, s20, v52\n" \
"v_mfma_f32_16x16x32_bf16 v[96:99], a[72:75], a[48:51], v[96:99]\n" \
"s_mov_b32 s47, 2883584\n" \
"s_add_u32 m0, s51, 57344\n" \
"ds_read_b64_tr_b16 a[24:25], v9 offset:16896\n" \
"v_mfma_f32_16x16x32_bf16 v[100:103], a[76:79], a[48:51], v[100:103]\n" \
"buffer_load_dwordx4 v21, s[32:35], s47 offen offset:0 lds\n" \
"v_mfma_f32_16x16x32_bf16 v[104:107], a[72:75], a[52:55], v[104:107]\n" \
"ds_read_b64_tr_b16 a[26:27], v10 offset:16896\n" \
"v_mul_f32 v53, s20, v53\n" \
"v_mfma_f32_16x16x32_bf16 v[108:111], a[76:79], a[52:55], v[108:111]\n" \
"ds_read_b64_tr_b16 a[28:29], v11 offset:16896\n" \
"v_cvt_pk_bf16_f32 v44, v50, v51\n" \
"v_mfma_f32_16x16x32_bf16 v[112:115], a[72:75], a[56:59], v[112:115]\n" \
"s_add_u32 m0, s51, 58240\n" \
"ds_read_b64_tr_b16 a[30:31], v12 offset:16896\n" \
"v_cvt_pk_bf16_f32 v45, v52, v53\n" \
"v_mfma_f32_16x16x32_bf16 v[116:119], a[76:79], a[56:59], v[116:119]\n" \
"buffer_load_dwordx4 v21, s[32:35], s47 offen offset:128 lds\n" \
"v_mfma_f32_16x16x32_bf16 v[120:123], a[72:75], a[60:63], v[120:123]\n" \
"ds_read_b128 a[0:3], v7 offset:0\n" \
"v_accvgpr_read_b32 v46, a160\n" \
"v_mfma_f32_16x16x32_bf16 v[124:127], a[76:79], a[60:63], v[124:127]\n" \
"ds_read_b128 a[4:7], v7 offset:2048\n" \
"v_accvgpr_read_b32 v47, a161\n" \
"v_mfma_f32_16x16x32_bf16 v[128:131], a[80:83], a[48:51], v[128:131]\n" \
"s_mov_b32 s46, 640\n" \
"s_add_u32 m0, s50, 32768\n" \
"ds_read_b128 a[8:11], v7 offset:4096\n" \
"v_mfma_f32_16x16x32_bf16 v[132:135], a[84:87], a[48:51], v[132:135]\n" \
"buffer_load_dwordx4 v13, s[28:31], s46 offen lds\n" \
"v_mfma_f32_16x16x32_bf16 v[136:139], a[80:83], a[52:55], v[136:139]\n" \
"ds_read_b128 a[12:15], v7 offset:6144\n" \
"v_accvgpr_read_b32 v48, a162\n" \
"v_mfma_f32_16x16x32_bf16 v[140:143], a[84:87], a[52:55], v[140:143]\n" \
"ds_read_b64_tr_b16 a[32:33], v9 offset:17408\n" \
"v_accvgpr_read_b32 v49, a163\n" \
"v_mfma_f32_16x16x32_bf16 v[144:147], a[80:83], a[56:59], v[144:147]\n" \
"s_add_u32 m0, s50, 36864\n" \
"ds_read_b64_tr_b16 a[34:35], v10 offset:17408\n" \
"v_mul_f32 v46, s20, v46\n" \
"v_mfma_f32_16x16x32_bf16 v[148:151], a[84:87], a[56:59], v[148:151]\n" \
"buffer_load_dwordx4 v14, s[28:31], s46 offen lds\n" \
"v_mfma_f32_16x16x32_bf16 v[152:155], a[80:83], a[60:63], v[152:155]\n" \
"ds_read_b64_tr_b16 a[36:37], v11 offset:17408\n" \
"v_mul_f32 v47, s20, v47\n" \
"v_mfma_f32_16x16x32_bf16 v[156:159], a[84:87], a[60:63], v[156:159]\n" \
"ds_read_b64_tr_b16 a[38:39], v12 offset:17408\n" \
"v_mul_f32 v48, s20, v48\n" \
"v_mfma_f32_16x16x32_bf16 v[160:163], a[88:91], a[48:51], v[160:163]\n" \
"s_add_u32 m0, s50, 40960\n" \
"ds_read_b64_tr_b16 a[40:41], v9 offset:17920\n" \
"v_mul_f32 v49, s20, v49\n" \
"v_mfma_f32_16x16x32_bf16 v[164:167], a[92:95], a[48:51], v[164:167]\n" \
"buffer_load_dwordx4 v15, s[28:31], s46 offen lds\n" \
"v_mfma_f32_16x16x32_bf16 v[168:171], a[88:91], a[52:55], v[168:171]\n" \
"ds_read_b64_tr_b16 a[42:43], v10 offset:17920\n" \
"v_cvt_pk_bf16_f32 v30, v46, v47\n" \
"v_mfma_f32_16x16x32_bf16 v[172:175], a[92:95], a[52:55], v[172:175]\n" \
"ds_read_b64_tr_b16 a[44:45], v11 offset:17920\n" \
"v_cvt_pk_bf16_f32 v31, v48, v49\n" \
"v_mfma_f32_16x16x32_bf16 v[176:179], a[88:91], a[56:59], v[176:179]\n" \
"s_add_u32 m0, s50, 45056\n" \
"ds_read_b64_tr_b16 a[46:47], v12 offset:17920\n" \
"v_accvgpr_read_b32 v50, a164\n" \
"v_mfma_f32_16x16x32_bf16 v[180:183], a[92:95], a[56:59], v[180:183]\n" \
"buffer_load_dwordx4 v16, s[28:31], s46 offen lds\n" \
"v_mfma_f32_16x16x32_bf16 v[184:187], a[88:91], a[60:63], v[184:187]\n" \
"ds_read_b64_tr_b16 a[16:17], v9 offset:16384\n" \
"v_accvgpr_read_b32 v51, a165\n" \
"v_mfma_f32_16x16x32_bf16 v[188:191], a[92:95], a[60:63], v[188:191]\n" \
"ds_read_b64_tr_b16 a[18:19], v10 offset:16384\n" \
"v_accvgpr_read_b32 v52, a166\n" \
"v_mfma_f32_16x16x32_bf16 v[64:67], a[64:67], a[48:51], v[64:67]\n" \
"s_mov_b32 s46, 640\n" \
"s_add_u32 m0, s50, 49152\n" \
"ds_read_b64_tr_b16 a[20:21], v11 offset:16384\n" \
"v_mfma_f32_16x16x32_bf16 v[68:71], a[68:71], a[48:51], v[68:71]\n" \
"buffer_load_dwordx4 v17, s[28:31], s46 offen lds\n" \
"v_mfma_f32_16x16x32_bf16 v[72:75], a[64:67], a[52:55], v[72:75]\n" \
"ds_read_b64_tr_b16 a[22:23], v12 offset:16384\n" \
"v_accvgpr_read_b32 v53, a167\n" \
"v_mfma_f32_16x16x32_bf16 v[76:79], a[68:71], a[52:55], v[76:79]\n" \
"buffer_store_dwordx4 v[232:235], v240, s[36:39], s60 offen offset:0 sc1\n" \
"v_mfma_f32_16x16x32_bf16 v[80:83], a[64:67], a[56:59], v[80:83]\n" \
"s_add_u32 m0, s50, 53248\n" \
"v_mul_f32 v50, s20, v50\n" \
"v_mul_f32 v51, s20, v51\n" \
"v_mfma_f32_16x16x32_bf16 v[84:87], a[68:71], a[56:59], v[84:87]\n" \
"buffer_load_dwordx4 v18, s[28:31], s46 offen lds\n" \
"v_mfma_f32_16x16x32_bf16 v[88:91], a[64:67], a[60:63], v[88:91]\n" \
"v_mul_f32 v52, s20, v52\n" \
"v_mul_f32 v53, s20, v53\n" \
"v_mfma_f32_16x16x32_bf16 v[92:95], a[68:71], a[60:63], v[92:95]\n" \
"s_waitcnt lgkmcnt(0)\n" \
"v_cvt_pk_bf16_f32 v32, v50, v51\n" \
"v_cvt_pk_bf16_f32 v33, v52, v53\n" \
"v_mfma_f32_16x16x32_bf16 v[96:99], a[24:27], a[0:3], v[96:99]\n" \
"s_add_u32 m0, s50, 57344\n" \
"ds_read_b64_tr_b16 a[72:73], v9 offset:25088\n" \
"v_accvgpr_read_b32 v46, a168\n" \
"v_mfma_f32_16x16x32_bf16 v[100:103], a[28:31], a[0:3], v[100:103]\n" \
"buffer_load_dwordx4 v19, s[28:31], s46 offen lds\n" \
"v_mfma_f32_16x16x32_bf16 v[104:107], a[24:27], a[4:7], v[104:107]\n" \
"ds_read_b64_tr_b16 a[74:75], v10 offset:25088\n" \
"v_accvgpr_read_b32 v47, a169\n" \
"v_mfma_f32_16x16x32_bf16 v[108:111], a[28:31], a[4:7], v[108:111]\n" \
"ds_read_b64_tr_b16 a[76:77], v11 offset:25088\n" \
"v_accvgpr_read_b32 v48, a170\n" \
"v_mfma_f32_16x16x32_bf16 v[112:115], a[24:27], a[8:11], v[112:115]\n" \
"ds_read_b64_tr_b16 a[78:79], v12 offset:25088\n" \
"v_accvgpr_read_b32 v49, a171\n" \
"v_mfma_f32_16x16x32_bf16 v[116:119], a[28:31], a[8:11], v[116:119]\n" \
"s_add_u32 m0, s50, 61440\n" \
"ds_read_b128 a[48:51], v8 offset:0\n" \
"v_mul_f32 v46, s20, v46\n" \
"v_mfma_f32_16x16x32_bf16 v[120:123], a[24:27], a[12:15], v[120:123]\n" \
"buffer_load_dwordx4 v20, s[28:31], s46 offen lds\n" \
"v_mfma_f32_16x16x32_bf16 v[124:127], a[28:31], a[12:15], v[124:127]\n" \
"ds_read_b128 a[52:55], v8 offset:2048\n" \
"v_mul_f32 v47, s20, v47\n" \
"v_mfma_f32_16x16x32_bf16 v[128:131], a[32:35], a[0:3], v[128:131]\n" \
"s_waitcnt vmcnt(63)\n" \
"v_mul_f32 v48, s20, v48\n" \
"v_mul_f32 v49, s20, v49\n" \
"v_mfma_f32_16x16x32_bf16 v[132:135], a[36:39], a[0:3], v[132:135]\n" \
"ds_read_b128 a[56:59], v8 offset:4096\n" \
"s_load_dwordx2 s[100:101], %0, 0x50\n" \
"v_mfma_f32_16x16x32_bf16 v[136:139], a[32:35], a[4:7], v[136:139]\n" \
"s_mov_b32 s47, 3145728\n" \
"s_add_u32 m0, s51, 0\n" \
"ds_read_b128 a[60:63], v8 offset:6144\n" \
"v_mfma_f32_16x16x32_bf16 v[140:143], a[36:39], a[4:7], v[140:143]\n" \
"buffer_load_dwordx4 v21, s[32:35], s47 offen offset:0 lds\n" \
"v_mfma_f32_16x16x32_bf16 v[144:147], a[32:35], a[8:11], v[144:147]\n" \
"s_lshr_b32 s64, %1, 3\n" \
"ds_read_b64_tr_b16 a[80:81], v9 offset:25600\n" \
"s_lshl_b32 s64, s64, 2\n" \
"v_mfma_f32_16x16x32_bf16 v[148:151], a[36:39], a[8:11], v[148:151]\n" \
"s_add_u32 s64, s64, %2\n" \
"ds_read_b64_tr_b16 a[82:83], v10 offset:25600\n" \
"s_lshl_b32 s64, s64, 11\n" \
"v_mfma_f32_16x16x32_bf16 v[152:155], a[32:35], a[12:15], v[152:155]\n" \
"s_bfe_u32 s65, %1, 0x20001\n" \
"ds_read_b64_tr_b16 a[84:85], v11 offset:25600\n" \
"s_lshl_b32 s65, s65, 21\n" \
"v_mfma_f32_16x16x32_bf16 v[156:159], a[36:39], a[12:15], v[156:159]\n" \
"LH10_%=:\ns_cbranch_execz LH14_%=\n" \
"s_add_u32 m0, s51, 896\n" \
"ds_read_b64_tr_b16 a[86:87], v12 offset:25600\n" \
"s_add_u32 s64, s64, s65\n" \
"v_mfma_f32_16x16x32_bf16 v[160:163], a[40:43], a[0:3], v[160:163]\n" \
"buffer_load_dwordx4 v21, s[32:35], s47 offen offset:128 lds\n" \
"v_mfma_f32_16x16x32_bf16 v[164:167], a[44:47], a[0:3], v[164:167]\n" \
"ds_read_b64_tr_b16 a[88:89], v9 offset:26112\n" \
"v_mfma_f32_16x16x32_bf16 v[168:171], a[40:43], a[4:7], v[168:171]\n" \
"ds_read_b64_tr_b16 a[90:91], v10 offset:26112\n" \
"v_mfma_f32_16x16x32_bf16 v[172:175], a[44:47], a[4:7], v[172:175]\n" \
"ds_read_b64_tr_b16 a[92:93], v11 offset:26112\n" \
"v_mfma_f32_16x16x32_bf16 v[176:179], a[40:43], a[8:11], v[176:179]\n" \
"s_mov_b32 s47, 3407872\n" \
"s_add_u32 m0, s51, 8192\n" \
"ds_read_b64_tr_b16 a[94:95], v12 offset:26112\n" \
"v_mfma_f32_16x16x32_bf16 v[180:183], a[44:47], a[8:11], v[180:183]\n" \
"buffer_load_dwordx4 v21, s[32:35], s47 offen offset:0 lds\n" \
"v_mfma_f32_16x16x32_bf16 v[184:187], a[40:43], a[12:15], v[184:187]\n" \
"ds_read_b64_tr_b16 a[64:65], v9 offset:24576\n" \
"v_mfma_f32_16x16x32_bf16 v[188:191], a[44:47], a[12:15], v[188:191]\n" \
"v_mfma_f32_16x16x32_bf16 v[24:27], a[240:243], a[192:195], 0\n" \
"v_mfma_f32_16x16x32_bf16 v[64:67], a[16:19], a[0:3], v[64:67]\n" \
"ds_read_b64_tr_b16 a[66:67], v10 offset:24576\n" \
"v_mfma_f32_16x16x32_bf16 v[68:71], a[20:23], a[0:3], v[68:71]\n" \
"s_add_u32 m0, s51, 9088\n" \
"ds_read_b64_tr_b16 a[68:69], v11 offset:24576\n" \
"v_mfma_f32_16x16x32_bf16 v[72:75], a[16:19], a[4:7], v[72:75]\n" \
"buffer_load_dwordx4 v21, s[32:35], s47 offen offset:128 lds\n" \
"v_mfma_f32_16x16x32_bf16 v[76:79], a[20:23], a[4:7], v[76:79]\n" \
"ds_read_b64_tr_b16 a[70:71], v12 offset:24576\n" \
"v_mfma_f32_16x16x32_bf16 v[80:83], a[16:19], a[8:11], v[80:83]\n" \
"buffer_store_dwordx4 v[236:239], v240, s[36:39], s60 offen offset:128 sc1\n" \
"v_mfma_f32_16x16x32_bf16 v[84:87], a[20:23], a[8:11], v[84:87]\n" \
"s_mov_b32 s66, 0x100001\n" \
"v_mfma_f32_16x16x32_bf16 v[24:27], a[244:247], a[196:199], v[24:27]\n" \
"v_mfma_f32_16x16x32_bf16 v[88:91], a[16:19], a[12:15], v[88:91]\n" \
"s_waitcnt vmcnt(19) lgkmcnt(0)\n" \
"s_mov_b32 s67, 0x10000100\n" \
"v_mfma_f32_16x16x32_bf16 v[92:95], a[20:23], a[12:15], v[92:95]\n" \
"s_barrier\n" \
"v_mfma_f32_16x16x32_bf16 v[96:99], a[72:75], a[48:51], v[96:99]\n" \
"s_mov_b32 s46, 768\n" \
"s_add_u32 m0, s50, 65536\n" \
"ds_read_b64_tr_b16 a[24:25], v9 offset:33280\n" \
"v_mfma_f32_16x16x32_bf16 v[100:103], a[76:79], a[48:51], v[100:103]\n" \
"buffer_load_dwordx4 v13, s[28:31], s46 offen lds\n" \
"v_mfma_f32_16x16x32_bf16 v[104:107], a[72:75], a[52:55], v[104:107]\n" \
"ds_read_b64_tr_b16 a[26:27], v10 offset:33280\n" \
"v_cndmask_b32 v23, 0, v24, s[66:67]\n" \
"v_mfma_f32_16x16x32_bf16 v[108:111], a[76:79], a[52:55], v[108:111]\n" \
"s_mov_b32 s66, 0x200002\n" \
"ds_read_b64_tr_b16 a[28:29], v11 offset:33280\n" \
"s_mov_b32 s67, 0x20000200\n" \
"v_mfma_f32_16x16x32_bf16 v[112:115], a[72:75], a[56:59], v[112:115]\n" \
"ds_read_b64_tr_b16 a[30:31], v12 offset:33280\n" \
"v_cndmask_b32 v23, v23, v25, s[66:67]\n" \
"v_mfma_f32_16x16x32_bf16 v[116:119], a[76:79], a[56:59], v[116:119]\n" \
"s_add_u32 m0, s50, 69632\n" \
"ds_read_b128 a[0:3], v5 offset:0\n" \
"s_mov_b32 s66, 0x400004\n" \
"v_mfma_f32_16x16x32_bf16 v[120:123], a[72:75], a[60:63], v[120:123]\n" \
"buffer_load_dwordx4 v14, s[28:31], s46 offen lds\n" \
"v_mfma_f32_16x16x32_bf16 v[124:127], a[76:79], a[60:63], v[124:127]\n" \
"s_mov_b32 s67, 0x40000400\n" \
"ds_read_b128 a[4:7], v5 offset:2048\n" \
"v_cndmask_b32 v23, v23, v26, s[66:67]\n" \
"v_mfma_f32_16x16x32_bf16 v[128:131], a[80:83], a[48:51], v[128:131]\n" \
"s_mov_b32 s66, 0x800008\n" \
"s_mov_b32 s67, 0x80000800\n" \
"v_cndmask_b32 v23, v23, v27, s[66:67]\n" \
"v_mfma_f32_16x16x32_bf16 v[132:135], a[84:87], a[48:51], v[132:135]\n" \
"ds_read_b128 a[8:11], v5 offset:4096\n" \
"v_mfma_f32_16x16x32_bf16 v[136:139], a[80:83], a[52:55], v[136:139]\n" \
"s_add_u32 m0, s50, 73728\n" \
"ds_read_b128 a[12:15], v5 offset:6144\n" \
"v_mfma_f32_16x16x32_bf16 v[140:143], a[84:87], a[52:55], v[140:143]\n" \
"buffer_load_dwordx4 v15, s[28:31], s46 offen lds\n" \
"v_mfma_f32_16x16x32_bf16 v[144:147], a[80:83], a[56:59], v[144:147]\n" \
"ds_read_b64_tr_b16 a[32:33], v9 offset:33792\n" \
"v_mfma_f32_16x16x32_bf16 v[148:151], a[84:87], a[56:59], v[148:151]\n" \
"ds_read_b64_tr_b16 a[34:35], v10 offset:33792\n" \
"v_mfma_f32_16x16x32_bf16 v[152:155], a[80:83], a[60:63], v[152:155]\n" \
"ds_read_b64_tr_b16 a[36:37], v11 offset:33792\n" \
"v_mfma_f32_16x16x32_bf16 v[156:159], a[84:87], a[60:63], v[156:159]\n" \
"s_add_u32 m0, s50, 77824\n" \
"ds_read_b64_tr_b16 a[38:39], v12 offset:33792\n" \
"v_mfma_f32_16x16x32_bf16 v[160:163], a[88:91], a[48:51], v[160:163]\n" \
"buffer_load_dwordx4 v16, s[28:31], s46 offen lds\n" \
"v_mfma_f32_16x16x32_bf16 v[164:167], a[92:95], a[48:51], v[164:167]\n" \
"ds_read_b64_tr_b16 a[40:41], v9 offset:34304\n" \
"v_mfma_f32_16x16x32_bf16 v[168:171], a[88:91], a[52:55], v[168:171]\n" \
"ds_read_b64_tr_b16 a[42:43], v10 offset:34304\n" \
"v_mfma_f32_16x16x32_bf16 v[172:175], a[92:95], a[52:55], v[172:175]\n" \
"ds_read_b64_tr_b16 a[44:45], v11 offset:34304\n" \
"v_mfma_f32_16x16x32_bf16 v[176:179], a[88:91], a[56:59], v[176:179]\n" \
"s_mov_b32 s46, 768\n" \
"s_add_u32 m0, s50, 81920\n" \
"ds_read_b64_tr_b16 a[46:47], v12 offset:34304\n" \
"v_mfma_f32_16x16x32_bf16 v[180:183], a[92:95], a[56:59], v[180:183]\n" \
"buffer_load_dwordx4 v17, s[28:31], s46 offen lds\n" \
"v_mfma_f32_16x16x32_bf16 v[184:187], a[88:91], a[60:63], v[184:187]\n" \
"ds_read_b64_tr_b16 a[16:17], v9 offset:32768\n" \
"v_mfma_f32_16x16x32_bf16 v[188:191], a[92:95], a[60:63], v[188:191]\n" \
"v_mfma_f32_16x16x32_bf16 v[24:27], a[248:251], a[192:195], 0\n" \
"v_mfma_f32_16x16x32_bf16 v[64:67], a[64:67], a[48:51], v[64:67]\n" \
"ds_read_b64_tr_b16 a[18:19], v10 offset:32768\n" \
"v_mfma_f32_16x16x32_bf16 v[68:71], a[68:71], a[48:51], v[68:71]\n" \
"s_add_u32 m0, s50, 86016\n" \
"ds_read_b64_tr_b16 a[20:21], v11 offset:32768\n" \
"v_mfma_f32_16x16x32_bf16 v[72:75], a[64:67], a[52:55], v[72:75]\n" \
"buffer_load_dwordx4 v18, s[28:31], s46 offen lds\n" \
"v_mfma_f32_16x16x32_bf16 v[76:79], a[68:71], a[52:55], v[76:79]\n" \
"ds_read_b64_tr_b16 a[22:23], v12 offset:32768\n" \
"v_mfma_f32_16x16x32_bf16 v[80:83], a[64:67], a[56:59], v[80:83]\n" \
"buffer_store_dwordx4 v[34:37], v4, s[36:39], s97 offen offset:0 sc1\n" \
"v_mfma_f32_16x16x32_bf16 v[84:87], a[68:71], a[56:59], v[84:87]\n" \
"s_add_u32 s96, s96, s92\n" \
"v_cvt_pk_bf16_f32 v34, v46, v47\n" \
"v_cvt_pk_bf16_f32 v35, v48, v49\n" \
"v_mfma_f32_16x16x32_bf16 v[88:91], a[64:67], a[60:63], v[88:91]\n" \
"v_accvgpr_read_b32 v50, a172\n" \
"v_accvgpr_read_b32 v51, a173\n" \
"v_mfma_f32_16x16x32_bf16 v[92:95], a[68:71], a[60:63], v[92:95]\n" \
"s_waitcnt lgkmcnt(0)\n" \
"v_accvgpr_read_b32 v52, a174\n" \
"v_mfma_f32_16x16x32_bf16 v[96:99], a[24:27], a[0:3], v[96:99]\n" \
"s_add_u32 m0, s50, 90112\n" \
"ds_read_b64_tr_b16 a[72:73], v9 offset:41472\n" \
"v_accvgpr_read_b32 v53, a175\n" \
"v_mfma_f32_16x16x32_bf16 v[100:103], a[28:31], a[0:3], v[100:103]\n" \
"buffer_load_dwordx4 v19, s[28:31], s46 offen lds\n" \
"v_mfma_f32_16x16x32_bf16 v[104:107], a[24:27], a[4:7], v[104:107]\n" \
"s_mov_b64 s[40:41], s[4:5]\n" \
"ds_read_b64_tr_b16 a[74:75], v10 offset:41472\n" \
"v_mul_f32 v50, s20, v50\n" \
"v_mfma_f32_16x16x32_bf16 v[108:111], a[28:31], a[4:7], v[108:111]\n" \
"s_and_b32 s41, s41, 0xffff\n" \
"ds_read_b64_tr_b16 a[76:77], v11 offset:41472\n" \
"v_mul_f32 v51, s20, v51\n" \
"v_mfma_f32_16x16x32_bf16 v[112:115], a[24:27], a[8:11], v[112:115]\n" \
"s_lshl_b32 s62, s6, 20\n" \
"ds_read_b64_tr_b16 a[78:79], v12 offset:41472\n" \
"v_mul_f32 v52, s20, v52\n" \
"v_mfma_f32_16x16x32_bf16 v[116:119], a[28:31], a[8:11], v[116:119]\n" \
"s_add_u32 m0, s50, 94208\n" \
"ds_read_b128 a[48:51], v6 offset:0\n" \
"v_mul_f32 v53, s20, v53\n" \
"v_mfma_f32_16x16x32_bf16 v[120:123], a[24:27], a[12:15], v[120:123]\n" \
"buffer_load_dwordx4 v20, s[28:31], s46 offen lds\n" \
"v_mfma_f32_16x16x32_bf16 v[124:127], a[28:31], a[12:15], v[124:127]\n" \
"s_add_u32 s16, s62, 33554432\n" \
"ds_read_b128 a[52:55], v6 offset:2048\n" \
"v_cvt_pk_bf16_f32 v36, v50, v51\n" \
"v_mfma_f32_16x16x32_bf16 v[128:131], a[32:35], a[0:3], v[128:131]\n" \
"s_lshl_b32 s63, s7, 16\n" \
"v_cvt_pk_bf16_f32 v37, v52, v53\n" \
"v_accvgpr_read_b32 v46, a176\n" \
"v_mfma_f32_16x16x32_bf16 v[132:135], a[36:39], a[0:3], v[132:135]\n" \
"s_sub_u32 s63, 1048576, s63\n" \
"ds_read_b128 a[56:59], v6 offset:4096\n" \
"v_accvgpr_read_b32 v47, a177\n" \
"v_mfma_f32_16x16x32_bf16 v[136:139], a[32:35], a[4:7], v[136:139]\n" \
"s_mov_b32 s47, 3670016\n" \
"s_add_u32 m0, s51, 16384\n" \
"ds_read_b128 a[60:63], v6 offset:6144\n" \
"v_mfma_f32_16x16x32_bf16 v[140:143], a[36:39], a[4:7], v[140:143]\n" \
"buffer_load_dwordx4 v21, s[32:35], s47 offen offset:0 lds\n" \
"v_mfma_f32_16x16x32_bf16 v[144:147], a[32:35], a[8:11], v[144:147]\n" \
"s_add_u32 s42, s16, s63\n" \
"ds_read_b64_tr_b16 a[80:81], v9 offset:41984\n" \
"v_accvgpr_read_b32 v48, a178\n" \
"v_mfma_f32_16x16x32_bf16 v[148:151], a[36:39], a[8:11], v[148:151]\n" \
"ds_read_b64_tr_b16 a[82:83], v10 offset:41984\n" \
"v_accvgpr_read_b32 v49, a179\n" \
"v_mfma_f32_16x16x32_bf16 v[152:155], a[32:35], a[12:15], v[152:155]\n" \
"ds_read_b64_tr_b16 a[84:85], v11 offset:41984\n" \
"v_mul_f32 v46, s20, v46\n" \
"v_mfma_f32_16x16x32_bf16 v[156:159], a[36:39], a[12:15], v[156:159]\n" \
"s_add_u32 m0, s51, 17280\n" \
"ds_read_b64_tr_b16 a[86:87], v12 offset:41984\n" \
"v_mul_f32 v47, s20, v47\n" \
"v_mfma_f32_16x16x32_bf16 v[160:163], a[40:43], a[0:3], v[160:163]\n" \
"buffer_load_dwordx4 v21, s[32:35], s47 offen offset:128 lds\n" \
"v_mfma_f32_16x16x32_bf16 v[164:167], a[44:47], a[0:3], v[164:167]\n" \
"ds_read_b64_tr_b16 a[88:89], v9 offset:42496\n" \
"v_mul_f32 v48, s20, v48\n" \
"v_mfma_f32_16x16x32_bf16 v[168:171], a[40:43], a[4:7], v[168:171]\n" \
"ds_read_b64_tr_b16 a[90:91], v10 offset:42496\n" \
"v_mul_f32 v49, s20, v49\n" \
"v_mfma_f32_16x16x32_bf16 v[172:175], a[44:47], a[4:7], v[172:175]\n" \
"s_add_u32 s100, s100, s64\ns_addc_u32 s101, s101, 0\n" \
"ds_read_b64_tr_b16 a[92:93], v11 offset:42496\n" \
"v_mfma_f32_16x16x32_bf16 v[176:179], a[40:43], a[8:11], v[176:179]\n" \
"s_mov_b32 s47, 3932160\n" \
"s_add_u32 m0, s51, 24576\n" \
"ds_read_b64_tr_b16 a[94:95], v12 offset:42496\n" \
"v_mfma_f32_16x16x32_bf16 v[180:183], a[44:47], a[8:11], v[180:183]\n" \
"buffer_load_dwordx4 v21, s[32:35], s47 offen offset:0 lds\n" \
"v_mfma_f32_16x16x32_bf16 v[184:187], a[40:43], a[12:15], v[184:187]\n" \
"ds_read_b64_tr_b16 a[64:65], v9 offset:40960\n" \
"v_mfma_f32_16x16x32_bf16 v[188:191], a[44:47], a[12:15], v[188:191]\n" \
"s_mov_b32 s66, 0x100001\n" \
"v_mfma_f32_16x16x32_bf16 v[24:27], a[252:255], a[196:199], v[24:27]\n" \
"v_mfma_f32_16x16x32_bf16 v[64:67], a[16:19], a[0:3], v[64:67]\n" \
"s_mov_b32 s67, 0x10000100\n" \
"ds_read_b64_tr_b16 a[66:67], v10 offset:40960\n" \
"v_mfma_f32_16x16x32_bf16 v[68:71], a[20:23], a[0:3], v[68:71]\n" \
"s_add_u32 m0, s51, 25472\n" \
"ds_read_b64_tr_b16 a[68:69], v11 offset:40960\n" \
"v_mfma_f32_16x16x32_bf16 v[72:75], a[16:19], a[4:7], v[72:75]\n" \
"buffer_load_dwordx4 v21, s[32:35], s47 offen offset:128 lds\n" \
"v_mfma_f32_16x16x32_bf16 v[76:79], a[20:23], a[4:7], v[76:79]\n" \
"ds_read_b64_tr_b16 a[70:71], v12 offset:40960\n" \
"v_cndmask_b32 v54, 0, v24, s[66:67]\n" \
"v_mfma_f32_16x16x32_bf16 v[80:83], a[16:19], a[8:11], v[80:83]\n" \
"buffer_store_dwordx4 v[38:41], v4, s[36:39], s96 offen offset:0 sc1\n" \
"v_mfma_f32_16x16x32_bf16 v[84:87], a[20:23], a[8:11], v[84:87]\n" \
"s_add_u32 s97, s96, s98\n" \
"v_cvt_pk_bf16_f32 v38, v46, v47\n" \
"v_cvt_pk_bf16_f32 v39, v48, v49\n" \
"v_mfma_f32_16x16x32_bf16 v[88:91], a[16:19], a[12:15], v[88:91]\n" \
"s_waitcnt vmcnt(19) lgkmcnt(0)\n" \
"v_accvgpr_read_b32 v50, a180\n" \
"v_accvgpr_read_b32 v51, a181\n" \
"v_mfma_f32_16x16x32_bf16 v[92:95], a[20:23], a[12:15], v[92:95]\n" \
"s_barrier\n" \
"v_accvgpr_read_b32 v52, a182\n" \
"s_mov_b32 s66, 0x200002\n" \
"v_mfma_f32_16x16x32_bf16 v[96:99], a[72:75], a[48:51], v[96:99]\n" \
"s_mov_b32 s46, 896\n" \
"s_add_u32 m0, s50, 0\n" \
"ds_read_b64_tr_b16 a[24:25], v9 offset:49664\n" \
"v_mfma_f32_16x16x32_bf16 v[100:103], a[76:79], a[48:51], v[100:103]\n" \
"buffer_load_dwordx4 v13, s[28:31], s46 offen lds\n" \
"v_mfma_f32_16x16x32_bf16 v[104:107], a[72:75], a[52:55], v[104:107]\n" \
"s_mov_b32 s67, 0x20000200\n" \
"ds_read_b64_tr_b16 a[26:27], v10 offset:49664\n" \
"v_accvgpr_read_b32 v53, a183\n" \
"v_mfma_f32_16x16x32_bf16 v[108:111], a[76:79], a[52:55], v[108:111]\n" \
"ds_read_b64_tr_b16 a[28:29], v11 offset:49664\n" \
"v_mul_f32 v50, s20, v50\n" \
"v_mfma_f32_16x16x32_bf16 v[112:115], a[72:75], a[56:59], v[112:115]\n" \
"ds_read_b64_tr_b16 a[30:31], v12 offset:49664\n" \
"v_mul_f32 v51, s20, v51\n" \
"v_mfma_f32_16x16x32_bf16 v[116:119], a[76:79], a[56:59], v[116:119]\n" \
"s_add_u32 m0, s50, 4096\n" \
"ds_read_b128 a[0:3], v5 offset:32768\n" \
"v_mul_f32 v52, s20, v52\n" \
"v_mfma_f32_16x16x32_bf16 v[120:123], a[72:75], a[60:63], v[120:123]\n" \
"buffer_load_dwordx4 v14, s[28:31], s46 offen lds\n" \
"v_mfma_f32_16x16x32_bf16 v[124:127], a[76:79], a[60:63], v[124:127]\n" \
"ds_read_b128 a[4:7], v5 offset:34816\n" \
"v_mul_f32 v53, s20, v53\n" \
"v_mfma_f32_16x16x32_bf16 v[128:131], a[80:83], a[48:51], v[128:131]\n" \
"v_cvt_pk_bf16_f32 v40, v50, v51\n" \
"v_cvt_pk_bf16_f32 v41, v52, v53\n" \
"v_mfma_f32_16x16x32_bf16 v[132:135], a[84:87], a[48:51], v[132:135]\n" \
"ds_read_b128 a[8:11], v5 offset:36864\n" \
"v_accvgpr_read_b32 v46, a184\n" \
"v_mfma_f32_16x16x32_bf16 v[136:139], a[80:83], a[52:55], v[136:139]\n" \
"s_add_u32 m0, s50, 8192\n" \
"ds_read_b128 a[12:15], v5 offset:38912\n" \
"v_accvgpr_read_b32 v47, a185\n" \
"v_mfma_f32_16x16x32_bf16 v[140:143], a[84:87], a[52:55], v[140:143]\n" \
"buffer_load_dwordx4 v15, s[28:31], s46 offen lds\n" \
"v_mfma_f32_16x16x32_bf16 v[144:147], a[80:83], a[56:59], v[144:147]\n" \
"ds_read_b64_tr_b16 a[32:33], v9 offset:50176\n" \
"v_accvgpr_read_b32 v48, a186\n" \
"v_mfma_f32_16x16x32_bf16 v[148:151], a[84:87], a[56:59], v[148:151]\n" \
"ds_read_b64_tr_b16 a[34:35], v10 offset:50176\n" \
"v_accvgpr_read_b32 v49, a187\n" \
"v_mfma_f32_16x16x32_bf16 v[152:155], a[80:83], a[60:63], v[152:155]\n" \
"ds_read_b64_tr_b16 a[36:37], v11 offset:50176\n" \
"v_mul_f32 v46, s20, v46\n" \
"v_mfma_f32_16x16x32_bf16 v[156:159], a[84:87], a[60:63], v[156:159]\n" \
"s_add_u32 m0, s50, 12288\n" \
"ds_read_b64_tr_b16 a[38:39], v12 offset:50176\n" \
"v_mul_f32 v47, s20, v47\n" \
"v_mfma_f32_16x16x32_bf16 v[160:163], a[88:91], a[48:51], v[160:163]\n" \
"buffer_load_dwordx4 v16, s[28:31], s46 offen lds\n" \
"v_mfma_f32_16x16x32_bf16 v[164:167], a[92:95], a[48:51], v[164:167]\n" \
"ds_read_b64_tr_b16 a[40:41], v9 offset:50688\n" \
"v_mul_f32 v48, s20, v48\n" \
"v_mfma_f32_16x16x32_bf16 v[168:171], a[88:91], a[52:55], v[168:171]\n" \
"ds_read_b64_tr_b16 a[42:43], v10 offset:50688\n" \
"v_mul_f32 v49, s20, v49\n" \
"v_mfma_f32_16x16x32_bf16 v[172:175], a[92:95], a[52:55], v[172:175]\n" \
"ds_read_b64_tr_b16 a[44:45], v11 offset:50688\n" \
"v_cndmask_b32 v54, v54, v25, s[66:67]\n" \
"v_mfma_f32_16x16x32_bf16 v[176:179], a[88:91], a[56:59], v[176:179]\n" \
"s_mov_b32 s46, 896\n" \
"s_add_u32 m0, s50, 16384\n" \
"ds_read_b64_tr_b16 a[46:47], v12 offset:50688\n" \
"v_mfma_f32_16x16x32_bf16 v[180:183], a[92:95], a[56:59], v[180:183]\n" \
"buffer_load_dwordx4 v17, s[28:31], s46 offen lds\n" \
"v_mfma_f32_16x16x32_bf16 v[184:187], a[88:91], a[60:63], v[184:187]\n" \
"s_mov_b32 s66, 0x400004\n" \
"ds_read_b64_tr_b16 a[16:17], v9 offset:49152\n" \
"s_mov_b32 s67, 0x40000400\n" \
"v_mfma_f32_16x16x32_bf16 v[188:191], a[92:95], a[60:63], v[188:191]\n" \
"v_cndmask_b32 v54, v54, v26, s[66:67]\n" \
"s_mov_b32 s66, 0x800008\n" \
"v_mfma_f32_16x16x32_bf16 v[64:67], a[64:67], a[48:51], v[64:67]\n" \
"s_mov_b32 s67, 0x80000800\n" \
"ds_read_b64_tr_b16 a[18:19], v10 offset:49152\n" \
"v_cndmask_b32 v54, v54, v27, s[66:67]\n" \
"v_mfma_f32_16x16x32_bf16 v[68:71], a[68:71], a[48:51], v[68:71]\n" \
"s_add_u32 m0, s50, 20480\n" \
"ds_read_b64_tr_b16 a[20:21], v11 offset:49152\n" \
"v_mfma_f32_16x16x32_bf16 v[72:75], a[64:67], a[52:55], v[72:75]\n" \
"buffer_load_dwordx4 v18, s[28:31], s46 offen lds\n" \
"v_mfma_f32_16x16x32_bf16 v[76:79], a[68:71], a[52:55], v[76:79]\n" \
"ds_read_b64_tr_b16 a[22:23], v12 offset:49152\n" \
"v_permlane32_swap_b32 v23, v54\n" \
"v_mfma_f32_16x16x32_bf16 v[80:83], a[64:67], a[56:59], v[80:83]\n" \
"buffer_store_dwordx4 v[42:45], v4, s[36:39], s97 offen offset:0 sc1\n" \
"v_mfma_f32_16x16x32_bf16 v[84:87], a[68:71], a[56:59], v[84:87]\n" \
"s_add_u32 s96, s96, s92\n" \
"v_cvt_pk_bf16_f32 v42, v46, v47\n" \
"v_cvt_pk_bf16_f32 v43, v48, v49\n" \
"v_mfma_f32_16x16x32_bf16 v[88:91], a[64:67], a[60:63], v[88:91]\n" \
"v_accvgpr_read_b32 v50, a188\n" \
"v_accvgpr_read_b32 v51, a189\n" \
"v_mfma_f32_16x16x32_bf16 v[92:95], a[68:71], a[60:63], v[92:95]\n" \
"s_waitcnt lgkmcnt(0)\n" \
"v_accvgpr_read_b32 v52, a190\n" \
"v_mfma_f32_16x16x32_bf16 v[96:99], a[24:27], a[0:3], v[96:99]\n" \
"s_add_u32 m0, s50, 24576\n" \
"ds_read_b64_tr_b16 a[72:73], v9 offset:57856\n" \
"v_accvgpr_read_b32 v53, a191\n" \
"v_mfma_f32_16x16x32_bf16 v[100:103], a[28:31], a[0:3], v[100:103]\n" \
"buffer_load_dwordx4 v19, s[28:31], s46 offen lds\n" \
"v_mfma_f32_16x16x32_bf16 v[104:107], a[24:27], a[4:7], v[104:107]\n" \
"ds_read_b64_tr_b16 a[74:75], v10 offset:57856\n" \
"v_mul_f32 v50, s20, v50\n" \
"v_mfma_f32_16x16x32_bf16 v[108:111], a[28:31], a[4:7], v[108:111]\n" \
"ds_read_b64_tr_b16 a[76:77], v11 offset:57856\n" \
"v_mul_f32 v51, s20, v51\n" \
"v_mfma_f32_16x16x32_bf16 v[112:115], a[24:27], a[8:11], v[112:115]\n" \
"ds_read_b64_tr_b16 a[78:79], v12 offset:57856\n" \
"v_mul_f32 v52, s20, v52\n" \
"v_mfma_f32_16x16x32_bf16 v[116:119], a[28:31], a[8:11], v[116:119]\n" \
"s_add_u32 m0, s50, 28672\n" \
"ds_read_b128 a[48:51], v6 offset:32768\n" \
"v_mul_f32 v53, s20, v53\n" \
"v_mfma_f32_16x16x32_bf16 v[120:123], a[24:27], a[12:15], v[120:123]\n" \
"buffer_load_dwordx4 v20, s[28:31], s46 offen lds\n" \
"v_mfma_f32_16x16x32_bf16 v[124:127], a[28:31], a[12:15], v[124:127]\n" \
"ds_read_b128 a[52:55], v6 offset:34816\n" \
"v_cvt_pk_bf16_f32 v44, v50, v51\n" \
"v_mfma_f32_16x16x32_bf16 v[128:131], a[32:35], a[0:3], v[128:131]\n" \
"v_cvt_pk_bf16_f32 v45, v52, v53\n" \
"v_add_f32 v23, v23, v54\n" \
"v_mfma_f32_16x16x32_bf16 v[132:135], a[36:39], a[0:3], v[132:135]\n" \
"ds_read_b128 a[56:59], v6 offset:36864\n" \
"v_mov_b32 v54, v23\n" \
"v_mfma_f32_16x16x32_bf16 v[136:139], a[32:35], a[4:7], v[136:139]\n" \
"s_mov_b32 s47, 4194304\n" \
"s_add_u32 m0, s51, 32768\n" \
"ds_read_b128 a[60:63], v6 offset:38912\n" \
"v_mfma_f32_16x16x32_bf16 v[140:143], a[36:39], a[4:7], v[140:143]\n" \
"buffer_load_dwordx4 v21, s[32:35], s47 offen offset:0 lds\n" \
"v_mfma_f32_16x16x32_bf16 v[144:147], a[32:35], a[8:11], v[144:147]\n" \
"ds_read_b64_tr_b16 a[80:81], v9 offset:58368\n" \
"v_permlane16_swap_b32 v23, v54\n" \
"v_mfma_f32_16x16x32_bf16 v[148:151], a[36:39], a[8:11], v[148:151]\n" \
"ds_read_b64_tr_b16 a[82:83], v10 offset:58368\n" \
"v_add_f32 v23, v23, v54\n" \
"v_mfma_f32_16x16x32_bf16 v[152:155], a[32:35], a[12:15], v[152:155]\n" \
"ds_read_b64_tr_b16 a[84:85], v11 offset:58368\n" \
"v_mfma_f32_16x16x32_bf16 v[156:159], a[36:39], a[12:15], v[156:159]\n" \
"s_add_u32 m0, s51, 33664\n" \
"ds_read_b64_tr_b16 a[86:87], v12 offset:58368\n" \
"v_add_f32 v23, v23, v23 row_ror:8 row_mask:0xf bank_mask:0xf\n" \
"v_mfma_f32_16x16x32_bf16 v[160:163], a[40:43], a[0:3], v[160:163]\n" \
"buffer_load_dwordx4 v21, s[32:35], s47 offen offset:128 lds\n" \
"v_mfma_f32_16x16x32_bf16 v[164:167], a[44:47], a[0:3], v[164:167]\n" \
"ds_read_b64_tr_b16 a[88:89], v9 offset:58880\n" \
"v_add_f32 v23, v23, v23 row_ror:4 row_mask:0xf bank_mask:0xf\n" \
"v_mfma_f32_16x16x32_bf16 v[168:171], a[40:43], a[4:7], v[168:171]\n" \
"ds_read_b64_tr_b16 a[90:91], v10 offset:58880\n" \
"v_mfma_f32_16x16x32_bf16 v[172:175], a[44:47], a[4:7], v[172:175]\n" \
"ds_read_b64_tr_b16 a[92:93], v11 offset:58880\n" \
"v_add_f32 v23, v23, v23 row_ror:2 row_mask:0xf bank_mask:0xf\n" \
"v_mfma_f32_16x16x32_bf16 v[176:179], a[40:43], a[8:11], v[176:179]\n" \
"s_mov_b32 s47, 4456448\n" \
"s_add_u32 m0, s51, 40960\n" \
"ds_read_b64_tr_b16 a[94:95], v12 offset:58880\n" \
"v_mfma_f32_16x16x32_bf16 v[180:183], a[44:47], a[8:11], v[180:183]\n" \
"buffer_load_dwordx4 v21, s[32:35], s47 offen offset:0 lds\n" \
"v_mfma_f32_16x16x32_bf16 v[184:187], a[40:43], a[12:15], v[184:187]\n" \
"ds_read_b64_tr_b16 a[64:65], v9 offset:57344\n" \
"v_add_f32 v23, v23, v23 row_ror:1 row_mask:0xf bank_mask:0xf\n" \
"v_mfma_f32_16x16x32_bf16 v[188:191], a[44:47], a[12:15], v[188:191]\n" \
"v_cvt_pk_bf16_f32 v23, v23, v23\n" \
"v_and_b32 v23, 0xffff0000, v23\n" \
"v_mfma_f32_16x16x32_bf16 v[64:67], a[16:19], a[0:3], v[64:67]\n" \
"ds_read_b64_tr_b16 a[66:67], v10 offset:57344\n" \
"v_mul_f32 v23, 0xbfb8aa3b, v23\n" \
"v_mfma_f32_16x16x32_bf16 v[68:71], a[20:23], a[0:3], v[68:71]\n" \
"s_add_u32 m0, s51, 41856\n" \
"ds_read_b64_tr_b16 a[68:69], v11 offset:57344\n" \
"v_mfma_f32_16x16x32_bf16 v[72:75], a[16:19], a[4:7], v[72:75]\n" \
"buffer_load_dwordx4 v21, s[32:35], s47 offen offset:128 lds\n" \
"v_mfma_f32_16x16x32_bf16 v[76:79], a[20:23], a[4:7], v[76:79]\n" \
"ds_read_b64_tr_b16 a[70:71], v12 offset:57344\n" \
"v_mfma_f32_16x16x32_bf16 v[80:83], a[16:19], a[8:11], v[80:83]\n" \
"buffer_store_dwordx4 v[30:33], v4, s[36:39], s96 offen offset:0 sc1\n" \
"v_mfma_f32_16x16x32_bf16 v[84:87], a[20:23], a[8:11], v[84:87]\n" \
"s_add_u32 s97, s96, s98\n" \
"v_exp_f32 v23, v23\n" \
"v_mfma_f32_16x16x32_bf16 v[88:91], a[16:19], a[12:15], v[88:91]\n" \
"s_waitcnt vmcnt(19) lgkmcnt(0)\n" \
"v_mfma_f32_16x16x32_bf16 v[92:95], a[20:23], a[12:15], v[92:95]\n" \
"s_barrier\n" \
"v_add_f32 v23, 1.0, v23\n" \
"v_mfma_f32_16x16x32_bf16 v[96:99], a[72:75], a[48:51], v[96:99]\n" \
"s_mov_b32 s46, 1024\n" \
"s_add_u32 m0, s50, 32768\n" \
"ds_read_b64_tr_b16 a[24:25], v9 offset:512\n" \
"v_mfma_f32_16x16x32_bf16 v[100:103], a[76:79], a[48:51], v[100:103]\n" \
"buffer_load_dwordx4 v13, s[28:31], s46 offen lds\n" \
"v_mfma_f32_16x16x32_bf16 v[104:107], a[72:75], a[52:55], v[104:107]\n" \
"ds_read_b64_tr_b16 a[26:27], v10 offset:512\n" \
"v_mfma_f32_16x16x32_bf16 v[108:111], a[76:79], a[52:55], v[108:111]\n" \
"ds_read_b64_tr_b16 a[28:29], v11 offset:512\n" \
"v_mfma_f32_16x16x32_bf16 v[112:115], a[72:75], a[56:59], v[112:115]\n" \
"ds_read_b64_tr_b16 a[30:31], v12 offset:512\n" \
"v_mfma_f32_16x16x32_bf16 v[116:119], a[76:79], a[56:59], v[116:119]\n" \
"s_add_u32 m0, s50, 36864\n" \
"ds_read_b128 a[0:3], v7 offset:0\n" \
"v_mfma_f32_16x16x32_bf16 v[120:123], a[72:75], a[60:63], v[120:123]\n" \
"buffer_load_dwordx4 v14, s[28:31], s46 offen lds\n" \
"v_mfma_f32_16x16x32_bf16 v[124:127], a[76:79], a[60:63], v[124:127]\n" \
"ds_read_b128 a[4:7], v7 offset:2048\n" \
"v_mfma_f32_16x16x32_bf16 v[128:131], a[80:83], a[48:51], v[128:131]\n" \
"v_rcp_f32 v23, v23\n" \
"v_mfma_f32_16x16x32_bf16 v[132:135], a[84:87], a[48:51], v[132:135]\n" \
"ds_read_b128 a[8:11], v7 offset:4096\n" \
"v_mfma_f32_16x16x32_bf16 v[136:139], a[80:83], a[52:55], v[136:139]\n" \
"s_add_u32 m0, s50, 40960\n" \
"ds_read_b128 a[12:15], v7 offset:6144\n" \
"v_cvt_pk_bf16_f32 v23, v23, v23\n" \
"v_mfma_f32_16x16x32_bf16 v[140:143], a[84:87], a[52:55], v[140:143]\n" \
"buffer_load_dwordx4 v15, s[28:31], s46 offen lds\n" \
"v_mfma_f32_16x16x32_bf16 v[144:147], a[80:83], a[56:59], v[144:147]\n" \
"ds_read_b64_tr_b16 a[32:33], v9 offset:1024\n" \
"v_and_b32 v23, 0xffff0000, v23\n" \
"v_mfma_f32_16x16x32_bf16 v[148:151], a[84:87], a[56:59], v[148:151]\n" \
"ds_read_b64_tr_b16 a[34:35], v10 offset:1024\n" \
"v_mfma_f32_16x16x32_bf16 v[152:155], a[80:83], a[60:63], v[152:155]\n" \
"ds_read_b64_tr_b16 a[36:37], v11 offset:1024\n" \
"v_mfma_f32_16x16x32_bf16 v[156:159], a[84:87], a[60:63], v[156:159]\n" \
"s_add_u32 m0, s50, 45056\n" \
"ds_read_b64_tr_b16 a[38:39], v12 offset:1024\n" \
"v_mfma_f32_16x16x32_bf16 v[160:163], a[88:91], a[48:51], v[160:163]\n" \
"buffer_load_dwordx4 v16, s[28:31], s46 offen lds\n" \
"v_mfma_f32_16x16x32_bf16 v[164:167], a[92:95], a[48:51], v[164:167]\n" \
"ds_read_b64_tr_b16 a[40:41], v9 offset:1536\n" \
"v_mfma_f32_16x16x32_bf16 v[168:171], a[88:91], a[52:55], v[168:171]\n" \
"ds_read_b64_tr_b16 a[42:43], v10 offset:1536\n" \
"v_mfma_f32_16x16x32_bf16 v[172:175], a[92:95], a[52:55], v[172:175]\n" \
"ds_read_b64_tr_b16 a[44:45], v11 offset:1536\n" \
"v_mfma_f32_16x16x32_bf16 v[176:179], a[88:91], a[56:59], v[176:179]\n" \
"s_mov_b32 s46, 1024\n" \
"s_add_u32 m0, s50, 49152\n" \
"ds_read_b64_tr_b16 a[46:47], v12 offset:1536\n" \
"v_mfma_f32_16x16x32_bf16 v[180:183], a[92:95], a[56:59], v[180:183]\n" \
"buffer_load_dwordx4 v17, s[28:31], s46 offen lds\n" \
"v_mfma_f32_16x16x32_bf16 v[184:187], a[88:91], a[60:63], v[184:187]\n" \
"ds_read_b64_tr_b16 a[16:17], v9 offset:0\n" \
"v_mfma_f32_16x16x32_bf16 v[188:191], a[92:95], a[60:63], v[188:191]\n" \
"buffer_store_dword v23, v61, s[36:39], 0 offen sc1\n" \
"v_mfma_f32_16x16x32_bf16 v[64:67], a[64:67], a[48:51], v[64:67]\n" \
"ds_read_b64_tr_b16 a[18:19], v10 offset:0\n" \
"v_mfma_f32_16x16x32_bf16 v[68:71], a[68:71], a[48:51], v[68:71]\n" \
"s_add_u32 m0, s50, 53248\n" \
"ds_read_b64_tr_b16 a[20:21], v11 offset:0\n" \
"v_mfma_f32_16x16x32_bf16 v[72:75], a[64:67], a[52:55], v[72:75]\n" \
"buffer_load_dwordx4 v18, s[28:31], s46 offen lds\n" \
"v_mfma_f32_16x16x32_bf16 v[76:79], a[68:71], a[52:55], v[76:79]\n" \
"ds_read_b64_tr_b16 a[22:23], v12 offset:0\n" \
"v_mfma_f32_16x16x32_bf16 v[80:83], a[64:67], a[56:59], v[80:83]\n" \
"buffer_store_dwordx4 v[34:37], v4, s[36:39], s97 offen offset:0 sc1\n" \
"v_mfma_f32_16x16x32_bf16 v[84:87], a[68:71], a[56:59], v[84:87]\n" \
"s_add_u32 s96, s96, s92\n" \
"v_mfma_f32_16x16x32_bf16 v[88:91], a[64:67], a[60:63], v[88:91]\n" \
"s_cmp_lt_u32 %2, 2\ns_cselect_b32 s62, 1064960, 0\ns_cselect_b32 s9, -1, 1\n" \
"v_mfma_f32_16x16x32_bf16 v[92:95], a[68:71], a[60:63], v[92:95]\n" \
"s_waitcnt lgkmcnt(0)\n" \
"s_add_u32 s16, s16, s62\n" \
"s_add_u32 s17, s16, 8192\n" \
"v_mfma_f32_16x16x32_bf16 v[96:99], a[24:27], a[0:3], v[96:99]\n" \
"s_add_u32 m0, s50, 57344\n" \
"ds_read_b64_tr_b16 a[72:73], v9 offset:8704\n" \
"s_lshl_b32 s9, s9, 14\n" \
"v_mfma_f32_16x16x32_bf16 v[100:103], a[28:31], a[0:3], v[100:103]\n" \
"buffer_load_dwordx4 v19, s[28:31], s46 offen lds\n" \
"v_mfma_f32_16x16x32_bf16 v[104:107], a[24:27], a[4:7], v[104:107]\n" \
"s_mov_b32 s19, s9\n" \
"ds_read_b64_tr_b16 a[74:75], v10 offset:8704\n" \
"s_and_b32 s63, %2, 1\n" \
"v_mfma_f32_16x16x32_bf16 v[108:111], a[28:31], a[4:7], v[108:111]\n" \
"s_lshl_b32 s63, s63, 12\n" \
"ds_read_b64_tr_b16 a[76:77], v11 offset:8704\n" \
"v_add_u32 v60, s63, v4\n" \
"v_mfma_f32_16x16x32_bf16 v[112:115], a[24:27], a[8:11], v[112:115]\n" \
"s_lshr_b32 s62, s6, 1\n" \
"ds_read_b64_tr_b16 a[78:79], v12 offset:8704\n" \
"s_lshl_b32 s62, s62, 7\n" \
"v_mfma_f32_16x16x32_bf16 v[116:119], a[28:31], a[8:11], v[116:119]\n" \
"s_add_u32 m0, s50, 61440\n" \
"ds_read_b128 a[48:51], v8 offset:0\n" \
"s_add_u32 s62, s62, 94375936\n" \
"v_mfma_f32_16x16x32_bf16 v[120:123], a[24:27], a[12:15], v[120:123]\n" \
"buffer_load_dwordx4 v20, s[28:31], s46 offen lds\n" \
"v_mfma_f32_16x16x32_bf16 v[124:127], a[28:31], a[12:15], v[124:127]\n" \
"ds_read_b128 a[52:55], v8 offset:2048\n" \
"v_bfe_u32 v57, v4, 4, 4\n" \
"v_mfma_f32_16x16x32_bf16 v[128:131], a[32:35], a[0:3], v[128:131]\n" \
"v_lshl_add_u32 v57, v57, 3, s62\n" \
"v_mfma_f32_16x16x32_bf16 v[132:135], a[36:39], a[0:3], v[132:135]\n" \
"ds_read_b128 a[56:59], v8 offset:4096\n" \
"v_mfma_f32_16x16x32_bf16 v[136:139], a[32:35], a[4:7], v[136:139]\n" \
"s_mov_b32 s47, 4718592\n" \
"s_add_u32 m0, s51, 49152\n" \
"ds_read_b128 a[60:63], v8 offset:6144\n" \
"v_mfma_f32_16x16x32_bf16 v[140:143], a[36:39], a[4:7], v[140:143]\n" \
"buffer_load_dwordx4 v21, s[32:35], s47 offen offset:0 lds\n" \
"v_mfma_f32_16x16x32_bf16 v[144:147], a[32:35], a[8:11], v[144:147]\n" \
"ds_read_b64_tr_b16 a[80:81], v9 offset:9216\n" \
"v_mfma_f32_16x16x32_bf16 v[148:151], a[36:39], a[8:11], v[148:151]\n" \
"ds_read_b64_tr_b16 a[82:83], v10 offset:9216\n" \
"v_mfma_f32_16x16x32_bf16 v[152:155], a[32:35], a[12:15], v[152:155]\n" \
"ds_read_b64_tr_b16 a[84:85], v11 offset:9216\n" \
"v_mfma_f32_16x16x32_bf16 v[156:159], a[36:39], a[12:15], v[156:159]\n" \
"s_add_u32 m0, s51, 50048\n" \
"ds_read_b64_tr_b16 a[86:87], v12 offset:9216\n" \
"v_mfma_f32_16x16x32_bf16 v[160:163], a[40:43], a[0:3], v[160:163]\n" \
"LH11_%=:\ns_cbranch_execz LH15_%=\n" \
"buffer_load_dwordx4 v21, s[32:35], s47 offen offset:128 lds\n" \
"v_mfma_f32_16x16x32_bf16 v[164:167], a[44:47], a[0:3], v[164:167]\n" \
"ds_read_b64_tr_b16 a[88:89], v9 offset:9728\n" \
"v_mfma_f32_16x16x32_bf16 v[168:171], a[40:43], a[4:7], v[168:171]\n" \
"ds_read_b64_tr_b16 a[90:91], v10 offset:9728\n" \
"v_mfma_f32_16x16x32_bf16 v[172:175], a[44:47], a[4:7], v[172:175]\n" \
"ds_read_b64_tr_b16 a[92:93], v11 offset:9728\n" \
"v_mfma_f32_16x16x32_bf16 v[176:179], a[40:43], a[8:11], v[176:179]\n" \
"s_mov_b32 s47, 4980736\n" \
"s_add_u32 m0, s51, 57344\n" \
"ds_read_b64_tr_b16 a[94:95], v12 offset:9728\n" \
"v_mfma_f32_16x16x32_bf16 v[180:183], a[44:47], a[8:11], v[180:183]\n" \
"buffer_load_dwordx4 v21, s[32:35], s47 offen offset:0 lds\n" \
"v_mfma_f32_16x16x32_bf16 v[184:187], a[40:43], a[12:15], v[184:187]\n" \
"ds_read_b64_tr_b16 a[64:65], v9 offset:8192\n" \
"v_mfma_f32_16x16x32_bf16 v[188:191], a[44:47], a[12:15], v[188:191]\n" \
"v_mfma_f32_16x16x32_bf16 v[64:67], a[16:19], a[0:3], v[64:67]\n" \
"ds_read_b64_tr_b16 a[66:67], v10 offset:8192\n" \
"v_mfma_f32_16x16x32_bf16 v[68:71], a[20:23], a[0:3], v[68:71]\n" \
"s_add_u32 m0, s51, 58240\n" \
"ds_read_b64_tr_b16 a[68:69], v11 offset:8192\n" \
"v_mfma_f32_16x16x32_bf16 v[72:75], a[16:19], a[4:7], v[72:75]\n" \
"buffer_load_dwordx4 v21, s[32:35], s47 offen offset:128 lds\n" \
"v_mfma_f32_16x16x32_bf16 v[76:79], a[20:23], a[4:7], v[76:79]\n" \
"ds_read_b64_tr_b16 a[70:71], v12 offset:8192\n" \
"v_mfma_f32_16x16x32_bf16 v[80:83], a[16:19], a[8:11], v[80:83]\n" \
"buffer_store_dwordx4 v[38:41], v4, s[36:39], s96 offen offset:0 sc1\n" \
"v_mfma_f32_16x16x32_bf16 v[84:87], a[20:23], a[8:11], v[84:87]\n" \
"s_add_u32 s97, s96, s98\n" \
"v_mfma_f32_16x16x32_bf16 v[88:91], a[16:19], a[12:15], v[88:91]\n" \
"s_waitcnt vmcnt(20) lgkmcnt(0)\n" \
"v_mfma_f32_16x16x32_bf16 v[92:95], a[20:23], a[12:15], v[92:95]\n" \
"s_barrier\n" \
"v_mfma_f32_16x16x32_bf16 v[96:99], a[72:75], a[48:51], v[96:99]\n" \
"s_mov_b32 s46, 1152\n" \
"s_add_u32 m0, s50, 65536\n" \
"ds_read_b64_tr_b16 a[24:25], v9 offset:16896\n" \
"v_mfma_f32_16x16x32_bf16 v[100:103], a[76:79], a[48:51], v[100:103]\n" \
"buffer_load_dwordx4 v13, s[28:31], s46 offen lds\n" \
"v_mfma_f32_16x16x32_bf16 v[104:107], a[72:75], a[52:55], v[104:107]\n" \
"ds_read_b64_tr_b16 a[26:27], v10 offset:16896\n" \
"v_mfma_f32_16x16x32_bf16 v[108:111], a[76:79], a[52:55], v[108:111]\n" \
"ds_read_b64_tr_b16 a[28:29], v11 offset:16896\n" \
"v_mfma_f32_16x16x32_bf16 v[112:115], a[72:75], a[56:59], v[112:115]\n" \
"ds_read_b64_tr_b16 a[30:31], v12 offset:16896\n" \
"v_mfma_f32_16x16x32_bf16 v[116:119], a[76:79], a[56:59], v[116:119]\n" \
"s_add_u32 m0, s50, 69632\n" \
"ds_read_b128 a[0:3], v5 offset:0\n" \
"v_mfma_f32_16x16x32_bf16 v[120:123], a[72:75], a[60:63], v[120:123]\n" \
"buffer_load_dwordx4 v14, s[28:31], s46 offen lds\n" \
"v_mfma_f32_16x16x32_bf16 v[124:127], a[76:79], a[60:63], v[124:127]\n" \
"ds_read_b128 a[4:7], v5 offset:2048\n" \
"v_mfma_f32_16x16x32_bf16 v[128:131], a[80:83], a[48:51], v[128:131]\n" \
"v_mfma_f32_16x16x32_bf16 v[132:135], a[84:87], a[48:51], v[132:135]\n" \
"ds_read_b128 a[8:11], v5 offset:4096\n" \
"v_mfma_f32_16x16x32_bf16 v[136:139], a[80:83], a[52:55], v[136:139]\n" \
"s_add_u32 m0, s50, 73728\n" \
"ds_read_b128 a[12:15], v5 offset:6144\n" \
"v_mfma_f32_16x16x32_bf16 v[140:143], a[84:87], a[52:55], v[140:143]\n" \
"buffer_load_dwordx4 v15, s[28:31], s46 offen lds\n" \
"v_mfma_f32_16x16x32_bf16 v[144:147], a[80:83], a[56:59], v[144:147]\n" \
"ds_read_b64_tr_b16 a[32:33], v9 offset:17408\n" \
"v_mfma_f32_16x16x32_bf16 v[148:151], a[84:87], a[56:59], v[148:151]\n" \
"ds_read_b64_tr_b16 a[34:35], v10 offset:17408\n" \
"v_mfma_f32_16x16x32_bf16 v[152:155], a[80:83], a[60:63], v[152:155]\n" \
"ds_read_b64_tr_b16 a[36:37], v11 offset:17408\n" \
"v_mfma_f32_16x16x32_bf16 v[156:159], a[84:87], a[60:63], v[156:159]\n" \
"s_add_u32 m0, s50, 77824\n" \
"ds_read_b64_tr_b16 a[38:39], v12 offset:17408\n" \
"v_mfma_f32_16x16x32_bf16 v[160:163], a[88:91], a[48:51], v[160:163]\n" \
"buffer_load_dwordx4 v16, s[28:31], s46 offen lds\n" \
"v_mfma_f32_16x16x32_bf16 v[164:167], a[92:95], a[48:51], v[164:167]\n" \
"ds_read_b64_tr_b16 a[40:41], v9 offset:17920\n" \
"v_mfma_f32_16x16x32_bf16 v[168:171], a[88:91], a[52:55], v[168:171]\n" \
"ds_read_b64_tr_b16 a[42:43], v10 offset:17920\n" \
"v_mfma_f32_16x16x32_bf16 v[172:175], a[92:95], a[52:55], v[172:175]\n" \
"ds_read_b64_tr_b16 a[44:45], v11 offset:17920\n" \
"v_mfma_f32_16x16x32_bf16 v[176:179], a[88:91], a[56:59], v[176:179]\n" \
"s_mov_b32 s46, 1152\n" \
"s_add_u32 m0, s50, 81920\n" \
"ds_read_b64_tr_b16 a[46:47], v12 offset:17920\n" \
"v_mfma_f32_16x16x32_bf16 v[180:183], a[92:95], a[56:59], v[180:183]\n" \
"buffer_load_dwordx4 v17, s[28:31], s46 offen lds\n" \
"v_mfma_f32_16x16x32_bf16 v[184:187], a[88:91], a[60:63], v[184:187]\n" \
"ds_read_b64_tr_b16 a[16:17], v9 offset:16384\n" \
"v_mfma_f32_16x16x32_bf16 v[188:191], a[92:95], a[60:63], v[188:191]\n" \
"v_mfma_f32_16x16x32_bf16 v[64:67], a[64:67], a[48:51], v[64:67]\n" \
"ds_read_b64_tr_b16 a[18:19], v10 offset:16384\n" \
"v_mfma_f32_16x16x32_bf16 v[68:71], a[68:71], a[48:51], v[68:71]\n" \
"s_add_u32 m0, s50, 86016\n" \
"ds_read_b64_tr_b16 a[20:21], v11 offset:16384\n" \
"v_mfma_f32_16x16x32_bf16 v[72:75], a[64:67], a[52:55], v[72:75]\n" \
"buffer_load_dwordx4 v18, s[28:31], s46 offen lds\n" \
"v_mfma_f32_16x16x32_bf16 v[76:79], a[68:71], a[52:55], v[76:79]\n" \
"ds_read_b64_tr_b16 a[22:23], v12 offset:16384\n" \
"v_mfma_f32_16x16x32_bf16 v[80:83], a[64:67], a[56:59], v[80:83]\n" \
"buffer_store_dwordx4 v[42:45], v4, s[36:39], s97 offen offset:0 sc1\n" \
"v_mfma_f32_16x16x32_bf16 v[84:87], a[68:71], a[56:59], v[84:87]\n" \
"v_mfma_f32_16x16x32_bf16 v[88:91], a[64:67], a[60:63], v[88:91]\n" \
"v_mfma_f32_16x16x32_bf16 v[92:95], a[68:71], a[60:63], v[92:95]\n" \
"s_waitcnt lgkmcnt(0)\n" \
"v_mfma_f32_16x16x32_bf16 v[96:99], a[24:27], a[0:3], v[96:99]\n" \
"s_add_u32 m0, s50, 90112\n" \
"ds_read_b64_tr_b16 a[72:73], v9 offset:25088\n" \
"v_mfma_f32_16x16x32_bf16 v[100:103], a[28:31], a[0:3], v[100:103]\n" \
"buffer_load_dwordx4 v19, s[28:31], s46 offen lds\n" \
"v_mfma_f32_16x16x32_bf16 v[104:107], a[24:27], a[4:7], v[104:107]\n" \
"ds_read_b64_tr_b16 a[74:75], v10 offset:25088\n" \
"v_mfma_f32_16x16x32_bf16 v[108:111], a[28:31], a[4:7], v[108:111]\n" \
"ds_read_b64_tr_b16 a[76:77], v11 offset:25088\n" \
"v_mfma_f32_16x16x32_bf16 v[112:115], a[24:27], a[8:11], v[112:115]\n" \
"ds_read_b64_tr_b16 a[78:79], v12 offset:25088\n" \
"v_mfma_f32_16x16x32_bf16 v[116:119], a[28:31], a[8:11], v[116:119]\n" \
"s_add_u32 m0, s50, 94208\n" \
"ds_read_b128 a[48:51], v6 offset:0\n" \
"v_mfma_f32_16x16x32_bf16 v[120:123], a[24:27], a[12:15], v[120:123]\n" \
"buffer_load_dwordx4 v20, s[28:31], s46 offen lds\n" \
"v_mfma_f32_16x16x32_bf16 v[124:127], a[28:31], a[12:15], v[124:127]\n" \
"ds_read_b128 a[52:55], v6 offset:2048\n" \
"v_mfma_f32_16x16x32_bf16 v[128:131], a[32:35], a[0:3], v[128:131]\n" \
"v_mfma_f32_16x16x32_bf16 v[132:135], a[36:39], a[0:3], v[132:135]\n" \
"ds_read_b128 a[56:59], v6 offset:4096\n" \
"v_mfma_f32_16x16x32_bf16 v[136:139], a[32:35], a[4:7], v[136:139]\n" \
"s_mov_b32 s47, 5242880\n" \
"s_add_u32 m0, s51, 0\n" \
"ds_read_b128 a[60:63], v6 offset:6144\n" \
"v_mfma_f32_16x16x32_bf16 v[140:143], a[36:39], a[4:7], v[140:143]\n" \
"buffer_load_dwordx4 v21, s[32:35], s47 offen offset:0 lds\n" \
"v_mfma_f32_16x16x32_bf16 v[144:147], a[32:35], a[8:11], v[144:147]\n" \
"ds_read_b64_tr_b16 a[80:81], v9 offset:25600\n" \
"v_mfma_f32_16x16x32_bf16 v[148:151], a[36:39], a[8:11], v[148:151]\n" \
"ds_read_b64_tr_b16 a[82:83], v10 offset:25600\n" \
"v_mfma_f32_16x16x32_bf16 v[152:155], a[32:35], a[12:15], v[152:155]\n" \
"ds_read_b64_tr_b16 a[84:85], v11 offset:25600\n" \
"v_mfma_f32_16x16x32_bf16 v[156:159], a[36:39], a[12:15], v[156:159]\n" \
"s_add_u32 m0, s51, 896\n" \
"ds_read_b64_tr_b16 a[86:87], v12 offset:25600\n" \
"v_mfma_f32_16x16x32_bf16 v[160:163], a[40:43], a[0:3], v[160:163]\n" \
"buffer_load_dwordx4 v21, s[32:35], s47 offen offset:128 lds\n" \
"v_mfma_f32_16x16x32_bf16 v[164:167], a[44:47], a[0:3], v[164:167]\n" \
"ds_read_b64_tr_b16 a[88:89], v9 offset:26112\n" \
"v_mfma_f32_16x16x32_bf16 v[168:171], a[40:43], a[4:7], v[168:171]\n" \
"ds_read_b64_tr_b16 a[90:91], v10 offset:26112\n" \
"v_mfma_f32_16x16x32_bf16 v[172:175], a[44:47], a[4:7], v[172:175]\n" \
"ds_read_b64_tr_b16 a[92:93], v11 offset:26112\n" \
"v_mfma_f32_16x16x32_bf16 v[176:179], a[40:43], a[8:11], v[176:179]\n" \
"s_mov_b32 s47, 5505024\n" \
"s_add_u32 m0, s51, 8192\n" \
"ds_read_b64_tr_b16 a[94:95], v12 offset:26112\n" \
"v_mfma_f32_16x16x32_bf16 v[180:183], a[44:47], a[8:11], v[180:183]\n" \
"buffer_load_dwordx4 v21, s[32:35], s47 offen offset:0 lds\n" \
"v_mfma_f32_16x16x32_bf16 v[184:187], a[40:43], a[12:15], v[184:187]\n" \
"ds_read_b64_tr_b16 a[64:65], v9 offset:24576\n" \
"v_mfma_f32_16x16x32_bf16 v[188:191], a[44:47], a[12:15], v[188:191]\n" \
"v_mfma_f32_16x16x32_bf16 v[64:67], a[16:19], a[0:3], v[64:67]\n" \
"ds_read_b64_tr_b16 a[66:67], v10 offset:24576\n" \
"v_mfma_f32_16x16x32_bf16 v[68:71], a[20:23], a[0:3], v[68:71]\n" \
"s_add_u32 m0, s51, 9088\n" \
"ds_read_b64_tr_b16 a[68:69], v11 offset:24576\n" \
"v_mfma_f32_16x16x32_bf16 v[72:75], a[16:19], a[4:7], v[72:75]\n" \
"buffer_load_dwordx4 v21, s[32:35], s47 offen offset:128 lds\n" \
"v_mfma_f32_16x16x32_bf16 v[76:79], a[20:23], a[4:7], v[76:79]\n" \
"ds_read_b64_tr_b16 a[70:71], v12 offset:24576\n" \
"v_mfma_f32_16x16x32_bf16 v[80:83], a[16:19], a[8:11], v[80:83]\n" \
"v_mfma_f32_16x16x32_bf16 v[84:87], a[20:23], a[8:11], v[84:87]\n" \
"v_mfma_f32_16x16x32_bf16 v[88:91], a[16:19], a[12:15], v[88:91]\n" \
"s_waitcnt vmcnt(18) lgkmcnt(0)\n" \
"v_mfma_f32_16x16x32_bf16 v[92:95], a[20:23], a[12:15], v[92:95]\n" \
"s_barrier\n" \
"v_mfma_f32_16x16x32_bf16 v[96:99], a[72:75], a[48:51], v[96:99]\n" \
"s_mov_b32 s46, 1280\n" \
"s_add_u32 m0, s50, 0\n" \
"ds_read_b64_tr_b16 a[24:25], v9 offset:33280\n" \
"v_mfma_f32_16x16x32_bf16 v[100:103], a[76:79], a[48:51], v[100:103]\n" \
"buffer_load_dwordx4 v13, s[28:31], s46 offen lds\n" \
"v_mfma_f32_16x16x32_bf16 v[104:107], a[72:75], a[52:55], v[104:107]\n" \
"ds_read_b64_tr_b16 a[26:27], v10 offset:33280\n" \
"v_mfma_f32_16x16x32_bf16 v[108:111], a[76:79], a[52:55], v[108:111]\n" \
"ds_read_b64_tr_b16 a[28:29], v11 offset:33280\n" \
"v_mfma_f32_16x16x32_bf16 v[112:115], a[72:75], a[56:59], v[112:115]\n" \
"ds_read_b64_tr_b16 a[30:31], v12 offset:33280\n" \
"v_mfma_f32_16x16x32_bf16 v[116:119], a[76:79], a[56:59], v[116:119]\n" \
"s_add_u32 m0, s50, 4096\n" \
"ds_read_b128 a[0:3], v5 offset:32768\n" \
"v_mfma_f32_16x16x32_bf16 v[120:123], a[72:75], a[60:63], v[120:123]\n" \
"buffer_load_dwordx4 v14, s[28:31], s46 offen lds\n" \
"v_mfma_f32_16x16x32_bf16 v[124:127], a[76:79], a[60:63], v[124:127]\n" \
"ds_read_b128 a[4:7], v5 offset:34816\n" \
"v_mfma_f32_16x16x32_bf16 v[128:131], a[80:83], a[48:51], v[128:131]\n" \
"v_mfma_f32_16x16x32_bf16 v[132:135], a[84:87], a[48:51], v[132:135]\n" \
"ds_read_b128 a[8:11], v5 offset:36864\n" \
"v_mfma_f32_16x16x32_bf16 v[136:139], a[80:83], a[52:55], v[136:139]\n" \
"s_add_u32 m0, s50, 8192\n" \
"ds_read_b128 a[12:15], v5 offset:38912\n" \
"v_mfma_f32_16x16x32_bf16 v[140:143], a[84:87], a[52:55], v[140:143]\n" \
"buffer_load_dwordx4 v15, s[28:31], s46 offen lds\n" \
"v_mfma_f32_16x16x32_bf16 v[144:147], a[80:83], a[56:59], v[144:147]\n" \
"ds_read_b64_tr_b16 a[32:33], v9 offset:33792\n" \
"v_mfma_f32_16x16x32_bf16 v[148:151], a[84:87], a[56:59], v[148:151]\n" \
"ds_read_b64_tr_b16 a[34:35], v10 offset:33792\n" \
"v_mfma_f32_16x16x32_bf16 v[152:155], a[80:83], a[60:63], v[152:155]\n" \
"ds_read_b64_tr_b16 a[36:37], v11 offset:33792\n" \
"v_mfma_f32_16x16x32_bf16 v[156:159], a[84:87], a[60:63], v[156:159]\n" \
"s_add_u32 m0, s50, 12288\n" \
"ds_read_b64_tr_b16 a[38:39], v12 offset:33792\n" \
"v_mfma_f32_16x16x32_bf16 v[160:163], a[88:91], a[48:51], v[160:163]\n" \
"buffer_load_dwordx4 v16, s[28:31], s46 offen lds\n" \
"v_mfma_f32_16x16x32_bf16 v[164:167], a[92:95], a[48:51], v[164:167]\n" \
"ds_read_b64_tr_b16 a[40:41], v9 offset:34304\n" \
"v_mfma_f32_16x16x32_bf16 v[168:171], a[88:91], a[52:55], v[168:171]\n" \
"ds_read_b64_tr_b16 a[42:43], v10 offset:34304\n" \
"v_mfma_f32_16x16x32_bf16 v[172:175], a[92:95], a[52:55], v[172:175]\n" \
"ds_read_b64_tr_b16 a[44:45], v11 offset:34304\n" \
"v_mfma_f32_16x16x32_bf16 v[176:179], a[88:91], a[56:59], v[176:179]\n" \
"s_mov_b32 s46, 1280\n" \
"s_add_u32 m0, s50, 16384\n" \
"ds_read_b64_tr_b16 a[46:47], v12 offset:34304\n" \
"v_mfma_f32_16x16x32_bf16 v[180:183], a[92:95], a[56:59], v[180:183]\n" \
"buffer_load_dwordx4 v17, s[28:31], s46 offen lds\n" \
"v_mfma_f32_16x16x32_bf16 v[184:187], a[88:91], a[60:63], v[184:187]\n" \
"ds_read_b64_tr_b16 a[16:17], v9 offset:32768\n" \
"v_mfma_f32_16x16x32_bf16 v[188:191], a[92:95], a[60:63], v[188:191]\n" \
"v_mfma_f32_16x16x32_bf16 v[64:67], a[64:67], a[48:51], v[64:67]\n" \
"ds_read_b64_tr_b16 a[18:19], v10 offset:32768\n" \
"v_mfma_f32_16x16x32_bf16 v[68:71], a[68:71], a[48:51], v[68:71]\n" \
"s_add_u32 m0, s50, 20480\n" \
"ds_read_b64_tr_b16 a[20:21], v11 offset:32768\n" \
"v_mfma_f32_16x16x32_bf16 v[72:75], a[64:67], a[52:55], v[72:75]\n" \
"buffer_load_dwordx4 v18, s[28:31], s46 offen lds\n" \
"v_mfma_f32_16x16x32_bf16 v[76:79], a[68:71], a[52:55], v[76:79]\n" \
"ds_read_b64_tr_b16 a[22:23], v12 offset:32768\n" \
"v_mfma_f32_16x16x32_bf16 v[80:83], a[64:67], a[56:59], v[80:83]\n" \
"v_mfma_f32_16x16x32_bf16 v[84:87], a[68:71], a[56:59], v[84:87]\n" \
"v_mfma_f32_16x16x32_bf16 v[88:91], a[64:67], a[60:63], v[88:91]\n" \
"v_mfma_f32_16x16x32_bf16 v[92:95], a[68:71], a[60:63], v[92:95]\n" \
"s_waitcnt lgkmcnt(0)\n" \
"v_mfma_f32_16x16x32_bf16 v[96:99], a[24:27], a[0:3], v[96:99]\n" \
"s_add_u32 m0, s50, 24576\n" \
"ds_read_b64_tr_b16 a[72:73], v9 offset:41472\n" \
"v_mfma_f32_16x16x32_bf16 v[100:103], a[28:31], a[0:3], v[100:103]\n" \
"buffer_load_dwordx4 v19, s[28:31], s46 offen lds\n" \
"v_mfma_f32_16x16x32_bf16 v[104:107], a[24:27], a[4:7], v[104:107]\n" \
"ds_read_b64_tr_b16 a[74:75], v10 offset:41472\n" \
"v_mfma_f32_16x16x32_bf16 v[108:111], a[28:31], a[4:7], v[108:111]\n" \
"ds_read_b64_tr_b16 a[76:77], v11 offset:41472\n" \
"v_mfma_f32_16x16x32_bf16 v[112:115], a[24:27], a[8:11], v[112:115]\n" \
"ds_read_b64_tr_b16 a[78:79], v12 offset:41472\n" \
"v_mfma_f32_16x16x32_bf16 v[116:119], a[28:31], a[8:11], v[116:119]\n" \
"s_add_u32 m0, s50, 28672\n" \
"ds_read_b128 a[48:51], v6 offset:32768\n" \
"v_mfma_f32_16x16x32_bf16 v[120:123], a[24:27], a[12:15], v[120:123]\n" \
"buffer_load_dwordx4 v20, s[28:31], s46 offen lds\n" \
"v_mfma_f32_16x16x32_bf16 v[124:127], a[28:31], a[12:15], v[124:127]\n" \
"ds_read_b128 a[52:55], v6 offset:34816\n" \
"v_mfma_f32_16x16x32_bf16 v[128:131], a[32:35], a[0:3], v[128:131]\n" \
"v_mfma_f32_16x16x32_bf16 v[132:135], a[36:39], a[0:3], v[132:135]\n" \
"ds_read_b128 a[56:59], v6 offset:36864\n" \
"v_mfma_f32_16x16x32_bf16 v[136:139], a[32:35], a[4:7], v[136:139]\n" \
"s_mov_b32 s47, 5767168\n" \
"s_add_u32 m0, s51, 16384\n" \
"ds_read_b128 a[60:63], v6 offset:38912\n" \
"v_mfma_f32_16x16x32_bf16 v[140:143], a[36:39], a[4:7], v[140:143]\n" \
"buffer_load_dwordx4 v21, s[32:35], s47 offen offset:0 lds\n" \
"v_mfma_f32_16x16x32_bf16 v[144:147], a[32:35], a[8:11], v[144:147]\n" \
"ds_read_b64_tr_b16 a[80:81], v9 offset:41984\n" \
"v_mfma_f32_16x16x32_bf16 v[148:151], a[36:39], a[8:11], v[148:151]\n" \
"ds_read_b64_tr_b16 a[82:83], v10 offset:41984\n" \
"v_mfma_f32_16x16x32_bf16 v[152:155], a[32:35], a[12:15], v[152:155]\n" \
"ds_read_b64_tr_b16 a[84:85], v11 offset:41984\n" \
"v_mfma_f32_16x16x32_bf16 v[156:159], a[36:39], a[12:15], v[156:159]\n" \
"s_add_u32 m0, s51, 17280\n" \
"ds_read_b64_tr_b16 a[86:87], v12 offset:41984\n" \
"v_mfma_f32_16x16x32_bf16 v[160:163], a[40:43], a[0:3], v[160:163]\n" \
"buffer_load_dwordx4 v21, s[32:35], s47 offen offset:128 lds\n" \
"v_mfma_f32_16x16x32_bf16 v[164:167], a[44:47], a[0:3], v[164:167]\n" \
"ds_read_b64_tr_b16 a[88:89], v9 offset:42496\n" \
"v_mfma_f32_16x16x32_bf16 v[168:171], a[40:43], a[4:7], v[168:171]\n" \
"ds_read_b64_tr_b16 a[90:91], v10 offset:42496\n" \
"v_mfma_f32_16x16x32_bf16 v[172:175], a[44:47], a[4:7], v[172:175]\n" \
"ds_read_b64_tr_b16 a[92:93], v11 offset:42496\n" \
"v_mfma_f32_16x16x32_bf16 v[176:179], a[40:43], a[8:11], v[176:179]\n" \
"s_mov_b32 s47, 6029312\n" \
"s_add_u32 m0, s51, 24576\n" \
"ds_read_b64_tr_b16 a[94:95], v12 offset:42496\n" \
"v_mfma_f32_16x16x32_bf16 v[180:183], a[44:47], a[8:11], v[180:183]\n" \
"buffer_load_dwordx4 v21, s[32:35], s47 offen offset:0 lds\n" \
"v_mfma_f32_16x16x32_bf16 v[184:187], a[40:43], a[12:15], v[184:187]\n" \
"ds_read_b64_tr_b16 a[64:65], v9 offset:40960\n" \
"v_mfma_f32_16x16x32_bf16 v[188:191], a[44:47], a[12:15], v[188:191]\n" \
"v_mfma_f32_16x16x32_bf16 v[64:67], a[16:19], a[0:3], v[64:67]\n" \
"ds_read_b64_tr_b16 a[66:67], v10 offset:40960\n" \
"v_mfma_f32_16x16x32_bf16 v[68:71], a[20:23], a[0:3], v[68:71]\n" \
"s_add_u32 m0, s51, 25472\n" \
"ds_read_b64_tr_b16 a[68:69], v11 offset:40960\n" \
"v_mfma_f32_16x16x32_bf16 v[72:75], a[16:19], a[4:7], v[72:75]\n" \
"buffer_load_dwordx4 v21, s[32:35], s47 offen offset:128 lds\n" \
"v_mfma_f32_16x16x32_bf16 v[76:79], a[20:23], a[4:7], v[76:79]\n" \
"ds_read_b64_tr_b16 a[70:71], v12 offset:40960\n" \
"v_mfma_f32_16x16x32_bf16 v[80:83], a[16:19], a[8:11], v[80:83]\n" \
"v_mfma_f32_16x16x32_bf16 v[84:87], a[20:23], a[8:11], v[84:87]\n" \
"v_mfma_f32_16x16x32_bf16 v[88:91], a[16:19], a[12:15], v[88:91]\n" \
"s_waitcnt vmcnt(16) lgkmcnt(0)\n" \
"v_mfma_f32_16x16x32_bf16 v[92:95], a[20:23], a[12:15], v[92:95]\n" \
"s_barrier\n" \
"v_mfma_f32_16x16x32_bf16 v[96:99], a[72:75], a[48:51], v[96:99]\n" \
"s_mov_b32 s46, 1408\n" \
"s_add_u32 m0, s50, 32768\n" \
"ds_read_b64_tr_b16 a[24:25], v9 offset:49664\n" \
"v_mfma_f32_16x16x32_bf16 v[100:103], a[76:79], a[48:51], v[100:103]\n" \
"buffer_load_dwordx4 v13, s[28:31], s46 offen lds\n" \
"v_mfma_f32_16x16x32_bf16 v[104:107], a[72:75], a[52:55], v[104:107]\n" \
"ds_read_b64_tr_b16 a[26:27], v10 offset:49664\n" \
"v_mfma_f32_16x16x32_bf16 v[108:111], a[76:79], a[52:55], v[108:111]\n" \
"ds_read_b64_tr_b16 a[28:29], v11 offset:49664\n" \
"v_mfma_f32_16x16x32_bf16 v[112:115], a[72:75], a[56:59], v[112:115]\n" \
"ds_read_b64_tr_b16 a[30:31], v12 offset:49664\n" \
"v_mfma_f32_16x16x32_bf16 v[116:119], a[76:79], a[56:59], v[116:119]\n" \
"s_add_u32 m0, s50, 36864\n" \
"ds_read_b128 a[0:3], v7 offset:0\n" \
"v_mfma_f32_16x16x32_bf16 v[120:123], a[72:75], a[60:63], v[120:123]\n" \
"buffer_load_dwordx4 v14, s[28:31], s46 offen lds\n" \
"v_mfma_f32_16x16x32_bf16 v[124:127], a[76:79], a[60:63], v[124:127]\n" \
"ds_read_b128 a[4:7], v7 offset:2048\n" \
"v_mfma_f32_16x16x32_bf16 v[128:131], a[80:83], a[48:51], v[128:131]\n" \
"v_mfma_f32_16x16x32_bf16 v[132:135], a[84:87], a[48:51], v[132:135]\n" \
"ds_read_b128 a[8:11], v7 offset:4096\n" \
"v_mfma_f32_16x16x32_bf16 v[136:139], a[80:83], a[52:55], v[136:139]\n" \
"s_add_u32 m0, s50, 40960\n" \
"ds_read_b128 a[12:15], v7 offset:6144\n" \
"v_mfma_f32_16x16x32_bf16 v[140:143], a[84:87], a[52:55], v[140:143]\n" \
"buffer_load_dwordx4 v15, s[28:31], s46 offen lds\n" \
"v_mfma_f32_16x16x32_bf16 v[144:147], a[80:83], a[56:59], v[144:147]\n" \
"ds_read_b64_tr_b16 a[32:33], v9 offset:50176\n" \
"v_mfma_f32_16x16x32_bf16 v[148:151], a[84:87], a[56:59], v[148:151]\n" \
"ds_read_b64_tr_b16 a[34:35], v10 offset:50176\n" \
"v_mfma_f32_16x16x32_bf16 v[152:155], a[80:83], a[60:63], v[152:155]\n" \
"ds_read_b64_tr_b16 a[36:37], v11 offset:50176\n" \
"v_mfma_f32_16x16x32_bf16 v[156:159], a[84:87], a[60:63], v[156:159]\n" \
"s_add_u32 m0, s50, 45056\n" \
"ds_read_b64_tr_b16 a[38:39], v12 offset:50176\n" \
"v_mfma_f32_16x16x32_bf16 v[160:163], a[88:91], a[48:51], v[160:163]\n" \
"buffer_load_dwordx4 v16, s[28:31], s46 offen lds\n" \
"v_mfma_f32_16x16x32_bf16 v[164:167], a[92:95], a[48:51], v[164:167]\n" \
"ds_read_b64_tr_b16 a[40:41], v9 offset:50688\n" \
"v_mfma_f32_16x16x32_bf16 v[168:171], a[88:91], a[52:55], v[168:171]\n" \
"ds_read_b64_tr_b16 a[42:43], v10 offset:50688\n" \
"v_mfma_f32_16x16x32_bf16 v[172:175], a[92:95], a[52:55], v[172:175]\n" \
"ds_read_b64_tr_b16 a[44:45], v11 offset:50688\n" \
"v_mfma_f32_16x16x32_bf16 v[176:179], a[88:91], a[56:59], v[176:179]\n" \
"s_mov_b32 s46, 1408\n" \
"s_add_u32 m0, s50, 49152\n" \
"ds_read_b64_tr_b16 a[46:47], v12 offset:50688\n" \
"v_mfma_f32_16x16x32_bf16 v[180:183], a[92:95], a[56:59], v[180:183]\n" \
"buffer_load_dwordx4 v17, s[28:31], s46 offen lds\n" \
"v_mfma_f32_16x16x32_bf16 v[184:187], a[88:91], a[60:63], v[184:187]\n" \
"ds_read_b64_tr_b16 a[16:17], v9 offset:49152\n" \
"v_mfma_f32_16x16x32_bf16 v[188:191], a[92:95], a[60:63], v[188:191]\n" \
"v_mfma_f32_16x16x32_bf16 v[64:67], a[64:67], a[48:51], v[64:67]\n" \
"ds_read_b64_tr_b16 a[18:19], v10 offset:49152\n" \
"v_mfma_f32_16x16x32_bf16 v[68:71], a[68:71], a[48:51], v[68:71]\n" \
"s_add_u32 m0, s50, 53248\n" \
"ds_read_b64_tr_b16 a[20:21], v11 offset:49152\n" \
"v_mfma_f32_16x16x32_bf16 v[72:75], a[64:67], a[52:55], v[72:75]\n" \
"buffer_load_dwordx4 v18, s[28:31], s46 offen lds\n" \
"v_mfma_f32_16x16x32_bf16 v[76:79], a[68:71], a[52:55], v[76:79]\n" \
"ds_read_b64_tr_b16 a[22:23], v12 offset:49152\n" \
"v_mfma_f32_16x16x32_bf16 v[80:83], a[64:67], a[56:59], v[80:83]\n" \
"v_mfma_f32_16x16x32_bf16 v[84:87], a[68:71], a[56:59], v[84:87]\n" \
"v_mfma_f32_16x16x32_bf16 v[88:91], a[64:67], a[60:63], v[88:91]\n" \
"v_mfma_f32_16x16x32_bf16 v[92:95], a[68:71], a[60:63], v[92:95]\n" \
"s_waitcnt lgkmcnt(0)\n" \
"v_mfma_f32_16x16x32_bf16 v[96:99], a[24:27], a[0:3], v[96:99]\n" \
"s_add_u32 m0, s50, 57344\n" \
"ds_read_b64_tr_b16 a[72:73], v9 offset:57856\n" \
"s_bfe_u32 s89, %1, 0x20001\n" \
"v_mfma_f32_16x16x32_bf16 v[100:103], a[28:31], a[0:3], v[100:103]\n" \
"buffer_load_dwordx4 v19, s[28:31], s46 offen lds\n" \
"v_mfma_f32_16x16x32_bf16 v[104:107], a[24:27], a[4:7], v[104:107]\n" \
"s_and_b32 s90, %1, 1\n" \
"ds_read_b64_tr_b16 a[74:75], v10 offset:57856\n" \
"s_lshl_b32 s90, s90, 2\n" \
"v_mfma_f32_16x16x32_bf16 v[108:111], a[28:31], a[4:7], v[108:111]\n" \
"s_bfe_u32 s92, %1, 0x20003\n" \
"ds_read_b64_tr_b16 a[76:77], v11 offset:57856\n" \
"s_add_u32 s90, s90, s92\n" \
"v_mfma_f32_16x16x32_bf16 v[112:115], a[24:27], a[8:11], v[112:115]\n" \
"s_lshl_b32 s92, %1, 2\n" \
"ds_read_b64_tr_b16 a[78:79], v12 offset:57856\n" \
"s_add_u32 s92, s92, %2\n" \
"v_mfma_f32_16x16x32_bf16 v[116:119], a[28:31], a[8:11], v[116:119]\n" \
"s_add_u32 m0, s50, 61440\n" \
"ds_read_b128 a[48:51], v8 offset:0\n" \
"s_mul_i32 s93, s92, 12288\n" \
"v_mfma_f32_16x16x32_bf16 v[120:123], a[24:27], a[12:15], v[120:123]\n" \
"buffer_load_dwordx4 v20, s[28:31], s46 offen lds\n" \
"v_mfma_f32_16x16x32_bf16 v[124:127], a[28:31], a[12:15], v[124:127]\n" \
"s_add_u32 s78, s93, 109051904\n" \
"ds_read_b128 a[52:55], v8 offset:2048\n" \
"s_lshl_b32 s88, s92, 3\n" \
"v_mfma_f32_16x16x32_bf16 v[128:131], a[32:35], a[0:3], v[128:131]\n" \
"s_mov_b64 exec, 0xffff\nglobal_load_dword v1, v2, s[100:101]\ns_mov_b64 exec, -1\n" \
"v_mfma_f32_16x16x32_bf16 v[132:135], a[36:39], a[0:3], v[132:135]\n" \
"s_add_u32 s88, s88, 94404608\n" \
"ds_read_b128 a[56:59], v8 offset:4096\n" \
"v_mfma_f32_16x16x32_bf16 v[136:139], a[32:35], a[4:7], v[136:139]\n" \
"s_mov_b32 s47, 6291456\n" \
"s_add_u32 m0, s51, 32768\n" \
"ds_read_b128 a[60:63], v8 offset:6144\n" \
"v_mfma_f32_16x16x32_bf16 v[140:143], a[36:39], a[4:7], v[140:143]\n" \
"buffer_load_dwordx4 v21, s[32:35], s47 offen offset:0 lds\n" \
"v_mfma_f32_16x16x32_bf16 v[144:147], a[32:35], a[8:11], v[144:147]\n" \
"s_cmp_eq_u32 s92, 0\ns_cselect_b32 s12, 94371840, -1\n" \
"ds_read_b64_tr_b16 a[80:81], v9 offset:58368\n" \
"v_mfma_f32_16x16x32_bf16 v[148:151], a[36:39], a[8:11], v[148:151]\n" \
"s_mul_i32 s80, s90, 12\n" \
"ds_read_b64_tr_b16 a[82:83], v10 offset:58368\n" \
"s_add_u32 s80, s80, s89\n" \
"v_mfma_f32_16x16x32_bf16 v[152:155], a[32:35], a[12:15], v[152:155]\n" \
"s_lshl_b32 s80, s80, 16\n" \
"ds_read_b64_tr_b16 a[84:85], v11 offset:58368\n" \
"s_add_u32 s92, %2, 0\n" \
"v_mfma_f32_16x16x32_bf16 v[156:159], a[36:39], a[12:15], v[156:159]\n" \
"s_add_u32 m0, s51, 33664\n" \
"ds_read_b64_tr_b16 a[86:87], v12 offset:58368\n" \
"s_mul_i32 s93, s92, 43\n" \
"v_mfma_f32_16x16x32_bf16 v[160:163], a[40:43], a[0:3], v[160:163]\n" \
"buffer_load_dwordx4 v21, s[32:35], s47 offen offset:128 lds\n" \
"v_mfma_f32_16x16x32_bf16 v[164:167], a[44:47], a[0:3], v[164:167]\n" \
"s_lshr_b32 s93, s93, 7\n" \
"ds_read_b64_tr_b16 a[88:89], v9 offset:58880\n" \
"s_lshl_b32 s93, s93, 12\n" \
"v_mfma_f32_16x16x32_bf16 v[168:171], a[40:43], a[4:7], v[168:171]\n" \
"s_add_u32 s64, s80, s93\n" \
"ds_read_b64_tr_b16 a[90:91], v10 offset:58880\n" \
"s_add_u32 s92, s92, s89\n" \
"v_mfma_f32_16x16x32_bf16 v[172:175], a[44:47], a[4:7], v[172:175]\n" \
"s_add_u32 s92, s92, 1\n" \
"ds_read_b64_tr_b16 a[92:93], v11 offset:58880\n" \
"s_mul_i32 s93, s92, 43\n" \
"v_mfma_f32_16x16x32_bf16 v[176:179], a[40:43], a[8:11], v[176:179]\n" \
"s_mov_b32 s47, 6553600\n" \
"s_add_u32 m0, s51, 40960\n" \
"ds_read_b64_tr_b16 a[94:95], v12 offset:58880\n" \
"v_mfma_f32_16x16x32_bf16 v[180:183], a[44:47], a[8:11], v[180:183]\n" \
"buffer_load_dwordx4 v21, s[32:35], s47 offen offset:0 lds\n" \
"v_mfma_f32_16x16x32_bf16 v[184:187], a[40:43], a[12:15], v[184:187]\n" \
"s_lshr_b32 s93, s93, 7\n" \
"ds_read_b64_tr_b16 a[64:65], v9 offset:57344\n" \
"s_mul_i32 s93, s93, 3\n" \
"v_mfma_f32_16x16x32_bf16 v[188:191], a[44:47], a[12:15], v[188:191]\n" \
"s_sub_u32 s93, s92, s93\n" \
"s_lshl_b32 s93, s93, 18\n" \
"s_add_u32 s64, s64, s93\n" \
"v_mfma_f32_16x16x32_bf16 v[64:67], a[16:19], a[0:3], v[64:67]\n" \
"s_add_u32 s92, %2, 4\n" \
"ds_read_b64_tr_b16 a[66:67], v10 offset:57344\n" \
"s_mul_i32 s93, s92, 43\n" \
"v_mfma_f32_16x16x32_bf16 v[68:71], a[20:23], a[0:3], v[68:71]\n" \
"s_add_u32 m0, s51, 41856\n" \
"ds_read_b64_tr_b16 a[68:69], v11 offset:57344\n" \
"s_lshr_b32 s93, s93, 7\n" \
"v_mfma_f32_16x16x32_bf16 v[72:75], a[16:19], a[4:7], v[72:75]\n" \
"buffer_load_dwordx4 v21, s[32:35], s47 offen offset:128 lds\n" \
"v_mfma_f32_16x16x32_bf16 v[76:79], a[20:23], a[4:7], v[76:79]\n" \
"s_lshl_b32 s93, s93, 12\n" \
"ds_read_b64_tr_b16 a[70:71], v12 offset:57344\n" \
"s_add_u32 s65, s80, s93\n" \
"v_mfma_f32_16x16x32_bf16 v[80:83], a[16:19], a[8:11], v[80:83]\n" \
"s_add_u32 s92, s92, s89\n" \
"s_add_u32 s92, s92, 1\n" \
"s_mul_i32 s93, s92, 43\n" \
"v_mfma_f32_16x16x32_bf16 v[84:87], a[20:23], a[8:11], v[84:87]\n" \
"s_lshr_b32 s93, s93, 7\n" \
"s_mul_i32 s93, s93, 3\n" \
"s_sub_u32 s93, s92, s93\n" \
"v_mfma_f32_16x16x32_bf16 v[88:91], a[16:19], a[12:15], v[88:91]\n" \
"s_waitcnt vmcnt(17) lgkmcnt(0)\n" \
"s_lshl_b32 s93, s93, 18\n" \
"s_add_u32 s65, s65, s93\n" \
"v_mfma_f32_16x16x32_bf16 v[92:95], a[20:23], a[12:15], v[92:95]\n" \
"s_barrier\n" \
"s_add_u32 s92, %2, 8\n" \
"s_mul_i32 s93, s92, 43\n" \
"v_mfma_f32_16x16x32_bf16 v[96:99], a[72:75], a[48:51], v[96:99]\n" \
"s_mov_b32 s46, 1536\n" \
"s_add_u32 m0, s50, 65536\n" \
"ds_read_b64_tr_b16 a[24:25], v9 offset:512\n" \
"v_mfma_f32_16x16x32_bf16 v[100:103], a[76:79], a[48:51], v[100:103]\n" \
"buffer_load_dwordx4 v13, s[28:31], s46 offen lds\n" \
"v_mfma_f32_16x16x32_bf16 v[104:107], a[72:75], a[52:55], v[104:107]\n" \
"s_lshr_b32 s93, s93, 7\n" \
"ds_read_b64_tr_b16 a[26:27], v10 offset:512\n" \
"v_mov_b32 v58, s14\n" \
"v_mfma_f32_16x16x32_bf16 v[108:111], a[76:79], a[52:55], v[108:111]\n" \
"s_lshl_b32 s93, s93, 12\n" \
"ds_read_b64_tr_b16 a[28:29], v11 offset:512\n" \
"v_mov_b32 v59, s15\n" \
"v_mfma_f32_16x16x32_bf16 v[112:115], a[72:75], a[56:59], v[112:115]\n" \
"s_add_u32 s66, s80, s93\n" \
"ds_read_b64_tr_b16 a[30:31], v12 offset:512\n" \
"s_add_u32 s92, s92, s89\n" \
"v_mfma_f32_16x16x32_bf16 v[116:119], a[76:79], a[56:59], v[116:119]\n" \
"s_add_u32 m0, s50, 69632\n" \
"ds_read_b128 a[0:3], v5 offset:0\n" \
"s_add_u32 s92, s92, 1\n" \
"v_mfma_f32_16x16x32_bf16 v[120:123], a[72:75], a[60:63], v[120:123]\n" \
"buffer_load_dwordx4 v14, s[28:31], s46 offen lds\n" \
"v_mfma_f32_16x16x32_bf16 v[124:127], a[76:79], a[60:63], v[124:127]\n" \
"s_mul_i32 s93, s92, 43\n" \
"ds_read_b128 a[4:7], v5 offset:2048\n" \
"s_lshr_b32 s93, s93, 7\n" \
"v_mfma_f32_16x16x32_bf16 v[128:131], a[80:83], a[48:51], v[128:131]\n" \
"s_mul_i32 s93, s93, 3\n" \
"s_sub_u32 s93, s92, s93\n" \
"s_lshl_b32 s93, s93, 18\n" \
"v_mfma_f32_16x16x32_bf16 v[132:135], a[84:87], a[48:51], v[132:135]\n" \
"s_add_u32 s66, s66, s93\n" \
"ds_read_b128 a[8:11], v5 offset:4096\n" \
"s_and_b32 s92, %2, 1\n" \
"v_mfma_f32_16x16x32_bf16 v[136:139], a[80:83], a[52:55], v[136:139]\n" \
"s_add_u32 m0, s50, 73728\n" \
"ds_read_b128 a[12:15], v5 offset:6144\n" \
"s_lshl_b32 s92, s92, 12\n" \
"v_mfma_f32_16x16x32_bf16 v[140:143], a[84:87], a[52:55], v[140:143]\n" \
"buffer_load_dwordx4 v15, s[28:31], s46 offen lds\n" \
"v_mfma_f32_16x16x32_bf16 v[144:147], a[80:83], a[56:59], v[144:147]\n" \
"s_sub_u32 s57, s66, s64\n" \
"ds_read_b64_tr_b16 a[32:33], v9 offset:1024\n" \
"s_sub_u32 s56, s64, s57\n" \
"v_mfma_f32_16x16x32_bf16 v[148:151], a[84:87], a[56:59], v[148:151]\n" \
"s_sub_u32 s56, s56, s92\n" \
"ds_read_b64_tr_b16 a[34:35], v10 offset:1024\n" \
"s_sub_u32 s59, s64, s65\n" \
"v_mfma_f32_16x16x32_bf16 v[152:155], a[80:83], a[60:63], v[152:155]\n" \
"s_add_u32 s59, s59, 16384\n" \
"ds_read_b64_tr_b16 a[36:37], v11 offset:1024\n" \
"s_sub_u32 s58, s65, s59\n" \
"v_mfma_f32_16x16x32_bf16 v[156:159], a[84:87], a[60:63], v[156:159]\n" \
"s_add_u32 m0, s50, 77824\n" \
"ds_read_b64_tr_b16 a[38:39], v12 offset:1024\n" \
"s_sub_u32 s58, s58, s92\n" \
"v_mfma_f32_16x16x32_bf16 v[160:163], a[88:91], a[48:51], v[160:163]\n" \
"buffer_load_dwordx4 v16, s[28:31], s46 offen lds\n" \
"v_mfma_f32_16x16x32_bf16 v[164:167], a[92:95], a[48:51], v[164:167]\n" \
"s_lshl_b32 s92, s90, 18\n" \
"ds_read_b64_tr_b16 a[40:41], v9 offset:1536\n" \
"s_lshl_b32 s93, s89, 16\n" \
"v_mfma_f32_16x16x32_bf16 v[168:171], a[88:91], a[52:55], v[168:171]\n" \
"LH12_%=:\ns_cbranch_execz LHR0_%=\n" \
"s_add_u32 s67, s92, s93\n" \
"ds_read_b64_tr_b16 a[42:43], v10 offset:1536\n" \
"s_add_u32 s67, s67, 6291456\n" \
"v_mfma_f32_16x16x32_bf16 v[172:175], a[92:95], a[52:55], v[172:175]\n" \
"s_lshl_b32 s92, s90, 20\n" \
"ds_read_b64_tr_b16 a[44:45], v11 offset:1536\n" \
"s_add_u32 s94, s92, s93\n" \
"v_mfma_f32_16x16x32_bf16 v[176:179], a[88:91], a[56:59], v[176:179]\n" \
"s_mov_b32 s46, 1536\n" \
"s_add_u32 m0, s50, 81920\n" \
"ds_read_b64_tr_b16 a[46:47], v12 offset:1536\n" \
"v_mfma_f32_16x16x32_bf16 v[180:183], a[92:95], a[56:59], v[180:183]\n" \
"buffer_load_dwordx4 v17, s[28:31], s46 offen lds\n" \
"v_mfma_f32_16x16x32_bf16 v[184:187], a[88:91], a[60:63], v[184:187]\n" \
"s_add_u32 s94, s94, 8388608\n" \
"ds_read_b64_tr_b16 a[16:17], v9 offset:0\n" \
"s_add_u32 s95, s89, 1\n" \
"v_mfma_f32_16x16x32_bf16 v[188:191], a[92:95], a[60:63], v[188:191]\n" \
"buffer_store_dwordx2 v[58:59], v56, s[36:39], s24 offen sc1\n" \
"v_mfma_f32_16x16x32_bf16 v[64:67], a[64:67], a[48:51], v[64:67]\n" \
"s_and_b32 s95, s95, 3\n" \
"ds_read_b64_tr_b16 a[18:19], v10 offset:0\n" \
"s_lshl_b32 s92, s95, 18\n" \
"v_mfma_f32_16x16x32_bf16 v[68:71], a[68:71], a[48:51], v[68:71]\n" \
"s_add_u32 m0, s50, 86016\n" \
"ds_read_b64_tr_b16 a[20:21], v11 offset:0\n" \
"s_add_u32 s68, s94, s92\n" \
"v_mfma_f32_16x16x32_bf16 v[72:75], a[64:67], a[52:55], v[72:75]\n" \
"buffer_load_dwordx4 v18, s[28:31], s46 offen lds\n" \
"v_mfma_f32_16x16x32_bf16 v[76:79], a[68:71], a[52:55], v[76:79]\n" \
"s_sub_u32 s92, s95, s89\n" \
"ds_read_b64_tr_b16 a[22:23], v12 offset:0\n" \
"s_mul_i32 s93, s92, 98304\n" \
"v_mfma_f32_16x16x32_bf16 v[80:83], a[64:67], a[56:59], v[80:83]\n" \
"s_add_u32 s75, s78, s93\n" \
"s_add_u32 s75, s75, 6144\n" \
"s_add_u32 s95, s89, 2\n" \
"v_mfma_f32_16x16x32_bf16 v[84:87], a[68:71], a[56:59], v[84:87]\n" \
"s_and_b32 s95, s95, 3\n" \
"s_lshl_b32 s92, s95, 18\n" \
"s_add_u32 s69, s94, s92\n" \
"v_mfma_f32_16x16x32_bf16 v[88:91], a[64:67], a[60:63], v[88:91]\n" \
"s_sub_u32 s92, s95, s89\n" \
"s_mul_i32 s93, s92, 98304\n" \
"s_add_u32 s76, s78, s93\n" \
"v_mfma_f32_16x16x32_bf16 v[92:95], a[68:71], a[60:63], v[92:95]\n" \
"s_waitcnt lgkmcnt(0)\n" \
"s_add_u32 s76, s76, 8192\n" \
"s_add_u32 s95, s89, 3\n" \
"v_mfma_f32_16x16x32_bf16 v[96:99], a[24:27], a[0:3], v[96:99]\n" \
"s_add_u32 m0, s50, 90112\n" \
"ds_read_b64_tr_b16 a[72:73], v9 offset:8704\n" \
"s_and_b32 s95, s95, 3\n" \
"v_mfma_f32_16x16x32_bf16 v[100:103], a[28:31], a[0:3], v[100:103]\n" \
"buffer_load_dwordx4 v19, s[28:31], s46 offen lds\n" \
"v_mfma_f32_16x16x32_bf16 v[104:107], a[24:27], a[4:7], v[104:107]\n" \
"s_lshl_b32 s92, s95, 18\n" \
"ds_read_b64_tr_b16 a[74:75], v10 offset:8704\n" \
"s_add_u32 s70, s94, s92\n" \
"v_mfma_f32_16x16x32_bf16 v[108:111], a[28:31], a[4:7], v[108:111]\n" \
"s_sub_u32 s92, s95, s89\n" \
"ds_read_b64_tr_b16 a[76:77], v11 offset:8704\n" \
"s_mul_i32 s93, s92, 98304\n" \
"v_mfma_f32_16x16x32_bf16 v[112:115], a[24:27], a[8:11], v[112:115]\n" \
"s_add_u32 s77, s78, s93\n" \
"ds_read_b64_tr_b16 a[78:79], v12 offset:8704\n" \
"s_add_u32 s77, s77, 10240\n" \
"v_mfma_f32_16x16x32_bf16 v[116:119], a[28:31], a[8:11], v[116:119]\n" \
"s_add_u32 m0, s50, 94208\n" \
"ds_read_b128 a[48:51], v6 offset:0\n" \
"s_add_u32 s95, s89, 4\n" \
"v_mfma_f32_16x16x32_bf16 v[120:123], a[24:27], a[12:15], v[120:123]\n" \
"buffer_load_dwordx4 v20, s[28:31], s46 offen lds\n" \
"v_mfma_f32_16x16x32_bf16 v[124:127], a[28:31], a[12:15], v[124:127]\n" \
"s_and_b32 s95, s95, 3\n" \
"ds_read_b128 a[52:55], v6 offset:2048\n" \
"s_lshl_b32 s92, s95, 18\n" \
"v_mfma_f32_16x16x32_bf16 v[128:131], a[32:35], a[0:3], v[128:131]\n" \
"s_add_u32 s71, s94, s92\n" \
"s_add_u32 s92, s89, 1\n" \
"s_mul_i32 s95, s92, 43\n" \
"v_mfma_f32_16x16x32_bf16 v[132:135], a[36:39], a[0:3], v[132:135]\n" \
"s_lshr_b32 s95, s95, 7\n" \
"ds_read_b128 a[56:59], v6 offset:4096\n" \
"s_mul_i32 s95, s95, 3\n" \
"v_mfma_f32_16x16x32_bf16 v[136:139], a[32:35], a[4:7], v[136:139]\n" \
"s_mov_b32 s47, 6815744\n" \
"s_add_u32 m0, s51, 49152\n" \
"ds_read_b128 a[60:63], v6 offset:6144\n" \
"v_mfma_f32_16x16x32_bf16 v[140:143], a[36:39], a[4:7], v[140:143]\n" \
"buffer_load_dwordx4 v21, s[32:35], s47 offen offset:0 lds\n" \
"v_mfma_f32_16x16x32_bf16 v[144:147], a[32:35], a[8:11], v[144:147]\n" \
"s_sub_u32 s95, s92, s95\n" \
"ds_read_b64_tr_b16 a[80:81], v9 offset:9216\n" \
"s_sub_u32 s92, s95, s89\n" \
"v_mfma_f32_16x16x32_bf16 v[148:151], a[36:39], a[8:11], v[148:151]\n" \
"s_mul_i32 s93, s92, 98304\n" \
"ds_read_b64_tr_b16 a[82:83], v10 offset:9216\n" \
"s_add_u32 s72, s78, s93\n" \
"v_mfma_f32_16x16x32_bf16 v[152:155], a[32:35], a[12:15], v[152:155]\n" \
"s_sub_u32 s92, s92, 1\n" \
"ds_read_b64_tr_b16 a[84:85], v11 offset:9216\n" \
"s_and_b32 s92, s92, 3\n" \
"v_mfma_f32_16x16x32_bf16 v[156:159], a[36:39], a[12:15], v[156:159]\n" \
"s_add_u32 m0, s51, 50048\n" \
"ds_read_b64_tr_b16 a[86:87], v12 offset:9216\n" \
"s_lshl_b32 s92, s92, 11\n" \
"v_mfma_f32_16x16x32_bf16 v[160:163], a[40:43], a[0:3], v[160:163]\n" \
"buffer_load_dwordx4 v21, s[32:35], s47 offen offset:128 lds\n" \
"v_mfma_f32_16x16x32_bf16 v[164:167], a[44:47], a[0:3], v[164:167]\n" \
"s_add_u32 s72, s72, s92\n" \
"ds_read_b64_tr_b16 a[88:89], v9 offset:9728\n" \
"s_add_u32 s92, s89, 2\n" \
"v_mfma_f32_16x16x32_bf16 v[168:171], a[40:43], a[4:7], v[168:171]\n" \
"s_mul_i32 s95, s92, 43\n" \
"ds_read_b64_tr_b16 a[90:91], v10 offset:9728\n" \
"s_lshr_b32 s95, s95, 7\n" \
"v_mfma_f32_16x16x32_bf16 v[172:175], a[44:47], a[4:7], v[172:175]\n" \
"s_mul_i32 s95, s95, 3\n" \
"ds_read_b64_tr_b16 a[92:93], v11 offset:9728\n" \
"s_sub_u32 s95, s92, s95\n" \
"v_mfma_f32_16x16x32_bf16 v[176:179], a[40:43], a[8:11], v[176:179]\n" \
"s_mov_b32 s47, 7077888\n" \
"s_add_u32 m0, s51, 57344\n" \
"ds_read_b64_tr_b16 a[94:95], v12 offset:9728\n" \
"v_mfma_f32_16x16x32_bf16 v[180:183], a[44:47], a[8:11], v[180:183]\n" \
"buffer_load_dwordx4 v21, s[32:35], s47 offen offset:0 lds\n" \
"v_mfma_f32_16x16x32_bf16 v[184:187], a[40:43], a[12:15], v[184:187]\n" \
"s_sub_u32 s92, s95, s89\n" \
"ds_read_b64_tr_b16 a[64:65], v9 offset:8192\n" \
"s_mul_i32 s93, s92, 98304\n" \
"v_mfma_f32_16x16x32_bf16 v[188:191], a[44:47], a[12:15], v[188:191]\n" \
"s_add_u32 s73, s78, s93\n" \
"s_sub_u32 s92, s92, 1\n" \
"s_and_b32 s92, s92, 3\n" \
"v_mfma_f32_16x16x32_bf16 v[64:67], a[16:19], a[0:3], v[64:67]\n" \
"s_lshl_b32 s92, s92, 11\n" \
"ds_read_b64_tr_b16 a[66:67], v10 offset:8192\n" \
"s_add_u32 s73, s73, s92\n" \
"v_mfma_f32_16x16x32_bf16 v[68:71], a[20:23], a[0:3], v[68:71]\n" \
"s_add_u32 m0, s51, 58240\n" \
"ds_read_b64_tr_b16 a[68:69], v11 offset:8192\n" \
"v_mfma_f32_16x16x32_bf16 v[72:75], a[16:19], a[4:7], v[72:75]\n" \
"buffer_load_dwordx4 v21, s[32:35], s47 offen offset:128 lds\n" \
"v_mfma_f32_16x16x32_bf16 v[76:79], a[20:23], a[4:7], v[76:79]\n" \
"s_cmp_lt_u32 s89, 3\ns_cselect_b32 s95, 3, 0\n" \
"ds_read_b64_tr_b16 a[70:71], v12 offset:8192\n" \
"v_mfma_f32_16x16x32_bf16 v[80:83], a[16:19], a[8:11], v[80:83]\n" \
"s_sub_u32 s92, s95, s89\n" \
"s_mul_i32 s93, s92, 98304\n" \
"s_add_u32 s74, s78, s93\n" \
"v_mfma_f32_16x16x32_bf16 v[84:87], a[20:23], a[8:11], v[84:87]\n" \
"s_sub_u32 s92, s92, 1\n" \
"s_and_b32 s92, s92, 3\n" \
"s_lshl_b32 s92, s92, 11\n" \
"v_mfma_f32_16x16x32_bf16 v[88:91], a[16:19], a[12:15], v[88:91]\n" \
"s_waitcnt vmcnt(18) lgkmcnt(0)\n" \
"s_add_u32 s74, s74, s92\n" \
"s_mul_i32 s96, s90, 3\n" \
"v_mfma_f32_16x16x32_bf16 v[92:95], a[20:23], a[12:15], v[92:95]\n" \
"s_barrier\n" \
"s_add_u32 s96, s96, s89\n" \
"s_add_u32 s92, s90, 24\n" \
"v_mfma_f32_16x16x32_bf16 v[96:99], a[72:75], a[48:51], v[96:99]\n" \
"s_mov_b32 s46, 1664\n" \
"s_add_u32 m0, s50, 0\n" \
"ds_read_b64_tr_b16 a[24:25], v9 offset:16896\n" \
"v_mfma_f32_16x16x32_bf16 v[100:103], a[76:79], a[48:51], v[100:103]\n" \
"buffer_load_dwordx4 v13, s[28:31], s46 offen lds\n" \
"v_mfma_f32_16x16x32_bf16 v[104:107], a[72:75], a[52:55], v[104:107]\n" \
"s_cmp_lt_u32 s89, 3\ns_cselect_b32 s96, s96, s92\n" \
"ds_read_b64_tr_b16 a[26:27], v10 offset:16896\n" \
"v_mfma_f32_16x16x32_bf16 v[108:111], a[76:79], a[52:55], v[108:111]\n" \
"s_lshl_b32 s96, s96, 5\n" \
"ds_read_b64_tr_b16 a[28:29], v11 offset:16896\n" \
"s_lshl_b32 s97, s90, 2\n" \
"v_mfma_f32_16x16x32_bf16 v[112:115], a[72:75], a[56:59], v[112:115]\n" \
"s_add_u32 s97, s97, s89\n" \
"ds_read_b64_tr_b16 a[30:31], v12 offset:16896\n" \
"s_add_u32 s97, s97, 32\n" \
"v_mfma_f32_16x16x32_bf16 v[116:119], a[76:79], a[56:59], v[116:119]\n" \
"s_add_u32 m0, s50, 4096\n" \
"ds_read_b128 a[0:3], v5 offset:32768\n" \
"s_lshl_b32 s97, s97, 5\n" \
"v_mfma_f32_16x16x32_bf16 v[120:123], a[72:75], a[60:63], v[120:123]\n" \
"buffer_load_dwordx4 v14, s[28:31], s46 offen lds\n" \
"v_mfma_f32_16x16x32_bf16 v[124:127], a[76:79], a[60:63], v[124:127]\n" \
"s_lshr_b32 s92, %1, 5\n" \
"ds_read_b128 a[4:7], v5 offset:34816\n" \
"s_lshl_b32 s92, s92, 2\n" \
"v_mfma_f32_16x16x32_bf16 v[128:131], a[80:83], a[48:51], v[128:131]\n" \
"s_add_u32 s92, s92, %2\n" \
"s_lshl_b32 s93, s92, 6\n" \
"s_lshl_b32 s92, s92, 17\n" \
"v_mfma_f32_16x16x32_bf16 v[132:135], a[84:87], a[48:51], v[132:135]\n" \
"s_add_u32 s92, s92, 67108864\n" \
"ds_read_b128 a[8:11], v5 offset:36864\n" \
"s_lshl_b32 s82, s96, 11\n" \
"v_mfma_f32_16x16x32_bf16 v[136:139], a[80:83], a[52:55], v[136:139]\n" \
"s_add_u32 m0, s50, 8192\n" \
"ds_read_b128 a[12:15], v5 offset:38912\n" \
"s_add_u32 s82, s82, s93\n" \
"v_mfma_f32_16x16x32_bf16 v[140:143], a[84:87], a[52:55], v[140:143]\n" \
"buffer_load_dwordx4 v15, s[28:31], s46 offen lds\n" \
"v_mfma_f32_16x16x32_bf16 v[144:147], a[80:83], a[56:59], v[144:147]\n" \
"s_lshl_b32 s86, s96, 2\n" \
"ds_read_b64_tr_b16 a[32:33], v9 offset:17408\n" \
"s_add_u32 s86, s86, 71303168\n" \
"v_mfma_f32_16x16x32_bf16 v[148:151], a[84:87], a[56:59], v[148:151]\n" \
"s_lshl_b32 s84, s96, 3\n" \
"ds_read_b64_tr_b16 a[34:35], v10 offset:17408\n" \
"s_add_u32 s84, s84, s92\n" \
"v_mfma_f32_16x16x32_bf16 v[152:155], a[80:83], a[60:63], v[152:155]\n" \
"s_lshl_b32 s83, s97, 11\n" \
"ds_read_b64_tr_b16 a[36:37], v11 offset:17408\n" \
"s_add_u32 s83, s83, s93\n" \
"v_mfma_f32_16x16x32_bf16 v[156:159], a[84:87], a[60:63], v[156:159]\n" \
"s_add_u32 m0, s50, 12288\n" \
"ds_read_b64_tr_b16 a[38:39], v12 offset:17408\n" \
"s_lshl_b32 s87, s97, 2\n" \
"v_mfma_f32_16x16x32_bf16 v[160:163], a[88:91], a[48:51], v[160:163]\n" \
"buffer_load_dwordx4 v16, s[28:31], s46 offen lds\n" \
"v_mfma_f32_16x16x32_bf16 v[164:167], a[92:95], a[48:51], v[164:167]\n" \
"s_add_u32 s87, s87, 71303168\n" \
"ds_read_b64_tr_b16 a[40:41], v9 offset:17920\n" \
"s_lshl_b32 s85, s97, 3\n" \
"v_mfma_f32_16x16x32_bf16 v[168:171], a[88:91], a[52:55], v[168:171]\n" \
"s_add_u32 s85, s85, s92\n" \
"ds_read_b64_tr_b16 a[42:43], v10 offset:17920\n" \
"s_lshl_b32 s93, s89, 6\n" \
"v_mfma_f32_16x16x32_bf16 v[172:175], a[92:95], a[52:55], v[172:175]\n" \
"s_add_u32 s93, s93, 94388224\n" \
"ds_read_b64_tr_b16 a[44:45], v11 offset:17920\n" \
"s_lshl_b32 s92, s90, 8\n" \
"v_mfma_f32_16x16x32_bf16 v[176:179], a[88:91], a[56:59], v[176:179]\n" \
"s_mov_b32 s46, 1664\n" \
"s_add_u32 m0, s50, 16384\n" \
"ds_read_b64_tr_b16 a[46:47], v12 offset:17920\n" \
"v_mfma_f32_16x16x32_bf16 v[180:183], a[92:95], a[56:59], v[180:183]\n" \
"buffer_load_dwordx4 v17, s[28:31], s46 offen lds\n" \
"v_mfma_f32_16x16x32_bf16 v[184:187], a[88:91], a[60:63], v[184:187]\n" \
"s_add_u32 s10, s93, s92\n" \
"ds_read_b64_tr_b16 a[16:17], v9 offset:16384\n" \
"s_add_u32 s10, s10, 6144\n" \
"v_mfma_f32_16x16x32_bf16 v[188:191], a[92:95], a[60:63], v[188:191]\n" \
"s_lshl_b32 s92, s90, 10\n" \
"s_add_u32 s11, s93, s92\n" \
"s_add_u32 s11, s11, 8192\n" \
"v_mfma_f32_16x16x32_bf16 v[64:67], a[64:67], a[48:51], v[64:67]\n" \
"s_mul_i32 s92, s90, 768\n" \
"ds_read_b64_tr_b16 a[18:19], v10 offset:16384\n" \
"s_add_u32 s98, s93, s92\n" \
"v_mfma_f32_16x16x32_bf16 v[68:71], a[68:71], a[48:51], v[68:71]\n" \
"s_add_u32 m0, s50, 20480\n" \
"ds_read_b64_tr_b16 a[20:21], v11 offset:16384\n" \
"v_mfma_f32_16x16x32_bf16 v[72:75], a[64:67], a[52:55], v[72:75]\n" \
"buffer_load_dwordx4 v18, s[28:31], s46 offen lds\n" \
"v_mfma_f32_16x16x32_bf16 v[76:79], a[68:71], a[52:55], v[76:79]\n" \
"ds_read_b64_tr_b16 a[22:23], v12 offset:16384\n" \
"v_mfma_f32_16x16x32_bf16 v[80:83], a[64:67], a[56:59], v[80:83]\n" \
"v_mfma_f32_16x16x32_bf16 v[84:87], a[68:71], a[56:59], v[84:87]\n" \
"v_mfma_f32_16x16x32_bf16 v[88:91], a[64:67], a[60:63], v[88:91]\n" \
"v_mfma_f32_16x16x32_bf16 v[92:95], a[68:71], a[60:63], v[92:95]\n" \
"s_waitcnt lgkmcnt(0)\n" \
"v_mfma_f32_16x16x32_bf16 v[96:99], a[24:27], a[0:3], v[96:99]\n" \
"s_add_u32 m0, s50, 24576\n" \
"ds_read_b64_tr_b16 a[72:73], v9 offset:25088\n" \
"v_mfma_f32_16x16x32_bf16 v[100:103], a[28:31], a[0:3], v[100:103]\n" \
"buffer_load_dwordx4 v19, s[28:31], s46 offen lds\n" \
"v_mfma_f32_16x16x32_bf16 v[104:107], a[24:27], a[4:7], v[104:107]\n" \
"ds_read_b64_tr_b16 a[74:75], v10 offset:25088\n" \
"v_mfma_f32_16x16x32_bf16 v[108:111], a[28:31], a[4:7], v[108:111]\n" \
"ds_read_b64_tr_b16 a[76:77], v11 offset:25088\n" \
"v_mfma_f32_16x16x32_bf16 v[112:115], a[24:27], a[8:11], v[112:115]\n" \
"ds_read_b64_tr_b16 a[78:79], v12 offset:25088\n" \
"v_mfma_f32_16x16x32_bf16 v[116:119], a[28:31], a[8:11], v[116:119]\n" \
"s_add_u32 m0, s50, 28672\n" \
"ds_read_b128 a[48:51], v6 offset:32768\n" \
"v_mfma_f32_16x16x32_bf16 v[120:123], a[24:27], a[12:15], v[120:123]\n" \
"buffer_load_dwordx4 v20, s[28:31], s46 offen lds\n" \
"v_mfma_f32_16x16x32_bf16 v[124:127], a[28:31], a[12:15], v[124:127]\n" \
"ds_read_b128 a[52:55], v6 offset:34816\n" \
"v_mfma_f32_16x16x32_bf16 v[128:131], a[32:35], a[0:3], v[128:131]\n" \
"v_mfma_f32_16x16x32_bf16 v[132:135], a[36:39], a[0:3], v[132:135]\n" \
"ds_read_b128 a[56:59], v6 offset:36864\n" \
"v_mfma_f32_16x16x32_bf16 v[136:139], a[32:35], a[4:7], v[136:139]\n" \
"s_mov_b32 s47, 7340032\n" \
"s_add_u32 m0, s51, 0\n" \
"ds_read_b128 a[60:63], v6 offset:38912\n" \
"v_mfma_f32_16x16x32_bf16 v[140:143], a[36:39], a[4:7], v[140:143]\n" \
"buffer_load_dwordx4 v21, s[32:35], s47 offen offset:0 lds\n" \
"v_mfma_f32_16x16x32_bf16 v[144:147], a[32:35], a[8:11], v[144:147]\n" \
"ds_read_b64_tr_b16 a[80:81], v9 offset:25600\n" \
"v_mfma_f32_16x16x32_bf16 v[148:151], a[36:39], a[8:11], v[148:151]\n" \
"ds_read_b64_tr_b16 a[82:83], v10 offset:25600\n" \
"v_mfma_f32_16x16x32_bf16 v[152:155], a[32:35], a[12:15], v[152:155]\n" \
"ds_read_b64_tr_b16 a[84:85], v11 offset:25600\n" \
"v_mfma_f32_16x16x32_bf16 v[156:159], a[36:39], a[12:15], v[156:159]\n" \
"s_add_u32 m0, s51, 896\n" \
"ds_read_b64_tr_b16 a[86:87], v12 offset:25600\n" \
"v_mfma_f32_16x16x32_bf16 v[160:163], a[40:43], a[0:3], v[160:163]\n" \
"buffer_load_dwordx4 v21, s[32:35], s47 offen offset:128 lds\n" \
"v_mfma_f32_16x16x32_bf16 v[164:167], a[44:47], a[0:3], v[164:167]\n" \
"ds_read_b64_tr_b16 a[88:89], v9 offset:26112\n" \
"v_mfma_f32_16x16x32_bf16 v[168:171], a[40:43], a[4:7], v[168:171]\n" \
"ds_read_b64_tr_b16 a[90:91], v10 offset:26112\n" \
"v_mfma_f32_16x16x32_bf16 v[172:175], a[44:47], a[4:7], v[172:175]\n" \
"ds_read_b64_tr_b16 a[92:93], v11 offset:26112\n" \
"v_mfma_f32_16x16x32_bf16 v[176:179], a[40:43], a[8:11], v[176:179]\n" \
"s_mov_b32 s47, 7602176\n" \
"s_add_u32 m0, s51, 8192\n" \
"ds_read_b64_tr_b16 a[94:95], v12 offset:26112\n" \
"v_mfma_f32_16x16x32_bf16 v[180:183], a[44:47], a[8:11], v[180:183]\n" \
"buffer_load_dwordx4 v21, s[32:35], s47 offen offset:0 lds\n" \
"v_mfma_f32_16x16x32_bf16 v[184:187], a[40:43], a[12:15], v[184:187]\n" \
"ds_read_b64_tr_b16 a[64:65], v9 offset:24576\n" \
"v_mfma_f32_16x16x32_bf16 v[188:191], a[44:47], a[12:15], v[188:191]\n" \
"v_mfma_f32_16x16x32_bf16 v[64:67], a[16:19], a[0:3], v[64:67]\n" \
"ds_read_b64_tr_b16 a[66:67], v10 offset:24576\n" \
"v_mfma_f32_16x16x32_bf16 v[68:71], a[20:23], a[0:3], v[68:71]\n" \
"s_add_u32 m0, s51, 9088\n" \
"ds_read_b64_tr_b16 a[68:69], v11 offset:24576\n" \
"v_mfma_f32_16x16x32_bf16 v[72:75], a[16:19], a[4:7], v[72:75]\n" \
"buffer_load_dwordx4 v21, s[32:35], s47 offen offset:128 lds\n" \
"v_mfma_f32_16x16x32_bf16 v[76:79], a[20:23], a[4:7], v[76:79]\n" \
"ds_read_b64_tr_b16 a[70:71], v12 offset:24576\n" \
"v_mfma_f32_16x16x32_bf16 v[80:83], a[16:19], a[8:11], v[80:83]\n" \
"v_mfma_f32_16x16x32_bf16 v[84:87], a[20:23], a[8:11], v[84:87]\n" \
"v_mfma_f32_16x16x32_bf16 v[88:91], a[16:19], a[12:15], v[88:91]\n" \
"s_waitcnt vmcnt(16) lgkmcnt(0)\n" \
"v_mfma_f32_16x16x32_bf16 v[92:95], a[20:23], a[12:15], v[92:95]\n" \
"s_barrier\n" \
"v_mfma_f32_16x16x32_bf16 v[96:99], a[72:75], a[48:51], v[96:99]\n" \
"s_mov_b32 s46, 1792\n" \
"s_add_u32 m0, s50, 32768\n" \
"ds_read_b64_tr_b16 a[24:25], v9 offset:33280\n" \
"v_mfma_f32_16x16x32_bf16 v[100:103], a[76:79], a[48:51], v[100:103]\n" \
"buffer_load_dwordx4 v13, s[28:31], s46 offen lds\n" \
"v_mfma_f32_16x16x32_bf16 v[104:107], a[72:75], a[52:55], v[104:107]\n" \
"ds_read_b64_tr_b16 a[26:27], v10 offset:33280\n" \
"v_mfma_f32_16x16x32_bf16 v[108:111], a[76:79], a[52:55], v[108:111]\n" \
"ds_read_b64_tr_b16 a[28:29], v11 offset:33280\n" \
"v_mfma_f32_16x16x32_bf16 v[112:115], a[72:75], a[56:59], v[112:115]\n" \
"ds_read_b64_tr_b16 a[30:31], v12 offset:33280\n" \
"v_mfma_f32_16x16x32_bf16 v[116:119], a[76:79], a[56:59], v[116:119]\n" \
"s_add_u32 m0, s50, 36864\n" \
"ds_read_b128 a[0:3], v7 offset:0\n" \
"v_mfma_f32_16x16x32_bf16 v[120:123], a[72:75], a[60:63], v[120:123]\n" \
"buffer_load_dwordx4 v14, s[28:31], s46 offen lds\n" \
"v_mfma_f32_16x16x32_bf16 v[124:127], a[76:79], a[60:63], v[124:127]\n" \
"ds_read_b128 a[4:7], v7 offset:2048\n" \
"v_mfma_f32_16x16x32_bf16 v[128:131], a[80:83], a[48:51], v[128:131]\n" \
"v_mfma_f32_16x16x32_bf16 v[132:135], a[84:87], a[48:51], v[132:135]\n" \
"ds_read_b128 a[8:11], v7 offset:4096\n" \
"v_mfma_f32_16x16x32_bf16 v[136:139], a[80:83], a[52:55], v[136:139]\n" \
"s_add_u32 m0, s50, 40960\n" \
"ds_read_b128 a[12:15], v7 offset:6144\n" \
"v_mfma_f32_16x16x32_bf16 v[140:143], a[84:87], a[52:55], v[140:143]\n" \
"buffer_load_dwordx4 v15, s[28:31], s46 offen lds\n" \
"v_mfma_f32_16x16x32_bf16 v[144:147], a[80:83], a[56:59], v[144:147]\n" \
"ds_read_b64_tr_b16 a[32:33], v9 offset:33792\n" \
"v_mfma_f32_16x16x32_bf16 v[148:151], a[84:87], a[56:59], v[148:151]\n" \
"ds_read_b64_tr_b16 a[34:35], v10 offset:33792\n" \
"v_mfma_f32_16x16x32_bf16 v[152:155], a[80:83], a[60:63], v[152:155]\n" \
"ds_read_b64_tr_b16 a[36:37], v11 offset:33792\n" \
"v_mfma_f32_16x16x32_bf16 v[156:159], a[84:87], a[60:63], v[156:159]\n" \
"s_add_u32 m0, s50, 45056\n" \
"ds_read_b64_tr_b16 a[38:39], v12 offset:33792\n" \
"v_mfma_f32_16x16x32_bf16 v[160:163], a[88:91], a[48:51], v[160:163]\n" \
"buffer_load_dwordx4 v16, s[28:31], s46 offen lds\n" \
"v_mfma_f32_16x16x32_bf16 v[164:167], a[92:95], a[48:51], v[164:167]\n" \
"ds_read_b64_tr_b16 a[40:41], v9 offset:34304\n" \
"v_mfma_f32_16x16x32_bf16 v[168:171], a[88:91], a[52:55], v[168:171]\n" \
"ds_read_b64_tr_b16 a[42:43], v10 offset:34304\n" \
"v_mfma_f32_16x16x32_bf16 v[172:175], a[92:95], a[52:55], v[172:175]\n" \
"ds_read_b64_tr_b16 a[44:45], v11 offset:34304\n" \
"v_mfma_f32_16x16x32_bf16 v[176:179], a[88:91], a[56:59], v[176:179]\n" \
"s_mov_b32 s46, 1792\n" \
"s_add_u32 m0, s50, 49152\n" \
"ds_read_b64_tr_b16 a[46:47], v12 offset:34304\n" \
"v_mfma_f32_16x16x32_bf16 v[180:183], a[92:95], a[56:59], v[180:183]\n" \
"buffer_load_dwordx4 v17, s[28:31], s46 offen lds\n" \
"v_mfma_f32_16x16x32_bf16 v[184:187], a[88:91], a[60:63], v[184:187]\n" \
"ds_read_b64_tr_b16 a[16:17], v9 offset:32768\n" \
"v_mfma_f32_16x16x32_bf16 v[188:191], a[92:95], a[60:63], v[188:191]\n" \
"v_mfma_f32_16x16x32_bf16 v[64:67], a[64:67], a[48:51], v[64:67]\n" \
"ds_read_b64_tr_b16 a[18:19], v10 offset:32768\n" \
"v_mfma_f32_16x16x32_bf16 v[68:71], a[68:71], a[48:51], v[68:71]\n" \
"s_add_u32 m0, s50, 53248\n" \
"ds_read_b64_tr_b16 a[20:21], v11 offset:32768\n" \
"v_mfma_f32_16x16x32_bf16 v[72:75], a[64:67], a[52:55], v[72:75]\n" \
"buffer_load_dwordx4 v18, s[28:31], s46 offen lds\n" \
"v_mfma_f32_16x16x32_bf16 v[76:79], a[68:71], a[52:55], v[76:79]\n" \
"ds_read_b64_tr_b16 a[22:23], v12 offset:32768\n" \
"v_mfma_f32_16x16x32_bf16 v[80:83], a[64:67], a[56:59], v[80:83]\n" \
"v_mfma_f32_16x16x32_bf16 v[84:87], a[68:71], a[56:59], v[84:87]\n" \
"v_mfma_f32_16x16x32_bf16 v[88:91], a[64:67], a[60:63], v[88:91]\n" \
"v_mfma_f32_16x16x32_bf16 v[92:95], a[68:71], a[60:63], v[92:95]\n" \
"s_waitcnt lgkmcnt(0)\n" \
"v_mfma_f32_16x16x32_bf16 v[96:99], a[24:27], a[0:3], v[96:99]\n" \
"s_add_u32 m0, s50, 57344\n" \
"ds_read_b64_tr_b16 a[72:73], v9 offset:41472\n" \
"v_mfma_f32_16x16x32_bf16 v[100:103], a[28:31], a[0:3], v[100:103]\n" \
"buffer_load_dwordx4 v19, s[28:31], s46 offen lds\n" \
"v_mfma_f32_16x16x32_bf16 v[104:107], a[24:27], a[4:7], v[104:107]\n" \
"ds_read_b64_tr_b16 a[74:75], v10 offset:41472\n" \
"v_mfma_f32_16x16x32_bf16 v[108:111], a[28:31], a[4:7], v[108:111]\n" \
"ds_read_b64_tr_b16 a[76:77], v11 offset:41472\n" \
"v_mfma_f32_16x16x32_bf16 v[112:115], a[24:27], a[8:11], v[112:115]\n" \
"ds_read_b64_tr_b16 a[78:79], v12 offset:41472\n" \
"v_mfma_f32_16x16x32_bf16 v[116:119], a[28:31], a[8:11], v[116:119]\n" \
"s_add_u32 m0, s50, 61440\n" \
"ds_read_b128 a[48:51], v8 offset:0\n" \
"v_mfma_f32_16x16x32_bf16 v[120:123], a[24:27], a[12:15], v[120:123]\n" \
"buffer_load_dwordx4 v20, s[28:31], s46 offen lds\n" \
"v_mfma_f32_16x16x32_bf16 v[124:127], a[28:31], a[12:15], v[124:127]\n" \
"ds_read_b128 a[52:55], v8 offset:2048\n" \
"v_mfma_f32_16x16x32_bf16 v[128:131], a[32:35], a[0:3], v[128:131]\n" \
"v_mfma_f32_16x16x32_bf16 v[132:135], a[36:39], a[0:3], v[132:135]\n" \
"ds_read_b128 a[56:59], v8 offset:4096\n" \
"v_mfma_f32_16x16x32_bf16 v[136:139], a[32:35], a[4:7], v[136:139]\n" \
"s_mov_b32 s47, 7864320\n" \
"s_add_u32 m0, s51, 16384\n" \
"ds_read_b128 a[60:63], v8 offset:6144\n" \
"v_mfma_f32_16x16x32_bf16 v[140:143], a[36:39], a[4:7], v[140:143]\n" \
"buffer_load_dwordx4 v21, s[32:35], s47 offen offset:0 lds\n" \
"v_mfma_f32_16x16x32_bf16 v[144:147], a[32:35], a[8:11], v[144:147]\n" \
"ds_read_b64_tr_b16 a[80:81], v9 offset:41984\n" \
"v_mfma_f32_16x16x32_bf16 v[148:151], a[36:39], a[8:11], v[148:151]\n" \
"ds_read_b64_tr_b16 a[82:83], v10 offset:41984\n" \
"v_mfma_f32_16x16x32_bf16 v[152:155], a[32:35], a[12:15], v[152:155]\n" \
"ds_read_b64_tr_b16 a[84:85], v11 offset:41984\n" \
"v_mfma_f32_16x16x32_bf16 v[156:159], a[36:39], a[12:15], v[156:159]\n" \
"s_add_u32 m0, s51, 17280\n" \
"ds_read_b64_tr_b16 a[86:87], v12 offset:41984\n" \
"v_mfma_f32_16x16x32_bf16 v[160:163], a[40:43], a[0:3], v[160:163]\n" \
"buffer_load_dwordx4 v21, s[32:35], s47 offen offset:128 lds\n" \
"v_mfma_f32_16x16x32_bf16 v[164:167], a[44:47], a[0:3], v[164:167]\n" \
"ds_read_b64_tr_b16 a[88:89], v9 offset:42496\n" \
"v_mfma_f32_16x16x32_bf16 v[168:171], a[40:43], a[4:7], v[168:171]\n" \
"ds_read_b64_tr_b16 a[90:91], v10 offset:42496\n" \
"v_mfma_f32_16x16x32_bf16 v[172:175], a[44:47], a[4:7], v[172:175]\n" \
"ds_read_b64_tr_b16 a[92:93], v11 offset:42496\n" \
"v_mfma_f32_16x16x32_bf16 v[176:179], a[40:43], a[8:11], v[176:179]\n" \
"s_mov_b32 s47, 8126464\n" \
"s_add_u32 m0, s51, 24576\n" \
"ds_read_b64_tr_b16 a[94:95], v12 offset:42496\n" \
"v_mfma_f32_16x16x32_bf16 v[180:183], a[44:47], a[8:11], v[180:183]\n" \
"buffer_load_dwordx4 v21, s[32:35], s47 offen offset:0 lds\n" \
"v_mfma_f32_16x16x32_bf16 v[184:187], a[40:43], a[12:15], v[184:187]\n" \
"ds_read_b64_tr_b16 a[64:65], v9 offset:40960\n" \
"v_mfma_f32_16x16x32_bf16 v[188:191], a[44:47], a[12:15], v[188:191]\n" \
"v_mfma_f32_16x16x32_bf16 v[64:67], a[16:19], a[0:3], v[64:67]\n" \
"ds_read_b64_tr_b16 a[66:67], v10 offset:40960\n" \
"v_mfma_f32_16x16x32_bf16 v[68:71], a[20:23], a[0:3], v[68:71]\n" \
"s_add_u32 m0, s51, 25472\n" \
"ds_read_b64_tr_b16 a[68:69], v11 offset:40960\n" \
"v_mfma_f32_16x16x32_bf16 v[72:75], a[16:19], a[4:7], v[72:75]\n" \
"buffer_load_dwordx4 v21, s[32:35], s47 offen offset:128 lds\n" \
"v_mfma_f32_16x16x32_bf16 v[76:79], a[20:23], a[4:7], v[76:79]\n" \
"ds_read_b64_tr_b16 a[70:71], v12 offset:40960\n" \
"v_mfma_f32_16x16x32_bf16 v[80:83], a[16:19], a[8:11], v[80:83]\n" \
"v_mfma_f32_16x16x32_bf16 v[84:87], a[20:23], a[8:11], v[84:87]\n" \
"v_mfma_f32_16x16x32_bf16 v[88:91], a[16:19], a[12:15], v[88:91]\n" \
"s_waitcnt vmcnt(16) lgkmcnt(0)\n" \
"v_mfma_f32_16x16x32_bf16 v[92:95], a[20:23], a[12:15], v[92:95]\n" \
"s_barrier\n" \
"v_mfma_f32_16x16x32_bf16 v[96:99], a[72:75], a[48:51], v[96:99]\n" \
"s_mov_b32 s46, 1920\n" \
"s_add_u32 m0, s50, 65536\n" \
"ds_read_b64_tr_b16 a[24:25], v9 offset:49664\n" \
"v_mfma_f32_16x16x32_bf16 v[100:103], a[76:79], a[48:51], v[100:103]\n" \
"buffer_load_dwordx4 v13, s[28:31], s46 offen lds\n" \
"v_mfma_f32_16x16x32_bf16 v[104:107], a[72:75], a[52:55], v[104:107]\n" \
"ds_read_b64_tr_b16 a[26:27], v10 offset:49664\n" \
"v_mfma_f32_16x16x32_bf16 v[108:111], a[76:79], a[52:55], v[108:111]\n" \
"ds_read_b64_tr_b16 a[28:29], v11 offset:49664\n" \
"v_mfma_f32_16x16x32_bf16 v[112:115], a[72:75], a[56:59], v[112:115]\n" \
"ds_read_b64_tr_b16 a[30:31], v12 offset:49664\n" \
"v_mfma_f32_16x16x32_bf16 v[116:119], a[76:79], a[56:59], v[116:119]\n" \
"s_add_u32 m0, s50, 69632\n" \
"ds_read_b128 a[0:3], v5 offset:0\n" \
"v_mfma_f32_16x16x32_bf16 v[120:123], a[72:75], a[60:63], v[120:123]\n" \
"buffer_load_dwordx4 v14, s[28:31], s46 offen lds\n" \
"v_mfma_f32_16x16x32_bf16 v[124:127], a[76:79], a[60:63], v[124:127]\n" \
"ds_read_b128 a[4:7], v5 offset:2048\n" \
"v_mfma_f32_16x16x32_bf16 v[128:131], a[80:83], a[48:51], v[128:131]\n" \
"v_mfma_f32_16x16x32_bf16 v[132:135], a[84:87], a[48:51], v[132:135]\n" \
"ds_read_b128 a[8:11], v5 offset:4096\n" \
"v_mfma_f32_16x16x32_bf16 v[136:139], a[80:83], a[52:55], v[136:139]\n" \
"s_add_u32 m0, s50, 73728\n" \
"ds_read_b128 a[12:15], v5 offset:6144\n" \
"v_mfma_f32_16x16x32_bf16 v[140:143], a[84:87], a[52:55], v[140:143]\n" \
"buffer_load_dwordx4 v15, s[28:31], s46 offen lds\n" \
"v_mfma_f32_16x16x32_bf16 v[144:147], a[80:83], a[56:59], v[144:147]\n" \
"ds_read_b64_tr_b16 a[32:33], v9 offset:50176\n" \
"v_mfma_f32_16x16x32_bf16 v[148:151], a[84:87], a[56:59], v[148:151]\n" \
"ds_read_b64_tr_b16 a[34:35], v10 offset:50176\n" \
"v_mfma_f32_16x16x32_bf16 v[152:155], a[80:83], a[60:63], v[152:155]\n" \
"ds_read_b64_tr_b16 a[36:37], v11 offset:50176\n" \
"v_mfma_f32_16x16x32_bf16 v[156:159], a[84:87], a[60:63], v[156:159]\n" \
"s_add_u32 m0, s50, 77824\n" \
"ds_read_b64_tr_b16 a[38:39], v12 offset:50176\n" \
"v_mfma_f32_16x16x32_bf16 v[160:163], a[88:91], a[48:51], v[160:163]\n" \
"buffer_load_dwordx4 v16, s[28:31], s46 offen lds\n" \
"v_mfma_f32_16x16x32_bf16 v[164:167], a[92:95], a[48:51], v[164:167]\n" \
"ds_read_b64_tr_b16 a[40:41], v9 offset:50688\n" \
"v_mfma_f32_16x16x32_bf16 v[168:171], a[88:91], a[52:55], v[168:171]\n" \
"ds_read_b64_tr_b16 a[42:43], v10 offset:50688\n" \
"v_mfma_f32_16x16x32_bf16 v[172:175], a[92:95], a[52:55], v[172:175]\n" \
"ds_read_b64_tr_b16 a[44:45], v11 offset:50688\n" \
"v_mfma_f32_16x16x32_bf16 v[176:179], a[88:91], a[56:59], v[176:179]\n" \
"s_mov_b32 s46, 1920\n" \
"s_add_u32 m0, s50, 81920\n" \
"ds_read_b64_tr_b16 a[46:47], v12 offset:50688\n" \
"v_mfma_f32_16x16x32_bf16 v[180:183], a[92:95], a[56:59], v[180:183]\n" \
"buffer_load_dwordx4 v17, s[28:31], s46 offen lds\n" \
"v_mfma_f32_16x16x32_bf16 v[184:187], a[88:91], a[60:63], v[184:187]\n" \
"ds_read_b64_tr_b16 a[16:17], v9 offset:49152\n" \
"v_mfma_f32_16x16x32_bf16 v[188:191], a[92:95], a[60:63], v[188:191]\n" \
"v_mfma_f32_16x16x32_bf16 v[64:67], a[64:67], a[48:51], v[64:67]\n" \
"ds_read_b64_tr_b16 a[18:19], v10 offset:49152\n" \
"v_mfma_f32_16x16x32_bf16 v[68:71], a[68:71], a[48:51], v[68:71]\n" \
"s_add_u32 m0, s50, 86016\n" \
"ds_read_b64_tr_b16 a[20:21], v11 offset:49152\n" \
"v_mfma_f32_16x16x32_bf16 v[72:75], a[64:67], a[52:55], v[72:75]\n" \
"buffer_load_dwordx4 v18, s[28:31], s46 offen lds\n" \
"v_mfma_f32_16x16x32_bf16 v[76:79], a[68:71], a[52:55], v[76:79]\n" \
"ds_read_b64_tr_b16 a[22:23], v12 offset:49152\n" \
"v_mfma_f32_16x16x32_bf16 v[80:83], a[64:67], a[56:59], v[80:83]\n" \
"v_mfma_f32_16x16x32_bf16 v[84:87], a[68:71], a[56:59], v[84:87]\n" \
"v_mfma_f32_16x16x32_bf16 v[88:91], a[64:67], a[60:63], v[88:91]\n" \
"v_mfma_f32_16x16x32_bf16 v[92:95], a[68:71], a[60:63], v[92:95]\n" \
"s_waitcnt lgkmcnt(0)\n" \
"v_mfma_f32_16x16x32_bf16 v[96:99], a[24:27], a[0:3], v[96:99]\n" \
"ds_read_b64_tr_b16 a[72:73], v9 offset:57856\n" \
"v_mfma_f32_16x16x32_bf16 v[100:103], a[28:31], a[0:3], v[100:103]\n" \
"buffer_load_dwordx2 v[58:59], v57, s[36:39], 0 offen sc1\n" \
"v_mfma_f32_16x16x32_bf16 v[104:107], a[24:27], a[4:7], v[104:107]\n" \
"ds_read_b64_tr_b16 a[74:75], v10 offset:57856\n" \
"v_mfma_f32_16x16x32_bf16 v[108:111], a[28:31], a[4:7], v[108:111]\n" \
"ds_read_b64_tr_b16 a[76:77], v11 offset:57856\n" \
"v_mfma_f32_16x16x32_bf16 v[112:115], a[24:27], a[8:11], v[112:115]\n" \
"s_add_u32 m0, s50, 90112\n" \
"ds_read_b64_tr_b16 a[78:79], v12 offset:57856\n" \
"v_mfma_f32_16x16x32_bf16 v[116:119], a[28:31], a[8:11], v[116:119]\n" \
"buffer_load_dwordx4 v19, s[28:31], s46 offen lds\n" \
"v_mfma_f32_16x16x32_bf16 v[120:123], a[24:27], a[12:15], v[120:123]\n" \
"ds_read_b128 a[48:51], v6 offset:0\n" \
"v_mfma_f32_16x16x32_bf16 v[124:127], a[28:31], a[12:15], v[124:127]\n" \
"ds_read_b128 a[52:55], v6 offset:2048\n" \
"v_mfma_f32_16x16x32_bf16 v[128:131], a[32:35], a[0:3], v[128:131]\n" \
"s_add_u32 m0, s50, 94208\n" \
"ds_read_b128 a[56:59], v6 offset:4096\n" \
"v_mfma_f32_16x16x32_bf16 v[132:135], a[36:39], a[0:3], v[132:135]\n" \
"buffer_load_dwordx4 v20, s[28:31], s46 offen lds\n" \
"v_mfma_f32_16x16x32_bf16 v[136:139], a[32:35], a[4:7], v[136:139]\n" \
"LH13_%=:\ns_cbranch_execz LHR0_%=\n" \
"s_add_u32 s100, s100, 262144\ns_addc_u32 s101, s101, 0\n" \
"ds_read_b128 a[60:63], v6 offset:6144\n" \
"v_mfma_f32_16x16x32_bf16 v[140:143], a[36:39], a[4:7], v[140:143]\n" \
"ds_read_b64_tr_b16 a[80:81], v9 offset:58368\n" \
"v_mfma_f32_16x16x32_bf16 v[144:147], a[32:35], a[8:11], v[144:147]\n" \
"ds_read_b64_tr_b16 a[82:83], v10 offset:58368\n" \
"v_mfma_f32_16x16x32_bf16 v[148:151], a[36:39], a[8:11], v[148:151]\n" \
"ds_read_b64_tr_b16 a[84:85], v11 offset:58368\n" \
"v_mfma_f32_16x16x32_bf16 v[152:155], a[32:35], a[12:15], v[152:155]\n" \
"ds_read_b64_tr_b16 a[86:87], v12 offset:58368\n" \
"v_mfma_f32_16x16x32_bf16 v[156:159], a[36:39], a[12:15], v[156:159]\n" \
"ds_read_b64_tr_b16 a[88:89], v9 offset:58880\n" \
"v_mfma_f32_16x16x32_bf16 v[160:163], a[40:43], a[0:3], v[160:163]\n" \
"ds_read_b64_tr_b16 a[90:91], v10 offset:58880\n" \
"v_mfma_f32_16x16x32_bf16 v[164:167], a[44:47], a[0:3], v[164:167]\n" \
"ds_read_b64_tr_b16 a[92:93], v11 offset:58880\n" \
"v_mfma_f32_16x16x32_bf16 v[168:171], a[40:43], a[4:7], v[168:171]\n" \
"ds_read_b64_tr_b16 a[94:95], v12 offset:58880\n" \
"v_mfma_f32_16x16x32_bf16 v[172:175], a[44:47], a[4:7], v[172:175]\n" \
"ds_read_b64_tr_b16 a[64:65], v9 offset:57344\n" \
"v_mfma_f32_16x16x32_bf16 v[176:179], a[40:43], a[8:11], v[176:179]\n" \
"ds_read_b64_tr_b16 a[66:67], v10 offset:57344\n" \
"v_mfma_f32_16x16x32_bf16 v[180:183], a[44:47], a[8:11], v[180:183]\n" \
"ds_read_b64_tr_b16 a[68:69], v11 offset:57344\n" \
"v_mfma_f32_16x16x32_bf16 v[184:187], a[40:43], a[12:15], v[184:187]\n" \
"ds_read_b64_tr_b16 a[70:71], v12 offset:57344\n" \
"v_mfma_f32_16x16x32_bf16 v[188:191], a[44:47], a[12:15], v[188:191]\n" \
"s_mov_b64 exec, 0xffff\nglobal_load_dword v1, v2, s[100:101]\ns_mov_b64 exec, -1\n" \
"v_mfma_f32_16x16x32_bf16 v[64:67], a[16:19], a[0:3], v[64:67]\n" \
"s_add_u32 s100, s100, 262144\ns_addc_u32 s101, s101, 0\n" \
"v_mfma_f32_16x16x32_bf16 v[68:71], a[20:23], a[0:3], v[68:71]\n" \
"s_mov_b64 exec, 0xffff\nglobal_load_dword v1, v2, s[100:101]\ns_mov_b64 exec, -1\n" \
"v_mfma_f32_16x16x32_bf16 v[72:75], a[16:19], a[4:7], v[72:75]\n" \
"s_mov_b32 s44, 0\n" \
"s_mov_b64 s[54:55], 0\n" \
"s_mov_b32 s49, s22\n" \
"v_mfma_f32_16x16x32_bf16 v[76:79], a[20:23], a[4:7], v[76:79]\n" \
"s_add_u32 s48, s22, 0x10000\n" \
"s_lshl_b32 s60, s7, 1\n" \
"s_add_u32 s60, s60, %2\n" \
"v_mfma_f32_16x16x32_bf16 v[80:83], a[16:19], a[8:11], v[80:83]\n" \
"s_waitcnt vmcnt(4)\n" \
"s_sub_u32 s46, s60, 1\n" \
"s_xor_b32 s62, %2, 3\n" \
"v_mfma_f32_16x16x32_bf16 v[84:87], a[20:23], a[8:11], v[84:87]\n" \
"v_cmp_ne_u64 vcc, s[14:15], v[58:59]\n" \
"v_mfma_f32_16x16x32_bf16 v[88:91], a[16:19], a[12:15], v[88:91]\n" \
"s_waitcnt lgkmcnt(0)\n" \
"v_mfma_f32_16x16x32_bf16 v[92:95], a[20:23], a[12:15], v[92:95]\n" \
"s_cbranch_vccnz LPK_%=\nLPKR_%=:\n" \
"s_barrier\n" \
"v_mfma_f32_16x16x32_bf16 v[96:99], a[72:75], a[48:51], v[96:99]\n" \
"ds_read_b64_tr_b16 a[24:25], v9 offset:512\n" \
"v_mfma_f32_16x16x32_bf16 v[100:103], a[76:79], a[48:51], v[100:103]\n" \
"ds_read_b64_tr_b16 a[26:27], v10 offset:512\n" \
"v_mfma_f32_16x16x32_bf16 v[104:107], a[72:75], a[52:55], v[104:107]\n" \
"ds_read_b64_tr_b16 a[28:29], v11 offset:512\n" \
"v_mfma_f32_16x16x32_bf16 v[108:111], a[76:79], a[52:55], v[108:111]\n" \
"ds_read_b64_tr_b16 a[30:31], v12 offset:512\n" \
"v_mfma_f32_16x16x32_bf16 v[112:115], a[72:75], a[56:59], v[112:115]\n" \
"ds_read_b128 a[0:3], v5 offset:32768\n" \
"v_mfma_f32_16x16x32_bf16 v[116:119], a[76:79], a[56:59], v[116:119]\n" \
"ds_read_b128 a[4:7], v5 offset:34816\n" \
"v_mfma_f32_16x16x32_bf16 v[120:123], a[72:75], a[60:63], v[120:123]\n" \
"ds_read_b128 a[8:11], v5 offset:36864\n" \
"v_mfma_f32_16x16x32_bf16 v[124:127], a[76:79], a[60:63], v[124:127]\n" \
"ds_read_b128 a[12:15], v5 offset:38912\n" \
"v_mfma_f32_16x16x32_bf16 v[128:131], a[80:83], a[48:51], v[128:131]\n" \
"ds_read_b64_tr_b16 a[32:33], v9 offset:1024\n" \
"v_mfma_f32_16x16x32_bf16 v[132:135], a[84:87], a[48:51], v[132:135]\n" \
"ds_read_b64_tr_b16 a[34:35], v10 offset:1024\n" \
"v_mfma_f32_16x16x32_bf16 v[136:139], a[80:83], a[52:55], v[136:139]\n" \
"ds_read_b64_tr_b16 a[36:37], v11 offset:1024\n" \
"v_mfma_f32_16x16x32_bf16 v[140:143], a[84:87], a[52:55], v[140:143]\n" \
"ds_read_b64_tr_b16 a[38:39], v12 offset:1024\n" \
"v_mfma_f32_16x16x32_bf16 v[144:147], a[80:83], a[56:59], v[144:147]\n" \
"ds_read_b64_tr_b16 a[40:41], v9 offset:1536\n" \
"v_mfma_f32_16x16x32_bf16 v[148:151], a[84:87], a[56:59], v[148:151]\n" \
"ds_read_b64_tr_b16 a[42:43], v10 offset:1536\n" \
"v_mfma_f32_16x16x32_bf16 v[152:155], a[80:83], a[60:63], v[152:155]\n" \
"ds_read_b64_tr_b16 a[44:45], v11 offset:1536\n" \
"v_mfma_f32_16x16x32_bf16 v[156:159], a[84:87], a[60:63], v[156:159]\n" \
"ds_read_b64_tr_b16 a[46:47], v12 offset:1536\n" \
"v_mfma_f32_16x16x32_bf16 v[160:163], a[88:91], a[48:51], v[160:163]\n" \
"ds_read_b64_tr_b16 a[16:17], v9 offset:0\n" \
"v_mfma_f32_16x16x32_bf16 v[164:167], a[92:95], a[48:51], v[164:167]\n" \
"ds_read_b64_tr_b16 a[18:19], v10 offset:0\n" \
"v_mfma_f32_16x16x32_bf16 v[168:171], a[88:91], a[52:55], v[168:171]\n" \
"ds_read_b64_tr_b16 a[20:21], v11 offset:0\n" \
"v_mfma_f32_16x16x32_bf16 v[172:175], a[92:95], a[52:55], v[172:175]\n" \
"ds_read_b64_tr_b16 a[22:23], v12 offset:0\n" \
"v_mfma_f32_16x16x32_bf16 v[176:179], a[88:91], a[56:59], v[176:179]\n" \
"s_cmp_lt_u32 %2, 2\ns_cselect_b32 s46, s46, 14\ns_cselect_b32 s62, s62, %2\ns_cselect_b32 s61, 56, 57\n" \
"v_mfma_f32_16x16x32_bf16 v[180:183], a[92:95], a[56:59], v[180:183]\n" \
"s_cmp_lt_u32 s7, 3\ns_cselect_b32 s45, 8, 0\n" \
"v_mfma_f32_16x16x32_bf16 v[184:187], a[88:91], a[60:63], v[184:187]\n" \
"s_cmp_lt_i32 s46, s45\ns_cselect_b32 s23, 99, s46\ns_cselect_b32 s24, s45, 99\n" \
"v_mfma_f32_16x16x32_bf16 v[188:191], a[92:95], a[60:63], v[188:191]\n" \
"s_min_i32 s46, s46, s45\n" \
"s_lshl_b32 s60, s60, 6\n" \
"s_add_u32 s60, s60, s6\n" \
"v_mfma_f32_16x16x32_bf16 v[64:67], a[64:67], a[48:51], v[64:67]\n" \
"s_lshl_b32 s50, s60, 13\n" \
"s_lshl_b32 s52, s60, 3\n" \
"s_add_u32 s52, s52, 94388224\n" \
"v_mfma_f32_16x16x32_bf16 v[68:71], a[68:71], a[48:51], v[68:71]\n" \
"s_lshr_b32 s60, %2, 1\n" \
"s_lshl_b32 s60, s60, 28\n" \
"s_add_u32 s52, s52, s60\n" \
"v_mfma_f32_16x16x32_bf16 v[72:75], a[64:67], a[52:55], v[72:75]\n" \
"s_lshl_b32 s62, s62, 1\n" \
"s_add_u32 s61, s61, s62\n" \
"s_lshl_b32 s60, s7, 2\n" \
"v_mfma_f32_16x16x32_bf16 v[76:79], a[68:71], a[52:55], v[76:79]\n" \
"s_sub_u32 s61, s61, s60\n" \
"s_lshl_b32 s61, s61, 5\n" \
"s_add_u32 s61, s61, s6\n" \
"v_mfma_f32_16x16x32_bf16 v[80:83], a[64:67], a[56:59], v[80:83]\n" \
"s_lshl_b32 s51, s61, 13\n" \
"s_lshl_b32 s53, s61, 3\n" \
"s_add_u32 s53, s53, 94388224\n" \
"v_mfma_f32_16x16x32_bf16 v[84:87], a[68:71], a[56:59], v[84:87]\n" \
"s_and_b32 s62, s62, 2\n" \
"s_lshl_b32 s62, s62, 12\n" \
"s_add_u32 s62, s62, 131072\n" \
"v_mfma_f32_16x16x32_bf16 v[88:91], a[64:67], a[60:63], v[88:91]\n" \
"s_or_b32 s60, s7, %2\n" \
"v_add_u32 v21, s62, v4\n" \
"v_mfma_f32_16x16x32_bf16 v[92:95], a[68:71], a[60:63], v[92:95]\n" \
"s_waitcnt lgkmcnt(0)\n" \
"s_cmp_eq_u32 s60, 0\ns_cselect_b32 s60, 0xff800000, 0\n" \
"v_mfma_f32_16x16x32_bf16 v[96:99], a[24:27], a[0:3], v[96:99]\n" \
"s_add_u32 m0, s22, 32768\n" \
"ds_read_b64_tr_b16 a[72:73], v9 offset:8704\n" \
"v_lshrrev_b32 v22, 6, v4\n" \
"v_mfma_f32_16x16x32_bf16 v[100:103], a[28:31], a[0:3], v[100:103]\n" \
"buffer_load_dwordx4 v60, s[40:43], s16 offen offset:0 sc1 lds\n" \
"v_mfma_f32_16x16x32_bf16 v[104:107], a[24:27], a[4:7], v[104:107]\n" \
"ds_read_b64_tr_b16 a[74:75], v10 offset:8704\n" \
"v_and_b32 v22, 12, v22\n" \
"v_mfma_f32_16x16x32_bf16 v[108:111], a[28:31], a[4:7], v[108:111]\n" \
"ds_read_b64_tr_b16 a[76:77], v11 offset:8704\n" \
"v_bfe_u32 v23, v4, 4, 4\n" \
"v_mfma_f32_16x16x32_bf16 v[112:115], a[24:27], a[8:11], v[112:115]\n" \
"ds_read_b64_tr_b16 a[78:79], v12 offset:8704\n" \
"v_sub_u32 v22, v23, v22\n" \
"v_mfma_f32_16x16x32_bf16 v[116:119], a[28:31], a[8:11], v[116:119]\n" \
"buffer_load_dwordx4 v60, s[40:43], s16 offen offset:1024 sc1 lds\n" \
"v_mfma_f32_16x16x32_bf16 v[120:123], a[24:27], a[12:15], v[120:123]\n" \
"ds_read_b128 a[48:51], v6 offset:32768\n" \
"v_subrev_u32 v240, 0, v22\n" \
"v_mfma_f32_16x16x32_bf16 v[124:127], a[28:31], a[12:15], v[124:127]\n" \
"ds_read_b128 a[52:55], v6 offset:34816\n" \
"v_ashrrev_i32 v240, 31, v240\n" \
"v_mfma_f32_16x16x32_bf16 v[128:131], a[32:35], a[0:3], v[128:131]\n" \
"ds_read_b128 a[56:59], v6 offset:36864\n" \
"v_and_b32 v240, 0xff800000, v240\n" \
"v_mfma_f32_16x16x32_bf16 v[132:135], a[36:39], a[0:3], v[132:135]\n" \
"buffer_load_dwordx4 v60, s[40:43], s16 offen offset:2048 sc1 lds\n" \
"v_mfma_f32_16x16x32_bf16 v[136:139], a[32:35], a[4:7], v[136:139]\n" \
"ds_read_b128 a[60:63], v6 offset:38912\n" \
"v_and_b32 v24, s60, v240\n" \
"v_mfma_f32_16x16x32_bf16 v[140:143], a[36:39], a[4:7], v[140:143]\n" \
"ds_read_b64_tr_b16 a[80:81], v9 offset:9216\n" \
"v_mov_b32 v244, s60\n" \
"v_mfma_f32_16x16x32_bf16 v[144:147], a[32:35], a[8:11], v[144:147]\n" \
"ds_read_b64_tr_b16 a[82:83], v10 offset:9216\n" \
"v_subrev_u32 v241, 1, v22\n" \
"v_mfma_f32_16x16x32_bf16 v[148:151], a[36:39], a[8:11], v[148:151]\n" \
"buffer_load_dwordx4 v60, s[40:43], s16 offen offset:3072 sc1 lds\n" \
"v_mfma_f32_16x16x32_bf16 v[152:155], a[32:35], a[12:15], v[152:155]\n" \
"ds_read_b64_tr_b16 a[84:85], v11 offset:9216\n" \
"v_ashrrev_i32 v241, 31, v241\n" \
"v_mfma_f32_16x16x32_bf16 v[156:159], a[36:39], a[12:15], v[156:159]\n" \
"ds_read_b64_tr_b16 a[86:87], v12 offset:9216\n" \
"v_and_b32 v241, 0xff800000, v241\n" \
"v_mfma_f32_16x16x32_bf16 v[160:163], a[40:43], a[0:3], v[160:163]\n" \
"s_add_u32 s16, s16, s9\n" \
"s_add_u32 m0, s22, 65536\n" \
"ds_read_b64_tr_b16 a[88:89], v9 offset:9728\n" \
"v_mfma_f32_16x16x32_bf16 v[164:167], a[44:47], a[0:3], v[164:167]\n" \
"buffer_load_dwordx4 v60, s[40:43], s16 offen offset:0 sc1 lds\n" \
"v_mfma_f32_16x16x32_bf16 v[168:171], a[40:43], a[4:7], v[168:171]\n" \
"ds_read_b64_tr_b16 a[90:91], v10 offset:9728\n" \
"v_and_b32 v25, s60, v241\n" \
"v_mfma_f32_16x16x32_bf16 v[172:175], a[44:47], a[4:7], v[172:175]\n" \
"ds_read_b64_tr_b16 a[92:93], v11 offset:9728\n" \
"v_mov_b32 v245, s60\n" \
"v_mfma_f32_16x16x32_bf16 v[176:179], a[40:43], a[8:11], v[176:179]\n" \
"ds_read_b64_tr_b16 a[94:95], v12 offset:9728\n" \
"v_subrev_u32 v242, 2, v22\n" \
"v_mfma_f32_16x16x32_bf16 v[180:183], a[44:47], a[8:11], v[180:183]\n" \
"buffer_load_dwordx4 v60, s[40:43], s16 offen offset:1024 sc1 lds\n" \
"v_mfma_f32_16x16x32_bf16 v[184:187], a[40:43], a[12:15], v[184:187]\n" \
"ds_read_b64_tr_b16 a[64:65], v9 offset:8192\n" \
"v_ashrrev_i32 v242, 31, v242\n" \
"v_mfma_f32_16x16x32_bf16 v[188:191], a[44:47], a[12:15], v[188:191]\n" \
"ds_read_b64_tr_b16 a[66:67], v10 offset:8192\n" \
"v_and_b32 v242, 0xff800000, v242\n" \
"v_mfma_f32_16x16x32_bf16 v[64:67], a[16:19], a[0:3], v[64:67]\n" \
"ds_read_b64_tr_b16 a[68:69], v11 offset:8192\n" \
"v_and_b32 v26, s60, v242\n" \
"v_mfma_f32_16x16x32_bf16 v[68:71], a[20:23], a[0:3], v[68:71]\n" \
"buffer_load_dwordx4 v60, s[40:43], s16 offen offset:2048 sc1 lds\n" \
"v_mfma_f32_16x16x32_bf16 v[72:75], a[16:19], a[4:7], v[72:75]\n" \
"ds_read_b64_tr_b16 a[70:71], v12 offset:8192\n" \
"v_mov_b32 v246, s60\n" \
"v_mfma_f32_16x16x32_bf16 v[76:79], a[20:23], a[4:7], v[76:79]\n" \
"v_subrev_u32 v243, 3, v22\n" \
"v_ashrrev_i32 v243, 31, v243\n" \
"v_mfma_f32_16x16x32_bf16 v[80:83], a[16:19], a[8:11], v[80:83]\n" \
"v_and_b32 v243, 0xff800000, v243\n" \
"v_and_b32 v27, s60, v243\n" \
"v_mfma_f32_16x16x32_bf16 v[84:87], a[20:23], a[8:11], v[84:87]\n" \
"buffer_load_dwordx4 v60, s[40:43], s16 offen offset:3072 sc1 lds\n" \
"v_mfma_f32_16x16x32_bf16 v[88:91], a[16:19], a[12:15], v[88:91]\n" \
"s_waitcnt vmcnt(10) lgkmcnt(0)\n" \
"v_mov_b32 v247, s60\n" \
"s_load_dwordx2 s[28:29], %0, 0x50\n" \
"v_mfma_f32_16x16x32_bf16 v[92:95], a[20:23], a[12:15], v[92:95]\n" \
"s_barrier\n" \
"s_load_dwordx2 s[32:33], %0, 0x60\n" \
"s_load_dwordx2 s[60:61], %0, 0x0\n" \
"v_mfma_f32_16x16x32_bf16 v[96:99], a[72:75], a[48:51], v[96:99]\n" \
"s_add_u32 m0, s22, 81920\n" \
"ds_read_b64_tr_b16 a[24:25], v9 offset:16896\n" \
"s_lshl_b32 s79, %2, 11\n" \
"v_mfma_f32_16x16x32_bf16 v[100:103], a[76:79], a[48:51], v[100:103]\n" \
"buffer_load_dwordx4 v60, s[40:43], s17 offen offset:0 sc1 lds\n" \
"v_mfma_f32_16x16x32_bf16 v[104:107], a[72:75], a[52:55], v[104:107]\n" \
"s_lshl_b32 s20, s89, 21\n" \
"ds_read_b64_tr_b16 a[26:27], v10 offset:16896\n" \
"s_lshr_b32 s21, %1, 5\n" \
"v_mfma_f32_16x16x32_bf16 v[108:111], a[76:79], a[52:55], v[108:111]\n" \
"s_lshl_b32 s21, s21, 8\n" \
"ds_read_b64_tr_b16 a[28:29], v11 offset:16896\n" \
"s_add_u32 s18, s20, s21\n" \
"v_mfma_f32_16x16x32_bf16 v[112:115], a[72:75], a[56:59], v[112:115]\n" \
"s_sub_u32 s30, 8388608, s18\n" \
"ds_read_b64_tr_b16 a[30:31], v12 offset:16896\n" \
"s_lshl_b32 s20, %2, 2\n" \
"v_mfma_f32_16x16x32_bf16 v[116:119], a[76:79], a[56:59], v[116:119]\n" \
"buffer_load_dwordx4 v60, s[40:43], s17 offen offset:1024 sc1 lds\n" \
"v_mfma_f32_16x16x32_bf16 v[120:123], a[72:75], a[60:63], v[120:123]\n" \
"s_lshl_b32 s21, %2, 1\n" \
"ds_read_b128 a[0:3], v7 offset:0\n" \
"v_bfe_u32 v22, v4, 6, 2\n" \
"v_mfma_f32_16x16x32_bf16 v[124:127], a[76:79], a[60:63], v[124:127]\n" \
"ds_read_b128 a[4:7], v7 offset:2048\n" \
"v_add_u32 v22, s20, v22\n" \
"v_mfma_f32_16x16x32_bf16 v[128:131], a[80:83], a[48:51], v[128:131]\n" \
"ds_read_b128 a[8:11], v7 offset:4096\n" \
"v_bfe_u32 v23, v4, 8, 1\n" \
"v_mfma_f32_16x16x32_bf16 v[132:135], a[84:87], a[48:51], v[132:135]\n" \
"buffer_load_dwordx4 v60, s[40:43], s17 offen offset:2048 sc1 lds\n" \
"v_mfma_f32_16x16x32_bf16 v[136:139], a[80:83], a[52:55], v[136:139]\n" \
"ds_read_b128 a[12:15], v7 offset:6144\n" \
"v_lshl_add_u32 v22, v23, 4, v22\n" \
"v_mfma_f32_16x16x32_bf16 v[140:143], a[84:87], a[52:55], v[140:143]\n" \
"ds_read_b64_tr_b16 a[32:33], v9 offset:17408\n" \
"v_lshlrev_b32 v22, 11, v22\n" \
"v_mfma_f32_16x16x32_bf16 v[144:147], a[80:83], a[56:59], v[144:147]\n" \
"ds_read_b64_tr_b16 a[34:35], v10 offset:17408\n" \
"v_add_u32 v23, s21, v23\n" \
"v_mfma_f32_16x16x32_bf16 v[148:151], a[84:87], a[56:59], v[148:151]\n" \
"buffer_load_dwordx4 v60, s[40:43], s17 offen offset:3072 sc1 lds\n" \
"v_mfma_f32_16x16x32_bf16 v[152:155], a[80:83], a[60:63], v[152:155]\n" \
"ds_read_b64_tr_b16 a[36:37], v11 offset:17408\n" \
"v_lshrrev_b32 v28, 4, v4\n" \
"v_mfma_f32_16x16x32_bf16 v[156:159], a[84:87], a[60:63], v[156:159]\n" \
"ds_read_b64_tr_b16 a[38:39], v12 offset:17408\n" \
"v_xor_b32 v23, v23, v28\n" \
"v_mfma_f32_16x16x32_bf16 v[160:163], a[88:91], a[48:51], v[160:163]\n" \
"s_add_u32 s16, s16, s9\n" \
"s_add_u32 m0, s22, 98304\n" \
"ds_read_b64_tr_b16 a[40:41], v9 offset:17920\n" \
"v_mfma_f32_16x16x32_bf16 v[164:167], a[92:95], a[48:51], v[164:167]\n" \
"buffer_load_dwordx4 v60, s[40:43], s16 offen offset:0 sc1 lds\n" \
"v_mfma_f32_16x16x32_bf16 v[168:171], a[88:91], a[52:55], v[168:171]\n" \
"ds_read_b64_tr_b16 a[42:43], v10 offset:17920\n" \
"v_and_b32 v23, 3, v23\n" \
"v_mfma_f32_16x16x32_bf16 v[172:175], a[92:95], a[52:55], v[172:175]\n" \
"ds_read_b64_tr_b16 a[44:45], v11 offset:17920\n" \
"v_lshrrev_b32 v28, 9, v4\n" \
"v_mfma_f32_16x16x32_bf16 v[176:179], a[88:91], a[56:59], v[176:179]\n" \
"ds_read_b64_tr_b16 a[46:47], v12 offset:17920\n" \
"v_lshl_add_u32 v23, v28, 2, v23\n" \
"v_mfma_f32_16x16x32_bf16 v[180:183], a[92:95], a[56:59], v[180:183]\n" \
"buffer_load_dwordx4 v60, s[40:43], s16 offen offset:1024 sc1 lds\n" \
"v_mfma_f32_16x16x32_bf16 v[184:187], a[88:91], a[60:63], v[184:187]\n" \
"s_lshl_b32 s20, %2, 9\n" \
"ds_read_b64_tr_b16 a[16:17], v9 offset:16384\n" \
"v_lshl_add_u32 v30, v23, 4, v22\n" \
"v_mfma_f32_16x16x32_bf16 v[188:191], a[92:95], a[60:63], v[188:191]\n" \
"s_add_u32 s20, s20, 0x20000\n" \
"ds_read_b64_tr_b16 a[18:19], v10 offset:16384\n" \
"v_add_u32 v13, s20, v9\n" \
"v_mfma_f32_16x16x32_bf16 v[64:67], a[64:67], a[48:51], v[64:67]\n" \
"ds_read_b64_tr_b16 a[20:21], v11 offset:16384\n" \
"v_add_u32 v14, s20, v10\n" \
"v_mfma_f32_16x16x32_bf16 v[68:71], a[68:71], a[48:51], v[68:71]\n" \
"buffer_load_dwordx4 v60, s[40:43], s16 offen offset:2048 sc1 lds\n" \
"v_mfma_f32_16x16x32_bf16 v[72:75], a[64:67], a[52:55], v[72:75]\n" \
"ds_read_b64_tr_b16 a[22:23], v12 offset:16384\n" \
"v_add_u32 v15, s20, v11\n" \
"v_mfma_f32_16x16x32_bf16 v[76:79], a[68:71], a[52:55], v[76:79]\n" \
"v_add_u32 v16, s20, v12\n" \
"v_lshlrev_b32 v20, 23, v4\n" \
"v_mfma_f32_16x16x32_bf16 v[80:83], a[64:67], a[56:59], v[80:83]\n" \
"s_add_u32 s28, s28, s18\ns_addc_u32 s29, s29, 0\n" \
"s_and_b32 s29, s29, 0xffff\n" \
"v_mfma_f32_16x16x32_bf16 v[84:87], a[68:71], a[56:59], v[84:87]\n" \
"buffer_load_dwordx4 v60, s[40:43], s16 offen offset:3072 sc1 lds\n" \
"v_mfma_f32_16x16x32_bf16 v[88:91], a[64:67], a[60:63], v[88:91]\n" \
"s_and_b32 s33, s33, 0xffff\n" \
"s_mov_b32 s34, 4194304\n" \
"s_mov_b32 s35, 0x00020000\n" \
"v_mfma_f32_16x16x32_bf16 v[92:95], a[68:71], a[60:63], v[92:95]\n" \
"s_waitcnt lgkmcnt(0)\n" \
"s_mov_b64 exec, 0\ns_cmp_eq_u32 %2, 0\ns_cbranch_scc1 LH16_%=\ns_cmp_eq_u32 %2, 1\ns_cbranch_scc1 LH17_%=\ns_cmp_eq_u32 %2, 2\ns_cbranch_scc1 LH18_%=\ns_cmp_eq_u32 %2, 3\ns_cbranch_scc1 LH19_%=\nLHR1_%=:\ns_mov_b64 exec, -1\n" \
"v_mfma_f32_16x16x32_bf16 v[64:67], a[16:19], a[0:3], v[64:67]\n" \
"ds_read_b128 a[48:51], v8 offset:0\n" \
"v_mfma_f32_16x16x32_bf16 v[68:71], a[20:23], a[0:3], v[68:71]\n" \
"ds_read_b128 a[52:55], v8 offset:2048\n" \
"v_mfma_f32_16x16x32_bf16 v[72:75], a[16:19], a[4:7], v[72:75]\n" \
"ds_read_b128 a[56:59], v8 offset:4096\n" \
"v_mfma_f32_16x16x32_bf16 v[76:79], a[20:23], a[4:7], v[76:79]\n" \
"ds_read_b128 a[60:63], v8 offset:6144\n" \
"v_mfma_f32_16x16x32_bf16 v[80:83], a[16:19], a[8:11], v[80:83]\n" \
"ds_read_b64_tr_b16 a[64:65], v9 offset:24576\n" \
"v_mfma_f32_16x16x32_bf16 v[84:87], a[20:23], a[8:11], v[84:87]\n" \
"ds_read_b64_tr_b16 a[66:67], v10 offset:24576\n" \
"v_mfma_f32_16x16x32_bf16 v[88:91], a[16:19], a[12:15], v[88:91]\n" \
"ds_read_b64_tr_b16 a[68:69], v11 offset:24576\n" \
"v_mfma_f32_16x16x32_bf16 v[92:95], a[20:23], a[12:15], v[92:95]\n" \
"ds_read_b64_tr_b16 a[70:71], v12 offset:24576\n" \
"v_mfma_f32_16x16x32_bf16 v[96:99], a[24:27], a[0:3], v[96:99]\n" \
"s_add_u32 s17, s17, s19\n" \
"s_add_u32 m0, s22, 114688\n" \
"ds_read_b64_tr_b16 a[72:73], v9 offset:25088\n" \
"v_mfma_f32_16x16x32_bf16 v[100:103], a[28:31], a[0:3], v[100:103]\n" \
"buffer_load_dwordx4 v60, s[40:43], s17 offen offset:0 sc1 lds\n" \
"v_mfma_f32_16x16x32_bf16 v[104:107], a[24:27], a[4:7], v[104:107]\n" \
"ds_read_b64_tr_b16 a[74:75], v10 offset:25088\n" \
"v_mfma_f32_16x16x32_bf16 v[108:111], a[28:31], a[4:7], v[108:111]\n" \
"ds_read_b64_tr_b16 a[76:77], v11 offset:25088\n" \
"v_mfma_f32_16x16x32_bf16 v[112:115], a[24:27], a[8:11], v[112:115]\n" \
"ds_read_b64_tr_b16 a[78:79], v12 offset:25088\n" \
"v_mfma_f32_16x16x32_bf16 v[116:119], a[28:31], a[8:11], v[116:119]\n" \
"buffer_load_dwordx4 v60, s[40:43], s17 offen offset:1024 sc1 lds\n" \
"v_mfma_f32_16x16x32_bf16 v[120:123], a[24:27], a[12:15], v[120:123]\n" \
"ds_read_b64_tr_b16 a[80:81], v9 offset:25600\n" \
"v_mfma_f32_16x16x32_bf16 v[124:127], a[28:31], a[12:15], v[124:127]\n" \
"ds_read_b64_tr_b16 a[82:83], v10 offset:25600\n" \
"s_waitcnt lgkmcnt(6)\n" \
"v_mfma_f32_16x16x32_bf16 v[64:67], a[64:67], a[48:51], v[64:67]\n" \
"s_lshl_b32 s25, s45, 2\n" \
"ds_read_b64_tr_b16 a[84:85], v11 offset:25600\n" \
"s_and_b32 s25, s25, 32\n" \
"v_mfma_f32_16x16x32_bf16 v[68:71], a[68:71], a[48:51], v[68:71]\n" \
"buffer_load_dwordx4 v60, s[40:43], s17 offen offset:2048 sc1 lds\n" \
"v_mfma_f32_16x16x32_bf16 v[72:75], a[64:67], a[52:55], v[72:75]\n" \
"s_add_u32 s25, s25, %2\n" \
"ds_read_b64_tr_b16 a[86:87], v12 offset:25600\n" \
"v_mfma_f32_16x16x32_bf16 v[76:79], a[68:71], a[52:55], v[76:79]\n" \
"s_cmp_ge_u32 s25, 34\ns_cselect_b64 vcc, -1, 0\n" \
"ds_read_b64_tr_b16 a[88:89], v9 offset:26112\n" \
"v_mfma_f32_16x16x32_bf16 v[80:83], a[64:67], a[56:59], v[80:83]\n" \
"ds_read_b64_tr_b16 a[90:91], v10 offset:26112\n" \
"v_cvt_pk_bf16_f32 v32, v64, v65\n" \
"v_mfma_f32_16x16x32_bf16 v[84:87], a[68:71], a[56:59], v[84:87]\n" \
"buffer_load_dwordx4 v60, s[40:43], s17 offen offset:3072 sc1 lds\n" \
"v_mfma_f32_16x16x32_bf16 v[88:91], a[64:67], a[60:63], v[88:91]\n" \
"ds_read_b64_tr_b16 a[92:93], v11 offset:26112\n" \
"v_cvt_pk_bf16_f32 v33, v66, v67\n" \
"v_mfma_f32_16x16x32_bf16 v[92:95], a[68:71], a[60:63], v[92:95]\n" \
"ds_read_b64_tr_b16 a[94:95], v12 offset:26112\n" \
"s_waitcnt vmcnt(16) lgkmcnt(0)\n" \
"v_mfma_f32_16x16x32_bf16 v[96:99], a[72:75], a[48:51], v[96:99]\n" \
"s_barrier\n" \
"v_cvt_pk_bf16_f32 v34, v68, v69\n" \
"v_cvt_pk_bf16_f32 v35, v70, v71\n" \
"v_mfma_f32_16x16x32_bf16 v[100:103], a[76:79], a[48:51], v[100:103]\n" \
"v_add_u32 v5, 0x2000, v4\n" \
"v_mov_b32 v6, v60\n" \
"v_mfma_f32_16x16x32_bf16 v[104:107], a[72:75], a[52:55], v[104:107]\n" \
"s_add_u32 s16, s16, s9\n" \
"v_add_u32 v7, 0x1000, v4\n" \
"s_add_u32 m0, s49, 0\n" \
"v_mfma_f32_16x16x32_bf16 v[108:111], a[76:79], a[52:55], v[108:111]\n" \
"buffer_load_dwordx4 v6, s[40:43], s16 offen offset:0 sc1 lds\n" \
"v_mfma_f32_16x16x32_bf16 v[112:115], a[72:75], a[56:59], v[112:115]\n" \
"ds_read_b128 a[128:131], v5 offset:32768\n" \
"v_cvt_pk_bf16_f32 v48, v72, v73\n" \
"v_mfma_f32_16x16x32_bf16 v[116:119], a[76:79], a[56:59], v[116:119]\n" \
"s_cbranch_vccz LQW1_%=\nds_write_b128 v21, v[32:35] offset:16384\nLQW1_%=:\n" \
"v_mfma_f32_16x16x32_bf16 v[120:123], a[72:75], a[60:63], v[120:123]\n" \
"v_cvt_pk_bf16_f32 v49, v74, v75\n" \
"v_cvt_pk_bf16_f32 v50, v76, v77\n" \
"v_mfma_f32_16x16x32_bf16 v[124:127], a[76:79], a[60:63], v[124:127]\n" \
"buffer_load_dwordx4 v6, s[40:43], s16 offen offset:1024 sc1 lds\n" \
"v_mfma_f32_16x16x32_bf16 v[128:131], a[32:35], a[0:3], v[128:131]\n" \
"ds_read_b128 a[132:135], v5 offset:33792\n" \
"v_cvt_pk_bf16_f32 v51, v78, v79\n" \
"v_mfma_f32_16x16x32_bf16 v[132:135], a[36:39], a[0:3], v[132:135]\n" \
"s_cbranch_vccz LQW2_%=\nds_write_b128 v21, v[48:51] offset:20480\nLQW2_%=:\n" \
"v_mfma_f32_16x16x32_bf16 v[136:139], a[32:35], a[4:7], v[136:139]\n" \
"v_cvt_pk_bf16_f32 v192, v80, v81\n" \
"v_cvt_pk_bf16_f32 v193, v82, v83\n" \
"v_mfma_f32_16x16x32_bf16 v[140:143], a[36:39], a[4:7], v[140:143]\n" \
"buffer_load_dwordx4 v6, s[40:43], s16 offen offset:2048 sc1 lds\n" \
"v_mfma_f32_16x16x32_bf16 v[144:147], a[32:35], a[8:11], v[144:147]\n" \
"ds_read_b128 a[144:147], v5 offset:36864\n" \
"v_cvt_pk_bf16_f32 v194, v84, v85\n" \
"v_mfma_f32_16x16x32_bf16 v[148:151], a[36:39], a[8:11], v[148:151]\n" \
"v_cvt_pk_bf16_f32 v195, v86, v87\n" \
"v_cvt_pk_bf16_f32 v208, v88, v89\n" \
"v_mfma_f32_16x16x32_bf16 v[152:155], a[32:35], a[12:15], v[152:155]\n" \
"s_cbranch_vccz LQW3_%=\nds_write_b128 v21, v[192:195] offset:0\nLQW3_%=:\n" \
"v_mfma_f32_16x16x32_bf16 v[156:159], a[36:39], a[12:15], v[156:159]\n" \
"buffer_load_dwordx4 v6, s[40:43], s16 offen offset:3072 sc1 lds\n" \
"v_mfma_f32_16x16x32_bf16 v[128:131], a[80:83], a[48:51], v[128:131]\n" \
"s_add_u32 s17, s17, s19\n" \
"ds_read_b128 a[148:151], v5 offset:37888\n" \
"s_add_u32 m0, s49, 16384\n" \
"v_mfma_f32_16x16x32_bf16 v[132:135], a[84:87], a[48:51], v[132:135]\n" \
"v_cvt_pk_bf16_f32 v209, v90, v91\n" \
"v_cvt_pk_bf16_f32 v210, v92, v93\n" \
"v_mfma_f32_16x16x32_bf16 v[136:139], a[80:83], a[52:55], v[136:139]\n" \
"v_cvt_pk_bf16_f32 v211, v94, v95\n" \
"v_cvt_pk_bf16_f32 v36, v96, v97\n" \
"v_mfma_f32_16x16x32_bf16 v[140:143], a[84:87], a[52:55], v[140:143]\n" \
"buffer_load_dwordx4 v6, s[40:43], s17 offen offset:0 sc1 lds\n" \
"v_mfma_f32_16x16x32_bf16 v[144:147], a[80:83], a[56:59], v[144:147]\n" \
"ds_read_b128 a[136:139], v5 offset:34816\n" \
"v_cvt_pk_bf16_f32 v37, v98, v99\n" \
"v_mfma_f32_16x16x32_bf16 v[148:151], a[84:87], a[56:59], v[148:151]\n" \
"s_cbranch_vccz LQW4_%=\nds_write_b128 v21, v[208:211] offset:4096\nLQW4_%=:\n" \
"v_mfma_f32_16x16x32_bf16 v[152:155], a[80:83], a[60:63], v[152:155]\n" \
"v_cvt_pk_bf16_f32 v38, v100, v101\n" \
"v_cvt_pk_bf16_f32 v39, v102, v103\n" \
"v_mfma_f32_16x16x32_bf16 v[156:159], a[84:87], a[60:63], v[156:159]\n" \
"buffer_load_dwordx4 v6, s[40:43], s17 offen offset:1024 sc1 lds\n" \
"v_mfma_f32_16x16x32_bf16 v[160:163], a[40:43], a[0:3], v[160:163]\n" \
"ds_read_b128 a[140:143], v5 offset:35840\n" \
"v_cvt_pk_bf16_f32 v52, v104, v105\n" \
"v_mfma_f32_16x16x32_bf16 v[164:167], a[44:47], a[0:3], v[164:167]\n" \
"s_cbranch_vccz LQW5_%=\nds_write_b128 v21, v[36:39] offset:17408\nLQW5_%=:\n" \
"v_mfma_f32_16x16x32_bf16 v[168:171], a[40:43], a[4:7], v[168:171]\n" \
"v_cvt_pk_bf16_f32 v53, v106, v107\n" \
"v_cvt_pk_bf16_f32 v54, v108, v109\n" \
"v_mfma_f32_16x16x32_bf16 v[172:175], a[44:47], a[4:7], v[172:175]\n" \
"buffer_load_dwordx4 v6, s[40:43], s17 offen offset:2048 sc1 lds\n" \
"v_mfma_f32_16x16x32_bf16 v[176:179], a[40:43], a[8:11], v[176:179]\n" \
"ds_read_b128 a[152:155], v5 offset:38912\n" \
"v_cvt_pk_bf16_f32 v55, v110, v111\n" \
"v_mfma_f32_16x16x32_bf16 v[180:183], a[44:47], a[8:11], v[180:183]\n" \
"s_cbranch_vccz LQW6_%=\nds_write_b128 v21, v[52:55] offset:21504\nLQW6_%=:\n" \
"v_mfma_f32_16x16x32_bf16 v[184:187], a[40:43], a[12:15], v[184:187]\n" \
"v_cvt_pk_bf16_f32 v196, v112, v113\n" \
"v_cvt_pk_bf16_f32 v197, v114, v115\n" \
"v_mfma_f32_16x16x32_bf16 v[188:191], a[44:47], a[12:15], v[188:191]\n" \
"buffer_load_dwordx4 v6, s[40:43], s17 offen offset:3072 sc1 lds\n" \
"v_mfma_f32_16x16x32_bf16 v[160:163], a[88:91], a[48:51], v[160:163]\n" \
"ds_read_b128 a[156:159], v5 offset:39936\n" \
"v_cvt_pk_bf16_f32 v198, v116, v117\n" \
"v_mfma_f32_16x16x32_bf16 v[164:167], a[92:95], a[48:51], v[164:167]\n" \
"v_cvt_pk_bf16_f32 v199, v118, v119\n" \
"v_cvt_pk_bf16_f32 v212, v120, v121\n" \
"v_mfma_f32_16x16x32_bf16 v[168:171], a[88:91], a[52:55], v[168:171]\n" \
"s_cbranch_vccz LQW7_%=\nds_write_b128 v21, v[196:199] offset:1024\nLQW7_%=:\n" \
"v_mfma_f32_16x16x32_bf16 v[172:175], a[92:95], a[52:55], v[172:175]\n" \
"v_cvt_pk_bf16_f32 v213, v122, v123\n" \
"v_cvt_pk_bf16_f32 v214, v124, v125\n" \
"v_mfma_f32_16x16x32_bf16 v[176:179], a[88:91], a[56:59], v[176:179]\n" \
"v_cvt_pk_bf16_f32 v215, v126, v127\n" \
"v_cvt_pk_bf16_f32 v40, v128, v129\n" \
"v_mfma_f32_16x16x32_bf16 v[180:183], a[92:95], a[56:59], v[180:183]\n" \
"s_cbranch_vccz LQW8_%=\nds_write_b128 v21, v[212:215] offset:5120\nLQW8_%=:\n" \
"v_mfma_f32_16x16x32_bf16 v[184:187], a[88:91], a[60:63], v[184:187]\n" \
"v_xor_b32 v5, 0x10000, v5\n" \
"v_cvt_pk_bf16_f32 v41, v130, v131\n" \
"v_mfma_f32_16x16x32_bf16 v[188:191], a[92:95], a[60:63], v[188:191]\n" \
"s_waitcnt lgkmcnt(0)\n" \
"v_cvt_pk_bf16_f32 v42, v132, v133\n" \
"v_cvt_pk_bf16_f32 v43, v134, v135\n" \
"v_mfma_f32_16x16x32_bf16 v[64:67], a[128:131], v[32:35], v[24:27]\n" \
"s_cbranch_vccz LQW9_%=\nds_write_b128 v21, v[40:43] offset:18432\nLQW9_%=:\n" \
"v_mfma_f32_16x16x32_bf16 v[64:67], a[132:135], v[36:39], v[64:67]\n" \
"s_waitcnt vmcnt(16)\n" \
"v_cvt_pk_bf16_f32 v56, v136, v137\n" \
"v_cvt_pk_bf16_f32 v57, v138, v139\n" \
"v_mfma_f32_16x16x32_bf16 v[72:75], a[128:131], v[48:51], 0\n" \
"s_barrier\n" \
"v_cvt_pk_bf16_f32 v58, v140, v141\n" \
"v_cvt_pk_bf16_f32 v59, v142, v143\n" \
"v_mfma_f32_16x16x32_bf16 v[72:75], a[132:135], v[52:55], v[72:75]\n" \
"s_cbranch_vccz LQW10_%=\nds_write_b128 v21, v[56:59] offset:22528\nLQW10_%=:\n" \
"v_mfma_f32_16x16x32_bf16 v[80:83], a[128:131], v[192:195], 0\n" \
"v_cvt_pk_bf16_f32 v200, v144, v145\n" \
"v_cvt_pk_bf16_f32 v201, v146, v147\n" \
"v_mfma_f32_16x16x32_bf16 v[80:83], a[132:135], v[196:199], v[80:83]\n" \
"v_cvt_pk_bf16_f32 v202, v148, v149\n" \
"v_cvt_pk_bf16_f32 v203, v150, v151\n" \
"v_mfma_f32_16x16x32_bf16 v[88:91], a[128:131], v[208:211], 0\n" \
"s_cbranch_vccz LQW11_%=\nds_write_b128 v21, v[200:203] offset:2048\nLQW11_%=:\n" \
"v_mfma_f32_16x16x32_bf16 v[88:91], a[132:135], v[212:215], v[88:91]\n" \
"ds_read_b128 a[128:131], v5 offset:0\n" \
"v_cvt_pk_bf16_f32 v216, v152, v153\n" \
"v_mfma_f32_16x16x32_bf16 v[68:71], a[144:147], v[32:35], v[244:247]\n" \
"ds_read_b128 a[132:135], v5 offset:1024\n" \
"v_cvt_pk_bf16_f32 v217, v154, v155\n" \
"v_mfma_f32_16x16x32_bf16 v[68:71], a[148:151], v[36:39], v[68:71]\n" \
"v_cvt_pk_bf16_f32 v218, v156, v157\n" \
"v_cvt_pk_bf16_f32 v219, v158, v159\n" \
"v_mfma_f32_16x16x32_bf16 v[76:79], a[144:147], v[48:51], v[24:27]\n" \
"s_cbranch_vccz LQW12_%=\nds_write_b128 v21, v[216:219] offset:6144\nLQW12_%=:\n" \
"v_mfma_f32_16x16x32_bf16 v[76:79], a[148:151], v[52:55], v[76:79]\n" \
"v_cvt_pk_bf16_f32 v44, v160, v161\n" \
"v_cvt_pk_bf16_f32 v45, v162, v163\n" \
"v_mfma_f32_16x16x32_bf16 v[84:87], a[144:147], v[192:195], 0\n" \
"v_cvt_pk_bf16_f32 v46, v164, v165\n" \
"v_cvt_pk_bf16_f32 v47, v166, v167\n" \
"v_mfma_f32_16x16x32_bf16 v[84:87], a[148:151], v[196:199], v[84:87]\n" \
"s_cbranch_vccz LQW13_%=\nds_write_b128 v21, v[44:47] offset:19456\nLQW13_%=:\n" \
"v_mfma_f32_16x16x32_bf16 v[92:95], a[144:147], v[208:211], 0\n" \
"v_cvt_pk_bf16_f32 v60, v168, v169\n" \
"v_cvt_pk_bf16_f32 v61, v170, v171\n" \
"v_mfma_f32_16x16x32_bf16 v[92:95], a[148:151], v[212:215], v[92:95]\n" \
"ds_read_b128 a[144:147], v5 offset:4096\n" \
"v_cvt_pk_bf16_f32 v62, v172, v173\n" \
"v_mfma_f32_16x16x32_bf16 v[64:67], a[136:139], v[40:43], v[64:67]\n" \
"ds_read_b128 a[148:151], v5 offset:5120\n" \
"v_cvt_pk_bf16_f32 v63, v174, v175\n" \
"v_mfma_f32_16x16x32_bf16 v[64:67], a[140:143], v[44:47], v[64:67]\n" \
"s_cbranch_vccz LQW14_%=\nds_write_b128 v21, v[60:63] offset:23552\nLQW14_%=:\n" \
"v_mfma_f32_16x16x32_bf16 v[72:75], a[136:139], v[56:59], v[72:75]\n" \
"v_cvt_pk_bf16_f32 v204, v176, v177\n" \
"v_cvt_pk_bf16_f32 v205, v178, v179\n" \
"v_cvt_pk_bf16_f32 v206, v180, v181\n" \
"v_cvt_pk_bf16_f32 v207, v182, v183\n" \
"v_mfma_f32_16x16x32_bf16 v[72:75], a[140:143], v[60:63], v[72:75]\n" \
"LH14_%=:\ns_cbranch_execz LHR0_%=\n" \
"s_cbranch_vccz LQW15_%=\nds_write_b128 v21, v[204:207] offset:3072\nLQW15_%=:\n" \
"v_mfma_f32_16x16x32_bf16 v[80:83], a[136:139], v[200:203], v[80:83]\n" \
"v_cvt_pk_bf16_f32 v220, v184, v185\n" \
"v_cvt_pk_bf16_f32 v221, v186, v187\n" \
"v_cvt_pk_bf16_f32 v222, v188, v189\n" \
"v_cvt_pk_bf16_f32 v223, v190, v191\n" \
"v_mfma_f32_16x16x32_bf16 v[80:83], a[140:143], v[204:207], v[80:83]\n" \
"s_cbranch_vccz LQW16_%=\nds_write_b128 v21, v[220:223] offset:7168\nLQW16_%=:\n" \
"v_mfma_f32_16x16x32_bf16 v[88:91], a[136:139], v[216:219], v[88:91]\n" \
"v_exp_f32 v64, v64\n" \
"v_mfma_f32_16x16x32_bf16 v[88:91], a[140:143], v[220:223], v[88:91]\n" \
"ds_read_b128 a[136:139], v5 offset:2048\n" \
"v_accvgpr_write_b32 a64, v192\n" \
"v_mfma_f32_16x16x32_bf16 v[68:71], a[152:155], v[40:43], v[68:71]\n" \
"ds_read_b128 a[140:143], v5 offset:3072\n" \
"v_accvgpr_write_b32 a65, v193\n" \
"v_mfma_f32_16x16x32_bf16 v[68:71], a[156:159], v[44:47], v[68:71]\n" \
"v_exp_f32 v65, v65\n" \
"v_mfma_f32_16x16x32_bf16 v[76:79], a[152:155], v[56:59], v[76:79]\n" \
"v_exp_f32 v66, v66\n" \
"v_mfma_f32_16x16x32_bf16 v[76:79], a[156:159], v[60:63], v[76:79]\n" \
"v_exp_f32 v67, v67\n" \
"v_mfma_f32_16x16x32_bf16 v[84:87], a[152:155], v[200:203], v[84:87]\n" \
"v_exp_f32 v72, v72\n" \
"v_mfma_f32_16x16x32_bf16 v[84:87], a[156:159], v[204:207], v[84:87]\n" \
"v_exp_f32 v73, v73\n" \
"v_mfma_f32_16x16x32_bf16 v[92:95], a[152:155], v[216:219], v[92:95]\n" \
"v_exp_f32 v74, v74\n" \
"v_mfma_f32_16x16x32_bf16 v[92:95], a[156:159], v[220:223], v[92:95]\n" \
"s_waitcnt lgkmcnt(0)\n" \
"v_exp_f32 v75, v75\n" \
"v_mfma_f32_16x16x32_bf16 v[96:99], a[128:131], v[32:35], v[244:247]\n" \
"v_exp_f32 v80, v80\n" \
"v_mfma_f32_16x16x32_bf16 v[96:99], a[132:135], v[36:39], v[96:99]\n" \
"s_add_u32 s16, s16, s9\n" \
"v_exp_f32 v81, v81\n" \
"v_mfma_f32_16x16x32_bf16 v[104:107], a[128:131], v[48:51], v[244:247]\n" \
"ds_read_b128 a[152:155], v5 offset:6144\n" \
"v_cvt_pk_bf16_f32 v128, v64, v65\n" \
"v_mfma_f32_16x16x32_bf16 v[104:107], a[132:135], v[52:55], v[104:107]\n" \
"s_add_u32 m0, s49, 32768\n" \
"v_exp_f32 v82, v82\n" \
"v_mfma_f32_16x16x32_bf16 v[112:115], a[128:131], v[192:195], v[24:27]\n" \
"buffer_load_dwordx4 v6, s[40:43], s16 offen offset:0 sc1 lds\n" \
"v_mfma_f32_16x16x32_bf16 v[112:115], a[132:135], v[196:199], v[112:115]\n" \
"v_exp_f32 v83, v83\n" \
"v_mfma_f32_16x16x32_bf16 v[120:123], a[128:131], v[208:211], 0\n" \
"ds_read_b128 a[156:159], v5 offset:7168\n" \
"v_cvt_pk_bf16_f32 v129, v66, v67\n" \
"v_mfma_f32_16x16x32_bf16 v[120:123], a[132:135], v[212:215], v[120:123]\n" \
"s_waitcnt lgkmcnt(4)\n" \
"v_exp_f32 v88, v88\n" \
"v_mfma_f32_16x16x32_bf16 v[96:99], a[136:139], v[40:43], v[96:99]\n" \
"v_exp_f32 v89, v89\n" \
"v_mfma_f32_16x16x32_bf16 v[96:99], a[140:143], v[44:47], v[96:99]\n" \
"v_exp_f32 v90, v90\n" \
"v_mfma_f32_16x16x32_bf16 v[104:107], a[136:139], v[56:59], v[104:107]\n" \
"ds_read_b128 a[128:131], v5 offset:16384\n" \
"v_cvt_pk_bf16_f32 v132, v72, v73\n" \
"v_mfma_f32_16x16x32_bf16 v[104:107], a[140:143], v[60:63], v[104:107]\n" \
"v_exp_f32 v91, v91\n" \
"v_mfma_f32_16x16x32_bf16 v[112:115], a[136:139], v[200:203], v[112:115]\n" \
"buffer_load_dwordx4 v6, s[40:43], s16 offen offset:1024 sc1 lds\n" \
"v_mfma_f32_16x16x32_bf16 v[112:115], a[140:143], v[204:207], v[112:115]\n" \
"v_exp_f32 v68, v68\n" \
"v_mfma_f32_16x16x32_bf16 v[120:123], a[136:139], v[216:219], v[120:123]\n" \
"ds_read_b128 a[132:135], v5 offset:17408\n" \
"v_cvt_pk_bf16_f32 v133, v74, v75\n" \
"v_mfma_f32_16x16x32_bf16 v[120:123], a[140:143], v[220:223], v[120:123]\n" \
"s_waitcnt lgkmcnt(4)\n" \
"v_exp_f32 v69, v69\n" \
"v_mfma_f32_16x16x32_bf16 v[100:103], a[144:147], v[32:35], v[244:247]\n" \
"v_exp_f32 v70, v70\n" \
"v_mfma_f32_16x16x32_bf16 v[100:103], a[148:151], v[36:39], v[100:103]\n" \
"v_exp_f32 v71, v71\n" \
"v_mfma_f32_16x16x32_bf16 v[108:111], a[144:147], v[48:51], v[244:247]\n" \
"ds_read_b128 a[136:139], v5 offset:18432\n" \
"v_cvt_pk_bf16_f32 v136, v80, v81\n" \
"v_mfma_f32_16x16x32_bf16 v[108:111], a[148:151], v[52:55], v[108:111]\n" \
"v_exp_f32 v76, v76\n" \
"v_accvgpr_write_b32 a66, v194\n" \
"v_accvgpr_write_b32 a67, v195\n" \
"v_mfma_f32_16x16x32_bf16 v[116:119], a[144:147], v[192:195], v[244:247]\n" \
"buffer_load_dwordx4 v6, s[40:43], s16 offen offset:2048 sc1 lds\n" \
"v_exp_f32 v77, v77\n" \
"v_accvgpr_write_b32 a80, v208\n" \
"v_accvgpr_write_b32 a81, v209\n" \
"v_mfma_f32_16x16x32_bf16 v[116:119], a[148:151], v[196:199], v[116:119]\n" \
"v_exp_f32 v78, v78\n" \
"v_accvgpr_write_b32 a82, v210\n" \
"v_accvgpr_write_b32 a83, v211\n" \
"v_mfma_f32_16x16x32_bf16 v[124:127], a[144:147], v[208:211], v[24:27]\n" \
"ds_read_b128 a[140:143], v5 offset:19456\n" \
"v_cvt_pk_bf16_f32 v137, v82, v83\n" \
"v_accvgpr_write_b32 a68, v196\n" \
"v_accvgpr_write_b32 a69, v197\n" \
"v_mfma_f32_16x16x32_bf16 v[124:127], a[148:151], v[212:215], v[124:127]\n" \
"s_waitcnt lgkmcnt(4)\n" \
"v_exp_f32 v79, v79\n" \
"v_accvgpr_write_b32 a70, v198\n" \
"v_accvgpr_write_b32 a71, v199\n" \
"v_mfma_f32_16x16x32_bf16 v[100:103], a[152:155], v[40:43], v[100:103]\n" \
"v_exp_f32 v84, v84\n" \
"v_accvgpr_write_b32 a84, v212\n" \
"v_accvgpr_write_b32 a85, v213\n" \
"v_mfma_f32_16x16x32_bf16 v[100:103], a[156:159], v[44:47], v[100:103]\n" \
"v_exp_f32 v85, v85\n" \
"v_accvgpr_write_b32 a86, v214\n" \
"v_accvgpr_write_b32 a87, v215\n" \
"v_mfma_f32_16x16x32_bf16 v[108:111], a[152:155], v[56:59], v[108:111]\n" \
"ds_read_b128 a[144:147], v5 offset:20480\n" \
"v_exp_f32 v86, v86\n" \
"v_accvgpr_write_b32 a72, v200\n" \
"v_accvgpr_write_b32 a73, v201\n" \
"v_mfma_f32_16x16x32_bf16 v[108:111], a[156:159], v[60:63], v[108:111]\n" \
"v_exp_f32 v87, v87\n" \
"v_accvgpr_write_b32 a74, v202\n" \
"v_accvgpr_write_b32 a75, v203\n" \
"v_mov_b32 v180, 0x3f803f80\n" \
"v_mov_b32 v181, 0x3f803f80\n" \
"v_mov_b32 v182, 0x3f803f80\n" \
"v_mov_b32 v183, 0x3f803f80\n" \
"v_mfma_f32_16x16x32_bf16 v[116:119], a[152:155], v[200:203], v[116:119]\n" \
"buffer_load_dwordx4 v6, s[40:43], s16 offen offset:3072 sc1 lds\n" \
"v_accvgpr_write_b32 a88, v216\n" \
"v_accvgpr_write_b32 a89, v217\n" \
"v_mfma_f32_16x16x32_bf16 v[116:119], a[156:159], v[204:207], v[116:119]\n" \
"v_exp_f32 v92, v92\n" \
"v_accvgpr_write_b32 a90, v218\n" \
"v_accvgpr_write_b32 a91, v219\n" \
"v_mov_b32 v176, v240\n" \
"v_mov_b32 v177, v241\n" \
"v_mov_b32 v178, v242\n" \
"v_mov_b32 v179, v243\n" \
"v_mfma_f32_16x16x32_bf16 v[124:127], a[152:155], v[216:219], v[124:127]\n" \
"ds_read_b128 a[148:151], v5 offset:21504\n" \
"v_exp_f32 v93, v93\n" \
"v_cvt_pk_bf16_f32 v140, v88, v89\n" \
"v_cvt_pk_bf16_f32 v141, v90, v91\n" \
"v_cvt_pk_bf16_f32 v130, v68, v69\n" \
"v_cvt_pk_bf16_f32 v131, v70, v71\n" \
"v_accvgpr_write_b32 a76, v204\n" \
"v_accvgpr_write_b32 a77, v205\n" \
"v_mfma_f32_16x16x32_bf16 v[124:127], a[156:159], v[220:223], v[124:127]\n" \
"s_waitcnt lgkmcnt(4)\n" \
"v_exp_f32 v94, v94\n" \
"v_cvt_pk_bf16_f32 v134, v76, v77\n" \
"v_cvt_pk_bf16_f32 v135, v78, v79\n" \
"v_accvgpr_write_b32 a78, v206\n" \
"v_accvgpr_write_b32 a79, v207\n" \
"v_mfma_f32_16x16x32_bf16 v[160:163], v[180:183], v[128:131], 0\n" \
"v_exp_f32 v95, v95\n" \
"v_cvt_pk_bf16_f32 v138, v84, v85\n" \
"v_cvt_pk_bf16_f32 v139, v86, v87\n" \
"v_accvgpr_write_b32 a92, v220\n" \
"v_accvgpr_write_b32 a93, v221\n" \
"v_mfma_f32_16x16x32_bf16 v[164:167], v[180:183], v[132:135], 0\n" \
"v_cvt_pk_bf16_f32 v142, v92, v93\n" \
"v_cvt_pk_bf16_f32 v143, v94, v95\n" \
"v_accvgpr_write_b32 a94, v222\n" \
"v_accvgpr_write_b32 a95, v223\n" \
"v_mfma_f32_16x16x32_bf16 v[168:171], v[180:183], v[136:139], 0\n" \
"v_exp_f32 v96, v96\n" \
"v_mfma_f32_16x16x32_bf16 v[172:175], v[180:183], v[140:143], 0\n" \
"v_lshrrev_b32 v9, 5, v4\n" \
"v_lshlrev_b32 v9, 27, v9\n" \
"v_mfma_f32_16x16x32_bf16 v[192:195], a[128:131], v[128:131], 0\n" \
"v_exp_f32 v97, v97\n" \
"v_mfma_f32_16x16x32_bf16 v[196:199], a[132:135], v[128:131], 0\n" \
"v_cvt_pk_bf16_f32 v144, v96, v97\n" \
"v_lshl_add_u32 v9, v4, 4, v9\n" \
"v_mfma_f32_16x16x32_bf16 v[224:227], a[128:131], v[132:135], 0\n" \
"ds_read_b128 a[152:155], v5 offset:22528\n" \
"v_mov_b32 v10, s14\n" \
"v_mfma_f32_16x16x32_bf16 v[228:231], a[132:135], v[132:135], 0\n" \
"v_exp_f32 v98, v98\n" \
"v_mfma_f32_16x16x32_bf16 a[0:3], a[128:131], v[136:139], 0\n" \
"v_exp_f32 v99, v99\n" \
"v_mfma_f32_16x16x32_bf16 a[4:7], a[132:135], v[136:139], 0\n" \
"v_cvt_pk_bf16_f32 v145, v98, v99\n" \
"v_mov_b32 v11, s15\n" \
"v_mfma_f32_16x16x32_bf16 a[32:35], a[128:131], v[140:143], 0\n" \
"ds_read_b128 a[156:159], v5 offset:23552\n" \
"s_waitcnt lgkmcnt(12)\n" \
"v_mfma_f32_16x16x32_bf16 a[36:39], a[132:135], v[140:143], 0\n" \
"s_waitcnt vmcnt(12) lgkmcnt(4)\n" \
"v_exp_f32 v100, v100\n" \
"v_mfma_f32_16x16x32_bf16 v[200:203], a[136:139], v[128:131], 0\n" \
"s_barrier\n" \
"v_exp_f32 v101, v101\n" \
"v_mfma_f32_16x16x32_bf16 v[204:207], a[140:143], v[128:131], 0\n" \
"s_add_u32 s17, s17, s19\n" \
"v_cvt_pk_bf16_f32 v146, v100, v101\n" \
"v_mfma_f32_16x16x32_bf16 v[232:235], a[136:139], v[132:135], 0\n" \
"ds_read_b128 a[128:131], v5 offset:32768\n" \
"v_mfma_f32_16x16x32_bf16 v[236:239], a[140:143], v[132:135], 0\n" \
"s_add_u32 m0, s49, 49152\n" \
"v_exp_f32 v102, v102\n" \
"v_mfma_f32_16x16x32_bf16 a[8:11], a[136:139], v[136:139], 0\n" \
"buffer_load_dwordx4 v6, s[40:43], s17 offen offset:0 sc1 lds\n" \
"v_mfma_f32_16x16x32_bf16 a[12:15], a[140:143], v[136:139], 0\n" \
"v_exp_f32 v103, v103\n" \
"v_mfma_f32_16x16x32_bf16 a[40:43], a[136:139], v[140:143], 0\n" \
"ds_read_b128 a[132:135], v5 offset:33792\n" \
"v_cvt_pk_bf16_f32 v147, v102, v103\n" \
"v_mfma_f32_16x16x32_bf16 a[44:47], a[140:143], v[140:143], 0\n" \
"s_waitcnt lgkmcnt(4)\n" \
"v_exp_f32 v104, v104\n" \
"v_mfma_f32_16x16x32_bf16 v[208:211], a[144:147], v[128:131], 0\n" \
"v_exp_f32 v105, v105\n" \
"v_mfma_f32_16x16x32_bf16 v[212:215], a[148:151], v[128:131], 0\n" \
"v_cvt_pk_bf16_f32 v148, v104, v105\n" \
"v_mfma_f32_16x16x32_bf16 v[240:243], a[144:147], v[132:135], 0\n" \
"ds_read_b128 a[136:139], v5 offset:34816\n" \
"v_mfma_f32_16x16x32_bf16 v[244:247], a[148:151], v[132:135], 0\n" \
"v_exp_f32 v106, v106\n" \
"v_mfma_f32_16x16x32_bf16 a[16:19], a[144:147], v[136:139], 0\n" \
"buffer_load_dwordx4 v6, s[40:43], s17 offen offset:1024 sc1 lds\n" \
"v_mfma_f32_16x16x32_bf16 a[20:23], a[148:151], v[136:139], 0\n" \
"v_exp_f32 v107, v107\n" \
"v_mfma_f32_16x16x32_bf16 a[48:51], a[144:147], v[140:143], 0\n" \
"ds_read_b128 a[140:143], v5 offset:35840\n" \
"v_cvt_pk_bf16_f32 v149, v106, v107\n" \
"v_mfma_f32_16x16x32_bf16 a[52:55], a[148:151], v[140:143], 0\n" \
"s_waitcnt lgkmcnt(4)\n" \
"v_exp_f32 v108, v108\n" \
"v_mfma_f32_16x16x32_bf16 v[216:219], a[152:155], v[128:131], 0\n" \
"buffer_load_dwordx4 v6, s[40:43], s17 offen offset:2048 sc1 lds\n" \
"v_mfma_f32_16x16x32_bf16 v[220:223], a[156:159], v[128:131], 0\n" \
"v_exp_f32 v109, v109\n" \
"v_mfma_f32_16x16x32_bf16 v[248:251], a[152:155], v[132:135], 0\n" \
"ds_read_b128 a[144:147], v5 offset:36864\n" \
"v_cvt_pk_bf16_f32 v150, v108, v109\n" \
"v_mfma_f32_16x16x32_bf16 v[252:255], a[156:159], v[132:135], 0\n" \
"v_exp_f32 v110, v110\n" \
"v_mfma_f32_16x16x32_bf16 a[24:27], a[152:155], v[136:139], 0\n" \
"buffer_load_dwordx4 v6, s[40:43], s17 offen offset:3072 sc1 lds\n" \
"v_mfma_f32_16x16x32_bf16 a[28:31], a[156:159], v[136:139], 0\n" \
"v_exp_f32 v111, v111\n" \
"v_mfma_f32_16x16x32_bf16 a[56:59], a[152:155], v[140:143], 0\n" \
"ds_read_b128 a[148:151], v5 offset:37888\n" \
"v_cvt_pk_bf16_f32 v151, v110, v111\n" \
"v_mfma_f32_16x16x32_bf16 a[60:63], a[156:159], v[140:143], 0\n" \
"s_waitcnt lgkmcnt(4)\n" \
"s_cmp_lt_i32 s46, 0\n" \
"s_cbranch_scc1 LAD2_%=\n" \
"LCHK_%=:\n" \
"s_cmp_eq_u32 s44, s46\n" \
"s_cbranch_scc1 LMID_%=\n" \
"s_nop 0\n" \
"LODD_%=:\n" \
"v_mfma_f32_16x16x32_bf16 v[64:67], a[128:131], v[32:35], 0\n" \
"v_exp_f32 v112, v112\n" \
"v_mfma_f32_16x16x32_bf16 v[64:67], a[132:135], v[36:39], v[64:67]\n" \
"s_add_u32 s16, s16, s9\n" \
"v_mfma_f32_16x16x32_bf16 v[72:75], a[128:131], v[48:51], 0\n" \
"ds_read_b128 a[152:155], v5 offset:38912\n" \
"v_mfma_f32_16x16x32_bf16 v[72:75], a[132:135], v[52:55], v[72:75]\n" \
"s_add_u32 m0, s48, 0\n" \
"v_exp_f32 v113, v113\n" \
"v_mfma_f32_16x16x32_bf16 v[80:83], a[128:131], a[64:67], 0\n" \
"buffer_load_dwordx4 v6, s[40:43], s16 offen offset:0 sc1 lds\n" \
"v_mfma_f32_16x16x32_bf16 v[80:83], a[132:135], a[68:71], v[80:83]\n" \
"v_exp_f32 v114, v114\n" \
"v_mfma_f32_16x16x32_bf16 v[88:91], a[128:131], a[80:83], 0\n" \
"ds_read_b128 a[156:159], v5 offset:39936\n" \
"v_cvt_pk_bf16_f32 v152, v112, v113\n" \
"v_mfma_f32_16x16x32_bf16 v[88:91], a[132:135], a[84:87], v[88:91]\n" \
"s_waitcnt lgkmcnt(4)\n" \
"v_exp_f32 v115, v115\n" \
"v_mfma_f32_16x16x32_bf16 v[64:67], a[136:139], v[40:43], v[64:67]\n" \
"v_exp_f32 v116, v116\n" \
"v_mfma_f32_16x16x32_bf16 v[64:67], a[140:143], v[44:47], v[64:67]\n" \
"s_xor_b32 s49, s49, 0x10000\n" \
"v_cvt_pk_bf16_f32 v153, v114, v115\n" \
"v_mfma_f32_16x16x32_bf16 v[72:75], a[136:139], v[56:59], v[72:75]\n" \
"ds_read_b128 a[128:131], v5 offset:49152\n" \
"v_mfma_f32_16x16x32_bf16 v[72:75], a[140:143], v[60:63], v[72:75]\n" \
"v_exp_f32 v117, v117\n" \
"v_mfma_f32_16x16x32_bf16 v[80:83], a[136:139], a[72:75], v[80:83]\n" \
"buffer_load_dwordx4 v6, s[40:43], s16 offen offset:1024 sc1 lds\n" \
"v_mfma_f32_16x16x32_bf16 v[80:83], a[140:143], a[76:79], v[80:83]\n" \
"v_exp_f32 v118, v118\n" \
"v_mfma_f32_16x16x32_bf16 v[88:91], a[136:139], a[88:91], v[88:91]\n" \
"ds_read_b128 a[132:135], v5 offset:50176\n" \
"v_cvt_pk_bf16_f32 v154, v116, v117\n" \
"v_mfma_f32_16x16x32_bf16 v[88:91], a[140:143], a[92:95], v[88:91]\n" \
"s_waitcnt lgkmcnt(4)\n" \
"v_exp_f32 v119, v119\n" \
"v_mfma_f32_16x16x32_bf16 v[68:71], a[144:147], v[32:35], 0\n" \
"v_exp_f32 v120, v120\n" \
"v_mfma_f32_16x16x32_bf16 v[68:71], a[148:151], v[36:39], v[68:71]\n" \
"v_cvt_pk_bf16_f32 v155, v118, v119\n" \
"v_mfma_f32_16x16x32_bf16 v[76:79], a[144:147], v[48:51], 0\n" \
"ds_read_b128 a[136:139], v5 offset:51200\n" \
"v_mfma_f32_16x16x32_bf16 v[76:79], a[148:151], v[52:55], v[76:79]\n" \
"v_exp_f32 v121, v121\n" \
"v_mfma_f32_16x16x32_bf16 v[84:87], a[144:147], a[64:67], 0\n" \
"buffer_load_dwordx4 v6, s[40:43], s16 offen offset:2048 sc1 lds\n" \
"v_mfma_f32_16x16x32_bf16 v[84:87], a[148:151], a[68:71], v[84:87]\n" \
"v_exp_f32 v122, v122\n" \
"v_mfma_f32_16x16x32_bf16 v[92:95], a[144:147], a[80:83], 0\n" \
"ds_read_b128 a[140:143], v5 offset:52224\n" \
"v_cvt_pk_bf16_f32 v156, v120, v121\n" \
"v_mfma_f32_16x16x32_bf16 v[92:95], a[148:151], a[84:87], v[92:95]\n" \
"s_waitcnt lgkmcnt(4)\n" \
"v_exp_f32 v123, v123\n" \
"v_mfma_f32_16x16x32_bf16 v[68:71], a[152:155], v[40:43], v[68:71]\n" \
"v_exp_f32 v124, v124\n" \
"v_mfma_f32_16x16x32_bf16 v[68:71], a[156:159], v[44:47], v[68:71]\n" \
"v_cvt_pk_bf16_f32 v157, v122, v123\n" \
"v_mfma_f32_16x16x32_bf16 v[76:79], a[152:155], v[56:59], v[76:79]\n" \
"ds_read_b128 a[144:147], v5 offset:53248\n" \
"v_mfma_f32_16x16x32_bf16 v[76:79], a[156:159], v[60:63], v[76:79]\n" \
"v_exp_f32 v125, v125\n" \
"v_mfma_f32_16x16x32_bf16 v[84:87], a[152:155], a[72:75], v[84:87]\n" \
"buffer_load_dwordx4 v6, s[40:43], s16 offen offset:3072 sc1 lds\n" \
"v_mfma_f32_16x16x32_bf16 v[84:87], a[156:159], a[76:79], v[84:87]\n" \
"v_exp_f32 v126, v126\n" \
"v_mfma_f32_16x16x32_bf16 v[92:95], a[152:155], a[88:91], v[92:95]\n" \
"ds_read_b128 a[148:151], v5 offset:54272\n" \
"v_cvt_pk_bf16_f32 v158, v124, v125\n" \
"v_mfma_f32_16x16x32_bf16 v[92:95], a[156:159], a[92:95], v[92:95]\n" \
"s_waitcnt lgkmcnt(4)\n" \
"v_exp_f32 v127, v127\n" \
"v_mfma_f32_16x16x32_bf16 v[160:163], v[180:183], v[144:147], v[160:163]\n" \
"v_cvt_pk_bf16_f32 v159, v126, v127\n" \
"v_mfma_f32_16x16x32_bf16 v[164:167], v[180:183], v[148:151], v[164:167]\n" \
"v_mfma_f32_16x16x32_bf16 v[168:171], v[180:183], v[152:155], v[168:171]\n" \
"v_exp_f32 v64, v64\n" \
"v_mfma_f32_16x16x32_bf16 v[172:175], v[180:183], v[156:159], v[172:175]\n" \
"v_mfma_f32_16x16x32_bf16 v[192:195], a[128:131], v[144:147], v[192:195]\n" \
"v_exp_f32 v65, v65\n" \
"v_mfma_f32_16x16x32_bf16 v[196:199], a[132:135], v[144:147], v[196:199]\n" \
"v_cvt_pk_bf16_f32 v128, v64, v65\n" \
"v_mfma_f32_16x16x32_bf16 v[224:227], a[128:131], v[148:151], v[224:227]\n" \
"ds_read_b128 a[152:155], v5 offset:55296\n" \
"v_mfma_f32_16x16x32_bf16 v[228:231], a[132:135], v[148:151], v[228:231]\n" \
"v_exp_f32 v66, v66\n" \
"v_mfma_f32_16x16x32_bf16 a[0:3], a[128:131], v[152:155], a[0:3]\n" \
"v_exp_f32 v67, v67\n" \
"v_mfma_f32_16x16x32_bf16 a[4:7], a[132:135], v[152:155], a[4:7]\n" \
"v_cvt_pk_bf16_f32 v129, v66, v67\n" \
"v_mfma_f32_16x16x32_bf16 a[32:35], a[128:131], v[156:159], a[32:35]\n" \
"ds_read_b128 a[156:159], v5 offset:56320\n" \
"v_mfma_f32_16x16x32_bf16 a[36:39], a[132:135], v[156:159], a[36:39]\n" \
"s_waitcnt vmcnt(12) lgkmcnt(4)\n" \
"v_exp_f32 v68, v68\n" \
"v_mfma_f32_16x16x32_bf16 v[200:203], a[136:139], v[144:147], v[200:203]\n" \
"s_barrier\n" \
"v_xor_b32 v5, 0x10000, v5\n" \
"v_mfma_f32_16x16x32_bf16 v[204:207], a[140:143], v[144:147], v[204:207]\n" \
"s_add_u32 s17, s17, s19\n" \
"v_exp_f32 v69, v69\n" \
"v_mfma_f32_16x16x32_bf16 v[232:235], a[136:139], v[148:151], v[232:235]\n" \
"ds_read_b128 a[128:131], v5 offset:0\n" \
"v_cvt_pk_bf16_f32 v130, v68, v69\n" \
"v_mfma_f32_16x16x32_bf16 v[236:239], a[140:143], v[148:151], v[236:239]\n" \
"s_add_u32 m0, s48, 16384\n" \
"v_exp_f32 v70, v70\n" \
"v_mfma_f32_16x16x32_bf16 a[8:11], a[136:139], v[152:155], a[8:11]\n" \
"buffer_load_dwordx4 v6, s[40:43], s17 offen offset:0 sc1 lds\n" \
"v_mfma_f32_16x16x32_bf16 a[12:15], a[140:143], v[152:155], a[12:15]\n" \
"v_exp_f32 v71, v71\n" \
"v_mfma_f32_16x16x32_bf16 a[40:43], a[136:139], v[156:159], a[40:43]\n" \
"ds_read_b128 a[132:135], v5 offset:1024\n" \
"v_cvt_pk_bf16_f32 v131, v70, v71\n" \
"v_mfma_f32_16x16x32_bf16 a[44:47], a[140:143], v[156:159], a[44:47]\n" \
"s_waitcnt lgkmcnt(4)\n" \
"v_exp_f32 v72, v72\n" \
"v_mfma_f32_16x16x32_bf16 v[208:211], a[144:147], v[144:147], v[208:211]\n" \
"v_exp_f32 v73, v73\n" \
"v_mfma_f32_16x16x32_bf16 v[212:215], a[148:151], v[144:147], v[212:215]\n" \
"v_cvt_pk_bf16_f32 v132, v72, v73\n" \
"v_mfma_f32_16x16x32_bf16 v[240:243], a[144:147], v[148:151], v[240:243]\n" \
"ds_read_b128 a[136:139], v5 offset:2048\n" \
"v_mfma_f32_16x16x32_bf16 v[244:247], a[148:151], v[148:151], v[244:247]\n" \
"v_exp_f32 v74, v74\n" \
"v_mfma_f32_16x16x32_bf16 a[16:19], a[144:147], v[152:155], a[16:19]\n" \
"buffer_load_dwordx4 v6, s[40:43], s17 offen offset:1024 sc1 lds\n" \
"v_mfma_f32_16x16x32_bf16 a[20:23], a[148:151], v[152:155], a[20:23]\n" \
"v_exp_f32 v75, v75\n" \
"v_mfma_f32_16x16x32_bf16 a[48:51], a[144:147], v[156:159], a[48:51]\n" \
"ds_read_b128 a[140:143], v5 offset:3072\n" \
"v_cvt_pk_bf16_f32 v133, v74, v75\n" \
"v_mfma_f32_16x16x32_bf16 a[52:55], a[148:151], v[156:159], a[52:55]\n" \
"s_waitcnt lgkmcnt(4)\n" \
"v_exp_f32 v76, v76\n" \
"v_mfma_f32_16x16x32_bf16 v[216:219], a[152:155], v[144:147], v[216:219]\n" \
"buffer_load_dwordx4 v6, s[40:43], s17 offen offset:2048 sc1 lds\n" \
"v_mfma_f32_16x16x32_bf16 v[220:223], a[156:159], v[144:147], v[220:223]\n" \
"s_add_u32 s44, s44, 1\n" \
"v_exp_f32 v77, v77\n" \
"v_mfma_f32_16x16x32_bf16 v[248:251], a[152:155], v[148:151], v[248:251]\n" \
"ds_read_b128 a[144:147], v5 offset:4096\n" \
"v_cvt_pk_bf16_f32 v134, v76, v77\n" \
"v_mfma_f32_16x16x32_bf16 v[252:255], a[156:159], v[148:151], v[252:255]\n" \
"v_exp_f32 v78, v78\n" \
"v_mfma_f32_16x16x32_bf16 a[24:27], a[152:155], v[152:155], a[24:27]\n" \
"buffer_load_dwordx4 v6, s[40:43], s17 offen offset:3072 sc1 lds\n" \
"v_mfma_f32_16x16x32_bf16 a[28:31], a[156:159], v[152:155], a[28:31]\n" \
"s_xor_b32 s48, s48, 0x10000\n" \
"v_exp_f32 v79, v79\n" \
"v_mfma_f32_16x16x32_bf16 a[56:59], a[152:155], v[156:159], a[56:59]\n" \
"ds_read_b128 a[148:151], v5 offset:5120\n" \
"v_cvt_pk_bf16_f32 v135, v78, v79\n" \
"v_mfma_f32_16x16x32_bf16 a[60:63], a[156:159], v[156:159], a[60:63]\n" \
"s_waitcnt lgkmcnt(4)\n" \
"LBOT_%=:\n" \
"s_cmp_ge_u32 s44, 14\n" \
"s_cbranch_scc1 LDISP_%=\n" \
"LEVEN_%=:\n" \
"v_mfma_f32_16x16x32_bf16 v[96:99], a[128:131], v[32:35], 0\n" \
"v_exp_f32 v80, v80\n" \
"v_mfma_f32_16x16x32_bf16 v[96:99], a[132:135], v[36:39], v[96:99]\n" \
"s_add_u32 s16, s16, s9\n" \
"v_mfma_f32_16x16x32_bf16 v[104:107], a[128:131], v[48:51], 0\n" \
"ds_read_b128 a[152:155], v5 offset:6144\n" \
"v_mfma_f32_16x16x32_bf16 v[104:107], a[132:135], v[52:55], v[104:107]\n" \
"s_add_u32 m0, s49, 32768\n" \
"v_exp_f32 v81, v81\n" \
"v_mfma_f32_16x16x32_bf16 v[112:115], a[128:131], a[64:67], 0\n" \
"buffer_load_dwordx4 v6, s[40:43], s16 offen offset:0 sc1 lds\n" \
"v_mfma_f32_16x16x32_bf16 v[112:115], a[132:135], a[68:71], v[112:115]\n" \
"v_exp_f32 v82, v82\n" \
"v_mfma_f32_16x16x32_bf16 v[120:123], a[128:131], a[80:83], 0\n" \
"ds_read_b128 a[156:159], v5 offset:7168\n" \
"v_cvt_pk_bf16_f32 v136, v80, v81\n" \
"v_mfma_f32_16x16x32_bf16 v[120:123], a[132:135], a[84:87], v[120:123]\n" \
"s_waitcnt lgkmcnt(4)\n" \
"v_exp_f32 v83, v83\n" \
"v_mfma_f32_16x16x32_bf16 v[96:99], a[136:139], v[40:43], v[96:99]\n" \
"v_exp_f32 v84, v84\n" \
"v_mfma_f32_16x16x32_bf16 v[96:99], a[140:143], v[44:47], v[96:99]\n" \
"v_cvt_pk_bf16_f32 v137, v82, v83\n" \
"v_mfma_f32_16x16x32_bf16 v[104:107], a[136:139], v[56:59], v[104:107]\n" \
"ds_read_b128 a[128:131], v5 offset:16384\n" \
"v_mfma_f32_16x16x32_bf16 v[104:107], a[140:143], v[60:63], v[104:107]\n" \
"v_exp_f32 v85, v85\n" \
"v_mfma_f32_16x16x32_bf16 v[112:115], a[136:139], a[72:75], v[112:115]\n" \
"buffer_load_dwordx4 v6, s[40:43], s16 offen offset:1024 sc1 lds\n" \
"v_mfma_f32_16x16x32_bf16 v[112:115], a[140:143], a[76:79], v[112:115]\n" \
"v_exp_f32 v86, v86\n" \
"v_mfma_f32_16x16x32_bf16 v[120:123], a[136:139], a[88:91], v[120:123]\n" \
"ds_read_b128 a[132:135], v5 offset:17408\n" \
"v_cvt_pk_bf16_f32 v138, v84, v85\n" \
"v_mfma_f32_16x16x32_bf16 v[120:123], a[140:143], a[92:95], v[120:123]\n" \
"s_waitcnt lgkmcnt(4)\n" \
"v_exp_f32 v87, v87\n" \
"v_mfma_f32_16x16x32_bf16 v[100:103], a[144:147], v[32:35], 0\n" \
"v_exp_f32 v88, v88\n" \
"v_mfma_f32_16x16x32_bf16 v[100:103], a[148:151], v[36:39], v[100:103]\n" \
"v_cvt_pk_bf16_f32 v139, v86, v87\n" \
"v_mfma_f32_16x16x32_bf16 v[108:111], a[144:147], v[48:51], 0\n" \
"ds_read_b128 a[136:139], v5 offset:18432\n" \
"v_mfma_f32_16x16x32_bf16 v[108:111], a[148:151], v[52:55], v[108:111]\n" \
"v_exp_f32 v89, v89\n" \
"v_mfma_f32_16x16x32_bf16 v[116:119], a[144:147], a[64:67], 0\n" \
"buffer_load_dwordx4 v6, s[40:43], s16 offen offset:2048 sc1 lds\n" \
"v_mfma_f32_16x16x32_bf16 v[116:119], a[148:151], a[68:71], v[116:119]\n" \
"v_exp_f32 v90, v90\n" \
"v_mfma_f32_16x16x32_bf16 v[124:127], a[144:147], a[80:83], 0\n" \
"ds_read_b128 a[140:143], v5 offset:19456\n" \
"v_cvt_pk_bf16_f32 v140, v88, v89\n" \
"v_mfma_f32_16x16x32_bf16 v[124:127], a[148:151], a[84:87], v[124:127]\n" \
"s_waitcnt lgkmcnt(4)\n" \
"v_exp_f32 v91, v91\n" \
"v_mfma_f32_16x16x32_bf16 v[100:103], a[152:155], v[40:43], v[100:103]\n" \
"v_exp_f32 v92, v92\n" \
"v_mfma_f32_16x16x32_bf16 v[100:103], a[156:159], v[44:47], v[100:103]\n" \
"v_cvt_pk_bf16_f32 v141, v90, v91\n" \
"v_mfma_f32_16x16x32_bf16 v[108:111], a[152:155], v[56:59], v[108:111]\n" \
"ds_read_b128 a[144:147], v5 offset:20480\n" \
"v_mfma_f32_16x16x32_bf16 v[108:111], a[156:159], v[60:63], v[108:111]\n" \
"v_exp_f32 v93, v93\n" \
"v_mfma_f32_16x16x32_bf16 v[116:119], a[152:155], a[72:75], v[116:119]\n" \
"buffer_load_dwordx4 v6, s[40:43], s16 offen offset:3072 sc1 lds\n" \
"v_mfma_f32_16x16x32_bf16 v[116:119], a[156:159], a[76:79], v[116:119]\n" \
"v_exp_f32 v94, v94\n" \
"v_mfma_f32_16x16x32_bf16 v[124:127], a[152:155], a[88:91], v[124:127]\n" \
"ds_read_b128 a[148:151], v5 offset:21504\n" \
"v_cvt_pk_bf16_f32 v142, v92, v93\n" \
"v_mfma_f32_16x16x32_bf16 v[124:127], a[156:159], a[92:95], v[124:127]\n" \
"s_waitcnt lgkmcnt(4)\n" \
"v_exp_f32 v95, v95\n" \
"v_mfma_f32_16x16x32_bf16 v[160:163], v[180:183], v[128:131], v[160:163]\n" \
"v_cvt_pk_bf16_f32 v143, v94, v95\n" \
"v_mfma_f32_16x16x32_bf16 v[164:167], v[180:183], v[132:135], v[164:167]\n" \
"v_mfma_f32_16x16x32_bf16 v[168:171], v[180:183], v[136:139], v[168:171]\n" \
"v_exp_f32 v96, v96\n" \
"v_mfma_f32_16x16x32_bf16 v[172:175], v[180:183], v[140:143], v[172:175]\n" \
"v_mfma_f32_16x16x32_bf16 v[192:195], a[128:131], v[128:131], v[192:195]\n" \
"v_exp_f32 v97, v97\n" \
"v_mfma_f32_16x16x32_bf16 v[196:199], a[132:135], v[128:131], v[196:199]\n" \
"v_cvt_pk_bf16_f32 v144, v96, v97\n" \
"v_mfma_f32_16x16x32_bf16 v[224:227], a[128:131], v[132:135], v[224:227]\n" \
"ds_read_b128 a[152:155], v5 offset:22528\n" \
"v_mfma_f32_16x16x32_bf16 v[228:231], a[132:135], v[132:135], v[228:231]\n" \
"v_exp_f32 v98, v98\n" \
"v_mfma_f32_16x16x32_bf16 a[0:3], a[128:131], v[136:139], a[0:3]\n" \
"v_exp_f32 v99, v99\n" \
"v_mfma_f32_16x16x32_bf16 a[4:7], a[132:135], v[136:139], a[4:7]\n" \
"v_cvt_pk_bf16_f32 v145, v98, v99\n" \
"v_mfma_f32_16x16x32_bf16 a[32:35], a[128:131], v[140:143], a[32:35]\n" \
"ds_read_b128 a[156:159], v5 offset:23552\n" \
"v_mfma_f32_16x16x32_bf16 a[36:39], a[132:135], v[140:143], a[36:39]\n" \
"s_waitcnt vmcnt(12) lgkmcnt(4)\n" \
"v_exp_f32 v100, v100\n" \
"v_mfma_f32_16x16x32_bf16 v[200:203], a[136:139], v[128:131], v[200:203]\n" \
"s_barrier\n" \
"v_exp_f32 v101, v101\n" \
"v_mfma_f32_16x16x32_bf16 v[204:207], a[140:143], v[128:131], v[204:207]\n" \
"s_add_u32 s17, s17, s19\n" \
"v_cvt_pk_bf16_f32 v146, v100, v101\n" \
"v_mfma_f32_16x16x32_bf16 v[232:235], a[136:139], v[132:135], v[232:235]\n" \
"ds_read_b128 a[128:131], v5 offset:32768\n" \
"v_mfma_f32_16x16x32_bf16 v[236:239], a[140:143], v[132:135], v[236:239]\n" \
"s_add_u32 m0, s49, 49152\n" \
"v_exp_f32 v102, v102\n" \
"v_mfma_f32_16x16x32_bf16 a[8:11], a[136:139], v[136:139], a[8:11]\n" \
"buffer_load_dwordx4 v6, s[40:43], s17 offen offset:0 sc1 lds\n" \
"v_mfma_f32_16x16x32_bf16 a[12:15], a[140:143], v[136:139], a[12:15]\n" \
"v_exp_f32 v103, v103\n" \
"v_mfma_f32_16x16x32_bf16 a[40:43], a[136:139], v[140:143], a[40:43]\n" \
"ds_read_b128 a[132:135], v5 offset:33792\n" \
"v_cvt_pk_bf16_f32 v147, v102, v103\n" \
"v_mfma_f32_16x16x32_bf16 a[44:47], a[140:143], v[140:143], a[44:47]\n" \
"s_waitcnt lgkmcnt(4)\n" \
"v_exp_f32 v104, v104\n" \
"v_mfma_f32_16x16x32_bf16 v[208:211], a[144:147], v[128:131], v[208:211]\n" \
"v_exp_f32 v105, v105\n" \
"v_mfma_f32_16x16x32_bf16 v[212:215], a[148:151], v[128:131], v[212:215]\n" \
"v_cvt_pk_bf16_f32 v148, v104, v105\n" \
"v_mfma_f32_16x16x32_bf16 v[240:243], a[144:147], v[132:135], v[240:243]\n" \
"ds_read_b128 a[136:139], v5 offset:34816\n" \
"v_mfma_f32_16x16x32_bf16 v[244:247], a[148:151], v[132:135], v[244:247]\n" \
"v_exp_f32 v106, v106\n" \
"v_mfma_f32_16x16x32_bf16 a[16:19], a[144:147], v[136:139], a[16:19]\n" \
"buffer_load_dwordx4 v6, s[40:43], s17 offen offset:1024 sc1 lds\n" \
"v_mfma_f32_16x16x32_bf16 a[20:23], a[148:151], v[136:139], a[20:23]\n" \
"v_exp_f32 v107, v107\n" \
"v_mfma_f32_16x16x32_bf16 a[48:51], a[144:147], v[140:143], a[48:51]\n" \
"ds_read_b128 a[140:143], v5 offset:35840\n" \
"v_cvt_pk_bf16_f32 v149, v106, v107\n" \
"v_mfma_f32_16x16x32_bf16 a[52:55], a[148:151], v[140:143], a[52:55]\n" \
"s_waitcnt lgkmcnt(4)\n" \
"v_exp_f32 v108, v108\n" \
"v_mfma_f32_16x16x32_bf16 v[216:219], a[152:155], v[128:131], v[216:219]\n" \
"buffer_load_dwordx4 v6, s[40:43], s17 offen offset:2048 sc1 lds\n" \
"v_mfma_f32_16x16x32_bf16 v[220:223], a[156:159], v[128:131], v[220:223]\n" \
"v_exp_f32 v109, v109\n" \
"v_mfma_f32_16x16x32_bf16 v[248:251], a[152:155], v[132:135], v[248:251]\n" \
"ds_read_b128 a[144:147], v5 offset:36864\n" \
"v_cvt_pk_bf16_f32 v150, v108, v109\n" \
"v_mfma_f32_16x16x32_bf16 v[252:255], a[156:159], v[132:135], v[252:255]\n" \
"v_exp_f32 v110, v110\n" \
"v_mfma_f32_16x16x32_bf16 a[24:27], a[152:155], v[136:139], a[24:27]\n" \
"buffer_load_dwordx4 v6, s[40:43], s17 offen offset:3072 sc1 lds\n" \
"v_mfma_f32_16x16x32_bf16 a[28:31], a[156:159], v[136:139], a[28:31]\n" \
"v_exp_f32 v111, v111\n" \
"v_mfma_f32_16x16x32_bf16 a[56:59], a[152:155], v[140:143], a[56:59]\n" \
"ds_read_b128 a[148:151], v5 offset:37888\n" \
"v_cvt_pk_bf16_f32 v151, v110, v111\n" \
"v_mfma_f32_16x16x32_bf16 a[60:63], a[156:159], v[140:143], a[60:63]\n" \
"s_waitcnt lgkmcnt(4)\n" \
"s_cmp_lg_u32 s44, s46\n" \
"s_cbranch_scc1 LODD_%=\n" \
"LMID_%=:\n" \
"s_cmp_eq_u32 s44, s45\n" \
"s_cbranch_scc1 LWS_%=\n" \
"s_cmp_lt_u32 %2, 2\n" \
"s_cbranch_scc1 LS1_%=\n" \
"s_cmp_eq_u32 s44, 15\n" \
"s_cbranch_scc1 LS1_%=\n" \
"s_lshl_b32 s46, s7, 2\n" \
"s_add_u32 s46, s46, %2\n" \
"s_waitcnt vmcnt(8)\n" \
"v_cmp_ne_u64 vcc, s[14:15], v[18:19]\n" \
"s_cmp_eq_u32 s46, 30\ns_cselect_b32 s46, 15, 99\n" \
"s_cbranch_vccnz LPE0_%=\nLPE0R_%=:\n" \
"s_branch LODD_%=\n" \
".p2align 6\n" \
"LH15_%=:\ns_cbranch_execz LHR0_%=\n" \
"s_nop 0\n" \
"LWS_%=:\n" \
"v_mfma_f32_16x16x32_bf16 v[64:67], a[128:131], v[32:35], 0\n" \
"s_mov_b32 s81, 0\n" \
"v_exp_f32 v112, v112\n" \
"v_mfma_f32_16x16x32_bf16 v[64:67], a[132:135], v[36:39], v[64:67]\n" \
"s_add_u32 s16, s16, s9\n" \
"v_mov_b32 v12, v30\n" \
"v_mov_b32 v28, -1\n" \
"v_mfma_f32_16x16x32_bf16 v[72:75], a[128:131], v[48:51], 0\n" \
"ds_read_b128 a[152:155], v5 offset:38912\n" \
"v_mov_b32 v29, -1\n" \
"v_mfma_f32_16x16x32_bf16 v[72:75], a[132:135], v[52:55], v[72:75]\n" \
"s_add_u32 m0, s48, 0\n" \
"v_exp_f32 v113, v113\n" \
"v_mfma_f32_16x16x32_bf16 v[80:83], a[128:131], a[64:67], 0\n" \
"buffer_load_dwordx4 v6, s[40:43], s16 offen offset:0 sc1 lds\n" \
"v_mfma_f32_16x16x32_bf16 v[80:83], a[132:135], a[68:71], v[80:83]\n" \
"v_exp_f32 v114, v114\n" \
"v_mfma_f32_16x16x32_bf16 v[88:91], a[128:131], a[80:83], 0\n" \
"ds_read_b128 a[156:159], v5 offset:39936\n" \
"v_cvt_pk_bf16_f32 v152, v112, v113\n" \
"v_mfma_f32_16x16x32_bf16 v[88:91], a[132:135], a[84:87], v[88:91]\n" \
"LH16_%=:\ns_cbranch_execz LH20_%=\n" \
"s_waitcnt lgkmcnt(4)\n" \
"v_exp_f32 v115, v115\n" \
"s_add_u32 m0, s79, 131072\n" \
"v_mfma_f32_16x16x32_bf16 v[64:67], a[136:139], v[40:43], v[64:67]\n" \
"buffer_load_dwordx4 v12, s[28:31], s81 offen offset:0 lds\n" \
"v_mfma_f32_16x16x32_bf16 v[64:67], a[140:143], v[44:47], v[64:67]\n" \
"s_xor_b32 s49, s49, 0x10000\n" \
"v_exp_f32 v116, v116\n" \
"v_mfma_f32_16x16x32_bf16 v[72:75], a[136:139], v[56:59], v[72:75]\n" \
"ds_read_b128 a[128:131], v5 offset:49152\n" \
"v_cvt_pk_bf16_f32 v153, v114, v115\n" \
"v_mfma_f32_16x16x32_bf16 v[72:75], a[140:143], v[60:63], v[72:75]\n" \
"s_add_u32 m0, s48, 0\n" \
"v_exp_f32 v117, v117\n" \
"v_mfma_f32_16x16x32_bf16 v[80:83], a[136:139], a[72:75], v[80:83]\n" \
"buffer_load_dwordx4 v6, s[40:43], s16 offen offset:1024 sc1 lds\n" \
"v_mfma_f32_16x16x32_bf16 v[80:83], a[140:143], a[76:79], v[80:83]\n" \
"v_exp_f32 v118, v118\n" \
"v_mfma_f32_16x16x32_bf16 v[88:91], a[136:139], a[88:91], v[88:91]\n" \
"ds_read_b128 a[132:135], v5 offset:50176\n" \
"v_cvt_pk_bf16_f32 v154, v116, v117\n" \
"v_mfma_f32_16x16x32_bf16 v[88:91], a[140:143], a[92:95], v[88:91]\n" \
"s_waitcnt lgkmcnt(4)\n" \
"v_exp_f32 v119, v119\n" \
"s_add_u32 m0, s79, 131968\n" \
"v_mfma_f32_16x16x32_bf16 v[68:71], a[144:147], v[32:35], 0\n" \
"buffer_load_dwordx4 v12, s[28:31], s81 offen offset:128 lds\n" \
"v_mfma_f32_16x16x32_bf16 v[68:71], a[148:151], v[36:39], v[68:71]\n" \
"s_mov_b32 s81, 65536\n" \
"v_exp_f32 v120, v120\n" \
"v_mfma_f32_16x16x32_bf16 v[76:79], a[144:147], v[48:51], 0\n" \
"ds_read_b128 a[136:139], v5 offset:51200\n" \
"v_cvt_pk_bf16_f32 v155, v118, v119\n" \
"v_mfma_f32_16x16x32_bf16 v[76:79], a[148:151], v[52:55], v[76:79]\n" \
"s_add_u32 m0, s48, 0\n" \
"v_exp_f32 v121, v121\n" \
"v_mfma_f32_16x16x32_bf16 v[84:87], a[144:147], a[64:67], 0\n" \
"buffer_load_dwordx4 v6, s[40:43], s16 offen offset:2048 sc1 lds\n" \
"v_mfma_f32_16x16x32_bf16 v[84:87], a[148:151], a[68:71], v[84:87]\n" \
"v_exp_f32 v122, v122\n" \
"v_mfma_f32_16x16x32_bf16 v[92:95], a[144:147], a[80:83], 0\n" \
"ds_read_b128 a[140:143], v5 offset:52224\n" \
"v_cvt_pk_bf16_f32 v156, v120, v121\n" \
"v_mfma_f32_16x16x32_bf16 v[92:95], a[148:151], a[84:87], v[92:95]\n" \
"s_waitcnt lgkmcnt(4)\n" \
"v_exp_f32 v123, v123\n" \
"s_add_u32 m0, s79, 139264\n" \
"v_mfma_f32_16x16x32_bf16 v[68:71], a[152:155], v[40:43], v[68:71]\n" \
"buffer_load_dwordx4 v12, s[28:31], s81 offen offset:0 lds\n" \
"v_mfma_f32_16x16x32_bf16 v[68:71], a[156:159], v[44:47], v[68:71]\n" \
"v_exp_f32 v124, v124\n" \
"v_mfma_f32_16x16x32_bf16 v[76:79], a[152:155], v[56:59], v[76:79]\n" \
"ds_read_b128 a[144:147], v5 offset:53248\n" \
"v_cvt_pk_bf16_f32 v157, v122, v123\n" \
"v_mfma_f32_16x16x32_bf16 v[76:79], a[156:159], v[60:63], v[76:79]\n" \
"s_add_u32 m0, s48, 0\n" \
"v_exp_f32 v125, v125\n" \
"v_mfma_f32_16x16x32_bf16 v[84:87], a[152:155], a[72:75], v[84:87]\n" \
"buffer_load_dwordx4 v6, s[40:43], s16 offen offset:3072 sc1 lds\n" \
"v_mfma_f32_16x16x32_bf16 v[84:87], a[156:159], a[76:79], v[84:87]\n" \
"v_exp_f32 v126, v126\n" \
"v_mfma_f32_16x16x32_bf16 v[92:95], a[152:155], a[88:91], v[92:95]\n" \
"ds_read_b128 a[148:151], v5 offset:54272\n" \
"v_cvt_pk_bf16_f32 v158, v124, v125\n" \
"v_mfma_f32_16x16x32_bf16 v[92:95], a[156:159], a[92:95], v[92:95]\n" \
"s_waitcnt lgkmcnt(4)\n" \
"v_exp_f32 v127, v127\n" \
"s_add_u32 m0, s79, 140160\n" \
"v_mfma_f32_16x16x32_bf16 v[160:163], v[180:183], v[144:147], v[160:163]\n" \
"buffer_load_dwordx4 v12, s[28:31], s81 offen offset:128 lds\n" \
"v_mfma_f32_16x16x32_bf16 v[164:167], v[180:183], v[148:151], v[164:167]\n" \
"s_mov_b32 s81, 131072\n" \
"v_cvt_pk_bf16_f32 v159, v126, v127\n" \
"v_mov_b32 v30, -1\n" \
"v_mfma_f32_16x16x32_bf16 v[168:171], v[180:183], v[152:155], v[168:171]\n" \
"v_exp_f32 v64, v64\n" \
"v_mfma_f32_16x16x32_bf16 v[172:175], v[180:183], v[156:159], v[172:175]\n" \
"s_add_u32 s80, s78, 4096\n" \
"v_mov_b32 v31, -1\n" \
"v_bfe_u32 v189, v4, 7, 1\n" \
"v_mfma_f32_16x16x32_bf16 v[192:195], a[128:131], v[144:147], v[192:195]\n" \
"buffer_store_dwordx4 v[28:31], v4, s[36:39], s80 offen offset:2048 sc1\n" \
"v_mfma_f32_16x16x32_bf16 v[196:199], a[132:135], v[144:147], v[196:199]\n" \
"v_bfe_u32 v190, v4, 4, 3\n" \
"v_lshl_add_u32 v189, v190, 1, v189\n" \
"v_mfma_f32_16x16x32_bf16 v[224:227], a[128:131], v[148:151], v[224:227]\n" \
"ds_read_b128 a[152:155], v5 offset:55296\n" \
"v_lshrrev_b32 v190, 8, v4\n" \
"v_mfma_f32_16x16x32_bf16 v[228:231], a[132:135], v[148:151], v[228:231]\n" \
"v_exp_f32 v65, v65\n" \
"v_mfma_f32_16x16x32_bf16 a[0:3], a[128:131], v[152:155], a[0:3]\n" \
"v_exp_f32 v66, v66\n" \
"v_mfma_f32_16x16x32_bf16 a[4:7], a[132:135], v[152:155], a[4:7]\n" \
"v_exp_f32 v67, v67\n" \
"v_mfma_f32_16x16x32_bf16 a[32:35], a[128:131], v[156:159], a[32:35]\n" \
"ds_read_b128 a[156:159], v5 offset:56320\n" \
"v_lshlrev_b32 v188, 11, v189\n" \
"v_mfma_f32_16x16x32_bf16 a[36:39], a[132:135], v[156:159], a[36:39]\n" \
"s_waitcnt vmcnt(17) lgkmcnt(4)\n" \
"v_exp_f32 v68, v68\n" \
"v_mfma_f32_16x16x32_bf16 v[200:203], a[136:139], v[144:147], v[200:203]\n" \
"s_barrier\n" \
"v_xor_b32 v5, 0x10000, v5\n" \
"v_and_b32 v189, 1, v190\n" \
"v_mfma_f32_16x16x32_bf16 v[204:207], a[140:143], v[144:147], v[204:207]\n" \
"s_add_u32 s17, s17, s19\n" \
"v_exp_f32 v69, v69\n" \
"v_mfma_f32_16x16x32_bf16 v[232:235], a[136:139], v[148:151], v[232:235]\n" \
"ds_read_b128 a[128:131], v5 offset:0\n" \
"v_lshl_add_u32 v188, v189, 5, v188\n" \
"v_mfma_f32_16x16x32_bf16 v[236:239], a[140:143], v[148:151], v[236:239]\n" \
"s_add_u32 m0, s48, 16384\n" \
"v_exp_f32 v70, v70\n" \
"v_mfma_f32_16x16x32_bf16 a[8:11], a[136:139], v[152:155], a[8:11]\n" \
"buffer_load_dwordx4 v6, s[40:43], s17 offen offset:0 sc1 lds\n" \
"v_mfma_f32_16x16x32_bf16 a[12:15], a[140:143], v[152:155], a[12:15]\n" \
"v_exp_f32 v71, v71\n" \
"v_mfma_f32_16x16x32_bf16 a[40:43], a[136:139], v[156:159], a[40:43]\n" \
"ds_read_b128 a[132:135], v5 offset:1024\n" \
"v_lshrrev_b32 v189, 1, v190\n" \
"v_mfma_f32_16x16x32_bf16 a[44:47], a[140:143], v[156:159], a[44:47]\n" \
"s_waitcnt lgkmcnt(4)\n" \
"v_exp_f32 v72, v72\n" \
"v_mfma_f32_16x16x32_bf16 v[208:211], a[144:147], v[144:147], v[208:211]\n" \
"v_exp_f32 v73, v73\n" \
"v_mfma_f32_16x16x32_bf16 v[212:215], a[148:151], v[144:147], v[212:215]\n" \
"v_mfma_f32_16x16x32_bf16 v[240:243], a[144:147], v[148:151], v[240:243]\n" \
"ds_read_b128 a[136:139], v5 offset:2048\n" \
"v_lshl_add_u32 v188, v189, 4, v188\n" \
"v_mfma_f32_16x16x32_bf16 v[244:247], a[148:151], v[148:151], v[244:247]\n" \
"s_add_u32 m0, s48, 16384\n" \
"v_exp_f32 v74, v74\n" \
"v_mfma_f32_16x16x32_bf16 a[16:19], a[144:147], v[152:155], a[16:19]\n" \
"buffer_load_dwordx4 v6, s[40:43], s17 offen offset:1024 sc1 lds\n" \
"v_mfma_f32_16x16x32_bf16 a[20:23], a[148:151], v[152:155], a[20:23]\n" \
"v_exp_f32 v75, v75\n" \
"v_mfma_f32_16x16x32_bf16 a[48:51], a[144:147], v[156:159], a[48:51]\n" \
"ds_read_b128 a[140:143], v5 offset:3072\n" \
"v_add_u32 v190, s83, v188\n" \
"v_mfma_f32_16x16x32_bf16 a[52:55], a[148:151], v[156:159], a[52:55]\n" \
"s_waitcnt lgkmcnt(4)\n" \
"s_add_u32 m0, s48, 16384\n" \
"v_add_u32 v188, s82, v188\n" \
"v_mfma_f32_16x16x32_bf16 v[216:219], a[152:155], v[144:147], v[216:219]\n" \
"buffer_load_dwordx4 v6, s[40:43], s17 offen offset:2048 sc1 lds\n" \
"v_mfma_f32_16x16x32_bf16 v[220:223], a[156:159], v[144:147], v[220:223]\n" \
"s_add_u32 s44, s44, 1\n" \
"v_exp_f32 v76, v76\n" \
"v_mfma_f32_16x16x32_bf16 v[248:251], a[152:155], v[148:151], v[248:251]\n" \
"ds_read_b128 a[144:147], v5 offset:4096\n" \
"v_add_u32 v189, 0x8000, v188\n" \
"v_mfma_f32_16x16x32_bf16 v[252:255], a[156:159], v[148:151], v[252:255]\n" \
"s_add_u32 m0, s48, 16384\n" \
"v_exp_f32 v77, v77\n" \
"v_mfma_f32_16x16x32_bf16 a[24:27], a[152:155], v[152:155], a[24:27]\n" \
"buffer_load_dwordx4 v6, s[40:43], s17 offen offset:3072 sc1 lds\n" \
"v_mfma_f32_16x16x32_bf16 a[28:31], a[156:159], v[152:155], a[28:31]\n" \
"s_xor_b32 s48, s48, 0x10000\n" \
"v_exp_f32 v78, v78\n" \
"v_mfma_f32_16x16x32_bf16 a[56:59], a[152:155], v[156:159], a[56:59]\n" \
"ds_read_b128 a[148:151], v5 offset:5120\n" \
"v_add_u32 v191, 0x8000, v190\n" \
"v_exp_f32 v79, v79\n" \
"v_mfma_f32_16x16x32_bf16 a[60:63], a[156:159], v[156:159], a[60:63]\n" \
"s_waitcnt lgkmcnt(4)\n" \
"v_cvt_pk_bf16_f32 v128, v64, v65\n" \
"v_cvt_pk_bf16_f32 v129, v66, v67\n" \
"v_cvt_pk_bf16_f32 v130, v68, v69\n" \
"v_cvt_pk_bf16_f32 v131, v70, v71\n" \
"v_cvt_pk_bf16_f32 v132, v72, v73\n" \
"v_cvt_pk_bf16_f32 v133, v74, v75\n" \
"v_cvt_pk_bf16_f32 v134, v76, v77\n" \
"v_cvt_pk_bf16_f32 v135, v78, v79\n" \
"v_mfma_f32_16x16x32_bf16 v[96:99], a[128:131], v[32:35], 0\n" \
"global_load_dwordx4 a[96:99], v188, s[60:61]\n" \
"v_mfma_f32_16x16x32_bf16 v[96:99], a[132:135], v[36:39], v[96:99]\n" \
"s_add_u32 s16, s16, s9\n" \
"v_exp_f32 v80, v80\n" \
"v_mfma_f32_16x16x32_bf16 v[104:107], a[128:131], v[48:51], 0\n" \
"ds_read_b128 a[152:155], v5 offset:6144\n" \
"v_mfma_f32_16x16x32_bf16 v[104:107], a[132:135], v[52:55], v[104:107]\n" \
"s_add_u32 m0, s49, 32768\n" \
"v_exp_f32 v81, v81\n" \
"v_mfma_f32_16x16x32_bf16 v[112:115], a[128:131], a[64:67], 0\n" \
"buffer_load_dwordx4 v6, s[40:43], s16 offen offset:0 sc1 lds\n" \
"v_mfma_f32_16x16x32_bf16 v[112:115], a[132:135], a[68:71], v[112:115]\n" \
"v_exp_f32 v82, v82\n" \
"v_mfma_f32_16x16x32_bf16 v[120:123], a[128:131], a[80:83], 0\n" \
"ds_read_b128 a[156:159], v5 offset:7168\n" \
"v_cvt_pk_bf16_f32 v136, v80, v81\n" \
"v_mfma_f32_16x16x32_bf16 v[120:123], a[132:135], a[84:87], v[120:123]\n" \
"s_waitcnt lgkmcnt(4)\n" \
"v_exp_f32 v83, v83\n" \
"s_add_u32 m0, s79, 147456\n" \
"v_mfma_f32_16x16x32_bf16 v[96:99], a[136:139], v[40:43], v[96:99]\n" \
"buffer_load_dwordx4 v12, s[28:31], s81 offen offset:0 lds\n" \
"v_mfma_f32_16x16x32_bf16 v[96:99], a[140:143], v[44:47], v[96:99]\n" \
"v_exp_f32 v84, v84\n" \
"v_mfma_f32_16x16x32_bf16 v[104:107], a[136:139], v[56:59], v[104:107]\n" \
"ds_read_b128 a[128:131], v5 offset:16384\n" \
"v_cvt_pk_bf16_f32 v137, v82, v83\n" \
"v_mfma_f32_16x16x32_bf16 v[104:107], a[140:143], v[60:63], v[104:107]\n" \
"s_add_u32 m0, s49, 32768\n" \
"v_exp_f32 v85, v85\n" \
"v_mfma_f32_16x16x32_bf16 v[112:115], a[136:139], a[72:75], v[112:115]\n" \
"buffer_load_dwordx4 v6, s[40:43], s16 offen offset:1024 sc1 lds\n" \
"v_mfma_f32_16x16x32_bf16 v[112:115], a[140:143], a[76:79], v[112:115]\n" \
"v_exp_f32 v86, v86\n" \
"v_mfma_f32_16x16x32_bf16 v[120:123], a[136:139], a[88:91], v[120:123]\n" \
"ds_read_b128 a[132:135], v5 offset:17408\n" \
"v_cvt_pk_bf16_f32 v138, v84, v85\n" \
"v_mfma_f32_16x16x32_bf16 v[120:123], a[140:143], a[92:95], v[120:123]\n" \
"s_waitcnt lgkmcnt(4)\n" \
"v_exp_f32 v87, v87\n" \
"s_add_u32 m0, s79, 148352\n" \
"v_mfma_f32_16x16x32_bf16 v[100:103], a[144:147], v[32:35], 0\n" \
"buffer_load_dwordx4 v12, s[28:31], s81 offen offset:128 lds\n" \
"v_mfma_f32_16x16x32_bf16 v[100:103], a[148:151], v[36:39], v[100:103]\n" \
"s_mov_b32 s81, 196608\n" \
"v_exp_f32 v88, v88\n" \
"v_mfma_f32_16x16x32_bf16 v[108:111], a[144:147], v[48:51], 0\n" \
"ds_read_b128 a[136:139], v5 offset:18432\n" \
"v_cvt_pk_bf16_f32 v139, v86, v87\n" \
"v_mfma_f32_16x16x32_bf16 v[108:111], a[148:151], v[52:55], v[108:111]\n" \
"s_add_u32 m0, s49, 32768\n" \
"v_exp_f32 v89, v89\n" \
"v_mfma_f32_16x16x32_bf16 v[116:119], a[144:147], a[64:67], 0\n" \
"buffer_load_dwordx4 v6, s[40:43], s16 offen offset:2048 sc1 lds\n" \
"v_mfma_f32_16x16x32_bf16 v[116:119], a[148:151], a[68:71], v[116:119]\n" \
"v_exp_f32 v90, v90\n" \
"v_mfma_f32_16x16x32_bf16 v[124:127], a[144:147], a[80:83], 0\n" \
"ds_read_b128 a[140:143], v5 offset:19456\n" \
"v_cvt_pk_bf16_f32 v140, v88, v89\n" \
"v_mfma_f32_16x16x32_bf16 v[124:127], a[148:151], a[84:87], v[124:127]\n" \
"s_waitcnt lgkmcnt(4)\n" \
"v_exp_f32 v91, v91\n" \
"s_add_u32 m0, s79, 155648\n" \
"v_mfma_f32_16x16x32_bf16 v[100:103], a[152:155], v[40:43], v[100:103]\n" \
"buffer_load_dwordx4 v12, s[28:31], s81 offen offset:0 lds\n" \
"v_mfma_f32_16x16x32_bf16 v[100:103], a[156:159], v[44:47], v[100:103]\n" \
"v_exp_f32 v92, v92\n" \
"v_mfma_f32_16x16x32_bf16 v[108:111], a[152:155], v[56:59], v[108:111]\n" \
"ds_read_b128 a[144:147], v5 offset:20480\n" \
"v_cvt_pk_bf16_f32 v141, v90, v91\n" \
"v_mfma_f32_16x16x32_bf16 v[108:111], a[156:159], v[60:63], v[108:111]\n" \
"s_add_u32 m0, s49, 32768\n" \
"v_exp_f32 v93, v93\n" \
"v_mfma_f32_16x16x32_bf16 v[116:119], a[152:155], a[72:75], v[116:119]\n" \
"buffer_load_dwordx4 v6, s[40:43], s16 offen offset:3072 sc1 lds\n" \
"v_mfma_f32_16x16x32_bf16 v[116:119], a[156:159], a[76:79], v[116:119]\n" \
"v_exp_f32 v94, v94\n" \
"v_mfma_f32_16x16x32_bf16 v[124:127], a[152:155], a[88:91], v[124:127]\n" \
"ds_read_b128 a[148:151], v5 offset:21504\n" \
"v_cvt_pk_bf16_f32 v142, v92, v93\n" \
"v_mfma_f32_16x16x32_bf16 v[124:127], a[156:159], a[92:95], v[124:127]\n" \
"s_waitcnt lgkmcnt(4)\n" \
"v_exp_f32 v95, v95\n" \
"s_add_u32 m0, s79, 156544\n" \
"v_mfma_f32_16x16x32_bf16 v[160:163], v[180:183], v[128:131], v[160:163]\n" \
"buffer_load_dwordx4 v12, s[28:31], s81 offen offset:128 lds\n" \
"v_mfma_f32_16x16x32_bf16 v[164:167], v[180:183], v[132:135], v[164:167]\n" \
"s_mov_b32 s81, 262144\n" \
"v_cvt_pk_bf16_f32 v143, v94, v95\n" \
"v_mfma_f32_16x16x32_bf16 v[168:171], v[180:183], v[136:139], v[168:171]\n" \
"buffer_store_dwordx4 v[28:31], v4, s[36:39], s80 offen offset:3072 sc1\n" \
"v_mfma_f32_16x16x32_bf16 v[172:175], v[180:183], v[140:143], v[172:175]\n" \
"s_add_u32 s80, s78, 8192\n" \
"v_exp_f32 v96, v96\n" \
"v_mfma_f32_16x16x32_bf16 v[192:195], a[128:131], v[128:131], v[192:195]\n" \
"global_load_dwordx4 a[100:103], v189, s[60:61]\n" \
"v_mfma_f32_16x16x32_bf16 v[196:199], a[132:135], v[128:131], v[196:199]\n" \
"v_exp_f32 v97, v97\n" \
"v_mfma_f32_16x16x32_bf16 v[224:227], a[128:131], v[132:135], v[224:227]\n" \
"ds_read_b128 a[152:155], v5 offset:22528\n" \
"v_cvt_pk_bf16_f32 v144, v96, v97\n" \
"v_mfma_f32_16x16x32_bf16 v[228:231], a[132:135], v[132:135], v[228:231]\n" \
"buffer_store_dwordx4 v[28:31], v4, s[36:39], s80 offen offset:0 sc1\n" \
"v_mfma_f32_16x16x32_bf16 a[0:3], a[128:131], v[136:139], a[0:3]\n" \
"v_exp_f32 v98, v98\n" \
"v_mfma_f32_16x16x32_bf16 a[4:7], a[132:135], v[136:139], a[4:7]\n" \
"global_load_dwordx4 a[104:107], v190, s[60:61]\n" \
"v_mfma_f32_16x16x32_bf16 a[32:35], a[128:131], v[140:143], a[32:35]\n" \
"ds_read_b128 a[156:159], v5 offset:23552\n" \
"v_mfma_f32_16x16x32_bf16 a[36:39], a[132:135], v[140:143], a[36:39]\n" \
"s_waitcnt vmcnt(18) lgkmcnt(4)\n" \
"v_exp_f32 v99, v99\n" \
"v_mfma_f32_16x16x32_bf16 v[200:203], a[136:139], v[128:131], v[200:203]\n" \
"s_barrier\n" \
"v_exp_f32 v100, v100\n" \
"v_mfma_f32_16x16x32_bf16 v[204:207], a[140:143], v[128:131], v[204:207]\n" \
"s_add_u32 s17, s17, s19\n" \
"v_exp_f32 v101, v101\n" \
"v_mfma_f32_16x16x32_bf16 v[232:235], a[136:139], v[132:135], v[232:235]\n" \
"ds_read_b128 a[128:131], v5 offset:32768\n" \
"v_cvt_pk_bf16_f32 v145, v98, v99\n" \
"v_mfma_f32_16x16x32_bf16 v[236:239], a[140:143], v[132:135], v[236:239]\n" \
"s_add_u32 m0, s49, 49152\n" \
"v_exp_f32 v102, v102\n" \
"v_mfma_f32_16x16x32_bf16 a[8:11], a[136:139], v[136:139], a[8:11]\n" \
"buffer_load_dwordx4 v6, s[40:43], s17 offen offset:0 sc1 lds\n" \
"v_mfma_f32_16x16x32_bf16 a[12:15], a[140:143], v[136:139], a[12:15]\n" \
"v_exp_f32 v103, v103\n" \
"v_mfma_f32_16x16x32_bf16 a[40:43], a[136:139], v[140:143], a[40:43]\n" \
"ds_read_b128 a[132:135], v5 offset:33792\n" \
"v_cvt_pk_bf16_f32 v146, v100, v101\n" \
"v_mfma_f32_16x16x32_bf16 a[44:47], a[140:143], v[140:143], a[44:47]\n" \
"s_waitcnt lgkmcnt(4)\n" \
"v_exp_f32 v104, v104\n" \
"v_mfma_f32_16x16x32_bf16 v[208:211], a[144:147], v[128:131], v[208:211]\n" \
"v_exp_f32 v105, v105\n" \
"v_mfma_f32_16x16x32_bf16 v[212:215], a[148:151], v[128:131], v[212:215]\n" \
"v_mfma_f32_16x16x32_bf16 v[240:243], a[144:147], v[132:135], v[240:243]\n" \
"ds_read_b128 a[136:139], v5 offset:34816\n" \
"v_cvt_pk_bf16_f32 v147, v102, v103\n" \
"v_mfma_f32_16x16x32_bf16 v[244:247], a[148:151], v[132:135], v[244:247]\n" \
"s_add_u32 m0, s49, 49152\n" \
"v_exp_f32 v106, v106\n" \
"v_mfma_f32_16x16x32_bf16 a[16:19], a[144:147], v[136:139], a[16:19]\n" \
"buffer_load_dwordx4 v6, s[40:43], s17 offen offset:1024 sc1 lds\n" \
"v_mfma_f32_16x16x32_bf16 a[20:23], a[148:151], v[136:139], a[20:23]\n" \
"v_exp_f32 v107, v107\n" \
"v_mfma_f32_16x16x32_bf16 a[48:51], a[144:147], v[140:143], a[48:51]\n" \
"ds_read_b128 a[140:143], v5 offset:35840\n" \
"v_cvt_pk_bf16_f32 v148, v104, v105\n" \
"v_mfma_f32_16x16x32_bf16 a[52:55], a[148:151], v[140:143], a[52:55]\n" \
"s_waitcnt lgkmcnt(4)\n" \
"s_add_u32 m0, s49, 49152\n" \
"ds_read_b64_tr_b16 a[160:161], v13 offset:0\n" \
"v_mfma_f32_16x16x32_bf16 v[216:219], a[152:155], v[128:131], v[216:219]\n" \
"buffer_load_dwordx4 v6, s[40:43], s17 offen offset:2048 sc1 lds\n" \
"v_mfma_f32_16x16x32_bf16 v[220:223], a[156:159], v[128:131], v[220:223]\n" \
"v_exp_f32 v108, v108\n" \
"v_mfma_f32_16x16x32_bf16 v[248:251], a[152:155], v[132:135], v[248:251]\n" \
"ds_read_b128 a[144:147], v5 offset:36864\n" \
"v_cvt_pk_bf16_f32 v149, v106, v107\n" \
"v_mfma_f32_16x16x32_bf16 v[252:255], a[156:159], v[132:135], v[252:255]\n" \
"s_add_u32 m0, s49, 49152\n" \
"v_exp_f32 v109, v109\n" \
"v_mfma_f32_16x16x32_bf16 a[24:27], a[152:155], v[136:139], a[24:27]\n" \
"buffer_load_dwordx4 v6, s[40:43], s17 offen offset:3072 sc1 lds\n" \
"v_mfma_f32_16x16x32_bf16 a[28:31], a[156:159], v[136:139], a[28:31]\n" \
"v_exp_f32 v110, v110\n" \
"v_mfma_f32_16x16x32_bf16 a[56:59], a[152:155], v[140:143], a[56:59]\n" \
"ds_read_b128 a[148:151], v5 offset:37888\n" \
"v_exp_f32 v111, v111\n" \
"v_mfma_f32_16x16x32_bf16 a[60:63], a[156:159], v[140:143], a[60:63]\n" \
"s_waitcnt lgkmcnt(4)\n" \
"ds_read_b64_tr_b16 a[162:163], v14 offset:0\n" \
"v_cvt_pk_bf16_f32 v150, v108, v109\n" \
"v_cvt_pk_bf16_f32 v151, v110, v111\n" \
"v_mfma_f32_16x16x32_bf16 v[64:67], a[128:131], v[32:35], 0\n" \
"global_load_dwordx4 a[108:111], v191, s[60:61]\n" \
"v_mfma_f32_16x16x32_bf16 v[64:67], a[132:135], v[36:39], v[64:67]\n" \
"s_add_u32 s16, s16, s9\n" \
"ds_read_b64_tr_b16 a[164:165], v15 offset:0\n" \
"s_lshl_b32 s92, %2, 9\n" \
"v_mfma_f32_16x16x32_bf16 v[72:75], a[128:131], v[48:51], 0\n" \
"s_add_u32 s92, s92, 94375936\n" \
"ds_read_b128 a[152:155], v5 offset:38912\n" \
"v_lshrrev_b32 v17, 1, v4\n" \
"v_mfma_f32_16x16x32_bf16 v[72:75], a[132:135], v[52:55], v[72:75]\n" \
"s_add_u32 m0, s48, 0\n" \
"ds_read_b64_tr_b16 a[166:167], v16 offset:0\n" \
"v_add_u32 v17, s92, v17\n" \
"v_mfma_f32_16x16x32_bf16 v[80:83], a[128:131], a[64:67], 0\n" \
"buffer_load_dwordx4 v6, s[40:43], s16 offen offset:0 sc1 lds\n" \
"v_mfma_f32_16x16x32_bf16 v[80:83], a[132:135], a[68:71], v[80:83]\n" \
"s_mov_b64 s[4:5], 0\n" \
"ds_read_b64_tr_b16 a[168:169], v13 offset:8192\n" \
"v_mfma_f32_16x16x32_bf16 v[88:91], a[128:131], a[80:83], 0\n" \
"s_add_u32 s100, s100, 262144\ns_addc_u32 s101, s101, 0\n" \
"ds_read_b128 a[156:159], v5 offset:39936\n" \
"v_mfma_f32_16x16x32_bf16 v[88:91], a[132:135], a[84:87], v[88:91]\n" \
"s_waitcnt lgkmcnt(4)\n" \
"v_exp_f32 v112, v112\n" \
"v_mfma_f32_16x16x32_bf16 v[64:67], a[136:139], v[40:43], v[64:67]\n" \
"s_mov_b64 exec, 0xffff\nglobal_load_dword v1, v2, s[100:101]\ns_mov_b64 exec, -1\n" \
"v_mfma_f32_16x16x32_bf16 v[64:67], a[140:143], v[44:47], v[64:67]\n" \
"s_xor_b32 s49, s49, 0x10000\n" \
"v_exp_f32 v113, v113\n" \
"v_mfma_f32_16x16x32_bf16 v[72:75], a[136:139], v[56:59], v[72:75]\n" \
"ds_read_b128 a[128:131], v5 offset:49152\n" \
"v_cvt_pk_bf16_f32 v152, v112, v113\n" \
"v_mfma_f32_16x16x32_bf16 v[72:75], a[140:143], v[60:63], v[72:75]\n" \
"s_add_u32 m0, s48, 0\n" \
"v_exp_f32 v114, v114\n" \
"v_mfma_f32_16x16x32_bf16 v[80:83], a[136:139], a[72:75], v[80:83]\n" \
"buffer_load_dwordx4 v6, s[40:43], s16 offen offset:1024 sc1 lds\n" \
"v_mfma_f32_16x16x32_bf16 v[80:83], a[140:143], a[76:79], v[80:83]\n" \
"v_exp_f32 v115, v115\n" \
"v_mfma_f32_16x16x32_bf16 v[88:91], a[136:139], a[88:91], v[88:91]\n" \
"ds_read_b128 a[132:135], v5 offset:50176\n" \
"v_cvt_pk_bf16_f32 v153, v114, v115\n" \
"v_mfma_f32_16x16x32_bf16 v[88:91], a[140:143], a[92:95], v[88:91]\n" \
"s_waitcnt lgkmcnt(4)\n" \
"v_exp_f32 v116, v116\n" \
"v_mfma_f32_16x16x32_bf16 v[68:71], a[144:147], v[32:35], 0\n" \
"v_exp_f32 v117, v117\n" \
"v_mfma_f32_16x16x32_bf16 v[68:71], a[148:151], v[36:39], v[68:71]\n" \
"v_exp_f32 v118, v118\n" \
"v_mfma_f32_16x16x32_bf16 v[76:79], a[144:147], v[48:51], 0\n" \
"ds_read_b128 a[136:139], v5 offset:51200\n" \
"v_cvt_pk_bf16_f32 v154, v116, v117\n" \
"v_mfma_f32_16x16x32_bf16 v[76:79], a[148:151], v[52:55], v[76:79]\n" \
"s_add_u32 m0, s48, 0\n" \
"v_exp_f32 v119, v119\n" \
"v_mfma_f32_16x16x32_bf16 v[84:87], a[144:147], a[64:67], 0\n" \
"buffer_load_dwordx4 v6, s[40:43], s16 offen offset:2048 sc1 lds\n" \
"v_mfma_f32_16x16x32_bf16 v[84:87], a[148:151], a[68:71], v[84:87]\n" \
"v_exp_f32 v120, v120\n" \
"v_mfma_f32_16x16x32_bf16 v[92:95], a[144:147], a[80:83], 0\n" \
"ds_read_b128 a[140:143], v5 offset:52224\n" \
"v_cvt_pk_bf16_f32 v155, v118, v119\n" \
"v_mfma_f32_16x16x32_bf16 v[92:95], a[148:151], a[84:87], v[92:95]\n" \
"s_waitcnt lgkmcnt(4)\n" \
"v_exp_f32 v121, v121\n" \
"v_mfma_f32_16x16x32_bf16 v[68:71], a[152:155], v[40:43], v[68:71]\n" \
"v_exp_f32 v122, v122\n" \
"v_mfma_f32_16x16x32_bf16 v[68:71], a[156:159], v[44:47], v[68:71]\n" \
"v_exp_f32 v123, v123\n" \
"v_mfma_f32_16x16x32_bf16 v[76:79], a[152:155], v[56:59], v[76:79]\n" \
"ds_read_b128 a[144:147], v5 offset:53248\n" \
"v_cvt_pk_bf16_f32 v156, v120, v121\n" \
"v_mfma_f32_16x16x32_bf16 v[76:79], a[156:159], v[60:63], v[76:79]\n" \
"s_add_u32 m0, s48, 0\n" \
"v_exp_f32 v124, v124\n" \
"v_mfma_f32_16x16x32_bf16 v[84:87], a[152:155], a[72:75], v[84:87]\n" \
"buffer_load_dwordx4 v6, s[40:43], s16 offen offset:3072 sc1 lds\n" \
"v_mfma_f32_16x16x32_bf16 v[84:87], a[156:159], a[76:79], v[84:87]\n" \
"v_exp_f32 v125, v125\n" \
"v_mfma_f32_16x16x32_bf16 v[92:95], a[152:155], a[88:91], v[92:95]\n" \
"ds_read_b128 a[148:151], v5 offset:54272\n" \
"v_cvt_pk_bf16_f32 v157, v122, v123\n" \
"v_mfma_f32_16x16x32_bf16 v[92:95], a[156:159], a[92:95], v[92:95]\n" \
"s_waitcnt lgkmcnt(4)\n" \
"v_exp_f32 v126, v126\n" \
"v_mfma_f32_16x16x32_bf16 v[160:163], v[180:183], v[144:147], v[160:163]\n" \
"v_exp_f32 v127, v127\n" \
"v_mfma_f32_16x16x32_bf16 v[164:167], v[180:183], v[148:151], v[164:167]\n" \
"ds_read_b64_tr_b16 a[170:171], v14 offset:8192\n" \
"v_cvt_pk_bf16_f32 v158, v124, v125\n" \
"v_cvt_pk_bf16_f32 v159, v126, v127\n" \
"v_mfma_f32_16x16x32_bf16 v[168:171], v[180:183], v[152:155], v[168:171]\n" \
"ds_read_b64_tr_b16 a[172:173], v15 offset:8192\n" \
"v_mfma_f32_16x16x32_bf16 v[172:175], v[180:183], v[156:159], v[172:175]\n" \
"ds_read_b64_tr_b16 a[174:175], v16 offset:8192\n" \
"v_mfma_f32_16x16x32_bf16 v[192:195], a[128:131], v[144:147], v[192:195]\n" \
"v_exp_f32 v64, v64\n" \
"v_mfma_f32_16x16x32_bf16 v[196:199], a[132:135], v[144:147], v[196:199]\n" \
"v_exp_f32 v65, v65\n" \
"v_mfma_f32_16x16x32_bf16 v[224:227], a[128:131], v[148:151], v[224:227]\n" \
"ds_read_b128 a[152:155], v5 offset:55296\n" \
"v_cvt_pk_bf16_f32 v128, v64, v65\n" \
"v_mfma_f32_16x16x32_bf16 v[228:231], a[132:135], v[148:151], v[228:231]\n" \
"v_exp_f32 v66, v66\n" \
"v_mfma_f32_16x16x32_bf16 a[0:3], a[128:131], v[152:155], a[0:3]\n" \
"v_exp_f32 v67, v67\n" \
"v_mfma_f32_16x16x32_bf16 a[4:7], a[132:135], v[152:155], a[4:7]\n" \
"s_waitcnt lgkmcnt(1)\n" \
"v_cvt_pk_bf16_f32 v129, v66, v67\n" \
"v_mfma_f32_16x16x32_bf16 a[32:35], a[128:131], v[156:159], a[32:35]\n" \
"ds_read_b128 a[156:159], v5 offset:56320\n" \
"v_mfma_f32_16x16x32_bf16 a[36:39], a[132:135], v[156:159], a[36:39]\n" \
"s_waitcnt vmcnt(14) lgkmcnt(4)\n" \
"v_exp_f32 v68, v68\n" \
"v_mfma_f32_16x16x32_bf16 v[200:203], a[136:139], v[144:147], v[200:203]\n" \
"s_barrier\n" \
"v_xor_b32 v5, 0x10000, v5\n" \
"v_mfma_f32_16x16x32_bf16 v[204:207], a[140:143], v[144:147], v[204:207]\n" \
"s_add_u32 s17, s17, s19\n" \
"v_exp_f32 v69, v69\n" \
"v_mfma_f32_16x16x32_bf16 v[232:235], a[136:139], v[148:151], v[232:235]\n" \
"ds_read_b128 a[128:131], v5 offset:0\n" \
"v_cvt_pk_bf16_f32 v130, v68, v69\n" \
"v_mfma_f32_16x16x32_bf16 v[236:239], a[140:143], v[148:151], v[236:239]\n" \
"s_add_u32 m0, s48, 16384\n" \
"v_exp_f32 v70, v70\n" \
"v_mfma_f32_16x16x32_bf16 a[8:11], a[136:139], v[152:155], a[8:11]\n" \
"buffer_load_dwordx4 v6, s[40:43], s17 offen offset:0 sc1 lds\n" \
"v_mfma_f32_16x16x32_bf16 a[12:15], a[140:143], v[152:155], a[12:15]\n" \
"v_exp_f32 v71, v71\n" \
"v_mfma_f32_16x16x32_bf16 a[40:43], a[136:139], v[156:159], a[40:43]\n" \
"ds_read_b128 a[132:135], v5 offset:1024\n" \
"v_cvt_pk_bf16_f32 v131, v70, v71\n" \
"v_mfma_f32_16x16x32_bf16 a[44:47], a[140:143], v[156:159], a[44:47]\n" \
"s_waitcnt lgkmcnt(4)\n" \
"v_exp_f32 v72, v72\n" \
"v_mfma_f32_16x16x32_bf16 v[208:211], a[144:147], v[144:147], v[208:211]\n" \
"v_exp_f32 v73, v73\n" \
"v_mfma_f32_16x16x32_bf16 v[212:215], a[148:151], v[144:147], v[212:215]\n" \
"v_mfma_f32_16x16x32_bf16 v[240:243], a[144:147], v[148:151], v[240:243]\n" \
"ds_read_b128 a[136:139], v5 offset:2048\n" \
"v_cvt_pk_bf16_f32 v132, v72, v73\n" \
"v_mfma_f32_16x16x32_bf16 v[244:247], a[148:151], v[148:151], v[244:247]\n" \
"s_add_u32 m0, s48, 16384\n" \
"v_exp_f32 v74, v74\n" \
"v_mfma_f32_16x16x32_bf16 a[16:19], a[144:147], v[152:155], a[16:19]\n" \
"buffer_load_dwordx4 v6, s[40:43], s17 offen offset:1024 sc1 lds\n" \
"v_mfma_f32_16x16x32_bf16 a[20:23], a[148:151], v[152:155], a[20:23]\n" \
"v_exp_f32 v75, v75\n" \
"v_mfma_f32_16x16x32_bf16 a[48:51], a[144:147], v[156:159], a[48:51]\n" \
"ds_read_b128 a[140:143], v5 offset:3072\n" \
"v_cvt_pk_bf16_f32 v133, v74, v75\n" \
"v_mfma_f32_16x16x32_bf16 a[52:55], a[148:151], v[156:159], a[52:55]\n" \
"s_waitcnt lgkmcnt(4)\n" \
"s_add_u32 m0, s48, 16384\n" \
"ds_read_b64_tr_b16 a[176:177], v13 offset:16384\n" \
"v_mfma_f32_16x16x32_bf16 v[216:219], a[152:155], v[144:147], v[216:219]\n" \
"buffer_load_dwordx4 v6, s[40:43], s17 offen offset:2048 sc1 lds\n" \
"v_mfma_f32_16x16x32_bf16 v[220:223], a[156:159], v[144:147], v[220:223]\n" \
"s_add_u32 s44, s44, 1\n" \
"v_exp_f32 v76, v76\n" \
"v_mfma_f32_16x16x32_bf16 v[248:251], a[152:155], v[148:151], v[248:251]\n" \
"ds_read_b128 a[144:147], v5 offset:4096\n" \
"v_mfma_f32_16x16x32_bf16 v[252:255], a[156:159], v[148:151], v[252:255]\n" \
"s_add_u32 m0, s48, 16384\n" \
"v_exp_f32 v77, v77\n" \
"v_mfma_f32_16x16x32_bf16 a[24:27], a[152:155], v[152:155], a[24:27]\n" \
"buffer_load_dwordx4 v6, s[40:43], s17 offen offset:3072 sc1 lds\n" \
"v_mfma_f32_16x16x32_bf16 a[28:31], a[156:159], v[152:155], a[28:31]\n" \
"s_xor_b32 s48, s48, 0x10000\n" \
"v_exp_f32 v78, v78\n" \
"v_mfma_f32_16x16x32_bf16 a[56:59], a[152:155], v[156:159], a[56:59]\n" \
"ds_read_b128 a[148:151], v5 offset:5120\n" \
"v_exp_f32 v79, v79\n" \
"v_mfma_f32_16x16x32_bf16 a[60:63], a[156:159], v[156:159], a[60:63]\n" \
"s_waitcnt lgkmcnt(4)\n" \
"ds_read_b64_tr_b16 a[178:179], v14 offset:16384\n" \
"v_cvt_pk_bf16_f32 v134, v76, v77\n" \
"v_cvt_pk_bf16_f32 v135, v78, v79\n" \
"v_mfma_f32_16x16x32_bf16 v[96:99], a[128:131], v[32:35], 0\n" \
"ds_read_b64_tr_b16 a[180:181], v15 offset:16384\n" \
"v_mfma_f32_16x16x32_bf16 v[96:99], a[132:135], v[36:39], v[96:99]\n" \
"s_add_u32 s16, s16, s9\n" \
"ds_read_b64_tr_b16 a[182:183], v16 offset:16384\n" \
"v_mfma_f32_16x16x32_bf16 v[104:107], a[128:131], v[48:51], 0\n" \
"ds_read_b128 a[152:155], v5 offset:6144\n" \
"v_mfma_f32_16x16x32_bf16 v[104:107], a[132:135], v[52:55], v[104:107]\n" \
"s_add_u32 m0, s49, 32768\n" \
"v_exp_f32 v80, v80\n" \
"v_mfma_f32_16x16x32_bf16 v[112:115], a[128:131], a[64:67], 0\n" \
"buffer_load_dwordx4 v6, s[40:43], s16 offen offset:0 sc1 lds\n" \
"v_mfma_f32_16x16x32_bf16 v[112:115], a[132:135], a[68:71], v[112:115]\n" \
"ds_read_b64_tr_b16 a[184:185], v13 offset:24576\n" \
"v_mfma_f32_16x16x32_bf16 v[120:123], a[128:131], a[80:83], 0\n" \
"ds_read_b128 a[156:159], v5 offset:7168\n" \
"v_mfma_f32_16x16x32_bf16 v[120:123], a[132:135], a[84:87], v[120:123]\n" \
"s_waitcnt lgkmcnt(4)\n" \
"v_exp_f32 v81, v81\n" \
"s_add_u32 m0, s79, 131072\n" \
"v_mfma_f32_16x16x32_bf16 v[96:99], a[136:139], v[40:43], v[96:99]\n" \
"buffer_load_dwordx4 v12, s[28:31], s81 offen offset:0 lds\n" \
"v_mfma_f32_16x16x32_bf16 v[96:99], a[140:143], v[44:47], v[96:99]\n" \
"v_exp_f32 v82, v82\n" \
"v_mfma_f32_16x16x32_bf16 v[104:107], a[136:139], v[56:59], v[104:107]\n" \
"ds_read_b128 a[128:131], v5 offset:16384\n" \
"v_cvt_pk_bf16_f32 v136, v80, v81\n" \
"v_mfma_f32_16x16x32_bf16 v[104:107], a[140:143], v[60:63], v[104:107]\n" \
"s_add_u32 m0, s49, 32768\n" \
"v_exp_f32 v83, v83\n" \
"v_mfma_f32_16x16x32_bf16 v[112:115], a[136:139], a[72:75], v[112:115]\n" \
"buffer_load_dwordx4 v6, s[40:43], s16 offen offset:1024 sc1 lds\n" \
"v_mfma_f32_16x16x32_bf16 v[112:115], a[140:143], a[76:79], v[112:115]\n" \
"ds_read_b64_tr_b16 a[186:187], v14 offset:24576\n" \
"v_cvt_pk_bf16_f32 v137, v82, v83\n" \
"v_mfma_f32_16x16x32_bf16 v[120:123], a[136:139], a[88:91], v[120:123]\n" \
"ds_read_b128 a[132:135], v5 offset:17408\n" \
"v_mfma_f32_16x16x32_bf16 v[120:123], a[140:143], a[92:95], v[120:123]\n" \
"s_waitcnt lgkmcnt(4)\n" \
"v_exp_f32 v84, v84\n" \
"v_mfma_f32_16x16x32_bf16 v[100:103], a[144:147], v[32:35], 0\n" \
"v_exp_f32 v85, v85\n" \
"v_mfma_f32_16x16x32_bf16 v[100:103], a[148:151], v[36:39], v[100:103]\n" \
"v_exp_f32 v86, v86\n" \
"v_mfma_f32_16x16x32_bf16 v[108:111], a[144:147], v[48:51], 0\n" \
"ds_read_b128 a[136:139], v5 offset:18432\n" \
"v_cvt_pk_bf16_f32 v138, v84, v85\n" \
"v_mfma_f32_16x16x32_bf16 v[108:111], a[148:151], v[52:55], v[108:111]\n" \
"s_add_u32 m0, s49, 32768\n" \
"v_exp_f32 v87, v87\n" \
"v_mfma_f32_16x16x32_bf16 v[116:119], a[144:147], a[64:67], 0\n" \
"buffer_load_dwordx4 v6, s[40:43], s16 offen offset:2048 sc1 lds\n" \
"v_mfma_f32_16x16x32_bf16 v[116:119], a[148:151], a[68:71], v[116:119]\n" \
"v_exp_f32 v88, v88\n" \
"v_mfma_f32_16x16x32_bf16 v[124:127], a[144:147], a[80:83], 0\n" \
"ds_read_b128 a[140:143], v5 offset:19456\n" \
"v_cvt_pk_bf16_f32 v139, v86, v87\n" \
"v_mfma_f32_16x16x32_bf16 v[124:127], a[148:151], a[84:87], v[124:127]\n" \
"LH17_%=:\ns_cbranch_execz LH21_%=\n" \
"s_waitcnt lgkmcnt(4)\n" \
"v_exp_f32 v89, v89\n" \
"v_mfma_f32_16x16x32_bf16 v[100:103], a[152:155], v[40:43], v[100:103]\n" \
"v_exp_f32 v90, v90\n" \
"v_mfma_f32_16x16x32_bf16 v[100:103], a[156:159], v[44:47], v[100:103]\n" \
"v_exp_f32 v91, v91\n" \
"v_mfma_f32_16x16x32_bf16 v[108:111], a[152:155], v[56:59], v[108:111]\n" \
"ds_read_b128 a[144:147], v5 offset:20480\n" \
"v_cvt_pk_bf16_f32 v140, v88, v89\n" \
"v_mfma_f32_16x16x32_bf16 v[108:111], a[156:159], v[60:63], v[108:111]\n" \
"s_add_u32 m0, s49, 32768\n" \
"v_exp_f32 v92, v92\n" \
"v_mfma_f32_16x16x32_bf16 v[116:119], a[152:155], a[72:75], v[116:119]\n" \
"buffer_load_dwordx4 v6, s[40:43], s16 offen offset:3072 sc1 lds\n" \
"v_mfma_f32_16x16x32_bf16 v[116:119], a[156:159], a[76:79], v[116:119]\n" \
"v_exp_f32 v93, v93\n" \
"v_mfma_f32_16x16x32_bf16 v[124:127], a[152:155], a[88:91], v[124:127]\n" \
"ds_read_b128 a[148:151], v5 offset:21504\n" \
"v_cvt_pk_bf16_f32 v141, v90, v91\n" \
"v_mfma_f32_16x16x32_bf16 v[124:127], a[156:159], a[92:95], v[124:127]\n" \
"s_waitcnt lgkmcnt(4)\n" \
"v_exp_f32 v94, v94\n" \
"v_mfma_f32_16x16x32_bf16 v[160:163], v[180:183], v[128:131], v[160:163]\n" \
"s_add_u32 m0, s79, 131968\n" \
"v_exp_f32 v95, v95\n" \
"v_mfma_f32_16x16x32_bf16 v[164:167], v[180:183], v[132:135], v[164:167]\n" \
"buffer_load_dwordx4 v12, s[28:31], s81 offen offset:128 lds\n" \
"v_cvt_pk_bf16_f32 v142, v92, v93\n" \
"v_cvt_pk_bf16_f32 v143, v94, v95\n" \
"v_mfma_f32_16x16x32_bf16 v[168:171], v[180:183], v[136:139], v[168:171]\n" \
"s_mov_b32 s81, 327680\n" \
"ds_read_b64_tr_b16 a[188:189], v15 offset:24576\n" \
"v_mfma_f32_16x16x32_bf16 v[172:175], v[180:183], v[140:143], v[172:175]\n" \
"ds_read_b64_tr_b16 a[190:191], v16 offset:24576\n" \
"v_mfma_f32_16x16x32_bf16 v[192:195], a[128:131], v[128:131], v[192:195]\n" \
"s_add_u32 m0, s79, 139264\n" \
"v_exp_f32 v96, v96\n" \
"v_mfma_f32_16x16x32_bf16 v[196:199], a[132:135], v[128:131], v[196:199]\n" \
"buffer_load_dwordx4 v12, s[28:31], s81 offen offset:0 lds\n" \
"v_mfma_f32_16x16x32_bf16 v[224:227], a[128:131], v[132:135], v[224:227]\n" \
"ds_read_b128 a[152:155], v5 offset:22528\n" \
"v_mfma_f32_16x16x32_bf16 v[228:231], a[132:135], v[132:135], v[228:231]\n" \
"v_exp_f32 v97, v97\n" \
"v_mfma_f32_16x16x32_bf16 a[0:3], a[128:131], v[136:139], a[0:3]\n" \
"v_exp_f32 v98, v98\n" \
"v_mfma_f32_16x16x32_bf16 a[4:7], a[132:135], v[136:139], a[4:7]\n" \
"s_waitcnt lgkmcnt(1)\n" \
"v_exp_f32 v99, v99\n" \
"v_mfma_f32_16x16x32_bf16 a[32:35], a[128:131], v[140:143], a[32:35]\n" \
"ds_read_b128 a[156:159], v5 offset:23552\n" \
"v_cvt_pk_bf16_f32 v144, v96, v97\n" \
"v_mfma_f32_16x16x32_bf16 a[36:39], a[132:135], v[140:143], a[36:39]\n" \
"s_waitcnt vmcnt(17) lgkmcnt(4)\n" \
"v_exp_f32 v100, v100\n" \
"v_mfma_f32_16x16x32_bf16 v[200:203], a[136:139], v[128:131], v[200:203]\n" \
"s_barrier\n" \
"v_exp_f32 v101, v101\n" \
"v_mfma_f32_16x16x32_bf16 v[204:207], a[140:143], v[128:131], v[204:207]\n" \
"s_add_u32 s17, s17, s19\n" \
"v_cvt_pk_bf16_f32 v145, v98, v99\n" \
"v_cvt_pk_bf16_f32 v146, v100, v101\n" \
"v_mfma_f32_16x16x32_bf16 v[232:235], a[136:139], v[132:135], v[232:235]\n" \
"ds_read_b128 a[128:131], v5 offset:32768\n" \
"v_mfma_f32_16x16x32_bf16 v[236:239], a[140:143], v[132:135], v[236:239]\n" \
"s_add_u32 m0, s49, 49152\n" \
"v_exp_f32 v102, v102\n" \
"v_mfma_f32_16x16x32_bf16 a[8:11], a[136:139], v[136:139], a[8:11]\n" \
"buffer_load_dwordx4 v6, s[40:43], s17 offen offset:0 sc1 lds\n" \
"v_mfma_f32_16x16x32_bf16 a[12:15], a[140:143], v[136:139], a[12:15]\n" \
"v_exp_f32 v103, v103\n" \
"v_mfma_f32_16x16x32_bf16 a[40:43], a[136:139], v[140:143], a[40:43]\n" \
"ds_read_b128 a[132:135], v5 offset:33792\n" \
"v_cvt_pk_bf16_f32 v147, v102, v103\n" \
"v_mfma_f32_16x16x32_bf16 a[44:47], a[140:143], v[140:143], a[44:47]\n" \
"s_waitcnt lgkmcnt(4)\n" \
"v_exp_f32 v104, v104\n" \
"v_mfma_f32_16x16x32_bf16 v[208:211], a[144:147], v[128:131], v[208:211]\n" \
"v_exp_f32 v105, v105\n" \
"v_mfma_f32_16x16x32_bf16 v[212:215], a[148:151], v[128:131], v[212:215]\n" \
"v_mfma_f32_16x16x32_bf16 v[240:243], a[144:147], v[132:135], v[240:243]\n" \
"ds_read_b128 a[136:139], v5 offset:34816\n" \
"v_cvt_pk_bf16_f32 v148, v104, v105\n" \
"v_mfma_f32_16x16x32_bf16 v[244:247], a[148:151], v[132:135], v[244:247]\n" \
"s_add_u32 m0, s49, 49152\n" \
"v_exp_f32 v106, v106\n" \
"v_mfma_f32_16x16x32_bf16 a[16:19], a[144:147], v[136:139], a[16:19]\n" \
"buffer_load_dwordx4 v6, s[40:43], s17 offen offset:1024 sc1 lds\n" \
"v_mfma_f32_16x16x32_bf16 a[20:23], a[148:151], v[136:139], a[20:23]\n" \
"v_exp_f32 v107, v107\n" \
"v_mfma_f32_16x16x32_bf16 a[48:51], a[144:147], v[140:143], a[48:51]\n" \
"ds_read_b128 a[140:143], v5 offset:35840\n" \
"v_cvt_pk_bf16_f32 v149, v106, v107\n" \
"v_mfma_f32_16x16x32_bf16 a[52:55], a[148:151], v[140:143], a[52:55]\n" \
"s_waitcnt lgkmcnt(4)\n" \
"s_add_u32 m0, s49, 49152\n" \
"v_mfma_f32_16x16x32_bf16 v[216:219], a[152:155], v[128:131], v[216:219]\n" \
"buffer_load_dwordx4 v6, s[40:43], s17 offen offset:2048 sc1 lds\n" \
"v_mfma_f32_16x16x32_bf16 v[220:223], a[156:159], v[128:131], v[220:223]\n" \
"v_exp_f32 v108, v108\n" \
"v_mfma_f32_16x16x32_bf16 v[248:251], a[152:155], v[132:135], v[248:251]\n" \
"ds_read_b128 a[144:147], v5 offset:36864\n" \
"v_mfma_f32_16x16x32_bf16 v[252:255], a[156:159], v[132:135], v[252:255]\n" \
"s_add_u32 m0, s49, 49152\n" \
"v_exp_f32 v109, v109\n" \
"v_mfma_f32_16x16x32_bf16 a[24:27], a[152:155], v[136:139], a[24:27]\n" \
"buffer_load_dwordx4 v6, s[40:43], s17 offen offset:3072 sc1 lds\n" \
"v_mfma_f32_16x16x32_bf16 a[28:31], a[156:159], v[136:139], a[28:31]\n" \
"v_exp_f32 v110, v110\n" \
"v_mfma_f32_16x16x32_bf16 a[56:59], a[152:155], v[140:143], a[56:59]\n" \
"ds_read_b128 a[148:151], v5 offset:37888\n" \
"v_exp_f32 v111, v111\n" \
"v_mfma_f32_16x16x32_bf16 a[60:63], a[156:159], v[140:143], a[60:63]\n" \
"s_waitcnt lgkmcnt(4)\n" \
"v_cvt_pk_bf16_f32 v150, v108, v109\n" \
"v_cvt_pk_bf16_f32 v151, v110, v111\n" \
"v_mfma_f32_16x16x32_bf16 v[64:67], a[128:131], v[32:35], 0\n" \
"buffer_load_dwordx2 v[18:19], v17, s[36:39], 0 offen sc1\n" \
"v_mfma_f32_16x16x32_bf16 v[64:67], a[132:135], v[36:39], v[64:67]\n" \
"s_add_u32 s16, s16, s9\n" \
"v_exp_f32 v112, v112\n" \
"v_mfma_f32_16x16x32_bf16 v[72:75], a[128:131], v[48:51], 0\n" \
"ds_read_b128 a[152:155], v5 offset:38912\n" \
"v_mfma_f32_16x16x32_bf16 v[72:75], a[132:135], v[52:55], v[72:75]\n" \
"s_add_u32 m0, s48, 0\n" \
"v_exp_f32 v113, v113\n" \
"v_mfma_f32_16x16x32_bf16 v[80:83], a[128:131], a[64:67], 0\n" \
"buffer_load_dwordx4 v6, s[40:43], s16 offen offset:0 sc1 lds\n" \
"v_mfma_f32_16x16x32_bf16 v[80:83], a[132:135], a[68:71], v[80:83]\n" \
"v_exp_f32 v114, v114\n" \
"v_mfma_f32_16x16x32_bf16 v[88:91], a[128:131], a[80:83], 0\n" \
"ds_read_b128 a[156:159], v5 offset:39936\n" \
"v_cvt_pk_bf16_f32 v152, v112, v113\n" \
"v_mfma_f32_16x16x32_bf16 v[88:91], a[132:135], a[84:87], v[88:91]\n" \
"s_waitcnt lgkmcnt(4)\n" \
"v_exp_f32 v115, v115\n" \
"s_add_u32 m0, s79, 140160\n" \
"v_mfma_f32_16x16x32_bf16 v[64:67], a[136:139], v[40:43], v[64:67]\n" \
"buffer_load_dwordx4 v12, s[28:31], s81 offen offset:128 lds\n" \
"v_mfma_f32_16x16x32_bf16 v[64:67], a[140:143], v[44:47], v[64:67]\n" \
"s_xor_b32 s49, s49, 0x10000\n" \
"s_mov_b32 s81, 393216\n" \
"v_cvt_pk_bf16_f32 v153, v114, v115\n" \
"v_mfma_f32_16x16x32_bf16 v[72:75], a[136:139], v[56:59], v[72:75]\n" \
"ds_read_b128 a[128:131], v5 offset:49152\n" \
"v_mfma_f32_16x16x32_bf16 v[72:75], a[140:143], v[60:63], v[72:75]\n" \
"s_add_u32 m0, s48, 0\n" \
"v_exp_f32 v116, v116\n" \
"v_mfma_f32_16x16x32_bf16 v[80:83], a[136:139], a[72:75], v[80:83]\n" \
"buffer_load_dwordx4 v6, s[40:43], s16 offen offset:1024 sc1 lds\n" \
"v_mfma_f32_16x16x32_bf16 v[80:83], a[140:143], a[76:79], v[80:83]\n" \
"v_exp_f32 v117, v117\n" \
"v_mfma_f32_16x16x32_bf16 v[88:91], a[136:139], a[88:91], v[88:91]\n" \
"ds_read_b128 a[132:135], v5 offset:50176\n" \
"v_cvt_pk_bf16_f32 v154, v116, v117\n" \
"v_mfma_f32_16x16x32_bf16 v[88:91], a[140:143], a[92:95], v[88:91]\n" \
"s_waitcnt lgkmcnt(4)\n" \
"v_exp_f32 v118, v118\n" \
"s_add_u32 m0, s79, 147456\n" \
"v_mfma_f32_16x16x32_bf16 v[68:71], a[144:147], v[32:35], 0\n" \
"buffer_load_dwordx4 v12, s[28:31], s81 offen offset:0 lds\n" \
"v_mfma_f32_16x16x32_bf16 v[68:71], a[148:151], v[36:39], v[68:71]\n" \
"v_exp_f32 v119, v119\n" \
"v_mfma_f32_16x16x32_bf16 v[76:79], a[144:147], v[48:51], 0\n" \
"ds_read_b128 a[136:139], v5 offset:51200\n" \
"v_cvt_pk_bf16_f32 v155, v118, v119\n" \
"v_mfma_f32_16x16x32_bf16 v[76:79], a[148:151], v[52:55], v[76:79]\n" \
"s_add_u32 m0, s48, 0\n" \
"v_exp_f32 v120, v120\n" \
"v_mfma_f32_16x16x32_bf16 v[84:87], a[144:147], a[64:67], 0\n" \
"buffer_load_dwordx4 v6, s[40:43], s16 offen offset:2048 sc1 lds\n" \
"v_mfma_f32_16x16x32_bf16 v[84:87], a[148:151], a[68:71], v[84:87]\n" \
"v_exp_f32 v121, v121\n" \
"v_mfma_f32_16x16x32_bf16 v[92:95], a[144:147], a[80:83], 0\n" \
"ds_read_b128 a[140:143], v5 offset:52224\n" \
"v_cvt_pk_bf16_f32 v156, v120, v121\n" \
"v_mfma_f32_16x16x32_bf16 v[92:95], a[148:151], a[84:87], v[92:95]\n" \
"s_waitcnt lgkmcnt(4)\n" \
"v_exp_f32 v122, v122\n" \
"s_add_u32 m0, s79, 148352\n" \
"v_mfma_f32_16x16x32_bf16 v[68:71], a[152:155], v[40:43], v[68:71]\n" \
"buffer_load_dwordx4 v12, s[28:31], s81 offen offset:128 lds\n" \
"v_mfma_f32_16x16x32_bf16 v[68:71], a[156:159], v[44:47], v[68:71]\n" \
"s_mov_b32 s81, 458752\n" \
"v_exp_f32 v123, v123\n" \
"v_mfma_f32_16x16x32_bf16 v[76:79], a[152:155], v[56:59], v[76:79]\n" \
"ds_read_b128 a[144:147], v5 offset:53248\n" \
"v_cvt_pk_bf16_f32 v157, v122, v123\n" \
"v_mfma_f32_16x16x32_bf16 v[76:79], a[156:159], v[60:63], v[76:79]\n" \
"s_add_u32 m0, s48, 0\n" \
"v_exp_f32 v124, v124\n" \
"v_mfma_f32_16x16x32_bf16 v[84:87], a[152:155], a[72:75], v[84:87]\n" \
"buffer_load_dwordx4 v6, s[40:43], s16 offen offset:3072 sc1 lds\n" \
"v_mfma_f32_16x16x32_bf16 v[84:87], a[156:159], a[76:79], v[84:87]\n" \
"v_exp_f32 v125, v125\n" \
"v_mfma_f32_16x16x32_bf16 v[92:95], a[152:155], a[88:91], v[92:95]\n" \
"ds_read_b128 a[148:151], v5 offset:54272\n" \
"v_cvt_pk_bf16_f32 v158, v124, v125\n" \
"v_mfma_f32_16x16x32_bf16 v[92:95], a[156:159], a[92:95], v[92:95]\n" \
"s_waitcnt lgkmcnt(4)\n" \
"v_exp_f32 v126, v126\n" \
"v_mfma_f32_16x16x32_bf16 v[160:163], v[180:183], v[144:147], v[160:163]\n" \
"s_add_u32 m0, s79, 155648\n" \
"v_exp_f32 v127, v127\n" \
"v_mfma_f32_16x16x32_bf16 v[164:167], v[180:183], v[148:151], v[164:167]\n" \
"buffer_load_dwordx4 v12, s[28:31], s81 offen offset:0 lds\n" \
"v_cvt_pk_bf16_f32 v159, v126, v127\n" \
"v_mfma_f32_16x16x32_bf16 v[168:171], v[180:183], v[152:155], v[168:171]\n" \
"v_exp_f32 v64, v64\n" \
"v_mfma_f32_16x16x32_bf16 v[172:175], v[180:183], v[156:159], v[172:175]\n" \
"buffer_store_dwordx4 v[28:31], v4, s[36:39], s80 offen offset:1024 sc1\n" \
"v_mfma_f32_16x16x32_bf16 v[192:195], a[128:131], v[144:147], v[192:195]\n" \
"s_add_u32 m0, s79, 156544\n" \
"v_exp_f32 v65, v65\n" \
"v_mfma_f32_16x16x32_bf16 v[196:199], a[132:135], v[144:147], v[196:199]\n" \
"buffer_load_dwordx4 v12, s[28:31], s81 offen offset:128 lds\n" \
"v_mfma_f32_16x16x32_bf16 v[224:227], a[128:131], v[148:151], v[224:227]\n" \
"s_mov_b32 s81, 524288\n" \
"ds_read_b128 a[152:155], v5 offset:55296\n" \
"v_cvt_pk_bf16_f32 v128, v64, v65\n" \
"v_mfma_f32_16x16x32_bf16 v[228:231], a[132:135], v[148:151], v[228:231]\n" \
"buffer_store_dwordx4 v[28:31], v4, s[36:39], s80 offen offset:2048 sc1\n" \
"v_mfma_f32_16x16x32_bf16 a[0:3], a[128:131], v[152:155], a[0:3]\n" \
"v_exp_f32 v66, v66\n" \
"v_mfma_f32_16x16x32_bf16 a[4:7], a[132:135], v[152:155], a[4:7]\n" \
"v_exp_f32 v67, v67\n" \
"v_mfma_f32_16x16x32_bf16 a[32:35], a[128:131], v[156:159], a[32:35]\n" \
"ds_read_b128 a[156:159], v5 offset:56320\n" \
"v_cvt_pk_bf16_f32 v129, v66, v67\n" \
"v_mfma_f32_16x16x32_bf16 a[36:39], a[132:135], v[156:159], a[36:39]\n" \
"s_waitcnt vmcnt(9) lgkmcnt(4)\n" \
"v_exp_f32 v68, v68\n" \
"v_mfma_f32_16x16x32_bf16 v[200:203], a[136:139], v[144:147], v[200:203]\n" \
"s_barrier\n" \
"v_xor_b32 v5, 0x10000, v5\n" \
"v_mfma_f32_16x16x32_bf16 v[204:207], a[140:143], v[144:147], v[204:207]\n" \
"s_add_u32 s17, s17, s19\n" \
"v_exp_f32 v69, v69\n" \
"v_mfma_f32_16x16x32_bf16 v[232:235], a[136:139], v[148:151], v[232:235]\n" \
"ds_read_b128 a[128:131], v5 offset:0\n" \
"v_cvt_pk_bf16_f32 v130, v68, v69\n" \
"v_mfma_f32_16x16x32_bf16 v[236:239], a[140:143], v[148:151], v[236:239]\n" \
"s_add_u32 m0, s48, 16384\n" \
"v_exp_f32 v70, v70\n" \
"v_mfma_f32_16x16x32_bf16 a[8:11], a[136:139], v[152:155], a[8:11]\n" \
"buffer_load_dwordx4 v6, s[40:43], s17 offen offset:0 sc1 lds\n" \
"v_mfma_f32_16x16x32_bf16 a[12:15], a[140:143], v[152:155], a[12:15]\n" \
"v_exp_f32 v71, v71\n" \
"v_mfma_f32_16x16x32_bf16 a[40:43], a[136:139], v[156:159], a[40:43]\n" \
"ds_read_b128 a[132:135], v5 offset:1024\n" \
"v_cvt_pk_bf16_f32 v131, v70, v71\n" \
"v_mfma_f32_16x16x32_bf16 a[44:47], a[140:143], v[156:159], a[44:47]\n" \
"s_waitcnt lgkmcnt(4)\n" \
"v_exp_f32 v72, v72\n" \
"v_mfma_f32_16x16x32_bf16 v[208:211], a[144:147], v[144:147], v[208:211]\n" \
"v_exp_f32 v73, v73\n" \
"v_mfma_f32_16x16x32_bf16 v[212:215], a[148:151], v[144:147], v[212:215]\n" \
"v_mfma_f32_16x16x32_bf16 v[240:243], a[144:147], v[148:151], v[240:243]\n" \
"ds_read_b128 a[136:139], v5 offset:2048\n" \
"v_cvt_pk_bf16_f32 v132, v72, v73\n" \
"v_mfma_f32_16x16x32_bf16 v[244:247], a[148:151], v[148:151], v[244:247]\n" \
"s_add_u32 m0, s48, 16384\n" \
"v_exp_f32 v74, v74\n" \
"v_mfma_f32_16x16x32_bf16 a[16:19], a[144:147], v[152:155], a[16:19]\n" \
"buffer_load_dwordx4 v6, s[40:43], s17 offen offset:1024 sc1 lds\n" \
"v_mfma_f32_16x16x32_bf16 a[20:23], a[148:151], v[152:155], a[20:23]\n" \
"v_exp_f32 v75, v75\n" \
"v_mfma_f32_16x16x32_bf16 a[48:51], a[144:147], v[156:159], a[48:51]\n" \
"ds_read_b128 a[140:143], v5 offset:3072\n" \
"v_cvt_pk_bf16_f32 v133, v74, v75\n" \
"v_mfma_f32_16x16x32_bf16 a[52:55], a[148:151], v[156:159], a[52:55]\n" \
"s_waitcnt lgkmcnt(4)\n" \
"s_add_u32 m0, s48, 16384\n" \
"ds_read_b64_tr_b16 a[192:193], v13 offset:0\n" \
"v_mfma_f32_16x16x32_bf16 v[216:219], a[152:155], v[144:147], v[216:219]\n" \
"buffer_load_dwordx4 v6, s[40:43], s17 offen offset:2048 sc1 lds\n" \
"v_mfma_f32_16x16x32_bf16 v[220:223], a[156:159], v[144:147], v[220:223]\n" \
"s_add_u32 s44, s44, 1\n" \
"v_exp_f32 v76, v76\n" \
"v_mfma_f32_16x16x32_bf16 v[248:251], a[152:155], v[148:151], v[248:251]\n" \
"ds_read_b128 a[144:147], v5 offset:4096\n" \
"v_mfma_f32_16x16x32_bf16 v[252:255], a[156:159], v[148:151], v[252:255]\n" \
"s_add_u32 m0, s48, 16384\n" \
"v_exp_f32 v77, v77\n" \
"v_mfma_f32_16x16x32_bf16 a[24:27], a[152:155], v[152:155], a[24:27]\n" \
"buffer_load_dwordx4 v6, s[40:43], s17 offen offset:3072 sc1 lds\n" \
"v_mfma_f32_16x16x32_bf16 a[28:31], a[156:159], v[152:155], a[28:31]\n" \
"s_xor_b32 s48, s48, 0x10000\n" \
"v_exp_f32 v78, v78\n" \
"v_mfma_f32_16x16x32_bf16 a[56:59], a[152:155], v[156:159], a[56:59]\n" \
"ds_read_b128 a[148:151], v5 offset:5120\n" \
"v_exp_f32 v79, v79\n" \
"v_mfma_f32_16x16x32_bf16 a[60:63], a[156:159], v[156:159], a[60:63]\n" \
"s_waitcnt lgkmcnt(4)\n" \
"ds_read_b64_tr_b16 a[194:195], v14 offset:0\n" \
"v_cvt_pk_bf16_f32 v134, v76, v77\n" \
"v_cvt_pk_bf16_f32 v135, v78, v79\n" \
"v_mfma_f32_16x16x32_bf16 v[96:99], a[128:131], v[32:35], 0\n" \
"s_add_u32 s100, s100, 262144\ns_addc_u32 s101, s101, 0\n" \
"ds_read_b64_tr_b16 a[196:197], v15 offset:0\n" \
"v_mfma_f32_16x16x32_bf16 v[96:99], a[132:135], v[36:39], v[96:99]\n" \
"s_add_u32 s16, s16, s9\n" \
"ds_read_b64_tr_b16 a[198:199], v16 offset:0\n" \
"v_mfma_f32_16x16x32_bf16 v[104:107], a[128:131], v[48:51], 0\n" \
"ds_read_b128 a[152:155], v5 offset:6144\n" \
"v_mfma_f32_16x16x32_bf16 v[104:107], a[132:135], v[52:55], v[104:107]\n" \
"s_add_u32 m0, s49, 32768\n" \
"v_exp_f32 v80, v80\n" \
"v_mfma_f32_16x16x32_bf16 v[112:115], a[128:131], a[64:67], 0\n" \
"buffer_load_dwordx4 v6, s[40:43], s16 offen offset:0 sc1 lds\n" \
"v_mfma_f32_16x16x32_bf16 v[112:115], a[132:135], a[68:71], v[112:115]\n" \
"ds_read_b64_tr_b16 a[200:201], v13 offset:8192\n" \
"v_mfma_f32_16x16x32_bf16 v[120:123], a[128:131], a[80:83], 0\n" \
"ds_read_b128 a[156:159], v5 offset:7168\n" \
"v_mfma_f32_16x16x32_bf16 v[120:123], a[132:135], a[84:87], v[120:123]\n" \
"s_waitcnt lgkmcnt(4)\n" \
"v_exp_f32 v81, v81\n" \
"v_mfma_f32_16x16x32_bf16 v[96:99], a[136:139], v[40:43], v[96:99]\n" \
"s_mov_b64 exec, 0xffff\nglobal_load_dword v1, v2, s[100:101]\ns_mov_b64 exec, -1\n" \
"v_mfma_f32_16x16x32_bf16 v[96:99], a[140:143], v[44:47], v[96:99]\n" \
"v_exp_f32 v82, v82\n" \
"v_mfma_f32_16x16x32_bf16 v[104:107], a[136:139], v[56:59], v[104:107]\n" \
"ds_read_b128 a[128:131], v5 offset:16384\n" \
"v_cvt_pk_bf16_f32 v136, v80, v81\n" \
"v_mfma_f32_16x16x32_bf16 v[104:107], a[140:143], v[60:63], v[104:107]\n" \
"s_add_u32 m0, s49, 32768\n" \
"v_exp_f32 v83, v83\n" \
"v_mfma_f32_16x16x32_bf16 v[112:115], a[136:139], a[72:75], v[112:115]\n" \
"buffer_load_dwordx4 v6, s[40:43], s16 offen offset:1024 sc1 lds\n" \
"v_mfma_f32_16x16x32_bf16 v[112:115], a[140:143], a[76:79], v[112:115]\n" \
"ds_read_b64_tr_b16 a[202:203], v14 offset:8192\n" \
"v_cvt_pk_bf16_f32 v137, v82, v83\n" \
"v_mfma_f32_16x16x32_bf16 v[120:123], a[136:139], a[88:91], v[120:123]\n" \
"ds_read_b128 a[132:135], v5 offset:17408\n" \
"v_mfma_f32_16x16x32_bf16 v[120:123], a[140:143], a[92:95], v[120:123]\n" \
"s_waitcnt lgkmcnt(4)\n" \
"v_exp_f32 v84, v84\n" \
"v_mfma_f32_16x16x32_bf16 v[100:103], a[144:147], v[32:35], 0\n" \
"v_exp_f32 v85, v85\n" \
"v_mfma_f32_16x16x32_bf16 v[100:103], a[148:151], v[36:39], v[100:103]\n" \
"v_exp_f32 v86, v86\n" \
"v_mfma_f32_16x16x32_bf16 v[108:111], a[144:147], v[48:51], 0\n" \
"ds_read_b128 a[136:139], v5 offset:18432\n" \
"v_cvt_pk_bf16_f32 v138, v84, v85\n" \
"v_mfma_f32_16x16x32_bf16 v[108:111], a[148:151], v[52:55], v[108:111]\n" \
"s_add_u32 m0, s49, 32768\n" \
"v_exp_f32 v87, v87\n" \
"v_mfma_f32_16x16x32_bf16 v[116:119], a[144:147], a[64:67], 0\n" \
"buffer_load_dwordx4 v6, s[40:43], s16 offen offset:2048 sc1 lds\n" \
"v_mfma_f32_16x16x32_bf16 v[116:119], a[148:151], a[68:71], v[116:119]\n" \
"v_exp_f32 v88, v88\n" \
"v_mfma_f32_16x16x32_bf16 v[124:127], a[144:147], a[80:83], 0\n" \
"ds_read_b128 a[140:143], v5 offset:19456\n" \
"v_cvt_pk_bf16_f32 v139, v86, v87\n" \
"v_mfma_f32_16x16x32_bf16 v[124:127], a[148:151], a[84:87], v[124:127]\n" \
"s_waitcnt lgkmcnt(4)\n" \
"v_exp_f32 v89, v89\n" \
"v_mfma_f32_16x16x32_bf16 v[100:103], a[152:155], v[40:43], v[100:103]\n" \
"v_exp_f32 v90, v90\n" \
"v_mfma_f32_16x16x32_bf16 v[100:103], a[156:159], v[44:47], v[100:103]\n" \
"v_exp_f32 v91, v91\n" \
"v_mfma_f32_16x16x32_bf16 v[108:111], a[152:155], v[56:59], v[108:111]\n" \
"ds_read_b128 a[144:147], v5 offset:20480\n" \
"v_cvt_pk_bf16_f32 v140, v88, v89\n" \
"v_mfma_f32_16x16x32_bf16 v[108:111], a[156:159], v[60:63], v[108:111]\n" \
"s_add_u32 m0, s49, 32768\n" \
"v_exp_f32 v92, v92\n" \
"v_mfma_f32_16x16x32_bf16 v[116:119], a[152:155], a[72:75], v[116:119]\n" \
"buffer_load_dwordx4 v6, s[40:43], s16 offen offset:3072 sc1 lds\n" \
"v_mfma_f32_16x16x32_bf16 v[116:119], a[156:159], a[76:79], v[116:119]\n" \
"v_exp_f32 v93, v93\n" \
"v_mfma_f32_16x16x32_bf16 v[124:127], a[152:155], a[88:91], v[124:127]\n" \
"ds_read_b128 a[148:151], v5 offset:21504\n" \
"v_cvt_pk_bf16_f32 v141, v90, v91\n" \
"v_mfma_f32_16x16x32_bf16 v[124:127], a[156:159], a[92:95], v[124:127]\n" \
"s_waitcnt lgkmcnt(4)\n" \
"v_exp_f32 v94, v94\n" \
"v_mfma_f32_16x16x32_bf16 v[160:163], v[180:183], v[128:131], v[160:163]\n" \
"s_waitcnt vmcnt(20)\n" \
"v_exp_f32 v95, v95\n" \
"v_mfma_f32_16x16x32_bf16 v[164:167], v[180:183], v[132:135], v[164:167]\n" \
"ds_read_b64_tr_b16 a[204:205], v15 offset:8192\n" \
"v_cmp_ne_u64 vcc, s[14:15], v[18:19]\n" \
"v_cvt_pk_bf16_f32 v142, v92, v93\n" \
"v_cvt_pk_bf16_f32 v143, v94, v95\n" \
"v_mfma_f32_16x16x32_bf16 v[168:171], v[180:183], v[136:139], v[168:171]\n" \
"ds_read_b64_tr_b16 a[206:207], v16 offset:8192\n" \
"s_nop 0\n" \
"v_mfma_f32_16x16x32_bf16 v[172:175], v[180:183], v[140:143], v[172:175]\n" \
"s_cbranch_vccnz LPZ_%=\nLPZR_%=:\n" \
"v_and_b32 v188, 0xf0, v4\n" \
"v_lshrrev_b32 v189, 8, v4\n" \
"v_mfma_f32_16x16x32_bf16 v[192:195], a[128:131], v[128:131], v[192:195]\n" \
"v_lshl_add_u32 v188, v189, 14, v188\n" \
"v_add_u32 v189, 0x10000, v188\n" \
"v_mfma_f32_16x16x32_bf16 v[196:199], a[132:135], v[128:131], v[196:199]\n" \
"v_exp_f32 v96, v96\n" \
"v_mfma_f32_16x16x32_bf16 v[224:227], a[128:131], v[132:135], v[224:227]\n" \
"ds_read_b128 a[152:155], v5 offset:22528\n" \
"v_mfma_f32_16x16x32_bf16 v[228:231], a[132:135], v[132:135], v[228:231]\n" \
"v_exp_f32 v97, v97\n" \
"v_mfma_f32_16x16x32_bf16 a[0:3], a[128:131], v[136:139], a[0:3]\n" \
"v_exp_f32 v98, v98\n" \
"v_mfma_f32_16x16x32_bf16 a[4:7], a[132:135], v[136:139], a[4:7]\n" \
"s_waitcnt lgkmcnt(1)\n" \
"v_exp_f32 v99, v99\n" \
"v_mfma_f32_16x16x32_bf16 a[32:35], a[128:131], v[140:143], a[32:35]\n" \
"ds_read_b128 a[156:159], v5 offset:23552\n" \
"v_cvt_pk_bf16_f32 v144, v96, v97\n" \
"v_mfma_f32_16x16x32_bf16 a[36:39], a[132:135], v[140:143], a[36:39]\n" \
"s_waitcnt vmcnt(10) lgkmcnt(4)\n" \
"v_exp_f32 v100, v100\n" \
"v_mfma_f32_16x16x32_bf16 v[200:203], a[136:139], v[128:131], v[200:203]\n" \
"s_barrier\n" \
"v_exp_f32 v101, v101\n" \
"v_mfma_f32_16x16x32_bf16 v[204:207], a[140:143], v[128:131], v[204:207]\n" \
"s_add_u32 s17, s17, s19\n" \
"ds_read_b64_tr_b16 a[208:209], v13 offset:16384\n" \
"v_cvt_pk_bf16_f32 v145, v98, v99\n" \
"v_mfma_f32_16x16x32_bf16 v[232:235], a[136:139], v[132:135], v[232:235]\n" \
"ds_read_b128 a[128:131], v5 offset:32768\n" \
"v_cvt_pk_bf16_f32 v146, v100, v101\n" \
"v_mfma_f32_16x16x32_bf16 v[236:239], a[140:143], v[132:135], v[236:239]\n" \
"s_add_u32 m0, s49, 49152\n" \
"v_exp_f32 v102, v102\n" \
"v_mfma_f32_16x16x32_bf16 a[8:11], a[136:139], v[136:139], a[8:11]\n" \
"buffer_load_dwordx4 v6, s[40:43], s17 offen offset:0 sc1 lds\n" \
"v_mfma_f32_16x16x32_bf16 a[12:15], a[140:143], v[136:139], a[12:15]\n" \
"v_exp_f32 v103, v103\n" \
"v_mfma_f32_16x16x32_bf16 a[40:43], a[136:139], v[140:143], a[40:43]\n" \
"ds_read_b128 a[132:135], v5 offset:33792\n" \
"v_cvt_pk_bf16_f32 v147, v102, v103\n" \
"v_mfma_f32_16x16x32_bf16 a[44:47], a[140:143], v[140:143], a[44:47]\n" \
"s_waitcnt lgkmcnt(4)\n" \
"v_exp_f32 v104, v104\n" \
"v_mfma_f32_16x16x32_bf16 v[208:211], a[144:147], v[128:131], v[208:211]\n" \
"v_exp_f32 v105, v105\n" \
"v_mfma_f32_16x16x32_bf16 v[212:215], a[148:151], v[128:131], v[212:215]\n" \
"v_mfma_f32_16x16x32_bf16 v[240:243], a[144:147], v[132:135], v[240:243]\n" \
"ds_read_b128 a[136:139], v5 offset:34816\n" \
"v_cvt_pk_bf16_f32 v148, v104, v105\n" \
"v_mfma_f32_16x16x32_bf16 v[244:247], a[148:151], v[132:135], v[244:247]\n" \
"s_add_u32 m0, s49, 49152\n" \
"v_exp_f32 v106, v106\n" \
"v_mfma_f32_16x16x32_bf16 a[16:19], a[144:147], v[136:139], a[16:19]\n" \
"buffer_load_dwordx4 v6, s[40:43], s17 offen offset:1024 sc1 lds\n" \
"v_mfma_f32_16x16x32_bf16 a[20:23], a[148:151], v[136:139], a[20:23]\n" \
"v_exp_f32 v107, v107\n" \
"v_mfma_f32_16x16x32_bf16 a[48:51], a[144:147], v[140:143], a[48:51]\n" \
"ds_read_b128 a[140:143], v5 offset:35840\n" \
"v_cvt_pk_bf16_f32 v149, v106, v107\n" \
"v_mfma_f32_16x16x32_bf16 a[52:55], a[148:151], v[140:143], a[52:55]\n" \
"s_waitcnt lgkmcnt(4)\n" \
"s_add_u32 m0, s49, 49152\n" \
"ds_read_b64_tr_b16 a[210:211], v14 offset:16384\n" \
"v_mfma_f32_16x16x32_bf16 v[216:219], a[152:155], v[128:131], v[216:219]\n" \
"buffer_load_dwordx4 v6, s[40:43], s17 offen offset:2048 sc1 lds\n" \
"v_mfma_f32_16x16x32_bf16 v[220:223], a[156:159], v[128:131], v[220:223]\n" \
"v_exp_f32 v108, v108\n" \
"v_mfma_f32_16x16x32_bf16 v[248:251], a[152:155], v[132:135], v[248:251]\n" \
"ds_read_b128 a[144:147], v5 offset:36864\n" \
"v_mfma_f32_16x16x32_bf16 v[252:255], a[156:159], v[132:135], v[252:255]\n" \
"s_add_u32 m0, s49, 49152\n" \
"v_exp_f32 v109, v109\n" \
"v_mfma_f32_16x16x32_bf16 a[24:27], a[152:155], v[136:139], a[24:27]\n" \
"buffer_load_dwordx4 v6, s[40:43], s17 offen offset:3072 sc1 lds\n" \
"v_mfma_f32_16x16x32_bf16 a[28:31], a[156:159], v[136:139], a[28:31]\n" \
"v_exp_f32 v110, v110\n" \
"v_mfma_f32_16x16x32_bf16 a[56:59], a[152:155], v[140:143], a[56:59]\n" \
"ds_read_b128 a[148:151], v5 offset:37888\n" \
"v_exp_f32 v111, v111\n" \
"v_mfma_f32_16x16x32_bf16 a[60:63], a[156:159], v[140:143], a[60:63]\n" \
"s_waitcnt lgkmcnt(4)\n" \
"ds_read_b64_tr_b16 a[212:213], v15 offset:16384\n" \
"v_cvt_pk_bf16_f32 v150, v108, v109\n" \
"v_cvt_pk_bf16_f32 v151, v110, v111\n" \
"v_mfma_f32_16x16x32_bf16 v[64:67], a[128:131], v[32:35], 0\n" \
"ds_read_b64_tr_b16 a[214:215], v16 offset:16384\n" \
"v_mfma_f32_16x16x32_bf16 v[64:67], a[132:135], v[36:39], v[64:67]\n" \
"s_add_u32 s16, s16, s9\n" \
"v_exp_f32 v112, v112\n" \
"v_mfma_f32_16x16x32_bf16 v[72:75], a[128:131], v[48:51], 0\n" \
"ds_read_b128 a[152:155], v5 offset:38912\n" \
"v_mfma_f32_16x16x32_bf16 v[72:75], a[132:135], v[52:55], v[72:75]\n" \
"s_add_u32 m0, s48, 0\n" \
"v_exp_f32 v113, v113\n" \
"v_mfma_f32_16x16x32_bf16 v[80:83], a[128:131], a[64:67], 0\n" \
"buffer_load_dwordx4 v6, s[40:43], s16 offen offset:0 sc1 lds\n" \
"v_mfma_f32_16x16x32_bf16 v[80:83], a[132:135], a[68:71], v[80:83]\n" \
"ds_read_b64_tr_b16 a[216:217], v13 offset:24576\n" \
"v_cvt_pk_bf16_f32 v152, v112, v113\n" \
"v_mfma_f32_16x16x32_bf16 v[88:91], a[128:131], a[80:83], 0\n" \
"ds_read_b128 a[156:159], v5 offset:39936\n" \
"v_mfma_f32_16x16x32_bf16 v[88:91], a[132:135], a[84:87], v[88:91]\n" \
"s_waitcnt lgkmcnt(4)\n" \
"v_exp_f32 v114, v114\n" \
"s_add_u32 m0, s79, 131072\n" \
"v_mfma_f32_16x16x32_bf16 v[64:67], a[136:139], v[40:43], v[64:67]\n" \
"buffer_load_dwordx4 v12, s[28:31], s81 offen offset:0 lds\n" \
"v_mfma_f32_16x16x32_bf16 v[64:67], a[140:143], v[44:47], v[64:67]\n" \
"s_xor_b32 s49, s49, 0x10000\n" \
"v_exp_f32 v115, v115\n" \
"v_mfma_f32_16x16x32_bf16 v[72:75], a[136:139], v[56:59], v[72:75]\n" \
"ds_read_b128 a[128:131], v5 offset:49152\n" \
"v_cvt_pk_bf16_f32 v153, v114, v115\n" \
"v_mfma_f32_16x16x32_bf16 v[72:75], a[140:143], v[60:63], v[72:75]\n" \
"s_add_u32 m0, s48, 0\n" \
"v_exp_f32 v116, v116\n" \
"v_mfma_f32_16x16x32_bf16 v[80:83], a[136:139], a[72:75], v[80:83]\n" \
"buffer_load_dwordx4 v6, s[40:43], s16 offen offset:1024 sc1 lds\n" \
"v_mfma_f32_16x16x32_bf16 v[80:83], a[140:143], a[76:79], v[80:83]\n" \
"ds_read_b64_tr_b16 a[218:219], v14 offset:24576\n" \
"v_mfma_f32_16x16x32_bf16 v[88:91], a[136:139], a[88:91], v[88:91]\n" \
"ds_read_b128 a[132:135], v5 offset:50176\n" \
"v_mfma_f32_16x16x32_bf16 v[88:91], a[140:143], a[92:95], v[88:91]\n" \
"s_waitcnt lgkmcnt(4)\n" \
"v_exp_f32 v117, v117\n" \
"s_add_u32 m0, s79, 131968\n" \
"v_mfma_f32_16x16x32_bf16 v[68:71], a[144:147], v[32:35], 0\n" \
"buffer_load_dwordx4 v12, s[28:31], s81 offen offset:128 lds\n" \
"v_mfma_f32_16x16x32_bf16 v[68:71], a[148:151], v[36:39], v[68:71]\n" \
"s_mov_b32 s81, 589824\n" \
"v_exp_f32 v118, v118\n" \
"v_mfma_f32_16x16x32_bf16 v[76:79], a[144:147], v[48:51], 0\n" \
"ds_read_b128 a[136:139], v5 offset:51200\n" \
"v_cvt_pk_bf16_f32 v154, v116, v117\n" \
"v_mfma_f32_16x16x32_bf16 v[76:79], a[148:151], v[52:55], v[76:79]\n" \
"s_add_u32 m0, s48, 0\n" \
"v_exp_f32 v119, v119\n" \
"v_mfma_f32_16x16x32_bf16 v[84:87], a[144:147], a[64:67], 0\n" \
"buffer_load_dwordx4 v6, s[40:43], s16 offen offset:2048 sc1 lds\n" \
"v_mfma_f32_16x16x32_bf16 v[84:87], a[148:151], a[68:71], v[84:87]\n" \
"v_exp_f32 v120, v120\n" \
"v_mfma_f32_16x16x32_bf16 v[92:95], a[144:147], a[80:83], 0\n" \
"ds_read_b128 a[140:143], v5 offset:52224\n" \
"v_cvt_pk_bf16_f32 v155, v118, v119\n" \
"v_mfma_f32_16x16x32_bf16 v[92:95], a[148:151], a[84:87], v[92:95]\n" \
"s_waitcnt lgkmcnt(4)\n" \
"v_exp_f32 v121, v121\n" \
"v_mfma_f32_16x16x32_bf16 v[68:71], a[152:155], v[40:43], v[68:71]\n" \
"v_exp_f32 v122, v122\n" \
"v_mfma_f32_16x16x32_bf16 v[68:71], a[156:159], v[44:47], v[68:71]\n" \
"v_exp_f32 v123, v123\n" \
"v_mfma_f32_16x16x32_bf16 v[76:79], a[152:155], v[56:59], v[76:79]\n" \
"ds_read_b128 a[144:147], v5 offset:53248\n" \
"v_cvt_pk_bf16_f32 v156, v120, v121\n" \
"v_mfma_f32_16x16x32_bf16 v[76:79], a[156:159], v[60:63], v[76:79]\n" \
"s_add_u32 m0, s48, 0\n" \
"v_exp_f32 v124, v124\n" \
"v_mfma_f32_16x16x32_bf16 v[84:87], a[152:155], a[72:75], v[84:87]\n" \
"buffer_load_dwordx4 v6, s[40:43], s16 offen offset:3072 sc1 lds\n" \
"v_mfma_f32_16x16x32_bf16 v[84:87], a[156:159], a[76:79], v[84:87]\n" \
"v_exp_f32 v125, v125\n" \
"v_mfma_f32_16x16x32_bf16 v[92:95], a[152:155], a[88:91], v[92:95]\n" \
"ds_read_b128 a[148:151], v5 offset:54272\n" \
"v_cvt_pk_bf16_f32 v157, v122, v123\n" \
"v_mfma_f32_16x16x32_bf16 v[92:95], a[156:159], a[92:95], v[92:95]\n" \
"s_waitcnt lgkmcnt(4)\n" \
"v_exp_f32 v126, v126\n" \
"v_mfma_f32_16x16x32_bf16 v[160:163], v[180:183], v[144:147], v[160:163]\n" \
"s_add_u32 m0, s79, 139264\n" \
"v_exp_f32 v127, v127\n" \
"v_mfma_f32_16x16x32_bf16 v[164:167], v[180:183], v[148:151], v[164:167]\n" \
"buffer_load_dwordx4 v12, s[28:31], s81 offen offset:0 lds\n" \
"v_cvt_pk_bf16_f32 v158, v124, v125\n" \
"v_cvt_pk_bf16_f32 v159, v126, v127\n" \
"v_mfma_f32_16x16x32_bf16 v[168:171], v[180:183], v[152:155], v[168:171]\n" \
"ds_read_b64_tr_b16 a[220:221], v15 offset:24576\n" \
"v_mfma_f32_16x16x32_bf16 v[172:175], v[180:183], v[156:159], v[172:175]\n" \
"ds_read_b64_tr_b16 a[222:223], v16 offset:24576\n" \
"v_mfma_f32_16x16x32_bf16 v[192:195], a[128:131], v[144:147], v[192:195]\n" \
"s_add_u32 m0, s79, 140160\n" \
"v_exp_f32 v64, v64\n" \
"v_mfma_f32_16x16x32_bf16 v[196:199], a[132:135], v[144:147], v[196:199]\n" \
"buffer_load_dwordx4 v12, s[28:31], s81 offen offset:128 lds\n" \
"v_mfma_f32_16x16x32_bf16 v[224:227], a[128:131], v[148:151], v[224:227]\n" \
"s_mov_b32 s81, 655360\n" \
"ds_read_b128 a[152:155], v5 offset:55296\n" \
"v_mfma_f32_16x16x32_bf16 v[228:231], a[132:135], v[148:151], v[228:231]\n" \
"v_exp_f32 v65, v65\n" \
"v_mfma_f32_16x16x32_bf16 a[0:3], a[128:131], v[152:155], a[0:3]\n" \
"v_exp_f32 v66, v66\n" \
"v_mfma_f32_16x16x32_bf16 a[4:7], a[132:135], v[152:155], a[4:7]\n" \
"s_waitcnt lgkmcnt(1)\n" \
"v_exp_f32 v67, v67\n" \
"v_mfma_f32_16x16x32_bf16 a[32:35], a[128:131], v[156:159], a[32:35]\n" \
"ds_read_b128 a[156:159], v5 offset:56320\n" \
"v_cvt_pk_bf16_f32 v128, v64, v65\n" \
"v_mfma_f32_16x16x32_bf16 a[36:39], a[132:135], v[156:159], a[36:39]\n" \
"s_waitcnt vmcnt(17) lgkmcnt(4)\n" \
"v_exp_f32 v68, v68\n" \
"v_mfma_f32_16x16x32_bf16 v[200:203], a[136:139], v[144:147], v[200:203]\n" \
"LH18_%=:\ns_cbranch_execz LHR1_%=\n" \
"s_barrier\n" \
"v_xor_b32 v5, 0x10000, v5\n" \
"v_cvt_pk_bf16_f32 v129, v66, v67\n" \
"v_mfma_f32_16x16x32_bf16 v[204:207], a[140:143], v[144:147], v[204:207]\n" \
"s_add_u32 s17, s17, s19\n" \
"v_exp_f32 v69, v69\n" \
"v_mfma_f32_16x16x32_bf16 v[232:235], a[136:139], v[148:151], v[232:235]\n" \
"ds_read_b128 a[128:131], v5 offset:0\n" \
"v_cvt_pk_bf16_f32 v130, v68, v69\n" \
"v_mfma_f32_16x16x32_bf16 v[236:239], a[140:143], v[148:151], v[236:239]\n" \
"s_add_u32 m0, s48, 16384\n" \
"v_exp_f32 v70, v70\n" \
"v_mfma_f32_16x16x32_bf16 a[8:11], a[136:139], v[152:155], a[8:11]\n" \
"buffer_load_dwordx4 v6, s[40:43], s17 offen offset:0 sc1 lds\n" \
"v_mfma_f32_16x16x32_bf16 a[12:15], a[140:143], v[152:155], a[12:15]\n" \
"v_exp_f32 v71, v71\n" \
"v_mfma_f32_16x16x32_bf16 a[40:43], a[136:139], v[156:159], a[40:43]\n" \
"ds_read_b128 a[132:135], v5 offset:1024\n" \
"v_cvt_pk_bf16_f32 v131, v70, v71\n" \
"v_mfma_f32_16x16x32_bf16 a[44:47], a[140:143], v[156:159], a[44:47]\n" \
"s_waitcnt lgkmcnt(4)\n" \
"v_exp_f32 v72, v72\n" \
"v_mfma_f32_16x16x32_bf16 v[208:211], a[144:147], v[144:147], v[208:211]\n" \
"v_exp_f32 v73, v73\n" \
"v_mfma_f32_16x16x32_bf16 v[212:215], a[148:151], v[144:147], v[212:215]\n" \
"v_mfma_f32_16x16x32_bf16 v[240:243], a[144:147], v[148:151], v[240:243]\n" \
"ds_read_b128 a[136:139], v5 offset:2048\n" \
"v_cvt_pk_bf16_f32 v132, v72, v73\n" \
"v_mfma_f32_16x16x32_bf16 v[244:247], a[148:151], v[148:151], v[244:247]\n" \
"s_add_u32 m0, s48, 16384\n" \
"v_exp_f32 v74, v74\n" \
"v_mfma_f32_16x16x32_bf16 a[16:19], a[144:147], v[152:155], a[16:19]\n" \
"buffer_load_dwordx4 v6, s[40:43], s17 offen offset:1024 sc1 lds\n" \
"v_mfma_f32_16x16x32_bf16 a[20:23], a[148:151], v[152:155], a[20:23]\n" \
"v_exp_f32 v75, v75\n" \
"v_mfma_f32_16x16x32_bf16 a[48:51], a[144:147], v[156:159], a[48:51]\n" \
"ds_read_b128 a[140:143], v5 offset:3072\n" \
"v_cvt_pk_bf16_f32 v133, v74, v75\n" \
"v_mfma_f32_16x16x32_bf16 a[52:55], a[148:151], v[156:159], a[52:55]\n" \
"s_waitcnt lgkmcnt(4)\n" \
"s_add_u32 m0, s48, 16384\n" \
"v_mfma_f32_16x16x32_bf16 v[216:219], a[152:155], v[144:147], v[216:219]\n" \
"buffer_load_dwordx4 v6, s[40:43], s17 offen offset:2048 sc1 lds\n" \
"v_mfma_f32_16x16x32_bf16 v[220:223], a[156:159], v[144:147], v[220:223]\n" \
"s_add_u32 s44, s44, 1\n" \
"v_exp_f32 v76, v76\n" \
"v_mfma_f32_16x16x32_bf16 v[248:251], a[152:155], v[148:151], v[248:251]\n" \
"ds_read_b128 a[144:147], v5 offset:4096\n" \
"v_mfma_f32_16x16x32_bf16 v[252:255], a[156:159], v[148:151], v[252:255]\n" \
"s_add_u32 m0, s48, 16384\n" \
"v_exp_f32 v77, v77\n" \
"v_mfma_f32_16x16x32_bf16 a[24:27], a[152:155], v[152:155], a[24:27]\n" \
"buffer_load_dwordx4 v6, s[40:43], s17 offen offset:3072 sc1 lds\n" \
"v_mfma_f32_16x16x32_bf16 a[28:31], a[156:159], v[152:155], a[28:31]\n" \
"s_xor_b32 s48, s48, 0x10000\n" \
"v_exp_f32 v78, v78\n" \
"v_mfma_f32_16x16x32_bf16 a[56:59], a[152:155], v[156:159], a[56:59]\n" \
"ds_read_b128 a[148:151], v5 offset:5120\n" \
"v_exp_f32 v79, v79\n" \
"v_mfma_f32_16x16x32_bf16 a[60:63], a[156:159], v[156:159], a[60:63]\n" \
"s_waitcnt lgkmcnt(4)\n" \
"v_cvt_pk_bf16_f32 v134, v76, v77\n" \
"v_cvt_pk_bf16_f32 v135, v78, v79\n" \
"v_mfma_f32_16x16x32_bf16 v[96:99], a[128:131], v[32:35], 0\n" \
"buffer_load_dwordx4 a[112:115], v188, s[36:39], s84 offen sc1\n" \
"v_mfma_f32_16x16x32_bf16 v[96:99], a[132:135], v[36:39], v[96:99]\n" \
"s_add_u32 s16, s16, s9\n" \
"v_exp_f32 v80, v80\n" \
"v_mfma_f32_16x16x32_bf16 v[104:107], a[128:131], v[48:51], 0\n" \
"ds_read_b128 a[152:155], v5 offset:6144\n" \
"v_mfma_f32_16x16x32_bf16 v[104:107], a[132:135], v[52:55], v[104:107]\n" \
"s_add_u32 m0, s49, 32768\n" \
"v_exp_f32 v81, v81\n" \
"v_mfma_f32_16x16x32_bf16 v[112:115], a[128:131], a[64:67], 0\n" \
"buffer_load_dwordx4 v6, s[40:43], s16 offen offset:0 sc1 lds\n" \
"v_mfma_f32_16x16x32_bf16 v[112:115], a[132:135], a[68:71], v[112:115]\n" \
"v_exp_f32 v82, v82\n" \
"v_mfma_f32_16x16x32_bf16 v[120:123], a[128:131], a[80:83], 0\n" \
"ds_read_b128 a[156:159], v5 offset:7168\n" \
"v_cvt_pk_bf16_f32 v136, v80, v81\n" \
"v_mfma_f32_16x16x32_bf16 v[120:123], a[132:135], a[84:87], v[120:123]\n" \
"s_waitcnt lgkmcnt(4)\n" \
"v_exp_f32 v83, v83\n" \
"s_add_u32 m0, s79, 147456\n" \
"v_mfma_f32_16x16x32_bf16 v[96:99], a[136:139], v[40:43], v[96:99]\n" \
"buffer_load_dwordx4 v12, s[28:31], s81 offen offset:0 lds\n" \
"v_mfma_f32_16x16x32_bf16 v[96:99], a[140:143], v[44:47], v[96:99]\n" \
"v_exp_f32 v84, v84\n" \
"v_mfma_f32_16x16x32_bf16 v[104:107], a[136:139], v[56:59], v[104:107]\n" \
"ds_read_b128 a[128:131], v5 offset:16384\n" \
"v_cvt_pk_bf16_f32 v137, v82, v83\n" \
"v_mfma_f32_16x16x32_bf16 v[104:107], a[140:143], v[60:63], v[104:107]\n" \
"s_add_u32 m0, s49, 32768\n" \
"v_exp_f32 v85, v85\n" \
"v_mfma_f32_16x16x32_bf16 v[112:115], a[136:139], a[72:75], v[112:115]\n" \
"buffer_load_dwordx4 v6, s[40:43], s16 offen offset:1024 sc1 lds\n" \
"v_mfma_f32_16x16x32_bf16 v[112:115], a[140:143], a[76:79], v[112:115]\n" \
"v_exp_f32 v86, v86\n" \
"v_mfma_f32_16x16x32_bf16 v[120:123], a[136:139], a[88:91], v[120:123]\n" \
"ds_read_b128 a[132:135], v5 offset:17408\n" \
"v_cvt_pk_bf16_f32 v138, v84, v85\n" \
"v_mfma_f32_16x16x32_bf16 v[120:123], a[140:143], a[92:95], v[120:123]\n" \
"s_waitcnt lgkmcnt(4)\n" \
"v_exp_f32 v87, v87\n" \
"s_add_u32 m0, s79, 148352\n" \
"v_mfma_f32_16x16x32_bf16 v[100:103], a[144:147], v[32:35], 0\n" \
"buffer_load_dwordx4 v12, s[28:31], s81 offen offset:128 lds\n" \
"v_mfma_f32_16x16x32_bf16 v[100:103], a[148:151], v[36:39], v[100:103]\n" \
"s_mov_b32 s81, 720896\n" \
"v_exp_f32 v88, v88\n" \
"v_mfma_f32_16x16x32_bf16 v[108:111], a[144:147], v[48:51], 0\n" \
"ds_read_b128 a[136:139], v5 offset:18432\n" \
"v_cvt_pk_bf16_f32 v139, v86, v87\n" \
"v_mfma_f32_16x16x32_bf16 v[108:111], a[148:151], v[52:55], v[108:111]\n" \
"s_add_u32 m0, s49, 32768\n" \
"v_exp_f32 v89, v89\n" \
"v_mfma_f32_16x16x32_bf16 v[116:119], a[144:147], a[64:67], 0\n" \
"buffer_load_dwordx4 v6, s[40:43], s16 offen offset:2048 sc1 lds\n" \
"v_mfma_f32_16x16x32_bf16 v[116:119], a[148:151], a[68:71], v[116:119]\n" \
"v_exp_f32 v90, v90\n" \
"v_mfma_f32_16x16x32_bf16 v[124:127], a[144:147], a[80:83], 0\n" \
"ds_read_b128 a[140:143], v5 offset:19456\n" \
"v_cvt_pk_bf16_f32 v140, v88, v89\n" \
"v_mfma_f32_16x16x32_bf16 v[124:127], a[148:151], a[84:87], v[124:127]\n" \
"s_waitcnt lgkmcnt(4)\n" \
"v_exp_f32 v91, v91\n" \
"s_add_u32 m0, s79, 155648\n" \
"v_mfma_f32_16x16x32_bf16 v[100:103], a[152:155], v[40:43], v[100:103]\n" \
"buffer_load_dwordx4 v12, s[28:31], s81 offen offset:0 lds\n" \
"v_mfma_f32_16x16x32_bf16 v[100:103], a[156:159], v[44:47], v[100:103]\n" \
"v_exp_f32 v92, v92\n" \
"v_mfma_f32_16x16x32_bf16 v[108:111], a[152:155], v[56:59], v[108:111]\n" \
"ds_read_b128 a[144:147], v5 offset:20480\n" \
"v_cvt_pk_bf16_f32 v141, v90, v91\n" \
"v_mfma_f32_16x16x32_bf16 v[108:111], a[156:159], v[60:63], v[108:111]\n" \
"s_add_u32 m0, s49, 32768\n" \
"v_exp_f32 v93, v93\n" \
"v_mfma_f32_16x16x32_bf16 v[116:119], a[152:155], a[72:75], v[116:119]\n" \
"buffer_load_dwordx4 v6, s[40:43], s16 offen offset:3072 sc1 lds\n" \
"v_mfma_f32_16x16x32_bf16 v[116:119], a[156:159], a[76:79], v[116:119]\n" \
"v_exp_f32 v94, v94\n" \
"v_mfma_f32_16x16x32_bf16 v[124:127], a[152:155], a[88:91], v[124:127]\n" \
"ds_read_b128 a[148:151], v5 offset:21504\n" \
"v_cvt_pk_bf16_f32 v142, v92, v93\n" \
"v_mfma_f32_16x16x32_bf16 v[124:127], a[156:159], a[92:95], v[124:127]\n" \
"s_waitcnt lgkmcnt(4)\n" \
"v_exp_f32 v95, v95\n" \
"s_add_u32 m0, s79, 156544\n" \
"v_mfma_f32_16x16x32_bf16 v[160:163], v[180:183], v[128:131], v[160:163]\n" \
"buffer_load_dwordx4 v12, s[28:31], s81 offen offset:128 lds\n" \
"v_mfma_f32_16x16x32_bf16 v[164:167], v[180:183], v[132:135], v[164:167]\n" \
"v_cvt_pk_bf16_f32 v143, v94, v95\n" \
"v_mfma_f32_16x16x32_bf16 v[168:171], v[180:183], v[136:139], v[168:171]\n" \
"buffer_store_dwordx4 v[28:31], v4, s[36:39], s80 offen offset:3072 sc1\n" \
"v_mfma_f32_16x16x32_bf16 v[172:175], v[180:183], v[140:143], v[172:175]\n" \
"v_exp_f32 v96, v96\n" \
"v_mfma_f32_16x16x32_bf16 v[192:195], a[128:131], v[128:131], v[192:195]\n" \
"buffer_load_dwordx4 a[116:119], v189, s[36:39], s84 offen sc1\n" \
"v_mfma_f32_16x16x32_bf16 v[196:199], a[132:135], v[128:131], v[196:199]\n" \
"v_bfe_u32 v28, v4, 7, 1\n" \
"v_bfe_u32 v29, v4, 4, 3\n" \
"v_mfma_f32_16x16x32_bf16 v[224:227], a[128:131], v[132:135], v[224:227]\n" \
"ds_read_b128 a[152:155], v5 offset:22528\n" \
"v_lshl_add_u32 v28, v29, 1, v28\n" \
"v_mfma_f32_16x16x32_bf16 v[228:231], a[132:135], v[132:135], v[228:231]\n" \
"v_lshrrev_b32 v29, 8, v4\n" \
"v_lshl_add_u32 v28, v29, 4, v28\n" \
"v_mfma_f32_16x16x32_bf16 a[0:3], a[128:131], v[136:139], a[0:3]\n" \
"v_exp_f32 v97, v97\n" \
"v_mfma_f32_16x16x32_bf16 a[4:7], a[132:135], v[136:139], a[4:7]\n" \
"v_exp_f32 v98, v98\n" \
"v_mfma_f32_16x16x32_bf16 a[32:35], a[128:131], v[140:143], a[32:35]\n" \
"ds_read_b128 a[156:159], v5 offset:23552\n" \
"v_lshlrev_b32 v28, 4, v28\n" \
"v_mfma_f32_16x16x32_bf16 a[36:39], a[132:135], v[140:143], a[36:39]\n" \
"s_waitcnt vmcnt(15) lgkmcnt(4)\n" \
"v_exp_f32 v99, v99\n" \
"v_mfma_f32_16x16x32_bf16 v[200:203], a[136:139], v[128:131], v[200:203]\n" \
"s_barrier\n" \
"v_exp_f32 v100, v100\n" \
"v_mfma_f32_16x16x32_bf16 v[204:207], a[140:143], v[128:131], v[204:207]\n" \
"s_add_u32 s17, s17, s19\n" \
"v_exp_f32 v101, v101\n" \
"v_mfma_f32_16x16x32_bf16 v[232:235], a[136:139], v[132:135], v[232:235]\n" \
"ds_read_b128 a[128:131], v5 offset:32768\n" \
"v_add_u32 v29, 0x10000, v28\n" \
"v_mfma_f32_16x16x32_bf16 v[236:239], a[140:143], v[132:135], v[236:239]\n" \
"s_add_u32 m0, s49, 49152\n" \
"v_exp_f32 v102, v102\n" \
"v_mfma_f32_16x16x32_bf16 a[8:11], a[136:139], v[136:139], a[8:11]\n" \
"buffer_load_dwordx4 v6, s[40:43], s17 offen offset:0 sc1 lds\n" \
"v_mfma_f32_16x16x32_bf16 a[12:15], a[140:143], v[136:139], a[12:15]\n" \
"v_exp_f32 v103, v103\n" \
"v_mfma_f32_16x16x32_bf16 a[40:43], a[136:139], v[140:143], a[40:43]\n" \
"ds_read_b128 a[132:135], v5 offset:33792\n" \
"v_add_u32 v30, 0x20000, v28\n" \
"v_mfma_f32_16x16x32_bf16 a[44:47], a[140:143], v[140:143], a[44:47]\n" \
"s_waitcnt lgkmcnt(4)\n" \
"v_exp_f32 v104, v104\n" \
"v_mfma_f32_16x16x32_bf16 v[208:211], a[144:147], v[128:131], v[208:211]\n" \
"v_exp_f32 v105, v105\n" \
"v_mfma_f32_16x16x32_bf16 v[212:215], a[148:151], v[128:131], v[212:215]\n" \
"v_mfma_f32_16x16x32_bf16 v[240:243], a[144:147], v[132:135], v[240:243]\n" \
"ds_read_b128 a[136:139], v5 offset:34816\n" \
"v_add_u32 v31, s22, v4\n" \
"v_mfma_f32_16x16x32_bf16 v[244:247], a[148:151], v[132:135], v[244:247]\n" \
"s_add_u32 m0, s49, 49152\n" \
"v_exp_f32 v106, v106\n" \
"v_mfma_f32_16x16x32_bf16 a[16:19], a[144:147], v[136:139], a[16:19]\n" \
"buffer_load_dwordx4 v6, s[40:43], s17 offen offset:1024 sc1 lds\n" \
"v_mfma_f32_16x16x32_bf16 a[20:23], a[148:151], v[136:139], a[20:23]\n" \
"v_exp_f32 v107, v107\n" \
"v_mfma_f32_16x16x32_bf16 a[48:51], a[144:147], v[140:143], a[48:51]\n" \
"ds_read_b128 a[140:143], v5 offset:35840\n" \
"v_cvt_pk_bf16_f32 v144, v96, v97\n" \
"v_mfma_f32_16x16x32_bf16 a[52:55], a[148:151], v[140:143], a[52:55]\n" \
"s_waitcnt lgkmcnt(4)\n" \
"s_add_u32 m0, s49, 49152\n" \
"ds_read_b64_tr_b16 a[224:225], v13 offset:0\n" \
"v_mfma_f32_16x16x32_bf16 v[216:219], a[152:155], v[128:131], v[216:219]\n" \
"buffer_load_dwordx4 v6, s[40:43], s17 offen offset:2048 sc1 lds\n" \
"v_mfma_f32_16x16x32_bf16 v[220:223], a[156:159], v[128:131], v[220:223]\n" \
"v_exp_f32 v108, v108\n" \
"v_mfma_f32_16x16x32_bf16 v[248:251], a[152:155], v[132:135], v[248:251]\n" \
"ds_read_b128 a[144:147], v5 offset:36864\n" \
"v_cvt_pk_bf16_f32 v145, v98, v99\n" \
"v_mfma_f32_16x16x32_bf16 v[252:255], a[156:159], v[132:135], v[252:255]\n" \
"s_add_u32 m0, s49, 49152\n" \
"v_exp_f32 v109, v109\n" \
"v_mfma_f32_16x16x32_bf16 a[24:27], a[152:155], v[136:139], a[24:27]\n" \
"buffer_load_dwordx4 v6, s[40:43], s17 offen offset:3072 sc1 lds\n" \
"v_mfma_f32_16x16x32_bf16 a[28:31], a[156:159], v[136:139], a[28:31]\n" \
"v_exp_f32 v110, v110\n" \
"v_mfma_f32_16x16x32_bf16 a[56:59], a[152:155], v[140:143], a[56:59]\n" \
"ds_read_b128 a[148:151], v5 offset:37888\n" \
"v_exp_f32 v111, v111\n" \
"v_mfma_f32_16x16x32_bf16 a[60:63], a[156:159], v[140:143], a[60:63]\n" \
"s_waitcnt lgkmcnt(4)\n" \
"ds_read_b64_tr_b16 a[226:227], v14 offset:0\n" \
"v_cvt_pk_bf16_f32 v146, v100, v101\n" \
"v_cvt_pk_bf16_f32 v147, v102, v103\n" \
"v_cvt_pk_bf16_f32 v148, v104, v105\n" \
"v_cvt_pk_bf16_f32 v149, v106, v107\n" \
"v_cvt_pk_bf16_f32 v150, v108, v109\n" \
"v_cvt_pk_bf16_f32 v151, v110, v111\n" \
"v_mfma_f32_16x16x32_bf16 v[64:67], a[128:131], v[32:35], 0\n" \
"buffer_load_dwordx4 a[120:123], v188, s[36:39], s85 offen sc1\n" \
"v_mfma_f32_16x16x32_bf16 v[64:67], a[132:135], v[36:39], v[64:67]\n" \
"s_add_u32 s16, s16, s9\n" \
"ds_read_b64_tr_b16 a[228:229], v15 offset:0\n" \
"v_mfma_f32_16x16x32_bf16 v[72:75], a[128:131], v[48:51], 0\n" \
"s_add_u32 s100, s100, 262144\ns_addc_u32 s101, s101, 0\n" \
"ds_read_b128 a[152:155], v5 offset:38912\n" \
"v_mfma_f32_16x16x32_bf16 v[72:75], a[132:135], v[52:55], v[72:75]\n" \
"s_add_u32 m0, s48, 0\n" \
"ds_read_b64_tr_b16 a[230:231], v16 offset:0\n" \
"v_mfma_f32_16x16x32_bf16 v[80:83], a[128:131], a[64:67], 0\n" \
"buffer_load_dwordx4 v6, s[40:43], s16 offen offset:0 sc1 lds\n" \
"v_mfma_f32_16x16x32_bf16 v[80:83], a[132:135], a[68:71], v[80:83]\n" \
"ds_read_b64_tr_b16 a[232:233], v13 offset:8192\n" \
"v_mfma_f32_16x16x32_bf16 v[88:91], a[128:131], a[80:83], 0\n" \
"ds_read_b128 a[156:159], v5 offset:39936\n" \
"v_mfma_f32_16x16x32_bf16 v[88:91], a[132:135], a[84:87], v[88:91]\n" \
"s_waitcnt lgkmcnt(4)\n" \
"v_exp_f32 v112, v112\n" \
"v_mfma_f32_16x16x32_bf16 v[64:67], a[136:139], v[40:43], v[64:67]\n" \
"buffer_load_dwordx4 a[124:127], v189, s[36:39], s85 offen sc1\n" \
"v_mfma_f32_16x16x32_bf16 v[64:67], a[140:143], v[44:47], v[64:67]\n" \
"s_xor_b32 s49, s49, 0x10000\n" \
"v_exp_f32 v113, v113\n" \
"v_mfma_f32_16x16x32_bf16 v[72:75], a[136:139], v[56:59], v[72:75]\n" \
"ds_read_b128 a[128:131], v5 offset:49152\n" \
"v_cvt_pk_bf16_f32 v152, v112, v113\n" \
"v_mfma_f32_16x16x32_bf16 v[72:75], a[140:143], v[60:63], v[72:75]\n" \
"s_add_u32 m0, s48, 0\n" \
"v_exp_f32 v114, v114\n" \
"v_mfma_f32_16x16x32_bf16 v[80:83], a[136:139], a[72:75], v[80:83]\n" \
"buffer_load_dwordx4 v6, s[40:43], s16 offen offset:1024 sc1 lds\n" \
"v_mfma_f32_16x16x32_bf16 v[80:83], a[140:143], a[76:79], v[80:83]\n" \
"v_exp_f32 v115, v115\n" \
"v_mfma_f32_16x16x32_bf16 v[88:91], a[136:139], a[88:91], v[88:91]\n" \
"ds_read_b128 a[132:135], v5 offset:50176\n" \
"v_cvt_pk_bf16_f32 v153, v114, v115\n" \
"v_mfma_f32_16x16x32_bf16 v[88:91], a[140:143], a[92:95], v[88:91]\n" \
"s_waitcnt lgkmcnt(4)\n" \
"v_exp_f32 v116, v116\n" \
"v_mfma_f32_16x16x32_bf16 v[68:71], a[144:147], v[32:35], 0\n" \
"v_exp_f32 v117, v117\n" \
"v_mfma_f32_16x16x32_bf16 v[68:71], a[148:151], v[36:39], v[68:71]\n" \
"v_exp_f32 v118, v118\n" \
"v_mfma_f32_16x16x32_bf16 v[76:79], a[144:147], v[48:51], 0\n" \
"ds_read_b128 a[136:139], v5 offset:51200\n" \
"v_cvt_pk_bf16_f32 v154, v116, v117\n" \
"v_mfma_f32_16x16x32_bf16 v[76:79], a[148:151], v[52:55], v[76:79]\n" \
"s_add_u32 m0, s48, 0\n" \
"v_exp_f32 v119, v119\n" \
"v_mfma_f32_16x16x32_bf16 v[84:87], a[144:147], a[64:67], 0\n" \
"buffer_load_dwordx4 v6, s[40:43], s16 offen offset:2048 sc1 lds\n" \
"v_mfma_f32_16x16x32_bf16 v[84:87], a[148:151], a[68:71], v[84:87]\n" \
"v_exp_f32 v120, v120\n" \
"v_mfma_f32_16x16x32_bf16 v[92:95], a[144:147], a[80:83], 0\n" \
"ds_read_b128 a[140:143], v5 offset:52224\n" \
"v_cvt_pk_bf16_f32 v155, v118, v119\n" \
"v_mfma_f32_16x16x32_bf16 v[92:95], a[148:151], a[84:87], v[92:95]\n" \
"s_waitcnt lgkmcnt(4)\n" \
"v_exp_f32 v121, v121\n" \
"v_mfma_f32_16x16x32_bf16 v[68:71], a[152:155], v[40:43], v[68:71]\n" \
"v_exp_f32 v122, v122\n" \
"v_mfma_f32_16x16x32_bf16 v[68:71], a[156:159], v[44:47], v[68:71]\n" \
"v_exp_f32 v123, v123\n" \
"v_mfma_f32_16x16x32_bf16 v[76:79], a[152:155], v[56:59], v[76:79]\n" \
"ds_read_b128 a[144:147], v5 offset:53248\n" \
"v_cvt_pk_bf16_f32 v156, v120, v121\n" \
"v_mfma_f32_16x16x32_bf16 v[76:79], a[156:159], v[60:63], v[76:79]\n" \
"s_add_u32 m0, s48, 0\n" \
"v_exp_f32 v124, v124\n" \
"v_mfma_f32_16x16x32_bf16 v[84:87], a[152:155], a[72:75], v[84:87]\n" \
"buffer_load_dwordx4 v6, s[40:43], s16 offen offset:3072 sc1 lds\n" \
"v_mfma_f32_16x16x32_bf16 v[84:87], a[156:159], a[76:79], v[84:87]\n" \
"v_exp_f32 v125, v125\n" \
"v_mfma_f32_16x16x32_bf16 v[92:95], a[152:155], a[88:91], v[92:95]\n" \
"ds_read_b128 a[148:151], v5 offset:54272\n" \
"v_cvt_pk_bf16_f32 v157, v122, v123\n" \
"v_mfma_f32_16x16x32_bf16 v[92:95], a[156:159], a[92:95], v[92:95]\n" \
"s_waitcnt lgkmcnt(4)\n" \
"v_exp_f32 v126, v126\n" \
"v_mfma_f32_16x16x32_bf16 v[160:163], v[180:183], v[144:147], v[160:163]\n" \
"s_lshl_b32 s25, s45, 2\n" \
"v_exp_f32 v127, v127\n" \
"v_mfma_f32_16x16x32_bf16 v[164:167], v[180:183], v[148:151], v[164:167]\n" \
"buffer_store_dwordx2 v[10:11], v20, s[36:39], s12 offen sc1\n" \
"v_cvt_pk_bf16_f32 v158, v124, v125\n" \
"v_cvt_pk_bf16_f32 v159, v126, v127\n" \
"v_mfma_f32_16x16x32_bf16 v[168:171], v[180:183], v[152:155], v[168:171]\n" \
"s_andn2_b32 s25, 32, s25\n" \
"ds_read_b64_tr_b16 a[234:235], v14 offset:8192\n" \
"s_add_u32 s25, s25, %2\n" \
"v_mfma_f32_16x16x32_bf16 v[172:175], v[180:183], v[156:159], v[172:175]\n" \
"s_mov_b64 exec, 0xffff\nglobal_load_dword v1, v2, s[100:101]\ns_mov_b64 exec, -1\n" \
"v_mfma_f32_16x16x32_bf16 v[192:195], a[128:131], v[144:147], v[192:195]\n" \
"s_cmp_ge_u32 s25, 34\ns_cselect_b64 vcc, -1, 0\n" \
"ds_read_b64_tr_b16 a[236:237], v15 offset:8192\n" \
"v_mfma_f32_16x16x32_bf16 v[196:199], a[132:135], v[144:147], v[196:199]\n" \
"ds_read_b64_tr_b16 a[238:239], v16 offset:8192\n" \
"v_bfe_u32 v190, v4, 7, 2\n" \
"v_mfma_f32_16x16x32_bf16 v[224:227], a[128:131], v[148:151], v[224:227]\n" \
"ds_read_b128 a[152:155], v5 offset:55296\n" \
"v_min_u32 v190, 2, v190\n" \
"v_mfma_f32_16x16x32_bf16 v[228:231], a[132:135], v[148:151], v[228:231]\n" \
"v_exp_f32 v64, v64\n" \
"v_mfma_f32_16x16x32_bf16 a[0:3], a[128:131], v[152:155], a[0:3]\n" \
"v_exp_f32 v65, v65\n" \
"v_mfma_f32_16x16x32_bf16 a[4:7], a[132:135], v[152:155], a[4:7]\n" \
"s_waitcnt lgkmcnt(1)\n" \
"v_exp_f32 v66, v66\n" \
"v_mfma_f32_16x16x32_bf16 a[32:35], a[128:131], v[156:159], a[32:35]\n" \
"ds_read_b128 a[156:159], v5 offset:56320\n" \
"v_lshl_add_u32 v17, v190, 8, s98\n" \
"v_mfma_f32_16x16x32_bf16 a[36:39], a[132:135], v[156:159], a[36:39]\n" \
"s_waitcnt vmcnt(14) lgkmcnt(4)\n" \
"v_exp_f32 v67, v67\n" \
"v_mfma_f32_16x16x32_bf16 v[200:203], a[136:139], v[144:147], v[200:203]\n" \
"s_barrier\n" \
"v_xor_b32 v5, 0x10000, v5\n" \
"v_bfe_u32 v190, v4, 4, 3\n" \
"v_mfma_f32_16x16x32_bf16 v[204:207], a[140:143], v[144:147], v[204:207]\n" \
"s_add_u32 s17, s17, s19\n" \
"v_exp_f32 v68, v68\n" \
"v_mfma_f32_16x16x32_bf16 v[232:235], a[136:139], v[148:151], v[232:235]\n" \
"s_lshr_b32 s92, %1, 5\n" \
"ds_read_b128 a[128:131], v5 offset:0\n" \
"v_lshl_add_u32 v17, v190, 3, v17\n" \
"v_mfma_f32_16x16x32_bf16 v[236:239], a[140:143], v[148:151], v[236:239]\n" \
"s_add_u32 m0, s48, 16384\n" \
"v_exp_f32 v69, v69\n" \
"v_mfma_f32_16x16x32_bf16 a[8:11], a[136:139], v[152:155], a[8:11]\n" \
"buffer_load_dwordx4 v6, s[40:43], s17 offen offset:0 sc1 lds\n" \
"v_mfma_f32_16x16x32_bf16 a[12:15], a[140:143], v[152:155], a[12:15]\n" \
"s_lshl_b32 s92, s92, 2\n" \
"v_exp_f32 v70, v70\n" \
"v_mfma_f32_16x16x32_bf16 a[40:43], a[136:139], v[156:159], a[40:43]\n" \
"s_add_u32 s92, s92, %2\n" \
"ds_read_b128 a[132:135], v5 offset:1024\n" \
"s_lshl_b32 s92, s92, 11\n" \
"v_mfma_f32_16x16x32_bf16 a[44:47], a[140:143], v[156:159], a[44:47]\n" \
"s_waitcnt lgkmcnt(4)\n" \
"v_exp_f32 v71, v71\n" \
"v_mfma_f32_16x16x32_bf16 v[208:211], a[144:147], v[144:147], v[208:211]\n" \
"s_waitcnt vmcnt(14)\n" \
"v_exp_f32 v72, v72\n" \
"v_mfma_f32_16x16x32_bf16 v[212:215], a[148:151], v[144:147], v[212:215]\n" \
"v_exp_f32 v73, v73\n" \
"v_mfma_f32_16x16x32_bf16 v[240:243], a[144:147], v[148:151], v[240:243]\n" \
"ds_read_b128 a[136:139], v5 offset:2048\n" \
"v_and_b32 v188, 0xf0, v4\n" \
"v_mfma_f32_16x16x32_bf16 v[244:247], a[148:151], v[148:151], v[244:247]\n" \
"s_add_u32 m0, s48, 16384\n" \
"v_exp_f32 v74, v74\n" \
"v_mfma_f32_16x16x32_bf16 a[16:19], a[144:147], v[152:155], a[16:19]\n" \
"buffer_load_dwordx4 v6, s[40:43], s17 offen offset:1024 sc1 lds\n" \
"v_mfma_f32_16x16x32_bf16 a[20:23], a[148:151], v[152:155], a[20:23]\n" \
"v_exp_f32 v75, v75\n" \
"v_mfma_f32_16x16x32_bf16 a[48:51], a[144:147], v[156:159], a[48:51]\n" \
"ds_read_b128 a[140:143], v5 offset:3072\n" \
"v_lshl_add_u32 v188, v188, 3, s92\n" \
"v_mfma_f32_16x16x32_bf16 a[52:55], a[148:151], v[156:159], a[52:55]\n" \
"s_waitcnt lgkmcnt(4)\n" \
"s_add_u32 m0, s48, 16384\n" \
"ds_read_b64_tr_b16 a[240:241], v13 offset:16384\n" \
"v_mfma_f32_16x16x32_bf16 v[216:219], a[152:155], v[144:147], v[216:219]\n" \
"buffer_load_dwordx4 v6, s[40:43], s17 offen offset:2048 sc1 lds\n" \
"v_mfma_f32_16x16x32_bf16 v[220:223], a[156:159], v[144:147], v[220:223]\n" \
"s_add_u32 s44, s44, 1\n" \
"v_exp_f32 v76, v76\n" \
"v_mfma_f32_16x16x32_bf16 v[248:251], a[152:155], v[148:151], v[248:251]\n" \
"ds_read_b128 a[144:147], v5 offset:4096\n" \
"v_lshrrev_b32 v189, 8, v4\n" \
"v_mfma_f32_16x16x32_bf16 v[252:255], a[156:159], v[148:151], v[252:255]\n" \
"s_add_u32 m0, s48, 16384\n" \
"v_exp_f32 v77, v77\n" \
"v_mfma_f32_16x16x32_bf16 a[24:27], a[152:155], v[152:155], a[24:27]\n" \
"buffer_load_dwordx4 v6, s[40:43], s17 offen offset:3072 sc1 lds\n" \
"v_mfma_f32_16x16x32_bf16 a[28:31], a[156:159], v[152:155], a[28:31]\n" \
"s_xor_b32 s48, s48, 0x10000\n" \
"v_exp_f32 v78, v78\n" \
"v_mfma_f32_16x16x32_bf16 a[56:59], a[152:155], v[156:159], a[56:59]\n" \
"ds_read_b128 a[148:151], v5 offset:5120\n" \
"v_lshl_add_u32 v190, v189, 18, v188\n" \
"v_exp_f32 v79, v79\n" \
"v_mfma_f32_16x16x32_bf16 a[60:63], a[156:159], v[156:159], a[60:63]\n" \
"s_waitcnt lgkmcnt(4)\n" \
"ds_read_b64_tr_b16 a[242:243], v14 offset:16384\n" \
"v_min_u32 v189, 2, v189\n" \
"v_cvt_pk_bf16_f32 v128, v64, v65\n" \
"v_cvt_pk_bf16_f32 v129, v66, v67\n" \
"v_cvt_pk_bf16_f32 v130, v68, v69\n" \
"v_cvt_pk_bf16_f32 v131, v70, v71\n" \
"v_cvt_pk_bf16_f32 v132, v72, v73\n" \
"v_cvt_pk_bf16_f32 v133, v74, v75\n" \
"v_cvt_pk_bf16_f32 v134, v76, v77\n" \
"v_cvt_pk_bf16_f32 v135, v78, v79\n" \
"v_mfma_f32_16x16x32_bf16 v[96:99], a[128:131], v[32:35], 0\n" \
"buffer_store_dwordx2 v[10:11], v20, s[36:39], s88 offen sc1\n" \
"v_mfma_f32_16x16x32_bf16 v[96:99], a[132:135], v[36:39], v[96:99]\n" \
"s_add_u32 s16, s16, s9\n" \
"ds_read_b64_tr_b16 a[244:245], v15 offset:16384\n" \
"s_mul_i32 s92, s90, 12\n" \
"v_mfma_f32_16x16x32_bf16 v[104:107], a[128:131], v[48:51], 0\n" \
"s_add_u32 s92, s92, s89\n" \
"ds_read_b128 a[152:155], v5 offset:6144\n" \
"s_lshl_b32 s92, s92, 16\n" \
"v_mfma_f32_16x16x32_bf16 v[104:107], a[132:135], v[52:55], v[104:107]\n" \
"s_add_u32 m0, s49, 32768\n" \
"ds_read_b64_tr_b16 a[246:247], v16 offset:16384\n" \
"v_lshl_add_u32 v191, v189, 18, v188\n" \
"v_mfma_f32_16x16x32_bf16 v[112:115], a[128:131], a[64:67], 0\n" \
"buffer_load_dwordx4 v6, s[40:43], s16 offen offset:0 sc1 lds\n" \
"v_mfma_f32_16x16x32_bf16 v[112:115], a[132:135], a[68:71], v[112:115]\n" \
"s_mov_b32 s46, s23\n" \
"ds_read_b64_tr_b16 a[248:249], v13 offset:24576\n" \
"v_add_u32 v191, s92, v191\n" \
"v_mfma_f32_16x16x32_bf16 v[120:123], a[128:131], a[80:83], 0\n" \
"s_lshl_b32 s92, %2, 13\n" \
"ds_read_b128 a[156:159], v5 offset:7168\n" \
"s_add_u32 s92, s92, 131072\n" \
"v_mfma_f32_16x16x32_bf16 v[120:123], a[132:135], a[84:87], v[120:123]\n" \
"s_waitcnt lgkmcnt(4)\n" \
"v_add_u32 v186, s92, v4\n" \
"v_xor_b32 v187, 0x6000, v186\n" \
"v_mfma_f32_16x16x32_bf16 v[96:99], a[136:139], v[40:43], v[96:99]\n" \
"s_lshl_b32 s92, %2, 9\n" \
"v_exp_f32 v80, v80\n" \
"v_mfma_f32_16x16x32_bf16 v[96:99], a[140:143], v[44:47], v[96:99]\n" \
"s_add_u32 s92, s92, 65536\n" \
"v_exp_f32 v81, v81\n" \
"v_mfma_f32_16x16x32_bf16 v[104:107], a[136:139], v[56:59], v[104:107]\n" \
"ds_read_b128 a[128:131], v5 offset:16384\n" \
"v_lshrrev_b32 v184, 2, v4\n" \
"v_mfma_f32_16x16x32_bf16 v[104:107], a[140:143], v[60:63], v[104:107]\n" \
"s_add_u32 m0, s49, 32768\n" \
"v_exp_f32 v82, v82\n" \
"v_mfma_f32_16x16x32_bf16 v[112:115], a[136:139], a[72:75], v[112:115]\n" \
"buffer_load_dwordx4 v6, s[40:43], s16 offen offset:1024 sc1 lds\n" \
"v_mfma_f32_16x16x32_bf16 v[112:115], a[140:143], a[76:79], v[112:115]\n" \
"v_exp_f32 v83, v83\n" \
"v_mfma_f32_16x16x32_bf16 v[120:123], a[136:139], a[88:91], v[120:123]\n" \
"ds_read_b128 a[132:135], v5 offset:17408\n" \
"v_add_u32 v184, s92, v184\n" \
"v_mfma_f32_16x16x32_bf16 v[120:123], a[140:143], a[92:95], v[120:123]\n" \
"s_waitcnt lgkmcnt(4)\n" \
"v_exp_f32 v84, v84\n" \
"v_mfma_f32_16x16x32_bf16 v[100:103], a[144:147], v[32:35], 0\n" \
"v_exp_f32 v85, v85\n" \
"v_mfma_f32_16x16x32_bf16 v[100:103], a[148:151], v[36:39], v[100:103]\n" \
"v_exp_f32 v86, v86\n" \
"v_mfma_f32_16x16x32_bf16 v[108:111], a[144:147], v[48:51], 0\n" \
"ds_read_b128 a[136:139], v5 offset:18432\n" \
"v_xor_b32 v185, 0x600, v184\n" \
"v_mfma_f32_16x16x32_bf16 v[108:111], a[148:151], v[52:55], v[108:111]\n" \
"s_add_u32 m0, s49, 32768\n" \
"v_exp_f32 v87, v87\n" \
"v_mfma_f32_16x16x32_bf16 v[116:119], a[144:147], a[64:67], 0\n" \
"buffer_load_dwordx4 v6, s[40:43], s16 offen offset:2048 sc1 lds\n" \
"v_mfma_f32_16x16x32_bf16 v[116:119], a[148:151], a[68:71], v[116:119]\n" \
"v_exp_f32 v88, v88\n" \
"v_mfma_f32_16x16x32_bf16 v[124:127], a[144:147], a[80:83], 0\n" \
"s_add_u32 s100, s100, 262144\ns_addc_u32 s101, s101, 0\n" \
"ds_read_b128 a[140:143], v5 offset:19456\n" \
"v_mfma_f32_16x16x32_bf16 v[124:127], a[148:151], a[84:87], v[124:127]\n" \
"s_waitcnt lgkmcnt(4)\n" \
"v_exp_f32 v89, v89\n" \
"v_mfma_f32_16x16x32_bf16 v[100:103], a[152:155], v[40:43], v[100:103]\n" \
"v_exp_f32 v90, v90\n" \
"v_mfma_f32_16x16x32_bf16 v[100:103], a[156:159], v[44:47], v[100:103]\n" \
"v_exp_f32 v91, v91\n" \
"v_mfma_f32_16x16x32_bf16 v[108:111], a[152:155], v[56:59], v[108:111]\n" \
"ds_read_b128 a[144:147], v5 offset:20480\n" \
"v_cvt_pk_bf16_f32 v136, v80, v81\n" \
"v_mfma_f32_16x16x32_bf16 v[108:111], a[156:159], v[60:63], v[108:111]\n" \
"s_add_u32 m0, s49, 32768\n" \
"v_exp_f32 v92, v92\n" \
"v_mfma_f32_16x16x32_bf16 v[116:119], a[152:155], a[72:75], v[116:119]\n" \
"buffer_load_dwordx4 v6, s[40:43], s16 offen offset:3072 sc1 lds\n" \
"v_mfma_f32_16x16x32_bf16 v[116:119], a[156:159], a[76:79], v[116:119]\n" \
"v_exp_f32 v93, v93\n" \
"v_mfma_f32_16x16x32_bf16 v[124:127], a[152:155], a[88:91], v[124:127]\n" \
"ds_read_b128 a[148:151], v5 offset:21504\n" \
"v_cvt_pk_bf16_f32 v137, v82, v83\n" \
"v_mfma_f32_16x16x32_bf16 v[124:127], a[156:159], a[92:95], v[124:127]\n" \
"s_waitcnt lgkmcnt(4)\n" \
"v_exp_f32 v94, v94\n" \
"v_mfma_f32_16x16x32_bf16 v[160:163], v[180:183], v[128:131], v[160:163]\n" \
"v_exp_f32 v95, v95\n" \
"v_cvt_pk_bf16_f32 v138, v84, v85\n" \
"v_cvt_pk_bf16_f32 v139, v86, v87\n" \
"v_mfma_f32_16x16x32_bf16 v[164:167], v[180:183], v[132:135], v[164:167]\n" \
"s_mov_b64 exec, 0xffff\nglobal_load_dword v1, v2, s[100:101]\ns_mov_b64 exec, -1\n" \
"v_cvt_pk_bf16_f32 v140, v88, v89\n" \
"v_cvt_pk_bf16_f32 v141, v90, v91\n" \
"v_cvt_pk_bf16_f32 v142, v92, v93\n" \
"v_cvt_pk_bf16_f32 v143, v94, v95\n" \
"v_mfma_f32_16x16x32_bf16 v[168:171], v[180:183], v[136:139], v[168:171]\n" \
"s_add_u32 s100, s100, 262144\ns_addc_u32 s101, s101, 0\n" \
"ds_read_b64_tr_b16 a[250:251], v14 offset:24576\n" \
"v_mfma_f32_16x16x32_bf16 v[172:175], v[180:183], v[140:143], v[172:175]\n" \
"ds_read_b64_tr_b16 a[252:253], v15 offset:24576\n" \
"v_mfma_f32_16x16x32_bf16 v[192:195], a[128:131], v[128:131], v[192:195]\n" \
"ds_read_b64_tr_b16 a[254:255], v16 offset:24576\n" \
"v_mfma_f32_16x16x32_bf16 v[196:199], a[132:135], v[128:131], v[196:199]\n" \
"s_mov_b64 exec, 0xffff\nglobal_load_dword v1, v2, s[100:101]\ns_mov_b64 exec, -1\n" \
"v_mfma_f32_16x16x32_bf16 v[224:227], a[128:131], v[132:135], v[224:227]\n" \
"ds_read_b128 a[152:155], v5 offset:22528\n" \
"v_mov_b32 v2, v190\n" \
"v_mfma_f32_16x16x32_bf16 v[228:231], a[132:135], v[132:135], v[228:231]\n" \
"v_exp_f32 v96, v96\n" \
"v_mfma_f32_16x16x32_bf16 a[0:3], a[128:131], v[136:139], a[0:3]\n" \
"v_exp_f32 v97, v97\n" \
"v_mfma_f32_16x16x32_bf16 a[4:7], a[132:135], v[136:139], a[4:7]\n" \
"s_waitcnt lgkmcnt(1)\n" \
"v_exp_f32 v98, v98\n" \
"v_mfma_f32_16x16x32_bf16 a[32:35], a[128:131], v[140:143], a[32:35]\n" \
"ds_read_b128 a[156:159], v5 offset:23552\n" \
"v_cvt_pk_bf16_f32 v144, v96, v97\n" \
"v_mfma_f32_16x16x32_bf16 a[36:39], a[132:135], v[140:143], a[36:39]\n" \
"s_waitcnt vmcnt(19) lgkmcnt(4)\n" \
"v_exp_f32 v99, v99\n" \
"v_mfma_f32_16x16x32_bf16 v[200:203], a[136:139], v[128:131], v[200:203]\n" \
"s_barrier\n" \
"v_exp_f32 v100, v100\n" \
"v_mfma_f32_16x16x32_bf16 v[204:207], a[140:143], v[128:131], v[204:207]\n" \
"s_add_u32 s17, s17, s19\n" \
"v_exp_f32 v101, v101\n" \
"v_mfma_f32_16x16x32_bf16 v[232:235], a[136:139], v[132:135], v[232:235]\n" \
"ds_read_b128 a[128:131], v5 offset:32768\n" \
"v_cvt_pk_bf16_f32 v145, v98, v99\n" \
"v_mfma_f32_16x16x32_bf16 v[236:239], a[140:143], v[132:135], v[236:239]\n" \
"s_add_u32 m0, s49, 49152\n" \
"v_exp_f32 v102, v102\n" \
"v_mfma_f32_16x16x32_bf16 a[8:11], a[136:139], v[136:139], a[8:11]\n" \
"buffer_load_dwordx4 v6, s[40:43], s17 offen offset:0 sc1 lds\n" \
"v_mfma_f32_16x16x32_bf16 a[12:15], a[140:143], v[136:139], a[12:15]\n" \
"v_exp_f32 v103, v103\n" \
"v_mfma_f32_16x16x32_bf16 a[40:43], a[136:139], v[140:143], a[40:43]\n" \
"ds_read_b128 a[132:135], v5 offset:33792\n" \
"v_cvt_pk_bf16_f32 v146, v100, v101\n" \
"v_mfma_f32_16x16x32_bf16 a[44:47], a[140:143], v[140:143], a[44:47]\n" \
"s_waitcnt lgkmcnt(4)\n" \
"v_exp_f32 v104, v104\n" \
"v_mfma_f32_16x16x32_bf16 v[208:211], a[144:147], v[128:131], v[208:211]\n" \
"LH19_%=:\ns_cbranch_execz LHR1_%=\n" \
"v_exp_f32 v105, v105\n" \
"v_mfma_f32_16x16x32_bf16 v[212:215], a[148:151], v[128:131], v[212:215]\n" \
"v_mfma_f32_16x16x32_bf16 v[240:243], a[144:147], v[132:135], v[240:243]\n" \
"ds_read_b128 a[136:139], v5 offset:34816\n" \
"v_cvt_pk_bf16_f32 v147, v102, v103\n" \
"v_mfma_f32_16x16x32_bf16 v[244:247], a[148:151], v[132:135], v[244:247]\n" \
"s_add_u32 m0, s49, 49152\n" \
"v_exp_f32 v106, v106\n" \
"v_mfma_f32_16x16x32_bf16 a[16:19], a[144:147], v[136:139], a[16:19]\n" \
"buffer_load_dwordx4 v6, s[40:43], s17 offen offset:1024 sc1 lds\n" \
"v_mfma_f32_16x16x32_bf16 a[20:23], a[148:151], v[136:139], a[20:23]\n" \
"v_exp_f32 v107, v107\n" \
"v_mfma_f32_16x16x32_bf16 a[48:51], a[144:147], v[140:143], a[48:51]\n" \
"ds_read_b128 a[140:143], v5 offset:35840\n" \
"v_cvt_pk_bf16_f32 v148, v104, v105\n" \
"v_mfma_f32_16x16x32_bf16 a[52:55], a[148:151], v[140:143], a[52:55]\n" \
"s_waitcnt lgkmcnt(4)\n" \
"s_add_u32 m0, s49, 49152\n" \
"v_cvt_pk_bf16_f32 v149, v106, v107\n" \
"v_mfma_f32_16x16x32_bf16 v[216:219], a[152:155], v[128:131], v[216:219]\n" \
"buffer_load_dwordx4 v6, s[40:43], s17 offen offset:2048 sc1 lds\n" \
"v_mfma_f32_16x16x32_bf16 v[220:223], a[156:159], v[128:131], v[220:223]\n" \
"v_exp_f32 v108, v108\n" \
"v_mfma_f32_16x16x32_bf16 v[248:251], a[152:155], v[132:135], v[248:251]\n" \
"ds_read_b128 a[144:147], v5 offset:36864\n" \
"v_mfma_f32_16x16x32_bf16 v[252:255], a[156:159], v[132:135], v[252:255]\n" \
"s_add_u32 m0, s49, 49152\n" \
"v_exp_f32 v109, v109\n" \
"v_mfma_f32_16x16x32_bf16 a[24:27], a[152:155], v[136:139], a[24:27]\n" \
"buffer_load_dwordx4 v6, s[40:43], s17 offen offset:3072 sc1 lds\n" \
"v_mfma_f32_16x16x32_bf16 a[28:31], a[156:159], v[136:139], a[28:31]\n" \
"v_exp_f32 v110, v110\n" \
"v_mfma_f32_16x16x32_bf16 a[56:59], a[152:155], v[140:143], a[56:59]\n" \
"ds_read_b128 a[148:151], v5 offset:37888\n" \
"v_exp_f32 v111, v111\n" \
"v_mfma_f32_16x16x32_bf16 a[60:63], a[156:159], v[140:143], a[60:63]\n" \
"s_waitcnt lgkmcnt(4)\n" \
"v_cvt_pk_bf16_f32 v150, v108, v109\n" \
"v_cvt_pk_bf16_f32 v151, v110, v111\n" \
"s_cbranch_vccz LQWB_%=\nds_write_b128 v21, v[32:35] offset:16384\n" \
"ds_write_b128 v21, v[36:39] offset:17408\n" \
"ds_write_b128 v21, v[40:43] offset:18432\n" \
"ds_write_b128 v21, v[44:47] offset:19456\n" \
"ds_write_b128 v21, v[48:51] offset:20480\n" \
"ds_write_b128 v21, v[52:55] offset:21504\n" \
"ds_write_b128 v21, v[56:59] offset:22528\n" \
"ds_write_b128 v21, v[60:63] offset:23552\n" \
"ds_write_b128 v21, a[64:67] offset:0\n" \
"ds_write_b128 v21, a[68:71] offset:1024\n" \
"ds_write_b128 v21, a[72:75] offset:2048\n" \
"ds_write_b128 v21, a[76:79] offset:3072\n" \
"ds_write_b128 v21, a[80:83] offset:4096\n" \
"ds_write_b128 v21, a[84:87] offset:5120\n" \
"ds_write_b128 v21, a[88:91] offset:6144\n" \
"ds_write_b128 v21, a[92:95] offset:7168\nLQWB_%=:\n" \
"s_mov_b64 exec, 0\ns_cmp_eq_u32 %2, 0\ns_cbranch_scc1 LH22_%=\ns_cmp_eq_u32 %2, 1\ns_cbranch_scc1 LH23_%=\ns_cmp_eq_u32 %2, 2\ns_cbranch_scc1 LH24_%=\ns_cmp_eq_u32 %2, 3\ns_cbranch_scc1 LH25_%=\nLHR2_%=:\ns_mov_b64 exec, -1\n" \
"s_branch LCHK_%=\n" \
"LAD2_%=:\n" \
"v_xor_b32 v8, 0x2000, v5\n" \
"ds_read_b128 v[32:35], v21 offset:0\n" \
"ds_read_b128 v[36:39], v21 offset:1024\n" \
"ds_read_b128 v[40:43], v21 offset:2048\n" \
"ds_read_b128 v[44:47], v21 offset:3072\n" \
"ds_read_b128 v[48:51], v21 offset:4096\n" \
"ds_read_b128 v[52:55], v21 offset:5120\n" \
"ds_read_b128 v[56:59], v21 offset:6144\n" \
"ds_read_b128 v[60:63], v21 offset:7168\n" \
"ds_read_b128 a[64:67], v21 offset:16384\n" \
"ds_read_b128 a[68:71], v21 offset:17408\n" \
"ds_read_b128 a[72:75], v21 offset:18432\n" \
"ds_read_b128 a[76:79], v21 offset:19456\n" \
"ds_read_b128 a[128:131], v5 offset:49152\n" \
"ds_read_b128 a[80:83], v21 offset:20480\n" \
"ds_read_b128 a[84:87], v21 offset:21504\n" \
"ds_read_b128 a[88:91], v21 offset:22528\n" \
"ds_read_b128 a[92:95], v21 offset:23552\n" \
"ds_read_b128 a[132:135], v5 offset:50176\n" \
"ds_read_b128 a[136:139], v5 offset:51200\n" \
"ds_read_b128 a[140:143], v5 offset:52224\n" \
"ds_read_b128 a[144:147], v5 offset:53248\n" \
"ds_read_b128 a[148:151], v5 offset:54272\n" \
"v_exp_f32 v112, v112\n" \
"s_nop 0\n" \
"v_exp_f32 v113, v113\n" \
"s_nop 0\n" \
"v_exp_f32 v114, v114\n" \
"s_nop 0\n" \
"v_exp_f32 v115, v115\n" \
"s_nop 0\n" \
"v_exp_f32 v120, v120\n" \
"s_nop 0\n" \
"v_exp_f32 v121, v121\n" \
"s_nop 0\n" \
"v_exp_f32 v122, v122\n" \
"s_nop 0\n" \
"v_exp_f32 v123, v123\n" \
"s_nop 0\n" \
"v_exp_f32 v124, v124\n" \
"s_nop 0\n" \
"v_exp_f32 v125, v125\n" \
"s_nop 0\n" \
"v_exp_f32 v126, v126\n" \
"s_nop 0\n" \
"v_exp_f32 v127, v127\n" \
"s_nop 0\n" \
"v_cvt_pk_bf16_f32 v152, v112, v113\n" \
"v_cvt_pk_bf16_f32 v153, v114, v115\n" \
"v_mov_b32 v154, 0\n" \
"v_mov_b32 v155, 0\n" \
"v_cvt_pk_bf16_f32 v156, v120, v121\n" \
"v_cvt_pk_bf16_f32 v157, v122, v123\n" \
"v_cvt_pk_bf16_f32 v158, v124, v125\n" \
"v_cvt_pk_bf16_f32 v159, v126, v127\n" \
"v_subrev_u32 v23, 0x1f800000, v160\n" \
"v_subrev_u32 v27, 0x1f800000, v164\n" \
"v_max_u32 v23, v23, v27\n" \
"v_cmp_le_u32 vcc, 0x40000000, v23\n" \
"s_or_b64 s[54:55], s[54:55], vcc\n" \
"s_nop 0\n" \
"v_rcp_f32 v22, v160\n" \
"s_nop 0\n" \
"v_rcp_f32 v26, v164\n" \
"s_nop 0\n" \
"s_waitcnt lgkmcnt(0)\n" \
"s_branch LS3_%=\n" \
".p2align 6\n" \
"LH20_%=:\ns_cbranch_execz LHR1_%=\n" \
"s_nop 0\n" \
"LS1_%=:\n" \
"v_mfma_f32_16x16x32_bf16 v[64:67], a[128:131], v[32:35], v[176:179]\n" \
"v_exp_f32 v112, v112\n" \
"v_mfma_f32_16x16x32_bf16 v[64:67], a[132:135], v[36:39], v[64:67]\n" \
"s_add_u32 s16, s16, s9\n" \
"v_mfma_f32_16x16x32_bf16 v[72:75], a[128:131], v[48:51], 0\n" \
"ds_read_b128 a[152:155], v5 offset:38912\n" \
"v_mfma_f32_16x16x32_bf16 v[72:75], a[132:135], v[52:55], v[72:75]\n" \
"s_add_u32 m0, s48, 0\n" \
"v_exp_f32 v113, v113\n" \
"v_mfma_f32_16x16x32_bf16 v[80:83], a[128:131], a[64:67], 0\n" \
"buffer_load_dwordx4 v6, s[40:43], s16 offen offset:0 sc1 lds\n" \
"v_mfma_f32_16x16x32_bf16 v[80:83], a[132:135], a[68:71], v[80:83]\n" \
"v_exp_f32 v114, v114\n" \
"v_mfma_f32_16x16x32_bf16 v[88:91], a[128:131], a[80:83], 0\n" \
"ds_read_b128 a[156:159], v5 offset:39936\n" \
"v_cvt_pk_bf16_f32 v152, v112, v113\n" \
"v_mfma_f32_16x16x32_bf16 v[88:91], a[132:135], a[84:87], v[88:91]\n" \
"s_waitcnt lgkmcnt(4)\n" \
"v_exp_f32 v115, v115\n" \
"v_mfma_f32_16x16x32_bf16 v[64:67], a[136:139], v[40:43], v[64:67]\n" \
"v_exp_f32 v116, v116\n" \
"v_mfma_f32_16x16x32_bf16 v[64:67], a[140:143], v[44:47], v[64:67]\n" \
"s_xor_b32 s49, s49, 0x10000\n" \
"v_cvt_pk_bf16_f32 v153, v114, v115\n" \
"v_mfma_f32_16x16x32_bf16 v[72:75], a[136:139], v[56:59], v[72:75]\n" \
"ds_read_b128 a[128:131], v5 offset:49152\n" \
"v_mfma_f32_16x16x32_bf16 v[72:75], a[140:143], v[60:63], v[72:75]\n" \
"v_exp_f32 v117, v117\n" \
"v_mfma_f32_16x16x32_bf16 v[80:83], a[136:139], a[72:75], v[80:83]\n" \
"buffer_load_dwordx4 v6, s[40:43], s16 offen offset:1024 sc1 lds\n" \
"v_mfma_f32_16x16x32_bf16 v[80:83], a[140:143], a[76:79], v[80:83]\n" \
"v_exp_f32 v118, v118\n" \
"v_mfma_f32_16x16x32_bf16 v[88:91], a[136:139], a[88:91], v[88:91]\n" \
"ds_read_b128 a[132:135], v5 offset:50176\n" \
"v_cvt_pk_bf16_f32 v154, v116, v117\n" \
"v_mfma_f32_16x16x32_bf16 v[88:91], a[140:143], a[92:95], v[88:91]\n" \
"s_waitcnt lgkmcnt(4)\n" \
"v_exp_f32 v119, v119\n" \
"v_mov_b32 v68, 0xff800000\n" \
"v_mov_b32 v69, 0xff800000\n" \
"v_exp_f32 v120, v120\n" \
"v_cvt_pk_bf16_f32 v155, v118, v119\n" \
"v_mfma_f32_16x16x32_bf16 v[76:79], a[144:147], v[48:51], v[176:179]\n" \
"ds_read_b128 a[136:139], v5 offset:51200\n" \
"v_mfma_f32_16x16x32_bf16 v[76:79], a[148:151], v[52:55], v[76:79]\n" \
"v_exp_f32 v121, v121\n" \
"v_mfma_f32_16x16x32_bf16 v[84:87], a[144:147], a[64:67], 0\n" \
"buffer_load_dwordx4 v6, s[40:43], s16 offen offset:2048 sc1 lds\n" \
"v_mfma_f32_16x16x32_bf16 v[84:87], a[148:151], a[68:71], v[84:87]\n" \
"v_exp_f32 v122, v122\n" \
"v_mfma_f32_16x16x32_bf16 v[92:95], a[144:147], a[80:83], 0\n" \
"ds_read_b128 a[140:143], v5 offset:52224\n" \
"v_cvt_pk_bf16_f32 v156, v120, v121\n" \
"v_mfma_f32_16x16x32_bf16 v[92:95], a[148:151], a[84:87], v[92:95]\n" \
"s_waitcnt lgkmcnt(4)\n" \
"v_exp_f32 v123, v123\n" \
"v_mov_b32 v70, 0xff800000\n" \
"v_mov_b32 v71, 0xff800000\n" \
"v_exp_f32 v124, v124\n" \
"v_cvt_pk_bf16_f32 v157, v122, v123\n" \
"v_mfma_f32_16x16x32_bf16 v[76:79], a[152:155], v[56:59], v[76:79]\n" \
"ds_read_b128 a[144:147], v5 offset:53248\n" \
"v_mfma_f32_16x16x32_bf16 v[76:79], a[156:159], v[60:63], v[76:79]\n" \
"v_exp_f32 v125, v125\n" \
"v_mfma_f32_16x16x32_bf16 v[84:87], a[152:155], a[72:75], v[84:87]\n" \
"buffer_load_dwordx4 v6, s[40:43], s16 offen offset:3072 sc1 lds\n" \
"v_mfma_f32_16x16x32_bf16 v[84:87], a[156:159], a[76:79], v[84:87]\n" \
"v_exp_f32 v126, v126\n" \
"v_mfma_f32_16x16x32_bf16 v[92:95], a[152:155], a[88:91], v[92:95]\n" \
"ds_read_b128 a[148:151], v5 offset:54272\n" \
"v_cvt_pk_bf16_f32 v158, v124, v125\n" \
"v_mfma_f32_16x16x32_bf16 v[92:95], a[156:159], a[92:95], v[92:95]\n" \
"s_waitcnt lgkmcnt(4)\n" \
"v_exp_f32 v127, v127\n" \
"v_mfma_f32_16x16x32_bf16 v[160:163], v[180:183], v[144:147], v[160:163]\n" \
"v_cvt_pk_bf16_f32 v159, v126, v127\n" \
"v_mfma_f32_16x16x32_bf16 v[164:167], v[180:183], v[148:151], v[164:167]\n" \
"v_mfma_f32_16x16x32_bf16 v[168:171], v[180:183], v[152:155], v[168:171]\n" \
"v_exp_f32 v64, v64\n" \
"v_mfma_f32_16x16x32_bf16 v[172:175], v[180:183], v[156:159], v[172:175]\n" \
"v_mfma_f32_16x16x32_bf16 v[192:195], a[128:131], v[144:147], v[192:195]\n" \
"v_exp_f32 v65, v65\n" \
"v_mfma_f32_16x16x32_bf16 v[196:199], a[132:135], v[144:147], v[196:199]\n" \
"v_cvt_pk_bf16_f32 v128, v64, v65\n" \
"v_mfma_f32_16x16x32_bf16 v[224:227], a[128:131], v[148:151], v[224:227]\n" \
"ds_read_b128 a[152:155], v5 offset:55296\n" \
"v_mfma_f32_16x16x32_bf16 v[228:231], a[132:135], v[148:151], v[228:231]\n" \
"v_exp_f32 v66, v66\n" \
"v_mfma_f32_16x16x32_bf16 a[0:3], a[128:131], v[152:155], a[0:3]\n" \
"v_exp_f32 v67, v67\n" \
"v_mfma_f32_16x16x32_bf16 a[4:7], a[132:135], v[152:155], a[4:7]\n" \
"v_cvt_pk_bf16_f32 v129, v66, v67\n" \
"v_mfma_f32_16x16x32_bf16 a[32:35], a[128:131], v[156:159], a[32:35]\n" \
"ds_read_b128 a[156:159], v5 offset:56320\n" \
"v_mfma_f32_16x16x32_bf16 a[36:39], a[132:135], v[156:159], a[36:39]\n" \
"s_waitcnt vmcnt(12) lgkmcnt(4)\n" \
"v_exp_f32 v68, v68\n" \
"v_mfma_f32_16x16x32_bf16 v[200:203], a[136:139], v[144:147], v[200:203]\n" \
"s_barrier\n" \
"v_xor_b32 v5, 0x10000, v5\n" \
"v_mfma_f32_16x16x32_bf16 v[204:207], a[140:143], v[144:147], v[204:207]\n" \
"s_add_u32 s17, s17, s19\n" \
"v_exp_f32 v69, v69\n" \
"v_mfma_f32_16x16x32_bf16 v[232:235], a[136:139], v[148:151], v[232:235]\n" \
"ds_read_b128 a[128:131], v5 offset:0\n" \
"v_cvt_pk_bf16_f32 v130, v68, v69\n" \
"v_mfma_f32_16x16x32_bf16 v[236:239], a[140:143], v[148:151], v[236:239]\n" \
"s_add_u32 m0, s48, 16384\n" \
"v_exp_f32 v70, v70\n" \
"v_mfma_f32_16x16x32_bf16 a[8:11], a[136:139], v[152:155], a[8:11]\n" \
"buffer_load_dwordx4 v6, s[40:43], s17 offen offset:0 sc1 lds\n" \
"v_mfma_f32_16x16x32_bf16 a[12:15], a[140:143], v[152:155], a[12:15]\n" \
"v_exp_f32 v71, v71\n" \
"v_mfma_f32_16x16x32_bf16 a[40:43], a[136:139], v[156:159], a[40:43]\n" \
"ds_read_b128 a[132:135], v5 offset:1024\n" \
"v_cvt_pk_bf16_f32 v131, v70, v71\n" \
"v_mfma_f32_16x16x32_bf16 a[44:47], a[140:143], v[156:159], a[44:47]\n" \
"s_waitcnt lgkmcnt(4)\n" \
"v_exp_f32 v72, v72\n" \
"v_mfma_f32_16x16x32_bf16 v[208:211], a[144:147], v[144:147], v[208:211]\n" \
"v_exp_f32 v73, v73\n" \
"v_mfma_f32_16x16x32_bf16 v[212:215], a[148:151], v[144:147], v[212:215]\n" \
"v_cvt_pk_bf16_f32 v132, v72, v73\n" \
"v_mfma_f32_16x16x32_bf16 v[240:243], a[144:147], v[148:151], v[240:243]\n" \
"ds_read_b128 a[136:139], v5 offset:2048\n" \
"v_mfma_f32_16x16x32_bf16 v[244:247], a[148:151], v[148:151], v[244:247]\n" \
"v_exp_f32 v74, v74\n" \
"v_mfma_f32_16x16x32_bf16 a[16:19], a[144:147], v[152:155], a[16:19]\n" \
"buffer_load_dwordx4 v6, s[40:43], s17 offen offset:1024 sc1 lds\n" \
"v_mfma_f32_16x16x32_bf16 a[20:23], a[148:151], v[152:155], a[20:23]\n" \
"v_exp_f32 v75, v75\n" \
"v_mfma_f32_16x16x32_bf16 a[48:51], a[144:147], v[156:159], a[48:51]\n" \
"ds_read_b128 a[140:143], v5 offset:3072\n" \
"v_cvt_pk_bf16_f32 v133, v74, v75\n" \
"v_mfma_f32_16x16x32_bf16 a[52:55], a[148:151], v[156:159], a[52:55]\n" \
"s_waitcnt lgkmcnt(4)\n" \
"v_exp_f32 v76, v76\n" \
"v_mfma_f32_16x16x32_bf16 v[216:219], a[152:155], v[144:147], v[216:219]\n" \
"buffer_load_dwordx4 v6, s[40:43], s17 offen offset:2048 sc1 lds\n" \
"v_mfma_f32_16x16x32_bf16 v[220:223], a[156:159], v[144:147], v[220:223]\n" \
"s_add_u32 s44, s44, 1\n" \
"v_exp_f32 v77, v77\n" \
"v_mfma_f32_16x16x32_bf16 v[248:251], a[152:155], v[148:151], v[248:251]\n" \
"ds_read_b128 a[144:147], v5 offset:4096\n" \
"v_cvt_pk_bf16_f32 v134, v76, v77\n" \
"v_mfma_f32_16x16x32_bf16 v[252:255], a[156:159], v[148:151], v[252:255]\n" \
"v_exp_f32 v78, v78\n" \
"v_mfma_f32_16x16x32_bf16 a[24:27], a[152:155], v[152:155], a[24:27]\n" \
"buffer_load_dwordx4 v6, s[40:43], s17 offen offset:3072 sc1 lds\n" \
"v_mfma_f32_16x16x32_bf16 a[28:31], a[156:159], v[152:155], a[28:31]\n" \
"s_xor_b32 s48, s48, 0x10000\n" \
"v_exp_f32 v79, v79\n" \
"v_mfma_f32_16x16x32_bf16 a[56:59], a[152:155], v[156:159], a[56:59]\n" \
"ds_read_b128 a[148:151], v5 offset:5120\n" \
"v_cvt_pk_bf16_f32 v135, v78, v79\n" \
"v_mfma_f32_16x16x32_bf16 a[60:63], a[156:159], v[156:159], a[60:63]\n" \
"s_waitcnt lgkmcnt(4)\n" \
"s_cmp_ge_u32 %2, 2\n" \
"s_cbranch_scc1 LFIN_%=\n" \
"s_cmp_eq_u32 s44, 15\ns_cselect_b32 s16, s56, s16\ns_cselect_b32 s9, s57, s9\ns_cselect_b32 s17, s58, s17\ns_cselect_b32 s19, s59, s19\n" \
"v_mfma_f32_16x16x32_bf16 v[112:115], a[128:131], a[64:67], v[176:179]\n" \
"v_xor_b32 v8, 0x2000, v5\n" \
"ds_read_b128 v[32:35], v21 offset:0\n" \
"v_mfma_f32_16x16x32_bf16 v[112:115], a[132:135], a[68:71], v[112:115]\n" \
"s_add_u32 s16, s16, s9\n" \
"ds_read_b128 a[152:155], v5 offset:6144\n" \
"s_add_u32 m0, s49, 32768\n" \
"v_mfma_f32_16x16x32_bf16 v[120:123], a[128:131], a[80:83], 0\n" \
"buffer_load_dwordx4 v6, s[40:43], s16 offen offset:0 sc1 lds\n" \
"v_mfma_f32_16x16x32_bf16 v[120:123], a[132:135], a[84:87], v[120:123]\n" \
"s_waitcnt lgkmcnt(4)\n" \
"ds_read_b128 a[156:159], v5 offset:7168\n" \
"v_mfma_f32_16x16x32_bf16 v[112:115], a[136:139], a[72:75], v[112:115]\n" \
"ds_read_b128 a[128:131], v5 offset:16384\n" \
"v_mfma_f32_16x16x32_bf16 v[112:115], a[140:143], a[76:79], v[112:115]\n" \
"ds_read_b128 a[132:135], v5 offset:17408\n" \
"v_exp_f32 v80, v80\n" \
"v_mfma_f32_16x16x32_bf16 v[120:123], a[136:139], a[88:91], v[120:123]\n" \
"v_exp_f32 v81, v81\n" \
"v_mfma_f32_16x16x32_bf16 v[120:123], a[140:143], a[92:95], v[120:123]\n" \
"s_waitcnt lgkmcnt(5)\n" \
"ds_read_b128 a[136:139], v5 offset:18432\n" \
"v_exp_f32 v82, v82\n" \
"v_mfma_f32_16x16x32_bf16 v[124:127], a[144:147], a[80:83], v[176:179]\n" \
"buffer_load_dwordx4 v6, s[40:43], s16 offen offset:1024 sc1 lds\n" \
"v_exp_f32 v83, v83\n" \
"v_mfma_f32_16x16x32_bf16 v[124:127], a[148:151], a[84:87], v[124:127]\n" \
"s_waitcnt lgkmcnt(3)\n" \
"ds_read_b128 a[140:143], v5 offset:19456\n" \
"v_exp_f32 v84, v84\n" \
"v_mfma_f32_16x16x32_bf16 v[124:127], a[152:155], a[88:91], v[124:127]\n" \
"ds_read_b128 a[144:147], v5 offset:20480\n" \
"v_exp_f32 v85, v85\n" \
"v_mfma_f32_16x16x32_bf16 v[124:127], a[156:159], a[92:95], v[124:127]\n" \
"ds_read_b128 a[148:151], v5 offset:21504\n" \
"v_exp_f32 v86, v86\n" \
"ds_read_b128 a[152:155], v5 offset:22528\n" \
"v_exp_f32 v87, v87\n" \
"v_cvt_pk_bf16_f32 v136, v80, v81\n" \
"buffer_load_dwordx4 v6, s[40:43], s16 offen offset:2048 sc1 lds\n" \
"v_exp_f32 v88, v88\n" \
"ds_read_b128 a[156:159], v5 offset:23552\n" \
"v_exp_f32 v89, v89\n" \
"v_cvt_pk_bf16_f32 v137, v82, v83\n" \
"v_exp_f32 v90, v90\n" \
"v_exp_f32 v91, v91\n" \
"v_exp_f32 v92, v92\n" \
"v_exp_f32 v93, v93\n" \
"v_exp_f32 v94, v94\n" \
"v_exp_f32 v95, v95\n" \
"v_cvt_pk_bf16_f32 v138, v84, v85\n" \
"v_cvt_pk_bf16_f32 v139, v86, v87\n" \
"v_cvt_pk_bf16_f32 v140, v88, v89\n" \
"v_cvt_pk_bf16_f32 v141, v90, v91\n" \
"buffer_load_dwordx4 v6, s[40:43], s16 offen offset:3072 sc1 lds\n" \
"v_cvt_pk_bf16_f32 v142, v92, v93\n" \
"v_cvt_pk_bf16_f32 v143, v94, v95\n" \
"v_mfma_f32_16x16x32_bf16 v[160:163], v[180:183], v[128:131], v[160:163]\n" \
"v_exp_f32 v112, v112\n" \
"v_mfma_f32_16x16x32_bf16 v[164:167], v[180:183], v[132:135], v[164:167]\n" \
"v_exp_f32 v113, v113\n" \
"v_mfma_f32_16x16x32_bf16 v[168:171], v[180:183], v[136:139], v[168:171]\n" \
"v_exp_f32 v114, v114\n" \
"v_mfma_f32_16x16x32_bf16 v[172:175], v[180:183], v[140:143], v[172:175]\n" \
"s_waitcnt lgkmcnt(6)\n" \
"v_exp_f32 v115, v115\n" \
"v_mfma_f32_16x16x32_bf16 v[192:195], a[128:131], v[128:131], v[192:195]\n" \
"v_exp_f32 v120, v120\n" \
"v_mfma_f32_16x16x32_bf16 v[196:199], a[132:135], v[128:131], v[196:199]\n" \
"v_exp_f32 v121, v121\n" \
"v_mfma_f32_16x16x32_bf16 v[224:227], a[128:131], v[132:135], v[224:227]\n" \
"v_exp_f32 v122, v122\n" \
"v_mfma_f32_16x16x32_bf16 v[228:231], a[132:135], v[132:135], v[228:231]\n" \
"v_exp_f32 v123, v123\n" \
"v_mfma_f32_16x16x32_bf16 a[0:3], a[128:131], v[136:139], a[0:3]\n" \
"v_exp_f32 v124, v124\n" \
"v_mfma_f32_16x16x32_bf16 a[4:7], a[132:135], v[136:139], a[4:7]\n" \
"v_exp_f32 v125, v125\n" \
"v_mfma_f32_16x16x32_bf16 a[32:35], a[128:131], v[140:143], a[32:35]\n" \
"v_exp_f32 v126, v126\n" \
"v_mfma_f32_16x16x32_bf16 a[36:39], a[132:135], v[140:143], a[36:39]\n" \
"s_waitcnt lgkmcnt(4)\n" \
"v_exp_f32 v127, v127\n" \
"v_mfma_f32_16x16x32_bf16 v[200:203], a[136:139], v[128:131], v[200:203]\n" \
"v_cvt_pk_bf16_f32 v152, v112, v113\n" \
"v_cvt_pk_bf16_f32 v153, v114, v115\n" \
"v_mfma_f32_16x16x32_bf16 v[204:207], a[140:143], v[128:131], v[204:207]\n" \
"v_mov_b32 v154, 0\n" \
"v_mov_b32 v155, 0\n" \
"v_mfma_f32_16x16x32_bf16 v[232:235], a[136:139], v[132:135], v[232:235]\n" \
"v_cvt_pk_bf16_f32 v156, v120, v121\n" \
"ds_read_b128 v[36:39], v21 offset:1024\n" \
"v_mfma_f32_16x16x32_bf16 v[236:239], a[140:143], v[132:135], v[236:239]\n" \
"v_cvt_pk_bf16_f32 v157, v122, v123\n" \
"v_cvt_pk_bf16_f32 v158, v124, v125\n" \
"v_mfma_f32_16x16x32_bf16 a[8:11], a[136:139], v[136:139], a[8:11]\n" \
"v_cvt_pk_bf16_f32 v159, v126, v127\n" \
"ds_read_b128 v[40:43], v21 offset:2048\n" \
"v_mfma_f32_16x16x32_bf16 a[12:15], a[140:143], v[136:139], a[12:15]\n" \
"ds_read_b128 v[44:47], v21 offset:3072\n" \
"v_subrev_u32 v23, 0x1f800000, v160\n" \
"v_mfma_f32_16x16x32_bf16 a[40:43], a[136:139], v[140:143], a[40:43]\n" \
"ds_read_b128 v[48:51], v21 offset:4096\n" \
"v_subrev_u32 v27, 0x1f800000, v164\n" \
"v_mfma_f32_16x16x32_bf16 a[44:47], a[140:143], v[140:143], a[44:47]\n" \
"s_waitcnt lgkmcnt(6)\n" \
"ds_read_b128 v[52:55], v21 offset:5120\n" \
"v_max_u32 v23, v23, v27\n" \
"v_mfma_f32_16x16x32_bf16 v[208:211], a[144:147], v[128:131], v[208:211]\n" \
"ds_read_b128 v[56:59], v21 offset:6144\n" \
"v_cmp_le_u32 vcc, 0x40000000, v23\n" \
"v_mfma_f32_16x16x32_bf16 v[212:215], a[148:151], v[128:131], v[212:215]\n" \
"s_or_b64 s[54:55], s[54:55], vcc\n" \
"ds_read_b128 v[60:63], v21 offset:7168\n" \
"s_nop 0\n" \
"v_mfma_f32_16x16x32_bf16 v[240:243], a[144:147], v[132:135], v[240:243]\n" \
"ds_read_b128 a[64:67], v21 offset:16384\n" \
"v_mfma_f32_16x16x32_bf16 v[244:247], a[148:151], v[132:135], v[244:247]\n" \
"s_add_u32 s17, s17, s19\n" \
"s_add_u32 m0, s49, 49152\n" \
"ds_read_b128 a[68:71], v21 offset:17408\n" \
"v_mfma_f32_16x16x32_bf16 a[16:19], a[144:147], v[136:139], a[16:19]\n" \
"s_waitcnt vmcnt(12) lgkmcnt(15)\n" \
"ds_read_b128 a[72:75], v21 offset:18432\n" \
"v_mfma_f32_16x16x32_bf16 a[20:23], a[148:151], v[136:139], a[20:23]\n" \
"s_barrier\n" \
"ds_read_b128 a[76:79], v21 offset:19456\n" \
"v_mfma_f32_16x16x32_bf16 a[48:51], a[144:147], v[140:143], a[48:51]\n" \
"buffer_load_dwordx4 v6, s[40:43], s17 offen offset:0 sc1 lds\n" \
"v_mfma_f32_16x16x32_bf16 a[52:55], a[148:151], v[140:143], a[52:55]\n" \
"s_waitcnt lgkmcnt(11)\n" \
"ds_read_b128 a[128:131], v5 offset:49152\n" \
"ds_read_b128 a[80:83], v21 offset:20480\n" \
"ds_read_b128 a[84:87], v21 offset:21504\n" \
"ds_read_b128 a[88:91], v21 offset:22528\n" \
"ds_read_b128 a[92:95], v21 offset:23552\n" \
"v_mfma_f32_16x16x32_bf16 v[216:219], a[152:155], v[128:131], v[216:219]\n" \
"buffer_load_dwordx4 v6, s[40:43], s17 offen offset:1024 sc1 lds\n" \
"v_mfma_f32_16x16x32_bf16 v[220:223], a[156:159], v[128:131], v[220:223]\n" \
"ds_read_b128 a[132:135], v5 offset:50176\n" \
"v_mfma_f32_16x16x32_bf16 v[248:251], a[152:155], v[132:135], v[248:251]\n" \
"buffer_load_dwordx4 v6, s[40:43], s17 offen offset:2048 sc1 lds\n" \
"v_mfma_f32_16x16x32_bf16 v[252:255], a[156:159], v[132:135], v[252:255]\n" \
"ds_read_b128 a[136:139], v5 offset:51200\n" \
"v_mfma_f32_16x16x32_bf16 a[24:27], a[152:155], v[136:139], a[24:27]\n" \
"buffer_load_dwordx4 v6, s[40:43], s17 offen offset:3072 sc1 lds\n" \
"v_mfma_f32_16x16x32_bf16 a[28:31], a[156:159], v[136:139], a[28:31]\n" \
"ds_read_b128 a[140:143], v5 offset:52224\n" \
"v_mfma_f32_16x16x32_bf16 a[56:59], a[152:155], v[140:143], a[56:59]\n" \
"ds_read_b128 a[144:147], v5 offset:53248\n" \
"v_mfma_f32_16x16x32_bf16 a[60:63], a[156:159], v[140:143], a[60:63]\n" \
"ds_read_b128 a[148:151], v5 offset:54272\n" \
"v_rcp_f32 v22, v160\n" \
"v_rcp_f32 v26, v164\n" \
"LS3_%=:\n" \
"v_mfma_f32_16x16x32_bf16 v[168:171], v[180:183], v[152:155], v[168:171]\n" \
"s_add_u32 s16, s16, s9\n" \
"ds_read_b128 a[152:155], v5 offset:55296\n" \
"s_add_u32 m0, s48, 0\n" \
"v_mfma_f32_16x16x32_bf16 v[172:175], v[180:183], v[156:159], v[172:175]\n" \
"s_waitcnt lgkmcnt(5)\n" \
"ds_read_b128 a[156:159], v5 offset:56320\n" \
"v_mfma_f32_16x16x32_bf16 a[0:3], a[128:131], v[152:155], a[0:3]\n" \
"buffer_load_dwordx4 v6, s[40:43], s16 offen offset:0 sc1 lds\n" \
"v_mfma_f32_16x16x32_bf16 a[4:7], a[132:135], v[152:155], a[4:7]\n" \
"v_mul_f32 v192, v22, v192\nv_mul_f32 v193, v22, v193\n" \
"v_mfma_f32_16x16x32_bf16 a[32:35], a[128:131], v[156:159], a[32:35]\n" \
"v_mul_f32 v194, v22, v194\nv_mul_f32 v195, v22, v195\n" \
"v_mfma_f32_16x16x32_bf16 a[36:39], a[132:135], v[156:159], a[36:39]\n" \
"s_waitcnt lgkmcnt(4)\n" \
"ds_read_b128 a[128:131], v8 offset:32768\n" \
"v_mfma_f32_16x16x32_bf16 a[8:11], a[136:139], v[152:155], a[8:11]\n" \
"buffer_load_dwordx4 v6, s[40:43], s16 offen offset:1024 sc1 lds\n" \
"v_mfma_f32_16x16x32_bf16 a[12:15], a[140:143], v[152:155], a[12:15]\n" \
"ds_read_b128 a[132:135], v8 offset:33792\n" \
"v_mfma_f32_16x16x32_bf16 a[40:43], a[136:139], v[156:159], a[40:43]\n" \
"v_mul_f32 v196, v22, v196\nv_mul_f32 v197, v22, v197\n" \
"v_mfma_f32_16x16x32_bf16 a[44:47], a[140:143], v[156:159], a[44:47]\n" \
"s_waitcnt lgkmcnt(4)\n" \
"ds_read_b128 a[136:139], v8 offset:34816\n" \
"v_accvgpr_read_b32 v80, a0\n" \
"v_mfma_f32_16x16x32_bf16 a[16:19], a[144:147], v[152:155], a[16:19]\n" \
"ds_read_b128 a[140:143], v8 offset:35840\n" \
"v_accvgpr_read_b32 v81, a1\n" \
"v_mfma_f32_16x16x32_bf16 a[20:23], a[148:151], v[152:155], a[20:23]\n" \
"v_mul_f32 v198, v22, v198\nv_mul_f32 v199, v22, v199\n" \
"v_mfma_f32_16x16x32_bf16 a[48:51], a[144:147], v[156:159], a[48:51]\n" \
"buffer_load_dwordx4 v6, s[40:43], s16 offen offset:2048 sc1 lds\n" \
"v_mfma_f32_16x16x32_bf16 a[52:55], a[148:151], v[156:159], a[52:55]\n" \
"s_waitcnt lgkmcnt(4)\n" \
"ds_read_b128 a[144:147], v8 offset:36864\n" \
"v_cvt_pk_bf16_f32 v192, v192, v193\n" \
"v_mfma_f32_16x16x32_bf16 a[24:27], a[152:155], v[152:155], a[24:27]\n" \
"ds_read_b128 a[148:151], v8 offset:37888\n" \
"v_cvt_pk_bf16_f32 v193, v194, v195\n" \
"v_mfma_f32_16x16x32_bf16 a[28:31], a[156:159], v[152:155], a[28:31]\n" \
"v_cvt_pk_bf16_f32 v194, v196, v197\n" \
"v_cvt_pk_bf16_f32 v195, v198, v199\n" \
"v_mfma_f32_16x16x32_bf16 a[56:59], a[152:155], v[156:159], a[56:59]\n" \
"buffer_store_dwordx4 v[192:195], v4, s[36:39], s50 offen offset:0 sc1\n" \
"v_mfma_f32_16x16x32_bf16 a[60:63], a[156:159], v[156:159], a[60:63]\n" \
"ds_read_b128 a[152:155], v8 offset:38912\n" \
"v_accvgpr_read_b32 v82, a2\n" \
"buffer_load_dwordx4 v6, s[40:43], s16 offen offset:3072 sc1 lds\n" \
"v_mul_f32 v224, v26, v224\nv_mul_f32 v225, v26, v225\n" \
"ds_read_b128 a[156:159], v8 offset:39936\n" \
"v_pk_mul_f32 v[226:227], v[226:227], v[26:27] op_sel_hi:[1,0]\n" \
"v_pk_mul_f32 v[228:229], v[228:229], v[26:27] op_sel_hi:[1,0]\n" \
"v_pk_mul_f32 v[230:231], v[230:231], v[26:27] op_sel_hi:[1,0]\n" \
"v_cvt_pk_bf16_f32 v224, v224, v225\n" \
"v_cvt_pk_bf16_f32 v225, v226, v227\n" \
"v_cvt_pk_bf16_f32 v226, v228, v229\n" \
"v_cvt_pk_bf16_f32 v227, v230, v231\n" \
"buffer_store_dwordx4 v[224:227], v4, s[36:39], s50 offen offset:1024 sc1\n" \
"v_pk_mul_f32 v[200:201], v[200:201], v[22:23] op_sel_hi:[1,0]\n" \
"v_pk_mul_f32 v[202:203], v[202:203], v[22:23] op_sel_hi:[1,0]\n" \
"v_pk_mul_f32 v[204:205], v[204:205], v[22:23] op_sel_hi:[1,0]\n" \
"v_pk_mul_f32 v[206:207], v[206:207], v[22:23] op_sel_hi:[1,0]\n" \
"v_cvt_pk_bf16_f32 v200, v200, v201\n" \
"v_cvt_pk_bf16_f32 v201, v202, v203\n" \
"v_cvt_pk_bf16_f32 v202, v204, v205\n" \
"v_cvt_pk_bf16_f32 v203, v206, v207\n" \
"v_pk_mul_f32 v[232:233], v[232:233], v[26:27] op_sel_hi:[1,0]\n" \
"v_pk_mul_f32 v[234:235], v[234:235], v[26:27] op_sel_hi:[1,0]\n" \
"v_pk_mul_f32 v[236:237], v[236:237], v[26:27] op_sel_hi:[1,0]\n" \
"v_pk_mul_f32 v[238:239], v[238:239], v[26:27] op_sel_hi:[1,0]\n" \
"v_cvt_pk_bf16_f32 v232, v232, v233\n" \
"v_cvt_pk_bf16_f32 v233, v234, v235\n" \
"buffer_store_dwordx4 v[200:203], v4, s[36:39], s50 offen offset:2048 sc1\n" \
"v_cvt_pk_bf16_f32 v234, v236, v237\n" \
"v_cvt_pk_bf16_f32 v235, v238, v239\n" \
"v_pk_mul_f32 v[208:209], v[208:209], v[22:23] op_sel_hi:[1,0]\n" \
"v_pk_mul_f32 v[210:211], v[210:211], v[22:23] op_sel_hi:[1,0]\n" \
"v_pk_mul_f32 v[212:213], v[212:213], v[22:23] op_sel_hi:[1,0]\n" \
"v_pk_mul_f32 v[214:215], v[214:215], v[22:23] op_sel_hi:[1,0]\n" \
"v_cvt_pk_bf16_f32 v208, v208, v209\n" \
"v_cvt_pk_bf16_f32 v209, v210, v211\n" \
"v_cvt_pk_bf16_f32 v210, v212, v213\n" \
"v_cvt_pk_bf16_f32 v211, v214, v215\n" \
"v_pk_mul_f32 v[240:241], v[240:241], v[26:27] op_sel_hi:[1,0]\n" \
"v_pk_mul_f32 v[242:243], v[242:243], v[26:27] op_sel_hi:[1,0]\n" \
"v_pk_mul_f32 v[244:245], v[244:245], v[26:27] op_sel_hi:[1,0]\n" \
"v_pk_mul_f32 v[246:247], v[246:247], v[26:27] op_sel_hi:[1,0]\n" \
"buffer_store_dwordx4 v[232:235], v4, s[36:39], s50 offen offset:3072 sc1\n" \
"v_cvt_pk_bf16_f32 v240, v240, v241\n" \
"v_cvt_pk_bf16_f32 v241, v242, v243\n" \
"v_cvt_pk_bf16_f32 v242, v244, v245\n" \
"v_cvt_pk_bf16_f32 v243, v246, v247\n" \
"v_pk_mul_f32 v[216:217], v[216:217], v[22:23] op_sel_hi:[1,0]\n" \
"v_pk_mul_f32 v[218:219], v[218:219], v[22:23] op_sel_hi:[1,0]\n" \
"v_pk_mul_f32 v[220:221], v[220:221], v[22:23] op_sel_hi:[1,0]\n" \
"v_pk_mul_f32 v[222:223], v[222:223], v[22:23] op_sel_hi:[1,0]\n" \
"v_cvt_pk_bf16_f32 v216, v216, v217\n" \
"v_cvt_pk_bf16_f32 v217, v218, v219\n" \
"v_cvt_pk_bf16_f32 v218, v220, v221\n" \
"v_cvt_pk_bf16_f32 v219, v222, v223\n" \
"v_pk_mul_f32 v[248:249], v[248:249], v[26:27] op_sel_hi:[1,0]\n" \
"v_pk_mul_f32 v[250:251], v[250:251], v[26:27] op_sel_hi:[1,0]\n" \
"s_waitcnt lgkmcnt(6)\n" \
"buffer_store_dwordx4 v[208:211], v7, s[36:39], s50 offen offset:0 sc1\n" \
"v_pk_mul_f32 v[252:253], v[252:253], v[26:27] op_sel_hi:[1,0]\n" \
"v_mfma_f32_16x16x32_bf16 v[64:67], a[128:131], v[32:35], v[176:179]\n" \
"v_mul_f32 v254, v26, v254\nv_mul_f32 v255, v26, v255\n" \
"v_mfma_f32_16x16x32_bf16 v[64:67], a[132:135], v[36:39], v[64:67]\n" \
"v_cvt_pk_bf16_f32 v248, v248, v249\n" \
"v_cvt_pk_bf16_f32 v249, v250, v251\n" \
"v_mfma_f32_16x16x32_bf16 v[72:75], a[128:131], v[48:51], 0\n" \
"s_add_u32 s63, s50, 262144\n" \
"v_cvt_pk_bf16_f32 v250, v252, v253\n" \
"v_cvt_pk_bf16_f32 v251, v254, v255\n" \
"v_mfma_f32_16x16x32_bf16 v[72:75], a[132:135], v[52:55], v[72:75]\n" \
"s_waitcnt lgkmcnt(4)\n" \
"v_subrev_u32 v23, 0x1f800000, v168\n" \
"v_mfma_f32_16x16x32_bf16 v[64:67], a[136:139], v[40:43], v[64:67]\n" \
"buffer_store_dwordx4 v[240:243], v7, s[36:39], s50 offen offset:1024 sc1\n" \
"v_mfma_f32_16x16x32_bf16 v[64:67], a[140:143], v[44:47], v[64:67]\n" \
"v_subrev_u32 v27, 0x1f800000, v172\n" \
"v_max_u32 v23, v23, v27\n" \
"v_mfma_f32_16x16x32_bf16 v[72:75], a[136:139], v[56:59], v[72:75]\n" \
"v_cmp_le_u32 vcc, 0x40000000, v23\n" \
"s_nop 0\n" \
"v_mfma_f32_16x16x32_bf16 v[72:75], a[140:143], v[60:63], v[72:75]\n" \
"s_waitcnt lgkmcnt(2)\n" \
"s_or_b64 s[54:55], s[54:55], vcc\n" \
"v_accvgpr_read_b32 v83, a3\n" \
"v_mfma_f32_16x16x32_bf16 v[76:79], a[144:147], v[48:51], v[176:179]\n" \
"buffer_store_dwordx4 v[216:219], v7, s[36:39], s50 offen offset:2048 sc1\n" \
"v_mfma_f32_16x16x32_bf16 v[76:79], a[148:151], v[52:55], v[76:79]\n" \
"s_waitcnt lgkmcnt(0)\n" \
"v_rcp_f32 v188, v168\n" \
"v_mfma_f32_16x16x32_bf16 v[76:79], a[152:155], v[56:59], v[76:79]\n" \
"v_rcp_f32 v24, v172\n" \
"v_mfma_f32_16x16x32_bf16 v[76:79], a[156:159], v[60:63], v[76:79]\n" \
"v_accvgpr_read_b32 v84, a4\n" \
"v_accvgpr_read_b32 v85, a5\n" \
"buffer_store_dwordx4 v[248:251], v7, s[36:39], s50 offen offset:3072 sc1\n" \
"v_accvgpr_read_b32 v86, a6\n" \
"v_accvgpr_read_b32 v87, a7\n" \
"v_pk_mul_f32 v[80:81], v[80:81], v[188:189] op_sel_hi:[1,0]\n" \
"v_pk_mul_f32 v[82:83], v[82:83], v[188:189] op_sel_hi:[1,0]\n" \
"v_pk_mul_f32 v[84:85], v[84:85], v[188:189] op_sel_hi:[1,0]\n" \
"v_pk_mul_f32 v[86:87], v[86:87], v[188:189] op_sel_hi:[1,0]\n" \
"v_exp_f32 v64, v64\n" \
"v_exp_f32 v65, v65\n" \
"v_exp_f32 v66, v66\n" \
"v_exp_f32 v67, v67\n" \
"v_exp_f32 v72, v72\n" \
"v_exp_f32 v73, v73\n" \
"v_exp_f32 v74, v74\n" \
"v_exp_f32 v75, v75\n" \
"v_exp_f32 v76, v76\n" \
"v_exp_f32 v77, v77\n" \
"v_exp_f32 v78, v78\n" \
"v_exp_f32 v79, v79\n" \
"v_cvt_pk_bf16_f32 v128, v64, v65\n" \
"v_cvt_pk_bf16_f32 v129, v66, v67\n" \
"v_mov_b32 v130, 0\n" \
"v_mov_b32 v131, 0\n" \
"s_add_u32 s17, s17, s19\n" \
"s_add_u32 m0, s48, 16384\n" \
"v_cvt_pk_bf16_f32 v132, v72, v73\n" \
"v_cvt_pk_bf16_f32 v133, v74, v75\n" \
"s_waitcnt vmcnt(20) lgkmcnt(0)\n" \
"v_cvt_pk_bf16_f32 v134, v76, v77\n" \
"v_cvt_pk_bf16_f32 v135, v78, v79\n" \
"v_cvt_pk_bf16_f32 v80, v80, v81\n" \
"s_barrier\n" \
"v_xor_b32 v5, 0x12000, v5\n" \
"v_cvt_pk_bf16_f32 v81, v82, v83\n" \
"v_cvt_pk_bf16_f32 v82, v84, v85\n" \
"buffer_load_dwordx4 v6, s[40:43], s17 offen offset:0 sc1 lds\n" \
"v_cvt_pk_bf16_f32 v83, v86, v87\n" \
"v_accvgpr_read_b32 v88, a32\n" \
"ds_read_b128 a[128:131], v5 offset:0\n" \
"v_accvgpr_read_b32 v89, a33\n" \
"v_accvgpr_read_b32 v90, a34\n" \
"v_accvgpr_read_b32 v91, a35\n" \
"buffer_load_dwordx4 v6, s[40:43], s17 offen offset:1024 sc1 lds\n" \
"v_accvgpr_read_b32 v92, a36\n" \
"v_accvgpr_read_b32 v93, a37\n" \
"ds_read_b128 a[132:135], v5 offset:1024\n" \
"v_accvgpr_read_b32 v94, a38\n" \
"v_accvgpr_read_b32 v95, a39\n" \
"v_accvgpr_read_b32 v136, a8\n" \
"buffer_load_dwordx4 v6, s[40:43], s17 offen offset:2048 sc1 lds\n" \
"v_pk_mul_f32 v[88:89], v[88:89], v[24:25] op_sel_hi:[1,0]\n" \
"v_pk_mul_f32 v[90:91], v[90:91], v[24:25] op_sel_hi:[1,0]\n" \
"ds_read_b128 a[136:139], v5 offset:2048\n" \
"v_pk_mul_f32 v[92:93], v[92:93], v[24:25] op_sel_hi:[1,0]\n" \
"v_pk_mul_f32 v[94:95], v[94:95], v[24:25] op_sel_hi:[1,0]\n" \
"v_cvt_pk_bf16_f32 v88, v88, v89\n" \
"s_xor_b32 s48, s48, 0x10000\n" \
"buffer_load_dwordx4 v6, s[40:43], s17 offen offset:3072 sc1 lds\n" \
"s_xor_b32 s49, s49, 0x10000\n" \
"s_add_u32 s44, s44, 1\n" \
"ds_read_b128 a[140:143], v5 offset:3072\n" \
"v_cvt_pk_bf16_f32 v89, v90, v91\n" \
"v_cvt_pk_bf16_f32 v90, v92, v93\n" \
"buffer_store_dwordx4 v[80:83], v4, s[36:39], s63 offen offset:0 sc1\n" \
"v_cvt_pk_bf16_f32 v91, v94, v95\n" \
"v_accvgpr_read_b32 v137, a9\n" \
"ds_read_b128 a[144:147], v5 offset:4096\n" \
"v_accvgpr_read_b32 v138, a10\n" \
"v_accvgpr_read_b32 v139, a11\n" \
"v_accvgpr_read_b32 v140, a12\n" \
"ds_read_b128 a[148:151], v5 offset:5120\n" \
"v_accvgpr_read_b32 v141, a13\n" \
"v_accvgpr_read_b32 v142, a14\n" \
"v_accvgpr_read_b32 v143, a15\n" \
"ds_read_b128 a[152:155], v5 offset:6144\n" \
"v_pk_mul_f32 v[136:137], v[136:137], v[188:189] op_sel_hi:[1,0]\n" \
"v_pk_mul_f32 v[138:139], v[138:139], v[188:189] op_sel_hi:[1,0]\n" \
"v_pk_mul_f32 v[140:141], v[140:141], v[188:189] op_sel_hi:[1,0]\n" \
"s_cmp_eq_u32 s44, 16\n" \
"s_cbranch_scc1 LAD32_%=\n" \
"LH21_%=:\ns_cbranch_execz LHR1_%=\n" \
"s_cmp_eq_u32 s44, 15\ns_cselect_b32 s16, s56, s16\ns_cselect_b32 s9, s57, s9\ns_cselect_b32 s17, s58, s17\ns_cselect_b32 s19, s59, s19\n" \
"buffer_store_dwordx4 v[88:91], v4, s[36:39], s63 offen offset:1024 sc1\n" \
"v_mul_f32 v142, v188, v142\nv_mul_f32 v143, v188, v143\n" \
"s_waitcnt lgkmcnt(5)\n" \
"ds_read_b128 a[156:159], v5 offset:7168\n" \
"s_add_u32 s16, s16, s9\n" \
"s_add_u32 m0, s49, 32768\n" \
"v_mfma_f32_16x16x32_bf16 v[96:99], a[128:131], v[32:35], 0\n" \
"buffer_load_dwordx4 v6, s[40:43], s16 offen offset:0 sc1 lds\n" \
"v_mfma_f32_16x16x32_bf16 v[96:99], a[132:135], v[36:39], v[96:99]\n" \
"v_cvt_pk_bf16_f32 v136, v136, v137\n" \
"v_cvt_pk_bf16_f32 v137, v138, v139\n" \
"v_mfma_f32_16x16x32_bf16 v[104:107], a[128:131], v[48:51], 0\n" \
"v_cvt_pk_bf16_f32 v138, v140, v141\n" \
"v_cvt_pk_bf16_f32 v139, v142, v143\n" \
"v_mfma_f32_16x16x32_bf16 v[104:107], a[132:135], v[52:55], v[104:107]\n" \
"buffer_store_dwordx4 v[136:139], v4, s[36:39], s63 offen offset:2048 sc1\n" \
"v_mfma_f32_16x16x32_bf16 v[112:115], a[128:131], a[64:67], v[176:179]\n" \
"v_accvgpr_read_b32 v80, a40\n" \
"v_accvgpr_read_b32 v81, a41\n" \
"v_mfma_f32_16x16x32_bf16 v[112:115], a[132:135], a[68:71], v[112:115]\n" \
"buffer_load_dwordx4 v6, s[40:43], s16 offen offset:1024 sc1 lds\n" \
"v_mfma_f32_16x16x32_bf16 v[120:123], a[128:131], a[80:83], 0\n" \
"v_accvgpr_read_b32 v82, a42\n" \
"v_accvgpr_read_b32 v83, a43\n" \
"v_mfma_f32_16x16x32_bf16 v[120:123], a[132:135], a[84:87], v[120:123]\n" \
"s_waitcnt lgkmcnt(4)\n" \
"ds_read_b128 a[128:131], v5 offset:16384\n" \
"v_accvgpr_read_b32 v84, a44\n" \
"v_mfma_f32_16x16x32_bf16 v[96:99], a[136:139], v[40:43], v[96:99]\n" \
"ds_read_b128 a[132:135], v5 offset:17408\n" \
"v_accvgpr_read_b32 v85, a45\n" \
"v_mfma_f32_16x16x32_bf16 v[96:99], a[140:143], v[44:47], v[96:99]\n" \
"v_accvgpr_read_b32 v86, a46\n" \
"v_accvgpr_read_b32 v87, a47\n" \
"v_mfma_f32_16x16x32_bf16 v[104:107], a[136:139], v[56:59], v[104:107]\n" \
"buffer_load_dwordx4 v6, s[40:43], s16 offen offset:2048 sc1 lds\n" \
"v_mfma_f32_16x16x32_bf16 v[104:107], a[140:143], v[60:63], v[104:107]\n" \
"v_mul_f32 v80, v24, v80\nv_mul_f32 v81, v24, v81\n" \
"v_mfma_f32_16x16x32_bf16 v[112:115], a[136:139], a[72:75], v[112:115]\n" \
"v_mul_f32 v82, v24, v82\nv_mul_f32 v83, v24, v83\n" \
"v_mfma_f32_16x16x32_bf16 v[112:115], a[140:143], a[76:79], v[112:115]\n" \
"v_mul_f32 v84, v24, v84\nv_mul_f32 v85, v24, v85\n" \
"v_mfma_f32_16x16x32_bf16 v[120:123], a[136:139], a[88:91], v[120:123]\n" \
"v_mul_f32 v86, v24, v86\nv_mul_f32 v87, v24, v87\n" \
"v_mfma_f32_16x16x32_bf16 v[120:123], a[140:143], a[92:95], v[120:123]\n" \
"s_waitcnt lgkmcnt(4)\n" \
"ds_read_b128 a[136:139], v5 offset:18432\n" \
"v_cvt_pk_bf16_f32 v80, v80, v81\n" \
"v_mfma_f32_16x16x32_bf16 v[100:103], a[144:147], v[32:35], 0\n" \
"buffer_load_dwordx4 v6, s[40:43], s16 offen offset:3072 sc1 lds\n" \
"v_mfma_f32_16x16x32_bf16 v[100:103], a[148:151], v[36:39], v[100:103]\n" \
"ds_read_b128 a[140:143], v5 offset:19456\n" \
"v_cvt_pk_bf16_f32 v81, v82, v83\n" \
"v_mfma_f32_16x16x32_bf16 v[108:111], a[144:147], v[48:51], 0\n" \
"v_cvt_pk_bf16_f32 v82, v84, v85\n" \
"v_cvt_pk_bf16_f32 v83, v86, v87\n" \
"v_mfma_f32_16x16x32_bf16 v[108:111], a[148:151], v[52:55], v[108:111]\n" \
"buffer_store_dwordx4 v[80:83], v4, s[36:39], s63 offen offset:3072 sc1\n" \
"v_mfma_f32_16x16x32_bf16 v[124:127], a[144:147], a[80:83], v[176:179]\n" \
"v_accvgpr_read_b32 v88, a16\n" \
"v_accvgpr_read_b32 v89, a17\n" \
"v_mfma_f32_16x16x32_bf16 v[124:127], a[148:151], a[84:87], v[124:127]\n" \
"s_waitcnt lgkmcnt(4)\n" \
"ds_read_b128 a[144:147], v5 offset:20480\n" \
"v_accvgpr_read_b32 v90, a18\n" \
"v_mfma_f32_16x16x32_bf16 v[100:103], a[152:155], v[40:43], v[100:103]\n" \
"ds_read_b128 a[148:151], v5 offset:21504\n" \
"v_accvgpr_read_b32 v91, a19\n" \
"v_mfma_f32_16x16x32_bf16 v[100:103], a[156:159], v[44:47], v[100:103]\n" \
"v_accvgpr_read_b32 v92, a20\n" \
"v_accvgpr_read_b32 v93, a21\n" \
"v_mfma_f32_16x16x32_bf16 v[108:111], a[152:155], v[56:59], v[108:111]\n" \
"v_accvgpr_read_b32 v94, a22\n" \
"v_accvgpr_read_b32 v95, a23\n" \
"v_mfma_f32_16x16x32_bf16 v[108:111], a[156:159], v[60:63], v[108:111]\n" \
"v_mul_f32 v88, v188, v88\nv_mul_f32 v89, v188, v89\n" \
"v_mfma_f32_16x16x32_bf16 v[124:127], a[152:155], a[88:91], v[124:127]\n" \
"v_mul_f32 v90, v188, v90\nv_mul_f32 v91, v188, v91\n" \
"v_mfma_f32_16x16x32_bf16 v[124:127], a[156:159], a[92:95], v[124:127]\n" \
"ds_read_b128 a[152:155], v5 offset:22528\n" \
"v_accvgpr_read_b32 v136, a48\n" \
"v_mfma_f32_16x16x32_bf16 v[160:163], v[180:183], v[128:131], 0\n" \
"ds_read_b128 a[156:159], v5 offset:23552\n" \
"v_accvgpr_read_b32 v137, a49\n" \
"v_mfma_f32_16x16x32_bf16 v[164:167], v[180:183], v[132:135], 0\n" \
"s_waitcnt lgkmcnt(6)\n" \
"v_mul_f32 v92, v188, v92\nv_mul_f32 v93, v188, v93\n" \
"v_mfma_f32_16x16x32_bf16 v[192:195], a[128:131], v[128:131], 0\n" \
"v_mul_f32 v94, v188, v94\nv_mul_f32 v95, v188, v95\n" \
"v_mfma_f32_16x16x32_bf16 v[196:199], a[132:135], v[128:131], 0\n" \
"v_exp_f32 v96, v96\n" \
"v_mfma_f32_16x16x32_bf16 v[224:227], a[128:131], v[132:135], 0\n" \
"v_exp_f32 v97, v97\n" \
"v_mfma_f32_16x16x32_bf16 v[228:231], a[132:135], v[132:135], 0\n" \
"s_waitcnt lgkmcnt(4)\n" \
"v_exp_f32 v98, v98\n" \
"v_mfma_f32_16x16x32_bf16 v[200:203], a[136:139], v[128:131], 0\n" \
"v_exp_f32 v99, v99\n" \
"v_mfma_f32_16x16x32_bf16 v[204:207], a[140:143], v[128:131], 0\n" \
"v_exp_f32 v100, v100\n" \
"v_mfma_f32_16x16x32_bf16 v[232:235], a[136:139], v[132:135], 0\n" \
"v_exp_f32 v101, v101\n" \
"v_mfma_f32_16x16x32_bf16 v[236:239], a[140:143], v[132:135], 0\n" \
"s_waitcnt lgkmcnt(2)\n" \
"v_exp_f32 v102, v102\n" \
"v_mfma_f32_16x16x32_bf16 v[208:211], a[144:147], v[128:131], 0\n" \
"v_exp_f32 v103, v103\n" \
"v_mfma_f32_16x16x32_bf16 v[212:215], a[148:151], v[128:131], 0\n" \
"v_exp_f32 v104, v104\n" \
"v_mfma_f32_16x16x32_bf16 v[240:243], a[144:147], v[132:135], 0\n" \
"v_exp_f32 v105, v105\n" \
"v_mfma_f32_16x16x32_bf16 v[244:247], a[148:151], v[132:135], 0\n" \
"s_waitcnt lgkmcnt(0)\n" \
"v_exp_f32 v106, v106\n" \
"v_mfma_f32_16x16x32_bf16 v[216:219], a[152:155], v[128:131], 0\n" \
"v_exp_f32 v107, v107\n" \
"v_mfma_f32_16x16x32_bf16 v[220:223], a[156:159], v[128:131], 0\n" \
"v_exp_f32 v108, v108\n" \
"v_mfma_f32_16x16x32_bf16 v[248:251], a[152:155], v[132:135], 0\n" \
"v_exp_f32 v109, v109\n" \
"v_mfma_f32_16x16x32_bf16 v[252:255], a[156:159], v[132:135], 0\n" \
"v_exp_f32 v110, v110\n" \
"v_exp_f32 v111, v111\n" \
"v_cvt_pk_bf16_f32 v144, v96, v97\n" \
"v_cvt_pk_bf16_f32 v145, v98, v99\n" \
"v_cvt_pk_bf16_f32 v146, v100, v101\n" \
"v_cvt_pk_bf16_f32 v147, v102, v103\n" \
"v_cvt_pk_bf16_f32 v148, v104, v105\n" \
"v_cvt_pk_bf16_f32 v149, v106, v107\n" \
"v_cvt_pk_bf16_f32 v150, v108, v109\n" \
"v_cvt_pk_bf16_f32 v151, v110, v111\n" \
"v_cvt_pk_bf16_f32 v88, v88, v89\n" \
"v_cvt_pk_bf16_f32 v89, v90, v91\n" \
"v_cvt_pk_bf16_f32 v90, v92, v93\n" \
"v_cvt_pk_bf16_f32 v91, v94, v95\n" \
"v_accvgpr_read_b32 v138, a50\n" \
"v_accvgpr_read_b32 v139, a51\n" \
"buffer_store_dwordx4 v[88:91], v7, s[36:39], s63 offen offset:0 sc1\n" \
"v_accvgpr_read_b32 v140, a52\n" \
"v_accvgpr_read_b32 v141, a53\n" \
"v_accvgpr_read_b32 v142, a54\n" \
"v_accvgpr_read_b32 v143, a55\n" \
"v_accvgpr_read_b32 v80, a24\n" \
"v_accvgpr_read_b32 v81, a25\n" \
"v_pk_mul_f32 v[136:137], v[136:137], v[24:25] op_sel_hi:[1,0]\n" \
"v_pk_mul_f32 v[138:139], v[138:139], v[24:25] op_sel_hi:[1,0]\n" \
"v_pk_mul_f32 v[140:141], v[140:141], v[24:25] op_sel_hi:[1,0]\n" \
"v_pk_mul_f32 v[142:143], v[142:143], v[24:25] op_sel_hi:[1,0]\n" \
"s_add_u32 s17, s17, s19\n" \
"s_add_u32 m0, s49, 49152\n" \
"v_cvt_pk_bf16_f32 v136, v136, v137\n" \
"v_cvt_pk_bf16_f32 v137, v138, v139\n" \
"s_waitcnt vmcnt(25) lgkmcnt(8)\n" \
"v_cvt_pk_bf16_f32 v138, v140, v141\n" \
"v_cvt_pk_bf16_f32 v139, v142, v143\n" \
"v_accvgpr_read_b32 v82, a26\n" \
"s_barrier\n" \
"buffer_store_dwordx4 v[136:139], v7, s[36:39], s63 offen offset:1024 sc1\n" \
"v_accvgpr_read_b32 v83, a27\n" \
"ds_read_b128 a[128:131], v5 offset:32768\n" \
"v_accvgpr_read_b32 v84, a28\n" \
"v_accvgpr_read_b32 v85, a29\n" \
"v_accvgpr_read_b32 v86, a30\n" \
"buffer_load_dwordx4 v6, s[40:43], s17 offen offset:0 sc1 lds\n" \
"v_accvgpr_read_b32 v87, a31\n" \
"v_accvgpr_read_b32 v88, a56\n" \
"ds_read_b128 a[132:135], v5 offset:33792\n" \
"v_pk_mul_f32 v[80:81], v[80:81], v[188:189] op_sel_hi:[1,0]\n" \
"v_pk_mul_f32 v[82:83], v[82:83], v[188:189] op_sel_hi:[1,0]\n" \
"v_pk_mul_f32 v[84:85], v[84:85], v[188:189] op_sel_hi:[1,0]\n" \
"buffer_load_dwordx4 v6, s[40:43], s17 offen offset:1024 sc1 lds\n" \
"v_pk_mul_f32 v[86:87], v[86:87], v[188:189] op_sel_hi:[1,0]\n" \
"v_cvt_pk_bf16_f32 v80, v80, v81\n" \
"ds_read_b128 a[136:139], v5 offset:34816\n" \
"v_cvt_pk_bf16_f32 v81, v82, v83\n" \
"v_cvt_pk_bf16_f32 v82, v84, v85\n" \
"v_cvt_pk_bf16_f32 v83, v86, v87\n" \
"buffer_load_dwordx4 v6, s[40:43], s17 offen offset:2048 sc1 lds\n" \
"v_accvgpr_read_b32 v89, a57\n" \
"v_accvgpr_read_b32 v90, a58\n" \
"ds_read_b128 a[140:143], v5 offset:35840\n" \
"v_accvgpr_read_b32 v91, a59\n" \
"v_accvgpr_read_b32 v92, a60\n" \
"v_accvgpr_read_b32 v93, a61\n" \
"buffer_load_dwordx4 v6, s[40:43], s17 offen offset:3072 sc1 lds\n" \
"v_accvgpr_read_b32 v94, a62\n" \
"v_accvgpr_read_b32 v95, a63\n" \
"ds_read_b128 a[144:147], v5 offset:36864\n" \
"v_pk_mul_f32 v[88:89], v[88:89], v[24:25] op_sel_hi:[1,0]\n" \
"v_pk_mul_f32 v[90:91], v[90:91], v[24:25] op_sel_hi:[1,0]\n" \
"v_pk_mul_f32 v[92:93], v[92:93], v[24:25] op_sel_hi:[1,0]\n" \
"buffer_store_dwordx4 v[80:83], v7, s[36:39], s63 offen offset:2048 sc1\n" \
"v_pk_mul_f32 v[94:95], v[94:95], v[24:25] op_sel_hi:[1,0]\n" \
"v_cvt_pk_bf16_f32 v88, v88, v89\n" \
"ds_read_b128 a[148:151], v5 offset:37888\n" \
"v_cvt_pk_bf16_f32 v89, v90, v91\n" \
"v_cvt_pk_bf16_f32 v90, v92, v93\n" \
"v_cvt_pk_bf16_f32 v91, v94, v95\n" \
"v_mov_b32 v116, 0xff800000\n" \
"v_mov_b32 v117, 0xff800000\n" \
"v_mov_b32 v118, 0xff800000\n" \
"v_mov_b32 v119, 0xff800000\n" \
"buffer_store_dwordx4 v[88:91], v7, s[36:39], s63 offen offset:3072 sc1\n" \
"s_waitcnt lgkmcnt(4)\n" \
"v_mfma_f32_16x16x32_bf16 v[64:67], a[128:131], v[32:35], 0\n" \
"v_exp_f32 v112, v112\n" \
"v_mfma_f32_16x16x32_bf16 v[64:67], a[132:135], v[36:39], v[64:67]\n" \
"s_add_u32 s16, s16, s9\n" \
"v_mfma_f32_16x16x32_bf16 v[72:75], a[128:131], v[48:51], 0\n" \
"ds_read_b128 a[152:155], v5 offset:38912\n" \
"v_mfma_f32_16x16x32_bf16 v[72:75], a[132:135], v[52:55], v[72:75]\n" \
"s_add_u32 m0, s48, 0\n" \
"v_exp_f32 v113, v113\n" \
"v_mfma_f32_16x16x32_bf16 v[80:83], a[128:131], a[64:67], 0\n" \
"buffer_load_dwordx4 v6, s[40:43], s16 offen offset:0 sc1 lds\n" \
"v_mfma_f32_16x16x32_bf16 v[80:83], a[132:135], a[68:71], v[80:83]\n" \
"v_exp_f32 v114, v114\n" \
"v_mfma_f32_16x16x32_bf16 v[88:91], a[128:131], a[80:83], 0\n" \
"ds_read_b128 a[156:159], v5 offset:39936\n" \
"v_cvt_pk_bf16_f32 v152, v112, v113\n" \
"v_mfma_f32_16x16x32_bf16 v[88:91], a[132:135], a[84:87], v[88:91]\n" \
"s_waitcnt lgkmcnt(4)\n" \
"v_exp_f32 v115, v115\n" \
"v_mfma_f32_16x16x32_bf16 v[64:67], a[136:139], v[40:43], v[64:67]\n" \
"v_exp_f32 v116, v116\n" \
"v_mfma_f32_16x16x32_bf16 v[64:67], a[140:143], v[44:47], v[64:67]\n" \
"s_xor_b32 s49, s49, 0x10000\n" \
"v_cvt_pk_bf16_f32 v153, v114, v115\n" \
"v_mfma_f32_16x16x32_bf16 v[72:75], a[136:139], v[56:59], v[72:75]\n" \
"ds_read_b128 a[128:131], v5 offset:49152\n" \
"v_mfma_f32_16x16x32_bf16 v[72:75], a[140:143], v[60:63], v[72:75]\n" \
"v_exp_f32 v117, v117\n" \
"v_mfma_f32_16x16x32_bf16 v[80:83], a[136:139], a[72:75], v[80:83]\n" \
"buffer_load_dwordx4 v6, s[40:43], s16 offen offset:1024 sc1 lds\n" \
"v_mfma_f32_16x16x32_bf16 v[80:83], a[140:143], a[76:79], v[80:83]\n" \
"v_exp_f32 v118, v118\n" \
"v_mfma_f32_16x16x32_bf16 v[88:91], a[136:139], a[88:91], v[88:91]\n" \
"ds_read_b128 a[132:135], v5 offset:50176\n" \
"v_cvt_pk_bf16_f32 v154, v116, v117\n" \
"v_mfma_f32_16x16x32_bf16 v[88:91], a[140:143], a[92:95], v[88:91]\n" \
"s_waitcnt lgkmcnt(4)\n" \
"v_exp_f32 v119, v119\n" \
"v_mfma_f32_16x16x32_bf16 v[68:71], a[144:147], v[32:35], 0\n" \
"v_exp_f32 v120, v120\n" \
"v_mfma_f32_16x16x32_bf16 v[68:71], a[148:151], v[36:39], v[68:71]\n" \
"v_cvt_pk_bf16_f32 v155, v118, v119\n" \
"v_mfma_f32_16x16x32_bf16 v[76:79], a[144:147], v[48:51], 0\n" \
"ds_read_b128 a[136:139], v5 offset:51200\n" \
"v_mfma_f32_16x16x32_bf16 v[76:79], a[148:151], v[52:55], v[76:79]\n" \
"v_exp_f32 v121, v121\n" \
"v_mfma_f32_16x16x32_bf16 v[84:87], a[144:147], a[64:67], 0\n" \
"buffer_load_dwordx4 v6, s[40:43], s16 offen offset:2048 sc1 lds\n" \
"v_mfma_f32_16x16x32_bf16 v[84:87], a[148:151], a[68:71], v[84:87]\n" \
"v_exp_f32 v122, v122\n" \
"v_mfma_f32_16x16x32_bf16 v[92:95], a[144:147], a[80:83], 0\n" \
"ds_read_b128 a[140:143], v5 offset:52224\n" \
"v_cvt_pk_bf16_f32 v156, v120, v121\n" \
"v_mfma_f32_16x16x32_bf16 v[92:95], a[148:151], a[84:87], v[92:95]\n" \
"s_waitcnt lgkmcnt(4)\n" \
"v_exp_f32 v123, v123\n" \
"v_mfma_f32_16x16x32_bf16 v[68:71], a[152:155], v[40:43], v[68:71]\n" \
"v_exp_f32 v124, v124\n" \
"v_mfma_f32_16x16x32_bf16 v[68:71], a[156:159], v[44:47], v[68:71]\n" \
"v_cvt_pk_bf16_f32 v157, v122, v123\n" \
"v_mfma_f32_16x16x32_bf16 v[76:79], a[152:155], v[56:59], v[76:79]\n" \
"ds_read_b128 a[144:147], v5 offset:53248\n" \
"v_mfma_f32_16x16x32_bf16 v[76:79], a[156:159], v[60:63], v[76:79]\n" \
"v_exp_f32 v125, v125\n" \
"v_mfma_f32_16x16x32_bf16 v[84:87], a[152:155], a[72:75], v[84:87]\n" \
"buffer_load_dwordx4 v6, s[40:43], s16 offen offset:3072 sc1 lds\n" \
"v_mfma_f32_16x16x32_bf16 v[84:87], a[156:159], a[76:79], v[84:87]\n" \
"v_exp_f32 v126, v126\n" \
"v_mfma_f32_16x16x32_bf16 v[92:95], a[152:155], a[88:91], v[92:95]\n" \
"ds_read_b128 a[148:151], v5 offset:54272\n" \
"v_cvt_pk_bf16_f32 v158, v124, v125\n" \
"v_mfma_f32_16x16x32_bf16 v[92:95], a[156:159], a[92:95], v[92:95]\n" \
"s_waitcnt lgkmcnt(4)\n" \
"v_exp_f32 v127, v127\n" \
"v_mfma_f32_16x16x32_bf16 v[160:163], v[180:183], v[144:147], v[160:163]\n" \
"v_cvt_pk_bf16_f32 v159, v126, v127\n" \
"v_mfma_f32_16x16x32_bf16 v[164:167], v[180:183], v[148:151], v[164:167]\n" \
"v_mfma_f32_16x16x32_bf16 v[168:171], v[180:183], v[152:155], 0\n" \
"v_exp_f32 v64, v64\n" \
"v_mfma_f32_16x16x32_bf16 v[172:175], v[180:183], v[156:159], 0\n" \
"v_mfma_f32_16x16x32_bf16 v[192:195], a[128:131], v[144:147], v[192:195]\n" \
"v_exp_f32 v65, v65\n" \
"v_mfma_f32_16x16x32_bf16 v[196:199], a[132:135], v[144:147], v[196:199]\n" \
"v_cvt_pk_bf16_f32 v128, v64, v65\n" \
"v_mfma_f32_16x16x32_bf16 v[224:227], a[128:131], v[148:151], v[224:227]\n" \
"ds_read_b128 a[152:155], v5 offset:55296\n" \
"v_mfma_f32_16x16x32_bf16 v[228:231], a[132:135], v[148:151], v[228:231]\n" \
"v_exp_f32 v66, v66\n" \
"v_mfma_f32_16x16x32_bf16 a[0:3], a[128:131], v[152:155], 0\n" \
"v_exp_f32 v67, v67\n" \
"v_mfma_f32_16x16x32_bf16 a[4:7], a[132:135], v[152:155], 0\n" \
"v_cvt_pk_bf16_f32 v129, v66, v67\n" \
"v_mfma_f32_16x16x32_bf16 a[32:35], a[128:131], v[156:159], 0\n" \
"ds_read_b128 a[156:159], v5 offset:56320\n" \
"v_mfma_f32_16x16x32_bf16 a[36:39], a[132:135], v[156:159], 0\n" \
"s_waitcnt vmcnt(20) lgkmcnt(4)\n" \
"v_exp_f32 v68, v68\n" \
"v_mfma_f32_16x16x32_bf16 v[200:203], a[136:139], v[144:147], v[200:203]\n" \
"s_barrier\n" \
"v_xor_b32 v5, 0x10000, v5\n" \
"v_mfma_f32_16x16x32_bf16 v[204:207], a[140:143], v[144:147], v[204:207]\n" \
"s_add_u32 s17, s17, s19\n" \
"v_exp_f32 v69, v69\n" \
"v_mfma_f32_16x16x32_bf16 v[232:235], a[136:139], v[148:151], v[232:235]\n" \
"ds_read_b128 a[128:131], v5 offset:0\n" \
"v_cvt_pk_bf16_f32 v130, v68, v69\n" \
"v_mfma_f32_16x16x32_bf16 v[236:239], a[140:143], v[148:151], v[236:239]\n" \
"s_add_u32 m0, s48, 16384\n" \
"v_exp_f32 v70, v70\n" \
"v_mfma_f32_16x16x32_bf16 a[8:11], a[136:139], v[152:155], 0\n" \
"buffer_load_dwordx4 v6, s[40:43], s17 offen offset:0 sc1 lds\n" \
"v_mfma_f32_16x16x32_bf16 a[12:15], a[140:143], v[152:155], 0\n" \
"s_waitcnt vmcnt(5)\n" \
"s_cmp_lg_u64 s[54:55], 0\ns_cbranch_scc1 LFBA_%=\nLFBAR_%=:\n" \
"v_mfma_f32_16x16x32_bf16 a[40:43], a[136:139], v[156:159], 0\n" \
"ds_read_b128 a[132:135], v5 offset:1024\n" \
"v_mfma_f32_16x16x32_bf16 a[44:47], a[140:143], v[156:159], 0\n" \
"s_waitcnt lgkmcnt(4)\n" \
"v_exp_f32 v71, v71\n" \
"v_mfma_f32_16x16x32_bf16 v[208:211], a[144:147], v[144:147], v[208:211]\n" \
"v_exp_f32 v72, v72\n" \
"v_mfma_f32_16x16x32_bf16 v[212:215], a[148:151], v[144:147], v[212:215]\n" \
"v_exp_f32 v73, v73\n" \
"v_mfma_f32_16x16x32_bf16 v[240:243], a[144:147], v[148:151], v[240:243]\n" \
"ds_read_b128 a[136:139], v5 offset:2048\n" \
"v_cvt_pk_bf16_f32 v131, v70, v71\n" \
"v_mfma_f32_16x16x32_bf16 v[244:247], a[148:151], v[148:151], v[244:247]\n" \
"v_exp_f32 v74, v74\n" \
"v_mfma_f32_16x16x32_bf16 a[16:19], a[144:147], v[152:155], 0\n" \
"buffer_load_dwordx4 v6, s[40:43], s17 offen offset:1024 sc1 lds\n" \
"v_mfma_f32_16x16x32_bf16 a[20:23], a[148:151], v[152:155], 0\n" \
"v_exp_f32 v75, v75\n" \
"v_mfma_f32_16x16x32_bf16 a[48:51], a[144:147], v[156:159], 0\n" \
"ds_read_b128 a[140:143], v5 offset:3072\n" \
"v_cvt_pk_bf16_f32 v132, v72, v73\n" \
"v_mfma_f32_16x16x32_bf16 a[52:55], a[148:151], v[156:159], 0\n" \
"s_waitcnt lgkmcnt(4)\n" \
"v_exp_f32 v76, v76\n" \
"v_mfma_f32_16x16x32_bf16 v[216:219], a[152:155], v[144:147], v[216:219]\n" \
"buffer_load_dwordx4 v6, s[40:43], s17 offen offset:2048 sc1 lds\n" \
"v_mfma_f32_16x16x32_bf16 v[220:223], a[156:159], v[144:147], v[220:223]\n" \
"s_add_u32 s44, s44, 1\n" \
"v_exp_f32 v77, v77\n" \
"v_mfma_f32_16x16x32_bf16 v[248:251], a[152:155], v[148:151], v[248:251]\n" \
"ds_read_b128 a[144:147], v5 offset:4096\n" \
"v_cvt_pk_bf16_f32 v133, v74, v75\n" \
"v_mfma_f32_16x16x32_bf16 v[252:255], a[156:159], v[148:151], v[252:255]\n" \
"v_exp_f32 v78, v78\n" \
"v_mfma_f32_16x16x32_bf16 a[24:27], a[152:155], v[152:155], 0\n" \
"buffer_load_dwordx4 v6, s[40:43], s17 offen offset:3072 sc1 lds\n" \
"v_mfma_f32_16x16x32_bf16 a[28:31], a[156:159], v[152:155], 0\n" \
"s_xor_b32 s48, s48, 0x10000\n" \
"v_exp_f32 v79, v79\n" \
"v_mfma_f32_16x16x32_bf16 a[56:59], a[152:155], v[156:159], 0\n" \
"ds_read_b128 a[148:151], v5 offset:5120\n" \
"v_cvt_pk_bf16_f32 v134, v76, v77\n" \
"v_mfma_f32_16x16x32_bf16 a[60:63], a[156:159], v[156:159], 0\n" \
"s_waitcnt lgkmcnt(4)\n" \
"v_cvt_pk_bf16_f32 v135, v78, v79\n" \
"buffer_store_dwordx2 v[10:11], v9, s[36:39], s52 offen sc1\n" \
"s_mov_b32 s46, s24\n" \
"s_branch LBOT_%=\n" \
"LAD32_%=:\n" \
"v_mov_b32 v96, 0\n" \
"v_mov_b32 v97, 0\n" \
"v_mov_b32 v98, 0\n" \
"v_mov_b32 v99, 0\n" \
"buffer_store_dwordx4 v[88:91], v4, s[36:39], s63 offen offset:1024 sc1\n" \
"v_pk_mul_f32 v[142:143], v[142:143], v[188:189] op_sel_hi:[1,0]\n" \
"v_cvt_pk_bf16_f32 v136, v136, v137\n" \
"v_cvt_pk_bf16_f32 v137, v138, v139\n" \
"v_cvt_pk_bf16_f32 v138, v140, v141\n" \
"v_cvt_pk_bf16_f32 v139, v142, v143\n" \
"v_accvgpr_read_b32 v80, a40\n" \
"buffer_store_dwordx4 v[136:139], v4, s[36:39], s63 offen offset:2048 sc1\n" \
"v_accvgpr_read_b32 v81, a41\n" \
"v_accvgpr_read_b32 v82, a42\n" \
"v_accvgpr_read_b32 v83, a43\n" \
"v_accvgpr_read_b32 v84, a44\n" \
"v_accvgpr_read_b32 v85, a45\n" \
"v_accvgpr_read_b32 v86, a46\n" \
"v_accvgpr_read_b32 v87, a47\n" \
"v_accvgpr_read_b32 v88, a16\n" \
"v_accvgpr_read_b32 v89, a17\n" \
"v_accvgpr_read_b32 v90, a18\n" \
"v_pk_mul_f32 v[80:81], v[80:81], v[24:25] op_sel_hi:[1,0]\n" \
"v_pk_mul_f32 v[82:83], v[82:83], v[24:25] op_sel_hi:[1,0]\n" \
"v_pk_mul_f32 v[84:85], v[84:85], v[24:25] op_sel_hi:[1,0]\n" \
"v_pk_mul_f32 v[86:87], v[86:87], v[24:25] op_sel_hi:[1,0]\n" \
"v_cvt_pk_bf16_f32 v80, v80, v81\n" \
"v_cvt_pk_bf16_f32 v81, v82, v83\n" \
"v_cvt_pk_bf16_f32 v82, v84, v85\n" \
"v_cvt_pk_bf16_f32 v83, v86, v87\n" \
"buffer_store_dwordx4 v[80:83], v4, s[36:39], s63 offen offset:3072 sc1\n" \
"v_accvgpr_read_b32 v91, a19\n" \
"v_accvgpr_read_b32 v92, a20\n" \
"v_accvgpr_read_b32 v93, a21\n" \
"v_accvgpr_read_b32 v94, a22\n" \
"v_accvgpr_read_b32 v95, a23\n" \
"v_accvgpr_read_b32 v136, a48\n" \
"v_pk_mul_f32 v[88:89], v[88:89], v[188:189] op_sel_hi:[1,0]\n" \
"v_pk_mul_f32 v[90:91], v[90:91], v[188:189] op_sel_hi:[1,0]\n" \
"v_pk_mul_f32 v[92:93], v[92:93], v[188:189] op_sel_hi:[1,0]\n" \
"v_pk_mul_f32 v[94:95], v[94:95], v[188:189] op_sel_hi:[1,0]\n" \
"v_cvt_pk_bf16_f32 v88, v88, v89\n" \
"v_cvt_pk_bf16_f32 v89, v90, v91\n" \
"v_cvt_pk_bf16_f32 v90, v92, v93\n" \
"v_cvt_pk_bf16_f32 v91, v94, v95\n" \
"buffer_store_dwordx4 v[88:91], v7, s[36:39], s63 offen offset:0 sc1\n" \
"v_accvgpr_read_b32 v137, a49\n" \
"v_accvgpr_read_b32 v138, a50\n" \
"v_accvgpr_read_b32 v139, a51\n" \
"v_accvgpr_read_b32 v140, a52\n" \
"v_accvgpr_read_b32 v141, a53\n" \
"v_accvgpr_read_b32 v142, a54\n" \
"v_accvgpr_read_b32 v143, a55\n" \
"v_accvgpr_read_b32 v80, a24\n" \
"v_accvgpr_read_b32 v81, a25\n" \
"v_accvgpr_read_b32 v82, a26\n" \
"v_pk_mul_f32 v[136:137], v[136:137], v[24:25] op_sel_hi:[1,0]\n" \
"v_pk_mul_f32 v[138:139], v[138:139], v[24:25] op_sel_hi:[1,0]\n" \
"v_pk_mul_f32 v[140:141], v[140:141], v[24:25] op_sel_hi:[1,0]\n" \
"v_pk_mul_f32 v[142:143], v[142:143], v[24:25] op_sel_hi:[1,0]\n" \
"v_cvt_pk_bf16_f32 v136, v136, v137\n" \
"v_cvt_pk_bf16_f32 v137, v138, v139\n" \
"v_cvt_pk_bf16_f32 v138, v140, v141\n" \
"v_cvt_pk_bf16_f32 v139, v142, v143\n" \
"buffer_store_dwordx4 v[136:139], v7, s[36:39], s63 offen offset:1024 sc1\n" \
"v_accvgpr_read_b32 v83, a27\n" \
"v_accvgpr_read_b32 v84, a28\n" \
"v_accvgpr_read_b32 v85, a29\n" \
"v_accvgpr_read_b32 v86, a30\n" \
"v_accvgpr_read_b32 v87, a31\n" \
"v_accvgpr_read_b32 v88, a56\n" \
"v_pk_mul_f32 v[80:81], v[80:81], v[188:189] op_sel_hi:[1,0]\n" \
"v_pk_mul_f32 v[82:83], v[82:83], v[188:189] op_sel_hi:[1,0]\n" \
"v_pk_mul_f32 v[84:85], v[84:85], v[188:189] op_sel_hi:[1,0]\n" \
"v_pk_mul_f32 v[86:87], v[86:87], v[188:189] op_sel_hi:[1,0]\n" \
"v_cvt_pk_bf16_f32 v80, v80, v81\n" \
"v_cvt_pk_bf16_f32 v81, v82, v83\n" \
"v_cvt_pk_bf16_f32 v82, v84, v85\n" \
"v_cvt_pk_bf16_f32 v83, v86, v87\n" \
"buffer_store_dwordx4 v[80:83], v7, s[36:39], s63 offen offset:2048 sc1\n" \
"v_accvgpr_read_b32 v89, a57\n" \
"v_accvgpr_read_b32 v90, a58\n" \
"v_accvgpr_read_b32 v91, a59\n" \
"v_accvgpr_read_b32 v92, a60\n" \
"v_accvgpr_read_b32 v93, a61\n" \
"v_accvgpr_read_b32 v94, a62\n" \
"v_accvgpr_read_b32 v95, a63\n" \
"v_pk_mul_f32 v[88:89], v[88:89], v[24:25] op_sel_hi:[1,0]\n" \
"v_pk_mul_f32 v[90:91], v[90:91], v[24:25] op_sel_hi:[1,0]\n" \
"v_pk_mul_f32 v[92:93], v[92:93], v[24:25] op_sel_hi:[1,0]\n" \
"v_pk_mul_f32 v[94:95], v[94:95], v[24:25] op_sel_hi:[1,0]\n" \
"v_cvt_pk_bf16_f32 v88, v88, v89\n" \
"v_cvt_pk_bf16_f32 v89, v90, v91\n" \
"v_cvt_pk_bf16_f32 v90, v92, v93\n" \
"v_cvt_pk_bf16_f32 v91, v94, v95\n" \
"buffer_store_dwordx4 v[88:91], v7, s[36:39], s63 offen offset:3072 sc1\n" \
"v_mfma_f32_32x32x16_bf16 v[192:207], v[96:99], v[96:99], 0\n" \
"v_mfma_f32_32x32x16_bf16 v[208:223], v[96:99], v[96:99], 0\n" \
"v_mfma_f32_32x32x16_bf16 v[224:239], v[96:99], v[96:99], 0\n" \
"v_mfma_f32_32x32x16_bf16 v[240:255], v[96:99], v[96:99], 0\n" \
"v_mfma_f32_32x32x16_bf16 a[0:15], v[96:99], v[96:99], 0\n" \
"v_mfma_f32_32x32x16_bf16 a[16:31], v[96:99], v[96:99], 0\n" \
"v_mfma_f32_32x32x16_bf16 a[32:47], v[96:99], v[96:99], 0\n" \
"v_mfma_f32_32x32x16_bf16 a[48:63], v[96:99], v[96:99], 0\n" \
"v_mfma_f32_32x32x16_bf16 v[160:175], v[96:99], v[96:99], 0\n" \
"v_mov_b32 v80, 0xff800000\n" \
"v_mov_b32 v81, 0xff800000\n" \
"v_mov_b32 v82, 0xff800000\n" \
"v_mov_b32 v83, 0xff800000\n" \
"v_mov_b32 v84, 0xff800000\n" \
"v_mov_b32 v85, 0xff800000\n" \
"v_mov_b32 v86, 0xff800000\n" \
"v_mov_b32 v87, 0xff800000\n" \
"v_mov_b32 v88, 0xff800000\n" \
"v_mov_b32 v89, 0xff800000\n" \
"v_mov_b32 v90, 0xff800000\n" \
"v_mov_b32 v91, 0xff800000\n" \
"v_mov_b32 v92, 0xff800000\n" \
"v_mov_b32 v93, 0xff800000\n" \
"v_mov_b32 v94, 0xff800000\n" \
"v_mov_b32 v95, 0xff800000\n" \
"s_branch LFIN_%=\n" \
".p2align 6\n" \
"LH22_%=:\ns_cbranch_execz LH26_%=\n" \
"s_nop 0\n" \
"LDISP_%=:\n" \
"s_cmp_eq_u32 s44, 16\n" \
"s_cbranch_scc1 LFIN_%=\n" \
"s_cmp_eq_u32 s44, 15\ns_cselect_b32 s16, s56, s16\ns_cselect_b32 s9, s57, s9\ns_cselect_b32 s17, s58, s17\ns_cselect_b32 s19, s59, s19\n" \
"s_cbranch_scc0 LD14_%=\n" \
"buffer_load_dword v1, v191, s[36:39], 0 offen sc1\n" \
"s_branch LEVEN_%=\n" \
"LD14_%=:\n" \
"buffer_load_dwordx2 v[18:19], v17, s[36:39], 0 offen sc1\n" \
"s_branch LEVEN_%=\n" \
".p2align 6\n" \
"LH23_%=:\ns_cbranch_execz LH27_%=\n" \
"s_nop 0\n" \
"LFIN_%=:\n" \
"s_waitcnt lgkmcnt(6)\n" \
"ds_read_b128 a[128:131], v5 offset:16384\n" \
"ds_read_b128 a[132:135], v5 offset:17408\n" \
"s_mov_b32 s81, 786432\n" \
"ds_read_b128 a[136:139], v5 offset:18432\n" \
"ds_read_b128 a[140:143], v5 offset:19456\n" \
"s_waitcnt vmcnt(8)\n" \
"s_barrier\n" \
"ds_read_b128 a[144:147], v5 offset:20480\n" \
"ds_read_b128 a[148:151], v5 offset:21504\n" \
"v_exp_f32 v80, v80\n" \
"ds_read_b128 a[152:155], v5 offset:22528\n" \
"ds_read_b128 a[156:159], v5 offset:23552\n" \
"v_exp_f32 v81, v81\n" \
"ds_read_b128 v[96:99], v29 offset:32768\n" \
"v_exp_f32 v82, v82\n" \
"v_cvt_pk_bf16_f32 v136, v80, v81\n" \
"v_mfma_f32_16x16x32_bf16 v[160:163], v[180:183], v[128:131], v[160:163]\n" \
"s_add_u32 m0, s79, 32768\n" \
"ds_read_b128 v[100:103], v29 offset:34816\n" \
"v_exp_f32 v83, v83\n" \
"v_mfma_f32_16x16x32_bf16 v[164:167], v[180:183], v[132:135], v[164:167]\n" \
"ds_read_b128 v[104:107], v29 offset:33792\n" \
"s_waitcnt lgkmcnt(9)\n" \
"v_cvt_pk_bf16_f32 v137, v82, v83\n" \
"v_mfma_f32_16x16x32_bf16 v[192:195], a[128:131], v[128:131], v[192:195]\n" \
"ds_read_b128 v[108:111], v29 offset:35840\n" \
"v_exp_f32 v84, v84\n" \
"v_mfma_f32_16x16x32_bf16 v[196:199], a[132:135], v[128:131], v[196:199]\n" \
"ds_read_b128 v[112:115], v29 offset:36864\n" \
"v_exp_f32 v85, v85\n" \
"v_mfma_f32_16x16x32_bf16 v[224:227], a[128:131], v[132:135], v[224:227]\n" \
"ds_read_b128 v[116:119], v29 offset:38912\n" \
"v_exp_f32 v86, v86\n" \
"v_mfma_f32_16x16x32_bf16 v[228:231], a[132:135], v[132:135], v[228:231]\n" \
"ds_read_b128 v[120:123], v29 offset:37888\n" \
"s_waitcnt lgkmcnt(7)\n" \
"v_cvt_pk_bf16_f32 v138, v84, v85\n" \
"v_mfma_f32_16x16x32_bf16 v[200:203], a[136:139], v[128:131], v[200:203]\n" \
"ds_read_b128 v[124:127], v29 offset:39936\n" \
"v_exp_f32 v87, v87\n" \
"v_mfma_f32_16x16x32_bf16 v[204:207], a[140:143], v[128:131], v[204:207]\n" \
"ds_write2_b32 v184, v160, v164 offset1:64\n" \
"v_exp_f32 v88, v88\n" \
"v_mfma_f32_16x16x32_bf16 v[232:235], a[136:139], v[132:135], v[232:235]\n" \
"buffer_load_dwordx4 v12, s[28:31], s81 offen offset:0 lds\n" \
"v_mfma_f32_16x16x32_bf16 v[236:239], a[140:143], v[132:135], v[236:239]\n" \
"s_add_u32 m0, s79, 33664\n" \
"v_exp_f32 v89, v89\n" \
"v_cvt_pk_bf16_f32 v139, v86, v87\n" \
"v_mfma_f32_16x16x32_bf16 v[208:211], a[144:147], v[128:131], v[208:211]\n" \
"v_exp_f32 v90, v90\n" \
"v_cvt_pk_bf16_f32 v140, v88, v89\n" \
"v_mfma_f32_16x16x32_bf16 v[212:215], a[148:151], v[128:131], v[212:215]\n" \
"v_exp_f32 v91, v91\n" \
"v_cvt_pk_bf16_f32 v192, v192, v193\n" \
"v_mfma_f32_16x16x32_bf16 v[240:243], a[144:147], v[132:135], v[240:243]\n" \
"v_exp_f32 v92, v92\n" \
"v_cvt_pk_bf16_f32 v141, v90, v91\n" \
"v_mfma_f32_16x16x32_bf16 v[244:247], a[148:151], v[132:135], v[244:247]\n" \
"v_exp_f32 v93, v93\n" \
"v_cvt_pk_bf16_f32 v193, v194, v195\n" \
"v_mfma_f32_16x16x32_bf16 v[216:219], a[152:155], v[128:131], v[216:219]\n" \
"v_exp_f32 v94, v94\n" \
"v_cvt_pk_bf16_f32 v142, v92, v93\n" \
"v_mfma_f32_16x16x32_bf16 v[220:223], a[156:159], v[128:131], v[220:223]\n" \
"v_exp_f32 v95, v95\n" \
"v_cvt_pk_bf16_f32 v194, v196, v197\n" \
"v_mfma_f32_16x16x32_bf16 v[248:251], a[152:155], v[132:135], v[248:251]\n" \
"v_cvt_pk_bf16_f32 v143, v94, v95\n" \
"v_cvt_pk_bf16_f32 v195, v198, v199\n" \
"v_cvt_pk_bf16_f32 v224, v224, v225\n" \
"v_mfma_f32_16x16x32_bf16 v[252:255], a[156:159], v[132:135], v[252:255]\n" \
"buffer_load_dwordx4 v12, s[28:31], s81 offen offset:128 lds\n" \
"v_mfma_f32_16x16x32_bf16 v[168:171], v[180:183], v[136:139], v[168:171]\n" \
"s_mov_b32 s81, 851968\n" \
"ds_write_b128 v186, v[192:195] offset:0\n" \
"v_cvt_pk_bf16_f32 v225, v226, v227\n" \
"v_cvt_pk_bf16_f32 v226, v228, v229\n" \
"v_mfma_f32_16x16x32_bf16 v[172:175], v[180:183], v[140:143], v[172:175]\n" \
"s_add_u32 m0, s79, 40960\n" \
"v_cvt_pk_bf16_f32 v227, v230, v231\n" \
"v_cvt_pk_bf16_f32 v200, v200, v201\n" \
"v_cvt_pk_bf16_f32 v201, v202, v203\n" \
"v_mfma_f32_16x16x32_bf16 a[0:3], a[128:131], v[136:139], a[0:3]\n" \
"ds_write_b128 v186, v[224:227] offset:4096\n" \
"v_cvt_pk_bf16_f32 v202, v204, v205\n" \
"v_cvt_pk_bf16_f32 v203, v206, v207\n" \
"v_mfma_f32_16x16x32_bf16 a[4:7], a[132:135], v[136:139], a[4:7]\n" \
"buffer_load_dwordx4 v12, s[28:31], s81 offen offset:0 lds\n" \
"v_mfma_f32_16x16x32_bf16 a[32:35], a[128:131], v[140:143], a[32:35]\n" \
"s_add_u32 m0, s79, 41856\n" \
"ds_write_b128 v186, v[200:203] offset:1024\n" \
"v_cvt_pk_bf16_f32 v232, v232, v233\n" \
"v_cvt_pk_bf16_f32 v233, v234, v235\n" \
"v_mfma_f32_16x16x32_bf16 a[36:39], a[132:135], v[140:143], a[36:39]\n" \
"v_cvt_pk_bf16_f32 v234, v236, v237\n" \
"v_cvt_pk_bf16_f32 v235, v238, v239\n" \
"v_cvt_pk_bf16_f32 v208, v208, v209\n" \
"v_mfma_f32_16x16x32_bf16 a[8:11], a[136:139], v[136:139], a[8:11]\n" \
"ds_write_b128 v186, v[232:235] offset:5120\n" \
"v_cvt_pk_bf16_f32 v209, v210, v211\n" \
"v_cvt_pk_bf16_f32 v210, v212, v213\n" \
"v_mfma_f32_16x16x32_bf16 a[12:15], a[140:143], v[136:139], a[12:15]\n" \
"buffer_load_dwordx4 v12, s[28:31], s81 offen offset:128 lds\n" \
"v_mfma_f32_16x16x32_bf16 a[40:43], a[136:139], v[140:143], a[40:43]\n" \
"s_add_u32 s80, s65, 16384\n" \
"v_cvt_pk_bf16_f32 v211, v214, v215\n" \
"v_cvt_pk_bf16_f32 v240, v240, v241\n" \
"v_cvt_pk_bf16_f32 v241, v242, v243\n" \
"v_mfma_f32_16x16x32_bf16 a[44:47], a[140:143], v[140:143], a[44:47]\n" \
"s_add_u32 m0, s22, 49152\n" \
"ds_write_b128 v186, v[208:211] offset:2048\n" \
"v_cvt_pk_bf16_f32 v242, v244, v245\n" \
"v_cvt_pk_bf16_f32 v243, v246, v247\n" \
"v_mfma_f32_16x16x32_bf16 a[16:19], a[144:147], v[136:139], a[16:19]\n" \
"ds_write_b128 v186, v[240:243] offset:6144\n" \
"v_cvt_pk_bf16_f32 v216, v216, v217\n" \
"v_cvt_pk_bf16_f32 v217, v218, v219\n" \
"v_mfma_f32_16x16x32_bf16 a[20:23], a[148:151], v[136:139], a[20:23]\n" \
"buffer_load_dwordx4 v4, s[36:39], s80 offen offset:0 sc1 lds\n" \
"v_mfma_f32_16x16x32_bf16 a[48:51], a[144:147], v[140:143], a[48:51]\n" \
"v_cvt_pk_bf16_f32 v218, v220, v221\n" \
"v_cvt_pk_bf16_f32 v219, v222, v223\n" \
"v_cvt_pk_bf16_f32 v248, v248, v249\n" \
"v_mfma_f32_16x16x32_bf16 a[52:55], a[148:151], v[140:143], a[52:55]\n" \
"ds_write_b128 v186, v[216:219] offset:3072\n" \
"v_cvt_pk_bf16_f32 v249, v250, v251\n" \
"v_cvt_pk_bf16_f32 v250, v252, v253\n" \
"v_mfma_f32_16x16x32_bf16 a[24:27], a[152:155], v[136:139], a[24:27]\n" \
"v_cvt_pk_bf16_f32 v251, v254, v255\n" \
"v_and_b32 v188, 16, v4\n" \
"v_mov_b32 v189, 0x3f80\n" \
"v_mfma_f32_16x16x32_bf16 a[28:31], a[156:159], v[136:139], a[28:31]\n" \
"buffer_load_dwordx4 v4, s[36:39], s80 offen offset:1024 sc1 lds\n" \
"v_mfma_f32_16x16x32_bf16 a[56:59], a[152:155], v[140:143], a[56:59]\n" \
"s_waitcnt lgkmcnt(14)\n" \
"ds_write_b128 v186, v[248:251] offset:7168\n" \
"v_lshlrev_b32 v188, v188, v189\n" \
"v_mov_b32 v22, 0\n" \
"v_mfma_f32_16x16x32_bf16 a[60:63], a[156:159], v[140:143], a[60:63]\n" \
"s_mov_b32 s94, 0x300003\n" \
"v_mov_b32 v23, 0\n" \
"v_mov_b32 v26, 0\n" \
"v_mov_b32 v27, 0\n" \
"v_mfma_f32_16x16x32_bf16 v[32:35], a[160:163], v[96:99], 0\n" \
"s_mov_b32 s95, 0x30000300\n" \
"v_cndmask_b32 v24, 0, v188, s[94:95]\n" \
"s_mov_b32 s94, 0xc0000c\n" \
"v_mfma_f32_16x16x32_bf16 v[32:35], a[168:171], v[100:103], v[32:35]\n" \
"s_waitcnt lgkmcnt(0)\n" \
"s_mov_b32 s95, 0xc0000c00\n" \
"v_cndmask_b32 v25, 0, v188, s[94:95]\n" \
"v_mfma_f32_16x16x32_bf16 v[36:39], a[164:167], v[96:99], 0\n" \
"s_waitcnt lgkmcnt(13)\n" \
"s_barrier\n" \
"ds_read2_b32 v[190:191], v185 offset1:64\n" \
"v_mfma_f32_16x16x32_bf16 v[36:39], a[172:175], v[100:103], v[36:39]\n" \
"buffer_load_dwordx4 v4, s[36:39], s80 offen offset:2048 sc1 lds\n" \
"v_mfma_f32_16x16x32_bf16 v[40:43], a[160:163], v[104:107], 0\n" \
"ds_read_b128 v[96:99], v29 offset:40960\n" \
"v_add_u32 v176, 0xfffe0000, v13\n" \
"v_mfma_f32_16x16x32_bf16 v[40:43], a[168:171], v[108:111], v[40:43]\n" \
"ds_read_b128 v[128:131], v187 offset:0\n" \
"v_add_u32 v177, 0xfffe0000, v14\n" \
"v_mfma_f32_16x16x32_bf16 v[44:47], a[164:167], v[104:107], 0\n" \
"s_waitcnt lgkmcnt(14)\n" \
"ds_read_b128 v[100:103], v29 offset:43008\n" \
"v_add_u32 v178, 0xfffe0000, v15\n" \
"v_mfma_f32_16x16x32_bf16 v[44:47], a[172:175], v[108:111], v[44:47]\n" \
"buffer_load_dwordx4 v4, s[36:39], s80 offen offset:3072 sc1 lds\n" \
"v_mfma_f32_16x16x32_bf16 v[48:51], a[160:163], v[112:115], 0\n" \
"s_mov_b32 s81, 917504\n" \
"ds_read_b128 v[104:107], v29 offset:41984\n" \
"s_add_u32 m0, s79, 81920\n" \
"v_mfma_f32_16x16x32_bf16 v[48:51], a[168:171], v[116:119], v[48:51]\n" \
"ds_read_b128 v[132:135], v187 offset:1024\n" \
"v_add_u32 v179, 0xfffe0000, v16\n" \
"v_mfma_f32_16x16x32_bf16 v[52:55], a[164:167], v[112:115], 0\n" \
"s_waitcnt lgkmcnt(15)\n" \
"ds_read_b128 v[108:111], v29 offset:44032\n" \
"s_waitcnt vmcnt(8)\n" \
"v_mfma_f32_16x16x32_bf16 v[52:55], a[172:175], v[116:119], v[52:55]\n" \
"buffer_load_dwordx4 v12, s[28:31], s81 offen offset:0 lds\n" \
"v_mfma_f32_16x16x32_bf16 v[56:59], a[160:163], v[120:123], 0\n" \
"s_add_u32 m0, s79, 82816\n" \
"ds_read_b128 v[112:115], v29 offset:45056\n" \
"v_add_u32 v180, 0xffff0000, v13\n" \
"v_mfma_f32_16x16x32_bf16 v[56:59], a[168:171], v[124:127], v[56:59]\n" \
"ds_read_b128 v[136:139], v187 offset:2048\n" \
"v_add_u32 v181, 0xffff0000, v14\n" \
"v_mfma_f32_16x16x32_bf16 v[60:63], a[164:167], v[120:123], 0\n" \
"ds_read_b128 v[116:119], v29 offset:47104\n" \
"v_add_u32 v182, 0xffff0000, v15\n" \
"v_mfma_f32_16x16x32_bf16 v[60:63], a[172:175], v[124:127], v[60:63]\n" \
"s_waitcnt vmcnt(17) lgkmcnt(6)\n" \
"s_barrier\n" \
"ds_read_b128 v[140:143], v187 offset:3072\n" \
"v_mfma_f32_16x16x32_bf16 v[64:67], a[160:163], v[96:99], 0\n" \
"ds_read_b128 v[120:123], v29 offset:46080\n" \
"v_add_u32 v183, 0xffff0000, v16\n" \
"v_mfma_f32_16x16x32_bf16 v[64:67], a[168:171], v[100:103], v[64:67]\n" \
"buffer_load_dwordx4 v12, s[28:31], s81 offen offset:128 lds\n" \
"v_mfma_f32_16x16x32_bf16 v[68:71], a[164:167], v[96:99], 0\n" \
"s_waitcnt lgkmcnt(5)\n" \
"ds_read_b128 v[124:127], v29 offset:48128\n" \
"s_mov_b32 s81, 983040\n" \
"v_mfma_f32_16x16x32_bf16 v[68:71], a[172:175], v[100:103], v[68:71]\n" \
"s_add_u32 m0, s79, 90112\n" \
"ds_read_b128 v[144:147], v187 offset:4096\n" \
"s_waitcnt lgkmcnt(13)\n" \
"v_mfma_f32_16x16x32_bf16 v[72:75], a[160:163], v[104:107], 0\n" \
"ds_read_b128 v[96:99], v29 offset:49152\n" \
"v_add_f32 v190, v168, v190\n" \
"v_mfma_f32_16x16x32_bf16 v[72:75], a[168:171], v[108:111], v[72:75]\n" \
"buffer_load_dwordx4 v12, s[28:31], s81 offen offset:0 lds\n" \
"v_mfma_f32_16x16x32_bf16 v[76:79], a[164:167], v[104:107], 0\n" \
"s_add_u32 m0, s79, 91008\n" \
"ds_read_b128 v[100:103], v29 offset:51200\n" \
"v_add_f32 v191, v172, v191\n" \
"v_accvgpr_read_b32 v192, a0\n" \
"v_accvgpr_read_b32 v193, a1\n" \
"v_accvgpr_read_b32 v194, a2\n" \
"v_accvgpr_read_b32 v195, a3\n" \
"v_accvgpr_read_b32 v196, a4\n" \
"v_accvgpr_read_b32 v197, a5\n" \
"v_accvgpr_read_b32 v198, a6\n" \
"v_accvgpr_read_b32 v199, a7\n" \
"v_mfma_f32_16x16x32_bf16 v[76:79], a[172:175], v[108:111], v[76:79]\n" \
"s_waitcnt lgkmcnt(13)\n" \
"ds_read_b128 v[148:151], v187 offset:5120\n" \
"v_subrev_u32 v188, 0x1f800000, v190\n" \
"v_mfma_f32_16x16x32_bf16 v[192:195], v[24:27], v[128:131], v[192:195]\n" \
"s_waitcnt lgkmcnt(7)\n" \
"ds_read_b128 v[152:155], v187 offset:6144\n" \
"v_subrev_u32 v189, 0x1f800000, v191\n" \
"v_mfma_f32_16x16x32_bf16 v[196:199], v[22:25], v[128:131], v[196:199]\n" \
"buffer_load_dwordx4 v12, s[28:31], s81 offen offset:128 lds\n" \
"v_mfma_f32_16x16x32_bf16 v[32:35], a[176:179], v[112:115], v[32:35]\n" \
"s_add_u32 s80, s66, 16384\n" \
"ds_read_b128 v[104:107], v29 offset:50176\n" \
"s_add_u32 m0, s22, 65536\n" \
"v_mfma_f32_16x16x32_bf16 v[32:35], a[184:187], v[116:119], v[32:35]\n" \
"ds_read_b128 v[156:159], v187 offset:7168\n" \
"v_max_u32 v188, v188, v189\n" \
"v_mfma_f32_16x16x32_bf16 v[36:39], a[180:183], v[112:115], v[36:39]\n" \
"s_waitcnt lgkmcnt(7)\n" \
"ds_read_b128 v[108:111], v29 offset:52224\n" \
"v_cmp_le_u32 vcc, 0x40000000, v188\n" \
"v_mfma_f32_16x16x32_bf16 v[36:39], a[188:191], v[116:119], v[36:39]\n" \
"buffer_load_dwordx4 v4, s[36:39], s80 offen offset:0 sc1 lds\n" \
"v_mfma_f32_16x16x32_bf16 v[40:43], a[176:179], v[120:123], v[40:43]\n" \
"s_or_b64 s[4:5], s[4:5], vcc\n" \
"ds_read_b128 v[112:115], v29 offset:53248\n" \
"s_nop 0\n" \
"v_mfma_f32_16x16x32_bf16 v[40:43], a[184:187], v[124:127], v[40:43]\n" \
"v_rcp_f32 v186, v190\n" \
"v_mfma_f32_16x16x32_bf16 v[44:47], a[180:183], v[120:123], v[44:47]\n" \
"ds_read_b128 v[116:119], v29 offset:55296\n" \
"v_accvgpr_read_b32 v200, a8\n" \
"v_mfma_f32_16x16x32_bf16 v[44:47], a[188:191], v[124:127], v[44:47]\n" \
"s_waitcnt vmcnt(9) lgkmcnt(3)\n" \
"s_barrier\n" \
"v_accvgpr_read_b32 v201, a9\n" \
"v_mfma_f32_16x16x32_bf16 v[48:51], a[176:179], v[96:99], v[48:51]\n" \
"ds_read_b128 v[120:123], v29 offset:54272\n" \
"v_accvgpr_read_b32 v202, a10\n" \
"v_mfma_f32_16x16x32_bf16 v[48:51], a[184:187], v[100:103], v[48:51]\n" \
"buffer_load_dwordx4 v4, s[36:39], s80 offen offset:1024 sc1 lds\n" \
"v_mfma_f32_16x16x32_bf16 v[52:55], a[180:183], v[96:99], v[52:55]\n" \
"ds_read_b128 v[124:127], v29 offset:56320\n" \
"v_accvgpr_read_b32 v203, a11\n" \
"v_accvgpr_read_b32 v204, a12\n" \
"v_accvgpr_read_b32 v205, a13\n" \
"v_accvgpr_read_b32 v206, a14\n" \
"v_accvgpr_read_b32 v207, a15\n" \
"v_mfma_f32_16x16x32_bf16 v[52:55], a[188:191], v[100:103], v[52:55]\n" \
"s_waitcnt lgkmcnt(15)\n" \
"ds_read_b64_tr_b16 a[64:65], v176 offset:32768\n" \
"v_accvgpr_read_b32 v208, a16\n" \
"v_mfma_f32_16x16x32_bf16 v[200:203], v[24:27], v[132:135], v[200:203]\n" \
"s_waitcnt lgkmcnt(5)\n" \
"ds_read_b64_tr_b16 a[66:67], v177 offset:32768\n" \
"v_accvgpr_read_b32 v209, a17\n" \
"v_mfma_f32_16x16x32_bf16 v[204:207], v[22:25], v[132:135], v[204:207]\n" \
"buffer_load_dwordx4 v4, s[36:39], s80 offen offset:2048 sc1 lds\n" \
"v_mfma_f32_16x16x32_bf16 v[56:59], a[176:179], v[104:107], v[56:59]\n" \
"ds_read_b128 v[96:99], v29 offset:57344\n" \
"v_accvgpr_read_b32 v210, a18\n" \
"v_mfma_f32_16x16x32_bf16 v[56:59], a[184:187], v[108:111], v[56:59]\n" \
"ds_read_b64_tr_b16 a[68:69], v178 offset:32768\n" \
"v_accvgpr_read_b32 v211, a19\n" \
"v_mfma_f32_16x16x32_bf16 v[60:63], a[180:183], v[104:107], v[60:63]\n" \
"s_waitcnt lgkmcnt(6)\n" \
"ds_read_b128 v[100:103], v29 offset:59392\n" \
"v_accvgpr_read_b32 v212, a20\n" \
"v_mfma_f32_16x16x32_bf16 v[60:63], a[188:191], v[108:111], v[60:63]\n" \
"buffer_load_dwordx4 v4, s[36:39], s80 offen offset:3072 sc1 lds\n" \
"v_mfma_f32_16x16x32_bf16 v[64:67], a[176:179], v[112:115], v[64:67]\n" \
"s_mov_b32 s81, 1048576\n" \
"ds_read_b128 v[104:107], v29 offset:58368\n" \
"s_add_u32 m0, s79, 98304\n" \
"v_mfma_f32_16x16x32_bf16 v[64:67], a[184:187], v[116:119], v[64:67]\n" \
"ds_read_b64_tr_b16 a[70:71], v179 offset:32768\n" \
"v_accvgpr_read_b32 v213, a21\n" \
"v_mfma_f32_16x16x32_bf16 v[68:71], a[180:183], v[112:115], v[68:71]\n" \
"s_waitcnt lgkmcnt(7)\n" \
"ds_read_b128 v[108:111], v29 offset:60416\n" \
"v_accvgpr_read_b32 v214, a22\n" \
"v_mfma_f32_16x16x32_bf16 v[68:71], a[188:191], v[116:119], v[68:71]\n" \
"buffer_load_dwordx4 v12, s[28:31], s81 offen offset:0 lds\n" \
"v_mfma_f32_16x16x32_bf16 v[72:75], a[176:179], v[120:123], v[72:75]\n" \
"s_add_u32 m0, s79, 99200\n" \
"ds_read_b128 v[112:115], v29 offset:61440\n" \
"v_accvgpr_read_b32 v215, a23\n" \
"v_mfma_f32_16x16x32_bf16 v[72:75], a[184:187], v[124:127], v[72:75]\n" \
"ds_read_b64_tr_b16 a[72:73], v176 offset:40960\n" \
"v_accvgpr_read_b32 v216, a24\n" \
"v_mfma_f32_16x16x32_bf16 v[76:79], a[180:183], v[120:123], v[76:79]\n" \
"ds_read_b128 v[116:119], v29 offset:63488\n" \
"v_accvgpr_read_b32 v217, a25\n" \
"v_mfma_f32_16x16x32_bf16 v[76:79], a[188:191], v[124:127], v[76:79]\n" \
"s_waitcnt lgkmcnt(15)\n" \
"ds_read_b64_tr_b16 a[74:75], v177 offset:40960\n" \
"v_accvgpr_read_b32 v218, a26\n" \
"v_mfma_f32_16x16x32_bf16 v[208:211], v[24:27], v[136:139], v[208:211]\n" \
"buffer_load_dwordx4 v12, s[28:31], s81 offen offset:128 lds\n" \
"v_mfma_f32_16x16x32_bf16 v[212:215], v[22:25], v[136:139], v[212:215]\n" \
"s_waitcnt vmcnt(22) lgkmcnt(7)\n" \
"s_barrier\n" \
"s_mov_b32 s81, 1114112\n" \
"v_mfma_f32_16x16x32_bf16 v[32:35], a[192:195], v[96:99], v[32:35]\n" \
"s_add_u32 m0, s79, 106496\n" \
"ds_read_b128 v[120:123], v29 offset:62464\n" \
"v_accvgpr_read_b32 v219, a27\n" \
"v_mfma_f32_16x16x32_bf16 v[32:35], a[200:203], v[100:103], v[32:35]\n" \
"ds_read_b64_tr_b16 a[76:77], v178 offset:40960\n" \
"v_accvgpr_read_b32 v220, a28\n" \
"v_mfma_f32_16x16x32_bf16 v[36:39], a[196:199], v[96:99], v[36:39]\n" \
"s_waitcnt lgkmcnt(6)\n" \
"ds_read_b128 v[124:127], v29 offset:64512\n" \
"v_accvgpr_read_b32 v221, a29\n" \
"v_mfma_f32_16x16x32_bf16 v[36:39], a[204:207], v[100:103], v[36:39]\n" \
"buffer_load_dwordx4 v12, s[28:31], s81 offen offset:0 lds\n" \
"v_mfma_f32_16x16x32_bf16 v[40:43], a[192:195], v[104:107], v[40:43]\n" \
"s_add_u32 m0, s79, 107392\n" \
"ds_read_b128 v[96:99], v28 offset:0\n" \
"v_accvgpr_read_b32 v222, a30\n" \
"v_mfma_f32_16x16x32_bf16 v[40:43], a[200:203], v[108:111], v[40:43]\n" \
"ds_read_b64_tr_b16 a[78:79], v179 offset:40960\n" \
"v_accvgpr_read_b32 v223, a31\n" \
"v_mfma_f32_16x16x32_bf16 v[44:47], a[196:199], v[104:107], v[44:47]\n" \
"s_waitcnt lgkmcnt(6)\n" \
"ds_read_b128 v[100:103], v28 offset:2048\n" \
"v_accvgpr_read_b32 v224, a32\n" \
"v_mfma_f32_16x16x32_bf16 v[44:47], a[204:207], v[108:111], v[44:47]\n" \
"buffer_load_dwordx4 v12, s[28:31], s81 offen offset:128 lds\n" \
"v_mfma_f32_16x16x32_bf16 v[48:51], a[192:195], v[112:115], v[48:51]\n" \
"s_add_u32 s80, s64, 32768\n" \
"ds_read_b128 v[104:107], v28 offset:1024\n" \
"s_add_u32 m0, s22, 131072\n" \
"v_mfma_f32_16x16x32_bf16 v[48:51], a[200:203], v[116:119], v[48:51]\n" \
"v_rcp_f32 v187, v191\n" \
"v_mfma_f32_16x16x32_bf16 v[52:55], a[196:199], v[112:115], v[52:55]\n" \
"ds_read_b128 v[108:111], v28 offset:3072\n" \
"v_bfe_u32 v188, v4, 7, 2\n" \
"v_mfma_f32_16x16x32_bf16 v[52:55], a[204:207], v[116:119], v[52:55]\n" \
"s_waitcnt lgkmcnt(15)\n" \
"s_cmp_lg_u64 s[54:55], 0\ns_cbranch_scc1 LFBC_%=\nLFBCR_%=:\n" \
"v_mfma_f32_16x16x32_bf16 v[216:219], v[24:27], v[140:143], v[216:219]\n" \
"s_waitcnt lgkmcnt(5)\n" \
"v_min_u32 v188, 0, v188\n" \
"v_lshl_add_u32 v17, v188, 8, s10\n" \
"v_mfma_f32_16x16x32_bf16 v[220:223], v[22:25], v[140:143], v[220:223]\n" \
"buffer_store_dwordx2 v[10:11], v9, s[36:39], s52 offen sc1\n" \
"v_mfma_f32_16x16x32_bf16 v[56:59], a[192:195], v[120:123], v[56:59]\n" \
"ds_read_b128 v[112:115], v28 offset:4096\n" \
"v_bfe_u32 v188, v4, 4, 3\n" \
"v_mfma_f32_16x16x32_bf16 v[56:59], a[200:203], v[124:127], v[56:59]\n" \
"buffer_load_dwordx4 v4, s[36:39], s80 offen offset:0 sc1 lds\n" \
"v_mfma_f32_16x16x32_bf16 v[60:63], a[196:199], v[120:123], v[60:63]\n" \
"s_andn2_b32 s94, s88, 0xc0\n" \
"ds_read_b128 v[116:119], v28 offset:6144\n" \
"v_lshl_add_u32 v17, v188, 3, v17\n" \
"v_mfma_f32_16x16x32_bf16 v[60:63], a[204:207], v[124:127], v[60:63]\n" \
"s_waitcnt lgkmcnt(4)\n" \
"s_barrier\n" \
"v_and_b32 v188, 0x30, v4\n" \
"v_mfma_f32_16x16x32_bf16 v[64:67], a[192:195], v[96:99], v[64:67]\n" \
"ds_read_b128 v[120:123], v28 offset:5120\n" \
"v_mul_f32 v192, v186, v192\n" \
"v_mfma_f32_16x16x32_bf16 v[64:67], a[200:203], v[100:103], v[64:67]\n" \
"buffer_load_dwordx4 v4, s[36:39], s80 offen offset:1024 sc1 lds\n" \
"v_mfma_f32_16x16x32_bf16 v[68:71], a[196:199], v[96:99], v[68:71]\n" \
"s_waitcnt lgkmcnt(3)\n" \
"ds_read_b128 v[124:127], v28 offset:7168\n" \
"v_mul_f32 v193, v186, v193\n" \
"v_mfma_f32_16x16x32_bf16 v[68:71], a[204:207], v[100:103], v[68:71]\n" \
"s_mov_b32 exec_lo, 0\nv_lshl_add_u32 v17, v188, 2, s94\ns_mov_b32 exec_lo, -1\n" \
"v_mfma_f32_16x16x32_bf16 v[72:75], a[192:195], v[104:107], v[72:75]\n" \
"s_sub_u32 s96, s72, 109051904\n" \
"ds_read_b128 v[96:99], v28 offset:8192\n" \
"s_lshr_b32 s96, s96, 11\n" \
"v_mfma_f32_16x16x32_bf16 v[72:75], a[200:203], v[108:111], v[72:75]\n" \
"buffer_load_dwordx4 v4, s[36:39], s80 offen offset:2048 sc1 lds\n" \
"v_mfma_f32_16x16x32_bf16 v[76:79], a[196:199], v[104:107], v[76:79]\n" \
"s_mul_i32 s97, s96, 10923\n" \
"ds_read_b128 v[100:103], v28 offset:10240\n" \
"s_lshr_b32 s97, s97, 16\n" \
"v_accvgpr_read_b32 v225, a33\n" \
"v_accvgpr_read_b32 v226, a34\n" \
"v_accvgpr_read_b32 v227, a35\n" \
"v_accvgpr_read_b32 v228, a36\n" \
"v_accvgpr_read_b32 v229, a37\n" \
"v_accvgpr_read_b32 v230, a38\n" \
"v_accvgpr_read_b32 v231, a39\n" \
"v_mfma_f32_16x16x32_bf16 v[76:79], a[204:207], v[108:111], v[76:79]\n" \
"s_waitcnt lgkmcnt(15)\n" \
"s_lshl_b32 s97, s97, 1\n" \
"s_sub_u32 s96, s96, s97\n" \
"v_mfma_f32_16x16x32_bf16 v[224:227], v[24:27], v[144:147], v[224:227]\n" \
"s_waitcnt lgkmcnt(4)\n" \
"s_lshl_b32 s96, s96, 3\n" \
"s_add_u32 s23, s96, 94437376\n" \
"v_mfma_f32_16x16x32_bf16 v[228:231], v[22:25], v[144:147], v[228:231]\n" \
"buffer_load_dwordx4 v4, s[36:39], s80 offen offset:3072 sc1 lds\n" \
"v_mfma_f32_16x16x32_bf16 v[32:35], a[208:211], v[112:115], v[32:35]\n" \
"s_mov_b32 s81, 1179648\n" \
"ds_read_b128 v[104:107], v28 offset:9216\n" \
"s_add_u32 m0, s79, 147456\n" \
"v_mfma_f32_16x16x32_bf16 v[32:35], a[216:219], v[116:119], v[32:35]\n" \
"s_sub_u32 s96, s73, 109051904\n" \
"s_lshr_b32 s96, s96, 11\n" \
"s_mul_i32 s97, s96, 10923\n" \
"v_mfma_f32_16x16x32_bf16 v[36:39], a[212:215], v[112:115], v[36:39]\n" \
"s_waitcnt lgkmcnt(3)\n" \
"ds_read_b128 v[108:111], v28 offset:11264\n" \
"s_lshr_b32 s97, s97, 16\n" \
"v_mfma_f32_16x16x32_bf16 v[36:39], a[220:223], v[116:119], v[36:39]\n" \
"buffer_load_dwordx4 v12, s[28:31], s81 offen offset:0 lds\n" \
"v_mfma_f32_16x16x32_bf16 v[40:43], a[208:211], v[120:123], v[40:43]\n" \
"s_add_u32 m0, s79, 148352\n" \
"ds_read_b128 v[112:115], v28 offset:12288\n" \
"s_lshl_b32 s97, s97, 1\n" \
"v_mfma_f32_16x16x32_bf16 v[40:43], a[216:219], v[124:127], v[40:43]\n" \
"s_sub_u32 s96, s96, s97\n" \
"s_lshl_b32 s96, s96, 3\n" \
"s_add_u32 s24, s96, 94437376\n" \
"v_mfma_f32_16x16x32_bf16 v[44:47], a[212:215], v[120:123], v[44:47]\n" \
"s_sub_u32 s96, s74, 109051904\n" \
"ds_read_b128 v[116:119], v28 offset:14336\n" \
"s_lshr_b32 s96, s96, 11\n" \
"v_mfma_f32_16x16x32_bf16 v[44:47], a[220:223], v[124:127], v[44:47]\n" \
"s_waitcnt vmcnt(14) lgkmcnt(4)\n" \
"s_barrier\n" \
"s_mul_i32 s97, s96, 10923\n" \
"v_mfma_f32_16x16x32_bf16 v[48:51], a[208:211], v[96:99], v[48:51]\n" \
"s_lshr_b32 s97, s97, 16\n" \
"ds_read_b128 v[120:123], v28 offset:13312\n" \
"s_lshl_b32 s97, s97, 1\n" \
"v_mfma_f32_16x16x32_bf16 v[48:51], a[216:219], v[100:103], v[48:51]\n" \
"buffer_load_dwordx4 v12, s[28:31], s81 offen offset:128 lds\n" \
"v_mfma_f32_16x16x32_bf16 v[52:55], a[212:215], v[96:99], v[52:55]\n" \
"s_mov_b32 s81, 1245184\n" \
"ds_read_b128 v[124:127], v28 offset:15360\n" \
"s_add_u32 m0, s79, 155648\n" \
"v_accvgpr_read_b32 v232, a40\n" \
"v_accvgpr_read_b32 v233, a41\n" \
"v_accvgpr_read_b32 v234, a42\n" \
"v_accvgpr_read_b32 v235, a43\n" \
"v_accvgpr_read_b32 v236, a44\n" \
"v_accvgpr_read_b32 v237, a45\n" \
"v_accvgpr_read_b32 v238, a46\n" \
"v_accvgpr_read_b32 v239, a47\n" \
"v_mfma_f32_16x16x32_bf16 v[52:55], a[220:223], v[100:103], v[52:55]\n" \
"s_waitcnt lgkmcnt(15)\n" \
"ds_read_b64_tr_b16 a[80:81], v180 offset:16384\n" \
"s_sub_u32 s96, s96, s97\n" \
"v_mfma_f32_16x16x32_bf16 v[232:235], v[24:27], v[148:151], v[232:235]\n" \
"s_waitcnt lgkmcnt(5)\n" \
"ds_read_b64_tr_b16 a[82:83], v181 offset:16384\n" \
"s_lshl_b32 s96, s96, 3\n" \
"v_mfma_f32_16x16x32_bf16 v[236:239], v[22:25], v[148:151], v[236:239]\n" \
"buffer_load_dwordx4 v12, s[28:31], s81 offen offset:0 lds\n" \
"v_mfma_f32_16x16x32_bf16 v[56:59], a[208:211], v[104:107], v[56:59]\n" \
"s_add_u32 m0, s79, 156544\n" \
"ds_read_b128 v[96:99], v28 offset:16384\n" \
"s_add_u32 s45, s96, 94437376\n" \
"v_mfma_f32_16x16x32_bf16 v[56:59], a[216:219], v[108:111], v[56:59]\n" \
"ds_read_b64_tr_b16 a[84:85], v182 offset:16384\n" \
"v_mul_f32 v194, v186, v194\n" \
"v_mfma_f32_16x16x32_bf16 v[60:63], a[212:215], v[104:107], v[60:63]\n" \
"s_waitcnt lgkmcnt(6)\n" \
"ds_read_b128 v[100:103], v28 offset:18432\n" \
"v_mul_f32 v195, v186, v195\n" \
"v_mfma_f32_16x16x32_bf16 v[60:63], a[220:223], v[108:111], v[60:63]\n" \
"buffer_load_dwordx2 v[18:19], v17, s[36:39], 0 offen sc1\n" \
"v_mfma_f32_16x16x32_bf16 v[64:67], a[208:211], v[112:115], v[64:67]\n" \
"ds_read_b128 v[104:107], v28 offset:17408\n" \
"v_mul_f32 v196, v186, v196\n" \
"v_mfma_f32_16x16x32_bf16 v[64:67], a[216:219], v[116:119], v[64:67]\n" \
"ds_read_b64_tr_b16 a[86:87], v183 offset:16384\n" \
"v_mul_f32 v197, v186, v197\n" \
"v_mfma_f32_16x16x32_bf16 v[68:71], a[212:215], v[112:115], v[68:71]\n" \
"s_waitcnt lgkmcnt(7)\n" \
"ds_read_b128 v[108:111], v28 offset:19456\n" \
"v_mul_f32 v198, v186, v198\n" \
"v_mfma_f32_16x16x32_bf16 v[68:71], a[220:223], v[116:119], v[68:71]\n" \
"buffer_load_dwordx4 v12, s[28:31], s81 offen offset:128 lds\n" \
"v_mfma_f32_16x16x32_bf16 v[72:75], a[208:211], v[120:123], v[72:75]\n" \
"s_add_u32 s80, s65, 32768\n" \
"ds_read_b128 v[112:115], v28 offset:20480\n" \
"s_add_u32 m0, s22, 32768\n" \
"v_mfma_f32_16x16x32_bf16 v[72:75], a[216:219], v[124:127], v[72:75]\n" \
"ds_read_b64_tr_b16 a[88:89], v180 offset:24576\n" \
"v_mul_f32 v199, v186, v199\n" \
"v_mfma_f32_16x16x32_bf16 v[76:79], a[212:215], v[120:123], v[76:79]\n" \
"ds_read_b128 v[116:119], v28 offset:22528\n" \
"v_cvt_pk_bf16_f32 v192, v192, v193\n" \
"v_accvgpr_read_b32 v240, a48\n" \
"v_accvgpr_read_b32 v241, a49\n" \
"v_accvgpr_read_b32 v242, a50\n" \
"v_accvgpr_read_b32 v243, a51\n" \
"v_accvgpr_read_b32 v244, a52\n" \
"v_accvgpr_read_b32 v245, a53\n" \
"v_accvgpr_read_b32 v246, a54\n" \
"v_accvgpr_read_b32 v247, a55\n" \
"v_mfma_f32_16x16x32_bf16 v[76:79], a[220:223], v[124:127], v[76:79]\n" \
"s_waitcnt lgkmcnt(15)\n" \
"ds_read_b64_tr_b16 a[90:91], v181 offset:24576\n" \
"v_cvt_pk_bf16_f32 v193, v194, v195\n" \
"v_mfma_f32_16x16x32_bf16 v[240:243], v[24:27], v[152:155], v[240:243]\n" \
"buffer_load_dwordx4 v4, s[36:39], s80 offen offset:0 sc1 lds\n" \
"v_mfma_f32_16x16x32_bf16 v[244:247], v[22:25], v[152:155], v[244:247]\n" \
"s_waitcnt lgkmcnt(7)\n" \
"s_barrier\n" \
"ds_read_b64_tr_b16 a[92:93], v182 offset:24576\n" \
"v_mfma_f32_16x16x32_bf16 v[32:35], a[224:227], v[96:99], v[32:35]\n" \
"LH24_%=:\ns_cbranch_execz LH28_%=\n" \
"ds_read_b128 v[120:123], v28 offset:21504\n" \
"v_cvt_pk_bf16_f32 v194, v196, v197\n" \
"v_mfma_f32_16x16x32_bf16 v[32:35], a[232:235], v[100:103], v[32:35]\n" \
"ds_read_b64_tr_b16 a[94:95], v183 offset:24576\n" \
"v_cvt_pk_bf16_f32 v195, v198, v199\n" \
"v_mfma_f32_16x16x32_bf16 v[36:39], a[228:231], v[96:99], v[36:39]\n" \
"s_waitcnt lgkmcnt(7)\n" \
"ds_read_b128 v[124:127], v28 offset:23552\n" \
"v_mul_f32 v200, v186, v200\n" \
"v_mfma_f32_16x16x32_bf16 v[36:39], a[236:239], v[100:103], v[36:39]\n" \
"buffer_store_dwordx4 v[192:195], v4, s[36:39], s51 offen offset:0 sc1\n" \
"v_mfma_f32_16x16x32_bf16 v[40:43], a[224:227], v[104:107], v[40:43]\n" \
"ds_read_b128 v[96:99], v28 offset:24576\n" \
"v_mul_f32 v201, v186, v201\n" \
"v_mfma_f32_16x16x32_bf16 v[40:43], a[232:235], v[108:111], v[40:43]\n" \
"v_mul_f32 v202, v186, v202\n" \
"v_mul_f32 v203, v186, v203\n" \
"v_mfma_f32_16x16x32_bf16 v[44:47], a[228:231], v[104:107], v[44:47]\n" \
"s_waitcnt lgkmcnt(6)\n" \
"ds_read_b128 v[100:103], v28 offset:26624\n" \
"v_mul_f32 v204, v186, v204\n" \
"v_mfma_f32_16x16x32_bf16 v[44:47], a[236:239], v[108:111], v[44:47]\n" \
"buffer_load_dwordx4 v4, s[36:39], s80 offen offset:1024 sc1 lds\n" \
"v_mfma_f32_16x16x32_bf16 v[48:51], a[224:227], v[112:115], v[48:51]\n" \
"ds_read_b128 v[104:107], v28 offset:25600\n" \
"v_mul_f32 v205, v186, v205\n" \
"v_mfma_f32_16x16x32_bf16 v[48:51], a[232:235], v[116:119], v[48:51]\n" \
"v_mul_f32 v206, v186, v206\n" \
"v_mul_f32 v207, v186, v207\n" \
"v_mfma_f32_16x16x32_bf16 v[52:55], a[228:231], v[112:115], v[52:55]\n" \
"ds_read_b128 v[108:111], v28 offset:27648\n" \
"v_cvt_pk_bf16_f32 v200, v200, v201\n" \
"v_accvgpr_read_b32 v248, a56\n" \
"v_accvgpr_read_b32 v249, a57\n" \
"v_accvgpr_read_b32 v250, a58\n" \
"v_accvgpr_read_b32 v251, a59\n" \
"v_accvgpr_read_b32 v252, a60\n" \
"v_accvgpr_read_b32 v253, a61\n" \
"v_accvgpr_read_b32 v254, a62\n" \
"v_accvgpr_read_b32 v255, a63\n" \
"v_mfma_f32_16x16x32_bf16 v[52:55], a[236:239], v[116:119], v[52:55]\n" \
"s_waitcnt lgkmcnt(15)\n" \
"v_cvt_pk_bf16_f32 v201, v202, v203\n" \
"v_mfma_f32_16x16x32_bf16 v[248:251], v[24:27], v[156:159], v[248:251]\n" \
"s_waitcnt lgkmcnt(4)\n" \
"v_cvt_pk_bf16_f32 v202, v204, v205\n" \
"v_cvt_pk_bf16_f32 v203, v206, v207\n" \
"v_mfma_f32_16x16x32_bf16 v[252:255], v[22:25], v[156:159], v[252:255]\n" \
"buffer_store_dwordx4 v[200:203], v4, s[36:39], s51 offen offset:2048 sc1\n" \
"v_mfma_f32_16x16x32_bf16 v[56:59], a[224:227], v[120:123], v[56:59]\n" \
"ds_read_b128 v[112:115], v28 offset:28672\n" \
"v_mul_f32 v208, v186, v208\n" \
"v_mfma_f32_16x16x32_bf16 v[56:59], a[232:235], v[124:127], v[56:59]\n" \
"v_mul_f32 v209, v186, v209\n" \
"v_mul_f32 v210, v186, v210\n" \
"v_mfma_f32_16x16x32_bf16 v[60:63], a[228:231], v[120:123], v[60:63]\n" \
"ds_read_b128 v[116:119], v28 offset:30720\n" \
"s_waitcnt vmcnt(5)\n" \
"v_mfma_f32_16x16x32_bf16 v[60:63], a[236:239], v[124:127], v[60:63]\n" \
"s_waitcnt vmcnt(14) lgkmcnt(4)\n" \
"s_barrier\n" \
"v_cmp_ne_u64 vcc, s[14:15], v[18:19]\n" \
"v_mfma_f32_16x16x32_bf16 v[64:67], a[224:227], v[96:99], v[64:67]\n" \
"ds_read_b128 v[120:123], v28 offset:29696\n" \
"s_nop 0\n" \
"v_mfma_f32_16x16x32_bf16 v[64:67], a[232:235], v[100:103], v[64:67]\n" \
"buffer_load_dwordx4 v4, s[36:39], s80 offen offset:2048 sc1 lds\n" \
"v_mfma_f32_16x16x32_bf16 v[68:71], a[228:231], v[96:99], v[68:71]\n" \
"s_waitcnt lgkmcnt(3)\n" \
"ds_read_b128 v[124:127], v28 offset:31744\n" \
"s_cbranch_vccnz LPS_%=\nLPSR_%=:\n" \
"v_mfma_f32_16x16x32_bf16 v[68:71], a[236:239], v[100:103], v[68:71]\n" \
"ds_read_b64_tr_b16 a[128:129], v180 offset:32768\n" \
"v_mul_f32 v211, v186, v211\n" \
"v_mfma_f32_16x16x32_bf16 v[72:75], a[224:227], v[104:107], v[72:75]\n" \
"ds_read_b128 v[96:99], v28 offset:49152\n" \
"v_mul_f32 v212, v186, v212\n" \
"v_mfma_f32_16x16x32_bf16 v[72:75], a[232:235], v[108:111], v[72:75]\n" \
"s_mov_b64 exec, 0xffff\nbuffer_load_dword v1, v2, s[36:39], s67 offen sc1\ns_mov_b64 exec, -1\n" \
"v_mfma_f32_16x16x32_bf16 v[76:79], a[228:231], v[104:107], v[76:79]\n" \
"s_waitcnt lgkmcnt(4)\n" \
"ds_read_b128 v[100:103], v28 offset:51200\n" \
"v_mul_f32 v213, v186, v213\n" \
"v_mfma_f32_16x16x32_bf16 v[76:79], a[236:239], v[108:111], v[76:79]\n" \
"ds_read_b64_tr_b16 a[130:131], v181 offset:32768\n" \
"v_mul_f32 v214, v186, v214\n" \
"v_mfma_f32_16x16x32_bf16 v[32:35], a[240:243], v[112:115], v[32:35]\n" \
"ds_read_b128 v[104:107], v28 offset:50176\n" \
"v_mul_f32 v215, v186, v215\n" \
"v_mfma_f32_16x16x32_bf16 v[32:35], a[248:251], v[116:119], v[32:35]\n" \
"buffer_load_dwordx4 v4, s[36:39], s80 offen offset:3072 sc1 lds\n" \
"v_mfma_f32_16x16x32_bf16 v[36:39], a[244:247], v[112:115], v[36:39]\n" \
"s_waitcnt lgkmcnt(5)\n" \
"ds_read_b128 v[108:111], v28 offset:52224\n" \
"s_add_u32 s80, s67, 0\n" \
"v_mfma_f32_16x16x32_bf16 v[36:39], a[252:255], v[116:119], v[36:39]\n" \
"s_add_u32 m0, s22, 114688\n" \
"ds_read_b64_tr_b16 a[132:133], v182 offset:32768\n" \
"v_cvt_pk_bf16_f32 v208, v208, v209\n" \
"v_mfma_f32_16x16x32_bf16 v[40:43], a[240:243], v[120:123], v[40:43]\n" \
"ds_read_b128 v[112:115], v28 offset:53248\n" \
"v_cvt_pk_bf16_f32 v209, v210, v211\n" \
"v_mfma_f32_16x16x32_bf16 v[40:43], a[248:251], v[124:127], v[40:43]\n" \
"buffer_load_dwordx4 v31, s[36:39], s80 offen offset:0 sc1 lds\n" \
"v_mfma_f32_16x16x32_bf16 v[44:47], a[244:247], v[120:123], v[44:47]\n" \
"ds_read_b128 v[116:119], v28 offset:55296\n" \
"v_cvt_pk_bf16_f32 v210, v212, v213\n" \
"v_mfma_f32_16x16x32_bf16 v[44:47], a[252:255], v[124:127], v[44:47]\n" \
"s_waitcnt lgkmcnt(6)\n" \
"s_barrier\n" \
"ds_read_b64_tr_b16 a[134:135], v183 offset:32768\n" \
"v_mfma_f32_16x16x32_bf16 v[48:51], a[240:243], v[96:99], v[48:51]\n" \
"ds_read_b128 v[120:123], v28 offset:54272\n" \
"v_cvt_pk_bf16_f32 v211, v214, v215\n" \
"v_mfma_f32_16x16x32_bf16 v[48:51], a[248:251], v[100:103], v[48:51]\n" \
"buffer_store_dwordx4 v[208:211], v7, s[36:39], s51 offen offset:0 sc1\n" \
"v_mfma_f32_16x16x32_bf16 v[52:55], a[244:247], v[96:99], v[52:55]\n" \
"s_waitcnt lgkmcnt(5)\n" \
"ds_read_b128 v[124:127], v28 offset:56320\n" \
"v_mul_f32 v216, v186, v216\n" \
"v_mfma_f32_16x16x32_bf16 v[52:55], a[252:255], v[100:103], v[52:55]\n" \
"ds_read_b64_tr_b16 a[136:137], v180 offset:40960\n" \
"v_mul_f32 v217, v186, v217\n" \
"v_mfma_f32_16x16x32_bf16 v[56:59], a[240:243], v[104:107], v[56:59]\n" \
"ds_read_b128 v[96:99], v28 offset:57344\n" \
"v_mul_f32 v218, v186, v218\n" \
"v_mfma_f32_16x16x32_bf16 v[56:59], a[248:251], v[108:111], v[56:59]\n" \
"ds_read_b64_tr_b16 a[138:139], v181 offset:40960\n" \
"v_mul_f32 v219, v186, v219\n" \
"v_mfma_f32_16x16x32_bf16 v[60:63], a[244:247], v[104:107], v[60:63]\n" \
"s_waitcnt lgkmcnt(6)\n" \
"ds_read_b128 v[100:103], v28 offset:59392\n" \
"v_mul_f32 v220, v186, v220\n" \
"v_mfma_f32_16x16x32_bf16 v[60:63], a[252:255], v[108:111], v[60:63]\n" \
"buffer_load_dwordx4 v31, s[36:39], s80 offen offset:1024 sc1 lds\n" \
"v_mfma_f32_16x16x32_bf16 v[64:67], a[240:243], v[112:115], v[64:67]\n" \
"ds_read_b128 v[104:107], v28 offset:58368\n" \
"v_mul_f32 v221, v186, v221\n" \
"v_mfma_f32_16x16x32_bf16 v[64:67], a[248:251], v[116:119], v[64:67]\n" \
"ds_read_b64_tr_b16 a[140:141], v182 offset:40960\n" \
"v_mul_f32 v222, v186, v222\n" \
"v_mfma_f32_16x16x32_bf16 v[68:71], a[244:247], v[112:115], v[68:71]\n" \
"s_waitcnt lgkmcnt(6)\n" \
"ds_read_b128 v[108:111], v28 offset:60416\n" \
"ds_read_b64_tr_b16 a[142:143], v183 offset:40960\n" \
"v_mfma_f32_16x16x32_bf16 v[68:71], a[252:255], v[116:119], v[68:71]\n" \
"buffer_load_dwordx4 v31, s[36:39], s80 offen offset:2048 sc1 lds\n" \
"v_mfma_f32_16x16x32_bf16 v[72:75], a[240:243], v[120:123], v[72:75]\n" \
"ds_read_b128 v[112:115], v28 offset:61440\n" \
"v_mul_f32 v223, v186, v223\n" \
"v_mfma_f32_16x16x32_bf16 v[72:75], a[248:251], v[124:127], v[72:75]\n" \
"v_cvt_pk_bf16_f32 v216, v216, v217\n" \
"v_cvt_pk_bf16_f32 v217, v218, v219\n" \
"v_mfma_f32_16x16x32_bf16 v[76:79], a[244:247], v[120:123], v[76:79]\n" \
"ds_read_b128 v[116:119], v28 offset:63488\n" \
"v_cvt_pk_bf16_f32 v218, v220, v221\n" \
"v_mfma_f32_16x16x32_bf16 v[76:79], a[252:255], v[124:127], v[76:79]\n" \
"s_waitcnt vmcnt(25) lgkmcnt(2)\n" \
"s_barrier\n" \
"v_cvt_pk_bf16_f32 v219, v222, v223\n" \
"v_mfma_f32_16x16x32_bf16 v[32:35], a[64:67], v[96:99], v[32:35]\n" \
"ds_read_b128 v[120:123], v28 offset:62464\n" \
"v_mul_f32 v224, v187, v224\n" \
"v_mfma_f32_16x16x32_bf16 v[32:35], a[72:75], v[100:103], v[32:35]\n" \
"buffer_store_dwordx4 v[216:219], v7, s[36:39], s51 offen offset:2048 sc1\n" \
"v_mfma_f32_16x16x32_bf16 v[36:39], a[68:71], v[96:99], v[36:39]\n" \
"s_waitcnt lgkmcnt(4)\n" \
"ds_read_b128 v[124:127], v28 offset:64512\n" \
"v_mul_f32 v225, v187, v225\n" \
"v_mfma_f32_16x16x32_bf16 v[36:39], a[76:79], v[100:103], v[36:39]\n" \
"v_mul_f32 v226, v187, v226\n" \
"v_mul_f32 v227, v187, v227\n" \
"v_mfma_f32_16x16x32_bf16 v[40:43], a[64:67], v[104:107], v[40:43]\n" \
"ds_read_b128 v[96:99], v29 offset:0\n" \
"v_mul_f32 v228, v187, v228\n" \
"v_mfma_f32_16x16x32_bf16 v[40:43], a[72:75], v[108:111], v[40:43]\n" \
"v_mul_f32 v229, v187, v229\n" \
"v_mul_f32 v230, v187, v230\n" \
"v_mfma_f32_16x16x32_bf16 v[44:47], a[68:71], v[104:107], v[44:47]\n" \
"s_waitcnt lgkmcnt(3)\n" \
"ds_read_b128 v[100:103], v29 offset:2048\n" \
"v_mul_f32 v231, v187, v231\n" \
"v_mfma_f32_16x16x32_bf16 v[44:47], a[76:79], v[108:111], v[44:47]\n" \
"buffer_load_dwordx4 v31, s[36:39], s80 offen offset:3072 sc1 lds\n" \
"v_mfma_f32_16x16x32_bf16 v[48:51], a[64:67], v[112:115], v[48:51]\n" \
"s_add_u32 s80, s67, 16384\n" \
"ds_read_b128 v[104:107], v29 offset:1024\n" \
"s_add_u32 m0, s22, 0\n" \
"v_mfma_f32_16x16x32_bf16 v[48:51], a[72:75], v[116:119], v[48:51]\n" \
"v_cvt_pk_bf16_f32 v224, v224, v225\n" \
"v_cvt_pk_bf16_f32 v225, v226, v227\n" \
"v_mfma_f32_16x16x32_bf16 v[52:55], a[68:71], v[112:115], v[52:55]\n" \
"s_waitcnt lgkmcnt(3)\n" \
"ds_read_b128 v[108:111], v29 offset:3072\n" \
"v_cvt_pk_bf16_f32 v226, v228, v229\n" \
"v_mfma_f32_16x16x32_bf16 v[52:55], a[76:79], v[116:119], v[52:55]\n" \
"buffer_load_dwordx4 v31, s[36:39], s80 offen offset:0 sc1 lds\n" \
"v_mfma_f32_16x16x32_bf16 v[56:59], a[64:67], v[120:123], v[56:59]\n" \
"ds_read_b128 v[112:115], v29 offset:4096\n" \
"v_cvt_pk_bf16_f32 v227, v230, v231\n" \
"v_mfma_f32_16x16x32_bf16 v[56:59], a[72:75], v[124:127], v[56:59]\n" \
"v_mul_f32 v232, v187, v232\n" \
"v_mul_f32 v233, v187, v233\n" \
"v_mfma_f32_16x16x32_bf16 v[60:63], a[68:71], v[120:123], v[60:63]\n" \
"ds_read_b128 v[116:119], v29 offset:6144\n" \
"v_mul_f32 v234, v187, v234\n" \
"v_mfma_f32_16x16x32_bf16 v[60:63], a[76:79], v[124:127], v[60:63]\n" \
"s_waitcnt vmcnt(14) lgkmcnt(4)\n" \
"s_barrier\n" \
"v_mul_f32 v235, v187, v235\n" \
"v_mfma_f32_16x16x32_bf16 v[64:67], a[64:67], v[96:99], v[64:67]\n" \
"ds_read_b128 v[120:123], v29 offset:5120\n" \
"v_mul_f32 v236, v187, v236\n" \
"v_mfma_f32_16x16x32_bf16 v[64:67], a[72:75], v[100:103], v[64:67]\n" \
"buffer_store_dwordx4 v[224:227], v4, s[36:39], s51 offen offset:1024 sc1\n" \
"v_mfma_f32_16x16x32_bf16 v[68:71], a[68:71], v[96:99], v[68:71]\n" \
"s_waitcnt lgkmcnt(3)\n" \
"ds_read_b128 v[124:127], v29 offset:7168\n" \
"v_mul_f32 v237, v187, v237\n" \
"v_mfma_f32_16x16x32_bf16 v[68:71], a[76:79], v[100:103], v[68:71]\n" \
"ds_read_b64_tr_b16 a[144:145], v13 offset:16384\n" \
"v_mul_f32 v238, v187, v238\n" \
"v_mfma_f32_16x16x32_bf16 v[72:75], a[64:67], v[104:107], v[72:75]\n" \
"ds_read_b128 v[96:99], v29 offset:8192\n" \
"v_mul_f32 v239, v187, v239\n" \
"v_mfma_f32_16x16x32_bf16 v[72:75], a[72:75], v[108:111], v[72:75]\n" \
"ds_read_b64_tr_b16 a[146:147], v14 offset:16384\n" \
"v_cvt_pk_bf16_f32 v232, v232, v233\n" \
"v_mfma_f32_16x16x32_bf16 v[76:79], a[68:71], v[104:107], v[76:79]\n" \
"s_waitcnt lgkmcnt(5)\n" \
"ds_read_b128 v[100:103], v29 offset:10240\n" \
"v_cvt_pk_bf16_f32 v233, v234, v235\n" \
"v_mfma_f32_16x16x32_bf16 v[76:79], a[76:79], v[108:111], v[76:79]\n" \
"buffer_load_dwordx4 v31, s[36:39], s80 offen offset:1024 sc1 lds\n" \
"v_mfma_f32_16x16x32_bf16 v[32:35], a[80:83], v[112:115], v[32:35]\n" \
"ds_read_b128 v[104:107], v29 offset:9216\n" \
"v_cvt_pk_bf16_f32 v234, v236, v237\n" \
"v_mfma_f32_16x16x32_bf16 v[32:35], a[88:91], v[116:119], v[32:35]\n" \
"ds_read_b64_tr_b16 a[148:149], v15 offset:16384\n" \
"v_cvt_pk_bf16_f32 v235, v238, v239\n" \
"v_mfma_f32_16x16x32_bf16 v[36:39], a[84:87], v[112:115], v[36:39]\n" \
"s_waitcnt lgkmcnt(6)\n" \
"ds_read_b128 v[108:111], v29 offset:11264\n" \
"v_mul_f32 v240, v187, v240\n" \
"v_mfma_f32_16x16x32_bf16 v[36:39], a[92:95], v[116:119], v[36:39]\n" \
"buffer_store_dwordx4 v[232:235], v4, s[36:39], s51 offen offset:3072 sc1\n" \
"v_mfma_f32_16x16x32_bf16 v[40:43], a[80:83], v[120:123], v[40:43]\n" \
"ds_read_b128 v[112:115], v29 offset:12288\n" \
"v_mul_f32 v241, v187, v241\n" \
"v_mfma_f32_16x16x32_bf16 v[40:43], a[88:91], v[124:127], v[40:43]\n" \
"ds_read_b64_tr_b16 a[150:151], v16 offset:16384\n" \
"v_mul_f32 v242, v187, v242\n" \
"v_mfma_f32_16x16x32_bf16 v[44:47], a[84:87], v[120:123], v[44:47]\n" \
"ds_read_b128 v[116:119], v29 offset:14336\n" \
"v_mul_f32 v243, v187, v243\n" \
"v_mfma_f32_16x16x32_bf16 v[44:47], a[92:95], v[124:127], v[44:47]\n" \
"s_waitcnt vmcnt(22) lgkmcnt(6)\n" \
"s_barrier\n" \
"ds_read_b64_tr_b16 a[152:153], v13 offset:24576\n" \
"v_mfma_f32_16x16x32_bf16 v[48:51], a[80:83], v[96:99], v[48:51]\n" \
"ds_read_b128 v[120:123], v29 offset:13312\n" \
"v_mul_f32 v244, v187, v244\n" \
"v_mfma_f32_16x16x32_bf16 v[48:51], a[88:91], v[100:103], v[48:51]\n" \
"ds_read_b64_tr_b16 a[154:155], v14 offset:24576\n" \
"v_mul_f32 v245, v187, v245\n" \
"v_mfma_f32_16x16x32_bf16 v[52:55], a[84:87], v[96:99], v[52:55]\n" \
"s_waitcnt lgkmcnt(6)\n" \
"ds_read_b128 v[124:127], v29 offset:15360\n" \
"v_mul_f32 v246, v187, v246\n" \
"v_mfma_f32_16x16x32_bf16 v[52:55], a[92:95], v[100:103], v[52:55]\n" \
"buffer_load_dwordx4 v31, s[36:39], s80 offen offset:2048 sc1 lds\n" \
"v_mfma_f32_16x16x32_bf16 v[56:59], a[80:83], v[104:107], v[56:59]\n" \
"ds_read_b128 v[96:99], v30 offset:0\n" \
"v_mul_f32 v247, v187, v247\n" \
"v_mfma_f32_16x16x32_bf16 v[56:59], a[88:91], v[108:111], v[56:59]\n" \
"ds_read_b64_tr_b16 a[156:157], v15 offset:24576\n" \
"v_cvt_pk_bf16_f32 v240, v240, v241\n" \
"v_mfma_f32_16x16x32_bf16 v[60:63], a[84:87], v[104:107], v[60:63]\n" \
"s_waitcnt lgkmcnt(6)\n" \
"ds_read_b128 v[100:103], v30 offset:2048\n" \
"v_cvt_pk_bf16_f32 v241, v242, v243\n" \
"v_mfma_f32_16x16x32_bf16 v[60:63], a[92:95], v[108:111], v[60:63]\n" \
"buffer_load_dwordx4 v31, s[36:39], s80 offen offset:3072 sc1 lds\n" \
"v_mfma_f32_16x16x32_bf16 v[64:67], a[80:83], v[112:115], v[64:67]\n" \
"s_add_u32 s80, s67, 32768\n" \
"ds_read_b128 v[104:107], v30 offset:1024\n" \
"s_add_u32 m0, s22, 81920\n" \
"v_mfma_f32_16x16x32_bf16 v[64:67], a[88:91], v[116:119], v[64:67]\n" \
"ds_read_b64_tr_b16 a[158:159], v16 offset:24576\n" \
"v_cvt_pk_bf16_f32 v242, v244, v245\n" \
"v_mfma_f32_16x16x32_bf16 v[68:71], a[84:87], v[112:115], v[68:71]\n" \
"s_waitcnt lgkmcnt(5)\n" \
"ds_read_b128 v[108:111], v30 offset:3072\n" \
"v_cvt_pk_bf16_f32 v243, v246, v247\n" \
"v_mfma_f32_16x16x32_bf16 v[68:71], a[92:95], v[116:119], v[68:71]\n" \
"buffer_store_dwordx4 v[240:243], v7, s[36:39], s51 offen offset:1024 sc1\n" \
"v_mfma_f32_16x16x32_bf16 v[72:75], a[80:83], v[120:123], v[72:75]\n" \
"ds_read_b128 v[112:115], v30 offset:4096\n" \
"v_mul_f32 v248, v187, v248\n" \
"v_mfma_f32_16x16x32_bf16 v[72:75], a[88:91], v[124:127], v[72:75]\n" \
"v_mul_f32 v249, v187, v249\n" \
"v_mul_f32 v250, v187, v250\n" \
"v_mfma_f32_16x16x32_bf16 v[76:79], a[84:87], v[120:123], v[76:79]\n" \
"ds_read_b128 v[116:119], v30 offset:6144\n" \
"v_mul_f32 v251, v187, v251\n" \
"v_mfma_f32_16x16x32_bf16 v[76:79], a[92:95], v[124:127], v[76:79]\n" \
"s_waitcnt vmcnt(7) lgkmcnt(3)\n" \
"s_barrier\n" \
"v_mul_f32 v252, v187, v252\n" \
"v_mfma_f32_16x16x32_bf16 v[32:35], a[128:131], v[96:99], v[32:35]\n" \
"ds_read_b128 v[120:123], v30 offset:5120\n" \
"v_mul_f32 v253, v187, v253\n" \
"v_mfma_f32_16x16x32_bf16 v[32:35], a[136:139], v[100:103], v[32:35]\n" \
"v_mul_f32 v254, v187, v254\n" \
"v_mul_f32 v255, v187, v255\n" \
"v_mfma_f32_16x16x32_bf16 v[36:39], a[132:135], v[96:99], v[36:39]\n" \
"s_waitcnt lgkmcnt(3)\n" \
"ds_read_b128 v[124:127], v30 offset:7168\n" \
"v_cvt_pk_bf16_f32 v248, v248, v249\n" \
"v_mfma_f32_16x16x32_bf16 v[36:39], a[140:143], v[100:103], v[36:39]\n" \
"buffer_load_dwordx4 v31, s[36:39], s80 offen offset:0 sc1 lds\n" \
"v_mfma_f32_16x16x32_bf16 v[40:43], a[128:131], v[104:107], v[40:43]\n" \
"ds_read_b128 v[96:99], v30 offset:8192\n" \
"v_cvt_pk_bf16_f32 v249, v250, v251\n" \
"v_mfma_f32_16x16x32_bf16 v[40:43], a[136:139], v[108:111], v[40:43]\n" \
"v_cvt_pk_bf16_f32 v250, v252, v253\n" \
"v_cvt_pk_bf16_f32 v251, v254, v255\n" \
"v_mfma_f32_16x16x32_bf16 v[44:47], a[132:135], v[104:107], v[44:47]\n" \
"s_waitcnt lgkmcnt(3)\n" \
"ds_read_b128 v[100:103], v30 offset:10240\n" \
"v_bfe_u32 v188, v4, 7, 1\n" \
"v_mfma_f32_16x16x32_bf16 v[44:47], a[140:143], v[108:111], v[44:47]\n" \
"buffer_store_dwordx4 v[248:251], v7, s[36:39], s51 offen offset:3072 sc1\n" \
"v_mfma_f32_16x16x32_bf16 v[48:51], a[128:131], v[112:115], v[48:51]\n" \
"ds_read_b128 v[104:107], v30 offset:9216\n" \
"v_bfe_u32 v189, v4, 4, 3\n" \
"v_mfma_f32_16x16x32_bf16 v[48:51], a[136:139], v[116:119], v[48:51]\n" \
"v_lshl_add_u32 v188, v189, 1, v188\n" \
"v_lshrrev_b32 v189, 8, v4\n" \
"v_mfma_f32_16x16x32_bf16 v[52:55], a[132:135], v[112:115], v[52:55]\n" \
"s_waitcnt lgkmcnt(3)\n" \
"ds_read_b128 v[108:111], v30 offset:11264\n" \
"v_lshlrev_b32 v184, 11, v188\n" \
"v_mfma_f32_16x16x32_bf16 v[52:55], a[140:143], v[116:119], v[52:55]\n" \
"v_and_b32 v188, 1, v189\n" \
"v_lshl_add_u32 v184, v188, 5, v184\n" \
"v_mfma_f32_16x16x32_bf16 v[56:59], a[128:131], v[120:123], v[56:59]\n" \
"ds_read_b128 v[112:115], v29 offset:49152\n" \
"v_lshrrev_b32 v188, 1, v189\n" \
"v_mfma_f32_16x16x32_bf16 v[56:59], a[136:139], v[124:127], v[56:59]\n" \
"v_lshl_add_u32 v184, v188, 4, v184\n" \
"v_bfe_u32 v188, v4, 7, 1\n" \
"v_mfma_f32_16x16x32_bf16 v[60:63], a[132:135], v[120:123], v[60:63]\n" \
"ds_read_b128 v[116:119], v29 offset:51200\n" \
"v_bfe_u32 v189, v4, 4, 3\n" \
"v_mfma_f32_16x16x32_bf16 v[60:63], a[140:143], v[124:127], v[60:63]\n" \
"s_waitcnt lgkmcnt(4)\n" \
"s_barrier\n" \
"v_lshl_add_u32 v188, v189, 1, v188\n" \
"v_mfma_f32_16x16x32_bf16 v[64:67], a[128:131], v[96:99], v[64:67]\n" \
"ds_read_b128 v[120:123], v29 offset:50176\n" \
"v_lshlrev_b32 v185, 3, v188\n" \
"v_mfma_f32_16x16x32_bf16 v[64:67], a[136:139], v[100:103], v[64:67]\n" \
"buffer_load_dwordx4 v31, s[36:39], s80 offen offset:1024 sc1 lds\n" \
"v_mfma_f32_16x16x32_bf16 v[68:71], a[132:135], v[96:99], v[68:71]\n" \
"s_waitcnt lgkmcnt(3)\n" \
"ds_read_b128 v[124:127], v29 offset:52224\n" \
"v_accvgpr_read_b32 v144, a96\n" \
"v_mfma_f32_16x16x32_bf16 v[68:71], a[140:143], v[100:103], v[68:71]\n" \
"v_accvgpr_read_b32 v145, a97\n" \
"v_accvgpr_read_b32 v146, a98\n" \
"v_mfma_f32_16x16x32_bf16 v[72:75], a[128:131], v[104:107], v[72:75]\n" \
"ds_read_b128 v[96:99], v29 offset:53248\n" \
"v_accvgpr_read_b32 v147, a99\n" \
"v_mfma_f32_16x16x32_bf16 v[72:75], a[136:139], v[108:111], v[72:75]\n" \
"buffer_load_dwordx4 v31, s[36:39], s80 offen offset:2048 sc1 lds\n" \
"v_mfma_f32_16x16x32_bf16 v[76:79], a[132:135], v[104:107], v[76:79]\n" \
"s_waitcnt lgkmcnt(3)\n" \
"ds_read_b128 v[100:103], v29 offset:55296\n" \
"v_accvgpr_read_b32 v148, a100\n" \
"v_mfma_f32_16x16x32_bf16 v[76:79], a[140:143], v[108:111], v[76:79]\n" \
"v_accvgpr_read_b32 v149, a101\n" \
"v_accvgpr_read_b32 v150, a102\n" \
"v_mfma_f32_16x16x32_bf16 v[80:83], a[160:163], v[112:115], 0\n" \
"ds_read_b128 v[104:107], v29 offset:54272\n" \
"v_accvgpr_read_b32 v151, a103\n" \
"v_mfma_f32_16x16x32_bf16 v[80:83], a[168:171], v[116:119], v[80:83]\n" \
"buffer_load_dwordx4 v31, s[36:39], s80 offen offset:3072 sc1 lds\n" \
"v_mfma_f32_16x16x32_bf16 v[84:87], a[164:167], v[112:115], 0\n" \
"s_waitcnt lgkmcnt(3)\n" \
"ds_read_b128 v[108:111], v29 offset:56320\n" \
"s_mov_b32 s81, 1310720\n" \
"v_mfma_f32_16x16x32_bf16 v[84:87], a[172:175], v[116:119], v[84:87]\n" \
"s_add_u32 m0, s79, 16384\n" \
"v_accvgpr_read_b32 v152, a104\n" \
"v_accvgpr_read_b32 v153, a105\n" \
"v_mfma_f32_16x16x32_bf16 v[88:91], a[160:163], v[120:123], 0\n" \
"ds_read_b128 v[112:115], v29 offset:57344\n" \
"v_accvgpr_read_b32 v154, a106\n" \
"v_mfma_f32_16x16x32_bf16 v[88:91], a[168:171], v[124:127], v[88:91]\n" \
"buffer_load_dwordx4 v12, s[28:31], s81 offen offset:0 lds\n" \
"v_mfma_f32_16x16x32_bf16 v[92:95], a[164:167], v[120:123], 0\n" \
"s_add_u32 m0, s79, 17280\n" \
"ds_read_b128 v[116:119], v29 offset:59392\n" \
"v_accvgpr_read_b32 v155, a107\n" \
"v_mfma_f32_16x16x32_bf16 v[92:95], a[172:175], v[124:127], v[92:95]\n" \
"s_waitcnt vmcnt(7) lgkmcnt(4)\n" \
"s_barrier\n" \
"v_accvgpr_read_b32 v156, a108\n" \
"v_mfma_f32_16x16x32_bf16 v[80:83], a[176:179], v[96:99], v[80:83]\n" \
"ds_read_b128 v[120:123], v29 offset:58368\n" \
"v_accvgpr_read_b32 v157, a109\n" \
"v_mfma_f32_16x16x32_bf16 v[80:83], a[184:187], v[100:103], v[80:83]\n" \
"buffer_load_dwordx4 v12, s[28:31], s81 offen offset:128 lds\n" \
"v_mfma_f32_16x16x32_bf16 v[84:87], a[180:183], v[96:99], v[84:87]\n" \
"s_waitcnt lgkmcnt(3)\n" \
"ds_read_b128 v[124:127], v29 offset:60416\n" \
"s_mov_b32 s81, 1376256\n" \
"v_mfma_f32_16x16x32_bf16 v[84:87], a[188:191], v[100:103], v[84:87]\n" \
"s_add_u32 m0, s79, 24576\n" \
"v_accvgpr_read_b32 v158, a110\n" \
"v_accvgpr_read_b32 v159, a111\n" \
"v_mfma_f32_16x16x32_bf16 v[88:91], a[176:179], v[104:107], v[88:91]\n" \
"ds_read_b128 v[96:99], v29 offset:61440\n" \
"v_accvgpr_read_b32 v128, a112\n" \
"v_mfma_f32_16x16x32_bf16 v[88:91], a[184:187], v[108:111], v[88:91]\n" \
"buffer_load_dwordx4 v12, s[28:31], s81 offen offset:0 lds\n" \
"v_mfma_f32_16x16x32_bf16 v[92:95], a[180:183], v[104:107], v[92:95]\n" \
"s_waitcnt lgkmcnt(3)\n" \
"ds_read_b128 v[100:103], v29 offset:63488\n" \
"s_add_u32 m0, s79, 25472\n" \
"v_mfma_f32_16x16x32_bf16 v[92:95], a[188:191], v[108:111], v[92:95]\n" \
"v_accvgpr_read_b32 v129, a113\n" \
"v_accvgpr_read_b32 v130, a114\n" \
"v_mfma_f32_16x16x32_bf16 v[80:83], a[192:195], v[112:115], v[80:83]\n" \
"ds_read_b128 v[104:107], v29 offset:62464\n" \
"v_accvgpr_read_b32 v131, a115\n" \
"v_mfma_f32_16x16x32_bf16 v[80:83], a[200:203], v[116:119], v[80:83]\n" \
"buffer_load_dwordx4 v12, s[28:31], s81 offen offset:128 lds\n" \
"v_mfma_f32_16x16x32_bf16 v[84:87], a[196:199], v[112:115], v[84:87]\n" \
"s_waitcnt lgkmcnt(3)\n" \
"ds_read_b128 v[108:111], v29 offset:64512\n" \
"s_mov_b32 s81, 1441792\n" \
"v_mfma_f32_16x16x32_bf16 v[84:87], a[204:207], v[116:119], v[84:87]\n" \
"s_add_u32 m0, s79, 98304\n" \
"v_accvgpr_read_b32 v132, a116\n" \
"v_accvgpr_read_b32 v133, a117\n" \
"v_mfma_f32_16x16x32_bf16 v[88:91], a[192:195], v[120:123], v[88:91]\n" \
"ds_read_b128 v[112:115], v28 offset:0\n" \
"v_accvgpr_read_b32 v134, a118\n" \
"v_mfma_f32_16x16x32_bf16 v[88:91], a[200:203], v[124:127], v[88:91]\n" \
"buffer_load_dwordx4 v12, s[28:31], s81 offen offset:0 lds\n" \
"v_mfma_f32_16x16x32_bf16 v[92:95], a[196:199], v[120:123], v[92:95]\n" \
"s_add_u32 m0, s79, 99200\n" \
"ds_read_b128 v[116:119], v28 offset:2048\n" \
"v_accvgpr_read_b32 v135, a119\n" \
"v_mfma_f32_16x16x32_bf16 v[92:95], a[204:207], v[124:127], v[92:95]\n" \
"s_waitcnt lgkmcnt(4)\n" \
"s_barrier\n" \
"v_accvgpr_read_b32 v136, a120\n" \
"v_mfma_f32_16x16x32_bf16 v[80:83], a[208:211], v[96:99], v[80:83]\n" \
"ds_read_b128 v[120:123], v28 offset:1024\n" \
"v_accvgpr_read_b32 v137, a121\n" \
"v_mfma_f32_16x16x32_bf16 v[80:83], a[216:219], v[100:103], v[80:83]\n" \
"buffer_load_dwordx4 v12, s[28:31], s81 offen offset:128 lds\n" \
"v_mfma_f32_16x16x32_bf16 v[84:87], a[212:215], v[96:99], v[84:87]\n" \
"s_waitcnt lgkmcnt(3)\n" \
"ds_read_b128 v[124:127], v28 offset:3072\n" \
"s_mov_b32 s81, 1507328\n" \
"v_mfma_f32_16x16x32_bf16 v[84:87], a[220:223], v[100:103], v[84:87]\n" \
"s_add_u32 m0, s79, 106496\n" \
"v_accvgpr_read_b32 v138, a122\n" \
"v_accvgpr_read_b32 v139, a123\n" \
"v_mfma_f32_16x16x32_bf16 v[88:91], a[208:211], v[104:107], v[88:91]\n" \
"ds_read_b128 v[96:99], v28 offset:4096\n" \
"v_accvgpr_read_b32 v140, a124\n" \
"v_mfma_f32_16x16x32_bf16 v[88:91], a[216:219], v[108:111], v[88:91]\n" \
"buffer_load_dwordx4 v12, s[28:31], s81 offen offset:0 lds\n" \
"v_mfma_f32_16x16x32_bf16 v[92:95], a[212:215], v[104:107], v[92:95]\n" \
"s_waitcnt lgkmcnt(3)\n" \
"ds_read_b128 v[100:103], v28 offset:6144\n" \
"s_add_u32 m0, s79, 107392\n" \
"v_mfma_f32_16x16x32_bf16 v[92:95], a[220:223], v[108:111], v[92:95]\n" \
"v_accvgpr_read_b32 v141, a125\n" \
"v_accvgpr_read_b32 v142, a126\n" \
"v_mfma_f32_16x16x32_bf16 v[80:83], a[224:227], v[112:115], v[80:83]\n" \
"ds_read_b128 v[104:107], v28 offset:5120\n" \
"v_accvgpr_read_b32 v143, a127\n" \
"v_mfma_f32_16x16x32_bf16 v[80:83], a[232:235], v[116:119], v[80:83]\n" \
"buffer_load_dwordx4 v12, s[28:31], s81 offen offset:128 lds\n" \
"v_mfma_f32_16x16x32_bf16 v[84:87], a[228:231], v[112:115], v[84:87]\n" \
"s_waitcnt lgkmcnt(3)\n" \
"ds_read_b128 v[108:111], v28 offset:7168\n" \
"s_add_u32 s80, s66, 32768\n" \
"v_mfma_f32_16x16x32_bf16 v[84:87], a[236:239], v[116:119], v[84:87]\n" \
"s_add_u32 m0, s22, 49152\n" \
"v_mfma_f32_16x16x32_bf16 v[88:91], a[224:227], v[120:123], v[88:91]\n" \
"ds_read_b128 v[112:115], v28 offset:8192\n" \
"v_mfma_f32_16x16x32_bf16 v[88:91], a[232:235], v[124:127], v[88:91]\n" \
"buffer_load_dwordx4 v4, s[36:39], s80 offen offset:0 sc1 lds\n" \
"v_mfma_f32_16x16x32_bf16 v[92:95], a[228:231], v[120:123], v[92:95]\n" \
"ds_read_b128 v[116:119], v28 offset:10240\n" \
"v_mfma_f32_16x16x32_bf16 v[92:95], a[236:239], v[124:127], v[92:95]\n" \
"s_waitcnt vmcnt(9) lgkmcnt(4)\n" \
"s_barrier\n" \
"v_mfma_f32_16x16x32_bf16 v[80:83], a[240:243], v[96:99], v[80:83]\n" \
"ds_read_b128 v[120:123], v28 offset:9216\n" \
"v_mfma_f32_16x16x32_bf16 v[80:83], a[248:251], v[100:103], v[80:83]\n" \
"buffer_load_dwordx4 v4, s[36:39], s80 offen offset:1024 sc1 lds\n" \
"v_mfma_f32_16x16x32_bf16 v[84:87], a[244:247], v[96:99], v[84:87]\n" \
"s_waitcnt lgkmcnt(3)\n" \
"ds_read_b128 v[124:127], v28 offset:11264\n" \
"v_mfma_f32_16x16x32_bf16 v[84:87], a[252:255], v[100:103], v[84:87]\n" \
"v_mfma_f32_16x16x32_bf16 v[88:91], a[240:243], v[104:107], v[88:91]\n" \
"ds_read_b128 v[96:99], v28 offset:12288\n" \
"v_mfma_f32_16x16x32_bf16 v[88:91], a[248:251], v[108:111], v[88:91]\n" \
"buffer_load_dwordx4 v4, s[36:39], s80 offen offset:2048 sc1 lds\n" \
"v_mfma_f32_16x16x32_bf16 v[92:95], a[244:247], v[104:107], v[92:95]\n" \
"s_waitcnt lgkmcnt(3)\n" \
"ds_read_b128 v[100:103], v28 offset:14336\n" \
"v_mfma_f32_16x16x32_bf16 v[92:95], a[252:255], v[108:111], v[92:95]\n" \
"v_mfma_f32_16x16x32_bf16 v[80:83], a[64:67], v[112:115], v[80:83]\n" \
"ds_read_b128 v[104:107], v28 offset:13312\n" \
"v_mfma_f32_16x16x32_bf16 v[80:83], a[72:75], v[116:119], v[80:83]\n" \
"buffer_load_dwordx4 v4, s[36:39], s80 offen offset:3072 sc1 lds\n" \
"v_mfma_f32_16x16x32_bf16 v[84:87], a[68:71], v[112:115], v[84:87]\n" \
"s_waitcnt lgkmcnt(3)\n" \
"ds_read_b128 v[108:111], v28 offset:15360\n" \
"s_mov_b32 s81, 1572864\n" \
"v_mfma_f32_16x16x32_bf16 v[84:87], a[76:79], v[116:119], v[84:87]\n" \
"s_add_u32 m0, s79, 65536\n" \
"v_mfma_f32_16x16x32_bf16 v[88:91], a[64:67], v[120:123], v[88:91]\n" \
"ds_read_b128 v[112:115], v29 offset:16384\n" \
"v_mfma_f32_16x16x32_bf16 v[88:91], a[72:75], v[124:127], v[88:91]\n" \
"buffer_load_dwordx4 v12, s[28:31], s81 offen offset:0 lds\n" \
"v_mfma_f32_16x16x32_bf16 v[92:95], a[68:71], v[120:123], v[92:95]\n" \
"ds_read_b128 v[116:119], v29 offset:18432\n" \
"s_waitcnt vmcnt(16)\n" \
"v_mfma_f32_16x16x32_bf16 v[92:95], a[76:79], v[124:127], v[92:95]\n" \
"s_waitcnt vmcnt(31) lgkmcnt(4)\n" \
"s_barrier\n" \
"s_add_u32 m0, s79, 66432\n" \
"v_mfma_f32_16x16x32_bf16 v[80:83], a[80:83], v[96:99], v[80:83]\n" \
"ds_read_b128 v[120:123], v29 offset:17408\n" \
"v_mfma_f32_16x16x32_bf16 v[80:83], a[88:91], v[100:103], v[80:83]\n" \
"buffer_load_dwordx4 v12, s[28:31], s81 offen offset:128 lds\n" \
"v_mfma_f32_16x16x32_bf16 v[84:87], a[84:87], v[96:99], v[84:87]\n" \
"s_waitcnt lgkmcnt(3)\n" \
"ds_read_b128 v[124:127], v29 offset:19456\n" \
"s_mov_b32 s81, 1638400\n" \
"v_mfma_f32_16x16x32_bf16 v[84:87], a[92:95], v[100:103], v[84:87]\n" \
"s_cmp_lg_u64 s[4:5], 0\ns_cbranch_scc1 LFBB_%=\nLFBBR_%=:\n" \
"s_add_u32 m0, s79, 73728\n" \
"v_mfma_f32_16x16x32_bf16 v[88:91], a[80:83], v[104:107], v[88:91]\n" \
"ds_read_b128 v[96:99], v30 offset:12288\n" \
"v_mfma_f32_16x16x32_bf16 v[88:91], a[88:91], v[108:111], v[88:91]\n" \
"buffer_store_dwordx2 v[10:11], v20, s[36:39], s53 offen sc1\n" \
"v_mfma_f32_16x16x32_bf16 v[92:95], a[84:87], v[104:107], v[92:95]\n" \
"s_waitcnt lgkmcnt(3)\n" \
"ds_read_b128 v[100:103], v30 offset:14336\n" \
"v_mfma_f32_16x16x32_bf16 v[92:95], a[92:95], v[108:111], v[92:95]\n" \
"buffer_load_dwordx4 v12, s[28:31], s81 offen offset:0 lds\n" \
"v_mfma_f32_16x16x32_bf16 v[80:83], a[128:131], v[112:115], v[80:83]\n" \
"s_add_u32 m0, s79, 74624\n" \
"ds_read_b128 v[104:107], v30 offset:13312\n" \
"v_mfma_f32_16x16x32_bf16 v[80:83], a[136:139], v[116:119], v[80:83]\n" \
"v_mfma_f32_16x16x32_bf16 v[84:87], a[132:135], v[112:115], v[84:87]\n" \
"s_waitcnt lgkmcnt(3)\n" \
"ds_read_b128 v[108:111], v30 offset:15360\n" \
"v_mfma_f32_16x16x32_bf16 v[84:87], a[140:143], v[116:119], v[84:87]\n" \
"buffer_load_dwordx4 v12, s[28:31], s81 offen offset:128 lds\n" \
"v_mfma_f32_16x16x32_bf16 v[88:91], a[128:131], v[120:123], v[88:91]\n" \
"s_add_u32 s80, s64, 49152\n" \
"ds_read_b128 v[112:115], v28 offset:32768\n" \
"s_add_u32 m0, s22, 147456\n" \
"v_mfma_f32_16x16x32_bf16 v[88:91], a[136:139], v[124:127], v[88:91]\n" \
"LH25_%=:\ns_cbranch_execz LHR2_%=\n" \
"v_mfma_f32_16x16x32_bf16 v[92:95], a[132:135], v[120:123], v[92:95]\n" \
"ds_read_b128 v[116:119], v28 offset:34816\n" \
"v_mfma_f32_16x16x32_bf16 v[92:95], a[140:143], v[124:127], v[92:95]\n" \
"s_waitcnt vmcnt(13) lgkmcnt(4)\n" \
"s_barrier\n" \
"v_mfma_f32_16x16x32_bf16 v[32:35], a[144:147], v[96:99], v[32:35]\n" \
"ds_read_b128 v[120:123], v28 offset:33792\n" \
"v_mfma_f32_16x16x32_bf16 v[32:35], a[152:155], v[100:103], v[32:35]\n" \
"buffer_load_dwordx4 v4, s[36:39], s80 offen offset:0 sc1 lds\n" \
"v_mfma_f32_16x16x32_bf16 v[36:39], a[148:151], v[96:99], v[36:39]\n" \
"s_waitcnt lgkmcnt(3)\n" \
"ds_read_b128 v[124:127], v28 offset:35840\n" \
"v_mfma_f32_16x16x32_bf16 v[36:39], a[156:159], v[100:103], v[36:39]\n" \
"ds_read_b64_tr_b16 a[0:1], v176 offset:16384\n" \
"v_mfma_f32_16x16x32_bf16 v[40:43], a[144:147], v[104:107], v[40:43]\n" \
"ds_read_b128 v[96:99], v28 offset:36864\n" \
"v_mfma_f32_16x16x32_bf16 v[40:43], a[152:155], v[108:111], v[40:43]\n" \
"buffer_load_dwordx4 v4, s[36:39], s80 offen offset:1024 sc1 lds\n" \
"v_mfma_f32_16x16x32_bf16 v[44:47], a[148:151], v[104:107], v[44:47]\n" \
"s_waitcnt lgkmcnt(4)\n" \
"ds_read_b128 v[100:103], v28 offset:38912\n" \
"v_mfma_f32_16x16x32_bf16 v[44:47], a[156:159], v[108:111], v[44:47]\n" \
"ds_read_b64_tr_b16 a[2:3], v177 offset:16384\n" \
"v_mfma_f32_16x16x32_bf16 v[48:51], a[144:147], v[112:115], v[48:51]\n" \
"ds_read_b128 v[104:107], v28 offset:37888\n" \
"v_mfma_f32_16x16x32_bf16 v[48:51], a[152:155], v[116:119], v[48:51]\n" \
"buffer_load_dwordx4 v4, s[36:39], s80 offen offset:2048 sc1 lds\n" \
"v_mfma_f32_16x16x32_bf16 v[52:55], a[148:151], v[112:115], v[52:55]\n" \
"s_waitcnt lgkmcnt(5)\n" \
"ds_read_b128 v[108:111], v28 offset:39936\n" \
"v_mfma_f32_16x16x32_bf16 v[52:55], a[156:159], v[116:119], v[52:55]\n" \
"ds_read_b64_tr_b16 a[4:5], v178 offset:16384\n" \
"v_mfma_f32_16x16x32_bf16 v[56:59], a[144:147], v[120:123], v[56:59]\n" \
"ds_read_b128 v[112:115], v29 offset:20480\n" \
"v_mfma_f32_16x16x32_bf16 v[56:59], a[152:155], v[124:127], v[56:59]\n" \
"buffer_load_dwordx4 v4, s[36:39], s80 offen offset:3072 sc1 lds\n" \
"v_mfma_f32_16x16x32_bf16 v[60:63], a[148:151], v[120:123], v[60:63]\n" \
"s_mov_b32 s81, 1703936\n" \
"ds_read_b128 v[116:119], v29 offset:22528\n" \
"s_add_u32 m0, s79, 114688\n" \
"v_mfma_f32_16x16x32_bf16 v[60:63], a[156:159], v[124:127], v[60:63]\n" \
"s_waitcnt vmcnt(13) lgkmcnt(6)\n" \
"s_barrier\n" \
"ds_read_b64_tr_b16 a[6:7], v179 offset:16384\n" \
"v_mfma_f32_16x16x32_bf16 v[64:67], a[144:147], v[96:99], v[64:67]\n" \
"ds_read_b128 v[120:123], v29 offset:21504\n" \
"v_mfma_f32_16x16x32_bf16 v[64:67], a[152:155], v[100:103], v[64:67]\n" \
"buffer_load_dwordx4 v12, s[28:31], s81 offen offset:0 lds\n" \
"v_mfma_f32_16x16x32_bf16 v[68:71], a[148:151], v[96:99], v[68:71]\n" \
"s_waitcnt lgkmcnt(5)\n" \
"ds_read_b128 v[124:127], v29 offset:23552\n" \
"s_add_u32 m0, s79, 115584\n" \
"v_mfma_f32_16x16x32_bf16 v[68:71], a[156:159], v[100:103], v[68:71]\n" \
"ds_read_b64_tr_b16 a[8:9], v176 offset:24576\n" \
"v_mfma_f32_16x16x32_bf16 v[72:75], a[144:147], v[104:107], v[72:75]\n" \
"ds_read_b128 v[96:99], v28 offset:40960\n" \
"v_mfma_f32_16x16x32_bf16 v[72:75], a[152:155], v[108:111], v[72:75]\n" \
"buffer_load_dwordx4 v12, s[28:31], s81 offen offset:128 lds\n" \
"v_mfma_f32_16x16x32_bf16 v[76:79], a[148:151], v[104:107], v[76:79]\n" \
"s_waitcnt lgkmcnt(5)\n" \
"ds_read_b128 v[100:103], v28 offset:43008\n" \
"s_mov_b32 s81, 1769472\n" \
"v_mfma_f32_16x16x32_bf16 v[76:79], a[156:159], v[108:111], v[76:79]\n" \
"s_add_u32 m0, s79, 122880\n" \
"ds_read_b64_tr_b16 a[10:11], v177 offset:24576\n" \
"v_mfma_f32_16x16x32_bf16 v[80:83], a[144:147], v[112:115], v[80:83]\n" \
"ds_read_b128 v[104:107], v28 offset:41984\n" \
"v_mfma_f32_16x16x32_bf16 v[80:83], a[152:155], v[116:119], v[80:83]\n" \
"buffer_load_dwordx4 v12, s[28:31], s81 offen offset:0 lds\n" \
"v_mfma_f32_16x16x32_bf16 v[84:87], a[148:151], v[112:115], v[84:87]\n" \
"s_waitcnt lgkmcnt(5)\n" \
"ds_read_b128 v[108:111], v28 offset:44032\n" \
"s_add_u32 m0, s79, 123776\n" \
"ds_read_b64_tr_b16 a[12:13], v178 offset:24576\n" \
"ds_read_b64_tr_b16 a[14:15], v179 offset:24576\n" \
"v_mfma_f32_16x16x32_bf16 v[84:87], a[156:159], v[116:119], v[84:87]\n" \
"ds_read_b64_tr_b16 a[16:17], v180 offset:32768\n" \
"v_mfma_f32_16x16x32_bf16 v[88:91], a[144:147], v[120:123], v[88:91]\n" \
"ds_read_b128 v[112:115], v28 offset:45056\n" \
"v_mfma_f32_16x16x32_bf16 v[88:91], a[152:155], v[124:127], v[88:91]\n" \
"buffer_load_dwordx4 v12, s[28:31], s81 offen offset:128 lds\n" \
"v_mfma_f32_16x16x32_bf16 v[92:95], a[148:151], v[120:123], v[92:95]\n" \
"s_add_u32 s80, s67, 49152\n" \
"ds_read_b128 v[116:119], v28 offset:47104\n" \
"s_add_u32 m0, s22, 0\n" \
"v_mfma_f32_16x16x32_bf16 v[92:95], a[156:159], v[124:127], v[92:95]\n" \
"s_waitcnt vmcnt(13) lgkmcnt(3)\n" \
"s_barrier\n" \
"ds_read_b64_tr_b16 a[18:19], v181 offset:32768\n" \
"v_mfma_f32_16x16x32_bf16 v[32:35], a[0:3], v[96:99], v[32:35]\n" \
"ds_read_b128 v[120:123], v28 offset:46080\n" \
"v_mfma_f32_16x16x32_bf16 v[32:35], a[8:11], v[100:103], v[32:35]\n" \
"buffer_load_dwordx4 v31, s[36:39], s80 offen offset:0 sc1 lds\n" \
"v_mfma_f32_16x16x32_bf16 v[36:39], a[4:7], v[96:99], v[36:39]\n" \
"s_waitcnt lgkmcnt(7)\n" \
"ds_read_b128 v[124:127], v28 offset:48128\n" \
"v_mfma_f32_16x16x32_bf16 v[36:39], a[12:15], v[100:103], v[36:39]\n" \
"ds_read_b64_tr_b16 a[20:21], v182 offset:32768\n" \
"v_mfma_f32_16x16x32_bf16 v[40:43], a[0:3], v[104:107], v[40:43]\n" \
"ds_read_b128 v[96:99], v28 offset:49152\n" \
"v_mfma_f32_16x16x32_bf16 v[40:43], a[8:11], v[108:111], v[40:43]\n" \
"buffer_load_dwordx4 v31, s[36:39], s80 offen offset:1024 sc1 lds\n" \
"v_mfma_f32_16x16x32_bf16 v[44:47], a[4:7], v[104:107], v[44:47]\n" \
"s_waitcnt lgkmcnt(5)\n" \
"ds_read_b128 v[100:103], v28 offset:51200\n" \
"v_mfma_f32_16x16x32_bf16 v[44:47], a[12:15], v[108:111], v[44:47]\n" \
"ds_read_b64_tr_b16 a[22:23], v183 offset:32768\n" \
"v_mfma_f32_16x16x32_bf16 v[48:51], a[0:3], v[112:115], v[48:51]\n" \
"ds_read_b128 v[104:107], v28 offset:50176\n" \
"v_mfma_f32_16x16x32_bf16 v[48:51], a[8:11], v[116:119], v[48:51]\n" \
"buffer_load_dwordx4 v31, s[36:39], s80 offen offset:2048 sc1 lds\n" \
"v_mfma_f32_16x16x32_bf16 v[52:55], a[4:7], v[112:115], v[52:55]\n" \
"s_waitcnt lgkmcnt(5)\n" \
"ds_read_b128 v[108:111], v28 offset:52224\n" \
"ds_read_b64_tr_b16 a[24:25], v180 offset:40960\n" \
"ds_read_b64_tr_b16 a[26:27], v181 offset:40960\n" \
"ds_read_b64_tr_b16 a[28:29], v182 offset:40960\n" \
"ds_read_b64_tr_b16 a[30:31], v183 offset:40960\n" \
"v_mfma_f32_16x16x32_bf16 v[52:55], a[12:15], v[116:119], v[52:55]\n" \
"v_mfma_f32_16x16x32_bf16 v[56:59], a[0:3], v[120:123], v[56:59]\n" \
"ds_read_b128 v[112:115], v29 offset:24576\n" \
"v_mfma_f32_16x16x32_bf16 v[56:59], a[8:11], v[124:127], v[56:59]\n" \
"buffer_load_dwordx4 v31, s[36:39], s80 offen offset:3072 sc1 lds\n" \
"v_mfma_f32_16x16x32_bf16 v[60:63], a[4:7], v[120:123], v[60:63]\n" \
"s_add_u32 s80, s65, 49152\n" \
"ds_read_b128 v[116:119], v29 offset:26624\n" \
"s_add_u32 m0, s22, 131072\n" \
"v_mfma_f32_16x16x32_bf16 v[60:63], a[12:15], v[124:127], v[60:63]\n" \
"s_waitcnt lgkmcnt(2)\n" \
"s_barrier\n" \
"v_mfma_f32_16x16x32_bf16 v[64:67], a[0:3], v[96:99], v[64:67]\n" \
"ds_read_b128 v[120:123], v29 offset:25600\n" \
"v_mfma_f32_16x16x32_bf16 v[64:67], a[8:11], v[100:103], v[64:67]\n" \
"buffer_load_dwordx4 v4, s[36:39], s80 offen offset:0 sc1 lds\n" \
"v_mfma_f32_16x16x32_bf16 v[68:71], a[4:7], v[96:99], v[68:71]\n" \
"s_waitcnt lgkmcnt(7)\n" \
"ds_read_b128 v[124:127], v29 offset:27648\n" \
"v_mfma_f32_16x16x32_bf16 v[68:71], a[12:15], v[100:103], v[68:71]\n" \
"v_mfma_f32_16x16x32_bf16 v[72:75], a[0:3], v[104:107], v[72:75]\n" \
"ds_read_b128 v[96:99], v28 offset:53248\n" \
"v_mfma_f32_16x16x32_bf16 v[72:75], a[8:11], v[108:111], v[72:75]\n" \
"buffer_load_dwordx4 v4, s[36:39], s80 offen offset:1024 sc1 lds\n" \
"v_mfma_f32_16x16x32_bf16 v[76:79], a[4:7], v[104:107], v[76:79]\n" \
"s_waitcnt lgkmcnt(3)\n" \
"ds_read_b128 v[100:103], v28 offset:55296\n" \
"v_mfma_f32_16x16x32_bf16 v[76:79], a[12:15], v[108:111], v[76:79]\n" \
"v_mfma_f32_16x16x32_bf16 v[80:83], a[0:3], v[112:115], v[80:83]\n" \
"ds_read_b128 v[104:107], v28 offset:54272\n" \
"v_mfma_f32_16x16x32_bf16 v[80:83], a[8:11], v[116:119], v[80:83]\n" \
"buffer_load_dwordx4 v4, s[36:39], s80 offen offset:2048 sc1 lds\n" \
"v_mfma_f32_16x16x32_bf16 v[84:87], a[4:7], v[112:115], v[84:87]\n" \
"s_waitcnt lgkmcnt(3)\n" \
"ds_read_b128 v[108:111], v28 offset:56320\n" \
"v_bfe_u32 v186, v4, 7, 2\n" \
"v_mfma_f32_16x16x32_bf16 v[84:87], a[12:15], v[116:119], v[84:87]\n" \
"v_min_u32 v186, 3, v186\n" \
"v_lshl_add_u32 v17, v186, 8, s11\n" \
"v_mfma_f32_16x16x32_bf16 v[88:91], a[0:3], v[120:123], v[88:91]\n" \
"ds_read_b128 v[112:115], v28 offset:57344\n" \
"v_bfe_u32 v186, v4, 4, 3\n" \
"v_mfma_f32_16x16x32_bf16 v[88:91], a[8:11], v[124:127], v[88:91]\n" \
"buffer_load_dwordx4 v4, s[36:39], s80 offen offset:3072 sc1 lds\n" \
"v_mfma_f32_16x16x32_bf16 v[92:95], a[4:7], v[120:123], v[92:95]\n" \
"s_mov_b32 s81, 1835008\n" \
"ds_read_b128 v[116:119], v28 offset:59392\n" \
"v_lshl_add_u32 v17, v186, 3, v17\n" \
"v_mfma_f32_16x16x32_bf16 v[92:95], a[12:15], v[124:127], v[92:95]\n" \
"s_waitcnt vmcnt(16) lgkmcnt(4)\n" \
"s_barrier\n" \
"s_add_u32 m0, s79, 16384\n" \
"v_mfma_f32_16x16x32_bf16 v[32:35], a[16:19], v[96:99], v[32:35]\n" \
"ds_read_b128 v[120:123], v28 offset:58368\n" \
"v_mfma_f32_16x16x32_bf16 v[32:35], a[24:27], v[100:103], v[32:35]\n" \
"buffer_load_dwordx4 v12, s[28:31], s81 offen offset:0 lds\n" \
"v_mfma_f32_16x16x32_bf16 v[36:39], a[20:23], v[96:99], v[36:39]\n" \
"s_waitcnt lgkmcnt(3)\n" \
"ds_read_b128 v[124:127], v28 offset:60416\n" \
"s_add_u32 m0, s79, 17280\n" \
"v_mfma_f32_16x16x32_bf16 v[36:39], a[28:31], v[100:103], v[36:39]\n" \
"ds_read_b64_tr_b16 a[32:33], v180 offset:0\n" \
"v_mfma_f32_16x16x32_bf16 v[40:43], a[16:19], v[104:107], v[40:43]\n" \
"ds_read_b128 v[96:99], v28 offset:61440\n" \
"v_mfma_f32_16x16x32_bf16 v[40:43], a[24:27], v[108:111], v[40:43]\n" \
"buffer_load_dwordx4 v12, s[28:31], s81 offen offset:128 lds\n" \
"v_mfma_f32_16x16x32_bf16 v[44:47], a[20:23], v[104:107], v[44:47]\n" \
"s_waitcnt lgkmcnt(4)\n" \
"ds_read_b128 v[100:103], v28 offset:63488\n" \
"s_mov_b32 s81, 1900544\n" \
"v_mfma_f32_16x16x32_bf16 v[44:47], a[28:31], v[108:111], v[44:47]\n" \
"s_add_u32 m0, s79, 24576\n" \
"ds_read_b64_tr_b16 a[34:35], v181 offset:0\n" \
"v_mfma_f32_16x16x32_bf16 v[48:51], a[16:19], v[112:115], v[48:51]\n" \
"ds_read_b128 v[104:107], v28 offset:62464\n" \
"v_mfma_f32_16x16x32_bf16 v[48:51], a[24:27], v[116:119], v[48:51]\n" \
"buffer_load_dwordx2 v[18:19], v17, s[36:39], 0 offen sc1\n" \
"v_mfma_f32_16x16x32_bf16 v[52:55], a[20:23], v[112:115], v[52:55]\n" \
"s_waitcnt lgkmcnt(5)\n" \
"ds_read_b128 v[108:111], v28 offset:64512\n" \
"v_mfma_f32_16x16x32_bf16 v[52:55], a[28:31], v[116:119], v[52:55]\n" \
"ds_read_b64_tr_b16 a[36:37], v182 offset:0\n" \
"v_mfma_f32_16x16x32_bf16 v[56:59], a[16:19], v[120:123], v[56:59]\n" \
"ds_read_b128 v[112:115], v29 offset:28672\n" \
"v_mfma_f32_16x16x32_bf16 v[56:59], a[24:27], v[124:127], v[56:59]\n" \
"buffer_load_dwordx4 v12, s[28:31], s81 offen offset:0 lds\n" \
"v_mfma_f32_16x16x32_bf16 v[60:63], a[20:23], v[120:123], v[60:63]\n" \
"s_add_u32 m0, s79, 25472\n" \
"ds_read_b128 v[116:119], v29 offset:30720\n" \
"v_mfma_f32_16x16x32_bf16 v[60:63], a[28:31], v[124:127], v[60:63]\n" \
"s_waitcnt vmcnt(16) lgkmcnt(6)\n" \
"s_barrier\n" \
"ds_read_b64_tr_b16 a[38:39], v183 offset:0\n" \
"v_mfma_f32_16x16x32_bf16 v[64:67], a[16:19], v[96:99], v[64:67]\n" \
"ds_read_b128 v[120:123], v29 offset:29696\n" \
"v_mfma_f32_16x16x32_bf16 v[64:67], a[24:27], v[100:103], v[64:67]\n" \
"buffer_load_dwordx4 v12, s[28:31], s81 offen offset:128 lds\n" \
"v_mfma_f32_16x16x32_bf16 v[68:71], a[20:23], v[96:99], v[68:71]\n" \
"s_waitcnt lgkmcnt(5)\n" \
"ds_read_b128 v[124:127], v29 offset:31744\n" \
"s_mov_b32 s81, 1966080\n" \
"v_mfma_f32_16x16x32_bf16 v[68:71], a[28:31], v[100:103], v[68:71]\n" \
"s_add_u32 m0, s79, 32768\n" \
"ds_read_b64_tr_b16 a[40:41], v180 offset:8192\n" \
"v_mfma_f32_16x16x32_bf16 v[72:75], a[16:19], v[104:107], v[72:75]\n" \
"ds_read_b128 v[96:99], v30 offset:16384\n" \
"v_mfma_f32_16x16x32_bf16 v[72:75], a[24:27], v[108:111], v[72:75]\n" \
"buffer_load_dwordx4 v12, s[28:31], s81 offen offset:0 lds\n" \
"v_mfma_f32_16x16x32_bf16 v[76:79], a[20:23], v[104:107], v[76:79]\n" \
"s_waitcnt lgkmcnt(5)\n" \
"ds_read_b128 v[100:103], v30 offset:18432\n" \
"s_add_u32 m0, s79, 33664\n" \
"v_mfma_f32_16x16x32_bf16 v[76:79], a[28:31], v[108:111], v[76:79]\n" \
"ds_read_b64_tr_b16 a[42:43], v181 offset:8192\n" \
"v_mfma_f32_16x16x32_bf16 v[80:83], a[16:19], v[112:115], v[80:83]\n" \
"ds_read_b128 v[104:107], v30 offset:17408\n" \
"v_mfma_f32_16x16x32_bf16 v[80:83], a[24:27], v[116:119], v[80:83]\n" \
"buffer_load_dwordx4 v12, s[28:31], s81 offen offset:128 lds\n" \
"v_mfma_f32_16x16x32_bf16 v[84:87], a[20:23], v[112:115], v[84:87]\n" \
"s_waitcnt lgkmcnt(5)\n" \
"ds_read_b128 v[108:111], v30 offset:19456\n" \
"s_mov_b32 s81, 2031616\n" \
"ds_read_b64_tr_b16 a[44:45], v182 offset:8192\n" \
"ds_read_b64_tr_b16 a[46:47], v183 offset:8192\n" \
"v_mfma_f32_16x16x32_bf16 v[84:87], a[28:31], v[116:119], v[84:87]\n" \
"s_add_u32 m0, s79, 40960\n" \
"v_mfma_f32_16x16x32_bf16 v[88:91], a[16:19], v[120:123], v[88:91]\n" \
"ds_read_b128 v[112:115], v30 offset:20480\n" \
"v_mfma_f32_16x16x32_bf16 v[88:91], a[24:27], v[124:127], v[88:91]\n" \
"buffer_load_dwordx4 v12, s[28:31], s81 offen offset:0 lds\n" \
"v_mfma_f32_16x16x32_bf16 v[92:95], a[20:23], v[120:123], v[92:95]\n" \
"s_add_u32 m0, s79, 41856\n" \
"ds_read_b128 v[116:119], v30 offset:22528\n" \
"v_mfma_f32_16x16x32_bf16 v[92:95], a[28:31], v[124:127], v[92:95]\n" \
"s_waitcnt vmcnt(12) lgkmcnt(2)\n" \
"s_barrier\n" \
"v_mfma_f32_16x16x32_bf16 v[32:35], a[32:35], v[96:99], v[32:35]\n" \
"ds_read_b128 v[120:123], v30 offset:21504\n" \
"v_mfma_f32_16x16x32_bf16 v[32:35], a[40:43], v[100:103], v[32:35]\n" \
"buffer_load_dwordx4 v12, s[28:31], s81 offen offset:128 lds\n" \
"v_mfma_f32_16x16x32_bf16 v[36:39], a[36:39], v[96:99], v[36:39]\n" \
"s_waitcnt lgkmcnt(5)\n" \
"ds_read_b128 v[124:127], v30 offset:23552\n" \
"s_add_u32 s80, s66, 49152\n" \
"v_mfma_f32_16x16x32_bf16 v[36:39], a[44:47], v[100:103], v[36:39]\n" \
"s_add_u32 m0, s22, 98304\n" \
"ds_read_b64_tr_b16 a[48:49], v180 offset:49152\n" \
"v_mfma_f32_16x16x32_bf16 v[40:43], a[32:35], v[104:107], v[40:43]\n" \
"ds_read_b128 v[96:99], v30 offset:24576\n" \
"v_mfma_f32_16x16x32_bf16 v[40:43], a[40:43], v[108:111], v[40:43]\n" \
"buffer_load_dwordx4 v4, s[36:39], s80 offen offset:0 sc1 lds\n" \
"v_mfma_f32_16x16x32_bf16 v[44:47], a[36:39], v[104:107], v[44:47]\n" \
"s_waitcnt lgkmcnt(4)\n" \
"ds_read_b128 v[100:103], v30 offset:26624\n" \
"v_mfma_f32_16x16x32_bf16 v[44:47], a[44:47], v[108:111], v[44:47]\n" \
"ds_read_b64_tr_b16 a[50:51], v181 offset:49152\n" \
"v_mfma_f32_16x16x32_bf16 v[48:51], a[32:35], v[112:115], v[48:51]\n" \
"ds_read_b128 v[104:107], v30 offset:25600\n" \
"v_mfma_f32_16x16x32_bf16 v[48:51], a[40:43], v[116:119], v[48:51]\n" \
"buffer_load_dwordx4 v4, s[36:39], s80 offen offset:1024 sc1 lds\n" \
"v_mfma_f32_16x16x32_bf16 v[52:55], a[36:39], v[112:115], v[52:55]\n" \
"s_waitcnt lgkmcnt(5)\n" \
"ds_read_b128 v[108:111], v30 offset:27648\n" \
"v_mfma_f32_16x16x32_bf16 v[52:55], a[44:47], v[116:119], v[52:55]\n" \
"ds_read_b64_tr_b16 a[52:53], v182 offset:49152\n" \
"v_mfma_f32_16x16x32_bf16 v[56:59], a[32:35], v[120:123], v[56:59]\n" \
"ds_read_b128 v[112:115], v28 offset:0\n" \
"v_mfma_f32_16x16x32_bf16 v[56:59], a[40:43], v[124:127], v[56:59]\n" \
"buffer_load_dwordx4 v4, s[36:39], s80 offen offset:2048 sc1 lds\n" \
"v_mfma_f32_16x16x32_bf16 v[60:63], a[36:39], v[120:123], v[60:63]\n" \
"ds_read_b128 v[116:119], v28 offset:2048\n" \
"v_mfma_f32_16x16x32_bf16 v[60:63], a[44:47], v[124:127], v[60:63]\n" \
"s_waitcnt vmcnt(12) lgkmcnt(6)\n" \
"s_barrier\n" \
"ds_read_b64_tr_b16 a[54:55], v183 offset:49152\n" \
"v_mfma_f32_16x16x32_bf16 v[64:67], a[32:35], v[96:99], v[64:67]\n" \
"ds_read_b128 v[120:123], v28 offset:1024\n" \
"s_waitcnt vmcnt(9)\n" \
"v_mfma_f32_16x16x32_bf16 v[64:67], a[40:43], v[100:103], v[64:67]\n" \
"buffer_load_dwordx4 v4, s[36:39], s80 offen offset:3072 sc1 lds\n" \
"v_mfma_f32_16x16x32_bf16 v[68:71], a[36:39], v[96:99], v[68:71]\n" \
"s_waitcnt lgkmcnt(5)\n" \
"ds_read_b128 v[124:127], v28 offset:3072\n" \
"v_cmp_ne_u64 vcc, s[14:15], v[18:19]\n" \
"v_mfma_f32_16x16x32_bf16 v[68:71], a[44:47], v[100:103], v[68:71]\n" \
"s_nop 0\n" \
"s_cbranch_vccnz LPL_%=\nLPLR_%=:\n" \
"v_mfma_f32_16x16x32_bf16 v[72:75], a[32:35], v[104:107], v[72:75]\n" \
"s_add_u32 s80, s68, 0\n" \
"ds_read_b128 v[96:99], v30 offset:28672\n" \
"s_add_u32 m0, s22, 49152\n" \
"v_mfma_f32_16x16x32_bf16 v[72:75], a[40:43], v[108:111], v[72:75]\n" \
"ds_read_b64_tr_b16 a[56:57], v180 offset:57344\n" \
"v_mfma_f32_16x16x32_bf16 v[76:79], a[36:39], v[104:107], v[76:79]\n" \
"s_waitcnt lgkmcnt(5)\n" \
"ds_read_b128 v[100:103], v30 offset:30720\n" \
"v_mfma_f32_16x16x32_bf16 v[76:79], a[44:47], v[108:111], v[76:79]\n" \
"buffer_load_dwordx4 v31, s[36:39], s80 offen offset:0 sc1 lds\n" \
"v_mfma_f32_16x16x32_bf16 v[80:83], a[32:35], v[112:115], v[80:83]\n" \
"ds_read_b128 v[104:107], v30 offset:29696\n" \
"v_mfma_f32_16x16x32_bf16 v[80:83], a[40:43], v[116:119], v[80:83]\n" \
"ds_read_b64_tr_b16 a[58:59], v181 offset:57344\n" \
"v_mfma_f32_16x16x32_bf16 v[84:87], a[36:39], v[112:115], v[84:87]\n" \
"s_waitcnt lgkmcnt(5)\n" \
"ds_read_b128 v[108:111], v30 offset:31744\n" \
"ds_read_b64_tr_b16 a[60:61], v182 offset:57344\n" \
"ds_read_b64_tr_b16 a[62:63], v183 offset:57344\n" \
"v_mfma_f32_16x16x32_bf16 v[84:87], a[44:47], v[116:119], v[84:87]\n" \
"buffer_load_dwordx4 v31, s[36:39], s80 offen offset:1024 sc1 lds\n" \
"v_mfma_f32_16x16x32_bf16 v[88:91], a[32:35], v[120:123], v[88:91]\n" \
"ds_read_b128 v[112:115], v30 offset:0\n" \
"v_mfma_f32_16x16x32_bf16 v[88:91], a[40:43], v[124:127], v[88:91]\n" \
"v_mfma_f32_16x16x32_bf16 v[92:95], a[36:39], v[120:123], v[92:95]\n" \
"ds_read_b128 v[116:119], v30 offset:2048\n" \
"v_mfma_f32_16x16x32_bf16 v[92:95], a[44:47], v[124:127], v[92:95]\n" \
"s_waitcnt vmcnt(10) lgkmcnt(2)\n" \
"s_barrier\n" \
"v_mfma_f32_16x16x32_bf16 v[32:35], a[48:51], v[96:99], v[32:35]\n" \
"ds_read_b128 v[120:123], v30 offset:1024\n" \
"v_mfma_f32_16x16x32_bf16 v[32:35], a[56:59], v[100:103], v[32:35]\n" \
"buffer_load_dwordx4 v31, s[36:39], s80 offen offset:2048 sc1 lds\n" \
"v_mfma_f32_16x16x32_bf16 v[36:39], a[52:55], v[96:99], v[36:39]\n" \
"s_waitcnt lgkmcnt(5)\n" \
"ds_read_b128 v[124:127], v30 offset:3072\n" \
"v_mfma_f32_16x16x32_bf16 v[36:39], a[60:63], v[100:103], v[36:39]\n" \
"ds_read_b64_tr_b16 a[96:97], v176 offset:16384\n" \
"v_mfma_f32_16x16x32_bf16 v[40:43], a[48:51], v[104:107], v[40:43]\n" \
"ds_read_b128 v[96:99], v30 offset:4096\n" \
"v_mfma_f32_16x16x32_bf16 v[40:43], a[56:59], v[108:111], v[40:43]\n" \
"buffer_load_dwordx4 v31, s[36:39], s80 offen offset:3072 sc1 lds\n" \
"v_mfma_f32_16x16x32_bf16 v[44:47], a[52:55], v[104:107], v[44:47]\n" \
"s_waitcnt lgkmcnt(4)\n" \
"ds_read_b128 v[100:103], v30 offset:6144\n" \
"s_add_u32 s80, s68, 16384\n" \
"v_mfma_f32_16x16x32_bf16 v[44:47], a[60:63], v[108:111], v[44:47]\n" \
"s_add_u32 m0, s22, 65536\n" \
"ds_read_b64_tr_b16 a[98:99], v177 offset:16384\n" \
"v_mfma_f32_16x16x32_bf16 v[48:51], a[48:51], v[112:115], v[48:51]\n" \
"ds_read_b128 v[104:107], v30 offset:5120\n" \
"v_mfma_f32_16x16x32_bf16 v[48:51], a[56:59], v[116:119], v[48:51]\n" \
"buffer_load_dwordx4 v31, s[36:39], s80 offen offset:0 sc1 lds\n" \
"v_mfma_f32_16x16x32_bf16 v[52:55], a[52:55], v[112:115], v[52:55]\n" \
"s_waitcnt lgkmcnt(5)\n" \
"ds_read_b128 v[108:111], v30 offset:7168\n" \
"v_mfma_f32_16x16x32_bf16 v[52:55], a[60:63], v[116:119], v[52:55]\n" \
"ds_read_b64_tr_b16 a[100:101], v178 offset:16384\n" \
"v_mfma_f32_16x16x32_bf16 v[56:59], a[48:51], v[120:123], v[56:59]\n" \
"ds_read_b128 v[112:115], v28 offset:4096\n" \
"v_mfma_f32_16x16x32_bf16 v[56:59], a[56:59], v[124:127], v[56:59]\n" \
"buffer_load_dwordx4 v31, s[36:39], s80 offen offset:1024 sc1 lds\n" \
"v_mfma_f32_16x16x32_bf16 v[60:63], a[52:55], v[120:123], v[60:63]\n" \
"ds_read_b128 v[116:119], v28 offset:6144\n" \
"v_mfma_f32_16x16x32_bf16 v[60:63], a[60:63], v[124:127], v[60:63]\n" \
"s_waitcnt lgkmcnt(6)\n" \
"s_barrier\n" \
"ds_read_b64_tr_b16 a[102:103], v179 offset:16384\n" \
"v_mfma_f32_16x16x32_bf16 v[64:67], a[48:51], v[96:99], v[64:67]\n" \
"ds_read_b128 v[120:123], v28 offset:5120\n" \
"v_mfma_f32_16x16x32_bf16 v[64:67], a[56:59], v[100:103], v[64:67]\n" \
"buffer_load_dwordx4 v31, s[36:39], s80 offen offset:2048 sc1 lds\n" \
"v_mfma_f32_16x16x32_bf16 v[68:71], a[52:55], v[96:99], v[68:71]\n" \
"s_waitcnt lgkmcnt(5)\n" \
"ds_read_b128 v[124:127], v28 offset:7168\n" \
"v_mfma_f32_16x16x32_bf16 v[68:71], a[60:63], v[100:103], v[68:71]\n" \
"ds_read_b64_tr_b16 a[104:105], v176 offset:24576\n" \
"v_mfma_f32_16x16x32_bf16 v[72:75], a[48:51], v[104:107], v[72:75]\n" \
"ds_read_b128 v[96:99], v30 offset:8192\n" \
"v_mfma_f32_16x16x32_bf16 v[72:75], a[56:59], v[108:111], v[72:75]\n" \
"buffer_load_dwordx4 v31, s[36:39], s80 offen offset:3072 sc1 lds\n" \
"v_mfma_f32_16x16x32_bf16 v[76:79], a[52:55], v[104:107], v[76:79]\n" \
"s_waitcnt lgkmcnt(5)\n" \
"ds_read_b128 v[100:103], v30 offset:10240\n" \
"s_add_u32 s80, s68, 32768\n" \
"v_mfma_f32_16x16x32_bf16 v[76:79], a[60:63], v[108:111], v[76:79]\n" \
"s_add_u32 m0, s22, 81920\n" \
"ds_read_b64_tr_b16 a[106:107], v177 offset:24576\n" \
"v_mfma_f32_16x16x32_bf16 v[80:83], a[48:51], v[112:115], v[80:83]\n" \
"ds_read_b128 v[104:107], v30 offset:9216\n" \
"v_mfma_f32_16x16x32_bf16 v[80:83], a[56:59], v[116:119], v[80:83]\n" \
"buffer_load_dwordx4 v31, s[36:39], s80 offen offset:0 sc1 lds\n" \
"v_mfma_f32_16x16x32_bf16 v[84:87], a[52:55], v[112:115], v[84:87]\n" \
"s_waitcnt lgkmcnt(5)\n" \
"ds_read_b128 v[108:111], v30 offset:11264\n" \
"ds_read_b64_tr_b16 a[108:109], v178 offset:24576\n" \
"ds_read_b64_tr_b16 a[110:111], v179 offset:24576\n" \
"v_mfma_f32_16x16x32_bf16 v[84:87], a[60:63], v[116:119], v[84:87]\n" \
"v_mfma_f32_16x16x32_bf16 v[88:91], a[48:51], v[120:123], v[88:91]\n" \
"ds_read_b128 v[112:115], v30 offset:12288\n" \
"v_mfma_f32_16x16x32_bf16 v[88:91], a[56:59], v[124:127], v[88:91]\n" \
"buffer_load_dwordx4 v31, s[36:39], s80 offen offset:1024 sc1 lds\n" \
"v_mfma_f32_16x16x32_bf16 v[92:95], a[52:55], v[120:123], v[92:95]\n" \
"ds_read_b128 v[116:119], v30 offset:14336\n" \
"v_mfma_f32_16x16x32_bf16 v[92:95], a[60:63], v[124:127], v[92:95]\n" \
"s_waitcnt vmcnt(10) lgkmcnt(2)\n" \
"s_barrier\n" \
"v_mfma_f32_16x16x32_bf16 v[32:35], a[96:99], v[96:99], v[32:35]\n" \
"ds_read_b128 v[120:123], v30 offset:13312\n" \
"v_mfma_f32_16x16x32_bf16 v[32:35], a[104:107], v[100:103], v[32:35]\n" \
"buffer_load_dwordx4 v31, s[36:39], s80 offen offset:2048 sc1 lds\n" \
"v_mfma_f32_16x16x32_bf16 v[36:39], a[100:103], v[96:99], v[36:39]\n" \
"s_waitcnt lgkmcnt(5)\n" \
"ds_read_b128 v[124:127], v30 offset:15360\n" \
"v_mfma_f32_16x16x32_bf16 v[36:39], a[108:111], v[100:103], v[36:39]\n" \
"ds_read_b64_tr_b16 a[112:113], v176 offset:32768\n" \
"v_mfma_f32_16x16x32_bf16 v[40:43], a[96:99], v[104:107], v[40:43]\n" \
"ds_read_b128 v[96:99], v29 offset:32768\n" \
"v_mfma_f32_16x16x32_bf16 v[40:43], a[104:107], v[108:111], v[40:43]\n" \
"buffer_load_dwordx4 v31, s[36:39], s80 offen offset:3072 sc1 lds\n" \
"v_mfma_f32_16x16x32_bf16 v[44:47], a[100:103], v[104:107], v[44:47]\n" \
"s_waitcnt lgkmcnt(4)\n" \
"ds_read_b128 v[100:103], v29 offset:34816\n" \
"s_add_u32 s80, s68, 49152\n" \
"v_mfma_f32_16x16x32_bf16 v[44:47], a[108:111], v[108:111], v[44:47]\n" \
"s_add_u32 m0, s22, 114688\n" \
"ds_read_b64_tr_b16 a[114:115], v177 offset:32768\n" \
"v_mfma_f32_16x16x32_bf16 v[48:51], a[96:99], v[112:115], v[48:51]\n" \
"ds_read_b128 v[104:107], v29 offset:33792\n" \
"v_mfma_f32_16x16x32_bf16 v[48:51], a[104:107], v[116:119], v[48:51]\n" \
"buffer_load_dwordx4 v31, s[36:39], s80 offen offset:0 sc1 lds\n" \
"v_mfma_f32_16x16x32_bf16 v[52:55], a[100:103], v[112:115], v[52:55]\n" \
"s_waitcnt lgkmcnt(5)\n" \
"ds_read_b128 v[108:111], v29 offset:35840\n" \
"v_mfma_f32_16x16x32_bf16 v[52:55], a[108:111], v[116:119], v[52:55]\n" \
"ds_read_b64_tr_b16 a[116:117], v178 offset:32768\n" \
"v_mfma_f32_16x16x32_bf16 v[56:59], a[96:99], v[120:123], v[56:59]\n" \
"ds_read_b128 v[112:115], v28 offset:8192\n" \
"v_mfma_f32_16x16x32_bf16 v[56:59], a[104:107], v[124:127], v[56:59]\n" \
"buffer_load_dwordx4 v31, s[36:39], s80 offen offset:1024 sc1 lds\n" \
"v_mfma_f32_16x16x32_bf16 v[60:63], a[100:103], v[120:123], v[60:63]\n" \
"ds_read_b128 v[116:119], v28 offset:10240\n" \
"v_mfma_f32_16x16x32_bf16 v[60:63], a[108:111], v[124:127], v[60:63]\n" \
"s_waitcnt lgkmcnt(6)\n" \
"s_barrier\n" \
"ds_read_b64_tr_b16 a[118:119], v179 offset:32768\n" \
"v_mfma_f32_16x16x32_bf16 v[64:67], a[96:99], v[96:99], v[64:67]\n" \
"ds_read_b128 v[120:123], v28 offset:9216\n" \
"v_mfma_f32_16x16x32_bf16 v[64:67], a[104:107], v[100:103], v[64:67]\n" \
"buffer_load_dwordx4 v31, s[36:39], s80 offen offset:2048 sc1 lds\n" \
"v_mfma_f32_16x16x32_bf16 v[68:71], a[100:103], v[96:99], v[68:71]\n" \
"s_waitcnt lgkmcnt(5)\n" \
"ds_read_b128 v[124:127], v28 offset:11264\n" \
"v_mfma_f32_16x16x32_bf16 v[68:71], a[108:111], v[100:103], v[68:71]\n" \
"ds_read_b64_tr_b16 a[120:121], v176 offset:40960\n" \
"v_mfma_f32_16x16x32_bf16 v[72:75], a[96:99], v[104:107], v[72:75]\n" \
"ds_read_b128 v[96:99], v29 offset:36864\n" \
"v_mfma_f32_16x16x32_bf16 v[72:75], a[104:107], v[108:111], v[72:75]\n" \
"buffer_load_dwordx4 v31, s[36:39], s80 offen offset:3072 sc1 lds\n" \
"v_mfma_f32_16x16x32_bf16 v[76:79], a[100:103], v[104:107], v[76:79]\n" \
"s_waitcnt lgkmcnt(5)\n" \
"ds_read_b128 v[100:103], v29 offset:38912\n" \
"s_add_u32 s80, s69, 0\n" \
"v_mfma_f32_16x16x32_bf16 v[76:79], a[108:111], v[108:111], v[76:79]\n" \
"s_add_u32 m0, s22, 147456\n" \
"ds_read_b64_tr_b16 a[122:123], v177 offset:40960\n" \
"v_mfma_f32_16x16x32_bf16 v[80:83], a[96:99], v[112:115], v[80:83]\n" \
"ds_read_b128 v[104:107], v29 offset:37888\n" \
"v_mfma_f32_16x16x32_bf16 v[80:83], a[104:107], v[116:119], v[80:83]\n" \
"buffer_load_dwordx4 v31, s[36:39], s80 offen offset:0 sc1 lds\n" \
"v_mfma_f32_16x16x32_bf16 v[84:87], a[100:103], v[112:115], v[84:87]\n" \
"s_waitcnt lgkmcnt(5)\n" \
"ds_read_b128 v[108:111], v29 offset:39936\n" \
"ds_read_b64_tr_b16 a[124:125], v178 offset:40960\n" \
"ds_read_b64_tr_b16 a[126:127], v179 offset:40960\n" \
"v_mfma_f32_16x16x32_bf16 v[84:87], a[108:111], v[116:119], v[84:87]\n" \
"v_mfma_f32_16x16x32_bf16 v[88:91], a[96:99], v[120:123], v[88:91]\n" \
"ds_read_b128 v[112:115], v29 offset:40960\n" \
"v_mfma_f32_16x16x32_bf16 v[88:91], a[104:107], v[124:127], v[88:91]\n" \
"buffer_load_dwordx4 v31, s[36:39], s80 offen offset:1024 sc1 lds\n" \
"v_mfma_f32_16x16x32_bf16 v[92:95], a[100:103], v[120:123], v[92:95]\n" \
"ds_read_b128 v[116:119], v29 offset:43008\n" \
"v_mfma_f32_16x16x32_bf16 v[92:95], a[108:111], v[124:127], v[92:95]\n" \
"s_waitcnt lgkmcnt(2)\n" \
"s_barrier\n" \
"v_mfma_f32_16x16x32_bf16 v[32:35], a[112:115], v[96:99], v[32:35]\n" \
"ds_read_b128 v[120:123], v29 offset:41984\n" \
"v_mfma_f32_16x16x32_bf16 v[32:35], a[120:123], v[100:103], v[32:35]\n" \
"buffer_load_dwordx2 v[176:177], v185, s[36:39], s86 offen offset:0 sc1\n" \
"v_mfma_f32_16x16x32_bf16 v[36:39], a[116:119], v[96:99], v[36:39]\n" \
"s_waitcnt lgkmcnt(5)\n" \
"ds_read_b128 v[124:127], v29 offset:44032\n" \
"v_mfma_f32_16x16x32_bf16 v[36:39], a[124:127], v[100:103], v[36:39]\n" \
"v_mfma_f32_16x16x32_bf16 v[40:43], a[112:115], v[104:107], v[40:43]\n" \
"ds_read_b128 v[96:99], v29 offset:45056\n" \
"v_mfma_f32_16x16x32_bf16 v[40:43], a[120:123], v[108:111], v[40:43]\n" \
"buffer_load_dwordx2 v[178:179], v185, s[36:39], s87 offen offset:0 sc1\n" \
"v_mfma_f32_16x16x32_bf16 v[44:47], a[116:119], v[104:107], v[44:47]\n" \
"s_waitcnt lgkmcnt(3)\n" \
"ds_read_b128 v[100:103], v29 offset:47104\n" \
"v_mfma_f32_16x16x32_bf16 v[44:47], a[124:127], v[108:111], v[44:47]\n" \
"v_mfma_f32_16x16x32_bf16 v[48:51], a[112:115], v[112:115], v[48:51]\n" \
"ds_read_b128 v[104:107], v29 offset:46080\n" \
"v_mfma_f32_16x16x32_bf16 v[48:51], a[120:123], v[116:119], v[48:51]\n" \
"buffer_load_dwordx4 v31, s[36:39], s80 offen offset:2048 sc1 lds\n" \
"v_mfma_f32_16x16x32_bf16 v[52:55], a[116:119], v[112:115], v[52:55]\n" \
"s_waitcnt lgkmcnt(3)\n" \
"ds_read_b128 v[108:111], v29 offset:48128\n" \
"v_mfma_f32_16x16x32_bf16 v[52:55], a[124:127], v[116:119], v[52:55]\n" \
"v_mfma_f32_16x16x32_bf16 v[56:59], a[112:115], v[120:123], v[56:59]\n" \
"ds_read_b128 v[112:115], v28 offset:12288\n" \
"v_mfma_f32_16x16x32_bf16 v[56:59], a[120:123], v[124:127], v[56:59]\n" \
"buffer_load_dwordx4 v31, s[36:39], s80 offen offset:3072 sc1 lds\n" \
"v_mfma_f32_16x16x32_bf16 v[60:63], a[116:119], v[120:123], v[60:63]\n" \
"s_add_u32 s80, s69, 16384\n" \
"ds_read_b128 v[116:119], v28 offset:14336\n" \
"s_add_u32 m0, s22, 16384\n" \
"v_mfma_f32_16x16x32_bf16 v[60:63], a[124:127], v[124:127], v[60:63]\n" \
"s_waitcnt vmcnt(18) lgkmcnt(4)\n" \
"s_barrier\n" \
"v_mfma_f32_16x16x32_bf16 v[64:67], a[112:115], v[96:99], v[64:67]\n" \
"ds_read_b128 v[120:123], v28 offset:13312\n" \
"v_mfma_f32_16x16x32_bf16 v[64:67], a[120:123], v[100:103], v[64:67]\n" \
"buffer_load_dwordx4 v31, s[36:39], s80 offen offset:0 sc1 lds\n" \
"v_mfma_f32_16x16x32_bf16 v[68:71], a[116:119], v[96:99], v[68:71]\n" \
"s_waitcnt lgkmcnt(3)\n" \
"ds_read_b128 v[124:127], v28 offset:15360\n" \
"v_mfma_f32_16x16x32_bf16 v[68:71], a[124:127], v[100:103], v[68:71]\n" \
"v_mfma_f32_16x16x32_bf16 v[72:75], a[112:115], v[104:107], v[72:75]\n" \
"ds_read_b128 v[96:99], v28 offset:49152\n" \
"v_mfma_f32_16x16x32_bf16 v[72:75], a[120:123], v[108:111], v[72:75]\n" \
"buffer_load_dwordx4 v31, s[36:39], s80 offen offset:1024 sc1 lds\n" \
"v_mfma_f32_16x16x32_bf16 v[76:79], a[116:119], v[104:107], v[76:79]\n" \
"s_waitcnt lgkmcnt(3)\n" \
"ds_read_b128 v[100:103], v28 offset:51200\n" \
"v_mfma_f32_16x16x32_bf16 v[76:79], a[124:127], v[108:111], v[76:79]\n" \
"v_mfma_f32_16x16x32_bf16 v[80:83], a[112:115], v[112:115], v[80:83]\n" \
"ds_read_b128 v[104:107], v28 offset:50176\n" \
"v_mfma_f32_16x16x32_bf16 v[80:83], a[120:123], v[116:119], v[80:83]\n" \
"buffer_load_dwordx4 v31, s[36:39], s80 offen offset:2048 sc1 lds\n" \
"v_mfma_f32_16x16x32_bf16 v[84:87], a[116:119], v[112:115], v[84:87]\n" \
"LH26_%=:\ns_cbranch_execz LHR2_%=\n" \
"s_waitcnt lgkmcnt(3)\n" \
"ds_read_b128 v[108:111], v28 offset:52224\n" \
"v_mfma_f32_16x16x32_bf16 v[84:87], a[124:127], v[116:119], v[84:87]\n" \
"v_mfma_f32_16x16x32_bf16 v[88:91], a[112:115], v[120:123], v[88:91]\n" \
"ds_read_b128 v[112:115], v28 offset:53248\n" \
"v_mfma_f32_16x16x32_bf16 v[88:91], a[120:123], v[124:127], v[88:91]\n" \
"buffer_load_dwordx4 v31, s[36:39], s80 offen offset:3072 sc1 lds\n" \
"v_mfma_f32_16x16x32_bf16 v[92:95], a[116:119], v[120:123], v[92:95]\n" \
"s_add_u32 s80, s69, 32768\n" \
"ds_read_b128 v[116:119], v28 offset:55296\n" \
"s_add_u32 m0, s22, 131072\n" \
"v_mfma_f32_16x16x32_bf16 v[92:95], a[124:127], v[124:127], v[92:95]\n" \
"s_waitcnt lgkmcnt(4)\n" \
"s_barrier\n" \
"v_mfma_f32_16x16x32_bf16 v[192:195], a[160:163], v[96:99], 0\n" \
"ds_read_b128 v[120:123], v28 offset:54272\n" \
"v_mfma_f32_16x16x32_bf16 v[192:195], a[168:171], v[100:103], v[192:195]\n" \
"buffer_load_dwordx4 v31, s[36:39], s80 offen offset:0 sc1 lds\n" \
"v_mfma_f32_16x16x32_bf16 v[196:199], a[164:167], v[96:99], 0\n" \
"s_waitcnt lgkmcnt(3)\n" \
"ds_read_b128 v[124:127], v28 offset:56320\n" \
"v_cvt_pk_bf16_f32 v32, v32, v33\n" \
"v_mfma_f32_16x16x32_bf16 v[196:199], a[172:175], v[100:103], v[196:199]\n" \
"v_cvt_pk_bf16_f32 v33, v34, v35\n" \
"v_cvt_pk_bf16_f32 v34, v36, v37\n" \
"v_mfma_f32_16x16x32_bf16 v[200:203], a[160:163], v[104:107], 0\n" \
"ds_read_b128 v[96:99], v28 offset:57344\n" \
"v_cvt_pk_bf16_f32 v35, v38, v39\n" \
"v_mfma_f32_16x16x32_bf16 v[200:203], a[168:171], v[108:111], v[200:203]\n" \
"buffer_store_dwordx4 v[32:35], v4, s[36:39], s72 offen offset:0 sc1\n" \
"v_mfma_f32_16x16x32_bf16 v[204:207], a[164:167], v[104:107], 0\n" \
"s_waitcnt lgkmcnt(3)\n" \
"ds_read_b128 v[100:103], v28 offset:59392\n" \
"v_cvt_pk_bf16_f32 v40, v40, v41\n" \
"v_mfma_f32_16x16x32_bf16 v[204:207], a[172:175], v[108:111], v[204:207]\n" \
"v_cvt_pk_bf16_f32 v41, v42, v43\n" \
"v_cvt_pk_bf16_f32 v42, v44, v45\n" \
"v_mfma_f32_16x16x32_bf16 v[192:195], a[176:179], v[112:115], v[192:195]\n" \
"ds_read_b128 v[104:107], v28 offset:58368\n" \
"v_cvt_pk_bf16_f32 v43, v46, v47\n" \
"v_mfma_f32_16x16x32_bf16 v[192:195], a[184:187], v[116:119], v[192:195]\n" \
"buffer_load_dwordx4 v31, s[36:39], s80 offen offset:1024 sc1 lds\n" \
"v_mfma_f32_16x16x32_bf16 v[196:199], a[180:183], v[112:115], v[196:199]\n" \
"s_waitcnt lgkmcnt(3)\n" \
"ds_read_b128 v[108:111], v28 offset:60416\n" \
"v_mfma_f32_16x16x32_bf16 v[196:199], a[188:191], v[116:119], v[196:199]\n" \
"v_mfma_f32_16x16x32_bf16 v[200:203], a[176:179], v[120:123], v[200:203]\n" \
"ds_read_b128 v[112:115], v28 offset:61440\n" \
"v_mfma_f32_16x16x32_bf16 v[200:203], a[184:187], v[124:127], v[200:203]\n" \
"buffer_store_dwordx4 v[40:43], v4, s[36:39], s72 offen offset:1024 sc1\n" \
"v_mfma_f32_16x16x32_bf16 v[204:207], a[180:183], v[120:123], v[204:207]\n" \
"ds_read_b128 v[116:119], v28 offset:63488\n" \
"v_mfma_f32_16x16x32_bf16 v[204:207], a[188:191], v[124:127], v[204:207]\n" \
"s_waitcnt vmcnt(22) lgkmcnt(4)\n" \
"s_barrier\n" \
"v_mfma_f32_16x16x32_bf16 v[192:195], a[192:195], v[96:99], v[192:195]\n" \
"ds_read_b128 v[120:123], v28 offset:62464\n" \
"v_mfma_f32_16x16x32_bf16 v[192:195], a[200:203], v[100:103], v[192:195]\n" \
"v_mfma_f32_16x16x32_bf16 v[196:199], a[196:199], v[96:99], v[196:199]\n" \
"s_waitcnt lgkmcnt(3)\n" \
"ds_read_b128 v[124:127], v28 offset:64512\n" \
"v_mfma_f32_16x16x32_bf16 v[196:199], a[204:207], v[100:103], v[196:199]\n" \
"v_mfma_f32_16x16x32_bf16 v[200:203], a[192:195], v[104:107], v[200:203]\n" \
"ds_read_b128 v[96:99], v29 offset:0\n" \
"v_mfma_f32_16x16x32_bf16 v[200:203], a[200:203], v[108:111], v[200:203]\n" \
"buffer_load_dwordx4 v31, s[36:39], s80 offen offset:2048 sc1 lds\n" \
"v_mfma_f32_16x16x32_bf16 v[204:207], a[196:199], v[104:107], v[204:207]\n" \
"s_waitcnt lgkmcnt(3)\n" \
"ds_read_b128 v[100:103], v29 offset:2048\n" \
"v_mfma_f32_16x16x32_bf16 v[204:207], a[204:207], v[108:111], v[204:207]\n" \
"s_waitcnt vmcnt(11)\n" \
"v_mfma_f32_16x16x32_bf16 v[192:195], a[208:211], v[112:115], v[192:195]\n" \
"ds_read_b128 v[104:107], v29 offset:1024\n" \
"v_mfma_f32_16x16x32_bf16 v[192:195], a[216:219], v[116:119], v[192:195]\n" \
"buffer_load_dwordx4 v31, s[36:39], s80 offen offset:3072 sc1 lds\n" \
"v_mfma_f32_16x16x32_bf16 v[196:199], a[212:215], v[112:115], v[196:199]\n" \
"s_waitcnt lgkmcnt(3)\n" \
"ds_read_b128 v[108:111], v29 offset:3072\n" \
"s_add_u32 s80, s69, 49152\n" \
"v_mfma_f32_16x16x32_bf16 v[196:199], a[220:223], v[116:119], v[196:199]\n" \
"s_add_u32 m0, s22, 32768\n" \
"v_mfma_f32_16x16x32_bf16 v[200:203], a[208:211], v[120:123], v[200:203]\n" \
"ds_read_b128 v[112:115], v29 offset:4096\n" \
"v_mfma_f32_16x16x32_bf16 v[200:203], a[216:219], v[124:127], v[200:203]\n" \
"buffer_load_dwordx4 v31, s[36:39], s80 offen offset:0 sc1 lds\n" \
"v_mfma_f32_16x16x32_bf16 v[204:207], a[212:215], v[120:123], v[204:207]\n" \
"s_waitcnt lgkmcnt(3)\n" \
"ds_read_b128 v[116:119], v29 offset:6144\n" \
"v_mfma_f32_16x16x32_bf16 v[204:207], a[220:223], v[124:127], v[204:207]\n" \
"v_mfma_f32_16x16x32_bf16 v[192:195], a[224:227], v[96:99], v[192:195]\n" \
"ds_read_b128 v[120:123], v29 offset:5120\n" \
"v_mfma_f32_16x16x32_bf16 v[192:195], a[232:235], v[100:103], v[192:195]\n" \
"buffer_load_dwordx4 v31, s[36:39], s80 offen offset:1024 sc1 lds\n" \
"v_mfma_f32_16x16x32_bf16 v[196:199], a[228:231], v[96:99], v[196:199]\n" \
"s_waitcnt lgkmcnt(3)\n" \
"ds_read_b128 v[124:127], v29 offset:7168\n" \
"v_mfma_f32_16x16x32_bf16 v[196:199], a[236:239], v[100:103], v[196:199]\n" \
"v_mfma_f32_16x16x32_bf16 v[200:203], a[224:227], v[104:107], v[200:203]\n" \
"ds_read_b128 v[96:99], v29 offset:8192\n" \
"v_mfma_f32_16x16x32_bf16 v[200:203], a[232:235], v[108:111], v[200:203]\n" \
"buffer_load_dwordx4 v31, s[36:39], s80 offen offset:2048 sc1 lds\n" \
"v_mfma_f32_16x16x32_bf16 v[204:207], a[228:231], v[104:107], v[204:207]\n" \
"s_waitcnt lgkmcnt(3)\n" \
"ds_read_b128 v[100:103], v29 offset:10240\n" \
"v_cvt_pk_bf16_f32 v48, v48, v49\n" \
"v_mfma_f32_16x16x32_bf16 v[204:207], a[236:239], v[108:111], v[204:207]\n" \
"v_cvt_pk_bf16_f32 v49, v50, v51\n" \
"v_cvt_pk_bf16_f32 v50, v52, v53\n" \
"v_mfma_f32_16x16x32_bf16 v[192:195], a[240:243], v[112:115], v[192:195]\n" \
"ds_read_b128 v[104:107], v29 offset:9216\n" \
"v_cvt_pk_bf16_f32 v51, v54, v55\n" \
"v_mfma_f32_16x16x32_bf16 v[192:195], a[248:251], v[116:119], v[192:195]\n" \
"buffer_store_dwordx4 v[48:51], v4, s[36:39], s73 offen offset:0 sc1\n" \
"v_mfma_f32_16x16x32_bf16 v[196:199], a[244:247], v[112:115], v[196:199]\n" \
"s_waitcnt lgkmcnt(3)\n" \
"ds_read_b128 v[108:111], v29 offset:11264\n" \
"v_cvt_pk_bf16_f32 v56, v56, v57\n" \
"v_mfma_f32_16x16x32_bf16 v[196:199], a[252:255], v[116:119], v[196:199]\n" \
"v_cvt_pk_bf16_f32 v57, v58, v59\n" \
"v_cvt_pk_bf16_f32 v58, v60, v61\n" \
"v_mfma_f32_16x16x32_bf16 v[200:203], a[240:243], v[120:123], v[200:203]\n" \
"ds_read_b128 v[112:115], v29 offset:12288\n" \
"v_cvt_pk_bf16_f32 v59, v62, v63\n" \
"v_mfma_f32_16x16x32_bf16 v[200:203], a[248:251], v[124:127], v[200:203]\n" \
"v_mfma_f32_16x16x32_bf16 v[204:207], a[244:247], v[120:123], v[204:207]\n" \
"ds_read_b128 v[116:119], v29 offset:14336\n" \
"v_mfma_f32_16x16x32_bf16 v[204:207], a[252:255], v[124:127], v[204:207]\n" \
"s_waitcnt vmcnt(24) lgkmcnt(4)\n" \
"s_barrier\n" \
"v_mfma_f32_16x16x32_bf16 v[192:195], a[64:67], v[96:99], v[192:195]\n" \
"ds_read_b128 v[120:123], v29 offset:13312\n" \
"v_mfma_f32_16x16x32_bf16 v[192:195], a[72:75], v[100:103], v[192:195]\n" \
"buffer_store_dwordx4 v[56:59], v4, s[36:39], s73 offen offset:1024 sc1\n" \
"v_mfma_f32_16x16x32_bf16 v[196:199], a[68:71], v[96:99], v[196:199]\n" \
"s_waitcnt lgkmcnt(3)\n" \
"ds_read_b128 v[124:127], v29 offset:15360\n" \
"v_permlane16_swap_b32 v144, v146\n" \
"v_mfma_f32_16x16x32_bf16 v[196:199], a[76:79], v[100:103], v[196:199]\n" \
"v_permlane16_swap_b32 v145, v147\n" \
"v_sub_f32 v5, 1.0, v176\n" \
"v_mfma_f32_16x16x32_bf16 v[200:203], a[64:67], v[104:107], v[200:203]\n" \
"ds_read_b128 v[96:99], v29 offset:16384\n" \
"v_cvt_pk_bf16_f32 v5, v5, v5\n" \
"v_mfma_f32_16x16x32_bf16 v[200:203], a[72:75], v[108:111], v[200:203]\n" \
"v_and_b32 v5, 0xffff0000, v5\n" \
"v_lshlrev_b32 v32, 16, v144\n" \
"v_mfma_f32_16x16x32_bf16 v[204:207], a[68:71], v[104:107], v[204:207]\n" \
"s_waitcnt lgkmcnt(3)\n" \
"ds_read_b128 v[100:103], v29 offset:18432\n" \
"v_and_b32 v33, 0xffff0000, v144\n" \
"v_mfma_f32_16x16x32_bf16 v[204:207], a[76:79], v[108:111], v[204:207]\n" \
"v_lshlrev_b32 v188, 16, v128\n" \
"v_and_b32 v189, 0xffff0000, v128\n" \
"v_mfma_f32_16x16x32_bf16 v[192:195], a[80:83], v[112:115], v[192:195]\n" \
"ds_read_b128 v[104:107], v29 offset:17408\n" \
"v_mul_f32 v32, v176, v32\n" \
"v_mfma_f32_16x16x32_bf16 v[192:195], a[88:91], v[116:119], v[192:195]\n" \
"buffer_load_dwordx4 v31, s[36:39], s80 offen offset:3072 sc1 lds\n" \
"v_mfma_f32_16x16x32_bf16 v[196:199], a[84:87], v[112:115], v[196:199]\n" \
"s_waitcnt lgkmcnt(3)\n" \
"ds_read_b128 v[108:111], v29 offset:19456\n" \
"s_add_u32 s80, s70, 0\n" \
"v_mfma_f32_16x16x32_bf16 v[196:199], a[92:95], v[116:119], v[196:199]\n" \
"s_add_u32 m0, s22, 0\n" \
"v_mul_f32 v33, v176, v33\n" \
"v_mul_f32 v32, v188, v32\n" \
"v_mfma_f32_16x16x32_bf16 v[200:203], a[80:83], v[120:123], v[200:203]\n" \
"ds_read_b128 v[112:115], v29 offset:20480\n" \
"v_mul_f32 v33, v189, v33\n" \
"v_mfma_f32_16x16x32_bf16 v[200:203], a[88:91], v[124:127], v[200:203]\n" \
"buffer_load_dwordx4 v31, s[36:39], s80 offen offset:0 sc1 lds\n" \
"v_mfma_f32_16x16x32_bf16 v[204:207], a[84:87], v[120:123], v[204:207]\n" \
"s_waitcnt lgkmcnt(3)\n" \
"ds_read_b128 v[116:119], v29 offset:22528\n" \
"v_lshlrev_b32 v34, 16, v145\n" \
"v_mfma_f32_16x16x32_bf16 v[204:207], a[92:95], v[124:127], v[204:207]\n" \
"v_and_b32 v35, 0xffff0000, v145\n" \
"v_lshlrev_b32 v188, 16, v129\n" \
"v_mfma_f32_16x16x32_bf16 v[192:195], a[128:131], v[96:99], v[192:195]\n" \
"ds_read_b128 v[120:123], v29 offset:21504\n" \
"v_and_b32 v189, 0xffff0000, v129\n" \
"v_mfma_f32_16x16x32_bf16 v[192:195], a[136:139], v[100:103], v[192:195]\n" \
"buffer_load_dwordx4 v31, s[36:39], s80 offen offset:1024 sc1 lds\n" \
"v_mfma_f32_16x16x32_bf16 v[196:199], a[132:135], v[96:99], v[196:199]\n" \
"s_waitcnt lgkmcnt(3)\n" \
"ds_read_b128 v[124:127], v29 offset:23552\n" \
"v_mul_f32 v34, v176, v34\n" \
"v_mfma_f32_16x16x32_bf16 v[196:199], a[140:143], v[100:103], v[196:199]\n" \
"v_mul_f32 v35, v176, v35\n" \
"v_mul_f32 v34, v188, v34\n" \
"v_mfma_f32_16x16x32_bf16 v[200:203], a[128:131], v[104:107], v[200:203]\n" \
"ds_read_b128 v[96:99], v29 offset:24576\n" \
"v_mul_f32 v35, v189, v35\n" \
"v_mfma_f32_16x16x32_bf16 v[200:203], a[136:139], v[108:111], v[200:203]\n" \
"buffer_load_dwordx4 v31, s[36:39], s80 offen offset:2048 sc1 lds\n" \
"v_mfma_f32_16x16x32_bf16 v[204:207], a[132:135], v[104:107], v[204:207]\n" \
"s_waitcnt lgkmcnt(3)\n" \
"ds_read_b128 v[100:103], v29 offset:26624\n" \
"v_lshlrev_b32 v36, 16, v146\n" \
"v_mfma_f32_16x16x32_bf16 v[204:207], a[140:143], v[108:111], v[204:207]\n" \
"v_and_b32 v37, 0xffff0000, v146\n" \
"v_lshlrev_b32 v188, 16, v132\n" \
"v_mfma_f32_16x16x32_bf16 v[192:195], a[144:147], v[112:115], v[192:195]\n" \
"ds_read_b128 v[104:107], v29 offset:25600\n" \
"v_and_b32 v189, 0xffff0000, v132\n" \
"v_mfma_f32_16x16x32_bf16 v[192:195], a[152:155], v[116:119], v[192:195]\n" \
"buffer_load_dwordx4 v31, s[36:39], s80 offen offset:3072 sc1 lds\n" \
"v_mfma_f32_16x16x32_bf16 v[196:199], a[148:151], v[112:115], v[196:199]\n" \
"s_waitcnt lgkmcnt(3)\n" \
"ds_read_b128 v[108:111], v29 offset:27648\n" \
"s_add_u32 s80, s70, 16384\n" \
"v_mfma_f32_16x16x32_bf16 v[196:199], a[156:159], v[116:119], v[196:199]\n" \
"s_add_u32 m0, s22, 98304\n" \
"s_cmp_eq_u32 s89, 3\ns_cbranch_scc1 LSW_%=\nLSWR_%=:\n" \
"v_mfma_f32_16x16x32_bf16 v[200:203], a[144:147], v[120:123], v[200:203]\n" \
"ds_read_b128 v[112:115], v29 offset:28672\n" \
"v_cvt_pk_bf16_f32 v80, v80, v81\n" \
"v_mfma_f32_16x16x32_bf16 v[200:203], a[152:155], v[124:127], v[200:203]\n" \
"buffer_load_dwordx4 v31, s[36:39], s80 offen offset:0 sc1 lds\n" \
"v_mfma_f32_16x16x32_bf16 v[204:207], a[148:151], v[120:123], v[204:207]\n" \
"ds_read_b128 v[116:119], v29 offset:30720\n" \
"v_cvt_pk_bf16_f32 v81, v82, v83\n" \
"v_mfma_f32_16x16x32_bf16 v[204:207], a[156:159], v[124:127], v[204:207]\n" \
"s_waitcnt vmcnt(27) lgkmcnt(4)\n" \
"s_barrier\n" \
"v_cvt_pk_bf16_f32 v82, v84, v85\n" \
"v_mfma_f32_16x16x32_bf16 v[192:195], a[0:3], v[96:99], v[192:195]\n" \
"ds_read_b128 v[120:123], v29 offset:29696\n" \
"v_cvt_pk_bf16_f32 v83, v86, v87\n" \
"v_mfma_f32_16x16x32_bf16 v[192:195], a[8:11], v[100:103], v[192:195]\n" \
"buffer_store_dwordx4 v[80:83], v4, s[36:39], s74 offen offset:0 sc1\n" \
"v_mfma_f32_16x16x32_bf16 v[196:199], a[4:7], v[96:99], v[196:199]\n" \
"s_waitcnt lgkmcnt(3)\n" \
"ds_read_b128 v[124:127], v29 offset:31744\n" \
"v_cvt_pk_bf16_f32 v88, v88, v89\n" \
"v_mfma_f32_16x16x32_bf16 v[196:199], a[12:15], v[100:103], v[196:199]\n" \
"s_waitcnt vmcnt(14)\n" \
"v_cvt_pk_bf16_f32 v89, v90, v91\n" \
"v_cvt_pk_bf16_f32 v90, v92, v93\n" \
"v_mfma_f32_16x16x32_bf16 v[200:203], a[0:3], v[104:107], v[200:203]\n" \
"ds_read_b128 v[96:99], v29 offset:49152\n" \
"v_cvt_pk_bf16_f32 v91, v94, v95\n" \
"v_mfma_f32_16x16x32_bf16 v[200:203], a[8:11], v[108:111], v[200:203]\n" \
"buffer_store_dwordx2 v[10:11], v20, s[36:39], s23 offen sc1\n" \
"v_mfma_f32_16x16x32_bf16 v[204:207], a[4:7], v[104:107], v[204:207]\n" \
"s_waitcnt lgkmcnt(3)\n" \
"ds_read_b128 v[100:103], v29 offset:51200\n" \
"v_mul_f32 v36, v176, v36\n" \
"v_mfma_f32_16x16x32_bf16 v[204:207], a[12:15], v[108:111], v[204:207]\n" \
"v_mul_f32 v37, v176, v37\n" \
"v_mul_f32 v36, v188, v36\n" \
"v_mfma_f32_16x16x32_bf16 v[192:195], a[16:19], v[112:115], v[192:195]\n" \
"ds_read_b128 v[104:107], v29 offset:50176\n" \
"v_mul_f32 v37, v189, v37\n" \
"v_mfma_f32_16x16x32_bf16 v[192:195], a[24:27], v[116:119], v[192:195]\n" \
"v_lshlrev_b32 v38, 16, v147\n" \
"v_and_b32 v39, 0xffff0000, v147\n" \
"v_mfma_f32_16x16x32_bf16 v[196:199], a[20:23], v[112:115], v[196:199]\n" \
"s_waitcnt lgkmcnt(3)\n" \
"ds_read_b128 v[108:111], v29 offset:52224\n" \
"v_lshlrev_b32 v188, 16, v133\n" \
"v_mfma_f32_16x16x32_bf16 v[196:199], a[28:31], v[116:119], v[196:199]\n" \
"buffer_store_dwordx4 v[88:91], v4, s[36:39], s74 offen offset:1024 sc1\n" \
"v_mfma_f32_16x16x32_bf16 v[200:203], a[16:19], v[120:123], v[200:203]\n" \
"ds_read_b128 v[112:115], v29 offset:53248\n" \
"v_and_b32 v189, 0xffff0000, v133\n" \
"v_mfma_f32_16x16x32_bf16 v[200:203], a[24:27], v[124:127], v[200:203]\n" \
"v_mul_f32 v38, v176, v38\n" \
"v_mul_f32 v39, v176, v39\n" \
"v_mfma_f32_16x16x32_bf16 v[204:207], a[20:23], v[120:123], v[204:207]\n" \
"s_waitcnt lgkmcnt(3)\n" \
"ds_read_b128 v[116:119], v29 offset:55296\n" \
"v_mul_f32 v38, v188, v38\n" \
"v_mfma_f32_16x16x32_bf16 v[204:207], a[28:31], v[124:127], v[204:207]\n" \
"v_mul_f32 v39, v189, v39\n" \
"v_permlane16_swap_b32 v148, v150\n" \
"v_mfma_f32_16x16x32_bf16 v[192:195], a[32:35], v[96:99], v[192:195]\n" \
"ds_read_b128 v[120:123], v29 offset:54272\n" \
"v_permlane16_swap_b32 v149, v151\n" \
"v_mfma_f32_16x16x32_bf16 v[192:195], a[40:43], v[100:103], v[192:195]\n" \
"v_sub_f32 v21, 1.0, v177\n" \
"v_cvt_pk_bf16_f32 v21, v21, v21\n" \
"v_mfma_f32_16x16x32_bf16 v[196:199], a[36:39], v[96:99], v[196:199]\n" \
"s_waitcnt lgkmcnt(3)\n" \
"ds_read_b128 v[124:127], v29 offset:56320\n" \
"v_and_b32 v21, 0xffff0000, v21\n" \
"v_mfma_f32_16x16x32_bf16 v[196:199], a[44:47], v[100:103], v[196:199]\n" \
"buffer_load_dwordx4 v31, s[36:39], s80 offen offset:1024 sc1 lds\n" \
"v_mfma_f32_16x16x32_bf16 v[200:203], a[32:35], v[104:107], v[200:203]\n" \
"ds_read_b128 v[96:99], v29 offset:57344\n" \
"v_lshlrev_b32 v40, 16, v148\n" \
"v_mfma_f32_16x16x32_bf16 v[200:203], a[40:43], v[108:111], v[200:203]\n" \
"v_and_b32 v41, 0xffff0000, v148\n" \
"v_lshlrev_b32 v188, 16, v130\n" \
"v_mfma_f32_16x16x32_bf16 v[204:207], a[36:39], v[104:107], v[204:207]\n" \
"s_waitcnt lgkmcnt(3)\n" \
"ds_read_b128 v[100:103], v29 offset:59392\n" \
"v_and_b32 v189, 0xffff0000, v130\n" \
"v_mfma_f32_16x16x32_bf16 v[204:207], a[44:47], v[108:111], v[204:207]\n" \
"buffer_load_dwordx4 v31, s[36:39], s80 offen offset:2048 sc1 lds\n" \
"v_mfma_f32_16x16x32_bf16 v[192:195], a[48:51], v[112:115], v[192:195]\n" \
"ds_read_b128 v[104:107], v29 offset:58368\n" \
"v_mul_f32 v40, v177, v40\n" \
"v_mfma_f32_16x16x32_bf16 v[192:195], a[56:59], v[116:119], v[192:195]\n" \
"v_mul_f32 v41, v177, v41\n" \
"v_mul_f32 v40, v188, v40\n" \
"v_mfma_f32_16x16x32_bf16 v[196:199], a[52:55], v[112:115], v[196:199]\n" \
"s_waitcnt lgkmcnt(3)\n" \
"ds_read_b128 v[108:111], v29 offset:60416\n" \
"v_mul_f32 v41, v189, v41\n" \
"v_mfma_f32_16x16x32_bf16 v[196:199], a[60:63], v[116:119], v[196:199]\n" \
"buffer_load_dwordx4 v31, s[36:39], s80 offen offset:3072 sc1 lds\n" \
"v_mfma_f32_16x16x32_bf16 v[200:203], a[48:51], v[120:123], v[200:203]\n" \
"s_add_u32 s80, s70, 32768\n" \
"ds_read_b128 v[112:115], v29 offset:61440\n" \
"s_add_u32 m0, s22, 49152\n" \
"v_mfma_f32_16x16x32_bf16 v[200:203], a[56:59], v[124:127], v[200:203]\n" \
"v_lshlrev_b32 v42, 16, v149\n" \
"v_and_b32 v43, 0xffff0000, v149\n" \
"v_mfma_f32_16x16x32_bf16 v[204:207], a[52:55], v[120:123], v[204:207]\n" \
"ds_read_b128 v[116:119], v29 offset:63488\n" \
"v_lshlrev_b32 v188, 16, v131\n" \
"v_mfma_f32_16x16x32_bf16 v[204:207], a[60:63], v[124:127], v[204:207]\n" \
"s_waitcnt vmcnt(27) lgkmcnt(4)\n" \
"s_barrier\n" \
"v_and_b32 v189, 0xffff0000, v131\n" \
"v_mfma_f32_16x16x32_bf16 v[192:195], a[96:99], v[96:99], v[192:195]\n" \
"ds_read_b128 v[120:123], v29 offset:62464\n" \
"v_mul_f32 v42, v177, v42\n" \
"v_mfma_f32_16x16x32_bf16 v[192:195], a[104:107], v[100:103], v[192:195]\n" \
"buffer_load_dwordx4 v31, s[36:39], s80 offen offset:0 sc1 lds\n" \
"v_mfma_f32_16x16x32_bf16 v[196:199], a[100:103], v[96:99], v[196:199]\n" \
"s_waitcnt lgkmcnt(3)\n" \
"ds_read_b128 v[124:127], v29 offset:64512\n" \
"v_mul_f32 v43, v177, v43\n" \
"v_mfma_f32_16x16x32_bf16 v[196:199], a[108:111], v[100:103], v[196:199]\n" \
"v_mul_f32 v42, v188, v42\n" \
"v_mul_f32 v43, v189, v43\n" \
"v_mfma_f32_16x16x32_bf16 v[200:203], a[96:99], v[104:107], v[200:203]\n" \
"ds_read_b128 v[96:99], v30 offset:16384\n" \
"v_lshlrev_b32 v44, 16, v150\n" \
"v_mfma_f32_16x16x32_bf16 v[200:203], a[104:107], v[108:111], v[200:203]\n" \
"buffer_load_dwordx4 v31, s[36:39], s80 offen offset:1024 sc1 lds\n" \
"v_mfma_f32_16x16x32_bf16 v[204:207], a[100:103], v[104:107], v[204:207]\n" \
"s_waitcnt lgkmcnt(3)\n" \
"ds_read_b128 v[100:103], v30 offset:18432\n" \
"v_and_b32 v45, 0xffff0000, v150\n" \
"v_mfma_f32_16x16x32_bf16 v[204:207], a[108:111], v[108:111], v[204:207]\n" \
"s_waitcnt vmcnt(14)\n" \
"v_lshlrev_b32 v188, 16, v134\n" \
"v_and_b32 v189, 0xffff0000, v134\n" \
"v_mfma_f32_16x16x32_bf16 v[192:195], a[112:115], v[112:115], v[192:195]\n" \
"ds_read_b128 v[104:107], v30 offset:17408\n" \
"v_mul_f32 v44, v177, v44\n" \
"v_mfma_f32_16x16x32_bf16 v[192:195], a[120:123], v[116:119], v[192:195]\n" \
"buffer_store_dwordx2 v[10:11], v20, s[36:39], s24 offen sc1\n" \
"v_mfma_f32_16x16x32_bf16 v[196:199], a[116:119], v[112:115], v[196:199]\n" \
"s_waitcnt lgkmcnt(3)\n" \
"ds_read_b128 v[108:111], v30 offset:19456\n" \
"v_mul_f32 v45, v177, v45\n" \
"v_mfma_f32_16x16x32_bf16 v[196:199], a[124:127], v[116:119], v[196:199]\n" \
"buffer_load_dwordx4 v31, s[36:39], s80 offen offset:2048 sc1 lds\n" \
"v_mfma_f32_16x16x32_bf16 v[200:203], a[112:115], v[120:123], v[200:203]\n" \
"ds_read_b128 v[112:115], v30 offset:20480\n" \
"v_mul_f32 v44, v188, v44\n" \
"v_mfma_f32_16x16x32_bf16 v[200:203], a[120:123], v[124:127], v[200:203]\n" \
"v_mul_f32 v45, v189, v45\n" \
"v_lshlrev_b32 v46, 16, v151\n" \
"v_mfma_f32_16x16x32_bf16 v[204:207], a[116:119], v[120:123], v[204:207]\n" \
"s_waitcnt lgkmcnt(3)\n" \
"ds_read_b128 v[116:119], v30 offset:22528\n" \
"v_and_b32 v47, 0xffff0000, v151\n" \
"v_mfma_f32_16x16x32_bf16 v[204:207], a[124:127], v[124:127], v[204:207]\n" \
"buffer_load_dwordx4 v31, s[36:39], s80 offen offset:3072 sc1 lds\n" \
"v_mfma_f32_16x16x32_bf16 v[208:211], a[160:163], v[96:99], 0\n" \
"s_add_u32 s80, s70, 49152\n" \
"ds_read_b128 v[120:123], v30 offset:21504\n" \
"s_add_u32 m0, s22, 65536\n" \
"v_mfma_f32_16x16x32_bf16 v[208:211], a[168:171], v[100:103], v[208:211]\n" \
"v_lshlrev_b32 v188, 16, v135\n" \
"v_and_b32 v189, 0xffff0000, v135\n" \
"v_mfma_f32_16x16x32_bf16 v[212:215], a[164:167], v[96:99], 0\n" \
"s_waitcnt lgkmcnt(3)\n" \
"ds_read_b128 v[124:127], v30 offset:23552\n" \
"v_cvt_pk_bf16_f32 v192, v192, v193\n" \
"v_mfma_f32_16x16x32_bf16 v[212:215], a[172:175], v[100:103], v[212:215]\n" \
"v_cvt_pk_bf16_f32 v193, v194, v195\n" \
"v_cvt_pk_bf16_f32 v194, v196, v197\n" \
"v_mfma_f32_16x16x32_bf16 v[216:219], a[160:163], v[104:107], 0\n" \
"ds_read_b128 v[96:99], v30 offset:24576\n" \
"v_cvt_pk_bf16_f32 v195, v198, v199\n" \
"v_mfma_f32_16x16x32_bf16 v[216:219], a[168:171], v[108:111], v[216:219]\n" \
"buffer_store_dwordx4 v[192:195], v4, s[36:39], s75 offen offset:0 sc1\n" \
"v_mfma_f32_16x16x32_bf16 v[220:223], a[164:167], v[104:107], 0\n" \
"s_waitcnt lgkmcnt(3)\n" \
"ds_read_b128 v[100:103], v30 offset:26624\n" \
"v_cvt_pk_bf16_f32 v200, v200, v201\n" \
"v_mfma_f32_16x16x32_bf16 v[220:223], a[172:175], v[108:111], v[220:223]\n" \
"v_cvt_pk_bf16_f32 v201, v202, v203\n" \
"v_cvt_pk_bf16_f32 v202, v204, v205\n" \
"v_mfma_f32_16x16x32_bf16 v[208:211], a[176:179], v[112:115], v[208:211]\n" \
"ds_read_b128 v[104:107], v30 offset:25600\n" \
"v_cvt_pk_bf16_f32 v203, v206, v207\n" \
"v_mfma_f32_16x16x32_bf16 v[208:211], a[184:187], v[116:119], v[208:211]\n" \
"buffer_load_dwordx4 v31, s[36:39], s80 offen offset:0 sc1 lds\n" \
"v_mfma_f32_16x16x32_bf16 v[212:215], a[180:183], v[112:115], v[212:215]\n" \
"s_waitcnt lgkmcnt(3)\n" \
"ds_read_b128 v[108:111], v30 offset:27648\n" \
"v_mul_f32 v46, v177, v46\n" \
"v_mfma_f32_16x16x32_bf16 v[212:215], a[188:191], v[116:119], v[212:215]\n" \
"v_mul_f32 v47, v177, v47\n" \
"v_mul_f32 v46, v188, v46\n" \
"v_mfma_f32_16x16x32_bf16 v[216:219], a[176:179], v[120:123], v[216:219]\n" \
"ds_read_b128 v[112:115], v30 offset:28672\n" \
"v_mul_f32 v47, v189, v47\n" \
"v_mfma_f32_16x16x32_bf16 v[216:219], a[184:187], v[124:127], v[216:219]\n" \
"buffer_store_dwordx4 v[200:203], v4, s[36:39], s75 offen offset:1024 sc1\n" \
"v_mfma_f32_16x16x32_bf16 v[220:223], a[180:183], v[120:123], v[220:223]\n" \
"ds_read_b128 v[116:119], v30 offset:30720\n" \
"v_permlane16_swap_b32 v152, v154\n" \
"v_mfma_f32_16x16x32_bf16 v[220:223], a[188:191], v[124:127], v[220:223]\n" \
"s_waitcnt vmcnt(31) lgkmcnt(4)\n" \
"s_barrier\n" \
"v_permlane16_swap_b32 v153, v155\n" \
"v_mfma_f32_16x16x32_bf16 v[208:211], a[192:195], v[96:99], v[208:211]\n" \
"ds_read_b128 v[120:123], v30 offset:29696\n" \
"v_sub_f32 v6, 1.0, v178\n" \
"v_mfma_f32_16x16x32_bf16 v[208:211], a[200:203], v[100:103], v[208:211]\n" \
"v_cvt_pk_bf16_f32 v6, v6, v6\n" \
"v_and_b32 v6, 0xffff0000, v6\n" \
"v_mfma_f32_16x16x32_bf16 v[212:215], a[196:199], v[96:99], v[212:215]\n" \
"s_waitcnt lgkmcnt(3)\n" \
"ds_read_b128 v[124:127], v30 offset:31744\n" \
"v_lshlrev_b32 v48, 16, v152\n" \
"v_mfma_f32_16x16x32_bf16 v[212:215], a[204:207], v[100:103], v[212:215]\n" \
"v_and_b32 v49, 0xffff0000, v152\n" \
"v_lshlrev_b32 v188, 16, v136\n" \
"v_mfma_f32_16x16x32_bf16 v[216:219], a[192:195], v[104:107], v[216:219]\n" \
"ds_read_b128 v[96:99], v28 offset:16384\n" \
"v_and_b32 v189, 0xffff0000, v136\n" \
"v_mfma_f32_16x16x32_bf16 v[216:219], a[200:203], v[108:111], v[216:219]\n" \
"buffer_load_dwordx4 v31, s[36:39], s80 offen offset:1024 sc1 lds\n" \
"v_mfma_f32_16x16x32_bf16 v[220:223], a[196:199], v[104:107], v[220:223]\n" \
"s_waitcnt lgkmcnt(3)\n" \
"ds_read_b128 v[100:103], v28 offset:18432\n" \
"v_mul_f32 v48, v178, v48\n" \
"v_mfma_f32_16x16x32_bf16 v[220:223], a[204:207], v[108:111], v[220:223]\n" \
"v_mul_f32 v49, v178, v49\n" \
"v_mul_f32 v48, v188, v48\n" \
"v_mfma_f32_16x16x32_bf16 v[208:211], a[208:211], v[112:115], v[208:211]\n" \
"ds_read_b128 v[104:107], v28 offset:17408\n" \
"v_mul_f32 v49, v189, v49\n" \
"v_mfma_f32_16x16x32_bf16 v[208:211], a[216:219], v[116:119], v[208:211]\n" \
"buffer_load_dwordx4 v31, s[36:39], s80 offen offset:2048 sc1 lds\n" \
"v_mfma_f32_16x16x32_bf16 v[212:215], a[212:215], v[112:115], v[212:215]\n" \
"s_waitcnt lgkmcnt(3)\n" \
"ds_read_b128 v[108:111], v28 offset:19456\n" \
"v_lshlrev_b32 v50, 16, v153\n" \
"v_mfma_f32_16x16x32_bf16 v[212:215], a[220:223], v[116:119], v[212:215]\n" \
"v_and_b32 v51, 0xffff0000, v153\n" \
"v_lshlrev_b32 v188, 16, v137\n" \
"v_mfma_f32_16x16x32_bf16 v[216:219], a[208:211], v[120:123], v[216:219]\n" \
"ds_read_b128 v[112:115], v28 offset:20480\n" \
"v_and_b32 v189, 0xffff0000, v137\n" \
"v_mfma_f32_16x16x32_bf16 v[216:219], a[216:219], v[124:127], v[216:219]\n" \
"buffer_load_dwordx4 v31, s[36:39], s80 offen offset:3072 sc1 lds\n" \
"v_mfma_f32_16x16x32_bf16 v[220:223], a[212:215], v[120:123], v[220:223]\n" \
"s_waitcnt lgkmcnt(3)\n" \
"ds_read_b128 v[116:119], v28 offset:22528\n" \
"v_mul_f32 v50, v178, v50\n" \
"v_mfma_f32_16x16x32_bf16 v[220:223], a[220:223], v[124:127], v[220:223]\n" \
"s_add_u32 s80, s71, 0\n" \
"s_add_u32 m0, s22, 81920\n" \
"v_mul_f32 v51, v178, v51\n" \
"v_mfma_f32_16x16x32_bf16 v[208:211], a[224:227], v[96:99], v[208:211]\n" \
"ds_read_b128 v[120:123], v28 offset:21504\n" \
"v_mul_f32 v50, v188, v50\n" \
"v_mfma_f32_16x16x32_bf16 v[208:211], a[232:235], v[100:103], v[208:211]\n" \
"s_waitcnt vmcnt(14)\n" \
"v_mul_f32 v51, v189, v51\n" \
"v_lshlrev_b32 v52, 16, v154\n" \
"v_mfma_f32_16x16x32_bf16 v[212:215], a[228:231], v[96:99], v[212:215]\n" \
"s_waitcnt lgkmcnt(3)\n" \
"ds_read_b128 v[124:127], v28 offset:23552\n" \
"v_and_b32 v53, 0xffff0000, v154\n" \
"v_mfma_f32_16x16x32_bf16 v[212:215], a[236:239], v[100:103], v[212:215]\n" \
"buffer_store_dwordx2 v[10:11], v20, s[36:39], s45 offen sc1\n" \
"v_mfma_f32_16x16x32_bf16 v[216:219], a[224:227], v[104:107], v[216:219]\n" \
"ds_read_b128 v[96:99], v28 offset:24576\n" \
"v_lshlrev_b32 v188, 16, v140\n" \
"v_mfma_f32_16x16x32_bf16 v[216:219], a[232:235], v[108:111], v[216:219]\n" \
"v_and_b32 v189, 0xffff0000, v140\n" \
"v_mul_f32 v52, v178, v52\n" \
"v_mfma_f32_16x16x32_bf16 v[220:223], a[228:231], v[104:107], v[220:223]\n" \
"s_waitcnt lgkmcnt(3)\n" \
"ds_read_b128 v[100:103], v28 offset:26624\n" \
"v_mul_f32 v53, v178, v53\n" \
"v_mfma_f32_16x16x32_bf16 v[220:223], a[236:239], v[108:111], v[220:223]\n" \
"buffer_load_dwordx4 v31, s[36:39], s80 offen offset:0 sc1 lds\n" \
"v_mfma_f32_16x16x32_bf16 v[208:211], a[240:243], v[112:115], v[208:211]\n" \
"ds_read_b128 v[104:107], v28 offset:25600\n" \
"v_mul_f32 v52, v188, v52\n" \
"v_mfma_f32_16x16x32_bf16 v[208:211], a[248:251], v[116:119], v[208:211]\n" \
"v_mul_f32 v53, v189, v53\n" \
"v_lshlrev_b32 v54, 16, v155\n" \
"v_mfma_f32_16x16x32_bf16 v[212:215], a[244:247], v[112:115], v[212:215]\n" \
"s_waitcnt lgkmcnt(3)\n" \
"ds_read_b128 v[108:111], v28 offset:27648\n" \
"v_and_b32 v55, 0xffff0000, v155\n" \
"v_mfma_f32_16x16x32_bf16 v[212:215], a[252:255], v[116:119], v[212:215]\n" \
"buffer_load_dwordx4 v31, s[36:39], s80 offen offset:1024 sc1 lds\n" \
"v_mfma_f32_16x16x32_bf16 v[216:219], a[240:243], v[120:123], v[216:219]\n" \
"ds_read_b128 v[112:115], v28 offset:28672\n" \
"v_lshlrev_b32 v188, 16, v141\n" \
"v_mfma_f32_16x16x32_bf16 v[216:219], a[248:251], v[124:127], v[216:219]\n" \
"v_and_b32 v189, 0xffff0000, v141\n" \
"v_mul_f32 v54, v178, v54\n" \
"v_mfma_f32_16x16x32_bf16 v[220:223], a[244:247], v[120:123], v[220:223]\n" \
"ds_read_b128 v[116:119], v28 offset:30720\n" \
"v_mul_f32 v55, v178, v55\n" \
"v_mfma_f32_16x16x32_bf16 v[220:223], a[252:255], v[124:127], v[220:223]\n" \
"s_waitcnt vmcnt(31) lgkmcnt(4)\n" \
"s_barrier\n" \
"v_mul_f32 v54, v188, v54\n" \
"v_mfma_f32_16x16x32_bf16 v[208:211], a[64:67], v[96:99], v[208:211]\n" \
"ds_read_b128 v[120:123], v28 offset:29696\n" \
"v_mul_f32 v55, v189, v55\n" \
"v_mfma_f32_16x16x32_bf16 v[208:211], a[72:75], v[100:103], v[208:211]\n" \
"buffer_load_dwordx4 v31, s[36:39], s80 offen offset:2048 sc1 lds\n" \
"v_mfma_f32_16x16x32_bf16 v[212:215], a[68:71], v[96:99], v[212:215]\n" \
"s_waitcnt lgkmcnt(3)\n" \
"ds_read_b128 v[124:127], v28 offset:31744\n" \
"v_permlane16_swap_b32 v156, v158\n" \
"v_mfma_f32_16x16x32_bf16 v[212:215], a[76:79], v[100:103], v[212:215]\n" \
"v_permlane16_swap_b32 v157, v159\n" \
"v_sub_f32 v8, 1.0, v179\n" \
"v_mfma_f32_16x16x32_bf16 v[216:219], a[64:67], v[104:107], v[216:219]\n" \
"ds_read_b128 v[96:99], v30 offset:0\n" \
"v_cvt_pk_bf16_f32 v8, v8, v8\n" \
"v_mfma_f32_16x16x32_bf16 v[216:219], a[72:75], v[108:111], v[216:219]\n" \
"buffer_load_dwordx4 v31, s[36:39], s80 offen offset:3072 sc1 lds\n" \
"v_mfma_f32_16x16x32_bf16 v[220:223], a[68:71], v[104:107], v[220:223]\n" \
"s_waitcnt lgkmcnt(3)\n" \
"ds_read_b128 v[100:103], v30 offset:2048\n" \
"v_and_b32 v8, 0xffff0000, v8\n" \
"v_mfma_f32_16x16x32_bf16 v[220:223], a[76:79], v[108:111], v[220:223]\n" \
"v_lshlrev_b32 v56, 16, v156\n" \
"v_and_b32 v57, 0xffff0000, v156\n" \
"v_mfma_f32_16x16x32_bf16 v[208:211], a[80:83], v[112:115], v[208:211]\n" \
"ds_read_b128 v[104:107], v30 offset:1024\n" \
"v_lshlrev_b32 v188, 16, v138\n" \
"v_mfma_f32_16x16x32_bf16 v[208:211], a[88:91], v[116:119], v[208:211]\n" \
"v_and_b32 v189, 0xffff0000, v138\n" \
"v_mul_f32 v56, v179, v56\n" \
"v_mfma_f32_16x16x32_bf16 v[212:215], a[84:87], v[112:115], v[212:215]\n" \
"s_waitcnt lgkmcnt(3)\n" \
"ds_read_b128 v[108:111], v30 offset:3072\n" \
"v_mul_f32 v57, v179, v57\n" \
"v_mfma_f32_16x16x32_bf16 v[212:215], a[92:95], v[116:119], v[212:215]\n" \
"v_mul_f32 v56, v188, v56\n" \
"v_mul_f32 v57, v189, v57\n" \
"v_mfma_f32_16x16x32_bf16 v[216:219], a[80:83], v[120:123], v[216:219]\n" \
"ds_read_b128 v[112:115], v30 offset:4096\n" \
"v_lshlrev_b32 v58, 16, v157\n" \
"v_mfma_f32_16x16x32_bf16 v[216:219], a[88:91], v[124:127], v[216:219]\n" \
"v_and_b32 v59, 0xffff0000, v157\n" \
"v_lshlrev_b32 v188, 16, v139\n" \
"v_mfma_f32_16x16x32_bf16 v[220:223], a[84:87], v[120:123], v[220:223]\n" \
"s_waitcnt lgkmcnt(3)\n" \
"ds_read_b128 v[116:119], v30 offset:6144\n" \
"v_and_b32 v189, 0xffff0000, v139\n" \
"v_mfma_f32_16x16x32_bf16 v[220:223], a[92:95], v[124:127], v[220:223]\n" \
"v_mul_f32 v58, v179, v58\n" \
"v_mul_f32 v59, v179, v59\n" \
"v_mfma_f32_16x16x32_bf16 v[208:211], a[128:131], v[96:99], v[208:211]\n" \
"ds_read_b128 v[120:123], v30 offset:5120\n" \
"v_mul_f32 v58, v188, v58\n" \
"v_mfma_f32_16x16x32_bf16 v[208:211], a[136:139], v[100:103], v[208:211]\n" \
"v_mul_f32 v59, v189, v59\n" \
"v_lshlrev_b32 v60, 16, v158\n" \
"v_mfma_f32_16x16x32_bf16 v[212:215], a[132:135], v[96:99], v[212:215]\n" \
"s_waitcnt lgkmcnt(3)\n" \
"ds_read_b128 v[124:127], v30 offset:7168\n" \
"v_and_b32 v61, 0xffff0000, v158\n" \
"v_mfma_f32_16x16x32_bf16 v[212:215], a[140:143], v[100:103], v[212:215]\n" \
"v_lshlrev_b32 v188, 16, v142\n" \
"v_and_b32 v189, 0xffff0000, v142\n" \
"v_mfma_f32_16x16x32_bf16 v[216:219], a[128:131], v[104:107], v[216:219]\n" \
"ds_read_b128 v[96:99], v30 offset:8192\n" \
"v_mul_f32 v60, v179, v60\n" \
"v_mfma_f32_16x16x32_bf16 v[216:219], a[136:139], v[108:111], v[216:219]\n" \
"v_mul_f32 v61, v179, v61\n" \
"v_mul_f32 v60, v188, v60\n" \
"v_mfma_f32_16x16x32_bf16 v[220:223], a[132:135], v[104:107], v[220:223]\n" \
"LH27_%=:\ns_cbranch_execz LHR2_%=\n" \
"s_waitcnt lgkmcnt(3)\n" \
"ds_read_b128 v[100:103], v30 offset:10240\n" \
"v_mul_f32 v61, v189, v61\n" \
"v_mfma_f32_16x16x32_bf16 v[220:223], a[140:143], v[108:111], v[220:223]\n" \
"v_lshlrev_b32 v62, 16, v159\n" \
"v_and_b32 v63, 0xffff0000, v159\n" \
"v_mfma_f32_16x16x32_bf16 v[208:211], a[144:147], v[112:115], v[208:211]\n" \
"ds_read_b128 v[104:107], v30 offset:9216\n" \
"v_lshlrev_b32 v188, 16, v143\n" \
"v_mfma_f32_16x16x32_bf16 v[208:211], a[152:155], v[116:119], v[208:211]\n" \
"v_and_b32 v189, 0xffff0000, v143\n" \
"v_mul_f32 v62, v179, v62\n" \
"v_mfma_f32_16x16x32_bf16 v[212:215], a[148:151], v[112:115], v[212:215]\n" \
"s_waitcnt lgkmcnt(3)\n" \
"ds_read_b128 v[108:111], v30 offset:11264\n" \
"v_mul_f32 v63, v179, v63\n" \
"v_mfma_f32_16x16x32_bf16 v[212:215], a[156:159], v[116:119], v[212:215]\n" \
"s_add_u32 s80, s71, 16384\n" \
"s_add_u32 m0, s22, 114688\n" \
"v_mul_f32 v62, v188, v62\n" \
"v_mfma_f32_16x16x32_bf16 v[216:219], a[144:147], v[120:123], v[216:219]\n" \
"ds_read_b128 v[112:115], v30 offset:12288\n" \
"v_mul_f32 v63, v189, v63\n" \
"v_mfma_f32_16x16x32_bf16 v[216:219], a[152:155], v[124:127], v[216:219]\n" \
"v_mfma_f32_16x16x32_bf16 v[220:223], a[148:151], v[120:123], v[220:223]\n" \
"ds_read_b128 v[116:119], v30 offset:14336\n" \
"v_mfma_f32_16x16x32_bf16 v[220:223], a[156:159], v[124:127], v[220:223]\n" \
"s_waitcnt vmcnt(27) lgkmcnt(4)\n" \
"s_barrier\n" \
"v_mfma_f32_16x16x32_bf16 v[208:211], a[0:3], v[96:99], v[208:211]\n" \
"ds_read_b128 v[120:123], v30 offset:13312\n" \
"v_mfma_f32_16x16x32_bf16 v[208:211], a[8:11], v[100:103], v[208:211]\n" \
"v_mfma_f32_16x16x32_bf16 v[212:215], a[4:7], v[96:99], v[212:215]\n" \
"s_waitcnt lgkmcnt(3)\n" \
"ds_read_b128 v[124:127], v30 offset:15360\n" \
"v_mfma_f32_16x16x32_bf16 v[212:215], a[12:15], v[100:103], v[212:215]\n" \
"buffer_load_dwordx4 v31, s[36:39], s80 offen offset:0 sc1 lds\n" \
"v_mfma_f32_16x16x32_bf16 v[216:219], a[0:3], v[104:107], v[216:219]\n" \
"ds_read_b128 v[96:99], v28 offset:32768\n" \
"v_mfma_f32_16x16x32_bf16 v[216:219], a[8:11], v[108:111], v[216:219]\n" \
"v_mfma_f32_16x16x32_bf16 v[220:223], a[4:7], v[104:107], v[220:223]\n" \
"s_waitcnt lgkmcnt(3)\n" \
"ds_read_b128 v[100:103], v28 offset:34816\n" \
"v_mfma_f32_16x16x32_bf16 v[220:223], a[12:15], v[108:111], v[220:223]\n" \
"buffer_load_dwordx4 v31, s[36:39], s80 offen offset:1024 sc1 lds\n" \
"v_mfma_f32_16x16x32_bf16 v[208:211], a[16:19], v[112:115], v[208:211]\n" \
"ds_read_b128 v[104:107], v28 offset:33792\n" \
"v_mfma_f32_16x16x32_bf16 v[208:211], a[24:27], v[116:119], v[208:211]\n" \
"v_mfma_f32_16x16x32_bf16 v[212:215], a[20:23], v[112:115], v[212:215]\n" \
"s_waitcnt lgkmcnt(3)\n" \
"ds_read_b128 v[108:111], v28 offset:35840\n" \
"v_mfma_f32_16x16x32_bf16 v[212:215], a[28:31], v[116:119], v[212:215]\n" \
"buffer_load_dwordx4 v31, s[36:39], s80 offen offset:2048 sc1 lds\n" \
"v_mfma_f32_16x16x32_bf16 v[216:219], a[16:19], v[120:123], v[216:219]\n" \
"ds_read_b128 v[112:115], v28 offset:36864\n" \
"v_mfma_f32_16x16x32_bf16 v[216:219], a[24:27], v[124:127], v[216:219]\n" \
"v_mfma_f32_16x16x32_bf16 v[220:223], a[20:23], v[120:123], v[220:223]\n" \
"s_waitcnt lgkmcnt(3)\n" \
"ds_read_b128 v[116:119], v28 offset:38912\n" \
"v_mfma_f32_16x16x32_bf16 v[220:223], a[28:31], v[124:127], v[220:223]\n" \
"buffer_load_dwordx4 v31, s[36:39], s80 offen offset:3072 sc1 lds\n" \
"v_mfma_f32_16x16x32_bf16 v[208:211], a[32:35], v[96:99], v[208:211]\n" \
"ds_read_b128 v[120:123], v28 offset:37888\n" \
"v_mfma_f32_16x16x32_bf16 v[208:211], a[40:43], v[100:103], v[208:211]\n" \
"v_mfma_f32_16x16x32_bf16 v[212:215], a[36:39], v[96:99], v[212:215]\n" \
"s_waitcnt lgkmcnt(3)\n" \
"ds_read_b128 v[124:127], v28 offset:39936\n" \
"v_mfma_f32_16x16x32_bf16 v[212:215], a[44:47], v[100:103], v[212:215]\n" \
"v_mfma_f32_16x16x32_bf16 v[216:219], a[32:35], v[104:107], v[216:219]\n" \
"ds_read_b128 v[96:99], v28 offset:40960\n" \
"v_mfma_f32_16x16x32_bf16 v[216:219], a[40:43], v[108:111], v[216:219]\n" \
"v_mfma_f32_16x16x32_bf16 v[220:223], a[36:39], v[104:107], v[220:223]\n" \
"s_waitcnt lgkmcnt(3)\n" \
"ds_read_b128 v[100:103], v28 offset:43008\n" \
"v_mfma_f32_16x16x32_bf16 v[220:223], a[44:47], v[108:111], v[220:223]\n" \
"v_mfma_f32_16x16x32_bf16 v[208:211], a[48:51], v[112:115], v[208:211]\n" \
"ds_read_b128 v[104:107], v28 offset:41984\n" \
"v_mfma_f32_16x16x32_bf16 v[208:211], a[56:59], v[116:119], v[208:211]\n" \
"v_mfma_f32_16x16x32_bf16 v[212:215], a[52:55], v[112:115], v[212:215]\n" \
"s_waitcnt lgkmcnt(3)\n" \
"ds_read_b128 v[108:111], v28 offset:44032\n" \
"v_mfma_f32_16x16x32_bf16 v[212:215], a[60:63], v[116:119], v[212:215]\n" \
"s_add_u32 s80, s71, 32768\n" \
"s_add_u32 m0, s22, 147456\n" \
"v_mfma_f32_16x16x32_bf16 v[216:219], a[48:51], v[120:123], v[216:219]\n" \
"s_sub_u32 s96, s88, 94404608\n" \
"ds_read_b128 v[112:115], v28 offset:45056\n" \
"s_lshl_b32 s96, s96, 2\n" \
"v_mfma_f32_16x16x32_bf16 v[216:219], a[56:59], v[124:127], v[216:219]\n" \
"s_add_u32 s96, s96, 94437376\n" \
"v_bfe_u32 v17, v4, 4, 2\n" \
"v_min_u32 v17, 2, v17\n" \
"v_mfma_f32_16x16x32_bf16 v[220:223], a[52:55], v[120:123], v[220:223]\n" \
"ds_read_b128 v[116:119], v28 offset:47104\n" \
"v_lshl_add_u32 v17, v17, 3, s96\n" \
"v_mfma_f32_16x16x32_bf16 v[220:223], a[60:63], v[124:127], v[220:223]\n" \
"s_waitcnt vmcnt(27) lgkmcnt(4)\n" \
"s_barrier\n" \
"v_mfma_f32_16x16x32_bf16 v[208:211], a[96:99], v[96:99], v[208:211]\n" \
"ds_read_b128 v[120:123], v28 offset:46080\n" \
"v_mfma_f32_16x16x32_bf16 v[208:211], a[104:107], v[100:103], v[208:211]\n" \
"v_mfma_f32_16x16x32_bf16 v[212:215], a[100:103], v[96:99], v[212:215]\n" \
"s_waitcnt lgkmcnt(3)\n" \
"ds_read_b128 v[124:127], v28 offset:48128\n" \
"v_mfma_f32_16x16x32_bf16 v[212:215], a[108:111], v[100:103], v[212:215]\n" \
"buffer_load_dwordx4 v31, s[36:39], s80 offen offset:0 sc1 lds\n" \
"v_mfma_f32_16x16x32_bf16 v[216:219], a[96:99], v[104:107], v[216:219]\n" \
"ds_read_b128 v[96:99], v28 offset:0\n" \
"v_mfma_f32_16x16x32_bf16 v[216:219], a[104:107], v[108:111], v[216:219]\n" \
"v_mfma_f32_16x16x32_bf16 v[220:223], a[100:103], v[104:107], v[220:223]\n" \
"s_waitcnt lgkmcnt(3)\n" \
"ds_read_b128 v[100:103], v28 offset:2048\n" \
"v_mfma_f32_16x16x32_bf16 v[220:223], a[108:111], v[108:111], v[220:223]\n" \
"buffer_load_dwordx4 v31, s[36:39], s80 offen offset:1024 sc1 lds\n" \
"v_mfma_f32_16x16x32_bf16 v[208:211], a[112:115], v[112:115], v[208:211]\n" \
"ds_read_b128 v[104:107], v28 offset:1024\n" \
"v_mfma_f32_16x16x32_bf16 v[208:211], a[120:123], v[116:119], v[208:211]\n" \
"v_mfma_f32_16x16x32_bf16 v[212:215], a[116:119], v[112:115], v[212:215]\n" \
"s_waitcnt lgkmcnt(3)\n" \
"ds_read_b128 v[108:111], v28 offset:3072\n" \
"v_mfma_f32_16x16x32_bf16 v[212:215], a[124:127], v[116:119], v[212:215]\n" \
"buffer_load_dwordx2 v[18:19], v17, s[36:39], 0 offen sc1\n" \
"v_mfma_f32_16x16x32_bf16 v[216:219], a[112:115], v[120:123], v[216:219]\n" \
"ds_read_b128 v[112:115], v28 offset:4096\n" \
"v_mfma_f32_16x16x32_bf16 v[216:219], a[120:123], v[124:127], v[216:219]\n" \
"v_mfma_f32_16x16x32_bf16 v[220:223], a[116:119], v[120:123], v[220:223]\n" \
"s_waitcnt lgkmcnt(3)\n" \
"ds_read_b128 v[116:119], v28 offset:6144\n" \
"v_mfma_f32_16x16x32_bf16 v[220:223], a[124:127], v[124:127], v[220:223]\n" \
"buffer_load_dwordx4 v31, s[36:39], s80 offen offset:2048 sc1 lds\n" \
"v_mfma_f32_16x16x32_bf16 v[192:195], a[160:163], v[96:99], 0\n" \
"ds_read_b128 v[120:123], v28 offset:5120\n" \
"v_mfma_f32_16x16x32_bf16 v[192:195], a[168:171], v[100:103], v[192:195]\n" \
"v_mfma_f32_16x16x32_bf16 v[196:199], a[164:167], v[96:99], 0\n" \
"s_waitcnt lgkmcnt(3)\n" \
"ds_read_b128 v[124:127], v28 offset:7168\n" \
"v_cvt_pk_bf16_f32 v208, v208, v209\n" \
"v_mfma_f32_16x16x32_bf16 v[196:199], a[172:175], v[100:103], v[196:199]\n" \
"buffer_load_dwordx4 v31, s[36:39], s80 offen offset:3072 sc1 lds\n" \
"v_mfma_f32_16x16x32_bf16 v[200:203], a[160:163], v[104:107], 0\n" \
"ds_read_b128 v[96:99], v28 offset:8192\n" \
"v_cvt_pk_bf16_f32 v209, v210, v211\n" \
"v_mfma_f32_16x16x32_bf16 v[200:203], a[168:171], v[108:111], v[200:203]\n" \
"v_cvt_pk_bf16_f32 v210, v212, v213\n" \
"v_cvt_pk_bf16_f32 v211, v214, v215\n" \
"v_mfma_f32_16x16x32_bf16 v[204:207], a[164:167], v[104:107], 0\n" \
"s_waitcnt lgkmcnt(3)\n" \
"ds_read_b128 v[100:103], v28 offset:10240\n" \
"v_cvt_pk_bf16_f32 v216, v216, v217\n" \
"v_mfma_f32_16x16x32_bf16 v[204:207], a[172:175], v[108:111], v[204:207]\n" \
"buffer_store_dwordx4 v[208:211], v4, s[36:39], s76 offen offset:0 sc1\n" \
"v_mfma_f32_16x16x32_bf16 v[192:195], a[176:179], v[112:115], v[192:195]\n" \
"ds_read_b128 v[104:107], v28 offset:9216\n" \
"v_cvt_pk_bf16_f32 v217, v218, v219\n" \
"v_mfma_f32_16x16x32_bf16 v[192:195], a[184:187], v[116:119], v[192:195]\n" \
"v_cvt_pk_bf16_f32 v218, v220, v221\n" \
"v_cvt_pk_bf16_f32 v219, v222, v223\n" \
"v_mfma_f32_16x16x32_bf16 v[196:199], a[180:183], v[112:115], v[196:199]\n" \
"s_waitcnt lgkmcnt(3)\n" \
"ds_read_b128 v[108:111], v28 offset:11264\n" \
"v_mfma_f32_16x16x32_bf16 v[196:199], a[188:191], v[116:119], v[196:199]\n" \
"s_add_u32 s80, s71, 49152\n" \
"s_add_u32 m0, s22, 16384\n" \
"v_mfma_f32_16x16x32_bf16 v[200:203], a[176:179], v[120:123], v[200:203]\n" \
"ds_read_b128 v[112:115], v28 offset:12288\n" \
"v_mfma_f32_16x16x32_bf16 v[200:203], a[184:187], v[124:127], v[200:203]\n" \
"v_mfma_f32_16x16x32_bf16 v[204:207], a[180:183], v[120:123], v[204:207]\n" \
"ds_read_b128 v[116:119], v28 offset:14336\n" \
"v_mfma_f32_16x16x32_bf16 v[204:207], a[188:191], v[124:127], v[204:207]\n" \
"s_waitcnt vmcnt(26) lgkmcnt(4)\n" \
"s_barrier\n" \
"v_mfma_f32_16x16x32_bf16 v[192:195], a[192:195], v[96:99], v[192:195]\n" \
"ds_read_b128 v[120:123], v28 offset:13312\n" \
"v_mfma_f32_16x16x32_bf16 v[192:195], a[200:203], v[100:103], v[192:195]\n" \
"buffer_store_dwordx4 v[216:219], v4, s[36:39], s76 offen offset:1024 sc1\n" \
"v_mfma_f32_16x16x32_bf16 v[196:199], a[196:199], v[96:99], v[196:199]\n" \
"s_waitcnt lgkmcnt(3)\n" \
"ds_read_b128 v[124:127], v28 offset:15360\n" \
"v_mfma_f32_16x16x32_bf16 v[196:199], a[204:207], v[100:103], v[196:199]\n" \
"v_mfma_f32_16x16x32_bf16 v[200:203], a[192:195], v[104:107], v[200:203]\n" \
"ds_read_b128 v[96:99], v29 offset:32768\n" \
"v_mfma_f32_16x16x32_bf16 v[200:203], a[200:203], v[108:111], v[200:203]\n" \
"buffer_load_dwordx4 v31, s[36:39], s80 offen offset:0 sc1 lds\n" \
"v_mfma_f32_16x16x32_bf16 v[204:207], a[196:199], v[104:107], v[204:207]\n" \
"s_waitcnt lgkmcnt(3)\n" \
"ds_read_b128 v[100:103], v29 offset:34816\n" \
"v_mfma_f32_16x16x32_bf16 v[204:207], a[204:207], v[108:111], v[204:207]\n" \
"v_mfma_f32_16x16x32_bf16 v[192:195], a[208:211], v[112:115], v[192:195]\n" \
"ds_read_b128 v[104:107], v29 offset:33792\n" \
"v_mfma_f32_16x16x32_bf16 v[192:195], a[216:219], v[116:119], v[192:195]\n" \
"buffer_load_dwordx4 v31, s[36:39], s80 offen offset:1024 sc1 lds\n" \
"v_mfma_f32_16x16x32_bf16 v[196:199], a[212:215], v[112:115], v[196:199]\n" \
"s_waitcnt lgkmcnt(3)\n" \
"ds_read_b128 v[108:111], v29 offset:35840\n" \
"v_mfma_f32_16x16x32_bf16 v[196:199], a[220:223], v[116:119], v[196:199]\n" \
"v_mfma_f32_16x16x32_bf16 v[200:203], a[208:211], v[120:123], v[200:203]\n" \
"ds_read_b128 v[112:115], v29 offset:36864\n" \
"v_mfma_f32_16x16x32_bf16 v[200:203], a[216:219], v[124:127], v[200:203]\n" \
"buffer_load_dwordx4 v31, s[36:39], s80 offen offset:2048 sc1 lds\n" \
"v_mfma_f32_16x16x32_bf16 v[204:207], a[212:215], v[120:123], v[204:207]\n" \
"s_waitcnt lgkmcnt(3)\n" \
"ds_read_b128 v[116:119], v29 offset:38912\n" \
"v_mfma_f32_16x16x32_bf16 v[204:207], a[220:223], v[124:127], v[204:207]\n" \
"v_mfma_f32_16x16x32_bf16 v[192:195], a[224:227], v[96:99], v[192:195]\n" \
"ds_read_b128 v[120:123], v29 offset:37888\n" \
"v_mfma_f32_16x16x32_bf16 v[192:195], a[232:235], v[100:103], v[192:195]\n" \
"buffer_load_dwordx4 v31, s[36:39], s80 offen offset:3072 sc1 lds\n" \
"v_mfma_f32_16x16x32_bf16 v[196:199], a[228:231], v[96:99], v[196:199]\n" \
"s_waitcnt lgkmcnt(3)\n" \
"ds_read_b128 v[124:127], v29 offset:39936\n" \
"v_mfma_f32_16x16x32_bf16 v[196:199], a[236:239], v[100:103], v[196:199]\n" \
"v_mfma_f32_16x16x32_bf16 v[200:203], a[224:227], v[104:107], v[200:203]\n" \
"ds_read_b128 v[96:99], v29 offset:40960\n" \
"v_mfma_f32_16x16x32_bf16 v[200:203], a[232:235], v[108:111], v[200:203]\n" \
"v_mfma_f32_16x16x32_bf16 v[204:207], a[228:231], v[104:107], v[204:207]\n" \
"s_waitcnt lgkmcnt(3)\n" \
"ds_read_b128 v[100:103], v29 offset:43008\n" \
"s_waitcnt vmcnt(8)\n" \
"v_mfma_f32_16x16x32_bf16 v[204:207], a[236:239], v[108:111], v[204:207]\n" \
"v_cmp_ne_u64 vcc, s[14:15], v[18:19]\n" \
"s_nop 0\n" \
"v_mfma_f32_16x16x32_bf16 v[192:195], a[240:243], v[112:115], v[192:195]\n" \
"ds_read_b128 v[104:107], v29 offset:41984\n" \
"s_cbranch_vccnz LPE_%=\nLPER_%=:\n" \
"v_mfma_f32_16x16x32_bf16 v[192:195], a[248:251], v[116:119], v[192:195]\n" \
"s_add_u32 s97, s78, 0\n" \
"v_mfma_f32_16x16x32_bf16 v[196:199], a[244:247], v[112:115], v[196:199]\n" \
"s_waitcnt lgkmcnt(3)\n" \
"ds_read_b128 v[108:111], v29 offset:44032\n" \
"v_mfma_f32_16x16x32_bf16 v[196:199], a[252:255], v[116:119], v[196:199]\n" \
"buffer_load_dwordx4 v[128:131], v4, s[36:39], s97 offen offset:0 sc1\n" \
"v_mfma_f32_16x16x32_bf16 v[200:203], a[240:243], v[120:123], v[200:203]\n" \
"ds_read_b128 v[112:115], v29 offset:45056\n" \
"v_mfma_f32_16x16x32_bf16 v[200:203], a[248:251], v[124:127], v[200:203]\n" \
"v_mfma_f32_16x16x32_bf16 v[204:207], a[244:247], v[120:123], v[204:207]\n" \
"ds_read_b128 v[116:119], v29 offset:47104\n" \
"v_mfma_f32_16x16x32_bf16 v[204:207], a[252:255], v[124:127], v[204:207]\n" \
"s_waitcnt vmcnt(27) lgkmcnt(4)\n" \
"s_barrier\n" \
"v_mfma_f32_16x16x32_bf16 v[192:195], a[64:67], v[96:99], v[192:195]\n" \
"ds_read_b128 v[120:123], v29 offset:46080\n" \
"v_mfma_f32_16x16x32_bf16 v[192:195], a[72:75], v[100:103], v[192:195]\n" \
"buffer_load_dwordx4 v[132:135], v4, s[36:39], s97 offen offset:1024 sc1\n" \
"v_mfma_f32_16x16x32_bf16 v[196:199], a[68:71], v[96:99], v[196:199]\n" \
"s_waitcnt lgkmcnt(3)\n" \
"ds_read_b128 v[124:127], v29 offset:48128\n" \
"v_mfma_f32_16x16x32_bf16 v[196:199], a[76:79], v[100:103], v[196:199]\n" \
"v_mfma_f32_16x16x32_bf16 v[200:203], a[64:67], v[104:107], v[200:203]\n" \
"ds_read_b128 v[96:99], v28 offset:49152\n" \
"v_mfma_f32_16x16x32_bf16 v[200:203], a[72:75], v[108:111], v[200:203]\n" \
"buffer_load_dwordx4 v[136:139], v4, s[36:39], s97 offen offset:2048 sc1\n" \
"v_mfma_f32_16x16x32_bf16 v[204:207], a[68:71], v[104:107], v[204:207]\n" \
"s_waitcnt lgkmcnt(3)\n" \
"ds_read_b128 v[100:103], v28 offset:51200\n" \
"v_mfma_f32_16x16x32_bf16 v[204:207], a[76:79], v[108:111], v[204:207]\n" \
"v_mfma_f32_16x16x32_bf16 v[192:195], a[80:83], v[112:115], v[192:195]\n" \
"ds_read_b128 v[104:107], v28 offset:50176\n" \
"v_mfma_f32_16x16x32_bf16 v[192:195], a[88:91], v[116:119], v[192:195]\n" \
"buffer_load_dwordx4 v[140:143], v4, s[36:39], s97 offen offset:3072 sc1\n" \
"v_mfma_f32_16x16x32_bf16 v[196:199], a[84:87], v[112:115], v[196:199]\n" \
"s_waitcnt lgkmcnt(3)\n" \
"ds_read_b128 v[108:111], v28 offset:52224\n" \
"s_add_u32 s97, s78, 4096\n" \
"v_mfma_f32_16x16x32_bf16 v[196:199], a[92:95], v[116:119], v[196:199]\n" \
"v_mfma_f32_16x16x32_bf16 v[200:203], a[80:83], v[120:123], v[200:203]\n" \
"ds_read_b128 v[112:115], v28 offset:53248\n" \
"v_mfma_f32_16x16x32_bf16 v[200:203], a[88:91], v[124:127], v[200:203]\n" \
"buffer_load_dwordx4 v[160:163], v4, s[36:39], s97 offen offset:0 sc1\n" \
"v_mfma_f32_16x16x32_bf16 v[204:207], a[84:87], v[120:123], v[204:207]\n" \
"s_waitcnt lgkmcnt(3)\n" \
"ds_read_b128 v[116:119], v28 offset:55296\n" \
"v_mfma_f32_16x16x32_bf16 v[204:207], a[92:95], v[124:127], v[204:207]\n" \
"v_mfma_f32_16x16x32_bf16 v[192:195], a[128:131], v[96:99], v[192:195]\n" \
"ds_read_b128 v[120:123], v28 offset:54272\n" \
"v_mfma_f32_16x16x32_bf16 v[192:195], a[136:139], v[100:103], v[192:195]\n" \
"buffer_load_dwordx4 v[164:167], v4, s[36:39], s97 offen offset:1024 sc1\n" \
"v_mfma_f32_16x16x32_bf16 v[196:199], a[132:135], v[96:99], v[196:199]\n" \
"s_waitcnt lgkmcnt(3)\n" \
"ds_read_b128 v[124:127], v28 offset:56320\n" \
"v_mfma_f32_16x16x32_bf16 v[196:199], a[140:143], v[100:103], v[196:199]\n" \
"v_mfma_f32_16x16x32_bf16 v[200:203], a[128:131], v[104:107], v[200:203]\n" \
"ds_read_b128 v[96:99], v28 offset:57344\n" \
"v_mfma_f32_16x16x32_bf16 v[200:203], a[136:139], v[108:111], v[200:203]\n" \
"v_mfma_f32_16x16x32_bf16 v[204:207], a[132:135], v[104:107], v[204:207]\n" \
"s_waitcnt lgkmcnt(3)\n" \
"ds_read_b128 v[100:103], v28 offset:59392\n" \
"v_mfma_f32_16x16x32_bf16 v[204:207], a[140:143], v[108:111], v[204:207]\n" \
"v_mfma_f32_16x16x32_bf16 v[192:195], a[144:147], v[112:115], v[192:195]\n" \
"ds_read_b128 v[104:107], v28 offset:58368\n" \
"v_mfma_f32_16x16x32_bf16 v[192:195], a[152:155], v[116:119], v[192:195]\n" \
"v_mfma_f32_16x16x32_bf16 v[196:199], a[148:151], v[112:115], v[196:199]\n" \
"s_waitcnt lgkmcnt(3)\n" \
"ds_read_b128 v[108:111], v28 offset:60416\n" \
"v_mfma_f32_16x16x32_bf16 v[196:199], a[156:159], v[116:119], v[196:199]\n" \
"v_mfma_f32_16x16x32_bf16 v[200:203], a[144:147], v[120:123], v[200:203]\n" \
"ds_read_b128 v[112:115], v28 offset:61440\n" \
"v_mfma_f32_16x16x32_bf16 v[200:203], a[152:155], v[124:127], v[200:203]\n" \
"v_mfma_f32_16x16x32_bf16 v[204:207], a[148:151], v[120:123], v[204:207]\n" \
"ds_read_b128 v[116:119], v28 offset:63488\n" \
"v_mfma_f32_16x16x32_bf16 v[204:207], a[156:159], v[124:127], v[204:207]\n" \
"s_waitcnt vmcnt(26) lgkmcnt(4)\n" \
"s_barrier\n" \
"v_mfma_f32_16x16x32_bf16 v[192:195], a[0:3], v[96:99], v[192:195]\n" \
"ds_read_b128 v[120:123], v28 offset:62464\n" \
"v_mfma_f32_16x16x32_bf16 v[192:195], a[8:11], v[100:103], v[192:195]\n" \
"v_mfma_f32_16x16x32_bf16 v[196:199], a[4:7], v[96:99], v[196:199]\n" \
"s_waitcnt lgkmcnt(3)\n" \
"ds_read_b128 v[124:127], v28 offset:64512\n" \
"v_mfma_f32_16x16x32_bf16 v[196:199], a[12:15], v[100:103], v[196:199]\n" \
"v_mfma_f32_16x16x32_bf16 v[200:203], a[0:3], v[104:107], v[200:203]\n" \
"ds_read_b128 v[96:99], v29 offset:0\n" \
"v_mfma_f32_16x16x32_bf16 v[200:203], a[8:11], v[108:111], v[200:203]\n" \
"v_mfma_f32_16x16x32_bf16 v[204:207], a[4:7], v[104:107], v[204:207]\n" \
"s_waitcnt lgkmcnt(3)\n" \
"ds_read_b128 v[100:103], v29 offset:2048\n" \
"v_mfma_f32_16x16x32_bf16 v[204:207], a[12:15], v[108:111], v[204:207]\n" \
"v_mfma_f32_16x16x32_bf16 v[192:195], a[16:19], v[112:115], v[192:195]\n" \
"ds_read_b128 v[104:107], v29 offset:1024\n" \
"v_mfma_f32_16x16x32_bf16 v[192:195], a[24:27], v[116:119], v[192:195]\n" \
"v_mfma_f32_16x16x32_bf16 v[196:199], a[20:23], v[112:115], v[196:199]\n" \
"s_waitcnt lgkmcnt(3)\n" \
"ds_read_b128 v[108:111], v29 offset:3072\n" \
"v_mfma_f32_16x16x32_bf16 v[196:199], a[28:31], v[116:119], v[196:199]\n" \
"v_mfma_f32_16x16x32_bf16 v[200:203], a[16:19], v[120:123], v[200:203]\n" \
"ds_read_b128 v[112:115], v29 offset:4096\n" \
"v_mfma_f32_16x16x32_bf16 v[200:203], a[24:27], v[124:127], v[200:203]\n" \
"v_mfma_f32_16x16x32_bf16 v[204:207], a[20:23], v[120:123], v[204:207]\n" \
"s_waitcnt lgkmcnt(3)\n" \
"ds_read_b128 v[116:119], v29 offset:6144\n" \
"v_mfma_f32_16x16x32_bf16 v[204:207], a[28:31], v[124:127], v[204:207]\n" \
"v_mfma_f32_16x16x32_bf16 v[192:195], a[32:35], v[96:99], v[192:195]\n" \
"ds_read_b128 v[120:123], v29 offset:5120\n" \
"v_mfma_f32_16x16x32_bf16 v[192:195], a[40:43], v[100:103], v[192:195]\n" \
"v_mfma_f32_16x16x32_bf16 v[196:199], a[36:39], v[96:99], v[196:199]\n" \
"s_waitcnt lgkmcnt(3)\n" \
"ds_read_b128 v[124:127], v29 offset:7168\n" \
"v_mfma_f32_16x16x32_bf16 v[196:199], a[44:47], v[100:103], v[196:199]\n" \
"v_mfma_f32_16x16x32_bf16 v[200:203], a[32:35], v[104:107], v[200:203]\n" \
"ds_read_b128 v[96:99], v29 offset:8192\n" \
"v_mfma_f32_16x16x32_bf16 v[200:203], a[40:43], v[108:111], v[200:203]\n" \
"v_mfma_f32_16x16x32_bf16 v[204:207], a[36:39], v[104:107], v[204:207]\n" \
"s_waitcnt lgkmcnt(3)\n" \
"ds_read_b128 v[100:103], v29 offset:10240\n" \
"v_mfma_f32_16x16x32_bf16 v[204:207], a[44:47], v[108:111], v[204:207]\n" \
"v_mfma_f32_16x16x32_bf16 v[192:195], a[48:51], v[112:115], v[192:195]\n" \
"ds_read_b128 v[104:107], v29 offset:9216\n" \
"v_mfma_f32_16x16x32_bf16 v[192:195], a[56:59], v[116:119], v[192:195]\n" \
"v_mfma_f32_16x16x32_bf16 v[196:199], a[52:55], v[112:115], v[196:199]\n" \
"s_waitcnt lgkmcnt(3)\n" \
"ds_read_b128 v[108:111], v29 offset:11264\n" \
"v_mfma_f32_16x16x32_bf16 v[196:199], a[60:63], v[116:119], v[196:199]\n" \
"v_mfma_f32_16x16x32_bf16 v[200:203], a[48:51], v[120:123], v[200:203]\n" \
"ds_read_b128 v[112:115], v29 offset:12288\n" \
"v_mfma_f32_16x16x32_bf16 v[200:203], a[56:59], v[124:127], v[200:203]\n" \
"v_mfma_f32_16x16x32_bf16 v[204:207], a[52:55], v[120:123], v[204:207]\n" \
"ds_read_b128 v[116:119], v29 offset:14336\n" \
"v_mfma_f32_16x16x32_bf16 v[204:207], a[60:63], v[124:127], v[204:207]\n" \
"s_waitcnt vmcnt(21) lgkmcnt(4)\n" \
"s_barrier\n" \
"v_mfma_f32_16x16x32_bf16 v[192:195], a[96:99], v[96:99], v[192:195]\n" \
"ds_read_b128 v[120:123], v29 offset:13312\n" \
"v_mfma_f32_16x16x32_bf16 v[192:195], a[104:107], v[100:103], v[192:195]\n" \
"v_mfma_f32_16x16x32_bf16 v[196:199], a[100:103], v[96:99], v[196:199]\n" \
"s_waitcnt lgkmcnt(3)\n" \
"ds_read_b128 v[124:127], v29 offset:15360\n" \
"v_mfma_f32_16x16x32_bf16 v[196:199], a[108:111], v[100:103], v[196:199]\n" \
"v_mfma_f32_16x16x32_bf16 v[200:203], a[96:99], v[104:107], v[200:203]\n" \
"ds_read_b128 v[96:99], v29 offset:16384\n" \
"v_mfma_f32_16x16x32_bf16 v[200:203], a[104:107], v[108:111], v[200:203]\n" \
"v_mfma_f32_16x16x32_bf16 v[204:207], a[100:103], v[104:107], v[204:207]\n" \
"s_waitcnt lgkmcnt(3)\n" \
"ds_read_b128 v[100:103], v29 offset:18432\n" \
"v_mfma_f32_16x16x32_bf16 v[204:207], a[108:111], v[108:111], v[204:207]\n" \
"v_mfma_f32_16x16x32_bf16 v[192:195], a[112:115], v[112:115], v[192:195]\n" \
"ds_read_b128 v[104:107], v29 offset:17408\n" \
"v_mfma_f32_16x16x32_bf16 v[192:195], a[120:123], v[116:119], v[192:195]\n" \
"v_mfma_f32_16x16x32_bf16 v[196:199], a[116:119], v[112:115], v[196:199]\n" \
"s_waitcnt lgkmcnt(3)\n" \
"ds_read_b128 v[108:111], v29 offset:19456\n" \
"v_mfma_f32_16x16x32_bf16 v[196:199], a[124:127], v[116:119], v[196:199]\n" \
"v_mfma_f32_16x16x32_bf16 v[200:203], a[112:115], v[120:123], v[200:203]\n" \
"ds_read_b128 v[112:115], v29 offset:20480\n" \
"v_mfma_f32_16x16x32_bf16 v[200:203], a[120:123], v[124:127], v[200:203]\n" \
"v_mfma_f32_16x16x32_bf16 v[204:207], a[116:119], v[120:123], v[204:207]\n" \
"ds_read_b128 v[116:119], v29 offset:22528\n" \
"v_mfma_f32_16x16x32_bf16 v[204:207], a[124:127], v[124:127], v[204:207]\n" \
"s_waitcnt vmcnt(0)\n" \
"v_mfma_f32_16x16x32_bf16 v[64:67], v[24:27], v[128:131], v[64:67]\n" \
"v_mfma_f32_16x16x32_bf16 v[68:71], v[22:25], v[128:131], v[68:71]\n" \
"v_mfma_f32_16x16x32_bf16 v[72:75], v[24:27], v[132:135], v[72:75]\n" \
"v_cvt_pk_bf16_f32 v192, v192, v193\n" \
"v_cvt_pk_bf16_f32 v193, v194, v195\n" \
"v_mfma_f32_16x16x32_bf16 v[76:79], v[22:25], v[132:135], v[76:79]\n" \
"v_cvt_pk_bf16_f32 v194, v196, v197\n" \
"v_cvt_pk_bf16_f32 v195, v198, v199\n" \
"v_mfma_f32_16x16x32_bf16 v[64:67], v[24:27], v[136:139], v[64:67]\n" \
"buffer_store_dwordx4 v[192:195], v4, s[36:39], s77 offen offset:0 sc1\n" \
"v_mfma_f32_16x16x32_bf16 v[68:71], v[22:25], v[136:139], v[68:71]\n" \
"v_cvt_pk_bf16_f32 v200, v200, v201\n" \
"v_cvt_pk_bf16_f32 v201, v202, v203\n" \
"v_mfma_f32_16x16x32_bf16 v[72:75], v[24:27], v[140:143], v[72:75]\n" \
"v_cvt_pk_bf16_f32 v202, v204, v205\n" \
"v_cvt_pk_bf16_f32 v203, v206, v207\n" \
"v_mfma_f32_16x16x32_bf16 v[76:79], v[22:25], v[140:143], v[76:79]\n" \
"v_mfma_f32_16x16x32_bf16 v[64:67], v[24:27], v[160:163], v[64:67]\n" \
"v_mfma_f32_16x16x32_bf16 v[68:71], v[22:25], v[160:163], v[68:71]\n" \
"v_mfma_f32_16x16x32_bf16 v[72:75], v[24:27], v[164:167], v[72:75]\n" \
"s_waitcnt lgkmcnt(4)\n" \
"v_mfma_f32_16x16x32_bf16 v[76:79], v[22:25], v[164:167], v[76:79]\n" \
"v_mfma_f32_16x16x32_bf16 v[240:243], a[160:163], v[96:99], 0\n" \
"ds_read_b128 v[120:123], v29 offset:21504\n" \
"v_mfma_f32_16x16x32_bf16 v[240:243], a[168:171], v[100:103], v[240:243]\n" \
"buffer_store_dwordx4 v[200:203], v4, s[36:39], s77 offen offset:1024 sc1\n" \
"v_mfma_f32_16x16x32_bf16 v[244:247], a[164:167], v[96:99], 0\n" \
"s_waitcnt lgkmcnt(3)\n" \
"ds_read_b128 v[124:127], v29 offset:23552\n" \
"v_fmac_f32 v32, v5, v64\n" \
"v_mfma_f32_16x16x32_bf16 v[244:247], a[172:175], v[100:103], v[244:247]\n" \
"v_fmac_f32 v33, v5, v65\n" \
"v_cvt_pk_bf16_f32 v80, v32, v33\n" \
"v_mfma_f32_16x16x32_bf16 v[248:251], a[160:163], v[104:107], 0\n" \
"ds_read_b128 v[96:99], v29 offset:24576\n" \
"v_fmac_f32 v34, v5, v66\n" \
"v_mfma_f32_16x16x32_bf16 v[248:251], a[168:171], v[108:111], v[248:251]\n" \
"v_fmac_f32 v35, v5, v67\n" \
"v_cvt_pk_bf16_f32 v81, v34, v35\n" \
"v_mfma_f32_16x16x32_bf16 v[252:255], a[164:167], v[104:107], 0\n" \
"s_waitcnt lgkmcnt(3)\n" \
"ds_read_b128 v[100:103], v29 offset:26624\n" \
"v_fmac_f32 v36, v5, v68\n" \
"v_mfma_f32_16x16x32_bf16 v[252:255], a[172:175], v[108:111], v[252:255]\n" \
"v_fmac_f32 v37, v5, v69\n" \
"v_cvt_pk_bf16_f32 v82, v36, v37\n" \
"v_mfma_f32_16x16x32_bf16 v[240:243], a[176:179], v[112:115], v[240:243]\n" \
"ds_read_b128 v[104:107], v29 offset:25600\n" \
"v_fmac_f32 v38, v5, v70\n" \
"v_mfma_f32_16x16x32_bf16 v[240:243], a[184:187], v[116:119], v[240:243]\n" \
"s_add_u32 s92, s82, 0\n" \
"v_fmac_f32 v39, v5, v71\n" \
"v_cvt_pk_bf16_f32 v83, v38, v39\n" \
"v_mfma_f32_16x16x32_bf16 v[244:247], a[180:183], v[112:115], v[244:247]\n" \
"s_waitcnt lgkmcnt(3)\n" \
"ds_read_b128 v[108:111], v29 offset:27648\n" \
"s_nop 0\n" \
"v_mfma_f32_16x16x32_bf16 v[244:247], a[188:191], v[116:119], v[244:247]\n" \
"v_permlane16_swap_b32 v80, v82\n" \
"v_permlane16_swap_b32 v81, v83\n" \
"v_mfma_f32_16x16x32_bf16 v[248:251], a[176:179], v[120:123], v[248:251]\n" \
"ds_read_b128 v[112:115], v29 offset:28672\n" \
"v_fmac_f32 v40, v21, v72\n" \
"v_mfma_f32_16x16x32_bf16 v[248:251], a[184:187], v[124:127], v[248:251]\n" \
"v_fmac_f32 v41, v21, v73\n" \
"v_cvt_pk_bf16_f32 v84, v40, v41\n" \
"v_mfma_f32_16x16x32_bf16 v[252:255], a[180:183], v[120:123], v[252:255]\n" \
"ds_read_b128 v[116:119], v29 offset:30720\n" \
"v_fmac_f32 v42, v21, v74\n" \
"v_mfma_f32_16x16x32_bf16 v[252:255], a[188:191], v[124:127], v[252:255]\n" \
"s_waitcnt vmcnt(19) lgkmcnt(4)\n" \
"s_barrier\n" \
"v_fmac_f32 v43, v21, v75\n" \
"v_mfma_f32_16x16x32_bf16 v[240:243], a[192:195], v[96:99], v[240:243]\n" \
"ds_read_b128 v[120:123], v29 offset:29696\n" \
"v_cvt_pk_bf16_f32 v85, v42, v43\n" \
"v_mfma_f32_16x16x32_bf16 v[240:243], a[200:203], v[100:103], v[240:243]\n" \
"v_fmac_f32 v44, v21, v76\n" \
"v_fmac_f32 v45, v21, v77\n" \
"v_mfma_f32_16x16x32_bf16 v[244:247], a[196:199], v[96:99], v[244:247]\n" \
"s_waitcnt lgkmcnt(3)\n" \
"ds_read_b128 v[124:127], v29 offset:31744\n" \
"v_cvt_pk_bf16_f32 v86, v44, v45\n" \
"v_mfma_f32_16x16x32_bf16 v[244:247], a[204:207], v[100:103], v[244:247]\n" \
"v_fmac_f32 v46, v21, v78\n" \
"v_fmac_f32 v47, v21, v79\n" \
"v_mfma_f32_16x16x32_bf16 v[248:251], a[192:195], v[104:107], v[248:251]\n" \
"s_add_u32 s97, s78, 4096\n" \
"ds_read_b128 v[96:99], v29 offset:49152\n" \
"v_cvt_pk_bf16_f32 v87, v46, v47\n" \
"v_mfma_f32_16x16x32_bf16 v[248:251], a[200:203], v[108:111], v[248:251]\n" \
"buffer_load_dwordx4 v[168:171], v4, s[36:39], s97 offen offset:2048 sc1\n" \
"v_mfma_f32_16x16x32_bf16 v[252:255], a[196:199], v[104:107], v[252:255]\n" \
"s_waitcnt lgkmcnt(3)\n" \
"ds_read_b128 v[100:103], v29 offset:51200\n" \
"s_add_u32 s93, s82, 32768\n" \
"v_mfma_f32_16x16x32_bf16 v[252:255], a[204:207], v[108:111], v[252:255]\n" \
"s_nop 0\n" \
"v_permlane16_swap_b32 v84, v86\n" \
"v_mfma_f32_16x16x32_bf16 v[240:243], a[208:211], v[112:115], v[240:243]\n" \
"ds_read_b128 v[104:107], v29 offset:50176\n" \
"v_permlane16_swap_b32 v85, v87\n" \
"v_mfma_f32_16x16x32_bf16 v[240:243], a[216:219], v[116:119], v[240:243]\n" \
"buffer_load_dwordx4 v[172:175], v4, s[36:39], s97 offen offset:3072 sc1\n" \
"v_mfma_f32_16x16x32_bf16 v[244:247], a[212:215], v[112:115], v[244:247]\n" \
"s_waitcnt lgkmcnt(3)\n" \
"ds_read_b128 v[108:111], v29 offset:52224\n" \
"s_add_u32 s97, s78, 8192\n" \
"v_mfma_f32_16x16x32_bf16 v[244:247], a[220:223], v[116:119], v[244:247]\n" \
"v_mfma_f32_16x16x32_bf16 v[248:251], a[208:211], v[120:123], v[248:251]\n" \
"ds_read_b128 v[112:115], v29 offset:53248\n" \
"v_mfma_f32_16x16x32_bf16 v[248:251], a[216:219], v[124:127], v[248:251]\n" \
"buffer_load_dwordx4 v[224:227], v4, s[36:39], s97 offen offset:0 sc1\n" \
"v_mfma_f32_16x16x32_bf16 v[252:255], a[212:215], v[120:123], v[252:255]\n" \
"s_waitcnt lgkmcnt(3)\n" \
"ds_read_b128 v[116:119], v29 offset:55296\n" \
"v_mfma_f32_16x16x32_bf16 v[252:255], a[220:223], v[124:127], v[252:255]\n" \
"v_mfma_f32_16x16x32_bf16 v[240:243], a[224:227], v[96:99], v[240:243]\n" \
"ds_read_b128 v[120:123], v29 offset:54272\n" \
"v_mfma_f32_16x16x32_bf16 v[240:243], a[232:235], v[100:103], v[240:243]\n" \
"buffer_load_dwordx4 v[228:231], v4, s[36:39], s97 offen offset:1024 sc1\n" \
"v_mfma_f32_16x16x32_bf16 v[244:247], a[228:231], v[96:99], v[244:247]\n" \
"s_waitcnt lgkmcnt(3)\n" \
"ds_read_b128 v[124:127], v29 offset:56320\n" \
"v_mfma_f32_16x16x32_bf16 v[244:247], a[236:239], v[100:103], v[244:247]\n" \
"v_mfma_f32_16x16x32_bf16 v[248:251], a[224:227], v[104:107], v[248:251]\n" \
"ds_read_b128 v[96:99], v29 offset:57344\n" \
"v_mfma_f32_16x16x32_bf16 v[248:251], a[232:235], v[108:111], v[248:251]\n" \
"v_mfma_f32_16x16x32_bf16 v[252:255], a[228:231], v[104:107], v[252:255]\n" \
"s_waitcnt lgkmcnt(3)\n" \
"ds_read_b128 v[100:103], v29 offset:59392\n" \
"v_mfma_f32_16x16x32_bf16 v[252:255], a[236:239], v[108:111], v[252:255]\n" \
"v_mfma_f32_16x16x32_bf16 v[240:243], a[240:243], v[112:115], v[240:243]\n" \
"ds_read_b128 v[104:107], v29 offset:58368\n" \
"v_mfma_f32_16x16x32_bf16 v[240:243], a[248:251], v[116:119], v[240:243]\n" \
"v_mfma_f32_16x16x32_bf16 v[244:247], a[244:247], v[112:115], v[244:247]\n" \
"s_waitcnt lgkmcnt(3)\n" \
"ds_read_b128 v[108:111], v29 offset:60416\n" \
"v_mfma_f32_16x16x32_bf16 v[244:247], a[252:255], v[116:119], v[244:247]\n" \
"v_mfma_f32_16x16x32_bf16 v[248:251], a[240:243], v[120:123], v[248:251]\n" \
"ds_read_b128 v[112:115], v29 offset:61440\n" \
"v_mfma_f32_16x16x32_bf16 v[248:251], a[248:251], v[124:127], v[248:251]\n" \
"v_mfma_f32_16x16x32_bf16 v[252:255], a[244:247], v[120:123], v[252:255]\n" \
"ds_read_b128 v[116:119], v29 offset:63488\n" \
"v_mfma_f32_16x16x32_bf16 v[252:255], a[252:255], v[124:127], v[252:255]\n" \
"s_waitcnt vmcnt(18) lgkmcnt(4)\n" \
"s_barrier\n" \
"v_mfma_f32_16x16x32_bf16 v[240:243], a[64:67], v[96:99], v[240:243]\n" \
"ds_read_b128 v[120:123], v29 offset:62464\n" \
"v_mfma_f32_16x16x32_bf16 v[240:243], a[72:75], v[100:103], v[240:243]\n" \
"v_mfma_f32_16x16x32_bf16 v[244:247], a[68:71], v[96:99], v[244:247]\n" \
"s_waitcnt lgkmcnt(3)\n" \
"ds_read_b128 v[124:127], v29 offset:64512\n" \
"v_mfma_f32_16x16x32_bf16 v[244:247], a[76:79], v[100:103], v[244:247]\n" \
"v_mfma_f32_16x16x32_bf16 v[248:251], a[64:67], v[104:107], v[248:251]\n" \
"ds_read_b128 v[96:99], v30 offset:16384\n" \
"v_mfma_f32_16x16x32_bf16 v[248:251], a[72:75], v[108:111], v[248:251]\n" \
"v_mfma_f32_16x16x32_bf16 v[252:255], a[68:71], v[104:107], v[252:255]\n" \
"s_waitcnt lgkmcnt(3)\n" \
"ds_read_b128 v[100:103], v30 offset:18432\n" \
"v_mfma_f32_16x16x32_bf16 v[252:255], a[76:79], v[108:111], v[252:255]\n" \
"v_mfma_f32_16x16x32_bf16 v[240:243], a[80:83], v[112:115], v[240:243]\n" \
"ds_read_b128 v[104:107], v30 offset:17408\n" \
"v_mfma_f32_16x16x32_bf16 v[240:243], a[88:91], v[116:119], v[240:243]\n" \
"v_mfma_f32_16x16x32_bf16 v[244:247], a[84:87], v[112:115], v[244:247]\n" \
"s_waitcnt lgkmcnt(3)\n" \
"ds_read_b128 v[108:111], v30 offset:19456\n" \
"v_mfma_f32_16x16x32_bf16 v[244:247], a[92:95], v[116:119], v[244:247]\n" \
"v_mfma_f32_16x16x32_bf16 v[248:251], a[80:83], v[120:123], v[248:251]\n" \
"ds_read_b128 v[112:115], v30 offset:20480\n" \
"v_mfma_f32_16x16x32_bf16 v[248:251], a[88:91], v[124:127], v[248:251]\n" \
"v_mfma_f32_16x16x32_bf16 v[252:255], a[84:87], v[120:123], v[252:255]\n" \
"s_waitcnt lgkmcnt(3)\n" \
"ds_read_b128 v[116:119], v30 offset:22528\n" \
"v_mfma_f32_16x16x32_bf16 v[252:255], a[92:95], v[124:127], v[252:255]\n" \
"v_mfma_f32_16x16x32_bf16 v[240:243], a[128:131], v[96:99], v[240:243]\n" \
"ds_read_b128 v[120:123], v30 offset:21504\n" \
"v_mfma_f32_16x16x32_bf16 v[240:243], a[136:139], v[100:103], v[240:243]\n" \
"v_mfma_f32_16x16x32_bf16 v[244:247], a[132:135], v[96:99], v[244:247]\n" \
"LH28_%=:\ns_cbranch_execz LHR2_%=\n" \
"s_waitcnt lgkmcnt(3)\n" \
"ds_read_b128 v[124:127], v30 offset:23552\n" \
"v_mfma_f32_16x16x32_bf16 v[244:247], a[140:143], v[100:103], v[244:247]\n" \
"v_mfma_f32_16x16x32_bf16 v[248:251], a[128:131], v[104:107], v[248:251]\n" \
"ds_read_b128 v[96:99], v30 offset:24576\n" \
"v_mfma_f32_16x16x32_bf16 v[248:251], a[136:139], v[108:111], v[248:251]\n" \
"v_mfma_f32_16x16x32_bf16 v[252:255], a[132:135], v[104:107], v[252:255]\n" \
"s_waitcnt lgkmcnt(3)\n" \
"ds_read_b128 v[100:103], v30 offset:26624\n" \
"v_mfma_f32_16x16x32_bf16 v[252:255], a[140:143], v[108:111], v[252:255]\n" \
"v_mfma_f32_16x16x32_bf16 v[240:243], a[144:147], v[112:115], v[240:243]\n" \
"ds_read_b128 v[104:107], v30 offset:25600\n" \
"v_mfma_f32_16x16x32_bf16 v[240:243], a[152:155], v[116:119], v[240:243]\n" \
"v_mfma_f32_16x16x32_bf16 v[244:247], a[148:151], v[112:115], v[244:247]\n" \
"s_waitcnt lgkmcnt(3)\n" \
"ds_read_b128 v[108:111], v30 offset:27648\n" \
"v_mfma_f32_16x16x32_bf16 v[244:247], a[156:159], v[116:119], v[244:247]\n" \
"v_mfma_f32_16x16x32_bf16 v[248:251], a[144:147], v[120:123], v[248:251]\n" \
"ds_read_b128 v[112:115], v30 offset:28672\n" \
"v_mfma_f32_16x16x32_bf16 v[248:251], a[152:155], v[124:127], v[248:251]\n" \
"v_mfma_f32_16x16x32_bf16 v[252:255], a[148:151], v[120:123], v[252:255]\n" \
"ds_read_b128 v[116:119], v30 offset:30720\n" \
"v_mfma_f32_16x16x32_bf16 v[252:255], a[156:159], v[124:127], v[252:255]\n" \
"s_waitcnt vmcnt(12) lgkmcnt(4)\n" \
"s_barrier\n" \
"v_mfma_f32_16x16x32_bf16 v[240:243], a[0:3], v[96:99], v[240:243]\n" \
"ds_read_b128 v[120:123], v30 offset:29696\n" \
"v_mfma_f32_16x16x32_bf16 v[240:243], a[8:11], v[100:103], v[240:243]\n" \
"v_mfma_f32_16x16x32_bf16 v[244:247], a[4:7], v[96:99], v[244:247]\n" \
"s_waitcnt lgkmcnt(3)\n" \
"ds_read_b128 v[124:127], v30 offset:31744\n" \
"v_mfma_f32_16x16x32_bf16 v[244:247], a[12:15], v[100:103], v[244:247]\n" \
"v_mfma_f32_16x16x32_bf16 v[248:251], a[0:3], v[104:107], v[248:251]\n" \
"ds_read_b128 v[96:99], v28 offset:16384\n" \
"v_mfma_f32_16x16x32_bf16 v[248:251], a[8:11], v[108:111], v[248:251]\n" \
"v_mfma_f32_16x16x32_bf16 v[252:255], a[4:7], v[104:107], v[252:255]\n" \
"s_waitcnt lgkmcnt(3)\n" \
"ds_read_b128 v[100:103], v28 offset:18432\n" \
"v_mfma_f32_16x16x32_bf16 v[252:255], a[12:15], v[108:111], v[252:255]\n" \
"v_mfma_f32_16x16x32_bf16 v[240:243], a[16:19], v[112:115], v[240:243]\n" \
"ds_read_b128 v[104:107], v28 offset:17408\n" \
"v_mfma_f32_16x16x32_bf16 v[240:243], a[24:27], v[116:119], v[240:243]\n" \
"v_mfma_f32_16x16x32_bf16 v[244:247], a[20:23], v[112:115], v[244:247]\n" \
"s_waitcnt lgkmcnt(3)\n" \
"ds_read_b128 v[108:111], v28 offset:19456\n" \
"s_add_u32 s97, s78, 8192\n" \
"v_mfma_f32_16x16x32_bf16 v[244:247], a[28:31], v[116:119], v[244:247]\n" \
"buffer_load_dwordx4 v[232:235], v4, s[36:39], s97 offen offset:2048 sc1\n" \
"v_mfma_f32_16x16x32_bf16 v[248:251], a[16:19], v[120:123], v[248:251]\n" \
"ds_read_b128 v[112:115], v28 offset:20480\n" \
"s_waitcnt vmcnt(1)\n" \
"v_mfma_f32_16x16x32_bf16 v[248:251], a[24:27], v[124:127], v[248:251]\n" \
"v_max_u32 v18, v168, v169\n" \
"v_max3_u32 v18, v18, v170, v171\n" \
"v_mfma_f32_16x16x32_bf16 v[252:255], a[20:23], v[120:123], v[252:255]\n" \
"s_waitcnt lgkmcnt(3)\n" \
"ds_read_b128 v[116:119], v28 offset:22528\n" \
"v_max3_u32 v18, v18, v172, v173\n" \
"v_mfma_f32_16x16x32_bf16 v[252:255], a[28:31], v[124:127], v[252:255]\n" \
"buffer_load_dwordx4 v[236:239], v4, s[36:39], s97 offen offset:3072 sc1\n" \
"v_mfma_f32_16x16x32_bf16 v[240:243], a[32:35], v[96:99], v[240:243]\n" \
"ds_read_b128 v[120:123], v28 offset:21504\n" \
"v_max3_u32 v18, v18, v174, v175\n" \
"v_mfma_f32_16x16x32_bf16 v[240:243], a[40:43], v[100:103], v[240:243]\n" \
"buffer_store_dwordx4 v[80:83], v184, s[32:35], s92 offen\n" \
"v_mfma_f32_16x16x32_bf16 v[244:247], a[36:39], v[96:99], v[244:247]\n" \
"s_waitcnt lgkmcnt(3)\n" \
"ds_read_b128 v[124:127], v28 offset:23552\n" \
"v_max3_u32 v18, v18, v224, v225\n" \
"v_mfma_f32_16x16x32_bf16 v[244:247], a[44:47], v[100:103], v[244:247]\n" \
"v_max3_u32 v18, v18, v226, v227\n" \
"v_max3_u32 v18, v18, v228, v229\n" \
"v_mfma_f32_16x16x32_bf16 v[248:251], a[32:35], v[104:107], v[248:251]\n" \
"ds_read_b128 v[96:99], v28 offset:24576\n" \
"v_max3_u32 v18, v18, v230, v231\n" \
"v_mfma_f32_16x16x32_bf16 v[248:251], a[40:43], v[108:111], v[248:251]\n" \
"v_cmp_eq_u32 vcc, -1, v18\n" \
"s_cbranch_vccnz LIM_%=\nLIMR_%=:\n" \
"v_mfma_f32_16x16x32_bf16 v[252:255], a[36:39], v[104:107], v[252:255]\n" \
"s_waitcnt lgkmcnt(3)\n" \
"ds_read_b128 v[100:103], v28 offset:26624\n" \
"v_mfma_f32_16x16x32_bf16 v[252:255], a[44:47], v[108:111], v[252:255]\n" \
"v_mfma_f32_16x16x32_bf16 v[240:243], a[48:51], v[112:115], v[240:243]\n" \
"ds_read_b128 v[104:107], v28 offset:25600\n" \
"v_mfma_f32_16x16x32_bf16 v[240:243], a[56:59], v[116:119], v[240:243]\n" \
"buffer_store_dwordx4 v[84:87], v184, s[32:35], s93 offen\n" \
"v_mfma_f32_16x16x32_bf16 v[244:247], a[52:55], v[112:115], v[244:247]\n" \
"s_waitcnt lgkmcnt(3)\n" \
"ds_read_b128 v[108:111], v28 offset:27648\n" \
"v_mfma_f32_16x16x32_bf16 v[244:247], a[60:63], v[116:119], v[244:247]\n" \
"v_mfma_f32_16x16x32_bf16 v[248:251], a[48:51], v[120:123], v[248:251]\n" \
"ds_read_b128 v[112:115], v28 offset:28672\n" \
"v_mfma_f32_16x16x32_bf16 v[248:251], a[56:59], v[124:127], v[248:251]\n" \
"v_mfma_f32_16x16x32_bf16 v[252:255], a[52:55], v[120:123], v[252:255]\n" \
"ds_read_b128 v[116:119], v28 offset:30720\n" \
"v_mfma_f32_16x16x32_bf16 v[252:255], a[60:63], v[124:127], v[252:255]\n" \
"s_waitcnt lgkmcnt(4)\n" \
"s_barrier\n" \
"v_mfma_f32_16x16x32_bf16 v[240:243], a[96:99], v[96:99], v[240:243]\n" \
"ds_read_b128 v[120:123], v28 offset:29696\n" \
"v_mfma_f32_16x16x32_bf16 v[240:243], a[104:107], v[100:103], v[240:243]\n" \
"v_mfma_f32_16x16x32_bf16 v[244:247], a[100:103], v[96:99], v[244:247]\n" \
"s_waitcnt lgkmcnt(3)\n" \
"ds_read_b128 v[124:127], v28 offset:31744\n" \
"v_mfma_f32_16x16x32_bf16 v[244:247], a[108:111], v[100:103], v[244:247]\n" \
"v_mfma_f32_16x16x32_bf16 v[248:251], a[96:99], v[104:107], v[248:251]\n" \
"v_mfma_f32_16x16x32_bf16 v[248:251], a[104:107], v[108:111], v[248:251]\n" \
"v_mfma_f32_16x16x32_bf16 v[252:255], a[100:103], v[104:107], v[252:255]\n" \
"s_waitcnt lgkmcnt(2)\n" \
"v_mfma_f32_16x16x32_bf16 v[252:255], a[108:111], v[108:111], v[252:255]\n" \
"v_mfma_f32_16x16x32_bf16 v[240:243], a[112:115], v[112:115], v[240:243]\n" \
"v_mfma_f32_16x16x32_bf16 v[240:243], a[120:123], v[116:119], v[240:243]\n" \
"v_mfma_f32_16x16x32_bf16 v[244:247], a[116:119], v[112:115], v[244:247]\n" \
"s_waitcnt lgkmcnt(0)\n" \
"v_mfma_f32_16x16x32_bf16 v[244:247], a[124:127], v[116:119], v[244:247]\n" \
"v_mfma_f32_16x16x32_bf16 v[248:251], a[112:115], v[120:123], v[248:251]\n" \
"v_mfma_f32_16x16x32_bf16 v[248:251], a[120:123], v[124:127], v[248:251]\n" \
"v_mfma_f32_16x16x32_bf16 v[252:255], a[116:119], v[120:123], v[252:255]\n" \
"v_mfma_f32_16x16x32_bf16 v[252:255], a[124:127], v[124:127], v[252:255]\n" \
"v_mfma_f32_16x16x32_bf16 v[240:243], v[24:27], v[168:171], v[240:243]\n" \
"v_mfma_f32_16x16x32_bf16 v[244:247], v[22:25], v[168:171], v[244:247]\n" \
"v_mfma_f32_16x16x32_bf16 v[248:251], v[24:27], v[172:175], v[248:251]\n" \
"v_mfma_f32_16x16x32_bf16 v[252:255], v[22:25], v[172:175], v[252:255]\n" \
"v_mfma_f32_16x16x32_bf16 v[240:243], v[24:27], v[224:227], v[240:243]\n" \
"v_mfma_f32_16x16x32_bf16 v[244:247], v[22:25], v[224:227], v[244:247]\n" \
"v_mfma_f32_16x16x32_bf16 v[248:251], v[24:27], v[228:231], v[248:251]\n" \
"v_mfma_f32_16x16x32_bf16 v[252:255], v[22:25], v[228:231], v[252:255]\n" \
"s_mov_b32 s92, s83\n" \
"s_add_u32 s93, s83, 32768\n" \
"s_waitcnt vmcnt(2)\n" \
"v_max_u32 v18, v232, v233\n" \
"v_max3_u32 v18, v18, v234, v235\n" \
"v_max3_u32 v18, v18, v236, v237\n" \
"v_max3_u32 v18, v18, v238, v239\n" \
"v_cmp_eq_u32 vcc, -1, v18\n" \
"s_cbranch_vccnz LIN_%=\nLINR_%=:\n" \
"v_mfma_f32_16x16x32_bf16 v[240:243], v[24:27], v[232:235], v[240:243]\n" \
"v_mfma_f32_16x16x32_bf16 v[244:247], v[22:25], v[232:235], v[244:247]\n" \
"v_mfma_f32_16x16x32_bf16 v[248:251], v[24:27], v[236:239], v[248:251]\n" \
"v_mfma_f32_16x16x32_bf16 v[252:255], v[22:25], v[236:239], v[252:255]\n" \
"s_nop 3\n" \
"v_pk_fma_f32 v[240:241], v[240:241], v[6:7], v[48:49] op_sel_hi:[1,0,1]\n" \
"v_pk_fma_f32 v[242:243], v[242:243], v[6:7], v[50:51] op_sel_hi:[1,0,1]\n" \
"v_pk_fma_f32 v[244:245], v[244:245], v[6:7], v[52:53] op_sel_hi:[1,0,1]\n" \
"v_pk_fma_f32 v[246:247], v[246:247], v[6:7], v[54:55] op_sel_hi:[1,0,1]\n" \
"v_cvt_pk_bf16_f32 v80, v240, v241\n" \
"v_cvt_pk_bf16_f32 v81, v242, v243\n" \
"v_cvt_pk_bf16_f32 v82, v244, v245\n" \
"v_cvt_pk_bf16_f32 v83, v246, v247\n" \
"s_nop 1\n" \
"v_permlane16_swap_b32 v80, v82\n" \
"v_permlane16_swap_b32 v81, v83\n" \
"buffer_store_dwordx4 v[80:83], v184, s[32:35], s92 offen\n" \
"v_pk_fma_f32 v[248:249], v[248:249], v[8:9], v[56:57] op_sel_hi:[1,0,1]\n" \
"v_pk_fma_f32 v[250:251], v[250:251], v[8:9], v[58:59] op_sel_hi:[1,0,1]\n" \
"v_pk_fma_f32 v[252:253], v[252:253], v[8:9], v[60:61] op_sel_hi:[1,0,1]\n" \
"v_pk_fma_f32 v[254:255], v[254:255], v[8:9], v[62:63] op_sel_hi:[1,0,1]\n" \
"v_cvt_pk_bf16_f32 v84, v248, v249\n" \
"v_cvt_pk_bf16_f32 v85, v250, v251\n" \
"v_cvt_pk_bf16_f32 v86, v252, v253\n" \
"v_cvt_pk_bf16_f32 v87, v254, v255\n" \
"s_nop 1\n" \
"v_permlane16_swap_b32 v84, v86\n" \
"v_permlane16_swap_b32 v85, v87\n" \
"buffer_store_dwordx4 v[84:87], v184, s[32:35], s93 offen\n" \
"s_branch LEND_%=\n" \
"LPUK_%=:\n" \
"s_mov_b32 s25, 0\n" \
"LPUKL_%=:\n" \
"s_sleep 2\n" \
"buffer_load_dwordx2 v[58:59], v57, s[36:39], 0 offen sc1\n" \
"s_add_u32 s25, s25, 1\n" \
"s_waitcnt vmcnt(0)\n" \
"v_cmp_ne_u64 vcc, s[14:15], v[58:59]\n" \
"s_cmp_gt_u32 s25, 262144\n" \
"s_cbranch_scc1 LPUKR_%=\n" \
"s_cbranch_vccnz LPUKL_%=\n" \
"s_branch LPUKR_%=\n" \
"LPUV_%=:\n" \
"s_mov_b32 s25, 0\n" \
"LPUVL_%=:\n" \
"s_sleep 2\n" \
"buffer_load_dwordx2 v[58:59], v57, s[36:39], 0 offen sc1\n" \
"s_add_u32 s25, s25, 1\n" \
"s_waitcnt vmcnt(0)\n" \
"v_cmp_ne_u64 vcc, s[14:15], v[58:59]\n" \
"s_cmp_gt_u32 s25, 262144\n" \
"s_cbranch_scc1 LPUVR_%=\n" \
"s_cbranch_vccnz LPUVL_%=\n" \
"s_branch LPUVR_%=\n" \
"LPK_%=:\n" \
"s_mov_b32 s25, 0\n" \
"LPKL_%=:\n" \
"s_sleep 2\n" \
"buffer_load_dwordx2 v[58:59], v57, s[36:39], 0 offen sc1\n" \
"s_add_u32 s25, s25, 1\n" \
"s_waitcnt vmcnt(0)\n" \
"v_cmp_ne_u64 vcc, s[14:15], v[58:59]\n" \
"s_cmp_gt_u32 s25, 262144\n" \
"s_cbranch_scc1 LPKR_%=\n" \
"s_cbranch_vccnz LPKL_%=\n" \
"s_branch LPKR_%=\n" \
"LSW_%=:\n" \
"v_swap_b32 v64, v80\n" \
"v_swap_b32 v65, v81\n" \
"v_swap_b32 v66, v82\n" \
"v_swap_b32 v67, v83\n" \
"v_swap_b32 v68, v84\n" \
"v_swap_b32 v69, v85\n" \
"v_swap_b32 v70, v86\n" \
"v_swap_b32 v71, v87\n" \
"v_swap_b32 v72, v88\n" \
"v_swap_b32 v73, v89\n" \
"v_swap_b32 v74, v90\n" \
"v_swap_b32 v75, v91\n" \
"v_swap_b32 v76, v92\n" \
"v_swap_b32 v77, v93\n" \
"v_swap_b32 v78, v94\n" \
"v_swap_b32 v79, v95\n" \
"s_branch LSWR_%=\n" \
"LIM_%=:\n" \
"s_mov_b32 s25, 0\n" \
"LIML_%=:\n" \
"s_sleep 1\n" \
"s_add_u32 s98, s78, 4096\n" \
"buffer_load_dwordx4 v[168:171], v4, s[36:39], s98 offen offset:2048 sc1\n" \
"buffer_load_dwordx4 v[172:175], v4, s[36:39], s98 offen offset:3072 sc1\n" \
"s_add_u32 s98, s78, 8192\n" \
"buffer_load_dwordx4 v[224:227], v4, s[36:39], s98 offen offset:0 sc1\n" \
"buffer_load_dwordx4 v[228:231], v4, s[36:39], s98 offen offset:1024 sc1\n" \
"s_add_u32 s25, s25, 1\n" \
"s_waitcnt vmcnt(0)\n" \
"v_max_u32 v18, v168, v169\n" \
"v_max3_u32 v18, v18, v170, v171\n" \
"v_max3_u32 v18, v18, v172, v173\n" \
"v_max3_u32 v18, v18, v174, v175\n" \
"v_max3_u32 v18, v18, v224, v225\n" \
"v_max3_u32 v18, v18, v226, v227\n" \
"v_max3_u32 v18, v18, v228, v229\n" \
"v_max3_u32 v18, v18, v230, v231\n" \
"v_cmp_eq_u32 vcc, -1, v18\n" \
"s_cmp_gt_u32 s25, 262144\n" \
"s_cbranch_scc1 LIMR_%=\n" \
"s_cbranch_vccnz LIML_%=\n" \
"s_branch LIMR_%=\n" \
"LIN_%=:\n" \
"s_mov_b32 s25, 0\n" \
"LINL_%=:\n" \
"s_sleep 1\n" \
"s_add_u32 s98, s78, 8192\n" \
"buffer_load_dwordx4 v[232:235], v4, s[36:39], s98 offen offset:2048 sc1\n" \
"buffer_load_dwordx4 v[236:239], v4, s[36:39], s98 offen offset:3072 sc1\n" \
"s_add_u32 s25, s25, 1\n" \
"s_waitcnt vmcnt(0)\n" \
"v_max_u32 v18, v232, v233\n" \
"v_max3_u32 v18, v18, v234, v235\n" \
"v_max3_u32 v18, v18, v236, v237\n" \
"v_max3_u32 v18, v18, v238, v239\n" \
"v_cmp_eq_u32 vcc, -1, v18\n" \
"s_cmp_gt_u32 s25, 262144\n" \
"s_cbranch_scc1 LINR_%=\n" \
"s_cbranch_vccnz LINL_%=\n" \
"s_branch LINR_%=\n" \
"LPZ_%=:\n" \
"s_mov_b32 s25, 0\n" \
"LPZL_%=:\n" \
"s_sleep 2\n" \
"buffer_load_dwordx2 v[18:19], v17, s[36:39], 0 offen sc1\n" \
"s_add_u32 s25, s25, 1\n" \
"s_waitcnt vmcnt(0)\n" \
"v_cmp_ne_u64 vcc, s[14:15], v[18:19]\n" \
"s_cmp_gt_u32 s25, 262144\n" \
"s_cbranch_scc1 LPZR_%=\n" \
"s_cbranch_vccnz LPZL_%=\n" \
"s_branch LPZR_%=\n" \
"LPS_%=:\n" \
"s_mov_b32 s25, 0\n" \
"LPSL_%=:\n" \
"s_sleep 2\n" \
"buffer_load_dwordx2 v[18:19], v17, s[36:39], 0 offen sc1\n" \
"s_add_u32 s25, s25, 1\n" \
"s_waitcnt vmcnt(0)\n" \
"v_cmp_ne_u64 vcc, s[14:15], v[18:19]\n" \
"s_cmp_gt_u32 s25, 262144\n" \
"s_cbranch_scc1 LPSR_%=\n" \
"s_cbranch_vccnz LPSL_%=\n" \
"s_branch LPSR_%=\n" \
"LPL_%=:\n" \
"s_mov_b32 s25, 0\n" \
"LPLL_%=:\n" \
"s_sleep 2\n" \
"buffer_load_dwordx2 v[18:19], v17, s[36:39], 0 offen sc1\n" \
"s_add_u32 s25, s25, 1\n" \
"s_waitcnt vmcnt(0)\n" \
"v_cmp_ne_u64 vcc, s[14:15], v[18:19]\n" \
"s_cmp_gt_u32 s25, 262144\n" \
"s_cbranch_scc1 LPLR_%=\n" \
"s_cbranch_vccnz LPLL_%=\n" \
"s_branch LPLR_%=\n" \
"LPE_%=:\n" \
"s_mov_b32 s25, 0\n" \
"LPEL_%=:\n" \
"s_sleep 2\n" \
"buffer_load_dwordx2 v[18:19], v17, s[36:39], 0 offen sc1\n" \
"s_add_u32 s25, s25, 1\n" \
"s_waitcnt vmcnt(0)\n" \
"v_cmp_ne_u64 vcc, s[14:15], v[18:19]\n" \
"s_cmp_gt_u32 s25, 262144\n" \
"s_cbranch_scc1 LPER_%=\n" \
"s_cbranch_vccnz LPEL_%=\n" \
"s_branch LPER_%=\n" \
"LPE0_%=:\n" \
"s_mov_b32 s25, 0\n" \
"LPE0L_%=:\n" \
"s_sleep 2\n" \
"buffer_load_dwordx2 v[18:19], v17, s[36:39], 0 offen sc1\n" \
"s_add_u32 s25, s25, 1\n" \
"s_waitcnt vmcnt(0)\n" \
"v_cmp_ne_u64 vcc, s[14:15], v[18:19]\n" \
"s_cmp_gt_u32 s25, 262144\n" \
"s_cbranch_scc1 LPE0R_%=\n" \
"s_cbranch_vccnz LPE0L_%=\n" \
"s_branch LPE0R_%=\n" \
"LFBA_%=:\n" \
"s_mov_b32 s99, 0\n" \
"s_branch LFB_%=\n" \
"LFBC_%=:\n" \
"s_mov_b32 s99, 2\n" \
"s_branch LFB_%=\n" \
"LFBB_%=:\n" \
"s_mov_b32 s99, 1\n" \
"LFB_%=:\n" \
"s_waitcnt vmcnt(0) lgkmcnt(0)\n" \
"s_nop 15\n" \
"s_lshl_b32 s25, %1, 2\n" \
"s_add_u32 s25, s25, %2\n" \
"s_lshl_b32 s25, s25, 13\n" \
"s_add_u32 s25, s25, 16777216\n" \
"buffer_store_dwordx4 v[64:67], v4, s[36:39], s25 offen offset:0\n" \
"buffer_store_dwordx4 v[68:71], v4, s[36:39], s25 offen offset:1024\n" \
"buffer_store_dwordx4 v[72:75], v4, s[36:39], s25 offen offset:2048\n" \
"buffer_store_dwordx4 v[76:79], v4, s[36:39], s25 offen offset:3072\n" \
"buffer_store_dwordx4 v[80:83], v7, s[36:39], s25 offen offset:0\n" \
"buffer_store_dwordx4 v[84:87], v7, s[36:39], s25 offen offset:1024\n" \
"buffer_store_dwordx4 v[88:91], v7, s[36:39], s25 offen offset:2048\n" \
"buffer_store_dwordx4 v[92:95], v7, s[36:39], s25 offen offset:3072\n" \
"s_add_u32 s25, s25, 8388608\n" \
"buffer_store_dwordx4 v[96:99], v4, s[36:39], s25 offen offset:0\n" \
"buffer_store_dwordx4 v[100:103], v4, s[36:39], s25 offen offset:1024\n" \
"buffer_store_dwordx4 v[104:107], v4, s[36:39], s25 offen offset:2048\n" \
"buffer_store_dwordx4 v[108:111], v4, s[36:39], s25 offen offset:3072\n" \
"buffer_store_dwordx4 v[112:115], v7, s[36:39], s25 offen offset:0\n" \
"buffer_store_dwordx4 v[116:119], v7, s[36:39], s25 offen offset:1024\n" \
"buffer_store_dwordx4 v[120:123], v7, s[36:39], s25 offen offset:2048\n" \
"buffer_store_dwordx4 v[124:127], v7, s[36:39], s25 offen offset:3072\n" \
"s_add_u32 s25, s25, 58720256\n" \
"buffer_store_dwordx4 v[128:131], v4, s[36:39], s25 offen offset:0\n" \
"buffer_store_dwordx4 v[132:135], v4, s[36:39], s25 offen offset:1024\n" \
"buffer_store_dwordx4 v[136:139], v4, s[36:39], s25 offen offset:2048\n" \
"buffer_store_dwordx4 v[140:143], v4, s[36:39], s25 offen offset:3072\n" \
"buffer_store_dwordx4 v[144:147], v7, s[36:39], s25 offen offset:0\n" \
"buffer_store_dwordx4 v[148:151], v7, s[36:39], s25 offen offset:1024\n" \
"buffer_store_dwordx4 v[152:155], v7, s[36:39], s25 offen offset:2048\n" \
"buffer_store_dwordx4 v[156:159], v7, s[36:39], s25 offen offset:3072\n" \
"s_add_u32 s25, s25, 37748736\n" \
"buffer_store_dwordx4 v[160:163], v4, s[36:39], s25 offen offset:0\n" \
"buffer_store_dwordx4 v[164:167], v4, s[36:39], s25 offen offset:1024\n" \
"buffer_store_dwordx4 v[168:171], v4, s[36:39], s25 offen offset:2048\n" \
"buffer_store_dwordx4 v[172:175], v4, s[36:39], s25 offen offset:3072\n" \
"s_waitcnt vmcnt(0)\n" \
"v_writelane_b32 v172, vcc_lo, 0\n" \
"v_writelane_b32 v172, vcc_hi, 1\n" \
"v_writelane_b32 v172, s64, 2\n" \
"v_writelane_b32 v172, s65, 3\n" \
"v_writelane_b32 v172, s66, 4\n" \
"v_writelane_b32 v172, s67, 5\n" \
"v_writelane_b32 v172, s68, 6\n" \
"v_writelane_b32 v172, s69, 7\n" \
"v_writelane_b32 v172, s70, 8\n" \
"v_writelane_b32 v172, s71, 9\n" \
"v_writelane_b32 v172, s72, 10\n" \
"v_writelane_b32 v172, s73, 11\n" \
"s_cmp_eq_u32 s99, 1\n" \
"s_cselect_b32 s64, s51, s50\n" \
"s_cselect_b32 s65, 2, 4\n" \
"s_cselect_b64 s[54:55], s[54:55], 0\n" \
"s_lshr_b32 s64, s64, 18\n" \
"s_lshl_b32 s64, s64, 5\n" \
"v_mov_b32 v160, 0x3f803f80\n" \
"v_mov_b32 v161, 0x3f803f80\n" \
"v_mov_b32 v162, 0x3f803f80\n" \
"v_mov_b32 v163, 0x3f803f80\n" \
"LFG_%=:\n" \
"s_mov_b32 s69, 0\n" \
"v_bfe_u32 v167, v4, 4, 4\n" \
"v_lshrrev_b32 v168, 8, v4\n" \
"v_add_u32 v165, s64, v167\n" \
"s_lshl_b32 s67, s6, 8\n" \
"v_lshl_add_u32 v128, v167, 1, s67\n" \
"v_lshl_add_u32 v128, v168, 16, v128\n" \
"v_add_u32 v129, 0x2000, v128\n" \
"v_add_u32 v130, 0x2000, v129\n" \
"v_add_u32 v131, 0x2000, v130\n" \
"v_add_u32 v132, 0x2000, v131\n" \
"v_add_u32 v133, 0x2000, v132\n" \
"v_add_u32 v134, 0x2000, v133\n" \
"v_add_u32 v135, 0x2000, v134\n" \
"LFQ_%=:\n" \
"s_load_dwordx2 s[70:71], %0, 0x0\n" \
"s_load_dwordx2 s[72:73], %0, 0x38\n" \
"v_lshlrev_b32 v166, 11, v165\n" \
"v_lshl_add_u32 v166, v168, 4, v166\n" \
"s_mov_b32 s66, 0\n" \
"v_mov_b32 v136, 0\n" \
"v_mov_b32 v137, 0\n" \
"v_mov_b32 v138, 0\n" \
"v_mov_b32 v139, 0\n" \
"v_mov_b32 v140, 0\n" \
"v_mov_b32 v141, 0\n" \
"v_mov_b32 v142, 0\n" \
"v_mov_b32 v143, 0\n" \
"s_waitcnt lgkmcnt(0)\n" \
"LFK_%=:\n" \
"global_load_dwordx4 v[112:115], v166, s[70:71]\n" \
"global_load_ushort v96, v128, s[72:73] offset:0\n" \
"global_load_ushort v97, v129, s[72:73] offset:0\n" \
"global_load_ushort v98, v130, s[72:73] offset:0\n" \
"global_load_ushort v99, v131, s[72:73] offset:0\n" \
"global_load_ushort v100, v132, s[72:73] offset:0\n" \
"global_load_ushort v101, v133, s[72:73] offset:0\n" \
"global_load_ushort v102, v134, s[72:73] offset:0\n" \
"global_load_ushort v103, v135, s[72:73] offset:0\n" \
"global_load_ushort v104, v128, s[72:73] offset:32\n" \
"global_load_ushort v105, v129, s[72:73] offset:32\n" \
"global_load_ushort v106, v130, s[72:73] offset:32\n" \
"global_load_ushort v107, v131, s[72:73] offset:32\n" \
"global_load_ushort v108, v132, s[72:73] offset:32\n" \
"global_load_ushort v109, v133, s[72:73] offset:32\n" \
"global_load_ushort v110, v134, s[72:73] offset:32\n" \
"global_load_ushort v111, v135, s[72:73] offset:32\n" \
"v_add_u32 v166, 64, v166\n" \
"s_add_u32 s72, s72, 262144\ns_addc_u32 s73, s73, 0\n" \
"s_add_u32 s66, s66, 1\n" \
"s_waitcnt vmcnt(0)\n" \
"v_lshl_or_b32 v116, v97, 16, v96\n" \
"v_lshl_or_b32 v117, v99, 16, v98\n" \
"v_lshl_or_b32 v118, v101, 16, v100\n" \
"v_lshl_or_b32 v119, v103, 16, v102\n" \
"v_lshl_or_b32 v120, v105, 16, v104\n" \
"v_lshl_or_b32 v121, v107, 16, v106\n" \
"v_lshl_or_b32 v122, v109, 16, v108\n" \
"v_lshl_or_b32 v123, v111, 16, v110\n" \
"s_nop 1\n" \
"v_mfma_f32_16x16x32_bf16 v[136:139], v[116:119], v[112:115], v[136:139]\n" \
"v_mfma_f32_16x16x32_bf16 v[140:143], v[120:123], v[112:115], v[140:143]\n" \
"s_cmp_lt_u32 s66, 32\n" \
"s_cbranch_scc1 LFK_%=\n" \
"s_nop 7\n" \
"v_mov_b32 v144, v148\n" \
"v_mov_b32 v145, v149\n" \
"v_mov_b32 v146, v150\n" \
"v_mov_b32 v147, v151\n" \
"v_mov_b32 v148, v152\n" \
"v_mov_b32 v149, v153\n" \
"v_mov_b32 v150, v154\n" \
"v_mov_b32 v151, v155\n" \
"v_mov_b32 v152, v156\n" \
"v_mov_b32 v153, v157\n" \
"v_mov_b32 v154, v158\n" \
"v_mov_b32 v155, v159\n" \
"v_cvt_pk_bf16_f32 v156, v136, v137\n" \
"v_cvt_pk_bf16_f32 v157, v138, v139\n" \
"v_cvt_pk_bf16_f32 v158, v140, v141\n" \
"v_cvt_pk_bf16_f32 v159, v142, v143\n" \
"v_add_u32 v128, 64, v128\n" \
"v_add_u32 v129, 64, v129\n" \
"v_add_u32 v130, 64, v130\n" \
"v_add_u32 v131, 64, v131\n" \
"v_add_u32 v132, 64, v132\n" \
"v_add_u32 v133, 64, v133\n" \
"v_add_u32 v134, 64, v134\n" \
"v_add_u32 v135, 64, v135\n" \
"s_add_u32 s69, s69, 1\n" \
"s_cmp_lt_u32 s69, 4\n" \
"s_cbranch_scc1 LFQ_%=\n" \
"v_mov_b32 v64, 0\n" \
"v_mov_b32 v65, 0\n" \
"v_mov_b32 v66, 0\n" \
"v_mov_b32 v67, 0\n" \
"v_mov_b32 v68, 0\n" \
"v_mov_b32 v69, 0\n" \
"v_mov_b32 v70, 0\n" \
"v_mov_b32 v71, 0\n" \
"v_mov_b32 v72, 0\n" \
"v_mov_b32 v73, 0\n" \
"v_mov_b32 v74, 0\n" \
"v_mov_b32 v75, 0\n" \
"v_mov_b32 v76, 0\n" \
"v_mov_b32 v77, 0\n" \
"v_mov_b32 v78, 0\n" \
"v_mov_b32 v79, 0\n" \
"v_mov_b32 v80, 0\n" \
"v_mov_b32 v81, 0\n" \
"v_mov_b32 v82, 0\n" \
"v_mov_b32 v83, 0\n" \
"v_mov_b32 v84, 0\n" \
"v_mov_b32 v85, 0\n" \
"v_mov_b32 v86, 0\n" \
"v_mov_b32 v87, 0\n" \
"v_mov_b32 v88, 0\n" \
"v_mov_b32 v89, 0\n" \
"v_mov_b32 v90, 0\n" \
"v_mov_b32 v91, 0\n" \
"v_mov_b32 v92, 0\n" \
"v_mov_b32 v93, 0\n" \
"v_mov_b32 v94, 0\n" \
"v_mov_b32 v95, 0\n" \
"v_mov_b32 v132, 0\n" \
"v_mov_b32 v133, 0\n" \
"v_mov_b32 v134, 0\n" \
"v_mov_b32 v135, 0\n" \
"v_mov_b32 v164, 0xff800000\n" \
"v_mov_b32 v169, 0xff800000\n" \
"s_mov_b32 s69, 0\n" \
"s_lshr_b32 s68, s64, 5\n" \
"LFP_%=:\n" \
"s_mov_b32 s66, 0\n" \
"s_lshl_b32 s67, s6, 20\n" \
"s_add_u32 s67, s67, 33554432\n" \
"v_lshlrev_b32 v166, 2, v168\n" \
"v_sub_u32 v166, v165, v166\n" \
"LFT_%=:\n" \
"buffer_load_dwordx4 v[96:99], v4, s[36:39], s67 offen offset:0 sc1\n" \
"buffer_load_dwordx4 v[100:103], v4, s[36:39], s67 offen offset:1024 sc1\n" \
"buffer_load_dwordx4 v[104:107], v4, s[36:39], s67 offen offset:2048 sc1\n" \
"buffer_load_dwordx4 v[108:111], v4, s[36:39], s67 offen offset:3072 sc1\n" \
"buffer_load_dwordx4 v[112:115], v7, s[36:39], s67 offen offset:0 sc1\n" \
"buffer_load_dwordx4 v[116:119], v7, s[36:39], s67 offen offset:1024 sc1\n" \
"buffer_load_dwordx4 v[120:123], v7, s[36:39], s67 offen offset:2048 sc1\n" \
"buffer_load_dwordx4 v[124:127], v7, s[36:39], s67 offen offset:3072 sc1\n" \
"s_add_u32 s67, s67, 8192\n" \
"s_waitcnt vmcnt(0)\n" \
"v_mfma_f32_16x16x32_bf16 v[136:139], v[96:99], v[144:147], 0\n" \
"v_mfma_f32_16x16x32_bf16 v[136:139], v[100:103], v[148:151], v[136:139]\n" \
"v_mfma_f32_16x16x32_bf16 v[136:139], v[104:107], v[152:155], v[136:139]\n" \
"v_mfma_f32_16x16x32_bf16 v[136:139], v[108:111], v[156:159], v[136:139]\n" \
"v_mfma_f32_16x16x32_bf16 v[140:143], v[112:115], v[144:147], 0\n" \
"v_mfma_f32_16x16x32_bf16 v[140:143], v[116:119], v[148:151], v[140:143]\n" \
"v_mfma_f32_16x16x32_bf16 v[140:143], v[120:123], v[152:155], v[140:143]\n" \
"v_mfma_f32_16x16x32_bf16 v[140:143], v[124:127], v[156:159], v[140:143]\n" \
"s_nop 7\n" \
"v_cmp_gt_i32 vcc, 0, v166\n" \
"s_nop 1\n" \
"v_cndmask_b32 v136, v136, v169, vcc\n" \
"v_cmp_gt_i32 vcc, 1, v166\n" \
"s_nop 1\n" \
"v_cndmask_b32 v137, v137, v169, vcc\n" \
"v_cmp_gt_i32 vcc, 2, v166\n" \
"s_nop 1\n" \
"v_cndmask_b32 v138, v138, v169, vcc\n" \
"v_cmp_gt_i32 vcc, 3, v166\n" \
"s_nop 1\n" \
"v_cndmask_b32 v139, v139, v169, vcc\n" \
"v_cmp_gt_i32 vcc, 16, v166\n" \
"s_nop 1\n" \
"v_cndmask_b32 v140, v140, v169, vcc\n" \
"v_cmp_gt_i32 vcc, 17, v166\n" \
"s_nop 1\n" \
"v_cndmask_b32 v141, v141, v169, vcc\n" \
"v_cmp_gt_i32 vcc, 18, v166\n" \
"s_nop 1\n" \
"v_cndmask_b32 v142, v142, v169, vcc\n" \
"v_cmp_gt_i32 vcc, 19, v166\n" \
"s_nop 1\n" \
"v_cndmask_b32 v143, v143, v169, vcc\n" \
"s_cmp_lg_u32 s69, 0\n" \
"s_cbranch_scc1 LF2_%=\n" \
"v_max3_f32 v164, v164, v136, v137\n" \
"v_max3_f32 v164, v164, v138, v139\n" \
"v_max3_f32 v164, v164, v140, v141\n" \
"v_max3_f32 v164, v164, v142, v143\n" \
"s_branch LFN_%=\n" \
"LF2_%=:\n" \
"buffer_load_dwordx4 v[96:99], v4, s[36:39], s67 offen offset:0 sc1\n" \
"buffer_load_dwordx4 v[100:103], v4, s[36:39], s67 offen offset:1024 sc1\n" \
"buffer_load_dwordx4 v[104:107], v4, s[36:39], s67 offen offset:2048 sc1\n" \
"buffer_load_dwordx4 v[108:111], v4, s[36:39], s67 offen offset:3072 sc1\n" \
"buffer_load_dwordx4 v[112:115], v7, s[36:39], s67 offen offset:0 sc1\n" \
"buffer_load_dwordx4 v[116:119], v7, s[36:39], s67 offen offset:1024 sc1\n" \
"buffer_load_dwordx4 v[120:123], v7, s[36:39], s67 offen offset:2048 sc1\n" \
"buffer_load_dwordx4 v[124:127], v7, s[36:39], s67 offen offset:3072 sc1\n" \
"v_sub_f32 v136, v136, v164\n" \
"v_sub_f32 v137, v137, v164\n" \
"v_sub_f32 v138, v138, v164\n" \
"v_sub_f32 v139, v139, v164\n" \
"v_sub_f32 v140, v140, v164\n" \
"v_sub_f32 v141, v141, v164\n" \
"v_sub_f32 v142, v142, v164\n" \
"v_sub_f32 v143, v143, v164\n" \
"v_exp_f32 v136, v136\n" \
"v_exp_f32 v137, v137\n" \
"v_exp_f32 v138, v138\n" \
"v_exp_f32 v139, v139\n" \
"v_exp_f32 v140, v140\n" \
"v_exp_f32 v141, v141\n" \
"v_exp_f32 v142, v142\n" \
"v_exp_f32 v143, v143\n" \
"s_nop 0\n" \
"v_cvt_pk_bf16_f32 v128, v136, v137\n" \
"v_cvt_pk_bf16_f32 v129, v138, v139\n" \
"v_cvt_pk_bf16_f32 v130, v140, v141\n" \
"v_cvt_pk_bf16_f32 v131, v142, v143\n" \
"s_waitcnt vmcnt(0)\n" \
"s_nop 1\n" \
"v_mfma_f32_16x16x32_bf16 v[132:135], v[160:163], v[128:131], v[132:135]\n" \
"v_mfma_f32_16x16x32_bf16 v[64:67], v[96:99], v[128:131], v[64:67]\n" \
"v_mfma_f32_16x16x32_bf16 v[68:71], v[100:103], v[128:131], v[68:71]\n" \
"v_mfma_f32_16x16x32_bf16 v[72:75], v[104:107], v[128:131], v[72:75]\n" \
"v_mfma_f32_16x16x32_bf16 v[76:79], v[108:111], v[128:131], v[76:79]\n" \
"v_mfma_f32_16x16x32_bf16 v[80:83], v[112:115], v[128:131], v[80:83]\n" \
"v_mfma_f32_16x16x32_bf16 v[84:87], v[116:119], v[128:131], v[84:87]\n" \
"v_mfma_f32_16x16x32_bf16 v[88:91], v[120:123], v[128:131], v[88:91]\n" \
"v_mfma_f32_16x16x32_bf16 v[92:95], v[124:127], v[128:131], v[92:95]\n" \
"LFN_%=:\n" \
"s_add_u32 s67, s67, 8192\n" \
"v_subrev_u32 v166, 32, v166\n" \
"s_add_u32 s66, s66, 1\n" \
"s_cmp_le_u32 s66, s68\n" \
"s_cbranch_scc1 LFT_%=\n" \
"v_mov_b32 v170, v164\n" \
"s_nop 1\n" \
"v_permlane32_swap_b32 v164, v170\n" \
"s_nop 0\n" \
"v_max_f32 v164, v164, v170\n" \
"v_mov_b32 v170, v164\n" \
"s_nop 1\n" \
"v_permlane16_swap_b32 v164, v170\n" \
"s_nop 0\n" \
"v_max_f32 v164, v164, v170\n" \
"s_add_u32 s69, s69, 1\n" \
"s_cmp_lt_u32 s69, 2\n" \
"s_cbranch_scc1 LFP_%=\n" \
"s_nop 7\n" \
"v_rcp_f32 v170, v132\n" \
"s_lshl_b32 s67, s68, 5\n" \
"s_add_u32 s67, s67, s6\n" \
"s_lshl_b32 s67, s67, 13\n" \
"s_and_b32 s66, s64, 16\n" \
"s_lshl_b32 s66, s66, 6\n" \
"s_add_u32 s67, s67, s66\n" \
"v_mul_f32 v64, v170, v64\n" \
"v_mul_f32 v65, v170, v65\n" \
"v_mul_f32 v66, v170, v66\n" \
"v_mul_f32 v67, v170, v67\n" \
"v_mul_f32 v68, v170, v68\n" \
"v_mul_f32 v69, v170, v69\n" \
"v_mul_f32 v70, v170, v70\n" \
"v_mul_f32 v71, v170, v71\n" \
"v_mul_f32 v72, v170, v72\n" \
"v_mul_f32 v73, v170, v73\n" \
"v_mul_f32 v74, v170, v74\n" \
"v_mul_f32 v75, v170, v75\n" \
"v_mul_f32 v76, v170, v76\n" \
"v_mul_f32 v77, v170, v77\n" \
"v_mul_f32 v78, v170, v78\n" \
"v_mul_f32 v79, v170, v79\n" \
"v_mul_f32 v80, v170, v80\n" \
"v_mul_f32 v81, v170, v81\n" \
"v_mul_f32 v82, v170, v82\n" \
"v_mul_f32 v83, v170, v83\n" \
"v_mul_f32 v84, v170, v84\n" \
"v_mul_f32 v85, v170, v85\n" \
"v_mul_f32 v86, v170, v86\n" \
"v_mul_f32 v87, v170, v87\n" \
"v_mul_f32 v88, v170, v88\n" \
"v_mul_f32 v89, v170, v89\n" \
"v_mul_f32 v90, v170, v90\n" \
"v_mul_f32 v91, v170, v91\n" \
"v_mul_f32 v92, v170, v92\n" \
"v_mul_f32 v93, v170, v93\n" \
"v_mul_f32 v94, v170, v94\n" \
"v_mul_f32 v95, v170, v95\n" \
"v_cvt_pk_bf16_f32 v64, v64, v65\n" \
"v_cvt_pk_bf16_f32 v65, v66, v67\n" \
"v_cvt_pk_bf16_f32 v66, v68, v69\n" \
"v_cvt_pk_bf16_f32 v67, v70, v71\n" \
"v_cvt_pk_bf16_f32 v68, v72, v73\n" \
"v_cvt_pk_bf16_f32 v69, v74, v75\n" \
"v_cvt_pk_bf16_f32 v70, v76, v77\n" \
"v_cvt_pk_bf16_f32 v71, v78, v79\n" \
"v_cvt_pk_bf16_f32 v72, v80, v81\n" \
"v_cvt_pk_bf16_f32 v73, v82, v83\n" \
"v_cvt_pk_bf16_f32 v74, v84, v85\n" \
"v_cvt_pk_bf16_f32 v75, v86, v87\n" \
"v_cvt_pk_bf16_f32 v76, v88, v89\n" \
"v_cvt_pk_bf16_f32 v77, v90, v91\n" \
"v_cvt_pk_bf16_f32 v78, v92, v93\n" \
"v_cvt_pk_bf16_f32 v79, v94, v95\n" \
"buffer_store_dwordx4 v[64:67], v4, s[36:39], s67 offen offset:0 sc1\n" \
"buffer_store_dwordx4 v[68:71], v4, s[36:39], s67 offen offset:2048 sc1\n" \
"buffer_store_dwordx4 v[72:75], v7, s[36:39], s67 offen offset:0 sc1\n" \
"buffer_store_dwordx4 v[76:79], v7, s[36:39], s67 offen offset:2048 sc1\n" \
"s_waitcnt vmcnt(0)\n" \
"s_add_u32 s64, s64, 16\n" \
"s_sub_u32 s65, s65, 1\n" \
"s_cmp_lg_u32 s65, 0\n" \
"s_cbranch_scc1 LFG_%=\n" \
"v_readlane_b32 vcc_lo, v172, 0\n" \
"v_readlane_b32 vcc_hi, v172, 1\n" \
"v_readlane_b32 s64, v172, 2\n" \
"v_readlane_b32 s65, v172, 3\n" \
"v_readlane_b32 s66, v172, 4\n" \
"v_readlane_b32 s67, v172, 5\n" \
"v_readlane_b32 s68, v172, 6\n" \
"v_readlane_b32 s69, v172, 7\n" \
"v_readlane_b32 s70, v172, 8\n" \
"v_readlane_b32 s71, v172, 9\n" \
"v_readlane_b32 s72, v172, 10\n" \
"v_readlane_b32 s73, v172, 11\n" \
"s_lshl_b32 s25, %1, 2\n" \
"s_add_u32 s25, s25, %2\n" \
"s_lshl_b32 s25, s25, 13\n" \
"s_add_u32 s25, s25, 16777216\n" \
"buffer_load_dwordx4 v[64:67], v4, s[36:39], s25 offen offset:0\n" \
"buffer_load_dwordx4 v[68:71], v4, s[36:39], s25 offen offset:1024\n" \
"buffer_load_dwordx4 v[72:75], v4, s[36:39], s25 offen offset:2048\n" \
"buffer_load_dwordx4 v[76:79], v4, s[36:39], s25 offen offset:3072\n" \
"buffer_load_dwordx4 v[80:83], v7, s[36:39], s25 offen offset:0\n" \
"buffer_load_dwordx4 v[84:87], v7, s[36:39], s25 offen offset:1024\n" \
"buffer_load_dwordx4 v[88:91], v7, s[36:39], s25 offen offset:2048\n" \
"buffer_load_dwordx4 v[92:95], v7, s[36:39], s25 offen offset:3072\n" \
"s_add_u32 s25, s25, 8388608\n" \
"buffer_load_dwordx4 v[96:99], v4, s[36:39], s25 offen offset:0\n" \
"buffer_load_dwordx4 v[100:103], v4, s[36:39], s25 offen offset:1024\n" \
"buffer_load_dwordx4 v[104:107], v4, s[36:39], s25 offen offset:2048\n" \
"buffer_load_dwordx4 v[108:111], v4, s[36:39], s25 offen offset:3072\n" \
"buffer_load_dwordx4 v[112:115], v7, s[36:39], s25 offen offset:0\n" \
"buffer_load_dwordx4 v[116:119], v7, s[36:39], s25 offen offset:1024\n" \
"buffer_load_dwordx4 v[120:123], v7, s[36:39], s25 offen offset:2048\n" \
"buffer_load_dwordx4 v[124:127], v7, s[36:39], s25 offen offset:3072\n" \
"s_add_u32 s25, s25, 58720256\n" \
"buffer_load_dwordx4 v[128:131], v4, s[36:39], s25 offen offset:0\n" \
"buffer_load_dwordx4 v[132:135], v4, s[36:39], s25 offen offset:1024\n" \
"buffer_load_dwordx4 v[136:139], v4, s[36:39], s25 offen offset:2048\n" \
"buffer_load_dwordx4 v[140:143], v4, s[36:39], s25 offen offset:3072\n" \
"buffer_load_dwordx4 v[144:147], v7, s[36:39], s25 offen offset:0\n" \
"buffer_load_dwordx4 v[148:151], v7, s[36:39], s25 offen offset:1024\n" \
"buffer_load_dwordx4 v[152:155], v7, s[36:39], s25 offen offset:2048\n" \
"buffer_load_dwordx4 v[156:159], v7, s[36:39], s25 offen offset:3072\n" \
"s_add_u32 s25, s25, 37748736\n" \
"buffer_load_dwordx4 v[160:163], v4, s[36:39], s25 offen offset:0\n" \
"buffer_load_dwordx4 v[164:167], v4, s[36:39], s25 offen offset:1024\n" \
"buffer_load_dwordx4 v[168:171], v4, s[36:39], s25 offen offset:2048\n" \
"buffer_load_dwordx4 v[172:175], v4, s[36:39], s25 offen offset:3072\n" \
"s_waitcnt vmcnt(0)\n" \
"s_cmp_eq_u32 s99, 0\n" \
"s_cbranch_scc1 LFBAR_%=\n" \
"s_cmp_eq_u32 s99, 1\n" \
"s_cbranch_scc1 LFBBR_%=\n" \
"s_branch LFBCR_%=\n" \
"LEND_%=:\n" \
"s_waitcnt vmcnt(0) lgkmcnt(0)\n" \
""
#define QKV_ASM_CLOBBERS "v1", "v2", "v3", "v4", "v5", "v6", "v7", "v8", "v9", "v10", "v11", "v12", "v13", "v14", "v15", "v16", "v17", "v18", "v19", "v20", "v21", "v22", "v23", "v24", "v25", "v26", "v27", "v28", "v29", "v30", "v31", "v32", "v33", "v34", "v35", "v36", "v37", "v38", "v39", "v40", "v41", "v42", "v43", "v44", "v45", "v46", "v47", "v48", "v49", "v50", "v51", "v52", "v53", "v54", "v55", "v56", "v57", "v58", "v59", "v60", "v61", "v62", "v63", "v64", "v65", "v66", "v67", "v68", "v69", "v70", "v71", "v72", "v73", "v74", "v75", "v76", "v77", "v78", "v79", "v80", "v81", "v82", "v83", "v84", "v85", "v86", "v87", "v88", "v89", "v90", "v91", "v92", "v93", "v94", "v95", "v96", "v97", "v98", "v99", "v100", "v101", "v102", "v103", "v104", "v105", "v106", "v107", "v108", "v109", "v110", "v111", "v112", "v113", "v114", "v115", "v116", "v117", "v118", "v119", "v120", "v121", "v122", "v123", "v124", "v125", "v126", "v127", "v128", "v129", "v130", "v131", "v132", "v133", "v134", "v135", "v136", "v137", "v138", "v139", "v140", "v141", "v142", "v143", "v144", "v145", "v146", "v147", "v148", "v149", "v150", "v151", "v152", "v153", "v154", "v155", "v156", "v157", "v158", "v159", "v160", "v161", "v162", "v163", "v164", "v165", "v166", "v167", "v168", "v169", "v170", "v171", "v172", "v173", "v174", "v175", "v176", "v177", "v178", "v179", "v180", "v181", "v182", "v183", "v184", "v185", "v186", "v187", "v188", "v189", "v190", "v191", "v192", "v193", "v194", "v195", "v196", "v197", "v198", "v199", "v200", "v201", "v202", "v203", "v204", "v205", "v206", "v207", "v208", "v209", "v210", "v211", "v212", "v213", "v214", "v215", "v216", "v217", "v218", "v219", "v220", "v221", "v222", "v223", "v224", "v225", "v226", "v227", "v228", "v229", "v230", "v231", "v232", "v233", "v234", "v235", "v236", "v237", "v238", "v239", "v240", "v241", "v242", "v243", "v244", "v245", "v246", "v247", "v248", "v249", "v250", "v251", "v252", "v253", "v254", "v255", "a0", "a1", "a2", "a3", "a4", "a5", "a6", "a7", "a8", "a9", "a10", "a11", "a12", "a13", "a14", "a15", "a16", "a17", "a18", "a19", "a20", "a21", "a22", "a23", "a24", "a25", "a26", "a27", "a28", "a29", "a30", "a31", "a32", "a33", "a34", "a35", "a36", "a37", "a38", "a39", "a40", "a41", "a42", "a43", "a44", "a45", "a46", "a47", "a48", "a49", "a50", "a51", "a52", "a53", "a54", "a55", "a56", "a57", "a58", "a59", "a60", "a61", "a62", "a63", "a64", "a65", "a66", "a67", "a68", "a69", "a70", "a71", "a72", "a73", "a74", "a75", "a76", "a77", "a78", "a79", "a80", "a81", "a82", "a83", "a84", "a85", "a86", "a87", "a88", "a89", "a90", "a91", "a92", "a93", "a94", "a95", "a96", "a97", "a98", "a99", "a100", "a101", "a102", "a103", "a104", "a105", "a106", "a107", "a108", "a109", "a110", "a111", "a112", "a113", "a114", "a115", "a116", "a117", "a118", "a119", "a120", "a121", "a122", "a123", "a124", "a125", "a126", "a127", "a128", "a129", "a130", "a131", "a132", "a133", "a134", "a135", "a136", "a137", "a138", "a139", "a140", "a141", "a142", "a143", "a144", "a145", "a146", "a147", "a148", "a149", "a150", "a151", "a152", "a153", "a154", "a155", "a156", "a157", "a158", "a159", "a160", "a161", "a162", "a163", "a164", "a165", "a166", "a167", "a168", "a169", "a170", "a171", "a172", "a173", "a174", "a175", "a176", "a177", "a178", "a179", "a180", "a181", "a182", "a183", "a184", "a185", "a186", "a187", "a188", "a189", "a190", "a191", "a192", "a193", "a194", "a195", "a196", "a197", "a198", "a199", "a200", "a201", "a202", "a203", "a204", "a205", "a206", "a207", "a208", "a209", "a210", "a211", "a212", "a213", "a214", "a215", "a216", "a217", "a218", "a219", "a220", "a221", "a222", "a223", "a224", "a225", "a226", "a227", "a228", "a229", "a230", "a231", "a232", "a233", "a234", "a235", "a236", "a237", "a238", "a239", "a240", "a241", "a242", "a243", "a244", "a245", "a246", "a247", "a248", "a249", "a250", "a251", "a252", "a253", "a254", "a255", "s4", "s5", "s6", "s7", "s8", "s9", "s10", "s11", "s12", "s13", "s14", "s15", "s16", "s17", "s18", "s19", "s20", "s21", "s22", "s23", "s24", "s25", "s26", "s27", "s28", "s29", "s30", "s31", "s32", "s33", "s34", "s35", "s36", "s37", "s38", "s39", "s40", "s41", "s42", "s43", "s44", "s45", "s46", "s47", "s48", "s49", "s50", "s51", "s52", "s53", "s54", "s55", "s56", "s57", "s58", "s59", "s60", "s61", "s62", "s63", "s64", "s65", "s66", "s67", "s68", "s69", "s70", "s71", "s72", "s73", "s74", "s75", "s76", "s77", "s78", "s79", "s80", "s81", "s82", "s83", "s84", "s85", "s86", "s87", "s88", "s89", "s90", "s91", "s92", "s93", "s94", "s95", "s96", "s97", "s98", "s99", "s100", "s101", "vcc", "scc", "memory"
constexpr int Q_LDS = 163840;
__global__ __launch_bounds__(256, 1) void fused_kernel(const void* x, const void* k, const void* v, const void* shk, const void* ssmk, const void* ssmD, const void* ckv,
                                                       const void* wq, const void* wuk, const void* wuv, const void* wo, const void* gw, void* out, void* ws) {
    const int w = __builtin_amdgcn_readfirstlane(threadIdx.x >> 6);
    const unsigned long long ka = (unsigned long long)__builtin_amdgcn_kernarg_segment_ptr();
    asm volatile(QKV_ASM_TEXT :: "s"(ka), "s"(blockIdx.x), "s"(w) : QKV_ASM_CLOBBERS);
}

extern "C" void kernel_launch(void* const* d_in, const int* in_sizes, int n_in, void* d_out, int out_size, void* d_ws, size_t ws_size,
                              hipStream_t stream) {
    (void)hipFuncSetAttribute((const void*)fused_kernel, hipFuncAttributeMaxDynamicSharedMemorySize, Q_LDS);
    fused_kernel<<<256, 256, Q_LDS, stream>>>(d_in[0], d_in[1], d_in[2], d_in[3], d_in[4], d_in[5], d_in[7], d_in[8], d_in[9], d_in[10], d_in[11], d_in[12], d_out, d_ws);
}
```

```cpp
#include <hip/hip_runtime.h>
#include <stdint.h>
#define QKV_ASM_TEXT \
"s_nop 0\n" \
"s_load_dwordx16 s[64:79], %0, 0x0\n" \
"s_load_dwordx8 s[80:87], %0, 0x40\n" \
"s_load_dwordx4 s[88:91], %0, 0x60\n" \
"v_mbcnt_lo_u32_b32 v23, -1, 0\n" \
"v_mbcnt_hi_u32_b32 v23, -1, v23\n" \
"s_bfe_u32 s58, %1, 0x30003\n" \
"s_and_b32 s59, %1, 7\n" \
"s_lshl_b32 s59, s59, 1\n" \
"s_bfe_u32 s60, %1, 0x10006\n" \
"s_add_u32 s59, s59, s60\n" \
"v_lshlrev_b32 v4, 4, v23\n" \
"s_getpc_b64 s[40:41]\nLCTBEG_%=:\n" \
"s_add_u32 s40, s40, LBEG_%=-LCTBEG_%=\ns_addc_u32 s41, s41, 0\n" \
"s_and_b32 s41, s41, 0xffff\n" \
"s_mov_b32 s42, LEND_%=-LBEG_%=\n" \
"s_mov_b32 s43, 0x00020000\n" \
"s_lshr_b32 s62, %1, 3\n" \
"s_lshl_b32 s62, s62, 2\n" \
"s_add_u32 s62, s62, %2\n" \
"s_and_b32 s62, s62, 31\n" \
"s_lshl_b32 s62, s62, 13\n" \
"v_lshlrev_b32 v64, 3, v4\n" \
"buffer_load_dword v64, v64, s[40:43], s62 offen\n" \
"s_lshl_b32 s60, %2, 2\n" \
"v_lshrrev_b32 v24, 4, v23\n" \
"v_add_u32 v25, s60, v24\n" \
"v_xor_b32 v25, v25, v23\n" \
"v_and_b32 v25, 7, v25\n" \
"s_lshl_b32 s61, %2, 3\n" \
"v_lshrrev_b32 v26, 3, v23\n" \
"v_add_u32 v26, s61, v26\n" \
"v_lshlrev_b32 v26, 10, v26\n" \
"v_lshl_add_u32 v13, v25, 4, v26\n" \
"v_add_u32 v63, v13, v26\n" \
"v_add_u32 v14, 0x8000, v13\n" \
"v_add_u32 v15, 0x8000, v14\n" \
"v_add_u32 v16, 0x8000, v15\n" \
"v_add_u32 v17, 0x8000, v16\n" \
"v_add_u32 v18, 0x8000, v17\n" \
"v_add_u32 v19, 0x8000, v18\n" \
"v_add_u32 v20, 0x8000, v19\n" \
"v_bfe_u32 v25, v23, 2, 3\n" \
"v_add_u32 v25, s61, v25\n" \
"v_lshlrev_b32 v25, 13, v25\n" \
"s_lshl_b32 s61, %2, 1\n" \
"v_and_b32 v26, 1, v24\n" \
"v_add_u32 v26, s61, v26\n" \
"v_xor_b32 v26, v26, v23\n" \
"v_and_b32 v26, 3, v26\n" \
"v_lshrrev_b32 v27, 5, v23\n" \
"v_lshl_add_u32 v26, v27, 2, v26\n" \
"v_lshl_add_u32 v21, v26, 4, v25\n" \
"s_lshl_b32 s50, %2, 10\n" \
"s_add_u32 s50, s50, 65536\n" \
"s_lshl_b32 s22, %2, 12\n" \
"s_lshl_b32 s51, %2, 11\n" \
"s_mov_b32 s31, 0x00020000\n" \
"s_mov_b32 s35, 0x00020000\n" \
"s_mov_b32 s30, 262144\n" \
"s_lshl_b32 s60, s58, 18\n" \
"s_lshl_b32 s61, s59, 9\n" \
"s_sub_u32 s34, 4194304, s61\n" \
"s_mov_b32 s46, s60\n" \
"s_mov_b32 s47, s61\n" \
"s_lshl_b32 s57, s59, 1\n" \
"s_and_b32 s60, %2, 1\n" \
"s_add_u32 s57, s57, s60\n" \
"s_lshl_b32 s57, s57, 20\n" \
"s_lshl_b32 s60, s58, 17\n" \
"s_add_u32 s57, s57, s60\n" \
"s_lshr_b32 s60, %2, 1\n" \
"s_lshl_b32 s60, s60, 16\n" \
"s_add_u32 s57, s57, s60\n" \
"s_add_u32 s57, s57, 33554432\n" \
"s_lshl_b32 s24, s59, 7\n" \
"s_lshl_b32 s60, s58, 3\n" \
"s_add_u32 s24, s24, s60\n" \
"s_lshl_b32 s60, %2, 27\n" \
"s_add_u32 s24, s24, s60\n" \
"s_and_b32 s23, %1, 7\n" \
"s_lshl_b32 s23, s23, 8\n" \
"s_add_u32 s23, s23, s60\n" \
"s_and_b32 s60, %1, 0xf8\n" \
"s_add_u32 s23, s23, s60\n" \
"s_add_u32 s23, s23, 94380032\n" \
"s_cmp_lt_u32 %1, 128\n" \
"s_cselect_b32 s60, 0, 8192\n" \
"s_cselect_b32 s61, 0, 64\n" \
"s_cselect_b32 s62, 14, 14\n" \
"s_cselect_b32 s63, 10, 11\n" \
"s_cselect_b32 s98, 12, 10\n" \
"s_cselect_b32 s20, 0x3e0293ee, 1.0\n" \
"s_add_u32 s57, s57, s60\n" \
"s_add_u32 s24, s24, s61\n" \
"s_add_u32 s24, s24, 94375936\n" \
"s_lshl_b32 s92, 1, s62\n" \
"s_lshl_b32 s93, 1, s63\n" \
"s_lshl_b32 s98, 1, s98\n" \
"s_and_b32 s6, %1, 7\n" \
"s_lshl_b32 s8, s6, 5\n" \
"s_lshl_b32 s6, s6, 2\n" \
"s_lshr_b32 s58, %1, 6\n" \
"s_add_u32 s6, s6, s58\n" \
"s_lshr_b32 s58, %1, 3\n" \
"s_add_u32 s8, s8, s58\n" \
"s_and_b32 s7, s58, 7\n" \
"s_lshl_b32 s58, s6, 8\n" \
"s_mov_b32 s94, s58\n" \
"s_sub_u32 s49, 8388608, s58\n" \
"s_waitcnt lgkmcnt(0)\n" \
"s_cmp_lt_u32 %1, 128\n" \
"s_cselect_b64 s[32:33], s[80:81], s[82:83]\n" \
"s_add_u32 s28, s76, s46\ns_addc_u32 s29, s77, 0\n" \
"s_and_b32 s29, s29, 0xffff\n" \
"s_add_u32 s32, s32, s47\ns_addc_u32 s33, s33, 0\n" \
"s_and_b32 s33, s33, 0xffff\n" \
"s_mov_b32 s99, 94371840\n" \
"s_load_dwordx2 s[14:15], s[90:91], s99\n" \
"s_mov_b32 s46, 0\n" \
"s_add_u32 m0, s50, 65536\n" \
"s_mov_b64 s[36:37], s[90:91]\n" \
"buffer_load_dwordx4 v13, s[28:31], s46 offen lds\n" \
"s_and_b32 s37, s37, 0xffff\n" \
"s_mov_b32 s38, 134217728\n" \
"s_mov_b32 s39, 0x00020000\n" \
"v_lshrrev_b32 v23, 4, v4\n" \
"s_add_u32 m0, s50, 69632\n" \
"v_and_b32 v24, 15, v23\n" \
"buffer_load_dwordx4 v14, s[28:31], s46 offen lds\n" \
"v_lshrrev_b32 v25, 4, v23\n" \
"s_lshr_b32 s58, %2, 1\n" \
"s_lshl_b32 s58, s58, 14\n" \
"s_and_b32 s54, %2, 1\n" \
"s_add_u32 m0, s50, 73728\n" \
"s_lshl_b32 s54, s54, 13\n" \
"buffer_load_dwordx4 v15, s[28:31], s46 offen lds\n" \
"s_add_u32 s58, s58, 65536\n" \
"v_lshrrev_b32 v26, 1, v24\n" \
"v_xor_b32 v26, v26, v25\n" \
"v_lshlrev_b32 v26, 4, v26\n" \
"s_add_u32 m0, s50, 77824\n" \
"v_lshl_add_u32 v26, v24, 7, v26\n" \
"buffer_load_dwordx4 v16, s[28:31], s46 offen lds\n" \
"v_add_u32 v5, s58, v26\n" \
"v_xor_b32 v6, 64, v5\n" \
"v_add_u32 v7, 0x10000, v5\n" \
"v_add_u32 v8, 0x10000, v6\n" \
"s_mov_b32 s47, 0\n" \
"s_add_u32 m0, s22, 0\n" \
"s_and_b32 s58, %2, 1\n" \
"buffer_load_dwordx4 v21, s[32:35], s47 offen offset:0 nt lds\n" \
"s_lshl_b32 s58, s58, 11\n" \
"v_lshrrev_b32 v26, 2, v24\n" \
"v_lshlrev_b32 v26, 6, v26\n" \
"v_and_b32 v27, 1, v23\n" \
"s_mov_b32 s46, 0\n" \
"s_add_u32 m0, s50, 81920\n" \
"v_lshl_add_u32 v26, v27, 3, v26\n" \
"buffer_load_dwordx4 v17, s[28:31], s46 offen lds\n" \
"v_bfe_u32 v27, v23, 1, 1\n" \
"v_lshlrev_b32 v28, 1, v25\n" \
"v_xor_b32 v27, v27, v28\n" \
"v_and_b32 v27, 3, v27\n" \
"s_add_u32 m0, s22, 1792\n" \
"v_lshl_add_u32 v26, v27, 4, v26\n" \
"buffer_load_dwordx4 v21, s[32:35], s47 offen offset:256 nt lds\n" \
"v_lshl_add_u32 v9, v25, 12, v26\n" \
"v_add_u32 v9, s58, v9\n" \
"v_lshl_add_u32 v62, v25, 11, s58\n" \
"v_xor_b32 v10, 0x110, v9\n" \
"s_add_u32 m0, s50, 86016\n" \
"v_xor_b32 v11, 32, v9\n" \
"buffer_load_dwordx4 v18, s[28:31], s46 offen lds\n" \
"v_xor_b32 v12, 0x130, v9\n" \
"s_and_b32 s62, %1, 7\n" \
"s_lshl_b32 s62, s62, 7\n" \
"s_lshl_b32 s63, %2, 6\n" \
"s_mov_b32 s47, 262144\n" \
"s_add_u32 m0, s22, 1024\n" \
"s_and_b32 s63, s63, 127\n" \
"buffer_load_dwordx4 v21, s[32:35], s47 offen offset:0 nt lds\n" \
"s_add_u32 s62, s62, s63\n" \
"v_bfe_u32 v24, v4, 4, 4\n" \
"v_lshl_add_u32 v24, v24, 2, s62\n" \
"s_lshr_b32 s62, %2, 1\n" \
"s_add_u32 m0, s50, 90112\n" \
"s_lshl_b32 s62, s62, 2\n" \
"buffer_load_dwordx4 v19, s[28:31], s46 offen lds\n" \
"s_and_b32 s63, %1, 0xf8\n" \
"s_add_u32 s63, s63, s62\n" \
"v_lshrrev_b32 v25, 8, v4\n" \
"v_lshrrev_b32 v23, 7, v4\n" \
"s_add_u32 m0, s22, 2816\n" \
"v_add_u32 v25, s63, v25\n" \
"buffer_load_dwordx4 v21, s[32:35], s47 offen offset:256 nt lds\n" \
"v_lshlrev_b32 v26, 14, v25\n" \
"v_lshl_add_u32 v59, v24, 1, v26\n" \
"v_add_u32 v54, 0xffffe800, v59\n" \
"v_add_u32 v55, 0xfffff800, v59\n" \
"s_add_u32 m0, s50, 94208\n" \
"v_lshlrev_b32 v60, 12, v24\n" \
"buffer_load_dwordx4 v20, s[28:31], s46 offen lds\n" \
"v_lshl_add_u32 v60, v25, 4, v60\n" \
"v_lshlrev_b32 v27, 3, v24\n" \
"s_lshl_b32 s62, %1, 2\n" \
"s_add_u32 s62, s62, %2\n" \
"s_mov_b32 s47, 0\n" \
"s_add_u32 m0, s22, 16256\n" \
"s_lshl_b32 s53, s62, 12\n" \
"buffer_load_dwordx4 v21, s[32:35], s47 offen offset:128 nt lds\n" \
"s_and_b32 s63, s62, 7\n" \
"s_lshl_b32 s63, s63, 4\n" \
"s_add_u32 s63, s63, 71303168\n" \
"v_and_b32 v61, 4, v23\n" \
"s_add_u32 m0, s22, 18048\n" \
"v_lshl_add_u32 v61, v61, 1, s63\n" \
"buffer_load_dwordx4 v21, s[32:35], s47 offen offset:384 nt lds\n" \
"s_bfe_u32 s63, s62, 0x10003\n" \
"s_lshl_b32 s63, s63, 2\n" \
"s_lshr_b32 s62, s62, 4\n" \
"s_lshl_b32 s62, s62, 7\n" \
"s_mov_b32 s47, 262144\n" \
"s_add_u32 m0, s22, 17280\n" \
"s_add_u32 s62, s62, s63\n" \
"buffer_load_dwordx4 v21, s[32:35], s47 offen offset:128 nt lds\n" \
"v_add_u32 v61, s62, v61\n" \
"v_lshlrev_b32 v2, 3, v4\n" \
"v_and_b32 v1, 0x380, v2\n" \
"v_lshrrev_b32 v23, 7, v4\n" \
"s_add_u32 m0, s22, 19072\n" \
"v_lshl_add_u32 v1, v23, 13, v1\n" \
"buffer_load_dwordx4 v21, s[32:35], s47 offen offset:384 nt lds\n" \
"s_lshr_b32 s58, %1, 3\n" \
"s_lshl_b32 s58, s58, 2\n" \
"s_add_u32 s58, s58, %2\n" \
"s_and_b32 s60, %1, 7\n" \
"s_mov_b32 s47, 524288\n" \
"s_add_u32 m0, s22, 32768\n" \
"s_lshl_b32 s60, s60, 10\n" \
"buffer_load_dwordx4 v21, s[32:35], s47 offen offset:0 nt lds\n" \
"s_lshl_b32 s59, s58, 14\n" \
"s_add_u32 s59, s59, s60\n" \
"s_mov_b64 s[4:5], s[90:91]\n" \
"s_lshl_b32 s95, s58, 11\n" \
"s_add_u32 m0, s22, 34560\n" \
"s_lshl_b32 s96, s60, 8\n" \
"buffer_load_dwordx4 v21, s[32:35], s47 offen offset:256 nt lds\n" \
"s_add_u32 s95, s95, s96\n" \
"s_add_u32 s96, s76, s95\ns_addc_u32 s97, s77, 0\n" \
"s_add_u32 s76, s78, s59\ns_addc_u32 s77, s79, 0\n" \
"s_and_b32 s59, s59, 1048575\n" \
"s_mov_b32 s47, 786432\n" \
"s_add_u32 m0, s22, 33792\n" \
"s_cmp_lt_u32 s58, 64\ns_cselect_b64 s[84:85], s[80:81], s[82:83]\n" \
"buffer_load_dwordx4 v21, s[32:35], s47 offen offset:0 nt lds\n" \
"s_add_u32 s84, s84, s59\ns_addc_u32 s85, s85, 0\n" \
"s_lshl_b32 s59, s8, 2\n" \
"s_add_u32 s59, s59, %2\n" \
"s_lshl_b32 s59, s59, 12\n" \
"s_add_u32 m0, s22, 35584\n" \
"s_add_u32 s88, s72, s59\ns_addc_u32 s89, s73, 0\n" \
"buffer_load_dwordx4 v21, s[32:35], s47 offen offset:256 nt lds\n" \
"s_mov_b64 s[4:5], s[90:91]\n" \
"s_mov_b64 s[10:11], s[72:73]\n" \
"s_mov_b64 s[12:13], s[74:75]\n" \
"s_add_u32 s44, s78, s94\ns_addc_u32 s45, s79, 0\n" \
"s_mov_b32 s46, 128\n" \
"s_add_u32 m0, s50, 0\n" \
"s_mov_b64 s[40:41], s[66:67]\n" \
"buffer_load_dwordx4 v13, s[28:31], s46 offen lds\n" \
"s_mov_b64 s[80:81], s[68:69]\n" \
"s_and_b32 s81, s81, 0xffff\n" \
"s_mov_b32 s82, 4194304\n" \
"s_mov_b32 s83, 0x00020000\n" \
"s_add_u32 m0, s50, 4096\n" \
"s_and_b32 s45, s45, 0xffff\n" \
"buffer_load_dwordx4 v14, s[28:31], s46 offen lds\n" \
"s_and_b32 s41, s41, 0xffff\n" \
"s_mov_b32 s42, 4194304\n" \
"s_mov_b32 s43, 0x00020000\n" \
"s_mov_b64 exec, 0xffff\nglobal_load_dword v22, v1, s[84:85]\ns_mov_b64 exec, -1\n" \
"s_mov_b64 exec, 0xffff\nglobal_load_dword v22, v2, s[96:97]\ns_mov_b64 exec, -1\n" \
"s_cmp_lt_u32 %1, 128\ns_cbranch_scc0 LHV_%=\ns_mov_b64 exec, 0\ns_cmp_eq_u32 %2, 0\ns_cbranch_scc1 LH0_%=\ns_cmp_eq_u32 %2, 1\ns_cbranch_scc1 LH1_%=\ns_cmp_eq_u32 %2, 2\ns_cbranch_scc1 LH2_%=\ns_cmp_eq_u32 %2, 3\ns_cbranch_scc1 LH3_%=\nLHV_%=:\ns_mov_b64 exec, 0\ns_cmp_eq_u32 %2, 0\ns_cbranch_scc1 LH4_%=\ns_cmp_eq_u32 %2, 1\ns_cbranch_scc1 LH5_%=\ns_cmp_eq_u32 %2, 2\ns_cbranch_scc1 LH6_%=\ns_cmp_eq_u32 %2, 3\ns_cbranch_scc1 LH7_%=\nLHR0_%=:\ns_mov_b64 exec, -1\n" \
"s_waitcnt vmcnt(8)\n" \
"s_barrier\n" \
"s_add_u32 m0, s50, 8192\n" \
"ds_read_b64_tr_b16 a[64:65], v9 offset:0\n" \
"ds_read_b64_tr_b16 a[66:67], v10 offset:0\n" \
"ds_read_b64_tr_b16 a[68:69], v9 offset:1024\n" \
"ds_read_b64_tr_b16 a[70:71], v10 offset:1024\n" \
"ds_read_b128 a[0:3], v7 offset:0\n" \
"ds_read_b128 a[4:7], v8 offset:0\n" \
"buffer_load_dwordx4 v15, s[28:31], s46 offen lds\n" \
"s_add_u32 m0, s50, 12288\n" \
"ds_read_b128 a[8:11], v7 offset:2048\n" \
"ds_read_b128 a[12:15], v8 offset:2048\n" \
"ds_read_b128 a[16:19], v7 offset:4096\n" \
"ds_read_b128 a[20:23], v8 offset:4096\n" \
"ds_read_b128 a[24:27], v7 offset:6144\n" \
"ds_read_b128 a[28:31], v8 offset:6144\n" \
"buffer_load_dwordx4 v16, s[28:31], s46 offen lds\n" \
"s_mov_b32 s46, 128\n" \
"s_add_u32 m0, s50, 16384\n" \
"ds_read_b128 a[32:35], v7 offset:8192\n" \
"ds_read_b128 a[36:39], v8 offset:8192\n" \
"ds_read_b128 a[40:43], v7 offset:10240\n" \
"ds_read_b128 a[44:47], v8 offset:10240\n" \
"ds_read_b128 a[48:51], v7 offset:12288\n" \
"ds_read_b128 a[52:55], v8 offset:12288\n" \
"buffer_load_dwordx4 v17, s[28:31], s46 offen lds\n" \
"s_add_u32 m0, s50, 20480\n" \
"ds_read_b128 a[56:59], v7 offset:14336\n" \
"ds_read_b128 a[60:63], v8 offset:14336\n" \
"ds_read_b64_tr_b16 a[72:73], v11 offset:0\n" \
"ds_read_b64_tr_b16 a[74:75], v12 offset:0\n" \
"ds_read_b64_tr_b16 a[76:77], v11 offset:1024\n" \
"ds_read_b64_tr_b16 a[78:79], v12 offset:1024\n" \
"buffer_load_dwordx4 v18, s[28:31], s46 offen lds\n" \
"s_add_u32 m0, s50, 24576\n" \
"ds_read_b64_tr_b16 a[80:81], v9 offset:512\n" \
"ds_read_b64_tr_b16 a[82:83], v10 offset:512\n" \
"ds_read_b64_tr_b16 a[84:85], v9 offset:1536\n" \
"ds_read_b64_tr_b16 a[86:87], v10 offset:1536\n" \
"ds_read_b64_tr_b16 a[88:89], v11 offset:512\n" \
"ds_read_b64_tr_b16 a[90:91], v12 offset:512\n" \
"buffer_load_dwordx4 v19, s[28:31], s46 offen lds\n" \
"s_add_u32 m0, s50, 28672\n" \
"ds_read_b64_tr_b16 a[92:93], v11 offset:1536\n" \
"ds_read_b64_tr_b16 a[94:95], v12 offset:1536\n" \
"buffer_load_dwordx4 v20, s[28:31], s46 offen lds\n" \
"s_waitcnt lgkmcnt(0)\n" \
"s_branch LBEG_%=\n" \
".p2align 12\n" \
"LBEG_%=:\n" \
"s_cmp_lt_u32 %1, 128\n" \
"s_cbranch_scc0 LAV_%=\n" \
"v_mfma_f32_16x16x32_bf16 v[64:67], a[64:67], a[0:3], 0\n" \
"s_mov_b32 s47, 524288\n" \
"s_add_u32 m0, s22, 49024\n" \
"v_mfma_f32_16x16x32_bf16 v[64:67], a[68:71], a[4:7], v[64:67]\n" \
"buffer_load_dwordx4 v21, s[32:35], s47 offen offset:128 nt lds\n" \
"v_mfma_f32_16x16x32_bf16 v[72:75], a[64:67], a[8:11], 0\n" \
"s_add_u32 s14, s14, 1\ns_addc_u32 s15, s15, 0\n" \
"v_mfma_f32_16x16x32_bf16 v[72:75], a[68:71], a[12:15], v[72:75]\n" \
"v_mfma_f32_16x16x32_bf16 v[80:83], a[64:67], a[16:19], 0\n" \
"s_add_u32 m0, s22, 50816\n" \
"ds_read_b64_tr_b16 a[96:97], v9 offset:16384\n" \
"v_mfma_f32_16x16x32_bf16 v[80:83], a[68:71], a[20:23], v[80:83]\n" \
"buffer_load_dwordx4 v21, s[32:35], s47 offen offset:384 nt lds\n" \
"v_mfma_f32_16x16x32_bf16 v[88:91], a[64:67], a[24:27], 0\n" \
"ds_read_b64_tr_b16 a[98:99], v10 offset:16384\n" \
"v_mfma_f32_16x16x32_bf16 v[88:91], a[68:71], a[28:31], v[88:91]\n" \
"v_mfma_f32_16x16x32_bf16 v[96:99], a[64:67], a[32:35], 0\n" \
"s_mov_b32 s47, 786432\n" \
"s_add_u32 m0, s22, 50048\n" \
"ds_read_b64_tr_b16 a[100:101], v9 offset:17408\n" \
"v_mfma_f32_16x16x32_bf16 v[96:99], a[68:71], a[36:39], v[96:99]\n" \
"buffer_load_dwordx4 v21, s[32:35], s47 offen offset:128 nt lds\n" \
"v_mfma_f32_16x16x32_bf16 v[104:107], a[64:67], a[40:43], 0\n" \
"ds_read_b64_tr_b16 a[102:103], v10 offset:17408\n" \
"v_mfma_f32_16x16x32_bf16 v[104:107], a[68:71], a[44:47], v[104:107]\n" \
"v_mfma_f32_16x16x32_bf16 v[112:115], a[64:67], a[48:51], 0\n" \
"s_add_u32 m0, s22, 51840\n" \
"ds_read_b64_tr_b16 a[104:105], v11 offset:16384\n" \
"v_mfma_f32_16x16x32_bf16 v[112:115], a[68:71], a[52:55], v[112:115]\n" \
"buffer_load_dwordx4 v21, s[32:35], s47 offen offset:384 nt lds\n" \
"v_mfma_f32_16x16x32_bf16 v[120:123], a[64:67], a[56:59], 0\n" \
"ds_read_b64_tr_b16 a[106:107], v12 offset:16384\n" \
"v_mfma_f32_16x16x32_bf16 v[120:123], a[68:71], a[60:63], v[120:123]\n" \
"v_mfma_f32_16x16x32_bf16 v[68:71], a[72:75], a[0:3], 0\n" \
"s_mov_b32 s46, 256\n" \
"s_add_u32 m0, s50, 32768\n" \
"ds_read_b64_tr_b16 a[108:109], v11 offset:17408\n" \
"v_mfma_f32_16x16x32_bf16 v[68:71], a[76:79], a[4:7], v[68:71]\n" \
"buffer_load_dwordx4 v13, s[28:31], s46 offen lds\n" \
"v_mfma_f32_16x16x32_bf16 v[76:79], a[72:75], a[8:11], 0\n" \
"ds_read_b64_tr_b16 a[110:111], v12 offset:17408\n" \
"v_mfma_f32_16x16x32_bf16 v[76:79], a[76:79], a[12:15], v[76:79]\n" \
"v_mfma_f32_16x16x32_bf16 v[84:87], a[72:75], a[16:19], 0\n" \
"s_add_u32 m0, s50, 36864\n" \
"ds_read_b64_tr_b16 a[112:113], v9 offset:16896\n" \
"v_mfma_f32_16x16x32_bf16 v[84:87], a[76:79], a[20:23], v[84:87]\n" \
"buffer_load_dwordx4 v14, s[28:31], s46 offen lds\n" \
"v_mfma_f32_16x16x32_bf16 v[92:95], a[72:75], a[24:27], 0\n" \
"ds_read_b64_tr_b16 a[114:115], v10 offset:16896\n" \
"v_mfma_f32_16x16x32_bf16 v[92:95], a[76:79], a[28:31], v[92:95]\n" \
"v_mfma_f32_16x16x32_bf16 v[100:103], a[72:75], a[32:35], 0\n" \
"s_add_u32 m0, s50, 40960\n" \
"ds_read_b64_tr_b16 a[116:117], v9 offset:17920\n" \
"v_mfma_f32_16x16x32_bf16 v[100:103], a[76:79], a[36:39], v[100:103]\n" \
"buffer_load_dwordx4 v15, s[28:31], s46 offen lds\n" \
"v_mfma_f32_16x16x32_bf16 v[108:111], a[72:75], a[40:43], 0\n" \
"ds_read_b64_tr_b16 a[118:119], v10 offset:17920\n" \
"v_mfma_f32_16x16x32_bf16 v[108:111], a[76:79], a[44:47], v[108:111]\n" \
"v_mfma_f32_16x16x32_bf16 v[116:119], a[72:75], a[48:51], 0\n" \
"s_add_u32 m0, s50, 45056\n" \
"ds_read_b64_tr_b16 a[120:121], v11 offset:16896\n" \
"v_mfma_f32_16x16x32_bf16 v[116:119], a[76:79], a[52:55], v[116:119]\n" \
"buffer_load_dwordx4 v16, s[28:31], s46 offen lds\n" \
"v_mfma_f32_16x16x32_bf16 v[124:127], a[72:75], a[56:59], 0\n" \
"ds_read_b64_tr_b16 a[122:123], v12 offset:16896\n" \
"v_mfma_f32_16x16x32_bf16 v[124:127], a[76:79], a[60:63], v[124:127]\n" \
"v_mfma_f32_16x16x32_bf16 v[128:131], a[80:83], a[0:3], 0\n" \
"s_mov_b32 s46, 256\n" \
"s_add_u32 m0, s50, 49152\n" \
"ds_read_b64_tr_b16 a[124:125], v11 offset:17920\n" \
"v_mfma_f32_16x16x32_bf16 v[128:131], a[84:87], a[4:7], v[128:131]\n" \
"buffer_load_dwordx4 v17, s[28:31], s46 offen lds\n" \
"v_mfma_f32_16x16x32_bf16 v[136:139], a[80:83], a[8:11], 0\n" \
"ds_read_b64_tr_b16 a[126:127], v12 offset:17920\n" \
"v_mfma_f32_16x16x32_bf16 v[136:139], a[84:87], a[12:15], v[136:139]\n" \
"v_mfma_f32_16x16x32_bf16 v[144:147], a[80:83], a[16:19], 0\n" \
"s_add_u32 m0, s50, 53248\n" \
"v_mfma_f32_16x16x32_bf16 v[144:147], a[84:87], a[20:23], v[144:147]\n" \
"buffer_load_dwordx4 v18, s[28:31], s46 offen lds\n" \
"v_mfma_f32_16x16x32_bf16 v[152:155], a[80:83], a[24:27], 0\n" \
"v_mfma_f32_16x16x32_bf16 v[152:155], a[84:87], a[28:31], v[152:155]\n" \
"v_mfma_f32_16x16x32_bf16 v[160:163], a[80:83], a[32:35], 0\n" \
"s_add_u32 m0, s50, 57344\n" \
"v_mfma_f32_16x16x32_bf16 v[160:163], a[84:87], a[36:39], v[160:163]\n" \
"buffer_load_dwordx4 v19, s[28:31], s46 offen lds\n" \
"v_mfma_f32_16x16x32_bf16 v[168:171], a[80:83], a[40:43], 0\n" \
"v_mfma_f32_16x16x32_bf16 v[168:171], a[84:87], a[44:47], v[168:171]\n" \
"s_waitcnt vmcnt(21) lgkmcnt(0)\n" \
"v_mfma_f32_16x16x32_bf16 v[176:179], a[80:83], a[48:51], 0\n" \
"s_add_u32 m0, s50, 61440\n" \
"s_barrier\n" \
"v_mfma_f32_16x16x32_bf16 v[176:179], a[84:87], a[52:55], v[176:179]\n" \
"buffer_load_dwordx4 v20, s[28:31], s46 offen lds\n" \
"v_mfma_f32_16x16x32_bf16 v[184:187], a[80:83], a[56:59], 0\n" \
"v_mfma_f32_16x16x32_bf16 v[184:187], a[84:87], a[60:63], v[184:187]\n" \
"v_mfma_f32_16x16x32_bf16 v[132:135], a[88:91], a[0:3], 0\n" \
"s_mov_b32 s47, 1048576\n" \
"s_add_u32 m0, s22, 0\n" \
"v_mfma_f32_16x16x32_bf16 v[132:135], a[92:95], a[4:7], v[132:135]\n" \
"buffer_load_dwordx4 v21, s[32:35], s47 offen offset:0 lds\n" \
"v_mfma_f32_16x16x32_bf16 v[140:143], a[88:91], a[8:11], 0\n" \
"v_mfma_f32_16x16x32_bf16 v[140:143], a[92:95], a[12:15], v[140:143]\n" \
"v_mfma_f32_16x16x32_bf16 v[148:151], a[88:91], a[16:19], 0\n" \
"s_add_u32 m0, s22, 1792\n" \
"v_mfma_f32_16x16x32_bf16 v[148:151], a[92:95], a[20:23], v[148:151]\n" \
"buffer_load_dwordx4 v21, s[32:35], s47 offen offset:256 lds\n" \
"v_mfma_f32_16x16x32_bf16 v[156:159], a[88:91], a[24:27], 0\n" \
"v_mfma_f32_16x16x32_bf16 v[156:159], a[92:95], a[28:31], v[156:159]\n" \
"v_mfma_f32_16x16x32_bf16 v[164:167], a[88:91], a[32:35], 0\n" \
"s_mov_b32 s47, 1310720\n" \
"s_add_u32 m0, s22, 1024\n" \
"v_mfma_f32_16x16x32_bf16 v[164:167], a[92:95], a[36:39], v[164:167]\n" \
"buffer_load_dwordx4 v21, s[32:35], s47 offen offset:0 lds\n" \
"v_mfma_f32_16x16x32_bf16 v[172:175], a[88:91], a[40:43], 0\n" \
"v_mfma_f32_16x16x32_bf16 v[172:175], a[92:95], a[44:47], v[172:175]\n" \
"v_mfma_f32_16x16x32_bf16 v[180:183], a[88:91], a[48:51], 0\n" \
"s_add_u32 m0, s22, 2816\n" \
"v_mfma_f32_16x16x32_bf16 v[180:183], a[92:95], a[52:55], v[180:183]\n" \
"buffer_load_dwordx4 v21, s[32:35], s47 offen offset:256 lds\n" \
"v_mfma_f32_16x16x32_bf16 v[188:191], a[88:91], a[56:59], 0\n" \
"v_mfma_f32_16x16x32_bf16 v[188:191], a[92:95], a[60:63], v[188:191]\n" \
"v_mfma_f32_16x16x32_bf16 v[192:195], a[96:99], a[0:3], 0\n" \
"s_mov_b32 s47, 1048576\n" \
"s_add_u32 m0, s22, 16256\n" \
"v_mfma_f32_16x16x32_bf16 v[192:195], a[100:103], a[4:7], v[192:195]\n" \
"buffer_load_dwordx4 v21, s[32:35], s47 offen offset:128 lds\n" \
"v_mfma_f32_16x16x32_bf16 v[200:203], a[96:99], a[8:11], 0\n" \
"v_mfma_f32_16x16x32_bf16 v[200:203], a[100:103], a[12:15], v[200:203]\n" \
"v_mfma_f32_16x16x32_bf16 v[208:211], a[96:99], a[16:19], 0\n" \
"s_mov_b32 s46, 384\n" \
"s_add_u32 m0, s50, 65536\n" \
"ds_read_b64_tr_b16 a[64:65], v9 offset:32768\n" \
"v_mfma_f32_16x16x32_bf16 v[208:211], a[100:103], a[20:23], v[208:211]\n" \
"buffer_load_dwordx4 v13, s[28:31], s46 offen lds\n" \
"v_mfma_f32_16x16x32_bf16 v[216:219], a[96:99], a[24:27], 0\n" \
"ds_read_b64_tr_b16 a[66:67], v10 offset:32768\n" \
"v_mfma_f32_16x16x32_bf16 v[216:219], a[100:103], a[28:31], v[216:219]\n" \
"v_mfma_f32_16x16x32_bf16 v[224:227], a[96:99], a[32:35], 0\n" \
"s_add_u32 m0, s22, 18048\n" \
"ds_read_b64_tr_b16 a[68:69], v9 offset:33792\n" \
"v_mfma_f32_16x16x32_bf16 v[224:227], a[100:103], a[36:39], v[224:227]\n" \
"buffer_load_dwordx4 v21, s[32:35], s47 offen offset:384 lds\n" \
"v_mfma_f32_16x16x32_bf16 v[232:235], a[96:99], a[40:43], 0\n" \
"ds_read_b64_tr_b16 a[70:71], v10 offset:33792\n" \
"v_mfma_f32_16x16x32_bf16 v[232:235], a[100:103], a[44:47], v[232:235]\n" \
"v_mfma_f32_16x16x32_bf16 v[240:243], a[96:99], a[48:51], 0\n" \
"s_add_u32 m0, s50, 69632\n" \
"ds_read_b64_tr_b16 a[72:73], v11 offset:32768\n" \
"v_mfma_f32_16x16x32_bf16 v[240:243], a[100:103], a[52:55], v[240:243]\n" \
"buffer_load_dwordx4 v14, s[28:31], s46 offen lds\n" \
"v_mfma_f32_16x16x32_bf16 v[248:251], a[96:99], a[56:59], 0\n" \
"ds_read_b64_tr_b16 a[74:75], v12 offset:32768\n" \
"v_mfma_f32_16x16x32_bf16 v[248:251], a[100:103], a[60:63], v[248:251]\n" \
"v_mfma_f32_16x16x32_bf16 v[196:199], a[104:107], a[0:3], 0\n" \
"s_mov_b32 s47, 1310720\n" \
"s_add_u32 m0, s22, 17280\n" \
"ds_read_b64_tr_b16 a[76:77], v11 offset:33792\n" \
"v_mfma_f32_16x16x32_bf16 v[196:199], a[108:111], a[4:7], v[196:199]\n" \
"buffer_load_dwordx4 v21, s[32:35], s47 offen offset:128 lds\n" \
"v_mfma_f32_16x16x32_bf16 v[204:207], a[104:107], a[8:11], 0\n" \
"ds_read_b64_tr_b16 a[78:79], v12 offset:33792\n" \
"v_mfma_f32_16x16x32_bf16 v[204:207], a[108:111], a[12:15], v[204:207]\n" \
"v_mfma_f32_16x16x32_bf16 v[212:215], a[104:107], a[16:19], 0\n" \
"s_add_u32 m0, s50, 73728\n" \
"ds_read_b64_tr_b16 a[80:81], v9 offset:33280\n" \
"v_mfma_f32_16x16x32_bf16 v[212:215], a[108:111], a[20:23], v[212:215]\n" \
"buffer_load_dwordx4 v15, s[28:31], s46 offen lds\n" \
"v_mfma_f32_16x16x32_bf16 v[220:223], a[104:107], a[24:27], 0\n" \
"ds_read_b64_tr_b16 a[82:83], v10 offset:33280\n" \
"v_mfma_f32_16x16x32_bf16 v[220:223], a[108:111], a[28:31], v[220:223]\n" \
"v_mfma_f32_16x16x32_bf16 v[228:231], a[104:107], a[32:35], 0\n" \
"s_add_u32 m0, s22, 19072\n" \
"ds_read_b64_tr_b16 a[84:85], v9 offset:34304\n" \
"v_mfma_f32_16x16x32_bf16 v[228:231], a[108:111], a[36:39], v[228:231]\n" \
"buffer_load_dwordx4 v21, s[32:35], s47 offen offset:384 lds\n" \
"v_mfma_f32_16x16x32_bf16 v[236:239], a[104:107], a[40:43], 0\n" \
"ds_read_b64_tr_b16 a[86:87], v10 offset:34304\n" \
"v_mfma_f32_16x16x32_bf16 v[236:239], a[108:111], a[44:47], v[236:239]\n" \
"v_mfma_f32_16x16x32_bf16 v[244:247], a[104:107], a[48:51], 0\n" \
"s_add_u32 m0, s50, 77824\n" \
"ds_read_b64_tr_b16 a[88:89], v11 offset:33280\n" \
"v_mfma_f32_16x16x32_bf16 v[244:247], a[108:111], a[52:55], v[244:247]\n" \
"buffer_load_dwordx4 v16, s[28:31], s46 offen lds\n" \
"v_mfma_f32_16x16x32_bf16 v[252:255], a[104:107], a[56:59], 0\n" \
"ds_read_b64_tr_b16 a[90:91], v12 offset:33280\n" \
"v_mfma_f32_16x16x32_bf16 v[252:255], a[108:111], a[60:63], v[252:255]\n" \
"v_mfma_f32_16x16x32_bf16 a[128:131], a[112:115], a[0:3], 0\n" \
"ds_read_b64_tr_b16 a[92:93], v11 offset:34304\n" \
"v_mfma_f32_16x16x32_bf16 a[128:131], a[116:119], a[4:7], a[128:131]\n" \
"global_load_dwordx4 a[230:233], v27, s[70:71] offset:0\n" \
"v_mfma_f32_16x16x32_bf16 a[136:139], a[112:115], a[8:11], 0\n" \
"ds_read_b64_tr_b16 a[94:95], v12 offset:34304\n" \
"v_mfma_f32_16x16x32_bf16 a[136:139], a[116:119], a[12:15], a[136:139]\n" \
"v_mfma_f32_16x16x32_bf16 a[144:147], a[112:115], a[16:19], 0\n" \
"v_mfma_f32_16x16x32_bf16 a[144:147], a[116:119], a[20:23], a[144:147]\n" \
"global_load_dwordx4 a[234:237], v27, s[70:71] offset:16\n" \
"v_mfma_f32_16x16x32_bf16 a[152:155], a[112:115], a[24:27], 0\n" \
"v_mfma_f32_16x16x32_bf16 a[152:155], a[116:119], a[28:31], a[152:155]\n" \
"v_mfma_f32_16x16x32_bf16 a[160:163], a[112:115], a[32:35], 0\n" \
"v_mfma_f32_16x16x32_bf16 a[160:163], a[116:119], a[36:39], a[160:163]\n" \
"buffer_load_dwordx2 a[192:193], v54, s[40:43], 0 offen offset:0 nt\n" \
"v_mfma_f32_16x16x32_bf16 a[168:171], a[112:115], a[40:43], 0\n" \
"v_mfma_f32_16x16x32_bf16 a[168:171], a[116:119], a[44:47], a[168:171]\n" \
"s_waitcnt vmcnt(23) lgkmcnt(0)\n" \
"v_mfma_f32_16x16x32_bf16 a[176:179], a[112:115], a[48:51], 0\n" \
"s_barrier\n" \
"v_mfma_f32_16x16x32_bf16 a[176:179], a[116:119], a[52:55], a[176:179]\n" \
"buffer_load_dwordx2 a[194:195], v54, s[40:43], 0 offen offset:2048 nt\n" \
"v_mfma_f32_16x16x32_bf16 a[184:187], a[112:115], a[56:59], 0\n" \
"v_mfma_f32_16x16x32_bf16 a[184:187], a[116:119], a[60:63], a[184:187]\n" \
"v_mfma_f32_16x16x32_bf16 a[132:135], a[120:123], a[0:3], 0\n" \
"v_mfma_f32_16x16x32_bf16 a[132:135], a[124:127], a[4:7], a[132:135]\n" \
"buffer_load_dwordx2 a[196:197], v55, s[40:43], 0 offen offset:0 nt\n" \
"v_mfma_f32_16x16x32_bf16 a[140:143], a[120:123], a[8:11], 0\n" \
"s_add_u32 s84, s84, 1048576\ns_addc_u32 s85, s85, 0\n" \
"ds_read_b128 a[0:3], v5 offset:0\n" \
"v_mfma_f32_16x16x32_bf16 a[140:143], a[124:127], a[12:15], a[140:143]\n" \
"s_mov_b32 s52, 0\n" \
"ds_read_b128 a[4:7], v6 offset:0\n" \
"v_mfma_f32_16x16x32_bf16 a[148:151], a[120:123], a[16:19], 0\n" \
"ds_read_b128 a[8:11], v5 offset:2048\n" \
"v_mfma_f32_16x16x32_bf16 a[148:151], a[124:127], a[20:23], a[148:151]\n" \
"ds_read_b128 a[12:15], v6 offset:2048\n" \
"v_mfma_f32_16x16x32_bf16 a[156:159], a[120:123], a[24:27], 0\n" \
"ds_read_b128 a[16:19], v5 offset:4096\n" \
"v_mfma_f32_16x16x32_bf16 a[156:159], a[124:127], a[28:31], a[156:159]\n" \
"ds_read_b128 a[20:23], v6 offset:4096\n" \
"v_mfma_f32_16x16x32_bf16 a[164:167], a[120:123], a[32:35], 0\n" \
"ds_read_b128 a[24:27], v5 offset:6144\n" \
"v_mfma_f32_16x16x32_bf16 a[164:167], a[124:127], a[36:39], a[164:167]\n" \
"ds_read_b128 a[28:31], v6 offset:6144\n" \
"v_mfma_f32_16x16x32_bf16 a[172:175], a[120:123], a[40:43], 0\n" \
"ds_read_b128 a[32:35], v5 offset:8192\n" \
"v_mfma_f32_16x16x32_bf16 a[172:175], a[124:127], a[44:47], a[172:175]\n" \
"ds_read_b128 a[36:39], v6 offset:8192\n" \
"v_mfma_f32_16x16x32_bf16 a[180:183], a[120:123], a[48:51], 0\n" \
"ds_read_b128 a[40:43], v5 offset:10240\n" \
"v_mfma_f32_16x16x32_bf16 a[180:183], a[124:127], a[52:55], a[180:183]\n" \
"ds_read_b128 a[44:47], v6 offset:10240\n" \
"v_mfma_f32_16x16x32_bf16 a[188:191], a[120:123], a[56:59], 0\n" \
"ds_read_b128 a[48:51], v5 offset:12288\n" \
"v_mfma_f32_16x16x32_bf16 a[188:191], a[124:127], a[60:63], a[188:191]\n" \
"s_waitcnt lgkmcnt(9)\n" \
"ds_read_b128 a[52:55], v6 offset:12288\n" \
"v_mfma_f32_16x16x32_bf16 v[64:67], a[64:67], a[0:3], v[64:67]\n" \
"s_mov_b32 s47, 1572864\n" \
"s_add_u32 m0, s22, 32768\n" \
"ds_read_b128 a[56:59], v5 offset:14336\n" \
"v_mfma_f32_16x16x32_bf16 v[64:67], a[68:71], a[4:7], v[64:67]\n" \
"buffer_load_dwordx4 v21, s[32:35], s47 offen offset:0 lds\n" \
"v_mfma_f32_16x16x32_bf16 v[72:75], a[64:67], a[8:11], v[72:75]\n" \
"s_waitcnt lgkmcnt(7)\n" \
"ds_read_b128 a[60:63], v6 offset:14336\n" \
"v_mfma_f32_16x16x32_bf16 v[72:75], a[68:71], a[12:15], v[72:75]\n" \
"v_mfma_f32_16x16x32_bf16 v[80:83], a[64:67], a[16:19], v[80:83]\n" \
"ds_read_b64_tr_b16 a[96:97], v9 offset:49152\n" \
"v_mfma_f32_16x16x32_bf16 v[80:83], a[68:71], a[20:23], v[80:83]\n" \
"s_mov_b64 exec, 0xffff\nglobal_load_dword v22, v1, s[84:85]\ns_mov_b64 exec, -1\n" \
"v_mfma_f32_16x16x32_bf16 v[88:91], a[64:67], a[24:27], v[88:91]\n" \
"s_waitcnt lgkmcnt(5)\n" \
"ds_read_b64_tr_b16 a[98:99], v10 offset:49152\n" \
"v_mfma_f32_16x16x32_bf16 v[88:91], a[68:71], a[28:31], v[88:91]\n" \
"v_mfma_f32_16x16x32_bf16 v[96:99], a[64:67], a[32:35], v[96:99]\n" \
"s_mov_b32 s46, 384\n" \
"s_add_u32 m0, s50, 81920\n" \
"ds_read_b64_tr_b16 a[100:101], v9 offset:50176\n" \
"v_mfma_f32_16x16x32_bf16 v[96:99], a[68:71], a[36:39], v[96:99]\n" \
"buffer_load_dwordx4 v17, s[28:31], s46 offen lds\n" \
"v_mfma_f32_16x16x32_bf16 v[104:107], a[64:67], a[40:43], v[104:107]\n" \
"s_waitcnt lgkmcnt(3)\n" \
"ds_read_b64_tr_b16 a[102:103], v10 offset:50176\n" \
"v_mfma_f32_16x16x32_bf16 v[104:107], a[68:71], a[44:47], v[104:107]\n" \
"v_mfma_f32_16x16x32_bf16 v[112:115], a[64:67], a[48:51], v[112:115]\n" \
"ds_read_b64_tr_b16 a[104:105], v11 offset:49152\n" \
"v_mfma_f32_16x16x32_bf16 v[112:115], a[68:71], a[52:55], v[112:115]\n" \
"buffer_load_dwordx2 a[198:199], v59, s[40:43], s52 offen offset:0 nt\n" \
"v_mfma_f32_16x16x32_bf16 v[120:123], a[64:67], a[56:59], v[120:123]\n" \
"ds_read_b64_tr_b16 a[106:107], v12 offset:49152\n" \
"v_mfma_f32_16x16x32_bf16 v[120:123], a[68:71], a[60:63], v[120:123]\n" \
"v_mfma_f32_16x16x32_bf16 v[68:71], a[72:75], a[0:3], v[68:71]\n" \
"s_add_u32 m0, s22, 34560\n" \
"ds_read_b64_tr_b16 a[108:109], v11 offset:50176\n" \
"v_mfma_f32_16x16x32_bf16 v[68:71], a[76:79], a[4:7], v[68:71]\n" \
"buffer_load_dwordx4 v21, s[32:35], s47 offen offset:256 lds\n" \
"v_mfma_f32_16x16x32_bf16 v[76:79], a[72:75], a[8:11], v[76:79]\n" \
"ds_read_b64_tr_b16 a[110:111], v12 offset:50176\n" \
"v_mfma_f32_16x16x32_bf16 v[76:79], a[76:79], a[12:15], v[76:79]\n" \
"v_mfma_f32_16x16x32_bf16 v[84:87], a[72:75], a[16:19], v[84:87]\n" \
"ds_read_b64_tr_b16 a[112:113], v9 offset:49664\n" \
"v_mfma_f32_16x16x32_bf16 v[84:87], a[76:79], a[20:23], v[84:87]\n" \
"buffer_load_dwordx2 a[214:215], v59, s[80:83], s52 offen offset:0 nt\n" \
"v_mfma_f32_16x16x32_bf16 v[92:95], a[72:75], a[24:27], v[92:95]\n" \
"ds_read_b64_tr_b16 a[114:115], v10 offset:49664\n" \
"v_mfma_f32_16x16x32_bf16 v[92:95], a[76:79], a[28:31], v[92:95]\n" \
"v_mfma_f32_16x16x32_bf16 v[100:103], a[72:75], a[32:35], v[100:103]\n" \
"s_add_u32 m0, s50, 86016\n" \
"ds_read_b64_tr_b16 a[116:117], v9 offset:50688\n" \
"v_mfma_f32_16x16x32_bf16 v[100:103], a[76:79], a[36:39], v[100:103]\n" \
"buffer_load_dwordx4 v18, s[28:31], s46 offen lds\n" \
"v_mfma_f32_16x16x32_bf16 v[108:111], a[72:75], a[40:43], v[108:111]\n" \
"ds_read_b64_tr_b16 a[118:119], v10 offset:50688\n" \
"v_mfma_f32_16x16x32_bf16 v[108:111], a[76:79], a[44:47], v[108:111]\n" \
"v_mfma_f32_16x16x32_bf16 v[116:119], a[72:75], a[48:51], v[116:119]\n" \
"ds_read_b64_tr_b16 a[120:121], v11 offset:49664\n" \
"v_mfma_f32_16x16x32_bf16 v[116:119], a[76:79], a[52:55], v[116:119]\n" \
"buffer_load_dwordx2 a[200:201], v59, s[40:43], s52 offen offset:2048 nt\n" \
"v_mfma_f32_16x16x32_bf16 v[124:127], a[72:75], a[56:59], v[124:127]\n" \
"ds_read_b64_tr_b16 a[122:123], v12 offset:49664\n" \
"v_mfma_f32_16x16x32_bf16 v[124:127], a[76:79], a[60:63], v[124:127]\n" \
"v_mfma_f32_16x16x32_bf16 v[128:131], a[80:83], a[0:3], v[128:131]\n" \
"s_mov_b32 s47, 1835008\n" \
"s_add_u32 m0, s22, 33792\n" \
"ds_read_b64_tr_b16 a[124:125], v11 offset:50688\n" \
"v_mfma_f32_16x16x32_bf16 v[128:131], a[84:87], a[4:7], v[128:131]\n" \
"buffer_load_dwordx4 v21, s[32:35], s47 offen offset:0 lds\n" \
"v_mfma_f32_16x16x32_bf16 v[136:139], a[80:83], a[8:11], v[136:139]\n" \
"ds_read_b64_tr_b16 a[126:127], v12 offset:50688\n" \
"v_mfma_f32_16x16x32_bf16 v[136:139], a[84:87], a[12:15], v[136:139]\n" \
"v_mfma_f32_16x16x32_bf16 v[144:147], a[80:83], a[16:19], v[144:147]\n" \
"v_mfma_f32_16x16x32_bf16 v[144:147], a[84:87], a[20:23], v[144:147]\n" \
"buffer_load_dwordx2 a[216:217], v59, s[80:83], s52 offen offset:2048 nt\n" \
"v_mfma_f32_16x16x32_bf16 v[152:155], a[80:83], a[24:27], v[152:155]\n" \
"s_mov_b32 s52, 4096\n" \
"v_mfma_f32_16x16x32_bf16 v[152:155], a[84:87], a[28:31], v[152:155]\n" \
"v_mfma_f32_16x16x32_bf16 v[160:163], a[80:83], a[32:35], v[160:163]\n" \
"s_add_u32 m0, s50, 90112\n" \
"v_mfma_f32_16x16x32_bf16 v[160:163], a[84:87], a[36:39], v[160:163]\n" \
"buffer_load_dwordx4 v19, s[28:31], s46 offen lds\n" \
"v_mfma_f32_16x16x32_bf16 v[168:171], a[80:83], a[40:43], v[168:171]\n" \
"v_mfma_f32_16x16x32_bf16 v[168:171], a[84:87], a[44:47], v[168:171]\n" \
"s_waitcnt vmcnt(24) lgkmcnt(0)\n" \
"v_mfma_f32_16x16x32_bf16 v[176:179], a[80:83], a[48:51], v[176:179]\n" \
"s_barrier\n" \
"v_mfma_f32_16x16x32_bf16 v[176:179], a[84:87], a[52:55], v[176:179]\n" \
"buffer_load_dwordx2 a[202:203], v59, s[40:43], s52 offen offset:0 nt\n" \
"v_mfma_f32_16x16x32_bf16 v[184:187], a[80:83], a[56:59], v[184:187]\n" \
"v_mfma_f32_16x16x32_bf16 v[184:187], a[84:87], a[60:63], v[184:187]\n" \
"v_mfma_f32_16x16x32_bf16 v[132:135], a[88:91], a[0:3], v[132:135]\n" \
"s_add_u32 m0, s22, 35584\n" \
"v_mfma_f32_16x16x32_bf16 v[132:135], a[92:95], a[4:7], v[132:135]\n" \
"buffer_load_dwordx4 v21, s[32:35], s47 offen offset:256 lds\n" \
"v_mfma_f32_16x16x32_bf16 v[140:143], a[88:91], a[8:11], v[140:143]\n" \
"v_mfma_f32_16x16x32_bf16 v[140:143], a[92:95], a[12:15], v[140:143]\n" \
"v_mfma_f32_16x16x32_bf16 v[148:151], a[88:91], a[16:19], v[148:151]\n" \
"v_mfma_f32_16x16x32_bf16 v[148:151], a[92:95], a[20:23], v[148:151]\n" \
"buffer_load_dwordx2 a[218:219], v59, s[80:83], s52 offen offset:0 nt\n" \
"v_mfma_f32_16x16x32_bf16 v[156:159], a[88:91], a[24:27], v[156:159]\n" \
"v_mfma_f32_16x16x32_bf16 v[156:159], a[92:95], a[28:31], v[156:159]\n" \
"v_mfma_f32_16x16x32_bf16 v[164:167], a[88:91], a[32:35], v[164:167]\n" \
"s_add_u32 m0, s50, 94208\n" \
"v_mfma_f32_16x16x32_bf16 v[164:167], a[92:95], a[36:39], v[164:167]\n" \
"buffer_load_dwordx4 v20, s[28:31], s46 offen lds\n" \
"v_mfma_f32_16x16x32_bf16 v[172:175], a[88:91], a[40:43], v[172:175]\n" \
"v_mfma_f32_16x16x32_bf16 v[172:175], a[92:95], a[44:47], v[172:175]\n" \
"v_mfma_f32_16x16x32_bf16 v[180:183], a[88:91], a[48:51], v[180:183]\n" \
"v_mfma_f32_16x16x32_bf16 v[180:183], a[92:95], a[52:55], v[180:183]\n" \
"buffer_load_dwordx2 a[204:205], v59, s[40:43], s52 offen offset:2048 nt\n" \
"v_mfma_f32_16x16x32_bf16 v[188:191], a[88:91], a[56:59], v[188:191]\n" \
"v_mfma_f32_16x16x32_bf16 v[188:191], a[92:95], a[60:63], v[188:191]\n" \
"v_mfma_f32_16x16x32_bf16 v[192:195], a[96:99], a[0:3], v[192:195]\n" \
"s_mov_b32 s47, 1572864\n" \
"s_add_u32 m0, s22, 49024\n" \
"v_mfma_f32_16x16x32_bf16 v[192:195], a[100:103], a[4:7], v[192:195]\n" \
"buffer_load_dwordx4 v21, s[32:35], s47 offen offset:128 lds\n" \
"v_mfma_f32_16x16x32_bf16 v[200:203], a[96:99], a[8:11], v[200:203]\n" \
"v_mfma_f32_16x16x32_bf16 v[200:203], a[100:103], a[12:15], v[200:203]\n" \
"v_mfma_f32_16x16x32_bf16 v[208:211], a[96:99], a[16:19], v[208:211]\n" \
"s_mov_b32 s46, 512\n" \
"s_add_u32 m0, s50, 0\n" \
"ds_read_b64_tr_b16 a[64:65], v9 offset:0\n" \
"v_mfma_f32_16x16x32_bf16 v[208:211], a[100:103], a[20:23], v[208:211]\n" \
"buffer_load_dwordx4 v13, s[28:31], s46 offen lds\n" \
"v_mfma_f32_16x16x32_bf16 v[216:219], a[96:99], a[24:27], v[216:219]\n" \
"ds_read_b64_tr_b16 a[66:67], v10 offset:0\n" \
"v_mfma_f32_16x16x32_bf16 v[216:219], a[100:103], a[28:31], v[216:219]\n" \
"v_mfma_f32_16x16x32_bf16 v[224:227], a[96:99], a[32:35], v[224:227]\n" \
"ds_read_b64_tr_b16 a[68:69], v9 offset:1024\n" \
"v_mfma_f32_16x16x32_bf16 v[224:227], a[100:103], a[36:39], v[224:227]\n" \
"buffer_load_dwordx2 a[220:221], v59, s[80:83], s52 offen offset:2048 nt\n" \
"v_mfma_f32_16x16x32_bf16 v[232:235], a[96:99], a[40:43], v[232:235]\n" \
"s_mov_b32 s52, 8192\n" \
"ds_read_b64_tr_b16 a[70:71], v10 offset:1024\n" \
"v_mfma_f32_16x16x32_bf16 v[232:235], a[100:103], a[44:47], v[232:235]\n" \
"v_mfma_f32_16x16x32_bf16 v[240:243], a[96:99], a[48:51], v[240:243]\n" \
"s_add_u32 m0, s22, 50816\n" \
"ds_read_b64_tr_b16 a[72:73], v11 offset:0\n" \
"v_mfma_f32_16x16x32_bf16 v[240:243], a[100:103], a[52:55], v[240:243]\n" \
"buffer_load_dwordx4 v21, s[32:35], s47 offen offset:384 lds\n" \
"v_mfma_f32_16x16x32_bf16 v[248:251], a[96:99], a[56:59], v[248:251]\n" \
"ds_read_b64_tr_b16 a[74:75], v12 offset:0\n" \
"v_mfma_f32_16x16x32_bf16 v[248:251], a[100:103], a[60:63], v[248:251]\n" \
"v_mfma_f32_16x16x32_bf16 v[196:199], a[104:107], a[0:3], v[196:199]\n" \
"s_add_u32 m0, s50, 4096\n" \
"ds_read_b64_tr_b16 a[76:77], v11 offset:1024\n" \
"v_mfma_f32_16x16x32_bf16 v[196:199], a[108:111], a[4:7], v[196:199]\n" \
"buffer_load_dwordx4 v14, s[28:31], s46 offen lds\n" \
"v_mfma_f32_16x16x32_bf16 v[204:207], a[104:107], a[8:11], v[204:207]\n" \
"ds_read_b64_tr_b16 a[78:79], v12 offset:1024\n" \
"v_mfma_f32_16x16x32_bf16 v[204:207], a[108:111], a[12:15], v[204:207]\n" \
"v_mfma_f32_16x16x32_bf16 v[212:215], a[104:107], a[16:19], v[212:215]\n" \
"ds_read_b64_tr_b16 a[80:81], v9 offset:512\n" \
"v_mfma_f32_16x16x32_bf16 v[212:215], a[108:111], a[20:23], v[212:215]\n" \
"buffer_load_dwordx2 a[206:207], v59, s[40:43], s52 offen offset:0 nt\n" \
"v_mfma_f32_16x16x32_bf16 v[220:223], a[104:107], a[24:27], v[220:223]\n" \
"ds_read_b64_tr_b16 a[82:83], v10 offset:512\n" \
"v_mfma_f32_16x16x32_bf16 v[220:223], a[108:111], a[28:31], v[220:223]\n" \
"v_mfma_f32_16x16x32_bf16 v[228:231], a[104:107], a[32:35], v[228:231]\n" \
"s_mov_b32 s47, 1835008\n" \
"s_add_u32 m0, s22, 50048\n" \
"ds_read_b64_tr_b16 a[84:85], v9 offset:1536\n" \
"v_mfma_f32_16x16x32_bf16 v[228:231], a[108:111], a[36:39], v[228:231]\n" \
"buffer_load_dwordx4 v21, s[32:35], s47 offen offset:128 lds\n" \
"v_mfma_f32_16x16x32_bf16 v[236:239], a[104:107], a[40:43], v[236:239]\n" \
"ds_read_b64_tr_b16 a[86:87], v10 offset:1536\n" \
"v_mfma_f32_16x16x32_bf16 v[236:239], a[108:111], a[44:47], v[236:239]\n" \
"v_mfma_f32_16x16x32_bf16 v[244:247], a[104:107], a[48:51], v[244:247]\n" \
"s_add_u32 m0, s50, 8192\n" \
"ds_read_b64_tr_b16 a[88:89], v11 offset:512\n" \
"v_mfma_f32_16x16x32_bf16 v[244:247], a[108:111], a[52:55], v[244:247]\n" \
"buffer_load_dwordx4 v15, s[28:31], s46 offen lds\n" \
"v_mfma_f32_16x16x32_bf16 v[252:255], a[104:107], a[56:59], v[252:255]\n" \
"ds_read_b64_tr_b16 a[90:91], v12 offset:512\n" \
"v_mfma_f32_16x16x32_bf16 v[252:255], a[108:111], a[60:63], v[252:255]\n" \
"v_mfma_f32_16x16x32_bf16 a[128:131], a[112:115], a[0:3], a[128:131]\n" \
"ds_read_b64_tr_b16 a[92:93], v11 offset:1536\n" \
"v_mfma_f32_16x16x32_bf16 a[128:131], a[116:119], a[4:7], a[128:131]\n" \
"buffer_load_dwordx2 a[222:223], v59, s[80:83], s52 offen offset:0 nt\n" \
"v_mfma_f32_16x16x32_bf16 a[136:139], a[112:115], a[8:11], a[136:139]\n" \
"ds_read_b64_tr_b16 a[94:95], v12 offset:1536\n" \
"v_mfma_f32_16x16x32_bf16 a[136:139], a[116:119], a[12:15], a[136:139]\n" \
"v_mfma_f32_16x16x32_bf16 a[144:147], a[112:115], a[16:19], a[144:147]\n" \
"s_add_u32 m0, s22, 51840\n" \
"v_mfma_f32_16x16x32_bf16 a[144:147], a[116:119], a[20:23], a[144:147]\n" \
"buffer_load_dwordx4 v21, s[32:35], s47 offen offset:384 lds\n" \
"v_mfma_f32_16x16x32_bf16 a[152:155], a[112:115], a[24:27], a[152:155]\n" \
"v_mfma_f32_16x16x32_bf16 a[152:155], a[116:119], a[28:31], a[152:155]\n" \
"v_mfma_f32_16x16x32_bf16 a[160:163], a[112:115], a[32:35], a[160:163]\n" \
"v_mfma_f32_16x16x32_bf16 a[160:163], a[116:119], a[36:39], a[160:163]\n" \
"buffer_load_dwordx2 a[208:209], v59, s[40:43], s52 offen offset:2048 nt\n" \
"v_mfma_f32_16x16x32_bf16 a[168:171], a[112:115], a[40:43], a[168:171]\n" \
"v_mfma_f32_16x16x32_bf16 a[168:171], a[116:119], a[44:47], a[168:171]\n" \
"s_waitcnt vmcnt(33) lgkmcnt(0)\n" \
"v_mfma_f32_16x16x32_bf16 a[176:179], a[112:115], a[48:51], a[176:179]\n" \
"s_add_u32 m0, s50, 12288\n" \
"s_barrier\n" \
"v_mfma_f32_16x16x32_bf16 a[176:179], a[116:119], a[52:55], a[176:179]\n" \
"buffer_load_dwordx4 v16, s[28:31], s46 offen lds\n" \
"v_mfma_f32_16x16x32_bf16 a[184:187], a[112:115], a[56:59], a[184:187]\n" \
"v_mfma_f32_16x16x32_bf16 a[184:187], a[116:119], a[60:63], a[184:187]\n" \
"v_mfma_f32_16x16x32_bf16 a[132:135], a[120:123], a[0:3], a[132:135]\n" \
"v_mfma_f32_16x16x32_bf16 a[132:135], a[124:127], a[4:7], a[132:135]\n" \
"buffer_load_dwordx2 a[224:225], v59, s[80:83], s52 offen offset:2048 nt\n" \
"v_mfma_f32_16x16x32_bf16 a[140:143], a[120:123], a[8:11], a[140:143]\n" \
"s_mov_b32 s52, 12288\n" \
"ds_read_b128 a[0:3], v5 offset:32768\n" \
"v_mfma_f32_16x16x32_bf16 a[140:143], a[124:127], a[12:15], a[140:143]\n" \
"ds_read_b128 a[4:7], v6 offset:32768\n" \
"v_mfma_f32_16x16x32_bf16 a[148:151], a[120:123], a[16:19], a[148:151]\n" \
"ds_read_b128 a[8:11], v5 offset:34816\n" \
"v_mfma_f32_16x16x32_bf16 a[148:151], a[124:127], a[20:23], a[148:151]\n" \
"ds_read_b128 a[12:15], v6 offset:34816\n" \
"v_mfma_f32_16x16x32_bf16 a[156:159], a[120:123], a[24:27], a[156:159]\n" \
"ds_read_b128 a[16:19], v5 offset:36864\n" \
"v_mfma_f32_16x16x32_bf16 a[156:159], a[124:127], a[28:31], a[156:159]\n" \
"ds_read_b128 a[20:23], v6 offset:36864\n" \
"v_mfma_f32_16x16x32_bf16 a[164:167], a[120:123], a[32:35], a[164:167]\n" \
"ds_read_b128 a[24:27], v5 offset:38912\n" \
"v_mfma_f32_16x16x32_bf16 a[164:167], a[124:127], a[36:39], a[164:167]\n" \
"ds_read_b128 a[28:31], v6 offset:38912\n" \
"v_mfma_f32_16x16x32_bf16 a[172:175], a[120:123], a[40:43], a[172:175]\n" \
"ds_read_b128 a[32:35], v5 offset:40960\n" \
"v_mfma_f32_16x16x32_bf16 a[172:175], a[124:127], a[44:47], a[172:175]\n" \
"ds_read_b128 a[36:39], v6 offset:40960\n" \
"s_waitcnt vmcnt(10)\n" \
"v_mfma_f32_16x16x32_bf16 a[180:183], a[120:123], a[48:51], a[180:183]\n" \
"ds_read_b128 a[40:43], v5 offset:43008\n" \
"v_accvgpr_read_b32 v43, a230\n" \
"v_mfma_f32_16x16x32_bf16 a[180:183], a[124:127], a[52:55], a[180:183]\n" \
"ds_read_b128 a[44:47], v6 offset:43008\n" \
"v_lshlrev_b32 v42, 16, v43\n" \
"v_mfma_f32_16x16x32_bf16 a[188:191], a[120:123], a[56:59], a[188:191]\n" \
"ds_read_b128 a[48:51], v5 offset:45056\n" \
"v_and_b32 v43, 0xffff0000, v43\n" \
"v_mfma_f32_16x16x32_bf16 a[188:191], a[124:127], a[60:63], a[188:191]\n" \
"s_waitcnt lgkmcnt(9)\n" \
"ds_read_b128 a[52:55], v6 offset:45056\n" \
"v_accvgpr_read_b32 v45, a231\n" \
"v_mfma_f32_16x16x32_bf16 v[64:67], a[64:67], a[0:3], v[64:67]\n" \
"s_mov_b32 s47, 2097152\n" \
"s_add_u32 m0, s22, 0\n" \
"ds_read_b128 a[56:59], v5 offset:47104\n" \
"v_mfma_f32_16x16x32_bf16 v[64:67], a[68:71], a[4:7], v[64:67]\n" \
"buffer_load_dwordx4 v21, s[32:35], s47 offen offset:0 lds\n" \
"v_mfma_f32_16x16x32_bf16 v[72:75], a[64:67], a[8:11], v[72:75]\n" \
"s_waitcnt lgkmcnt(7)\n" \
"ds_read_b128 a[60:63], v6 offset:47104\n" \
"v_lshlrev_b32 v44, 16, v45\n" \
"v_mfma_f32_16x16x32_bf16 v[72:75], a[68:71], a[12:15], v[72:75]\n" \
"v_and_b32 v45, 0xffff0000, v45\n" \
"v_accvgpr_read_b32 v47, a232\n" \
"v_mfma_f32_16x16x32_bf16 v[80:83], a[64:67], a[16:19], v[80:83]\n" \
"ds_read_b64_tr_b16 a[96:97], v9 offset:16384\n" \
"v_lshlrev_b32 v46, 16, v47\n" \
"v_mfma_f32_16x16x32_bf16 v[80:83], a[68:71], a[20:23], v[80:83]\n" \
"buffer_load_dwordx2 a[210:211], v59, s[40:43], s52 offen offset:0 nt\n" \
"v_mfma_f32_16x16x32_bf16 v[88:91], a[64:67], a[24:27], v[88:91]\n" \
"s_waitcnt lgkmcnt(5)\n" \
"ds_read_b64_tr_b16 a[98:99], v10 offset:16384\n" \
"v_and_b32 v47, 0xffff0000, v47\n" \
"v_mfma_f32_16x16x32_bf16 v[88:91], a[68:71], a[28:31], v[88:91]\n" \
"v_accvgpr_read_b32 v49, a233\n" \
"v_lshlrev_b32 v48, 16, v49\n" \
"v_mfma_f32_16x16x32_bf16 v[96:99], a[64:67], a[32:35], v[96:99]\n" \
"s_mov_b32 s46, 512\n" \
"s_add_u32 m0, s50, 16384\n" \
"ds_read_b64_tr_b16 a[100:101], v9 offset:17408\n" \
"v_mfma_f32_16x16x32_bf16 v[96:99], a[68:71], a[36:39], v[96:99]\n" \
"buffer_load_dwordx4 v17, s[28:31], s46 offen lds\n" \
"v_mfma_f32_16x16x32_bf16 v[104:107], a[64:67], a[40:43], v[104:107]\n" \
"s_waitcnt lgkmcnt(3)\n" \
"ds_read_b64_tr_b16 a[102:103], v10 offset:17408\n" \
"v_and_b32 v49, 0xffff0000, v49\n" \
"v_mfma_f32_16x16x32_bf16 v[104:107], a[68:71], a[44:47], v[104:107]\n" \
"LH0_%=:\ns_cbranch_execz LH8_%=\n" \
"v_accvgpr_read_b32 v36, a192\n" \
"v_lshlrev_b32 v30, 16, v36\n" \
"v_mfma_f32_16x16x32_bf16 v[112:115], a[64:67], a[48:51], v[112:115]\n" \
"ds_read_b64_tr_b16 a[104:105], v11 offset:16384\n" \
"v_and_b32 v36, 0xffff0000, v36\n" \
"v_mfma_f32_16x16x32_bf16 v[112:115], a[68:71], a[52:55], v[112:115]\n" \
"buffer_load_dwordx2 a[226:227], v59, s[80:83], s52 offen offset:0 nt\n" \
"v_mfma_f32_16x16x32_bf16 v[120:123], a[64:67], a[56:59], v[120:123]\n" \
"ds_read_b64_tr_b16 a[106:107], v12 offset:16384\n" \
"v_accvgpr_read_b32 v37, a194\n" \
"v_mfma_f32_16x16x32_bf16 v[120:123], a[68:71], a[60:63], v[120:123]\n" \
"v_lshlrev_b32 v31, 16, v37\n" \
"v_and_b32 v37, 0xffff0000, v37\n" \
"v_mfma_f32_16x16x32_bf16 v[68:71], a[72:75], a[0:3], v[68:71]\n" \
"s_add_u32 m0, s22, 1792\n" \
"ds_read_b64_tr_b16 a[108:109], v11 offset:17408\n" \
"v_accvgpr_read_b32 v38, a196\n" \
"v_mfma_f32_16x16x32_bf16 v[68:71], a[76:79], a[4:7], v[68:71]\n" \
"buffer_load_dwordx4 v21, s[32:35], s47 offen offset:256 lds\n" \
"v_mfma_f32_16x16x32_bf16 v[76:79], a[72:75], a[8:11], v[76:79]\n" \
"ds_read_b64_tr_b16 a[110:111], v12 offset:17408\n" \
"v_lshlrev_b32 v32, 16, v38\n" \
"v_mfma_f32_16x16x32_bf16 v[76:79], a[76:79], a[12:15], v[76:79]\n" \
"v_and_b32 v38, 0xffff0000, v38\n" \
"v_accvgpr_read_b32 v39, a198\n" \
"v_mfma_f32_16x16x32_bf16 v[84:87], a[72:75], a[16:19], v[84:87]\n" \
"ds_read_b64_tr_b16 a[112:113], v9 offset:16896\n" \
"v_lshlrev_b32 v33, 16, v39\n" \
"v_mfma_f32_16x16x32_bf16 v[84:87], a[76:79], a[20:23], v[84:87]\n" \
"buffer_load_dwordx2 a[212:213], v59, s[40:43], s52 offen offset:2048 nt\n" \
"v_mfma_f32_16x16x32_bf16 v[92:95], a[72:75], a[24:27], v[92:95]\n" \
"ds_read_b64_tr_b16 a[114:115], v10 offset:16896\n" \
"v_and_b32 v39, 0xffff0000, v39\n" \
"v_mfma_f32_16x16x32_bf16 v[92:95], a[76:79], a[28:31], v[92:95]\n" \
"v_mul_f32 v25, v30, v45\n" \
"v_mul_f32 v26, v36, v49\n" \
"v_mfma_f32_16x16x32_bf16 v[100:103], a[72:75], a[32:35], v[100:103]\n" \
"s_add_u32 m0, s50, 20480\n" \
"ds_read_b64_tr_b16 a[116:117], v9 offset:17920\n" \
"v_fmac_f32 v25, v31, v44\n" \
"v_mfma_f32_16x16x32_bf16 v[100:103], a[76:79], a[36:39], v[100:103]\n" \
"buffer_load_dwordx4 v18, s[28:31], s46 offen lds\n" \
"v_mfma_f32_16x16x32_bf16 v[108:111], a[72:75], a[40:43], v[108:111]\n" \
"ds_read_b64_tr_b16 a[118:119], v10 offset:17920\n" \
"v_fmac_f32 v26, v37, v48\n" \
"v_mfma_f32_16x16x32_bf16 v[108:111], a[76:79], a[44:47], v[108:111]\n" \
"v_fmac_f32 v25, v32, v43\n" \
"v_fmac_f32 v26, v38, v47\n" \
"v_mfma_f32_16x16x32_bf16 v[116:119], a[72:75], a[48:51], v[116:119]\n" \
"ds_read_b64_tr_b16 a[120:121], v11 offset:16896\n" \
"v_fmac_f32 v25, v33, v42\n" \
"v_mfma_f32_16x16x32_bf16 v[116:119], a[76:79], a[52:55], v[116:119]\n" \
"buffer_load_dwordx2 a[228:229], v59, s[80:83], s52 offen offset:2048 nt\n" \
"v_mfma_f32_16x16x32_bf16 v[124:127], a[72:75], a[56:59], v[124:127]\n" \
"ds_read_b64_tr_b16 a[122:123], v12 offset:16896\n" \
"v_fmac_f32 v26, v39, v46\n" \
"v_mfma_f32_16x16x32_bf16 v[124:127], a[76:79], a[60:63], v[124:127]\n" \
"v_accvgpr_read_b32 v28, a214\n" \
"v_lshlrev_b32 v27, 16, v28\n" \
"v_mfma_f32_16x16x32_bf16 v[128:131], a[80:83], a[0:3], v[128:131]\n" \
"s_mov_b32 s47, 2359296\n" \
"s_add_u32 m0, s22, 1024\n" \
"ds_read_b64_tr_b16 a[124:125], v11 offset:17920\n" \
"v_mfma_f32_16x16x32_bf16 v[128:131], a[84:87], a[4:7], v[128:131]\n" \
"buffer_load_dwordx4 v21, s[32:35], s47 offen offset:0 lds\n" \
"v_mfma_f32_16x16x32_bf16 v[136:139], a[80:83], a[8:11], v[136:139]\n" \
"ds_read_b64_tr_b16 a[126:127], v12 offset:17920\n" \
"v_and_b32 v28, 0xffff0000, v28\n" \
"v_mfma_f32_16x16x32_bf16 v[136:139], a[84:87], a[12:15], v[136:139]\n" \
"v_mul_f32 v29, v25, v27\n" \
"v_mul_f32 v58, v26, v28\n" \
"v_mfma_f32_16x16x32_bf16 v[144:147], a[80:83], a[16:19], v[144:147]\n" \
"v_accvgpr_read_b32 v40, a200\n" \
"v_lshlrev_b32 v34, 16, v40\n" \
"v_mfma_f32_16x16x32_bf16 v[144:147], a[84:87], a[20:23], v[144:147]\n" \
"v_and_b32 v40, 0xffff0000, v40\n" \
"v_mfma_f32_16x16x32_bf16 v[152:155], a[80:83], a[24:27], v[152:155]\n" \
"v_mul_f32 v25, v31, v45\n" \
"v_mul_f32 v26, v37, v49\n" \
"v_mfma_f32_16x16x32_bf16 v[152:155], a[84:87], a[28:31], v[152:155]\n" \
"v_fmac_f32 v25, v32, v44\n" \
"v_fmac_f32 v26, v38, v48\n" \
"v_mfma_f32_16x16x32_bf16 v[160:163], a[80:83], a[32:35], v[160:163]\n" \
"s_add_u32 m0, s50, 24576\n" \
"v_fmac_f32 v25, v33, v43\n" \
"v_mfma_f32_16x16x32_bf16 v[160:163], a[84:87], a[36:39], v[160:163]\n" \
"buffer_load_dwordx4 v19, s[28:31], s46 offen lds\n" \
"v_mfma_f32_16x16x32_bf16 v[168:171], a[80:83], a[40:43], v[168:171]\n" \
"v_fmac_f32 v26, v39, v47\n" \
"v_fmac_f32 v25, v34, v42\n" \
"v_mfma_f32_16x16x32_bf16 v[168:171], a[84:87], a[44:47], v[168:171]\n" \
"s_waitcnt vmcnt(26) lgkmcnt(0)\n" \
"v_fmac_f32 v26, v40, v46\n" \
"v_accvgpr_read_b32 v28, a216\n" \
"v_mfma_f32_16x16x32_bf16 v[176:179], a[80:83], a[48:51], v[176:179]\n" \
"s_barrier\n" \
"v_lshlrev_b32 v27, 16, v28\n" \
"v_mfma_f32_16x16x32_bf16 v[176:179], a[84:87], a[52:55], v[176:179]\n" \
"v_and_b32 v28, 0xffff0000, v28\n" \
"v_mul_f32 v25, v25, v27\n" \
"v_mfma_f32_16x16x32_bf16 v[184:187], a[80:83], a[56:59], v[184:187]\n" \
"v_mul_f32 v26, v26, v28\n" \
"v_cvt_pk_bf16_f32 v53, v25, v29\n" \
"v_mfma_f32_16x16x32_bf16 v[184:187], a[84:87], a[60:63], v[184:187]\n" \
"v_cvt_pk_bf16_f32 v57, v26, v58\n" \
"v_mfma_f32_16x16x32_bf16 v[132:135], a[88:91], a[0:3], v[132:135]\n" \
"s_add_u32 m0, s22, 2816\n" \
"v_accvgpr_read_b32 v41, a202\n" \
"v_lshlrev_b32 v35, 16, v41\n" \
"v_mfma_f32_16x16x32_bf16 v[132:135], a[92:95], a[4:7], v[132:135]\n" \
"buffer_load_dwordx4 v21, s[32:35], s47 offen offset:256 lds\n" \
"v_mfma_f32_16x16x32_bf16 v[140:143], a[88:91], a[8:11], v[140:143]\n" \
"s_add_u32 s84, s84, 1048576\ns_addc_u32 s85, s85, 0\n" \
"v_and_b32 v41, 0xffff0000, v41\n" \
"v_mfma_f32_16x16x32_bf16 v[140:143], a[92:95], a[12:15], v[140:143]\n" \
"v_mul_f32 v25, v32, v45\n" \
"v_mul_f32 v26, v38, v49\n" \
"v_mfma_f32_16x16x32_bf16 v[148:151], a[88:91], a[16:19], v[148:151]\n" \
"v_fmac_f32 v25, v33, v44\n" \
"v_fmac_f32 v26, v39, v48\n" \
"v_mfma_f32_16x16x32_bf16 v[148:151], a[92:95], a[20:23], v[148:151]\n" \
"v_fmac_f32 v25, v34, v43\n" \
"v_mfma_f32_16x16x32_bf16 v[156:159], a[88:91], a[24:27], v[156:159]\n" \
"v_fmac_f32 v26, v40, v47\n" \
"v_fmac_f32 v25, v35, v42\n" \
"v_mfma_f32_16x16x32_bf16 v[156:159], a[92:95], a[28:31], v[156:159]\n" \
"v_fmac_f32 v26, v41, v46\n" \
"v_accvgpr_read_b32 v28, a218\n" \
"v_mfma_f32_16x16x32_bf16 v[164:167], a[88:91], a[32:35], v[164:167]\n" \
"s_add_u32 m0, s50, 28672\n" \
"v_lshlrev_b32 v27, 16, v28\n" \
"v_mfma_f32_16x16x32_bf16 v[164:167], a[92:95], a[36:39], v[164:167]\n" \
"buffer_load_dwordx4 v20, s[28:31], s46 offen lds\n" \
"v_mfma_f32_16x16x32_bf16 v[172:175], a[88:91], a[40:43], v[172:175]\n" \
"v_and_b32 v28, 0xffff0000, v28\n" \
"v_mul_f32 v29, v25, v27\n" \
"v_mfma_f32_16x16x32_bf16 v[172:175], a[92:95], a[44:47], v[172:175]\n" \
"v_mul_f32 v58, v26, v28\n" \
"v_accvgpr_read_b32 v36, a204\n" \
"v_mfma_f32_16x16x32_bf16 v[180:183], a[88:91], a[48:51], v[180:183]\n" \
"v_lshlrev_b32 v30, 16, v36\n" \
"v_mfma_f32_16x16x32_bf16 v[180:183], a[92:95], a[52:55], v[180:183]\n" \
"s_mov_b64 exec, 0xffff\nglobal_load_dword v22, v1, s[84:85]\ns_mov_b64 exec, -1\n" \
"v_mfma_f32_16x16x32_bf16 v[188:191], a[88:91], a[56:59], v[188:191]\n" \
"v_and_b32 v36, 0xffff0000, v36\n" \
"v_mul_f32 v25, v33, v45\n" \
"v_mfma_f32_16x16x32_bf16 v[188:191], a[92:95], a[60:63], v[188:191]\n" \
"v_mul_f32 v26, v39, v49\n" \
"v_fmac_f32 v25, v34, v44\n" \
"v_mfma_f32_16x16x32_bf16 v[192:195], a[96:99], a[0:3], v[192:195]\n" \
"s_mov_b32 s47, 2097152\n" \
"s_add_u32 m0, s22, 16256\n" \
"v_fmac_f32 v26, v40, v48\n" \
"v_mfma_f32_16x16x32_bf16 v[192:195], a[100:103], a[4:7], v[192:195]\n" \
"buffer_load_dwordx4 v21, s[32:35], s47 offen offset:128 lds\n" \
"v_mfma_f32_16x16x32_bf16 v[200:203], a[96:99], a[8:11], v[200:203]\n" \
"v_fmac_f32 v25, v35, v43\n" \
"v_fmac_f32 v26, v41, v47\n" \
"v_mfma_f32_16x16x32_bf16 v[200:203], a[100:103], a[12:15], v[200:203]\n" \
"v_fmac_f32 v25, v30, v42\n" \
"v_fmac_f32 v26, v36, v46\n" \
"v_mfma_f32_16x16x32_bf16 v[208:211], a[96:99], a[16:19], v[208:211]\n" \
"s_mov_b32 s46, 640\n" \
"s_add_u32 m0, s50, 32768\n" \
"ds_read_b64_tr_b16 a[64:65], v9 offset:32768\n" \
"v_mfma_f32_16x16x32_bf16 v[208:211], a[100:103], a[20:23], v[208:211]\n" \
"buffer_load_dwordx4 v13, s[28:31], s46 offen lds\n" \
"v_mfma_f32_16x16x32_bf16 v[216:219], a[96:99], a[24:27], v[216:219]\n" \
"ds_read_b64_tr_b16 a[66:67], v10 offset:32768\n" \
"v_accvgpr_read_b32 v28, a220\n" \
"v_mfma_f32_16x16x32_bf16 v[216:219], a[100:103], a[28:31], v[216:219]\n" \
"v_lshlrev_b32 v27, 16, v28\n" \
"v_and_b32 v28, 0xffff0000, v28\n" \
"v_mfma_f32_16x16x32_bf16 v[224:227], a[96:99], a[32:35], v[224:227]\n" \
"ds_read_b64_tr_b16 a[68:69], v9 offset:33792\n" \
"v_mul_f32 v25, v25, v27\n" \
"v_mfma_f32_16x16x32_bf16 v[224:227], a[100:103], a[36:39], v[224:227]\n" \
"v_mul_f32 v26, v26, v28\n" \
"v_cvt_pk_bf16_f32 v52, v25, v29\n" \
"v_mfma_f32_16x16x32_bf16 v[232:235], a[96:99], a[40:43], v[232:235]\n" \
"ds_read_b64_tr_b16 a[70:71], v10 offset:33792\n" \
"v_cvt_pk_bf16_f32 v56, v26, v58\n" \
"v_mfma_f32_16x16x32_bf16 v[232:235], a[100:103], a[44:47], v[232:235]\n" \
"v_mfma_f32_16x16x32_bf16 v[240:243], a[96:99], a[48:51], v[240:243]\n" \
"s_add_u32 m0, s22, 18048\n" \
"ds_read_b64_tr_b16 a[72:73], v11 offset:32768\n" \
"v_mfma_f32_16x16x32_bf16 v[240:243], a[100:103], a[52:55], v[240:243]\n" \
"buffer_load_dwordx4 v21, s[32:35], s47 offen offset:384 lds\n" \
"v_mfma_f32_16x16x32_bf16 v[248:251], a[96:99], a[56:59], v[248:251]\n" \
"ds_read_b64_tr_b16 a[74:75], v12 offset:32768\n" \
"v_mfma_f32_16x16x32_bf16 v[248:251], a[100:103], a[60:63], v[248:251]\n" \
"s_waitcnt vmcnt(8)\n" \
"v_accvgpr_read_b32 v37, a206\n" \
"v_lshlrev_b32 v31, 16, v37\n" \
"v_mfma_f32_16x16x32_bf16 v[196:199], a[104:107], a[0:3], v[196:199]\n" \
"s_add_u32 m0, s50, 36864\n" \
"ds_read_b64_tr_b16 a[76:77], v11 offset:33792\n" \
"v_and_b32 v37, 0xffff0000, v37\n" \
"v_mfma_f32_16x16x32_bf16 v[196:199], a[108:111], a[4:7], v[196:199]\n" \
"buffer_load_dwordx4 v14, s[28:31], s46 offen lds\n" \
"v_mfma_f32_16x16x32_bf16 v[204:207], a[104:107], a[8:11], v[204:207]\n" \
"ds_read_b64_tr_b16 a[78:79], v12 offset:33792\n" \
"v_mul_f32 v25, v34, v45\n" \
"v_mfma_f32_16x16x32_bf16 v[204:207], a[108:111], a[12:15], v[204:207]\n" \
"v_mul_f32 v26, v40, v49\n" \
"v_fmac_f32 v25, v35, v44\n" \
"v_mfma_f32_16x16x32_bf16 v[212:215], a[104:107], a[16:19], v[212:215]\n" \
"ds_read_b64_tr_b16 a[80:81], v9 offset:33280\n" \
"v_fmac_f32 v26, v41, v48\n" \
"v_mfma_f32_16x16x32_bf16 v[212:215], a[108:111], a[20:23], v[212:215]\n" \
"v_fmac_f32 v25, v30, v43\n" \
"v_fmac_f32 v26, v36, v47\n" \
"v_mfma_f32_16x16x32_bf16 v[220:223], a[104:107], a[24:27], v[220:223]\n" \
"ds_read_b64_tr_b16 a[82:83], v10 offset:33280\n" \
"v_fmac_f32 v25, v31, v42\n" \
"v_mfma_f32_16x16x32_bf16 v[220:223], a[108:111], a[28:31], v[220:223]\n" \
"v_fmac_f32 v26, v37, v46\n" \
"v_accvgpr_read_b32 v28, a222\n" \
"v_mfma_f32_16x16x32_bf16 v[228:231], a[104:107], a[32:35], v[228:231]\n" \
"s_mov_b32 s47, 2359296\n" \
"s_add_u32 m0, s22, 17280\n" \
"ds_read_b64_tr_b16 a[84:85], v9 offset:34304\n" \
"v_mfma_f32_16x16x32_bf16 v[228:231], a[108:111], a[36:39], v[228:231]\n" \
"buffer_load_dwordx4 v21, s[32:35], s47 offen offset:128 lds\n" \
"v_mfma_f32_16x16x32_bf16 v[236:239], a[104:107], a[40:43], v[236:239]\n" \
"ds_read_b64_tr_b16 a[86:87], v10 offset:34304\n" \
"v_lshlrev_b32 v27, 16, v28\n" \
"v_mfma_f32_16x16x32_bf16 v[236:239], a[108:111], a[44:47], v[236:239]\n" \
"v_and_b32 v28, 0xffff0000, v28\n" \
"v_mul_f32 v29, v25, v27\n" \
"v_mfma_f32_16x16x32_bf16 v[244:247], a[104:107], a[48:51], v[244:247]\n" \
"s_add_u32 m0, s50, 40960\n" \
"ds_read_b64_tr_b16 a[88:89], v11 offset:33280\n" \
"v_mul_f32 v58, v26, v28\n" \
"v_mfma_f32_16x16x32_bf16 v[244:247], a[108:111], a[52:55], v[244:247]\n" \
"buffer_load_dwordx4 v15, s[28:31], s46 offen lds\n" \
"v_mfma_f32_16x16x32_bf16 v[252:255], a[104:107], a[56:59], v[252:255]\n" \
"ds_read_b64_tr_b16 a[90:91], v12 offset:33280\n" \
"v_accvgpr_read_b32 v38, a208\n" \
"v_mfma_f32_16x16x32_bf16 v[252:255], a[108:111], a[60:63], v[252:255]\n" \
"v_lshlrev_b32 v32, 16, v38\n" \
"v_and_b32 v38, 0xffff0000, v38\n" \
"v_mfma_f32_16x16x32_bf16 a[128:131], a[112:115], a[0:3], a[128:131]\n" \
"ds_read_b64_tr_b16 a[92:93], v11 offset:34304\n" \
"v_mul_f32 v25, v35, v45\n" \
"v_mfma_f32_16x16x32_bf16 a[128:131], a[116:119], a[4:7], a[128:131]\n" \
"v_mul_f32 v26, v41, v49\n" \
"v_fmac_f32 v25, v30, v44\n" \
"v_mfma_f32_16x16x32_bf16 a[136:139], a[112:115], a[8:11], a[136:139]\n" \
"ds_read_b64_tr_b16 a[94:95], v12 offset:34304\n" \
"v_fmac_f32 v26, v36, v48\n" \
"v_mfma_f32_16x16x32_bf16 a[136:139], a[116:119], a[12:15], a[136:139]\n" \
"v_fmac_f32 v25, v31, v43\n" \
"v_fmac_f32 v26, v37, v47\n" \
"v_mfma_f32_16x16x32_bf16 a[144:147], a[112:115], a[16:19], a[144:147]\n" \
"s_add_u32 m0, s22, 19072\n" \
"v_fmac_f32 v25, v32, v42\n" \
"v_fmac_f32 v26, v38, v46\n" \
"v_mfma_f32_16x16x32_bf16 a[144:147], a[116:119], a[20:23], a[144:147]\n" \
"buffer_load_dwordx4 v21, s[32:35], s47 offen offset:384 lds\n" \
"v_mfma_f32_16x16x32_bf16 a[152:155], a[112:115], a[24:27], a[152:155]\n" \
"v_accvgpr_read_b32 v28, a224\n" \
"v_lshlrev_b32 v27, 16, v28\n" \
"v_mfma_f32_16x16x32_bf16 a[152:155], a[116:119], a[28:31], a[152:155]\n" \
"v_and_b32 v28, 0xffff0000, v28\n" \
"v_mul_f32 v25, v25, v27\n" \
"v_mfma_f32_16x16x32_bf16 a[160:163], a[112:115], a[32:35], a[160:163]\n" \
"v_mul_f32 v26, v26, v28\n" \
"v_mfma_f32_16x16x32_bf16 a[160:163], a[116:119], a[36:39], a[160:163]\n" \
"v_cvt_pk_bf16_f32 v51, v25, v29\n" \
"v_cvt_pk_bf16_f32 v55, v26, v58\n" \
"v_mfma_f32_16x16x32_bf16 a[168:171], a[112:115], a[40:43], a[168:171]\n" \
"v_accvgpr_read_b32 v39, a210\n" \
"v_lshlrev_b32 v33, 16, v39\n" \
"v_mfma_f32_16x16x32_bf16 a[168:171], a[116:119], a[44:47], a[168:171]\n" \
"s_waitcnt vmcnt(23) lgkmcnt(0)\n" \
"v_and_b32 v39, 0xffff0000, v39\n" \
"v_mfma_f32_16x16x32_bf16 a[176:179], a[112:115], a[48:51], a[176:179]\n" \
"s_add_u32 m0, s50, 45056\n" \
"s_barrier\n" \
"v_mul_f32 v25, v30, v45\n" \
"v_mfma_f32_16x16x32_bf16 a[176:179], a[116:119], a[52:55], a[176:179]\n" \
"buffer_load_dwordx4 v16, s[28:31], s46 offen lds\n" \
"v_mfma_f32_16x16x32_bf16 a[184:187], a[112:115], a[56:59], a[184:187]\n" \
"v_mul_f32 v26, v36, v49\n" \
"v_fmac_f32 v25, v31, v44\n" \
"v_mfma_f32_16x16x32_bf16 a[184:187], a[116:119], a[60:63], a[184:187]\n" \
"v_fmac_f32 v26, v37, v48\n" \
"v_fmac_f32 v25, v32, v43\n" \
"v_mfma_f32_16x16x32_bf16 a[132:135], a[120:123], a[0:3], a[132:135]\n" \
"v_fmac_f32 v26, v38, v47\n" \
"v_mfma_f32_16x16x32_bf16 a[132:135], a[124:127], a[4:7], a[132:135]\n" \
"v_fmac_f32 v25, v33, v42\n" \
"v_fmac_f32 v26, v39, v46\n" \
"v_mfma_f32_16x16x32_bf16 a[140:143], a[120:123], a[8:11], a[140:143]\n" \
"ds_read_b128 a[0:3], v7 offset:0\n" \
"v_accvgpr_read_b32 v28, a226\n" \
"v_mfma_f32_16x16x32_bf16 a[140:143], a[124:127], a[12:15], a[140:143]\n" \
"ds_read_b128 a[4:7], v8 offset:0\n" \
"v_lshlrev_b32 v27, 16, v28\n" \
"v_mfma_f32_16x16x32_bf16 a[148:151], a[120:123], a[16:19], a[148:151]\n" \
"ds_read_b128 a[8:11], v7 offset:2048\n" \
"v_and_b32 v28, 0xffff0000, v28\n" \
"v_mfma_f32_16x16x32_bf16 a[148:151], a[124:127], a[20:23], a[148:151]\n" \
"ds_read_b128 a[12:15], v8 offset:2048\n" \
"v_mul_f32 v29, v25, v27\n" \
"v_mfma_f32_16x16x32_bf16 a[156:159], a[120:123], a[24:27], a[156:159]\n" \
"ds_read_b128 a[16:19], v7 offset:4096\n" \
"v_mul_f32 v58, v26, v28\n" \
"v_mfma_f32_16x16x32_bf16 a[156:159], a[124:127], a[28:31], a[156:159]\n" \
"ds_read_b128 a[20:23], v8 offset:4096\n" \
"v_accvgpr_read_b32 v40, a212\n" \
"v_mfma_f32_16x16x32_bf16 a[164:167], a[120:123], a[32:35], a[164:167]\n" \
"ds_read_b128 a[24:27], v7 offset:6144\n" \
"v_lshlrev_b32 v34, 16, v40\n" \
"v_mfma_f32_16x16x32_bf16 a[164:167], a[124:127], a[36:39], a[164:167]\n" \
"ds_read_b128 a[28:31], v8 offset:6144\n" \
"v_and_b32 v40, 0xffff0000, v40\n" \
"v_mfma_f32_16x16x32_bf16 a[172:175], a[120:123], a[40:43], a[172:175]\n" \
"ds_read_b128 a[32:35], v7 offset:8192\n" \
"v_mul_f32 v25, v31, v45\n" \
"v_mfma_f32_16x16x32_bf16 a[172:175], a[124:127], a[44:47], a[172:175]\n" \
"ds_read_b128 a[36:39], v8 offset:8192\n" \
"v_mul_f32 v26, v37, v49\n" \
"v_mfma_f32_16x16x32_bf16 a[180:183], a[120:123], a[48:51], a[180:183]\n" \
"ds_read_b128 a[40:43], v7 offset:10240\n" \
"v_fmac_f32 v25, v32, v44\n" \
"v_mfma_f32_16x16x32_bf16 a[180:183], a[124:127], a[52:55], a[180:183]\n" \
"ds_read_b128 a[44:47], v8 offset:10240\n" \
"v_fmac_f32 v26, v38, v48\n" \
"v_mfma_f32_16x16x32_bf16 a[188:191], a[120:123], a[56:59], a[188:191]\n" \
"ds_read_b128 a[48:51], v7 offset:12288\n" \
"v_fmac_f32 v25, v33, v43\n" \
"v_mfma_f32_16x16x32_bf16 a[188:191], a[124:127], a[60:63], a[188:191]\n" \
"s_waitcnt lgkmcnt(9)\n" \
"ds_read_b128 a[52:55], v8 offset:12288\n" \
"v_fmac_f32 v26, v39, v47\n" \
"v_mfma_f32_16x16x32_bf16 v[64:67], a[64:67], a[0:3], v[64:67]\n" \
"s_mov_b32 s47, 2621440\n" \
"s_add_u32 m0, s22, 32768\n" \
"ds_read_b128 a[56:59], v7 offset:14336\n" \
"v_mfma_f32_16x16x32_bf16 v[64:67], a[68:71], a[4:7], v[64:67]\n" \
"buffer_load_dwordx4 v21, s[32:35], s47 offen offset:0 lds\n" \
"v_mfma_f32_16x16x32_bf16 v[72:75], a[64:67], a[8:11], v[72:75]\n" \
"s_waitcnt lgkmcnt(7)\n" \
"ds_read_b128 a[60:63], v8 offset:14336\n" \
"v_fmac_f32 v25, v34, v42\n" \
"v_mfma_f32_16x16x32_bf16 v[72:75], a[68:71], a[12:15], v[72:75]\n" \
"v_fmac_f32 v26, v40, v46\n" \
"v_accvgpr_read_b32 v28, a228\n" \
"v_mfma_f32_16x16x32_bf16 v[80:83], a[64:67], a[16:19], v[80:83]\n" \
"s_mov_b32 s46, 640\n" \
"s_add_u32 m0, s50, 49152\n" \
"ds_read_b64_tr_b16 a[96:97], v9 offset:49152\n" \
"v_mfma_f32_16x16x32_bf16 v[80:83], a[68:71], a[20:23], v[80:83]\n" \
"buffer_load_dwordx4 v17, s[28:31], s46 offen lds\n" \
"v_mfma_f32_16x16x32_bf16 v[88:91], a[64:67], a[24:27], v[88:91]\n" \
"s_waitcnt lgkmcnt(5)\n" \
"ds_read_b64_tr_b16 a[98:99], v10 offset:49152\n" \
"v_lshlrev_b32 v27, 16, v28\n" \
"v_mfma_f32_16x16x32_bf16 v[88:91], a[68:71], a[28:31], v[88:91]\n" \
"v_and_b32 v28, 0xffff0000, v28\n" \
"v_mul_f32 v25, v25, v27\n" \
"v_mfma_f32_16x16x32_bf16 v[96:99], a[64:67], a[32:35], v[96:99]\n" \
"s_add_u32 m0, s22, 34560\n" \
"ds_read_b64_tr_b16 a[100:101], v9 offset:50176\n" \
"v_mul_f32 v26, v26, v28\n" \
"v_mfma_f32_16x16x32_bf16 v[96:99], a[68:71], a[36:39], v[96:99]\n" \
"buffer_load_dwordx4 v21, s[32:35], s47 offen offset:256 lds\n" \
"v_mfma_f32_16x16x32_bf16 v[104:107], a[64:67], a[40:43], v[104:107]\n" \
"s_waitcnt lgkmcnt(3)\n" \
"ds_read_b64_tr_b16 a[102:103], v10 offset:50176\n" \
"v_cvt_pk_bf16_f32 v50, v25, v29\n" \
"v_mfma_f32_16x16x32_bf16 v[104:107], a[68:71], a[44:47], v[104:107]\n" \
"s_mov_b32 s94, 79691776\n" \
"v_cvt_pk_bf16_f32 v54, v26, v58\n" \
"v_accvgpr_read_b32 v43, a234\n" \
"v_mfma_f32_16x16x32_bf16 v[112:115], a[64:67], a[48:51], v[112:115]\n" \
"s_add_u32 m0, s50, 53248\n" \
"ds_read_b64_tr_b16 a[104:105], v11 offset:49152\n" \
"v_lshlrev_b32 v42, 16, v43\n" \
"v_mfma_f32_16x16x32_bf16 v[112:115], a[68:71], a[52:55], v[112:115]\n" \
"buffer_load_dwordx4 v18, s[28:31], s46 offen lds\n" \
"v_mfma_f32_16x16x32_bf16 v[120:123], a[64:67], a[56:59], v[120:123]\n" \
"ds_read_b64_tr_b16 a[106:107], v12 offset:49152\n" \
"v_and_b32 v43, 0xffff0000, v43\n" \
"v_mfma_f32_16x16x32_bf16 v[120:123], a[68:71], a[60:63], v[120:123]\n" \
"buffer_store_dwordx4 v[50:53], v60, s[36:39], s94 offen offset:0 sc1\n" \
"v_mfma_f32_16x16x32_bf16 v[68:71], a[72:75], a[0:3], v[68:71]\n" \
"s_mov_b32 s47, 2883584\n" \
"s_add_u32 m0, s22, 33792\n" \
"ds_read_b64_tr_b16 a[108:109], v11 offset:50176\n" \
"v_mfma_f32_16x16x32_bf16 v[68:71], a[76:79], a[4:7], v[68:71]\n" \
"buffer_load_dwordx4 v21, s[32:35], s47 offen offset:0 lds\n" \
"v_mfma_f32_16x16x32_bf16 v[76:79], a[72:75], a[8:11], v[76:79]\n" \
"s_mov_b32 s95, 79695872\n" \
"ds_read_b64_tr_b16 a[110:111], v12 offset:50176\n" \
"v_accvgpr_read_b32 v45, a235\n" \
"v_mfma_f32_16x16x32_bf16 v[76:79], a[76:79], a[12:15], v[76:79]\n" \
"v_lshlrev_b32 v44, 16, v45\n" \
"v_and_b32 v45, 0xffff0000, v45\n" \
"v_mfma_f32_16x16x32_bf16 v[84:87], a[72:75], a[16:19], v[84:87]\n" \
"s_add_u32 m0, s50, 57344\n" \
"ds_read_b64_tr_b16 a[112:113], v9 offset:49664\n" \
"v_accvgpr_read_b32 v47, a236\n" \
"v_mfma_f32_16x16x32_bf16 v[84:87], a[76:79], a[20:23], v[84:87]\n" \
"buffer_load_dwordx4 v19, s[28:31], s46 offen lds\n" \
"v_mfma_f32_16x16x32_bf16 v[92:95], a[72:75], a[24:27], v[92:95]\n" \
"ds_read_b64_tr_b16 a[114:115], v10 offset:49664\n" \
"v_lshlrev_b32 v46, 16, v47\n" \
"v_mfma_f32_16x16x32_bf16 v[92:95], a[76:79], a[28:31], v[92:95]\n" \
"v_and_b32 v47, 0xffff0000, v47\n" \
"v_accvgpr_read_b32 v49, a237\n" \
"v_mfma_f32_16x16x32_bf16 v[100:103], a[72:75], a[32:35], v[100:103]\n" \
"s_add_u32 m0, s22, 35584\n" \
"ds_read_b64_tr_b16 a[116:117], v9 offset:50688\n" \
"v_lshlrev_b32 v48, 16, v49\n" \
"v_mfma_f32_16x16x32_bf16 v[100:103], a[76:79], a[36:39], v[100:103]\n" \
"buffer_load_dwordx4 v21, s[32:35], s47 offen offset:256 lds\n" \
"v_mfma_f32_16x16x32_bf16 v[108:111], a[72:75], a[40:43], v[108:111]\n" \
"s_add_u32 s84, s84, 1048576\ns_addc_u32 s85, s85, 0\n" \
"ds_read_b64_tr_b16 a[118:119], v10 offset:50688\n" \
"v_mfma_f32_16x16x32_bf16 v[108:111], a[76:79], a[44:47], v[108:111]\n" \
"v_and_b32 v49, 0xffff0000, v49\n" \
"v_accvgpr_read_b32 v36, a193\n" \
"v_mfma_f32_16x16x32_bf16 v[116:119], a[72:75], a[48:51], v[116:119]\n" \
"s_add_u32 m0, s50, 61440\n" \
"ds_read_b64_tr_b16 a[120:121], v11 offset:49664\n" \
"v_lshlrev_b32 v30, 16, v36\n" \
"v_mfma_f32_16x16x32_bf16 v[116:119], a[76:79], a[52:55], v[116:119]\n" \
"buffer_load_dwordx4 v20, s[28:31], s46 offen lds\n" \
"v_mfma_f32_16x16x32_bf16 v[124:127], a[72:75], a[56:59], v[124:127]\n" \
"ds_read_b64_tr_b16 a[122:123], v12 offset:49664\n" \
"v_and_b32 v36, 0xffff0000, v36\n" \
"v_mfma_f32_16x16x32_bf16 v[124:127], a[76:79], a[60:63], v[124:127]\n" \
"buffer_store_dwordx4 v[54:57], v60, s[36:39], s95 offen offset:0 sc1\n" \
"v_mfma_f32_16x16x32_bf16 v[128:131], a[80:83], a[0:3], v[128:131]\n" \
"ds_read_b64_tr_b16 a[124:125], v11 offset:50688\n" \
"v_accvgpr_read_b32 v37, a195\n" \
"v_mfma_f32_16x16x32_bf16 v[128:131], a[84:87], a[4:7], v[128:131]\n" \
"s_mov_b64 exec, 0xffff\nglobal_load_dword v22, v1, s[84:85]\ns_mov_b64 exec, -1\n" \
"v_mfma_f32_16x16x32_bf16 v[136:139], a[80:83], a[8:11], v[136:139]\n" \
"ds_read_b64_tr_b16 a[126:127], v12 offset:50688\n" \
"v_lshlrev_b32 v31, 16, v37\n" \
"v_mfma_f32_16x16x32_bf16 v[136:139], a[84:87], a[12:15], v[136:139]\n" \
"v_and_b32 v37, 0xffff0000, v37\n" \
"v_accvgpr_read_b32 v38, a197\n" \
"v_mfma_f32_16x16x32_bf16 v[144:147], a[80:83], a[16:19], v[144:147]\n" \
"v_lshlrev_b32 v32, 16, v38\n" \
"v_and_b32 v38, 0xffff0000, v38\n" \
"v_mfma_f32_16x16x32_bf16 v[144:147], a[84:87], a[20:23], v[144:147]\n" \
"s_mov_b64 exec, 0xffff\nglobal_load_dword v22, v1, s[76:77]\ns_mov_b64 exec, -1\n" \
"v_mfma_f32_16x16x32_bf16 v[152:155], a[80:83], a[24:27], v[152:155]\n" \
"v_accvgpr_read_b32 v39, a199\n" \
"v_lshlrev_b32 v33, 16, v39\n" \
"v_mfma_f32_16x16x32_bf16 v[152:155], a[84:87], a[28:31], v[152:155]\n" \
"v_and_b32 v39, 0xffff0000, v39\n" \
"v_mul_f32 v25, v30, v45\n" \
"v_mfma_f32_16x16x32_bf16 v[160:163], a[80:83], a[32:35], v[160:163]\n" \
"v_mul_f32 v26, v36, v49\n" \
"v_mfma_f32_16x16x32_bf16 v[160:163], a[84:87], a[36:39], v[160:163]\n" \
"v_fmac_f32 v25, v31, v44\n" \
"v_fmac_f32 v26, v37, v48\n" \
"v_mfma_f32_16x16x32_bf16 v[168:171], a[80:83], a[40:43], v[168:171]\n" \
"v_fmac_f32 v25, v32, v43\n" \
"v_fmac_f32 v26, v38, v47\n" \
"v_mfma_f32_16x16x32_bf16 v[168:171], a[84:87], a[44:47], v[168:171]\n" \
"s_waitcnt vmcnt(22) lgkmcnt(0)\n" \
"v_fmac_f32 v25, v33, v42\n" \
"v_mfma_f32_16x16x32_bf16 v[176:179], a[80:83], a[48:51], v[176:179]\n" \
"s_barrier\n" \
"v_fmac_f32 v26, v39, v46\n" \
"v_accvgpr_read_b32 v28, a215\n" \
"v_mfma_f32_16x16x32_bf16 v[176:179], a[84:87], a[52:55], v[176:179]\n" \
"v_lshlrev_b32 v27, 16, v28\n" \
"v_and_b32 v28, 0xffff0000, v28\n" \
"v_mfma_f32_16x16x32_bf16 v[184:187], a[80:83], a[56:59], v[184:187]\n" \
"v_mul_f32 v29, v25, v27\n" \
"v_mfma_f32_16x16x32_bf16 v[184:187], a[84:87], a[60:63], v[184:187]\n" \
"v_mul_f32 v58, v26, v28\n" \
"v_accvgpr_read_b32 v40, a201\n" \
"v_mfma_f32_16x16x32_bf16 v[132:135], a[88:91], a[0:3], v[132:135]\n" \
"v_lshlrev_b32 v34, 16, v40\n" \
"v_and_b32 v40, 0xffff0000, v40\n" \
"v_mfma_f32_16x16x32_bf16 v[132:135], a[92:95], a[4:7], v[132:135]\n" \
"v_mul_f32 v25, v31, v45\n" \
"v_mfma_f32_16x16x32_bf16 v[140:143], a[88:91], a[8:11], v[140:143]\n" \
"v_mul_f32 v26, v37, v49\n" \
"v_fmac_f32 v25, v32, v44\n" \
"v_mfma_f32_16x16x32_bf16 v[140:143], a[92:95], a[12:15], v[140:143]\n" \
"v_fmac_f32 v26, v38, v48\n" \
"v_fmac_f32 v25, v33, v43\n" \
"v_mfma_f32_16x16x32_bf16 v[148:151], a[88:91], a[16:19], v[148:151]\n" \
"v_fmac_f32 v26, v39, v47\n" \
"v_mfma_f32_16x16x32_bf16 v[148:151], a[92:95], a[20:23], v[148:151]\n" \
"v_fmac_f32 v25, v34, v42\n" \
"v_fmac_f32 v26, v40, v46\n" \
"v_mfma_f32_16x16x32_bf16 v[156:159], a[88:91], a[24:27], v[156:159]\n" \
"v_accvgpr_read_b32 v28, a217\n" \
"v_lshlrev_b32 v27, 16, v28\n" \
"v_mfma_f32_16x16x32_bf16 v[156:159], a[92:95], a[28:31], v[156:159]\n" \
"v_and_b32 v28, 0xffff0000, v28\n" \
"v_mfma_f32_16x16x32_bf16 v[164:167], a[88:91], a[32:35], v[164:167]\n" \
"v_mul_f32 v25, v25, v27\n" \
"v_mul_f32 v26, v26, v28\n" \
"v_mfma_f32_16x16x32_bf16 v[164:167], a[92:95], a[36:39], v[164:167]\n" \
"v_cvt_pk_bf16_f32 v53, v25, v29\n" \
"v_cvt_pk_bf16_f32 v57, v26, v58\n" \
"v_mfma_f32_16x16x32_bf16 v[172:175], a[88:91], a[40:43], v[172:175]\n" \
"v_accvgpr_read_b32 v41, a203\n" \
"v_mfma_f32_16x16x32_bf16 v[172:175], a[92:95], a[44:47], v[172:175]\n" \
"v_lshlrev_b32 v35, 16, v41\n" \
"v_and_b32 v41, 0xffff0000, v41\n" \
"v_mfma_f32_16x16x32_bf16 v[180:183], a[88:91], a[48:51], v[180:183]\n" \
"v_mul_f32 v25, v32, v45\n" \
"v_mul_f32 v26, v38, v49\n" \
"v_mfma_f32_16x16x32_bf16 v[180:183], a[92:95], a[52:55], v[180:183]\n" \
"v_fmac_f32 v25, v33, v44\n" \
"v_mfma_f32_16x16x32_bf16 v[188:191], a[88:91], a[56:59], v[188:191]\n" \
"v_fmac_f32 v26, v39, v48\n" \
"v_fmac_f32 v25, v34, v43\n" \
"v_mfma_f32_16x16x32_bf16 v[188:191], a[92:95], a[60:63], v[188:191]\n" \
"v_fmac_f32 v26, v40, v47\n" \
"v_fmac_f32 v25, v35, v42\n" \
"v_mfma_f32_16x16x32_bf16 v[192:195], a[96:99], a[0:3], v[192:195]\n" \
"v_fmac_f32 v26, v41, v46\n" \
"v_mfma_f32_16x16x32_bf16 v[192:195], a[100:103], a[4:7], v[192:195]\n" \
"v_accvgpr_read_b32 v28, a219\n" \
"v_lshlrev_b32 v27, 16, v28\n" \
"v_mfma_f32_16x16x32_bf16 v[200:203], a[96:99], a[8:11], v[200:203]\n" \
"v_and_b32 v28, 0xffff0000, v28\n" \
"v_mul_f32 v29, v25, v27\n" \
"v_mfma_f32_16x16x32_bf16 v[200:203], a[100:103], a[12:15], v[200:203]\n" \
"v_mul_f32 v58, v26, v28\n" \
"v_mfma_f32_16x16x32_bf16 v[208:211], a[96:99], a[16:19], v[208:211]\n" \
"ds_read_b64_tr_b16 a[64:65], v9 offset:0\n" \
"v_accvgpr_read_b32 v36, a205\n" \
"v_mfma_f32_16x16x32_bf16 v[208:211], a[100:103], a[20:23], v[208:211]\n" \
"v_lshlrev_b32 v30, 16, v36\n" \
"v_and_b32 v36, 0xffff0000, v36\n" \
"v_mfma_f32_16x16x32_bf16 v[216:219], a[96:99], a[24:27], v[216:219]\n" \
"ds_read_b64_tr_b16 a[66:67], v10 offset:0\n" \
"v_mul_f32 v25, v33, v45\n" \
"v_mfma_f32_16x16x32_bf16 v[216:219], a[100:103], a[28:31], v[216:219]\n" \
"v_mul_f32 v26, v39, v49\n" \
"v_fmac_f32 v25, v34, v44\n" \
"v_mfma_f32_16x16x32_bf16 v[224:227], a[96:99], a[32:35], v[224:227]\n" \
"ds_read_b64_tr_b16 a[68:69], v9 offset:1024\n" \
"v_fmac_f32 v26, v40, v48\n" \
"v_mfma_f32_16x16x32_bf16 v[224:227], a[100:103], a[36:39], v[224:227]\n" \
"v_fmac_f32 v25, v35, v43\n" \
"v_fmac_f32 v26, v41, v47\n" \
"v_mfma_f32_16x16x32_bf16 v[232:235], a[96:99], a[40:43], v[232:235]\n" \
"ds_read_b64_tr_b16 a[70:71], v10 offset:1024\n" \
"v_fmac_f32 v25, v30, v42\n" \
"v_mfma_f32_16x16x32_bf16 v[232:235], a[100:103], a[44:47], v[232:235]\n" \
"v_fmac_f32 v26, v36, v46\n" \
"v_accvgpr_read_b32 v28, a221\n" \
"v_mfma_f32_16x16x32_bf16 v[240:243], a[96:99], a[48:51], v[240:243]\n" \
"ds_read_b64_tr_b16 a[72:73], v11 offset:0\n" \
"v_lshlrev_b32 v27, 16, v28\n" \
"v_mfma_f32_16x16x32_bf16 v[240:243], a[100:103], a[52:55], v[240:243]\n" \
"v_and_b32 v28, 0xffff0000, v28\n" \
"v_mul_f32 v25, v25, v27\n" \
"v_mfma_f32_16x16x32_bf16 v[248:251], a[96:99], a[56:59], v[248:251]\n" \
"ds_read_b64_tr_b16 a[74:75], v12 offset:0\n" \
"v_mul_f32 v26, v26, v28\n" \
"v_mfma_f32_16x16x32_bf16 v[248:251], a[100:103], a[60:63], v[248:251]\n" \
"v_cvt_pk_bf16_f32 v52, v25, v29\n" \
"v_cvt_pk_bf16_f32 v56, v26, v58\n" \
"v_mfma_f32_16x16x32_bf16 v[196:199], a[104:107], a[0:3], v[196:199]\n" \
"ds_read_b64_tr_b16 a[76:77], v11 offset:1024\n" \
"v_accvgpr_read_b32 v37, a207\n" \
"v_mfma_f32_16x16x32_bf16 v[196:199], a[108:111], a[4:7], v[196:199]\n" \
"v_lshlrev_b32 v31, 16, v37\n" \
"v_and_b32 v37, 0xffff0000, v37\n" \
"v_mfma_f32_16x16x32_bf16 v[204:207], a[104:107], a[8:11], v[204:207]\n" \
"ds_read_b64_tr_b16 a[78:79], v12 offset:1024\n" \
"v_mul_f32 v25, v34, v45\n" \
"v_mfma_f32_16x16x32_bf16 v[204:207], a[108:111], a[12:15], v[204:207]\n" \
"v_mul_f32 v26, v40, v49\n" \
"v_fmac_f32 v25, v35, v44\n" \
"v_mfma_f32_16x16x32_bf16 v[212:215], a[104:107], a[16:19], v[212:215]\n" \
"ds_read_b64_tr_b16 a[80:81], v9 offset:512\n" \
"v_fmac_f32 v26, v41, v48\n" \
"v_mfma_f32_16x16x32_bf16 v[212:215], a[108:111], a[20:23], v[212:215]\n" \
"v_fmac_f32 v25, v30, v43\n" \
"v_fmac_f32 v26, v36, v47\n" \
"v_mfma_f32_16x16x32_bf16 v[220:223], a[104:107], a[24:27], v[220:223]\n" \
"ds_read_b64_tr_b16 a[82:83], v10 offset:512\n" \
"v_fmac_f32 v25, v31, v42\n" \
"v_mfma_f32_16x16x32_bf16 v[220:223], a[108:111], a[28:31], v[220:223]\n" \
"LH1_%=:\ns_cbranch_execz LH9_%=\n" \
"v_fmac_f32 v26, v37, v46\n" \
"v_accvgpr_read_b32 v28, a223\n" \
"v_mfma_f32_16x16x32_bf16 v[228:231], a[104:107], a[32:35], v[228:231]\n" \
"ds_read_b64_tr_b16 a[84:85], v9 offset:1536\n" \
"v_lshlrev_b32 v27, 16, v28\n" \
"v_mfma_f32_16x16x32_bf16 v[228:231], a[108:111], a[36:39], v[228:231]\n" \
"v_and_b32 v28, 0xffff0000, v28\n" \
"v_mul_f32 v29, v25, v27\n" \
"v_mfma_f32_16x16x32_bf16 v[236:239], a[104:107], a[40:43], v[236:239]\n" \
"ds_read_b64_tr_b16 a[86:87], v10 offset:1536\n" \
"v_mul_f32 v58, v26, v28\n" \
"v_mfma_f32_16x16x32_bf16 v[236:239], a[108:111], a[44:47], v[236:239]\n" \
"v_accvgpr_read_b32 v38, a209\n" \
"v_lshlrev_b32 v32, 16, v38\n" \
"v_mfma_f32_16x16x32_bf16 v[244:247], a[104:107], a[48:51], v[244:247]\n" \
"ds_read_b64_tr_b16 a[88:89], v11 offset:512\n" \
"v_and_b32 v38, 0xffff0000, v38\n" \
"v_mfma_f32_16x16x32_bf16 v[244:247], a[108:111], a[52:55], v[244:247]\n" \
"v_mul_f32 v25, v35, v45\n" \
"v_mul_f32 v26, v41, v49\n" \
"v_mfma_f32_16x16x32_bf16 v[252:255], a[104:107], a[56:59], v[252:255]\n" \
"ds_read_b64_tr_b16 a[90:91], v12 offset:512\n" \
"v_fmac_f32 v25, v30, v44\n" \
"v_mfma_f32_16x16x32_bf16 v[252:255], a[108:111], a[60:63], v[252:255]\n" \
"v_fmac_f32 v26, v36, v48\n" \
"v_fmac_f32 v25, v31, v43\n" \
"v_mfma_f32_16x16x32_bf16 a[128:131], a[112:115], a[0:3], a[128:131]\n" \
"ds_read_b64_tr_b16 a[92:93], v11 offset:1536\n" \
"v_fmac_f32 v26, v37, v47\n" \
"v_mfma_f32_16x16x32_bf16 a[128:131], a[116:119], a[4:7], a[128:131]\n" \
"v_fmac_f32 v25, v32, v42\n" \
"v_fmac_f32 v26, v38, v46\n" \
"v_mfma_f32_16x16x32_bf16 a[136:139], a[112:115], a[8:11], a[136:139]\n" \
"ds_read_b64_tr_b16 a[94:95], v12 offset:1536\n" \
"v_accvgpr_read_b32 v28, a225\n" \
"v_mfma_f32_16x16x32_bf16 a[136:139], a[116:119], a[12:15], a[136:139]\n" \
"v_lshlrev_b32 v27, 16, v28\n" \
"v_and_b32 v28, 0xffff0000, v28\n" \
"v_mfma_f32_16x16x32_bf16 a[144:147], a[112:115], a[16:19], a[144:147]\n" \
"v_mul_f32 v25, v25, v27\n" \
"v_mul_f32 v26, v26, v28\n" \
"v_mfma_f32_16x16x32_bf16 a[144:147], a[116:119], a[20:23], a[144:147]\n" \
"v_cvt_pk_bf16_f32 v51, v25, v29\n" \
"v_mfma_f32_16x16x32_bf16 a[152:155], a[112:115], a[24:27], a[152:155]\n" \
"v_cvt_pk_bf16_f32 v55, v26, v58\n" \
"v_accvgpr_read_b32 v39, a211\n" \
"v_mfma_f32_16x16x32_bf16 a[152:155], a[116:119], a[28:31], a[152:155]\n" \
"v_lshlrev_b32 v33, 16, v39\n" \
"v_and_b32 v39, 0xffff0000, v39\n" \
"v_mfma_f32_16x16x32_bf16 a[160:163], a[112:115], a[32:35], a[160:163]\n" \
"v_mul_f32 v25, v30, v45\n" \
"v_mfma_f32_16x16x32_bf16 a[160:163], a[116:119], a[36:39], a[160:163]\n" \
"v_mul_f32 v26, v36, v49\n" \
"v_fmac_f32 v25, v31, v44\n" \
"v_mfma_f32_16x16x32_bf16 a[168:171], a[112:115], a[40:43], a[168:171]\n" \
"v_fmac_f32 v26, v37, v48\n" \
"v_fmac_f32 v25, v32, v43\n" \
"v_mfma_f32_16x16x32_bf16 a[168:171], a[116:119], a[44:47], a[168:171]\n" \
"s_waitcnt vmcnt(13) lgkmcnt(0)\n" \
"v_fmac_f32 v26, v38, v47\n" \
"v_mfma_f32_16x16x32_bf16 a[176:179], a[112:115], a[48:51], a[176:179]\n" \
"s_barrier\n" \
"v_fmac_f32 v25, v33, v42\n" \
"v_fmac_f32 v26, v39, v46\n" \
"v_mfma_f32_16x16x32_bf16 a[176:179], a[116:119], a[52:55], a[176:179]\n" \
"v_accvgpr_read_b32 v28, a227\n" \
"v_lshlrev_b32 v27, 16, v28\n" \
"v_mfma_f32_16x16x32_bf16 a[184:187], a[112:115], a[56:59], a[184:187]\n" \
"v_and_b32 v28, 0xffff0000, v28\n" \
"v_mfma_f32_16x16x32_bf16 a[184:187], a[116:119], a[60:63], a[184:187]\n" \
"v_mul_f32 v29, v25, v27\n" \
"v_mul_f32 v58, v26, v28\n" \
"v_mfma_f32_16x16x32_bf16 a[132:135], a[120:123], a[0:3], a[132:135]\n" \
"v_accvgpr_read_b32 v40, a213\n" \
"v_lshlrev_b32 v34, 16, v40\n" \
"v_mfma_f32_16x16x32_bf16 a[132:135], a[124:127], a[4:7], a[132:135]\n" \
"v_and_b32 v40, 0xffff0000, v40\n" \
"v_mfma_f32_16x16x32_bf16 a[140:143], a[120:123], a[8:11], a[140:143]\n" \
"ds_read_b128 a[0:3], v5 offset:0\n" \
"v_mul_f32 v25, v31, v45\n" \
"v_mfma_f32_16x16x32_bf16 a[140:143], a[124:127], a[12:15], a[140:143]\n" \
"ds_read_b128 a[4:7], v6 offset:0\n" \
"v_mul_f32 v26, v37, v49\n" \
"v_mfma_f32_16x16x32_bf16 a[148:151], a[120:123], a[16:19], a[148:151]\n" \
"ds_read_b128 a[8:11], v5 offset:2048\n" \
"v_fmac_f32 v25, v32, v44\n" \
"v_mfma_f32_16x16x32_bf16 a[148:151], a[124:127], a[20:23], a[148:151]\n" \
"ds_read_b128 a[12:15], v6 offset:2048\n" \
"v_fmac_f32 v26, v38, v48\n" \
"v_mfma_f32_16x16x32_bf16 a[156:159], a[120:123], a[24:27], a[156:159]\n" \
"ds_read_b128 a[16:19], v5 offset:4096\n" \
"v_fmac_f32 v25, v33, v43\n" \
"v_mfma_f32_16x16x32_bf16 a[156:159], a[124:127], a[28:31], a[156:159]\n" \
"ds_read_b128 a[20:23], v6 offset:4096\n" \
"v_fmac_f32 v26, v39, v47\n" \
"v_mfma_f32_16x16x32_bf16 a[164:167], a[120:123], a[32:35], a[164:167]\n" \
"ds_read_b128 a[24:27], v5 offset:6144\n" \
"v_fmac_f32 v25, v34, v42\n" \
"v_mfma_f32_16x16x32_bf16 a[164:167], a[124:127], a[36:39], a[164:167]\n" \
"ds_read_b128 a[28:31], v6 offset:6144\n" \
"v_fmac_f32 v26, v40, v46\n" \
"v_mfma_f32_16x16x32_bf16 a[172:175], a[120:123], a[40:43], a[172:175]\n" \
"ds_read_b128 a[32:35], v5 offset:8192\n" \
"v_accvgpr_read_b32 v28, a229\n" \
"v_mfma_f32_16x16x32_bf16 a[172:175], a[124:127], a[44:47], a[172:175]\n" \
"ds_read_b128 a[36:39], v6 offset:8192\n" \
"v_lshlrev_b32 v27, 16, v28\n" \
"v_mfma_f32_16x16x32_bf16 a[180:183], a[120:123], a[48:51], a[180:183]\n" \
"ds_read_b128 a[40:43], v5 offset:10240\n" \
"v_and_b32 v28, 0xffff0000, v28\n" \
"v_mfma_f32_16x16x32_bf16 a[180:183], a[124:127], a[52:55], a[180:183]\n" \
"ds_read_b128 a[44:47], v6 offset:10240\n" \
"v_mul_f32 v25, v25, v27\n" \
"v_mfma_f32_16x16x32_bf16 a[188:191], a[120:123], a[56:59], a[188:191]\n" \
"ds_read_b128 a[48:51], v5 offset:12288\n" \
"v_mul_f32 v26, v26, v28\n" \
"v_mfma_f32_16x16x32_bf16 a[188:191], a[124:127], a[60:63], a[188:191]\n" \
"s_waitcnt lgkmcnt(9)\n" \
"ds_read_b128 a[52:55], v6 offset:12288\n" \
"v_cvt_pk_bf16_f32 v50, v25, v29\n" \
"v_mfma_f32_16x16x32_bf16 v[64:67], a[64:67], a[0:3], v[64:67]\n" \
"s_mov_b32 s47, 2621440\n" \
"s_add_u32 m0, s22, 49024\n" \
"ds_read_b128 a[56:59], v5 offset:14336\n" \
"v_mfma_f32_16x16x32_bf16 v[64:67], a[68:71], a[4:7], v[64:67]\n" \
"buffer_load_dwordx4 v21, s[32:35], s47 offen offset:128 lds\n" \
"v_mfma_f32_16x16x32_bf16 v[72:75], a[64:67], a[8:11], v[72:75]\n" \
"s_waitcnt lgkmcnt(7)\n" \
"ds_read_b128 a[60:63], v6 offset:14336\n" \
"v_cvt_pk_bf16_f32 v54, v26, v58\n" \
"v_mfma_f32_16x16x32_bf16 v[72:75], a[68:71], a[12:15], v[72:75]\n" \
"s_mov_b32 s94, 79699968\n" \
"v_mfma_f32_16x16x32_bf16 v[80:83], a[64:67], a[16:19], v[80:83]\n" \
"s_add_u32 m0, s22, 50816\n" \
"ds_read_b64_tr_b16 a[96:97], v9 offset:16384\n" \
"v_mfma_f32_16x16x32_bf16 v[80:83], a[68:71], a[20:23], v[80:83]\n" \
"buffer_load_dwordx4 v21, s[32:35], s47 offen offset:384 lds\n" \
"v_mfma_f32_16x16x32_bf16 v[88:91], a[64:67], a[24:27], v[88:91]\n" \
"s_waitcnt lgkmcnt(5)\n" \
"ds_read_b64_tr_b16 a[98:99], v10 offset:16384\n" \
"v_mfma_f32_16x16x32_bf16 v[88:91], a[68:71], a[28:31], v[88:91]\n" \
"buffer_store_dwordx4 v[50:53], v60, s[36:39], s94 offen offset:0 sc1\n" \
"v_mfma_f32_16x16x32_bf16 v[96:99], a[64:67], a[32:35], v[96:99]\n" \
"s_mov_b32 s47, 2883584\n" \
"s_add_u32 m0, s22, 50048\n" \
"ds_read_b64_tr_b16 a[100:101], v9 offset:17408\n" \
"v_mfma_f32_16x16x32_bf16 v[96:99], a[68:71], a[36:39], v[96:99]\n" \
"buffer_load_dwordx4 v21, s[32:35], s47 offen offset:128 lds\n" \
"v_mfma_f32_16x16x32_bf16 v[104:107], a[64:67], a[40:43], v[104:107]\n" \
"s_waitcnt lgkmcnt(3)\n" \
"ds_read_b64_tr_b16 a[102:103], v10 offset:17408\n" \
"s_mov_b32 s95, 79704064\n" \
"v_mfma_f32_16x16x32_bf16 v[104:107], a[68:71], a[44:47], v[104:107]\n" \
"v_mfma_f32_16x16x32_bf16 v[112:115], a[64:67], a[48:51], v[112:115]\n" \
"s_add_u32 m0, s22, 51840\n" \
"ds_read_b64_tr_b16 a[104:105], v11 offset:16384\n" \
"v_mfma_f32_16x16x32_bf16 v[112:115], a[68:71], a[52:55], v[112:115]\n" \
"buffer_load_dwordx4 v21, s[32:35], s47 offen offset:384 lds\n" \
"v_mfma_f32_16x16x32_bf16 v[120:123], a[64:67], a[56:59], v[120:123]\n" \
"ds_read_b64_tr_b16 a[106:107], v12 offset:16384\n" \
"v_mfma_f32_16x16x32_bf16 v[120:123], a[68:71], a[60:63], v[120:123]\n" \
"v_mfma_f32_16x16x32_bf16 v[68:71], a[72:75], a[0:3], v[68:71]\n" \
"s_mov_b32 s46, 768\n" \
"s_add_u32 m0, s50, 65536\n" \
"ds_read_b64_tr_b16 a[108:109], v11 offset:17408\n" \
"v_mfma_f32_16x16x32_bf16 v[68:71], a[76:79], a[4:7], v[68:71]\n" \
"buffer_load_dwordx4 v13, s[28:31], s46 offen lds\n" \
"v_mfma_f32_16x16x32_bf16 v[76:79], a[72:75], a[8:11], v[76:79]\n" \
"ds_read_b64_tr_b16 a[110:111], v12 offset:17408\n" \
"v_mfma_f32_16x16x32_bf16 v[76:79], a[76:79], a[12:15], v[76:79]\n" \
"v_mfma_f32_16x16x32_bf16 v[84:87], a[72:75], a[16:19], v[84:87]\n" \
"s_add_u32 m0, s50, 69632\n" \
"ds_read_b64_tr_b16 a[112:113], v9 offset:16896\n" \
"v_mfma_f32_16x16x32_bf16 v[84:87], a[76:79], a[20:23], v[84:87]\n" \
"buffer_load_dwordx4 v14, s[28:31], s46 offen lds\n" \
"v_mfma_f32_16x16x32_bf16 v[92:95], a[72:75], a[24:27], v[92:95]\n" \
"ds_read_b64_tr_b16 a[114:115], v10 offset:16896\n" \
"v_mfma_f32_16x16x32_bf16 v[92:95], a[76:79], a[28:31], v[92:95]\n" \
"buffer_store_dwordx4 v[54:57], v60, s[36:39], s95 offen offset:0 sc1\n" \
"v_mfma_f32_16x16x32_bf16 v[100:103], a[72:75], a[32:35], v[100:103]\n" \
"s_add_u32 m0, s50, 73728\n" \
"ds_read_b64_tr_b16 a[116:117], v9 offset:17920\n" \
"s_lshl_b32 s62, s8, 2\n" \
"v_mfma_f32_16x16x32_bf16 v[100:103], a[76:79], a[36:39], v[100:103]\n" \
"buffer_load_dwordx4 v15, s[28:31], s46 offen lds\n" \
"v_mfma_f32_16x16x32_bf16 v[108:111], a[72:75], a[40:43], v[108:111]\n" \
"s_add_u32 s62, s62, %2\n" \
"ds_read_b64_tr_b16 a[118:119], v10 offset:17920\n" \
"s_lshl_b32 s63, s62, 12\n" \
"v_mfma_f32_16x16x32_bf16 v[108:111], a[76:79], a[44:47], v[108:111]\n" \
"s_add_u32 s68, s10, s63\ns_addc_u32 s69, s11, 0\n" \
"s_and_b32 s69, s69, 0xffff\n" \
"v_mfma_f32_16x16x32_bf16 v[116:119], a[72:75], a[48:51], v[116:119]\n" \
"s_add_u32 m0, s50, 77824\n" \
"ds_read_b64_tr_b16 a[120:121], v11 offset:16896\n" \
"s_mov_b32 s70, 4096\n" \
"v_mfma_f32_16x16x32_bf16 v[116:119], a[76:79], a[52:55], v[116:119]\n" \
"buffer_load_dwordx4 v16, s[28:31], s46 offen lds\n" \
"v_mfma_f32_16x16x32_bf16 v[124:127], a[72:75], a[56:59], v[124:127]\n" \
"s_mov_b32 s71, 0x00020000\n" \
"ds_read_b64_tr_b16 a[122:123], v12 offset:16896\n" \
"s_add_u32 s63, s63, 79691776\n" \
"v_mfma_f32_16x16x32_bf16 v[124:127], a[76:79], a[60:63], v[124:127]\n" \
"s_add_u32 s72, s4, s63\ns_addc_u32 s73, s5, 0\n" \
"s_and_b32 s73, s73, 0xffff\n" \
"v_mfma_f32_16x16x32_bf16 v[128:131], a[80:83], a[0:3], v[128:131]\n" \
"s_mov_b32 s46, 768\n" \
"s_add_u32 m0, s50, 81920\n" \
"ds_read_b64_tr_b16 a[124:125], v11 offset:17920\n" \
"v_mfma_f32_16x16x32_bf16 v[128:131], a[84:87], a[4:7], v[128:131]\n" \
"buffer_load_dwordx4 v17, s[28:31], s46 offen lds\n" \
"v_mfma_f32_16x16x32_bf16 v[136:139], a[80:83], a[8:11], v[136:139]\n" \
"s_mov_b32 s74, 4096\n" \
"ds_read_b64_tr_b16 a[126:127], v12 offset:17920\n" \
"s_mov_b32 s75, 0x00020000\n" \
"v_mfma_f32_16x16x32_bf16 v[136:139], a[84:87], a[12:15], v[136:139]\n" \
"v_add_u32 v23, -32, v4\n" \
"v_add_u32 v24, 992, v4\n" \
"v_mfma_f32_16x16x32_bf16 v[144:147], a[80:83], a[16:19], v[144:147]\n" \
"s_add_u32 m0, s50, 86016\n" \
"v_add_u32 v25, -16, v4\n" \
"v_add_u32 v26, -512, v4\n" \
"v_mfma_f32_16x16x32_bf16 v[144:147], a[84:87], a[20:23], v[144:147]\n" \
"buffer_load_dwordx4 v18, s[28:31], s46 offen lds\n" \
"v_mfma_f32_16x16x32_bf16 v[152:155], a[80:83], a[24:27], v[152:155]\n" \
"s_lshr_b32 s63, %2, 1\n" \
"v_add_u32 v27, 512, v4\n" \
"s_lshl_b32 s63, s63, 16\n" \
"v_mfma_f32_16x16x32_bf16 v[152:155], a[84:87], a[28:31], v[152:155]\n" \
"s_lshl_b32 s56, %2, 14\n" \
"s_add_u32 s56, s56, s63\n" \
"v_add_u32 v55, s56, v4\n" \
"v_mfma_f32_16x16x32_bf16 v[160:163], a[80:83], a[32:35], v[160:163]\n" \
"s_add_u32 m0, s50, 90112\n" \
"v_mov_b32 v28, 0\n" \
"s_lshl_b32 s62, s62, 1\n" \
"v_mfma_f32_16x16x32_bf16 v[160:163], a[84:87], a[36:39], v[160:163]\n" \
"buffer_load_dwordx4 v19, s[28:31], s46 offen lds\n" \
"v_mfma_f32_16x16x32_bf16 v[168:171], a[80:83], a[40:43], v[168:171]\n" \
"v_mov_b32 v29, s62\n" \
"v_lshlrev_b32 v56, 23, v4\n" \
"v_mfma_f32_16x16x32_bf16 v[168:171], a[84:87], a[44:47], v[168:171]\n" \
"s_waitcnt vmcnt(17) lgkmcnt(0)\n" \
"s_and_b32 s62, %1, 7\n" \
"s_lshl_b32 s62, s62, 8\n" \
"v_mfma_f32_16x16x32_bf16 v[176:179], a[80:83], a[48:51], v[176:179]\n" \
"s_add_u32 m0, s50, 94208\n" \
"s_barrier\n" \
"s_add_u32 s62, s62, 94380032\n" \
"v_mfma_f32_16x16x32_bf16 v[176:179], a[84:87], a[52:55], v[176:179]\n" \
"buffer_load_dwordx4 v20, s[28:31], s46 offen lds\n" \
"v_mfma_f32_16x16x32_bf16 v[184:187], a[80:83], a[56:59], v[184:187]\n" \
"v_bfe_u32 v57, v4, 4, 5\n" \
"v_lshl_add_u32 v57, v57, 3, s62\n" \
"v_mfma_f32_16x16x32_bf16 v[184:187], a[84:87], a[60:63], v[184:187]\n" \
"v_mfma_f32_16x16x32_bf16 v[132:135], a[88:91], a[0:3], v[132:135]\n" \
"s_mov_b32 s47, 3145728\n" \
"s_add_u32 m0, s22, 0\n" \
"v_mfma_f32_16x16x32_bf16 v[132:135], a[92:95], a[4:7], v[132:135]\n" \
"buffer_load_dwordx4 v21, s[32:35], s47 offen offset:0 lds\n" \
"v_mfma_f32_16x16x32_bf16 v[140:143], a[88:91], a[8:11], v[140:143]\n" \
"v_mfma_f32_16x16x32_bf16 v[140:143], a[92:95], a[12:15], v[140:143]\n" \
"v_mfma_f32_16x16x32_bf16 v[148:151], a[88:91], a[16:19], v[148:151]\n" \
"s_add_u32 m0, s22, 1792\n" \
"v_mfma_f32_16x16x32_bf16 v[148:151], a[92:95], a[20:23], v[148:151]\n" \
"buffer_load_dwordx4 v21, s[32:35], s47 offen offset:256 lds\n" \
"v_mfma_f32_16x16x32_bf16 v[156:159], a[88:91], a[24:27], v[156:159]\n" \
"v_mfma_f32_16x16x32_bf16 v[156:159], a[92:95], a[28:31], v[156:159]\n" \
"v_mfma_f32_16x16x32_bf16 v[164:167], a[88:91], a[32:35], v[164:167]\n" \
"s_mov_b32 s47, 3407872\n" \
"s_add_u32 m0, s22, 1024\n" \
"v_mfma_f32_16x16x32_bf16 v[164:167], a[92:95], a[36:39], v[164:167]\n" \
"buffer_load_dwordx4 v21, s[32:35], s47 offen offset:0 lds\n" \
"v_mfma_f32_16x16x32_bf16 v[172:175], a[88:91], a[40:43], v[172:175]\n" \
"s_add_u32 s90, s64, s53\ns_addc_u32 s91, s65, 0\n" \
"v_mfma_f32_16x16x32_bf16 v[172:175], a[92:95], a[44:47], v[172:175]\n" \
"v_mfma_f32_16x16x32_bf16 v[180:183], a[88:91], a[48:51], v[180:183]\n" \
"s_add_u32 m0, s22, 2816\n" \
"v_mfma_f32_16x16x32_bf16 v[180:183], a[92:95], a[52:55], v[180:183]\n" \
"buffer_load_dwordx4 v21, s[32:35], s47 offen offset:256 lds\n" \
"v_mfma_f32_16x16x32_bf16 v[188:191], a[88:91], a[56:59], v[188:191]\n" \
"v_mfma_f32_16x16x32_bf16 v[188:191], a[92:95], a[60:63], v[188:191]\n" \
"v_mfma_f32_16x16x32_bf16 v[192:195], a[96:99], a[0:3], v[192:195]\n" \
"v_mfma_f32_16x16x32_bf16 v[192:195], a[100:103], a[4:7], v[192:195]\n" \
"global_load_dwordx4 a[240:243], v4, s[90:91] offset:0\n" \
"v_mfma_f32_16x16x32_bf16 v[200:203], a[96:99], a[8:11], v[200:203]\n" \
"v_mfma_f32_16x16x32_bf16 v[200:203], a[100:103], a[12:15], v[200:203]\n" \
"v_mfma_f32_16x16x32_bf16 v[208:211], a[96:99], a[16:19], v[208:211]\n" \
"ds_read_b64_tr_b16 a[64:65], v9 offset:32768\n" \
"v_mfma_f32_16x16x32_bf16 v[208:211], a[100:103], a[20:23], v[208:211]\n" \
"global_load_dwordx4 a[244:247], v4, s[90:91] offset:1024\n" \
"v_mfma_f32_16x16x32_bf16 v[216:219], a[96:99], a[24:27], v[216:219]\n" \
"ds_read_b64_tr_b16 a[66:67], v10 offset:32768\n" \
"v_mfma_f32_16x16x32_bf16 v[216:219], a[100:103], a[28:31], v[216:219]\n" \
"v_mfma_f32_16x16x32_bf16 v[224:227], a[96:99], a[32:35], v[224:227]\n" \
"ds_read_b64_tr_b16 a[68:69], v9 offset:33792\n" \
"v_mfma_f32_16x16x32_bf16 v[224:227], a[100:103], a[36:39], v[224:227]\n" \
"global_load_dwordx4 a[248:251], v4, s[90:91] offset:2048\n" \
"v_mfma_f32_16x16x32_bf16 v[232:235], a[96:99], a[40:43], v[232:235]\n" \
"ds_read_b64_tr_b16 a[70:71], v10 offset:33792\n" \
"v_mfma_f32_16x16x32_bf16 v[232:235], a[100:103], a[44:47], v[232:235]\n" \
"v_mfma_f32_16x16x32_bf16 v[240:243], a[96:99], a[48:51], v[240:243]\n" \
"ds_read_b64_tr_b16 a[72:73], v11 offset:32768\n" \
"v_mfma_f32_16x16x32_bf16 v[240:243], a[100:103], a[52:55], v[240:243]\n" \
"global_load_dwordx4 a[252:255], v4, s[90:91] offset:3072\n" \
"v_mfma_f32_16x16x32_bf16 v[248:251], a[96:99], a[56:59], v[248:251]\n" \
"ds_read_b64_tr_b16 a[74:75], v12 offset:32768\n" \
"v_mfma_f32_16x16x32_bf16 v[248:251], a[100:103], a[60:63], v[248:251]\n" \
"v_mfma_f32_16x16x32_bf16 v[196:199], a[104:107], a[0:3], v[196:199]\n" \
"ds_read_b64_tr_b16 a[76:77], v11 offset:33792\n" \
"v_mfma_f32_16x16x32_bf16 v[196:199], a[108:111], a[4:7], v[196:199]\n" \
"v_mfma_f32_16x16x32_bf16 v[204:207], a[104:107], a[8:11], v[204:207]\n" \
"ds_read_b64_tr_b16 a[78:79], v12 offset:33792\n" \
"v_mfma_f32_16x16x32_bf16 v[204:207], a[108:111], a[12:15], v[204:207]\n" \
"v_mfma_f32_16x16x32_bf16 v[212:215], a[104:107], a[16:19], v[212:215]\n" \
"ds_read_b64_tr_b16 a[80:81], v9 offset:33280\n" \
"v_mfma_f32_16x16x32_bf16 v[212:215], a[108:111], a[20:23], v[212:215]\n" \
"v_mfma_f32_16x16x32_bf16 v[220:223], a[104:107], a[24:27], v[220:223]\n" \
"ds_read_b64_tr_b16 a[82:83], v10 offset:33280\n" \
"v_mfma_f32_16x16x32_bf16 v[220:223], a[108:111], a[28:31], v[220:223]\n" \
"v_mfma_f32_16x16x32_bf16 v[228:231], a[104:107], a[32:35], v[228:231]\n" \
"ds_read_b64_tr_b16 a[84:85], v9 offset:34304\n" \
"v_mfma_f32_16x16x32_bf16 v[228:231], a[108:111], a[36:39], v[228:231]\n" \
"v_mfma_f32_16x16x32_bf16 v[236:239], a[104:107], a[40:43], v[236:239]\n" \
"ds_read_b64_tr_b16 a[86:87], v10 offset:34304\n" \
"v_mfma_f32_16x16x32_bf16 v[236:239], a[108:111], a[44:47], v[236:239]\n" \
"v_mfma_f32_16x16x32_bf16 v[244:247], a[104:107], a[48:51], v[244:247]\n" \
"ds_read_b64_tr_b16 a[88:89], v11 offset:33280\n" \
"v_mfma_f32_16x16x32_bf16 v[244:247], a[108:111], a[52:55], v[244:247]\n" \
"v_mfma_f32_16x16x32_bf16 v[252:255], a[104:107], a[56:59], v[252:255]\n" \
"ds_read_b64_tr_b16 a[90:91], v12 offset:33280\n" \
"v_mfma_f32_16x16x32_bf16 v[252:255], a[108:111], a[60:63], v[252:255]\n" \
"v_mfma_f32_16x16x32_bf16 a[128:131], a[112:115], a[0:3], a[128:131]\n" \
"ds_read_b64_tr_b16 a[92:93], v11 offset:34304\n" \
"v_mfma_f32_16x16x32_bf16 a[128:131], a[116:119], a[4:7], a[128:131]\n" \
"v_mfma_f32_16x16x32_bf16 a[136:139], a[112:115], a[8:11], a[136:139]\n" \
"ds_read_b64_tr_b16 a[94:95], v12 offset:34304\n" \
"v_mfma_f32_16x16x32_bf16 a[136:139], a[116:119], a[12:15], a[136:139]\n" \
"v_mfma_f32_16x16x32_bf16 a[144:147], a[112:115], a[16:19], a[144:147]\n" \
"v_mfma_f32_16x16x32_bf16 a[144:147], a[116:119], a[20:23], a[144:147]\n" \
"v_mfma_f32_16x16x32_bf16 a[152:155], a[112:115], a[24:27], a[152:155]\n" \
"v_mfma_f32_16x16x32_bf16 a[152:155], a[116:119], a[28:31], a[152:155]\n" \
"global_load_dwordx4 a[192:195], v4, s[86:87] offset:0\n" \
"v_mfma_f32_16x16x32_bf16 a[160:163], a[112:115], a[32:35], a[160:163]\n" \
"v_mov_b32 v58, s14\n" \
"v_mfma_f32_16x16x32_bf16 a[160:163], a[116:119], a[36:39], a[160:163]\n" \
"v_mov_b32 v59, s15\n" \
"v_mfma_f32_16x16x32_bf16 a[168:171], a[112:115], a[40:43], a[168:171]\n" \
"v_mfma_f32_16x16x32_bf16 a[168:171], a[116:119], a[44:47], a[168:171]\n" \
"s_waitcnt vmcnt(15) lgkmcnt(0)\n" \
"v_mfma_f32_16x16x32_bf16 a[176:179], a[112:115], a[48:51], a[176:179]\n" \
"s_barrier\n" \
"v_mfma_f32_16x16x32_bf16 a[176:179], a[116:119], a[52:55], a[176:179]\n" \
"v_mfma_f32_16x16x32_bf16 a[184:187], a[112:115], a[56:59], a[184:187]\n" \
"v_mfma_f32_16x16x32_bf16 a[184:187], a[116:119], a[60:63], a[184:187]\n" \
"buffer_store_dwordx2 v[58:59], v56, s[36:39], s23 offen sc1\n" \
"v_mfma_f32_16x16x32_bf16 a[132:135], a[120:123], a[0:3], a[132:135]\n" \
"v_mfma_f32_16x16x32_bf16 a[132:135], a[124:127], a[4:7], a[132:135]\n" \
"v_mfma_f32_16x16x32_bf16 a[140:143], a[120:123], a[8:11], a[140:143]\n" \
"ds_read_b128 a[0:3], v5 offset:32768\n" \
"v_mfma_f32_16x16x32_bf16 a[140:143], a[124:127], a[12:15], a[140:143]\n" \
"ds_read_b128 a[4:7], v6 offset:32768\n" \
"v_mfma_f32_16x16x32_bf16 a[148:151], a[120:123], a[16:19], a[148:151]\n" \
"ds_read_b128 a[8:11], v5 offset:34816\n" \
"v_mfma_f32_16x16x32_bf16 a[148:151], a[124:127], a[20:23], a[148:151]\n" \
"ds_read_b128 a[12:15], v6 offset:34816\n" \
"v_mfma_f32_16x16x32_bf16 a[156:159], a[120:123], a[24:27], a[156:159]\n" \
"ds_read_b128 a[16:19], v5 offset:36864\n" \
"v_mfma_f32_16x16x32_bf16 a[156:159], a[124:127], a[28:31], a[156:159]\n" \
"ds_read_b128 a[20:23], v6 offset:36864\n" \
"v_mfma_f32_16x16x32_bf16 a[164:167], a[120:123], a[32:35], a[164:167]\n" \
"ds_read_b128 a[24:27], v5 offset:38912\n" \
"v_mfma_f32_16x16x32_bf16 a[164:167], a[124:127], a[36:39], a[164:167]\n" \
"ds_read_b128 a[28:31], v6 offset:38912\n" \
"v_mfma_f32_16x16x32_bf16 a[172:175], a[120:123], a[40:43], a[172:175]\n" \
"ds_read_b128 a[32:35], v5 offset:40960\n" \
"v_mfma_f32_16x16x32_bf16 a[172:175], a[124:127], a[44:47], a[172:175]\n" \
"ds_read_b128 a[36:39], v6 offset:40960\n" \
"v_mfma_f32_16x16x32_bf16 a[180:183], a[120:123], a[48:51], a[180:183]\n" \
"ds_read_b128 a[40:43], v5 offset:43008\n" \
"v_mfma_f32_16x16x32_bf16 a[180:183], a[124:127], a[52:55], a[180:183]\n" \
"ds_read_b128 a[44:47], v6 offset:43008\n" \
"v_mfma_f32_16x16x32_bf16 a[188:191], a[120:123], a[56:59], a[188:191]\n" \
"ds_read_b128 a[48:51], v5 offset:45056\n" \
"v_mfma_f32_16x16x32_bf16 a[188:191], a[124:127], a[60:63], a[188:191]\n" \
"s_waitcnt lgkmcnt(9)\n" \
"ds_read_b128 a[52:55], v6 offset:45056\n" \
"v_mfma_f32_16x16x32_bf16 v[64:67], a[64:67], a[0:3], v[64:67]\n" \
"s_mov_b32 s47, 3145728\n" \
"s_add_u32 m0, s22, 16256\n" \
"ds_read_b128 a[56:59], v5 offset:47104\n" \
"v_mfma_f32_16x16x32_bf16 v[64:67], a[68:71], a[4:7], v[64:67]\n" \
"buffer_load_dwordx4 v21, s[32:35], s47 offen offset:128 lds\n" \
"v_mfma_f32_16x16x32_bf16 v[72:75], a[64:67], a[8:11], v[72:75]\n" \
"s_waitcnt lgkmcnt(7)\n" \
"ds_read_b128 a[60:63], v6 offset:47104\n" \
"v_mfma_f32_16x16x32_bf16 v[72:75], a[68:71], a[12:15], v[72:75]\n" \
"v_mfma_f32_16x16x32_bf16 v[80:83], a[64:67], a[16:19], v[80:83]\n" \
"s_add_u32 m0, s22, 18048\n" \
"ds_read_b64_tr_b16 a[96:97], v9 offset:49152\n" \
"v_mfma_f32_16x16x32_bf16 v[80:83], a[68:71], a[20:23], v[80:83]\n" \
"buffer_load_dwordx4 v21, s[32:35], s47 offen offset:384 lds\n" \
"v_mfma_f32_16x16x32_bf16 v[88:91], a[64:67], a[24:27], v[88:91]\n" \
"s_waitcnt lgkmcnt(5)\n" \
"ds_read_b64_tr_b16 a[98:99], v10 offset:49152\n" \
"v_mfma_f32_16x16x32_bf16 v[88:91], a[68:71], a[28:31], v[88:91]\n" \
"v_mfma_f32_16x16x32_bf16 v[96:99], a[64:67], a[32:35], v[96:99]\n" \
"s_mov_b32 s47, 3407872\n" \
"s_add_u32 m0, s22, 17280\n" \
"ds_read_b64_tr_b16 a[100:101], v9 offset:50176\n" \
"v_mfma_f32_16x16x32_bf16 v[96:99], a[68:71], a[36:39], v[96:99]\n" \
"buffer_load_dwordx4 v21, s[32:35], s47 offen offset:128 lds\n" \
"v_mfma_f32_16x16x32_bf16 v[104:107], a[64:67], a[40:43], v[104:107]\n" \
"s_waitcnt lgkmcnt(3)\n" \
"ds_read_b64_tr_b16 a[102:103], v10 offset:50176\n" \
"v_mfma_f32_16x16x32_bf16 v[104:107], a[68:71], a[44:47], v[104:107]\n" \
"v_mfma_f32_16x16x32_bf16 v[112:115], a[64:67], a[48:51], v[112:115]\n" \
"s_add_u32 m0, s22, 19072\n" \
"ds_read_b64_tr_b16 a[104:105], v11 offset:49152\n" \
"v_mfma_f32_16x16x32_bf16 v[112:115], a[68:71], a[52:55], v[112:115]\n" \
"buffer_load_dwordx4 v21, s[32:35], s47 offen offset:384 lds\n" \
"v_mfma_f32_16x16x32_bf16 v[120:123], a[64:67], a[56:59], v[120:123]\n" \
"ds_read_b64_tr_b16 a[106:107], v12 offset:49152\n" \
"v_mfma_f32_16x16x32_bf16 v[120:123], a[68:71], a[60:63], v[120:123]\n" \
"v_mfma_f32_16x16x32_bf16 v[68:71], a[72:75], a[0:3], v[68:71]\n" \
"s_mov_b32 s46, 896\n" \
"s_add_u32 m0, s50, 0\n" \
"ds_read_b64_tr_b16 a[108:109], v11 offset:50176\n" \
"v_mfma_f32_16x16x32_bf16 v[68:71], a[76:79], a[4:7], v[68:71]\n" \
"buffer_load_dwordx4 v13, s[28:31], s46 offen lds\n" \
"v_mfma_f32_16x16x32_bf16 v[76:79], a[72:75], a[8:11], v[76:79]\n" \
"ds_read_b64_tr_b16 a[110:111], v12 offset:50176\n" \
"v_mfma_f32_16x16x32_bf16 v[76:79], a[76:79], a[12:15], v[76:79]\n" \
"v_mfma_f32_16x16x32_bf16 v[84:87], a[72:75], a[16:19], v[84:87]\n" \
"s_add_u32 m0, s50, 4096\n" \
"ds_read_b64_tr_b16 a[112:113], v9 offset:49664\n" \
"v_mfma_f32_16x16x32_bf16 v[84:87], a[76:79], a[20:23], v[84:87]\n" \
"buffer_load_dwordx4 v14, s[28:31], s46 offen lds\n" \
"v_mfma_f32_16x16x32_bf16 v[92:95], a[72:75], a[24:27], v[92:95]\n" \
"ds_read_b64_tr_b16 a[114:115], v10 offset:49664\n" \
"v_mfma_f32_16x16x32_bf16 v[92:95], a[76:79], a[28:31], v[92:95]\n" \
"v_mfma_f32_16x16x32_bf16 v[100:103], a[72:75], a[32:35], v[100:103]\n" \
"s_add_u32 m0, s50, 8192\n" \
"ds_read_b64_tr_b16 a[116:117], v9 offset:50688\n" \
"v_mfma_f32_16x16x32_bf16 v[100:103], a[76:79], a[36:39], v[100:103]\n" \
"buffer_load_dwordx4 v15, s[28:31], s46 offen lds\n" \
"v_mfma_f32_16x16x32_bf16 v[108:111], a[72:75], a[40:43], v[108:111]\n" \
"ds_read_b64_tr_b16 a[118:119], v10 offset:50688\n" \
"v_mfma_f32_16x16x32_bf16 v[108:111], a[76:79], a[44:47], v[108:111]\n" \
"v_mfma_f32_16x16x32_bf16 v[116:119], a[72:75], a[48:51], v[116:119]\n" \
"s_add_u32 m0, s50, 12288\n" \
"ds_read_b64_tr_b16 a[120:121], v11 offset:49664\n" \
"v_mfma_f32_16x16x32_bf16 v[116:119], a[76:79], a[52:55], v[116:119]\n" \
"buffer_load_dwordx4 v16, s[28:31], s46 offen lds\n" \
"v_mfma_f32_16x16x32_bf16 v[124:127], a[72:75], a[56:59], v[124:127]\n" \
"ds_read_b64_tr_b16 a[122:123], v12 offset:49664\n" \
"v_mfma_f32_16x16x32_bf16 v[124:127], a[76:79], a[60:63], v[124:127]\n" \
"v_mfma_f32_16x16x32_bf16 v[128:131], a[80:83], a[0:3], v[128:131]\n" \
"s_mov_b32 s46, 896\n" \
"s_add_u32 m0, s50, 16384\n" \
"ds_read_b64_tr_b16 a[124:125], v11 offset:50688\n" \
"v_mfma_f32_16x16x32_bf16 v[128:131], a[84:87], a[4:7], v[128:131]\n" \
"buffer_load_dwordx4 v17, s[28:31], s46 offen lds\n" \
"v_mfma_f32_16x16x32_bf16 v[136:139], a[80:83], a[8:11], v[136:139]\n" \
"ds_read_b64_tr_b16 a[126:127], v12 offset:50688\n" \
"v_mfma_f32_16x16x32_bf16 v[136:139], a[84:87], a[12:15], v[136:139]\n" \
"v_mfma_f32_16x16x32_bf16 v[144:147], a[80:83], a[16:19], v[144:147]\n" \
"s_add_u32 m0, s50, 20480\n" \
"v_mfma_f32_16x16x32_bf16 v[144:147], a[84:87], a[20:23], v[144:147]\n" \
"buffer_load_dwordx4 v18, s[28:31], s46 offen lds\n" \
"v_mfma_f32_16x16x32_bf16 v[152:155], a[80:83], a[24:27], v[152:155]\n" \
"v_mfma_f32_16x16x32_bf16 v[152:155], a[84:87], a[28:31], v[152:155]\n" \
"v_mfma_f32_16x16x32_bf16 v[160:163], a[80:83], a[32:35], v[160:163]\n" \
"s_add_u32 m0, s50, 24576\n" \
"v_mfma_f32_16x16x32_bf16 v[160:163], a[84:87], a[36:39], v[160:163]\n" \
"buffer_load_dwordx4 v19, s[28:31], s46 offen lds\n" \
"v_mfma_f32_16x16x32_bf16 v[168:171], a[80:83], a[40:43], v[168:171]\n" \
"v_mfma_f32_16x16x32_bf16 v[168:171], a[84:87], a[44:47], v[168:171]\n" \
"s_waitcnt vmcnt(17) lgkmcnt(0)\n" \
"v_mfma_f32_16x16x32_bf16 v[176:179], a[80:83], a[48:51], v[176:179]\n" \
"s_add_u32 m0, s50, 28672\n" \
"s_barrier\n" \
"v_mfma_f32_16x16x32_bf16 v[176:179], a[84:87], a[52:55], v[176:179]\n" \
"buffer_load_dwordx4 v20, s[28:31], s46 offen lds\n" \
"v_mfma_f32_16x16x32_bf16 v[184:187], a[80:83], a[56:59], v[184:187]\n" \
"v_mfma_f32_16x16x32_bf16 v[184:187], a[84:87], a[60:63], v[184:187]\n" \
"v_mfma_f32_16x16x32_bf16 v[132:135], a[88:91], a[0:3], v[132:135]\n" \
"s_mov_b32 s47, 3670016\n" \
"s_add_u32 m0, s22, 32768\n" \
"v_mfma_f32_16x16x32_bf16 v[132:135], a[92:95], a[4:7], v[132:135]\n" \
"buffer_load_dwordx4 v21, s[32:35], s47 offen offset:0 lds\n" \
"v_mfma_f32_16x16x32_bf16 v[140:143], a[88:91], a[8:11], v[140:143]\n" \
"v_mfma_f32_16x16x32_bf16 v[140:143], a[92:95], a[12:15], v[140:143]\n" \
"v_mfma_f32_16x16x32_bf16 v[148:151], a[88:91], a[16:19], v[148:151]\n" \
"s_add_u32 m0, s22, 34560\n" \
"v_mfma_f32_16x16x32_bf16 v[148:151], a[92:95], a[20:23], v[148:151]\n" \
"buffer_load_dwordx4 v21, s[32:35], s47 offen offset:256 lds\n" \
"v_mfma_f32_16x16x32_bf16 v[156:159], a[88:91], a[24:27], v[156:159]\n" \
"v_mfma_f32_16x16x32_bf16 v[156:159], a[92:95], a[28:31], v[156:159]\n" \
"v_mfma_f32_16x16x32_bf16 v[164:167], a[88:91], a[32:35], v[164:167]\n" \
"s_mov_b32 s47, 3932160\n" \
"s_add_u32 m0, s22, 33792\n" \
"v_mfma_f32_16x16x32_bf16 v[164:167], a[92:95], a[36:39], v[164:167]\n" \
"buffer_load_dwordx4 v21, s[32:35], s47 offen offset:0 lds\n" \
"v_mfma_f32_16x16x32_bf16 v[172:175], a[88:91], a[40:43], v[172:175]\n" \
"v_mfma_f32_16x16x32_bf16 v[172:175], a[92:95], a[44:47], v[172:175]\n" \
"v_mfma_f32_16x16x32_bf16 v[180:183], a[88:91], a[48:51], v[180:183]\n" \
"s_add_u32 m0, s22, 35584\n" \
"v_mfma_f32_16x16x32_bf16 v[180:183], a[92:95], a[52:55], v[180:183]\n" \
"buffer_load_dwordx4 v21, s[32:35], s47 offen offset:256 lds\n" \
"v_mfma_f32_16x16x32_bf16 v[188:191], a[88:91], a[56:59], v[188:191]\n" \
"v_mfma_f32_16x16x32_bf16 v[188:191], a[92:95], a[60:63], v[188:191]\n" \
"v_mfma_f32_16x16x32_bf16 v[192:195], a[96:99], a[0:3], v[192:195]\n" \
"v_mfma_f32_16x16x32_bf16 v[192:195], a[100:103], a[4:7], v[192:195]\n" \
"buffer_load_dwordx4 v[30:33], v23, s[68:71], 0 offen offset:0 nt\n" \
"v_mfma_f32_16x16x32_bf16 v[200:203], a[96:99], a[8:11], v[200:203]\n" \
"s_mov_b64 s[28:29], s[64:65]\n" \
"v_mfma_f32_16x16x32_bf16 v[200:203], a[100:103], a[12:15], v[200:203]\n" \
"v_mfma_f32_16x16x32_bf16 v[208:211], a[96:99], a[16:19], v[208:211]\n" \
"ds_read_b64_tr_b16 a[64:65], v9 offset:0\n" \
"v_mfma_f32_16x16x32_bf16 v[208:211], a[100:103], a[20:23], v[208:211]\n" \
"buffer_load_dwordx4 v[34:37], v24, s[68:71], 0 offen offset:0 nt\n" \
"v_mfma_f32_16x16x32_bf16 v[216:219], a[96:99], a[24:27], v[216:219]\n" \
"s_and_b32 s29, s29, 0xffff\n" \
"ds_read_b64_tr_b16 a[66:67], v10 offset:0\n" \
"v_mfma_f32_16x16x32_bf16 v[216:219], a[100:103], a[28:31], v[216:219]\n" \
"v_mfma_f32_16x16x32_bf16 v[224:227], a[96:99], a[32:35], v[224:227]\n" \
"ds_read_b64_tr_b16 a[68:69], v9 offset:1024\n" \
"v_mfma_f32_16x16x32_bf16 v[224:227], a[100:103], a[36:39], v[224:227]\n" \
"buffer_load_dwordx4 v[38:41], v24, s[68:71], 0 offen offset:1024 nt\n" \
"v_mfma_f32_16x16x32_bf16 v[232:235], a[96:99], a[40:43], v[232:235]\n" \
"s_mov_b32 s30, 4194304\n" \
"ds_read_b64_tr_b16 a[70:71], v10 offset:1024\n" \
"v_mfma_f32_16x16x32_bf16 v[232:235], a[100:103], a[44:47], v[232:235]\n" \
"v_mfma_f32_16x16x32_bf16 v[240:243], a[96:99], a[48:51], v[240:243]\n" \
"ds_read_b64_tr_b16 a[72:73], v11 offset:0\n" \
"v_mfma_f32_16x16x32_bf16 v[240:243], a[100:103], a[52:55], v[240:243]\n" \
"buffer_load_dwordx4 v[42:45], v24, s[68:71], 0 offen offset:2048 nt\n" \
"v_mfma_f32_16x16x32_bf16 v[248:251], a[96:99], a[56:59], v[248:251]\n" \
"s_lshl_b32 s60, s7, 18\n" \
"ds_read_b64_tr_b16 a[74:75], v12 offset:0\n" \
"v_mfma_f32_16x16x32_bf16 v[248:251], a[100:103], a[60:63], v[248:251]\n" \
"v_mfma_f32_16x16x32_bf16 v[196:199], a[104:107], a[0:3], v[196:199]\n" \
"ds_read_b64_tr_b16 a[76:77], v11 offset:1024\n" \
"v_mfma_f32_16x16x32_bf16 v[196:199], a[108:111], a[4:7], v[196:199]\n" \
"buffer_load_dwordx4 v[46:49], v24, s[68:71], 0 offen offset:3072 nt\n" \
"v_mfma_f32_16x16x32_bf16 v[204:207], a[104:107], a[8:11], v[204:207]\n" \
"s_sub_u32 s61, 3932160, s60\n" \
"ds_read_b64_tr_b16 a[78:79], v12 offset:1024\n" \
"v_mfma_f32_16x16x32_bf16 v[204:207], a[108:111], a[12:15], v[204:207]\n" \
"LH2_%=:\ns_cbranch_execz LH10_%=\n" \
"v_mfma_f32_16x16x32_bf16 v[212:215], a[104:107], a[16:19], v[212:215]\n" \
"ds_read_b64_tr_b16 a[80:81], v9 offset:512\n" \
"v_mfma_f32_16x16x32_bf16 v[212:215], a[108:111], a[20:23], v[212:215]\n" \
"buffer_load_dword v50, v25, s[68:71], 0 offen offset:0\n" \
"v_mfma_f32_16x16x32_bf16 v[220:223], a[104:107], a[24:27], v[220:223]\n" \
"ds_read_b64_tr_b16 a[82:83], v10 offset:512\n" \
"v_add_u32 v13, s60, v63\n" \
"v_mfma_f32_16x16x32_bf16 v[220:223], a[108:111], a[28:31], v[220:223]\n" \
"v_mfma_f32_16x16x32_bf16 v[228:231], a[104:107], a[32:35], v[228:231]\n" \
"ds_read_b64_tr_b16 a[84:85], v9 offset:1536\n" \
"v_mfma_f32_16x16x32_bf16 v[228:231], a[108:111], a[36:39], v[228:231]\n" \
"buffer_load_dword v51, v24, s[68:71], 0 offen offset:16\n" \
"v_mfma_f32_16x16x32_bf16 v[236:239], a[104:107], a[40:43], v[236:239]\n" \
"ds_read_b64_tr_b16 a[86:87], v10 offset:1536\n" \
"v_add_u32 v17, s61, v63\n" \
"v_mfma_f32_16x16x32_bf16 v[236:239], a[108:111], a[44:47], v[236:239]\n" \
"v_mfma_f32_16x16x32_bf16 v[244:247], a[104:107], a[48:51], v[244:247]\n" \
"ds_read_b64_tr_b16 a[88:89], v11 offset:512\n" \
"v_mfma_f32_16x16x32_bf16 v[244:247], a[108:111], a[52:55], v[244:247]\n" \
"buffer_load_dword v52, v24, s[68:71], 0 offen offset:1040\n" \
"v_mfma_f32_16x16x32_bf16 v[252:255], a[104:107], a[56:59], v[252:255]\n" \
"ds_read_b64_tr_b16 a[90:91], v12 offset:512\n" \
"v_add_u32 v14, 0x10000, v13\n" \
"v_mfma_f32_16x16x32_bf16 v[252:255], a[108:111], a[60:63], v[252:255]\n" \
"v_mfma_f32_16x16x32_bf16 a[128:131], a[112:115], a[0:3], a[128:131]\n" \
"ds_read_b64_tr_b16 a[92:93], v11 offset:1536\n" \
"v_mfma_f32_16x16x32_bf16 a[128:131], a[116:119], a[4:7], a[128:131]\n" \
"buffer_load_dword v53, v24, s[68:71], 0 offen offset:2064\n" \
"v_mfma_f32_16x16x32_bf16 a[136:139], a[112:115], a[8:11], a[136:139]\n" \
"ds_read_b64_tr_b16 a[94:95], v12 offset:1536\n" \
"v_add_u32 v18, 0x10000, v17\n" \
"v_mfma_f32_16x16x32_bf16 a[136:139], a[116:119], a[12:15], a[136:139]\n" \
"v_mfma_f32_16x16x32_bf16 a[144:147], a[112:115], a[16:19], a[144:147]\n" \
"v_mfma_f32_16x16x32_bf16 a[144:147], a[116:119], a[20:23], a[144:147]\n" \
"buffer_load_dword v54, v24, s[68:71], 0 offen offset:3088\n" \
"v_mfma_f32_16x16x32_bf16 a[152:155], a[112:115], a[24:27], a[152:155]\n" \
"v_add_u32 v15, 0x10000, v14\n" \
"v_mfma_f32_16x16x32_bf16 a[152:155], a[116:119], a[28:31], a[152:155]\n" \
"v_mfma_f32_16x16x32_bf16 a[160:163], a[112:115], a[32:35], a[160:163]\n" \
"v_mfma_f32_16x16x32_bf16 a[160:163], a[116:119], a[36:39], a[160:163]\n" \
"global_load_short_d16_hi v28, v29, s[12:13]\n" \
"v_mfma_f32_16x16x32_bf16 a[168:171], a[112:115], a[40:43], a[168:171]\n" \
"v_add_u32 v19, 0x10000, v18\n" \
"v_mfma_f32_16x16x32_bf16 a[168:171], a[116:119], a[44:47], a[168:171]\n" \
"s_waitcnt vmcnt(23) lgkmcnt(0)\n" \
"v_mfma_f32_16x16x32_bf16 a[176:179], a[112:115], a[48:51], a[176:179]\n" \
"s_barrier\n" \
"v_mfma_f32_16x16x32_bf16 a[176:179], a[116:119], a[52:55], a[176:179]\n" \
"global_load_dwordx4 a[196:199], v4, s[86:87] offset:1024\n" \
"v_mfma_f32_16x16x32_bf16 a[184:187], a[112:115], a[56:59], a[184:187]\n" \
"v_add_u32 v16, 0x10000, v15\n" \
"v_mfma_f32_16x16x32_bf16 a[184:187], a[116:119], a[60:63], a[184:187]\n" \
"v_mfma_f32_16x16x32_bf16 a[132:135], a[120:123], a[0:3], a[132:135]\n" \
"v_mfma_f32_16x16x32_bf16 a[132:135], a[124:127], a[4:7], a[132:135]\n" \
"v_mfma_f32_16x16x32_bf16 a[140:143], a[120:123], a[8:11], a[140:143]\n" \
"ds_read_b128 a[0:3], v7 offset:0\n" \
"v_add_u32 v20, 0x10000, v19\n" \
"v_mfma_f32_16x16x32_bf16 a[140:143], a[124:127], a[12:15], a[140:143]\n" \
"ds_read_b128 a[4:7], v8 offset:0\n" \
"v_mfma_f32_16x16x32_bf16 a[148:151], a[120:123], a[16:19], a[148:151]\n" \
"ds_read_b128 a[8:11], v7 offset:2048\n" \
"v_mfma_f32_16x16x32_bf16 a[148:151], a[124:127], a[20:23], a[148:151]\n" \
"ds_read_b128 a[12:15], v8 offset:2048\n" \
"v_mfma_f32_16x16x32_bf16 a[156:159], a[120:123], a[24:27], a[156:159]\n" \
"ds_read_b128 a[16:19], v7 offset:4096\n" \
"v_mfma_f32_16x16x32_bf16 a[156:159], a[124:127], a[28:31], a[156:159]\n" \
"ds_read_b128 a[20:23], v8 offset:4096\n" \
"v_mfma_f32_16x16x32_bf16 a[164:167], a[120:123], a[32:35], a[164:167]\n" \
"ds_read_b128 a[24:27], v7 offset:6144\n" \
"v_mfma_f32_16x16x32_bf16 a[164:167], a[124:127], a[36:39], a[164:167]\n" \
"ds_read_b128 a[28:31], v8 offset:6144\n" \
"v_mfma_f32_16x16x32_bf16 a[172:175], a[120:123], a[40:43], a[172:175]\n" \
"ds_read_b128 a[32:35], v7 offset:8192\n" \
"v_mfma_f32_16x16x32_bf16 a[172:175], a[124:127], a[44:47], a[172:175]\n" \
"ds_read_b128 a[36:39], v8 offset:8192\n" \
"v_mfma_f32_16x16x32_bf16 a[180:183], a[120:123], a[48:51], a[180:183]\n" \
"ds_read_b128 a[40:43], v7 offset:10240\n" \
"v_mfma_f32_16x16x32_bf16 a[180:183], a[124:127], a[52:55], a[180:183]\n" \
"ds_read_b128 a[44:47], v8 offset:10240\n" \
"v_mfma_f32_16x16x32_bf16 a[188:191], a[120:123], a[56:59], a[188:191]\n" \
"ds_read_b128 a[48:51], v7 offset:12288\n" \
"v_mfma_f32_16x16x32_bf16 a[188:191], a[124:127], a[60:63], a[188:191]\n" \
"s_waitcnt lgkmcnt(9)\n" \
"ds_read_b128 a[52:55], v8 offset:12288\n" \
"v_mfma_f32_16x16x32_bf16 v[64:67], a[64:67], a[0:3], v[64:67]\n" \
"ds_read_b128 a[56:59], v7 offset:14336\n" \
"v_mfma_f32_16x16x32_bf16 v[64:67], a[68:71], a[4:7], v[64:67]\n" \
"buffer_load_dwordx2 v[58:59], v57, s[36:39], 0 offen sc1\n" \
"v_mfma_f32_16x16x32_bf16 v[72:75], a[64:67], a[8:11], v[72:75]\n" \
"s_waitcnt lgkmcnt(7)\n" \
"ds_read_b128 a[60:63], v8 offset:14336\n" \
"v_mfma_f32_16x16x32_bf16 v[72:75], a[68:71], a[12:15], v[72:75]\n" \
"v_mfma_f32_16x16x32_bf16 v[80:83], a[64:67], a[16:19], v[80:83]\n" \
"s_mov_b32 s47, 3670016\n" \
"s_add_u32 m0, s22, 49024\n" \
"ds_read_b64_tr_b16 a[96:97], v9 offset:16384\n" \
"v_mfma_f32_16x16x32_bf16 v[80:83], a[68:71], a[20:23], v[80:83]\n" \
"buffer_load_dwordx4 v21, s[32:35], s47 offen offset:128 lds\n" \
"v_mfma_f32_16x16x32_bf16 v[88:91], a[64:67], a[24:27], v[88:91]\n" \
"s_waitcnt lgkmcnt(5)\n" \
"ds_read_b64_tr_b16 a[98:99], v10 offset:16384\n" \
"v_mfma_f32_16x16x32_bf16 v[88:91], a[68:71], a[28:31], v[88:91]\n" \
"v_mfma_f32_16x16x32_bf16 v[96:99], a[64:67], a[32:35], v[96:99]\n" \
"s_add_u32 m0, s22, 50816\n" \
"ds_read_b64_tr_b16 a[100:101], v9 offset:17408\n" \
"v_mfma_f32_16x16x32_bf16 v[96:99], a[68:71], a[36:39], v[96:99]\n" \
"buffer_load_dwordx4 v21, s[32:35], s47 offen offset:384 lds\n" \
"v_mfma_f32_16x16x32_bf16 v[104:107], a[64:67], a[40:43], v[104:107]\n" \
"s_waitcnt lgkmcnt(3)\n" \
"ds_read_b64_tr_b16 a[102:103], v10 offset:17408\n" \
"v_mfma_f32_16x16x32_bf16 v[104:107], a[68:71], a[44:47], v[104:107]\n" \
"v_mfma_f32_16x16x32_bf16 v[112:115], a[64:67], a[48:51], v[112:115]\n" \
"s_mov_b32 s47, 3932160\n" \
"s_add_u32 m0, s22, 50048\n" \
"ds_read_b64_tr_b16 a[104:105], v11 offset:16384\n" \
"v_mfma_f32_16x16x32_bf16 v[112:115], a[68:71], a[52:55], v[112:115]\n" \
"buffer_load_dwordx4 v21, s[32:35], s47 offen offset:128 lds\n" \
"v_mfma_f32_16x16x32_bf16 v[120:123], a[64:67], a[56:59], v[120:123]\n" \
"ds_read_b64_tr_b16 a[106:107], v12 offset:16384\n" \
"v_mfma_f32_16x16x32_bf16 v[120:123], a[68:71], a[60:63], v[120:123]\n" \
"s_add_u32 s76, s76, 2097152\ns_addc_u32 s77, s77, 0\n" \
"v_mfma_f32_16x16x32_bf16 v[68:71], a[72:75], a[0:3], v[68:71]\n" \
"s_add_u32 m0, s22, 51840\n" \
"ds_read_b64_tr_b16 a[108:109], v11 offset:17408\n" \
"v_mfma_f32_16x16x32_bf16 v[68:71], a[76:79], a[4:7], v[68:71]\n" \
"buffer_load_dwordx4 v21, s[32:35], s47 offen offset:384 lds\n" \
"v_mfma_f32_16x16x32_bf16 v[76:79], a[72:75], a[8:11], v[76:79]\n" \
"ds_read_b64_tr_b16 a[110:111], v12 offset:17408\n" \
"v_mfma_f32_16x16x32_bf16 v[76:79], a[76:79], a[12:15], v[76:79]\n" \
"v_mfma_f32_16x16x32_bf16 v[84:87], a[72:75], a[16:19], v[84:87]\n" \
"ds_read_b64_tr_b16 a[112:113], v9 offset:16896\n" \
"v_mfma_f32_16x16x32_bf16 v[84:87], a[76:79], a[20:23], v[84:87]\n" \
"v_mfma_f32_16x16x32_bf16 v[92:95], a[72:75], a[24:27], v[92:95]\n" \
"ds_read_b64_tr_b16 a[114:115], v10 offset:16896\n" \
"v_mfma_f32_16x16x32_bf16 v[92:95], a[76:79], a[28:31], v[92:95]\n" \
"s_mov_b64 exec, 0xffff\nglobal_load_dword v22, v1, s[76:77]\ns_mov_b64 exec, -1\n" \
"v_mfma_f32_16x16x32_bf16 v[100:103], a[72:75], a[32:35], v[100:103]\n" \
"s_add_u32 s76, s76, 2097152\ns_addc_u32 s77, s77, 0\n" \
"ds_read_b64_tr_b16 a[116:117], v9 offset:17920\n" \
"v_mfma_f32_16x16x32_bf16 v[100:103], a[76:79], a[36:39], v[100:103]\n" \
"v_mfma_f32_16x16x32_bf16 v[108:111], a[72:75], a[40:43], v[108:111]\n" \
"ds_read_b64_tr_b16 a[118:119], v10 offset:17920\n" \
"v_mfma_f32_16x16x32_bf16 v[108:111], a[76:79], a[44:47], v[108:111]\n" \
"s_mov_b64 exec, 0xffff\nglobal_load_dword v22, v1, s[76:77]\ns_mov_b64 exec, -1\n" \
"v_mfma_f32_16x16x32_bf16 v[116:119], a[72:75], a[48:51], v[116:119]\n" \
"ds_read_b64_tr_b16 a[120:121], v11 offset:16896\n" \
"v_mfma_f32_16x16x32_bf16 v[116:119], a[76:79], a[52:55], v[116:119]\n" \
"v_mfma_f32_16x16x32_bf16 v[124:127], a[72:75], a[56:59], v[124:127]\n" \
"ds_read_b64_tr_b16 a[122:123], v12 offset:16896\n" \
"v_mfma_f32_16x16x32_bf16 v[124:127], a[76:79], a[60:63], v[124:127]\n" \
"v_mfma_f32_16x16x32_bf16 v[128:131], a[80:83], a[0:3], v[128:131]\n" \
"ds_read_b64_tr_b16 a[124:125], v11 offset:17920\n" \
"v_mfma_f32_16x16x32_bf16 v[128:131], a[84:87], a[4:7], v[128:131]\n" \
"v_mfma_f32_16x16x32_bf16 v[136:139], a[80:83], a[8:11], v[136:139]\n" \
"ds_read_b64_tr_b16 a[126:127], v12 offset:17920\n" \
"v_mfma_f32_16x16x32_bf16 v[136:139], a[84:87], a[12:15], v[136:139]\n" \
"v_mfma_f32_16x16x32_bf16 v[144:147], a[80:83], a[16:19], v[144:147]\n" \
"v_mfma_f32_16x16x32_bf16 v[144:147], a[84:87], a[20:23], v[144:147]\n" \
"v_mfma_f32_16x16x32_bf16 v[152:155], a[80:83], a[24:27], v[152:155]\n" \
"v_mfma_f32_16x16x32_bf16 v[152:155], a[84:87], a[28:31], v[152:155]\n" \
"v_mfma_f32_16x16x32_bf16 v[160:163], a[80:83], a[32:35], v[160:163]\n" \
"v_mfma_f32_16x16x32_bf16 v[160:163], a[84:87], a[36:39], v[160:163]\n" \
"v_mfma_f32_16x16x32_bf16 v[168:171], a[80:83], a[40:43], v[168:171]\n" \
"v_mfma_f32_16x16x32_bf16 v[168:171], a[84:87], a[44:47], v[168:171]\n" \
"s_waitcnt vmcnt(6) lgkmcnt(0)\n" \
"v_mfma_f32_16x16x32_bf16 v[176:179], a[80:83], a[48:51], v[176:179]\n" \
"s_barrier\n" \
"v_mfma_f32_16x16x32_bf16 v[176:179], a[84:87], a[52:55], v[176:179]\n" \
"v_cmp_ne_u64 vcc, s[14:15], v[58:59]\n" \
"v_mfma_f32_16x16x32_bf16 v[184:187], a[80:83], a[56:59], v[184:187]\n" \
"v_mfma_f32_16x16x32_bf16 v[184:187], a[84:87], a[60:63], v[184:187]\n" \
"s_cbranch_vccnz LPUK_%=\nLPUKR_%=:\n" \
"v_mfma_f32_16x16x32_bf16 v[132:135], a[88:91], a[0:3], v[132:135]\n" \
"v_mfma_f32_16x16x32_bf16 v[132:135], a[92:95], a[4:7], v[132:135]\n" \
"v_mfma_f32_16x16x32_bf16 v[140:143], a[88:91], a[8:11], v[140:143]\n" \
"v_mfma_f32_16x16x32_bf16 v[140:143], a[92:95], a[12:15], v[140:143]\n" \
"v_mfma_f32_16x16x32_bf16 v[148:151], a[88:91], a[16:19], v[148:151]\n" \
"v_mfma_f32_16x16x32_bf16 v[148:151], a[92:95], a[20:23], v[148:151]\n" \
"v_mfma_f32_16x16x32_bf16 v[156:159], a[88:91], a[24:27], v[156:159]\n" \
"v_mfma_f32_16x16x32_bf16 v[156:159], a[92:95], a[28:31], v[156:159]\n" \
"v_mfma_f32_16x16x32_bf16 v[164:167], a[88:91], a[32:35], v[164:167]\n" \
"v_mfma_f32_16x16x32_bf16 v[164:167], a[92:95], a[36:39], v[164:167]\n" \
"v_mfma_f32_16x16x32_bf16 v[172:175], a[88:91], a[40:43], v[172:175]\n" \
"v_mfma_f32_16x16x32_bf16 v[172:175], a[92:95], a[44:47], v[172:175]\n" \
"v_mfma_f32_16x16x32_bf16 v[180:183], a[88:91], a[48:51], v[180:183]\n" \
"v_mfma_f32_16x16x32_bf16 v[180:183], a[92:95], a[52:55], v[180:183]\n" \
"v_mfma_f32_16x16x32_bf16 v[188:191], a[88:91], a[56:59], v[188:191]\n" \
"v_mfma_f32_16x16x32_bf16 v[188:191], a[92:95], a[60:63], v[188:191]\n" \
"v_mfma_f32_16x16x32_bf16 v[192:195], a[96:99], a[0:3], v[192:195]\n" \
"s_add_u32 m0, s56, 10304\n" \
"v_mfma_f32_16x16x32_bf16 v[192:195], a[100:103], a[4:7], v[192:195]\n" \
"buffer_load_dwordx4 v26, s[72:75], 0 offen offset:0 sc1 lds\n" \
"v_mfma_f32_16x16x32_bf16 v[200:203], a[96:99], a[8:11], v[200:203]\n" \
"s_mov_b64 s[32:33], s[44:45]\n" \
"v_mfma_f32_16x16x32_bf16 v[200:203], a[100:103], a[12:15], v[200:203]\n" \
"v_mfma_f32_16x16x32_bf16 v[208:211], a[96:99], a[16:19], v[208:211]\n" \
"s_add_u32 m0, s56, 11328\n" \
"ds_read_b64_tr_b16 a[64:65], v9 offset:32768\n" \
"s_waitcnt vmcnt(9)\n" \
"v_mfma_f32_16x16x32_bf16 v[208:211], a[100:103], a[20:23], v[208:211]\n" \
"buffer_load_dwordx4 v27, s[72:75], 0 offen offset:0 sc1 lds\n" \
"v_mfma_f32_16x16x32_bf16 v[216:219], a[96:99], a[24:27], v[216:219]\n" \
"s_mov_b32 s34, s49\n" \
"ds_read_b64_tr_b16 a[66:67], v10 offset:32768\n" \
"v_alignbyte_b32 v30, v31, v30, 2\n" \
"v_mfma_f32_16x16x32_bf16 v[216:219], a[100:103], a[28:31], v[216:219]\n" \
"v_alignbyte_b32 v31, v32, v31, 2\n" \
"v_alignbyte_b32 v32, v33, v32, 2\n" \
"v_mfma_f32_16x16x32_bf16 v[224:227], a[96:99], a[32:35], v[224:227]\n" \
"s_add_u32 m0, s56, 11328\n" \
"ds_read_b64_tr_b16 a[68:69], v9 offset:33792\n" \
"v_alignbyte_b32 v33, v50, v33, 2\n" \
"v_mfma_f32_16x16x32_bf16 v[224:227], a[100:103], a[36:39], v[224:227]\n" \
"buffer_load_dwordx4 v27, s[72:75], 0 offen offset:1024 sc1 lds\n" \
"v_mfma_f32_16x16x32_bf16 v[232:235], a[96:99], a[40:43], v[232:235]\n" \
"ds_read_b64_tr_b16 a[70:71], v10 offset:33792\n" \
"v_mfma_f32_16x16x32_bf16 v[232:235], a[100:103], a[44:47], v[232:235]\n" \
"ds_write_b128 v55, v[30:33] offset:5184\n" \
"v_alignbyte_b32 v34, v35, v34, 2\n" \
"v_mfma_f32_16x16x32_bf16 v[240:243], a[96:99], a[48:51], v[240:243]\n" \
"s_add_u32 m0, s56, 11328\n" \
"ds_read_b64_tr_b16 a[72:73], v11 offset:32768\n" \
"v_alignbyte_b32 v35, v36, v35, 2\n" \
"v_mfma_f32_16x16x32_bf16 v[240:243], a[100:103], a[52:55], v[240:243]\n" \
"buffer_load_dwordx4 v27, s[72:75], 0 offen offset:2048 sc1 lds\n" \
"v_mfma_f32_16x16x32_bf16 v[248:251], a[96:99], a[56:59], v[248:251]\n" \
"ds_read_b64_tr_b16 a[74:75], v12 offset:32768\n" \
"v_alignbyte_b32 v36, v37, v36, 2\n" \
"v_mfma_f32_16x16x32_bf16 v[248:251], a[100:103], a[60:63], v[248:251]\n" \
"v_alignbyte_b32 v37, v51, v37, 2\n" \
"ds_write_b128 v55, v[34:37] offset:6208\n" \
"v_mfma_f32_16x16x32_bf16 v[196:199], a[104:107], a[0:3], v[196:199]\n" \
"s_add_u32 m0, s56, 11328\n" \
"ds_read_b64_tr_b16 a[76:77], v11 offset:33792\n" \
"v_alignbyte_b32 v38, v39, v38, 2\n" \
"v_mfma_f32_16x16x32_bf16 v[196:199], a[108:111], a[4:7], v[196:199]\n" \
"buffer_load_dwordx4 v27, s[72:75], 0 offen offset:3072 sc1 lds\n" \
"v_mfma_f32_16x16x32_bf16 v[204:207], a[104:107], a[8:11], v[204:207]\n" \
"ds_read_b64_tr_b16 a[78:79], v12 offset:33792\n" \
"v_alignbyte_b32 v39, v40, v39, 2\n" \
"v_mfma_f32_16x16x32_bf16 v[204:207], a[108:111], a[12:15], v[204:207]\n" \
"v_alignbyte_b32 v40, v41, v40, 2\n" \
"v_alignbyte_b32 v41, v52, v41, 2\n" \
"v_mfma_f32_16x16x32_bf16 v[212:215], a[104:107], a[16:19], v[212:215]\n" \
"s_add_u32 m0, s56, 0\n" \
"ds_read_b64_tr_b16 a[80:81], v9 offset:33280\n" \
"v_mfma_f32_16x16x32_bf16 v[212:215], a[108:111], a[20:23], v[212:215]\n" \
"buffer_load_dwordx4 v23, s[68:71], 0 offen offset:0 lds\n" \
"v_mfma_f32_16x16x32_bf16 v[220:223], a[104:107], a[24:27], v[220:223]\n" \
"ds_read_b64_tr_b16 a[82:83], v10 offset:33280\n" \
"v_mfma_f32_16x16x32_bf16 v[220:223], a[108:111], a[28:31], v[220:223]\n" \
"ds_write_b128 v55, v[38:41] offset:7232\n" \
"v_alignbyte_b32 v42, v43, v42, 2\n" \
"v_mfma_f32_16x16x32_bf16 v[228:231], a[104:107], a[32:35], v[228:231]\n" \
"s_add_u32 m0, s56, 1024\n" \
"ds_read_b64_tr_b16 a[84:85], v9 offset:34304\n" \
"v_alignbyte_b32 v43, v44, v43, 2\n" \
"v_mfma_f32_16x16x32_bf16 v[228:231], a[108:111], a[36:39], v[228:231]\n" \
"buffer_load_dwordx4 v24, s[68:71], 0 offen offset:0 lds\n" \
"v_mfma_f32_16x16x32_bf16 v[236:239], a[104:107], a[40:43], v[236:239]\n" \
"ds_read_b64_tr_b16 a[86:87], v10 offset:34304\n" \
"v_alignbyte_b32 v44, v45, v44, 2\n" \
"v_mfma_f32_16x16x32_bf16 v[236:239], a[108:111], a[44:47], v[236:239]\n" \
"v_alignbyte_b32 v45, v53, v45, 2\n" \
"ds_write_b128 v55, v[42:45] offset:8256\n" \
"v_mfma_f32_16x16x32_bf16 v[244:247], a[104:107], a[48:51], v[244:247]\n" \
"s_add_u32 m0, s56, 1024\n" \
"ds_read_b64_tr_b16 a[88:89], v11 offset:33280\n" \
"v_alignbyte_b32 v46, v47, v46, 2\n" \
"v_mfma_f32_16x16x32_bf16 v[244:247], a[108:111], a[52:55], v[244:247]\n" \
"buffer_load_dwordx4 v24, s[68:71], 0 offen offset:1024 lds\n" \
"v_mfma_f32_16x16x32_bf16 v[252:255], a[104:107], a[56:59], v[252:255]\n" \
"ds_read_b64_tr_b16 a[90:91], v12 offset:33280\n" \
"v_alignbyte_b32 v47, v48, v47, 2\n" \
"v_mfma_f32_16x16x32_bf16 v[252:255], a[108:111], a[60:63], v[252:255]\n" \
"v_alignbyte_b32 v48, v49, v48, 2\n" \
"v_alignbyte_b32 v49, v54, v49, 2\n" \
"v_mfma_f32_16x16x32_bf16 a[128:131], a[112:115], a[0:3], a[128:131]\n" \
"s_add_u32 m0, s56, 1024\n" \
"ds_read_b64_tr_b16 a[92:93], v11 offset:34304\n" \
"v_mfma_f32_16x16x32_bf16 a[128:131], a[116:119], a[4:7], a[128:131]\n" \
"buffer_load_dwordx4 v24, s[68:71], 0 offen offset:2048 lds\n" \
"v_mfma_f32_16x16x32_bf16 a[136:139], a[112:115], a[8:11], a[136:139]\n" \
"ds_read_b64_tr_b16 a[94:95], v12 offset:34304\n" \
"v_mfma_f32_16x16x32_bf16 a[136:139], a[116:119], a[12:15], a[136:139]\n" \
"ds_write_b128 v55, v[46:49] offset:9280\n" \
"v_lshrrev_b32 v30, 4, v4\n" \
"v_mfma_f32_16x16x32_bf16 a[144:147], a[112:115], a[16:19], a[144:147]\n" \
"s_add_u32 m0, s56, 1024\n" \
"v_and_b32 v31, 15, v30\n" \
"v_lshrrev_b32 v32, 4, v30\n" \
"v_mfma_f32_16x16x32_bf16 a[144:147], a[116:119], a[20:23], a[144:147]\n" \
"buffer_load_dwordx4 v24, s[68:71], 0 offen offset:3072 lds\n" \
"v_mfma_f32_16x16x32_bf16 a[152:155], a[112:115], a[24:27], a[152:155]\n" \
"v_add_u32 v33, 1, v31\n" \
"v_and_b32 v34, 1, v33\n" \
"v_mfma_f32_16x16x32_bf16 a[152:155], a[116:119], a[28:31], a[152:155]\n" \
"v_and_b32 v33, 30, v33\n" \
"v_mul_u32_u24 v34, 5184, v34\n" \
"v_mfma_f32_16x16x32_bf16 a[160:163], a[112:115], a[32:35], a[160:163]\n" \
"s_mov_b32 s46, 0\n" \
"s_add_u32 m0, s50, 65536\n" \
"v_lshl_add_u32 v33, v33, 1, v34\n" \
"v_mfma_f32_16x16x32_bf16 a[160:163], a[116:119], a[36:39], a[160:163]\n" \
"buffer_load_dwordx4 v13, s[28:31], s46 offen lds\n" \
"v_mfma_f32_16x16x32_bf16 a[168:171], a[112:115], a[40:43], a[168:171]\n" \
"v_lshl_add_u32 v33, v32, 4, v33\n" \
"v_add_u32 v46, s56, v33\n" \
"v_mfma_f32_16x16x32_bf16 a[168:171], a[116:119], a[44:47], a[168:171]\n" \
"s_waitcnt vmcnt(13) lgkmcnt(0)\n" \
"v_add_u32 v47, 1024, v46\n" \
"v_add_u32 v48, 2048, v46\n" \
"v_mfma_f32_16x16x32_bf16 a[176:179], a[112:115], a[48:51], a[176:179]\n" \
"s_add_u32 m0, s50, 69632\n" \
"s_barrier\n" \
"v_add_u32 v49, 3072, v46\n" \
"v_mfma_f32_16x16x32_bf16 a[176:179], a[116:119], a[52:55], a[176:179]\n" \
"buffer_load_dwordx4 v14, s[28:31], s46 offen lds\n" \
"v_mfma_f32_16x16x32_bf16 a[184:187], a[112:115], a[56:59], a[184:187]\n" \
"v_lshlrev_b32 v33, 1, v31\n" \
"v_sub_u32 v33, v33, v32\n" \
"v_mfma_f32_16x16x32_bf16 a[184:187], a[116:119], a[60:63], a[184:187]\n" \
"v_lshlrev_b32 v33, 4, v33\n" \
"v_add_u32 v33, 10320, v33\n" \
"v_mfma_f32_16x16x32_bf16 a[132:135], a[120:123], a[0:3], a[132:135]\n" \
"s_add_u32 m0, s50, 73728\n" \
"v_add_u32 v50, s56, v33\n" \
"v_mfma_f32_16x16x32_bf16 a[132:135], a[124:127], a[4:7], a[132:135]\n" \
"buffer_load_dwordx4 v15, s[28:31], s46 offen lds\n" \
"v_mfma_f32_16x16x32_bf16 a[140:143], a[120:123], a[8:11], a[140:143]\n" \
"ds_read_b128 a[0:3], v5 offset:0\n" \
"v_lshlrev_b32 v33, 5, v31\n" \
"v_mfma_f32_16x16x32_bf16 a[140:143], a[124:127], a[12:15], a[140:143]\n" \
"ds_read_b128 a[4:7], v6 offset:0\n" \
"v_lshl_add_u32 v55, v32, 3, v33\n" \
"v_mfma_f32_16x16x32_bf16 a[148:151], a[120:123], a[16:19], a[148:151]\n" \
"ds_read_b128 a[8:11], v5 offset:2048\n" \
"v_add_u32 v55, s22, v55\n" \
"v_mfma_f32_16x16x32_bf16 a[148:151], a[124:127], a[20:23], a[148:151]\n" \
"ds_read_b128 a[12:15], v6 offset:2048\n" \
"v_add_u32 v33, s56, v33\n" \
"v_mfma_f32_16x16x32_bf16 a[156:159], a[120:123], a[24:27], a[156:159]\n" \
"ds_read_b128 a[16:19], v5 offset:4096\n" \
"v_xor_b32 v32, 1, v32\n" \
"v_mfma_f32_16x16x32_bf16 a[156:159], a[124:127], a[28:31], a[156:159]\n" \
"ds_read_b128 a[20:23], v6 offset:4096\n" \
"v_lshl_add_u32 v51, v32, 3, v33\n" \
"v_mfma_f32_16x16x32_bf16 a[164:167], a[120:123], a[32:35], a[164:167]\n" \
"ds_read_b128 a[24:27], v5 offset:6144\n" \
"v_mfma_f32_16x16x32_bf16 a[164:167], a[124:127], a[36:39], a[164:167]\n" \
"ds_read_b128 a[28:31], v6 offset:6144\n" \
"v_mfma_f32_16x16x32_bf16 a[172:175], a[120:123], a[40:43], a[172:175]\n" \
"ds_read_b128 a[32:35], v5 offset:8192\n" \
"v_mfma_f32_16x16x32_bf16 a[172:175], a[124:127], a[44:47], a[172:175]\n" \
"ds_read_b128 a[36:39], v6 offset:8192\n" \
"v_mfma_f32_16x16x32_bf16 a[180:183], a[120:123], a[48:51], a[180:183]\n" \
"ds_read_b128 a[40:43], v5 offset:10240\n" \
"v_mfma_f32_16x16x32_bf16 a[180:183], a[124:127], a[52:55], a[180:183]\n" \
"ds_read_b128 a[44:47], v6 offset:10240\n" \
"v_mfma_f32_16x16x32_bf16 a[188:191], a[120:123], a[56:59], a[188:191]\n" \
"ds_read_b128 a[48:51], v5 offset:12288\n" \
"v_mfma_f32_16x16x32_bf16 a[188:191], a[124:127], a[60:63], a[188:191]\n" \
"s_waitcnt lgkmcnt(9)\n" \
"ds_read_b128 a[52:55], v6 offset:12288\n" \
"v_mfma_f32_16x16x32_bf16 v[64:67], a[64:67], a[0:3], v[64:67]\n" \
"s_add_u32 m0, s50, 77824\n" \
"ds_read_b128 a[56:59], v5 offset:14336\n" \
"v_mfma_f32_16x16x32_bf16 v[64:67], a[68:71], a[4:7], v[64:67]\n" \
"buffer_load_dwordx4 v16, s[28:31], s46 offen lds\n" \
"v_mfma_f32_16x16x32_bf16 v[72:75], a[64:67], a[8:11], v[72:75]\n" \
"s_waitcnt lgkmcnt(7)\n" \
"ds_read_b128 a[60:63], v6 offset:14336\n" \
"v_mfma_f32_16x16x32_bf16 v[72:75], a[68:71], a[12:15], v[72:75]\n" \
"v_mfma_f32_16x16x32_bf16 v[80:83], a[64:67], a[16:19], v[80:83]\n" \
"s_mov_b32 s46, 0\n" \
"s_add_u32 m0, s50, 81920\n" \
"ds_read_b64_tr_b16 a[96:97], v9 offset:49152\n" \
"v_mfma_f32_16x16x32_bf16 v[80:83], a[68:71], a[20:23], v[80:83]\n" \
"buffer_load_dwordx4 v17, s[28:31], s46 offen lds\n" \
"v_mfma_f32_16x16x32_bf16 v[88:91], a[64:67], a[24:27], v[88:91]\n" \
"s_waitcnt lgkmcnt(5)\n" \
"ds_read_b64_tr_b16 a[98:99], v10 offset:49152\n" \
"v_mfma_f32_16x16x32_bf16 v[88:91], a[68:71], a[28:31], v[88:91]\n" \
"v_mfma_f32_16x16x32_bf16 v[96:99], a[64:67], a[32:35], v[96:99]\n" \
"s_add_u32 m0, s50, 86016\n" \
"ds_read_b64_tr_b16 a[100:101], v9 offset:50176\n" \
"v_mfma_f32_16x16x32_bf16 v[96:99], a[68:71], a[36:39], v[96:99]\n" \
"buffer_load_dwordx4 v18, s[28:31], s46 offen lds\n" \
"v_mfma_f32_16x16x32_bf16 v[104:107], a[64:67], a[40:43], v[104:107]\n" \
"s_waitcnt lgkmcnt(3)\n" \
"ds_read_b64_tr_b16 a[102:103], v10 offset:50176\n" \
"v_mfma_f32_16x16x32_bf16 v[104:107], a[68:71], a[44:47], v[104:107]\n" \
"v_mfma_f32_16x16x32_bf16 v[112:115], a[64:67], a[48:51], v[112:115]\n" \
"s_add_u32 m0, s50, 90112\n" \
"ds_read_b64_tr_b16 a[104:105], v11 offset:49152\n" \
"v_mfma_f32_16x16x32_bf16 v[112:115], a[68:71], a[52:55], v[112:115]\n" \
"buffer_load_dwordx4 v19, s[28:31], s46 offen lds\n" \
"v_mfma_f32_16x16x32_bf16 v[120:123], a[64:67], a[56:59], v[120:123]\n" \
"ds_read_b64_tr_b16 a[106:107], v12 offset:49152\n" \
"v_mfma_f32_16x16x32_bf16 v[120:123], a[68:71], a[60:63], v[120:123]\n" \
"v_mfma_f32_16x16x32_bf16 v[68:71], a[72:75], a[0:3], v[68:71]\n" \
"s_add_u32 m0, s50, 94208\n" \
"ds_read_b64_tr_b16 a[108:109], v11 offset:50176\n" \
"v_mfma_f32_16x16x32_bf16 v[68:71], a[76:79], a[4:7], v[68:71]\n" \
"buffer_load_dwordx4 v20, s[28:31], s46 offen lds\n" \
"v_mfma_f32_16x16x32_bf16 v[76:79], a[72:75], a[8:11], v[76:79]\n" \
"ds_read_b64_tr_b16 a[110:111], v12 offset:50176\n" \
"v_mfma_f32_16x16x32_bf16 v[76:79], a[76:79], a[12:15], v[76:79]\n" \
"v_mfma_f32_16x16x32_bf16 v[84:87], a[72:75], a[16:19], v[84:87]\n" \
"s_mov_b32 s47, 0\n" \
"s_add_u32 m0, s51, 32768\n" \
"ds_read_b64_tr_b16 a[112:113], v9 offset:49664\n" \
"v_mfma_f32_16x16x32_bf16 v[84:87], a[76:79], a[20:23], v[84:87]\n" \
"buffer_load_dwordx4 v21, s[32:35], s47 offen offset:0 lds\n" \
"v_mfma_f32_16x16x32_bf16 v[92:95], a[72:75], a[24:27], v[92:95]\n" \
"ds_read_b64_tr_b16 a[114:115], v10 offset:49664\n" \
"v_mfma_f32_16x16x32_bf16 v[92:95], a[76:79], a[28:31], v[92:95]\n" \
"v_mfma_f32_16x16x32_bf16 v[100:103], a[72:75], a[32:35], v[100:103]\n" \
"s_add_u32 m0, s51, 33664\n" \
"ds_read_b64_tr_b16 a[116:117], v9 offset:50688\n" \
"v_mfma_f32_16x16x32_bf16 v[100:103], a[76:79], a[36:39], v[100:103]\n" \
"buffer_load_dwordx4 v21, s[32:35], s47 offen offset:128 lds\n" \
"v_mfma_f32_16x16x32_bf16 v[108:111], a[72:75], a[40:43], v[108:111]\n" \
"ds_read_b64_tr_b16 a[118:119], v10 offset:50688\n" \
"v_mfma_f32_16x16x32_bf16 v[108:111], a[76:79], a[44:47], v[108:111]\n" \
"v_mfma_f32_16x16x32_bf16 v[116:119], a[72:75], a[48:51], v[116:119]\n" \
"s_mov_b32 s47, 262144\n" \
"s_add_u32 m0, s51, 40960\n" \
"ds_read_b64_tr_b16 a[120:121], v11 offset:49664\n" \
"v_mfma_f32_16x16x32_bf16 v[116:119], a[76:79], a[52:55], v[116:119]\n" \
"buffer_load_dwordx4 v21, s[32:35], s47 offen offset:0 lds\n" \
"v_mfma_f32_16x16x32_bf16 v[124:127], a[72:75], a[56:59], v[124:127]\n" \
"ds_read_b64_tr_b16 a[122:123], v12 offset:49664\n" \
"v_mfma_f32_16x16x32_bf16 v[124:127], a[76:79], a[60:63], v[124:127]\n" \
"v_mfma_f32_16x16x32_bf16 v[128:131], a[80:83], a[0:3], v[128:131]\n" \
"s_add_u32 m0, s51, 41856\n" \
"ds_read_b64_tr_b16 a[124:125], v11 offset:50688\n" \
"v_mfma_f32_16x16x32_bf16 v[128:131], a[84:87], a[4:7], v[128:131]\n" \
"buffer_load_dwordx4 v21, s[32:35], s47 offen offset:128 lds\n" \
"v_mfma_f32_16x16x32_bf16 v[136:139], a[80:83], a[8:11], v[136:139]\n" \
"s_mov_b32 s96, s57\n" \
"ds_read_b64_tr_b16 a[126:127], v12 offset:50688\n" \
"v_mul_f32 v64, s20, v64\n" \
"v_mfma_f32_16x16x32_bf16 v[136:139], a[84:87], a[12:15], v[136:139]\n" \
"v_mul_f32 v65, s20, v65\n" \
"v_mul_f32 v66, s20, v66\n" \
"v_mfma_f32_16x16x32_bf16 v[144:147], a[80:83], a[16:19], v[144:147]\n" \
"v_mul_f32 v67, s20, v67\n" \
"v_cvt_pk_bf16_f32 v30, v64, v65\n" \
"v_mfma_f32_16x16x32_bf16 v[144:147], a[84:87], a[20:23], v[144:147]\n" \
"v_cvt_pk_bf16_f32 v31, v66, v67\n" \
"v_mfma_f32_16x16x32_bf16 v[152:155], a[80:83], a[24:27], v[152:155]\n" \
"v_mul_f32 v68, s20, v68\n" \
"v_mul_f32 v69, s20, v69\n" \
"v_mfma_f32_16x16x32_bf16 v[152:155], a[84:87], a[28:31], v[152:155]\n" \
"v_mul_f32 v70, s20, v70\n" \
"v_mul_f32 v71, s20, v71\n" \
"v_mfma_f32_16x16x32_bf16 v[160:163], a[80:83], a[32:35], v[160:163]\n" \
"v_cvt_pk_bf16_f32 v32, v68, v69\n" \
"v_mfma_f32_16x16x32_bf16 v[160:163], a[84:87], a[36:39], v[160:163]\n" \
"v_cvt_pk_bf16_f32 v33, v70, v71\n" \
"v_mul_f32 v72, s20, v72\n" \
"v_mfma_f32_16x16x32_bf16 v[168:171], a[80:83], a[40:43], v[168:171]\n" \
"buffer_store_dwordx4 v[30:33], v4, s[36:39], s96 offen offset:0 sc1\n" \
"v_mfma_f32_16x16x32_bf16 v[168:171], a[84:87], a[44:47], v[168:171]\n" \
"s_waitcnt lgkmcnt(0)\n" \
"s_add_u32 s97, s96, s98\n" \
"v_mul_f32 v73, s20, v73\n" \
"v_mfma_f32_16x16x32_bf16 v[176:179], a[80:83], a[48:51], v[176:179]\n" \
"s_barrier\n" \
"v_mul_f32 v74, s20, v74\n" \
"v_mul_f32 v75, s20, v75\n" \
"v_mfma_f32_16x16x32_bf16 v[176:179], a[84:87], a[52:55], v[176:179]\n" \
"v_cvt_pk_bf16_f32 v34, v72, v73\n" \
"v_cvt_pk_bf16_f32 v35, v74, v75\n" \
"v_mfma_f32_16x16x32_bf16 v[184:187], a[80:83], a[56:59], v[184:187]\n" \
"v_mul_f32 v76, s20, v76\n" \
"v_mfma_f32_16x16x32_bf16 v[184:187], a[84:87], a[60:63], v[184:187]\n" \
"v_add_u32 v5, s54, v5\n" \
"v_mul_f32 v77, s20, v77\n" \
"v_mfma_f32_16x16x32_bf16 v[132:135], a[88:91], a[0:3], v[132:135]\n" \
"v_mul_f32 v78, s20, v78\n" \
"v_mul_f32 v79, s20, v79\n" \
"v_mfma_f32_16x16x32_bf16 v[132:135], a[92:95], a[4:7], v[132:135]\n" \
"v_cvt_pk_bf16_f32 v36, v76, v77\n" \
"v_mfma_f32_16x16x32_bf16 v[140:143], a[88:91], a[8:11], v[140:143]\n" \
"v_add_u32 v6, s54, v6\n" \
"v_cvt_pk_bf16_f32 v37, v78, v79\n" \
"v_mfma_f32_16x16x32_bf16 v[140:143], a[92:95], a[12:15], v[140:143]\n" \
"v_add_u32 v7, s54, v7\n" \
"v_mul_f32 v80, s20, v80\n" \
"v_mfma_f32_16x16x32_bf16 v[148:151], a[88:91], a[16:19], v[148:151]\n" \
"v_mul_f32 v81, s20, v81\n" \
"v_mfma_f32_16x16x32_bf16 v[148:151], a[92:95], a[20:23], v[148:151]\n" \
"v_mul_f32 v82, s20, v82\n" \
"v_mul_f32 v83, s20, v83\n" \
"v_mfma_f32_16x16x32_bf16 v[156:159], a[88:91], a[24:27], v[156:159]\n" \
"v_add_u32 v8, s54, v8\n" \
"v_cvt_pk_bf16_f32 v38, v80, v81\n" \
"v_mfma_f32_16x16x32_bf16 v[156:159], a[92:95], a[28:31], v[156:159]\n" \
"v_sub_u32 v9, v9, v62\n" \
"v_mfma_f32_16x16x32_bf16 v[164:167], a[88:91], a[32:35], v[164:167]\n" \
"buffer_store_dwordx4 v[34:37], v4, s[36:39], s97 offen offset:0 sc1\n" \
"v_mfma_f32_16x16x32_bf16 v[164:167], a[92:95], a[36:39], v[164:167]\n" \
"s_add_u32 s96, s96, s92\n" \
"v_cvt_pk_bf16_f32 v39, v82, v83\n" \
"v_mul_f32 v84, s20, v84\n" \
"v_mfma_f32_16x16x32_bf16 v[172:175], a[88:91], a[40:43], v[172:175]\n" \
"v_sub_u32 v10, v10, v62\n" \
"v_mul_f32 v85, s20, v85\n" \
"v_mfma_f32_16x16x32_bf16 v[172:175], a[92:95], a[44:47], v[172:175]\n" \
"v_sub_u32 v11, v11, v62\n" \
"v_mfma_f32_16x16x32_bf16 v[180:183], a[88:91], a[48:51], v[180:183]\n" \
"v_mul_f32 v86, s20, v86\n" \
"v_mul_f32 v87, s20, v87\n" \
"v_mfma_f32_16x16x32_bf16 v[180:183], a[92:95], a[52:55], v[180:183]\n" \
"v_cvt_pk_bf16_f32 v40, v84, v85\n" \
"v_cvt_pk_bf16_f32 v41, v86, v87\n" \
"v_mfma_f32_16x16x32_bf16 v[188:191], a[88:91], a[56:59], v[188:191]\n" \
"v_sub_u32 v12, v12, v62\n" \
"v_mfma_f32_16x16x32_bf16 v[188:191], a[92:95], a[60:63], v[188:191]\n" \
"v_mul_f32 v88, s20, v88\n" \
"v_mul_f32 v89, s20, v89\n" \
"v_mfma_f32_16x16x32_bf16 v[192:195], a[96:99], a[0:3], v[192:195]\n" \
"s_mov_b32 s46, 128\n" \
"s_add_u32 m0, s50, 0\n" \
"s_waitcnt vmcnt(14) lgkmcnt(0)\n" \
"v_mfma_f32_16x16x32_bf16 v[192:195], a[100:103], a[4:7], v[192:195]\n" \
"buffer_load_dwordx4 v13, s[28:31], s46 offen lds\n" \
"v_mfma_f32_16x16x32_bf16 v[200:203], a[96:99], a[8:11], v[200:203]\n" \
"ds_read_b128 a[200:203], v50 offset:512\n" \
"v_mul_f32 v90, s20, v90\n" \
"v_mfma_f32_16x16x32_bf16 v[200:203], a[100:103], a[12:15], v[200:203]\n" \
"v_mul_f32 v91, s20, v91\n" \
"v_cvt_pk_bf16_f32 v42, v88, v89\n" \
"v_mfma_f32_16x16x32_bf16 v[208:211], a[96:99], a[16:19], v[208:211]\n" \
"ds_read2_b32 a[64:65], v46 offset0:0 offset1:1\n" \
"v_cvt_pk_bf16_f32 v43, v90, v91\n" \
"v_mfma_f32_16x16x32_bf16 v[208:211], a[100:103], a[20:23], v[208:211]\n" \
"v_mul_f32 v92, s20, v92\n" \
"v_mul_f32 v93, s20, v93\n" \
"v_mfma_f32_16x16x32_bf16 v[216:219], a[96:99], a[24:27], v[216:219]\n" \
"ds_read2_b32 a[66:67], v46 offset0:2 offset1:3\n" \
"v_mul_f32 v94, s20, v94\n" \
"v_mfma_f32_16x16x32_bf16 v[216:219], a[100:103], a[28:31], v[216:219]\n" \
"buffer_store_dwordx4 v[38:41], v4, s[36:39], s96 offen offset:0 sc1\n" \
"v_mfma_f32_16x16x32_bf16 v[224:227], a[96:99], a[32:35], v[224:227]\n" \
"s_add_u32 m0, s50, 4096\n" \
"ds_read_b128 a[204:207], v50 offset:1024\n" \
"s_add_u32 s97, s96, s98\n" \
"v_mfma_f32_16x16x32_bf16 v[224:227], a[100:103], a[36:39], v[224:227]\n" \
"LH3_%=:\ns_cbranch_execz LH11_%=\n" \
"buffer_load_dwordx4 v14, s[28:31], s46 offen lds\n" \
"v_mfma_f32_16x16x32_bf16 v[232:235], a[96:99], a[40:43], v[232:235]\n" \
"ds_read2_b32 a[68:69], v46 offset0:128 offset1:129\n" \
"v_mul_f32 v95, s20, v95\n" \
"v_mfma_f32_16x16x32_bf16 v[232:235], a[100:103], a[44:47], v[232:235]\n" \
"v_cvt_pk_bf16_f32 v44, v92, v93\n" \
"v_cvt_pk_bf16_f32 v45, v94, v95\n" \
"v_mfma_f32_16x16x32_bf16 v[240:243], a[96:99], a[48:51], v[240:243]\n" \
"ds_read2_b32 a[70:71], v46 offset0:130 offset1:131\n" \
"v_mul_f32 v96, s20, v96\n" \
"v_mfma_f32_16x16x32_bf16 v[240:243], a[100:103], a[52:55], v[240:243]\n" \
"v_mul_f32 v97, s20, v97\n" \
"v_mul_f32 v98, s20, v98\n" \
"v_mfma_f32_16x16x32_bf16 v[248:251], a[96:99], a[56:59], v[248:251]\n" \
"ds_read_b128 a[208:211], v50 offset:1536\n" \
"v_mul_f32 v99, s20, v99\n" \
"v_mfma_f32_16x16x32_bf16 v[248:251], a[100:103], a[60:63], v[248:251]\n" \
"v_cvt_pk_bf16_f32 v30, v96, v97\n" \
"v_cvt_pk_bf16_f32 v31, v98, v99\n" \
"v_mfma_f32_16x16x32_bf16 v[196:199], a[104:107], a[0:3], v[196:199]\n" \
"s_add_u32 m0, s50, 8192\n" \
"ds_read2_b32 a[72:73], v47 offset0:0 offset1:1\n" \
"v_mul_f32 v100, s20, v100\n" \
"v_mfma_f32_16x16x32_bf16 v[196:199], a[108:111], a[4:7], v[196:199]\n" \
"buffer_load_dwordx4 v15, s[28:31], s46 offen lds\n" \
"v_mfma_f32_16x16x32_bf16 v[204:207], a[104:107], a[8:11], v[204:207]\n" \
"ds_read2_b32 a[74:75], v47 offset0:2 offset1:3\n" \
"v_mul_f32 v101, s20, v101\n" \
"v_mfma_f32_16x16x32_bf16 v[204:207], a[108:111], a[12:15], v[204:207]\n" \
"v_mul_f32 v102, s20, v102\n" \
"v_mul_f32 v103, s20, v103\n" \
"v_mfma_f32_16x16x32_bf16 v[212:215], a[104:107], a[16:19], v[212:215]\n" \
"ds_read_b128 a[212:215], v50 offset:2048\n" \
"v_cvt_pk_bf16_f32 v32, v100, v101\n" \
"v_mfma_f32_16x16x32_bf16 v[212:215], a[108:111], a[20:23], v[212:215]\n" \
"buffer_store_dwordx4 v[42:45], v4, s[36:39], s97 offen offset:0 sc1\n" \
"v_mfma_f32_16x16x32_bf16 v[220:223], a[104:107], a[24:27], v[220:223]\n" \
"s_add_u32 s96, s96, s92\n" \
"ds_read2_b32 a[76:77], v47 offset0:128 offset1:129\n" \
"v_cvt_pk_bf16_f32 v33, v102, v103\n" \
"v_mfma_f32_16x16x32_bf16 v[220:223], a[108:111], a[28:31], v[220:223]\n" \
"v_mul_f32 v104, s20, v104\n" \
"v_mul_f32 v105, s20, v105\n" \
"v_mfma_f32_16x16x32_bf16 v[228:231], a[104:107], a[32:35], v[228:231]\n" \
"s_add_u32 m0, s50, 12288\n" \
"ds_read2_b32 a[78:79], v47 offset0:130 offset1:131\n" \
"v_mul_f32 v106, s20, v106\n" \
"v_mfma_f32_16x16x32_bf16 v[228:231], a[108:111], a[36:39], v[228:231]\n" \
"buffer_load_dwordx4 v16, s[28:31], s46 offen lds\n" \
"v_mfma_f32_16x16x32_bf16 v[236:239], a[104:107], a[40:43], v[236:239]\n" \
"ds_read_b128 a[216:219], v50 offset:2560\n" \
"v_mul_f32 v107, s20, v107\n" \
"v_mfma_f32_16x16x32_bf16 v[236:239], a[108:111], a[44:47], v[236:239]\n" \
"v_cvt_pk_bf16_f32 v34, v104, v105\n" \
"v_cvt_pk_bf16_f32 v35, v106, v107\n" \
"v_mfma_f32_16x16x32_bf16 v[244:247], a[104:107], a[48:51], v[244:247]\n" \
"ds_read2_b32 a[80:81], v48 offset0:0 offset1:1\n" \
"v_mul_f32 v108, s20, v108\n" \
"v_mfma_f32_16x16x32_bf16 v[244:247], a[108:111], a[52:55], v[244:247]\n" \
"v_mul_f32 v109, s20, v109\n" \
"v_mul_f32 v110, s20, v110\n" \
"v_mfma_f32_16x16x32_bf16 v[252:255], a[104:107], a[56:59], v[252:255]\n" \
"ds_read2_b32 a[82:83], v48 offset0:2 offset1:3\n" \
"v_mul_f32 v111, s20, v111\n" \
"v_mfma_f32_16x16x32_bf16 v[252:255], a[108:111], a[60:63], v[252:255]\n" \
"v_cvt_pk_bf16_f32 v36, v108, v109\n" \
"v_cvt_pk_bf16_f32 v37, v110, v111\n" \
"v_mfma_f32_16x16x32_bf16 a[128:131], a[112:115], a[0:3], a[128:131]\n" \
"s_mov_b32 s46, 128\n" \
"s_add_u32 m0, s50, 16384\n" \
"ds_read_b128 a[220:223], v50 offset:3072\n" \
"v_mfma_f32_16x16x32_bf16 a[128:131], a[116:119], a[4:7], a[128:131]\n" \
"buffer_load_dwordx4 v17, s[28:31], s46 offen lds\n" \
"v_mfma_f32_16x16x32_bf16 a[136:139], a[112:115], a[8:11], a[136:139]\n" \
"ds_read2_b32 a[84:85], v48 offset0:128 offset1:129\n" \
"v_mul_f32 v112, s20, v112\n" \
"v_mfma_f32_16x16x32_bf16 a[136:139], a[116:119], a[12:15], a[136:139]\n" \
"buffer_store_dwordx4 v[30:33], v4, s[36:39], s96 offen offset:0 sc1\n" \
"v_mfma_f32_16x16x32_bf16 a[144:147], a[112:115], a[16:19], a[144:147]\n" \
"s_add_u32 s97, s96, s98\n" \
"ds_read2_b32 a[86:87], v48 offset0:130 offset1:131\n" \
"v_mul_f32 v113, s20, v113\n" \
"v_mfma_f32_16x16x32_bf16 a[144:147], a[116:119], a[20:23], a[144:147]\n" \
"v_mul_f32 v114, s20, v114\n" \
"v_mul_f32 v115, s20, v115\n" \
"v_mfma_f32_16x16x32_bf16 a[152:155], a[112:115], a[24:27], a[152:155]\n" \
"ds_read_b128 a[224:227], v50 offset:3584\n" \
"v_cvt_pk_bf16_f32 v38, v112, v113\n" \
"v_mfma_f32_16x16x32_bf16 a[152:155], a[116:119], a[28:31], a[152:155]\n" \
"v_cvt_pk_bf16_f32 v39, v114, v115\n" \
"v_mul_f32 v116, s20, v116\n" \
"v_mfma_f32_16x16x32_bf16 a[160:163], a[112:115], a[32:35], a[160:163]\n" \
"s_add_u32 m0, s50, 20480\n" \
"ds_read2_b32 a[88:89], v49 offset0:0 offset1:1\n" \
"v_mul_f32 v117, s20, v117\n" \
"v_mfma_f32_16x16x32_bf16 a[160:163], a[116:119], a[36:39], a[160:163]\n" \
"buffer_load_dwordx4 v18, s[28:31], s46 offen lds\n" \
"v_mfma_f32_16x16x32_bf16 a[168:171], a[112:115], a[40:43], a[168:171]\n" \
"ds_read2_b32 a[90:91], v49 offset0:2 offset1:3\n" \
"v_mul_f32 v118, s20, v118\n" \
"v_mfma_f32_16x16x32_bf16 a[168:171], a[116:119], a[44:47], a[168:171]\n" \
"v_mul_f32 v119, s20, v119\n" \
"v_cvt_pk_bf16_f32 v40, v116, v117\n" \
"v_mfma_f32_16x16x32_bf16 a[176:179], a[112:115], a[48:51], a[176:179]\n" \
"ds_read_b128 a[228:231], v50 offset:4096\n" \
"v_cvt_pk_bf16_f32 v41, v118, v119\n" \
"v_mfma_f32_16x16x32_bf16 a[176:179], a[116:119], a[52:55], a[176:179]\n" \
"v_mul_f32 v120, s20, v120\n" \
"v_mul_f32 v121, s20, v121\n" \
"v_mfma_f32_16x16x32_bf16 a[184:187], a[112:115], a[56:59], a[184:187]\n" \
"ds_read2_b32 a[92:93], v49 offset0:128 offset1:129\n" \
"v_mul_f32 v122, s20, v122\n" \
"v_mfma_f32_16x16x32_bf16 a[184:187], a[116:119], a[60:63], a[184:187]\n" \
"v_mul_f32 v123, s20, v123\n" \
"v_cvt_pk_bf16_f32 v42, v120, v121\n" \
"v_mfma_f32_16x16x32_bf16 a[132:135], a[120:123], a[0:3], a[132:135]\n" \
"s_add_u32 m0, s50, 24576\n" \
"ds_read2_b32 a[94:95], v49 offset0:130 offset1:131\n" \
"v_cvt_pk_bf16_f32 v43, v122, v123\n" \
"v_mfma_f32_16x16x32_bf16 a[132:135], a[124:127], a[4:7], a[132:135]\n" \
"buffer_load_dwordx4 v19, s[28:31], s46 offen lds\n" \
"v_mfma_f32_16x16x32_bf16 a[140:143], a[120:123], a[8:11], a[140:143]\n" \
"v_mul_f32 v124, s20, v124\n" \
"v_mul_f32 v125, s20, v125\n" \
"v_mfma_f32_16x16x32_bf16 a[140:143], a[124:127], a[12:15], a[140:143]\n" \
"buffer_store_dwordx4 v[34:37], v4, s[36:39], s97 offen offset:0 sc1\n" \
"v_mfma_f32_16x16x32_bf16 a[148:151], a[120:123], a[16:19], a[148:151]\n" \
"s_add_u32 s96, s96, s92\n" \
"v_mul_f32 v126, s20, v126\n" \
"v_mul_f32 v127, s20, v127\n" \
"v_mfma_f32_16x16x32_bf16 a[148:151], a[124:127], a[20:23], a[148:151]\n" \
"v_cvt_pk_bf16_f32 v44, v124, v125\n" \
"v_cvt_pk_bf16_f32 v45, v126, v127\n" \
"v_mfma_f32_16x16x32_bf16 a[156:159], a[120:123], a[24:27], a[156:159]\n" \
"v_mul_f32 v128, s20, v128\n" \
"v_mfma_f32_16x16x32_bf16 a[156:159], a[124:127], a[28:31], a[156:159]\n" \
"v_mul_f32 v129, s20, v129\n" \
"v_mul_f32 v130, s20, v130\n" \
"v_mfma_f32_16x16x32_bf16 a[164:167], a[120:123], a[32:35], a[164:167]\n" \
"s_add_u32 m0, s50, 28672\n" \
"v_mul_f32 v131, s20, v131\n" \
"v_cvt_pk_bf16_f32 v30, v128, v129\n" \
"v_mfma_f32_16x16x32_bf16 a[164:167], a[124:127], a[36:39], a[164:167]\n" \
"buffer_load_dwordx4 v20, s[28:31], s46 offen lds\n" \
"v_mfma_f32_16x16x32_bf16 a[172:175], a[120:123], a[40:43], a[172:175]\n" \
"v_cvt_pk_bf16_f32 v31, v130, v131\n" \
"v_mul_f32 v132, s20, v132\n" \
"v_mfma_f32_16x16x32_bf16 a[172:175], a[124:127], a[44:47], a[172:175]\n" \
"v_mul_f32 v133, s20, v133\n" \
"v_mul_f32 v134, s20, v134\n" \
"v_mfma_f32_16x16x32_bf16 a[180:183], a[120:123], a[48:51], a[180:183]\n" \
"v_mul_f32 v135, s20, v135\n" \
"v_mfma_f32_16x16x32_bf16 a[180:183], a[124:127], a[52:55], a[180:183]\n" \
"v_cvt_pk_bf16_f32 v32, v132, v133\n" \
"v_cvt_pk_bf16_f32 v33, v134, v135\n" \
"v_mfma_f32_16x16x32_bf16 a[188:191], a[120:123], a[56:59], a[188:191]\n" \
"s_waitcnt lgkmcnt(0)\n" \
"v_mul_f32 v136, s20, v136\n" \
"v_mul_f32 v137, s20, v137\n" \
"v_mfma_f32_16x16x32_bf16 a[188:191], a[124:127], a[60:63], a[188:191]\n" \
"v_mul_f32 v138, s20, v138\n" \
"s_branch LC_%=\n" \
"LAV_%=:\n" \
"v_mfma_f32_16x16x32_bf16 v[64:67], a[0:3], a[64:67], 0\n" \
"s_mov_b32 s47, 524288\n" \
"s_add_u32 m0, s22, 49024\n" \
"v_mfma_f32_16x16x32_bf16 v[64:67], a[4:7], a[68:71], v[64:67]\n" \
"buffer_load_dwordx4 v21, s[32:35], s47 offen offset:128 nt lds\n" \
"v_mfma_f32_16x16x32_bf16 v[68:71], a[8:11], a[64:67], 0\n" \
"s_add_u32 s14, s14, 1\ns_addc_u32 s15, s15, 0\n" \
"v_mfma_f32_16x16x32_bf16 v[68:71], a[12:15], a[68:71], v[68:71]\n" \
"v_mfma_f32_16x16x32_bf16 v[80:83], a[16:19], a[64:67], 0\n" \
"s_add_u32 m0, s22, 50816\n" \
"ds_read_b64_tr_b16 a[96:97], v9 offset:16384\n" \
"v_mfma_f32_16x16x32_bf16 v[80:83], a[20:23], a[68:71], v[80:83]\n" \
"buffer_load_dwordx4 v21, s[32:35], s47 offen offset:384 nt lds\n" \
"v_mfma_f32_16x16x32_bf16 v[84:87], a[24:27], a[64:67], 0\n" \
"ds_read_b64_tr_b16 a[98:99], v10 offset:16384\n" \
"v_mfma_f32_16x16x32_bf16 v[84:87], a[28:31], a[68:71], v[84:87]\n" \
"v_mfma_f32_16x16x32_bf16 v[96:99], a[32:35], a[64:67], 0\n" \
"s_mov_b32 s47, 786432\n" \
"s_add_u32 m0, s22, 50048\n" \
"ds_read_b64_tr_b16 a[100:101], v9 offset:17408\n" \
"v_mfma_f32_16x16x32_bf16 v[96:99], a[36:39], a[68:71], v[96:99]\n" \
"buffer_load_dwordx4 v21, s[32:35], s47 offen offset:128 nt lds\n" \
"v_mfma_f32_16x16x32_bf16 v[100:103], a[40:43], a[64:67], 0\n" \
"ds_read_b64_tr_b16 a[102:103], v10 offset:17408\n" \
"v_mfma_f32_16x16x32_bf16 v[100:103], a[44:47], a[68:71], v[100:103]\n" \
"v_mfma_f32_16x16x32_bf16 v[112:115], a[48:51], a[64:67], 0\n" \
"s_add_u32 m0, s22, 51840\n" \
"ds_read_b64_tr_b16 a[104:105], v11 offset:16384\n" \
"v_mfma_f32_16x16x32_bf16 v[112:115], a[52:55], a[68:71], v[112:115]\n" \
"buffer_load_dwordx4 v21, s[32:35], s47 offen offset:384 nt lds\n" \
"v_mfma_f32_16x16x32_bf16 v[116:119], a[56:59], a[64:67], 0\n" \
"ds_read_b64_tr_b16 a[106:107], v12 offset:16384\n" \
"v_mfma_f32_16x16x32_bf16 v[116:119], a[60:63], a[68:71], v[116:119]\n" \
"v_mfma_f32_16x16x32_bf16 v[72:75], a[0:3], a[72:75], 0\n" \
"s_mov_b32 s46, 256\n" \
"s_add_u32 m0, s50, 32768\n" \
"ds_read_b64_tr_b16 a[108:109], v11 offset:17408\n" \
"v_mfma_f32_16x16x32_bf16 v[72:75], a[4:7], a[76:79], v[72:75]\n" \
"buffer_load_dwordx4 v13, s[28:31], s46 offen lds\n" \
"v_mfma_f32_16x16x32_bf16 v[76:79], a[8:11], a[72:75], 0\n" \
"ds_read_b64_tr_b16 a[110:111], v12 offset:17408\n" \
"v_mfma_f32_16x16x32_bf16 v[76:79], a[12:15], a[76:79], v[76:79]\n" \
"v_mfma_f32_16x16x32_bf16 v[88:91], a[16:19], a[72:75], 0\n" \
"s_add_u32 m0, s50, 36864\n" \
"ds_read_b64_tr_b16 a[112:113], v9 offset:16896\n" \
"v_mfma_f32_16x16x32_bf16 v[88:91], a[20:23], a[76:79], v[88:91]\n" \
"buffer_load_dwordx4 v14, s[28:31], s46 offen lds\n" \
"v_mfma_f32_16x16x32_bf16 v[92:95], a[24:27], a[72:75], 0\n" \
"ds_read_b64_tr_b16 a[114:115], v10 offset:16896\n" \
"v_mfma_f32_16x16x32_bf16 v[92:95], a[28:31], a[76:79], v[92:95]\n" \
"v_mfma_f32_16x16x32_bf16 v[104:107], a[32:35], a[72:75], 0\n" \
"s_add_u32 m0, s50, 40960\n" \
"ds_read_b64_tr_b16 a[116:117], v9 offset:17920\n" \
"v_mfma_f32_16x16x32_bf16 v[104:107], a[36:39], a[76:79], v[104:107]\n" \
"buffer_load_dwordx4 v15, s[28:31], s46 offen lds\n" \
"v_mfma_f32_16x16x32_bf16 v[108:111], a[40:43], a[72:75], 0\n" \
"ds_read_b64_tr_b16 a[118:119], v10 offset:17920\n" \
"v_mfma_f32_16x16x32_bf16 v[108:111], a[44:47], a[76:79], v[108:111]\n" \
"v_mfma_f32_16x16x32_bf16 v[120:123], a[48:51], a[72:75], 0\n" \
"s_add_u32 m0, s50, 45056\n" \
"ds_read_b64_tr_b16 a[120:121], v11 offset:16896\n" \
"v_mfma_f32_16x16x32_bf16 v[120:123], a[52:55], a[76:79], v[120:123]\n" \
"buffer_load_dwordx4 v16, s[28:31], s46 offen lds\n" \
"v_mfma_f32_16x16x32_bf16 v[124:127], a[56:59], a[72:75], 0\n" \
"ds_read_b64_tr_b16 a[122:123], v12 offset:16896\n" \
"v_mfma_f32_16x16x32_bf16 v[124:127], a[60:63], a[76:79], v[124:127]\n" \
"v_mfma_f32_16x16x32_bf16 v[128:131], a[0:3], a[80:83], 0\n" \
"s_mov_b32 s46, 256\n" \
"s_add_u32 m0, s50, 49152\n" \
"ds_read_b64_tr_b16 a[124:125], v11 offset:17920\n" \
"v_mfma_f32_16x16x32_bf16 v[128:131], a[4:7], a[84:87], v[128:131]\n" \
"buffer_load_dwordx4 v17, s[28:31], s46 offen lds\n" \
"v_mfma_f32_16x16x32_bf16 v[132:135], a[8:11], a[80:83], 0\n" \
"ds_read_b64_tr_b16 a[126:127], v12 offset:17920\n" \
"v_mfma_f32_16x16x32_bf16 v[132:135], a[12:15], a[84:87], v[132:135]\n" \
"v_mfma_f32_16x16x32_bf16 v[144:147], a[16:19], a[80:83], 0\n" \
"s_add_u32 m0, s50, 53248\n" \
"v_mfma_f32_16x16x32_bf16 v[144:147], a[20:23], a[84:87], v[144:147]\n" \
"buffer_load_dwordx4 v18, s[28:31], s46 offen lds\n" \
"v_mfma_f32_16x16x32_bf16 v[148:151], a[24:27], a[80:83], 0\n" \
"v_mfma_f32_16x16x32_bf16 v[148:151], a[28:31], a[84:87], v[148:151]\n" \
"v_mfma_f32_16x16x32_bf16 v[160:163], a[32:35], a[80:83], 0\n" \
"s_add_u32 m0, s50, 57344\n" \
"v_mfma_f32_16x16x32_bf16 v[160:163], a[36:39], a[84:87], v[160:163]\n" \
"buffer_load_dwordx4 v19, s[28:31], s46 offen lds\n" \
"v_mfma_f32_16x16x32_bf16 v[164:167], a[40:43], a[80:83], 0\n" \
"v_mfma_f32_16x16x32_bf16 v[164:167], a[44:47], a[84:87], v[164:167]\n" \
"s_waitcnt vmcnt(21) lgkmcnt(0)\n" \
"v_mfma_f32_16x16x32_bf16 v[176:179], a[48:51], a[80:83], 0\n" \
"s_add_u32 m0, s50, 61440\n" \
"s_barrier\n" \
"v_mfma_f32_16x16x32_bf16 v[176:179], a[52:55], a[84:87], v[176:179]\n" \
"buffer_load_dwordx4 v20, s[28:31], s46 offen lds\n" \
"v_mfma_f32_16x16x32_bf16 v[180:183], a[56:59], a[80:83], 0\n" \
"v_mfma_f32_16x16x32_bf16 v[180:183], a[60:63], a[84:87], v[180:183]\n" \
"v_mfma_f32_16x16x32_bf16 v[136:139], a[0:3], a[88:91], 0\n" \
"s_mov_b32 s47, 1048576\n" \
"s_add_u32 m0, s22, 0\n" \
"v_mfma_f32_16x16x32_bf16 v[136:139], a[4:7], a[92:95], v[136:139]\n" \
"buffer_load_dwordx4 v21, s[32:35], s47 offen offset:0 lds\n" \
"v_mfma_f32_16x16x32_bf16 v[140:143], a[8:11], a[88:91], 0\n" \
"v_mfma_f32_16x16x32_bf16 v[140:143], a[12:15], a[92:95], v[140:143]\n" \
"v_mfma_f32_16x16x32_bf16 v[152:155], a[16:19], a[88:91], 0\n" \
"s_add_u32 m0, s22, 1792\n" \
"v_mfma_f32_16x16x32_bf16 v[152:155], a[20:23], a[92:95], v[152:155]\n" \
"buffer_load_dwordx4 v21, s[32:35], s47 offen offset:256 lds\n" \
"v_mfma_f32_16x16x32_bf16 v[156:159], a[24:27], a[88:91], 0\n" \
"v_mfma_f32_16x16x32_bf16 v[156:159], a[28:31], a[92:95], v[156:159]\n" \
"v_mfma_f32_16x16x32_bf16 v[168:171], a[32:35], a[88:91], 0\n" \
"s_mov_b32 s47, 1310720\n" \
"s_add_u32 m0, s22, 1024\n" \
"v_mfma_f32_16x16x32_bf16 v[168:171], a[36:39], a[92:95], v[168:171]\n" \
"buffer_load_dwordx4 v21, s[32:35], s47 offen offset:0 lds\n" \
"v_mfma_f32_16x16x32_bf16 v[172:175], a[40:43], a[88:91], 0\n" \
"v_mfma_f32_16x16x32_bf16 v[172:175], a[44:47], a[92:95], v[172:175]\n" \
"v_mfma_f32_16x16x32_bf16 v[184:187], a[48:51], a[88:91], 0\n" \
"s_add_u32 m0, s22, 2816\n" \
"v_mfma_f32_16x16x32_bf16 v[184:187], a[52:55], a[92:95], v[184:187]\n" \
"buffer_load_dwordx4 v21, s[32:35], s47 offen offset:256 lds\n" \
"v_mfma_f32_16x16x32_bf16 v[188:191], a[56:59], a[88:91], 0\n" \
"v_mfma_f32_16x16x32_bf16 v[188:191], a[60:63], a[92:95], v[188:191]\n" \
"v_mfma_f32_16x16x32_bf16 v[192:195], a[0:3], a[96:99], 0\n" \
"s_mov_b32 s47, 1048576\n" \
"s_add_u32 m0, s22, 16256\n" \
"v_mfma_f32_16x16x32_bf16 v[192:195], a[4:7], a[100:103], v[192:195]\n" \
"buffer_load_dwordx4 v21, s[32:35], s47 offen offset:128 lds\n" \
"v_mfma_f32_16x16x32_bf16 v[196:199], a[8:11], a[96:99], 0\n" \
"v_mfma_f32_16x16x32_bf16 v[196:199], a[12:15], a[100:103], v[196:199]\n" \
"v_mfma_f32_16x16x32_bf16 v[208:211], a[16:19], a[96:99], 0\n" \
"s_mov_b32 s46, 384\n" \
"s_add_u32 m0, s50, 65536\n" \
"ds_read_b64_tr_b16 a[64:65], v9 offset:32768\n" \
"v_mfma_f32_16x16x32_bf16 v[208:211], a[20:23], a[100:103], v[208:211]\n" \
"buffer_load_dwordx4 v13, s[28:31], s46 offen lds\n" \
"v_mfma_f32_16x16x32_bf16 v[212:215], a[24:27], a[96:99], 0\n" \
"ds_read_b64_tr_b16 a[66:67], v10 offset:32768\n" \
"v_mfma_f32_16x16x32_bf16 v[212:215], a[28:31], a[100:103], v[212:215]\n" \
"v_mfma_f32_16x16x32_bf16 v[224:227], a[32:35], a[96:99], 0\n" \
"s_add_u32 m0, s22, 18048\n" \
"ds_read_b64_tr_b16 a[68:69], v9 offset:33792\n" \
"v_mfma_f32_16x16x32_bf16 v[224:227], a[36:39], a[100:103], v[224:227]\n" \
"buffer_load_dwordx4 v21, s[32:35], s47 offen offset:384 lds\n" \
"v_mfma_f32_16x16x32_bf16 v[228:231], a[40:43], a[96:99], 0\n" \
"ds_read_b64_tr_b16 a[70:71], v10 offset:33792\n" \
"v_mfma_f32_16x16x32_bf16 v[228:231], a[44:47], a[100:103], v[228:231]\n" \
"v_mfma_f32_16x16x32_bf16 v[240:243], a[48:51], a[96:99], 0\n" \
"s_add_u32 m0, s50, 69632\n" \
"ds_read_b64_tr_b16 a[72:73], v11 offset:32768\n" \
"v_mfma_f32_16x16x32_bf16 v[240:243], a[52:55], a[100:103], v[240:243]\n" \
"buffer_load_dwordx4 v14, s[28:31], s46 offen lds\n" \
"v_mfma_f32_16x16x32_bf16 v[244:247], a[56:59], a[96:99], 0\n" \
"ds_read_b64_tr_b16 a[74:75], v12 offset:32768\n" \
"v_mfma_f32_16x16x32_bf16 v[244:247], a[60:63], a[100:103], v[244:247]\n" \
"v_mfma_f32_16x16x32_bf16 v[200:203], a[0:3], a[104:107], 0\n" \
"s_mov_b32 s47, 1310720\n" \
"s_add_u32 m0, s22, 17280\n" \
"ds_read_b64_tr_b16 a[76:77], v11 offset:33792\n" \
"v_mfma_f32_16x16x32_bf16 v[200:203], a[4:7], a[108:111], v[200:203]\n" \
"buffer_load_dwordx4 v21, s[32:35], s47 offen offset:128 lds\n" \
"v_mfma_f32_16x16x32_bf16 v[204:207], a[8:11], a[104:107], 0\n" \
"ds_read_b64_tr_b16 a[78:79], v12 offset:33792\n" \
"v_mfma_f32_16x16x32_bf16 v[204:207], a[12:15], a[108:111], v[204:207]\n" \
"v_mfma_f32_16x16x32_bf16 v[216:219], a[16:19], a[104:107], 0\n" \
"s_add_u32 m0, s50, 73728\n" \
"ds_read_b64_tr_b16 a[80:81], v9 offset:33280\n" \
"v_mfma_f32_16x16x32_bf16 v[216:219], a[20:23], a[108:111], v[216:219]\n" \
"buffer_load_dwordx4 v15, s[28:31], s46 offen lds\n" \
"v_mfma_f32_16x16x32_bf16 v[220:223], a[24:27], a[104:107], 0\n" \
"ds_read_b64_tr_b16 a[82:83], v10 offset:33280\n" \
"v_mfma_f32_16x16x32_bf16 v[220:223], a[28:31], a[108:111], v[220:223]\n" \
"v_mfma_f32_16x16x32_bf16 v[232:235], a[32:35], a[104:107], 0\n" \
"s_add_u32 m0, s22, 19072\n" \
"ds_read_b64_tr_b16 a[84:85], v9 offset:34304\n" \
"v_mfma_f32_16x16x32_bf16 v[232:235], a[36:39], a[108:111], v[232:235]\n" \
"buffer_load_dwordx4 v21, s[32:35], s47 offen offset:384 lds\n" \
"v_mfma_f32_16x16x32_bf16 v[236:239], a[40:43], a[104:107], 0\n" \
"ds_read_b64_tr_b16 a[86:87], v10 offset:34304\n" \
"v_mfma_f32_16x16x32_bf16 v[236:239], a[44:47], a[108:111], v[236:239]\n" \
"v_mfma_f32_16x16x32_bf16 v[248:251], a[48:51], a[104:107], 0\n" \
"s_add_u32 m0, s50, 77824\n" \
"ds_read_b64_tr_b16 a[88:89], v11 offset:33280\n" \
"v_mfma_f32_16x16x32_bf16 v[248:251], a[52:55], a[108:111], v[248:251]\n" \
"buffer_load_dwordx4 v16, s[28:31], s46 offen lds\n" \
"v_mfma_f32_16x16x32_bf16 v[252:255], a[56:59], a[104:107], 0\n" \
"ds_read_b64_tr_b16 a[90:91], v12 offset:33280\n" \
"v_mfma_f32_16x16x32_bf16 v[252:255], a[60:63], a[108:111], v[252:255]\n" \
"v_mfma_f32_16x16x32_bf16 a[128:131], a[0:3], a[112:115], 0\n" \
"ds_read_b64_tr_b16 a[92:93], v11 offset:34304\n" \
"v_mfma_f32_16x16x32_bf16 a[128:131], a[4:7], a[116:119], a[128:131]\n" \
"global_load_dwordx4 a[230:233], v27, s[70:71] offset:0\n" \
"v_mfma_f32_16x16x32_bf16 a[132:135], a[8:11], a[112:115], 0\n" \
"ds_read_b64_tr_b16 a[94:95], v12 offset:34304\n" \
"v_mfma_f32_16x16x32_bf16 a[132:135], a[12:15], a[116:119], a[132:135]\n" \
"v_mfma_f32_16x16x32_bf16 a[144:147], a[16:19], a[112:115], 0\n" \
"v_mfma_f32_16x16x32_bf16 a[144:147], a[20:23], a[116:119], a[144:147]\n" \
"global_load_dwordx4 a[234:237], v27, s[70:71] offset:16\n" \
"v_mfma_f32_16x16x32_bf16 a[148:151], a[24:27], a[112:115], 0\n" \
"v_mfma_f32_16x16x32_bf16 a[148:151], a[28:31], a[116:119], a[148:151]\n" \
"v_mfma_f32_16x16x32_bf16 a[160:163], a[32:35], a[112:115], 0\n" \
"v_mfma_f32_16x16x32_bf16 a[160:163], a[36:39], a[116:119], a[160:163]\n" \
"buffer_load_dwordx2 a[192:193], v54, s[40:43], 0 offen offset:0 nt\n" \
"v_mfma_f32_16x16x32_bf16 a[164:167], a[40:43], a[112:115], 0\n" \
"v_mfma_f32_16x16x32_bf16 a[164:167], a[44:47], a[116:119], a[164:167]\n" \
"s_waitcnt vmcnt(23) lgkmcnt(0)\n" \
"v_mfma_f32_16x16x32_bf16 a[176:179], a[48:51], a[112:115], 0\n" \
"s_barrier\n" \
"v_mfma_f32_16x16x32_bf16 a[176:179], a[52:55], a[116:119], a[176:179]\n" \
"buffer_load_dwordx2 a[194:195], v54, s[40:43], 0 offen offset:2048 nt\n" \
"v_mfma_f32_16x16x32_bf16 a[180:183], a[56:59], a[112:115], 0\n" \
"v_mfma_f32_16x16x32_bf16 a[180:183], a[60:63], a[116:119], a[180:183]\n" \
"v_mfma_f32_16x16x32_bf16 a[136:139], a[0:3], a[120:123], 0\n" \
"v_mfma_f32_16x16x32_bf16 a[136:139], a[4:7], a[124:127], a[136:139]\n" \
"buffer_load_dwordx2 a[196:197], v55, s[40:43], 0 offen offset:0 nt\n" \
"v_mfma_f32_16x16x32_bf16 a[140:143], a[8:11], a[120:123], 0\n" \
"s_add_u32 s84, s84, 1048576\ns_addc_u32 s85, s85, 0\n" \
"ds_read_b128 a[0:3], v5 offset:0\n" \
"v_mfma_f32_16x16x32_bf16 a[140:143], a[12:15], a[124:127], a[140:143]\n" \
"s_mov_b32 s52, 0\n" \
"ds_read_b128 a[4:7], v6 offset:0\n" \
"v_mfma_f32_16x16x32_bf16 a[152:155], a[16:19], a[120:123], 0\n" \
"ds_read_b128 a[8:11], v5 offset:2048\n" \
"v_mfma_f32_16x16x32_bf16 a[152:155], a[20:23], a[124:127], a[152:155]\n" \
"ds_read_b128 a[12:15], v6 offset:2048\n" \
"v_mfma_f32_16x16x32_bf16 a[156:159], a[24:27], a[120:123], 0\n" \
"ds_read_b128 a[16:19], v5 offset:4096\n" \
"v_mfma_f32_16x16x32_bf16 a[156:159], a[28:31], a[124:127], a[156:159]\n" \
"ds_read_b128 a[20:23], v6 offset:4096\n" \
"v_mfma_f32_16x16x32_bf16 a[168:171], a[32:35], a[120:123], 0\n" \
"ds_read_b128 a[24:27], v5 offset:6144\n" \
"v_mfma_f32_16x16x32_bf16 a[168:171], a[36:39], a[124:127], a[168:171]\n" \
"ds_read_b128 a[28:31], v6 offset:6144\n" \
"v_mfma_f32_16x16x32_bf16 a[172:175], a[40:43], a[120:123], 0\n" \
"ds_read_b128 a[32:35], v5 offset:8192\n" \
"v_mfma_f32_16x16x32_bf16 a[172:175], a[44:47], a[124:127], a[172:175]\n" \
"ds_read_b128 a[36:39], v6 offset:8192\n" \
"v_mfma_f32_16x16x32_bf16 a[184:187], a[48:51], a[120:123], 0\n" \
"ds_read_b128 a[40:43], v5 offset:10240\n" \
"v_mfma_f32_16x16x32_bf16 a[184:187], a[52:55], a[124:127], a[184:187]\n" \
"ds_read_b128 a[44:47], v6 offset:10240\n" \
"v_mfma_f32_16x16x32_bf16 a[188:191], a[56:59], a[120:123], 0\n" \
"ds_read_b128 a[48:51], v5 offset:12288\n" \
"v_mfma_f32_16x16x32_bf16 a[188:191], a[60:63], a[124:127], a[188:191]\n" \
"s_waitcnt lgkmcnt(9)\n" \
"ds_read_b128 a[52:55], v6 offset:12288\n" \
"v_mfma_f32_16x16x32_bf16 v[64:67], a[0:3], a[64:67], v[64:67]\n" \
"s_mov_b32 s47, 1572864\n" \
"s_add_u32 m0, s22, 32768\n" \
"ds_read_b128 a[56:59], v5 offset:14336\n" \
"v_mfma_f32_16x16x32_bf16 v[64:67], a[4:7], a[68:71], v[64:67]\n" \
"buffer_load_dwordx4 v21, s[32:35], s47 offen offset:0 lds\n" \
"v_mfma_f32_16x16x32_bf16 v[68:71], a[8:11], a[64:67], v[68:71]\n" \
"s_waitcnt lgkmcnt(7)\n" \
"ds_read_b128 a[60:63], v6 offset:14336\n" \
"v_mfma_f32_16x16x32_bf16 v[68:71], a[12:15], a[68:71], v[68:71]\n" \
"v_mfma_f32_16x16x32_bf16 v[80:83], a[16:19], a[64:67], v[80:83]\n" \
"ds_read_b64_tr_b16 a[96:97], v9 offset:49152\n" \
"v_mfma_f32_16x16x32_bf16 v[80:83], a[20:23], a[68:71], v[80:83]\n" \
"s_mov_b64 exec, 0xffff\nglobal_load_dword v22, v1, s[84:85]\ns_mov_b64 exec, -1\n" \
"v_mfma_f32_16x16x32_bf16 v[84:87], a[24:27], a[64:67], v[84:87]\n" \
"s_waitcnt lgkmcnt(5)\n" \
"ds_read_b64_tr_b16 a[98:99], v10 offset:49152\n" \
"v_mfma_f32_16x16x32_bf16 v[84:87], a[28:31], a[68:71], v[84:87]\n" \
"v_mfma_f32_16x16x32_bf16 v[96:99], a[32:35], a[64:67], v[96:99]\n" \
"s_mov_b32 s46, 384\n" \
"s_add_u32 m0, s50, 81920\n" \
"ds_read_b64_tr_b16 a[100:101], v9 offset:50176\n" \
"v_mfma_f32_16x16x32_bf16 v[96:99], a[36:39], a[68:71], v[96:99]\n" \
"buffer_load_dwordx4 v17, s[28:31], s46 offen lds\n" \
"v_mfma_f32_16x16x32_bf16 v[100:103], a[40:43], a[64:67], v[100:103]\n" \
"s_waitcnt lgkmcnt(3)\n" \
"ds_read_b64_tr_b16 a[102:103], v10 offset:50176\n" \
"v_mfma_f32_16x16x32_bf16 v[100:103], a[44:47], a[68:71], v[100:103]\n" \
"v_mfma_f32_16x16x32_bf16 v[112:115], a[48:51], a[64:67], v[112:115]\n" \
"ds_read_b64_tr_b16 a[104:105], v11 offset:49152\n" \
"v_mfma_f32_16x16x32_bf16 v[112:115], a[52:55], a[68:71], v[112:115]\n" \
"buffer_load_dwordx2 a[198:199], v59, s[40:43], s52 offen offset:0 nt\n" \
"v_mfma_f32_16x16x32_bf16 v[116:119], a[56:59], a[64:67], v[116:119]\n" \
"ds_read_b64_tr_b16 a[106:107], v12 offset:49152\n" \
"v_mfma_f32_16x16x32_bf16 v[116:119], a[60:63], a[68:71], v[116:119]\n" \
"v_mfma_f32_16x16x32_bf16 v[72:75], a[0:3], a[72:75], v[72:75]\n" \
"s_add_u32 m0, s22, 34560\n" \
"ds_read_b64_tr_b16 a[108:109], v11 offset:50176\n" \
"v_mfma_f32_16x16x32_bf16 v[72:75], a[4:7], a[76:79], v[72:75]\n" \
"buffer_load_dwordx4 v21, s[32:35], s47 offen offset:256 lds\n" \
"v_mfma_f32_16x16x32_bf16 v[76:79], a[8:11], a[72:75], v[76:79]\n" \
"ds_read_b64_tr_b16 a[110:111], v12 offset:50176\n" \
"v_mfma_f32_16x16x32_bf16 v[76:79], a[12:15], a[76:79], v[76:79]\n" \
"v_mfma_f32_16x16x32_bf16 v[88:91], a[16:19], a[72:75], v[88:91]\n" \
"ds_read_b64_tr_b16 a[112:113], v9 offset:49664\n" \
"v_mfma_f32_16x16x32_bf16 v[88:91], a[20:23], a[76:79], v[88:91]\n" \
"buffer_load_dwordx2 a[214:215], v59, s[80:83], s52 offen offset:0 nt\n" \
"v_mfma_f32_16x16x32_bf16 v[92:95], a[24:27], a[72:75], v[92:95]\n" \
"ds_read_b64_tr_b16 a[114:115], v10 offset:49664\n" \
"v_mfma_f32_16x16x32_bf16 v[92:95], a[28:31], a[76:79], v[92:95]\n" \
"v_mfma_f32_16x16x32_bf16 v[104:107], a[32:35], a[72:75], v[104:107]\n" \
"s_add_u32 m0, s50, 86016\n" \
"ds_read_b64_tr_b16 a[116:117], v9 offset:50688\n" \
"v_mfma_f32_16x16x32_bf16 v[104:107], a[36:39], a[76:79], v[104:107]\n" \
"buffer_load_dwordx4 v18, s[28:31], s46 offen lds\n" \
"v_mfma_f32_16x16x32_bf16 v[108:111], a[40:43], a[72:75], v[108:111]\n" \
"ds_read_b64_tr_b16 a[118:119], v10 offset:50688\n" \
"v_mfma_f32_16x16x32_bf16 v[108:111], a[44:47], a[76:79], v[108:111]\n" \
"v_mfma_f32_16x16x32_bf16 v[120:123], a[48:51], a[72:75], v[120:123]\n" \
"ds_read_b64_tr_b16 a[120:121], v11 offset:49664\n" \
"v_mfma_f32_16x16x32_bf16 v[120:123], a[52:55], a[76:79], v[120:123]\n" \
"buffer_load_dwordx2 a[200:201], v59, s[40:43], s52 offen offset:2048 nt\n" \
"v_mfma_f32_16x16x32_bf16 v[124:127], a[56:59], a[72:75], v[124:127]\n" \
"ds_read_b64_tr_b16 a[122:123], v12 offset:49664\n" \
"v_mfma_f32_16x16x32_bf16 v[124:127], a[60:63], a[76:79], v[124:127]\n" \
"v_mfma_f32_16x16x32_bf16 v[128:131], a[0:3], a[80:83], v[128:131]\n" \
"s_mov_b32 s47, 1835008\n" \
"s_add_u32 m0, s22, 33792\n" \
"ds_read_b64_tr_b16 a[124:125], v11 offset:50688\n" \
"v_mfma_f32_16x16x32_bf16 v[128:131], a[4:7], a[84:87], v[128:131]\n" \
"buffer_load_dwordx4 v21, s[32:35], s47 offen offset:0 lds\n" \
"v_mfma_f32_16x16x32_bf16 v[132:135], a[8:11], a[80:83], v[132:135]\n" \
"ds_read_b64_tr_b16 a[126:127], v12 offset:50688\n" \
"v_mfma_f32_16x16x32_bf16 v[132:135], a[12:15], a[84:87], v[132:135]\n" \
"v_mfma_f32_16x16x32_bf16 v[144:147], a[16:19], a[80:83], v[144:147]\n" \
"v_mfma_f32_16x16x32_bf16 v[144:147], a[20:23], a[84:87], v[144:147]\n" \
"buffer_load_dwordx2 a[216:217], v59, s[80:83], s52 offen offset:2048 nt\n" \
"v_mfma_f32_16x16x32_bf16 v[148:151], a[24:27], a[80:83], v[148:151]\n" \
"s_mov_b32 s52, 4096\n" \
"v_mfma_f32_16x16x32_bf16 v[148:151], a[28:31], a[84:87], v[148:151]\n" \
"v_mfma_f32_16x16x32_bf16 v[160:163], a[32:35], a[80:83], v[160:163]\n" \
"s_add_u32 m0, s50, 90112\n" \
"v_mfma_f32_16x16x32_bf16 v[160:163], a[36:39], a[84:87], v[160:163]\n" \
"buffer_load_dwordx4 v19, s[28:31], s46 offen lds\n" \
"v_mfma_f32_16x16x32_bf16 v[164:167], a[40:43], a[80:83], v[164:167]\n" \
"v_mfma_f32_16x16x32_bf16 v[164:167], a[44:47], a[84:87], v[164:167]\n" \
"s_waitcnt vmcnt(24) lgkmcnt(0)\n" \
"v_mfma_f32_16x16x32_bf16 v[176:179], a[48:51], a[80:83], v[176:179]\n" \
"s_barrier\n" \
"v_mfma_f32_16x16x32_bf16 v[176:179], a[52:55], a[84:87], v[176:179]\n" \
"buffer_load_dwordx2 a[202:203], v59, s[40:43], s52 offen offset:0 nt\n" \
"v_mfma_f32_16x16x32_bf16 v[180:183], a[56:59], a[80:83], v[180:183]\n" \
"v_mfma_f32_16x16x32_bf16 v[180:183], a[60:63], a[84:87], v[180:183]\n" \
"v_mfma_f32_16x16x32_bf16 v[136:139], a[0:3], a[88:91], v[136:139]\n" \
"s_add_u32 m0, s22, 35584\n" \
"v_mfma_f32_16x16x32_bf16 v[136:139], a[4:7], a[92:95], v[136:139]\n" \
"buffer_load_dwordx4 v21, s[32:35], s47 offen offset:256 lds\n" \
"v_mfma_f32_16x16x32_bf16 v[140:143], a[8:11], a[88:91], v[140:143]\n" \
"v_mfma_f32_16x16x32_bf16 v[140:143], a[12:15], a[92:95], v[140:143]\n" \
"v_mfma_f32_16x16x32_bf16 v[152:155], a[16:19], a[88:91], v[152:155]\n" \
"v_mfma_f32_16x16x32_bf16 v[152:155], a[20:23], a[92:95], v[152:155]\n" \
"buffer_load_dwordx2 a[218:219], v59, s[80:83], s52 offen offset:0 nt\n" \
"v_mfma_f32_16x16x32_bf16 v[156:159], a[24:27], a[88:91], v[156:159]\n" \
"v_mfma_f32_16x16x32_bf16 v[156:159], a[28:31], a[92:95], v[156:159]\n" \
"v_mfma_f32_16x16x32_bf16 v[168:171], a[32:35], a[88:91], v[168:171]\n" \
"s_add_u32 m0, s50, 94208\n" \
"v_mfma_f32_16x16x32_bf16 v[168:171], a[36:39], a[92:95], v[168:171]\n" \
"buffer_load_dwordx4 v20, s[28:31], s46 offen lds\n" \
"v_mfma_f32_16x16x32_bf16 v[172:175], a[40:43], a[88:91], v[172:175]\n" \
"v_mfma_f32_16x16x32_bf16 v[172:175], a[44:47], a[92:95], v[172:175]\n" \
"v_mfma_f32_16x16x32_bf16 v[184:187], a[48:51], a[88:91], v[184:187]\n" \
"v_mfma_f32_16x16x32_bf16 v[184:187], a[52:55], a[92:95], v[184:187]\n" \
"buffer_load_dwordx2 a[204:205], v59, s[40:43], s52 offen offset:2048 nt\n" \
"v_mfma_f32_16x16x32_bf16 v[188:191], a[56:59], a[88:91], v[188:191]\n" \
"v_mfma_f32_16x16x32_bf16 v[188:191], a[60:63], a[92:95], v[188:191]\n" \
"v_mfma_f32_16x16x32_bf16 v[192:195], a[0:3], a[96:99], v[192:195]\n" \
"s_mov_b32 s47, 1572864\n" \
"s_add_u32 m0, s22, 49024\n" \
"v_mfma_f32_16x16x32_bf16 v[192:195], a[4:7], a[100:103], v[192:195]\n" \
"buffer_load_dwordx4 v21, s[32:35], s47 offen offset:128 lds\n" \
"v_mfma_f32_16x16x32_bf16 v[196:199], a[8:11], a[96:99], v[196:199]\n" \
"v_mfma_f32_16x16x32_bf16 v[196:199], a[12:15], a[100:103], v[196:199]\n" \
"v_mfma_f32_16x16x32_bf16 v[208:211], a[16:19], a[96:99], v[208:211]\n" \
"s_mov_b32 s46, 512\n" \
"s_add_u32 m0, s50, 0\n" \
"ds_read_b64_tr_b16 a[64:65], v9 offset:0\n" \
"v_mfma_f32_16x16x32_bf16 v[208:211], a[20:23], a[100:103], v[208:211]\n" \
"buffer_load_dwordx4 v13, s[28:31], s46 offen lds\n" \
"v_mfma_f32_16x16x32_bf16 v[212:215], a[24:27], a[96:99], v[212:215]\n" \
"ds_read_b64_tr_b16 a[66:67], v10 offset:0\n" \
"v_mfma_f32_16x16x32_bf16 v[212:215], a[28:31], a[100:103], v[212:215]\n" \
"v_mfma_f32_16x16x32_bf16 v[224:227], a[32:35], a[96:99], v[224:227]\n" \
"ds_read_b64_tr_b16 a[68:69], v9 offset:1024\n" \
"v_mfma_f32_16x16x32_bf16 v[224:227], a[36:39], a[100:103], v[224:227]\n" \
"LH4_%=:\ns_cbranch_execz LH8_%=\n" \
"buffer_load_dwordx2 a[220:221], v59, s[80:83], s52 offen offset:2048 nt\n" \
"v_mfma_f32_16x16x32_bf16 v[228:231], a[40:43], a[96:99], v[228:231]\n" \
"s_mov_b32 s52, 8192\n" \
"ds_read_b64_tr_b16 a[70:71], v10 offset:1024\n" \
"v_mfma_f32_16x16x32_bf16 v[228:231], a[44:47], a[100:103], v[228:231]\n" \
"v_mfma_f32_16x16x32_bf16 v[240:243], a[48:51], a[96:99], v[240:243]\n" \
"s_add_u32 m0, s22, 50816\n" \
"ds_read_b64_tr_b16 a[72:73], v11 offset:0\n" \
"v_mfma_f32_16x16x32_bf16 v[240:243], a[52:55], a[100:103], v[240:243]\n" \
"buffer_load_dwordx4 v21, s[32:35], s47 offen offset:384 lds\n" \
"v_mfma_f32_16x16x32_bf16 v[244:247], a[56:59], a[96:99], v[244:247]\n" \
"ds_read_b64_tr_b16 a[74:75], v12 offset:0\n" \
"v_mfma_f32_16x16x32_bf16 v[244:247], a[60:63], a[100:103], v[244:247]\n" \
"v_mfma_f32_16x16x32_bf16 v[200:203], a[0:3], a[104:107], v[200:203]\n" \
"s_add_u32 m0, s50, 4096\n" \
"ds_read_b64_tr_b16 a[76:77], v11 offset:1024\n" \
"v_mfma_f32_16x16x32_bf16 v[200:203], a[4:7], a[108:111], v[200:203]\n" \
"buffer_load_dwordx4 v14, s[28:31], s46 offen lds\n" \
"v_mfma_f32_16x16x32_bf16 v[204:207], a[8:11], a[104:107], v[204:207]\n" \
"ds_read_b64_tr_b16 a[78:79], v12 offset:1024\n" \
"v_mfma_f32_16x16x32_bf16 v[204:207], a[12:15], a[108:111], v[204:207]\n" \
"v_mfma_f32_16x16x32_bf16 v[216:219], a[16:19], a[104:107], v[216:219]\n" \
"ds_read_b64_tr_b16 a[80:81], v9 offset:512\n" \
"v_mfma_f32_16x16x32_bf16 v[216:219], a[20:23], a[108:111], v[216:219]\n" \
"buffer_load_dwordx2 a[206:207], v59, s[40:43], s52 offen offset:0 nt\n" \
"v_mfma_f32_16x16x32_bf16 v[220:223], a[24:27], a[104:107], v[220:223]\n" \
"ds_read_b64_tr_b16 a[82:83], v10 offset:512\n" \
"v_mfma_f32_16x16x32_bf16 v[220:223], a[28:31], a[108:111], v[220:223]\n" \
"v_mfma_f32_16x16x32_bf16 v[232:235], a[32:35], a[104:107], v[232:235]\n" \
"s_mov_b32 s47, 1835008\n" \
"s_add_u32 m0, s22, 50048\n" \
"ds_read_b64_tr_b16 a[84:85], v9 offset:1536\n" \
"v_mfma_f32_16x16x32_bf16 v[232:235], a[36:39], a[108:111], v[232:235]\n" \
"buffer_load_dwordx4 v21, s[32:35], s47 offen offset:128 lds\n" \
"v_mfma_f32_16x16x32_bf16 v[236:239], a[40:43], a[104:107], v[236:239]\n" \
"ds_read_b64_tr_b16 a[86:87], v10 offset:1536\n" \
"v_mfma_f32_16x16x32_bf16 v[236:239], a[44:47], a[108:111], v[236:239]\n" \
"v_mfma_f32_16x16x32_bf16 v[248:251], a[48:51], a[104:107], v[248:251]\n" \
"s_add_u32 m0, s50, 8192\n" \
"ds_read_b64_tr_b16 a[88:89], v11 offset:512\n" \
"v_mfma_f32_16x16x32_bf16 v[248:251], a[52:55], a[108:111], v[248:251]\n" \
"buffer_load_dwordx4 v15, s[28:31], s46 offen lds\n" \
"v_mfma_f32_16x16x32_bf16 v[252:255], a[56:59], a[104:107], v[252:255]\n" \
"ds_read_b64_tr_b16 a[90:91], v12 offset:512\n" \
"v_mfma_f32_16x16x32_bf16 v[252:255], a[60:63], a[108:111], v[252:255]\n" \
"v_mfma_f32_16x16x32_bf16 a[128:131], a[0:3], a[112:115], a[128:131]\n" \
"ds_read_b64_tr_b16 a[92:93], v11 offset:1536\n" \
"v_mfma_f32_16x16x32_bf16 a[128:131], a[4:7], a[116:119], a[128:131]\n" \
"buffer_load_dwordx2 a[222:223], v59, s[80:83], s52 offen offset:0 nt\n" \
"v_mfma_f32_16x16x32_bf16 a[132:135], a[8:11], a[112:115], a[132:135]\n" \
"ds_read_b64_tr_b16 a[94:95], v12 offset:1536\n" \
"v_mfma_f32_16x16x32_bf16 a[132:135], a[12:15], a[116:119], a[132:135]\n" \
"v_mfma_f32_16x16x32_bf16 a[144:147], a[16:19], a[112:115], a[144:147]\n" \
"s_add_u32 m0, s22, 51840\n" \
"v_mfma_f32_16x16x32_bf16 a[144:147], a[20:23], a[116:119], a[144:147]\n" \
"buffer_load_dwordx4 v21, s[32:35], s47 offen offset:384 lds\n" \
"v_mfma_f32_16x16x32_bf16 a[148:151], a[24:27], a[112:115], a[148:151]\n" \
"v_mfma_f32_16x16x32_bf16 a[148:151], a[28:31], a[116:119], a[148:151]\n" \
"v_mfma_f32_16x16x32_bf16 a[160:163], a[32:35], a[112:115], a[160:163]\n" \
"v_mfma_f32_16x16x32_bf16 a[160:163], a[36:39], a[116:119], a[160:163]\n" \
"buffer_load_dwordx2 a[208:209], v59, s[40:43], s52 offen offset:2048 nt\n" \
"v_mfma_f32_16x16x32_bf16 a[164:167], a[40:43], a[112:115], a[164:167]\n" \
"v_mfma_f32_16x16x32_bf16 a[164:167], a[44:47], a[116:119], a[164:167]\n" \
"s_waitcnt vmcnt(33) lgkmcnt(0)\n" \
"v_mfma_f32_16x16x32_bf16 a[176:179], a[48:51], a[112:115], a[176:179]\n" \
"s_add_u32 m0, s50, 12288\n" \
"s_barrier\n" \
"v_mfma_f32_16x16x32_bf16 a[176:179], a[52:55], a[116:119], a[176:179]\n" \
"buffer_load_dwordx4 v16, s[28:31], s46 offen lds\n" \
"v_mfma_f32_16x16x32_bf16 a[180:183], a[56:59], a[112:115], a[180:183]\n" \
"v_mfma_f32_16x16x32_bf16 a[180:183], a[60:63], a[116:119], a[180:183]\n" \
"v_mfma_f32_16x16x32_bf16 a[136:139], a[0:3], a[120:123], a[136:139]\n" \
"v_mfma_f32_16x16x32_bf16 a[136:139], a[4:7], a[124:127], a[136:139]\n" \
"buffer_load_dwordx2 a[224:225], v59, s[80:83], s52 offen offset:2048 nt\n" \
"v_mfma_f32_16x16x32_bf16 a[140:143], a[8:11], a[120:123], a[140:143]\n" \
"s_mov_b32 s52, 12288\n" \
"ds_read_b128 a[0:3], v5 offset:32768\n" \
"v_mfma_f32_16x16x32_bf16 a[140:143], a[12:15], a[124:127], a[140:143]\n" \
"ds_read_b128 a[4:7], v6 offset:32768\n" \
"v_mfma_f32_16x16x32_bf16 a[152:155], a[16:19], a[120:123], a[152:155]\n" \
"ds_read_b128 a[8:11], v5 offset:34816\n" \
"v_mfma_f32_16x16x32_bf16 a[152:155], a[20:23], a[124:127], a[152:155]\n" \
"ds_read_b128 a[12:15], v6 offset:34816\n" \
"v_mfma_f32_16x16x32_bf16 a[156:159], a[24:27], a[120:123], a[156:159]\n" \
"ds_read_b128 a[16:19], v5 offset:36864\n" \
"v_mfma_f32_16x16x32_bf16 a[156:159], a[28:31], a[124:127], a[156:159]\n" \
"ds_read_b128 a[20:23], v6 offset:36864\n" \
"v_mfma_f32_16x16x32_bf16 a[168:171], a[32:35], a[120:123], a[168:171]\n" \
"ds_read_b128 a[24:27], v5 offset:38912\n" \
"v_mfma_f32_16x16x32_bf16 a[168:171], a[36:39], a[124:127], a[168:171]\n" \
"ds_read_b128 a[28:31], v6 offset:38912\n" \
"v_mfma_f32_16x16x32_bf16 a[172:175], a[40:43], a[120:123], a[172:175]\n" \
"ds_read_b128 a[32:35], v5 offset:40960\n" \
"v_mfma_f32_16x16x32_bf16 a[172:175], a[44:47], a[124:127], a[172:175]\n" \
"ds_read_b128 a[36:39], v6 offset:40960\n" \
"s_waitcnt vmcnt(10)\n" \
"v_mfma_f32_16x16x32_bf16 a[184:187], a[48:51], a[120:123], a[184:187]\n" \
"ds_read_b128 a[40:43], v5 offset:43008\n" \
"v_accvgpr_read_b32 v43, a230\n" \
"v_mfma_f32_16x16x32_bf16 a[184:187], a[52:55], a[124:127], a[184:187]\n" \
"ds_read_b128 a[44:47], v6 offset:43008\n" \
"v_lshlrev_b32 v42, 16, v43\n" \
"v_mfma_f32_16x16x32_bf16 a[188:191], a[56:59], a[120:123], a[188:191]\n" \
"ds_read_b128 a[48:51], v5 offset:45056\n" \
"v_and_b32 v43, 0xffff0000, v43\n" \
"v_mfma_f32_16x16x32_bf16 a[188:191], a[60:63], a[124:127], a[188:191]\n" \
"s_waitcnt lgkmcnt(9)\n" \
"ds_read_b128 a[52:55], v6 offset:45056\n" \
"v_accvgpr_read_b32 v45, a231\n" \
"v_mfma_f32_16x16x32_bf16 v[64:67], a[0:3], a[64:67], v[64:67]\n" \
"s_mov_b32 s47, 2097152\n" \
"s_add_u32 m0, s22, 0\n" \
"ds_read_b128 a[56:59], v5 offset:47104\n" \
"v_mfma_f32_16x16x32_bf16 v[64:67], a[4:7], a[68:71], v[64:67]\n" \
"buffer_load_dwordx4 v21, s[32:35], s47 offen offset:0 lds\n" \
"v_mfma_f32_16x16x32_bf16 v[68:71], a[8:11], a[64:67], v[68:71]\n" \
"s_waitcnt lgkmcnt(7)\n" \
"ds_read_b128 a[60:63], v6 offset:47104\n" \
"v_lshlrev_b32 v44, 16, v45\n" \
"v_mfma_f32_16x16x32_bf16 v[68:71], a[12:15], a[68:71], v[68:71]\n" \
"v_and_b32 v45, 0xffff0000, v45\n" \
"v_accvgpr_read_b32 v47, a232\n" \
"v_mfma_f32_16x16x32_bf16 v[80:83], a[16:19], a[64:67], v[80:83]\n" \
"ds_read_b64_tr_b16 a[96:97], v9 offset:16384\n" \
"v_lshlrev_b32 v46, 16, v47\n" \
"v_mfma_f32_16x16x32_bf16 v[80:83], a[20:23], a[68:71], v[80:83]\n" \
"buffer_load_dwordx2 a[210:211], v59, s[40:43], s52 offen offset:0 nt\n" \
"v_mfma_f32_16x16x32_bf16 v[84:87], a[24:27], a[64:67], v[84:87]\n" \
"s_waitcnt lgkmcnt(5)\n" \
"ds_read_b64_tr_b16 a[98:99], v10 offset:16384\n" \
"v_and_b32 v47, 0xffff0000, v47\n" \
"v_mfma_f32_16x16x32_bf16 v[84:87], a[28:31], a[68:71], v[84:87]\n" \
"v_accvgpr_read_b32 v49, a233\n" \
"v_lshlrev_b32 v48, 16, v49\n" \
"v_mfma_f32_16x16x32_bf16 v[96:99], a[32:35], a[64:67], v[96:99]\n" \
"s_mov_b32 s46, 512\n" \
"s_add_u32 m0, s50, 16384\n" \
"ds_read_b64_tr_b16 a[100:101], v9 offset:17408\n" \
"v_mfma_f32_16x16x32_bf16 v[96:99], a[36:39], a[68:71], v[96:99]\n" \
"buffer_load_dwordx4 v17, s[28:31], s46 offen lds\n" \
"v_mfma_f32_16x16x32_bf16 v[100:103], a[40:43], a[64:67], v[100:103]\n" \
"s_waitcnt lgkmcnt(3)\n" \
"ds_read_b64_tr_b16 a[102:103], v10 offset:17408\n" \
"v_and_b32 v49, 0xffff0000, v49\n" \
"v_mfma_f32_16x16x32_bf16 v[100:103], a[44:47], a[68:71], v[100:103]\n" \
"v_accvgpr_read_b32 v36, a192\n" \
"v_lshlrev_b32 v30, 16, v36\n" \
"v_mfma_f32_16x16x32_bf16 v[112:115], a[48:51], a[64:67], v[112:115]\n" \
"ds_read_b64_tr_b16 a[104:105], v11 offset:16384\n" \
"v_and_b32 v36, 0xffff0000, v36\n" \
"v_mfma_f32_16x16x32_bf16 v[112:115], a[52:55], a[68:71], v[112:115]\n" \
"buffer_load_dwordx2 a[226:227], v59, s[80:83], s52 offen offset:0 nt\n" \
"v_mfma_f32_16x16x32_bf16 v[116:119], a[56:59], a[64:67], v[116:119]\n" \
"ds_read_b64_tr_b16 a[106:107], v12 offset:16384\n" \
"v_accvgpr_read_b32 v37, a194\n" \
"v_mfma_f32_16x16x32_bf16 v[116:119], a[60:63], a[68:71], v[116:119]\n" \
"v_lshlrev_b32 v31, 16, v37\n" \
"v_and_b32 v37, 0xffff0000, v37\n" \
"v_mfma_f32_16x16x32_bf16 v[72:75], a[0:3], a[72:75], v[72:75]\n" \
"s_add_u32 m0, s22, 1792\n" \
"ds_read_b64_tr_b16 a[108:109], v11 offset:17408\n" \
"v_accvgpr_read_b32 v38, a196\n" \
"v_mfma_f32_16x16x32_bf16 v[72:75], a[4:7], a[76:79], v[72:75]\n" \
"buffer_load_dwordx4 v21, s[32:35], s47 offen offset:256 lds\n" \
"v_mfma_f32_16x16x32_bf16 v[76:79], a[8:11], a[72:75], v[76:79]\n" \
"ds_read_b64_tr_b16 a[110:111], v12 offset:17408\n" \
"v_lshlrev_b32 v32, 16, v38\n" \
"v_mfma_f32_16x16x32_bf16 v[76:79], a[12:15], a[76:79], v[76:79]\n" \
"v_and_b32 v38, 0xffff0000, v38\n" \
"v_accvgpr_read_b32 v39, a198\n" \
"v_mfma_f32_16x16x32_bf16 v[88:91], a[16:19], a[72:75], v[88:91]\n" \
"ds_read_b64_tr_b16 a[112:113], v9 offset:16896\n" \
"v_lshlrev_b32 v33, 16, v39\n" \
"v_mfma_f32_16x16x32_bf16 v[88:91], a[20:23], a[76:79], v[88:91]\n" \
"buffer_load_dwordx2 a[212:213], v59, s[40:43], s52 offen offset:2048 nt\n" \
"v_mfma_f32_16x16x32_bf16 v[92:95], a[24:27], a[72:75], v[92:95]\n" \
"ds_read_b64_tr_b16 a[114:115], v10 offset:16896\n" \
"v_and_b32 v39, 0xffff0000, v39\n" \
"v_mfma_f32_16x16x32_bf16 v[92:95], a[28:31], a[76:79], v[92:95]\n" \
"v_mul_f32 v25, v30, v45\n" \
"v_mul_f32 v26, v36, v49\n" \
"v_mfma_f32_16x16x32_bf16 v[104:107], a[32:35], a[72:75], v[104:107]\n" \
"s_add_u32 m0, s50, 20480\n" \
"ds_read_b64_tr_b16 a[116:117], v9 offset:17920\n" \
"v_fmac_f32 v25, v31, v44\n" \
"v_mfma_f32_16x16x32_bf16 v[104:107], a[36:39], a[76:79], v[104:107]\n" \
"buffer_load_dwordx4 v18, s[28:31], s46 offen lds\n" \
"v_mfma_f32_16x16x32_bf16 v[108:111], a[40:43], a[72:75], v[108:111]\n" \
"ds_read_b64_tr_b16 a[118:119], v10 offset:17920\n" \
"v_fmac_f32 v26, v37, v48\n" \
"v_mfma_f32_16x16x32_bf16 v[108:111], a[44:47], a[76:79], v[108:111]\n" \
"v_fmac_f32 v25, v32, v43\n" \
"v_fmac_f32 v26, v38, v47\n" \
"v_mfma_f32_16x16x32_bf16 v[120:123], a[48:51], a[72:75], v[120:123]\n" \
"ds_read_b64_tr_b16 a[120:121], v11 offset:16896\n" \
"v_fmac_f32 v25, v33, v42\n" \
"v_mfma_f32_16x16x32_bf16 v[120:123], a[52:55], a[76:79], v[120:123]\n" \
"buffer_load_dwordx2 a[228:229], v59, s[80:83], s52 offen offset:2048 nt\n" \
"v_mfma_f32_16x16x32_bf16 v[124:127], a[56:59], a[72:75], v[124:127]\n" \
"ds_read_b64_tr_b16 a[122:123], v12 offset:16896\n" \
"v_fmac_f32 v26, v39, v46\n" \
"v_mfma_f32_16x16x32_bf16 v[124:127], a[60:63], a[76:79], v[124:127]\n" \
"v_accvgpr_read_b32 v28, a214\n" \
"v_lshlrev_b32 v27, 16, v28\n" \
"v_mfma_f32_16x16x32_bf16 v[128:131], a[0:3], a[80:83], v[128:131]\n" \
"s_mov_b32 s47, 2359296\n" \
"s_add_u32 m0, s22, 1024\n" \
"ds_read_b64_tr_b16 a[124:125], v11 offset:17920\n" \
"v_mfma_f32_16x16x32_bf16 v[128:131], a[4:7], a[84:87], v[128:131]\n" \
"buffer_load_dwordx4 v21, s[32:35], s47 offen offset:0 lds\n" \
"v_mfma_f32_16x16x32_bf16 v[132:135], a[8:11], a[80:83], v[132:135]\n" \
"ds_read_b64_tr_b16 a[126:127], v12 offset:17920\n" \
"v_and_b32 v28, 0xffff0000, v28\n" \
"v_mfma_f32_16x16x32_bf16 v[132:135], a[12:15], a[84:87], v[132:135]\n" \
"v_mul_f32 v29, v25, v27\n" \
"v_mul_f32 v58, v26, v28\n" \
"v_mfma_f32_16x16x32_bf16 v[144:147], a[16:19], a[80:83], v[144:147]\n" \
"v_accvgpr_read_b32 v40, a200\n" \
"v_lshlrev_b32 v34, 16, v40\n" \
"v_mfma_f32_16x16x32_bf16 v[144:147], a[20:23], a[84:87], v[144:147]\n" \
"v_and_b32 v40, 0xffff0000, v40\n" \
"v_mfma_f32_16x16x32_bf16 v[148:151], a[24:27], a[80:83], v[148:151]\n" \
"v_mul_f32 v25, v31, v45\n" \
"v_mul_f32 v26, v37, v49\n" \
"v_mfma_f32_16x16x32_bf16 v[148:151], a[28:31], a[84:87], v[148:151]\n" \
"v_fmac_f32 v25, v32, v44\n" \
"v_fmac_f32 v26, v38, v48\n" \
"v_mfma_f32_16x16x32_bf16 v[160:163], a[32:35], a[80:83], v[160:163]\n" \
"s_add_u32 m0, s50, 24576\n" \
"v_fmac_f32 v25, v33, v43\n" \
"v_mfma_f32_16x16x32_bf16 v[160:163], a[36:39], a[84:87], v[160:163]\n" \
"buffer_load_dwordx4 v19, s[28:31], s46 offen lds\n" \
"v_mfma_f32_16x16x32_bf16 v[164:167], a[40:43], a[80:83], v[164:167]\n" \
"v_fmac_f32 v26, v39, v47\n" \
"v_fmac_f32 v25, v34, v42\n" \
"v_mfma_f32_16x16x32_bf16 v[164:167], a[44:47], a[84:87], v[164:167]\n" \
"s_waitcnt vmcnt(26) lgkmcnt(0)\n" \
"v_fmac_f32 v26, v40, v46\n" \
"v_accvgpr_read_b32 v28, a216\n" \
"v_mfma_f32_16x16x32_bf16 v[176:179], a[48:51], a[80:83], v[176:179]\n" \
"s_barrier\n" \
"v_lshlrev_b32 v27, 16, v28\n" \
"v_mfma_f32_16x16x32_bf16 v[176:179], a[52:55], a[84:87], v[176:179]\n" \
"v_and_b32 v28, 0xffff0000, v28\n" \
"v_mul_f32 v25, v25, v27\n" \
"v_mfma_f32_16x16x32_bf16 v[180:183], a[56:59], a[80:83], v[180:183]\n" \
"v_mul_f32 v26, v26, v28\n" \
"v_cvt_pk_bf16_f32 v53, v25, v29\n" \
"v_mfma_f32_16x16x32_bf16 v[180:183], a[60:63], a[84:87], v[180:183]\n" \
"v_cvt_pk_bf16_f32 v57, v26, v58\n" \
"v_mfma_f32_16x16x32_bf16 v[136:139], a[0:3], a[88:91], v[136:139]\n" \
"s_add_u32 m0, s22, 2816\n" \
"v_accvgpr_read_b32 v41, a202\n" \
"v_lshlrev_b32 v35, 16, v41\n" \
"v_mfma_f32_16x16x32_bf16 v[136:139], a[4:7], a[92:95], v[136:139]\n" \
"buffer_load_dwordx4 v21, s[32:35], s47 offen offset:256 lds\n" \
"v_mfma_f32_16x16x32_bf16 v[140:143], a[8:11], a[88:91], v[140:143]\n" \
"s_add_u32 s84, s84, 1048576\ns_addc_u32 s85, s85, 0\n" \
"v_and_b32 v41, 0xffff0000, v41\n" \
"v_mfma_f32_16x16x32_bf16 v[140:143], a[12:15], a[92:95], v[140:143]\n" \
"v_mul_f32 v25, v32, v45\n" \
"v_mul_f32 v26, v38, v49\n" \
"v_mfma_f32_16x16x32_bf16 v[152:155], a[16:19], a[88:91], v[152:155]\n" \
"v_fmac_f32 v25, v33, v44\n" \
"v_fmac_f32 v26, v39, v48\n" \
"v_mfma_f32_16x16x32_bf16 v[152:155], a[20:23], a[92:95], v[152:155]\n" \
"v_fmac_f32 v25, v34, v43\n" \
"v_mfma_f32_16x16x32_bf16 v[156:159], a[24:27], a[88:91], v[156:159]\n" \
"v_fmac_f32 v26, v40, v47\n" \
"v_fmac_f32 v25, v35, v42\n" \
"v_mfma_f32_16x16x32_bf16 v[156:159], a[28:31], a[92:95], v[156:159]\n" \
"v_fmac_f32 v26, v41, v46\n" \
"v_accvgpr_read_b32 v28, a218\n" \
"v_mfma_f32_16x16x32_bf16 v[168:171], a[32:35], a[88:91], v[168:171]\n" \
"s_add_u32 m0, s50, 28672\n" \
"v_lshlrev_b32 v27, 16, v28\n" \
"v_mfma_f32_16x16x32_bf16 v[168:171], a[36:39], a[92:95], v[168:171]\n" \
"buffer_load_dwordx4 v20, s[28:31], s46 offen lds\n" \
"v_mfma_f32_16x16x32_bf16 v[172:175], a[40:43], a[88:91], v[172:175]\n" \
"v_and_b32 v28, 0xffff0000, v28\n" \
"v_mul_f32 v29, v25, v27\n" \
"v_mfma_f32_16x16x32_bf16 v[172:175], a[44:47], a[92:95], v[172:175]\n" \
"v_mul_f32 v58, v26, v28\n" \
"v_accvgpr_read_b32 v36, a204\n" \
"v_mfma_f32_16x16x32_bf16 v[184:187], a[48:51], a[88:91], v[184:187]\n" \
"v_lshlrev_b32 v30, 16, v36\n" \
"v_mfma_f32_16x16x32_bf16 v[184:187], a[52:55], a[92:95], v[184:187]\n" \
"s_mov_b64 exec, 0xffff\nglobal_load_dword v22, v1, s[84:85]\ns_mov_b64 exec, -1\n" \
"v_mfma_f32_16x16x32_bf16 v[188:191], a[56:59], a[88:91], v[188:191]\n" \
"v_and_b32 v36, 0xffff0000, v36\n" \
"v_mul_f32 v25, v33, v45\n" \
"v_mfma_f32_16x16x32_bf16 v[188:191], a[60:63], a[92:95], v[188:191]\n" \
"v_mul_f32 v26, v39, v49\n" \
"v_fmac_f32 v25, v34, v44\n" \
"v_mfma_f32_16x16x32_bf16 v[192:195], a[0:3], a[96:99], v[192:195]\n" \
"s_mov_b32 s47, 2097152\n" \
"s_add_u32 m0, s22, 16256\n" \
"v_fmac_f32 v26, v40, v48\n" \
"v_mfma_f32_16x16x32_bf16 v[192:195], a[4:7], a[100:103], v[192:195]\n" \
"buffer_load_dwordx4 v21, s[32:35], s47 offen offset:128 lds\n" \
"v_mfma_f32_16x16x32_bf16 v[196:199], a[8:11], a[96:99], v[196:199]\n" \
"v_fmac_f32 v25, v35, v43\n" \
"v_fmac_f32 v26, v41, v47\n" \
"v_mfma_f32_16x16x32_bf16 v[196:199], a[12:15], a[100:103], v[196:199]\n" \
"v_fmac_f32 v25, v30, v42\n" \
"v_fmac_f32 v26, v36, v46\n" \
"v_mfma_f32_16x16x32_bf16 v[208:211], a[16:19], a[96:99], v[208:211]\n" \
"s_mov_b32 s46, 640\n" \
"s_add_u32 m0, s50, 32768\n" \
"ds_read_b64_tr_b16 a[64:65], v9 offset:32768\n" \
"v_mfma_f32_16x16x32_bf16 v[208:211], a[20:23], a[100:103], v[208:211]\n" \
"buffer_load_dwordx4 v13, s[28:31], s46 offen lds\n" \
"v_mfma_f32_16x16x32_bf16 v[212:215], a[24:27], a[96:99], v[212:215]\n" \
"ds_read_b64_tr_b16 a[66:67], v10 offset:32768\n" \
"v_accvgpr_read_b32 v28, a220\n" \
"v_mfma_f32_16x16x32_bf16 v[212:215], a[28:31], a[100:103], v[212:215]\n" \
"v_lshlrev_b32 v27, 16, v28\n" \
"v_and_b32 v28, 0xffff0000, v28\n" \
"v_mfma_f32_16x16x32_bf16 v[224:227], a[32:35], a[96:99], v[224:227]\n" \
"ds_read_b64_tr_b16 a[68:69], v9 offset:33792\n" \
"v_mul_f32 v25, v25, v27\n" \
"v_mfma_f32_16x16x32_bf16 v[224:227], a[36:39], a[100:103], v[224:227]\n" \
"v_mul_f32 v26, v26, v28\n" \
"v_cvt_pk_bf16_f32 v52, v25, v29\n" \
"v_mfma_f32_16x16x32_bf16 v[228:231], a[40:43], a[96:99], v[228:231]\n" \
"ds_read_b64_tr_b16 a[70:71], v10 offset:33792\n" \
"v_cvt_pk_bf16_f32 v56, v26, v58\n" \
"v_mfma_f32_16x16x32_bf16 v[228:231], a[44:47], a[100:103], v[228:231]\n" \
"v_mfma_f32_16x16x32_bf16 v[240:243], a[48:51], a[96:99], v[240:243]\n" \
"s_add_u32 m0, s22, 18048\n" \
"ds_read_b64_tr_b16 a[72:73], v11 offset:32768\n" \
"v_mfma_f32_16x16x32_bf16 v[240:243], a[52:55], a[100:103], v[240:243]\n" \
"buffer_load_dwordx4 v21, s[32:35], s47 offen offset:384 lds\n" \
"v_mfma_f32_16x16x32_bf16 v[244:247], a[56:59], a[96:99], v[244:247]\n" \
"ds_read_b64_tr_b16 a[74:75], v12 offset:32768\n" \
"v_mfma_f32_16x16x32_bf16 v[244:247], a[60:63], a[100:103], v[244:247]\n" \
"s_waitcnt vmcnt(8)\n" \
"v_accvgpr_read_b32 v37, a206\n" \
"v_lshlrev_b32 v31, 16, v37\n" \
"v_mfma_f32_16x16x32_bf16 v[200:203], a[0:3], a[104:107], v[200:203]\n" \
"s_add_u32 m0, s50, 36864\n" \
"ds_read_b64_tr_b16 a[76:77], v11 offset:33792\n" \
"v_and_b32 v37, 0xffff0000, v37\n" \
"v_mfma_f32_16x16x32_bf16 v[200:203], a[4:7], a[108:111], v[200:203]\n" \
"buffer_load_dwordx4 v14, s[28:31], s46 offen lds\n" \
"v_mfma_f32_16x16x32_bf16 v[204:207], a[8:11], a[104:107], v[204:207]\n" \
"ds_read_b64_tr_b16 a[78:79], v12 offset:33792\n" \
"v_mul_f32 v25, v34, v45\n" \
"v_mfma_f32_16x16x32_bf16 v[204:207], a[12:15], a[108:111], v[204:207]\n" \
"v_mul_f32 v26, v40, v49\n" \
"v_fmac_f32 v25, v35, v44\n" \
"v_mfma_f32_16x16x32_bf16 v[216:219], a[16:19], a[104:107], v[216:219]\n" \
"ds_read_b64_tr_b16 a[80:81], v9 offset:33280\n" \
"v_fmac_f32 v26, v41, v48\n" \
"v_mfma_f32_16x16x32_bf16 v[216:219], a[20:23], a[108:111], v[216:219]\n" \
"v_fmac_f32 v25, v30, v43\n" \
"v_fmac_f32 v26, v36, v47\n" \
"v_mfma_f32_16x16x32_bf16 v[220:223], a[24:27], a[104:107], v[220:223]\n" \
"ds_read_b64_tr_b16 a[82:83], v10 offset:33280\n" \
"v_fmac_f32 v25, v31, v42\n" \
"v_mfma_f32_16x16x32_bf16 v[220:223], a[28:31], a[108:111], v[220:223]\n" \
"v_fmac_f32 v26, v37, v46\n" \
"v_accvgpr_read_b32 v28, a222\n" \
"v_mfma_f32_16x16x32_bf16 v[232:235], a[32:35], a[104:107], v[232:235]\n" \
"s_mov_b32 s47, 2359296\n" \
"s_add_u32 m0, s22, 17280\n" \
"ds_read_b64_tr_b16 a[84:85], v9 offset:34304\n" \
"v_mfma_f32_16x16x32_bf16 v[232:235], a[36:39], a[108:111], v[232:235]\n" \
"buffer_load_dwordx4 v21, s[32:35], s47 offen offset:128 lds\n" \
"v_mfma_f32_16x16x32_bf16 v[236:239], a[40:43], a[104:107], v[236:239]\n" \
"ds_read_b64_tr_b16 a[86:87], v10 offset:34304\n" \
"v_lshlrev_b32 v27, 16, v28\n" \
"v_mfma_f32_16x16x32_bf16 v[236:239], a[44:47], a[108:111], v[236:239]\n" \
"v_and_b32 v28, 0xffff0000, v28\n" \
"v_mul_f32 v29, v25, v27\n" \
"v_mfma_f32_16x16x32_bf16 v[248:251], a[48:51], a[104:107], v[248:251]\n" \
"s_add_u32 m0, s50, 40960\n" \
"ds_read_b64_tr_b16 a[88:89], v11 offset:33280\n" \
"v_mul_f32 v58, v26, v28\n" \
"v_mfma_f32_16x16x32_bf16 v[248:251], a[52:55], a[108:111], v[248:251]\n" \
"buffer_load_dwordx4 v15, s[28:31], s46 offen lds\n" \
"v_mfma_f32_16x16x32_bf16 v[252:255], a[56:59], a[104:107], v[252:255]\n" \
"ds_read_b64_tr_b16 a[90:91], v12 offset:33280\n" \
"v_accvgpr_read_b32 v38, a208\n" \
"v_mfma_f32_16x16x32_bf16 v[252:255], a[60:63], a[108:111], v[252:255]\n" \
"v_lshlrev_b32 v32, 16, v38\n" \
"v_and_b32 v38, 0xffff0000, v38\n" \
"v_mfma_f32_16x16x32_bf16 a[128:131], a[0:3], a[112:115], a[128:131]\n" \
"ds_read_b64_tr_b16 a[92:93], v11 offset:34304\n" \
"v_mul_f32 v25, v35, v45\n" \
"v_mfma_f32_16x16x32_bf16 a[128:131], a[4:7], a[116:119], a[128:131]\n" \
"v_mul_f32 v26, v41, v49\n" \
"v_fmac_f32 v25, v30, v44\n" \
"v_mfma_f32_16x16x32_bf16 a[132:135], a[8:11], a[112:115], a[132:135]\n" \
"ds_read_b64_tr_b16 a[94:95], v12 offset:34304\n" \
"v_fmac_f32 v26, v36, v48\n" \
"v_mfma_f32_16x16x32_bf16 a[132:135], a[12:15], a[116:119], a[132:135]\n" \
"v_fmac_f32 v25, v31, v43\n" \
"v_fmac_f32 v26, v37, v47\n" \
"v_mfma_f32_16x16x32_bf16 a[144:147], a[16:19], a[112:115], a[144:147]\n" \
"s_add_u32 m0, s22, 19072\n" \
"v_fmac_f32 v25, v32, v42\n" \
"v_fmac_f32 v26, v38, v46\n" \
"v_mfma_f32_16x16x32_bf16 a[144:147], a[20:23], a[116:119], a[144:147]\n" \
"buffer_load_dwordx4 v21, s[32:35], s47 offen offset:384 lds\n" \
"v_mfma_f32_16x16x32_bf16 a[148:151], a[24:27], a[112:115], a[148:151]\n" \
"v_accvgpr_read_b32 v28, a224\n" \
"v_lshlrev_b32 v27, 16, v28\n" \
"v_mfma_f32_16x16x32_bf16 a[148:151], a[28:31], a[116:119], a[148:151]\n" \
"v_and_b32 v28, 0xffff0000, v28\n" \
"v_mul_f32 v25, v25, v27\n" \
"v_mfma_f32_16x16x32_bf16 a[160:163], a[32:35], a[112:115], a[160:163]\n" \
"v_mul_f32 v26, v26, v28\n" \
"v_mfma_f32_16x16x32_bf16 a[160:163], a[36:39], a[116:119], a[160:163]\n" \
"v_cvt_pk_bf16_f32 v51, v25, v29\n" \
"v_cvt_pk_bf16_f32 v55, v26, v58\n" \
"v_mfma_f32_16x16x32_bf16 a[164:167], a[40:43], a[112:115], a[164:167]\n" \
"v_accvgpr_read_b32 v39, a210\n" \
"v_lshlrev_b32 v33, 16, v39\n" \
"v_mfma_f32_16x16x32_bf16 a[164:167], a[44:47], a[116:119], a[164:167]\n" \
"s_waitcnt vmcnt(23) lgkmcnt(0)\n" \
"v_and_b32 v39, 0xffff0000, v39\n" \
"v_mfma_f32_16x16x32_bf16 a[176:179], a[48:51], a[112:115], a[176:179]\n" \
"s_add_u32 m0, s50, 45056\n" \
"s_barrier\n" \
"v_mul_f32 v25, v30, v45\n" \
"v_mfma_f32_16x16x32_bf16 a[176:179], a[52:55], a[116:119], a[176:179]\n" \
"buffer_load_dwordx4 v16, s[28:31], s46 offen lds\n" \
"v_mfma_f32_16x16x32_bf16 a[180:183], a[56:59], a[112:115], a[180:183]\n" \
"v_mul_f32 v26, v36, v49\n" \
"v_fmac_f32 v25, v31, v44\n" \
"v_mfma_f32_16x16x32_bf16 a[180:183], a[60:63], a[116:119], a[180:183]\n" \
"v_fmac_f32 v26, v37, v48\n" \
"v_fmac_f32 v25, v32, v43\n" \
"v_mfma_f32_16x16x32_bf16 a[136:139], a[0:3], a[120:123], a[136:139]\n" \
"v_fmac_f32 v26, v38, v47\n" \
"v_mfma_f32_16x16x32_bf16 a[136:139], a[4:7], a[124:127], a[136:139]\n" \
"v_fmac_f32 v25, v33, v42\n" \
"v_fmac_f32 v26, v39, v46\n" \
"v_mfma_f32_16x16x32_bf16 a[140:143], a[8:11], a[120:123], a[140:143]\n" \
"ds_read_b128 a[0:3], v7 offset:0\n" \
"v_accvgpr_read_b32 v28, a226\n" \
"v_mfma_f32_16x16x32_bf16 a[140:143], a[12:15], a[124:127], a[140:143]\n" \
"ds_read_b128 a[4:7], v8 offset:0\n" \
"v_lshlrev_b32 v27, 16, v28\n" \
"v_mfma_f32_16x16x32_bf16 a[152:155], a[16:19], a[120:123], a[152:155]\n" \
"ds_read_b128 a[8:11], v7 offset:2048\n" \
"v_and_b32 v28, 0xffff0000, v28\n" \
"v_mfma_f32_16x16x32_bf16 a[152:155], a[20:23], a[124:127], a[152:155]\n" \
"ds_read_b128 a[12:15], v8 offset:2048\n" \
"v_mul_f32 v29, v25, v27\n" \
"v_mfma_f32_16x16x32_bf16 a[156:159], a[24:27], a[120:123], a[156:159]\n" \
"ds_read_b128 a[16:19], v7 offset:4096\n" \
"v_mul_f32 v58, v26, v28\n" \
"v_mfma_f32_16x16x32_bf16 a[156:159], a[28:31], a[124:127], a[156:159]\n" \
"ds_read_b128 a[20:23], v8 offset:4096\n" \
"v_accvgpr_read_b32 v40, a212\n" \
"v_mfma_f32_16x16x32_bf16 a[168:171], a[32:35], a[120:123], a[168:171]\n" \
"ds_read_b128 a[24:27], v7 offset:6144\n" \
"v_lshlrev_b32 v34, 16, v40\n" \
"v_mfma_f32_16x16x32_bf16 a[168:171], a[36:39], a[124:127], a[168:171]\n" \
"ds_read_b128 a[28:31], v8 offset:6144\n" \
"v_and_b32 v40, 0xffff0000, v40\n" \
"v_mfma_f32_16x16x32_bf16 a[172:175], a[40:43], a[120:123], a[172:175]\n" \
"ds_read_b128 a[32:35], v7 offset:8192\n" \
"v_mul_f32 v25, v31, v45\n" \
"v_mfma_f32_16x16x32_bf16 a[172:175], a[44:47], a[124:127], a[172:175]\n" \
"ds_read_b128 a[36:39], v8 offset:8192\n" \
"v_mul_f32 v26, v37, v49\n" \
"v_mfma_f32_16x16x32_bf16 a[184:187], a[48:51], a[120:123], a[184:187]\n" \
"ds_read_b128 a[40:43], v7 offset:10240\n" \
"v_fmac_f32 v25, v32, v44\n" \
"v_mfma_f32_16x16x32_bf16 a[184:187], a[52:55], a[124:127], a[184:187]\n" \
"ds_read_b128 a[44:47], v8 offset:10240\n" \
"v_fmac_f32 v26, v38, v48\n" \
"v_mfma_f32_16x16x32_bf16 a[188:191], a[56:59], a[120:123], a[188:191]\n" \
"ds_read_b128 a[48:51], v7 offset:12288\n" \
"v_fmac_f32 v25, v33, v43\n" \
"v_mfma_f32_16x16x32_bf16 a[188:191], a[60:63], a[124:127], a[188:191]\n" \
"s_waitcnt lgkmcnt(9)\n" \
"ds_read_b128 a[52:55], v8 offset:12288\n" \
"v_fmac_f32 v26, v39, v47\n" \
"v_mfma_f32_16x16x32_bf16 v[64:67], a[0:3], a[64:67], v[64:67]\n" \
"s_mov_b32 s47, 2621440\n" \
"s_add_u32 m0, s22, 32768\n" \
"ds_read_b128 a[56:59], v7 offset:14336\n" \
"v_mfma_f32_16x16x32_bf16 v[64:67], a[4:7], a[68:71], v[64:67]\n" \
"buffer_load_dwordx4 v21, s[32:35], s47 offen offset:0 lds\n" \
"v_mfma_f32_16x16x32_bf16 v[68:71], a[8:11], a[64:67], v[68:71]\n" \
"s_waitcnt lgkmcnt(7)\n" \
"ds_read_b128 a[60:63], v8 offset:14336\n" \
"v_fmac_f32 v25, v34, v42\n" \
"v_mfma_f32_16x16x32_bf16 v[68:71], a[12:15], a[68:71], v[68:71]\n" \
"v_fmac_f32 v26, v40, v46\n" \
"v_accvgpr_read_b32 v28, a228\n" \
"v_mfma_f32_16x16x32_bf16 v[80:83], a[16:19], a[64:67], v[80:83]\n" \
"s_mov_b32 s46, 640\n" \
"s_add_u32 m0, s50, 49152\n" \
"ds_read_b64_tr_b16 a[96:97], v9 offset:49152\n" \
"v_mfma_f32_16x16x32_bf16 v[80:83], a[20:23], a[68:71], v[80:83]\n" \
"buffer_load_dwordx4 v17, s[28:31], s46 offen lds\n" \
"v_mfma_f32_16x16x32_bf16 v[84:87], a[24:27], a[64:67], v[84:87]\n" \
"s_waitcnt lgkmcnt(5)\n" \
"ds_read_b64_tr_b16 a[98:99], v10 offset:49152\n" \
"v_lshlrev_b32 v27, 16, v28\n" \
"v_mfma_f32_16x16x32_bf16 v[84:87], a[28:31], a[68:71], v[84:87]\n" \
"v_and_b32 v28, 0xffff0000, v28\n" \
"v_mul_f32 v25, v25, v27\n" \
"v_mfma_f32_16x16x32_bf16 v[96:99], a[32:35], a[64:67], v[96:99]\n" \
"s_add_u32 m0, s22, 34560\n" \
"ds_read_b64_tr_b16 a[100:101], v9 offset:50176\n" \
"v_mul_f32 v26, v26, v28\n" \
"v_mfma_f32_16x16x32_bf16 v[96:99], a[36:39], a[68:71], v[96:99]\n" \
"buffer_load_dwordx4 v21, s[32:35], s47 offen offset:256 lds\n" \
"v_mfma_f32_16x16x32_bf16 v[100:103], a[40:43], a[64:67], v[100:103]\n" \
"s_waitcnt lgkmcnt(3)\n" \
"ds_read_b64_tr_b16 a[102:103], v10 offset:50176\n" \
"v_cvt_pk_bf16_f32 v50, v25, v29\n" \
"v_mfma_f32_16x16x32_bf16 v[100:103], a[44:47], a[68:71], v[100:103]\n" \
"s_mov_b32 s94, 79691776\n" \
"v_cvt_pk_bf16_f32 v54, v26, v58\n" \
"v_accvgpr_read_b32 v43, a234\n" \
"v_mfma_f32_16x16x32_bf16 v[112:115], a[48:51], a[64:67], v[112:115]\n" \
"s_add_u32 m0, s50, 53248\n" \
"ds_read_b64_tr_b16 a[104:105], v11 offset:49152\n" \
"v_lshlrev_b32 v42, 16, v43\n" \
"v_mfma_f32_16x16x32_bf16 v[112:115], a[52:55], a[68:71], v[112:115]\n" \
"buffer_load_dwordx4 v18, s[28:31], s46 offen lds\n" \
"v_mfma_f32_16x16x32_bf16 v[116:119], a[56:59], a[64:67], v[116:119]\n" \
"ds_read_b64_tr_b16 a[106:107], v12 offset:49152\n" \
"v_and_b32 v43, 0xffff0000, v43\n" \
"v_mfma_f32_16x16x32_bf16 v[116:119], a[60:63], a[68:71], v[116:119]\n" \
"buffer_store_dwordx4 v[50:53], v60, s[36:39], s94 offen offset:0 sc1\n" \
"v_mfma_f32_16x16x32_bf16 v[72:75], a[0:3], a[72:75], v[72:75]\n" \
"s_mov_b32 s47, 2883584\n" \
"s_add_u32 m0, s22, 33792\n" \
"ds_read_b64_tr_b16 a[108:109], v11 offset:50176\n" \
"v_mfma_f32_16x16x32_bf16 v[72:75], a[4:7], a[76:79], v[72:75]\n" \
"buffer_load_dwordx4 v21, s[32:35], s47 offen offset:0 lds\n" \
"v_mfma_f32_16x16x32_bf16 v[76:79], a[8:11], a[72:75], v[76:79]\n" \
"s_mov_b32 s95, 79695872\n" \
"ds_read_b64_tr_b16 a[110:111], v12 offset:50176\n" \
"v_accvgpr_read_b32 v45, a235\n" \
"v_mfma_f32_16x16x32_bf16 v[76:79], a[12:15], a[76:79], v[76:79]\n" \
"v_lshlrev_b32 v44, 16, v45\n" \
"v_and_b32 v45, 0xffff0000, v45\n" \
"v_mfma_f32_16x16x32_bf16 v[88:91], a[16:19], a[72:75], v[88:91]\n" \
"s_add_u32 m0, s50, 57344\n" \
"ds_read_b64_tr_b16 a[112:113], v9 offset:49664\n" \
"v_accvgpr_read_b32 v47, a236\n" \
"v_mfma_f32_16x16x32_bf16 v[88:91], a[20:23], a[76:79], v[88:91]\n" \
"buffer_load_dwordx4 v19, s[28:31], s46 offen lds\n" \
"v_mfma_f32_16x16x32_bf16 v[92:95], a[24:27], a[72:75], v[92:95]\n" \
"ds_read_b64_tr_b16 a[114:115], v10 offset:49664\n" \
"v_lshlrev_b32 v46, 16, v47\n" \
"v_mfma_f32_16x16x32_bf16 v[92:95], a[28:31], a[76:79], v[92:95]\n" \
"v_and_b32 v47, 0xffff0000, v47\n" \
"v_accvgpr_read_b32 v49, a237\n" \
"v_mfma_f32_16x16x32_bf16 v[104:107], a[32:35], a[72:75], v[104:107]\n" \
"s_add_u32 m0, s22, 35584\n" \
"ds_read_b64_tr_b16 a[116:117], v9 offset:50688\n" \
"v_lshlrev_b32 v48, 16, v49\n" \
"v_mfma_f32_16x16x32_bf16 v[104:107], a[36:39], a[76:79], v[104:107]\n" \
"buffer_load_dwordx4 v21, s[32:35], s47 offen offset:256 lds\n" \
"v_mfma_f32_16x16x32_bf16 v[108:111], a[40:43], a[72:75], v[108:111]\n" \
"s_add_u32 s84, s84, 1048576\ns_addc_u32 s85, s85, 0\n" \
"ds_read_b64_tr_b16 a[118:119], v10 offset:50688\n" \
"v_mfma_f32_16x16x32_bf16 v[108:111], a[44:47], a[76:79], v[108:111]\n" \
"v_and_b32 v49, 0xffff0000, v49\n" \
"v_accvgpr_read_b32 v36, a193\n" \
"v_mfma_f32_16x16x32_bf16 v[120:123], a[48:51], a[72:75], v[120:123]\n" \
"LH5_%=:\ns_cbranch_execz LH9_%=\n" \
"s_add_u32 m0, s50, 61440\n" \
"ds_read_b64_tr_b16 a[120:121], v11 offset:49664\n" \
"v_lshlrev_b32 v30, 16, v36\n" \
"v_mfma_f32_16x16x32_bf16 v[120:123], a[52:55], a[76:79], v[120:123]\n" \
"buffer_load_dwordx4 v20, s[28:31], s46 offen lds\n" \
"v_mfma_f32_16x16x32_bf16 v[124:127], a[56:59], a[72:75], v[124:127]\n" \
"ds_read_b64_tr_b16 a[122:123], v12 offset:49664\n" \
"v_and_b32 v36, 0xffff0000, v36\n" \
"v_mfma_f32_16x16x32_bf16 v[124:127], a[60:63], a[76:79], v[124:127]\n" \
"buffer_store_dwordx4 v[54:57], v60, s[36:39], s95 offen offset:0 sc1\n" \
"v_mfma_f32_16x16x32_bf16 v[128:131], a[0:3], a[80:83], v[128:131]\n" \
"ds_read_b64_tr_b16 a[124:125], v11 offset:50688\n" \
"v_accvgpr_read_b32 v37, a195\n" \
"v_mfma_f32_16x16x32_bf16 v[128:131], a[4:7], a[84:87], v[128:131]\n" \
"s_mov_b64 exec, 0xffff\nglobal_load_dword v22, v1, s[84:85]\ns_mov_b64 exec, -1\n" \
"v_mfma_f32_16x16x32_bf16 v[132:135], a[8:11], a[80:83], v[132:135]\n" \
"ds_read_b64_tr_b16 a[126:127], v12 offset:50688\n" \
"v_lshlrev_b32 v31, 16, v37\n" \
"v_mfma_f32_16x16x32_bf16 v[132:135], a[12:15], a[84:87], v[132:135]\n" \
"v_and_b32 v37, 0xffff0000, v37\n" \
"v_accvgpr_read_b32 v38, a197\n" \
"v_mfma_f32_16x16x32_bf16 v[144:147], a[16:19], a[80:83], v[144:147]\n" \
"v_lshlrev_b32 v32, 16, v38\n" \
"v_and_b32 v38, 0xffff0000, v38\n" \
"v_mfma_f32_16x16x32_bf16 v[144:147], a[20:23], a[84:87], v[144:147]\n" \
"s_mov_b64 exec, 0xffff\nglobal_load_dword v22, v1, s[76:77]\ns_mov_b64 exec, -1\n" \
"v_mfma_f32_16x16x32_bf16 v[148:151], a[24:27], a[80:83], v[148:151]\n" \
"v_accvgpr_read_b32 v39, a199\n" \
"v_lshlrev_b32 v33, 16, v39\n" \
"v_mfma_f32_16x16x32_bf16 v[148:151], a[28:31], a[84:87], v[148:151]\n" \
"v_and_b32 v39, 0xffff0000, v39\n" \
"v_mul_f32 v25, v30, v45\n" \
"v_mfma_f32_16x16x32_bf16 v[160:163], a[32:35], a[80:83], v[160:163]\n" \
"v_mul_f32 v26, v36, v49\n" \
"v_mfma_f32_16x16x32_bf16 v[160:163], a[36:39], a[84:87], v[160:163]\n" \
"v_fmac_f32 v25, v31, v44\n" \
"v_fmac_f32 v26, v37, v48\n" \
"v_mfma_f32_16x16x32_bf16 v[164:167], a[40:43], a[80:83], v[164:167]\n" \
"v_fmac_f32 v25, v32, v43\n" \
"v_fmac_f32 v26, v38, v47\n" \
"v_mfma_f32_16x16x32_bf16 v[164:167], a[44:47], a[84:87], v[164:167]\n" \
"s_waitcnt vmcnt(22) lgkmcnt(0)\n" \
"v_fmac_f32 v25, v33, v42\n" \
"v_mfma_f32_16x16x32_bf16 v[176:179], a[48:51], a[80:83], v[176:179]\n" \
"s_barrier\n" \
"v_fmac_f32 v26, v39, v46\n" \
"v_accvgpr_read_b32 v28, a215\n" \
"v_mfma_f32_16x16x32_bf16 v[176:179], a[52:55], a[84:87], v[176:179]\n" \
"v_lshlrev_b32 v27, 16, v28\n" \
"v_and_b32 v28, 0xffff0000, v28\n" \
"v_mfma_f32_16x16x32_bf16 v[180:183], a[56:59], a[80:83], v[180:183]\n" \
"v_mul_f32 v29, v25, v27\n" \
"v_mfma_f32_16x16x32_bf16 v[180:183], a[60:63], a[84:87], v[180:183]\n" \
"v_mul_f32 v58, v26, v28\n" \
"v_accvgpr_read_b32 v40, a201\n" \
"v_mfma_f32_16x16x32_bf16 v[136:139], a[0:3], a[88:91], v[136:139]\n" \
"v_lshlrev_b32 v34, 16, v40\n" \
"v_and_b32 v40, 0xffff0000, v40\n" \
"v_mfma_f32_16x16x32_bf16 v[136:139], a[4:7], a[92:95], v[136:139]\n" \
"v_mul_f32 v25, v31, v45\n" \
"v_mfma_f32_16x16x32_bf16 v[140:143], a[8:11], a[88:91], v[140:143]\n" \
"v_mul_f32 v26, v37, v49\n" \
"v_fmac_f32 v25, v32, v44\n" \
"v_mfma_f32_16x16x32_bf16 v[140:143], a[12:15], a[92:95], v[140:143]\n" \
"v_fmac_f32 v26, v38, v48\n" \
"v_fmac_f32 v25, v33, v43\n" \
"v_mfma_f32_16x16x32_bf16 v[152:155], a[16:19], a[88:91], v[152:155]\n" \
"v_fmac_f32 v26, v39, v47\n" \
"v_mfma_f32_16x16x32_bf16 v[152:155], a[20:23], a[92:95], v[152:155]\n" \
"v_fmac_f32 v25, v34, v42\n" \
"v_fmac_f32 v26, v40, v46\n" \
"v_mfma_f32_16x16x32_bf16 v[156:159], a[24:27], a[88:91], v[156:159]\n" \
"v_accvgpr_read_b32 v28, a217\n" \
"v_lshlrev_b32 v27, 16, v28\n" \
"v_mfma_f32_16x16x32_bf16 v[156:159], a[28:31], a[92:95], v[156:159]\n" \
"v_and_b32 v28, 0xffff0000, v28\n" \
"v_mfma_f32_16x16x32_bf16 v[168:171], a[32:35], a[88:91], v[168:171]\n" \
"v_mul_f32 v25, v25, v27\n" \
"v_mul_f32 v26, v26, v28\n" \
"v_mfma_f32_16x16x32_bf16 v[168:171], a[36:39], a[92:95], v[168:171]\n" \
"v_cvt_pk_bf16_f32 v53, v25, v29\n" \
"v_cvt_pk_bf16_f32 v57, v26, v58\n" \
"v_mfma_f32_16x16x32_bf16 v[172:175], a[40:43], a[88:91], v[172:175]\n" \
"v_accvgpr_read_b32 v41, a203\n" \
"v_mfma_f32_16x16x32_bf16 v[172:175], a[44:47], a[92:95], v[172:175]\n" \
"v_lshlrev_b32 v35, 16, v41\n" \
"v_and_b32 v41, 0xffff0000, v41\n" \
"v_mfma_f32_16x16x32_bf16 v[184:187], a[48:51], a[88:91], v[184:187]\n" \
"v_mul_f32 v25, v32, v45\n" \
"v_mul_f32 v26, v38, v49\n" \
"v_mfma_f32_16x16x32_bf16 v[184:187], a[52:55], a[92:95], v[184:187]\n" \
"v_fmac_f32 v25, v33, v44\n" \
"v_mfma_f32_16x16x32_bf16 v[188:191], a[56:59], a[88:91], v[188:191]\n" \
"v_fmac_f32 v26, v39, v48\n" \
"v_fmac_f32 v25, v34, v43\n" \
"v_mfma_f32_16x16x32_bf16 v[188:191], a[60:63], a[92:95], v[188:191]\n" \
"v_fmac_f32 v26, v40, v47\n" \
"v_fmac_f32 v25, v35, v42\n" \
"v_mfma_f32_16x16x32_bf16 v[192:195], a[0:3], a[96:99], v[192:195]\n" \
"v_fmac_f32 v26, v41, v46\n" \
"v_mfma_f32_16x16x32_bf16 v[192:195], a[4:7], a[100:103], v[192:195]\n" \
"v_accvgpr_read_b32 v28, a219\n" \
"v_lshlrev_b32 v27, 16, v28\n" \
"v_mfma_f32_16x16x32_bf16 v[196:199], a[8:11], a[96:99], v[196:199]\n" \
"v_and_b32 v28, 0xffff0000, v28\n" \
"v_mul_f32 v29, v25, v27\n" \
"v_mfma_f32_16x16x32_bf16 v[196:199], a[12:15], a[100:103], v[196:199]\n" \
"v_mul_f32 v58, v26, v28\n" \
"v_mfma_f32_16x16x32_bf16 v[208:211], a[16:19], a[96:99], v[208:211]\n" \
"ds_read_b64_tr_b16 a[64:65], v9 offset:0\n" \
"v_accvgpr_read_b32 v36, a205\n" \
"v_mfma_f32_16x16x32_bf16 v[208:211], a[20:23], a[100:103], v[208:211]\n" \
"v_lshlrev_b32 v30, 16, v36\n" \
"v_and_b32 v36, 0xffff0000, v36\n" \
"v_mfma_f32_16x16x32_bf16 v[212:215], a[24:27], a[96:99], v[212:215]\n" \
"ds_read_b64_tr_b16 a[66:67], v10 offset:0\n" \
"v_mul_f32 v25, v33, v45\n" \
"v_mfma_f32_16x16x32_bf16 v[212:215], a[28:31], a[100:103], v[212:215]\n" \
"v_mul_f32 v26, v39, v49\n" \
"v_fmac_f32 v25, v34, v44\n" \
"v_mfma_f32_16x16x32_bf16 v[224:227], a[32:35], a[96:99], v[224:227]\n" \
"ds_read_b64_tr_b16 a[68:69], v9 offset:1024\n" \
"v_fmac_f32 v26, v40, v48\n" \
"v_mfma_f32_16x16x32_bf16 v[224:227], a[36:39], a[100:103], v[224:227]\n" \
"v_fmac_f32 v25, v35, v43\n" \
"v_fmac_f32 v26, v41, v47\n" \
"v_mfma_f32_16x16x32_bf16 v[228:231], a[40:43], a[96:99], v[228:231]\n" \
"ds_read_b64_tr_b16 a[70:71], v10 offset:1024\n" \
"v_fmac_f32 v25, v30, v42\n" \
"v_mfma_f32_16x16x32_bf16 v[228:231], a[44:47], a[100:103], v[228:231]\n" \
"v_fmac_f32 v26, v36, v46\n" \
"v_accvgpr_read_b32 v28, a221\n" \
"v_mfma_f32_16x16x32_bf16 v[240:243], a[48:51], a[96:99], v[240:243]\n" \
"ds_read_b64_tr_b16 a[72:73], v11 offset:0\n" \
"v_lshlrev_b32 v27, 16, v28\n" \
"v_mfma_f32_16x16x32_bf16 v[240:243], a[52:55], a[100:103], v[240:243]\n" \
"v_and_b32 v28, 0xffff0000, v28\n" \
"v_mul_f32 v25, v25, v27\n" \
"v_mfma_f32_16x16x32_bf16 v[244:247], a[56:59], a[96:99], v[244:247]\n" \
"ds_read_b64_tr_b16 a[74:75], v12 offset:0\n" \
"v_mul_f32 v26, v26, v28\n" \
"v_mfma_f32_16x16x32_bf16 v[244:247], a[60:63], a[100:103], v[244:247]\n" \
"v_cvt_pk_bf16_f32 v52, v25, v29\n" \
"v_cvt_pk_bf16_f32 v56, v26, v58\n" \
"v_mfma_f32_16x16x32_bf16 v[200:203], a[0:3], a[104:107], v[200:203]\n" \
"ds_read_b64_tr_b16 a[76:77], v11 offset:1024\n" \
"v_accvgpr_read_b32 v37, a207\n" \
"v_mfma_f32_16x16x32_bf16 v[200:203], a[4:7], a[108:111], v[200:203]\n" \
"v_lshlrev_b32 v31, 16, v37\n" \
"v_and_b32 v37, 0xffff0000, v37\n" \
"v_mfma_f32_16x16x32_bf16 v[204:207], a[8:11], a[104:107], v[204:207]\n" \
"ds_read_b64_tr_b16 a[78:79], v12 offset:1024\n" \
"v_mul_f32 v25, v34, v45\n" \
"v_mfma_f32_16x16x32_bf16 v[204:207], a[12:15], a[108:111], v[204:207]\n" \
"v_mul_f32 v26, v40, v49\n" \
"v_fmac_f32 v25, v35, v44\n" \
"v_mfma_f32_16x16x32_bf16 v[216:219], a[16:19], a[104:107], v[216:219]\n" \
"ds_read_b64_tr_b16 a[80:81], v9 offset:512\n" \
"v_fmac_f32 v26, v41, v48\n" \
"v_mfma_f32_16x16x32_bf16 v[216:219], a[20:23], a[108:111], v[216:219]\n" \
"v_fmac_f32 v25, v30, v43\n" \
"v_fmac_f32 v26, v36, v47\n" \
"v_mfma_f32_16x16x32_bf16 v[220:223], a[24:27], a[104:107], v[220:223]\n" \
"ds_read_b64_tr_b16 a[82:83], v10 offset:512\n" \
"v_fmac_f32 v25, v31, v42\n" \
"v_mfma_f32_16x16x32_bf16 v[220:223], a[28:31], a[108:111], v[220:223]\n" \
"v_fmac_f32 v26, v37, v46\n" \
"v_accvgpr_read_b32 v28, a223\n" \
"v_mfma_f32_16x16x32_bf16 v[232:235], a[32:35], a[104:107], v[232:235]\n" \
"ds_read_b64_tr_b16 a[84:85], v9 offset:1536\n" \
"v_lshlrev_b32 v27, 16, v28\n" \
"v_mfma_f32_16x16x32_bf16 v[232:235], a[36:39], a[108:111], v[232:235]\n" \
"v_and_b32 v28, 0xffff0000, v28\n" \
"v_mul_f32 v29, v25, v27\n" \
"v_mfma_f32_16x16x32_bf16 v[236:239], a[40:43], a[104:107], v[236:239]\n" \
"ds_read_b64_tr_b16 a[86:87], v10 offset:1536\n" \
"v_mul_f32 v58, v26, v28\n" \
"v_mfma_f32_16x16x32_bf16 v[236:239], a[44:47], a[108:111], v[236:239]\n" \
"v_accvgpr_read_b32 v38, a209\n" \
"v_lshlrev_b32 v32, 16, v38\n" \
"v_mfma_f32_16x16x32_bf16 v[248:251], a[48:51], a[104:107], v[248:251]\n" \
"ds_read_b64_tr_b16 a[88:89], v11 offset:512\n" \
"v_and_b32 v38, 0xffff0000, v38\n" \
"v_mfma_f32_16x16x32_bf16 v[248:251], a[52:55], a[108:111], v[248:251]\n" \
"v_mul_f32 v25, v35, v45\n" \
"v_mul_f32 v26, v41, v49\n" \
"v_mfma_f32_16x16x32_bf16 v[252:255], a[56:59], a[104:107], v[252:255]\n" \
"ds_read_b64_tr_b16 a[90:91], v12 offset:512\n" \
"v_fmac_f32 v25, v30, v44\n" \
"v_mfma_f32_16x16x32_bf16 v[252:255], a[60:63], a[108:111], v[252:255]\n" \
"v_fmac_f32 v26, v36, v48\n" \
"v_fmac_f32 v25, v31, v43\n" \
"v_mfma_f32_16x16x32_bf16 a[128:131], a[0:3], a[112:115], a[128:131]\n" \
"ds_read_b64_tr_b16 a[92:93], v11 offset:1536\n" \
"v_fmac_f32 v26, v37, v47\n" \
"v_mfma_f32_16x16x32_bf16 a[128:131], a[4:7], a[116:119], a[128:131]\n" \
"v_fmac_f32 v25, v32, v42\n" \
"v_fmac_f32 v26, v38, v46\n" \
"v_mfma_f32_16x16x32_bf16 a[132:135], a[8:11], a[112:115], a[132:135]\n" \
"ds_read_b64_tr_b16 a[94:95], v12 offset:1536\n" \
"v_accvgpr_read_b32 v28, a225\n" \
"v_mfma_f32_16x16x32_bf16 a[132:135], a[12:15], a[116:119], a[132:135]\n" \
"v_lshlrev_b32 v27, 16, v28\n" \
"v_and_b32 v28, 0xffff0000, v28\n" \
"v_mfma_f32_16x16x32_bf16 a[144:147], a[16:19], a[112:115], a[144:147]\n" \
"v_mul_f32 v25, v25, v27\n" \
"v_mul_f32 v26, v26, v28\n" \
"v_mfma_f32_16x16x32_bf16 a[144:147], a[20:23], a[116:119], a[144:147]\n" \
"v_cvt_pk_bf16_f32 v51, v25, v29\n" \
"v_mfma_f32_16x16x32_bf16 a[148:151], a[24:27], a[112:115], a[148:151]\n" \
"v_cvt_pk_bf16_f32 v55, v26, v58\n" \
"v_accvgpr_read_b32 v39, a211\n" \
"v_mfma_f32_16x16x32_bf16 a[148:151], a[28:31], a[116:119], a[148:151]\n" \
"v_lshlrev_b32 v33, 16, v39\n" \
"v_and_b32 v39, 0xffff0000, v39\n" \
"v_mfma_f32_16x16x32_bf16 a[160:163], a[32:35], a[112:115], a[160:163]\n" \
"v_mul_f32 v25, v30, v45\n" \
"v_mfma_f32_16x16x32_bf16 a[160:163], a[36:39], a[116:119], a[160:163]\n" \
"v_mul_f32 v26, v36, v49\n" \
"v_fmac_f32 v25, v31, v44\n" \
"v_mfma_f32_16x16x32_bf16 a[164:167], a[40:43], a[112:115], a[164:167]\n" \
"v_fmac_f32 v26, v37, v48\n" \
"v_fmac_f32 v25, v32, v43\n" \
"v_mfma_f32_16x16x32_bf16 a[164:167], a[44:47], a[116:119], a[164:167]\n" \
"s_waitcnt vmcnt(13) lgkmcnt(0)\n" \
"v_fmac_f32 v26, v38, v47\n" \
"v_mfma_f32_16x16x32_bf16 a[176:179], a[48:51], a[112:115], a[176:179]\n" \
"s_barrier\n" \
"v_fmac_f32 v25, v33, v42\n" \
"v_fmac_f32 v26, v39, v46\n" \
"v_mfma_f32_16x16x32_bf16 a[176:179], a[52:55], a[116:119], a[176:179]\n" \
"v_accvgpr_read_b32 v28, a227\n" \
"v_lshlrev_b32 v27, 16, v28\n" \
"v_mfma_f32_16x16x32_bf16 a[180:183], a[56:59], a[112:115], a[180:183]\n" \
"v_and_b32 v28, 0xffff0000, v28\n" \
"v_mfma_f32_16x16x32_bf16 a[180:183], a[60:63], a[116:119], a[180:183]\n" \
"v_mul_f32 v29, v25, v27\n" \
"v_mul_f32 v58, v26, v28\n" \
"v_mfma_f32_16x16x32_bf16 a[136:139], a[0:3], a[120:123], a[136:139]\n" \
"v_accvgpr_read_b32 v40, a213\n" \
"v_lshlrev_b32 v34, 16, v40\n" \
"v_mfma_f32_16x16x32_bf16 a[136:139], a[4:7], a[124:127], a[136:139]\n" \
"v_and_b32 v40, 0xffff0000, v40\n" \
"v_mfma_f32_16x16x32_bf16 a[140:143], a[8:11], a[120:123], a[140:143]\n" \
"ds_read_b128 a[0:3], v5 offset:0\n" \
"v_mul_f32 v25, v31, v45\n" \
"v_mfma_f32_16x16x32_bf16 a[140:143], a[12:15], a[124:127], a[140:143]\n" \
"ds_read_b128 a[4:7], v6 offset:0\n" \
"v_mul_f32 v26, v37, v49\n" \
"v_mfma_f32_16x16x32_bf16 a[152:155], a[16:19], a[120:123], a[152:155]\n" \
"ds_read_b128 a[8:11], v5 offset:2048\n" \
"v_fmac_f32 v25, v32, v44\n" \
"v_mfma_f32_16x16x32_bf16 a[152:155], a[20:23], a[124:127], a[152:155]\n" \
"ds_read_b128 a[12:15], v6 offset:2048\n" \
"v_fmac_f32 v26, v38, v48\n" \
"v_mfma_f32_16x16x32_bf16 a[156:159], a[24:27], a[120:123], a[156:159]\n" \
"ds_read_b128 a[16:19], v5 offset:4096\n" \
"v_fmac_f32 v25, v33, v43\n" \
"v_mfma_f32_16x16x32_bf16 a[156:159], a[28:31], a[124:127], a[156:159]\n" \
"ds_read_b128 a[20:23], v6 offset:4096\n" \
"v_fmac_f32 v26, v39, v47\n" \
"v_mfma_f32_16x16x32_bf16 a[168:171], a[32:35], a[120:123], a[168:171]\n" \
"ds_read_b128 a[24:27], v5 offset:6144\n" \
"v_fmac_f32 v25, v34, v42\n" \
"v_mfma_f32_16x16x32_bf16 a[168:171], a[36:39], a[124:127], a[168:171]\n" \
"ds_read_b128 a[28:31], v6 offset:6144\n" \
"v_fmac_f32 v26, v40, v46\n" \
"v_mfma_f32_16x16x32_bf16 a[172:175], a[40:43], a[120:123], a[172:175]\n" \
"ds_read_b128 a[32:35], v5 offset:8192\n" \
"v_accvgpr_read_b32 v28, a229\n" \
"v_mfma_f32_16x16x32_bf16 a[172:175], a[44:47], a[124:127], a[172:175]\n" \
"ds_read_b128 a[36:39], v6 offset:8192\n" \
"v_lshlrev_b32 v27, 16, v28\n" \
"v_mfma_f32_16x16x32_bf16 a[184:187], a[48:51], a[120:123], a[184:187]\n" \
"ds_read_b128 a[40:43], v5 offset:10240\n" \
"v_and_b32 v28, 0xffff0000, v28\n" \
"v_mfma_f32_16x16x32_bf16 a[184:187], a[52:55], a[124:127], a[184:187]\n" \
"ds_read_b128 a[44:47], v6 offset:10240\n" \
"v_mul_f32 v25, v25, v27\n" \
"v_mfma_f32_16x16x32_bf16 a[188:191], a[56:59], a[120:123], a[188:191]\n" \
"ds_read_b128 a[48:51], v5 offset:12288\n" \
"v_mul_f32 v26, v26, v28\n" \
"v_mfma_f32_16x16x32_bf16 a[188:191], a[60:63], a[124:127], a[188:191]\n" \
"s_waitcnt lgkmcnt(9)\n" \
"ds_read_b128 a[52:55], v6 offset:12288\n" \
"v_cvt_pk_bf16_f32 v50, v25, v29\n" \
"v_mfma_f32_16x16x32_bf16 v[64:67], a[0:3], a[64:67], v[64:67]\n" \
"s_mov_b32 s47, 2621440\n" \
"s_add_u32 m0, s22, 49024\n" \
"ds_read_b128 a[56:59], v5 offset:14336\n" \
"v_mfma_f32_16x16x32_bf16 v[64:67], a[4:7], a[68:71], v[64:67]\n" \
"buffer_load_dwordx4 v21, s[32:35], s47 offen offset:128 lds\n" \
"v_mfma_f32_16x16x32_bf16 v[68:71], a[8:11], a[64:67], v[68:71]\n" \
"s_waitcnt lgkmcnt(7)\n" \
"ds_read_b128 a[60:63], v6 offset:14336\n" \
"v_cvt_pk_bf16_f32 v54, v26, v58\n" \
"v_mfma_f32_16x16x32_bf16 v[68:71], a[12:15], a[68:71], v[68:71]\n" \
"s_mov_b32 s94, 79699968\n" \
"v_mfma_f32_16x16x32_bf16 v[80:83], a[16:19], a[64:67], v[80:83]\n" \
"s_add_u32 m0, s22, 50816\n" \
"ds_read_b64_tr_b16 a[96:97], v9 offset:16384\n" \
"v_mfma_f32_16x16x32_bf16 v[80:83], a[20:23], a[68:71], v[80:83]\n" \
"buffer_load_dwordx4 v21, s[32:35], s47 offen offset:384 lds\n" \
"v_mfma_f32_16x16x32_bf16 v[84:87], a[24:27], a[64:67], v[84:87]\n" \
"s_waitcnt lgkmcnt(5)\n" \
"ds_read_b64_tr_b16 a[98:99], v10 offset:16384\n" \
"v_mfma_f32_16x16x32_bf16 v[84:87], a[28:31], a[68:71], v[84:87]\n" \
"buffer_store_dwordx4 v[50:53], v60, s[36:39], s94 offen offset:0 sc1\n" \
"v_mfma_f32_16x16x32_bf16 v[96:99], a[32:35], a[64:67], v[96:99]\n" \
"s_mov_b32 s47, 2883584\n" \
"s_add_u32 m0, s22, 50048\n" \
"ds_read_b64_tr_b16 a[100:101], v9 offset:17408\n" \
"v_mfma_f32_16x16x32_bf16 v[96:99], a[36:39], a[68:71], v[96:99]\n" \
"buffer_load_dwordx4 v21, s[32:35], s47 offen offset:128 lds\n" \
"v_mfma_f32_16x16x32_bf16 v[100:103], a[40:43], a[64:67], v[100:103]\n" \
"s_waitcnt lgkmcnt(3)\n" \
"ds_read_b64_tr_b16 a[102:103], v10 offset:17408\n" \
"s_mov_b32 s95, 79704064\n" \
"v_mfma_f32_16x16x32_bf16 v[100:103], a[44:47], a[68:71], v[100:103]\n" \
"v_mfma_f32_16x16x32_bf16 v[112:115], a[48:51], a[64:67], v[112:115]\n" \
"s_add_u32 m0, s22, 51840\n" \
"ds_read_b64_tr_b16 a[104:105], v11 offset:16384\n" \
"v_mfma_f32_16x16x32_bf16 v[112:115], a[52:55], a[68:71], v[112:115]\n" \
"buffer_load_dwordx4 v21, s[32:35], s47 offen offset:384 lds\n" \
"v_mfma_f32_16x16x32_bf16 v[116:119], a[56:59], a[64:67], v[116:119]\n" \
"ds_read_b64_tr_b16 a[106:107], v12 offset:16384\n" \
"v_mfma_f32_16x16x32_bf16 v[116:119], a[60:63], a[68:71], v[116:119]\n" \
"v_mfma_f32_16x16x32_bf16 v[72:75], a[0:3], a[72:75], v[72:75]\n" \
"s_mov_b32 s46, 768\n" \
"s_add_u32 m0, s50, 65536\n" \
"ds_read_b64_tr_b16 a[108:109], v11 offset:17408\n" \
"v_mfma_f32_16x16x32_bf16 v[72:75], a[4:7], a[76:79], v[72:75]\n" \
"buffer_load_dwordx4 v13, s[28:31], s46 offen lds\n" \
"v_mfma_f32_16x16x32_bf16 v[76:79], a[8:11], a[72:75], v[76:79]\n" \
"ds_read_b64_tr_b16 a[110:111], v12 offset:17408\n" \
"v_mfma_f32_16x16x32_bf16 v[76:79], a[12:15], a[76:79], v[76:79]\n" \
"v_mfma_f32_16x16x32_bf16 v[88:91], a[16:19], a[72:75], v[88:91]\n" \
"s_add_u32 m0, s50, 69632\n" \
"ds_read_b64_tr_b16 a[112:113], v9 offset:16896\n" \
"v_mfma_f32_16x16x32_bf16 v[88:91], a[20:23], a[76:79], v[88:91]\n" \
"buffer_load_dwordx4 v14, s[28:31], s46 offen lds\n" \
"v_mfma_f32_16x16x32_bf16 v[92:95], a[24:27], a[72:75], v[92:95]\n" \
"ds_read_b64_tr_b16 a[114:115], v10 offset:16896\n" \
"v_mfma_f32_16x16x32_bf16 v[92:95], a[28:31], a[76:79], v[92:95]\n" \
"buffer_store_dwordx4 v[54:57], v60, s[36:39], s95 offen offset:0 sc1\n" \
"v_mfma_f32_16x16x32_bf16 v[104:107], a[32:35], a[72:75], v[104:107]\n" \
"s_add_u32 m0, s50, 73728\n" \
"ds_read_b64_tr_b16 a[116:117], v9 offset:17920\n" \
"s_lshl_b32 s62, s8, 2\n" \
"v_mfma_f32_16x16x32_bf16 v[104:107], a[36:39], a[76:79], v[104:107]\n" \
"buffer_load_dwordx4 v15, s[28:31], s46 offen lds\n" \
"v_mfma_f32_16x16x32_bf16 v[108:111], a[40:43], a[72:75], v[108:111]\n" \
"s_add_u32 s62, s62, %2\n" \
"ds_read_b64_tr_b16 a[118:119], v10 offset:17920\n" \
"s_lshl_b32 s63, s62, 12\n" \
"v_mfma_f32_16x16x32_bf16 v[108:111], a[44:47], a[76:79], v[108:111]\n" \
"s_add_u32 s68, s10, s63\ns_addc_u32 s69, s11, 0\n" \
"s_and_b32 s69, s69, 0xffff\n" \
"v_mfma_f32_16x16x32_bf16 v[120:123], a[48:51], a[72:75], v[120:123]\n" \
"s_add_u32 m0, s50, 77824\n" \
"ds_read_b64_tr_b16 a[120:121], v11 offset:16896\n" \
"s_mov_b32 s70, 4096\n" \
"v_mfma_f32_16x16x32_bf16 v[120:123], a[52:55], a[76:79], v[120:123]\n" \
"buffer_load_dwordx4 v16, s[28:31], s46 offen lds\n" \
"v_mfma_f32_16x16x32_bf16 v[124:127], a[56:59], a[72:75], v[124:127]\n" \
"s_mov_b32 s71, 0x00020000\n" \
"ds_read_b64_tr_b16 a[122:123], v12 offset:16896\n" \
"s_add_u32 s63, s63, 79691776\n" \
"v_mfma_f32_16x16x32_bf16 v[124:127], a[60:63], a[76:79], v[124:127]\n" \
"s_add_u32 s72, s4, s63\ns_addc_u32 s73, s5, 0\n" \
"s_and_b32 s73, s73, 0xffff\n" \
"v_mfma_f32_16x16x32_bf16 v[128:131], a[0:3], a[80:83], v[128:131]\n" \
"s_mov_b32 s46, 768\n" \
"s_add_u32 m0, s50, 81920\n" \
"ds_read_b64_tr_b16 a[124:125], v11 offset:17920\n" \
"v_mfma_f32_16x16x32_bf16 v[128:131], a[4:7], a[84:87], v[128:131]\n" \
"buffer_load_dwordx4 v17, s[28:31], s46 offen lds\n" \
"v_mfma_f32_16x16x32_bf16 v[132:135], a[8:11], a[80:83], v[132:135]\n" \
"s_mov_b32 s74, 4096\n" \
"ds_read_b64_tr_b16 a[126:127], v12 offset:17920\n" \
"s_mov_b32 s75, 0x00020000\n" \
"v_mfma_f32_16x16x32_bf16 v[132:135], a[12:15], a[84:87], v[132:135]\n" \
"v_add_u32 v23, -32, v4\n" \
"v_add_u32 v24, 992, v4\n" \
"v_mfma_f32_16x16x32_bf16 v[144:147], a[16:19], a[80:83], v[144:147]\n" \
"s_add_u32 m0, s50, 86016\n" \
"v_add_u32 v25, -16, v4\n" \
"v_add_u32 v26, -512, v4\n" \
"v_mfma_f32_16x16x32_bf16 v[144:147], a[20:23], a[84:87], v[144:147]\n" \
"buffer_load_dwordx4 v18, s[28:31], s46 offen lds\n" \
"v_mfma_f32_16x16x32_bf16 v[148:151], a[24:27], a[80:83], v[148:151]\n" \
"s_lshr_b32 s63, %2, 1\n" \
"v_add_u32 v27, 512, v4\n" \
"s_lshl_b32 s63, s63, 16\n" \
"v_mfma_f32_16x16x32_bf16 v[148:151], a[28:31], a[84:87], v[148:151]\n" \
"s_lshl_b32 s56, %2, 14\n" \
"s_add_u32 s56, s56, s63\n" \
"v_add_u32 v55, s56, v4\n" \
"v_mfma_f32_16x16x32_bf16 v[160:163], a[32:35], a[80:83], v[160:163]\n" \
"s_add_u32 m0, s50, 90112\n" \
"v_mov_b32 v28, 0\n" \
"s_lshl_b32 s62, s62, 1\n" \
"v_mfma_f32_16x16x32_bf16 v[160:163], a[36:39], a[84:87], v[160:163]\n" \
"buffer_load_dwordx4 v19, s[28:31], s46 offen lds\n" \
"v_mfma_f32_16x16x32_bf16 v[164:167], a[40:43], a[80:83], v[164:167]\n" \
"v_mov_b32 v29, s62\n" \
"v_lshlrev_b32 v56, 23, v4\n" \
"v_mfma_f32_16x16x32_bf16 v[164:167], a[44:47], a[84:87], v[164:167]\n" \
"s_waitcnt vmcnt(17) lgkmcnt(0)\n" \
"s_and_b32 s62, %1, 7\n" \
"s_lshl_b32 s62, s62, 8\n" \
"v_mfma_f32_16x16x32_bf16 v[176:179], a[48:51], a[80:83], v[176:179]\n" \
"s_add_u32 m0, s50, 94208\n" \
"s_barrier\n" \
"s_add_u32 s62, s62, 94380032\n" \
"v_mfma_f32_16x16x32_bf16 v[176:179], a[52:55], a[84:87], v[176:179]\n" \
"buffer_load_dwordx4 v20, s[28:31], s46 offen lds\n" \
"v_mfma_f32_16x16x32_bf16 v[180:183], a[56:59], a[80:83], v[180:183]\n" \
"v_bfe_u32 v57, v4, 4, 5\n" \
"v_lshl_add_u32 v57, v57, 3, s62\n" \
"v_mfma_f32_16x16x32_bf16 v[180:183], a[60:63], a[84:87], v[180:183]\n" \
"v_mfma_f32_16x16x32_bf16 v[136:139], a[0:3], a[88:91], v[136:139]\n" \
"s_mov_b32 s47, 3145728\n" \
"s_add_u32 m0, s22, 0\n" \
"v_mfma_f32_16x16x32_bf16 v[136:139], a[4:7], a[92:95], v[136:139]\n" \
"buffer_load_dwordx4 v21, s[32:35], s47 offen offset:0 lds\n" \
"v_mfma_f32_16x16x32_bf16 v[140:143], a[8:11], a[88:91], v[140:143]\n" \
"v_mfma_f32_16x16x32_bf16 v[140:143], a[12:15], a[92:95], v[140:143]\n" \
"v_mfma_f32_16x16x32_bf16 v[152:155], a[16:19], a[88:91], v[152:155]\n" \
"s_add_u32 m0, s22, 1792\n" \
"v_mfma_f32_16x16x32_bf16 v[152:155], a[20:23], a[92:95], v[152:155]\n" \
"buffer_load_dwordx4 v21, s[32:35], s47 offen offset:256 lds\n" \
"v_mfma_f32_16x16x32_bf16 v[156:159], a[24:27], a[88:91], v[156:159]\n" \
"v_mfma_f32_16x16x32_bf16 v[156:159], a[28:31], a[92:95], v[156:159]\n" \
"v_mfma_f32_16x16x32_bf16 v[168:171], a[32:35], a[88:91], v[168:171]\n" \
"s_mov_b32 s47, 3407872\n" \
"s_add_u32 m0, s22, 1024\n" \
"v_mfma_f32_16x16x32_bf16 v[168:171], a[36:39], a[92:95], v[168:171]\n" \
"buffer_load_dwordx4 v21, s[32:35], s47 offen offset:0 lds\n" \
"v_mfma_f32_16x16x32_bf16 v[172:175], a[40:43], a[88:91], v[172:175]\n" \
"s_add_u32 s90, s64, s53\ns_addc_u32 s91, s65, 0\n" \
"v_mfma_f32_16x16x32_bf16 v[172:175], a[44:47], a[92:95], v[172:175]\n" \
"v_mfma_f32_16x16x32_bf16 v[184:187], a[48:51], a[88:91], v[184:187]\n" \
"s_add_u32 m0, s22, 2816\n" \
"v_mfma_f32_16x16x32_bf16 v[184:187], a[52:55], a[92:95], v[184:187]\n" \
"buffer_load_dwordx4 v21, s[32:35], s47 offen offset:256 lds\n" \
"v_mfma_f32_16x16x32_bf16 v[188:191], a[56:59], a[88:91], v[188:191]\n" \
"v_mfma_f32_16x16x32_bf16 v[188:191], a[60:63], a[92:95], v[188:191]\n" \
"v_mfma_f32_16x16x32_bf16 v[192:195], a[0:3], a[96:99], v[192:195]\n" \
"v_mfma_f32_16x16x32_bf16 v[192:195], a[4:7], a[100:103], v[192:195]\n" \
"global_load_dwordx4 a[240:243], v4, s[90:91] offset:0\n" \
"v_mfma_f32_16x16x32_bf16 v[196:199], a[8:11], a[96:99], v[196:199]\n" \
"v_mfma_f32_16x16x32_bf16 v[196:199], a[12:15], a[100:103], v[196:199]\n" \
"v_mfma_f32_16x16x32_bf16 v[208:211], a[16:19], a[96:99], v[208:211]\n" \
"ds_read_b64_tr_b16 a[64:65], v9 offset:32768\n" \
"v_mfma_f32_16x16x32_bf16 v[208:211], a[20:23], a[100:103], v[208:211]\n" \
"global_load_dwordx4 a[244:247], v4, s[90:91] offset:1024\n" \
"v_mfma_f32_16x16x32_bf16 v[212:215], a[24:27], a[96:99], v[212:215]\n" \
"ds_read_b64_tr_b16 a[66:67], v10 offset:32768\n" \
"v_mfma_f32_16x16x32_bf16 v[212:215], a[28:31], a[100:103], v[212:215]\n" \
"v_mfma_f32_16x16x32_bf16 v[224:227], a[32:35], a[96:99], v[224:227]\n" \
"ds_read_b64_tr_b16 a[68:69], v9 offset:33792\n" \
"v_mfma_f32_16x16x32_bf16 v[224:227], a[36:39], a[100:103], v[224:227]\n" \
"global_load_dwordx4 a[248:251], v4, s[90:91] offset:2048\n" \
"v_mfma_f32_16x16x32_bf16 v[228:231], a[40:43], a[96:99], v[228:231]\n" \
"ds_read_b64_tr_b16 a[70:71], v10 offset:33792\n" \
"v_mfma_f32_16x16x32_bf16 v[228:231], a[44:47], a[100:103], v[228:231]\n" \
"v_mfma_f32_16x16x32_bf16 v[240:243], a[48:51], a[96:99], v[240:243]\n" \
"ds_read_b64_tr_b16 a[72:73], v11 offset:32768\n" \
"v_mfma_f32_16x16x32_bf16 v[240:243], a[52:55], a[100:103], v[240:243]\n" \
"global_load_dwordx4 a[252:255], v4, s[90:91] offset:3072\n" \
"v_mfma_f32_16x16x32_bf16 v[244:247], a[56:59], a[96:99], v[244:247]\n" \
"ds_read_b64_tr_b16 a[74:75], v12 offset:32768\n" \
"v_mfma_f32_16x16x32_bf16 v[244:247], a[60:63], a[100:103], v[244:247]\n" \
"v_mfma_f32_16x16x32_bf16 v[200:203], a[0:3], a[104:107], v[200:203]\n" \
"ds_read_b64_tr_b16 a[76:77], v11 offset:33792\n" \
"v_mfma_f32_16x16x32_bf16 v[200:203], a[4:7], a[108:111], v[200:203]\n" \
"v_mfma_f32_16x16x32_bf16 v[204:207], a[8:11], a[104:107], v[204:207]\n" \
"ds_read_b64_tr_b16 a[78:79], v12 offset:33792\n" \
"v_mfma_f32_16x16x32_bf16 v[204:207], a[12:15], a[108:111], v[204:207]\n" \
"v_mfma_f32_16x16x32_bf16 v[216:219], a[16:19], a[104:107], v[216:219]\n" \
"ds_read_b64_tr_b16 a[80:81], v9 offset:33280\n" \
"v_mfma_f32_16x16x32_bf16 v[216:219], a[20:23], a[108:111], v[216:219]\n" \
"v_mfma_f32_16x16x32_bf16 v[220:223], a[24:27], a[104:107], v[220:223]\n" \
"ds_read_b64_tr_b16 a[82:83], v10 offset:33280\n" \
"v_mfma_f32_16x16x32_bf16 v[220:223], a[28:31], a[108:111], v[220:223]\n" \
"v_mfma_f32_16x16x32_bf16 v[232:235], a[32:35], a[104:107], v[232:235]\n" \
"ds_read_b64_tr_b16 a[84:85], v9 offset:34304\n" \
"v_mfma_f32_16x16x32_bf16 v[232:235], a[36:39], a[108:111], v[232:235]\n" \
"v_mfma_f32_16x16x32_bf16 v[236:239], a[40:43], a[104:107], v[236:239]\n" \
"ds_read_b64_tr_b16 a[86:87], v10 offset:34304\n" \
"v_mfma_f32_16x16x32_bf16 v[236:239], a[44:47], a[108:111], v[236:239]\n" \
"v_mfma_f32_16x16x32_bf16 v[248:251], a[48:51], a[104:107], v[248:251]\n" \
"ds_read_b64_tr_b16 a[88:89], v11 offset:33280\n" \
"v_mfma_f32_16x16x32_bf16 v[248:251], a[52:55], a[108:111], v[248:251]\n" \
"v_mfma_f32_16x16x32_bf16 v[252:255], a[56:59], a[104:107], v[252:255]\n" \
"ds_read_b64_tr_b16 a[90:91], v12 offset:33280\n" \
"v_mfma_f32_16x16x32_bf16 v[252:255], a[60:63], a[108:111], v[252:255]\n" \
"v_mfma_f32_16x16x32_bf16 a[128:131], a[0:3], a[112:115], a[128:131]\n" \
"ds_read_b64_tr_b16 a[92:93], v11 offset:34304\n" \
"v_mfma_f32_16x16x32_bf16 a[128:131], a[4:7], a[116:119], a[128:131]\n" \
"v_mfma_f32_16x16x32_bf16 a[132:135], a[8:11], a[112:115], a[132:135]\n" \
"ds_read_b64_tr_b16 a[94:95], v12 offset:34304\n" \
"v_mfma_f32_16x16x32_bf16 a[132:135], a[12:15], a[116:119], a[132:135]\n" \
"v_mfma_f32_16x16x32_bf16 a[144:147], a[16:19], a[112:115], a[144:147]\n" \
"v_mfma_f32_16x16x32_bf16 a[144:147], a[20:23], a[116:119], a[144:147]\n" \
"v_mfma_f32_16x16x32_bf16 a[148:151], a[24:27], a[112:115], a[148:151]\n" \
"v_mfma_f32_16x16x32_bf16 a[148:151], a[28:31], a[116:119], a[148:151]\n" \
"global_load_dwordx4 a[192:195], v4, s[86:87] offset:0\n" \
"v_mfma_f32_16x16x32_bf16 a[160:163], a[32:35], a[112:115], a[160:163]\n" \
"v_mov_b32 v58, s14\n" \
"v_mfma_f32_16x16x32_bf16 a[160:163], a[36:39], a[116:119], a[160:163]\n" \
"v_mov_b32 v59, s15\n" \
"v_mfma_f32_16x16x32_bf16 a[164:167], a[40:43], a[112:115], a[164:167]\n" \
"v_mfma_f32_16x16x32_bf16 a[164:167], a[44:47], a[116:119], a[164:167]\n" \
"s_waitcnt vmcnt(15) lgkmcnt(0)\n" \
"v_mfma_f32_16x16x32_bf16 a[176:179], a[48:51], a[112:115], a[176:179]\n" \
"s_barrier\n" \
"v_mfma_f32_16x16x32_bf16 a[176:179], a[52:55], a[116:119], a[176:179]\n" \
"v_mfma_f32_16x16x32_bf16 a[180:183], a[56:59], a[112:115], a[180:183]\n" \
"v_mfma_f32_16x16x32_bf16 a[180:183], a[60:63], a[116:119], a[180:183]\n" \
"buffer_store_dwordx2 v[58:59], v56, s[36:39], s23 offen sc1\n" \
"v_mfma_f32_16x16x32_bf16 a[136:139], a[0:3], a[120:123], a[136:139]\n" \
"v_mfma_f32_16x16x32_bf16 a[136:139], a[4:7], a[124:127], a[136:139]\n" \
"v_mfma_f32_16x16x32_bf16 a[140:143], a[8:11], a[120:123], a[140:143]\n" \
"ds_read_b128 a[0:3], v5 offset:32768\n" \
"v_mfma_f32_16x16x32_bf16 a[140:143], a[12:15], a[124:127], a[140:143]\n" \
"ds_read_b128 a[4:7], v6 offset:32768\n" \
"v_mfma_f32_16x16x32_bf16 a[152:155], a[16:19], a[120:123], a[152:155]\n" \
"ds_read_b128 a[8:11], v5 offset:34816\n" \
"v_mfma_f32_16x16x32_bf16 a[152:155], a[20:23], a[124:127], a[152:155]\n" \
"ds_read_b128 a[12:15], v6 offset:34816\n" \
"v_mfma_f32_16x16x32_bf16 a[156:159], a[24:27], a[120:123], a[156:159]\n" \
"ds_read_b128 a[16:19], v5 offset:36864\n" \
"v_mfma_f32_16x16x32_bf16 a[156:159], a[28:31], a[124:127], a[156:159]\n" \
"ds_read_b128 a[20:23], v6 offset:36864\n" \
"v_mfma_f32_16x16x32_bf16 a[168:171], a[32:35], a[120:123], a[168:171]\n" \
"ds_read_b128 a[24:27], v5 offset:38912\n" \
"v_mfma_f32_16x16x32_bf16 a[168:171], a[36:39], a[124:127], a[168:171]\n" \
"ds_read_b128 a[28:31], v6 offset:38912\n" \
"v_mfma_f32_16x16x32_bf16 a[172:175], a[40:43], a[120:123], a[172:175]\n" \
"ds_read_b128 a[32:35], v5 offset:40960\n" \
"v_mfma_f32_16x16x32_bf16 a[172:175], a[44:47], a[124:127], a[172:175]\n" \
"ds_read_b128 a[36:39], v6 offset:40960\n" \
"v_mfma_f32_16x16x32_bf16 a[184:187], a[48:51], a[120:123], a[184:187]\n" \
"ds_read_b128 a[40:43], v5 offset:43008\n" \
"v_mfma_f32_16x16x32_bf16 a[184:187], a[52:55], a[124:127], a[184:187]\n" \
"ds_read_b128 a[44:47], v6 offset:43008\n" \
"v_mfma_f32_16x16x32_bf16 a[188:191], a[56:59], a[120:123], a[188:191]\n" \
"ds_read_b128 a[48:51], v5 offset:45056\n" \
"v_mfma_f32_16x16x32_bf16 a[188:191], a[60:63], a[124:127], a[188:191]\n" \
"s_waitcnt lgkmcnt(9)\n" \
"ds_read_b128 a[52:55], v6 offset:45056\n" \
"v_mfma_f32_16x16x32_bf16 v[64:67], a[0:3], a[64:67], v[64:67]\n" \
"s_mov_b32 s47, 3145728\n" \
"s_add_u32 m0, s22, 16256\n" \
"ds_read_b128 a[56:59], v5 offset:47104\n" \
"v_mfma_f32_16x16x32_bf16 v[64:67], a[4:7], a[68:71], v[64:67]\n" \
"buffer_load_dwordx4 v21, s[32:35], s47 offen offset:128 lds\n" \
"v_mfma_f32_16x16x32_bf16 v[68:71], a[8:11], a[64:67], v[68:71]\n" \
"s_waitcnt lgkmcnt(7)\n" \
"ds_read_b128 a[60:63], v6 offset:47104\n" \
"v_mfma_f32_16x16x32_bf16 v[68:71], a[12:15], a[68:71], v[68:71]\n" \
"v_mfma_f32_16x16x32_bf16 v[80:83], a[16:19], a[64:67], v[80:83]\n" \
"s_add_u32 m0, s22, 18048\n" \
"ds_read_b64_tr_b16 a[96:97], v9 offset:49152\n" \
"v_mfma_f32_16x16x32_bf16 v[80:83], a[20:23], a[68:71], v[80:83]\n" \
"buffer_load_dwordx4 v21, s[32:35], s47 offen offset:384 lds\n" \
"v_mfma_f32_16x16x32_bf16 v[84:87], a[24:27], a[64:67], v[84:87]\n" \
"s_waitcnt lgkmcnt(5)\n" \
"ds_read_b64_tr_b16 a[98:99], v10 offset:49152\n" \
"v_mfma_f32_16x16x32_bf16 v[84:87], a[28:31], a[68:71], v[84:87]\n" \
"v_mfma_f32_16x16x32_bf16 v[96:99], a[32:35], a[64:67], v[96:99]\n" \
"s_mov_b32 s47, 3407872\n" \
"s_add_u32 m0, s22, 17280\n" \
"ds_read_b64_tr_b16 a[100:101], v9 offset:50176\n" \
"v_mfma_f32_16x16x32_bf16 v[96:99], a[36:39], a[68:71], v[96:99]\n" \
"LH6_%=:\ns_cbranch_execz LH10_%=\n" \
"buffer_load_dwordx4 v21, s[32:35], s47 offen offset:128 lds\n" \
"v_mfma_f32_16x16x32_bf16 v[100:103], a[40:43], a[64:67], v[100:103]\n" \
"s_waitcnt lgkmcnt(3)\n" \
"ds_read_b64_tr_b16 a[102:103], v10 offset:50176\n" \
"v_mfma_f32_16x16x32_bf16 v[100:103], a[44:47], a[68:71], v[100:103]\n" \
"v_mfma_f32_16x16x32_bf16 v[112:115], a[48:51], a[64:67], v[112:115]\n" \
"s_add_u32 m0, s22, 19072\n" \
"ds_read_b64_tr_b16 a[104:105], v11 offset:49152\n" \
"v_mfma_f32_16x16x32_bf16 v[112:115], a[52:55], a[68:71], v[112:115]\n" \
"buffer_load_dwordx4 v21, s[32:35], s47 offen offset:384 lds\n" \
"v_mfma_f32_16x16x32_bf16 v[116:119], a[56:59], a[64:67], v[116:119]\n" \
"ds_read_b64_tr_b16 a[106:107], v12 offset:49152\n" \
"v_mfma_f32_16x16x32_bf16 v[116:119], a[60:63], a[68:71], v[116:119]\n" \
"v_mfma_f32_16x16x32_bf16 v[72:75], a[0:3], a[72:75], v[72:75]\n" \
"s_mov_b32 s46, 896\n" \
"s_add_u32 m0, s50, 0\n" \
"ds_read_b64_tr_b16 a[108:109], v11 offset:50176\n" \
"v_mfma_f32_16x16x32_bf16 v[72:75], a[4:7], a[76:79], v[72:75]\n" \
"buffer_load_dwordx4 v13, s[28:31], s46 offen lds\n" \
"v_mfma_f32_16x16x32_bf16 v[76:79], a[8:11], a[72:75], v[76:79]\n" \
"ds_read_b64_tr_b16 a[110:111], v12 offset:50176\n" \
"v_mfma_f32_16x16x32_bf16 v[76:79], a[12:15], a[76:79], v[76:79]\n" \
"v_mfma_f32_16x16x32_bf16 v[88:91], a[16:19], a[72:75], v[88:91]\n" \
"s_add_u32 m0, s50, 4096\n" \
"ds_read_b64_tr_b16 a[112:113], v9 offset:49664\n" \
"v_mfma_f32_16x16x32_bf16 v[88:91], a[20:23], a[76:79], v[88:91]\n" \
"buffer_load_dwordx4 v14, s[28:31], s46 offen lds\n" \
"v_mfma_f32_16x16x32_bf16 v[92:95], a[24:27], a[72:75], v[92:95]\n" \
"ds_read_b64_tr_b16 a[114:115], v10 offset:49664\n" \
"v_mfma_f32_16x16x32_bf16 v[92:95], a[28:31], a[76:79], v[92:95]\n" \
"v_mfma_f32_16x16x32_bf16 v[104:107], a[32:35], a[72:75], v[104:107]\n" \
"s_add_u32 m0, s50, 8192\n" \
"ds_read_b64_tr_b16 a[116:117], v9 offset:50688\n" \
"v_mfma_f32_16x16x32_bf16 v[104:107], a[36:39], a[76:79], v[104:107]\n" \
"buffer_load_dwordx4 v15, s[28:31], s46 offen lds\n" \
"v_mfma_f32_16x16x32_bf16 v[108:111], a[40:43], a[72:75], v[108:111]\n" \
"ds_read_b64_tr_b16 a[118:119], v10 offset:50688\n" \
"v_mfma_f32_16x16x32_bf16 v[108:111], a[44:47], a[76:79], v[108:111]\n" \
"v_mfma_f32_16x16x32_bf16 v[120:123], a[48:51], a[72:75], v[120:123]\n" \
"s_add_u32 m0, s50, 12288\n" \
"ds_read_b64_tr_b16 a[120:121], v11 offset:49664\n" \
"v_mfma_f32_16x16x32_bf16 v[120:123], a[52:55], a[76:79], v[120:123]\n" \
"buffer_load_dwordx4 v16, s[28:31], s46 offen lds\n" \
"v_mfma_f32_16x16x32_bf16 v[124:127], a[56:59], a[72:75], v[124:127]\n" \
"ds_read_b64_tr_b16 a[122:123], v12 offset:49664\n" \
"v_mfma_f32_16x16x32_bf16 v[124:127], a[60:63], a[76:79], v[124:127]\n" \
"v_mfma_f32_16x16x32_bf16 v[128:131], a[0:3], a[80:83], v[128:131]\n" \
"s_mov_b32 s46, 896\n" \
"s_add_u32 m0, s50, 16384\n" \
"ds_read_b64_tr_b16 a[124:125], v11 offset:50688\n" \
"v_mfma_f32_16x16x32_bf16 v[128:131], a[4:7], a[84:87], v[128:131]\n" \
"buffer_load_dwordx4 v17, s[28:31], s46 offen lds\n" \
"v_mfma_f32_16x16x32_bf16 v[132:135], a[8:11], a[80:83], v[132:135]\n" \
"ds_read_b64_tr_b16 a[126:127], v12 offset:50688\n" \
"v_mfma_f32_16x16x32_bf16 v[132:135], a[12:15], a[84:87], v[132:135]\n" \
"v_mfma_f32_16x16x32_bf16 v[144:147], a[16:19], a[80:83], v[144:147]\n" \
"s_add_u32 m0, s50, 20480\n" \
"v_mfma_f32_16x16x32_bf16 v[144:147], a[20:23], a[84:87], v[144:147]\n" \
"buffer_load_dwordx4 v18, s[28:31], s46 offen lds\n" \
"v_mfma_f32_16x16x32_bf16 v[148:151], a[24:27], a[80:83], v[148:151]\n" \
"v_mfma_f32_16x16x32_bf16 v[148:151], a[28:31], a[84:87], v[148:151]\n" \
"v_mfma_f32_16x16x32_bf16 v[160:163], a[32:35], a[80:83], v[160:163]\n" \
"s_add_u32 m0, s50, 24576\n" \
"v_mfma_f32_16x16x32_bf16 v[160:163], a[36:39], a[84:87], v[160:163]\n" \
"buffer_load_dwordx4 v19, s[28:31], s46 offen lds\n" \
"v_mfma_f32_16x16x32_bf16 v[164:167], a[40:43], a[80:83], v[164:167]\n" \
"v_mfma_f32_16x16x32_bf16 v[164:167], a[44:47], a[84:87], v[164:167]\n" \
"s_waitcnt vmcnt(17) lgkmcnt(0)\n" \
"v_mfma_f32_16x16x32_bf16 v[176:179], a[48:51], a[80:83], v[176:179]\n" \
"s_add_u32 m0, s50, 28672\n" \
"s_barrier\n" \
"v_mfma_f32_16x16x32_bf16 v[176:179], a[52:55], a[84:87], v[176:179]\n" \
"buffer_load_dwordx4 v20, s[28:31], s46 offen lds\n" \
"v_mfma_f32_16x16x32_bf16 v[180:183], a[56:59], a[80:83], v[180:183]\n" \
"v_mfma_f32_16x16x32_bf16 v[180:183], a[60:63], a[84:87], v[180:183]\n" \
"v_mfma_f32_16x16x32_bf16 v[136:139], a[0:3], a[88:91], v[136:139]\n" \
"s_mov_b32 s47, 3670016\n" \
"s_add_u32 m0, s22, 32768\n" \
"v_mfma_f32_16x16x32_bf16 v[136:139], a[4:7], a[92:95], v[136:139]\n" \
"buffer_load_dwordx4 v21, s[32:35], s47 offen offset:0 lds\n" \
"v_mfma_f32_16x16x32_bf16 v[140:143], a[8:11], a[88:91], v[140:143]\n" \
"v_mfma_f32_16x16x32_bf16 v[140:143], a[12:15], a[92:95], v[140:143]\n" \
"v_mfma_f32_16x16x32_bf16 v[152:155], a[16:19], a[88:91], v[152:155]\n" \
"s_add_u32 m0, s22, 34560\n" \
"v_mfma_f32_16x16x32_bf16 v[152:155], a[20:23], a[92:95], v[152:155]\n" \
"buffer_load_dwordx4 v21, s[32:35], s47 offen offset:256 lds\n" \
"v_mfma_f32_16x16x32_bf16 v[156:159], a[24:27], a[88:91], v[156:159]\n" \
"v_mfma_f32_16x16x32_bf16 v[156:159], a[28:31], a[92:95], v[156:159]\n" \
"v_mfma_f32_16x16x32_bf16 v[168:171], a[32:35], a[88:91], v[168:171]\n" \
"s_mov_b32 s47, 3932160\n" \
"s_add_u32 m0, s22, 33792\n" \
"v_mfma_f32_16x16x32_bf16 v[168:171], a[36:39], a[92:95], v[168:171]\n" \
"buffer_load_dwordx4 v21, s[32:35], s47 offen offset:0 lds\n" \
"v_mfma_f32_16x16x32_bf16 v[172:175], a[40:43], a[88:91], v[172:175]\n" \
"v_mfma_f32_16x16x32_bf16 v[172:175], a[44:47], a[92:95], v[172:175]\n" \
"v_mfma_f32_16x16x32_bf16 v[184:187], a[48:51], a[88:91], v[184:187]\n" \
"s_add_u32 m0, s22, 35584\n" \
"v_mfma_f32_16x16x32_bf16 v[184:187], a[52:55], a[92:95], v[184:187]\n" \
"buffer_load_dwordx4 v21, s[32:35], s47 offen offset:256 lds\n" \
"v_mfma_f32_16x16x32_bf16 v[188:191], a[56:59], a[88:91], v[188:191]\n" \
"v_mfma_f32_16x16x32_bf16 v[188:191], a[60:63], a[92:95], v[188:191]\n" \
"v_mfma_f32_16x16x32_bf16 v[192:195], a[0:3], a[96:99], v[192:195]\n" \
"v_mfma_f32_16x16x32_bf16 v[192:195], a[4:7], a[100:103], v[192:195]\n" \
"buffer_load_dwordx4 v[30:33], v23, s[68:71], 0 offen offset:0 nt\n" \
"v_mfma_f32_16x16x32_bf16 v[196:199], a[8:11], a[96:99], v[196:199]\n" \
"s_mov_b64 s[28:29], s[64:65]\n" \
"v_mfma_f32_16x16x32_bf16 v[196:199], a[12:15], a[100:103], v[196:199]\n" \
"v_mfma_f32_16x16x32_bf16 v[208:211], a[16:19], a[96:99], v[208:211]\n" \
"ds_read_b64_tr_b16 a[64:65], v9 offset:0\n" \
"v_mfma_f32_16x16x32_bf16 v[208:211], a[20:23], a[100:103], v[208:211]\n" \
"buffer_load_dwordx4 v[34:37], v24, s[68:71], 0 offen offset:0 nt\n" \
"v_mfma_f32_16x16x32_bf16 v[212:215], a[24:27], a[96:99], v[212:215]\n" \
"s_and_b32 s29, s29, 0xffff\n" \
"ds_read_b64_tr_b16 a[66:67], v10 offset:0\n" \
"v_mfma_f32_16x16x32_bf16 v[212:215], a[28:31], a[100:103], v[212:215]\n" \
"v_mfma_f32_16x16x32_bf16 v[224:227], a[32:35], a[96:99], v[224:227]\n" \
"ds_read_b64_tr_b16 a[68:69], v9 offset:1024\n" \
"v_mfma_f32_16x16x32_bf16 v[224:227], a[36:39], a[100:103], v[224:227]\n" \
"buffer_load_dwordx4 v[38:41], v24, s[68:71], 0 offen offset:1024 nt\n" \
"v_mfma_f32_16x16x32_bf16 v[228:231], a[40:43], a[96:99], v[228:231]\n" \
"s_mov_b32 s30, 4194304\n" \
"ds_read_b64_tr_b16 a[70:71], v10 offset:1024\n" \
"v_mfma_f32_16x16x32_bf16 v[228:231], a[44:47], a[100:103], v[228:231]\n" \
"v_mfma_f32_16x16x32_bf16 v[240:243], a[48:51], a[96:99], v[240:243]\n" \
"ds_read_b64_tr_b16 a[72:73], v11 offset:0\n" \
"v_mfma_f32_16x16x32_bf16 v[240:243], a[52:55], a[100:103], v[240:243]\n" \
"buffer_load_dwordx4 v[42:45], v24, s[68:71], 0 offen offset:2048 nt\n" \
"v_mfma_f32_16x16x32_bf16 v[244:247], a[56:59], a[96:99], v[244:247]\n" \
"s_lshl_b32 s60, s7, 18\n" \
"ds_read_b64_tr_b16 a[74:75], v12 offset:0\n" \
"v_mfma_f32_16x16x32_bf16 v[244:247], a[60:63], a[100:103], v[244:247]\n" \
"v_mfma_f32_16x16x32_bf16 v[200:203], a[0:3], a[104:107], v[200:203]\n" \
"ds_read_b64_tr_b16 a[76:77], v11 offset:1024\n" \
"v_mfma_f32_16x16x32_bf16 v[200:203], a[4:7], a[108:111], v[200:203]\n" \
"buffer_load_dwordx4 v[46:49], v24, s[68:71], 0 offen offset:3072 nt\n" \
"v_mfma_f32_16x16x32_bf16 v[204:207], a[8:11], a[104:107], v[204:207]\n" \
"s_sub_u32 s61, 3932160, s60\n" \
"ds_read_b64_tr_b16 a[78:79], v12 offset:1024\n" \
"v_mfma_f32_16x16x32_bf16 v[204:207], a[12:15], a[108:111], v[204:207]\n" \
"v_mfma_f32_16x16x32_bf16 v[216:219], a[16:19], a[104:107], v[216:219]\n" \
"ds_read_b64_tr_b16 a[80:81], v9 offset:512\n" \
"v_mfma_f32_16x16x32_bf16 v[216:219], a[20:23], a[108:111], v[216:219]\n" \
"buffer_load_dword v50, v25, s[68:71], 0 offen offset:0\n" \
"v_mfma_f32_16x16x32_bf16 v[220:223], a[24:27], a[104:107], v[220:223]\n" \
"ds_read_b64_tr_b16 a[82:83], v10 offset:512\n" \
"v_add_u32 v13, s60, v63\n" \
"v_mfma_f32_16x16x32_bf16 v[220:223], a[28:31], a[108:111], v[220:223]\n" \
"v_mfma_f32_16x16x32_bf16 v[232:235], a[32:35], a[104:107], v[232:235]\n" \
"ds_read_b64_tr_b16 a[84:85], v9 offset:1536\n" \
"v_mfma_f32_16x16x32_bf16 v[232:235], a[36:39], a[108:111], v[232:235]\n" \
"buffer_load_dword v51, v24, s[68:71], 0 offen offset:16\n" \
"v_mfma_f32_16x16x32_bf16 v[236:239], a[40:43], a[104:107], v[236:239]\n" \
"ds_read_b64_tr_b16 a[86:87], v10 offset:1536\n" \
"v_add_u32 v17, s61, v63\n" \
"v_mfma_f32_16x16x32_bf16 v[236:239], a[44:47], a[108:111], v[236:239]\n" \
"v_mfma_f32_16x16x32_bf16 v[248:251], a[48:51], a[104:107], v[248:251]\n" \
"ds_read_b64_tr_b16 a[88:89], v11 offset:512\n" \
"v_mfma_f32_16x16x32_bf16 v[248:251], a[52:55], a[108:111], v[248:251]\n" \
"buffer_load_dword v52, v24, s[68:71], 0 offen offset:1040\n" \
"v_mfma_f32_16x16x32_bf16 v[252:255], a[56:59], a[104:107], v[252:255]\n" \
"ds_read_b64_tr_b16 a[90:91], v12 offset:512\n" \
"v_add_u32 v14, 0x10000, v13\n" \
"v_mfma_f32_16x16x32_bf16 v[252:255], a[60:63], a[108:111], v[252:255]\n" \
"v_mfma_f32_16x16x32_bf16 a[128:131], a[0:3], a[112:115], a[128:131]\n" \
"ds_read_b64_tr_b16 a[92:93], v11 offset:1536\n" \
"v_mfma_f32_16x16x32_bf16 a[128:131], a[4:7], a[116:119], a[128:131]\n" \
"buffer_load_dword v53, v24, s[68:71], 0 offen offset:2064\n" \
"v_mfma_f32_16x16x32_bf16 a[132:135], a[8:11], a[112:115], a[132:135]\n" \
"ds_read_b64_tr_b16 a[94:95], v12 offset:1536\n" \
"v_add_u32 v18, 0x10000, v17\n" \
"v_mfma_f32_16x16x32_bf16 a[132:135], a[12:15], a[116:119], a[132:135]\n" \
"v_mfma_f32_16x16x32_bf16 a[144:147], a[16:19], a[112:115], a[144:147]\n" \
"v_mfma_f32_16x16x32_bf16 a[144:147], a[20:23], a[116:119], a[144:147]\n" \
"buffer_load_dword v54, v24, s[68:71], 0 offen offset:3088\n" \
"v_mfma_f32_16x16x32_bf16 a[148:151], a[24:27], a[112:115], a[148:151]\n" \
"v_add_u32 v15, 0x10000, v14\n" \
"v_mfma_f32_16x16x32_bf16 a[148:151], a[28:31], a[116:119], a[148:151]\n" \
"v_mfma_f32_16x16x32_bf16 a[160:163], a[32:35], a[112:115], a[160:163]\n" \
"v_mfma_f32_16x16x32_bf16 a[160:163], a[36:39], a[116:119], a[160:163]\n" \
"global_load_short_d16_hi v28, v29, s[12:13]\n" \
"v_mfma_f32_16x16x32_bf16 a[164:167], a[40:43], a[112:115], a[164:167]\n" \
"v_add_u32 v19, 0x10000, v18\n" \
"v_mfma_f32_16x16x32_bf16 a[164:167], a[44:47], a[116:119], a[164:167]\n" \
"s_waitcnt vmcnt(23) lgkmcnt(0)\n" \
"v_mfma_f32_16x16x32_bf16 a[176:179], a[48:51], a[112:115], a[176:179]\n" \
"s_barrier\n" \
"v_mfma_f32_16x16x32_bf16 a[176:179], a[52:55], a[116:119], a[176:179]\n" \
"global_load_dwordx4 a[196:199], v4, s[86:87] offset:1024\n" \
"v_mfma_f32_16x16x32_bf16 a[180:183], a[56:59], a[112:115], a[180:183]\n" \
"v_add_u32 v16, 0x10000, v15\n" \
"v_mfma_f32_16x16x32_bf16 a[180:183], a[60:63], a[116:119], a[180:183]\n" \
"v_mfma_f32_16x16x32_bf16 a[136:139], a[0:3], a[120:123], a[136:139]\n" \
"v_mfma_f32_16x16x32_bf16 a[136:139], a[4:7], a[124:127], a[136:139]\n" \
"v_mfma_f32_16x16x32_bf16 a[140:143], a[8:11], a[120:123], a[140:143]\n" \
"ds_read_b128 a[0:3], v7 offset:0\n" \
"v_add_u32 v20, 0x10000, v19\n" \
"v_mfma_f32_16x16x32_bf16 a[140:143], a[12:15], a[124:127], a[140:143]\n" \
"ds_read_b128 a[4:7], v8 offset:0\n" \
"v_mfma_f32_16x16x32_bf16 a[152:155], a[16:19], a[120:123], a[152:155]\n" \
"ds_read_b128 a[8:11], v7 offset:2048\n" \
"v_mfma_f32_16x16x32_bf16 a[152:155], a[20:23], a[124:127], a[152:155]\n" \
"ds_read_b128 a[12:15], v8 offset:2048\n" \
"v_mfma_f32_16x16x32_bf16 a[156:159], a[24:27], a[120:123], a[156:159]\n" \
"ds_read_b128 a[16:19], v7 offset:4096\n" \
"v_mfma_f32_16x16x32_bf16 a[156:159], a[28:31], a[124:127], a[156:159]\n" \
"ds_read_b128 a[20:23], v8 offset:4096\n" \
"v_mfma_f32_16x16x32_bf16 a[168:171], a[32:35], a[120:123], a[168:171]\n" \
"ds_read_b128 a[24:27], v7 offset:6144\n" \
"v_mfma_f32_16x16x32_bf16 a[168:171], a[36:39], a[124:127], a[168:171]\n" \
"ds_read_b128 a[28:31], v8 offset:6144\n" \
"v_mfma_f32_16x16x32_bf16 a[172:175], a[40:43], a[120:123], a[172:175]\n" \
"ds_read_b128 a[32:35], v7 offset:8192\n" \
"v_mfma_f32_16x16x32_bf16 a[172:175], a[44:47], a[124:127], a[172:175]\n" \
"ds_read_b128 a[36:39], v8 offset:8192\n" \
"v_mfma_f32_16x16x32_bf16 a[184:187], a[48:51], a[120:123], a[184:187]\n" \
"ds_read_b128 a[40:43], v7 offset:10240\n" \
"v_mfma_f32_16x16x32_bf16 a[184:187], a[52:55], a[124:127], a[184:187]\n" \
"ds_read_b128 a[44:47], v8 offset:10240\n" \
"v_mfma_f32_16x16x32_bf16 a[188:191], a[56:59], a[120:123], a[188:191]\n" \
"ds_read_b128 a[48:51], v7 offset:12288\n" \
"v_mfma_f32_16x16x32_bf16 a[188:191], a[60:63], a[124:127], a[188:191]\n" \
"s_waitcnt lgkmcnt(9)\n" \
"ds_read_b128 a[52:55], v8 offset:12288\n" \
"v_mfma_f32_16x16x32_bf16 v[64:67], a[0:3], a[64:67], v[64:67]\n" \
"ds_read_b128 a[56:59], v7 offset:14336\n" \
"v_mfma_f32_16x16x32_bf16 v[64:67], a[4:7], a[68:71], v[64:67]\n" \
"buffer_load_dwordx2 v[58:59], v57, s[36:39], 0 offen sc1\n" \
"v_mfma_f32_16x16x32_bf16 v[68:71], a[8:11], a[64:67], v[68:71]\n" \
"s_waitcnt lgkmcnt(7)\n" \
"ds_read_b128 a[60:63], v8 offset:14336\n" \
"v_mfma_f32_16x16x32_bf16 v[68:71], a[12:15], a[68:71], v[68:71]\n" \
"v_mfma_f32_16x16x32_bf16 v[80:83], a[16:19], a[64:67], v[80:83]\n" \
"s_mov_b32 s47, 3670016\n" \
"s_add_u32 m0, s22, 49024\n" \
"ds_read_b64_tr_b16 a[96:97], v9 offset:16384\n" \
"v_mfma_f32_16x16x32_bf16 v[80:83], a[20:23], a[68:71], v[80:83]\n" \
"buffer_load_dwordx4 v21, s[32:35], s47 offen offset:128 lds\n" \
"v_mfma_f32_16x16x32_bf16 v[84:87], a[24:27], a[64:67], v[84:87]\n" \
"s_waitcnt lgkmcnt(5)\n" \
"ds_read_b64_tr_b16 a[98:99], v10 offset:16384\n" \
"v_mfma_f32_16x16x32_bf16 v[84:87], a[28:31], a[68:71], v[84:87]\n" \
"v_mfma_f32_16x16x32_bf16 v[96:99], a[32:35], a[64:67], v[96:99]\n" \
"s_add_u32 m0, s22, 50816\n" \
"ds_read_b64_tr_b16 a[100:101], v9 offset:17408\n" \
"v_mfma_f32_16x16x32_bf16 v[96:99], a[36:39], a[68:71], v[96:99]\n" \
"buffer_load_dwordx4 v21, s[32:35], s47 offen offset:384 lds\n" \
"v_mfma_f32_16x16x32_bf16 v[100:103], a[40:43], a[64:67], v[100:103]\n" \
"s_waitcnt lgkmcnt(3)\n" \
"ds_read_b64_tr_b16 a[102:103], v10 offset:17408\n" \
"v_mfma_f32_16x16x32_bf16 v[100:103], a[44:47], a[68:71], v[100:103]\n" \
"v_mfma_f32_16x16x32_bf16 v[112:115], a[48:51], a[64:67], v[112:115]\n" \
"s_mov_b32 s47, 3932160\n" \
"s_add_u32 m0, s22, 50048\n" \
"ds_read_b64_tr_b16 a[104:105], v11 offset:16384\n" \
"v_mfma_f32_16x16x32_bf16 v[112:115], a[52:55], a[68:71], v[112:115]\n" \
"buffer_load_dwordx4 v21, s[32:35], s47 offen offset:128 lds\n" \
"v_mfma_f32_16x16x32_bf16 v[116:119], a[56:59], a[64:67], v[116:119]\n" \
"ds_read_b64_tr_b16 a[106:107], v12 offset:16384\n" \
"v_mfma_f32_16x16x32_bf16 v[116:119], a[60:63], a[68:71], v[116:119]\n" \
"s_add_u32 s76, s76, 2097152\ns_addc_u32 s77, s77, 0\n" \
"v_mfma_f32_16x16x32_bf16 v[72:75], a[0:3], a[72:75], v[72:75]\n" \
"s_add_u32 m0, s22, 51840\n" \
"ds_read_b64_tr_b16 a[108:109], v11 offset:17408\n" \
"v_mfma_f32_16x16x32_bf16 v[72:75], a[4:7], a[76:79], v[72:75]\n" \
"buffer_load_dwordx4 v21, s[32:35], s47 offen offset:384 lds\n" \
"v_mfma_f32_16x16x32_bf16 v[76:79], a[8:11], a[72:75], v[76:79]\n" \
"ds_read_b64_tr_b16 a[110:111], v12 offset:17408\n" \
"v_mfma_f32_16x16x32_bf16 v[76:79], a[12:15], a[76:79], v[76:79]\n" \
"v_mfma_f32_16x16x32_bf16 v[88:91], a[16:19], a[72:75], v[88:91]\n" \
"ds_read_b64_tr_b16 a[112:113], v9 offset:16896\n" \
"v_mfma_f32_16x16x32_bf16 v[88:91], a[20:23], a[76:79], v[88:91]\n" \
"v_mfma_f32_16x16x32_bf16 v[92:95], a[24:27], a[72:75], v[92:95]\n" \
"ds_read_b64_tr_b16 a[114:115], v10 offset:16896\n" \
"v_mfma_f32_16x16x32_bf16 v[92:95], a[28:31], a[76:79], v[92:95]\n" \
"s_mov_b64 exec, 0xffff\nglobal_load_dword v22, v1, s[76:77]\ns_mov_b64 exec, -1\n" \
"v_mfma_f32_16x16x32_bf16 v[104:107], a[32:35], a[72:75], v[104:107]\n" \
"s_add_u32 s76, s76, 2097152\ns_addc_u32 s77, s77, 0\n" \
"ds_read_b64_tr_b16 a[116:117], v9 offset:17920\n" \
"v_mfma_f32_16x16x32_bf16 v[104:107], a[36:39], a[76:79], v[104:107]\n" \
"v_mfma_f32_16x16x32_bf16 v[108:111], a[40:43], a[72:75], v[108:111]\n" \
"ds_read_b64_tr_b16 a[118:119], v10 offset:17920\n" \
"v_mfma_f32_16x16x32_bf16 v[108:111], a[44:47], a[76:79], v[108:111]\n" \
"s_mov_b64 exec, 0xffff\nglobal_load_dword v22, v1, s[76:77]\ns_mov_b64 exec, -1\n" \
"v_mfma_f32_16x16x32_bf16 v[120:123], a[48:51], a[72:75], v[120:123]\n" \
"ds_read_b64_tr_b16 a[120:121], v11 offset:16896\n" \
"v_mfma_f32_16x16x32_bf16 v[120:123], a[52:55], a[76:79], v[120:123]\n" \
"v_mfma_f32_16x16x32_bf16 v[124:127], a[56:59], a[72:75], v[124:127]\n" \
"ds_read_b64_tr_b16 a[122:123], v12 offset:16896\n" \
"v_mfma_f32_16x16x32_bf16 v[124:127], a[60:63], a[76:79], v[124:127]\n" \
"v_mfma_f32_16x16x32_bf16 v[128:131], a[0:3], a[80:83], v[128:131]\n" \
"ds_read_b64_tr_b16 a[124:125], v11 offset:17920\n" \
"v_mfma_f32_16x16x32_bf16 v[128:131], a[4:7], a[84:87], v[128:131]\n" \
"v_mfma_f32_16x16x32_bf16 v[132:135], a[8:11], a[80:83], v[132:135]\n" \
"ds_read_b64_tr_b16 a[126:127], v12 offset:17920\n" \
"v_mfma_f32_16x16x32_bf16 v[132:135], a[12:15], a[84:87], v[132:135]\n" \
"v_mfma_f32_16x16x32_bf16 v[144:147], a[16:19], a[80:83], v[144:147]\n" \
"v_mfma_f32_16x16x32_bf16 v[144:147], a[20:23], a[84:87], v[144:147]\n" \
"v_mfma_f32_16x16x32_bf16 v[148:151], a[24:27], a[80:83], v[148:151]\n" \
"v_mfma_f32_16x16x32_bf16 v[148:151], a[28:31], a[84:87], v[148:151]\n" \
"v_mfma_f32_16x16x32_bf16 v[160:163], a[32:35], a[80:83], v[160:163]\n" \
"v_mfma_f32_16x16x32_bf16 v[160:163], a[36:39], a[84:87], v[160:163]\n" \
"v_mfma_f32_16x16x32_bf16 v[164:167], a[40:43], a[80:83], v[164:167]\n" \
"v_mfma_f32_16x16x32_bf16 v[164:167], a[44:47], a[84:87], v[164:167]\n" \
"s_waitcnt vmcnt(6) lgkmcnt(0)\n" \
"v_mfma_f32_16x16x32_bf16 v[176:179], a[48:51], a[80:83], v[176:179]\n" \
"s_barrier\n" \
"v_mfma_f32_16x16x32_bf16 v[176:179], a[52:55], a[84:87], v[176:179]\n" \
"v_cmp_ne_u64 vcc, s[14:15], v[58:59]\n" \
"v_mfma_f32_16x16x32_bf16 v[180:183], a[56:59], a[80:83], v[180:183]\n" \
"v_mfma_f32_16x16x32_bf16 v[180:183], a[60:63], a[84:87], v[180:183]\n" \
"s_cbranch_vccnz LPUV_%=\nLPUVR_%=:\n" \
"v_mfma_f32_16x16x32_bf16 v[136:139], a[0:3], a[88:91], v[136:139]\n" \
"v_mfma_f32_16x16x32_bf16 v[136:139], a[4:7], a[92:95], v[136:139]\n" \
"v_mfma_f32_16x16x32_bf16 v[140:143], a[8:11], a[88:91], v[140:143]\n" \
"v_mfma_f32_16x16x32_bf16 v[140:143], a[12:15], a[92:95], v[140:143]\n" \
"v_mfma_f32_16x16x32_bf16 v[152:155], a[16:19], a[88:91], v[152:155]\n" \
"v_mfma_f32_16x16x32_bf16 v[152:155], a[20:23], a[92:95], v[152:155]\n" \
"v_mfma_f32_16x16x32_bf16 v[156:159], a[24:27], a[88:91], v[156:159]\n" \
"v_mfma_f32_16x16x32_bf16 v[156:159], a[28:31], a[92:95], v[156:159]\n" \
"v_mfma_f32_16x16x32_bf16 v[168:171], a[32:35], a[88:91], v[168:171]\n" \
"v_mfma_f32_16x16x32_bf16 v[168:171], a[36:39], a[92:95], v[168:171]\n" \
"v_mfma_f32_16x16x32_bf16 v[172:175], a[40:43], a[88:91], v[172:175]\n" \
"v_mfma_f32_16x16x32_bf16 v[172:175], a[44:47], a[92:95], v[172:175]\n" \
"v_mfma_f32_16x16x32_bf16 v[184:187], a[48:51], a[88:91], v[184:187]\n" \
"v_mfma_f32_16x16x32_bf16 v[184:187], a[52:55], a[92:95], v[184:187]\n" \
"v_mfma_f32_16x16x32_bf16 v[188:191], a[56:59], a[88:91], v[188:191]\n" \
"v_mfma_f32_16x16x32_bf16 v[188:191], a[60:63], a[92:95], v[188:191]\n" \
"v_mfma_f32_16x16x32_bf16 v[192:195], a[0:3], a[96:99], v[192:195]\n" \
"s_add_u32 m0, s56, 10304\n" \
"v_mfma_f32_16x16x32_bf16 v[192:195], a[4:7], a[100:103], v[192:195]\n" \
"buffer_load_dwordx4 v26, s[72:75], 0 offen offset:0 sc1 lds\n" \
"v_mfma_f32_16x16x32_bf16 v[196:199], a[8:11], a[96:99], v[196:199]\n" \
"s_mov_b64 s[32:33], s[44:45]\n" \
"v_mfma_f32_16x16x32_bf16 v[196:199], a[12:15], a[100:103], v[196:199]\n" \
"v_mfma_f32_16x16x32_bf16 v[208:211], a[16:19], a[96:99], v[208:211]\n" \
"s_add_u32 m0, s56, 11328\n" \
"ds_read_b64_tr_b16 a[64:65], v9 offset:32768\n" \
"s_waitcnt vmcnt(9)\n" \
"v_mfma_f32_16x16x32_bf16 v[208:211], a[20:23], a[100:103], v[208:211]\n" \
"buffer_load_dwordx4 v27, s[72:75], 0 offen offset:0 sc1 lds\n" \
"v_mfma_f32_16x16x32_bf16 v[212:215], a[24:27], a[96:99], v[212:215]\n" \
"s_mov_b32 s34, s49\n" \
"ds_read_b64_tr_b16 a[66:67], v10 offset:32768\n" \
"v_alignbyte_b32 v30, v31, v30, 2\n" \
"v_mfma_f32_16x16x32_bf16 v[212:215], a[28:31], a[100:103], v[212:215]\n" \
"v_alignbyte_b32 v31, v32, v31, 2\n" \
"v_alignbyte_b32 v32, v33, v32, 2\n" \
"v_mfma_f32_16x16x32_bf16 v[224:227], a[32:35], a[96:99], v[224:227]\n" \
"s_add_u32 m0, s56, 11328\n" \
"ds_read_b64_tr_b16 a[68:69], v9 offset:33792\n" \
"v_alignbyte_b32 v33, v50, v33, 2\n" \
"v_mfma_f32_16x16x32_bf16 v[224:227], a[36:39], a[100:103], v[224:227]\n" \
"buffer_load_dwordx4 v27, s[72:75], 0 offen offset:1024 sc1 lds\n" \
"v_mfma_f32_16x16x32_bf16 v[228:231], a[40:43], a[96:99], v[228:231]\n" \
"ds_read_b64_tr_b16 a[70:71], v10 offset:33792\n" \
"v_mfma_f32_16x16x32_bf16 v[228:231], a[44:47], a[100:103], v[228:231]\n" \
"ds_write_b128 v55, v[30:33] offset:5184\n" \
"v_alignbyte_b32 v34, v35, v34, 2\n" \
"v_mfma_f32_16x16x32_bf16 v[240:243], a[48:51], a[96:99], v[240:243]\n" \
"s_add_u32 m0, s56, 11328\n" \
"ds_read_b64_tr_b16 a[72:73], v11 offset:32768\n" \
"v_alignbyte_b32 v35, v36, v35, 2\n" \
"v_mfma_f32_16x16x32_bf16 v[240:243], a[52:55], a[100:103], v[240:243]\n" \
"buffer_load_dwordx4 v27, s[72:75], 0 offen offset:2048 sc1 lds\n" \
"v_mfma_f32_16x16x32_bf16 v[244:247], a[56:59], a[96:99], v[244:247]\n" \
"ds_read_b64_tr_b16 a[74:75], v12 offset:32768\n" \
"v_alignbyte_b32 v36, v37, v36, 2\n" \
"v_mfma_f32_16x16x32_bf16 v[244:247], a[60:63], a[100:103], v[244:247]\n" \
"v_alignbyte_b32 v37, v51, v37, 2\n" \
"ds_write_b128 v55, v[34:37] offset:6208\n" \
"v_mfma_f32_16x16x32_bf16 v[200:203], a[0:3], a[104:107], v[200:203]\n" \
"s_add_u32 m0, s56, 11328\n" \
"ds_read_b64_tr_b16 a[76:77], v11 offset:33792\n" \
"v_alignbyte_b32 v38, v39, v38, 2\n" \
"v_mfma_f32_16x16x32_bf16 v[200:203], a[4:7], a[108:111], v[200:203]\n" \
"buffer_load_dwordx4 v27, s[72:75], 0 offen offset:3072 sc1 lds\n" \
"v_mfma_f32_16x16x32_bf16 v[204:207], a[8:11], a[104:107], v[204:207]\n" \
"ds_read_b64_tr_b16 a[78:79], v12 offset:33792\n" \
"v_alignbyte_b32 v39, v40, v39, 2\n" \
"v_mfma_f32_16x16x32_bf16 v[204:207], a[12:15], a[108:111], v[204:207]\n" \
"v_alignbyte_b32 v40, v41, v40, 2\n" \
"v_alignbyte_b32 v41, v52, v41, 2\n" \
"v_mfma_f32_16x16x32_bf16 v[216:219], a[16:19], a[104:107], v[216:219]\n" \
"s_add_u32 m0, s56, 0\n" \
"ds_read_b64_tr_b16 a[80:81], v9 offset:33280\n" \
"v_mfma_f32_16x16x32_bf16 v[216:219], a[20:23], a[108:111], v[216:219]\n" \
"buffer_load_dwordx4 v23, s[68:71], 0 offen offset:0 lds\n" \
"v_mfma_f32_16x16x32_bf16 v[220:223], a[24:27], a[104:107], v[220:223]\n" \
"ds_read_b64_tr_b16 a[82:83], v10 offset:33280\n" \
"v_mfma_f32_16x16x32_bf16 v[220:223], a[28:31], a[108:111], v[220:223]\n" \
"ds_write_b128 v55, v[38:41] offset:7232\n" \
"v_alignbyte_b32 v42, v43, v42, 2\n" \
"v_mfma_f32_16x16x32_bf16 v[232:235], a[32:35], a[104:107], v[232:235]\n" \
"s_add_u32 m0, s56, 1024\n" \
"ds_read_b64_tr_b16 a[84:85], v9 offset:34304\n" \
"v_alignbyte_b32 v43, v44, v43, 2\n" \
"v_mfma_f32_16x16x32_bf16 v[232:235], a[36:39], a[108:111], v[232:235]\n" \
"buffer_load_dwordx4 v24, s[68:71], 0 offen offset:0 lds\n" \
"v_mfma_f32_16x16x32_bf16 v[236:239], a[40:43], a[104:107], v[236:239]\n" \
"ds_read_b64_tr_b16 a[86:87], v10 offset:34304\n" \
"v_alignbyte_b32 v44, v45, v44, 2\n" \
"v_mfma_f32_16x16x32_bf16 v[236:239], a[44:47], a[108:111], v[236:239]\n" \
"v_alignbyte_b32 v45, v53, v45, 2\n" \
"ds_write_b128 v55, v[42:45] offset:8256\n" \
"v_mfma_f32_16x16x32_bf16 v[248:251], a[48:51], a[104:107], v[248:251]\n" \
"s_add_u32 m0, s56, 1024\n" \
"ds_read_b64_tr_b16 a[88:89], v11 offset:33280\n" \
"v_alignbyte_b32 v46, v47, v46, 2\n" \
"v_mfma_f32_16x16x32_bf16 v[248:251], a[52:55], a[108:111], v[248:251]\n" \
"buffer_load_dwordx4 v24, s[68:71], 0 offen offset:1024 lds\n" \
"v_mfma_f32_16x16x32_bf16 v[252:255], a[56:59], a[104:107], v[252:255]\n" \
"ds_read_b64_tr_b16 a[90:91], v12 offset:33280\n" \
"v_alignbyte_b32 v47, v48, v47, 2\n" \
"v_mfma_f32_16x16x32_bf16 v[252:255], a[60:63], a[108:111], v[252:255]\n" \
"v_alignbyte_b32 v48, v49, v48, 2\n" \
"v_alignbyte_b32 v49, v54, v49, 2\n" \
"v_mfma_f32_16x16x32_bf16 a[128:131], a[0:3], a[112:115], a[128:131]\n" \
"s_add_u32 m0, s56, 1024\n" \
"ds_read_b64_tr_b16 a[92:93], v11 offset:34304\n" \
"v_mfma_f32_16x16x32_bf16 a[128:131], a[4:7], a[116:119], a[128:131]\n" \
"buffer_load_dwordx4 v24, s[68:71], 0 offen offset:2048 lds\n" \
"v_mfma_f32_16x16x32_bf16 a[132:135], a[8:11], a[112:115], a[132:135]\n" \
"ds_read_b64_tr_b16 a[94:95], v12 offset:34304\n" \
"v_mfma_f32_16x16x32_bf16 a[132:135], a[12:15], a[116:119], a[132:135]\n" \
"ds_write_b128 v55, v[46:49] offset:9280\n" \
"v_lshrrev_b32 v30, 4, v4\n" \
"v_mfma_f32_16x16x32_bf16 a[144:147], a[16:19], a[112:115], a[144:147]\n" \
"s_add_u32 m0, s56, 1024\n" \
"v_and_b32 v31, 15, v30\n" \
"v_lshrrev_b32 v32, 4, v30\n" \
"v_mfma_f32_16x16x32_bf16 a[144:147], a[20:23], a[116:119], a[144:147]\n" \
"buffer_load_dwordx4 v24, s[68:71], 0 offen offset:3072 lds\n" \
"v_mfma_f32_16x16x32_bf16 a[148:151], a[24:27], a[112:115], a[148:151]\n" \
"v_add_u32 v33, 1, v31\n" \
"v_and_b32 v34, 1, v33\n" \
"v_mfma_f32_16x16x32_bf16 a[148:151], a[28:31], a[116:119], a[148:151]\n" \
"v_and_b32 v33, 30, v33\n" \
"v_mul_u32_u24 v34, 5184, v34\n" \
"v_mfma_f32_16x16x32_bf16 a[160:163], a[32:35], a[112:115], a[160:163]\n" \
"s_mov_b32 s46, 0\n" \
"s_add_u32 m0, s50, 65536\n" \
"v_lshl_add_u32 v33, v33, 1, v34\n" \
"v_mfma_f32_16x16x32_bf16 a[160:163], a[36:39], a[116:119], a[160:163]\n" \
"buffer_load_dwordx4 v13, s[28:31], s46 offen lds\n" \
"v_mfma_f32_16x16x32_bf16 a[164:167], a[40:43], a[112:115], a[164:167]\n" \
"v_lshl_add_u32 v33, v32, 4, v33\n" \
"v_add_u32 v46, s56, v33\n" \
"v_mfma_f32_16x16x32_bf16 a[164:167], a[44:47], a[116:119], a[164:167]\n" \
"s_waitcnt vmcnt(13) lgkmcnt(0)\n" \
"v_add_u32 v47, 1024, v46\n" \
"v_add_u32 v48, 2048, v46\n" \
"v_mfma_f32_16x16x32_bf16 a[176:179], a[48:51], a[112:115], a[176:179]\n" \
"s_add_u32 m0, s50, 69632\n" \
"s_barrier\n" \
"v_add_u32 v49, 3072, v46\n" \
"v_mfma_f32_16x16x32_bf16 a[176:179], a[52:55], a[116:119], a[176:179]\n" \
"buffer_load_dwordx4 v14, s[28:31], s46 offen lds\n" \
"v_mfma_f32_16x16x32_bf16 a[180:183], a[56:59], a[112:115], a[180:183]\n" \
"v_lshlrev_b32 v33, 1, v31\n" \
"v_sub_u32 v33, v33, v32\n" \
"v_mfma_f32_16x16x32_bf16 a[180:183], a[60:63], a[116:119], a[180:183]\n" \
"v_lshlrev_b32 v33, 4, v33\n" \
"v_add_u32 v33, 10320, v33\n" \
"v_mfma_f32_16x16x32_bf16 a[136:139], a[0:3], a[120:123], a[136:139]\n" \
"s_add_u32 m0, s50, 73728\n" \
"v_add_u32 v50, s56, v33\n" \
"v_mfma_f32_16x16x32_bf16 a[136:139], a[4:7], a[124:127], a[136:139]\n" \
"buffer_load_dwordx4 v15, s[28:31], s46 offen lds\n" \
"v_mfma_f32_16x16x32_bf16 a[140:143], a[8:11], a[120:123], a[140:143]\n" \
"ds_read_b128 a[0:3], v5 offset:0\n" \
"v_lshlrev_b32 v33, 5, v31\n" \
"v_mfma_f32_16x16x32_bf16 a[140:143], a[12:15], a[124:127], a[140:143]\n" \
"ds_read_b128 a[4:7], v6 offset:0\n" \
"v_lshl_add_u32 v55, v32, 3, v33\n" \
"v_mfma_f32_16x16x32_bf16 a[152:155], a[16:19], a[120:123], a[152:155]\n" \
"ds_read_b128 a[8:11], v5 offset:2048\n" \
"v_add_u32 v55, s22, v55\n" \
"v_mfma_f32_16x16x32_bf16 a[152:155], a[20:23], a[124:127], a[152:155]\n" \
"ds_read_b128 a[12:15], v6 offset:2048\n" \
"v_add_u32 v33, s56, v33\n" \
"v_mfma_f32_16x16x32_bf16 a[156:159], a[24:27], a[120:123], a[156:159]\n" \
"ds_read_b128 a[16:19], v5 offset:4096\n" \
"v_xor_b32 v32, 1, v32\n" \
"v_mfma_f32_16x16x32_bf16 a[156:159], a[28:31], a[124:127], a[156:159]\n" \
"ds_read_b128 a[20:23], v6 offset:4096\n" \
"v_lshl_add_u32 v51, v32, 3, v33\n" \
"v_mfma_f32_16x16x32_bf16 a[168:171], a[32:35], a[120:123], a[168:171]\n" \
"ds_read_b128 a[24:27], v5 offset:6144\n" \
"v_mfma_f32_16x16x32_bf16 a[168:171], a[36:39], a[124:127], a[168:171]\n" \
"ds_read_b128 a[28:31], v6 offset:6144\n" \
"v_mfma_f32_16x16x32_bf16 a[172:175], a[40:43], a[120:123], a[172:175]\n" \
"ds_read_b128 a[32:35], v5 offset:8192\n" \
"v_mfma_f32_16x16x32_bf16 a[172:175], a[44:47], a[124:127], a[172:175]\n" \
"ds_read_b128 a[36:39], v6 offset:8192\n" \
"v_mfma_f32_16x16x32_bf16 a[184:187], a[48:51], a[120:123], a[184:187]\n" \
"ds_read_b128 a[40:43], v5 offset:10240\n" \
"v_mfma_f32_16x16x32_bf16 a[184:187], a[52:55], a[124:127], a[184:187]\n" \
"ds_read_b128 a[44:47], v6 offset:10240\n" \
"v_mfma_f32_16x16x32_bf16 a[188:191], a[56:59], a[120:123], a[188:191]\n" \
"ds_read_b128 a[48:51], v5 offset:12288\n" \
"v_mfma_f32_16x16x32_bf16 a[188:191], a[60:63], a[124:127], a[188:191]\n" \
"s_waitcnt lgkmcnt(9)\n" \
"ds_read_b128 a[52:55], v6 offset:12288\n" \
"v_mfma_f32_16x16x32_bf16 v[64:67], a[0:3], a[64:67], v[64:67]\n" \
"s_add_u32 m0, s50, 77824\n" \
"ds_read_b128 a[56:59], v5 offset:14336\n" \
"v_mfma_f32_16x16x32_bf16 v[64:67], a[4:7], a[68:71], v[64:67]\n" \
"buffer_load_dwordx4 v16, s[28:31], s46 offen lds\n" \
"v_mfma_f32_16x16x32_bf16 v[68:71], a[8:11], a[64:67], v[68:71]\n" \
"s_waitcnt lgkmcnt(7)\n" \
"ds_read_b128 a[60:63], v6 offset:14336\n" \
"v_mfma_f32_16x16x32_bf16 v[68:71], a[12:15], a[68:71], v[68:71]\n" \
"v_mfma_f32_16x16x32_bf16 v[80:83], a[16:19], a[64:67], v[80:83]\n" \
"s_mov_b32 s46, 0\n" \
"s_add_u32 m0, s50, 81920\n" \
"ds_read_b64_tr_b16 a[96:97], v9 offset:49152\n" \
"v_mfma_f32_16x16x32_bf16 v[80:83], a[20:23], a[68:71], v[80:83]\n" \
"buffer_load_dwordx4 v17, s[28:31], s46 offen lds\n" \
"v_mfma_f32_16x16x32_bf16 v[84:87], a[24:27], a[64:67], v[84:87]\n" \
"s_waitcnt lgkmcnt(5)\n" \
"ds_read_b64_tr_b16 a[98:99], v10 offset:49152\n" \
"v_mfma_f32_16x16x32_bf16 v[84:87], a[28:31], a[68:71], v[84:87]\n" \
"v_mfma_f32_16x16x32_bf16 v[96:99], a[32:35], a[64:67], v[96:99]\n" \
"s_add_u32 m0, s50, 86016\n" \
"ds_read_b64_tr_b16 a[100:101], v9 offset:50176\n" \
"v_mfma_f32_16x16x32_bf16 v[96:99], a[36:39], a[68:71], v[96:99]\n" \
"LH7_%=:\ns_cbranch_execz LH11_%=\n" \
"buffer_load_dwordx4 v18, s[28:31], s46 offen lds\n" \
"v_mfma_f32_16x16x32_bf16 v[100:103], a[40:43], a[64:67], v[100:103]\n" \
"s_waitcnt lgkmcnt(3)\n" \
"ds_read_b64_tr_b16 a[102:103], v10 offset:50176\n" \
"v_mfma_f32_16x16x32_bf16 v[100:103], a[44:47], a[68:71], v[100:103]\n" \
"v_mfma_f32_16x16x32_bf16 v[112:115], a[48:51], a[64:67], v[112:115]\n" \
"s_add_u32 m0, s50, 90112\n" \
"ds_read_b64_tr_b16 a[104:105], v11 offset:49152\n" \
"v_mfma_f32_16x16x32_bf16 v[112:115], a[52:55], a[68:71], v[112:115]\n" \
"buffer_load_dwordx4 v19, s[28:31], s46 offen lds\n" \
"v_mfma_f32_16x16x32_bf16 v[116:119], a[56:59], a[64:67], v[116:119]\n" \
"ds_read_b64_tr_b16 a[106:107], v12 offset:49152\n" \
"v_mfma_f32_16x16x32_bf16 v[116:119], a[60:63], a[68:71], v[116:119]\n" \
"v_mfma_f32_16x16x32_bf16 v[72:75], a[0:3], a[72:75], v[72:75]\n" \
"s_add_u32 m0, s50, 94208\n" \
"ds_read_b64_tr_b16 a[108:109], v11 offset:50176\n" \
"v_mfma_f32_16x16x32_bf16 v[72:75], a[4:7], a[76:79], v[72:75]\n" \
"buffer_load_dwordx4 v20, s[28:31], s46 offen lds\n" \
"v_mfma_f32_16x16x32_bf16 v[76:79], a[8:11], a[72:75], v[76:79]\n" \
"ds_read_b64_tr_b16 a[110:111], v12 offset:50176\n" \
"v_mfma_f32_16x16x32_bf16 v[76:79], a[12:15], a[76:79], v[76:79]\n" \
"v_mfma_f32_16x16x32_bf16 v[88:91], a[16:19], a[72:75], v[88:91]\n" \
"s_mov_b32 s47, 0\n" \
"s_add_u32 m0, s51, 32768\n" \
"ds_read_b64_tr_b16 a[112:113], v9 offset:49664\n" \
"v_mfma_f32_16x16x32_bf16 v[88:91], a[20:23], a[76:79], v[88:91]\n" \
"buffer_load_dwordx4 v21, s[32:35], s47 offen offset:0 lds\n" \
"v_mfma_f32_16x16x32_bf16 v[92:95], a[24:27], a[72:75], v[92:95]\n" \
"ds_read_b64_tr_b16 a[114:115], v10 offset:49664\n" \
"v_mfma_f32_16x16x32_bf16 v[92:95], a[28:31], a[76:79], v[92:95]\n" \
"v_mfma_f32_16x16x32_bf16 v[104:107], a[32:35], a[72:75], v[104:107]\n" \
"s_add_u32 m0, s51, 33664\n" \
"ds_read_b64_tr_b16 a[116:117], v9 offset:50688\n" \
"v_mfma_f32_16x16x32_bf16 v[104:107], a[36:39], a[76:79], v[104:107]\n" \
"buffer_load_dwordx4 v21, s[32:35], s47 offen offset:128 lds\n" \
"v_mfma_f32_16x16x32_bf16 v[108:111], a[40:43], a[72:75], v[108:111]\n" \
"ds_read_b64_tr_b16 a[118:119], v10 offset:50688\n" \
"v_mfma_f32_16x16x32_bf16 v[108:111], a[44:47], a[76:79], v[108:111]\n" \
"v_mfma_f32_16x16x32_bf16 v[120:123], a[48:51], a[72:75], v[120:123]\n" \
"s_mov_b32 s47, 262144\n" \
"s_add_u32 m0, s51, 40960\n" \
"ds_read_b64_tr_b16 a[120:121], v11 offset:49664\n" \
"v_mfma_f32_16x16x32_bf16 v[120:123], a[52:55], a[76:79], v[120:123]\n" \
"buffer_load_dwordx4 v21, s[32:35], s47 offen offset:0 lds\n" \
"v_mfma_f32_16x16x32_bf16 v[124:127], a[56:59], a[72:75], v[124:127]\n" \
"ds_read_b64_tr_b16 a[122:123], v12 offset:49664\n" \
"v_mfma_f32_16x16x32_bf16 v[124:127], a[60:63], a[76:79], v[124:127]\n" \
"v_mfma_f32_16x16x32_bf16 v[128:131], a[0:3], a[80:83], v[128:131]\n" \
"s_add_u32 m0, s51, 41856\n" \
"ds_read_b64_tr_b16 a[124:125], v11 offset:50688\n" \
"v_mfma_f32_16x16x32_bf16 v[128:131], a[4:7], a[84:87], v[128:131]\n" \
"buffer_load_dwordx4 v21, s[32:35], s47 offen offset:128 lds\n" \
"v_mfma_f32_16x16x32_bf16 v[132:135], a[8:11], a[80:83], v[132:135]\n" \
"s_mov_b32 s96, s57\n" \
"ds_read_b64_tr_b16 a[126:127], v12 offset:50688\n" \
"v_mul_f32 v64, s20, v64\n" \
"v_mfma_f32_16x16x32_bf16 v[132:135], a[12:15], a[84:87], v[132:135]\n" \
"v_mul_f32 v65, s20, v65\n" \
"v_mul_f32 v66, s20, v66\n" \
"v_mfma_f32_16x16x32_bf16 v[144:147], a[16:19], a[80:83], v[144:147]\n" \
"v_mul_f32 v67, s20, v67\n" \
"v_cvt_pk_bf16_f32 v30, v64, v65\n" \
"v_mfma_f32_16x16x32_bf16 v[144:147], a[20:23], a[84:87], v[144:147]\n" \
"v_cvt_pk_bf16_f32 v31, v66, v67\n" \
"v_mfma_f32_16x16x32_bf16 v[148:151], a[24:27], a[80:83], v[148:151]\n" \
"v_mul_f32 v68, s20, v68\n" \
"v_mul_f32 v69, s20, v69\n" \
"v_mfma_f32_16x16x32_bf16 v[148:151], a[28:31], a[84:87], v[148:151]\n" \
"v_mul_f32 v70, s20, v70\n" \
"v_mul_f32 v71, s20, v71\n" \
"v_mfma_f32_16x16x32_bf16 v[160:163], a[32:35], a[80:83], v[160:163]\n" \
"v_cvt_pk_bf16_f32 v32, v68, v69\n" \
"v_mfma_f32_16x16x32_bf16 v[160:163], a[36:39], a[84:87], v[160:163]\n" \
"v_cvt_pk_bf16_f32 v33, v70, v71\n" \
"v_mul_f32 v72, s20, v72\n" \
"v_mfma_f32_16x16x32_bf16 v[164:167], a[40:43], a[80:83], v[164:167]\n" \
"buffer_store_dwordx4 v[30:33], v4, s[36:39], s96 offen offset:0 sc1\n" \
"v_mfma_f32_16x16x32_bf16 v[164:167], a[44:47], a[84:87], v[164:167]\n" \
"s_waitcnt lgkmcnt(0)\n" \
"s_add_u32 s97, s96, s98\n" \
"v_mul_f32 v73, s20, v73\n" \
"v_mfma_f32_16x16x32_bf16 v[176:179], a[48:51], a[80:83], v[176:179]\n" \
"s_barrier\n" \
"v_mul_f32 v74, s20, v74\n" \
"v_mul_f32 v75, s20, v75\n" \
"v_mfma_f32_16x16x32_bf16 v[176:179], a[52:55], a[84:87], v[176:179]\n" \
"v_cvt_pk_bf16_f32 v34, v72, v73\n" \
"v_cvt_pk_bf16_f32 v35, v74, v75\n" \
"v_mfma_f32_16x16x32_bf16 v[180:183], a[56:59], a[80:83], v[180:183]\n" \
"v_mul_f32 v76, s20, v76\n" \
"v_mfma_f32_16x16x32_bf16 v[180:183], a[60:63], a[84:87], v[180:183]\n" \
"v_add_u32 v5, s54, v5\n" \
"v_mul_f32 v77, s20, v77\n" \
"v_mfma_f32_16x16x32_bf16 v[136:139], a[0:3], a[88:91], v[136:139]\n" \
"v_mul_f32 v78, s20, v78\n" \
"v_mul_f32 v79, s20, v79\n" \
"v_mfma_f32_16x16x32_bf16 v[136:139], a[4:7], a[92:95], v[136:139]\n" \
"v_cvt_pk_bf16_f32 v36, v76, v77\n" \
"v_mfma_f32_16x16x32_bf16 v[140:143], a[8:11], a[88:91], v[140:143]\n" \
"v_add_u32 v6, s54, v6\n" \
"v_cvt_pk_bf16_f32 v37, v78, v79\n" \
"v_mfma_f32_16x16x32_bf16 v[140:143], a[12:15], a[92:95], v[140:143]\n" \
"v_add_u32 v7, s54, v7\n" \
"v_mul_f32 v80, s20, v80\n" \
"v_mfma_f32_16x16x32_bf16 v[152:155], a[16:19], a[88:91], v[152:155]\n" \
"v_mul_f32 v81, s20, v81\n" \
"v_mfma_f32_16x16x32_bf16 v[152:155], a[20:23], a[92:95], v[152:155]\n" \
"v_mul_f32 v82, s20, v82\n" \
"v_mul_f32 v83, s20, v83\n" \
"v_mfma_f32_16x16x32_bf16 v[156:159], a[24:27], a[88:91], v[156:159]\n" \
"v_add_u32 v8, s54, v8\n" \
"v_cvt_pk_bf16_f32 v38, v80, v81\n" \
"v_mfma_f32_16x16x32_bf16 v[156:159], a[28:31], a[92:95], v[156:159]\n" \
"v_sub_u32 v9, v9, v62\n" \
"v_mfma_f32_16x16x32_bf16 v[168:171], a[32:35], a[88:91], v[168:171]\n" \
"buffer_store_dwordx4 v[34:37], v4, s[36:39], s97 offen offset:0 sc1\n" \
"v_mfma_f32_16x16x32_bf16 v[168:171], a[36:39], a[92:95], v[168:171]\n" \
"s_add_u32 s96, s96, s92\n" \
"v_cvt_pk_bf16_f32 v39, v82, v83\n" \
"v_mul_f32 v84, s20, v84\n" \
"v_mfma_f32_16x16x32_bf16 v[172:175], a[40:43], a[88:91], v[172:175]\n" \
"v_sub_u32 v10, v10, v62\n" \
"v_mul_f32 v85, s20, v85\n" \
"v_mfma_f32_16x16x32_bf16 v[172:175], a[44:47], a[92:95], v[172:175]\n" \
"v_sub_u32 v11, v11, v62\n" \
"v_mfma_f32_16x16x32_bf16 v[184:187], a[48:51], a[88:91], v[184:187]\n" \
"v_mul_f32 v86, s20, v86\n" \
"v_mul_f32 v87, s20, v87\n" \
"v_mfma_f32_16x16x32_bf16 v[184:187], a[52:55], a[92:95], v[184:187]\n" \
"v_cvt_pk_bf16_f32 v40, v84, v85\n" \
"v_cvt_pk_bf16_f32 v41, v86, v87\n" \
"v_mfma_f32_16x16x32_bf16 v[188:191], a[56:59], a[88:91], v[188:191]\n" \
"v_sub_u32 v12, v12, v62\n" \
"v_mfma_f32_16x16x32_bf16 v[188:191], a[60:63], a[92:95], v[188:191]\n" \
"v_mul_f32 v88, s20, v88\n" \
"v_mul_f32 v89, s20, v89\n" \
"v_mfma_f32_16x16x32_bf16 v[192:195], a[0:3], a[96:99], v[192:195]\n" \
"s_mov_b32 s46, 128\n" \
"s_add_u32 m0, s50, 0\n" \
"s_waitcnt vmcnt(14) lgkmcnt(0)\n" \
"v_mfma_f32_16x16x32_bf16 v[192:195], a[4:7], a[100:103], v[192:195]\n" \
"buffer_load_dwordx4 v13, s[28:31], s46 offen lds\n" \
"v_mfma_f32_16x16x32_bf16 v[196:199], a[8:11], a[96:99], v[196:199]\n" \
"ds_read_b128 a[200:203], v50 offset:512\n" \
"v_mul_f32 v90, s20, v90\n" \
"v_mfma_f32_16x16x32_bf16 v[196:199], a[12:15], a[100:103], v[196:199]\n" \
"v_mul_f32 v91, s20, v91\n" \
"v_cvt_pk_bf16_f32 v42, v88, v89\n" \
"v_mfma_f32_16x16x32_bf16 v[208:211], a[16:19], a[96:99], v[208:211]\n" \
"ds_read2_b32 a[64:65], v46 offset0:0 offset1:1\n" \
"v_cvt_pk_bf16_f32 v43, v90, v91\n" \
"v_mfma_f32_16x16x32_bf16 v[208:211], a[20:23], a[100:103], v[208:211]\n" \
"v_mul_f32 v92, s20, v92\n" \
"v_mul_f32 v93, s20, v93\n" \
"v_mfma_f32_16x16x32_bf16 v[212:215], a[24:27], a[96:99], v[212:215]\n" \
"ds_read2_b32 a[66:67], v46 offset0:2 offset1:3\n" \
"v_mul_f32 v94, s20, v94\n" \
"v_mfma_f32_16x16x32_bf16 v[212:215], a[28:31], a[100:103], v[212:215]\n" \
"buffer_store_dwordx4 v[38:41], v4, s[36:39], s96 offen offset:0 sc1\n" \
"v_mfma_f32_16x16x32_bf16 v[224:227], a[32:35], a[96:99], v[224:227]\n" \
"s_add_u32 m0, s50, 4096\n" \
"ds_read_b128 a[204:207], v50 offset:1024\n" \
"s_add_u32 s97, s96, s98\n" \
"v_mfma_f32_16x16x32_bf16 v[224:227], a[36:39], a[100:103], v[224:227]\n" \
"buffer_load_dwordx4 v14, s[28:31], s46 offen lds\n" \
"v_mfma_f32_16x16x32_bf16 v[228:231], a[40:43], a[96:99], v[228:231]\n" \
"ds_read2_b32 a[68:69], v46 offset0:128 offset1:129\n" \
"v_mul_f32 v95, s20, v95\n" \
"v_mfma_f32_16x16x32_bf16 v[228:231], a[44:47], a[100:103], v[228:231]\n" \
"v_cvt_pk_bf16_f32 v44, v92, v93\n" \
"v_cvt_pk_bf16_f32 v45, v94, v95\n" \
"v_mfma_f32_16x16x32_bf16 v[240:243], a[48:51], a[96:99], v[240:243]\n" \
"ds_read2_b32 a[70:71], v46 offset0:130 offset1:131\n" \
"v_mul_f32 v96, s20, v96\n" \
"v_mfma_f32_16x16x32_bf16 v[240:243], a[52:55], a[100:103], v[240:243]\n" \
"v_mul_f32 v97, s20, v97\n" \
"v_mul_f32 v98, s20, v98\n" \
"v_mfma_f32_16x16x32_bf16 v[244:247], a[56:59], a[96:99], v[244:247]\n" \
"ds_read_b128 a[208:211], v50 offset:1536\n" \
"v_mul_f32 v99, s20, v99\n" \
"v_mfma_f32_16x16x32_bf16 v[244:247], a[60:63], a[100:103], v[244:247]\n" \
"v_cvt_pk_bf16_f32 v30, v96, v97\n" \
"v_cvt_pk_bf16_f32 v31, v98, v99\n" \
"v_mfma_f32_16x16x32_bf16 v[200:203], a[0:3], a[104:107], v[200:203]\n" \
"s_add_u32 m0, s50, 8192\n" \
"ds_read2_b32 a[72:73], v47 offset0:0 offset1:1\n" \
"v_mul_f32 v100, s20, v100\n" \
"v_mfma_f32_16x16x32_bf16 v[200:203], a[4:7], a[108:111], v[200:203]\n" \
"buffer_load_dwordx4 v15, s[28:31], s46 offen lds\n" \
"v_mfma_f32_16x16x32_bf16 v[204:207], a[8:11], a[104:107], v[204:207]\n" \
"ds_read2_b32 a[74:75], v47 offset0:2 offset1:3\n" \
"v_mul_f32 v101, s20, v101\n" \
"v_mfma_f32_16x16x32_bf16 v[204:207], a[12:15], a[108:111], v[204:207]\n" \
"v_mul_f32 v102, s20, v102\n" \
"v_mul_f32 v103, s20, v103\n" \
"v_mfma_f32_16x16x32_bf16 v[216:219], a[16:19], a[104:107], v[216:219]\n" \
"ds_read_b128 a[212:215], v50 offset:2048\n" \
"v_cvt_pk_bf16_f32 v32, v100, v101\n" \
"v_mfma_f32_16x16x32_bf16 v[216:219], a[20:23], a[108:111], v[216:219]\n" \
"buffer_store_dwordx4 v[42:45], v4, s[36:39], s97 offen offset:0 sc1\n" \
"v_mfma_f32_16x16x32_bf16 v[220:223], a[24:27], a[104:107], v[220:223]\n" \
"s_add_u32 s96, s96, s92\n" \
"ds_read2_b32 a[76:77], v47 offset0:128 offset1:129\n" \
"v_cvt_pk_bf16_f32 v33, v102, v103\n" \
"v_mfma_f32_16x16x32_bf16 v[220:223], a[28:31], a[108:111], v[220:223]\n" \
"v_mul_f32 v104, s20, v104\n" \
"v_mul_f32 v105, s20, v105\n" \
"v_mfma_f32_16x16x32_bf16 v[232:235], a[32:35], a[104:107], v[232:235]\n" \
"s_add_u32 m0, s50, 12288\n" \
"ds_read2_b32 a[78:79], v47 offset0:130 offset1:131\n" \
"v_mul_f32 v106, s20, v106\n" \
"v_mfma_f32_16x16x32_bf16 v[232:235], a[36:39], a[108:111], v[232:235]\n" \
"buffer_load_dwordx4 v16, s[28:31], s46 offen lds\n" \
"v_mfma_f32_16x16x32_bf16 v[236:239], a[40:43], a[104:107], v[236:239]\n" \
"ds_read_b128 a[216:219], v50 offset:2560\n" \
"v_mul_f32 v107, s20, v107\n" \
"v_mfma_f32_16x16x32_bf16 v[236:239], a[44:47], a[108:111], v[236:239]\n" \
"v_cvt_pk_bf16_f32 v34, v104, v105\n" \
"v_cvt_pk_bf16_f32 v35, v106, v107\n" \
"v_mfma_f32_16x16x32_bf16 v[248:251], a[48:51], a[104:107], v[248:251]\n" \
"ds_read2_b32 a[80:81], v48 offset0:0 offset1:1\n" \
"v_mul_f32 v108, s20, v108\n" \
"v_mfma_f32_16x16x32_bf16 v[248:251], a[52:55], a[108:111], v[248:251]\n" \
"v_mul_f32 v109, s20, v109\n" \
"v_mul_f32 v110, s20, v110\n" \
"v_mfma_f32_16x16x32_bf16 v[252:255], a[56:59], a[104:107], v[252:255]\n" \
"ds_read2_b32 a[82:83], v48 offset0:2 offset1:3\n" \
"v_mul_f32 v111, s20, v111\n" \
"v_mfma_f32_16x16x32_bf16 v[252:255], a[60:63], a[108:111], v[252:255]\n" \
"v_cvt_pk_bf16_f32 v36, v108, v109\n" \
"v_cvt_pk_bf16_f32 v37, v110, v111\n" \
"v_mfma_f32_16x16x32_bf16 a[128:131], a[0:3], a[112:115], a[128:131]\n" \
"s_mov_b32 s46, 128\n" \
"s_add_u32 m0, s50, 16384\n" \
"ds_read_b128 a[220:223], v50 offset:3072\n" \
"v_mfma_f32_16x16x32_bf16 a[128:131], a[4:7], a[116:119], a[128:131]\n" \
"buffer_load_dwordx4 v17, s[28:31], s46 offen lds\n" \
"v_mfma_f32_16x16x32_bf16 a[132:135], a[8:11], a[112:115], a[132:135]\n" \
"ds_read2_b32 a[84:85], v48 offset0:128 offset1:129\n" \
"v_mul_f32 v112, s20, v112\n" \
"v_mfma_f32_16x16x32_bf16 a[132:135], a[12:15], a[116:119], a[132:135]\n" \
"buffer_store_dwordx4 v[30:33], v4, s[36:39], s96 offen offset:0 sc1\n" \
"v_mfma_f32_16x16x32_bf16 a[144:147], a[16:19], a[112:115], a[144:147]\n" \
"s_add_u32 s97, s96, s98\n" \
"ds_read2_b32 a[86:87], v48 offset0:130 offset1:131\n" \
"v_mul_f32 v113, s20, v113\n" \
"v_mfma_f32_16x16x32_bf16 a[144:147], a[20:23], a[116:119], a[144:147]\n" \
"v_mul_f32 v114, s20, v114\n" \
"v_mul_f32 v115, s20, v115\n" \
"v_mfma_f32_16x16x32_bf16 a[148:151], a[24:27], a[112:115], a[148:151]\n" \
"ds_read_b128 a[224:227], v50 offset:3584\n" \
"v_cvt_pk_bf16_f32 v38, v112, v113\n" \
"v_mfma_f32_16x16x32_bf16 a[148:151], a[28:31], a[116:119], a[148:151]\n" \
"v_cvt_pk_bf16_f32 v39, v114, v115\n" \
"v_mul_f32 v116, s20, v116\n" \
"v_mfma_f32_16x16x32_bf16 a[160:163], a[32:35], a[112:115], a[160:163]\n" \
"s_add_u32 m0, s50, 20480\n" \
"ds_read2_b32 a[88:89], v49 offset0:0 offset1:1\n" \
"v_mul_f32 v117, s20, v117\n" \
"v_mfma_f32_16x16x32_bf16 a[160:163], a[36:39], a[116:119], a[160:163]\n" \
"buffer_load_dwordx4 v18, s[28:31], s46 offen lds\n" \
"v_mfma_f32_16x16x32_bf16 a[164:167], a[40:43], a[112:115], a[164:167]\n" \
"ds_read2_b32 a[90:91], v49 offset0:2 offset1:3\n" \
"v_mul_f32 v118, s20, v118\n" \
"v_mfma_f32_16x16x32_bf16 a[164:167], a[44:47], a[116:119], a[164:167]\n" \
"v_mul_f32 v119, s20, v119\n" \
"v_cvt_pk_bf16_f32 v40, v116, v117\n" \
"v_mfma_f32_16x16x32_bf16 a[176:179], a[48:51], a[112:115], a[176:179]\n" \
"ds_read_b128 a[228:231], v50 offset:4096\n" \
"v_cvt_pk_bf16_f32 v41, v118, v119\n" \
"v_mfma_f32_16x16x32_bf16 a[176:179], a[52:55], a[116:119], a[176:179]\n" \
"v_mul_f32 v120, s20, v120\n" \
"v_mul_f32 v121, s20, v121\n" \
"v_mfma_f32_16x16x32_bf16 a[180:183], a[56:59], a[112:115], a[180:183]\n" \
"ds_read2_b32 a[92:93], v49 offset0:128 offset1:129\n" \
"v_mul_f32 v122, s20, v122\n" \
"v_mfma_f32_16x16x32_bf16 a[180:183], a[60:63], a[116:119], a[180:183]\n" \
"v_mul_f32 v123, s20, v123\n" \
"v_cvt_pk_bf16_f32 v42, v120, v121\n" \
"v_mfma_f32_16x16x32_bf16 a[136:139], a[0:3], a[120:123], a[136:139]\n" \
"s_add_u32 m0, s50, 24576\n" \
"ds_read2_b32 a[94:95], v49 offset0:130 offset1:131\n" \
"v_cvt_pk_bf16_f32 v43, v122, v123\n" \
"v_mfma_f32_16x16x32_bf16 a[136:139], a[4:7], a[124:127], a[136:139]\n" \
"buffer_load_dwordx4 v19, s[28:31], s46 offen lds\n" \
"v_mfma_f32_16x16x32_bf16 a[140:143], a[8:11], a[120:123], a[140:143]\n" \
"v_mul_f32 v124, s20, v124\n" \
"v_mul_f32 v125, s20, v125\n" \
"v_mfma_f32_16x16x32_bf16 a[140:143], a[12:15], a[124:127], a[140:143]\n" \
"buffer_store_dwordx4 v[34:37], v4, s[36:39], s97 offen offset:0 sc1\n" \
"v_mfma_f32_16x16x32_bf16 a[152:155], a[16:19], a[120:123], a[152:155]\n" \
"s_add_u32 s96, s96, s92\n" \
"v_mul_f32 v126, s20, v126\n" \
"v_mul_f32 v127, s20, v127\n" \
"v_mfma_f32_16x16x32_bf16 a[152:155], a[20:23], a[124:127], a[152:155]\n" \
"v_cvt_pk_bf16_f32 v44, v124, v125\n" \
"v_cvt_pk_bf16_f32 v45, v126, v127\n" \
"v_mfma_f32_16x16x32_bf16 a[156:159], a[24:27], a[120:123], a[156:159]\n" \
"v_mul_f32 v128, s20, v128\n" \
"v_mfma_f32_16x16x32_bf16 a[156:159], a[28:31], a[124:127], a[156:159]\n" \
"v_mul_f32 v129, s20, v129\n" \
"v_mul_f32 v130, s20, v130\n" \
"v_mfma_f32_16x16x32_bf16 a[168:171], a[32:35], a[120:123], a[168:171]\n" \
"s_add_u32 m0, s50, 28672\n" \
"v_mul_f32 v131, s20, v131\n" \
"v_cvt_pk_bf16_f32 v30, v128, v129\n" \
"v_mfma_f32_16x16x32_bf16 a[168:171], a[36:39], a[124:127], a[168:171]\n" \
"buffer_load_dwordx4 v20, s[28:31], s46 offen lds\n" \
"v_mfma_f32_16x16x32_bf16 a[172:175], a[40:43], a[120:123], a[172:175]\n" \
"v_cvt_pk_bf16_f32 v31, v130, v131\n" \
"v_mul_f32 v132, s20, v132\n" \
"v_mfma_f32_16x16x32_bf16 a[172:175], a[44:47], a[124:127], a[172:175]\n" \
"v_mul_f32 v133, s20, v133\n" \
"v_mul_f32 v134, s20, v134\n" \
"v_mfma_f32_16x16x32_bf16 a[184:187], a[48:51], a[120:123], a[184:187]\n" \
"v_mul_f32 v135, s20, v135\n" \
"v_mfma_f32_16x16x32_bf16 a[184:187], a[52:55], a[124:127], a[184:187]\n" \
"v_cvt_pk_bf16_f32 v32, v132, v133\n" \
"v_cvt_pk_bf16_f32 v33, v134, v135\n" \
"v_mfma_f32_16x16x32_bf16 a[188:191], a[56:59], a[120:123], a[188:191]\n" \
"s_waitcnt lgkmcnt(0)\n" \
"v_mul_f32 v136, s20, v136\n" \
"v_mul_f32 v137, s20, v137\n" \
"v_mfma_f32_16x16x32_bf16 a[188:191], a[60:63], a[124:127], a[188:191]\n" \
"v_mul_f32 v138, s20, v138\n" \
"LC_%=:\n" \
"v_mfma_f32_16x16x32_bf16 v[64:67], a[64:67], a[200:203], 0\n" \
"ds_read_b128 a[32:35], v50 offset:448\n" \
"v_mul_f32 v139, s20, v139\n" \
"v_mfma_f32_16x16x32_bf16 v[68:71], a[64:67], a[204:207], 0\n" \
"buffer_store_dwordx4 v[38:41], v4, s[36:39], s96 offen offset:0 sc1\n" \
"v_mfma_f32_16x16x32_bf16 v[68:71], a[68:71], a[200:203], v[68:71]\n" \
"s_add_u32 s97, s96, s98\n" \
"ds_read2_b32 a[0:1], v46 offset0:16 offset1:17\n" \
"v_cvt_pk_bf16_f32 v34, v136, v137\n" \
"v_mfma_f32_16x16x32_bf16 v[72:75], a[64:67], a[208:211], 0\n" \
"ds_read2_b32 a[2:3], v46 offset0:18 offset1:19\n" \
"v_cvt_pk_bf16_f32 v35, v138, v139\n" \
"v_mfma_f32_16x16x32_bf16 v[72:75], a[68:71], a[204:207], v[72:75]\n" \
"s_mov_b32 s47, 524288\n" \
"s_add_u32 m0, s51, 49152\n" \
"ds_read_b128 a[36:39], v50 offset:960\n" \
"v_mfma_f32_16x16x32_bf16 v[72:75], a[72:75], a[200:203], v[72:75]\n" \
"buffer_load_dwordx4 v21, s[32:35], s47 offen offset:0 lds\n" \
"v_mfma_f32_16x16x32_bf16 v[76:79], a[64:67], a[212:215], 0\n" \
"ds_read2_b32 a[4:5], v46 offset0:144 offset1:145\n" \
"v_mul_f32 v140, s20, v140\n" \
"v_mfma_f32_16x16x32_bf16 v[76:79], a[68:71], a[208:211], v[76:79]\n" \
"ds_read2_b32 a[6:7], v46 offset0:146 offset1:147\n" \
"v_mul_f32 v141, s20, v141\n" \
"v_mfma_f32_16x16x32_bf16 v[76:79], a[72:75], a[204:207], v[76:79]\n" \
"ds_read_b128 a[40:43], v50 offset:1472\n" \
"v_mul_f32 v142, s20, v142\n" \
"v_mfma_f32_16x16x32_bf16 v[76:79], a[76:79], a[200:203], v[76:79]\n" \
"v_mul_f32 v143, s20, v143\n" \
"v_cvt_pk_bf16_f32 v36, v140, v141\n" \
"v_mfma_f32_16x16x32_bf16 v[80:83], a[64:67], a[216:219], 0\n" \
"ds_read2_b32 a[8:9], v47 offset0:16 offset1:17\n" \
"v_cvt_pk_bf16_f32 v37, v142, v143\n" \
"v_mfma_f32_16x16x32_bf16 v[80:83], a[68:71], a[212:215], v[80:83]\n" \
"ds_read2_b32 a[10:11], v47 offset0:18 offset1:19\n" \
"v_mul_f32 v144, s20, v144\n" \
"v_mfma_f32_16x16x32_bf16 v[80:83], a[72:75], a[208:211], v[80:83]\n" \
"s_add_u32 m0, s51, 50048\n" \
"ds_read_b128 a[44:47], v50 offset:1984\n" \
"v_mul_f32 v145, s20, v145\n" \
"v_mfma_f32_16x16x32_bf16 v[80:83], a[76:79], a[204:207], v[80:83]\n" \
"buffer_load_dwordx4 v21, s[32:35], s47 offen offset:128 lds\n" \
"v_mfma_f32_16x16x32_bf16 v[80:83], a[80:83], a[200:203], v[80:83]\n" \
"ds_read2_b32 a[12:13], v47 offset0:144 offset1:145\n" \
"v_mul_f32 v146, s20, v146\n" \
"v_mfma_f32_16x16x32_bf16 v[84:87], a[64:67], a[220:223], 0\n" \
"ds_read2_b32 a[14:15], v47 offset0:146 offset1:147\n" \
"v_mul_f32 v147, s20, v147\n" \
"v_mfma_f32_16x16x32_bf16 v[84:87], a[68:71], a[216:219], v[84:87]\n" \
"ds_read_b128 a[48:51], v50 offset:2496\n" \
"v_cvt_pk_bf16_f32 v38, v144, v145\n" \
"v_mfma_f32_16x16x32_bf16 v[84:87], a[72:75], a[212:215], v[84:87]\n" \
"buffer_store_dwordx4 v[42:45], v4, s[36:39], s97 offen offset:0 sc1\n" \
"v_mfma_f32_16x16x32_bf16 v[84:87], a[76:79], a[208:211], v[84:87]\n" \
"s_add_u32 s57, s57, s93\n" \
"ds_read2_b32 a[16:17], v48 offset0:16 offset1:17\n" \
"s_mov_b32 s96, s57\n" \
"v_mfma_f32_16x16x32_bf16 v[84:87], a[80:83], a[204:207], v[84:87]\n" \
"ds_read2_b32 a[18:19], v48 offset0:18 offset1:19\n" \
"v_cvt_pk_bf16_f32 v39, v146, v147\n" \
"v_mfma_f32_16x16x32_bf16 v[84:87], a[84:87], a[200:203], v[84:87]\n" \
"s_mov_b32 s47, 786432\n" \
"s_add_u32 m0, s51, 57344\n" \
"ds_read_b128 a[52:55], v50 offset:3008\n" \
"v_mfma_f32_16x16x32_bf16 v[88:91], a[64:67], a[224:227], 0\n" \
"buffer_load_dwordx4 v21, s[32:35], s47 offen offset:0 lds\n" \
"v_mfma_f32_16x16x32_bf16 v[88:91], a[68:71], a[220:223], v[88:91]\n" \
"ds_read2_b32 a[20:21], v48 offset0:144 offset1:145\n" \
"v_mul_f32 v148, s20, v148\n" \
"v_mfma_f32_16x16x32_bf16 v[88:91], a[72:75], a[216:219], v[88:91]\n" \
"ds_read2_b32 a[22:23], v48 offset0:146 offset1:147\n" \
"v_mul_f32 v149, s20, v149\n" \
"v_mfma_f32_16x16x32_bf16 v[88:91], a[76:79], a[212:215], v[88:91]\n" \
"ds_read_b128 a[56:59], v50 offset:3520\n" \
"v_mul_f32 v150, s20, v150\n" \
"v_mfma_f32_16x16x32_bf16 v[88:91], a[80:83], a[208:211], v[88:91]\n" \
"v_mul_f32 v151, s20, v151\n" \
"v_cvt_pk_bf16_f32 v40, v148, v149\n" \
"v_mfma_f32_16x16x32_bf16 v[88:91], a[84:87], a[204:207], v[88:91]\n" \
"ds_read2_b32 a[24:25], v49 offset0:16 offset1:17\n" \
"v_cvt_pk_bf16_f32 v41, v150, v151\n" \
"v_mfma_f32_16x16x32_bf16 v[88:91], a[88:91], a[200:203], v[88:91]\n" \
"ds_read2_b32 a[26:27], v49 offset0:18 offset1:19\n" \
"v_mul_f32 v152, s20, v152\n" \
"v_mfma_f32_16x16x32_bf16 v[92:95], a[64:67], a[228:231], 0\n" \
"s_add_u32 m0, s51, 58240\n" \
"ds_read_b128 a[60:63], v50 offset:4032\n" \
"v_mul_f32 v153, s20, v153\n" \
"v_mfma_f32_16x16x32_bf16 v[92:95], a[68:71], a[224:227], v[92:95]\n" \
"buffer_load_dwordx4 v21, s[32:35], s47 offen offset:128 lds\n" \
"v_mfma_f32_16x16x32_bf16 v[92:95], a[72:75], a[220:223], v[92:95]\n" \
"ds_read2_b32 a[28:29], v49 offset0:144 offset1:145\n" \
"v_mul_f32 v154, s20, v154\n" \
"v_mfma_f32_16x16x32_bf16 v[92:95], a[76:79], a[216:219], v[92:95]\n" \
"ds_read2_b32 a[30:31], v49 offset0:146 offset1:147\n" \
"v_mul_f32 v155, s20, v155\n" \
"v_mfma_f32_16x16x32_bf16 v[92:95], a[80:83], a[212:215], v[92:95]\n" \
"buffer_store_dwordx4 v[30:33], v4, s[36:39], s96 offen offset:0 sc1\n" \
"v_mfma_f32_16x16x32_bf16 v[92:95], a[84:87], a[208:211], v[92:95]\n" \
"s_add_u32 s97, s96, s98\n" \
"v_cvt_pk_bf16_f32 v42, v152, v153\n" \
"v_cvt_pk_bf16_f32 v43, v154, v155\n" \
"v_mfma_f32_16x16x32_bf16 v[92:95], a[88:91], a[204:207], v[92:95]\n" \
"v_mul_f32 v156, s20, v156\n" \
"v_mul_f32 v157, s20, v157\n" \
"v_mfma_f32_16x16x32_bf16 v[92:95], a[92:95], a[200:203], v[92:95]\n" \
"s_waitcnt lgkmcnt(0)\n" \
"v_mul_f32 v158, s20, v158\n" \
"v_mfma_f32_16x16x32_bf16 v[64:67], a[0:3], a[32:35], v[64:67]\n" \
"ds_read_b128 a[200:203], v50 offset:384\n" \
"v_mul_f32 v159, s20, v159\n" \
"v_mfma_f32_16x16x32_bf16 v[68:71], a[0:3], a[36:39], v[68:71]\n" \
"v_cvt_pk_bf16_f32 v44, v156, v157\n" \
"v_cvt_pk_bf16_f32 v45, v158, v159\n" \
"v_mfma_f32_16x16x32_bf16 v[68:71], a[4:7], a[32:35], v[68:71]\n" \
"ds_read2_b32 a[64:65], v46 offset0:32 offset1:33\n" \
"v_mul_f32 v160, s20, v160\n" \
"v_mfma_f32_16x16x32_bf16 v[72:75], a[0:3], a[40:43], v[72:75]\n" \
"ds_read2_b32 a[66:67], v46 offset0:34 offset1:35\n" \
"v_mul_f32 v161, s20, v161\n" \
"v_mfma_f32_16x16x32_bf16 v[72:75], a[4:7], a[36:39], v[72:75]\n" \
"ds_read_b128 a[204:207], v50 offset:896\n" \
"v_mul_f32 v162, s20, v162\n" \
"v_mfma_f32_16x16x32_bf16 v[72:75], a[8:11], a[32:35], v[72:75]\n" \
"v_mul_f32 v163, s20, v163\n" \
"v_cvt_pk_bf16_f32 v30, v160, v161\n" \
"v_mfma_f32_16x16x32_bf16 v[76:79], a[0:3], a[44:47], v[76:79]\n" \
"ds_read2_b32 a[68:69], v46 offset0:160 offset1:161\n" \
"v_cvt_pk_bf16_f32 v31, v162, v163\n" \
"v_mfma_f32_16x16x32_bf16 v[76:79], a[4:7], a[40:43], v[76:79]\n" \
"ds_read2_b32 a[70:71], v46 offset0:162 offset1:163\n" \
"v_mul_f32 v164, s20, v164\n" \
"v_mfma_f32_16x16x32_bf16 v[76:79], a[8:11], a[36:39], v[76:79]\n" \
"ds_read_b128 a[208:211], v50 offset:1408\n" \
"v_mul_f32 v165, s20, v165\n" \
"v_mfma_f32_16x16x32_bf16 v[76:79], a[12:15], a[32:35], v[76:79]\n" \
"buffer_store_dwordx4 v[34:37], v4, s[36:39], s97 offen offset:0 sc1\n" \
"v_mfma_f32_16x16x32_bf16 v[80:83], a[0:3], a[48:51], v[80:83]\n" \
"s_add_u32 s96, s96, s92\n" \
"ds_read2_b32 a[72:73], v47 offset0:32 offset1:33\n" \
"v_mul_f32 v166, s20, v166\n" \
"v_mfma_f32_16x16x32_bf16 v[80:83], a[4:7], a[44:47], v[80:83]\n" \
"ds_read2_b32 a[74:75], v47 offset0:34 offset1:35\n" \
"v_mul_f32 v167, s20, v167\n" \
"v_mfma_f32_16x16x32_bf16 v[80:83], a[8:11], a[40:43], v[80:83]\n" \
"ds_read_b128 a[212:215], v50 offset:1920\n" \
"v_cvt_pk_bf16_f32 v32, v164, v165\n" \
"v_mfma_f32_16x16x32_bf16 v[80:83], a[12:15], a[36:39], v[80:83]\n" \
"v_cvt_pk_bf16_f32 v33, v166, v167\n" \
"v_mul_f32 v168, s20, v168\n" \
"v_mfma_f32_16x16x32_bf16 v[80:83], a[16:19], a[32:35], v[80:83]\n" \
"ds_read2_b32 a[76:77], v47 offset0:160 offset1:161\n" \
"v_mul_f32 v169, s20, v169\n" \
"v_mfma_f32_16x16x32_bf16 v[84:87], a[0:3], a[52:55], v[84:87]\n" \
"ds_read2_b32 a[78:79], v47 offset0:162 offset1:163\n" \
"v_mul_f32 v170, s20, v170\n" \
"v_mfma_f32_16x16x32_bf16 v[84:87], a[4:7], a[48:51], v[84:87]\n" \
"ds_read_b128 a[216:219], v50 offset:2432\n" \
"v_mul_f32 v171, s20, v171\n" \
"v_mfma_f32_16x16x32_bf16 v[84:87], a[8:11], a[44:47], v[84:87]\n" \
"v_cvt_pk_bf16_f32 v34, v168, v169\n" \
"v_cvt_pk_bf16_f32 v35, v170, v171\n" \
"v_mfma_f32_16x16x32_bf16 v[84:87], a[12:15], a[40:43], v[84:87]\n" \
"ds_read2_b32 a[80:81], v48 offset0:32 offset1:33\n" \
"v_mul_f32 v172, s20, v172\n" \
"v_mfma_f32_16x16x32_bf16 v[84:87], a[16:19], a[36:39], v[84:87]\n" \
"ds_read2_b32 a[82:83], v48 offset0:34 offset1:35\n" \
"v_mul_f32 v173, s20, v173\n" \
"v_mfma_f32_16x16x32_bf16 v[84:87], a[20:23], a[32:35], v[84:87]\n" \
"ds_read_b128 a[220:223], v50 offset:2944\n" \
"v_mul_f32 v174, s20, v174\n" \
"v_mfma_f32_16x16x32_bf16 v[88:91], a[0:3], a[56:59], v[88:91]\n" \
"buffer_store_dwordx4 v[38:41], v4, s[36:39], s96 offen offset:0 sc1\n" \
"v_mfma_f32_16x16x32_bf16 v[88:91], a[4:7], a[52:55], v[88:91]\n" \
"s_add_u32 s97, s96, s98\n" \
"ds_read2_b32 a[84:85], v48 offset0:160 offset1:161\n" \
"v_mul_f32 v175, s20, v175\n" \
"v_mfma_f32_16x16x32_bf16 v[88:91], a[8:11], a[48:51], v[88:91]\n" \
"ds_read2_b32 a[86:87], v48 offset0:162 offset1:163\n" \
"v_cvt_pk_bf16_f32 v36, v172, v173\n" \
"v_mfma_f32_16x16x32_bf16 v[88:91], a[12:15], a[44:47], v[88:91]\n" \
"ds_read_b128 a[224:227], v50 offset:3456\n" \
"v_cvt_pk_bf16_f32 v37, v174, v175\n" \
"v_mfma_f32_16x16x32_bf16 v[88:91], a[16:19], a[40:43], v[88:91]\n" \
"v_mul_f32 v176, s20, v176\n" \
"v_mul_f32 v177, s20, v177\n" \
"v_mfma_f32_16x16x32_bf16 v[88:91], a[20:23], a[36:39], v[88:91]\n" \
"ds_read2_b32 a[88:89], v49 offset0:32 offset1:33\n" \
"v_mul_f32 v178, s20, v178\n" \
"v_mfma_f32_16x16x32_bf16 v[88:91], a[24:27], a[32:35], v[88:91]\n" \
"ds_read2_b32 a[90:91], v49 offset0:34 offset1:35\n" \
"v_mul_f32 v179, s20, v179\n" \
"v_mfma_f32_16x16x32_bf16 v[92:95], a[0:3], a[60:63], v[92:95]\n" \
"ds_read_b128 a[228:231], v50 offset:3968\n" \
"v_cvt_pk_bf16_f32 v38, v176, v177\n" \
"v_mfma_f32_16x16x32_bf16 v[92:95], a[4:7], a[56:59], v[92:95]\n" \
"v_cvt_pk_bf16_f32 v39, v178, v179\n" \
"v_mul_f32 v180, s20, v180\n" \
"v_mfma_f32_16x16x32_bf16 v[92:95], a[8:11], a[52:55], v[92:95]\n" \
"ds_read2_b32 a[92:93], v49 offset0:160 offset1:161\n" \
"v_mul_f32 v181, s20, v181\n" \
"v_mfma_f32_16x16x32_bf16 v[92:95], a[12:15], a[48:51], v[92:95]\n" \
"ds_read2_b32 a[94:95], v49 offset0:162 offset1:163\n" \
"v_mul_f32 v182, s20, v182\n" \
"v_mfma_f32_16x16x32_bf16 v[92:95], a[16:19], a[44:47], v[92:95]\n" \
"buffer_store_dwordx4 v[42:45], v4, s[36:39], s97 offen offset:0 sc1\n" \
"v_mfma_f32_16x16x32_bf16 v[92:95], a[20:23], a[40:43], v[92:95]\n" \
"s_add_u32 s96, s96, s92\n" \
"v_mul_f32 v183, s20, v183\n" \
"v_cvt_pk_bf16_f32 v40, v180, v181\n" \
"v_mfma_f32_16x16x32_bf16 v[92:95], a[24:27], a[36:39], v[92:95]\n" \
"v_cvt_pk_bf16_f32 v41, v182, v183\n" \
"v_mul_f32 v184, s20, v184\n" \
"v_mfma_f32_16x16x32_bf16 v[92:95], a[28:31], a[32:35], v[92:95]\n" \
"s_waitcnt lgkmcnt(0)\n" \
"v_mul_f32 v185, s20, v185\n" \
"v_mfma_f32_16x16x32_bf16 v[64:67], a[64:67], a[200:203], v[64:67]\n" \
"ds_read_b128 a[32:35], v50 offset:320\n" \
"v_mul_f32 v186, s20, v186\n" \
"v_mfma_f32_16x16x32_bf16 v[68:71], a[64:67], a[204:207], v[68:71]\n" \
"v_mul_f32 v187, s20, v187\n" \
"v_cvt_pk_bf16_f32 v42, v184, v185\n" \
"v_mfma_f32_16x16x32_bf16 v[68:71], a[68:71], a[200:203], v[68:71]\n" \
"ds_read2_b32 a[0:1], v46 offset0:48 offset1:49\n" \
"v_cvt_pk_bf16_f32 v43, v186, v187\n" \
"v_mfma_f32_16x16x32_bf16 v[72:75], a[64:67], a[208:211], v[72:75]\n" \
"ds_read2_b32 a[2:3], v46 offset0:50 offset1:51\n" \
"v_mul_f32 v188, s20, v188\n" \
"v_mfma_f32_16x16x32_bf16 v[72:75], a[68:71], a[204:207], v[72:75]\n" \
"ds_read_b128 a[36:39], v50 offset:832\n" \
"v_mul_f32 v189, s20, v189\n" \
"v_mfma_f32_16x16x32_bf16 v[72:75], a[72:75], a[200:203], v[72:75]\n" \
"v_mul_f32 v190, s20, v190\n" \
"v_mul_f32 v191, s20, v191\n" \
"v_mfma_f32_16x16x32_bf16 v[76:79], a[64:67], a[212:215], v[76:79]\n" \
"ds_read2_b32 a[4:5], v46 offset0:176 offset1:177\n" \
"v_cvt_pk_bf16_f32 v44, v188, v189\n" \
"v_mfma_f32_16x16x32_bf16 v[76:79], a[68:71], a[208:211], v[76:79]\n" \
"ds_read2_b32 a[6:7], v46 offset0:178 offset1:179\n" \
"v_cvt_pk_bf16_f32 v45, v190, v191\n" \
"v_mfma_f32_16x16x32_bf16 v[76:79], a[72:75], a[204:207], v[76:79]\n" \
"ds_read_b128 a[40:43], v50 offset:1344\n" \
"v_mul_f32 v192, s20, v192\n" \
"v_mfma_f32_16x16x32_bf16 v[76:79], a[76:79], a[200:203], v[76:79]\n" \
"buffer_store_dwordx4 v[30:33], v4, s[36:39], s96 offen offset:0 sc1\n" \
"v_mfma_f32_16x16x32_bf16 v[80:83], a[64:67], a[216:219], v[80:83]\n" \
"s_add_u32 s97, s96, s98\n" \
"ds_read2_b32 a[8:9], v47 offset0:48 offset1:49\n" \
"v_mul_f32 v193, s20, v193\n" \
"v_mfma_f32_16x16x32_bf16 v[80:83], a[68:71], a[212:215], v[80:83]\n" \
"ds_read2_b32 a[10:11], v47 offset0:50 offset1:51\n" \
"v_mul_f32 v194, s20, v194\n" \
"v_mfma_f32_16x16x32_bf16 v[80:83], a[72:75], a[208:211], v[80:83]\n" \
"ds_read_b128 a[44:47], v50 offset:1856\n" \
"v_mul_f32 v195, s20, v195\n" \
"v_mfma_f32_16x16x32_bf16 v[80:83], a[76:79], a[204:207], v[80:83]\n" \
"v_cvt_pk_bf16_f32 v30, v192, v193\n" \
"v_cvt_pk_bf16_f32 v31, v194, v195\n" \
"v_mfma_f32_16x16x32_bf16 v[80:83], a[80:83], a[200:203], v[80:83]\n" \
"ds_read2_b32 a[12:13], v47 offset0:176 offset1:177\n" \
"v_mul_f32 v196, s20, v196\n" \
"v_mfma_f32_16x16x32_bf16 v[84:87], a[64:67], a[220:223], v[84:87]\n" \
"ds_read2_b32 a[14:15], v47 offset0:178 offset1:179\n" \
"v_mul_f32 v197, s20, v197\n" \
"v_mfma_f32_16x16x32_bf16 v[84:87], a[68:71], a[216:219], v[84:87]\n" \
"ds_read_b128 a[48:51], v50 offset:2368\n" \
"v_mul_f32 v198, s20, v198\n" \
"v_mfma_f32_16x16x32_bf16 v[84:87], a[72:75], a[212:215], v[84:87]\n" \
"v_mul_f32 v199, s20, v199\n" \
"v_cvt_pk_bf16_f32 v32, v196, v197\n" \
"v_mfma_f32_16x16x32_bf16 v[84:87], a[76:79], a[208:211], v[84:87]\n" \
"LH8_%=:\ns_cbranch_execz LH12_%=\n" \
"ds_read2_b32 a[16:17], v48 offset0:48 offset1:49\n" \
"v_cvt_pk_bf16_f32 v33, v198, v199\n" \
"v_mfma_f32_16x16x32_bf16 v[84:87], a[80:83], a[204:207], v[84:87]\n" \
"ds_read2_b32 a[18:19], v48 offset0:50 offset1:51\n" \
"v_mul_f32 v200, s20, v200\n" \
"v_mfma_f32_16x16x32_bf16 v[84:87], a[84:87], a[200:203], v[84:87]\n" \
"ds_read_b128 a[52:55], v50 offset:2880\n" \
"v_mul_f32 v201, s20, v201\n" \
"v_mfma_f32_16x16x32_bf16 v[88:91], a[64:67], a[224:227], v[88:91]\n" \
"buffer_store_dwordx4 v[34:37], v4, s[36:39], s97 offen offset:0 sc1\n" \
"v_mfma_f32_16x16x32_bf16 v[88:91], a[68:71], a[220:223], v[88:91]\n" \
"s_add_u32 s96, s96, s92\n" \
"ds_read2_b32 a[20:21], v48 offset0:176 offset1:177\n" \
"v_mul_f32 v202, s20, v202\n" \
"v_mfma_f32_16x16x32_bf16 v[88:91], a[72:75], a[216:219], v[88:91]\n" \
"ds_read2_b32 a[22:23], v48 offset0:178 offset1:179\n" \
"v_mul_f32 v203, s20, v203\n" \
"v_mfma_f32_16x16x32_bf16 v[88:91], a[76:79], a[212:215], v[88:91]\n" \
"ds_read_b128 a[56:59], v50 offset:3392\n" \
"v_cvt_pk_bf16_f32 v34, v200, v201\n" \
"v_mfma_f32_16x16x32_bf16 v[88:91], a[80:83], a[208:211], v[88:91]\n" \
"v_cvt_pk_bf16_f32 v35, v202, v203\n" \
"v_mul_f32 v204, s20, v204\n" \
"v_mfma_f32_16x16x32_bf16 v[88:91], a[84:87], a[204:207], v[88:91]\n" \
"ds_read2_b32 a[24:25], v49 offset0:48 offset1:49\n" \
"v_mul_f32 v205, s20, v205\n" \
"v_mfma_f32_16x16x32_bf16 v[88:91], a[88:91], a[200:203], v[88:91]\n" \
"ds_read2_b32 a[26:27], v49 offset0:50 offset1:51\n" \
"v_mul_f32 v206, s20, v206\n" \
"v_mfma_f32_16x16x32_bf16 v[92:95], a[64:67], a[228:231], v[92:95]\n" \
"ds_read_b128 a[60:63], v50 offset:3904\n" \
"v_mul_f32 v207, s20, v207\n" \
"v_mfma_f32_16x16x32_bf16 v[92:95], a[68:71], a[224:227], v[92:95]\n" \
"v_cvt_pk_bf16_f32 v36, v204, v205\n" \
"v_cvt_pk_bf16_f32 v37, v206, v207\n" \
"v_mfma_f32_16x16x32_bf16 v[92:95], a[72:75], a[220:223], v[92:95]\n" \
"ds_read2_b32 a[28:29], v49 offset0:176 offset1:177\n" \
"v_mul_f32 v208, s20, v208\n" \
"v_mfma_f32_16x16x32_bf16 v[92:95], a[76:79], a[216:219], v[92:95]\n" \
"ds_read2_b32 a[30:31], v49 offset0:178 offset1:179\n" \
"v_mul_f32 v209, s20, v209\n" \
"v_mfma_f32_16x16x32_bf16 v[92:95], a[80:83], a[212:215], v[92:95]\n" \
"buffer_store_dwordx4 v[38:41], v4, s[36:39], s96 offen offset:0 sc1\n" \
"v_mfma_f32_16x16x32_bf16 v[92:95], a[84:87], a[208:211], v[92:95]\n" \
"s_add_u32 s97, s96, s98\n" \
"v_mul_f32 v210, s20, v210\n" \
"v_mul_f32 v211, s20, v211\n" \
"v_mfma_f32_16x16x32_bf16 v[92:95], a[88:91], a[204:207], v[92:95]\n" \
"v_cvt_pk_bf16_f32 v38, v208, v209\n" \
"v_cvt_pk_bf16_f32 v39, v210, v211\n" \
"v_mfma_f32_16x16x32_bf16 v[92:95], a[92:95], a[200:203], v[92:95]\n" \
"s_waitcnt lgkmcnt(0)\n" \
"v_mul_f32 v212, s20, v212\n" \
"v_mfma_f32_16x16x32_bf16 v[64:67], a[0:3], a[32:35], v[64:67]\n" \
"ds_read_b128 a[200:203], v50 offset:256\n" \
"v_mul_f32 v213, s20, v213\n" \
"v_mfma_f32_16x16x32_bf16 v[68:71], a[0:3], a[36:39], v[68:71]\n" \
"v_mul_f32 v214, s20, v214\n" \
"v_mul_f32 v215, s20, v215\n" \
"v_mfma_f32_16x16x32_bf16 v[68:71], a[4:7], a[32:35], v[68:71]\n" \
"ds_read2_b32 a[64:65], v46 offset0:64 offset1:65\n" \
"v_cvt_pk_bf16_f32 v40, v212, v213\n" \
"v_mfma_f32_16x16x32_bf16 v[72:75], a[0:3], a[40:43], v[72:75]\n" \
"ds_read2_b32 a[66:67], v46 offset0:66 offset1:67\n" \
"v_cvt_pk_bf16_f32 v41, v214, v215\n" \
"v_mfma_f32_16x16x32_bf16 v[72:75], a[4:7], a[36:39], v[72:75]\n" \
"ds_read_b128 a[204:207], v50 offset:768\n" \
"v_mul_f32 v216, s20, v216\n" \
"v_mfma_f32_16x16x32_bf16 v[72:75], a[8:11], a[32:35], v[72:75]\n" \
"v_mul_f32 v217, s20, v217\n" \
"v_mul_f32 v218, s20, v218\n" \
"v_mfma_f32_16x16x32_bf16 v[76:79], a[0:3], a[44:47], v[76:79]\n" \
"ds_read2_b32 a[68:69], v46 offset0:192 offset1:193\n" \
"v_mul_f32 v219, s20, v219\n" \
"v_mfma_f32_16x16x32_bf16 v[76:79], a[4:7], a[40:43], v[76:79]\n" \
"ds_read2_b32 a[70:71], v46 offset0:194 offset1:195\n" \
"v_mfma_f32_16x16x32_bf16 v[76:79], a[8:11], a[36:39], v[76:79]\n" \
"ds_read_b128 a[208:211], v50 offset:1280\n" \
"v_mfma_f32_16x16x32_bf16 v[76:79], a[12:15], a[32:35], v[76:79]\n" \
"buffer_store_dwordx4 v[42:45], v4, s[36:39], s97 offen offset:0 sc1\n" \
"v_mfma_f32_16x16x32_bf16 v[80:83], a[0:3], a[48:51], v[80:83]\n" \
"s_add_u32 s57, s57, s93\n" \
"ds_read2_b32 a[72:73], v47 offset0:64 offset1:65\n" \
"s_mov_b32 s96, s57\n" \
"v_mfma_f32_16x16x32_bf16 v[80:83], a[4:7], a[44:47], v[80:83]\n" \
"ds_read2_b32 a[74:75], v47 offset0:66 offset1:67\n" \
"v_cvt_pk_bf16_f32 v42, v216, v217\n" \
"v_mfma_f32_16x16x32_bf16 v[80:83], a[8:11], a[40:43], v[80:83]\n" \
"ds_read_b128 a[212:215], v50 offset:1792\n" \
"v_cvt_pk_bf16_f32 v43, v218, v219\n" \
"v_mfma_f32_16x16x32_bf16 v[80:83], a[12:15], a[36:39], v[80:83]\n" \
"v_mul_f32 v220, s20, v220\n" \
"v_mul_f32 v221, s20, v221\n" \
"v_mfma_f32_16x16x32_bf16 v[80:83], a[16:19], a[32:35], v[80:83]\n" \
"ds_read2_b32 a[76:77], v47 offset0:192 offset1:193\n" \
"v_mul_f32 v222, s20, v222\n" \
"v_mfma_f32_16x16x32_bf16 v[84:87], a[0:3], a[52:55], v[84:87]\n" \
"ds_read2_b32 a[78:79], v47 offset0:194 offset1:195\n" \
"v_mul_f32 v223, s20, v223\n" \
"v_mfma_f32_16x16x32_bf16 v[84:87], a[4:7], a[48:51], v[84:87]\n" \
"ds_read_b128 a[216:219], v50 offset:2304\n" \
"v_cvt_pk_bf16_f32 v44, v220, v221\n" \
"v_mfma_f32_16x16x32_bf16 v[84:87], a[8:11], a[44:47], v[84:87]\n" \
"v_cvt_pk_bf16_f32 v45, v222, v223\n" \
"v_mul_f32 v224, s20, v224\n" \
"v_mfma_f32_16x16x32_bf16 v[84:87], a[12:15], a[40:43], v[84:87]\n" \
"ds_read2_b32 a[80:81], v48 offset0:64 offset1:65\n" \
"v_mul_f32 v225, s20, v225\n" \
"v_mfma_f32_16x16x32_bf16 v[84:87], a[16:19], a[36:39], v[84:87]\n" \
"ds_read2_b32 a[82:83], v48 offset0:66 offset1:67\n" \
"v_mul_f32 v226, s20, v226\n" \
"v_mfma_f32_16x16x32_bf16 v[84:87], a[20:23], a[32:35], v[84:87]\n" \
"ds_read_b128 a[220:223], v50 offset:2816\n" \
"v_mul_f32 v227, s20, v227\n" \
"v_mfma_f32_16x16x32_bf16 v[88:91], a[0:3], a[56:59], v[88:91]\n" \
"buffer_store_dwordx4 v[30:33], v4, s[36:39], s96 offen offset:0 sc1\n" \
"v_mfma_f32_16x16x32_bf16 v[88:91], a[4:7], a[52:55], v[88:91]\n" \
"s_add_u32 s97, s96, s98\n" \
"ds_read2_b32 a[84:85], v48 offset0:192 offset1:193\n" \
"v_cvt_pk_bf16_f32 v30, v224, v225\n" \
"v_mfma_f32_16x16x32_bf16 v[88:91], a[8:11], a[48:51], v[88:91]\n" \
"ds_read2_b32 a[86:87], v48 offset0:194 offset1:195\n" \
"v_cvt_pk_bf16_f32 v31, v226, v227\n" \
"v_mfma_f32_16x16x32_bf16 v[88:91], a[12:15], a[44:47], v[88:91]\n" \
"ds_read_b128 a[224:227], v50 offset:3328\n" \
"v_mul_f32 v228, s20, v228\n" \
"v_mfma_f32_16x16x32_bf16 v[88:91], a[16:19], a[40:43], v[88:91]\n" \
"v_mul_f32 v229, s20, v229\n" \
"v_mul_f32 v230, s20, v230\n" \
"v_mfma_f32_16x16x32_bf16 v[88:91], a[20:23], a[36:39], v[88:91]\n" \
"ds_read2_b32 a[88:89], v49 offset0:64 offset1:65\n" \
"v_mul_f32 v231, s20, v231\n" \
"v_mfma_f32_16x16x32_bf16 v[88:91], a[24:27], a[32:35], v[88:91]\n" \
"ds_read2_b32 a[90:91], v49 offset0:66 offset1:67\n" \
"v_cvt_pk_bf16_f32 v32, v228, v229\n" \
"v_mfma_f32_16x16x32_bf16 v[92:95], a[0:3], a[60:63], v[92:95]\n" \
"ds_read_b128 a[228:231], v50 offset:3840\n" \
"v_cvt_pk_bf16_f32 v33, v230, v231\n" \
"v_mfma_f32_16x16x32_bf16 v[92:95], a[4:7], a[56:59], v[92:95]\n" \
"v_mul_f32 v232, s20, v232\n" \
"v_mul_f32 v233, s20, v233\n" \
"v_mfma_f32_16x16x32_bf16 v[92:95], a[8:11], a[52:55], v[92:95]\n" \
"ds_read2_b32 a[92:93], v49 offset0:192 offset1:193\n" \
"v_mul_f32 v234, s20, v234\n" \
"v_mfma_f32_16x16x32_bf16 v[92:95], a[12:15], a[48:51], v[92:95]\n" \
"ds_read2_b32 a[94:95], v49 offset0:194 offset1:195\n" \
"v_mul_f32 v235, s20, v235\n" \
"v_mfma_f32_16x16x32_bf16 v[92:95], a[16:19], a[44:47], v[92:95]\n" \
"buffer_store_dwordx4 v[34:37], v4, s[36:39], s97 offen offset:0 sc1\n" \
"v_mfma_f32_16x16x32_bf16 v[92:95], a[20:23], a[40:43], v[92:95]\n" \
"s_add_u32 s96, s96, s92\n" \
"v_cvt_pk_bf16_f32 v34, v232, v233\n" \
"v_cvt_pk_bf16_f32 v35, v234, v235\n" \
"v_mfma_f32_16x16x32_bf16 v[92:95], a[24:27], a[36:39], v[92:95]\n" \
"v_mul_f32 v236, s20, v236\n" \
"v_mul_f32 v237, s20, v237\n" \
"v_mfma_f32_16x16x32_bf16 v[92:95], a[28:31], a[32:35], v[92:95]\n" \
"s_waitcnt lgkmcnt(0)\n" \
"v_mul_f32 v238, s20, v238\n" \
"v_mfma_f32_16x16x32_bf16 v[64:67], a[64:67], a[200:203], v[64:67]\n" \
"ds_read_b128 a[32:35], v50 offset:192\n" \
"v_mul_f32 v239, s20, v239\n" \
"v_mfma_f32_16x16x32_bf16 v[68:71], a[64:67], a[204:207], v[68:71]\n" \
"v_cvt_pk_bf16_f32 v36, v236, v237\n" \
"v_cvt_pk_bf16_f32 v37, v238, v239\n" \
"v_mfma_f32_16x16x32_bf16 v[68:71], a[68:71], a[200:203], v[68:71]\n" \
"ds_read2_b32 a[0:1], v46 offset0:80 offset1:81\n" \
"v_mul_f32 v240, s20, v240\n" \
"v_mfma_f32_16x16x32_bf16 v[72:75], a[64:67], a[208:211], v[72:75]\n" \
"ds_read2_b32 a[2:3], v46 offset0:82 offset1:83\n" \
"v_mul_f32 v241, s20, v241\n" \
"v_mfma_f32_16x16x32_bf16 v[72:75], a[68:71], a[204:207], v[72:75]\n" \
"ds_read_b128 a[36:39], v50 offset:704\n" \
"v_mul_f32 v242, s20, v242\n" \
"v_mfma_f32_16x16x32_bf16 v[72:75], a[72:75], a[200:203], v[72:75]\n" \
"v_mul_f32 v243, s20, v243\n" \
"v_mfma_f32_16x16x32_bf16 v[76:79], a[64:67], a[212:215], v[76:79]\n" \
"ds_read2_b32 a[4:5], v46 offset0:208 offset1:209\n" \
"v_mfma_f32_16x16x32_bf16 v[76:79], a[68:71], a[208:211], v[76:79]\n" \
"ds_read2_b32 a[6:7], v46 offset0:210 offset1:211\n" \
"v_mfma_f32_16x16x32_bf16 v[76:79], a[72:75], a[204:207], v[76:79]\n" \
"ds_read_b128 a[40:43], v50 offset:1216\n" \
"v_mfma_f32_16x16x32_bf16 v[76:79], a[76:79], a[200:203], v[76:79]\n" \
"buffer_store_dwordx4 v[38:41], v4, s[36:39], s96 offen offset:0 sc1\n" \
"v_mfma_f32_16x16x32_bf16 v[80:83], a[64:67], a[216:219], v[80:83]\n" \
"s_add_u32 s97, s96, s98\n" \
"ds_read2_b32 a[8:9], v47 offset0:80 offset1:81\n" \
"v_cvt_pk_bf16_f32 v38, v240, v241\n" \
"v_mfma_f32_16x16x32_bf16 v[80:83], a[68:71], a[212:215], v[80:83]\n" \
"ds_read2_b32 a[10:11], v47 offset0:82 offset1:83\n" \
"v_cvt_pk_bf16_f32 v39, v242, v243\n" \
"v_mfma_f32_16x16x32_bf16 v[80:83], a[72:75], a[208:211], v[80:83]\n" \
"ds_read_b128 a[44:47], v50 offset:1728\n" \
"v_mul_f32 v244, s20, v244\n" \
"v_mfma_f32_16x16x32_bf16 v[80:83], a[76:79], a[204:207], v[80:83]\n" \
"v_mul_f32 v245, s20, v245\n" \
"v_mul_f32 v246, s20, v246\n" \
"v_mfma_f32_16x16x32_bf16 v[80:83], a[80:83], a[200:203], v[80:83]\n" \
"ds_read2_b32 a[12:13], v47 offset0:208 offset1:209\n" \
"v_mul_f32 v247, s20, v247\n" \
"v_mfma_f32_16x16x32_bf16 v[84:87], a[64:67], a[220:223], v[84:87]\n" \
"ds_read2_b32 a[14:15], v47 offset0:210 offset1:211\n" \
"v_cvt_pk_bf16_f32 v40, v244, v245\n" \
"v_mfma_f32_16x16x32_bf16 v[84:87], a[68:71], a[216:219], v[84:87]\n" \
"ds_read_b128 a[48:51], v50 offset:2240\n" \
"v_cvt_pk_bf16_f32 v41, v246, v247\n" \
"v_mfma_f32_16x16x32_bf16 v[84:87], a[72:75], a[212:215], v[84:87]\n" \
"v_mul_f32 v248, s20, v248\n" \
"v_mul_f32 v249, s20, v249\n" \
"v_mfma_f32_16x16x32_bf16 v[84:87], a[76:79], a[208:211], v[84:87]\n" \
"ds_read2_b32 a[16:17], v48 offset0:80 offset1:81\n" \
"v_mul_f32 v250, s20, v250\n" \
"v_mfma_f32_16x16x32_bf16 v[84:87], a[80:83], a[204:207], v[84:87]\n" \
"ds_read2_b32 a[18:19], v48 offset0:82 offset1:83\n" \
"v_mul_f32 v251, s20, v251\n" \
"v_mfma_f32_16x16x32_bf16 v[84:87], a[84:87], a[200:203], v[84:87]\n" \
"ds_read_b128 a[52:55], v50 offset:2752\n" \
"v_mfma_f32_16x16x32_bf16 v[88:91], a[64:67], a[224:227], v[88:91]\n" \
"buffer_store_dwordx4 v[42:45], v4, s[36:39], s97 offen offset:0 sc1\n" \
"v_mfma_f32_16x16x32_bf16 v[88:91], a[68:71], a[220:223], v[88:91]\n" \
"s_add_u32 s96, s96, s92\n" \
"ds_read2_b32 a[20:21], v48 offset0:208 offset1:209\n" \
"v_cvt_pk_bf16_f32 v42, v248, v249\n" \
"v_mfma_f32_16x16x32_bf16 v[88:91], a[72:75], a[216:219], v[88:91]\n" \
"ds_read2_b32 a[22:23], v48 offset0:210 offset1:211\n" \
"v_cvt_pk_bf16_f32 v43, v250, v251\n" \
"v_mfma_f32_16x16x32_bf16 v[88:91], a[76:79], a[212:215], v[88:91]\n" \
"ds_read_b128 a[56:59], v50 offset:3264\n" \
"v_mul_f32 v252, s20, v252\n" \
"v_mfma_f32_16x16x32_bf16 v[88:91], a[80:83], a[208:211], v[88:91]\n" \
"v_mul_f32 v253, s20, v253\n" \
"v_mul_f32 v254, s20, v254\n" \
"v_mfma_f32_16x16x32_bf16 v[88:91], a[84:87], a[204:207], v[88:91]\n" \
"ds_read2_b32 a[24:25], v49 offset0:80 offset1:81\n" \
"v_mul_f32 v255, s20, v255\n" \
"v_mfma_f32_16x16x32_bf16 v[88:91], a[88:91], a[200:203], v[88:91]\n" \
"ds_read2_b32 a[26:27], v49 offset0:82 offset1:83\n" \
"v_cvt_pk_bf16_f32 v44, v252, v253\n" \
"v_mfma_f32_16x16x32_bf16 v[92:95], a[64:67], a[228:231], v[92:95]\n" \
"ds_read_b128 a[60:63], v50 offset:3776\n" \
"v_cvt_pk_bf16_f32 v45, v254, v255\n" \
"v_mfma_f32_16x16x32_bf16 v[92:95], a[68:71], a[224:227], v[92:95]\n" \
"v_accvgpr_read_b32 v248, a128\n" \
"v_accvgpr_read_b32 v249, a129\n" \
"v_mfma_f32_16x16x32_bf16 v[92:95], a[72:75], a[220:223], v[92:95]\n" \
"ds_read2_b32 a[28:29], v49 offset0:208 offset1:209\n" \
"v_accvgpr_read_b32 v250, a130\n" \
"v_mfma_f32_16x16x32_bf16 v[92:95], a[76:79], a[216:219], v[92:95]\n" \
"ds_read2_b32 a[30:31], v49 offset0:210 offset1:211\n" \
"v_accvgpr_read_b32 v251, a131\n" \
"v_mfma_f32_16x16x32_bf16 v[92:95], a[80:83], a[212:215], v[92:95]\n" \
"buffer_store_dwordx4 v[30:33], v4, s[36:39], s96 offen offset:0 sc1\n" \
"v_mfma_f32_16x16x32_bf16 v[92:95], a[84:87], a[208:211], v[92:95]\n" \
"s_add_u32 s97, s96, s98\n" \
"v_mul_f32 v248, s20, v248\n" \
"v_mul_f32 v249, s20, v249\n" \
"v_mfma_f32_16x16x32_bf16 v[92:95], a[88:91], a[204:207], v[92:95]\n" \
"v_mul_f32 v250, s20, v250\n" \
"v_mul_f32 v251, s20, v251\n" \
"v_mfma_f32_16x16x32_bf16 v[92:95], a[92:95], a[200:203], v[92:95]\n" \
"s_waitcnt lgkmcnt(0)\n" \
"v_cvt_pk_bf16_f32 v30, v248, v249\n" \
"v_mfma_f32_16x16x32_bf16 v[64:67], a[0:3], a[32:35], v[64:67]\n" \
"ds_read_b128 a[200:203], v50 offset:128\n" \
"v_cvt_pk_bf16_f32 v31, v250, v251\n" \
"v_mfma_f32_16x16x32_bf16 v[68:71], a[0:3], a[36:39], v[68:71]\n" \
"v_accvgpr_read_b32 v252, a132\n" \
"v_accvgpr_read_b32 v253, a133\n" \
"v_mfma_f32_16x16x32_bf16 v[68:71], a[4:7], a[32:35], v[68:71]\n" \
"ds_read2_b32 a[64:65], v46 offset0:96 offset1:97\n" \
"v_accvgpr_read_b32 v254, a134\n" \
"v_mfma_f32_16x16x32_bf16 v[72:75], a[0:3], a[40:43], v[72:75]\n" \
"ds_read2_b32 a[66:67], v46 offset0:98 offset1:99\n" \
"v_accvgpr_read_b32 v255, a135\n" \
"v_mfma_f32_16x16x32_bf16 v[72:75], a[4:7], a[36:39], v[72:75]\n" \
"ds_read_b128 a[204:207], v50 offset:640\n" \
"v_mul_f32 v252, s20, v252\n" \
"v_mfma_f32_16x16x32_bf16 v[72:75], a[8:11], a[32:35], v[72:75]\n" \
"v_mul_f32 v253, s20, v253\n" \
"v_mul_f32 v254, s20, v254\n" \
"v_mfma_f32_16x16x32_bf16 v[76:79], a[0:3], a[44:47], v[76:79]\n" \
"ds_read2_b32 a[68:69], v46 offset0:224 offset1:225\n" \
"v_mul_f32 v255, s20, v255\n" \
"v_mfma_f32_16x16x32_bf16 v[76:79], a[4:7], a[40:43], v[76:79]\n" \
"ds_read2_b32 a[70:71], v46 offset0:226 offset1:227\n" \
"v_cvt_pk_bf16_f32 v32, v252, v253\n" \
"v_mfma_f32_16x16x32_bf16 v[76:79], a[8:11], a[36:39], v[76:79]\n" \
"ds_read_b128 a[208:211], v50 offset:1152\n" \
"v_cvt_pk_bf16_f32 v33, v254, v255\n" \
"v_mfma_f32_16x16x32_bf16 v[76:79], a[12:15], a[32:35], v[76:79]\n" \
"buffer_store_dwordx4 v[34:37], v4, s[36:39], s97 offen offset:0 sc1\n" \
"v_mfma_f32_16x16x32_bf16 v[80:83], a[0:3], a[48:51], v[80:83]\n" \
"s_add_u32 s96, s96, s92\n" \
"ds_read2_b32 a[72:73], v47 offset0:96 offset1:97\n" \
"v_accvgpr_read_b32 v248, a136\n" \
"v_mfma_f32_16x16x32_bf16 v[80:83], a[4:7], a[44:47], v[80:83]\n" \
"ds_read2_b32 a[74:75], v47 offset0:98 offset1:99\n" \
"v_accvgpr_read_b32 v249, a137\n" \
"v_mfma_f32_16x16x32_bf16 v[80:83], a[8:11], a[40:43], v[80:83]\n" \
"ds_read_b128 a[212:215], v50 offset:1664\n" \
"v_accvgpr_read_b32 v250, a138\n" \
"v_mfma_f32_16x16x32_bf16 v[80:83], a[12:15], a[36:39], v[80:83]\n" \
"v_accvgpr_read_b32 v251, a139\n" \
"v_mul_f32 v248, s20, v248\n" \
"v_mfma_f32_16x16x32_bf16 v[80:83], a[16:19], a[32:35], v[80:83]\n" \
"ds_read2_b32 a[76:77], v47 offset0:224 offset1:225\n" \
"v_mul_f32 v249, s20, v249\n" \
"v_mfma_f32_16x16x32_bf16 v[84:87], a[0:3], a[52:55], v[84:87]\n" \
"ds_read2_b32 a[78:79], v47 offset0:226 offset1:227\n" \
"v_mul_f32 v250, s20, v250\n" \
"v_mfma_f32_16x16x32_bf16 v[84:87], a[4:7], a[48:51], v[84:87]\n" \
"ds_read_b128 a[216:219], v50 offset:2176\n" \
"v_mul_f32 v251, s20, v251\n" \
"v_mfma_f32_16x16x32_bf16 v[84:87], a[8:11], a[44:47], v[84:87]\n" \
"v_cvt_pk_bf16_f32 v34, v248, v249\n" \
"v_cvt_pk_bf16_f32 v35, v250, v251\n" \
"v_mfma_f32_16x16x32_bf16 v[84:87], a[12:15], a[40:43], v[84:87]\n" \
"ds_read2_b32 a[80:81], v48 offset0:96 offset1:97\n" \
"v_accvgpr_read_b32 v252, a140\n" \
"v_mfma_f32_16x16x32_bf16 v[84:87], a[16:19], a[36:39], v[84:87]\n" \
"ds_read2_b32 a[82:83], v48 offset0:98 offset1:99\n" \
"v_accvgpr_read_b32 v253, a141\n" \
"v_mfma_f32_16x16x32_bf16 v[84:87], a[20:23], a[32:35], v[84:87]\n" \
"ds_read_b128 a[220:223], v50 offset:2688\n" \
"v_accvgpr_read_b32 v254, a142\n" \
"v_mfma_f32_16x16x32_bf16 v[88:91], a[0:3], a[56:59], v[88:91]\n" \
"buffer_store_dwordx4 v[38:41], v4, s[36:39], s96 offen offset:0 sc1\n" \
"v_mfma_f32_16x16x32_bf16 v[88:91], a[4:7], a[52:55], v[88:91]\n" \
"s_add_u32 s97, s96, s98\n" \
"ds_read2_b32 a[84:85], v48 offset0:224 offset1:225\n" \
"v_accvgpr_read_b32 v255, a143\n" \
"v_mfma_f32_16x16x32_bf16 v[88:91], a[8:11], a[48:51], v[88:91]\n" \
"ds_read2_b32 a[86:87], v48 offset0:226 offset1:227\n" \
"v_mul_f32 v252, s20, v252\n" \
"v_mfma_f32_16x16x32_bf16 v[88:91], a[12:15], a[44:47], v[88:91]\n" \
"ds_read_b128 a[224:227], v50 offset:3200\n" \
"v_mul_f32 v253, s20, v253\n" \
"v_mfma_f32_16x16x32_bf16 v[88:91], a[16:19], a[40:43], v[88:91]\n" \
"v_mul_f32 v254, s20, v254\n" \
"v_mul_f32 v255, s20, v255\n" \
"v_mfma_f32_16x16x32_bf16 v[88:91], a[20:23], a[36:39], v[88:91]\n" \
"ds_read2_b32 a[88:89], v49 offset0:96 offset1:97\n" \
"v_cvt_pk_bf16_f32 v36, v252, v253\n" \
"v_mfma_f32_16x16x32_bf16 v[88:91], a[24:27], a[32:35], v[88:91]\n" \
"ds_read2_b32 a[90:91], v49 offset0:98 offset1:99\n" \
"v_cvt_pk_bf16_f32 v37, v254, v255\n" \
"v_mfma_f32_16x16x32_bf16 v[92:95], a[0:3], a[60:63], v[92:95]\n" \
"ds_read_b128 a[228:231], v50 offset:3712\n" \
"v_accvgpr_read_b32 v248, a144\n" \
"v_mfma_f32_16x16x32_bf16 v[92:95], a[4:7], a[56:59], v[92:95]\n" \
"v_accvgpr_read_b32 v249, a145\n" \
"v_accvgpr_read_b32 v250, a146\n" \
"v_mfma_f32_16x16x32_bf16 v[92:95], a[8:11], a[52:55], v[92:95]\n" \
"ds_read2_b32 a[92:93], v49 offset0:224 offset1:225\n" \
"v_accvgpr_read_b32 v251, a147\n" \
"v_mfma_f32_16x16x32_bf16 v[92:95], a[12:15], a[48:51], v[92:95]\n" \
"ds_read2_b32 a[94:95], v49 offset0:226 offset1:227\n" \
"v_mul_f32 v248, s20, v248\n" \
"v_mfma_f32_16x16x32_bf16 v[92:95], a[16:19], a[44:47], v[92:95]\n" \
"buffer_store_dwordx4 v[42:45], v4, s[36:39], s97 offen offset:0 sc1\n" \
"v_mfma_f32_16x16x32_bf16 v[92:95], a[20:23], a[40:43], v[92:95]\n" \
"s_add_u32 s57, s57, s93\n" \
"s_mov_b32 s96, s57\n" \
"v_mul_f32 v249, s20, v249\n" \
"v_mfma_f32_16x16x32_bf16 v[92:95], a[24:27], a[36:39], v[92:95]\n" \
"v_mul_f32 v250, s20, v250\n" \
"v_mul_f32 v251, s20, v251\n" \
"v_mfma_f32_16x16x32_bf16 v[92:95], a[28:31], a[32:35], v[92:95]\n" \
"s_waitcnt lgkmcnt(0)\n" \
"v_cvt_pk_bf16_f32 v38, v248, v249\n" \
"v_cvt_pk_bf16_f32 v39, v250, v251\n" \
"v_mfma_f32_16x16x32_bf16 v[64:67], a[64:67], a[200:203], v[64:67]\n" \
"ds_read_b128 a[32:35], v50 offset:64\n" \
"v_accvgpr_read_b32 v252, a148\n" \
"v_mfma_f32_16x16x32_bf16 v[68:71], a[64:67], a[204:207], v[68:71]\n" \
"v_accvgpr_read_b32 v253, a149\n" \
"v_accvgpr_read_b32 v254, a150\n" \
"v_mfma_f32_16x16x32_bf16 v[68:71], a[68:71], a[200:203], v[68:71]\n" \
"ds_read2_b32 a[0:1], v46 offset0:112 offset1:113\n" \
"v_accvgpr_read_b32 v255, a151\n" \
"v_mfma_f32_16x16x32_bf16 v[72:75], a[64:67], a[208:211], v[72:75]\n" \
"ds_read2_b32 a[2:3], v46 offset0:114 offset1:115\n" \
"v_mul_f32 v252, s20, v252\n" \
"v_mfma_f32_16x16x32_bf16 v[72:75], a[68:71], a[204:207], v[72:75]\n" \
"ds_read_b128 a[36:39], v50 offset:576\n" \
"v_mul_f32 v253, s20, v253\n" \
"v_mfma_f32_16x16x32_bf16 v[72:75], a[72:75], a[200:203], v[72:75]\n" \
"v_mul_f32 v254, s20, v254\n" \
"v_mul_f32 v255, s20, v255\n" \
"v_mfma_f32_16x16x32_bf16 v[76:79], a[64:67], a[212:215], v[76:79]\n" \
"ds_read2_b32 a[4:5], v46 offset0:240 offset1:241\n" \
"v_cvt_pk_bf16_f32 v40, v252, v253\n" \
"v_mfma_f32_16x16x32_bf16 v[76:79], a[68:71], a[208:211], v[76:79]\n" \
"ds_read2_b32 a[6:7], v46 offset0:242 offset1:243\n" \
"v_cvt_pk_bf16_f32 v41, v254, v255\n" \
"v_mfma_f32_16x16x32_bf16 v[76:79], a[72:75], a[204:207], v[76:79]\n" \
"ds_read_b128 a[40:43], v50 offset:1088\n" \
"v_accvgpr_read_b32 v248, a152\n" \
"v_mfma_f32_16x16x32_bf16 v[76:79], a[76:79], a[200:203], v[76:79]\n" \
"buffer_store_dwordx4 v[30:33], v4, s[36:39], s96 offen offset:0 sc1\n" \
"v_mfma_f32_16x16x32_bf16 v[80:83], a[64:67], a[216:219], v[80:83]\n" \
"s_add_u32 s97, s96, s98\n" \
"ds_read2_b32 a[8:9], v47 offset0:112 offset1:113\n" \
"v_accvgpr_read_b32 v249, a153\n" \
"v_mfma_f32_16x16x32_bf16 v[80:83], a[68:71], a[212:215], v[80:83]\n" \
"ds_read2_b32 a[10:11], v47 offset0:114 offset1:115\n" \
"v_accvgpr_read_b32 v250, a154\n" \
"v_mfma_f32_16x16x32_bf16 v[80:83], a[72:75], a[208:211], v[80:83]\n" \
"ds_read_b128 a[44:47], v50 offset:1600\n" \
"v_accvgpr_read_b32 v251, a155\n" \
"v_mfma_f32_16x16x32_bf16 v[80:83], a[76:79], a[204:207], v[80:83]\n" \
"v_mul_f32 v248, s20, v248\n" \
"v_mul_f32 v249, s20, v249\n" \
"v_mfma_f32_16x16x32_bf16 v[80:83], a[80:83], a[200:203], v[80:83]\n" \
"ds_read2_b32 a[12:13], v47 offset0:240 offset1:241\n" \
"v_mul_f32 v250, s20, v250\n" \
"v_mfma_f32_16x16x32_bf16 v[84:87], a[64:67], a[220:223], v[84:87]\n" \
"ds_read2_b32 a[14:15], v47 offset0:242 offset1:243\n" \
"v_mul_f32 v251, s20, v251\n" \
"v_mfma_f32_16x16x32_bf16 v[84:87], a[68:71], a[216:219], v[84:87]\n" \
"ds_read_b128 a[48:51], v50 offset:2112\n" \
"v_cvt_pk_bf16_f32 v42, v248, v249\n" \
"v_mfma_f32_16x16x32_bf16 v[84:87], a[72:75], a[212:215], v[84:87]\n" \
"v_cvt_pk_bf16_f32 v43, v250, v251\n" \
"v_accvgpr_read_b32 v252, a156\n" \
"v_mfma_f32_16x16x32_bf16 v[84:87], a[76:79], a[208:211], v[84:87]\n" \
"ds_read2_b32 a[16:17], v48 offset0:112 offset1:113\n" \
"v_accvgpr_read_b32 v253, a157\n" \
"v_mfma_f32_16x16x32_bf16 v[84:87], a[80:83], a[204:207], v[84:87]\n" \
"ds_read2_b32 a[18:19], v48 offset0:114 offset1:115\n" \
"v_accvgpr_read_b32 v254, a158\n" \
"v_mfma_f32_16x16x32_bf16 v[84:87], a[84:87], a[200:203], v[84:87]\n" \
"ds_read_b128 a[52:55], v50 offset:2624\n" \
"v_accvgpr_read_b32 v255, a159\n" \
"v_mfma_f32_16x16x32_bf16 v[88:91], a[64:67], a[224:227], v[88:91]\n" \
"buffer_store_dwordx4 v[34:37], v4, s[36:39], s97 offen offset:0 sc1\n" \
"v_mfma_f32_16x16x32_bf16 v[88:91], a[68:71], a[220:223], v[88:91]\n" \
"s_add_u32 s96, s96, s92\n" \
"ds_read2_b32 a[20:21], v48 offset0:240 offset1:241\n" \
"v_mul_f32 v252, s20, v252\n" \
"v_mfma_f32_16x16x32_bf16 v[88:91], a[72:75], a[216:219], v[88:91]\n" \
"ds_read2_b32 a[22:23], v48 offset0:242 offset1:243\n" \
"v_mul_f32 v253, s20, v253\n" \
"v_mfma_f32_16x16x32_bf16 v[88:91], a[76:79], a[212:215], v[88:91]\n" \
"ds_read_b128 a[56:59], v50 offset:3136\n" \
"v_mul_f32 v254, s20, v254\n" \
"v_mfma_f32_16x16x32_bf16 v[88:91], a[80:83], a[208:211], v[88:91]\n" \
"v_mul_f32 v255, s20, v255\n" \
"v_cvt_pk_bf16_f32 v44, v252, v253\n" \
"v_mfma_f32_16x16x32_bf16 v[88:91], a[84:87], a[204:207], v[88:91]\n" \
"ds_read2_b32 a[24:25], v49 offset0:112 offset1:113\n" \
"v_cvt_pk_bf16_f32 v45, v254, v255\n" \
"v_mfma_f32_16x16x32_bf16 v[88:91], a[88:91], a[200:203], v[88:91]\n" \
"ds_read2_b32 a[26:27], v49 offset0:114 offset1:115\n" \
"v_accvgpr_read_b32 v248, a160\n" \
"v_mfma_f32_16x16x32_bf16 v[92:95], a[64:67], a[228:231], v[92:95]\n" \
"ds_read_b128 a[60:63], v50 offset:3648\n" \
"v_accvgpr_read_b32 v249, a161\n" \
"v_mfma_f32_16x16x32_bf16 v[92:95], a[68:71], a[224:227], v[92:95]\n" \
"v_accvgpr_read_b32 v250, a162\n" \
"v_accvgpr_read_b32 v251, a163\n" \
"v_mfma_f32_16x16x32_bf16 v[92:95], a[72:75], a[220:223], v[92:95]\n" \
"ds_read2_b32 a[28:29], v49 offset0:240 offset1:241\n" \
"v_mul_f32 v248, s20, v248\n" \
"v_mfma_f32_16x16x32_bf16 v[92:95], a[76:79], a[216:219], v[92:95]\n" \
"ds_read2_b32 a[30:31], v49 offset0:242 offset1:243\n" \
"v_mul_f32 v249, s20, v249\n" \
"v_mfma_f32_16x16x32_bf16 v[92:95], a[80:83], a[212:215], v[92:95]\n" \
"buffer_store_dwordx4 v[38:41], v4, s[36:39], s96 offen offset:0 sc1\n" \
"v_mfma_f32_16x16x32_bf16 v[92:95], a[84:87], a[208:211], v[92:95]\n" \
"s_add_u32 s97, s96, s98\n" \
"v_mul_f32 v250, s20, v250\n" \
"v_mul_f32 v251, s20, v251\n" \
"v_mfma_f32_16x16x32_bf16 v[92:95], a[88:91], a[204:207], v[92:95]\n" \
"v_cvt_pk_bf16_f32 v30, v248, v249\n" \
"v_cvt_pk_bf16_f32 v31, v250, v251\n" \
"v_mfma_f32_16x16x32_bf16 v[92:95], a[92:95], a[200:203], v[92:95]\n" \
"s_waitcnt lgkmcnt(0)\n" \
"v_accvgpr_read_b32 v252, a164\n" \
"v_mfma_f32_16x16x32_bf16 v[64:67], a[0:3], a[32:35], v[64:67]\n" \
"s_lshl_b32 s59, %2, 10\n" \
"ds_read_b64 v[192:193], v51 offset:10816\n" \
"v_add_u32 v243, s59, v4\n" \
"v_mfma_f32_16x16x32_bf16 v[68:71], a[0:3], a[36:39], v[68:71]\n" \
"ds_read_b64 v[194:195], v51 offset:11328\n" \
"v_and_b32 v240, 0x70, v243\n" \
"v_mfma_f32_16x16x32_bf16 v[68:71], a[4:7], a[32:35], v[68:71]\n" \
"ds_read_b64 v[196:197], v51 offset:11840\n" \
"v_lshrrev_b32 v243, 7, v243\n" \
"v_mfma_f32_16x16x32_bf16 v[72:75], a[0:3], a[40:43], v[72:75]\n" \
"ds_read_b64 v[198:199], v51 offset:12352\n" \
"v_lshl_add_u32 v240, v243, 8, v240\n" \
"v_mfma_f32_16x16x32_bf16 v[72:75], a[4:7], a[36:39], v[72:75]\n" \
"ds_read_b64 v[200:201], v51 offset:12864\n" \
"v_lshrrev_b32 v241, 2, v240\n" \
"v_mfma_f32_16x16x32_bf16 v[72:75], a[8:11], a[32:35], v[72:75]\n" \
"s_lshl_b32 s59, s8, 14\n" \
"ds_read_b64 v[202:203], v51 offset:13376\n" \
"v_add_u32 v242, 32, v241\n" \
"v_mfma_f32_16x16x32_bf16 v[76:79], a[0:3], a[44:47], v[76:79]\n" \
"s_add_u32 s59, s59, 67108864\n" \
"ds_read_b64 v[204:205], v51 offset:13888\n" \
"s_add_u32 s60, s59, 8192\n" \
"v_mfma_f32_16x16x32_bf16 v[76:79], a[4:7], a[40:43], v[76:79]\n" \
"s_mov_b32 s94, 0x05040100\n" \
"ds_read_b64 v[206:207], v51 offset:14400\n" \
"s_mov_b32 s95, 0x07060302\n" \
"v_mfma_f32_16x16x32_bf16 v[76:79], a[8:11], a[36:39], v[76:79]\n" \
"buffer_store_dwordx4 v[42:45], v4, s[36:39], s97 offen offset:0 sc1\n" \
"v_mfma_f32_16x16x32_bf16 v[76:79], a[12:15], a[32:35], v[76:79]\n" \
"s_add_u32 s96, s96, s92\n" \
"v_accvgpr_read_b32 v253, a165\n" \
"v_accvgpr_read_b32 v254, a166\n" \
"v_mfma_f32_16x16x32_bf16 v[80:83], a[0:3], a[48:51], v[80:83]\n" \
"v_accvgpr_read_b32 v255, a167\n" \
"v_mul_f32 v252, s20, v252\n" \
"v_mfma_f32_16x16x32_bf16 v[80:83], a[4:7], a[44:47], v[80:83]\n" \
"s_waitcnt vmcnt(40) lgkmcnt(0)\n" \
"v_mul_f32 v253, s20, v253\n" \
"v_mfma_f32_16x16x32_bf16 v[80:83], a[8:11], a[40:43], v[80:83]\n" \
"s_barrier\n" \
"ds_read_b64_tr_b16 a[64:65], v9 offset:33280\n" \
"v_and_b32 v208, 0xffff0000, v193\n" \
"v_mfma_f32_16x16x32_bf16 v[80:83], a[12:15], a[36:39], v[80:83]\n" \
"ds_read_b64_tr_b16 a[66:67], v10 offset:33280\n" \
"v_lshlrev_b32 v209, 16, v193\n" \
"v_mfma_f32_16x16x32_bf16 v[80:83], a[16:19], a[32:35], v[80:83]\n" \
"ds_read_b64_tr_b16 a[68:69], v9 offset:41472\n" \
"v_and_b32 v210, 0xffff0000, v192\n" \
"v_mfma_f32_16x16x32_bf16 v[84:87], a[0:3], a[52:55], v[84:87]\n" \
"ds_read_b64_tr_b16 a[70:71], v10 offset:41472\n" \
"v_lshlrev_b32 v211, 16, v192\n" \
"v_mfma_f32_16x16x32_bf16 v[84:87], a[4:7], a[48:51], v[84:87]\n" \
"s_mov_b32 s46, 256\n" \
"s_add_u32 m0, s50, 32768\n" \
"ds_read_b128 a[200:203], v7 offset:0\n" \
"v_mfma_f32_16x16x32_bf16 v[84:87], a[8:11], a[44:47], v[84:87]\n" \
"buffer_load_dwordx4 v13, s[28:31], s46 offen lds\n" \
"v_mfma_f32_16x16x32_bf16 v[84:87], a[12:15], a[40:43], v[84:87]\n" \
"ds_read_b128 a[204:207], v8 offset:0\n" \
"v_fma_f32 v208, v208, v28, v64\n" \
"v_mfma_f32_16x16x32_bf16 v[84:87], a[16:19], a[36:39], v[84:87]\n" \
"ds_read_b128 a[208:211], v7 offset:2048\n" \
"v_fma_f32 v209, v209, v28, v65\n" \
"v_mfma_f32_16x16x32_bf16 v[84:87], a[20:23], a[32:35], v[84:87]\n" \
"ds_read_b128 a[212:215], v8 offset:2048\n" \
"v_fma_f32 v210, v210, v28, v66\n" \
"v_mfma_f32_16x16x32_bf16 v[88:91], a[0:3], a[56:59], v[88:91]\n" \
"ds_read_b128 a[216:219], v7 offset:4096\n" \
"v_fma_f32 v211, v211, v28, v67\n" \
"v_mfma_f32_16x16x32_bf16 v[88:91], a[4:7], a[52:55], v[88:91]\n" \
"s_add_u32 m0, s50, 36864\n" \
"ds_read_b128 a[220:223], v8 offset:4096\n" \
"v_and_b32 v212, 0xffff0000, v195\n" \
"v_mfma_f32_16x16x32_bf16 v[88:91], a[8:11], a[48:51], v[88:91]\n" \
"buffer_load_dwordx4 v14, s[28:31], s46 offen lds\n" \
"v_mfma_f32_16x16x32_bf16 v[88:91], a[12:15], a[44:47], v[88:91]\n" \
"ds_read_b128 a[224:227], v7 offset:6144\n" \
"v_lshlrev_b32 v213, 16, v195\n" \
"v_mfma_f32_16x16x32_bf16 v[88:91], a[16:19], a[40:43], v[88:91]\n" \
"ds_read_b128 a[228:231], v8 offset:6144\n" \
"v_and_b32 v214, 0xffff0000, v194\n" \
"v_mfma_f32_16x16x32_bf16 v[88:91], a[20:23], a[36:39], v[88:91]\n" \
"ds_read_b64_tr_b16 a[72:73], v11 offset:33280\n" \
"v_lshlrev_b32 v215, 16, v194\n" \
"v_mfma_f32_16x16x32_bf16 v[88:91], a[24:27], a[32:35], v[88:91]\n" \
"ds_read_b64_tr_b16 a[74:75], v12 offset:33280\n" \
"v_fma_f32 v212, v212, v28, v68\n" \
"v_mfma_f32_16x16x32_bf16 v[92:95], a[0:3], a[60:63], v[92:95]\n" \
"s_add_u32 m0, s50, 40960\n" \
"ds_read_b64_tr_b16 a[76:77], v11 offset:41472\n" \
"v_fma_f32 v213, v213, v28, v69\n" \
"v_mfma_f32_16x16x32_bf16 v[92:95], a[4:7], a[56:59], v[92:95]\n" \
"buffer_load_dwordx4 v15, s[28:31], s46 offen lds\n" \
"v_mfma_f32_16x16x32_bf16 v[92:95], a[8:11], a[52:55], v[92:95]\n" \
"LH9_%=:\ns_cbranch_execz LH13_%=\n" \
"ds_read_b64_tr_b16 a[78:79], v12 offset:41472\n" \
"v_fma_f32 v214, v214, v28, v70\n" \
"v_mfma_f32_16x16x32_bf16 v[92:95], a[12:15], a[48:51], v[92:95]\n" \
"ds_read_b64_tr_b16 a[80:81], v9 offset:33792\n" \
"v_fma_f32 v215, v215, v28, v71\n" \
"v_mfma_f32_16x16x32_bf16 v[92:95], a[16:19], a[44:47], v[92:95]\n" \
"ds_read_b64_tr_b16 a[82:83], v10 offset:33792\n" \
"v_and_b32 v216, 0xffff0000, v197\n" \
"v_mfma_f32_16x16x32_bf16 v[92:95], a[20:23], a[40:43], v[92:95]\n" \
"ds_read_b64_tr_b16 a[84:85], v9 offset:41984\n" \
"v_lshlrev_b32 v217, 16, v197\n" \
"v_mfma_f32_16x16x32_bf16 v[92:95], a[24:27], a[36:39], v[92:95]\n" \
"ds_read_b64_tr_b16 a[86:87], v10 offset:41984\n" \
"v_and_b32 v218, 0xffff0000, v196\n" \
"v_mfma_f32_16x16x32_bf16 v[92:95], a[28:31], a[32:35], v[92:95]\n" \
"s_waitcnt lgkmcnt(8)\n" \
"v_lshlrev_b32 v219, 16, v196\n" \
"v_fma_f32 v216, v216, v28, v72\n" \
"s_branch LHR3_%=\ns_nop 0\ns_nop 0\ns_nop 0\ns_nop 0\ns_nop 0\ns_nop 0\ns_nop 0\ns_nop 0\ns_nop 0\nLHR3_%=:\n" \
"v_mfma_f32_16x16x32_bf16 v[96:99], a[64:67], a[200:203], 0\n" \
"s_add_u32 m0, s50, 45056\n" \
"ds_read_b64_tr_b16 a[88:89], v11 offset:33792\n" \
"v_fma_f32 v217, v217, v28, v73\n" \
"v_mfma_f32_16x16x32_bf16 v[96:99], a[68:71], a[204:207], v[96:99]\n" \
"buffer_load_dwordx4 v16, s[28:31], s46 offen lds\n" \
"v_mfma_f32_16x16x32_bf16 v[104:107], a[64:67], a[208:211], 0\n" \
"ds_read_b64_tr_b16 a[90:91], v12 offset:33792\n" \
"v_fma_f32 v218, v218, v28, v74\n" \
"v_mfma_f32_16x16x32_bf16 v[104:107], a[68:71], a[212:215], v[104:107]\n" \
"ds_read_b64_tr_b16 a[92:93], v11 offset:41984\n" \
"v_fma_f32 v219, v219, v28, v75\n" \
"v_mfma_f32_16x16x32_bf16 v[112:115], a[64:67], a[216:219], 0\n" \
"s_mov_b32 s46, 256\n" \
"s_add_u32 m0, s50, 49152\n" \
"ds_read_b64_tr_b16 a[94:95], v12 offset:41984\n" \
"v_mfma_f32_16x16x32_bf16 v[112:115], a[68:71], a[220:223], v[112:115]\n" \
"buffer_load_dwordx4 v17, s[28:31], s46 offen lds\n" \
"v_mfma_f32_16x16x32_bf16 v[120:123], a[64:67], a[224:227], 0\n" \
"s_waitcnt lgkmcnt(8)\n" \
"ds_read_b64_tr_b16 a[96:97], v9 offset:34304\n" \
"v_and_b32 v220, 0xffff0000, v199\n" \
"v_mfma_f32_16x16x32_bf16 v[120:123], a[68:71], a[228:231], v[120:123]\n" \
"ds_read_b64_tr_b16 a[98:99], v10 offset:34304\n" \
"v_lshlrev_b32 v221, 16, v199\n" \
"v_mfma_f32_16x16x32_bf16 v[100:103], a[72:75], a[200:203], 0\n" \
"s_add_u32 m0, s50, 53248\n" \
"ds_read_b64_tr_b16 a[100:101], v9 offset:42496\n" \
"v_and_b32 v222, 0xffff0000, v198\n" \
"v_mfma_f32_16x16x32_bf16 v[100:103], a[76:79], a[204:207], v[100:103]\n" \
"buffer_load_dwordx4 v18, s[28:31], s46 offen lds\n" \
"v_mfma_f32_16x16x32_bf16 v[108:111], a[72:75], a[208:211], 0\n" \
"ds_read_b64_tr_b16 a[102:103], v10 offset:42496\n" \
"v_lshlrev_b32 v223, 16, v198\n" \
"v_mfma_f32_16x16x32_bf16 v[108:111], a[76:79], a[212:215], v[108:111]\n" \
"ds_read_b64_tr_b16 a[104:105], v11 offset:34304\n" \
"v_fma_f32 v220, v220, v28, v76\n" \
"v_mfma_f32_16x16x32_bf16 v[116:119], a[72:75], a[216:219], 0\n" \
"s_add_u32 m0, s50, 57344\n" \
"ds_read_b64_tr_b16 a[106:107], v12 offset:34304\n" \
"v_fma_f32 v221, v221, v28, v77\n" \
"v_mfma_f32_16x16x32_bf16 v[116:119], a[76:79], a[220:223], v[116:119]\n" \
"buffer_load_dwordx4 v19, s[28:31], s46 offen lds\n" \
"v_mfma_f32_16x16x32_bf16 v[124:127], a[72:75], a[224:227], 0\n" \
"s_waitcnt lgkmcnt(10)\n" \
"ds_read_b64_tr_b16 a[108:109], v11 offset:42496\n" \
"v_fma_f32 v222, v222, v28, v78\n" \
"v_mfma_f32_16x16x32_bf16 v[124:127], a[76:79], a[228:231], v[124:127]\n" \
"ds_read_b64_tr_b16 a[110:111], v12 offset:42496\n" \
"v_fma_f32 v223, v223, v28, v79\n" \
"v_mfma_f32_16x16x32_bf16 v[128:131], a[80:83], a[200:203], 0\n" \
"s_add_u32 m0, s50, 61440\n" \
"ds_read_b64_tr_b16 a[64:65], v9 offset:32768\n" \
"v_and_b32 v224, 0xffff0000, v201\n" \
"v_mfma_f32_16x16x32_bf16 v[128:131], a[84:87], a[204:207], v[128:131]\n" \
"buffer_load_dwordx4 v20, s[28:31], s46 offen lds\n" \
"v_mfma_f32_16x16x32_bf16 v[136:139], a[80:83], a[208:211], 0\n" \
"ds_read_b64_tr_b16 a[66:67], v10 offset:32768\n" \
"v_lshlrev_b32 v225, 16, v201\n" \
"v_mfma_f32_16x16x32_bf16 v[136:139], a[84:87], a[212:215], v[136:139]\n" \
"ds_read_b64_tr_b16 a[68:69], v9 offset:40960\n" \
"v_and_b32 v226, 0xffff0000, v200\n" \
"v_mfma_f32_16x16x32_bf16 v[144:147], a[80:83], a[216:219], 0\n" \
"s_mov_b32 s47, 1048576\n" \
"s_add_u32 m0, s51, 0\n" \
"ds_read_b64_tr_b16 a[70:71], v10 offset:40960\n" \
"v_mfma_f32_16x16x32_bf16 v[144:147], a[84:87], a[220:223], v[144:147]\n" \
"buffer_load_dwordx4 v21, s[32:35], s47 offen offset:0 lds\n" \
"v_mfma_f32_16x16x32_bf16 v[152:155], a[80:83], a[224:227], 0\n" \
"s_waitcnt lgkmcnt(12)\n" \
"ds_read_b64_tr_b16 a[72:73], v11 offset:32768\n" \
"v_lshlrev_b32 v227, 16, v200\n" \
"v_mfma_f32_16x16x32_bf16 v[152:155], a[84:87], a[228:231], v[152:155]\n" \
"ds_read_b64_tr_b16 a[74:75], v12 offset:32768\n" \
"v_fma_f32 v224, v224, v28, v80\n" \
"v_mfma_f32_16x16x32_bf16 v[132:135], a[88:91], a[200:203], 0\n" \
"s_add_u32 m0, s51, 896\n" \
"ds_read_b64_tr_b16 a[76:77], v11 offset:40960\n" \
"v_fma_f32 v225, v225, v28, v81\n" \
"v_mfma_f32_16x16x32_bf16 v[132:135], a[92:95], a[204:207], v[132:135]\n" \
"buffer_load_dwordx4 v21, s[32:35], s47 offen offset:128 lds\n" \
"v_mfma_f32_16x16x32_bf16 v[140:143], a[88:91], a[208:211], 0\n" \
"ds_read_b64_tr_b16 a[78:79], v12 offset:40960\n" \
"v_fma_f32 v226, v226, v28, v82\n" \
"v_mfma_f32_16x16x32_bf16 v[140:143], a[92:95], a[212:215], v[140:143]\n" \
"v_fma_f32 v227, v227, v28, v83\n" \
"v_and_b32 v228, 0xffff0000, v203\n" \
"v_mfma_f32_16x16x32_bf16 v[148:151], a[88:91], a[216:219], 0\n" \
"s_mov_b32 s47, 1310720\n" \
"s_add_u32 m0, s51, 8192\n" \
"v_lshlrev_b32 v229, 16, v203\n" \
"v_mfma_f32_16x16x32_bf16 v[148:151], a[92:95], a[220:223], v[148:151]\n" \
"buffer_load_dwordx4 v21, s[32:35], s47 offen offset:0 lds\n" \
"v_mfma_f32_16x16x32_bf16 v[156:159], a[88:91], a[224:227], 0\n" \
"s_waitcnt vmcnt(31) lgkmcnt(0)\n" \
"v_and_b32 v230, 0xffff0000, v202\n" \
"v_lshlrev_b32 v231, 16, v202\n" \
"v_mfma_f32_16x16x32_bf16 v[156:159], a[92:95], a[228:231], v[156:159]\n" \
"s_barrier\n" \
"v_fma_f32 v228, v228, v28, v84\n" \
"v_fma_f32 v229, v229, v28, v85\n" \
"v_mfma_f32_16x16x32_bf16 v[160:163], a[96:99], a[200:203], 0\n" \
"s_add_u32 m0, s51, 9088\n" \
"ds_read_b64_tr_b16 a[80:81], v9 offset:49664\n" \
"v_fma_f32 v230, v230, v28, v86\n" \
"v_mfma_f32_16x16x32_bf16 v[160:163], a[100:103], a[204:207], v[160:163]\n" \
"buffer_load_dwordx4 v21, s[32:35], s47 offen offset:128 lds\n" \
"v_mfma_f32_16x16x32_bf16 v[168:171], a[96:99], a[208:211], 0\n" \
"ds_read_b64_tr_b16 a[82:83], v10 offset:49664\n" \
"v_fma_f32 v231, v231, v28, v87\n" \
"v_mfma_f32_16x16x32_bf16 v[168:171], a[100:103], a[212:215], v[168:171]\n" \
"ds_read_b64_tr_b16 a[84:85], v9 offset:57856\n" \
"v_and_b32 v232, 0xffff0000, v205\n" \
"v_mfma_f32_16x16x32_bf16 v[176:179], a[96:99], a[216:219], 0\n" \
"ds_read_b64_tr_b16 a[86:87], v10 offset:57856\n" \
"v_lshlrev_b32 v233, 16, v205\n" \
"v_mfma_f32_16x16x32_bf16 v[176:179], a[100:103], a[220:223], v[176:179]\n" \
"s_mov_b32 s46, 384\n" \
"s_add_u32 m0, s50, 65536\n" \
"ds_read_b128 a[0:3], v5 offset:0\n" \
"v_mfma_f32_16x16x32_bf16 v[184:187], a[96:99], a[224:227], 0\n" \
"buffer_load_dwordx4 v13, s[28:31], s46 offen lds\n" \
"v_mfma_f32_16x16x32_bf16 v[184:187], a[100:103], a[228:231], v[184:187]\n" \
"ds_read_b128 a[4:7], v6 offset:0\n" \
"v_and_b32 v234, 0xffff0000, v204\n" \
"v_mfma_f32_16x16x32_bf16 v[164:167], a[104:107], a[200:203], 0\n" \
"ds_read_b128 a[8:11], v5 offset:2048\n" \
"v_lshlrev_b32 v235, 16, v204\n" \
"v_mfma_f32_16x16x32_bf16 v[164:167], a[108:111], a[204:207], v[164:167]\n" \
"ds_read_b128 a[12:15], v6 offset:2048\n" \
"v_fma_f32 v232, v232, v28, v88\n" \
"v_mfma_f32_16x16x32_bf16 v[172:175], a[104:107], a[208:211], 0\n" \
"s_add_u32 m0, s50, 69632\n" \
"ds_read_b128 a[16:19], v5 offset:4096\n" \
"v_fma_f32 v233, v233, v28, v89\n" \
"v_mfma_f32_16x16x32_bf16 v[172:175], a[108:111], a[212:215], v[172:175]\n" \
"buffer_load_dwordx4 v14, s[28:31], s46 offen lds\n" \
"v_fma_f32 v234, v234, v28, v90\n" \
"v_fma_f32 v235, v235, v28, v91\n" \
"v_and_b32 v236, 0xffff0000, v207\n" \
"v_lshlrev_b32 v237, 16, v207\n" \
"v_and_b32 v238, 0xffff0000, v206\n" \
"v_lshlrev_b32 v239, 16, v206\n" \
"v_fma_f32 v236, v236, v28, v92\n" \
"v_fma_f32 v237, v237, v28, v93\n" \
"v_fma_f32 v238, v238, v28, v94\n" \
"v_fma_f32 v239, v239, v28, v95\n" \
"v_mfma_f32_16x16x32_bf16 v[180:183], a[104:107], a[216:219], 0\n" \
"ds_read_b128 a[20:23], v6 offset:4096\n" \
"v_cvt_pk_bf16_f32 v192, v208, v209\n" \
"v_mfma_f32_16x16x32_bf16 v[180:183], a[108:111], a[220:223], v[180:183]\n" \
"ds_read_b128 a[24:27], v5 offset:6144\n" \
"v_cvt_pk_bf16_f32 v193, v210, v211\n" \
"v_mfma_f32_16x16x32_bf16 v[188:191], a[104:107], a[224:227], 0\n" \
"ds_read_b128 a[28:31], v6 offset:6144\n" \
"v_mul_f32 v254, s20, v254\n" \
"v_mfma_f32_16x16x32_bf16 v[188:191], a[108:111], a[228:231], v[188:191]\n" \
"s_add_u32 m0, s50, 73728\n" \
"ds_read_b64_tr_b16 a[88:89], v11 offset:49664\n" \
"v_mul_f32 v255, s20, v255\n" \
"v_mfma_f32_16x16x32_bf16 v[64:67], a[64:67], a[200:203], 0\n" \
"buffer_load_dwordx4 v15, s[28:31], s46 offen lds\n" \
"v_mfma_f32_16x16x32_bf16 v[64:67], a[68:71], a[204:207], v[64:67]\n" \
"ds_read_b64_tr_b16 a[90:91], v12 offset:49664\n" \
"v_cvt_pk_bf16_f32 v32, v252, v253\n" \
"v_mfma_f32_16x16x32_bf16 v[72:75], a[64:67], a[208:211], 0\n" \
"ds_read_b64_tr_b16 a[92:93], v11 offset:57856\n" \
"v_cvt_pk_bf16_f32 v33, v254, v255\n" \
"v_mfma_f32_16x16x32_bf16 v[72:75], a[68:71], a[212:215], v[72:75]\n" \
"ds_read_b64_tr_b16 a[94:95], v12 offset:57856\n" \
"v_accvgpr_read_b32 v248, a168\n" \
"v_mfma_f32_16x16x32_bf16 v[80:83], a[64:67], a[216:219], 0\n" \
"s_add_u32 m0, s50, 77824\n" \
"ds_read_b64_tr_b16 a[96:97], v9 offset:50176\n" \
"v_accvgpr_read_b32 v249, a169\n" \
"v_mfma_f32_16x16x32_bf16 v[80:83], a[68:71], a[220:223], v[80:83]\n" \
"buffer_load_dwordx4 v16, s[28:31], s46 offen lds\n" \
"v_mfma_f32_16x16x32_bf16 v[88:91], a[64:67], a[224:227], 0\n" \
"ds_read_b64_tr_b16 a[98:99], v10 offset:50176\n" \
"v_accvgpr_read_b32 v250, a170\n" \
"v_mfma_f32_16x16x32_bf16 v[88:91], a[68:71], a[228:231], v[88:91]\n" \
"ds_read_b64_tr_b16 a[100:101], v9 offset:58368\n" \
"v_accvgpr_read_b32 v251, a171\n" \
"v_mfma_f32_16x16x32_bf16 v[68:71], a[72:75], a[200:203], 0\n" \
"ds_read_b64_tr_b16 a[102:103], v10 offset:58368\n" \
"v_mul_f32 v248, s20, v248\n" \
"v_mfma_f32_16x16x32_bf16 v[68:71], a[76:79], a[204:207], v[68:71]\n" \
"ds_read_b64_tr_b16 a[104:105], v11 offset:50176\n" \
"v_mul_f32 v249, s20, v249\n" \
"v_mfma_f32_16x16x32_bf16 v[76:79], a[72:75], a[208:211], 0\n" \
"ds_read_b64_tr_b16 a[106:107], v12 offset:50176\n" \
"v_mul_f32 v250, s20, v250\n" \
"v_mfma_f32_16x16x32_bf16 v[76:79], a[76:79], a[212:215], v[76:79]\n" \
"ds_read_b64_tr_b16 a[108:109], v11 offset:58368\n" \
"ds_write_b64 v55, v[192:193] offset:16384\n" \
"v_cvt_pk_bf16_f32 v194, v212, v213\n" \
"v_cvt_pk_bf16_f32 v195, v214, v215\n" \
"ds_write_b64 v55, v[194:195] offset:16896\n" \
"v_cvt_pk_bf16_f32 v196, v216, v217\n" \
"v_cvt_pk_bf16_f32 v197, v218, v219\n" \
"ds_write_b64 v55, v[196:197] offset:17408\n" \
"v_cvt_pk_bf16_f32 v198, v220, v221\n" \
"v_cvt_pk_bf16_f32 v199, v222, v223\n" \
"ds_write_b64 v55, v[198:199] offset:17920\n" \
"v_cvt_pk_bf16_f32 v200, v224, v225\n" \
"v_cvt_pk_bf16_f32 v201, v226, v227\n" \
"ds_write_b64 v55, v[200:201] offset:18432\n" \
"v_cvt_pk_bf16_f32 v202, v228, v229\n" \
"v_cvt_pk_bf16_f32 v203, v230, v231\n" \
"ds_write_b64 v55, v[202:203] offset:18944\n" \
"v_cvt_pk_bf16_f32 v204, v232, v233\n" \
"v_cvt_pk_bf16_f32 v205, v234, v235\n" \
"ds_write_b64 v55, v[204:205] offset:19456\n" \
"v_cvt_pk_bf16_f32 v206, v236, v237\n" \
"v_cvt_pk_bf16_f32 v207, v238, v239\n" \
"ds_write_b64 v55, v[206:207] offset:19968\n" \
"v_mfma_f32_16x16x32_bf16 v[84:87], a[72:75], a[216:219], 0\n" \
"ds_read_b64_tr_b16 a[110:111], v12 offset:58368\n" \
"v_mul_f32 v251, s20, v251\n" \
"v_mfma_f32_16x16x32_bf16 v[84:87], a[76:79], a[220:223], v[84:87]\n" \
"buffer_store_dwordx4 v[30:33], v4, s[36:39], s96 offen offset:0 sc1\n" \
"v_mfma_f32_16x16x32_bf16 v[92:95], a[72:75], a[224:227], 0\n" \
"s_add_u32 s97, s96, s98\n" \
"v_cvt_pk_bf16_f32 v34, v248, v249\n" \
"v_cvt_pk_bf16_f32 v35, v250, v251\n" \
"v_mfma_f32_16x16x32_bf16 v[92:95], a[76:79], a[228:231], v[92:95]\n" \
"s_waitcnt lgkmcnt(15)\n" \
"s_waitcnt lgkmcnt(1)\n" \
"s_barrier\n" \
"v_mfma_f32_16x16x32_bf16 v[96:99], a[80:83], a[0:3], v[96:99]\n" \
"s_mov_b32 s46, 384\n" \
"s_add_u32 m0, s50, 81920\n" \
"ds_read_b64_tr_b16 a[64:65], v9 offset:50688\n" \
"v_mfma_f32_16x16x32_bf16 v[96:99], a[84:87], a[4:7], v[96:99]\n" \
"buffer_load_dwordx4 v17, s[28:31], s46 offen lds\n" \
"v_mfma_f32_16x16x32_bf16 v[104:107], a[80:83], a[8:11], v[104:107]\n" \
"ds_read_b64_tr_b16 a[66:67], v10 offset:50688\n" \
"v_accvgpr_read_b32 v252, a172\n" \
"v_mfma_f32_16x16x32_bf16 v[104:107], a[84:87], a[12:15], v[104:107]\n" \
"ds_read_b64_tr_b16 a[68:69], v9 offset:58880\n" \
"v_accvgpr_read_b32 v253, a173\n" \
"v_mfma_f32_16x16x32_bf16 v[112:115], a[80:83], a[16:19], v[112:115]\n" \
"s_add_u32 m0, s50, 86016\n" \
"ds_read_b64_tr_b16 a[70:71], v10 offset:58880\n" \
"v_accvgpr_read_b32 v254, a174\n" \
"v_mfma_f32_16x16x32_bf16 v[112:115], a[84:87], a[20:23], v[112:115]\n" \
"buffer_load_dwordx4 v18, s[28:31], s46 offen lds\n" \
"v_mfma_f32_16x16x32_bf16 v[120:123], a[80:83], a[24:27], v[120:123]\n" \
"s_waitcnt lgkmcnt(15)\n" \
"ds_read_b64_tr_b16 a[72:73], v11 offset:50688\n" \
"v_accvgpr_read_b32 v255, a175\n" \
"v_mfma_f32_16x16x32_bf16 v[120:123], a[84:87], a[28:31], v[120:123]\n" \
"ds_read_b64_tr_b16 a[74:75], v12 offset:50688\n" \
"v_mul_f32 v252, s20, v252\n" \
"v_mfma_f32_16x16x32_bf16 v[100:103], a[88:91], a[0:3], v[100:103]\n" \
"s_add_u32 m0, s50, 90112\n" \
"ds_read_b64_tr_b16 a[76:77], v11 offset:58880\n" \
"v_mul_f32 v253, s20, v253\n" \
"v_mfma_f32_16x16x32_bf16 v[100:103], a[92:95], a[4:7], v[100:103]\n" \
"buffer_load_dwordx4 v19, s[28:31], s46 offen lds\n" \
"v_mfma_f32_16x16x32_bf16 v[108:111], a[88:91], a[8:11], v[108:111]\n" \
"ds_read_b64_tr_b16 a[78:79], v12 offset:58880\n" \
"v_mul_f32 v254, s20, v254\n" \
"v_mfma_f32_16x16x32_bf16 v[108:111], a[92:95], a[12:15], v[108:111]\n" \
"ds_read_b64_tr_b16 a[80:81], v9 offset:49152\n" \
"v_mul_f32 v255, s20, v255\n" \
"v_mfma_f32_16x16x32_bf16 v[116:119], a[88:91], a[16:19], v[116:119]\n" \
"s_add_u32 m0, s50, 94208\n" \
"ds_read_b64_tr_b16 a[82:83], v10 offset:49152\n" \
"v_cvt_pk_bf16_f32 v36, v252, v253\n" \
"v_mfma_f32_16x16x32_bf16 v[116:119], a[92:95], a[20:23], v[116:119]\n" \
"buffer_load_dwordx4 v20, s[28:31], s46 offen lds\n" \
"v_mfma_f32_16x16x32_bf16 v[124:127], a[88:91], a[24:27], v[124:127]\n" \
"s_waitcnt lgkmcnt(15)\n" \
"ds_read_b64_tr_b16 a[84:85], v9 offset:57344\n" \
"v_cvt_pk_bf16_f32 v37, v254, v255\n" \
"v_mfma_f32_16x16x32_bf16 v[124:127], a[92:95], a[28:31], v[124:127]\n" \
"ds_read_b64_tr_b16 a[86:87], v10 offset:57344\n" \
"v_accvgpr_read_b32 v248, a176\n" \
"v_mfma_f32_16x16x32_bf16 v[128:131], a[96:99], a[0:3], v[128:131]\n" \
"s_mov_b32 s47, 2097152\n" \
"s_add_u32 m0, s51, 32768\n" \
"ds_read_b64_tr_b16 a[88:89], v11 offset:49152\n" \
"v_mfma_f32_16x16x32_bf16 v[128:131], a[100:103], a[4:7], v[128:131]\n" \
"buffer_load_dwordx4 v21, s[32:35], s47 offen offset:0 lds\n" \
"v_mfma_f32_16x16x32_bf16 v[136:139], a[96:99], a[8:11], v[136:139]\n" \
"ds_read_b64_tr_b16 a[90:91], v12 offset:49152\n" \
"v_accvgpr_read_b32 v249, a177\n" \
"v_mfma_f32_16x16x32_bf16 v[136:139], a[100:103], a[12:15], v[136:139]\n" \
"ds_read_b64_tr_b16 a[92:93], v11 offset:57344\n" \
"v_accvgpr_read_b32 v250, a178\n" \
"v_mfma_f32_16x16x32_bf16 v[144:147], a[96:99], a[16:19], v[144:147]\n" \
"s_add_u32 m0, s51, 33664\n" \
"ds_read_b64_tr_b16 a[94:95], v12 offset:57344\n" \
"v_accvgpr_read_b32 v251, a179\n" \
"v_mfma_f32_16x16x32_bf16 v[144:147], a[100:103], a[20:23], v[144:147]\n" \
"buffer_load_dwordx4 v21, s[32:35], s47 offen offset:128 lds\n" \
"v_mfma_f32_16x16x32_bf16 v[152:155], a[96:99], a[24:27], v[152:155]\n" \
"s_waitcnt lgkmcnt(15)\n" \
"ds_read2st64_b32 v[208:209], v241 offset0:64 offset1:80\n" \
"v_mul_f32 v248, s20, v248\n" \
"v_mfma_f32_16x16x32_bf16 v[152:155], a[100:103], a[28:31], v[152:155]\n" \
"ds_read2st64_b32 v[210:211], v241 offset0:96 offset1:112\n" \
"v_mul_f32 v249, s20, v249\n" \
"v_mfma_f32_16x16x32_bf16 v[132:135], a[104:107], a[0:3], v[132:135]\n" \
"s_mov_b32 s47, 2359296\n" \
"s_add_u32 m0, s51, 40960\n" \
"ds_read2st64_b32 v[212:213], v242 offset0:64 offset1:80\n" \
"v_mfma_f32_16x16x32_bf16 v[132:135], a[108:111], a[4:7], v[132:135]\n" \
"buffer_load_dwordx4 v21, s[32:35], s47 offen offset:0 lds\n" \
"v_mfma_f32_16x16x32_bf16 v[140:143], a[104:107], a[8:11], v[140:143]\n" \
"ds_read2st64_b32 v[214:215], v242 offset0:96 offset1:112\n" \
"v_mul_f32 v250, s20, v250\n" \
"v_mfma_f32_16x16x32_bf16 v[140:143], a[108:111], a[12:15], v[140:143]\n" \
"buffer_store_dwordx4 v[34:37], v4, s[36:39], s97 offen offset:0 sc1\n" \
"ds_read2st64_b32 v[216:217], v241 offset0:72 offset1:88\n" \
"ds_read2st64_b32 v[218:219], v241 offset0:104 offset1:120\n" \
"ds_read2st64_b32 v[220:221], v242 offset0:72 offset1:88\n" \
"ds_read2st64_b32 v[222:223], v242 offset0:104 offset1:120\n" \
"v_mfma_f32_16x16x32_bf16 v[148:151], a[104:107], a[16:19], v[148:151]\n" \
"s_add_u32 m0, s51, 41856\n" \
"s_add_u32 s96, s96, s92\n" \
"v_mul_f32 v251, s20, v251\n" \
"v_mfma_f32_16x16x32_bf16 v[148:151], a[108:111], a[20:23], v[148:151]\n" \
"buffer_load_dwordx4 v21, s[32:35], s47 offen offset:128 lds\n" \
"v_mfma_f32_16x16x32_bf16 v[156:159], a[104:107], a[24:27], v[156:159]\n" \
"s_waitcnt vmcnt(14) lgkmcnt(0)\n" \
"v_cvt_pk_bf16_f32 v38, v248, v249\n" \
"v_cvt_pk_bf16_f32 v39, v250, v251\n" \
"v_mfma_f32_16x16x32_bf16 v[156:159], a[108:111], a[28:31], v[156:159]\n" \
"s_barrier\n" \
"v_accvgpr_read_b32 v252, a180\n" \
"v_accvgpr_read_b32 v253, a181\n" \
"v_mfma_f32_16x16x32_bf16 v[160:163], a[64:67], a[0:3], v[160:163]\n" \
"s_mov_b32 s47, 1572864\n" \
"s_add_u32 m0, s51, 16384\n" \
"ds_read_b64_tr_b16 a[96:97], v9 offset:512\n" \
"v_mfma_f32_16x16x32_bf16 v[160:163], a[68:71], a[4:7], v[160:163]\n" \
"buffer_load_dwordx4 v21, s[32:35], s47 offen offset:0 lds\n" \
"v_mfma_f32_16x16x32_bf16 v[168:171], a[64:67], a[8:11], v[168:171]\n" \
"ds_read_b64_tr_b16 a[98:99], v10 offset:512\n" \
"v_perm_b32 v224, v209, v208, s94\n" \
"v_mfma_f32_16x16x32_bf16 v[168:171], a[68:71], a[12:15], v[168:171]\n" \
"ds_read_b64_tr_b16 a[100:101], v9 offset:8704\n" \
"v_perm_b32 v225, v211, v210, s94\n" \
"v_mfma_f32_16x16x32_bf16 v[176:179], a[64:67], a[16:19], v[176:179]\n" \
"ds_read_b64_tr_b16 a[102:103], v10 offset:8704\n" \
"v_perm_b32 v226, v213, v212, s94\n" \
"v_mfma_f32_16x16x32_bf16 v[176:179], a[68:71], a[20:23], v[176:179]\n" \
"s_add_u32 m0, s51, 17280\n" \
"ds_read_b128 a[200:203], v5 offset:32768\n" \
"v_perm_b32 v227, v215, v214, s94\n" \
"v_mfma_f32_16x16x32_bf16 v[184:187], a[64:67], a[24:27], v[184:187]\n" \
"buffer_load_dwordx4 v21, s[32:35], s47 offen offset:128 lds\n" \
"v_mfma_f32_16x16x32_bf16 v[184:187], a[68:71], a[28:31], v[184:187]\n" \
"ds_read_b128 a[204:207], v6 offset:32768\n" \
"v_perm_b32 v228, v209, v208, s95\n" \
"v_mfma_f32_16x16x32_bf16 v[164:167], a[72:75], a[0:3], v[164:167]\n" \
"ds_read_b128 a[208:211], v5 offset:34816\n" \
"v_perm_b32 v229, v211, v210, s95\n" \
"v_mfma_f32_16x16x32_bf16 v[164:167], a[76:79], a[4:7], v[164:167]\n" \
"ds_read_b128 a[212:215], v6 offset:34816\n" \
"v_perm_b32 v230, v213, v212, s95\n" \
"v_mfma_f32_16x16x32_bf16 v[172:175], a[72:75], a[8:11], v[172:175]\n" \
"s_mov_b32 s47, 1835008\n" \
"s_add_u32 m0, s51, 24576\n" \
"ds_read_b128 a[216:219], v5 offset:36864\n" \
"v_mfma_f32_16x16x32_bf16 v[172:175], a[76:79], a[12:15], v[172:175]\n" \
"buffer_load_dwordx4 v21, s[32:35], s47 offen offset:0 lds\n" \
"v_mfma_f32_16x16x32_bf16 v[180:183], a[72:75], a[16:19], v[180:183]\n" \
"ds_read_b128 a[220:223], v6 offset:36864\n" \
"v_perm_b32 v231, v215, v214, s95\n" \
"v_mfma_f32_16x16x32_bf16 v[180:183], a[76:79], a[20:23], v[180:183]\n" \
"ds_read_b128 a[224:227], v5 offset:38912\n" \
"v_perm_b32 v232, v217, v216, s94\n" \
"v_mfma_f32_16x16x32_bf16 v[188:191], a[72:75], a[24:27], v[188:191]\n" \
"ds_read_b128 a[228:231], v6 offset:38912\n" \
"v_perm_b32 v233, v219, v218, s94\n" \
"v_mfma_f32_16x16x32_bf16 v[188:191], a[76:79], a[28:31], v[188:191]\n" \
"s_add_u32 m0, s51, 25472\n" \
"ds_read_b64_tr_b16 a[104:105], v11 offset:512\n" \
"v_perm_b32 v234, v221, v220, s94\n" \
"v_mfma_f32_16x16x32_bf16 v[64:67], a[80:83], a[0:3], v[64:67]\n" \
"buffer_load_dwordx4 v21, s[32:35], s47 offen offset:128 lds\n" \
"v_mfma_f32_16x16x32_bf16 v[64:67], a[84:87], a[4:7], v[64:67]\n" \
"ds_read_b64_tr_b16 a[106:107], v12 offset:512\n" \
"v_perm_b32 v235, v223, v222, s94\n" \
"v_mfma_f32_16x16x32_bf16 v[72:75], a[80:83], a[8:11], v[72:75]\n" \
"ds_read_b64_tr_b16 a[108:109], v11 offset:8704\n" \
"v_perm_b32 v236, v217, v216, s95\n" \
"v_mfma_f32_16x16x32_bf16 v[72:75], a[84:87], a[12:15], v[72:75]\n" \
"ds_read_b64_tr_b16 a[110:111], v12 offset:8704\n" \
"v_perm_b32 v237, v219, v218, s95\n" \
"v_mfma_f32_16x16x32_bf16 v[80:83], a[80:83], a[16:19], v[80:83]\n" \
"s_mov_b32 s46, 512\n" \
"s_add_u32 m0, s50, 0\n" \
"ds_read_b64_tr_b16 a[64:65], v9 offset:1024\n" \
"v_mfma_f32_16x16x32_bf16 v[80:83], a[84:87], a[20:23], v[80:83]\n" \
"buffer_load_dwordx4 v13, s[28:31], s46 offen lds\n" \
"v_mfma_f32_16x16x32_bf16 v[88:91], a[80:83], a[24:27], v[88:91]\n" \
"ds_read_b64_tr_b16 a[66:67], v10 offset:1024\n" \
"v_perm_b32 v238, v221, v220, s95\n" \
"v_mfma_f32_16x16x32_bf16 v[88:91], a[84:87], a[28:31], v[88:91]\n" \
"ds_read_b64_tr_b16 a[68:69], v9 offset:9216\n" \
"v_perm_b32 v239, v223, v222, s95\n" \
"v_mfma_f32_16x16x32_bf16 v[68:71], a[88:91], a[0:3], v[68:71]\n" \
"ds_read_b64_tr_b16 a[70:71], v10 offset:9216\n" \
"v_accvgpr_read_b32 v254, a182\n" \
"v_mfma_f32_16x16x32_bf16 v[68:71], a[92:95], a[4:7], v[68:71]\n" \
"s_add_u32 m0, s50, 4096\n" \
"ds_read_b64_tr_b16 a[72:73], v11 offset:1024\n" \
"v_accvgpr_read_b32 v255, a183\n" \
"v_mfma_f32_16x16x32_bf16 v[76:79], a[88:91], a[8:11], v[76:79]\n" \
"buffer_load_dwordx4 v14, s[28:31], s46 offen lds\n" \
"v_mfma_f32_16x16x32_bf16 v[76:79], a[92:95], a[12:15], v[76:79]\n" \
"ds_read_b64_tr_b16 a[74:75], v12 offset:1024\n" \
"v_mul_f32 v252, s20, v252\n" \
"v_mfma_f32_16x16x32_bf16 v[84:87], a[88:91], a[16:19], v[84:87]\n" \
"ds_read_b64_tr_b16 a[76:77], v11 offset:9216\n" \
"v_mul_f32 v253, s20, v253\n" \
"v_mfma_f32_16x16x32_bf16 v[84:87], a[92:95], a[20:23], v[84:87]\n" \
"ds_read_b64_tr_b16 a[78:79], v12 offset:9216\n" \
"v_mul_f32 v254, s20, v254\n" \
"v_mfma_f32_16x16x32_bf16 v[92:95], a[88:91], a[24:27], v[92:95]\n" \
"buffer_store_dwordx4 v[224:227], v240, s[36:39], s59 offen offset:0 sc1\n" \
"v_mfma_f32_16x16x32_bf16 v[92:95], a[92:95], a[28:31], v[92:95]\n" \
"s_waitcnt lgkmcnt(12)\n" \
"v_mul_f32 v255, s20, v255\n" \
"v_cvt_pk_bf16_f32 v40, v252, v253\n" \
"v_mfma_f32_16x16x32_bf16 v[96:99], a[96:99], a[200:203], v[96:99]\n" \
"s_add_u32 m0, s50, 8192\n" \
"ds_read_b64_tr_b16 a[80:81], v9 offset:1536\n" \
"v_cvt_pk_bf16_f32 v41, v254, v255\n" \
"v_mfma_f32_16x16x32_bf16 v[96:99], a[100:103], a[204:207], v[96:99]\n" \
"buffer_load_dwordx4 v15, s[28:31], s46 offen lds\n" \
"v_mfma_f32_16x16x32_bf16 v[104:107], a[96:99], a[208:211], v[104:107]\n" \
"ds_read_b64_tr_b16 a[82:83], v10 offset:1536\n" \
"v_accvgpr_read_b32 v248, a184\n" \
"v_mfma_f32_16x16x32_bf16 v[104:107], a[100:103], a[212:215], v[104:107]\n" \
"ds_read_b64_tr_b16 a[84:85], v9 offset:9728\n" \
"v_accvgpr_read_b32 v249, a185\n" \
"v_mfma_f32_16x16x32_bf16 v[112:115], a[96:99], a[216:219], v[112:115]\n" \
"s_add_u32 m0, s50, 12288\n" \
"ds_read_b64_tr_b16 a[86:87], v10 offset:9728\n" \
"v_accvgpr_read_b32 v250, a186\n" \
"v_mfma_f32_16x16x32_bf16 v[112:115], a[100:103], a[220:223], v[112:115]\n" \
"buffer_load_dwordx4 v16, s[28:31], s46 offen lds\n" \
"v_mfma_f32_16x16x32_bf16 v[120:123], a[96:99], a[224:227], v[120:123]\n" \
"s_waitcnt lgkmcnt(12)\n" \
"ds_read_b64_tr_b16 a[88:89], v11 offset:1536\n" \
"v_accvgpr_read_b32 v251, a187\n" \
"v_mfma_f32_16x16x32_bf16 v[120:123], a[100:103], a[228:231], v[120:123]\n" \
"ds_read_b64_tr_b16 a[90:91], v12 offset:1536\n" \
"v_mul_f32 v248, s20, v248\n" \
"v_mfma_f32_16x16x32_bf16 v[100:103], a[104:107], a[200:203], v[100:103]\n" \
"s_mov_b32 s46, 512\n" \
"s_add_u32 m0, s50, 16384\n" \
"ds_read_b64_tr_b16 a[92:93], v11 offset:9728\n" \
"v_mfma_f32_16x16x32_bf16 v[100:103], a[108:111], a[204:207], v[100:103]\n" \
"buffer_load_dwordx4 v17, s[28:31], s46 offen lds\n" \
"v_mfma_f32_16x16x32_bf16 v[108:111], a[104:107], a[208:211], v[108:111]\n" \
"ds_read_b64_tr_b16 a[94:95], v12 offset:9728\n" \
"v_mul_f32 v249, s20, v249\n" \
"v_mfma_f32_16x16x32_bf16 v[108:111], a[108:111], a[212:215], v[108:111]\n" \
"ds_read_b64_tr_b16 a[96:97], v9 offset:0\n" \
"v_mul_f32 v250, s20, v250\n" \
"v_mfma_f32_16x16x32_bf16 v[116:119], a[104:107], a[216:219], v[116:119]\n" \
"s_add_u32 m0, s50, 20480\n" \
"ds_read_b64_tr_b16 a[98:99], v10 offset:0\n" \
"v_mul_f32 v251, s20, v251\n" \
"v_mfma_f32_16x16x32_bf16 v[116:119], a[108:111], a[220:223], v[116:119]\n" \
"buffer_load_dwordx4 v18, s[28:31], s46 offen lds\n" \
"v_mfma_f32_16x16x32_bf16 v[124:127], a[104:107], a[224:227], v[124:127]\n" \
"s_waitcnt lgkmcnt(14)\n" \
"ds_read_b64_tr_b16 a[100:101], v9 offset:8192\n" \
"v_cvt_pk_bf16_f32 v42, v248, v249\n" \
"v_mfma_f32_16x16x32_bf16 v[124:127], a[108:111], a[228:231], v[124:127]\n" \
"ds_read_b64_tr_b16 a[102:103], v10 offset:8192\n" \
"v_cvt_pk_bf16_f32 v43, v250, v251\n" \
"v_mfma_f32_16x16x32_bf16 v[128:131], a[64:67], a[200:203], v[128:131]\n" \
"s_add_u32 m0, s50, 24576\n" \
"ds_read_b64_tr_b16 a[104:105], v11 offset:0\n" \
"v_accvgpr_read_b32 v252, a188\n" \
"v_mfma_f32_16x16x32_bf16 v[128:131], a[68:71], a[204:207], v[128:131]\n" \
"buffer_load_dwordx4 v19, s[28:31], s46 offen lds\n" \
"v_mfma_f32_16x16x32_bf16 v[136:139], a[64:67], a[208:211], v[136:139]\n" \
"ds_read_b64_tr_b16 a[106:107], v12 offset:0\n" \
"v_accvgpr_read_b32 v253, a189\n" \
"v_mfma_f32_16x16x32_bf16 v[136:139], a[68:71], a[212:215], v[136:139]\n" \
"ds_read_b64_tr_b16 a[108:109], v11 offset:8192\n" \
"v_accvgpr_read_b32 v254, a190\n" \
"v_mfma_f32_16x16x32_bf16 v[144:147], a[64:67], a[216:219], v[144:147]\n" \
"s_add_u32 m0, s50, 28672\n" \
"ds_read_b64_tr_b16 a[110:111], v12 offset:8192\n" \
"v_accvgpr_read_b32 v255, a191\n" \
"v_mfma_f32_16x16x32_bf16 v[144:147], a[68:71], a[220:223], v[144:147]\n" \
"buffer_load_dwordx4 v20, s[28:31], s46 offen lds\n" \
"v_mfma_f32_16x16x32_bf16 v[152:155], a[64:67], a[224:227], v[152:155]\n" \
"s_waitcnt lgkmcnt(15)\n" \
"v_mul_f32 v252, s20, v252\n" \
"v_mul_f32 v253, s20, v253\n" \
"v_mfma_f32_16x16x32_bf16 v[152:155], a[68:71], a[228:231], v[152:155]\n" \
"v_mul_f32 v254, s20, v254\n" \
"v_mul_f32 v255, s20, v255\n" \
"v_mfma_f32_16x16x32_bf16 v[132:135], a[72:75], a[200:203], v[132:135]\n" \
"s_mov_b32 s47, 2621440\n" \
"s_add_u32 m0, s51, 49152\n" \
"v_cvt_pk_bf16_f32 v44, v252, v253\n" \
"v_mfma_f32_16x16x32_bf16 v[132:135], a[76:79], a[204:207], v[132:135]\n" \
"buffer_load_dwordx4 v21, s[32:35], s47 offen offset:0 lds\n" \
"v_mfma_f32_16x16x32_bf16 v[140:143], a[72:75], a[208:211], v[140:143]\n" \
"v_cvt_pk_bf16_f32 v45, v254, v255\n" \
"v_mfma_f32_16x16x32_bf16 v[140:143], a[76:79], a[212:215], v[140:143]\n" \
"v_mfma_f32_16x16x32_bf16 v[148:151], a[72:75], a[216:219], v[148:151]\n" \
"s_add_u32 m0, s51, 50048\n" \
"v_mfma_f32_16x16x32_bf16 v[148:151], a[76:79], a[220:223], v[148:151]\n" \
"buffer_load_dwordx4 v21, s[32:35], s47 offen offset:128 lds\n" \
"v_mfma_f32_16x16x32_bf16 v[156:159], a[72:75], a[224:227], v[156:159]\n" \
"s_waitcnt vmcnt(11) lgkmcnt(0)\n" \
"v_mfma_f32_16x16x32_bf16 v[156:159], a[76:79], a[228:231], v[156:159]\n" \
"LH10_%=:\ns_cbranch_execz LH14_%=\n" \
"s_barrier\n" \
"v_mfma_f32_16x16x32_bf16 v[160:163], a[80:83], a[200:203], v[160:163]\n" \
"s_mov_b32 s47, 2883584\n" \
"s_add_u32 m0, s51, 57344\n" \
"ds_read_b64_tr_b16 a[64:65], v9 offset:16896\n" \
"v_mfma_f32_16x16x32_bf16 v[160:163], a[84:87], a[204:207], v[160:163]\n" \
"buffer_load_dwordx4 v21, s[32:35], s47 offen offset:0 lds\n" \
"v_mfma_f32_16x16x32_bf16 v[168:171], a[80:83], a[208:211], v[168:171]\n" \
"ds_read_b64_tr_b16 a[66:67], v10 offset:16896\n" \
"v_mfma_f32_16x16x32_bf16 v[168:171], a[84:87], a[212:215], v[168:171]\n" \
"ds_read_b64_tr_b16 a[68:69], v9 offset:25088\n" \
"v_mfma_f32_16x16x32_bf16 v[176:179], a[80:83], a[216:219], v[176:179]\n" \
"ds_read_b64_tr_b16 a[70:71], v10 offset:25088\n" \
"v_mfma_f32_16x16x32_bf16 v[176:179], a[84:87], a[220:223], v[176:179]\n" \
"s_add_u32 m0, s51, 58240\n" \
"ds_read_b128 a[0:3], v7 offset:0\n" \
"v_mfma_f32_16x16x32_bf16 v[184:187], a[80:83], a[224:227], v[184:187]\n" \
"buffer_load_dwordx4 v21, s[32:35], s47 offen offset:128 lds\n" \
"v_mfma_f32_16x16x32_bf16 v[184:187], a[84:87], a[228:231], v[184:187]\n" \
"ds_read_b128 a[4:7], v8 offset:0\n" \
"v_mfma_f32_16x16x32_bf16 v[164:167], a[88:91], a[200:203], v[164:167]\n" \
"ds_read_b128 a[8:11], v7 offset:2048\n" \
"v_mfma_f32_16x16x32_bf16 v[164:167], a[92:95], a[204:207], v[164:167]\n" \
"ds_read_b128 a[12:15], v8 offset:2048\n" \
"v_mfma_f32_16x16x32_bf16 v[172:175], a[88:91], a[208:211], v[172:175]\n" \
"s_mov_b32 s46, 640\n" \
"s_add_u32 m0, s50, 32768\n" \
"ds_read_b128 a[16:19], v7 offset:4096\n" \
"v_mfma_f32_16x16x32_bf16 v[172:175], a[92:95], a[212:215], v[172:175]\n" \
"buffer_load_dwordx4 v13, s[28:31], s46 offen lds\n" \
"v_mfma_f32_16x16x32_bf16 v[180:183], a[88:91], a[216:219], v[180:183]\n" \
"ds_read_b128 a[20:23], v8 offset:4096\n" \
"v_mfma_f32_16x16x32_bf16 v[180:183], a[92:95], a[220:223], v[180:183]\n" \
"ds_read_b128 a[24:27], v7 offset:6144\n" \
"v_mfma_f32_16x16x32_bf16 v[188:191], a[88:91], a[224:227], v[188:191]\n" \
"ds_read_b128 a[28:31], v8 offset:6144\n" \
"v_mfma_f32_16x16x32_bf16 v[188:191], a[92:95], a[228:231], v[188:191]\n" \
"s_add_u32 m0, s50, 36864\n" \
"ds_read_b64_tr_b16 a[72:73], v11 offset:16896\n" \
"v_mfma_f32_16x16x32_bf16 v[64:67], a[96:99], a[200:203], v[64:67]\n" \
"buffer_load_dwordx4 v14, s[28:31], s46 offen lds\n" \
"v_mfma_f32_16x16x32_bf16 v[64:67], a[100:103], a[204:207], v[64:67]\n" \
"ds_read_b64_tr_b16 a[74:75], v12 offset:16896\n" \
"v_mfma_f32_16x16x32_bf16 v[72:75], a[96:99], a[208:211], v[72:75]\n" \
"ds_read_b64_tr_b16 a[76:77], v11 offset:25088\n" \
"v_mfma_f32_16x16x32_bf16 v[72:75], a[100:103], a[212:215], v[72:75]\n" \
"ds_read_b64_tr_b16 a[78:79], v12 offset:25088\n" \
"v_mfma_f32_16x16x32_bf16 v[80:83], a[96:99], a[216:219], v[80:83]\n" \
"s_add_u32 m0, s50, 40960\n" \
"ds_read_b64_tr_b16 a[80:81], v9 offset:17408\n" \
"v_mfma_f32_16x16x32_bf16 v[80:83], a[100:103], a[220:223], v[80:83]\n" \
"buffer_load_dwordx4 v15, s[28:31], s46 offen lds\n" \
"v_mfma_f32_16x16x32_bf16 v[88:91], a[96:99], a[224:227], v[88:91]\n" \
"ds_read_b64_tr_b16 a[82:83], v10 offset:17408\n" \
"v_mfma_f32_16x16x32_bf16 v[88:91], a[100:103], a[228:231], v[88:91]\n" \
"ds_read_b64_tr_b16 a[84:85], v9 offset:25600\n" \
"v_mfma_f32_16x16x32_bf16 v[68:71], a[104:107], a[200:203], v[68:71]\n" \
"ds_read_b64_tr_b16 a[86:87], v10 offset:25600\n" \
"v_mfma_f32_16x16x32_bf16 v[68:71], a[108:111], a[204:207], v[68:71]\n" \
"s_add_u32 m0, s50, 45056\n" \
"ds_read_b64_tr_b16 a[88:89], v11 offset:17408\n" \
"v_mfma_f32_16x16x32_bf16 v[76:79], a[104:107], a[208:211], v[76:79]\n" \
"buffer_load_dwordx4 v16, s[28:31], s46 offen lds\n" \
"v_mfma_f32_16x16x32_bf16 v[76:79], a[108:111], a[212:215], v[76:79]\n" \
"ds_read_b64_tr_b16 a[90:91], v12 offset:17408\n" \
"v_mfma_f32_16x16x32_bf16 v[84:87], a[104:107], a[216:219], v[84:87]\n" \
"ds_read_b64_tr_b16 a[92:93], v11 offset:25600\n" \
"v_mfma_f32_16x16x32_bf16 v[84:87], a[108:111], a[220:223], v[84:87]\n" \
"ds_read_b64_tr_b16 a[94:95], v12 offset:25600\n" \
"v_mfma_f32_16x16x32_bf16 v[92:95], a[104:107], a[224:227], v[92:95]\n" \
"buffer_store_dwordx4 v[228:231], v240, s[36:39], s59 offen offset:128 sc1\n" \
"v_mfma_f32_16x16x32_bf16 v[92:95], a[108:111], a[228:231], v[92:95]\n" \
"s_waitcnt lgkmcnt(12)\n" \
"v_mfma_f32_16x16x32_bf16 v[96:99], a[64:67], a[0:3], v[96:99]\n" \
"s_mov_b32 s46, 640\n" \
"s_add_u32 m0, s50, 49152\n" \
"ds_read_b64_tr_b16 a[96:97], v9 offset:17920\n" \
"v_mfma_f32_16x16x32_bf16 v[96:99], a[68:71], a[4:7], v[96:99]\n" \
"buffer_load_dwordx4 v17, s[28:31], s46 offen lds\n" \
"v_mfma_f32_16x16x32_bf16 v[104:107], a[64:67], a[8:11], v[104:107]\n" \
"ds_read_b64_tr_b16 a[98:99], v10 offset:17920\n" \
"s_waitcnt vmcnt(63)\n" \
"v_mfma_f32_16x16x32_bf16 v[104:107], a[68:71], a[12:15], v[104:107]\n" \
"ds_read_b64_tr_b16 a[100:101], v9 offset:26112\n" \
"s_load_dwordx2 s[100:101], %0, 0x50\n" \
"v_mfma_f32_16x16x32_bf16 v[112:115], a[64:67], a[16:19], v[112:115]\n" \
"s_add_u32 m0, s50, 53248\n" \
"ds_read_b64_tr_b16 a[102:103], v10 offset:26112\n" \
"s_lshr_b32 s64, %1, 3\n" \
"v_mfma_f32_16x16x32_bf16 v[112:115], a[68:71], a[20:23], v[112:115]\n" \
"buffer_load_dwordx4 v18, s[28:31], s46 offen lds\n" \
"v_mfma_f32_16x16x32_bf16 v[120:123], a[64:67], a[24:27], v[120:123]\n" \
"s_waitcnt lgkmcnt(12)\n" \
"ds_read_b64_tr_b16 a[104:105], v11 offset:17920\n" \
"s_lshl_b32 s64, s64, 2\n" \
"v_mfma_f32_16x16x32_bf16 v[120:123], a[68:71], a[28:31], v[120:123]\n" \
"s_add_u32 s64, s64, %2\n" \
"v_mfma_f32_16x16x32_bf16 v[24:27], a[240:243], a[192:195], 0\n" \
"v_mfma_f32_16x16x32_bf16 v[100:103], a[72:75], a[0:3], v[100:103]\n" \
"s_add_u32 m0, s50, 57344\n" \
"ds_read_b64_tr_b16 a[106:107], v12 offset:17920\n" \
"s_lshl_b32 s64, s64, 11\n" \
"v_mfma_f32_16x16x32_bf16 v[100:103], a[76:79], a[4:7], v[100:103]\n" \
"buffer_load_dwordx4 v19, s[28:31], s46 offen lds\n" \
"v_mfma_f32_16x16x32_bf16 v[108:111], a[72:75], a[8:11], v[108:111]\n" \
"s_bfe_u32 s65, %1, 0x20001\n" \
"ds_read_b64_tr_b16 a[108:109], v11 offset:26112\n" \
"s_lshl_b32 s65, s65, 21\n" \
"v_mfma_f32_16x16x32_bf16 v[108:111], a[76:79], a[12:15], v[108:111]\n" \
"s_add_u32 s64, s64, s65\n" \
"ds_read_b64_tr_b16 a[110:111], v12 offset:26112\n" \
"v_mfma_f32_16x16x32_bf16 v[116:119], a[72:75], a[16:19], v[116:119]\n" \
"s_add_u32 m0, s50, 61440\n" \
"ds_read_b64_tr_b16 a[64:65], v9 offset:16384\n" \
"v_mfma_f32_16x16x32_bf16 v[116:119], a[76:79], a[20:23], v[116:119]\n" \
"buffer_load_dwordx4 v20, s[28:31], s46 offen lds\n" \
"v_mfma_f32_16x16x32_bf16 v[124:127], a[72:75], a[24:27], v[124:127]\n" \
"s_waitcnt lgkmcnt(13)\n" \
"ds_read_b64_tr_b16 a[66:67], v10 offset:16384\n" \
"v_mfma_f32_16x16x32_bf16 v[124:127], a[76:79], a[28:31], v[124:127]\n" \
"ds_read_b64_tr_b16 a[68:69], v9 offset:24576\n" \
"v_mfma_f32_16x16x32_bf16 v[128:131], a[80:83], a[0:3], v[128:131]\n" \
"s_mov_b32 s47, 3145728\n" \
"s_add_u32 m0, s51, 0\n" \
"ds_read_b64_tr_b16 a[70:71], v10 offset:24576\n" \
"v_mfma_f32_16x16x32_bf16 v[128:131], a[84:87], a[4:7], v[128:131]\n" \
"buffer_load_dwordx4 v21, s[32:35], s47 offen offset:0 lds\n" \
"v_mfma_f32_16x16x32_bf16 v[136:139], a[80:83], a[8:11], v[136:139]\n" \
"ds_read_b64_tr_b16 a[72:73], v11 offset:16384\n" \
"v_mfma_f32_16x16x32_bf16 v[136:139], a[84:87], a[12:15], v[136:139]\n" \
"s_mov_b32 s66, 0x100001\n" \
"v_mfma_f32_16x16x32_bf16 v[24:27], a[244:247], a[196:199], v[24:27]\n" \
"v_mfma_f32_16x16x32_bf16 v[144:147], a[80:83], a[16:19], v[144:147]\n" \
"s_add_u32 m0, s51, 896\n" \
"ds_read_b64_tr_b16 a[74:75], v12 offset:16384\n" \
"s_mov_b32 s67, 0x10000100\n" \
"v_mfma_f32_16x16x32_bf16 v[144:147], a[84:87], a[20:23], v[144:147]\n" \
"buffer_load_dwordx4 v21, s[32:35], s47 offen offset:128 lds\n" \
"v_mfma_f32_16x16x32_bf16 v[152:155], a[80:83], a[24:27], v[152:155]\n" \
"s_waitcnt lgkmcnt(14)\n" \
"ds_read_b64_tr_b16 a[76:77], v11 offset:24576\n" \
"v_cndmask_b32 v23, 0, v24, s[66:67]\n" \
"v_mfma_f32_16x16x32_bf16 v[152:155], a[84:87], a[28:31], v[152:155]\n" \
"s_mov_b32 s66, 0x200002\n" \
"ds_read_b64_tr_b16 a[78:79], v12 offset:24576\n" \
"s_mov_b32 s67, 0x20000200\n" \
"v_mfma_f32_16x16x32_bf16 v[132:135], a[88:91], a[0:3], v[132:135]\n" \
"s_mov_b32 s47, 3407872\n" \
"s_add_u32 m0, s51, 8192\n" \
"v_cndmask_b32 v23, v23, v25, s[66:67]\n" \
"v_mfma_f32_16x16x32_bf16 v[132:135], a[92:95], a[4:7], v[132:135]\n" \
"buffer_load_dwordx4 v21, s[32:35], s47 offen offset:0 lds\n" \
"v_mfma_f32_16x16x32_bf16 v[140:143], a[88:91], a[8:11], v[140:143]\n" \
"s_mov_b32 s66, 0x400004\n" \
"s_mov_b32 s67, 0x40000400\n" \
"v_cndmask_b32 v23, v23, v26, s[66:67]\n" \
"v_mfma_f32_16x16x32_bf16 v[140:143], a[92:95], a[12:15], v[140:143]\n" \
"s_mov_b32 s66, 0x800008\n" \
"s_mov_b32 s67, 0x80000800\n" \
"v_cndmask_b32 v23, v23, v27, s[66:67]\n" \
"v_mfma_f32_16x16x32_bf16 v[148:151], a[88:91], a[16:19], v[148:151]\n" \
"s_add_u32 m0, s51, 9088\n" \
"v_mfma_f32_16x16x32_bf16 v[24:27], a[248:251], a[192:195], 0\n" \
"v_mfma_f32_16x16x32_bf16 v[148:151], a[92:95], a[20:23], v[148:151]\n" \
"buffer_load_dwordx4 v21, s[32:35], s47 offen offset:128 lds\n" \
"v_mfma_f32_16x16x32_bf16 v[156:159], a[88:91], a[24:27], v[156:159]\n" \
"s_waitcnt vmcnt(17) lgkmcnt(0)\n" \
"v_mfma_f32_16x16x32_bf16 v[24:27], a[252:255], a[196:199], v[24:27]\n" \
"v_mfma_f32_16x16x32_bf16 v[156:159], a[92:95], a[28:31], v[156:159]\n" \
"s_barrier\n" \
"s_mov_b32 s66, 0x100001\n" \
"s_mov_b32 s67, 0x10000100\n" \
"v_mfma_f32_16x16x32_bf16 v[160:163], a[96:99], a[0:3], v[160:163]\n" \
"ds_read_b64_tr_b16 a[80:81], v9 offset:33280\n" \
"v_mfma_f32_16x16x32_bf16 v[160:163], a[100:103], a[4:7], v[160:163]\n" \
"ds_read_b64_tr_b16 a[82:83], v10 offset:33280\n" \
"v_cndmask_b32 v54, 0, v24, s[66:67]\n" \
"v_mfma_f32_16x16x32_bf16 v[168:171], a[96:99], a[8:11], v[168:171]\n" \
"s_mov_b32 s46, 768\n" \
"s_add_u32 m0, s50, 65536\n" \
"ds_read_b64_tr_b16 a[84:85], v9 offset:41472\n" \
"v_mfma_f32_16x16x32_bf16 v[168:171], a[100:103], a[12:15], v[168:171]\n" \
"buffer_load_dwordx4 v13, s[28:31], s46 offen lds\n" \
"v_mfma_f32_16x16x32_bf16 v[176:179], a[96:99], a[16:19], v[176:179]\n" \
"s_mov_b32 s66, 0x200002\n" \
"ds_read_b64_tr_b16 a[86:87], v10 offset:41472\n" \
"s_mov_b32 s67, 0x20000200\n" \
"v_mfma_f32_16x16x32_bf16 v[176:179], a[100:103], a[20:23], v[176:179]\n" \
"ds_read_b128 a[200:203], v5 offset:0\n" \
"v_cndmask_b32 v54, v54, v25, s[66:67]\n" \
"v_mfma_f32_16x16x32_bf16 v[184:187], a[96:99], a[24:27], v[184:187]\n" \
"s_mov_b32 s66, 0x400004\n" \
"ds_read_b128 a[204:207], v6 offset:0\n" \
"s_mov_b32 s67, 0x40000400\n" \
"v_mfma_f32_16x16x32_bf16 v[184:187], a[100:103], a[28:31], v[184:187]\n" \
"ds_read_b128 a[208:211], v5 offset:2048\n" \
"v_cndmask_b32 v54, v54, v26, s[66:67]\n" \
"v_mfma_f32_16x16x32_bf16 v[164:167], a[104:107], a[0:3], v[164:167]\n" \
"s_mov_b32 s66, 0x800008\n" \
"ds_read_b128 a[212:215], v6 offset:2048\n" \
"s_mov_b32 s67, 0x80000800\n" \
"v_mfma_f32_16x16x32_bf16 v[164:167], a[108:111], a[4:7], v[164:167]\n" \
"ds_read_b128 a[216:219], v5 offset:4096\n" \
"v_cndmask_b32 v54, v54, v27, s[66:67]\n" \
"v_mfma_f32_16x16x32_bf16 v[172:175], a[104:107], a[8:11], v[172:175]\n" \
"s_add_u32 m0, s50, 69632\n" \
"ds_read_b128 a[220:223], v6 offset:4096\n" \
"v_mfma_f32_16x16x32_bf16 v[172:175], a[108:111], a[12:15], v[172:175]\n" \
"buffer_load_dwordx4 v14, s[28:31], s46 offen lds\n" \
"v_mfma_f32_16x16x32_bf16 v[180:183], a[104:107], a[16:19], v[180:183]\n" \
"ds_read_b128 a[224:227], v5 offset:6144\n" \
"v_permlane32_swap_b32 v23, v54\n" \
"v_mfma_f32_16x16x32_bf16 v[180:183], a[108:111], a[20:23], v[180:183]\n" \
"ds_read_b128 a[228:231], v6 offset:6144\n" \
"v_add_f32 v23, v23, v54\n" \
"v_mfma_f32_16x16x32_bf16 v[188:191], a[104:107], a[24:27], v[188:191]\n" \
"ds_read_b64_tr_b16 a[88:89], v11 offset:33280\n" \
"v_mov_b32 v54, v23\n" \
"v_mfma_f32_16x16x32_bf16 v[188:191], a[108:111], a[28:31], v[188:191]\n" \
"ds_read_b64_tr_b16 a[90:91], v12 offset:33280\n" \
"v_mfma_f32_16x16x32_bf16 v[64:67], a[64:67], a[0:3], v[64:67]\n" \
"ds_read_b64_tr_b16 a[92:93], v11 offset:41472\n" \
"v_permlane16_swap_b32 v23, v54\n" \
"v_mfma_f32_16x16x32_bf16 v[64:67], a[68:71], a[4:7], v[64:67]\n" \
"ds_read_b64_tr_b16 a[94:95], v12 offset:41472\n" \
"v_add_f32 v23, v23, v54\n" \
"v_mfma_f32_16x16x32_bf16 v[72:75], a[64:67], a[8:11], v[72:75]\n" \
"s_add_u32 m0, s50, 73728\n" \
"ds_read_b64_tr_b16 a[96:97], v9 offset:33792\n" \
"v_mfma_f32_16x16x32_bf16 v[72:75], a[68:71], a[12:15], v[72:75]\n" \
"buffer_load_dwordx4 v15, s[28:31], s46 offen lds\n" \
"v_mfma_f32_16x16x32_bf16 v[80:83], a[64:67], a[16:19], v[80:83]\n" \
"ds_read_b64_tr_b16 a[98:99], v10 offset:33792\n" \
"v_add_f32 v23, v23, v23 row_ror:8 row_mask:0xf bank_mask:0xf\n" \
"v_mfma_f32_16x16x32_bf16 v[80:83], a[68:71], a[20:23], v[80:83]\n" \
"ds_read_b64_tr_b16 a[100:101], v9 offset:41984\n" \
"v_mfma_f32_16x16x32_bf16 v[88:91], a[64:67], a[24:27], v[88:91]\n" \
"ds_read_b64_tr_b16 a[102:103], v10 offset:41984\n" \
"v_add_f32 v23, v23, v23 row_ror:4 row_mask:0xf bank_mask:0xf\n" \
"v_mfma_f32_16x16x32_bf16 v[88:91], a[68:71], a[28:31], v[88:91]\n" \
"ds_read_b64_tr_b16 a[104:105], v11 offset:33792\n" \
"v_mfma_f32_16x16x32_bf16 v[68:71], a[72:75], a[0:3], v[68:71]\n" \
"ds_read_b64_tr_b16 a[106:107], v12 offset:33792\n" \
"v_add_f32 v23, v23, v23 row_ror:2 row_mask:0xf bank_mask:0xf\n" \
"v_mfma_f32_16x16x32_bf16 v[68:71], a[76:79], a[4:7], v[68:71]\n" \
"ds_read_b64_tr_b16 a[108:109], v11 offset:41984\n" \
"v_mfma_f32_16x16x32_bf16 v[76:79], a[72:75], a[8:11], v[76:79]\n" \
"s_add_u32 m0, s50, 77824\n" \
"ds_read_b64_tr_b16 a[110:111], v12 offset:41984\n" \
"v_add_f32 v23, v23, v23 row_ror:1 row_mask:0xf bank_mask:0xf\n" \
"v_mfma_f32_16x16x32_bf16 v[76:79], a[76:79], a[12:15], v[76:79]\n" \
"buffer_load_dwordx4 v16, s[28:31], s46 offen lds\n" \
"v_mfma_f32_16x16x32_bf16 v[84:87], a[72:75], a[16:19], v[84:87]\n" \
"v_cvt_pk_bf16_f32 v23, v23, v23\n" \
"v_and_b32 v23, 0xffff0000, v23\n" \
"v_mfma_f32_16x16x32_bf16 v[84:87], a[76:79], a[20:23], v[84:87]\n" \
"buffer_store_dwordx4 v[232:235], v240, s[36:39], s60 offen offset:0 sc1\n" \
"v_mfma_f32_16x16x32_bf16 v[92:95], a[72:75], a[24:27], v[92:95]\n" \
"v_mul_f32 v23, 0xbfb8aa3b, v23\n" \
"v_mfma_f32_16x16x32_bf16 v[92:95], a[76:79], a[28:31], v[92:95]\n" \
"s_waitcnt lgkmcnt(12)\n" \
"v_exp_f32 v23, v23\n" \
"v_mfma_f32_16x16x32_bf16 v[96:99], a[80:83], a[200:203], v[96:99]\n" \
"s_mov_b32 s46, 768\n" \
"s_add_u32 m0, s50, 81920\n" \
"ds_read_b64_tr_b16 a[64:65], v9 offset:34304\n" \
"v_mfma_f32_16x16x32_bf16 v[96:99], a[84:87], a[204:207], v[96:99]\n" \
"buffer_load_dwordx4 v17, s[28:31], s46 offen lds\n" \
"v_mfma_f32_16x16x32_bf16 v[104:107], a[80:83], a[208:211], v[104:107]\n" \
"ds_read_b64_tr_b16 a[66:67], v10 offset:34304\n" \
"v_add_f32 v23, 1.0, v23\n" \
"v_mfma_f32_16x16x32_bf16 v[104:107], a[84:87], a[212:215], v[104:107]\n" \
"s_add_u32 s100, s100, s64\ns_addc_u32 s101, s101, 0\n" \
"ds_read_b64_tr_b16 a[68:69], v9 offset:42496\n" \
"v_mfma_f32_16x16x32_bf16 v[112:115], a[80:83], a[216:219], v[112:115]\n" \
"s_add_u32 m0, s50, 86016\n" \
"ds_read_b64_tr_b16 a[70:71], v10 offset:42496\n" \
"v_mfma_f32_16x16x32_bf16 v[112:115], a[84:87], a[220:223], v[112:115]\n" \
"buffer_load_dwordx4 v18, s[28:31], s46 offen lds\n" \
"v_mfma_f32_16x16x32_bf16 v[120:123], a[80:83], a[224:227], v[120:123]\n" \
"s_waitcnt lgkmcnt(12)\n" \
"ds_read_b64_tr_b16 a[72:73], v11 offset:34304\n" \
"v_mfma_f32_16x16x32_bf16 v[120:123], a[84:87], a[228:231], v[120:123]\n" \
"v_rcp_f32 v23, v23\n" \
"v_mfma_f32_16x16x32_bf16 v[100:103], a[88:91], a[200:203], v[100:103]\n" \
"s_add_u32 m0, s50, 90112\n" \
"ds_read_b64_tr_b16 a[74:75], v12 offset:34304\n" \
"v_mfma_f32_16x16x32_bf16 v[100:103], a[92:95], a[204:207], v[100:103]\n" \
"buffer_load_dwordx4 v19, s[28:31], s46 offen lds\n" \
"v_mfma_f32_16x16x32_bf16 v[108:111], a[88:91], a[208:211], v[108:111]\n" \
"ds_read_b64_tr_b16 a[76:77], v11 offset:42496\n" \
"v_cvt_pk_bf16_f32 v23, v23, v23\n" \
"v_mfma_f32_16x16x32_bf16 v[108:111], a[92:95], a[212:215], v[108:111]\n" \
"ds_read_b64_tr_b16 a[78:79], v12 offset:42496\n" \
"v_and_b32 v23, 0xffff0000, v23\n" \
"v_mfma_f32_16x16x32_bf16 v[116:119], a[88:91], a[216:219], v[116:119]\n" \
"s_add_u32 m0, s50, 94208\n" \
"ds_read_b64_tr_b16 a[80:81], v9 offset:32768\n" \
"v_mfma_f32_16x16x32_bf16 v[116:119], a[92:95], a[220:223], v[116:119]\n" \
"buffer_load_dwordx4 v20, s[28:31], s46 offen lds\n" \
"v_mfma_f32_16x16x32_bf16 v[124:127], a[88:91], a[224:227], v[124:127]\n" \
"s_waitcnt lgkmcnt(13)\n" \
"ds_read_b64_tr_b16 a[82:83], v10 offset:32768\n" \
"v_mfma_f32_16x16x32_bf16 v[124:127], a[92:95], a[228:231], v[124:127]\n" \
"ds_read_b64_tr_b16 a[84:85], v9 offset:40960\n" \
"v_mfma_f32_16x16x32_bf16 v[128:131], a[96:99], a[200:203], v[128:131]\n" \
"s_mov_b32 s47, 3670016\n" \
"s_add_u32 m0, s51, 16384\n" \
"ds_read_b64_tr_b16 a[86:87], v10 offset:40960\n" \
"v_mfma_f32_16x16x32_bf16 v[128:131], a[100:103], a[204:207], v[128:131]\n" \
"buffer_load_dwordx4 v21, s[32:35], s47 offen offset:0 lds\n" \
"v_mfma_f32_16x16x32_bf16 v[136:139], a[96:99], a[208:211], v[136:139]\n" \
"ds_read_b64_tr_b16 a[88:89], v11 offset:32768\n" \
"v_mfma_f32_16x16x32_bf16 v[136:139], a[100:103], a[212:215], v[136:139]\n" \
"buffer_store_dword v23, v61, s[36:39], 0 offen sc1\n" \
"v_mfma_f32_16x16x32_bf16 v[144:147], a[96:99], a[216:219], v[144:147]\n" \
"s_add_u32 m0, s51, 17280\n" \
"ds_read_b64_tr_b16 a[90:91], v12 offset:32768\n" \
"v_mfma_f32_16x16x32_bf16 v[144:147], a[100:103], a[220:223], v[144:147]\n" \
"buffer_load_dwordx4 v21, s[32:35], s47 offen offset:128 lds\n" \
"v_mfma_f32_16x16x32_bf16 v[152:155], a[96:99], a[224:227], v[152:155]\n" \
"s_waitcnt lgkmcnt(14)\n" \
"ds_read_b64_tr_b16 a[92:93], v11 offset:40960\n" \
"v_mfma_f32_16x16x32_bf16 v[152:155], a[100:103], a[228:231], v[152:155]\n" \
"ds_read_b64_tr_b16 a[94:95], v12 offset:40960\n" \
"v_mfma_f32_16x16x32_bf16 v[132:135], a[104:107], a[200:203], v[132:135]\n" \
"s_mov_b32 s47, 3932160\n" \
"s_add_u32 m0, s51, 24576\n" \
"v_mfma_f32_16x16x32_bf16 v[132:135], a[108:111], a[204:207], v[132:135]\n" \
"buffer_load_dwordx4 v21, s[32:35], s47 offen offset:0 lds\n" \
"v_mfma_f32_16x16x32_bf16 v[140:143], a[104:107], a[208:211], v[140:143]\n" \
"v_mfma_f32_16x16x32_bf16 v[140:143], a[108:111], a[212:215], v[140:143]\n" \
"buffer_store_dwordx4 v[236:239], v240, s[36:39], s60 offen offset:128 sc1\n" \
"v_mfma_f32_16x16x32_bf16 v[148:151], a[104:107], a[216:219], v[148:151]\n" \
"s_add_u32 m0, s51, 25472\n" \
"s_mov_b64 s[40:41], s[4:5]\n" \
"s_and_b32 s41, s41, 0xffff\n" \
"v_mfma_f32_16x16x32_bf16 v[148:151], a[108:111], a[220:223], v[148:151]\n" \
"buffer_load_dwordx4 v21, s[32:35], s47 offen offset:128 lds\n" \
"v_mfma_f32_16x16x32_bf16 v[156:159], a[104:107], a[224:227], v[156:159]\n" \
"s_waitcnt vmcnt(19) lgkmcnt(0)\n" \
"s_lshl_b32 s62, s6, 20\n" \
"s_add_u32 s16, s62, 33554432\n" \
"v_mfma_f32_16x16x32_bf16 v[156:159], a[108:111], a[228:231], v[156:159]\n" \
"s_barrier\n" \
"s_lshl_b32 s63, s7, 16\n" \
"s_sub_u32 s63, 1048576, s63\n" \
"v_mfma_f32_16x16x32_bf16 v[160:163], a[64:67], a[200:203], v[160:163]\n" \
"s_add_u32 s42, s16, s63\n" \
"ds_read_b64_tr_b16 a[96:97], v9 offset:49664\n" \
"v_mfma_f32_16x16x32_bf16 v[160:163], a[68:71], a[204:207], v[160:163]\n" \
"ds_read_b64_tr_b16 a[98:99], v10 offset:49664\n" \
"v_mfma_f32_16x16x32_bf16 v[168:171], a[64:67], a[208:211], v[168:171]\n" \
"s_mov_b32 s46, 896\n" \
"s_add_u32 m0, s50, 0\n" \
"ds_read_b64_tr_b16 a[100:101], v9 offset:57856\n" \
"v_mfma_f32_16x16x32_bf16 v[168:171], a[68:71], a[212:215], v[168:171]\n" \
"buffer_load_dwordx4 v13, s[28:31], s46 offen lds\n" \
"v_mfma_f32_16x16x32_bf16 v[176:179], a[64:67], a[216:219], v[176:179]\n" \
"ds_read_b64_tr_b16 a[102:103], v10 offset:57856\n" \
"v_mfma_f32_16x16x32_bf16 v[176:179], a[68:71], a[220:223], v[176:179]\n" \
"ds_read_b128 a[0:3], v5 offset:32768\n" \
"v_mfma_f32_16x16x32_bf16 v[184:187], a[64:67], a[224:227], v[184:187]\n" \
"ds_read_b128 a[4:7], v6 offset:32768\n" \
"v_mfma_f32_16x16x32_bf16 v[184:187], a[68:71], a[228:231], v[184:187]\n" \
"ds_read_b128 a[8:11], v5 offset:34816\n" \
"v_mfma_f32_16x16x32_bf16 v[164:167], a[72:75], a[200:203], v[164:167]\n" \
"ds_read_b128 a[12:15], v6 offset:34816\n" \
"v_mfma_f32_16x16x32_bf16 v[164:167], a[76:79], a[204:207], v[164:167]\n" \
"ds_read_b128 a[16:19], v5 offset:36864\n" \
"v_mfma_f32_16x16x32_bf16 v[172:175], a[72:75], a[208:211], v[172:175]\n" \
"s_add_u32 m0, s50, 4096\n" \
"ds_read_b128 a[20:23], v6 offset:36864\n" \
"v_mfma_f32_16x16x32_bf16 v[172:175], a[76:79], a[212:215], v[172:175]\n" \
"buffer_load_dwordx4 v14, s[28:31], s46 offen lds\n" \
"v_mfma_f32_16x16x32_bf16 v[180:183], a[72:75], a[216:219], v[180:183]\n" \
"ds_read_b128 a[24:27], v5 offset:38912\n" \
"v_mfma_f32_16x16x32_bf16 v[180:183], a[76:79], a[220:223], v[180:183]\n" \
"ds_read_b128 a[28:31], v6 offset:38912\n" \
"v_mfma_f32_16x16x32_bf16 v[188:191], a[72:75], a[224:227], v[188:191]\n" \
"ds_read_b64_tr_b16 a[104:105], v11 offset:49664\n" \
"v_mfma_f32_16x16x32_bf16 v[188:191], a[76:79], a[228:231], v[188:191]\n" \
"ds_read_b64_tr_b16 a[106:107], v12 offset:49664\n" \
"v_mfma_f32_16x16x32_bf16 v[64:67], a[80:83], a[200:203], v[64:67]\n" \
"ds_read_b64_tr_b16 a[108:109], v11 offset:57856\n" \
"v_mfma_f32_16x16x32_bf16 v[64:67], a[84:87], a[204:207], v[64:67]\n" \
"ds_read_b64_tr_b16 a[110:111], v12 offset:57856\n" \
"v_mfma_f32_16x16x32_bf16 v[72:75], a[80:83], a[208:211], v[72:75]\n" \
"s_add_u32 m0, s50, 8192\n" \
"ds_read_b64_tr_b16 a[64:65], v9 offset:50176\n" \
"v_mfma_f32_16x16x32_bf16 v[72:75], a[84:87], a[212:215], v[72:75]\n" \
"buffer_load_dwordx4 v15, s[28:31], s46 offen lds\n" \
"v_mfma_f32_16x16x32_bf16 v[80:83], a[80:83], a[216:219], v[80:83]\n" \
"ds_read_b64_tr_b16 a[66:67], v10 offset:50176\n" \
"v_mfma_f32_16x16x32_bf16 v[80:83], a[84:87], a[220:223], v[80:83]\n" \
"ds_read_b64_tr_b16 a[68:69], v9 offset:58368\n" \
"v_mfma_f32_16x16x32_bf16 v[88:91], a[80:83], a[224:227], v[88:91]\n" \
"ds_read_b64_tr_b16 a[70:71], v10 offset:58368\n" \
"v_mfma_f32_16x16x32_bf16 v[88:91], a[84:87], a[228:231], v[88:91]\n" \
"ds_read_b64_tr_b16 a[72:73], v11 offset:50176\n" \
"v_mfma_f32_16x16x32_bf16 v[68:71], a[88:91], a[200:203], v[68:71]\n" \
"ds_read_b64_tr_b16 a[74:75], v12 offset:50176\n" \
"v_mfma_f32_16x16x32_bf16 v[68:71], a[92:95], a[204:207], v[68:71]\n" \
"ds_read_b64_tr_b16 a[76:77], v11 offset:58368\n" \
"v_mfma_f32_16x16x32_bf16 v[76:79], a[88:91], a[208:211], v[76:79]\n" \
"s_add_u32 m0, s50, 12288\n" \
"ds_read_b64_tr_b16 a[78:79], v12 offset:58368\n" \
"v_mfma_f32_16x16x32_bf16 v[76:79], a[92:95], a[212:215], v[76:79]\n" \
"buffer_load_dwordx4 v16, s[28:31], s46 offen lds\n" \
"v_mfma_f32_16x16x32_bf16 v[84:87], a[88:91], a[216:219], v[84:87]\n" \
"s_cmp_lt_u32 %2, 2\ns_cselect_b32 s62, 1064960, 0\ns_cselect_b32 s9, -1, 1\n" \
"v_mfma_f32_16x16x32_bf16 v[84:87], a[92:95], a[220:223], v[84:87]\n" \
"buffer_store_dwordx4 v[38:41], v4, s[36:39], s96 offen offset:0 sc1\n" \
"v_mfma_f32_16x16x32_bf16 v[92:95], a[88:91], a[224:227], v[92:95]\n" \
"s_add_u32 s97, s96, s98\n" \
"s_add_u32 s16, s16, s62\n" \
"s_add_u32 s17, s16, 8192\n" \
"v_mfma_f32_16x16x32_bf16 v[92:95], a[92:95], a[228:231], v[92:95]\n" \
"s_waitcnt lgkmcnt(12)\n" \
"s_lshl_b32 s9, s9, 14\n" \
"s_mov_b32 s19, s9\n" \
"v_mfma_f32_16x16x32_bf16 v[96:99], a[96:99], a[0:3], v[96:99]\n" \
"s_mov_b32 s46, 896\n" \
"s_add_u32 m0, s50, 16384\n" \
"ds_read_b64_tr_b16 a[80:81], v9 offset:50688\n" \
"v_mfma_f32_16x16x32_bf16 v[96:99], a[100:103], a[4:7], v[96:99]\n" \
"buffer_load_dwordx4 v17, s[28:31], s46 offen lds\n" \
"v_mfma_f32_16x16x32_bf16 v[104:107], a[96:99], a[8:11], v[104:107]\n" \
"s_and_b32 s63, %2, 1\n" \
"ds_read_b64_tr_b16 a[82:83], v10 offset:50688\n" \
"s_lshl_b32 s63, s63, 12\n" \
"v_mfma_f32_16x16x32_bf16 v[104:107], a[100:103], a[12:15], v[104:107]\n" \
"s_lshr_b32 s62, s6, 1\n" \
"ds_read_b64_tr_b16 a[84:85], v9 offset:58880\n" \
"v_add_u32 v60, s63, v4\n" \
"v_mfma_f32_16x16x32_bf16 v[112:115], a[96:99], a[16:19], v[112:115]\n" \
"s_add_u32 m0, s50, 20480\n" \
"ds_read_b64_tr_b16 a[86:87], v10 offset:58880\n" \
"s_lshl_b32 s62, s62, 7\n" \
"v_mfma_f32_16x16x32_bf16 v[112:115], a[100:103], a[20:23], v[112:115]\n" \
"buffer_load_dwordx4 v18, s[28:31], s46 offen lds\n" \
"v_mfma_f32_16x16x32_bf16 v[120:123], a[96:99], a[24:27], v[120:123]\n" \
"s_waitcnt lgkmcnt(12)\n" \
"ds_read_b64_tr_b16 a[88:89], v11 offset:50688\n" \
"s_add_u32 s62, s62, 94375936\n" \
"v_mfma_f32_16x16x32_bf16 v[120:123], a[100:103], a[28:31], v[120:123]\n" \
"v_bfe_u32 v57, v4, 4, 4\n" \
"v_lshl_add_u32 v57, v57, 3, s62\n" \
"v_mfma_f32_16x16x32_bf16 v[100:103], a[104:107], a[0:3], v[100:103]\n" \
"s_add_u32 m0, s50, 24576\n" \
"ds_read_b64_tr_b16 a[90:91], v12 offset:50688\n" \
"v_mfma_f32_16x16x32_bf16 v[100:103], a[108:111], a[4:7], v[100:103]\n" \
"buffer_load_dwordx4 v19, s[28:31], s46 offen lds\n" \
"v_mfma_f32_16x16x32_bf16 v[108:111], a[104:107], a[8:11], v[108:111]\n" \
"ds_read_b64_tr_b16 a[92:93], v11 offset:58880\n" \
"v_mfma_f32_16x16x32_bf16 v[108:111], a[108:111], a[12:15], v[108:111]\n" \
"ds_read_b64_tr_b16 a[94:95], v12 offset:58880\n" \
"v_mfma_f32_16x16x32_bf16 v[116:119], a[104:107], a[16:19], v[116:119]\n" \
"s_add_u32 m0, s50, 28672\n" \
"ds_read_b64_tr_b16 a[96:97], v9 offset:49152\n" \
"v_mfma_f32_16x16x32_bf16 v[116:119], a[108:111], a[20:23], v[116:119]\n" \
"buffer_load_dwordx4 v20, s[28:31], s46 offen lds\n" \
"v_mfma_f32_16x16x32_bf16 v[124:127], a[104:107], a[24:27], v[124:127]\n" \
"s_waitcnt lgkmcnt(13)\n" \
"ds_read_b64_tr_b16 a[98:99], v10 offset:49152\n" \
"v_mfma_f32_16x16x32_bf16 v[124:127], a[108:111], a[28:31], v[124:127]\n" \
"ds_read_b64_tr_b16 a[100:101], v9 offset:57344\n" \
"v_mfma_f32_16x16x32_bf16 v[128:131], a[64:67], a[0:3], v[128:131]\n" \
"s_mov_b32 s47, 4194304\n" \
"s_add_u32 m0, s51, 32768\n" \
"ds_read_b64_tr_b16 a[102:103], v10 offset:57344\n" \
"v_mfma_f32_16x16x32_bf16 v[128:131], a[68:71], a[4:7], v[128:131]\n" \
"buffer_load_dwordx4 v21, s[32:35], s47 offen offset:0 lds\n" \
"v_mfma_f32_16x16x32_bf16 v[136:139], a[64:67], a[8:11], v[136:139]\n" \
"ds_read_b64_tr_b16 a[104:105], v11 offset:49152\n" \
"v_mfma_f32_16x16x32_bf16 v[136:139], a[68:71], a[12:15], v[136:139]\n" \
"v_mfma_f32_16x16x32_bf16 v[144:147], a[64:67], a[16:19], v[144:147]\n" \
"s_add_u32 m0, s51, 33664\n" \
"ds_read_b64_tr_b16 a[106:107], v12 offset:49152\n" \
"v_mfma_f32_16x16x32_bf16 v[144:147], a[68:71], a[20:23], v[144:147]\n" \
"buffer_load_dwordx4 v21, s[32:35], s47 offen offset:128 lds\n" \
"v_mfma_f32_16x16x32_bf16 v[152:155], a[64:67], a[24:27], v[152:155]\n" \
"s_waitcnt lgkmcnt(14)\n" \
"ds_read_b64_tr_b16 a[108:109], v11 offset:57344\n" \
"v_mfma_f32_16x16x32_bf16 v[152:155], a[68:71], a[28:31], v[152:155]\n" \
"ds_read_b64_tr_b16 a[110:111], v12 offset:57344\n" \
"v_mfma_f32_16x16x32_bf16 v[132:135], a[72:75], a[0:3], v[132:135]\n" \
"s_mov_b32 s47, 4456448\n" \
"s_add_u32 m0, s51, 40960\n" \
"v_mfma_f32_16x16x32_bf16 v[132:135], a[76:79], a[4:7], v[132:135]\n" \
"buffer_load_dwordx4 v21, s[32:35], s47 offen offset:0 lds\n" \
"v_mfma_f32_16x16x32_bf16 v[140:143], a[72:75], a[8:11], v[140:143]\n" \
"v_mfma_f32_16x16x32_bf16 v[140:143], a[76:79], a[12:15], v[140:143]\n" \
"buffer_store_dwordx4 v[42:45], v4, s[36:39], s97 offen offset:0 sc1\n" \
"v_mfma_f32_16x16x32_bf16 v[148:151], a[72:75], a[16:19], v[148:151]\n" \
"s_add_u32 m0, s51, 41856\n" \
"v_mfma_f32_16x16x32_bf16 v[148:151], a[76:79], a[20:23], v[148:151]\n" \
"buffer_load_dwordx4 v21, s[32:35], s47 offen offset:128 lds\n" \
"v_mfma_f32_16x16x32_bf16 v[156:159], a[72:75], a[24:27], v[156:159]\n" \
"s_waitcnt vmcnt(20) lgkmcnt(0)\n" \
"v_mfma_f32_16x16x32_bf16 v[156:159], a[76:79], a[28:31], v[156:159]\n" \
"s_barrier\n" \
"v_mfma_f32_16x16x32_bf16 v[160:163], a[80:83], a[0:3], v[160:163]\n" \
"ds_read_b64_tr_b16 a[64:65], v9 offset:512\n" \
"v_mfma_f32_16x16x32_bf16 v[160:163], a[84:87], a[4:7], v[160:163]\n" \
"ds_read_b64_tr_b16 a[66:67], v10 offset:512\n" \
"v_mfma_f32_16x16x32_bf16 v[168:171], a[80:83], a[8:11], v[168:171]\n" \
"s_mov_b32 s46, 1024\n" \
"s_add_u32 m0, s50, 32768\n" \
"ds_read_b64_tr_b16 a[68:69], v9 offset:8704\n" \
"v_mfma_f32_16x16x32_bf16 v[168:171], a[84:87], a[12:15], v[168:171]\n" \
"buffer_load_dwordx4 v13, s[28:31], s46 offen lds\n" \
"v_mfma_f32_16x16x32_bf16 v[176:179], a[80:83], a[16:19], v[176:179]\n" \
"ds_read_b64_tr_b16 a[70:71], v10 offset:8704\n" \
"v_mfma_f32_16x16x32_bf16 v[176:179], a[84:87], a[20:23], v[176:179]\n" \
"ds_read_b128 a[200:203], v7 offset:0\n" \
"v_mfma_f32_16x16x32_bf16 v[184:187], a[80:83], a[24:27], v[184:187]\n" \
"ds_read_b128 a[204:207], v8 offset:0\n" \
"v_mfma_f32_16x16x32_bf16 v[184:187], a[84:87], a[28:31], v[184:187]\n" \
"ds_read_b128 a[208:211], v7 offset:2048\n" \
"v_mfma_f32_16x16x32_bf16 v[164:167], a[88:91], a[0:3], v[164:167]\n" \
"ds_read_b128 a[212:215], v8 offset:2048\n" \
"v_mfma_f32_16x16x32_bf16 v[164:167], a[92:95], a[4:7], v[164:167]\n" \
"ds_read_b128 a[216:219], v7 offset:4096\n" \
"v_mfma_f32_16x16x32_bf16 v[172:175], a[88:91], a[8:11], v[172:175]\n" \
"s_add_u32 m0, s50, 36864\n" \
"ds_read_b128 a[220:223], v8 offset:4096\n" \
"v_mfma_f32_16x16x32_bf16 v[172:175], a[92:95], a[12:15], v[172:175]\n" \
"buffer_load_dwordx4 v14, s[28:31], s46 offen lds\n" \
"v_mfma_f32_16x16x32_bf16 v[180:183], a[88:91], a[16:19], v[180:183]\n" \
"ds_read_b128 a[224:227], v7 offset:6144\n" \
"v_mfma_f32_16x16x32_bf16 v[180:183], a[92:95], a[20:23], v[180:183]\n" \
"ds_read_b128 a[228:231], v8 offset:6144\n" \
"v_mfma_f32_16x16x32_bf16 v[188:191], a[88:91], a[24:27], v[188:191]\n" \
"LH11_%=:\ns_cbranch_execz LH15_%=\n" \
"ds_read_b64_tr_b16 a[72:73], v11 offset:512\n" \
"v_mfma_f32_16x16x32_bf16 v[188:191], a[92:95], a[28:31], v[188:191]\n" \
"ds_read_b64_tr_b16 a[74:75], v12 offset:512\n" \
"v_mfma_f32_16x16x32_bf16 v[64:67], a[96:99], a[0:3], v[64:67]\n" \
"ds_read_b64_tr_b16 a[76:77], v11 offset:8704\n" \
"v_mfma_f32_16x16x32_bf16 v[64:67], a[100:103], a[4:7], v[64:67]\n" \
"ds_read_b64_tr_b16 a[78:79], v12 offset:8704\n" \
"v_mfma_f32_16x16x32_bf16 v[72:75], a[96:99], a[8:11], v[72:75]\n" \
"s_add_u32 m0, s50, 40960\n" \
"ds_read_b64_tr_b16 a[80:81], v9 offset:1024\n" \
"v_mfma_f32_16x16x32_bf16 v[72:75], a[100:103], a[12:15], v[72:75]\n" \
"buffer_load_dwordx4 v15, s[28:31], s46 offen lds\n" \
"v_mfma_f32_16x16x32_bf16 v[80:83], a[96:99], a[16:19], v[80:83]\n" \
"ds_read_b64_tr_b16 a[82:83], v10 offset:1024\n" \
"v_mfma_f32_16x16x32_bf16 v[80:83], a[100:103], a[20:23], v[80:83]\n" \
"ds_read_b64_tr_b16 a[84:85], v9 offset:9216\n" \
"v_mfma_f32_16x16x32_bf16 v[88:91], a[96:99], a[24:27], v[88:91]\n" \
"ds_read_b64_tr_b16 a[86:87], v10 offset:9216\n" \
"v_mfma_f32_16x16x32_bf16 v[88:91], a[100:103], a[28:31], v[88:91]\n" \
"ds_read_b64_tr_b16 a[88:89], v11 offset:1024\n" \
"v_mfma_f32_16x16x32_bf16 v[68:71], a[104:107], a[0:3], v[68:71]\n" \
"ds_read_b64_tr_b16 a[90:91], v12 offset:1024\n" \
"v_mfma_f32_16x16x32_bf16 v[68:71], a[108:111], a[4:7], v[68:71]\n" \
"ds_read_b64_tr_b16 a[92:93], v11 offset:9216\n" \
"v_mfma_f32_16x16x32_bf16 v[76:79], a[104:107], a[8:11], v[76:79]\n" \
"s_add_u32 m0, s50, 45056\n" \
"ds_read_b64_tr_b16 a[94:95], v12 offset:9216\n" \
"v_mfma_f32_16x16x32_bf16 v[76:79], a[108:111], a[12:15], v[76:79]\n" \
"buffer_load_dwordx4 v16, s[28:31], s46 offen lds\n" \
"v_mfma_f32_16x16x32_bf16 v[84:87], a[104:107], a[16:19], v[84:87]\n" \
"v_mfma_f32_16x16x32_bf16 v[84:87], a[108:111], a[20:23], v[84:87]\n" \
"v_mfma_f32_16x16x32_bf16 v[92:95], a[104:107], a[24:27], v[92:95]\n" \
"v_mfma_f32_16x16x32_bf16 v[92:95], a[108:111], a[28:31], v[92:95]\n" \
"s_waitcnt lgkmcnt(12)\n" \
"v_mfma_f32_16x16x32_bf16 v[96:99], a[64:67], a[200:203], v[96:99]\n" \
"s_mov_b32 s46, 1024\n" \
"s_add_u32 m0, s50, 49152\n" \
"ds_read_b64_tr_b16 a[96:97], v9 offset:1536\n" \
"v_mfma_f32_16x16x32_bf16 v[96:99], a[68:71], a[204:207], v[96:99]\n" \
"buffer_load_dwordx4 v17, s[28:31], s46 offen lds\n" \
"v_mfma_f32_16x16x32_bf16 v[104:107], a[64:67], a[208:211], v[104:107]\n" \
"ds_read_b64_tr_b16 a[98:99], v10 offset:1536\n" \
"v_mfma_f32_16x16x32_bf16 v[104:107], a[68:71], a[212:215], v[104:107]\n" \
"ds_read_b64_tr_b16 a[100:101], v9 offset:9728\n" \
"v_mfma_f32_16x16x32_bf16 v[112:115], a[64:67], a[216:219], v[112:115]\n" \
"s_add_u32 m0, s50, 53248\n" \
"ds_read_b64_tr_b16 a[102:103], v10 offset:9728\n" \
"v_mfma_f32_16x16x32_bf16 v[112:115], a[68:71], a[220:223], v[112:115]\n" \
"buffer_load_dwordx4 v18, s[28:31], s46 offen lds\n" \
"v_mfma_f32_16x16x32_bf16 v[120:123], a[64:67], a[224:227], v[120:123]\n" \
"s_waitcnt lgkmcnt(12)\n" \
"ds_read_b64_tr_b16 a[104:105], v11 offset:1536\n" \
"v_mfma_f32_16x16x32_bf16 v[120:123], a[68:71], a[228:231], v[120:123]\n" \
"v_mfma_f32_16x16x32_bf16 v[100:103], a[72:75], a[200:203], v[100:103]\n" \
"s_add_u32 m0, s50, 57344\n" \
"ds_read_b64_tr_b16 a[106:107], v12 offset:1536\n" \
"v_mfma_f32_16x16x32_bf16 v[100:103], a[76:79], a[204:207], v[100:103]\n" \
"buffer_load_dwordx4 v19, s[28:31], s46 offen lds\n" \
"v_mfma_f32_16x16x32_bf16 v[108:111], a[72:75], a[208:211], v[108:111]\n" \
"ds_read_b64_tr_b16 a[108:109], v11 offset:9728\n" \
"v_mfma_f32_16x16x32_bf16 v[108:111], a[76:79], a[212:215], v[108:111]\n" \
"ds_read_b64_tr_b16 a[110:111], v12 offset:9728\n" \
"v_mfma_f32_16x16x32_bf16 v[116:119], a[72:75], a[216:219], v[116:119]\n" \
"s_add_u32 m0, s50, 61440\n" \
"ds_read_b64_tr_b16 a[64:65], v9 offset:0\n" \
"v_mfma_f32_16x16x32_bf16 v[116:119], a[76:79], a[220:223], v[116:119]\n" \
"buffer_load_dwordx4 v20, s[28:31], s46 offen lds\n" \
"v_mfma_f32_16x16x32_bf16 v[124:127], a[72:75], a[224:227], v[124:127]\n" \
"s_waitcnt lgkmcnt(13)\n" \
"ds_read_b64_tr_b16 a[66:67], v10 offset:0\n" \
"v_mfma_f32_16x16x32_bf16 v[124:127], a[76:79], a[228:231], v[124:127]\n" \
"ds_read_b64_tr_b16 a[68:69], v9 offset:8192\n" \
"v_mfma_f32_16x16x32_bf16 v[128:131], a[80:83], a[200:203], v[128:131]\n" \
"s_mov_b32 s47, 4718592\n" \
"s_add_u32 m0, s51, 49152\n" \
"ds_read_b64_tr_b16 a[70:71], v10 offset:8192\n" \
"v_mfma_f32_16x16x32_bf16 v[128:131], a[84:87], a[204:207], v[128:131]\n" \
"buffer_load_dwordx4 v21, s[32:35], s47 offen offset:0 lds\n" \
"v_mfma_f32_16x16x32_bf16 v[136:139], a[80:83], a[208:211], v[136:139]\n" \
"ds_read_b64_tr_b16 a[72:73], v11 offset:0\n" \
"v_mfma_f32_16x16x32_bf16 v[136:139], a[84:87], a[212:215], v[136:139]\n" \
"v_mfma_f32_16x16x32_bf16 v[144:147], a[80:83], a[216:219], v[144:147]\n" \
"s_add_u32 m0, s51, 50048\n" \
"ds_read_b64_tr_b16 a[74:75], v12 offset:0\n" \
"v_mfma_f32_16x16x32_bf16 v[144:147], a[84:87], a[220:223], v[144:147]\n" \
"buffer_load_dwordx4 v21, s[32:35], s47 offen offset:128 lds\n" \
"v_mfma_f32_16x16x32_bf16 v[152:155], a[80:83], a[224:227], v[152:155]\n" \
"s_waitcnt lgkmcnt(14)\n" \
"ds_read_b64_tr_b16 a[76:77], v11 offset:8192\n" \
"v_mfma_f32_16x16x32_bf16 v[152:155], a[84:87], a[228:231], v[152:155]\n" \
"ds_read_b64_tr_b16 a[78:79], v12 offset:8192\n" \
"v_mfma_f32_16x16x32_bf16 v[132:135], a[88:91], a[200:203], v[132:135]\n" \
"s_mov_b32 s47, 4980736\n" \
"s_add_u32 m0, s51, 57344\n" \
"v_mfma_f32_16x16x32_bf16 v[132:135], a[92:95], a[204:207], v[132:135]\n" \
"buffer_load_dwordx4 v21, s[32:35], s47 offen offset:0 lds\n" \
"v_mfma_f32_16x16x32_bf16 v[140:143], a[88:91], a[208:211], v[140:143]\n" \
"s_add_u32 s76, s76, 2097152\ns_addc_u32 s77, s77, 0\n" \
"v_mfma_f32_16x16x32_bf16 v[140:143], a[92:95], a[212:215], v[140:143]\n" \
"v_mfma_f32_16x16x32_bf16 v[148:151], a[88:91], a[216:219], v[148:151]\n" \
"s_add_u32 m0, s51, 58240\n" \
"v_mfma_f32_16x16x32_bf16 v[148:151], a[92:95], a[220:223], v[148:151]\n" \
"buffer_load_dwordx4 v21, s[32:35], s47 offen offset:128 lds\n" \
"v_mfma_f32_16x16x32_bf16 v[156:159], a[88:91], a[224:227], v[156:159]\n" \
"s_waitcnt vmcnt(17) lgkmcnt(0)\n" \
"v_mfma_f32_16x16x32_bf16 v[156:159], a[92:95], a[228:231], v[156:159]\n" \
"s_barrier\n" \
"v_mfma_f32_16x16x32_bf16 v[160:163], a[96:99], a[200:203], v[160:163]\n" \
"ds_read_b64_tr_b16 a[80:81], v9 offset:16896\n" \
"v_mfma_f32_16x16x32_bf16 v[160:163], a[100:103], a[204:207], v[160:163]\n" \
"ds_read_b64_tr_b16 a[82:83], v10 offset:16896\n" \
"v_mfma_f32_16x16x32_bf16 v[168:171], a[96:99], a[208:211], v[168:171]\n" \
"s_mov_b32 s46, 1152\n" \
"s_add_u32 m0, s50, 65536\n" \
"ds_read_b64_tr_b16 a[84:85], v9 offset:25088\n" \
"v_mfma_f32_16x16x32_bf16 v[168:171], a[100:103], a[212:215], v[168:171]\n" \
"buffer_load_dwordx4 v13, s[28:31], s46 offen lds\n" \
"v_mfma_f32_16x16x32_bf16 v[176:179], a[96:99], a[216:219], v[176:179]\n" \
"ds_read_b64_tr_b16 a[86:87], v10 offset:25088\n" \
"v_mfma_f32_16x16x32_bf16 v[176:179], a[100:103], a[220:223], v[176:179]\n" \
"ds_read_b128 a[0:3], v5 offset:0\n" \
"v_mfma_f32_16x16x32_bf16 v[184:187], a[96:99], a[224:227], v[184:187]\n" \
"ds_read_b128 a[4:7], v6 offset:0\n" \
"v_mfma_f32_16x16x32_bf16 v[184:187], a[100:103], a[228:231], v[184:187]\n" \
"ds_read_b128 a[8:11], v5 offset:2048\n" \
"v_mfma_f32_16x16x32_bf16 v[164:167], a[104:107], a[200:203], v[164:167]\n" \
"ds_read_b128 a[12:15], v6 offset:2048\n" \
"v_mfma_f32_16x16x32_bf16 v[164:167], a[108:111], a[204:207], v[164:167]\n" \
"ds_read_b128 a[16:19], v5 offset:4096\n" \
"v_mfma_f32_16x16x32_bf16 v[172:175], a[104:107], a[208:211], v[172:175]\n" \
"s_add_u32 m0, s50, 69632\n" \
"ds_read_b128 a[20:23], v6 offset:4096\n" \
"v_mfma_f32_16x16x32_bf16 v[172:175], a[108:111], a[212:215], v[172:175]\n" \
"buffer_load_dwordx4 v14, s[28:31], s46 offen lds\n" \
"v_mfma_f32_16x16x32_bf16 v[180:183], a[104:107], a[216:219], v[180:183]\n" \
"ds_read_b128 a[24:27], v5 offset:6144\n" \
"v_mfma_f32_16x16x32_bf16 v[180:183], a[108:111], a[220:223], v[180:183]\n" \
"ds_read_b128 a[28:31], v6 offset:6144\n" \
"v_mfma_f32_16x16x32_bf16 v[188:191], a[104:107], a[224:227], v[188:191]\n" \
"ds_read_b64_tr_b16 a[88:89], v11 offset:16896\n" \
"v_mfma_f32_16x16x32_bf16 v[188:191], a[108:111], a[228:231], v[188:191]\n" \
"ds_read_b64_tr_b16 a[90:91], v12 offset:16896\n" \
"v_mfma_f32_16x16x32_bf16 v[64:67], a[64:67], a[200:203], v[64:67]\n" \
"ds_read_b64_tr_b16 a[92:93], v11 offset:25088\n" \
"v_mfma_f32_16x16x32_bf16 v[64:67], a[68:71], a[204:207], v[64:67]\n" \
"ds_read_b64_tr_b16 a[94:95], v12 offset:25088\n" \
"v_mfma_f32_16x16x32_bf16 v[72:75], a[64:67], a[208:211], v[72:75]\n" \
"s_add_u32 m0, s50, 73728\n" \
"ds_read_b64_tr_b16 a[96:97], v9 offset:17408\n" \
"v_mfma_f32_16x16x32_bf16 v[72:75], a[68:71], a[212:215], v[72:75]\n" \
"buffer_load_dwordx4 v15, s[28:31], s46 offen lds\n" \
"v_mfma_f32_16x16x32_bf16 v[80:83], a[64:67], a[216:219], v[80:83]\n" \
"ds_read_b64_tr_b16 a[98:99], v10 offset:17408\n" \
"v_mfma_f32_16x16x32_bf16 v[80:83], a[68:71], a[220:223], v[80:83]\n" \
"ds_read_b64_tr_b16 a[100:101], v9 offset:25600\n" \
"v_mfma_f32_16x16x32_bf16 v[88:91], a[64:67], a[224:227], v[88:91]\n" \
"ds_read_b64_tr_b16 a[102:103], v10 offset:25600\n" \
"v_mfma_f32_16x16x32_bf16 v[88:91], a[68:71], a[228:231], v[88:91]\n" \
"ds_read_b64_tr_b16 a[104:105], v11 offset:17408\n" \
"v_mfma_f32_16x16x32_bf16 v[68:71], a[72:75], a[200:203], v[68:71]\n" \
"ds_read_b64_tr_b16 a[106:107], v12 offset:17408\n" \
"v_mfma_f32_16x16x32_bf16 v[68:71], a[76:79], a[204:207], v[68:71]\n" \
"ds_read_b64_tr_b16 a[108:109], v11 offset:25600\n" \
"v_mfma_f32_16x16x32_bf16 v[76:79], a[72:75], a[208:211], v[76:79]\n" \
"s_add_u32 m0, s50, 77824\n" \
"ds_read_b64_tr_b16 a[110:111], v12 offset:25600\n" \
"v_mfma_f32_16x16x32_bf16 v[76:79], a[76:79], a[212:215], v[76:79]\n" \
"buffer_load_dwordx4 v16, s[28:31], s46 offen lds\n" \
"v_mfma_f32_16x16x32_bf16 v[84:87], a[72:75], a[216:219], v[84:87]\n" \
"v_mfma_f32_16x16x32_bf16 v[84:87], a[76:79], a[220:223], v[84:87]\n" \
"s_mov_b64 exec, 0xffff\nglobal_load_dword v22, v1, s[76:77]\ns_mov_b64 exec, -1\n" \
"v_mfma_f32_16x16x32_bf16 v[92:95], a[72:75], a[224:227], v[92:95]\n" \
"v_mfma_f32_16x16x32_bf16 v[92:95], a[76:79], a[228:231], v[92:95]\n" \
"s_waitcnt lgkmcnt(12)\n" \
"v_mfma_f32_16x16x32_bf16 v[96:99], a[80:83], a[0:3], v[96:99]\n" \
"s_mov_b32 s46, 1152\n" \
"s_add_u32 m0, s50, 81920\n" \
"ds_read_b64_tr_b16 a[64:65], v9 offset:17920\n" \
"v_mfma_f32_16x16x32_bf16 v[96:99], a[84:87], a[4:7], v[96:99]\n" \
"buffer_load_dwordx4 v17, s[28:31], s46 offen lds\n" \
"v_mfma_f32_16x16x32_bf16 v[104:107], a[80:83], a[8:11], v[104:107]\n" \
"ds_read_b64_tr_b16 a[66:67], v10 offset:17920\n" \
"v_mfma_f32_16x16x32_bf16 v[104:107], a[84:87], a[12:15], v[104:107]\n" \
"ds_read_b64_tr_b16 a[68:69], v9 offset:26112\n" \
"v_mfma_f32_16x16x32_bf16 v[112:115], a[80:83], a[16:19], v[112:115]\n" \
"s_add_u32 m0, s50, 86016\n" \
"ds_read_b64_tr_b16 a[70:71], v10 offset:26112\n" \
"v_mfma_f32_16x16x32_bf16 v[112:115], a[84:87], a[20:23], v[112:115]\n" \
"buffer_load_dwordx4 v18, s[28:31], s46 offen lds\n" \
"v_mfma_f32_16x16x32_bf16 v[120:123], a[80:83], a[24:27], v[120:123]\n" \
"s_waitcnt lgkmcnt(12)\n" \
"ds_read_b64_tr_b16 a[72:73], v11 offset:17920\n" \
"v_mfma_f32_16x16x32_bf16 v[120:123], a[84:87], a[28:31], v[120:123]\n" \
"v_mfma_f32_16x16x32_bf16 v[100:103], a[88:91], a[0:3], v[100:103]\n" \
"s_add_u32 m0, s50, 90112\n" \
"ds_read_b64_tr_b16 a[74:75], v12 offset:17920\n" \
"v_mfma_f32_16x16x32_bf16 v[100:103], a[92:95], a[4:7], v[100:103]\n" \
"buffer_load_dwordx4 v19, s[28:31], s46 offen lds\n" \
"v_mfma_f32_16x16x32_bf16 v[108:111], a[88:91], a[8:11], v[108:111]\n" \
"ds_read_b64_tr_b16 a[76:77], v11 offset:26112\n" \
"v_mfma_f32_16x16x32_bf16 v[108:111], a[92:95], a[12:15], v[108:111]\n" \
"ds_read_b64_tr_b16 a[78:79], v12 offset:26112\n" \
"v_mfma_f32_16x16x32_bf16 v[116:119], a[88:91], a[16:19], v[116:119]\n" \
"s_add_u32 m0, s50, 94208\n" \
"ds_read_b64_tr_b16 a[80:81], v9 offset:16384\n" \
"v_mfma_f32_16x16x32_bf16 v[116:119], a[92:95], a[20:23], v[116:119]\n" \
"buffer_load_dwordx4 v20, s[28:31], s46 offen lds\n" \
"v_mfma_f32_16x16x32_bf16 v[124:127], a[88:91], a[24:27], v[124:127]\n" \
"s_waitcnt lgkmcnt(13)\n" \
"ds_read_b64_tr_b16 a[82:83], v10 offset:16384\n" \
"v_mfma_f32_16x16x32_bf16 v[124:127], a[92:95], a[28:31], v[124:127]\n" \
"ds_read_b64_tr_b16 a[84:85], v9 offset:24576\n" \
"v_mfma_f32_16x16x32_bf16 v[128:131], a[96:99], a[0:3], v[128:131]\n" \
"s_mov_b32 s47, 5242880\n" \
"s_add_u32 m0, s51, 0\n" \
"ds_read_b64_tr_b16 a[86:87], v10 offset:24576\n" \
"v_mfma_f32_16x16x32_bf16 v[128:131], a[100:103], a[4:7], v[128:131]\n" \
"buffer_load_dwordx4 v21, s[32:35], s47 offen offset:0 lds\n" \
"v_mfma_f32_16x16x32_bf16 v[136:139], a[96:99], a[8:11], v[136:139]\n" \
"ds_read_b64_tr_b16 a[88:89], v11 offset:16384\n" \
"v_mfma_f32_16x16x32_bf16 v[136:139], a[100:103], a[12:15], v[136:139]\n" \
"v_mfma_f32_16x16x32_bf16 v[144:147], a[96:99], a[16:19], v[144:147]\n" \
"s_add_u32 m0, s51, 896\n" \
"ds_read_b64_tr_b16 a[90:91], v12 offset:16384\n" \
"v_mfma_f32_16x16x32_bf16 v[144:147], a[100:103], a[20:23], v[144:147]\n" \
"buffer_load_dwordx4 v21, s[32:35], s47 offen offset:128 lds\n" \
"v_mfma_f32_16x16x32_bf16 v[152:155], a[96:99], a[24:27], v[152:155]\n" \
"s_waitcnt lgkmcnt(14)\n" \
"ds_read_b64_tr_b16 a[92:93], v11 offset:24576\n" \
"v_mfma_f32_16x16x32_bf16 v[152:155], a[100:103], a[28:31], v[152:155]\n" \
"ds_read_b64_tr_b16 a[94:95], v12 offset:24576\n" \
"v_mfma_f32_16x16x32_bf16 v[132:135], a[104:107], a[0:3], v[132:135]\n" \
"s_mov_b32 s47, 5505024\n" \
"s_add_u32 m0, s51, 8192\n" \
"v_mfma_f32_16x16x32_bf16 v[132:135], a[108:111], a[4:7], v[132:135]\n" \
"buffer_load_dwordx4 v21, s[32:35], s47 offen offset:0 lds\n" \
"v_mfma_f32_16x16x32_bf16 v[140:143], a[104:107], a[8:11], v[140:143]\n" \
"v_mfma_f32_16x16x32_bf16 v[140:143], a[108:111], a[12:15], v[140:143]\n" \
"v_mfma_f32_16x16x32_bf16 v[148:151], a[104:107], a[16:19], v[148:151]\n" \
"s_add_u32 m0, s51, 9088\n" \
"v_mfma_f32_16x16x32_bf16 v[148:151], a[108:111], a[20:23], v[148:151]\n" \
"buffer_load_dwordx4 v21, s[32:35], s47 offen offset:128 lds\n" \
"v_mfma_f32_16x16x32_bf16 v[156:159], a[104:107], a[24:27], v[156:159]\n" \
"s_waitcnt vmcnt(17) lgkmcnt(0)\n" \
"v_mfma_f32_16x16x32_bf16 v[156:159], a[108:111], a[28:31], v[156:159]\n" \
"s_barrier\n" \
"v_mfma_f32_16x16x32_bf16 v[160:163], a[64:67], a[0:3], v[160:163]\n" \
"ds_read_b64_tr_b16 a[96:97], v9 offset:33280\n" \
"v_mfma_f32_16x16x32_bf16 v[160:163], a[68:71], a[4:7], v[160:163]\n" \
"ds_read_b64_tr_b16 a[98:99], v10 offset:33280\n" \
"v_mfma_f32_16x16x32_bf16 v[168:171], a[64:67], a[8:11], v[168:171]\n" \
"s_mov_b32 s46, 1280\n" \
"s_add_u32 m0, s50, 0\n" \
"ds_read_b64_tr_b16 a[100:101], v9 offset:41472\n" \
"v_mfma_f32_16x16x32_bf16 v[168:171], a[68:71], a[12:15], v[168:171]\n" \
"buffer_load_dwordx4 v13, s[28:31], s46 offen lds\n" \
"v_mfma_f32_16x16x32_bf16 v[176:179], a[64:67], a[16:19], v[176:179]\n" \
"ds_read_b64_tr_b16 a[102:103], v10 offset:41472\n" \
"v_mfma_f32_16x16x32_bf16 v[176:179], a[68:71], a[20:23], v[176:179]\n" \
"ds_read_b128 a[200:203], v5 offset:32768\n" \
"v_mfma_f32_16x16x32_bf16 v[184:187], a[64:67], a[24:27], v[184:187]\n" \
"ds_read_b128 a[204:207], v6 offset:32768\n" \
"v_mfma_f32_16x16x32_bf16 v[184:187], a[68:71], a[28:31], v[184:187]\n" \
"ds_read_b128 a[208:211], v5 offset:34816\n" \
"v_mfma_f32_16x16x32_bf16 v[164:167], a[72:75], a[0:3], v[164:167]\n" \
"ds_read_b128 a[212:215], v6 offset:34816\n" \
"v_mfma_f32_16x16x32_bf16 v[164:167], a[76:79], a[4:7], v[164:167]\n" \
"ds_read_b128 a[216:219], v5 offset:36864\n" \
"v_mfma_f32_16x16x32_bf16 v[172:175], a[72:75], a[8:11], v[172:175]\n" \
"s_add_u32 m0, s50, 4096\n" \
"ds_read_b128 a[220:223], v6 offset:36864\n" \
"v_mfma_f32_16x16x32_bf16 v[172:175], a[76:79], a[12:15], v[172:175]\n" \
"buffer_load_dwordx4 v14, s[28:31], s46 offen lds\n" \
"v_mfma_f32_16x16x32_bf16 v[180:183], a[72:75], a[16:19], v[180:183]\n" \
"ds_read_b128 a[224:227], v5 offset:38912\n" \
"v_mfma_f32_16x16x32_bf16 v[180:183], a[76:79], a[20:23], v[180:183]\n" \
"ds_read_b128 a[228:231], v6 offset:38912\n" \
"v_mfma_f32_16x16x32_bf16 v[188:191], a[72:75], a[24:27], v[188:191]\n" \
"ds_read_b64_tr_b16 a[104:105], v11 offset:33280\n" \
"v_mfma_f32_16x16x32_bf16 v[188:191], a[76:79], a[28:31], v[188:191]\n" \
"ds_read_b64_tr_b16 a[106:107], v12 offset:33280\n" \
"v_mfma_f32_16x16x32_bf16 v[64:67], a[80:83], a[0:3], v[64:67]\n" \
"ds_read_b64_tr_b16 a[108:109], v11 offset:41472\n" \
"v_mfma_f32_16x16x32_bf16 v[64:67], a[84:87], a[4:7], v[64:67]\n" \
"ds_read_b64_tr_b16 a[110:111], v12 offset:41472\n" \
"v_mfma_f32_16x16x32_bf16 v[72:75], a[80:83], a[8:11], v[72:75]\n" \
"s_add_u32 m0, s50, 8192\n" \
"ds_read_b64_tr_b16 a[64:65], v9 offset:33792\n" \
"v_mfma_f32_16x16x32_bf16 v[72:75], a[84:87], a[12:15], v[72:75]\n" \
"buffer_load_dwordx4 v15, s[28:31], s46 offen lds\n" \
"v_mfma_f32_16x16x32_bf16 v[80:83], a[80:83], a[16:19], v[80:83]\n" \
"ds_read_b64_tr_b16 a[66:67], v10 offset:33792\n" \
"v_mfma_f32_16x16x32_bf16 v[80:83], a[84:87], a[20:23], v[80:83]\n" \
"ds_read_b64_tr_b16 a[68:69], v9 offset:41984\n" \
"v_mfma_f32_16x16x32_bf16 v[88:91], a[80:83], a[24:27], v[88:91]\n" \
"ds_read_b64_tr_b16 a[70:71], v10 offset:41984\n" \
"v_mfma_f32_16x16x32_bf16 v[88:91], a[84:87], a[28:31], v[88:91]\n" \
"ds_read_b64_tr_b16 a[72:73], v11 offset:33792\n" \
"v_mfma_f32_16x16x32_bf16 v[68:71], a[88:91], a[0:3], v[68:71]\n" \
"ds_read_b64_tr_b16 a[74:75], v12 offset:33792\n" \
"v_mfma_f32_16x16x32_bf16 v[68:71], a[92:95], a[4:7], v[68:71]\n" \
"ds_read_b64_tr_b16 a[76:77], v11 offset:41984\n" \
"v_mfma_f32_16x16x32_bf16 v[76:79], a[88:91], a[8:11], v[76:79]\n" \
"s_add_u32 m0, s50, 12288\n" \
"ds_read_b64_tr_b16 a[78:79], v12 offset:41984\n" \
"v_mfma_f32_16x16x32_bf16 v[76:79], a[92:95], a[12:15], v[76:79]\n" \
"buffer_load_dwordx4 v16, s[28:31], s46 offen lds\n" \
"v_mfma_f32_16x16x32_bf16 v[84:87], a[88:91], a[16:19], v[84:87]\n" \
"v_mfma_f32_16x16x32_bf16 v[84:87], a[92:95], a[20:23], v[84:87]\n" \
"v_mfma_f32_16x16x32_bf16 v[92:95], a[88:91], a[24:27], v[92:95]\n" \
"v_mfma_f32_16x16x32_bf16 v[92:95], a[92:95], a[28:31], v[92:95]\n" \
"s_waitcnt lgkmcnt(12)\n" \
"v_mfma_f32_16x16x32_bf16 v[96:99], a[96:99], a[200:203], v[96:99]\n" \
"s_mov_b32 s46, 1280\n" \
"s_add_u32 m0, s50, 16384\n" \
"ds_read_b64_tr_b16 a[80:81], v9 offset:34304\n" \
"v_mfma_f32_16x16x32_bf16 v[96:99], a[100:103], a[204:207], v[96:99]\n" \
"buffer_load_dwordx4 v17, s[28:31], s46 offen lds\n" \
"v_mfma_f32_16x16x32_bf16 v[104:107], a[96:99], a[208:211], v[104:107]\n" \
"ds_read_b64_tr_b16 a[82:83], v10 offset:34304\n" \
"v_mfma_f32_16x16x32_bf16 v[104:107], a[100:103], a[212:215], v[104:107]\n" \
"ds_read_b64_tr_b16 a[84:85], v9 offset:42496\n" \
"v_mfma_f32_16x16x32_bf16 v[112:115], a[96:99], a[216:219], v[112:115]\n" \
"s_add_u32 m0, s50, 20480\n" \
"ds_read_b64_tr_b16 a[86:87], v10 offset:42496\n" \
"v_mfma_f32_16x16x32_bf16 v[112:115], a[100:103], a[220:223], v[112:115]\n" \
"buffer_load_dwordx4 v18, s[28:31], s46 offen lds\n" \
"v_mfma_f32_16x16x32_bf16 v[120:123], a[96:99], a[224:227], v[120:123]\n" \
"s_waitcnt lgkmcnt(12)\n" \
"ds_read_b64_tr_b16 a[88:89], v11 offset:34304\n" \
"v_mfma_f32_16x16x32_bf16 v[120:123], a[100:103], a[228:231], v[120:123]\n" \
"v_mfma_f32_16x16x32_bf16 v[100:103], a[104:107], a[200:203], v[100:103]\n" \
"s_add_u32 m0, s50, 24576\n" \
"ds_read_b64_tr_b16 a[90:91], v12 offset:34304\n" \
"v_mfma_f32_16x16x32_bf16 v[100:103], a[108:111], a[204:207], v[100:103]\n" \
"buffer_load_dwordx4 v19, s[28:31], s46 offen lds\n" \
"v_mfma_f32_16x16x32_bf16 v[108:111], a[104:107], a[208:211], v[108:111]\n" \
"ds_read_b64_tr_b16 a[92:93], v11 offset:42496\n" \
"v_mfma_f32_16x16x32_bf16 v[108:111], a[108:111], a[212:215], v[108:111]\n" \
"ds_read_b64_tr_b16 a[94:95], v12 offset:42496\n" \
"v_mfma_f32_16x16x32_bf16 v[116:119], a[104:107], a[216:219], v[116:119]\n" \
"s_add_u32 m0, s50, 28672\n" \
"ds_read_b64_tr_b16 a[96:97], v9 offset:32768\n" \
"v_mfma_f32_16x16x32_bf16 v[116:119], a[108:111], a[220:223], v[116:119]\n" \
"buffer_load_dwordx4 v20, s[28:31], s46 offen lds\n" \
"v_mfma_f32_16x16x32_bf16 v[124:127], a[104:107], a[224:227], v[124:127]\n" \
"s_waitcnt lgkmcnt(13)\n" \
"ds_read_b64_tr_b16 a[98:99], v10 offset:32768\n" \
"v_mfma_f32_16x16x32_bf16 v[124:127], a[108:111], a[228:231], v[124:127]\n" \
"ds_read_b64_tr_b16 a[100:101], v9 offset:40960\n" \
"v_mfma_f32_16x16x32_bf16 v[128:131], a[64:67], a[200:203], v[128:131]\n" \
"s_mov_b32 s47, 5767168\n" \
"s_add_u32 m0, s51, 16384\n" \
"ds_read_b64_tr_b16 a[102:103], v10 offset:40960\n" \
"v_mfma_f32_16x16x32_bf16 v[128:131], a[68:71], a[204:207], v[128:131]\n" \
"buffer_load_dwordx4 v21, s[32:35], s47 offen offset:0 lds\n" \
"v_mfma_f32_16x16x32_bf16 v[136:139], a[64:67], a[208:211], v[136:139]\n" \
"ds_read_b64_tr_b16 a[104:105], v11 offset:32768\n" \
"v_mfma_f32_16x16x32_bf16 v[136:139], a[68:71], a[212:215], v[136:139]\n" \
"v_mfma_f32_16x16x32_bf16 v[144:147], a[64:67], a[216:219], v[144:147]\n" \
"s_add_u32 m0, s51, 17280\n" \
"ds_read_b64_tr_b16 a[106:107], v12 offset:32768\n" \
"v_mfma_f32_16x16x32_bf16 v[144:147], a[68:71], a[220:223], v[144:147]\n" \
"buffer_load_dwordx4 v21, s[32:35], s47 offen offset:128 lds\n" \
"v_mfma_f32_16x16x32_bf16 v[152:155], a[64:67], a[224:227], v[152:155]\n" \
"s_waitcnt lgkmcnt(14)\n" \
"ds_read_b64_tr_b16 a[108:109], v11 offset:40960\n" \
"v_mfma_f32_16x16x32_bf16 v[152:155], a[68:71], a[228:231], v[152:155]\n" \
"ds_read_b64_tr_b16 a[110:111], v12 offset:40960\n" \
"v_mfma_f32_16x16x32_bf16 v[132:135], a[72:75], a[200:203], v[132:135]\n" \
"s_mov_b32 s47, 6029312\n" \
"s_add_u32 m0, s51, 24576\n" \
"v_mfma_f32_16x16x32_bf16 v[132:135], a[76:79], a[204:207], v[132:135]\n" \
"buffer_load_dwordx4 v21, s[32:35], s47 offen offset:0 lds\n" \
"v_mfma_f32_16x16x32_bf16 v[140:143], a[72:75], a[208:211], v[140:143]\n" \
"v_mfma_f32_16x16x32_bf16 v[140:143], a[76:79], a[212:215], v[140:143]\n" \
"v_mfma_f32_16x16x32_bf16 v[148:151], a[72:75], a[216:219], v[148:151]\n" \
"s_add_u32 m0, s51, 25472\n" \
"v_mfma_f32_16x16x32_bf16 v[148:151], a[76:79], a[220:223], v[148:151]\n" \
"buffer_load_dwordx4 v21, s[32:35], s47 offen offset:128 lds\n" \
"v_mfma_f32_16x16x32_bf16 v[156:159], a[72:75], a[224:227], v[156:159]\n" \
"s_waitcnt vmcnt(16) lgkmcnt(0)\n" \
"v_mfma_f32_16x16x32_bf16 v[156:159], a[76:79], a[228:231], v[156:159]\n" \
"s_barrier\n" \
"v_mfma_f32_16x16x32_bf16 v[160:163], a[80:83], a[200:203], v[160:163]\n" \
"ds_read_b64_tr_b16 a[64:65], v9 offset:49664\n" \
"v_mfma_f32_16x16x32_bf16 v[160:163], a[84:87], a[204:207], v[160:163]\n" \
"ds_read_b64_tr_b16 a[66:67], v10 offset:49664\n" \
"v_mfma_f32_16x16x32_bf16 v[168:171], a[80:83], a[208:211], v[168:171]\n" \
"s_mov_b32 s46, 1408\n" \
"s_add_u32 m0, s50, 32768\n" \
"ds_read_b64_tr_b16 a[68:69], v9 offset:57856\n" \
"v_mfma_f32_16x16x32_bf16 v[168:171], a[84:87], a[212:215], v[168:171]\n" \
"buffer_load_dwordx4 v13, s[28:31], s46 offen lds\n" \
"v_mfma_f32_16x16x32_bf16 v[176:179], a[80:83], a[216:219], v[176:179]\n" \
"ds_read_b64_tr_b16 a[70:71], v10 offset:57856\n" \
"v_mfma_f32_16x16x32_bf16 v[176:179], a[84:87], a[220:223], v[176:179]\n" \
"ds_read_b128 a[0:3], v7 offset:0\n" \
"v_mfma_f32_16x16x32_bf16 v[184:187], a[80:83], a[224:227], v[184:187]\n" \
"ds_read_b128 a[4:7], v8 offset:0\n" \
"v_mfma_f32_16x16x32_bf16 v[184:187], a[84:87], a[228:231], v[184:187]\n" \
"ds_read_b128 a[8:11], v7 offset:2048\n" \
"v_mfma_f32_16x16x32_bf16 v[164:167], a[88:91], a[200:203], v[164:167]\n" \
"ds_read_b128 a[12:15], v8 offset:2048\n" \
"v_mfma_f32_16x16x32_bf16 v[164:167], a[92:95], a[204:207], v[164:167]\n" \
"ds_read_b128 a[16:19], v7 offset:4096\n" \
"v_mfma_f32_16x16x32_bf16 v[172:175], a[88:91], a[208:211], v[172:175]\n" \
"s_add_u32 m0, s50, 36864\n" \
"ds_read_b128 a[20:23], v8 offset:4096\n" \
"v_mfma_f32_16x16x32_bf16 v[172:175], a[92:95], a[212:215], v[172:175]\n" \
"buffer_load_dwordx4 v14, s[28:31], s46 offen lds\n" \
"v_mfma_f32_16x16x32_bf16 v[180:183], a[88:91], a[216:219], v[180:183]\n" \
"ds_read_b128 a[24:27], v7 offset:6144\n" \
"v_mfma_f32_16x16x32_bf16 v[180:183], a[92:95], a[220:223], v[180:183]\n" \
"ds_read_b128 a[28:31], v8 offset:6144\n" \
"v_mfma_f32_16x16x32_bf16 v[188:191], a[88:91], a[224:227], v[188:191]\n" \
"ds_read_b64_tr_b16 a[72:73], v11 offset:49664\n" \
"v_mfma_f32_16x16x32_bf16 v[188:191], a[92:95], a[228:231], v[188:191]\n" \
"ds_read_b64_tr_b16 a[74:75], v12 offset:49664\n" \
"v_mfma_f32_16x16x32_bf16 v[64:67], a[96:99], a[200:203], v[64:67]\n" \
"ds_read_b64_tr_b16 a[76:77], v11 offset:57856\n" \
"v_mfma_f32_16x16x32_bf16 v[64:67], a[100:103], a[204:207], v[64:67]\n" \
"ds_read_b64_tr_b16 a[78:79], v12 offset:57856\n" \
"v_mfma_f32_16x16x32_bf16 v[72:75], a[96:99], a[208:211], v[72:75]\n" \
"s_add_u32 m0, s50, 40960\n" \
"ds_read_b64_tr_b16 a[80:81], v9 offset:50176\n" \
"v_mfma_f32_16x16x32_bf16 v[72:75], a[100:103], a[212:215], v[72:75]\n" \
"buffer_load_dwordx4 v15, s[28:31], s46 offen lds\n" \
"v_mfma_f32_16x16x32_bf16 v[80:83], a[96:99], a[216:219], v[80:83]\n" \
"ds_read_b64_tr_b16 a[82:83], v10 offset:50176\n" \
"v_mfma_f32_16x16x32_bf16 v[80:83], a[100:103], a[220:223], v[80:83]\n" \
"ds_read_b64_tr_b16 a[84:85], v9 offset:58368\n" \
"v_mfma_f32_16x16x32_bf16 v[88:91], a[96:99], a[224:227], v[88:91]\n" \
"ds_read_b64_tr_b16 a[86:87], v10 offset:58368\n" \
"v_mfma_f32_16x16x32_bf16 v[88:91], a[100:103], a[228:231], v[88:91]\n" \
"ds_read_b64_tr_b16 a[88:89], v11 offset:50176\n" \
"v_mfma_f32_16x16x32_bf16 v[68:71], a[104:107], a[200:203], v[68:71]\n" \
"ds_read_b64_tr_b16 a[90:91], v12 offset:50176\n" \
"v_mfma_f32_16x16x32_bf16 v[68:71], a[108:111], a[204:207], v[68:71]\n" \
"ds_read_b64_tr_b16 a[92:93], v11 offset:58368\n" \
"v_mfma_f32_16x16x32_bf16 v[76:79], a[104:107], a[208:211], v[76:79]\n" \
"s_add_u32 m0, s50, 45056\n" \
"ds_read_b64_tr_b16 a[94:95], v12 offset:58368\n" \
"v_mfma_f32_16x16x32_bf16 v[76:79], a[108:111], a[212:215], v[76:79]\n" \
"buffer_load_dwordx4 v16, s[28:31], s46 offen lds\n" \
"v_mfma_f32_16x16x32_bf16 v[84:87], a[104:107], a[216:219], v[84:87]\n" \
"v_mfma_f32_16x16x32_bf16 v[84:87], a[108:111], a[220:223], v[84:87]\n" \
"v_mfma_f32_16x16x32_bf16 v[92:95], a[104:107], a[224:227], v[92:95]\n" \
"v_mfma_f32_16x16x32_bf16 v[92:95], a[108:111], a[228:231], v[92:95]\n" \
"s_waitcnt lgkmcnt(12)\n" \
"v_mfma_f32_16x16x32_bf16 v[96:99], a[64:67], a[0:3], v[96:99]\n" \
"s_mov_b32 s46, 1408\n" \
"s_add_u32 m0, s50, 49152\n" \
"ds_read_b64_tr_b16 a[96:97], v9 offset:50688\n" \
"v_mfma_f32_16x16x32_bf16 v[96:99], a[68:71], a[4:7], v[96:99]\n" \
"buffer_load_dwordx4 v17, s[28:31], s46 offen lds\n" \
"v_mfma_f32_16x16x32_bf16 v[104:107], a[64:67], a[8:11], v[104:107]\n" \
"s_bfe_u32 s89, %1, 0x20001\n" \
"ds_read_b64_tr_b16 a[98:99], v10 offset:50688\n" \
"s_and_b32 s90, %1, 1\n" \
"v_mfma_f32_16x16x32_bf16 v[104:107], a[68:71], a[12:15], v[104:107]\n" \
"s_lshl_b32 s90, s90, 2\n" \
"ds_read_b64_tr_b16 a[100:101], v9 offset:58880\n" \
"s_bfe_u32 s92, %1, 0x20003\n" \
"v_mfma_f32_16x16x32_bf16 v[112:115], a[64:67], a[16:19], v[112:115]\n" \
"s_add_u32 m0, s50, 53248\n" \
"ds_read_b64_tr_b16 a[102:103], v10 offset:58880\n" \
"s_add_u32 s90, s90, s92\n" \
"v_mfma_f32_16x16x32_bf16 v[112:115], a[68:71], a[20:23], v[112:115]\n" \
"buffer_load_dwordx4 v18, s[28:31], s46 offen lds\n" \
"v_mfma_f32_16x16x32_bf16 v[120:123], a[64:67], a[24:27], v[120:123]\n" \
"s_waitcnt lgkmcnt(12)\n" \
"ds_read_b64_tr_b16 a[104:105], v11 offset:50688\n" \
"s_lshl_b32 s92, %1, 2\n" \
"v_mfma_f32_16x16x32_bf16 v[120:123], a[68:71], a[28:31], v[120:123]\n" \
"s_mov_b64 exec, 0xffff\nglobal_load_dword v1, v2, s[100:101]\ns_mov_b64 exec, -1\n" \
"v_mfma_f32_16x16x32_bf16 v[100:103], a[72:75], a[0:3], v[100:103]\n" \
"s_add_u32 m0, s50, 57344\n" \
"ds_read_b64_tr_b16 a[106:107], v12 offset:50688\n" \
"s_add_u32 s92, s92, %2\n" \
"v_mfma_f32_16x16x32_bf16 v[100:103], a[76:79], a[4:7], v[100:103]\n" \
"buffer_load_dwordx4 v19, s[28:31], s46 offen lds\n" \
"v_mfma_f32_16x16x32_bf16 v[108:111], a[72:75], a[8:11], v[108:111]\n" \
"s_mul_i32 s93, s92, 12288\n" \
"ds_read_b64_tr_b16 a[108:109], v11 offset:58880\n" \
"s_add_u32 s78, s93, 109051904\n" \
"v_mfma_f32_16x16x32_bf16 v[108:111], a[76:79], a[12:15], v[108:111]\n" \
"s_lshl_b32 s88, s92, 3\n" \
"ds_read_b64_tr_b16 a[110:111], v12 offset:58880\n" \
"s_add_u32 s88, s88, 94404608\n" \
"v_mfma_f32_16x16x32_bf16 v[116:119], a[72:75], a[16:19], v[116:119]\n" \
"s_add_u32 m0, s50, 61440\n" \
"ds_read_b64_tr_b16 a[64:65], v9 offset:49152\n" \
"v_mfma_f32_16x16x32_bf16 v[116:119], a[76:79], a[20:23], v[116:119]\n" \
"buffer_load_dwordx4 v20, s[28:31], s46 offen lds\n" \
"v_mfma_f32_16x16x32_bf16 v[124:127], a[72:75], a[24:27], v[124:127]\n" \
"s_waitcnt lgkmcnt(13)\n" \
"ds_read_b64_tr_b16 a[66:67], v10 offset:49152\n" \
"v_mfma_f32_16x16x32_bf16 v[124:127], a[76:79], a[28:31], v[124:127]\n" \
"s_cmp_eq_u32 s92, 0\ns_cselect_b32 s12, 94371840, -1\n" \
"ds_read_b64_tr_b16 a[68:69], v9 offset:57344\n" \
"v_mfma_f32_16x16x32_bf16 v[128:131], a[80:83], a[0:3], v[128:131]\n" \
"s_mov_b32 s47, 6291456\n" \
"s_add_u32 m0, s51, 32768\n" \
"ds_read_b64_tr_b16 a[70:71], v10 offset:57344\n" \
"v_mfma_f32_16x16x32_bf16 v[128:131], a[84:87], a[4:7], v[128:131]\n" \
"buffer_load_dwordx4 v21, s[32:35], s47 offen offset:0 lds\n" \
"v_mfma_f32_16x16x32_bf16 v[136:139], a[80:83], a[8:11], v[136:139]\n" \
"s_mul_i32 s80, s90, 12\n" \
"ds_read_b64_tr_b16 a[72:73], v11 offset:49152\n" \
"s_add_u32 s80, s80, s89\n" \
"v_mfma_f32_16x16x32_bf16 v[136:139], a[84:87], a[12:15], v[136:139]\n" \
"s_lshl_b32 s80, s80, 16\n" \
"s_add_u32 s92, %2, 0\n" \
"s_mul_i32 s93, s92, 43\n" \
"v_mfma_f32_16x16x32_bf16 v[144:147], a[80:83], a[16:19], v[144:147]\n" \
"s_add_u32 m0, s51, 33664\n" \
"ds_read_b64_tr_b16 a[74:75], v12 offset:49152\n" \
"s_lshr_b32 s93, s93, 7\n" \
"v_mfma_f32_16x16x32_bf16 v[144:147], a[84:87], a[20:23], v[144:147]\n" \
"buffer_load_dwordx4 v21, s[32:35], s47 offen offset:128 lds\n" \
"v_mfma_f32_16x16x32_bf16 v[152:155], a[80:83], a[24:27], v[152:155]\n" \
"s_waitcnt lgkmcnt(14)\n" \
"ds_read_b64_tr_b16 a[76:77], v11 offset:57344\n" \
"s_lshl_b32 s93, s93, 12\n" \
"v_mfma_f32_16x16x32_bf16 v[152:155], a[84:87], a[28:31], v[152:155]\n" \
"LH12_%=:\ns_cbranch_execz LHR0_%=\n" \
"s_add_u32 s64, s80, s93\n" \
"ds_read_b64_tr_b16 a[78:79], v12 offset:57344\n" \
"s_add_u32 s92, s92, s89\n" \
"v_mfma_f32_16x16x32_bf16 v[132:135], a[88:91], a[0:3], v[132:135]\n" \
"s_mov_b32 s47, 6553600\n" \
"s_add_u32 m0, s51, 40960\n" \
"s_add_u32 s92, s92, 1\n" \
"v_mfma_f32_16x16x32_bf16 v[132:135], a[92:95], a[4:7], v[132:135]\n" \
"buffer_load_dwordx4 v21, s[32:35], s47 offen offset:0 lds\n" \
"v_mfma_f32_16x16x32_bf16 v[140:143], a[88:91], a[8:11], v[140:143]\n" \
"s_mul_i32 s93, s92, 43\n" \
"s_lshr_b32 s93, s93, 7\n" \
"s_mul_i32 s93, s93, 3\n" \
"v_mfma_f32_16x16x32_bf16 v[140:143], a[92:95], a[12:15], v[140:143]\n" \
"s_sub_u32 s93, s92, s93\n" \
"s_lshl_b32 s93, s93, 18\n" \
"s_add_u32 s64, s64, s93\n" \
"v_mfma_f32_16x16x32_bf16 v[148:151], a[88:91], a[16:19], v[148:151]\n" \
"s_add_u32 m0, s51, 41856\n" \
"s_add_u32 s92, %2, 4\n" \
"s_mul_i32 s93, s92, 43\n" \
"v_mfma_f32_16x16x32_bf16 v[148:151], a[92:95], a[20:23], v[148:151]\n" \
"buffer_load_dwordx4 v21, s[32:35], s47 offen offset:128 lds\n" \
"v_mfma_f32_16x16x32_bf16 v[156:159], a[88:91], a[24:27], v[156:159]\n" \
"s_waitcnt vmcnt(17) lgkmcnt(0)\n" \
"s_lshr_b32 s93, s93, 7\n" \
"s_lshl_b32 s93, s93, 12\n" \
"v_mfma_f32_16x16x32_bf16 v[156:159], a[92:95], a[28:31], v[156:159]\n" \
"s_barrier\n" \
"s_add_u32 s65, s80, s93\n" \
"s_add_u32 s92, s92, s89\n" \
"v_mfma_f32_16x16x32_bf16 v[160:163], a[96:99], a[0:3], v[160:163]\n" \
"s_add_u32 s92, s92, 1\n" \
"ds_read_b64_tr_b16 a[80:81], v9 offset:512\n" \
"s_mul_i32 s93, s92, 43\n" \
"v_mfma_f32_16x16x32_bf16 v[160:163], a[100:103], a[4:7], v[160:163]\n" \
"s_lshr_b32 s93, s93, 7\n" \
"ds_read_b64_tr_b16 a[82:83], v10 offset:512\n" \
"s_mul_i32 s93, s93, 3\n" \
"v_mfma_f32_16x16x32_bf16 v[168:171], a[96:99], a[8:11], v[168:171]\n" \
"s_mov_b32 s46, 1536\n" \
"s_add_u32 m0, s50, 65536\n" \
"ds_read_b64_tr_b16 a[84:85], v9 offset:8704\n" \
"v_mov_b32 v58, s14\n" \
"v_mfma_f32_16x16x32_bf16 v[168:171], a[100:103], a[12:15], v[168:171]\n" \
"buffer_load_dwordx4 v13, s[28:31], s46 offen lds\n" \
"v_mov_b32 v59, s15\n" \
"v_mfma_f32_16x16x32_bf16 v[176:179], a[96:99], a[16:19], v[176:179]\n" \
"s_sub_u32 s93, s92, s93\n" \
"ds_read_b64_tr_b16 a[86:87], v10 offset:8704\n" \
"s_lshl_b32 s93, s93, 18\n" \
"v_mfma_f32_16x16x32_bf16 v[176:179], a[100:103], a[20:23], v[176:179]\n" \
"s_add_u32 s65, s65, s93\n" \
"ds_read_b128 a[200:203], v5 offset:0\n" \
"s_add_u32 s92, %2, 8\n" \
"v_mfma_f32_16x16x32_bf16 v[184:187], a[96:99], a[24:27], v[184:187]\n" \
"s_mul_i32 s93, s92, 43\n" \
"ds_read_b128 a[204:207], v6 offset:0\n" \
"s_lshr_b32 s93, s93, 7\n" \
"v_mfma_f32_16x16x32_bf16 v[184:187], a[100:103], a[28:31], v[184:187]\n" \
"s_lshl_b32 s93, s93, 12\n" \
"ds_read_b128 a[208:211], v5 offset:2048\n" \
"s_add_u32 s66, s80, s93\n" \
"v_mfma_f32_16x16x32_bf16 v[164:167], a[104:107], a[0:3], v[164:167]\n" \
"s_add_u32 s92, s92, s89\n" \
"ds_read_b128 a[212:215], v6 offset:2048\n" \
"s_add_u32 s92, s92, 1\n" \
"v_mfma_f32_16x16x32_bf16 v[164:167], a[108:111], a[4:7], v[164:167]\n" \
"s_mul_i32 s93, s92, 43\n" \
"ds_read_b128 a[216:219], v5 offset:4096\n" \
"s_lshr_b32 s93, s93, 7\n" \
"v_mfma_f32_16x16x32_bf16 v[172:175], a[104:107], a[8:11], v[172:175]\n" \
"s_add_u32 m0, s50, 69632\n" \
"ds_read_b128 a[220:223], v6 offset:4096\n" \
"s_mul_i32 s93, s93, 3\n" \
"v_mfma_f32_16x16x32_bf16 v[172:175], a[108:111], a[12:15], v[172:175]\n" \
"buffer_load_dwordx4 v14, s[28:31], s46 offen lds\n" \
"v_mfma_f32_16x16x32_bf16 v[180:183], a[104:107], a[16:19], v[180:183]\n" \
"s_sub_u32 s93, s92, s93\n" \
"ds_read_b128 a[224:227], v5 offset:6144\n" \
"s_lshl_b32 s93, s93, 18\n" \
"v_mfma_f32_16x16x32_bf16 v[180:183], a[108:111], a[20:23], v[180:183]\n" \
"s_add_u32 s66, s66, s93\n" \
"ds_read_b128 a[228:231], v6 offset:6144\n" \
"s_and_b32 s92, %2, 1\n" \
"v_mfma_f32_16x16x32_bf16 v[188:191], a[104:107], a[24:27], v[188:191]\n" \
"s_lshl_b32 s92, s92, 12\n" \
"ds_read_b64_tr_b16 a[88:89], v11 offset:512\n" \
"s_sub_u32 s57, s66, s64\n" \
"v_mfma_f32_16x16x32_bf16 v[188:191], a[108:111], a[28:31], v[188:191]\n" \
"s_sub_u32 s56, s64, s57\n" \
"ds_read_b64_tr_b16 a[90:91], v12 offset:512\n" \
"s_sub_u32 s56, s56, s92\n" \
"v_mfma_f32_16x16x32_bf16 v[64:67], a[64:67], a[0:3], v[64:67]\n" \
"s_sub_u32 s59, s64, s65\n" \
"ds_read_b64_tr_b16 a[92:93], v11 offset:8704\n" \
"s_add_u32 s59, s59, 16384\n" \
"v_mfma_f32_16x16x32_bf16 v[64:67], a[68:71], a[4:7], v[64:67]\n" \
"s_sub_u32 s58, s65, s59\n" \
"ds_read_b64_tr_b16 a[94:95], v12 offset:8704\n" \
"s_sub_u32 s58, s58, s92\n" \
"v_mfma_f32_16x16x32_bf16 v[72:75], a[64:67], a[8:11], v[72:75]\n" \
"s_add_u32 m0, s50, 73728\n" \
"ds_read_b64_tr_b16 a[96:97], v9 offset:1024\n" \
"s_lshl_b32 s92, s90, 18\n" \
"v_mfma_f32_16x16x32_bf16 v[72:75], a[68:71], a[12:15], v[72:75]\n" \
"buffer_load_dwordx4 v15, s[28:31], s46 offen lds\n" \
"v_mfma_f32_16x16x32_bf16 v[80:83], a[64:67], a[16:19], v[80:83]\n" \
"s_lshl_b32 s93, s89, 16\n" \
"ds_read_b64_tr_b16 a[98:99], v10 offset:1024\n" \
"s_add_u32 s67, s92, s93\n" \
"v_mfma_f32_16x16x32_bf16 v[80:83], a[68:71], a[20:23], v[80:83]\n" \
"s_add_u32 s67, s67, 6291456\n" \
"ds_read_b64_tr_b16 a[100:101], v9 offset:9216\n" \
"s_lshl_b32 s92, s90, 20\n" \
"v_mfma_f32_16x16x32_bf16 v[88:91], a[64:67], a[24:27], v[88:91]\n" \
"s_add_u32 s94, s92, s93\n" \
"ds_read_b64_tr_b16 a[102:103], v10 offset:9216\n" \
"s_add_u32 s94, s94, 8388608\n" \
"v_mfma_f32_16x16x32_bf16 v[88:91], a[68:71], a[28:31], v[88:91]\n" \
"ds_read_b64_tr_b16 a[104:105], v11 offset:1024\n" \
"buffer_store_dwordx2 v[58:59], v56, s[36:39], s24 offen sc1\n" \
"v_mfma_f32_16x16x32_bf16 v[68:71], a[72:75], a[0:3], v[68:71]\n" \
"s_add_u32 s95, s89, 1\n" \
"ds_read_b64_tr_b16 a[106:107], v12 offset:1024\n" \
"s_and_b32 s95, s95, 3\n" \
"v_mfma_f32_16x16x32_bf16 v[68:71], a[76:79], a[4:7], v[68:71]\n" \
"s_lshl_b32 s92, s95, 18\n" \
"ds_read_b64_tr_b16 a[108:109], v11 offset:9216\n" \
"s_add_u32 s68, s94, s92\n" \
"v_mfma_f32_16x16x32_bf16 v[76:79], a[72:75], a[8:11], v[76:79]\n" \
"s_add_u32 m0, s50, 77824\n" \
"ds_read_b64_tr_b16 a[110:111], v12 offset:9216\n" \
"s_sub_u32 s92, s95, s89\n" \
"v_mfma_f32_16x16x32_bf16 v[76:79], a[76:79], a[12:15], v[76:79]\n" \
"buffer_load_dwordx4 v16, s[28:31], s46 offen lds\n" \
"v_mfma_f32_16x16x32_bf16 v[84:87], a[72:75], a[16:19], v[84:87]\n" \
"s_mul_i32 s93, s92, 98304\n" \
"s_add_u32 s75, s78, s93\n" \
"s_add_u32 s75, s75, 6144\n" \
"v_mfma_f32_16x16x32_bf16 v[84:87], a[76:79], a[20:23], v[84:87]\n" \
"s_add_u32 s95, s89, 2\n" \
"s_and_b32 s95, s95, 3\n" \
"s_lshl_b32 s92, s95, 18\n" \
"v_mfma_f32_16x16x32_bf16 v[92:95], a[72:75], a[24:27], v[92:95]\n" \
"s_add_u32 s69, s94, s92\n" \
"s_sub_u32 s92, s95, s89\n" \
"s_mul_i32 s93, s92, 98304\n" \
"v_mfma_f32_16x16x32_bf16 v[92:95], a[76:79], a[28:31], v[92:95]\n" \
"s_waitcnt lgkmcnt(12)\n" \
"s_add_u32 s76, s78, s93\n" \
"s_add_u32 s76, s76, 8192\n" \
"v_mfma_f32_16x16x32_bf16 v[96:99], a[80:83], a[200:203], v[96:99]\n" \
"s_mov_b32 s46, 1536\n" \
"s_add_u32 m0, s50, 81920\n" \
"ds_read_b64_tr_b16 a[64:65], v9 offset:1536\n" \
"v_mfma_f32_16x16x32_bf16 v[96:99], a[84:87], a[204:207], v[96:99]\n" \
"buffer_load_dwordx4 v17, s[28:31], s46 offen lds\n" \
"v_mfma_f32_16x16x32_bf16 v[104:107], a[80:83], a[208:211], v[104:107]\n" \
"s_add_u32 s95, s89, 3\n" \
"ds_read_b64_tr_b16 a[66:67], v10 offset:1536\n" \
"s_and_b32 s95, s95, 3\n" \
"v_mfma_f32_16x16x32_bf16 v[104:107], a[84:87], a[212:215], v[104:107]\n" \
"s_lshl_b32 s92, s95, 18\n" \
"ds_read_b64_tr_b16 a[68:69], v9 offset:9728\n" \
"s_add_u32 s70, s94, s92\n" \
"v_mfma_f32_16x16x32_bf16 v[112:115], a[80:83], a[216:219], v[112:115]\n" \
"s_add_u32 m0, s50, 86016\n" \
"ds_read_b64_tr_b16 a[70:71], v10 offset:9728\n" \
"s_sub_u32 s92, s95, s89\n" \
"v_mfma_f32_16x16x32_bf16 v[112:115], a[84:87], a[220:223], v[112:115]\n" \
"buffer_load_dwordx4 v18, s[28:31], s46 offen lds\n" \
"v_mfma_f32_16x16x32_bf16 v[120:123], a[80:83], a[224:227], v[120:123]\n" \
"s_waitcnt lgkmcnt(12)\n" \
"ds_read_b64_tr_b16 a[72:73], v11 offset:1536\n" \
"s_mul_i32 s93, s92, 98304\n" \
"v_mfma_f32_16x16x32_bf16 v[120:123], a[84:87], a[228:231], v[120:123]\n" \
"s_add_u32 s77, s78, s93\n" \
"s_add_u32 s77, s77, 10240\n" \
"s_add_u32 s95, s89, 4\n" \
"v_mfma_f32_16x16x32_bf16 v[100:103], a[88:91], a[200:203], v[100:103]\n" \
"s_add_u32 m0, s50, 90112\n" \
"ds_read_b64_tr_b16 a[74:75], v12 offset:1536\n" \
"s_and_b32 s95, s95, 3\n" \
"v_mfma_f32_16x16x32_bf16 v[100:103], a[92:95], a[204:207], v[100:103]\n" \
"buffer_load_dwordx4 v19, s[28:31], s46 offen lds\n" \
"v_mfma_f32_16x16x32_bf16 v[108:111], a[88:91], a[208:211], v[108:111]\n" \
"s_lshl_b32 s92, s95, 18\n" \
"ds_read_b64_tr_b16 a[76:77], v11 offset:9728\n" \
"s_add_u32 s71, s94, s92\n" \
"v_mfma_f32_16x16x32_bf16 v[108:111], a[92:95], a[212:215], v[108:111]\n" \
"s_add_u32 s92, s89, 1\n" \
"ds_read_b64_tr_b16 a[78:79], v12 offset:9728\n" \
"s_mul_i32 s95, s92, 43\n" \
"v_mfma_f32_16x16x32_bf16 v[116:119], a[88:91], a[216:219], v[116:119]\n" \
"s_add_u32 m0, s50, 94208\n" \
"ds_read_b64_tr_b16 a[80:81], v9 offset:0\n" \
"s_lshr_b32 s95, s95, 7\n" \
"v_mfma_f32_16x16x32_bf16 v[116:119], a[92:95], a[220:223], v[116:119]\n" \
"buffer_load_dwordx4 v20, s[28:31], s46 offen lds\n" \
"v_mfma_f32_16x16x32_bf16 v[124:127], a[88:91], a[224:227], v[124:127]\n" \
"s_waitcnt lgkmcnt(13)\n" \
"ds_read_b64_tr_b16 a[82:83], v10 offset:0\n" \
"s_mul_i32 s95, s95, 3\n" \
"v_mfma_f32_16x16x32_bf16 v[124:127], a[92:95], a[228:231], v[124:127]\n" \
"s_sub_u32 s95, s92, s95\n" \
"ds_read_b64_tr_b16 a[84:85], v9 offset:8192\n" \
"s_sub_u32 s92, s95, s89\n" \
"v_mfma_f32_16x16x32_bf16 v[128:131], a[96:99], a[200:203], v[128:131]\n" \
"s_mov_b32 s47, 6815744\n" \
"s_add_u32 m0, s51, 49152\n" \
"ds_read_b64_tr_b16 a[86:87], v10 offset:8192\n" \
"v_mfma_f32_16x16x32_bf16 v[128:131], a[100:103], a[204:207], v[128:131]\n" \
"buffer_load_dwordx4 v21, s[32:35], s47 offen offset:0 lds\n" \
"v_mfma_f32_16x16x32_bf16 v[136:139], a[96:99], a[208:211], v[136:139]\n" \
"s_mul_i32 s93, s92, 98304\n" \
"ds_read_b64_tr_b16 a[88:89], v11 offset:0\n" \
"s_add_u32 s72, s78, s93\n" \
"v_mfma_f32_16x16x32_bf16 v[136:139], a[100:103], a[212:215], v[136:139]\n" \
"s_sub_u32 s92, s92, 1\n" \
"s_and_b32 s92, s92, 3\n" \
"s_lshl_b32 s92, s92, 11\n" \
"v_mfma_f32_16x16x32_bf16 v[144:147], a[96:99], a[216:219], v[144:147]\n" \
"s_add_u32 m0, s51, 50048\n" \
"ds_read_b64_tr_b16 a[90:91], v12 offset:0\n" \
"s_add_u32 s72, s72, s92\n" \
"v_mfma_f32_16x16x32_bf16 v[144:147], a[100:103], a[220:223], v[144:147]\n" \
"buffer_load_dwordx4 v21, s[32:35], s47 offen offset:128 lds\n" \
"v_mfma_f32_16x16x32_bf16 v[152:155], a[96:99], a[224:227], v[152:155]\n" \
"s_waitcnt lgkmcnt(14)\n" \
"ds_read_b64_tr_b16 a[92:93], v11 offset:8192\n" \
"s_add_u32 s92, s89, 2\n" \
"v_mfma_f32_16x16x32_bf16 v[152:155], a[100:103], a[228:231], v[152:155]\n" \
"s_mul_i32 s95, s92, 43\n" \
"ds_read_b64_tr_b16 a[94:95], v12 offset:8192\n" \
"s_lshr_b32 s95, s95, 7\n" \
"v_mfma_f32_16x16x32_bf16 v[132:135], a[104:107], a[200:203], v[132:135]\n" \
"s_mov_b32 s47, 7077888\n" \
"s_add_u32 m0, s51, 57344\n" \
"s_mul_i32 s95, s95, 3\n" \
"v_mfma_f32_16x16x32_bf16 v[132:135], a[108:111], a[204:207], v[132:135]\n" \
"buffer_load_dwordx4 v21, s[32:35], s47 offen offset:0 lds\n" \
"v_mfma_f32_16x16x32_bf16 v[140:143], a[104:107], a[208:211], v[140:143]\n" \
"s_sub_u32 s95, s92, s95\n" \
"s_sub_u32 s92, s95, s89\n" \
"s_mul_i32 s93, s92, 98304\n" \
"v_mfma_f32_16x16x32_bf16 v[140:143], a[108:111], a[212:215], v[140:143]\n" \
"s_add_u32 s73, s78, s93\n" \
"s_sub_u32 s92, s92, 1\n" \
"s_and_b32 s92, s92, 3\n" \
"v_mfma_f32_16x16x32_bf16 v[148:151], a[104:107], a[216:219], v[148:151]\n" \
"s_add_u32 m0, s51, 58240\n" \
"s_lshl_b32 s92, s92, 11\n" \
"s_add_u32 s73, s73, s92\n" \
"v_mfma_f32_16x16x32_bf16 v[148:151], a[108:111], a[220:223], v[148:151]\n" \
"buffer_load_dwordx4 v21, s[32:35], s47 offen offset:128 lds\n" \
"v_mfma_f32_16x16x32_bf16 v[156:159], a[104:107], a[224:227], v[156:159]\n" \
"s_waitcnt vmcnt(17) lgkmcnt(0)\n" \
"s_cmp_lt_u32 s89, 3\ns_cselect_b32 s95, 3, 0\n" \
"v_mfma_f32_16x16x32_bf16 v[156:159], a[108:111], a[228:231], v[156:159]\n" \
"s_barrier\n" \
"s_sub_u32 s92, s95, s89\n" \
"s_mul_i32 s93, s92, 98304\n" \
"v_mfma_f32_16x16x32_bf16 v[160:163], a[64:67], a[200:203], v[160:163]\n" \
"s_add_u32 s74, s78, s93\n" \
"ds_read_b64_tr_b16 a[96:97], v9 offset:16896\n" \
"s_sub_u32 s92, s92, 1\n" \
"v_mfma_f32_16x16x32_bf16 v[160:163], a[68:71], a[204:207], v[160:163]\n" \
"s_and_b32 s92, s92, 3\n" \
"ds_read_b64_tr_b16 a[98:99], v10 offset:16896\n" \
"s_lshl_b32 s92, s92, 11\n" \
"v_mfma_f32_16x16x32_bf16 v[168:171], a[64:67], a[208:211], v[168:171]\n" \
"s_mov_b32 s46, 1664\n" \
"s_add_u32 m0, s50, 0\n" \
"ds_read_b64_tr_b16 a[100:101], v9 offset:25088\n" \
"v_mfma_f32_16x16x32_bf16 v[168:171], a[68:71], a[212:215], v[168:171]\n" \
"buffer_load_dwordx4 v13, s[28:31], s46 offen lds\n" \
"v_mfma_f32_16x16x32_bf16 v[176:179], a[64:67], a[216:219], v[176:179]\n" \
"s_add_u32 s74, s74, s92\n" \
"ds_read_b64_tr_b16 a[102:103], v10 offset:25088\n" \
"s_mul_i32 s96, s90, 3\n" \
"v_mfma_f32_16x16x32_bf16 v[176:179], a[68:71], a[220:223], v[176:179]\n" \
"s_add_u32 s96, s96, s89\n" \
"ds_read_b128 a[0:3], v5 offset:32768\n" \
"s_add_u32 s92, s90, 24\n" \
"v_mfma_f32_16x16x32_bf16 v[184:187], a[64:67], a[224:227], v[184:187]\n" \
"s_cmp_lt_u32 s89, 3\ns_cselect_b32 s96, s96, s92\n" \
"ds_read_b128 a[4:7], v6 offset:32768\n" \
"v_mfma_f32_16x16x32_bf16 v[184:187], a[68:71], a[228:231], v[184:187]\n" \
"s_lshl_b32 s96, s96, 5\n" \
"ds_read_b128 a[8:11], v5 offset:34816\n" \
"s_lshl_b32 s97, s90, 2\n" \
"v_mfma_f32_16x16x32_bf16 v[164:167], a[72:75], a[200:203], v[164:167]\n" \
"s_add_u32 s97, s97, s89\n" \
"ds_read_b128 a[12:15], v6 offset:34816\n" \
"s_add_u32 s97, s97, 32\n" \
"v_mfma_f32_16x16x32_bf16 v[164:167], a[76:79], a[204:207], v[164:167]\n" \
"s_lshl_b32 s97, s97, 5\n" \
"ds_read_b128 a[16:19], v5 offset:36864\n" \
"s_lshr_b32 s92, %1, 5\n" \
"v_mfma_f32_16x16x32_bf16 v[172:175], a[72:75], a[208:211], v[172:175]\n" \
"s_add_u32 m0, s50, 4096\n" \
"ds_read_b128 a[20:23], v6 offset:36864\n" \
"s_lshl_b32 s92, s92, 2\n" \
"v_mfma_f32_16x16x32_bf16 v[172:175], a[76:79], a[212:215], v[172:175]\n" \
"buffer_load_dwordx4 v14, s[28:31], s46 offen lds\n" \
"v_mfma_f32_16x16x32_bf16 v[180:183], a[72:75], a[216:219], v[180:183]\n" \
"s_add_u32 s92, s92, %2\n" \
"ds_read_b128 a[24:27], v5 offset:38912\n" \
"s_lshl_b32 s93, s92, 6\n" \
"v_mfma_f32_16x16x32_bf16 v[180:183], a[76:79], a[220:223], v[180:183]\n" \
"s_lshl_b32 s92, s92, 17\n" \
"ds_read_b128 a[28:31], v6 offset:38912\n" \
"s_add_u32 s92, s92, 67108864\n" \
"v_mfma_f32_16x16x32_bf16 v[188:191], a[72:75], a[224:227], v[188:191]\n" \
"s_lshl_b32 s82, s96, 11\n" \
"ds_read_b64_tr_b16 a[104:105], v11 offset:16896\n" \
"s_add_u32 s82, s82, s93\n" \
"v_mfma_f32_16x16x32_bf16 v[188:191], a[76:79], a[228:231], v[188:191]\n" \
"s_lshl_b32 s86, s96, 2\n" \
"ds_read_b64_tr_b16 a[106:107], v12 offset:16896\n" \
"s_add_u32 s86, s86, 71303168\n" \
"v_mfma_f32_16x16x32_bf16 v[64:67], a[80:83], a[200:203], v[64:67]\n" \
"s_lshl_b32 s84, s96, 3\n" \
"ds_read_b64_tr_b16 a[108:109], v11 offset:25088\n" \
"s_add_u32 s84, s84, s92\n" \
"v_mfma_f32_16x16x32_bf16 v[64:67], a[84:87], a[204:207], v[64:67]\n" \
"s_lshl_b32 s83, s97, 11\n" \
"ds_read_b64_tr_b16 a[110:111], v12 offset:25088\n" \
"s_add_u32 s83, s83, s93\n" \
"v_mfma_f32_16x16x32_bf16 v[72:75], a[80:83], a[208:211], v[72:75]\n" \
"s_add_u32 m0, s50, 8192\n" \
"ds_read_b64_tr_b16 a[64:65], v9 offset:17408\n" \
"s_lshl_b32 s87, s97, 2\n" \
"v_mfma_f32_16x16x32_bf16 v[72:75], a[84:87], a[212:215], v[72:75]\n" \
"buffer_load_dwordx4 v15, s[28:31], s46 offen lds\n" \
"v_mfma_f32_16x16x32_bf16 v[80:83], a[80:83], a[216:219], v[80:83]\n" \
"s_add_u32 s87, s87, 71303168\n" \
"ds_read_b64_tr_b16 a[66:67], v10 offset:17408\n" \
"s_lshl_b32 s85, s97, 3\n" \
"v_mfma_f32_16x16x32_bf16 v[80:83], a[84:87], a[220:223], v[80:83]\n" \
"s_add_u32 s85, s85, s92\n" \
"ds_read_b64_tr_b16 a[68:69], v9 offset:25600\n" \
"s_lshl_b32 s93, s89, 6\n" \
"v_mfma_f32_16x16x32_bf16 v[88:91], a[80:83], a[224:227], v[88:91]\n" \
"s_add_u32 s93, s93, 94388224\n" \
"ds_read_b64_tr_b16 a[70:71], v10 offset:25600\n" \
"s_lshl_b32 s92, s90, 8\n" \
"v_mfma_f32_16x16x32_bf16 v[88:91], a[84:87], a[228:231], v[88:91]\n" \
"s_add_u32 s10, s93, s92\n" \
"ds_read_b64_tr_b16 a[72:73], v11 offset:17408\n" \
"s_add_u32 s10, s10, 6144\n" \
"v_mfma_f32_16x16x32_bf16 v[68:71], a[88:91], a[200:203], v[68:71]\n" \
"s_lshl_b32 s92, s90, 10\n" \
"ds_read_b64_tr_b16 a[74:75], v12 offset:17408\n" \
"s_add_u32 s11, s93, s92\n" \
"v_mfma_f32_16x16x32_bf16 v[68:71], a[92:95], a[204:207], v[68:71]\n" \
"s_add_u32 s11, s11, 8192\n" \
"ds_read_b64_tr_b16 a[76:77], v11 offset:25600\n" \
"s_mul_i32 s92, s90, 768\n" \
"v_mfma_f32_16x16x32_bf16 v[76:79], a[88:91], a[208:211], v[76:79]\n" \
"s_add_u32 m0, s50, 12288\n" \
"ds_read_b64_tr_b16 a[78:79], v12 offset:25600\n" \
"s_add_u32 s98, s93, s92\n" \
"v_mfma_f32_16x16x32_bf16 v[76:79], a[92:95], a[212:215], v[76:79]\n" \
"buffer_load_dwordx4 v16, s[28:31], s46 offen lds\n" \
"v_mfma_f32_16x16x32_bf16 v[84:87], a[88:91], a[216:219], v[84:87]\n" \
"v_mfma_f32_16x16x32_bf16 v[84:87], a[92:95], a[220:223], v[84:87]\n" \
"v_mfma_f32_16x16x32_bf16 v[92:95], a[88:91], a[224:227], v[92:95]\n" \
"v_mfma_f32_16x16x32_bf16 v[92:95], a[92:95], a[228:231], v[92:95]\n" \
"s_waitcnt lgkmcnt(12)\n" \
"v_mfma_f32_16x16x32_bf16 v[96:99], a[96:99], a[0:3], v[96:99]\n" \
"s_mov_b32 s46, 1664\n" \
"s_add_u32 m0, s50, 16384\n" \
"ds_read_b64_tr_b16 a[80:81], v9 offset:17920\n" \
"v_mfma_f32_16x16x32_bf16 v[96:99], a[100:103], a[4:7], v[96:99]\n" \
"buffer_load_dwordx4 v17, s[28:31], s46 offen lds\n" \
"v_mfma_f32_16x16x32_bf16 v[104:107], a[96:99], a[8:11], v[104:107]\n" \
"ds_read_b64_tr_b16 a[82:83], v10 offset:17920\n" \
"v_mfma_f32_16x16x32_bf16 v[104:107], a[100:103], a[12:15], v[104:107]\n" \
"ds_read_b64_tr_b16 a[84:85], v9 offset:26112\n" \
"v_mfma_f32_16x16x32_bf16 v[112:115], a[96:99], a[16:19], v[112:115]\n" \
"s_add_u32 m0, s50, 20480\n" \
"ds_read_b64_tr_b16 a[86:87], v10 offset:26112\n" \
"v_mfma_f32_16x16x32_bf16 v[112:115], a[100:103], a[20:23], v[112:115]\n" \
"buffer_load_dwordx4 v18, s[28:31], s46 offen lds\n" \
"v_mfma_f32_16x16x32_bf16 v[120:123], a[96:99], a[24:27], v[120:123]\n" \
"s_waitcnt lgkmcnt(12)\n" \
"ds_read_b64_tr_b16 a[88:89], v11 offset:17920\n" \
"v_mfma_f32_16x16x32_bf16 v[120:123], a[100:103], a[28:31], v[120:123]\n" \
"v_mfma_f32_16x16x32_bf16 v[100:103], a[104:107], a[0:3], v[100:103]\n" \
"s_add_u32 m0, s50, 24576\n" \
"ds_read_b64_tr_b16 a[90:91], v12 offset:17920\n" \
"v_mfma_f32_16x16x32_bf16 v[100:103], a[108:111], a[4:7], v[100:103]\n" \
"buffer_load_dwordx4 v19, s[28:31], s46 offen lds\n" \
"v_mfma_f32_16x16x32_bf16 v[108:111], a[104:107], a[8:11], v[108:111]\n" \
"ds_read_b64_tr_b16 a[92:93], v11 offset:26112\n" \
"v_mfma_f32_16x16x32_bf16 v[108:111], a[108:111], a[12:15], v[108:111]\n" \
"ds_read_b64_tr_b16 a[94:95], v12 offset:26112\n" \
"v_mfma_f32_16x16x32_bf16 v[116:119], a[104:107], a[16:19], v[116:119]\n" \
"s_add_u32 m0, s50, 28672\n" \
"ds_read_b64_tr_b16 a[96:97], v9 offset:16384\n" \
"v_mfma_f32_16x16x32_bf16 v[116:119], a[108:111], a[20:23], v[116:119]\n" \
"buffer_load_dwordx4 v20, s[28:31], s46 offen lds\n" \
"v_mfma_f32_16x16x32_bf16 v[124:127], a[104:107], a[24:27], v[124:127]\n" \
"s_waitcnt lgkmcnt(13)\n" \
"ds_read_b64_tr_b16 a[98:99], v10 offset:16384\n" \
"v_mfma_f32_16x16x32_bf16 v[124:127], a[108:111], a[28:31], v[124:127]\n" \
"ds_read_b64_tr_b16 a[100:101], v9 offset:24576\n" \
"v_mfma_f32_16x16x32_bf16 v[128:131], a[64:67], a[0:3], v[128:131]\n" \
"s_mov_b32 s47, 7340032\n" \
"s_add_u32 m0, s51, 0\n" \
"ds_read_b64_tr_b16 a[102:103], v10 offset:24576\n" \
"v_mfma_f32_16x16x32_bf16 v[128:131], a[68:71], a[4:7], v[128:131]\n" \
"buffer_load_dwordx4 v21, s[32:35], s47 offen offset:0 lds\n" \
"v_mfma_f32_16x16x32_bf16 v[136:139], a[64:67], a[8:11], v[136:139]\n" \
"ds_read_b64_tr_b16 a[104:105], v11 offset:16384\n" \
"v_mfma_f32_16x16x32_bf16 v[136:139], a[68:71], a[12:15], v[136:139]\n" \
"v_mfma_f32_16x16x32_bf16 v[144:147], a[64:67], a[16:19], v[144:147]\n" \
"s_add_u32 m0, s51, 896\n" \
"ds_read_b64_tr_b16 a[106:107], v12 offset:16384\n" \
"v_mfma_f32_16x16x32_bf16 v[144:147], a[68:71], a[20:23], v[144:147]\n" \
"buffer_load_dwordx4 v21, s[32:35], s47 offen offset:128 lds\n" \
"v_mfma_f32_16x16x32_bf16 v[152:155], a[64:67], a[24:27], v[152:155]\n" \
"s_waitcnt lgkmcnt(14)\n" \
"ds_read_b64_tr_b16 a[108:109], v11 offset:24576\n" \
"v_mfma_f32_16x16x32_bf16 v[152:155], a[68:71], a[28:31], v[152:155]\n" \
"ds_read_b64_tr_b16 a[110:111], v12 offset:24576\n" \
"v_mfma_f32_16x16x32_bf16 v[132:135], a[72:75], a[0:3], v[132:135]\n" \
"s_mov_b32 s47, 7602176\n" \
"s_add_u32 m0, s51, 8192\n" \
"v_mfma_f32_16x16x32_bf16 v[132:135], a[76:79], a[4:7], v[132:135]\n" \
"buffer_load_dwordx4 v21, s[32:35], s47 offen offset:0 lds\n" \
"v_mfma_f32_16x16x32_bf16 v[140:143], a[72:75], a[8:11], v[140:143]\n" \
"v_mfma_f32_16x16x32_bf16 v[140:143], a[76:79], a[12:15], v[140:143]\n" \
"v_mfma_f32_16x16x32_bf16 v[148:151], a[72:75], a[16:19], v[148:151]\n" \
"s_add_u32 m0, s51, 9088\n" \
"v_mfma_f32_16x16x32_bf16 v[148:151], a[76:79], a[20:23], v[148:151]\n" \
"buffer_load_dwordx4 v21, s[32:35], s47 offen offset:128 lds\n" \
"v_mfma_f32_16x16x32_bf16 v[156:159], a[72:75], a[24:27], v[156:159]\n" \
"s_waitcnt vmcnt(16) lgkmcnt(0)\n" \
"v_mfma_f32_16x16x32_bf16 v[156:159], a[76:79], a[28:31], v[156:159]\n" \
"s_barrier\n" \
"v_mfma_f32_16x16x32_bf16 v[160:163], a[80:83], a[0:3], v[160:163]\n" \
"ds_read_b64_tr_b16 a[64:65], v9 offset:33280\n" \
"v_mfma_f32_16x16x32_bf16 v[160:163], a[84:87], a[4:7], v[160:163]\n" \
"ds_read_b64_tr_b16 a[66:67], v10 offset:33280\n" \
"v_mfma_f32_16x16x32_bf16 v[168:171], a[80:83], a[8:11], v[168:171]\n" \
"s_mov_b32 s46, 1792\n" \
"s_add_u32 m0, s50, 32768\n" \
"ds_read_b64_tr_b16 a[68:69], v9 offset:41472\n" \
"v_mfma_f32_16x16x32_bf16 v[168:171], a[84:87], a[12:15], v[168:171]\n" \
"buffer_load_dwordx4 v13, s[28:31], s46 offen lds\n" \
"v_mfma_f32_16x16x32_bf16 v[176:179], a[80:83], a[16:19], v[176:179]\n" \
"ds_read_b64_tr_b16 a[70:71], v10 offset:41472\n" \
"v_mfma_f32_16x16x32_bf16 v[176:179], a[84:87], a[20:23], v[176:179]\n" \
"ds_read_b128 a[200:203], v7 offset:0\n" \
"v_mfma_f32_16x16x32_bf16 v[184:187], a[80:83], a[24:27], v[184:187]\n" \
"ds_read_b128 a[204:207], v8 offset:0\n" \
"v_mfma_f32_16x16x32_bf16 v[184:187], a[84:87], a[28:31], v[184:187]\n" \
"ds_read_b128 a[208:211], v7 offset:2048\n" \
"v_mfma_f32_16x16x32_bf16 v[164:167], a[88:91], a[0:3], v[164:167]\n" \
"ds_read_b128 a[212:215], v8 offset:2048\n" \
"v_mfma_f32_16x16x32_bf16 v[164:167], a[92:95], a[4:7], v[164:167]\n" \
"ds_read_b128 a[216:219], v7 offset:4096\n" \
"v_mfma_f32_16x16x32_bf16 v[172:175], a[88:91], a[8:11], v[172:175]\n" \
"s_add_u32 m0, s50, 36864\n" \
"ds_read_b128 a[220:223], v8 offset:4096\n" \
"v_mfma_f32_16x16x32_bf16 v[172:175], a[92:95], a[12:15], v[172:175]\n" \
"buffer_load_dwordx4 v14, s[28:31], s46 offen lds\n" \
"v_mfma_f32_16x16x32_bf16 v[180:183], a[88:91], a[16:19], v[180:183]\n" \
"ds_read_b128 a[224:227], v7 offset:6144\n" \
"v_mfma_f32_16x16x32_bf16 v[180:183], a[92:95], a[20:23], v[180:183]\n" \
"ds_read_b128 a[228:231], v8 offset:6144\n" \
"v_mfma_f32_16x16x32_bf16 v[188:191], a[88:91], a[24:27], v[188:191]\n" \
"ds_read_b64_tr_b16 a[72:73], v11 offset:33280\n" \
"v_mfma_f32_16x16x32_bf16 v[188:191], a[92:95], a[28:31], v[188:191]\n" \
"ds_read_b64_tr_b16 a[74:75], v12 offset:33280\n" \
"v_mfma_f32_16x16x32_bf16 v[64:67], a[96:99], a[0:3], v[64:67]\n" \
"ds_read_b64_tr_b16 a[76:77], v11 offset:41472\n" \
"v_mfma_f32_16x16x32_bf16 v[64:67], a[100:103], a[4:7], v[64:67]\n" \
"ds_read_b64_tr_b16 a[78:79], v12 offset:41472\n" \
"v_mfma_f32_16x16x32_bf16 v[72:75], a[96:99], a[8:11], v[72:75]\n" \
"s_add_u32 m0, s50, 40960\n" \
"ds_read_b64_tr_b16 a[80:81], v9 offset:33792\n" \
"v_mfma_f32_16x16x32_bf16 v[72:75], a[100:103], a[12:15], v[72:75]\n" \
"buffer_load_dwordx4 v15, s[28:31], s46 offen lds\n" \
"v_mfma_f32_16x16x32_bf16 v[80:83], a[96:99], a[16:19], v[80:83]\n" \
"ds_read_b64_tr_b16 a[82:83], v10 offset:33792\n" \
"v_mfma_f32_16x16x32_bf16 v[80:83], a[100:103], a[20:23], v[80:83]\n" \
"ds_read_b64_tr_b16 a[84:85], v9 offset:41984\n" \
"v_mfma_f32_16x16x32_bf16 v[88:91], a[96:99], a[24:27], v[88:91]\n" \
"ds_read_b64_tr_b16 a[86:87], v10 offset:41984\n" \
"v_mfma_f32_16x16x32_bf16 v[88:91], a[100:103], a[28:31], v[88:91]\n" \
"ds_read_b64_tr_b16 a[88:89], v11 offset:33792\n" \
"v_mfma_f32_16x16x32_bf16 v[68:71], a[104:107], a[0:3], v[68:71]\n" \
"ds_read_b64_tr_b16 a[90:91], v12 offset:33792\n" \
"v_mfma_f32_16x16x32_bf16 v[68:71], a[108:111], a[4:7], v[68:71]\n" \
"ds_read_b64_tr_b16 a[92:93], v11 offset:41984\n" \
"v_mfma_f32_16x16x32_bf16 v[76:79], a[104:107], a[8:11], v[76:79]\n" \
"s_add_u32 m0, s50, 45056\n" \
"ds_read_b64_tr_b16 a[94:95], v12 offset:41984\n" \
"v_mfma_f32_16x16x32_bf16 v[76:79], a[108:111], a[12:15], v[76:79]\n" \
"buffer_load_dwordx4 v16, s[28:31], s46 offen lds\n" \
"v_mfma_f32_16x16x32_bf16 v[84:87], a[104:107], a[16:19], v[84:87]\n" \
"v_mfma_f32_16x16x32_bf16 v[84:87], a[108:111], a[20:23], v[84:87]\n" \
"v_mfma_f32_16x16x32_bf16 v[92:95], a[104:107], a[24:27], v[92:95]\n" \
"v_mfma_f32_16x16x32_bf16 v[92:95], a[108:111], a[28:31], v[92:95]\n" \
"s_waitcnt lgkmcnt(12)\n" \
"v_mfma_f32_16x16x32_bf16 v[96:99], a[64:67], a[200:203], v[96:99]\n" \
"s_mov_b32 s46, 1792\n" \
"s_add_u32 m0, s50, 49152\n" \
"ds_read_b64_tr_b16 a[96:97], v9 offset:34304\n" \
"v_mfma_f32_16x16x32_bf16 v[96:99], a[68:71], a[204:207], v[96:99]\n" \
"buffer_load_dwordx4 v17, s[28:31], s46 offen lds\n" \
"v_mfma_f32_16x16x32_bf16 v[104:107], a[64:67], a[208:211], v[104:107]\n" \
"ds_read_b64_tr_b16 a[98:99], v10 offset:34304\n" \
"v_mfma_f32_16x16x32_bf16 v[104:107], a[68:71], a[212:215], v[104:107]\n" \
"ds_read_b64_tr_b16 a[100:101], v9 offset:42496\n" \
"v_mfma_f32_16x16x32_bf16 v[112:115], a[64:67], a[216:219], v[112:115]\n" \
"s_add_u32 m0, s50, 53248\n" \
"ds_read_b64_tr_b16 a[102:103], v10 offset:42496\n" \
"v_mfma_f32_16x16x32_bf16 v[112:115], a[68:71], a[220:223], v[112:115]\n" \
"buffer_load_dwordx4 v18, s[28:31], s46 offen lds\n" \
"v_mfma_f32_16x16x32_bf16 v[120:123], a[64:67], a[224:227], v[120:123]\n" \
"s_waitcnt lgkmcnt(12)\n" \
"ds_read_b64_tr_b16 a[104:105], v11 offset:34304\n" \
"v_mfma_f32_16x16x32_bf16 v[120:123], a[68:71], a[228:231], v[120:123]\n" \
"v_mfma_f32_16x16x32_bf16 v[100:103], a[72:75], a[200:203], v[100:103]\n" \
"s_add_u32 m0, s50, 57344\n" \
"ds_read_b64_tr_b16 a[106:107], v12 offset:34304\n" \
"v_mfma_f32_16x16x32_bf16 v[100:103], a[76:79], a[204:207], v[100:103]\n" \
"buffer_load_dwordx4 v19, s[28:31], s46 offen lds\n" \
"v_mfma_f32_16x16x32_bf16 v[108:111], a[72:75], a[208:211], v[108:111]\n" \
"ds_read_b64_tr_b16 a[108:109], v11 offset:42496\n" \
"v_mfma_f32_16x16x32_bf16 v[108:111], a[76:79], a[212:215], v[108:111]\n" \
"ds_read_b64_tr_b16 a[110:111], v12 offset:42496\n" \
"v_mfma_f32_16x16x32_bf16 v[116:119], a[72:75], a[216:219], v[116:119]\n" \
"s_add_u32 m0, s50, 61440\n" \
"ds_read_b64_tr_b16 a[64:65], v9 offset:32768\n" \
"v_mfma_f32_16x16x32_bf16 v[116:119], a[76:79], a[220:223], v[116:119]\n" \
"buffer_load_dwordx4 v20, s[28:31], s46 offen lds\n" \
"v_mfma_f32_16x16x32_bf16 v[124:127], a[72:75], a[224:227], v[124:127]\n" \
"s_waitcnt lgkmcnt(13)\n" \
"ds_read_b64_tr_b16 a[66:67], v10 offset:32768\n" \
"v_mfma_f32_16x16x32_bf16 v[124:127], a[76:79], a[228:231], v[124:127]\n" \
"ds_read_b64_tr_b16 a[68:69], v9 offset:40960\n" \
"v_mfma_f32_16x16x32_bf16 v[128:131], a[80:83], a[200:203], v[128:131]\n" \
"s_mov_b32 s47, 7864320\n" \
"s_add_u32 m0, s51, 16384\n" \
"ds_read_b64_tr_b16 a[70:71], v10 offset:40960\n" \
"v_mfma_f32_16x16x32_bf16 v[128:131], a[84:87], a[204:207], v[128:131]\n" \
"buffer_load_dwordx4 v21, s[32:35], s47 offen offset:0 lds\n" \
"v_mfma_f32_16x16x32_bf16 v[136:139], a[80:83], a[208:211], v[136:139]\n" \
"ds_read_b64_tr_b16 a[72:73], v11 offset:32768\n" \
"v_mfma_f32_16x16x32_bf16 v[136:139], a[84:87], a[212:215], v[136:139]\n" \
"v_mfma_f32_16x16x32_bf16 v[144:147], a[80:83], a[216:219], v[144:147]\n" \
"s_add_u32 m0, s51, 17280\n" \
"ds_read_b64_tr_b16 a[74:75], v12 offset:32768\n" \
"v_mfma_f32_16x16x32_bf16 v[144:147], a[84:87], a[220:223], v[144:147]\n" \
"buffer_load_dwordx4 v21, s[32:35], s47 offen offset:128 lds\n" \
"v_mfma_f32_16x16x32_bf16 v[152:155], a[80:83], a[224:227], v[152:155]\n" \
"s_waitcnt lgkmcnt(14)\n" \
"ds_read_b64_tr_b16 a[76:77], v11 offset:40960\n" \
"v_mfma_f32_16x16x32_bf16 v[152:155], a[84:87], a[228:231], v[152:155]\n" \
"ds_read_b64_tr_b16 a[78:79], v12 offset:40960\n" \
"v_mfma_f32_16x16x32_bf16 v[132:135], a[88:91], a[200:203], v[132:135]\n" \
"s_mov_b32 s47, 8126464\n" \
"s_add_u32 m0, s51, 24576\n" \
"v_mfma_f32_16x16x32_bf16 v[132:135], a[92:95], a[204:207], v[132:135]\n" \
"buffer_load_dwordx4 v21, s[32:35], s47 offen offset:0 lds\n" \
"v_mfma_f32_16x16x32_bf16 v[140:143], a[88:91], a[208:211], v[140:143]\n" \
"v_mfma_f32_16x16x32_bf16 v[140:143], a[92:95], a[212:215], v[140:143]\n" \
"v_mfma_f32_16x16x32_bf16 v[148:151], a[88:91], a[216:219], v[148:151]\n" \
"s_add_u32 m0, s51, 25472\n" \
"v_mfma_f32_16x16x32_bf16 v[148:151], a[92:95], a[220:223], v[148:151]\n" \
"buffer_load_dwordx4 v21, s[32:35], s47 offen offset:128 lds\n" \
"v_mfma_f32_16x16x32_bf16 v[156:159], a[88:91], a[224:227], v[156:159]\n" \
"s_waitcnt vmcnt(16) lgkmcnt(0)\n" \
"v_mfma_f32_16x16x32_bf16 v[156:159], a[92:95], a[228:231], v[156:159]\n" \
"s_barrier\n" \
"v_mfma_f32_16x16x32_bf16 v[160:163], a[96:99], a[200:203], v[160:163]\n" \
"ds_read_b64_tr_b16 a[80:81], v9 offset:49664\n" \
"v_mfma_f32_16x16x32_bf16 v[160:163], a[100:103], a[204:207], v[160:163]\n" \
"ds_read_b64_tr_b16 a[82:83], v10 offset:49664\n" \
"v_mfma_f32_16x16x32_bf16 v[168:171], a[96:99], a[208:211], v[168:171]\n" \
"s_mov_b32 s46, 1920\n" \
"s_add_u32 m0, s50, 65536\n" \
"ds_read_b64_tr_b16 a[84:85], v9 offset:57856\n" \
"v_mfma_f32_16x16x32_bf16 v[168:171], a[100:103], a[212:215], v[168:171]\n" \
"LH13_%=:\ns_cbranch_execz LHR0_%=\n" \
"buffer_load_dwordx4 v13, s[28:31], s46 offen lds\n" \
"v_mfma_f32_16x16x32_bf16 v[176:179], a[96:99], a[216:219], v[176:179]\n" \
"ds_read_b64_tr_b16 a[86:87], v10 offset:57856\n" \
"v_mfma_f32_16x16x32_bf16 v[176:179], a[100:103], a[220:223], v[176:179]\n" \
"ds_read_b128 a[0:3], v5 offset:0\n" \
"v_mfma_f32_16x16x32_bf16 v[184:187], a[96:99], a[224:227], v[184:187]\n" \
"ds_read_b128 a[4:7], v6 offset:0\n" \
"v_mfma_f32_16x16x32_bf16 v[184:187], a[100:103], a[228:231], v[184:187]\n" \
"ds_read_b128 a[8:11], v5 offset:2048\n" \
"v_mfma_f32_16x16x32_bf16 v[164:167], a[104:107], a[200:203], v[164:167]\n" \
"ds_read_b128 a[12:15], v6 offset:2048\n" \
"v_mfma_f32_16x16x32_bf16 v[164:167], a[108:111], a[204:207], v[164:167]\n" \
"ds_read_b128 a[16:19], v5 offset:4096\n" \
"v_mfma_f32_16x16x32_bf16 v[172:175], a[104:107], a[208:211], v[172:175]\n" \
"s_add_u32 m0, s50, 69632\n" \
"ds_read_b128 a[20:23], v6 offset:4096\n" \
"v_mfma_f32_16x16x32_bf16 v[172:175], a[108:111], a[212:215], v[172:175]\n" \
"buffer_load_dwordx4 v14, s[28:31], s46 offen lds\n" \
"v_mfma_f32_16x16x32_bf16 v[180:183], a[104:107], a[216:219], v[180:183]\n" \
"ds_read_b128 a[24:27], v5 offset:6144\n" \
"v_mfma_f32_16x16x32_bf16 v[180:183], a[108:111], a[220:223], v[180:183]\n" \
"ds_read_b128 a[28:31], v6 offset:6144\n" \
"v_mfma_f32_16x16x32_bf16 v[188:191], a[104:107], a[224:227], v[188:191]\n" \
"ds_read_b64_tr_b16 a[88:89], v11 offset:49664\n" \
"v_mfma_f32_16x16x32_bf16 v[188:191], a[108:111], a[228:231], v[188:191]\n" \
"ds_read_b64_tr_b16 a[90:91], v12 offset:49664\n" \
"v_mfma_f32_16x16x32_bf16 v[64:67], a[64:67], a[200:203], v[64:67]\n" \
"ds_read_b64_tr_b16 a[92:93], v11 offset:57856\n" \
"v_mfma_f32_16x16x32_bf16 v[64:67], a[68:71], a[204:207], v[64:67]\n" \
"ds_read_b64_tr_b16 a[94:95], v12 offset:57856\n" \
"v_mfma_f32_16x16x32_bf16 v[72:75], a[64:67], a[208:211], v[72:75]\n" \
"s_add_u32 m0, s50, 73728\n" \
"ds_read_b64_tr_b16 a[96:97], v9 offset:50176\n" \
"v_mfma_f32_16x16x32_bf16 v[72:75], a[68:71], a[212:215], v[72:75]\n" \
"buffer_load_dwordx4 v15, s[28:31], s46 offen lds\n" \
"v_mfma_f32_16x16x32_bf16 v[80:83], a[64:67], a[216:219], v[80:83]\n" \
"ds_read_b64_tr_b16 a[98:99], v10 offset:50176\n" \
"v_mfma_f32_16x16x32_bf16 v[80:83], a[68:71], a[220:223], v[80:83]\n" \
"ds_read_b64_tr_b16 a[100:101], v9 offset:58368\n" \
"v_mfma_f32_16x16x32_bf16 v[88:91], a[64:67], a[224:227], v[88:91]\n" \
"ds_read_b64_tr_b16 a[102:103], v10 offset:58368\n" \
"v_mfma_f32_16x16x32_bf16 v[88:91], a[68:71], a[228:231], v[88:91]\n" \
"ds_read_b64_tr_b16 a[104:105], v11 offset:50176\n" \
"v_mfma_f32_16x16x32_bf16 v[68:71], a[72:75], a[200:203], v[68:71]\n" \
"ds_read_b64_tr_b16 a[106:107], v12 offset:50176\n" \
"v_mfma_f32_16x16x32_bf16 v[68:71], a[76:79], a[204:207], v[68:71]\n" \
"ds_read_b64_tr_b16 a[108:109], v11 offset:58368\n" \
"v_mfma_f32_16x16x32_bf16 v[76:79], a[72:75], a[208:211], v[76:79]\n" \
"s_add_u32 m0, s50, 77824\n" \
"ds_read_b64_tr_b16 a[110:111], v12 offset:58368\n" \
"v_mfma_f32_16x16x32_bf16 v[76:79], a[76:79], a[212:215], v[76:79]\n" \
"buffer_load_dwordx4 v16, s[28:31], s46 offen lds\n" \
"v_mfma_f32_16x16x32_bf16 v[84:87], a[72:75], a[216:219], v[84:87]\n" \
"v_mfma_f32_16x16x32_bf16 v[84:87], a[76:79], a[220:223], v[84:87]\n" \
"v_mfma_f32_16x16x32_bf16 v[92:95], a[72:75], a[224:227], v[92:95]\n" \
"v_mfma_f32_16x16x32_bf16 v[92:95], a[76:79], a[228:231], v[92:95]\n" \
"s_waitcnt lgkmcnt(12)\n" \
"v_mfma_f32_16x16x32_bf16 v[96:99], a[80:83], a[0:3], v[96:99]\n" \
"ds_read_b64_tr_b16 a[64:65], v9 offset:50688\n" \
"v_mfma_f32_16x16x32_bf16 v[96:99], a[84:87], a[4:7], v[96:99]\n" \
"buffer_load_dwordx2 v[58:59], v57, s[36:39], 0 offen sc1\n" \
"v_mfma_f32_16x16x32_bf16 v[104:107], a[80:83], a[8:11], v[104:107]\n" \
"ds_read_b64_tr_b16 a[66:67], v10 offset:50688\n" \
"v_mfma_f32_16x16x32_bf16 v[104:107], a[84:87], a[12:15], v[104:107]\n" \
"ds_read_b64_tr_b16 a[68:69], v9 offset:58880\n" \
"v_mfma_f32_16x16x32_bf16 v[112:115], a[80:83], a[16:19], v[112:115]\n" \
"s_mov_b32 s46, 1920\n" \
"s_add_u32 m0, s50, 81920\n" \
"ds_read_b64_tr_b16 a[70:71], v10 offset:58880\n" \
"v_mfma_f32_16x16x32_bf16 v[112:115], a[84:87], a[20:23], v[112:115]\n" \
"buffer_load_dwordx4 v17, s[28:31], s46 offen lds\n" \
"v_mfma_f32_16x16x32_bf16 v[120:123], a[80:83], a[24:27], v[120:123]\n" \
"s_waitcnt lgkmcnt(12)\n" \
"ds_read_b64_tr_b16 a[72:73], v11 offset:50688\n" \
"v_mfma_f32_16x16x32_bf16 v[120:123], a[84:87], a[28:31], v[120:123]\n" \
"ds_read_b64_tr_b16 a[74:75], v12 offset:50688\n" \
"v_mfma_f32_16x16x32_bf16 v[100:103], a[88:91], a[0:3], v[100:103]\n" \
"s_add_u32 m0, s50, 86016\n" \
"ds_read_b64_tr_b16 a[76:77], v11 offset:58880\n" \
"v_mfma_f32_16x16x32_bf16 v[100:103], a[92:95], a[4:7], v[100:103]\n" \
"buffer_load_dwordx4 v18, s[28:31], s46 offen lds\n" \
"v_mfma_f32_16x16x32_bf16 v[108:111], a[88:91], a[8:11], v[108:111]\n" \
"ds_read_b64_tr_b16 a[78:79], v12 offset:58880\n" \
"v_mfma_f32_16x16x32_bf16 v[108:111], a[92:95], a[12:15], v[108:111]\n" \
"ds_read_b64_tr_b16 a[80:81], v9 offset:49152\n" \
"v_mfma_f32_16x16x32_bf16 v[116:119], a[88:91], a[16:19], v[116:119]\n" \
"s_add_u32 m0, s50, 90112\n" \
"ds_read_b64_tr_b16 a[82:83], v10 offset:49152\n" \
"v_mfma_f32_16x16x32_bf16 v[116:119], a[92:95], a[20:23], v[116:119]\n" \
"buffer_load_dwordx4 v19, s[28:31], s46 offen lds\n" \
"v_mfma_f32_16x16x32_bf16 v[124:127], a[88:91], a[24:27], v[124:127]\n" \
"s_waitcnt lgkmcnt(14)\n" \
"ds_read_b64_tr_b16 a[84:85], v9 offset:57344\n" \
"v_mfma_f32_16x16x32_bf16 v[124:127], a[92:95], a[28:31], v[124:127]\n" \
"ds_read_b64_tr_b16 a[86:87], v10 offset:57344\n" \
"v_mfma_f32_16x16x32_bf16 v[128:131], a[96:99], a[0:3], v[128:131]\n" \
"s_add_u32 m0, s50, 94208\n" \
"ds_read_b64_tr_b16 a[88:89], v11 offset:49152\n" \
"v_mfma_f32_16x16x32_bf16 v[128:131], a[100:103], a[4:7], v[128:131]\n" \
"buffer_load_dwordx4 v20, s[28:31], s46 offen lds\n" \
"v_mfma_f32_16x16x32_bf16 v[136:139], a[96:99], a[8:11], v[136:139]\n" \
"ds_read_b64_tr_b16 a[90:91], v12 offset:49152\n" \
"v_mfma_f32_16x16x32_bf16 v[136:139], a[100:103], a[12:15], v[136:139]\n" \
"s_add_u32 s100, s100, 262144\ns_addc_u32 s101, s101, 0\n" \
"ds_read_b64_tr_b16 a[92:93], v11 offset:57344\n" \
"v_mfma_f32_16x16x32_bf16 v[144:147], a[96:99], a[16:19], v[144:147]\n" \
"s_mov_b32 s44, 0\n" \
"ds_read_b64_tr_b16 a[94:95], v12 offset:57344\n" \
"s_mov_b64 s[54:55], 0\n" \
"v_mfma_f32_16x16x32_bf16 v[144:147], a[100:103], a[20:23], v[144:147]\n" \
"s_mov_b64 exec, 0xffff\nglobal_load_dword v1, v2, s[100:101]\ns_mov_b64 exec, -1\n" \
"v_mfma_f32_16x16x32_bf16 v[152:155], a[96:99], a[24:27], v[152:155]\n" \
"s_waitcnt lgkmcnt(15)\n" \
"s_add_u32 s100, s100, 262144\ns_addc_u32 s101, s101, 0\n" \
"v_mfma_f32_16x16x32_bf16 v[152:155], a[100:103], a[28:31], v[152:155]\n" \
"s_mov_b64 exec, 0xffff\nglobal_load_dword v1, v2, s[100:101]\ns_mov_b64 exec, -1\n" \
"v_mfma_f32_16x16x32_bf16 v[132:135], a[104:107], a[0:3], v[132:135]\n" \
"s_load_dwordx2 s[28:29], %0, 0x50\n" \
"s_load_dwordx2 s[32:33], %0, 0x60\n" \
"s_lshl_b32 s79, %2, 11\n" \
"v_mfma_f32_16x16x32_bf16 v[132:135], a[108:111], a[4:7], v[132:135]\n" \
"s_lshl_b32 s20, s89, 21\n" \
"s_lshr_b32 s21, %1, 5\n" \
"s_lshl_b32 s21, s21, 8\n" \
"v_mfma_f32_16x16x32_bf16 v[140:143], a[104:107], a[8:11], v[140:143]\n" \
"s_add_u32 s18, s20, s21\n" \
"s_sub_u32 s30, 8388608, s18\n" \
"s_lshl_b32 s20, %2, 2\n" \
"v_mfma_f32_16x16x32_bf16 v[140:143], a[108:111], a[12:15], v[140:143]\n" \
"s_lshl_b32 s21, %2, 1\n" \
"v_bfe_u32 v29, v4, 6, 2\n" \
"v_add_u32 v29, s20, v29\n" \
"v_mfma_f32_16x16x32_bf16 v[148:151], a[104:107], a[16:19], v[148:151]\n" \
"s_waitcnt vmcnt(6)\n" \
"v_bfe_u32 v31, v4, 8, 1\n" \
"v_lshl_add_u32 v29, v31, 4, v29\n" \
"v_mfma_f32_16x16x32_bf16 v[148:151], a[108:111], a[20:23], v[148:151]\n" \
"s_mov_b32 s49, s22\n" \
"v_cmp_ne_u64 vcc, s[14:15], v[58:59]\n" \
"s_add_u32 s48, s22, 0x10000\n" \
"v_mfma_f32_16x16x32_bf16 v[156:159], a[104:107], a[24:27], v[156:159]\n" \
"s_waitcnt lgkmcnt(0)\n" \
"v_lshlrev_b32 v29, 11, v29\n" \
"v_add_u32 v31, s21, v31\n" \
"v_mfma_f32_16x16x32_bf16 v[156:159], a[108:111], a[28:31], v[156:159]\n" \
"s_cbranch_vccnz LPK_%=\nLPKR_%=:\n" \
"s_barrier\n" \
"v_lshrrev_b32 v28, 4, v4\n" \
"v_mfma_f32_16x16x32_bf16 v[160:163], a[64:67], a[0:3], v[160:163]\n" \
"s_lshl_b32 s60, s7, 1\n" \
"ds_read_b64_tr_b16 a[96:97], v9 offset:512\n" \
"v_xor_b32 v31, v31, v28\n" \
"v_mfma_f32_16x16x32_bf16 v[160:163], a[68:71], a[4:7], v[160:163]\n" \
"s_add_u32 s60, s60, %2\n" \
"ds_read_b64_tr_b16 a[98:99], v10 offset:512\n" \
"v_and_b32 v31, 3, v31\n" \
"v_mfma_f32_16x16x32_bf16 v[168:171], a[64:67], a[8:11], v[168:171]\n" \
"s_sub_u32 s46, s60, 1\n" \
"ds_read_b64_tr_b16 a[100:101], v9 offset:8704\n" \
"v_lshrrev_b32 v28, 9, v4\n" \
"v_mfma_f32_16x16x32_bf16 v[168:171], a[68:71], a[12:15], v[168:171]\n" \
"s_xor_b32 s62, %2, 3\n" \
"ds_read_b64_tr_b16 a[102:103], v10 offset:8704\n" \
"v_lshl_add_u32 v31, v28, 2, v31\n" \
"v_mfma_f32_16x16x32_bf16 v[176:179], a[64:67], a[16:19], v[176:179]\n" \
"s_lshl_b32 s20, %2, 9\n" \
"ds_read_b128 a[200:203], v5 offset:32768\n" \
"v_lshl_add_u32 v30, v31, 4, v29\n" \
"v_mfma_f32_16x16x32_bf16 v[176:179], a[68:71], a[20:23], v[176:179]\n" \
"s_add_u32 s20, s20, 0x20000\n" \
"ds_read_b128 a[204:207], v6 offset:32768\n" \
"v_add_u32 v13, s20, v9\n" \
"v_mfma_f32_16x16x32_bf16 v[184:187], a[64:67], a[24:27], v[184:187]\n" \
"ds_read_b128 a[208:211], v5 offset:34816\n" \
"v_add_u32 v14, s20, v10\n" \
"v_mfma_f32_16x16x32_bf16 v[184:187], a[68:71], a[28:31], v[184:187]\n" \
"ds_read_b128 a[212:215], v6 offset:34816\n" \
"v_add_u32 v15, s20, v11\n" \
"v_mfma_f32_16x16x32_bf16 v[164:167], a[72:75], a[0:3], v[164:167]\n" \
"ds_read_b128 a[216:219], v5 offset:36864\n" \
"v_add_u32 v16, s20, v12\n" \
"v_mfma_f32_16x16x32_bf16 v[164:167], a[76:79], a[4:7], v[164:167]\n" \
"ds_read_b128 a[220:223], v6 offset:36864\n" \
"v_lshlrev_b32 v20, 23, v4\n" \
"v_mfma_f32_16x16x32_bf16 v[172:175], a[72:75], a[8:11], v[172:175]\n" \
"ds_read_b128 a[224:227], v5 offset:38912\n" \
"v_mfma_f32_16x16x32_bf16 v[172:175], a[76:79], a[12:15], v[172:175]\n" \
"ds_read_b128 a[228:231], v6 offset:38912\n" \
"v_mfma_f32_16x16x32_bf16 v[180:183], a[72:75], a[16:19], v[180:183]\n" \
"ds_read_b64_tr_b16 a[104:105], v11 offset:512\n" \
"v_mfma_f32_16x16x32_bf16 v[180:183], a[76:79], a[20:23], v[180:183]\n" \
"ds_read_b64_tr_b16 a[106:107], v12 offset:512\n" \
"v_mfma_f32_16x16x32_bf16 v[188:191], a[72:75], a[24:27], v[188:191]\n" \
"ds_read_b64_tr_b16 a[108:109], v11 offset:8704\n" \
"v_mfma_f32_16x16x32_bf16 v[188:191], a[76:79], a[28:31], v[188:191]\n" \
"ds_read_b64_tr_b16 a[110:111], v12 offset:8704\n" \
"v_mfma_f32_16x16x32_bf16 v[64:67], a[80:83], a[0:3], v[64:67]\n" \
"ds_read_b64_tr_b16 a[64:65], v9 offset:1024\n" \
"v_mfma_f32_16x16x32_bf16 v[64:67], a[84:87], a[4:7], v[64:67]\n" \
"ds_read_b64_tr_b16 a[66:67], v10 offset:1024\n" \
"v_mfma_f32_16x16x32_bf16 v[72:75], a[80:83], a[8:11], v[72:75]\n" \
"ds_read_b64_tr_b16 a[68:69], v9 offset:9216\n" \
"v_mfma_f32_16x16x32_bf16 v[72:75], a[84:87], a[12:15], v[72:75]\n" \
"ds_read_b64_tr_b16 a[70:71], v10 offset:9216\n" \
"v_mfma_f32_16x16x32_bf16 v[80:83], a[80:83], a[16:19], v[80:83]\n" \
"ds_read_b64_tr_b16 a[72:73], v11 offset:1024\n" \
"v_mfma_f32_16x16x32_bf16 v[80:83], a[84:87], a[20:23], v[80:83]\n" \
"ds_read_b64_tr_b16 a[74:75], v12 offset:1024\n" \
"v_mfma_f32_16x16x32_bf16 v[88:91], a[80:83], a[24:27], v[88:91]\n" \
"ds_read_b64_tr_b16 a[76:77], v11 offset:9216\n" \
"v_mfma_f32_16x16x32_bf16 v[88:91], a[84:87], a[28:31], v[88:91]\n" \
"ds_read_b64_tr_b16 a[78:79], v12 offset:9216\n" \
"v_mfma_f32_16x16x32_bf16 v[68:71], a[88:91], a[0:3], v[68:71]\n" \
"s_cmp_lt_u32 %2, 2\ns_cselect_b32 s46, s46, 14\ns_cselect_b32 s62, s62, %2\ns_cselect_b32 s61, 56, 57\n" \
"v_mfma_f32_16x16x32_bf16 v[68:71], a[92:95], a[4:7], v[68:71]\n" \
"s_cmp_lt_u32 s7, 3\ns_cselect_b32 s45, 8, 0\n" \
"v_mfma_f32_16x16x32_bf16 v[76:79], a[88:91], a[8:11], v[76:79]\n" \
"s_cmp_lt_i32 s46, s45\ns_cselect_b32 s23, 99, s46\ns_cselect_b32 s24, s45, 99\n" \
"v_mfma_f32_16x16x32_bf16 v[76:79], a[92:95], a[12:15], v[76:79]\n" \
"s_min_i32 s46, s46, s45\n" \
"s_lshl_b32 s60, s60, 6\n" \
"s_add_u32 s60, s60, s6\n" \
"v_mfma_f32_16x16x32_bf16 v[84:87], a[88:91], a[16:19], v[84:87]\n" \
"s_lshl_b32 s50, s60, 13\n" \
"s_lshl_b32 s52, s60, 3\n" \
"s_add_u32 s52, s52, 94388224\n" \
"v_mfma_f32_16x16x32_bf16 v[84:87], a[92:95], a[20:23], v[84:87]\n" \
"s_lshr_b32 s60, %2, 1\n" \
"s_lshl_b32 s60, s60, 28\n" \
"s_add_u32 s52, s52, s60\n" \
"v_mfma_f32_16x16x32_bf16 v[92:95], a[88:91], a[24:27], v[92:95]\n" \
"s_lshl_b32 s62, s62, 1\n" \
"s_add_u32 s61, s61, s62\n" \
"s_lshl_b32 s60, s7, 2\n" \
"v_mfma_f32_16x16x32_bf16 v[92:95], a[92:95], a[28:31], v[92:95]\n" \
"s_waitcnt lgkmcnt(12)\n" \
"s_sub_u32 s61, s61, s60\n" \
"s_lshl_b32 s61, s61, 5\n" \
"v_mfma_f32_16x16x32_bf16 v[96:99], a[96:99], a[200:203], v[96:99]\n" \
"s_add_u32 m0, s22, 32768\n" \
"ds_read_b64_tr_b16 a[80:81], v9 offset:1536\n" \
"s_add_u32 s61, s61, s6\n" \
"v_mfma_f32_16x16x32_bf16 v[96:99], a[100:103], a[204:207], v[96:99]\n" \
"buffer_load_dwordx4 v60, s[40:43], s16 offen offset:0 sc1 lds\n" \
"v_mfma_f32_16x16x32_bf16 v[104:107], a[96:99], a[208:211], v[104:107]\n" \
"s_lshl_b32 s51, s61, 13\n" \
"ds_read_b64_tr_b16 a[82:83], v10 offset:1536\n" \
"s_lshl_b32 s53, s61, 3\n" \
"v_mfma_f32_16x16x32_bf16 v[104:107], a[100:103], a[212:215], v[104:107]\n" \
"s_add_u32 s53, s53, 94388224\n" \
"ds_read_b64_tr_b16 a[84:85], v9 offset:9728\n" \
"s_and_b32 s62, s62, 2\n" \
"v_mfma_f32_16x16x32_bf16 v[112:115], a[96:99], a[216:219], v[112:115]\n" \
"s_lshl_b32 s62, s62, 12\n" \
"ds_read_b64_tr_b16 a[86:87], v10 offset:9728\n" \
"s_add_u32 s62, s62, 131072\n" \
"v_mfma_f32_16x16x32_bf16 v[112:115], a[100:103], a[220:223], v[112:115]\n" \
"buffer_load_dwordx4 v60, s[40:43], s16 offen offset:1024 sc1 lds\n" \
"v_mfma_f32_16x16x32_bf16 v[120:123], a[96:99], a[224:227], v[120:123]\n" \
"s_waitcnt lgkmcnt(12)\n" \
"ds_read_b64_tr_b16 a[88:89], v11 offset:1536\n" \
"v_add_u32 v21, s62, v4\n" \
"v_mfma_f32_16x16x32_bf16 v[120:123], a[100:103], a[228:231], v[120:123]\n" \
"s_or_b32 s60, s7, %2\n" \
"ds_read_b64_tr_b16 a[90:91], v12 offset:1536\n" \
"v_mfma_f32_16x16x32_bf16 v[100:103], a[104:107], a[200:203], v[100:103]\n" \
"s_cmp_eq_u32 s60, 0\ns_cselect_b32 s60, 0xff800000, 0\n" \
"ds_read_b64_tr_b16 a[92:93], v11 offset:9728\n" \
"v_mfma_f32_16x16x32_bf16 v[100:103], a[108:111], a[204:207], v[100:103]\n" \
"buffer_load_dwordx4 v60, s[40:43], s16 offen offset:2048 sc1 lds\n" \
"v_mfma_f32_16x16x32_bf16 v[108:111], a[104:107], a[208:211], v[108:111]\n" \
"ds_read_b64_tr_b16 a[94:95], v12 offset:9728\n" \
"v_lshrrev_b32 v22, 6, v4\n" \
"v_mfma_f32_16x16x32_bf16 v[108:111], a[108:111], a[212:215], v[108:111]\n" \
"ds_read_b64_tr_b16 a[96:97], v9 offset:0\n" \
"v_and_b32 v22, 12, v22\n" \
"v_mfma_f32_16x16x32_bf16 v[116:119], a[104:107], a[216:219], v[116:119]\n" \
"ds_read_b64_tr_b16 a[98:99], v10 offset:0\n" \
"v_bfe_u32 v23, v4, 4, 4\n" \
"v_mfma_f32_16x16x32_bf16 v[116:119], a[108:111], a[220:223], v[116:119]\n" \
"buffer_load_dwordx4 v60, s[40:43], s16 offen offset:3072 sc1 lds\n" \
"v_mfma_f32_16x16x32_bf16 v[124:127], a[104:107], a[224:227], v[124:127]\n" \
"s_waitcnt lgkmcnt(14)\n" \
"ds_read_b64_tr_b16 a[100:101], v9 offset:8192\n" \
"v_sub_u32 v22, v23, v22\n" \
"v_mfma_f32_16x16x32_bf16 v[124:127], a[108:111], a[228:231], v[124:127]\n" \
"ds_read_b64_tr_b16 a[102:103], v10 offset:8192\n" \
"v_subrev_u32 v240, 0, v22\n" \
"v_mfma_f32_16x16x32_bf16 v[128:131], a[64:67], a[200:203], v[128:131]\n" \
"s_add_u32 s16, s16, s9\n" \
"s_add_u32 m0, s22, 65536\n" \
"ds_read_b64_tr_b16 a[104:105], v11 offset:0\n" \
"v_mfma_f32_16x16x32_bf16 v[128:131], a[68:71], a[204:207], v[128:131]\n" \
"buffer_load_dwordx4 v60, s[40:43], s16 offen offset:0 sc1 lds\n" \
"v_mfma_f32_16x16x32_bf16 v[136:139], a[64:67], a[208:211], v[136:139]\n" \
"ds_read_b64_tr_b16 a[106:107], v12 offset:0\n" \
"v_ashrrev_i32 v240, 31, v240\n" \
"v_mfma_f32_16x16x32_bf16 v[136:139], a[68:71], a[212:215], v[136:139]\n" \
"ds_read_b64_tr_b16 a[108:109], v11 offset:8192\n" \
"v_and_b32 v240, 0xff800000, v240\n" \
"v_mfma_f32_16x16x32_bf16 v[144:147], a[64:67], a[216:219], v[144:147]\n" \
"ds_read_b64_tr_b16 a[110:111], v12 offset:8192\n" \
"v_and_b32 v24, s60, v240\n" \
"v_mfma_f32_16x16x32_bf16 v[144:147], a[68:71], a[220:223], v[144:147]\n" \
"buffer_load_dwordx4 v60, s[40:43], s16 offen offset:1024 sc1 lds\n" \
"v_mfma_f32_16x16x32_bf16 v[152:155], a[64:67], a[224:227], v[152:155]\n" \
"s_waitcnt lgkmcnt(15)\n" \
"v_mov_b32 v244, s60\n" \
"v_subrev_u32 v241, 1, v22\n" \
"v_mfma_f32_16x16x32_bf16 v[152:155], a[68:71], a[228:231], v[152:155]\n" \
"v_ashrrev_i32 v241, 31, v241\n" \
"v_and_b32 v241, 0xff800000, v241\n" \
"v_mfma_f32_16x16x32_bf16 v[132:135], a[72:75], a[200:203], v[132:135]\n" \
"v_and_b32 v25, s60, v241\n" \
"v_mfma_f32_16x16x32_bf16 v[132:135], a[76:79], a[204:207], v[132:135]\n" \
"buffer_load_dwordx4 v60, s[40:43], s16 offen offset:2048 sc1 lds\n" \
"v_mfma_f32_16x16x32_bf16 v[140:143], a[72:75], a[208:211], v[140:143]\n" \
"v_mov_b32 v245, s60\n" \
"v_subrev_u32 v242, 2, v22\n" \
"v_mfma_f32_16x16x32_bf16 v[140:143], a[76:79], a[212:215], v[140:143]\n" \
"v_ashrrev_i32 v242, 31, v242\n" \
"v_and_b32 v242, 0xff800000, v242\n" \
"v_mfma_f32_16x16x32_bf16 v[148:151], a[72:75], a[216:219], v[148:151]\n" \
"v_and_b32 v26, s60, v242\n" \
"v_mfma_f32_16x16x32_bf16 v[148:151], a[76:79], a[220:223], v[148:151]\n" \
"buffer_load_dwordx4 v60, s[40:43], s16 offen offset:3072 sc1 lds\n" \
"v_mfma_f32_16x16x32_bf16 v[156:159], a[72:75], a[224:227], v[156:159]\n" \
"s_waitcnt vmcnt(10) lgkmcnt(0)\n" \
"v_mov_b32 v246, s60\n" \
"v_subrev_u32 v243, 3, v22\n" \
"v_mfma_f32_16x16x32_bf16 v[156:159], a[76:79], a[228:231], v[156:159]\n" \
"s_barrier\n" \
"v_ashrrev_i32 v243, 31, v243\n" \
"v_and_b32 v243, 0xff800000, v243\n" \
"v_mfma_f32_16x16x32_bf16 v[160:163], a[80:83], a[200:203], v[160:163]\n" \
"s_add_u32 m0, s22, 81920\n" \
"ds_read_b64_tr_b16 a[64:65], v9 offset:16384\n" \
"v_and_b32 v27, s60, v243\n" \
"v_mfma_f32_16x16x32_bf16 v[160:163], a[84:87], a[204:207], v[160:163]\n" \
"buffer_load_dwordx4 v60, s[40:43], s17 offen offset:0 sc1 lds\n" \
"v_mfma_f32_16x16x32_bf16 v[168:171], a[80:83], a[208:211], v[168:171]\n" \
"s_add_u32 s28, s28, s18\ns_addc_u32 s29, s29, 0\n" \
"ds_read_b64_tr_b16 a[66:67], v10 offset:16384\n" \
"v_mfma_f32_16x16x32_bf16 v[168:171], a[84:87], a[212:215], v[168:171]\n" \
"s_and_b32 s29, s29, 0xffff\n" \
"ds_read_b64_tr_b16 a[68:69], v9 offset:24576\n" \
"s_and_b32 s33, s33, 0xffff\n" \
"v_mfma_f32_16x16x32_bf16 v[176:179], a[80:83], a[216:219], v[176:179]\n" \
"s_mov_b32 s34, 4194304\n" \
"ds_read_b64_tr_b16 a[70:71], v10 offset:24576\n" \
"s_mov_b32 s35, 0x00020000\n" \
"v_mfma_f32_16x16x32_bf16 v[176:179], a[84:87], a[220:223], v[176:179]\n" \
"buffer_load_dwordx4 v60, s[40:43], s17 offen offset:1024 sc1 lds\n" \
"v_mfma_f32_16x16x32_bf16 v[184:187], a[80:83], a[224:227], v[184:187]\n" \
"ds_read_b128 a[0:3], v7 offset:0\n" \
"v_mov_b32 v247, s60\n" \
"v_mfma_f32_16x16x32_bf16 v[184:187], a[84:87], a[228:231], v[184:187]\n" \
"ds_read_b128 a[4:7], v8 offset:0\n" \
"s_load_dwordx2 s[60:61], %0, 0x0\n" \
"v_mfma_f32_16x16x32_bf16 v[164:167], a[88:91], a[200:203], v[164:167]\n" \
"ds_read_b128 a[8:11], v7 offset:2048\n" \
"v_mfma_f32_16x16x32_bf16 v[164:167], a[92:95], a[204:207], v[164:167]\n" \
"buffer_load_dwordx4 v60, s[40:43], s17 offen offset:2048 sc1 lds\n" \
"v_mfma_f32_16x16x32_bf16 v[172:175], a[88:91], a[208:211], v[172:175]\n" \
"ds_read_b128 a[12:15], v8 offset:2048\n" \
"v_mfma_f32_16x16x32_bf16 v[172:175], a[92:95], a[212:215], v[172:175]\n" \
"ds_read_b128 a[16:19], v7 offset:4096\n" \
"v_mfma_f32_16x16x32_bf16 v[180:183], a[88:91], a[216:219], v[180:183]\n" \
"ds_read_b128 a[20:23], v8 offset:4096\n" \
"v_mfma_f32_16x16x32_bf16 v[180:183], a[92:95], a[220:223], v[180:183]\n" \
"buffer_load_dwordx4 v60, s[40:43], s17 offen offset:3072 sc1 lds\n" \
"v_mfma_f32_16x16x32_bf16 v[188:191], a[88:91], a[224:227], v[188:191]\n" \
"ds_read_b128 a[24:27], v7 offset:6144\n" \
"v_mfma_f32_16x16x32_bf16 v[188:191], a[92:95], a[228:231], v[188:191]\n" \
"ds_read_b128 a[28:31], v8 offset:6144\n" \
"v_mfma_f32_16x16x32_bf16 v[64:67], a[96:99], a[200:203], v[64:67]\n" \
"s_add_u32 s16, s16, s9\n" \
"s_add_u32 m0, s22, 98304\n" \
"ds_read_b64_tr_b16 a[72:73], v11 offset:16384\n" \
"v_mfma_f32_16x16x32_bf16 v[64:67], a[100:103], a[204:207], v[64:67]\n" \
"buffer_load_dwordx4 v60, s[40:43], s16 offen offset:0 sc1 lds\n" \
"v_mfma_f32_16x16x32_bf16 v[72:75], a[96:99], a[208:211], v[72:75]\n" \
"ds_read_b64_tr_b16 a[74:75], v12 offset:16384\n" \
"v_mfma_f32_16x16x32_bf16 v[72:75], a[100:103], a[212:215], v[72:75]\n" \
"ds_read_b64_tr_b16 a[76:77], v11 offset:24576\n" \
"v_mfma_f32_16x16x32_bf16 v[80:83], a[96:99], a[216:219], v[80:83]\n" \
"ds_read_b64_tr_b16 a[78:79], v12 offset:24576\n" \
"v_mfma_f32_16x16x32_bf16 v[80:83], a[100:103], a[220:223], v[80:83]\n" \
"buffer_load_dwordx4 v60, s[40:43], s16 offen offset:1024 sc1 lds\n" \
"v_mfma_f32_16x16x32_bf16 v[88:91], a[96:99], a[224:227], v[88:91]\n" \
"ds_read_b64_tr_b16 a[80:81], v9 offset:16896\n" \
"v_mfma_f32_16x16x32_bf16 v[88:91], a[100:103], a[228:231], v[88:91]\n" \
"ds_read_b64_tr_b16 a[82:83], v10 offset:16896\n" \
"v_mfma_f32_16x16x32_bf16 v[68:71], a[104:107], a[200:203], v[68:71]\n" \
"ds_read_b64_tr_b16 a[84:85], v9 offset:25088\n" \
"v_mfma_f32_16x16x32_bf16 v[68:71], a[108:111], a[204:207], v[68:71]\n" \
"buffer_load_dwordx4 v60, s[40:43], s16 offen offset:2048 sc1 lds\n" \
"v_mfma_f32_16x16x32_bf16 v[76:79], a[104:107], a[208:211], v[76:79]\n" \
"ds_read_b64_tr_b16 a[86:87], v10 offset:25088\n" \
"v_mfma_f32_16x16x32_bf16 v[76:79], a[108:111], a[212:215], v[76:79]\n" \
"ds_read_b64_tr_b16 a[88:89], v11 offset:16896\n" \
"v_mfma_f32_16x16x32_bf16 v[84:87], a[104:107], a[216:219], v[84:87]\n" \
"ds_read_b64_tr_b16 a[90:91], v12 offset:16896\n" \
"v_mfma_f32_16x16x32_bf16 v[84:87], a[108:111], a[220:223], v[84:87]\n" \
"buffer_load_dwordx4 v60, s[40:43], s16 offen offset:3072 sc1 lds\n" \
"v_mfma_f32_16x16x32_bf16 v[92:95], a[104:107], a[224:227], v[92:95]\n" \
"ds_read_b64_tr_b16 a[92:93], v11 offset:25088\n" \
"v_mfma_f32_16x16x32_bf16 v[92:95], a[108:111], a[228:231], v[92:95]\n" \
"s_waitcnt lgkmcnt(11)\n" \
"ds_read_b64_tr_b16 a[94:95], v12 offset:25088\n" \
"s_mov_b64 exec, 0\ns_cmp_eq_u32 %2, 0\ns_cbranch_scc1 LH16_%=\ns_cmp_eq_u32 %2, 1\ns_cbranch_scc1 LH17_%=\ns_cmp_eq_u32 %2, 2\ns_cbranch_scc1 LH18_%=\ns_cmp_eq_u32 %2, 3\ns_cbranch_scc1 LH19_%=\nLHR1_%=:\ns_mov_b64 exec, -1\n" \
"v_mfma_f32_16x16x32_bf16 v[64:67], a[64:67], a[0:3], v[64:67]\n" \
"ds_read_b64_tr_b16 a[96:97], v9 offset:17408\n" \
"v_mfma_f32_16x16x32_bf16 v[64:67], a[68:71], a[4:7], v[64:67]\n" \
"ds_read_b64_tr_b16 a[98:99], v10 offset:17408\n" \
"v_mfma_f32_16x16x32_bf16 v[72:75], a[64:67], a[8:11], v[72:75]\n" \
"ds_read_b64_tr_b16 a[100:101], v9 offset:25600\n" \
"v_mfma_f32_16x16x32_bf16 v[72:75], a[68:71], a[12:15], v[72:75]\n" \
"ds_read_b64_tr_b16 a[102:103], v10 offset:25600\n" \
"v_mfma_f32_16x16x32_bf16 v[80:83], a[64:67], a[16:19], v[80:83]\n" \
"s_add_u32 s17, s17, s19\n" \
"s_add_u32 m0, s22, 114688\n" \
"ds_read_b64_tr_b16 a[104:105], v11 offset:17408\n" \
"v_mfma_f32_16x16x32_bf16 v[80:83], a[68:71], a[20:23], v[80:83]\n" \
"buffer_load_dwordx4 v60, s[40:43], s17 offen offset:0 sc1 lds\n" \
"v_mfma_f32_16x16x32_bf16 v[88:91], a[64:67], a[24:27], v[88:91]\n" \
"s_waitcnt lgkmcnt(13)\n" \
"ds_read_b64_tr_b16 a[106:107], v12 offset:17408\n" \
"v_mfma_f32_16x16x32_bf16 v[88:91], a[68:71], a[28:31], v[88:91]\n" \
"ds_read_b64_tr_b16 a[108:109], v11 offset:25600\n" \
"v_mfma_f32_16x16x32_bf16 v[68:71], a[72:75], a[0:3], v[68:71]\n" \
"ds_read_b64_tr_b16 a[110:111], v12 offset:25600\n" \
"v_mfma_f32_16x16x32_bf16 v[68:71], a[76:79], a[4:7], v[68:71]\n" \
"ds_read_b64_tr_b16 a[64:65], v9 offset:17920\n" \
"v_mfma_f32_16x16x32_bf16 v[76:79], a[72:75], a[8:11], v[76:79]\n" \
"ds_read_b64_tr_b16 a[66:67], v10 offset:17920\n" \
"v_mfma_f32_16x16x32_bf16 v[76:79], a[76:79], a[12:15], v[76:79]\n" \
"buffer_load_dwordx4 v60, s[40:43], s17 offen offset:1024 sc1 lds\n" \
"v_mfma_f32_16x16x32_bf16 v[84:87], a[72:75], a[16:19], v[84:87]\n" \
"ds_read_b64_tr_b16 a[68:69], v9 offset:26112\n" \
"v_cvt_pk_bf16_f32 v32, v64, v65\n" \
"v_mfma_f32_16x16x32_bf16 v[84:87], a[76:79], a[20:23], v[84:87]\n" \
"ds_read_b64_tr_b16 a[70:71], v10 offset:26112\n" \
"v_cvt_pk_bf16_f32 v33, v66, v67\n" \
"v_mfma_f32_16x16x32_bf16 v[92:95], a[72:75], a[24:27], v[92:95]\n" \
"s_waitcnt lgkmcnt(15)\n" \
"v_cvt_pk_bf16_f32 v34, v68, v69\n" \
"v_cvt_pk_bf16_f32 v35, v70, v71\n" \
"v_mfma_f32_16x16x32_bf16 v[92:95], a[76:79], a[28:31], v[92:95]\n" \
"v_cvt_pk_bf16_f32 v48, v72, v73\n" \
"v_cvt_pk_bf16_f32 v49, v74, v75\n" \
"v_mfma_f32_16x16x32_bf16 v[96:99], a[80:83], a[0:3], v[96:99]\n" \
"s_lshl_b32 s25, s45, 2\n" \
"ds_read_b64_tr_b16 a[72:73], v11 offset:17920\n" \
"s_and_b32 s25, s25, 32\n" \
"v_mfma_f32_16x16x32_bf16 v[96:99], a[84:87], a[4:7], v[96:99]\n" \
"buffer_load_dwordx4 v60, s[40:43], s17 offen offset:2048 sc1 lds\n" \
"v_mfma_f32_16x16x32_bf16 v[104:107], a[80:83], a[8:11], v[104:107]\n" \
"s_add_u32 s25, s25, %2\n" \
"ds_read_b64_tr_b16 a[74:75], v12 offset:17920\n" \
"v_cvt_pk_bf16_f32 v50, v76, v77\n" \
"v_mfma_f32_16x16x32_bf16 v[104:107], a[84:87], a[12:15], v[104:107]\n" \
"s_cmp_ge_u32 s25, 34\ns_cselect_b64 vcc, -1, 0\n" \
"ds_read_b64_tr_b16 a[76:77], v11 offset:26112\n" \
"v_mfma_f32_16x16x32_bf16 v[112:115], a[80:83], a[16:19], v[112:115]\n" \
"ds_read_b64_tr_b16 a[78:79], v12 offset:26112\n" \
"v_cvt_pk_bf16_f32 v51, v78, v79\n" \
"v_mfma_f32_16x16x32_bf16 v[112:115], a[84:87], a[20:23], v[112:115]\n" \
"buffer_load_dwordx4 v60, s[40:43], s17 offen offset:3072 sc1 lds\n" \
"v_mfma_f32_16x16x32_bf16 v[120:123], a[80:83], a[24:27], v[120:123]\n" \
"s_waitcnt lgkmcnt(15)\n" \
"v_cvt_pk_bf16_f32 v192, v80, v81\n" \
"v_cvt_pk_bf16_f32 v193, v82, v83\n" \
"v_mfma_f32_16x16x32_bf16 v[120:123], a[84:87], a[28:31], v[120:123]\n" \
"v_cvt_pk_bf16_f32 v194, v84, v85\n" \
"v_cvt_pk_bf16_f32 v195, v86, v87\n" \
"v_mfma_f32_16x16x32_bf16 v[100:103], a[88:91], a[0:3], v[100:103]\n" \
"v_cvt_pk_bf16_f32 v208, v88, v89\n" \
"v_mfma_f32_16x16x32_bf16 v[100:103], a[92:95], a[4:7], v[100:103]\n" \
"s_waitcnt vmcnt(16) lgkmcnt(0)\n" \
"v_cvt_pk_bf16_f32 v209, v90, v91\n" \
"v_cvt_pk_bf16_f32 v210, v92, v93\n" \
"v_mfma_f32_16x16x32_bf16 v[108:111], a[88:91], a[8:11], v[108:111]\n" \
"s_barrier\n" \
"v_cvt_pk_bf16_f32 v211, v94, v95\n" \
"v_mfma_f32_16x16x32_bf16 v[108:111], a[92:95], a[12:15], v[108:111]\n" \
"v_add_u32 v5, 0x2000, v4\n" \
"v_mov_b32 v6, v60\n" \
"v_mfma_f32_16x16x32_bf16 v[116:119], a[88:91], a[16:19], v[116:119]\n" \
"s_add_u32 s16, s16, s9\n" \
"v_add_u32 v7, 0x1000, v4\n" \
"s_add_u32 m0, s49, 0\n" \
"v_mfma_f32_16x16x32_bf16 v[116:119], a[92:95], a[20:23], v[116:119]\n" \
"buffer_load_dwordx4 v6, s[40:43], s16 offen offset:0 sc1 lds\n" \
"v_mfma_f32_16x16x32_bf16 v[124:127], a[88:91], a[24:27], v[124:127]\n" \
"ds_read_b128 a[128:131], v5 offset:32768\n" \
"v_cvt_pk_bf16_f32 v36, v96, v97\n" \
"v_mfma_f32_16x16x32_bf16 v[124:127], a[92:95], a[28:31], v[124:127]\n" \
"s_cbranch_vccz LQW1_%=\nds_write_b128 v21, v[32:35] offset:16384\nLQW1_%=:\n" \
"v_mfma_f32_16x16x32_bf16 v[128:131], a[96:99], a[0:3], v[128:131]\n" \
"s_cbranch_vccz LQW2_%=\nds_write_b128 v21, v[48:51] offset:20480\nLQW2_%=:\n" \
"v_mfma_f32_16x16x32_bf16 v[128:131], a[100:103], a[4:7], v[128:131]\n" \
"buffer_load_dwordx4 v6, s[40:43], s16 offen offset:1024 sc1 lds\n" \
"v_mfma_f32_16x16x32_bf16 v[136:139], a[96:99], a[8:11], v[136:139]\n" \
"ds_read_b128 a[132:135], v5 offset:33792\n" \
"v_cvt_pk_bf16_f32 v37, v98, v99\n" \
"v_mfma_f32_16x16x32_bf16 v[136:139], a[100:103], a[12:15], v[136:139]\n" \
"s_cbranch_vccz LQW3_%=\nds_write_b128 v21, v[192:195] offset:0\nLQW3_%=:\n" \
"v_mfma_f32_16x16x32_bf16 v[144:147], a[96:99], a[16:19], v[144:147]\n" \
"v_cvt_pk_bf16_f32 v38, v100, v101\n" \
"v_cvt_pk_bf16_f32 v39, v102, v103\n" \
"v_mfma_f32_16x16x32_bf16 v[144:147], a[100:103], a[20:23], v[144:147]\n" \
"buffer_load_dwordx4 v6, s[40:43], s16 offen offset:2048 sc1 lds\n" \
"v_mfma_f32_16x16x32_bf16 v[152:155], a[96:99], a[24:27], v[152:155]\n" \
"ds_read_b128 a[144:147], v5 offset:36864\n" \
"v_cvt_pk_bf16_f32 v52, v104, v105\n" \
"v_mfma_f32_16x16x32_bf16 v[152:155], a[100:103], a[28:31], v[152:155]\n" \
"s_cbranch_vccz LQW4_%=\nds_write_b128 v21, v[208:211] offset:4096\nLQW4_%=:\n" \
"v_mfma_f32_16x16x32_bf16 v[132:135], a[104:107], a[0:3], v[132:135]\n" \
"s_cbranch_vccz LQW5_%=\nds_write_b128 v21, v[36:39] offset:17408\nLQW5_%=:\n" \
"v_mfma_f32_16x16x32_bf16 v[132:135], a[108:111], a[4:7], v[132:135]\n" \
"buffer_load_dwordx4 v6, s[40:43], s16 offen offset:3072 sc1 lds\n" \
"v_mfma_f32_16x16x32_bf16 v[140:143], a[104:107], a[8:11], v[140:143]\n" \
"s_add_u32 s17, s17, s19\n" \
"ds_read_b128 a[148:151], v5 offset:37888\n" \
"s_add_u32 m0, s49, 16384\n" \
"v_mfma_f32_16x16x32_bf16 v[140:143], a[108:111], a[12:15], v[140:143]\n" \
"v_cvt_pk_bf16_f32 v53, v106, v107\n" \
"v_cvt_pk_bf16_f32 v54, v108, v109\n" \
"v_mfma_f32_16x16x32_bf16 v[148:151], a[104:107], a[16:19], v[148:151]\n" \
"v_cvt_pk_bf16_f32 v55, v110, v111\n" \
"v_cvt_pk_bf16_f32 v196, v112, v113\n" \
"v_mfma_f32_16x16x32_bf16 v[148:151], a[108:111], a[20:23], v[148:151]\n" \
"buffer_load_dwordx4 v6, s[40:43], s17 offen offset:0 sc1 lds\n" \
"v_mfma_f32_16x16x32_bf16 v[156:159], a[104:107], a[24:27], v[156:159]\n" \
"ds_read_b128 a[136:139], v5 offset:34816\n" \
"v_cvt_pk_bf16_f32 v197, v114, v115\n" \
"v_mfma_f32_16x16x32_bf16 v[156:159], a[108:111], a[28:31], v[156:159]\n" \
"s_cbranch_vccz LQW6_%=\nds_write_b128 v21, v[52:55] offset:21504\nLQW6_%=:\n" \
"v_mfma_f32_16x16x32_bf16 v[160:163], a[64:67], a[0:3], v[160:163]\n" \
"v_cvt_pk_bf16_f32 v198, v116, v117\n" \
"v_cvt_pk_bf16_f32 v199, v118, v119\n" \
"v_mfma_f32_16x16x32_bf16 v[160:163], a[68:71], a[4:7], v[160:163]\n" \
"LH14_%=:\ns_cbranch_execz LHR0_%=\n" \
"buffer_load_dwordx4 v6, s[40:43], s17 offen offset:1024 sc1 lds\n" \
"v_mfma_f32_16x16x32_bf16 v[168:171], a[64:67], a[8:11], v[168:171]\n" \
"ds_read_b128 a[140:143], v5 offset:35840\n" \
"v_cvt_pk_bf16_f32 v212, v120, v121\n" \
"v_mfma_f32_16x16x32_bf16 v[168:171], a[68:71], a[12:15], v[168:171]\n" \
"s_cbranch_vccz LQW7_%=\nds_write_b128 v21, v[196:199] offset:1024\nLQW7_%=:\n" \
"v_mfma_f32_16x16x32_bf16 v[176:179], a[64:67], a[16:19], v[176:179]\n" \
"v_cvt_pk_bf16_f32 v213, v122, v123\n" \
"v_cvt_pk_bf16_f32 v214, v124, v125\n" \
"v_mfma_f32_16x16x32_bf16 v[176:179], a[68:71], a[20:23], v[176:179]\n" \
"buffer_load_dwordx4 v6, s[40:43], s17 offen offset:2048 sc1 lds\n" \
"v_mfma_f32_16x16x32_bf16 v[184:187], a[64:67], a[24:27], v[184:187]\n" \
"ds_read_b128 a[152:155], v5 offset:38912\n" \
"v_cvt_pk_bf16_f32 v215, v126, v127\n" \
"v_mfma_f32_16x16x32_bf16 v[184:187], a[68:71], a[28:31], v[184:187]\n" \
"s_cbranch_vccz LQW8_%=\nds_write_b128 v21, v[212:215] offset:5120\nLQW8_%=:\n" \
"v_mfma_f32_16x16x32_bf16 v[164:167], a[72:75], a[0:3], v[164:167]\n" \
"v_cvt_pk_bf16_f32 v40, v128, v129\n" \
"v_cvt_pk_bf16_f32 v41, v130, v131\n" \
"v_mfma_f32_16x16x32_bf16 v[164:167], a[76:79], a[4:7], v[164:167]\n" \
"buffer_load_dwordx4 v6, s[40:43], s17 offen offset:3072 sc1 lds\n" \
"v_mfma_f32_16x16x32_bf16 v[172:175], a[72:75], a[8:11], v[172:175]\n" \
"ds_read_b128 a[156:159], v5 offset:39936\n" \
"v_cvt_pk_bf16_f32 v42, v132, v133\n" \
"v_mfma_f32_16x16x32_bf16 v[172:175], a[76:79], a[12:15], v[172:175]\n" \
"v_cvt_pk_bf16_f32 v43, v134, v135\n" \
"v_cvt_pk_bf16_f32 v56, v136, v137\n" \
"v_mfma_f32_16x16x32_bf16 v[180:183], a[72:75], a[16:19], v[180:183]\n" \
"s_cbranch_vccz LQW9_%=\nds_write_b128 v21, v[40:43] offset:18432\nLQW9_%=:\n" \
"v_mfma_f32_16x16x32_bf16 v[180:183], a[76:79], a[20:23], v[180:183]\n" \
"v_cvt_pk_bf16_f32 v57, v138, v139\n" \
"v_cvt_pk_bf16_f32 v58, v140, v141\n" \
"v_mfma_f32_16x16x32_bf16 v[188:191], a[72:75], a[24:27], v[188:191]\n" \
"v_xor_b32 v5, 0x10000, v5\n" \
"v_cvt_pk_bf16_f32 v59, v142, v143\n" \
"v_mfma_f32_16x16x32_bf16 v[188:191], a[76:79], a[28:31], v[188:191]\n" \
"s_waitcnt lgkmcnt(0)\n" \
"s_cbranch_vccz LQW10_%=\nds_write_b128 v21, v[56:59] offset:22528\nLQW10_%=:\n" \
"v_mfma_f32_16x16x32_bf16 v[64:67], a[128:131], v[32:35], v[24:27]\n" \
"v_cvt_pk_bf16_f32 v200, v144, v145\n" \
"v_cvt_pk_bf16_f32 v201, v146, v147\n" \
"v_mfma_f32_16x16x32_bf16 v[64:67], a[132:135], v[36:39], v[64:67]\n" \
"s_waitcnt vmcnt(16)\n" \
"v_cvt_pk_bf16_f32 v202, v148, v149\n" \
"v_cvt_pk_bf16_f32 v203, v150, v151\n" \
"v_mfma_f32_16x16x32_bf16 v[72:75], a[128:131], v[48:51], 0\n" \
"s_barrier\n" \
"s_cbranch_vccz LQW11_%=\nds_write_b128 v21, v[200:203] offset:2048\nLQW11_%=:\n" \
"v_mfma_f32_16x16x32_bf16 v[72:75], a[132:135], v[52:55], v[72:75]\n" \
"v_cvt_pk_bf16_f32 v216, v152, v153\n" \
"v_cvt_pk_bf16_f32 v217, v154, v155\n" \
"v_mfma_f32_16x16x32_bf16 v[80:83], a[128:131], v[192:195], 0\n" \
"v_cvt_pk_bf16_f32 v218, v156, v157\n" \
"v_cvt_pk_bf16_f32 v219, v158, v159\n" \
"v_mfma_f32_16x16x32_bf16 v[80:83], a[132:135], v[196:199], v[80:83]\n" \
"s_cbranch_vccz LQW12_%=\nds_write_b128 v21, v[216:219] offset:6144\nLQW12_%=:\n" \
"v_mfma_f32_16x16x32_bf16 v[88:91], a[128:131], v[208:211], 0\n" \
"v_cvt_pk_bf16_f32 v44, v160, v161\n" \
"v_cvt_pk_bf16_f32 v45, v162, v163\n" \
"v_mfma_f32_16x16x32_bf16 v[88:91], a[132:135], v[212:215], v[88:91]\n" \
"ds_read_b128 a[128:131], v5 offset:0\n" \
"v_cvt_pk_bf16_f32 v46, v164, v165\n" \
"v_mfma_f32_16x16x32_bf16 v[68:71], a[144:147], v[32:35], v[244:247]\n" \
"ds_read_b128 a[132:135], v5 offset:1024\n" \
"v_cvt_pk_bf16_f32 v47, v166, v167\n" \
"v_mfma_f32_16x16x32_bf16 v[68:71], a[148:151], v[36:39], v[68:71]\n" \
"s_cbranch_vccz LQW13_%=\nds_write_b128 v21, v[44:47] offset:19456\nLQW13_%=:\n" \
"v_mfma_f32_16x16x32_bf16 v[76:79], a[144:147], v[48:51], v[24:27]\n" \
"v_cvt_pk_bf16_f32 v60, v168, v169\n" \
"v_cvt_pk_bf16_f32 v61, v170, v171\n" \
"v_mfma_f32_16x16x32_bf16 v[76:79], a[148:151], v[52:55], v[76:79]\n" \
"v_cvt_pk_bf16_f32 v62, v172, v173\n" \
"v_cvt_pk_bf16_f32 v63, v174, v175\n" \
"v_mfma_f32_16x16x32_bf16 v[84:87], a[144:147], v[192:195], 0\n" \
"s_cbranch_vccz LQW14_%=\nds_write_b128 v21, v[60:63] offset:23552\nLQW14_%=:\n" \
"v_mfma_f32_16x16x32_bf16 v[84:87], a[148:151], v[196:199], v[84:87]\n" \
"v_cvt_pk_bf16_f32 v204, v176, v177\n" \
"v_cvt_pk_bf16_f32 v205, v178, v179\n" \
"v_mfma_f32_16x16x32_bf16 v[92:95], a[144:147], v[208:211], 0\n" \
"v_cvt_pk_bf16_f32 v206, v180, v181\n" \
"v_cvt_pk_bf16_f32 v207, v182, v183\n" \
"v_mfma_f32_16x16x32_bf16 v[92:95], a[148:151], v[212:215], v[92:95]\n" \
"ds_read_b128 a[144:147], v5 offset:4096\n" \
"v_cvt_pk_bf16_f32 v220, v184, v185\n" \
"v_mfma_f32_16x16x32_bf16 v[64:67], a[136:139], v[40:43], v[64:67]\n" \
"ds_read_b128 a[148:151], v5 offset:5120\n" \
"v_cvt_pk_bf16_f32 v221, v186, v187\n" \
"v_mfma_f32_16x16x32_bf16 v[64:67], a[140:143], v[44:47], v[64:67]\n" \
"s_cbranch_vccz LQW15_%=\nds_write_b128 v21, v[204:207] offset:3072\nLQW15_%=:\n" \
"v_mfma_f32_16x16x32_bf16 v[72:75], a[136:139], v[56:59], v[72:75]\n" \
"v_cvt_pk_bf16_f32 v222, v188, v189\n" \
"v_cvt_pk_bf16_f32 v223, v190, v191\n" \
"v_mfma_f32_16x16x32_bf16 v[72:75], a[140:143], v[60:63], v[72:75]\n" \
"s_cbranch_vccz LQW16_%=\nds_write_b128 v21, v[220:223] offset:7168\nLQW16_%=:\n" \
"v_mfma_f32_16x16x32_bf16 v[80:83], a[136:139], v[200:203], v[80:83]\n" \
"v_exp_f32 v64, v64\n" \
"v_mfma_f32_16x16x32_bf16 v[80:83], a[140:143], v[204:207], v[80:83]\n" \
"v_exp_f32 v65, v65\n" \
"v_mfma_f32_16x16x32_bf16 v[88:91], a[136:139], v[216:219], v[88:91]\n" \
"v_exp_f32 v66, v66\n" \
"v_mfma_f32_16x16x32_bf16 v[88:91], a[140:143], v[220:223], v[88:91]\n" \
"ds_read_b128 a[136:139], v5 offset:2048\n" \
"v_accvgpr_write_b32 a64, v192\n" \
"v_mfma_f32_16x16x32_bf16 v[68:71], a[152:155], v[40:43], v[68:71]\n" \
"ds_read_b128 a[140:143], v5 offset:3072\n" \
"v_accvgpr_write_b32 a65, v193\n" \
"v_mfma_f32_16x16x32_bf16 v[68:71], a[156:159], v[44:47], v[68:71]\n" \
"v_exp_f32 v67, v67\n" \
"v_mfma_f32_16x16x32_bf16 v[76:79], a[152:155], v[56:59], v[76:79]\n" \
"v_exp_f32 v72, v72\n" \
"v_mfma_f32_16x16x32_bf16 v[76:79], a[156:159], v[60:63], v[76:79]\n" \
"v_exp_f32 v73, v73\n" \
"v_mfma_f32_16x16x32_bf16 v[84:87], a[152:155], v[200:203], v[84:87]\n" \
"v_exp_f32 v74, v74\n" \
"v_mfma_f32_16x16x32_bf16 v[84:87], a[156:159], v[204:207], v[84:87]\n" \
"v_exp_f32 v75, v75\n" \
"v_mfma_f32_16x16x32_bf16 v[92:95], a[152:155], v[216:219], v[92:95]\n" \
"v_exp_f32 v80, v80\n" \
"v_mfma_f32_16x16x32_bf16 v[92:95], a[156:159], v[220:223], v[92:95]\n" \
"s_waitcnt lgkmcnt(0)\n" \
"v_exp_f32 v81, v81\n" \
"v_mfma_f32_16x16x32_bf16 v[96:99], a[128:131], v[32:35], v[244:247]\n" \
"v_exp_f32 v82, v82\n" \
"v_mfma_f32_16x16x32_bf16 v[96:99], a[132:135], v[36:39], v[96:99]\n" \
"s_add_u32 s16, s16, s9\n" \
"v_exp_f32 v83, v83\n" \
"v_mfma_f32_16x16x32_bf16 v[104:107], a[128:131], v[48:51], v[244:247]\n" \
"ds_read_b128 a[152:155], v5 offset:6144\n" \
"v_cvt_pk_bf16_f32 v128, v64, v65\n" \
"v_mfma_f32_16x16x32_bf16 v[104:107], a[132:135], v[52:55], v[104:107]\n" \
"s_add_u32 m0, s49, 32768\n" \
"v_exp_f32 v88, v88\n" \
"v_mfma_f32_16x16x32_bf16 v[112:115], a[128:131], v[192:195], v[24:27]\n" \
"buffer_load_dwordx4 v6, s[40:43], s16 offen offset:0 sc1 lds\n" \
"v_mfma_f32_16x16x32_bf16 v[112:115], a[132:135], v[196:199], v[112:115]\n" \
"v_exp_f32 v89, v89\n" \
"v_mfma_f32_16x16x32_bf16 v[120:123], a[128:131], v[208:211], 0\n" \
"ds_read_b128 a[156:159], v5 offset:7168\n" \
"v_cvt_pk_bf16_f32 v129, v66, v67\n" \
"v_mfma_f32_16x16x32_bf16 v[120:123], a[132:135], v[212:215], v[120:123]\n" \
"s_waitcnt lgkmcnt(4)\n" \
"v_exp_f32 v90, v90\n" \
"v_mfma_f32_16x16x32_bf16 v[96:99], a[136:139], v[40:43], v[96:99]\n" \
"v_exp_f32 v91, v91\n" \
"v_mfma_f32_16x16x32_bf16 v[96:99], a[140:143], v[44:47], v[96:99]\n" \
"v_exp_f32 v68, v68\n" \
"v_mfma_f32_16x16x32_bf16 v[104:107], a[136:139], v[56:59], v[104:107]\n" \
"ds_read_b128 a[128:131], v5 offset:16384\n" \
"v_cvt_pk_bf16_f32 v132, v72, v73\n" \
"v_mfma_f32_16x16x32_bf16 v[104:107], a[140:143], v[60:63], v[104:107]\n" \
"v_exp_f32 v69, v69\n" \
"v_mfma_f32_16x16x32_bf16 v[112:115], a[136:139], v[200:203], v[112:115]\n" \
"buffer_load_dwordx4 v6, s[40:43], s16 offen offset:1024 sc1 lds\n" \
"v_mfma_f32_16x16x32_bf16 v[112:115], a[140:143], v[204:207], v[112:115]\n" \
"v_exp_f32 v70, v70\n" \
"v_mfma_f32_16x16x32_bf16 v[120:123], a[136:139], v[216:219], v[120:123]\n" \
"ds_read_b128 a[132:135], v5 offset:17408\n" \
"v_cvt_pk_bf16_f32 v133, v74, v75\n" \
"v_mfma_f32_16x16x32_bf16 v[120:123], a[140:143], v[220:223], v[120:123]\n" \
"s_waitcnt lgkmcnt(4)\n" \
"v_exp_f32 v71, v71\n" \
"v_mfma_f32_16x16x32_bf16 v[100:103], a[144:147], v[32:35], v[244:247]\n" \
"v_exp_f32 v76, v76\n" \
"v_mfma_f32_16x16x32_bf16 v[100:103], a[148:151], v[36:39], v[100:103]\n" \
"v_exp_f32 v77, v77\n" \
"v_mfma_f32_16x16x32_bf16 v[108:111], a[144:147], v[48:51], v[244:247]\n" \
"ds_read_b128 a[136:139], v5 offset:18432\n" \
"v_cvt_pk_bf16_f32 v136, v80, v81\n" \
"v_mfma_f32_16x16x32_bf16 v[108:111], a[148:151], v[52:55], v[108:111]\n" \
"v_exp_f32 v78, v78\n" \
"v_accvgpr_write_b32 a66, v194\n" \
"v_accvgpr_write_b32 a67, v195\n" \
"v_mfma_f32_16x16x32_bf16 v[116:119], a[144:147], v[192:195], v[244:247]\n" \
"buffer_load_dwordx4 v6, s[40:43], s16 offen offset:2048 sc1 lds\n" \
"v_accvgpr_write_b32 a80, v208\n" \
"v_accvgpr_write_b32 a81, v209\n" \
"v_mfma_f32_16x16x32_bf16 v[116:119], a[148:151], v[196:199], v[116:119]\n" \
"v_exp_f32 v79, v79\n" \
"v_accvgpr_write_b32 a82, v210\n" \
"v_accvgpr_write_b32 a83, v211\n" \
"v_mfma_f32_16x16x32_bf16 v[124:127], a[144:147], v[208:211], v[24:27]\n" \
"ds_read_b128 a[140:143], v5 offset:19456\n" \
"v_cvt_pk_bf16_f32 v137, v82, v83\n" \
"v_accvgpr_write_b32 a68, v196\n" \
"v_accvgpr_write_b32 a69, v197\n" \
"v_mfma_f32_16x16x32_bf16 v[124:127], a[148:151], v[212:215], v[124:127]\n" \
"s_waitcnt lgkmcnt(4)\n" \
"v_exp_f32 v84, v84\n" \
"v_accvgpr_write_b32 a70, v198\n" \
"v_accvgpr_write_b32 a71, v199\n" \
"v_mfma_f32_16x16x32_bf16 v[100:103], a[152:155], v[40:43], v[100:103]\n" \
"v_exp_f32 v85, v85\n" \
"v_accvgpr_write_b32 a84, v212\n" \
"v_accvgpr_write_b32 a85, v213\n" \
"v_mfma_f32_16x16x32_bf16 v[100:103], a[156:159], v[44:47], v[100:103]\n" \
"v_exp_f32 v86, v86\n" \
"v_accvgpr_write_b32 a86, v214\n" \
"v_accvgpr_write_b32 a87, v215\n" \
"v_mfma_f32_16x16x32_bf16 v[108:111], a[152:155], v[56:59], v[108:111]\n" \
"ds_read_b128 a[144:147], v5 offset:20480\n" \
"v_cvt_pk_bf16_f32 v140, v88, v89\n" \
"v_accvgpr_write_b32 a72, v200\n" \
"v_accvgpr_write_b32 a73, v201\n" \
"v_mfma_f32_16x16x32_bf16 v[108:111], a[156:159], v[60:63], v[108:111]\n" \
"v_exp_f32 v87, v87\n" \
"v_accvgpr_write_b32 a74, v202\n" \
"v_accvgpr_write_b32 a75, v203\n" \
"v_mov_b32 v180, 0x3f803f80\n" \
"v_mov_b32 v181, 0x3f803f80\n" \
"v_mov_b32 v182, 0x3f803f80\n" \
"v_mov_b32 v183, 0x3f803f80\n" \
"v_mfma_f32_16x16x32_bf16 v[116:119], a[152:155], v[200:203], v[116:119]\n" \
"buffer_load_dwordx4 v6, s[40:43], s16 offen offset:3072 sc1 lds\n" \
"v_accvgpr_write_b32 a88, v216\n" \
"v_accvgpr_write_b32 a89, v217\n" \
"v_mfma_f32_16x16x32_bf16 v[116:119], a[156:159], v[204:207], v[116:119]\n" \
"v_exp_f32 v92, v92\n" \
"v_accvgpr_write_b32 a90, v218\n" \
"v_accvgpr_write_b32 a91, v219\n" \
"v_mov_b32 v176, v240\n" \
"v_mov_b32 v177, v241\n" \
"v_mov_b32 v178, v242\n" \
"v_mov_b32 v179, v243\n" \
"v_mfma_f32_16x16x32_bf16 v[124:127], a[152:155], v[216:219], v[124:127]\n" \
"ds_read_b128 a[148:151], v5 offset:21504\n" \
"v_exp_f32 v93, v93\n" \
"v_cvt_pk_bf16_f32 v141, v90, v91\n" \
"v_cvt_pk_bf16_f32 v130, v68, v69\n" \
"v_cvt_pk_bf16_f32 v131, v70, v71\n" \
"v_accvgpr_write_b32 a76, v204\n" \
"v_accvgpr_write_b32 a77, v205\n" \
"v_mfma_f32_16x16x32_bf16 v[124:127], a[156:159], v[220:223], v[124:127]\n" \
"s_waitcnt lgkmcnt(4)\n" \
"v_exp_f32 v94, v94\n" \
"v_cvt_pk_bf16_f32 v134, v76, v77\n" \
"v_cvt_pk_bf16_f32 v135, v78, v79\n" \
"v_accvgpr_write_b32 a78, v206\n" \
"v_accvgpr_write_b32 a79, v207\n" \
"v_mfma_f32_16x16x32_bf16 v[160:163], v[180:183], v[128:131], 0\n" \
"v_exp_f32 v95, v95\n" \
"v_cvt_pk_bf16_f32 v138, v84, v85\n" \
"v_cvt_pk_bf16_f32 v139, v86, v87\n" \
"v_accvgpr_write_b32 a92, v220\n" \
"v_accvgpr_write_b32 a93, v221\n" \
"v_mfma_f32_16x16x32_bf16 v[164:167], v[180:183], v[132:135], 0\n" \
"v_cvt_pk_bf16_f32 v142, v92, v93\n" \
"v_cvt_pk_bf16_f32 v143, v94, v95\n" \
"v_accvgpr_write_b32 a94, v222\n" \
"v_accvgpr_write_b32 a95, v223\n" \
"v_mfma_f32_16x16x32_bf16 v[168:171], v[180:183], v[136:139], 0\n" \
"v_exp_f32 v96, v96\n" \
"v_mfma_f32_16x16x32_bf16 v[172:175], v[180:183], v[140:143], 0\n" \
"v_lshrrev_b32 v9, 5, v4\n" \
"v_lshlrev_b32 v9, 27, v9\n" \
"v_mfma_f32_16x16x32_bf16 v[192:195], a[128:131], v[128:131], 0\n" \
"v_exp_f32 v97, v97\n" \
"v_mfma_f32_16x16x32_bf16 v[196:199], a[132:135], v[128:131], 0\n" \
"v_cvt_pk_bf16_f32 v144, v96, v97\n" \
"v_lshl_add_u32 v9, v4, 4, v9\n" \
"v_mfma_f32_16x16x32_bf16 v[224:227], a[128:131], v[132:135], 0\n" \
"ds_read_b128 a[152:155], v5 offset:22528\n" \
"v_mov_b32 v10, s14\n" \
"v_mfma_f32_16x16x32_bf16 v[228:231], a[132:135], v[132:135], 0\n" \
"v_exp_f32 v98, v98\n" \
"v_mfma_f32_16x16x32_bf16 a[0:3], a[128:131], v[136:139], 0\n" \
"v_exp_f32 v99, v99\n" \
"v_mfma_f32_16x16x32_bf16 a[4:7], a[132:135], v[136:139], 0\n" \
"v_cvt_pk_bf16_f32 v145, v98, v99\n" \
"v_mov_b32 v11, s15\n" \
"v_mfma_f32_16x16x32_bf16 a[32:35], a[128:131], v[140:143], 0\n" \
"ds_read_b128 a[156:159], v5 offset:23552\n" \
"s_waitcnt lgkmcnt(12)\n" \
"v_mfma_f32_16x16x32_bf16 a[36:39], a[132:135], v[140:143], 0\n" \
"s_waitcnt vmcnt(12) lgkmcnt(4)\n" \
"v_exp_f32 v100, v100\n" \
"v_mfma_f32_16x16x32_bf16 v[200:203], a[136:139], v[128:131], 0\n" \
"s_barrier\n" \
"v_exp_f32 v101, v101\n" \
"v_mfma_f32_16x16x32_bf16 v[204:207], a[140:143], v[128:131], 0\n" \
"s_add_u32 s17, s17, s19\n" \
"v_cvt_pk_bf16_f32 v146, v100, v101\n" \
"v_mfma_f32_16x16x32_bf16 v[232:235], a[136:139], v[132:135], 0\n" \
"ds_read_b128 a[128:131], v5 offset:32768\n" \
"v_mfma_f32_16x16x32_bf16 v[236:239], a[140:143], v[132:135], 0\n" \
"s_add_u32 m0, s49, 49152\n" \
"v_exp_f32 v102, v102\n" \
"v_mfma_f32_16x16x32_bf16 a[8:11], a[136:139], v[136:139], 0\n" \
"buffer_load_dwordx4 v6, s[40:43], s17 offen offset:0 sc1 lds\n" \
"v_mfma_f32_16x16x32_bf16 a[12:15], a[140:143], v[136:139], 0\n" \
"v_exp_f32 v103, v103\n" \
"v_mfma_f32_16x16x32_bf16 a[40:43], a[136:139], v[140:143], 0\n" \
"ds_read_b128 a[132:135], v5 offset:33792\n" \
"v_cvt_pk_bf16_f32 v147, v102, v103\n" \
"v_mfma_f32_16x16x32_bf16 a[44:47], a[140:143], v[140:143], 0\n" \
"s_waitcnt lgkmcnt(4)\n" \
"v_exp_f32 v104, v104\n" \
"v_mfma_f32_16x16x32_bf16 v[208:211], a[144:147], v[128:131], 0\n" \
"v_exp_f32 v105, v105\n" \
"v_mfma_f32_16x16x32_bf16 v[212:215], a[148:151], v[128:131], 0\n" \
"v_cvt_pk_bf16_f32 v148, v104, v105\n" \
"v_mfma_f32_16x16x32_bf16 v[240:243], a[144:147], v[132:135], 0\n" \
"ds_read_b128 a[136:139], v5 offset:34816\n" \
"v_mfma_f32_16x16x32_bf16 v[244:247], a[148:151], v[132:135], 0\n" \
"v_exp_f32 v106, v106\n" \
"v_mfma_f32_16x16x32_bf16 a[16:19], a[144:147], v[136:139], 0\n" \
"buffer_load_dwordx4 v6, s[40:43], s17 offen offset:1024 sc1 lds\n" \
"v_mfma_f32_16x16x32_bf16 a[20:23], a[148:151], v[136:139], 0\n" \
"v_exp_f32 v107, v107\n" \
"v_mfma_f32_16x16x32_bf16 a[48:51], a[144:147], v[140:143], 0\n" \
"ds_read_b128 a[140:143], v5 offset:35840\n" \
"v_cvt_pk_bf16_f32 v149, v106, v107\n" \
"v_mfma_f32_16x16x32_bf16 a[52:55], a[148:151], v[140:143], 0\n" \
"s_waitcnt lgkmcnt(4)\n" \
"v_exp_f32 v108, v108\n" \
"v_mfma_f32_16x16x32_bf16 v[216:219], a[152:155], v[128:131], 0\n" \
"buffer_load_dwordx4 v6, s[40:43], s17 offen offset:2048 sc1 lds\n" \
"v_mfma_f32_16x16x32_bf16 v[220:223], a[156:159], v[128:131], 0\n" \
"v_exp_f32 v109, v109\n" \
"v_mfma_f32_16x16x32_bf16 v[248:251], a[152:155], v[132:135], 0\n" \
"ds_read_b128 a[144:147], v5 offset:36864\n" \
"v_cvt_pk_bf16_f32 v150, v108, v109\n" \
"v_mfma_f32_16x16x32_bf16 v[252:255], a[156:159], v[132:135], 0\n" \
"v_exp_f32 v110, v110\n" \
"v_mfma_f32_16x16x32_bf16 a[24:27], a[152:155], v[136:139], 0\n" \
"buffer_load_dwordx4 v6, s[40:43], s17 offen offset:3072 sc1 lds\n" \
"v_mfma_f32_16x16x32_bf16 a[28:31], a[156:159], v[136:139], 0\n" \
"v_exp_f32 v111, v111\n" \
"v_mfma_f32_16x16x32_bf16 a[56:59], a[152:155], v[140:143], 0\n" \
"ds_read_b128 a[148:151], v5 offset:37888\n" \
"v_cvt_pk_bf16_f32 v151, v110, v111\n" \
"v_mfma_f32_16x16x32_bf16 a[60:63], a[156:159], v[140:143], 0\n" \
"s_waitcnt lgkmcnt(4)\n" \
"s_cmp_lt_i32 s46, 0\n" \
"s_cbranch_scc1 LAD2_%=\n" \
"LCHK_%=:\n" \
"s_cmp_eq_u32 s44, s46\n" \
"s_cbranch_scc1 LMID_%=\n" \
"s_branch LODD_%=\n" \
".p2align 12\n" \
"LH15_%=:\ns_cbranch_execz LHR0_%=\n" \
"s_nop 0\n" \
"LODD_%=:\n" \
"v_mfma_f32_16x16x32_bf16 v[64:67], a[128:131], v[32:35], 0\n" \
"v_exp_f32 v112, v112\n" \
"v_mfma_f32_16x16x32_bf16 v[64:67], a[132:135], v[36:39], v[64:67]\n" \
"s_add_u32 s16, s16, s9\n" \
"v_mfma_f32_16x16x32_bf16 v[72:75], a[128:131], v[48:51], 0\n" \
"ds_read_b128 a[152:155], v5 offset:38912\n" \
"v_mfma_f32_16x16x32_bf16 v[72:75], a[132:135], v[52:55], v[72:75]\n" \
"s_add_u32 m0, s48, 0\n" \
"v_exp_f32 v113, v113\n" \
"v_mfma_f32_16x16x32_bf16 v[80:83], a[128:131], a[64:67], 0\n" \
"buffer_load_dwordx4 v6, s[40:43], s16 offen offset:0 sc1 lds\n" \
"v_mfma_f32_16x16x32_bf16 v[80:83], a[132:135], a[68:71], v[80:83]\n" \
"v_exp_f32 v114, v114\n" \
"v_mfma_f32_16x16x32_bf16 v[88:91], a[128:131], a[80:83], 0\n" \
"ds_read_b128 a[156:159], v5 offset:39936\n" \
"v_cvt_pk_bf16_f32 v152, v112, v113\n" \
"v_mfma_f32_16x16x32_bf16 v[88:91], a[132:135], a[84:87], v[88:91]\n" \
"s_waitcnt lgkmcnt(4)\n" \
"v_exp_f32 v115, v115\n" \
"v_mfma_f32_16x16x32_bf16 v[64:67], a[136:139], v[40:43], v[64:67]\n" \
"v_exp_f32 v116, v116\n" \
"v_mfma_f32_16x16x32_bf16 v[64:67], a[140:143], v[44:47], v[64:67]\n" \
"s_xor_b32 s49, s49, 0x10000\n" \
"v_cvt_pk_bf16_f32 v153, v114, v115\n" \
"v_mfma_f32_16x16x32_bf16 v[72:75], a[136:139], v[56:59], v[72:75]\n" \
"ds_read_b128 a[128:131], v5 offset:49152\n" \
"v_mfma_f32_16x16x32_bf16 v[72:75], a[140:143], v[60:63], v[72:75]\n" \
"v_exp_f32 v117, v117\n" \
"v_mfma_f32_16x16x32_bf16 v[80:83], a[136:139], a[72:75], v[80:83]\n" \
"buffer_load_dwordx4 v6, s[40:43], s16 offen offset:1024 sc1 lds\n" \
"v_mfma_f32_16x16x32_bf16 v[80:83], a[140:143], a[76:79], v[80:83]\n" \
"v_exp_f32 v118, v118\n" \
"v_mfma_f32_16x16x32_bf16 v[88:91], a[136:139], a[88:91], v[88:91]\n" \
"ds_read_b128 a[132:135], v5 offset:50176\n" \
"v_cvt_pk_bf16_f32 v154, v116, v117\n" \
"v_mfma_f32_16x16x32_bf16 v[88:91], a[140:143], a[92:95], v[88:91]\n" \
"s_waitcnt lgkmcnt(4)\n" \
"v_exp_f32 v119, v119\n" \
"v_mfma_f32_16x16x32_bf16 v[68:71], a[144:147], v[32:35], 0\n" \
"v_exp_f32 v120, v120\n" \
"v_mfma_f32_16x16x32_bf16 v[68:71], a[148:151], v[36:39], v[68:71]\n" \
"v_cvt_pk_bf16_f32 v155, v118, v119\n" \
"v_mfma_f32_16x16x32_bf16 v[76:79], a[144:147], v[48:51], 0\n" \
"ds_read_b128 a[136:139], v5 offset:51200\n" \
"v_mfma_f32_16x16x32_bf16 v[76:79], a[148:151], v[52:55], v[76:79]\n" \
"v_exp_f32 v121, v121\n" \
"v_mfma_f32_16x16x32_bf16 v[84:87], a[144:147], a[64:67], 0\n" \
"buffer_load_dwordx4 v6, s[40:43], s16 offen offset:2048 sc1 lds\n" \
"v_mfma_f32_16x16x32_bf16 v[84:87], a[148:151], a[68:71], v[84:87]\n" \
"v_exp_f32 v122, v122\n" \
"v_mfma_f32_16x16x32_bf16 v[92:95], a[144:147], a[80:83], 0\n" \
"ds_read_b128 a[140:143], v5 offset:52224\n" \
"v_cvt_pk_bf16_f32 v156, v120, v121\n" \
"v_mfma_f32_16x16x32_bf16 v[92:95], a[148:151], a[84:87], v[92:95]\n" \
"s_waitcnt lgkmcnt(4)\n" \
"v_exp_f32 v123, v123\n" \
"v_mfma_f32_16x16x32_bf16 v[68:71], a[152:155], v[40:43], v[68:71]\n" \
"v_exp_f32 v124, v124\n" \
"v_mfma_f32_16x16x32_bf16 v[68:71], a[156:159], v[44:47], v[68:71]\n" \
"v_cvt_pk_bf16_f32 v157, v122, v123\n" \
"v_mfma_f32_16x16x32_bf16 v[76:79], a[152:155], v[56:59], v[76:79]\n" \
"ds_read_b128 a[144:147], v5 offset:53248\n" \
"v_mfma_f32_16x16x32_bf16 v[76:79], a[156:159], v[60:63], v[76:79]\n" \
"v_exp_f32 v125, v125\n" \
"v_mfma_f32_16x16x32_bf16 v[84:87], a[152:155], a[72:75], v[84:87]\n" \
"buffer_load_dwordx4 v6, s[40:43], s16 offen offset:3072 sc1 lds\n" \
"v_mfma_f32_16x16x32_bf16 v[84:87], a[156:159], a[76:79], v[84:87]\n" \
"v_exp_f32 v126, v126\n" \
"v_mfma_f32_16x16x32_bf16 v[92:95], a[152:155], a[88:91], v[92:95]\n" \
"ds_read_b128 a[148:151], v5 offset:54272\n" \
"v_cvt_pk_bf16_f32 v158, v124, v125\n" \
"v_mfma_f32_16x16x32_bf16 v[92:95], a[156:159], a[92:95], v[92:95]\n" \
"s_waitcnt lgkmcnt(4)\n" \
"v_exp_f32 v127, v127\n" \
"v_mfma_f32_16x16x32_bf16 v[160:163], v[180:183], v[144:147], v[160:163]\n" \
"v_cvt_pk_bf16_f32 v159, v126, v127\n" \
"v_mfma_f32_16x16x32_bf16 v[164:167], v[180:183], v[148:151], v[164:167]\n" \
"v_mfma_f32_16x16x32_bf16 v[168:171], v[180:183], v[152:155], v[168:171]\n" \
"v_exp_f32 v64, v64\n" \
"v_mfma_f32_16x16x32_bf16 v[172:175], v[180:183], v[156:159], v[172:175]\n" \
"v_mfma_f32_16x16x32_bf16 v[192:195], a[128:131], v[144:147], v[192:195]\n" \
"v_exp_f32 v65, v65\n" \
"v_mfma_f32_16x16x32_bf16 v[196:199], a[132:135], v[144:147], v[196:199]\n" \
"v_cvt_pk_bf16_f32 v128, v64, v65\n" \
"v_mfma_f32_16x16x32_bf16 v[224:227], a[128:131], v[148:151], v[224:227]\n" \
"ds_read_b128 a[152:155], v5 offset:55296\n" \
"v_mfma_f32_16x16x32_bf16 v[228:231], a[132:135], v[148:151], v[228:231]\n" \
"v_exp_f32 v66, v66\n" \
"v_mfma_f32_16x16x32_bf16 a[0:3], a[128:131], v[152:155], a[0:3]\n" \
"v_exp_f32 v67, v67\n" \
"v_mfma_f32_16x16x32_bf16 a[4:7], a[132:135], v[152:155], a[4:7]\n" \
"v_cvt_pk_bf16_f32 v129, v66, v67\n" \
"v_mfma_f32_16x16x32_bf16 a[32:35], a[128:131], v[156:159], a[32:35]\n" \
"ds_read_b128 a[156:159], v5 offset:56320\n" \
"v_mfma_f32_16x16x32_bf16 a[36:39], a[132:135], v[156:159], a[36:39]\n" \
"s_waitcnt vmcnt(12) lgkmcnt(4)\n" \
"v_exp_f32 v68, v68\n" \
"v_mfma_f32_16x16x32_bf16 v[200:203], a[136:139], v[144:147], v[200:203]\n" \
"s_barrier\n" \
"v_xor_b32 v5, 0x10000, v5\n" \
"v_mfma_f32_16x16x32_bf16 v[204:207], a[140:143], v[144:147], v[204:207]\n" \
"s_add_u32 s17, s17, s19\n" \
"v_exp_f32 v69, v69\n" \
"v_mfma_f32_16x16x32_bf16 v[232:235], a[136:139], v[148:151], v[232:235]\n" \
"ds_read_b128 a[128:131], v5 offset:0\n" \
"v_cvt_pk_bf16_f32 v130, v68, v69\n" \
"v_mfma_f32_16x16x32_bf16 v[236:239], a[140:143], v[148:151], v[236:239]\n" \
"s_add_u32 m0, s48, 16384\n" \
"v_exp_f32 v70, v70\n" \
"v_mfma_f32_16x16x32_bf16 a[8:11], a[136:139], v[152:155], a[8:11]\n" \
"buffer_load_dwordx4 v6, s[40:43], s17 offen offset:0 sc1 lds\n" \
"v_mfma_f32_16x16x32_bf16 a[12:15], a[140:143], v[152:155], a[12:15]\n" \
"v_exp_f32 v71, v71\n" \
"v_mfma_f32_16x16x32_bf16 a[40:43], a[136:139], v[156:159], a[40:43]\n" \
"ds_read_b128 a[132:135], v5 offset:1024\n" \
"v_cvt_pk_bf16_f32 v131, v70, v71\n" \
"v_mfma_f32_16x16x32_bf16 a[44:47], a[140:143], v[156:159], a[44:47]\n" \
"s_waitcnt lgkmcnt(4)\n" \
"v_exp_f32 v72, v72\n" \
"v_mfma_f32_16x16x32_bf16 v[208:211], a[144:147], v[144:147], v[208:211]\n" \
"v_exp_f32 v73, v73\n" \
"v_mfma_f32_16x16x32_bf16 v[212:215], a[148:151], v[144:147], v[212:215]\n" \
"v_cvt_pk_bf16_f32 v132, v72, v73\n" \
"v_mfma_f32_16x16x32_bf16 v[240:243], a[144:147], v[148:151], v[240:243]\n" \
"ds_read_b128 a[136:139], v5 offset:2048\n" \
"v_mfma_f32_16x16x32_bf16 v[244:247], a[148:151], v[148:151], v[244:247]\n" \
"v_exp_f32 v74, v74\n" \
"v_mfma_f32_16x16x32_bf16 a[16:19], a[144:147], v[152:155], a[16:19]\n" \
"buffer_load_dwordx4 v6, s[40:43], s17 offen offset:1024 sc1 lds\n" \
"v_mfma_f32_16x16x32_bf16 a[20:23], a[148:151], v[152:155], a[20:23]\n" \
"v_exp_f32 v75, v75\n" \
"v_mfma_f32_16x16x32_bf16 a[48:51], a[144:147], v[156:159], a[48:51]\n" \
"ds_read_b128 a[140:143], v5 offset:3072\n" \
"v_cvt_pk_bf16_f32 v133, v74, v75\n" \
"v_mfma_f32_16x16x32_bf16 a[52:55], a[148:151], v[156:159], a[52:55]\n" \
"s_waitcnt lgkmcnt(4)\n" \
"v_exp_f32 v76, v76\n" \
"v_mfma_f32_16x16x32_bf16 v[216:219], a[152:155], v[144:147], v[216:219]\n" \
"buffer_load_dwordx4 v6, s[40:43], s17 offen offset:2048 sc1 lds\n" \
"v_mfma_f32_16x16x32_bf16 v[220:223], a[156:159], v[144:147], v[220:223]\n" \
"s_add_u32 s44, s44, 1\n" \
"v_exp_f32 v77, v77\n" \
"v_mfma_f32_16x16x32_bf16 v[248:251], a[152:155], v[148:151], v[248:251]\n" \
"ds_read_b128 a[144:147], v5 offset:4096\n" \
"v_cvt_pk_bf16_f32 v134, v76, v77\n" \
"v_mfma_f32_16x16x32_bf16 v[252:255], a[156:159], v[148:151], v[252:255]\n" \
"v_exp_f32 v78, v78\n" \
"v_mfma_f32_16x16x32_bf16 a[24:27], a[152:155], v[152:155], a[24:27]\n" \
"buffer_load_dwordx4 v6, s[40:43], s17 offen offset:3072 sc1 lds\n" \
"v_mfma_f32_16x16x32_bf16 a[28:31], a[156:159], v[152:155], a[28:31]\n" \
"s_xor_b32 s48, s48, 0x10000\n" \
"v_exp_f32 v79, v79\n" \
"v_mfma_f32_16x16x32_bf16 a[56:59], a[152:155], v[156:159], a[56:59]\n" \
"ds_read_b128 a[148:151], v5 offset:5120\n" \
"v_cvt_pk_bf16_f32 v135, v78, v79\n" \
"v_mfma_f32_16x16x32_bf16 a[60:63], a[156:159], v[156:159], a[60:63]\n" \
"s_waitcnt lgkmcnt(4)\n" \
"LBOT_%=:\n" \
"s_cmp_ge_u32 s44, 14\n" \
"s_cbranch_scc1 LDISP_%=\n" \
"LEVEN_%=:\n" \
"v_mfma_f32_16x16x32_bf16 v[96:99], a[128:131], v[32:35], 0\n" \
"v_exp_f32 v80, v80\n" \
"v_mfma_f32_16x16x32_bf16 v[96:99], a[132:135], v[36:39], v[96:99]\n" \
"s_add_u32 s16, s16, s9\n" \
"v_mfma_f32_16x16x32_bf16 v[104:107], a[128:131], v[48:51], 0\n" \
"ds_read_b128 a[152:155], v5 offset:6144\n" \
"v_mfma_f32_16x16x32_bf16 v[104:107], a[132:135], v[52:55], v[104:107]\n" \
"s_add_u32 m0, s49, 32768\n" \
"v_exp_f32 v81, v81\n" \
"v_mfma_f32_16x16x32_bf16 v[112:115], a[128:131], a[64:67], 0\n" \
"buffer_load_dwordx4 v6, s[40:43], s16 offen offset:0 sc1 lds\n" \
"v_mfma_f32_16x16x32_bf16 v[112:115], a[132:135], a[68:71], v[112:115]\n" \
"v_exp_f32 v82, v82\n" \
"v_mfma_f32_16x16x32_bf16 v[120:123], a[128:131], a[80:83], 0\n" \
"ds_read_b128 a[156:159], v5 offset:7168\n" \
"v_cvt_pk_bf16_f32 v136, v80, v81\n" \
"v_mfma_f32_16x16x32_bf16 v[120:123], a[132:135], a[84:87], v[120:123]\n" \
"s_waitcnt lgkmcnt(4)\n" \
"v_exp_f32 v83, v83\n" \
"v_mfma_f32_16x16x32_bf16 v[96:99], a[136:139], v[40:43], v[96:99]\n" \
"v_exp_f32 v84, v84\n" \
"v_mfma_f32_16x16x32_bf16 v[96:99], a[140:143], v[44:47], v[96:99]\n" \
"v_cvt_pk_bf16_f32 v137, v82, v83\n" \
"v_mfma_f32_16x16x32_bf16 v[104:107], a[136:139], v[56:59], v[104:107]\n" \
"ds_read_b128 a[128:131], v5 offset:16384\n" \
"v_mfma_f32_16x16x32_bf16 v[104:107], a[140:143], v[60:63], v[104:107]\n" \
"v_exp_f32 v85, v85\n" \
"v_mfma_f32_16x16x32_bf16 v[112:115], a[136:139], a[72:75], v[112:115]\n" \
"buffer_load_dwordx4 v6, s[40:43], s16 offen offset:1024 sc1 lds\n" \
"v_mfma_f32_16x16x32_bf16 v[112:115], a[140:143], a[76:79], v[112:115]\n" \
"v_exp_f32 v86, v86\n" \
"v_mfma_f32_16x16x32_bf16 v[120:123], a[136:139], a[88:91], v[120:123]\n" \
"ds_read_b128 a[132:135], v5 offset:17408\n" \
"v_cvt_pk_bf16_f32 v138, v84, v85\n" \
"v_mfma_f32_16x16x32_bf16 v[120:123], a[140:143], a[92:95], v[120:123]\n" \
"s_waitcnt lgkmcnt(4)\n" \
"v_exp_f32 v87, v87\n" \
"v_mfma_f32_16x16x32_bf16 v[100:103], a[144:147], v[32:35], 0\n" \
"v_exp_f32 v88, v88\n" \
"v_mfma_f32_16x16x32_bf16 v[100:103], a[148:151], v[36:39], v[100:103]\n" \
"v_cvt_pk_bf16_f32 v139, v86, v87\n" \
"v_mfma_f32_16x16x32_bf16 v[108:111], a[144:147], v[48:51], 0\n" \
"ds_read_b128 a[136:139], v5 offset:18432\n" \
"v_mfma_f32_16x16x32_bf16 v[108:111], a[148:151], v[52:55], v[108:111]\n" \
"v_exp_f32 v89, v89\n" \
"v_mfma_f32_16x16x32_bf16 v[116:119], a[144:147], a[64:67], 0\n" \
"buffer_load_dwordx4 v6, s[40:43], s16 offen offset:2048 sc1 lds\n" \
"v_mfma_f32_16x16x32_bf16 v[116:119], a[148:151], a[68:71], v[116:119]\n" \
"v_exp_f32 v90, v90\n" \
"v_mfma_f32_16x16x32_bf16 v[124:127], a[144:147], a[80:83], 0\n" \
"ds_read_b128 a[140:143], v5 offset:19456\n" \
"v_cvt_pk_bf16_f32 v140, v88, v89\n" \
"v_mfma_f32_16x16x32_bf16 v[124:127], a[148:151], a[84:87], v[124:127]\n" \
"s_waitcnt lgkmcnt(4)\n" \
"v_exp_f32 v91, v91\n" \
"v_mfma_f32_16x16x32_bf16 v[100:103], a[152:155], v[40:43], v[100:103]\n" \
"v_exp_f32 v92, v92\n" \
"v_mfma_f32_16x16x32_bf16 v[100:103], a[156:159], v[44:47], v[100:103]\n" \
"v_cvt_pk_bf16_f32 v141, v90, v91\n" \
"v_mfma_f32_16x16x32_bf16 v[108:111], a[152:155], v[56:59], v[108:111]\n" \
"ds_read_b128 a[144:147], v5 offset:20480\n" \
"v_mfma_f32_16x16x32_bf16 v[108:111], a[156:159], v[60:63], v[108:111]\n" \
"v_exp_f32 v93, v93\n" \
"v_mfma_f32_16x16x32_bf16 v[116:119], a[152:155], a[72:75], v[116:119]\n" \
"buffer_load_dwordx4 v6, s[40:43], s16 offen offset:3072 sc1 lds\n" \
"v_mfma_f32_16x16x32_bf16 v[116:119], a[156:159], a[76:79], v[116:119]\n" \
"v_exp_f32 v94, v94\n" \
"v_mfma_f32_16x16x32_bf16 v[124:127], a[152:155], a[88:91], v[124:127]\n" \
"ds_read_b128 a[148:151], v5 offset:21504\n" \
"v_cvt_pk_bf16_f32 v142, v92, v93\n" \
"v_mfma_f32_16x16x32_bf16 v[124:127], a[156:159], a[92:95], v[124:127]\n" \
"s_waitcnt lgkmcnt(4)\n" \
"v_exp_f32 v95, v95\n" \
"v_mfma_f32_16x16x32_bf16 v[160:163], v[180:183], v[128:131], v[160:163]\n" \
"v_cvt_pk_bf16_f32 v143, v94, v95\n" \
"v_mfma_f32_16x16x32_bf16 v[164:167], v[180:183], v[132:135], v[164:167]\n" \
"v_mfma_f32_16x16x32_bf16 v[168:171], v[180:183], v[136:139], v[168:171]\n" \
"v_exp_f32 v96, v96\n" \
"v_mfma_f32_16x16x32_bf16 v[172:175], v[180:183], v[140:143], v[172:175]\n" \
"v_mfma_f32_16x16x32_bf16 v[192:195], a[128:131], v[128:131], v[192:195]\n" \
"v_exp_f32 v97, v97\n" \
"v_mfma_f32_16x16x32_bf16 v[196:199], a[132:135], v[128:131], v[196:199]\n" \
"v_cvt_pk_bf16_f32 v144, v96, v97\n" \
"v_mfma_f32_16x16x32_bf16 v[224:227], a[128:131], v[132:135], v[224:227]\n" \
"ds_read_b128 a[152:155], v5 offset:22528\n" \
"v_mfma_f32_16x16x32_bf16 v[228:231], a[132:135], v[132:135], v[228:231]\n" \
"v_exp_f32 v98, v98\n" \
"v_mfma_f32_16x16x32_bf16 a[0:3], a[128:131], v[136:139], a[0:3]\n" \
"v_exp_f32 v99, v99\n" \
"v_mfma_f32_16x16x32_bf16 a[4:7], a[132:135], v[136:139], a[4:7]\n" \
"v_cvt_pk_bf16_f32 v145, v98, v99\n" \
"v_mfma_f32_16x16x32_bf16 a[32:35], a[128:131], v[140:143], a[32:35]\n" \
"ds_read_b128 a[156:159], v5 offset:23552\n" \
"v_mfma_f32_16x16x32_bf16 a[36:39], a[132:135], v[140:143], a[36:39]\n" \
"s_waitcnt vmcnt(12) lgkmcnt(4)\n" \
"v_exp_f32 v100, v100\n" \
"v_mfma_f32_16x16x32_bf16 v[200:203], a[136:139], v[128:131], v[200:203]\n" \
"s_barrier\n" \
"v_exp_f32 v101, v101\n" \
"v_mfma_f32_16x16x32_bf16 v[204:207], a[140:143], v[128:131], v[204:207]\n" \
"s_add_u32 s17, s17, s19\n" \
"v_cvt_pk_bf16_f32 v146, v100, v101\n" \
"v_mfma_f32_16x16x32_bf16 v[232:235], a[136:139], v[132:135], v[232:235]\n" \
"ds_read_b128 a[128:131], v5 offset:32768\n" \
"v_mfma_f32_16x16x32_bf16 v[236:239], a[140:143], v[132:135], v[236:239]\n" \
"s_add_u32 m0, s49, 49152\n" \
"v_exp_f32 v102, v102\n" \
"v_mfma_f32_16x16x32_bf16 a[8:11], a[136:139], v[136:139], a[8:11]\n" \
"buffer_load_dwordx4 v6, s[40:43], s17 offen offset:0 sc1 lds\n" \
"v_mfma_f32_16x16x32_bf16 a[12:15], a[140:143], v[136:139], a[12:15]\n" \
"v_exp_f32 v103, v103\n" \
"v_mfma_f32_16x16x32_bf16 a[40:43], a[136:139], v[140:143], a[40:43]\n" \
"ds_read_b128 a[132:135], v5 offset:33792\n" \
"v_cvt_pk_bf16_f32 v147, v102, v103\n" \
"v_mfma_f32_16x16x32_bf16 a[44:47], a[140:143], v[140:143], a[44:47]\n" \
"s_waitcnt lgkmcnt(4)\n" \
"v_exp_f32 v104, v104\n" \
"v_mfma_f32_16x16x32_bf16 v[208:211], a[144:147], v[128:131], v[208:211]\n" \
"v_exp_f32 v105, v105\n" \
"v_mfma_f32_16x16x32_bf16 v[212:215], a[148:151], v[128:131], v[212:215]\n" \
"v_cvt_pk_bf16_f32 v148, v104, v105\n" \
"v_mfma_f32_16x16x32_bf16 v[240:243], a[144:147], v[132:135], v[240:243]\n" \
"ds_read_b128 a[136:139], v5 offset:34816\n" \
"v_mfma_f32_16x16x32_bf16 v[244:247], a[148:151], v[132:135], v[244:247]\n" \
"v_exp_f32 v106, v106\n" \
"v_mfma_f32_16x16x32_bf16 a[16:19], a[144:147], v[136:139], a[16:19]\n" \
"buffer_load_dwordx4 v6, s[40:43], s17 offen offset:1024 sc1 lds\n" \
"v_mfma_f32_16x16x32_bf16 a[20:23], a[148:151], v[136:139], a[20:23]\n" \
"v_exp_f32 v107, v107\n" \
"v_mfma_f32_16x16x32_bf16 a[48:51], a[144:147], v[140:143], a[48:51]\n" \
"ds_read_b128 a[140:143], v5 offset:35840\n" \
"v_cvt_pk_bf16_f32 v149, v106, v107\n" \
"v_mfma_f32_16x16x32_bf16 a[52:55], a[148:151], v[140:143], a[52:55]\n" \
"s_waitcnt lgkmcnt(4)\n" \
"v_exp_f32 v108, v108\n" \
"v_mfma_f32_16x16x32_bf16 v[216:219], a[152:155], v[128:131], v[216:219]\n" \
"buffer_load_dwordx4 v6, s[40:43], s17 offen offset:2048 sc1 lds\n" \
"v_mfma_f32_16x16x32_bf16 v[220:223], a[156:159], v[128:131], v[220:223]\n" \
"v_exp_f32 v109, v109\n" \
"v_mfma_f32_16x16x32_bf16 v[248:251], a[152:155], v[132:135], v[248:251]\n" \
"ds_read_b128 a[144:147], v5 offset:36864\n" \
"v_cvt_pk_bf16_f32 v150, v108, v109\n" \
"v_mfma_f32_16x16x32_bf16 v[252:255], a[156:159], v[132:135], v[252:255]\n" \
"v_exp_f32 v110, v110\n" \
"v_mfma_f32_16x16x32_bf16 a[24:27], a[152:155], v[136:139], a[24:27]\n" \
"buffer_load_dwordx4 v6, s[40:43], s17 offen offset:3072 sc1 lds\n" \
"v_mfma_f32_16x16x32_bf16 a[28:31], a[156:159], v[136:139], a[28:31]\n" \
"v_exp_f32 v111, v111\n" \
"v_mfma_f32_16x16x32_bf16 a[56:59], a[152:155], v[140:143], a[56:59]\n" \
"ds_read_b128 a[148:151], v5 offset:37888\n" \
"v_cvt_pk_bf16_f32 v151, v110, v111\n" \
"v_mfma_f32_16x16x32_bf16 a[60:63], a[156:159], v[140:143], a[60:63]\n" \
"s_waitcnt lgkmcnt(4)\n" \
"s_cmp_lg_u32 s44, s46\n" \
"s_cbranch_scc1 LODD_%=\n" \
"LMID_%=:\n" \
"s_cmp_eq_u32 s44, s45\n" \
"s_cbranch_scc1 LWS_%=\n" \
"s_cmp_lt_u32 %2, 2\n" \
"s_cbranch_scc1 LS1_%=\n" \
"s_cmp_eq_u32 s44, 15\n" \
"s_cbranch_scc1 LS1_%=\n" \
"s_lshl_b32 s46, s7, 2\n" \
"s_add_u32 s46, s46, %2\n" \
"s_waitcnt vmcnt(8)\n" \
"v_cmp_ne_u64 vcc, s[14:15], v[18:19]\n" \
"s_cmp_eq_u32 s46, 30\ns_cselect_b32 s46, 15, 99\n" \
"s_cbranch_vccnz LPE0_%=\nLPE0R_%=:\n" \
"s_branch LODD_%=\n" \
".p2align 6\n" \
"LH16_%=:\ns_cbranch_execz LH20_%=\n" \
"s_nop 0\n" \
"LWS_%=:\n" \
"v_mfma_f32_16x16x32_bf16 v[64:67], a[128:131], v[32:35], 0\n" \
"s_mov_b32 s81, 0\n" \
"v_exp_f32 v112, v112\n" \
"v_mfma_f32_16x16x32_bf16 v[64:67], a[132:135], v[36:39], v[64:67]\n" \
"s_add_u32 s16, s16, s9\n" \
"v_mov_b32 v12, v30\n" \
"v_mov_b32 v28, -1\n" \
"v_mfma_f32_16x16x32_bf16 v[72:75], a[128:131], v[48:51], 0\n" \
"ds_read_b128 a[152:155], v5 offset:38912\n" \
"v_mov_b32 v29, -1\n" \
"v_mfma_f32_16x16x32_bf16 v[72:75], a[132:135], v[52:55], v[72:75]\n" \
"s_add_u32 m0, s48, 0\n" \
"v_exp_f32 v113, v113\n" \
"v_mfma_f32_16x16x32_bf16 v[80:83], a[128:131], a[64:67], 0\n" \
"buffer_load_dwordx4 v6, s[40:43], s16 offen offset:0 sc1 lds\n" \
"v_mfma_f32_16x16x32_bf16 v[80:83], a[132:135], a[68:71], v[80:83]\n" \
"v_exp_f32 v114, v114\n" \
"v_mfma_f32_16x16x32_bf16 v[88:91], a[128:131], a[80:83], 0\n" \
"ds_read_b128 a[156:159], v5 offset:39936\n" \
"v_cvt_pk_bf16_f32 v152, v112, v113\n" \
"v_mfma_f32_16x16x32_bf16 v[88:91], a[132:135], a[84:87], v[88:91]\n" \
"s_waitcnt lgkmcnt(4)\n" \
"v_exp_f32 v115, v115\n" \
"s_add_u32 m0, s79, 131072\n" \
"v_mfma_f32_16x16x32_bf16 v[64:67], a[136:139], v[40:43], v[64:67]\n" \
"buffer_load_dwordx4 v12, s[28:31], s81 offen offset:0 lds\n" \
"v_mfma_f32_16x16x32_bf16 v[64:67], a[140:143], v[44:47], v[64:67]\n" \
"s_xor_b32 s49, s49, 0x10000\n" \
"v_exp_f32 v116, v116\n" \
"v_mfma_f32_16x16x32_bf16 v[72:75], a[136:139], v[56:59], v[72:75]\n" \
"ds_read_b128 a[128:131], v5 offset:49152\n" \
"v_cvt_pk_bf16_f32 v153, v114, v115\n" \
"v_mfma_f32_16x16x32_bf16 v[72:75], a[140:143], v[60:63], v[72:75]\n" \
"s_add_u32 m0, s48, 0\n" \
"v_exp_f32 v117, v117\n" \
"v_mfma_f32_16x16x32_bf16 v[80:83], a[136:139], a[72:75], v[80:83]\n" \
"buffer_load_dwordx4 v6, s[40:43], s16 offen offset:1024 sc1 lds\n" \
"v_mfma_f32_16x16x32_bf16 v[80:83], a[140:143], a[76:79], v[80:83]\n" \
"v_exp_f32 v118, v118\n" \
"v_mfma_f32_16x16x32_bf16 v[88:91], a[136:139], a[88:91], v[88:91]\n" \
"ds_read_b128 a[132:135], v5 offset:50176\n" \
"v_cvt_pk_bf16_f32 v154, v116, v117\n" \
"v_mfma_f32_16x16x32_bf16 v[88:91], a[140:143], a[92:95], v[88:91]\n" \
"s_waitcnt lgkmcnt(4)\n" \
"v_exp_f32 v119, v119\n" \
"s_add_u32 m0, s79, 131968\n" \
"v_mfma_f32_16x16x32_bf16 v[68:71], a[144:147], v[32:35], 0\n" \
"buffer_load_dwordx4 v12, s[28:31], s81 offen offset:128 lds\n" \
"v_mfma_f32_16x16x32_bf16 v[68:71], a[148:151], v[36:39], v[68:71]\n" \
"s_mov_b32 s81, 65536\n" \
"v_exp_f32 v120, v120\n" \
"v_mfma_f32_16x16x32_bf16 v[76:79], a[144:147], v[48:51], 0\n" \
"ds_read_b128 a[136:139], v5 offset:51200\n" \
"v_cvt_pk_bf16_f32 v155, v118, v119\n" \
"v_mfma_f32_16x16x32_bf16 v[76:79], a[148:151], v[52:55], v[76:79]\n" \
"s_add_u32 m0, s48, 0\n" \
"v_exp_f32 v121, v121\n" \
"v_mfma_f32_16x16x32_bf16 v[84:87], a[144:147], a[64:67], 0\n" \
"buffer_load_dwordx4 v6, s[40:43], s16 offen offset:2048 sc1 lds\n" \
"v_mfma_f32_16x16x32_bf16 v[84:87], a[148:151], a[68:71], v[84:87]\n" \
"v_exp_f32 v122, v122\n" \
"v_mfma_f32_16x16x32_bf16 v[92:95], a[144:147], a[80:83], 0\n" \
"ds_read_b128 a[140:143], v5 offset:52224\n" \
"v_cvt_pk_bf16_f32 v156, v120, v121\n" \
"v_mfma_f32_16x16x32_bf16 v[92:95], a[148:151], a[84:87], v[92:95]\n" \
"s_waitcnt lgkmcnt(4)\n" \
"v_exp_f32 v123, v123\n" \
"s_add_u32 m0, s79, 139264\n" \
"v_mfma_f32_16x16x32_bf16 v[68:71], a[152:155], v[40:43], v[68:71]\n" \
"buffer_load_dwordx4 v12, s[28:31], s81 offen offset:0 lds\n" \
"v_mfma_f32_16x16x32_bf16 v[68:71], a[156:159], v[44:47], v[68:71]\n" \
"v_exp_f32 v124, v124\n" \
"v_mfma_f32_16x16x32_bf16 v[76:79], a[152:155], v[56:59], v[76:79]\n" \
"ds_read_b128 a[144:147], v5 offset:53248\n" \
"v_cvt_pk_bf16_f32 v157, v122, v123\n" \
"v_mfma_f32_16x16x32_bf16 v[76:79], a[156:159], v[60:63], v[76:79]\n" \
"s_add_u32 m0, s48, 0\n" \
"v_exp_f32 v125, v125\n" \
"v_mfma_f32_16x16x32_bf16 v[84:87], a[152:155], a[72:75], v[84:87]\n" \
"buffer_load_dwordx4 v6, s[40:43], s16 offen offset:3072 sc1 lds\n" \
"v_mfma_f32_16x16x32_bf16 v[84:87], a[156:159], a[76:79], v[84:87]\n" \
"v_exp_f32 v126, v126\n" \
"v_mfma_f32_16x16x32_bf16 v[92:95], a[152:155], a[88:91], v[92:95]\n" \
"ds_read_b128 a[148:151], v5 offset:54272\n" \
"v_cvt_pk_bf16_f32 v158, v124, v125\n" \
"v_mfma_f32_16x16x32_bf16 v[92:95], a[156:159], a[92:95], v[92:95]\n" \
"s_waitcnt lgkmcnt(4)\n" \
"v_exp_f32 v127, v127\n" \
"s_add_u32 m0, s79, 140160\n" \
"v_mfma_f32_16x16x32_bf16 v[160:163], v[180:183], v[144:147], v[160:163]\n" \
"buffer_load_dwordx4 v12, s[28:31], s81 offen offset:128 lds\n" \
"v_mfma_f32_16x16x32_bf16 v[164:167], v[180:183], v[148:151], v[164:167]\n" \
"s_mov_b32 s81, 131072\n" \
"v_cvt_pk_bf16_f32 v159, v126, v127\n" \
"v_mov_b32 v30, -1\n" \
"v_mfma_f32_16x16x32_bf16 v[168:171], v[180:183], v[152:155], v[168:171]\n" \
"v_exp_f32 v64, v64\n" \
"v_mfma_f32_16x16x32_bf16 v[172:175], v[180:183], v[156:159], v[172:175]\n" \
"s_add_u32 s80, s78, 4096\n" \
"v_mov_b32 v31, -1\n" \
"v_bfe_u32 v189, v4, 7, 1\n" \
"v_mfma_f32_16x16x32_bf16 v[192:195], a[128:131], v[144:147], v[192:195]\n" \
"buffer_store_dwordx4 v[28:31], v4, s[36:39], s80 offen offset:2048 sc1\n" \
"v_mfma_f32_16x16x32_bf16 v[196:199], a[132:135], v[144:147], v[196:199]\n" \
"v_bfe_u32 v190, v4, 4, 3\n" \
"v_lshl_add_u32 v189, v190, 1, v189\n" \
"v_mfma_f32_16x16x32_bf16 v[224:227], a[128:131], v[148:151], v[224:227]\n" \
"ds_read_b128 a[152:155], v5 offset:55296\n" \
"v_lshrrev_b32 v190, 8, v4\n" \
"v_mfma_f32_16x16x32_bf16 v[228:231], a[132:135], v[148:151], v[228:231]\n" \
"v_exp_f32 v65, v65\n" \
"v_mfma_f32_16x16x32_bf16 a[0:3], a[128:131], v[152:155], a[0:3]\n" \
"v_exp_f32 v66, v66\n" \
"v_mfma_f32_16x16x32_bf16 a[4:7], a[132:135], v[152:155], a[4:7]\n" \
"v_exp_f32 v67, v67\n" \
"v_mfma_f32_16x16x32_bf16 a[32:35], a[128:131], v[156:159], a[32:35]\n" \
"ds_read_b128 a[156:159], v5 offset:56320\n" \
"v_lshlrev_b32 v188, 11, v189\n" \
"v_mfma_f32_16x16x32_bf16 a[36:39], a[132:135], v[156:159], a[36:39]\n" \
"s_waitcnt vmcnt(17) lgkmcnt(4)\n" \
"v_exp_f32 v68, v68\n" \
"v_mfma_f32_16x16x32_bf16 v[200:203], a[136:139], v[144:147], v[200:203]\n" \
"s_barrier\n" \
"v_xor_b32 v5, 0x10000, v5\n" \
"v_and_b32 v189, 1, v190\n" \
"v_mfma_f32_16x16x32_bf16 v[204:207], a[140:143], v[144:147], v[204:207]\n" \
"s_add_u32 s17, s17, s19\n" \
"v_exp_f32 v69, v69\n" \
"v_mfma_f32_16x16x32_bf16 v[232:235], a[136:139], v[148:151], v[232:235]\n" \
"ds_read_b128 a[128:131], v5 offset:0\n" \
"v_lshl_add_u32 v188, v189, 5, v188\n" \
"v_mfma_f32_16x16x32_bf16 v[236:239], a[140:143], v[148:151], v[236:239]\n" \
"s_add_u32 m0, s48, 16384\n" \
"v_exp_f32 v70, v70\n" \
"v_mfma_f32_16x16x32_bf16 a[8:11], a[136:139], v[152:155], a[8:11]\n" \
"buffer_load_dwordx4 v6, s[40:43], s17 offen offset:0 sc1 lds\n" \
"v_mfma_f32_16x16x32_bf16 a[12:15], a[140:143], v[152:155], a[12:15]\n" \
"v_exp_f32 v71, v71\n" \
"v_mfma_f32_16x16x32_bf16 a[40:43], a[136:139], v[156:159], a[40:43]\n" \
"ds_read_b128 a[132:135], v5 offset:1024\n" \
"v_lshrrev_b32 v189, 1, v190\n" \
"v_mfma_f32_16x16x32_bf16 a[44:47], a[140:143], v[156:159], a[44:47]\n" \
"s_waitcnt lgkmcnt(4)\n" \
"v_exp_f32 v72, v72\n" \
"v_mfma_f32_16x16x32_bf16 v[208:211], a[144:147], v[144:147], v[208:211]\n" \
"v_exp_f32 v73, v73\n" \
"v_mfma_f32_16x16x32_bf16 v[212:215], a[148:151], v[144:147], v[212:215]\n" \
"v_mfma_f32_16x16x32_bf16 v[240:243], a[144:147], v[148:151], v[240:243]\n" \
"ds_read_b128 a[136:139], v5 offset:2048\n" \
"v_lshl_add_u32 v188, v189, 4, v188\n" \
"v_mfma_f32_16x16x32_bf16 v[244:247], a[148:151], v[148:151], v[244:247]\n" \
"s_add_u32 m0, s48, 16384\n" \
"v_exp_f32 v74, v74\n" \
"v_mfma_f32_16x16x32_bf16 a[16:19], a[144:147], v[152:155], a[16:19]\n" \
"buffer_load_dwordx4 v6, s[40:43], s17 offen offset:1024 sc1 lds\n" \
"v_mfma_f32_16x16x32_bf16 a[20:23], a[148:151], v[152:155], a[20:23]\n" \
"v_exp_f32 v75, v75\n" \
"v_mfma_f32_16x16x32_bf16 a[48:51], a[144:147], v[156:159], a[48:51]\n" \
"ds_read_b128 a[140:143], v5 offset:3072\n" \
"v_add_u32 v190, s83, v188\n" \
"v_mfma_f32_16x16x32_bf16 a[52:55], a[148:151], v[156:159], a[52:55]\n" \
"s_waitcnt lgkmcnt(4)\n" \
"s_add_u32 m0, s48, 16384\n" \
"v_add_u32 v188, s82, v188\n" \
"v_mfma_f32_16x16x32_bf16 v[216:219], a[152:155], v[144:147], v[216:219]\n" \
"buffer_load_dwordx4 v6, s[40:43], s17 offen offset:2048 sc1 lds\n" \
"v_mfma_f32_16x16x32_bf16 v[220:223], a[156:159], v[144:147], v[220:223]\n" \
"s_add_u32 s44, s44, 1\n" \
"v_exp_f32 v76, v76\n" \
"v_mfma_f32_16x16x32_bf16 v[248:251], a[152:155], v[148:151], v[248:251]\n" \
"ds_read_b128 a[144:147], v5 offset:4096\n" \
"v_add_u32 v189, 0x8000, v188\n" \
"v_mfma_f32_16x16x32_bf16 v[252:255], a[156:159], v[148:151], v[252:255]\n" \
"s_add_u32 m0, s48, 16384\n" \
"v_exp_f32 v77, v77\n" \
"v_mfma_f32_16x16x32_bf16 a[24:27], a[152:155], v[152:155], a[24:27]\n" \
"buffer_load_dwordx4 v6, s[40:43], s17 offen offset:3072 sc1 lds\n" \
"v_mfma_f32_16x16x32_bf16 a[28:31], a[156:159], v[152:155], a[28:31]\n" \
"s_xor_b32 s48, s48, 0x10000\n" \
"v_exp_f32 v78, v78\n" \
"v_mfma_f32_16x16x32_bf16 a[56:59], a[152:155], v[156:159], a[56:59]\n" \
"ds_read_b128 a[148:151], v5 offset:5120\n" \
"v_add_u32 v191, 0x8000, v190\n" \
"v_exp_f32 v79, v79\n" \
"v_mfma_f32_16x16x32_bf16 a[60:63], a[156:159], v[156:159], a[60:63]\n" \
"s_waitcnt lgkmcnt(4)\n" \
"v_cvt_pk_bf16_f32 v128, v64, v65\n" \
"v_cvt_pk_bf16_f32 v129, v66, v67\n" \
"v_cvt_pk_bf16_f32 v130, v68, v69\n" \
"v_cvt_pk_bf16_f32 v131, v70, v71\n" \
"v_cvt_pk_bf16_f32 v132, v72, v73\n" \
"v_cvt_pk_bf16_f32 v133, v74, v75\n" \
"v_cvt_pk_bf16_f32 v134, v76, v77\n" \
"v_cvt_pk_bf16_f32 v135, v78, v79\n" \
"v_mfma_f32_16x16x32_bf16 v[96:99], a[128:131], v[32:35], 0\n" \
"global_load_dwordx4 a[96:99], v188, s[60:61]\n" \
"v_mfma_f32_16x16x32_bf16 v[96:99], a[132:135], v[36:39], v[96:99]\n" \
"s_add_u32 s16, s16, s9\n" \
"v_exp_f32 v80, v80\n" \
"v_mfma_f32_16x16x32_bf16 v[104:107], a[128:131], v[48:51], 0\n" \
"ds_read_b128 a[152:155], v5 offset:6144\n" \
"v_mfma_f32_16x16x32_bf16 v[104:107], a[132:135], v[52:55], v[104:107]\n" \
"s_add_u32 m0, s49, 32768\n" \
"v_exp_f32 v81, v81\n" \
"v_mfma_f32_16x16x32_bf16 v[112:115], a[128:131], a[64:67], 0\n" \
"buffer_load_dwordx4 v6, s[40:43], s16 offen offset:0 sc1 lds\n" \
"v_mfma_f32_16x16x32_bf16 v[112:115], a[132:135], a[68:71], v[112:115]\n" \
"v_exp_f32 v82, v82\n" \
"v_mfma_f32_16x16x32_bf16 v[120:123], a[128:131], a[80:83], 0\n" \
"ds_read_b128 a[156:159], v5 offset:7168\n" \
"v_cvt_pk_bf16_f32 v136, v80, v81\n" \
"v_mfma_f32_16x16x32_bf16 v[120:123], a[132:135], a[84:87], v[120:123]\n" \
"s_waitcnt lgkmcnt(4)\n" \
"v_exp_f32 v83, v83\n" \
"s_add_u32 m0, s79, 147456\n" \
"v_mfma_f32_16x16x32_bf16 v[96:99], a[136:139], v[40:43], v[96:99]\n" \
"buffer_load_dwordx4 v12, s[28:31], s81 offen offset:0 lds\n" \
"v_mfma_f32_16x16x32_bf16 v[96:99], a[140:143], v[44:47], v[96:99]\n" \
"v_exp_f32 v84, v84\n" \
"v_mfma_f32_16x16x32_bf16 v[104:107], a[136:139], v[56:59], v[104:107]\n" \
"ds_read_b128 a[128:131], v5 offset:16384\n" \
"v_cvt_pk_bf16_f32 v137, v82, v83\n" \
"v_mfma_f32_16x16x32_bf16 v[104:107], a[140:143], v[60:63], v[104:107]\n" \
"s_add_u32 m0, s49, 32768\n" \
"v_exp_f32 v85, v85\n" \
"v_mfma_f32_16x16x32_bf16 v[112:115], a[136:139], a[72:75], v[112:115]\n" \
"buffer_load_dwordx4 v6, s[40:43], s16 offen offset:1024 sc1 lds\n" \
"v_mfma_f32_16x16x32_bf16 v[112:115], a[140:143], a[76:79], v[112:115]\n" \
"v_exp_f32 v86, v86\n" \
"v_mfma_f32_16x16x32_bf16 v[120:123], a[136:139], a[88:91], v[120:123]\n" \
"ds_read_b128 a[132:135], v5 offset:17408\n" \
"v_cvt_pk_bf16_f32 v138, v84, v85\n" \
"v_mfma_f32_16x16x32_bf16 v[120:123], a[140:143], a[92:95], v[120:123]\n" \
"s_waitcnt lgkmcnt(4)\n" \
"v_exp_f32 v87, v87\n" \
"s_add_u32 m0, s79, 148352\n" \
"v_mfma_f32_16x16x32_bf16 v[100:103], a[144:147], v[32:35], 0\n" \
"buffer_load_dwordx4 v12, s[28:31], s81 offen offset:128 lds\n" \
"v_mfma_f32_16x16x32_bf16 v[100:103], a[148:151], v[36:39], v[100:103]\n" \
"s_mov_b32 s81, 196608\n" \
"v_exp_f32 v88, v88\n" \
"v_mfma_f32_16x16x32_bf16 v[108:111], a[144:147], v[48:51], 0\n" \
"ds_read_b128 a[136:139], v5 offset:18432\n" \
"v_cvt_pk_bf16_f32 v139, v86, v87\n" \
"v_mfma_f32_16x16x32_bf16 v[108:111], a[148:151], v[52:55], v[108:111]\n" \
"s_add_u32 m0, s49, 32768\n" \
"v_exp_f32 v89, v89\n" \
"v_mfma_f32_16x16x32_bf16 v[116:119], a[144:147], a[64:67], 0\n" \
"buffer_load_dwordx4 v6, s[40:43], s16 offen offset:2048 sc1 lds\n" \
"v_mfma_f32_16x16x32_bf16 v[116:119], a[148:151], a[68:71], v[116:119]\n" \
"v_exp_f32 v90, v90\n" \
"v_mfma_f32_16x16x32_bf16 v[124:127], a[144:147], a[80:83], 0\n" \
"ds_read_b128 a[140:143], v5 offset:19456\n" \
"v_cvt_pk_bf16_f32 v140, v88, v89\n" \
"v_mfma_f32_16x16x32_bf16 v[124:127], a[148:151], a[84:87], v[124:127]\n" \
"s_waitcnt lgkmcnt(4)\n" \
"v_exp_f32 v91, v91\n" \
"s_add_u32 m0, s79, 155648\n" \
"v_mfma_f32_16x16x32_bf16 v[100:103], a[152:155], v[40:43], v[100:103]\n" \
"buffer_load_dwordx4 v12, s[28:31], s81 offen offset:0 lds\n" \
"v_mfma_f32_16x16x32_bf16 v[100:103], a[156:159], v[44:47], v[100:103]\n" \
"v_exp_f32 v92, v92\n" \
"v_mfma_f32_16x16x32_bf16 v[108:111], a[152:155], v[56:59], v[108:111]\n" \
"ds_read_b128 a[144:147], v5 offset:20480\n" \
"v_cvt_pk_bf16_f32 v141, v90, v91\n" \
"v_mfma_f32_16x16x32_bf16 v[108:111], a[156:159], v[60:63], v[108:111]\n" \
"s_add_u32 m0, s49, 32768\n" \
"v_exp_f32 v93, v93\n" \
"v_mfma_f32_16x16x32_bf16 v[116:119], a[152:155], a[72:75], v[116:119]\n" \
"buffer_load_dwordx4 v6, s[40:43], s16 offen offset:3072 sc1 lds\n" \
"v_mfma_f32_16x16x32_bf16 v[116:119], a[156:159], a[76:79], v[116:119]\n" \
"LH17_%=:\ns_cbranch_execz LH21_%=\n" \
"v_exp_f32 v94, v94\n" \
"v_mfma_f32_16x16x32_bf16 v[124:127], a[152:155], a[88:91], v[124:127]\n" \
"ds_read_b128 a[148:151], v5 offset:21504\n" \
"v_cvt_pk_bf16_f32 v142, v92, v93\n" \
"v_mfma_f32_16x16x32_bf16 v[124:127], a[156:159], a[92:95], v[124:127]\n" \
"s_waitcnt lgkmcnt(4)\n" \
"v_exp_f32 v95, v95\n" \
"s_add_u32 m0, s79, 156544\n" \
"v_mfma_f32_16x16x32_bf16 v[160:163], v[180:183], v[128:131], v[160:163]\n" \
"buffer_load_dwordx4 v12, s[28:31], s81 offen offset:128 lds\n" \
"v_mfma_f32_16x16x32_bf16 v[164:167], v[180:183], v[132:135], v[164:167]\n" \
"s_mov_b32 s81, 262144\n" \
"v_cvt_pk_bf16_f32 v143, v94, v95\n" \
"v_mfma_f32_16x16x32_bf16 v[168:171], v[180:183], v[136:139], v[168:171]\n" \
"buffer_store_dwordx4 v[28:31], v4, s[36:39], s80 offen offset:3072 sc1\n" \
"v_mfma_f32_16x16x32_bf16 v[172:175], v[180:183], v[140:143], v[172:175]\n" \
"s_add_u32 s80, s78, 8192\n" \
"v_exp_f32 v96, v96\n" \
"v_mfma_f32_16x16x32_bf16 v[192:195], a[128:131], v[128:131], v[192:195]\n" \
"global_load_dwordx4 a[100:103], v189, s[60:61]\n" \
"v_mfma_f32_16x16x32_bf16 v[196:199], a[132:135], v[128:131], v[196:199]\n" \
"v_exp_f32 v97, v97\n" \
"v_mfma_f32_16x16x32_bf16 v[224:227], a[128:131], v[132:135], v[224:227]\n" \
"ds_read_b128 a[152:155], v5 offset:22528\n" \
"v_cvt_pk_bf16_f32 v144, v96, v97\n" \
"v_mfma_f32_16x16x32_bf16 v[228:231], a[132:135], v[132:135], v[228:231]\n" \
"buffer_store_dwordx4 v[28:31], v4, s[36:39], s80 offen offset:0 sc1\n" \
"v_mfma_f32_16x16x32_bf16 a[0:3], a[128:131], v[136:139], a[0:3]\n" \
"v_exp_f32 v98, v98\n" \
"v_mfma_f32_16x16x32_bf16 a[4:7], a[132:135], v[136:139], a[4:7]\n" \
"global_load_dwordx4 a[104:107], v190, s[60:61]\n" \
"v_mfma_f32_16x16x32_bf16 a[32:35], a[128:131], v[140:143], a[32:35]\n" \
"ds_read_b128 a[156:159], v5 offset:23552\n" \
"v_mfma_f32_16x16x32_bf16 a[36:39], a[132:135], v[140:143], a[36:39]\n" \
"s_waitcnt vmcnt(18) lgkmcnt(4)\n" \
"v_exp_f32 v99, v99\n" \
"v_mfma_f32_16x16x32_bf16 v[200:203], a[136:139], v[128:131], v[200:203]\n" \
"s_barrier\n" \
"v_exp_f32 v100, v100\n" \
"v_mfma_f32_16x16x32_bf16 v[204:207], a[140:143], v[128:131], v[204:207]\n" \
"s_add_u32 s17, s17, s19\n" \
"v_exp_f32 v101, v101\n" \
"v_mfma_f32_16x16x32_bf16 v[232:235], a[136:139], v[132:135], v[232:235]\n" \
"ds_read_b128 a[128:131], v5 offset:32768\n" \
"v_cvt_pk_bf16_f32 v145, v98, v99\n" \
"v_mfma_f32_16x16x32_bf16 v[236:239], a[140:143], v[132:135], v[236:239]\n" \
"s_add_u32 m0, s49, 49152\n" \
"v_exp_f32 v102, v102\n" \
"v_mfma_f32_16x16x32_bf16 a[8:11], a[136:139], v[136:139], a[8:11]\n" \
"buffer_load_dwordx4 v6, s[40:43], s17 offen offset:0 sc1 lds\n" \
"v_mfma_f32_16x16x32_bf16 a[12:15], a[140:143], v[136:139], a[12:15]\n" \
"v_exp_f32 v103, v103\n" \
"v_mfma_f32_16x16x32_bf16 a[40:43], a[136:139], v[140:143], a[40:43]\n" \
"ds_read_b128 a[132:135], v5 offset:33792\n" \
"v_cvt_pk_bf16_f32 v146, v100, v101\n" \
"v_mfma_f32_16x16x32_bf16 a[44:47], a[140:143], v[140:143], a[44:47]\n" \
"s_waitcnt lgkmcnt(4)\n" \
"v_exp_f32 v104, v104\n" \
"v_mfma_f32_16x16x32_bf16 v[208:211], a[144:147], v[128:131], v[208:211]\n" \
"v_exp_f32 v105, v105\n" \
"v_mfma_f32_16x16x32_bf16 v[212:215], a[148:151], v[128:131], v[212:215]\n" \
"v_mfma_f32_16x16x32_bf16 v[240:243], a[144:147], v[132:135], v[240:243]\n" \
"ds_read_b128 a[136:139], v5 offset:34816\n" \
"v_cvt_pk_bf16_f32 v147, v102, v103\n" \
"v_mfma_f32_16x16x32_bf16 v[244:247], a[148:151], v[132:135], v[244:247]\n" \
"s_add_u32 m0, s49, 49152\n" \
"v_exp_f32 v106, v106\n" \
"v_mfma_f32_16x16x32_bf16 a[16:19], a[144:147], v[136:139], a[16:19]\n" \
"buffer_load_dwordx4 v6, s[40:43], s17 offen offset:1024 sc1 lds\n" \
"v_mfma_f32_16x16x32_bf16 a[20:23], a[148:151], v[136:139], a[20:23]\n" \
"v_exp_f32 v107, v107\n" \
"v_mfma_f32_16x16x32_bf16 a[48:51], a[144:147], v[140:143], a[48:51]\n" \
"ds_read_b128 a[140:143], v5 offset:35840\n" \
"v_cvt_pk_bf16_f32 v148, v104, v105\n" \
"v_mfma_f32_16x16x32_bf16 a[52:55], a[148:151], v[140:143], a[52:55]\n" \
"s_waitcnt lgkmcnt(4)\n" \
"s_add_u32 m0, s49, 49152\n" \
"ds_read_b64_tr_b16 a[160:161], v13 offset:0\n" \
"v_mfma_f32_16x16x32_bf16 v[216:219], a[152:155], v[128:131], v[216:219]\n" \
"buffer_load_dwordx4 v6, s[40:43], s17 offen offset:2048 sc1 lds\n" \
"v_mfma_f32_16x16x32_bf16 v[220:223], a[156:159], v[128:131], v[220:223]\n" \
"v_exp_f32 v108, v108\n" \
"v_mfma_f32_16x16x32_bf16 v[248:251], a[152:155], v[132:135], v[248:251]\n" \
"ds_read_b128 a[144:147], v5 offset:36864\n" \
"v_cvt_pk_bf16_f32 v149, v106, v107\n" \
"v_mfma_f32_16x16x32_bf16 v[252:255], a[156:159], v[132:135], v[252:255]\n" \
"s_add_u32 m0, s49, 49152\n" \
"v_exp_f32 v109, v109\n" \
"v_mfma_f32_16x16x32_bf16 a[24:27], a[152:155], v[136:139], a[24:27]\n" \
"buffer_load_dwordx4 v6, s[40:43], s17 offen offset:3072 sc1 lds\n" \
"v_mfma_f32_16x16x32_bf16 a[28:31], a[156:159], v[136:139], a[28:31]\n" \
"v_exp_f32 v110, v110\n" \
"v_mfma_f32_16x16x32_bf16 a[56:59], a[152:155], v[140:143], a[56:59]\n" \
"ds_read_b128 a[148:151], v5 offset:37888\n" \
"v_exp_f32 v111, v111\n" \
"v_mfma_f32_16x16x32_bf16 a[60:63], a[156:159], v[140:143], a[60:63]\n" \
"s_waitcnt lgkmcnt(4)\n" \
"ds_read_b64_tr_b16 a[162:163], v14 offset:0\n" \
"v_cvt_pk_bf16_f32 v150, v108, v109\n" \
"v_cvt_pk_bf16_f32 v151, v110, v111\n" \
"v_mfma_f32_16x16x32_bf16 v[64:67], a[128:131], v[32:35], 0\n" \
"global_load_dwordx4 a[108:111], v191, s[60:61]\n" \
"v_mfma_f32_16x16x32_bf16 v[64:67], a[132:135], v[36:39], v[64:67]\n" \
"s_add_u32 s16, s16, s9\n" \
"ds_read_b64_tr_b16 a[164:165], v15 offset:0\n" \
"s_lshl_b32 s92, %2, 9\n" \
"v_mfma_f32_16x16x32_bf16 v[72:75], a[128:131], v[48:51], 0\n" \
"s_add_u32 s92, s92, 94375936\n" \
"ds_read_b128 a[152:155], v5 offset:38912\n" \
"v_lshrrev_b32 v17, 1, v4\n" \
"v_mfma_f32_16x16x32_bf16 v[72:75], a[132:135], v[52:55], v[72:75]\n" \
"s_add_u32 m0, s48, 0\n" \
"ds_read_b64_tr_b16 a[166:167], v16 offset:0\n" \
"v_add_u32 v17, s92, v17\n" \
"v_mfma_f32_16x16x32_bf16 v[80:83], a[128:131], a[64:67], 0\n" \
"buffer_load_dwordx4 v6, s[40:43], s16 offen offset:0 sc1 lds\n" \
"v_mfma_f32_16x16x32_bf16 v[80:83], a[132:135], a[68:71], v[80:83]\n" \
"s_mov_b64 s[4:5], 0\n" \
"ds_read_b64_tr_b16 a[168:169], v13 offset:8192\n" \
"v_mfma_f32_16x16x32_bf16 v[88:91], a[128:131], a[80:83], 0\n" \
"s_add_u32 s100, s100, 262144\ns_addc_u32 s101, s101, 0\n" \
"ds_read_b128 a[156:159], v5 offset:39936\n" \
"v_mfma_f32_16x16x32_bf16 v[88:91], a[132:135], a[84:87], v[88:91]\n" \
"s_waitcnt lgkmcnt(4)\n" \
"v_exp_f32 v112, v112\n" \
"v_mfma_f32_16x16x32_bf16 v[64:67], a[136:139], v[40:43], v[64:67]\n" \
"s_mov_b64 exec, 0xffff\nglobal_load_dword v1, v2, s[100:101]\ns_mov_b64 exec, -1\n" \
"v_mfma_f32_16x16x32_bf16 v[64:67], a[140:143], v[44:47], v[64:67]\n" \
"s_xor_b32 s49, s49, 0x10000\n" \
"v_exp_f32 v113, v113\n" \
"v_mfma_f32_16x16x32_bf16 v[72:75], a[136:139], v[56:59], v[72:75]\n" \
"ds_read_b128 a[128:131], v5 offset:49152\n" \
"v_cvt_pk_bf16_f32 v152, v112, v113\n" \
"v_mfma_f32_16x16x32_bf16 v[72:75], a[140:143], v[60:63], v[72:75]\n" \
"s_add_u32 m0, s48, 0\n" \
"v_exp_f32 v114, v114\n" \
"v_mfma_f32_16x16x32_bf16 v[80:83], a[136:139], a[72:75], v[80:83]\n" \
"buffer_load_dwordx4 v6, s[40:43], s16 offen offset:1024 sc1 lds\n" \
"v_mfma_f32_16x16x32_bf16 v[80:83], a[140:143], a[76:79], v[80:83]\n" \
"v_exp_f32 v115, v115\n" \
"v_mfma_f32_16x16x32_bf16 v[88:91], a[136:139], a[88:91], v[88:91]\n" \
"ds_read_b128 a[132:135], v5 offset:50176\n" \
"v_cvt_pk_bf16_f32 v153, v114, v115\n" \
"v_mfma_f32_16x16x32_bf16 v[88:91], a[140:143], a[92:95], v[88:91]\n" \
"s_waitcnt lgkmcnt(4)\n" \
"v_exp_f32 v116, v116\n" \
"v_mfma_f32_16x16x32_bf16 v[68:71], a[144:147], v[32:35], 0\n" \
"v_exp_f32 v117, v117\n" \
"v_mfma_f32_16x16x32_bf16 v[68:71], a[148:151], v[36:39], v[68:71]\n" \
"v_exp_f32 v118, v118\n" \
"v_mfma_f32_16x16x32_bf16 v[76:79], a[144:147], v[48:51], 0\n" \
"ds_read_b128 a[136:139], v5 offset:51200\n" \
"v_cvt_pk_bf16_f32 v154, v116, v117\n" \
"v_mfma_f32_16x16x32_bf16 v[76:79], a[148:151], v[52:55], v[76:79]\n" \
"s_add_u32 m0, s48, 0\n" \
"v_exp_f32 v119, v119\n" \
"v_mfma_f32_16x16x32_bf16 v[84:87], a[144:147], a[64:67], 0\n" \
"buffer_load_dwordx4 v6, s[40:43], s16 offen offset:2048 sc1 lds\n" \
"v_mfma_f32_16x16x32_bf16 v[84:87], a[148:151], a[68:71], v[84:87]\n" \
"v_exp_f32 v120, v120\n" \
"v_mfma_f32_16x16x32_bf16 v[92:95], a[144:147], a[80:83], 0\n" \
"ds_read_b128 a[140:143], v5 offset:52224\n" \
"v_cvt_pk_bf16_f32 v155, v118, v119\n" \
"v_mfma_f32_16x16x32_bf16 v[92:95], a[148:151], a[84:87], v[92:95]\n" \
"s_waitcnt lgkmcnt(4)\n" \
"v_exp_f32 v121, v121\n" \
"v_mfma_f32_16x16x32_bf16 v[68:71], a[152:155], v[40:43], v[68:71]\n" \
"v_exp_f32 v122, v122\n" \
"v_mfma_f32_16x16x32_bf16 v[68:71], a[156:159], v[44:47], v[68:71]\n" \
"v_exp_f32 v123, v123\n" \
"v_mfma_f32_16x16x32_bf16 v[76:79], a[152:155], v[56:59], v[76:79]\n" \
"ds_read_b128 a[144:147], v5 offset:53248\n" \
"v_cvt_pk_bf16_f32 v156, v120, v121\n" \
"v_mfma_f32_16x16x32_bf16 v[76:79], a[156:159], v[60:63], v[76:79]\n" \
"s_add_u32 m0, s48, 0\n" \
"v_exp_f32 v124, v124\n" \
"v_mfma_f32_16x16x32_bf16 v[84:87], a[152:155], a[72:75], v[84:87]\n" \
"buffer_load_dwordx4 v6, s[40:43], s16 offen offset:3072 sc1 lds\n" \
"v_mfma_f32_16x16x32_bf16 v[84:87], a[156:159], a[76:79], v[84:87]\n" \
"v_exp_f32 v125, v125\n" \
"v_mfma_f32_16x16x32_bf16 v[92:95], a[152:155], a[88:91], v[92:95]\n" \
"ds_read_b128 a[148:151], v5 offset:54272\n" \
"v_cvt_pk_bf16_f32 v157, v122, v123\n" \
"v_mfma_f32_16x16x32_bf16 v[92:95], a[156:159], a[92:95], v[92:95]\n" \
"s_waitcnt lgkmcnt(4)\n" \
"v_exp_f32 v126, v126\n" \
"v_mfma_f32_16x16x32_bf16 v[160:163], v[180:183], v[144:147], v[160:163]\n" \
"v_exp_f32 v127, v127\n" \
"v_mfma_f32_16x16x32_bf16 v[164:167], v[180:183], v[148:151], v[164:167]\n" \
"ds_read_b64_tr_b16 a[170:171], v14 offset:8192\n" \
"v_cvt_pk_bf16_f32 v158, v124, v125\n" \
"v_cvt_pk_bf16_f32 v159, v126, v127\n" \
"v_mfma_f32_16x16x32_bf16 v[168:171], v[180:183], v[152:155], v[168:171]\n" \
"ds_read_b64_tr_b16 a[172:173], v15 offset:8192\n" \
"v_mfma_f32_16x16x32_bf16 v[172:175], v[180:183], v[156:159], v[172:175]\n" \
"ds_read_b64_tr_b16 a[174:175], v16 offset:8192\n" \
"v_mfma_f32_16x16x32_bf16 v[192:195], a[128:131], v[144:147], v[192:195]\n" \
"v_exp_f32 v64, v64\n" \
"v_mfma_f32_16x16x32_bf16 v[196:199], a[132:135], v[144:147], v[196:199]\n" \
"v_exp_f32 v65, v65\n" \
"v_mfma_f32_16x16x32_bf16 v[224:227], a[128:131], v[148:151], v[224:227]\n" \
"ds_read_b128 a[152:155], v5 offset:55296\n" \
"v_cvt_pk_bf16_f32 v128, v64, v65\n" \
"v_mfma_f32_16x16x32_bf16 v[228:231], a[132:135], v[148:151], v[228:231]\n" \
"v_exp_f32 v66, v66\n" \
"v_mfma_f32_16x16x32_bf16 a[0:3], a[128:131], v[152:155], a[0:3]\n" \
"v_exp_f32 v67, v67\n" \
"v_mfma_f32_16x16x32_bf16 a[4:7], a[132:135], v[152:155], a[4:7]\n" \
"s_waitcnt lgkmcnt(1)\n" \
"v_cvt_pk_bf16_f32 v129, v66, v67\n" \
"v_mfma_f32_16x16x32_bf16 a[32:35], a[128:131], v[156:159], a[32:35]\n" \
"ds_read_b128 a[156:159], v5 offset:56320\n" \
"v_mfma_f32_16x16x32_bf16 a[36:39], a[132:135], v[156:159], a[36:39]\n" \
"s_waitcnt vmcnt(14) lgkmcnt(4)\n" \
"v_exp_f32 v68, v68\n" \
"v_mfma_f32_16x16x32_bf16 v[200:203], a[136:139], v[144:147], v[200:203]\n" \
"s_barrier\n" \
"v_xor_b32 v5, 0x10000, v5\n" \
"v_mfma_f32_16x16x32_bf16 v[204:207], a[140:143], v[144:147], v[204:207]\n" \
"s_add_u32 s17, s17, s19\n" \
"v_exp_f32 v69, v69\n" \
"v_mfma_f32_16x16x32_bf16 v[232:235], a[136:139], v[148:151], v[232:235]\n" \
"ds_read_b128 a[128:131], v5 offset:0\n" \
"v_cvt_pk_bf16_f32 v130, v68, v69\n" \
"v_mfma_f32_16x16x32_bf16 v[236:239], a[140:143], v[148:151], v[236:239]\n" \
"s_add_u32 m0, s48, 16384\n" \
"v_exp_f32 v70, v70\n" \
"v_mfma_f32_16x16x32_bf16 a[8:11], a[136:139], v[152:155], a[8:11]\n" \
"buffer_load_dwordx4 v6, s[40:43], s17 offen offset:0 sc1 lds\n" \
"v_mfma_f32_16x16x32_bf16 a[12:15], a[140:143], v[152:155], a[12:15]\n" \
"v_exp_f32 v71, v71\n" \
"v_mfma_f32_16x16x32_bf16 a[40:43], a[136:139], v[156:159], a[40:43]\n" \
"ds_read_b128 a[132:135], v5 offset:1024\n" \
"v_cvt_pk_bf16_f32 v131, v70, v71\n" \
"v_mfma_f32_16x16x32_bf16 a[44:47], a[140:143], v[156:159], a[44:47]\n" \
"s_waitcnt lgkmcnt(4)\n" \
"v_exp_f32 v72, v72\n" \
"v_mfma_f32_16x16x32_bf16 v[208:211], a[144:147], v[144:147], v[208:211]\n" \
"v_exp_f32 v73, v73\n" \
"v_mfma_f32_16x16x32_bf16 v[212:215], a[148:151], v[144:147], v[212:215]\n" \
"v_mfma_f32_16x16x32_bf16 v[240:243], a[144:147], v[148:151], v[240:243]\n" \
"ds_read_b128 a[136:139], v5 offset:2048\n" \
"v_cvt_pk_bf16_f32 v132, v72, v73\n" \
"v_mfma_f32_16x16x32_bf16 v[244:247], a[148:151], v[148:151], v[244:247]\n" \
"s_add_u32 m0, s48, 16384\n" \
"v_exp_f32 v74, v74\n" \
"v_mfma_f32_16x16x32_bf16 a[16:19], a[144:147], v[152:155], a[16:19]\n" \
"buffer_load_dwordx4 v6, s[40:43], s17 offen offset:1024 sc1 lds\n" \
"v_mfma_f32_16x16x32_bf16 a[20:23], a[148:151], v[152:155], a[20:23]\n" \
"v_exp_f32 v75, v75\n" \
"v_mfma_f32_16x16x32_bf16 a[48:51], a[144:147], v[156:159], a[48:51]\n" \
"ds_read_b128 a[140:143], v5 offset:3072\n" \
"v_cvt_pk_bf16_f32 v133, v74, v75\n" \
"v_mfma_f32_16x16x32_bf16 a[52:55], a[148:151], v[156:159], a[52:55]\n" \
"s_waitcnt lgkmcnt(4)\n" \
"s_add_u32 m0, s48, 16384\n" \
"ds_read_b64_tr_b16 a[176:177], v13 offset:16384\n" \
"v_mfma_f32_16x16x32_bf16 v[216:219], a[152:155], v[144:147], v[216:219]\n" \
"buffer_load_dwordx4 v6, s[40:43], s17 offen offset:2048 sc1 lds\n" \
"v_mfma_f32_16x16x32_bf16 v[220:223], a[156:159], v[144:147], v[220:223]\n" \
"s_add_u32 s44, s44, 1\n" \
"v_exp_f32 v76, v76\n" \
"v_mfma_f32_16x16x32_bf16 v[248:251], a[152:155], v[148:151], v[248:251]\n" \
"ds_read_b128 a[144:147], v5 offset:4096\n" \
"v_mfma_f32_16x16x32_bf16 v[252:255], a[156:159], v[148:151], v[252:255]\n" \
"s_add_u32 m0, s48, 16384\n" \
"v_exp_f32 v77, v77\n" \
"v_mfma_f32_16x16x32_bf16 a[24:27], a[152:155], v[152:155], a[24:27]\n" \
"buffer_load_dwordx4 v6, s[40:43], s17 offen offset:3072 sc1 lds\n" \
"v_mfma_f32_16x16x32_bf16 a[28:31], a[156:159], v[152:155], a[28:31]\n" \
"s_xor_b32 s48, s48, 0x10000\n" \
"v_exp_f32 v78, v78\n" \
"v_mfma_f32_16x16x32_bf16 a[56:59], a[152:155], v[156:159], a[56:59]\n" \
"ds_read_b128 a[148:151], v5 offset:5120\n" \
"v_exp_f32 v79, v79\n" \
"v_mfma_f32_16x16x32_bf16 a[60:63], a[156:159], v[156:159], a[60:63]\n" \
"s_waitcnt lgkmcnt(4)\n" \
"ds_read_b64_tr_b16 a[178:179], v14 offset:16384\n" \
"v_cvt_pk_bf16_f32 v134, v76, v77\n" \
"v_cvt_pk_bf16_f32 v135, v78, v79\n" \
"v_mfma_f32_16x16x32_bf16 v[96:99], a[128:131], v[32:35], 0\n" \
"ds_read_b64_tr_b16 a[180:181], v15 offset:16384\n" \
"v_mfma_f32_16x16x32_bf16 v[96:99], a[132:135], v[36:39], v[96:99]\n" \
"s_add_u32 s16, s16, s9\n" \
"ds_read_b64_tr_b16 a[182:183], v16 offset:16384\n" \
"v_mfma_f32_16x16x32_bf16 v[104:107], a[128:131], v[48:51], 0\n" \
"ds_read_b128 a[152:155], v5 offset:6144\n" \
"v_mfma_f32_16x16x32_bf16 v[104:107], a[132:135], v[52:55], v[104:107]\n" \
"s_add_u32 m0, s49, 32768\n" \
"v_exp_f32 v80, v80\n" \
"v_mfma_f32_16x16x32_bf16 v[112:115], a[128:131], a[64:67], 0\n" \
"buffer_load_dwordx4 v6, s[40:43], s16 offen offset:0 sc1 lds\n" \
"v_mfma_f32_16x16x32_bf16 v[112:115], a[132:135], a[68:71], v[112:115]\n" \
"ds_read_b64_tr_b16 a[184:185], v13 offset:24576\n" \
"v_mfma_f32_16x16x32_bf16 v[120:123], a[128:131], a[80:83], 0\n" \
"ds_read_b128 a[156:159], v5 offset:7168\n" \
"v_mfma_f32_16x16x32_bf16 v[120:123], a[132:135], a[84:87], v[120:123]\n" \
"s_waitcnt lgkmcnt(4)\n" \
"v_exp_f32 v81, v81\n" \
"s_add_u32 m0, s79, 131072\n" \
"v_mfma_f32_16x16x32_bf16 v[96:99], a[136:139], v[40:43], v[96:99]\n" \
"buffer_load_dwordx4 v12, s[28:31], s81 offen offset:0 lds\n" \
"v_mfma_f32_16x16x32_bf16 v[96:99], a[140:143], v[44:47], v[96:99]\n" \
"v_exp_f32 v82, v82\n" \
"v_mfma_f32_16x16x32_bf16 v[104:107], a[136:139], v[56:59], v[104:107]\n" \
"ds_read_b128 a[128:131], v5 offset:16384\n" \
"v_cvt_pk_bf16_f32 v136, v80, v81\n" \
"v_mfma_f32_16x16x32_bf16 v[104:107], a[140:143], v[60:63], v[104:107]\n" \
"s_add_u32 m0, s49, 32768\n" \
"v_exp_f32 v83, v83\n" \
"v_mfma_f32_16x16x32_bf16 v[112:115], a[136:139], a[72:75], v[112:115]\n" \
"buffer_load_dwordx4 v6, s[40:43], s16 offen offset:1024 sc1 lds\n" \
"v_mfma_f32_16x16x32_bf16 v[112:115], a[140:143], a[76:79], v[112:115]\n" \
"ds_read_b64_tr_b16 a[186:187], v14 offset:24576\n" \
"v_cvt_pk_bf16_f32 v137, v82, v83\n" \
"v_mfma_f32_16x16x32_bf16 v[120:123], a[136:139], a[88:91], v[120:123]\n" \
"ds_read_b128 a[132:135], v5 offset:17408\n" \
"v_mfma_f32_16x16x32_bf16 v[120:123], a[140:143], a[92:95], v[120:123]\n" \
"s_waitcnt lgkmcnt(4)\n" \
"v_exp_f32 v84, v84\n" \
"v_mfma_f32_16x16x32_bf16 v[100:103], a[144:147], v[32:35], 0\n" \
"v_exp_f32 v85, v85\n" \
"v_mfma_f32_16x16x32_bf16 v[100:103], a[148:151], v[36:39], v[100:103]\n" \
"v_exp_f32 v86, v86\n" \
"v_mfma_f32_16x16x32_bf16 v[108:111], a[144:147], v[48:51], 0\n" \
"ds_read_b128 a[136:139], v5 offset:18432\n" \
"v_cvt_pk_bf16_f32 v138, v84, v85\n" \
"v_mfma_f32_16x16x32_bf16 v[108:111], a[148:151], v[52:55], v[108:111]\n" \
"s_add_u32 m0, s49, 32768\n" \
"v_exp_f32 v87, v87\n" \
"v_mfma_f32_16x16x32_bf16 v[116:119], a[144:147], a[64:67], 0\n" \
"buffer_load_dwordx4 v6, s[40:43], s16 offen offset:2048 sc1 lds\n" \
"v_mfma_f32_16x16x32_bf16 v[116:119], a[148:151], a[68:71], v[116:119]\n" \
"v_exp_f32 v88, v88\n" \
"v_mfma_f32_16x16x32_bf16 v[124:127], a[144:147], a[80:83], 0\n" \
"ds_read_b128 a[140:143], v5 offset:19456\n" \
"v_cvt_pk_bf16_f32 v139, v86, v87\n" \
"v_mfma_f32_16x16x32_bf16 v[124:127], a[148:151], a[84:87], v[124:127]\n" \
"s_waitcnt lgkmcnt(4)\n" \
"v_exp_f32 v89, v89\n" \
"v_mfma_f32_16x16x32_bf16 v[100:103], a[152:155], v[40:43], v[100:103]\n" \
"v_exp_f32 v90, v90\n" \
"v_mfma_f32_16x16x32_bf16 v[100:103], a[156:159], v[44:47], v[100:103]\n" \
"v_exp_f32 v91, v91\n" \
"v_mfma_f32_16x16x32_bf16 v[108:111], a[152:155], v[56:59], v[108:111]\n" \
"ds_read_b128 a[144:147], v5 offset:20480\n" \
"v_cvt_pk_bf16_f32 v140, v88, v89\n" \
"v_mfma_f32_16x16x32_bf16 v[108:111], a[156:159], v[60:63], v[108:111]\n" \
"s_add_u32 m0, s49, 32768\n" \
"v_exp_f32 v92, v92\n" \
"v_mfma_f32_16x16x32_bf16 v[116:119], a[152:155], a[72:75], v[116:119]\n" \
"buffer_load_dwordx4 v6, s[40:43], s16 offen offset:3072 sc1 lds\n" \
"v_mfma_f32_16x16x32_bf16 v[116:119], a[156:159], a[76:79], v[116:119]\n" \
"v_exp_f32 v93, v93\n" \
"v_mfma_f32_16x16x32_bf16 v[124:127], a[152:155], a[88:91], v[124:127]\n" \
"ds_read_b128 a[148:151], v5 offset:21504\n" \
"v_cvt_pk_bf16_f32 v141, v90, v91\n" \
"v_mfma_f32_16x16x32_bf16 v[124:127], a[156:159], a[92:95], v[124:127]\n" \
"s_waitcnt lgkmcnt(4)\n" \
"v_exp_f32 v94, v94\n" \
"v_mfma_f32_16x16x32_bf16 v[160:163], v[180:183], v[128:131], v[160:163]\n" \
"s_add_u32 m0, s79, 131968\n" \
"v_exp_f32 v95, v95\n" \
"v_mfma_f32_16x16x32_bf16 v[164:167], v[180:183], v[132:135], v[164:167]\n" \
"buffer_load_dwordx4 v12, s[28:31], s81 offen offset:128 lds\n" \
"v_cvt_pk_bf16_f32 v142, v92, v93\n" \
"v_cvt_pk_bf16_f32 v143, v94, v95\n" \
"v_mfma_f32_16x16x32_bf16 v[168:171], v[180:183], v[136:139], v[168:171]\n" \
"s_mov_b32 s81, 327680\n" \
"ds_read_b64_tr_b16 a[188:189], v15 offset:24576\n" \
"v_mfma_f32_16x16x32_bf16 v[172:175], v[180:183], v[140:143], v[172:175]\n" \
"ds_read_b64_tr_b16 a[190:191], v16 offset:24576\n" \
"v_mfma_f32_16x16x32_bf16 v[192:195], a[128:131], v[128:131], v[192:195]\n" \
"s_add_u32 m0, s79, 139264\n" \
"v_exp_f32 v96, v96\n" \
"v_mfma_f32_16x16x32_bf16 v[196:199], a[132:135], v[128:131], v[196:199]\n" \
"buffer_load_dwordx4 v12, s[28:31], s81 offen offset:0 lds\n" \
"v_mfma_f32_16x16x32_bf16 v[224:227], a[128:131], v[132:135], v[224:227]\n" \
"ds_read_b128 a[152:155], v5 offset:22528\n" \
"v_mfma_f32_16x16x32_bf16 v[228:231], a[132:135], v[132:135], v[228:231]\n" \
"v_exp_f32 v97, v97\n" \
"v_mfma_f32_16x16x32_bf16 a[0:3], a[128:131], v[136:139], a[0:3]\n" \
"v_exp_f32 v98, v98\n" \
"v_mfma_f32_16x16x32_bf16 a[4:7], a[132:135], v[136:139], a[4:7]\n" \
"s_waitcnt lgkmcnt(1)\n" \
"v_exp_f32 v99, v99\n" \
"v_mfma_f32_16x16x32_bf16 a[32:35], a[128:131], v[140:143], a[32:35]\n" \
"ds_read_b128 a[156:159], v5 offset:23552\n" \
"v_cvt_pk_bf16_f32 v144, v96, v97\n" \
"v_mfma_f32_16x16x32_bf16 a[36:39], a[132:135], v[140:143], a[36:39]\n" \
"s_waitcnt vmcnt(17) lgkmcnt(4)\n" \
"v_exp_f32 v100, v100\n" \
"v_mfma_f32_16x16x32_bf16 v[200:203], a[136:139], v[128:131], v[200:203]\n" \
"s_barrier\n" \
"v_exp_f32 v101, v101\n" \
"v_mfma_f32_16x16x32_bf16 v[204:207], a[140:143], v[128:131], v[204:207]\n" \
"s_add_u32 s17, s17, s19\n" \
"v_cvt_pk_bf16_f32 v145, v98, v99\n" \
"v_cvt_pk_bf16_f32 v146, v100, v101\n" \
"v_mfma_f32_16x16x32_bf16 v[232:235], a[136:139], v[132:135], v[232:235]\n" \
"ds_read_b128 a[128:131], v5 offset:32768\n" \
"v_mfma_f32_16x16x32_bf16 v[236:239], a[140:143], v[132:135], v[236:239]\n" \
"s_add_u32 m0, s49, 49152\n" \
"v_exp_f32 v102, v102\n" \
"v_mfma_f32_16x16x32_bf16 a[8:11], a[136:139], v[136:139], a[8:11]\n" \
"buffer_load_dwordx4 v6, s[40:43], s17 offen offset:0 sc1 lds\n" \
"v_mfma_f32_16x16x32_bf16 a[12:15], a[140:143], v[136:139], a[12:15]\n" \
"v_exp_f32 v103, v103\n" \
"v_mfma_f32_16x16x32_bf16 a[40:43], a[136:139], v[140:143], a[40:43]\n" \
"ds_read_b128 a[132:135], v5 offset:33792\n" \
"v_cvt_pk_bf16_f32 v147, v102, v103\n" \
"v_mfma_f32_16x16x32_bf16 a[44:47], a[140:143], v[140:143], a[44:47]\n" \
"s_waitcnt lgkmcnt(4)\n" \
"v_exp_f32 v104, v104\n" \
"v_mfma_f32_16x16x32_bf16 v[208:211], a[144:147], v[128:131], v[208:211]\n" \
"v_exp_f32 v105, v105\n" \
"v_mfma_f32_16x16x32_bf16 v[212:215], a[148:151], v[128:131], v[212:215]\n" \
"v_mfma_f32_16x16x32_bf16 v[240:243], a[144:147], v[132:135], v[240:243]\n" \
"ds_read_b128 a[136:139], v5 offset:34816\n" \
"v_cvt_pk_bf16_f32 v148, v104, v105\n" \
"v_mfma_f32_16x16x32_bf16 v[244:247], a[148:151], v[132:135], v[244:247]\n" \
"s_add_u32 m0, s49, 49152\n" \
"v_exp_f32 v106, v106\n" \
"v_mfma_f32_16x16x32_bf16 a[16:19], a[144:147], v[136:139], a[16:19]\n" \
"buffer_load_dwordx4 v6, s[40:43], s17 offen offset:1024 sc1 lds\n" \
"v_mfma_f32_16x16x32_bf16 a[20:23], a[148:151], v[136:139], a[20:23]\n" \
"v_exp_f32 v107, v107\n" \
"v_mfma_f32_16x16x32_bf16 a[48:51], a[144:147], v[140:143], a[48:51]\n" \
"ds_read_b128 a[140:143], v5 offset:35840\n" \
"v_cvt_pk_bf16_f32 v149, v106, v107\n" \
"v_mfma_f32_16x16x32_bf16 a[52:55], a[148:151], v[140:143], a[52:55]\n" \
"s_waitcnt lgkmcnt(4)\n" \
"s_add_u32 m0, s49, 49152\n" \
"v_mfma_f32_16x16x32_bf16 v[216:219], a[152:155], v[128:131], v[216:219]\n" \
"buffer_load_dwordx4 v6, s[40:43], s17 offen offset:2048 sc1 lds\n" \
"v_mfma_f32_16x16x32_bf16 v[220:223], a[156:159], v[128:131], v[220:223]\n" \
"v_exp_f32 v108, v108\n" \
"v_mfma_f32_16x16x32_bf16 v[248:251], a[152:155], v[132:135], v[248:251]\n" \
"ds_read_b128 a[144:147], v5 offset:36864\n" \
"v_mfma_f32_16x16x32_bf16 v[252:255], a[156:159], v[132:135], v[252:255]\n" \
"s_add_u32 m0, s49, 49152\n" \
"v_exp_f32 v109, v109\n" \
"v_mfma_f32_16x16x32_bf16 a[24:27], a[152:155], v[136:139], a[24:27]\n" \
"buffer_load_dwordx4 v6, s[40:43], s17 offen offset:3072 sc1 lds\n" \
"v_mfma_f32_16x16x32_bf16 a[28:31], a[156:159], v[136:139], a[28:31]\n" \
"v_exp_f32 v110, v110\n" \
"v_mfma_f32_16x16x32_bf16 a[56:59], a[152:155], v[140:143], a[56:59]\n" \
"ds_read_b128 a[148:151], v5 offset:37888\n" \
"v_exp_f32 v111, v111\n" \
"v_mfma_f32_16x16x32_bf16 a[60:63], a[156:159], v[140:143], a[60:63]\n" \
"s_waitcnt lgkmcnt(4)\n" \
"v_cvt_pk_bf16_f32 v150, v108, v109\n" \
"v_cvt_pk_bf16_f32 v151, v110, v111\n" \
"v_mfma_f32_16x16x32_bf16 v[64:67], a[128:131], v[32:35], 0\n" \
"buffer_load_dwordx2 v[18:19], v17, s[36:39], 0 offen sc1\n" \
"v_mfma_f32_16x16x32_bf16 v[64:67], a[132:135], v[36:39], v[64:67]\n" \
"s_add_u32 s16, s16, s9\n" \
"v_exp_f32 v112, v112\n" \
"v_mfma_f32_16x16x32_bf16 v[72:75], a[128:131], v[48:51], 0\n" \
"ds_read_b128 a[152:155], v5 offset:38912\n" \
"v_mfma_f32_16x16x32_bf16 v[72:75], a[132:135], v[52:55], v[72:75]\n" \
"s_add_u32 m0, s48, 0\n" \
"v_exp_f32 v113, v113\n" \
"v_mfma_f32_16x16x32_bf16 v[80:83], a[128:131], a[64:67], 0\n" \
"buffer_load_dwordx4 v6, s[40:43], s16 offen offset:0 sc1 lds\n" \
"v_mfma_f32_16x16x32_bf16 v[80:83], a[132:135], a[68:71], v[80:83]\n" \
"v_exp_f32 v114, v114\n" \
"v_mfma_f32_16x16x32_bf16 v[88:91], a[128:131], a[80:83], 0\n" \
"ds_read_b128 a[156:159], v5 offset:39936\n" \
"v_cvt_pk_bf16_f32 v152, v112, v113\n" \
"v_mfma_f32_16x16x32_bf16 v[88:91], a[132:135], a[84:87], v[88:91]\n" \
"s_waitcnt lgkmcnt(4)\n" \
"v_exp_f32 v115, v115\n" \
"s_add_u32 m0, s79, 140160\n" \
"v_mfma_f32_16x16x32_bf16 v[64:67], a[136:139], v[40:43], v[64:67]\n" \
"buffer_load_dwordx4 v12, s[28:31], s81 offen offset:128 lds\n" \
"v_mfma_f32_16x16x32_bf16 v[64:67], a[140:143], v[44:47], v[64:67]\n" \
"s_xor_b32 s49, s49, 0x10000\n" \
"s_mov_b32 s81, 393216\n" \
"v_cvt_pk_bf16_f32 v153, v114, v115\n" \
"v_mfma_f32_16x16x32_bf16 v[72:75], a[136:139], v[56:59], v[72:75]\n" \
"ds_read_b128 a[128:131], v5 offset:49152\n" \
"v_mfma_f32_16x16x32_bf16 v[72:75], a[140:143], v[60:63], v[72:75]\n" \
"s_add_u32 m0, s48, 0\n" \
"v_exp_f32 v116, v116\n" \
"v_mfma_f32_16x16x32_bf16 v[80:83], a[136:139], a[72:75], v[80:83]\n" \
"buffer_load_dwordx4 v6, s[40:43], s16 offen offset:1024 sc1 lds\n" \
"v_mfma_f32_16x16x32_bf16 v[80:83], a[140:143], a[76:79], v[80:83]\n" \
"v_exp_f32 v117, v117\n" \
"v_mfma_f32_16x16x32_bf16 v[88:91], a[136:139], a[88:91], v[88:91]\n" \
"ds_read_b128 a[132:135], v5 offset:50176\n" \
"v_cvt_pk_bf16_f32 v154, v116, v117\n" \
"v_mfma_f32_16x16x32_bf16 v[88:91], a[140:143], a[92:95], v[88:91]\n" \
"s_waitcnt lgkmcnt(4)\n" \
"v_exp_f32 v118, v118\n" \
"s_add_u32 m0, s79, 147456\n" \
"v_mfma_f32_16x16x32_bf16 v[68:71], a[144:147], v[32:35], 0\n" \
"buffer_load_dwordx4 v12, s[28:31], s81 offen offset:0 lds\n" \
"v_mfma_f32_16x16x32_bf16 v[68:71], a[148:151], v[36:39], v[68:71]\n" \
"v_exp_f32 v119, v119\n" \
"v_mfma_f32_16x16x32_bf16 v[76:79], a[144:147], v[48:51], 0\n" \
"ds_read_b128 a[136:139], v5 offset:51200\n" \
"v_cvt_pk_bf16_f32 v155, v118, v119\n" \
"v_mfma_f32_16x16x32_bf16 v[76:79], a[148:151], v[52:55], v[76:79]\n" \
"s_add_u32 m0, s48, 0\n" \
"v_exp_f32 v120, v120\n" \
"v_mfma_f32_16x16x32_bf16 v[84:87], a[144:147], a[64:67], 0\n" \
"buffer_load_dwordx4 v6, s[40:43], s16 offen offset:2048 sc1 lds\n" \
"v_mfma_f32_16x16x32_bf16 v[84:87], a[148:151], a[68:71], v[84:87]\n" \
"v_exp_f32 v121, v121\n" \
"v_mfma_f32_16x16x32_bf16 v[92:95], a[144:147], a[80:83], 0\n" \
"ds_read_b128 a[140:143], v5 offset:52224\n" \
"v_cvt_pk_bf16_f32 v156, v120, v121\n" \
"v_mfma_f32_16x16x32_bf16 v[92:95], a[148:151], a[84:87], v[92:95]\n" \
"s_waitcnt lgkmcnt(4)\n" \
"v_exp_f32 v122, v122\n" \
"s_add_u32 m0, s79, 148352\n" \
"v_mfma_f32_16x16x32_bf16 v[68:71], a[152:155], v[40:43], v[68:71]\n" \
"buffer_load_dwordx4 v12, s[28:31], s81 offen offset:128 lds\n" \
"v_mfma_f32_16x16x32_bf16 v[68:71], a[156:159], v[44:47], v[68:71]\n" \
"s_mov_b32 s81, 458752\n" \
"v_exp_f32 v123, v123\n" \
"v_mfma_f32_16x16x32_bf16 v[76:79], a[152:155], v[56:59], v[76:79]\n" \
"ds_read_b128 a[144:147], v5 offset:53248\n" \
"v_cvt_pk_bf16_f32 v157, v122, v123\n" \
"v_mfma_f32_16x16x32_bf16 v[76:79], a[156:159], v[60:63], v[76:79]\n" \
"s_add_u32 m0, s48, 0\n" \
"v_exp_f32 v124, v124\n" \
"v_mfma_f32_16x16x32_bf16 v[84:87], a[152:155], a[72:75], v[84:87]\n" \
"buffer_load_dwordx4 v6, s[40:43], s16 offen offset:3072 sc1 lds\n" \
"v_mfma_f32_16x16x32_bf16 v[84:87], a[156:159], a[76:79], v[84:87]\n" \
"v_exp_f32 v125, v125\n" \
"v_mfma_f32_16x16x32_bf16 v[92:95], a[152:155], a[88:91], v[92:95]\n" \
"ds_read_b128 a[148:151], v5 offset:54272\n" \
"v_cvt_pk_bf16_f32 v158, v124, v125\n" \
"v_mfma_f32_16x16x32_bf16 v[92:95], a[156:159], a[92:95], v[92:95]\n" \
"s_waitcnt lgkmcnt(4)\n" \
"v_exp_f32 v126, v126\n" \
"v_mfma_f32_16x16x32_bf16 v[160:163], v[180:183], v[144:147], v[160:163]\n" \
"s_add_u32 m0, s79, 155648\n" \
"v_exp_f32 v127, v127\n" \
"v_mfma_f32_16x16x32_bf16 v[164:167], v[180:183], v[148:151], v[164:167]\n" \
"buffer_load_dwordx4 v12, s[28:31], s81 offen offset:0 lds\n" \
"v_cvt_pk_bf16_f32 v159, v126, v127\n" \
"v_mfma_f32_16x16x32_bf16 v[168:171], v[180:183], v[152:155], v[168:171]\n" \
"v_exp_f32 v64, v64\n" \
"v_mfma_f32_16x16x32_bf16 v[172:175], v[180:183], v[156:159], v[172:175]\n" \
"buffer_store_dwordx4 v[28:31], v4, s[36:39], s80 offen offset:1024 sc1\n" \
"v_mfma_f32_16x16x32_bf16 v[192:195], a[128:131], v[144:147], v[192:195]\n" \
"s_add_u32 m0, s79, 156544\n" \
"v_exp_f32 v65, v65\n" \
"v_mfma_f32_16x16x32_bf16 v[196:199], a[132:135], v[144:147], v[196:199]\n" \
"buffer_load_dwordx4 v12, s[28:31], s81 offen offset:128 lds\n" \
"v_mfma_f32_16x16x32_bf16 v[224:227], a[128:131], v[148:151], v[224:227]\n" \
"s_mov_b32 s81, 524288\n" \
"ds_read_b128 a[152:155], v5 offset:55296\n" \
"v_cvt_pk_bf16_f32 v128, v64, v65\n" \
"v_mfma_f32_16x16x32_bf16 v[228:231], a[132:135], v[148:151], v[228:231]\n" \
"buffer_store_dwordx4 v[28:31], v4, s[36:39], s80 offen offset:2048 sc1\n" \
"v_mfma_f32_16x16x32_bf16 a[0:3], a[128:131], v[152:155], a[0:3]\n" \
"v_exp_f32 v66, v66\n" \
"v_mfma_f32_16x16x32_bf16 a[4:7], a[132:135], v[152:155], a[4:7]\n" \
"v_exp_f32 v67, v67\n" \
"v_mfma_f32_16x16x32_bf16 a[32:35], a[128:131], v[156:159], a[32:35]\n" \
"ds_read_b128 a[156:159], v5 offset:56320\n" \
"v_cvt_pk_bf16_f32 v129, v66, v67\n" \
"v_mfma_f32_16x16x32_bf16 a[36:39], a[132:135], v[156:159], a[36:39]\n" \
"s_waitcnt vmcnt(9) lgkmcnt(4)\n" \
"v_exp_f32 v68, v68\n" \
"v_mfma_f32_16x16x32_bf16 v[200:203], a[136:139], v[144:147], v[200:203]\n" \
"s_barrier\n" \
"v_xor_b32 v5, 0x10000, v5\n" \
"v_mfma_f32_16x16x32_bf16 v[204:207], a[140:143], v[144:147], v[204:207]\n" \
"s_add_u32 s17, s17, s19\n" \
"v_exp_f32 v69, v69\n" \
"v_mfma_f32_16x16x32_bf16 v[232:235], a[136:139], v[148:151], v[232:235]\n" \
"ds_read_b128 a[128:131], v5 offset:0\n" \
"v_cvt_pk_bf16_f32 v130, v68, v69\n" \
"v_mfma_f32_16x16x32_bf16 v[236:239], a[140:143], v[148:151], v[236:239]\n" \
"s_add_u32 m0, s48, 16384\n" \
"v_exp_f32 v70, v70\n" \
"v_mfma_f32_16x16x32_bf16 a[8:11], a[136:139], v[152:155], a[8:11]\n" \
"buffer_load_dwordx4 v6, s[40:43], s17 offen offset:0 sc1 lds\n" \
"v_mfma_f32_16x16x32_bf16 a[12:15], a[140:143], v[152:155], a[12:15]\n" \
"v_exp_f32 v71, v71\n" \
"v_mfma_f32_16x16x32_bf16 a[40:43], a[136:139], v[156:159], a[40:43]\n" \
"ds_read_b128 a[132:135], v5 offset:1024\n" \
"v_cvt_pk_bf16_f32 v131, v70, v71\n" \
"v_mfma_f32_16x16x32_bf16 a[44:47], a[140:143], v[156:159], a[44:47]\n" \
"LH18_%=:\ns_cbranch_execz LH22_%=\n" \
"s_waitcnt lgkmcnt(4)\n" \
"v_exp_f32 v72, v72\n" \
"v_mfma_f32_16x16x32_bf16 v[208:211], a[144:147], v[144:147], v[208:211]\n" \
"v_exp_f32 v73, v73\n" \
"v_mfma_f32_16x16x32_bf16 v[212:215], a[148:151], v[144:147], v[212:215]\n" \
"v_mfma_f32_16x16x32_bf16 v[240:243], a[144:147], v[148:151], v[240:243]\n" \
"ds_read_b128 a[136:139], v5 offset:2048\n" \
"v_cvt_pk_bf16_f32 v132, v72, v73\n" \
"v_mfma_f32_16x16x32_bf16 v[244:247], a[148:151], v[148:151], v[244:247]\n" \
"s_add_u32 m0, s48, 16384\n" \
"v_exp_f32 v74, v74\n" \
"v_mfma_f32_16x16x32_bf16 a[16:19], a[144:147], v[152:155], a[16:19]\n" \
"buffer_load_dwordx4 v6, s[40:43], s17 offen offset:1024 sc1 lds\n" \
"v_mfma_f32_16x16x32_bf16 a[20:23], a[148:151], v[152:155], a[20:23]\n" \
"v_exp_f32 v75, v75\n" \
"v_mfma_f32_16x16x32_bf16 a[48:51], a[144:147], v[156:159], a[48:51]\n" \
"ds_read_b128 a[140:143], v5 offset:3072\n" \
"v_cvt_pk_bf16_f32 v133, v74, v75\n" \
"v_mfma_f32_16x16x32_bf16 a[52:55], a[148:151], v[156:159], a[52:55]\n" \
"s_waitcnt lgkmcnt(4)\n" \
"s_add_u32 m0, s48, 16384\n" \
"ds_read_b64_tr_b16 a[192:193], v13 offset:0\n" \
"v_mfma_f32_16x16x32_bf16 v[216:219], a[152:155], v[144:147], v[216:219]\n" \
"buffer_load_dwordx4 v6, s[40:43], s17 offen offset:2048 sc1 lds\n" \
"v_mfma_f32_16x16x32_bf16 v[220:223], a[156:159], v[144:147], v[220:223]\n" \
"s_add_u32 s44, s44, 1\n" \
"v_exp_f32 v76, v76\n" \
"v_mfma_f32_16x16x32_bf16 v[248:251], a[152:155], v[148:151], v[248:251]\n" \
"ds_read_b128 a[144:147], v5 offset:4096\n" \
"v_mfma_f32_16x16x32_bf16 v[252:255], a[156:159], v[148:151], v[252:255]\n" \
"s_add_u32 m0, s48, 16384\n" \
"v_exp_f32 v77, v77\n" \
"v_mfma_f32_16x16x32_bf16 a[24:27], a[152:155], v[152:155], a[24:27]\n" \
"buffer_load_dwordx4 v6, s[40:43], s17 offen offset:3072 sc1 lds\n" \
"v_mfma_f32_16x16x32_bf16 a[28:31], a[156:159], v[152:155], a[28:31]\n" \
"s_xor_b32 s48, s48, 0x10000\n" \
"v_exp_f32 v78, v78\n" \
"v_mfma_f32_16x16x32_bf16 a[56:59], a[152:155], v[156:159], a[56:59]\n" \
"ds_read_b128 a[148:151], v5 offset:5120\n" \
"v_exp_f32 v79, v79\n" \
"v_mfma_f32_16x16x32_bf16 a[60:63], a[156:159], v[156:159], a[60:63]\n" \
"s_waitcnt lgkmcnt(4)\n" \
"ds_read_b64_tr_b16 a[194:195], v14 offset:0\n" \
"v_cvt_pk_bf16_f32 v134, v76, v77\n" \
"v_cvt_pk_bf16_f32 v135, v78, v79\n" \
"v_mfma_f32_16x16x32_bf16 v[96:99], a[128:131], v[32:35], 0\n" \
"s_add_u32 s100, s100, 262144\ns_addc_u32 s101, s101, 0\n" \
"ds_read_b64_tr_b16 a[196:197], v15 offset:0\n" \
"v_mfma_f32_16x16x32_bf16 v[96:99], a[132:135], v[36:39], v[96:99]\n" \
"s_add_u32 s16, s16, s9\n" \
"ds_read_b64_tr_b16 a[198:199], v16 offset:0\n" \
"v_mfma_f32_16x16x32_bf16 v[104:107], a[128:131], v[48:51], 0\n" \
"ds_read_b128 a[152:155], v5 offset:6144\n" \
"v_mfma_f32_16x16x32_bf16 v[104:107], a[132:135], v[52:55], v[104:107]\n" \
"s_add_u32 m0, s49, 32768\n" \
"v_exp_f32 v80, v80\n" \
"v_mfma_f32_16x16x32_bf16 v[112:115], a[128:131], a[64:67], 0\n" \
"buffer_load_dwordx4 v6, s[40:43], s16 offen offset:0 sc1 lds\n" \
"v_mfma_f32_16x16x32_bf16 v[112:115], a[132:135], a[68:71], v[112:115]\n" \
"ds_read_b64_tr_b16 a[200:201], v13 offset:8192\n" \
"v_mfma_f32_16x16x32_bf16 v[120:123], a[128:131], a[80:83], 0\n" \
"ds_read_b128 a[156:159], v5 offset:7168\n" \
"v_mfma_f32_16x16x32_bf16 v[120:123], a[132:135], a[84:87], v[120:123]\n" \
"s_waitcnt lgkmcnt(4)\n" \
"v_exp_f32 v81, v81\n" \
"v_mfma_f32_16x16x32_bf16 v[96:99], a[136:139], v[40:43], v[96:99]\n" \
"s_mov_b64 exec, 0xffff\nglobal_load_dword v1, v2, s[100:101]\ns_mov_b64 exec, -1\n" \
"v_mfma_f32_16x16x32_bf16 v[96:99], a[140:143], v[44:47], v[96:99]\n" \
"v_exp_f32 v82, v82\n" \
"v_mfma_f32_16x16x32_bf16 v[104:107], a[136:139], v[56:59], v[104:107]\n" \
"ds_read_b128 a[128:131], v5 offset:16384\n" \
"v_cvt_pk_bf16_f32 v136, v80, v81\n" \
"v_mfma_f32_16x16x32_bf16 v[104:107], a[140:143], v[60:63], v[104:107]\n" \
"s_add_u32 m0, s49, 32768\n" \
"v_exp_f32 v83, v83\n" \
"v_mfma_f32_16x16x32_bf16 v[112:115], a[136:139], a[72:75], v[112:115]\n" \
"buffer_load_dwordx4 v6, s[40:43], s16 offen offset:1024 sc1 lds\n" \
"v_mfma_f32_16x16x32_bf16 v[112:115], a[140:143], a[76:79], v[112:115]\n" \
"ds_read_b64_tr_b16 a[202:203], v14 offset:8192\n" \
"v_cvt_pk_bf16_f32 v137, v82, v83\n" \
"v_mfma_f32_16x16x32_bf16 v[120:123], a[136:139], a[88:91], v[120:123]\n" \
"ds_read_b128 a[132:135], v5 offset:17408\n" \
"v_mfma_f32_16x16x32_bf16 v[120:123], a[140:143], a[92:95], v[120:123]\n" \
"s_waitcnt lgkmcnt(4)\n" \
"v_exp_f32 v84, v84\n" \
"v_mfma_f32_16x16x32_bf16 v[100:103], a[144:147], v[32:35], 0\n" \
"v_exp_f32 v85, v85\n" \
"v_mfma_f32_16x16x32_bf16 v[100:103], a[148:151], v[36:39], v[100:103]\n" \
"v_exp_f32 v86, v86\n" \
"v_mfma_f32_16x16x32_bf16 v[108:111], a[144:147], v[48:51], 0\n" \
"ds_read_b128 a[136:139], v5 offset:18432\n" \
"v_cvt_pk_bf16_f32 v138, v84, v85\n" \
"v_mfma_f32_16x16x32_bf16 v[108:111], a[148:151], v[52:55], v[108:111]\n" \
"s_add_u32 m0, s49, 32768\n" \
"v_exp_f32 v87, v87\n" \
"v_mfma_f32_16x16x32_bf16 v[116:119], a[144:147], a[64:67], 0\n" \
"buffer_load_dwordx4 v6, s[40:43], s16 offen offset:2048 sc1 lds\n" \
"v_mfma_f32_16x16x32_bf16 v[116:119], a[148:151], a[68:71], v[116:119]\n" \
"v_exp_f32 v88, v88\n" \
"v_mfma_f32_16x16x32_bf16 v[124:127], a[144:147], a[80:83], 0\n" \
"ds_read_b128 a[140:143], v5 offset:19456\n" \
"v_cvt_pk_bf16_f32 v139, v86, v87\n" \
"v_mfma_f32_16x16x32_bf16 v[124:127], a[148:151], a[84:87], v[124:127]\n" \
"s_waitcnt lgkmcnt(4)\n" \
"v_exp_f32 v89, v89\n" \
"v_mfma_f32_16x16x32_bf16 v[100:103], a[152:155], v[40:43], v[100:103]\n" \
"v_exp_f32 v90, v90\n" \
"v_mfma_f32_16x16x32_bf16 v[100:103], a[156:159], v[44:47], v[100:103]\n" \
"v_exp_f32 v91, v91\n" \
"v_mfma_f32_16x16x32_bf16 v[108:111], a[152:155], v[56:59], v[108:111]\n" \
"ds_read_b128 a[144:147], v5 offset:20480\n" \
"v_cvt_pk_bf16_f32 v140, v88, v89\n" \
"v_mfma_f32_16x16x32_bf16 v[108:111], a[156:159], v[60:63], v[108:111]\n" \
"s_add_u32 m0, s49, 32768\n" \
"v_exp_f32 v92, v92\n" \
"v_mfma_f32_16x16x32_bf16 v[116:119], a[152:155], a[72:75], v[116:119]\n" \
"buffer_load_dwordx4 v6, s[40:43], s16 offen offset:3072 sc1 lds\n" \
"v_mfma_f32_16x16x32_bf16 v[116:119], a[156:159], a[76:79], v[116:119]\n" \
"v_exp_f32 v93, v93\n" \
"v_mfma_f32_16x16x32_bf16 v[124:127], a[152:155], a[88:91], v[124:127]\n" \
"ds_read_b128 a[148:151], v5 offset:21504\n" \
"v_cvt_pk_bf16_f32 v141, v90, v91\n" \
"v_mfma_f32_16x16x32_bf16 v[124:127], a[156:159], a[92:95], v[124:127]\n" \
"s_waitcnt lgkmcnt(4)\n" \
"v_exp_f32 v94, v94\n" \
"v_mfma_f32_16x16x32_bf16 v[160:163], v[180:183], v[128:131], v[160:163]\n" \
"s_waitcnt vmcnt(20)\n" \
"v_exp_f32 v95, v95\n" \
"v_mfma_f32_16x16x32_bf16 v[164:167], v[180:183], v[132:135], v[164:167]\n" \
"ds_read_b64_tr_b16 a[204:205], v15 offset:8192\n" \
"v_cmp_ne_u64 vcc, s[14:15], v[18:19]\n" \
"v_cvt_pk_bf16_f32 v142, v92, v93\n" \
"v_cvt_pk_bf16_f32 v143, v94, v95\n" \
"v_mfma_f32_16x16x32_bf16 v[168:171], v[180:183], v[136:139], v[168:171]\n" \
"ds_read_b64_tr_b16 a[206:207], v16 offset:8192\n" \
"s_nop 0\n" \
"v_mfma_f32_16x16x32_bf16 v[172:175], v[180:183], v[140:143], v[172:175]\n" \
"s_cbranch_vccnz LPZ_%=\nLPZR_%=:\n" \
"v_and_b32 v188, 0xf0, v4\n" \
"v_lshrrev_b32 v189, 8, v4\n" \
"v_mfma_f32_16x16x32_bf16 v[192:195], a[128:131], v[128:131], v[192:195]\n" \
"v_lshl_add_u32 v188, v189, 14, v188\n" \
"v_add_u32 v189, 0x10000, v188\n" \
"v_mfma_f32_16x16x32_bf16 v[196:199], a[132:135], v[128:131], v[196:199]\n" \
"v_exp_f32 v96, v96\n" \
"v_mfma_f32_16x16x32_bf16 v[224:227], a[128:131], v[132:135], v[224:227]\n" \
"ds_read_b128 a[152:155], v5 offset:22528\n" \
"v_mfma_f32_16x16x32_bf16 v[228:231], a[132:135], v[132:135], v[228:231]\n" \
"v_exp_f32 v97, v97\n" \
"v_mfma_f32_16x16x32_bf16 a[0:3], a[128:131], v[136:139], a[0:3]\n" \
"v_exp_f32 v98, v98\n" \
"v_mfma_f32_16x16x32_bf16 a[4:7], a[132:135], v[136:139], a[4:7]\n" \
"s_waitcnt lgkmcnt(1)\n" \
"v_exp_f32 v99, v99\n" \
"v_mfma_f32_16x16x32_bf16 a[32:35], a[128:131], v[140:143], a[32:35]\n" \
"ds_read_b128 a[156:159], v5 offset:23552\n" \
"v_cvt_pk_bf16_f32 v144, v96, v97\n" \
"v_mfma_f32_16x16x32_bf16 a[36:39], a[132:135], v[140:143], a[36:39]\n" \
"s_waitcnt vmcnt(10) lgkmcnt(4)\n" \
"v_exp_f32 v100, v100\n" \
"v_mfma_f32_16x16x32_bf16 v[200:203], a[136:139], v[128:131], v[200:203]\n" \
"s_barrier\n" \
"v_exp_f32 v101, v101\n" \
"v_mfma_f32_16x16x32_bf16 v[204:207], a[140:143], v[128:131], v[204:207]\n" \
"s_add_u32 s17, s17, s19\n" \
"ds_read_b64_tr_b16 a[208:209], v13 offset:16384\n" \
"v_cvt_pk_bf16_f32 v145, v98, v99\n" \
"v_mfma_f32_16x16x32_bf16 v[232:235], a[136:139], v[132:135], v[232:235]\n" \
"ds_read_b128 a[128:131], v5 offset:32768\n" \
"v_cvt_pk_bf16_f32 v146, v100, v101\n" \
"v_mfma_f32_16x16x32_bf16 v[236:239], a[140:143], v[132:135], v[236:239]\n" \
"s_add_u32 m0, s49, 49152\n" \
"v_exp_f32 v102, v102\n" \
"v_mfma_f32_16x16x32_bf16 a[8:11], a[136:139], v[136:139], a[8:11]\n" \
"buffer_load_dwordx4 v6, s[40:43], s17 offen offset:0 sc1 lds\n" \
"v_mfma_f32_16x16x32_bf16 a[12:15], a[140:143], v[136:139], a[12:15]\n" \
"v_exp_f32 v103, v103\n" \
"v_mfma_f32_16x16x32_bf16 a[40:43], a[136:139], v[140:143], a[40:43]\n" \
"ds_read_b128 a[132:135], v5 offset:33792\n" \
"v_cvt_pk_bf16_f32 v147, v102, v103\n" \
"v_mfma_f32_16x16x32_bf16 a[44:47], a[140:143], v[140:143], a[44:47]\n" \
"s_waitcnt lgkmcnt(4)\n" \
"v_exp_f32 v104, v104\n" \
"v_mfma_f32_16x16x32_bf16 v[208:211], a[144:147], v[128:131], v[208:211]\n" \
"v_exp_f32 v105, v105\n" \
"v_mfma_f32_16x16x32_bf16 v[212:215], a[148:151], v[128:131], v[212:215]\n" \
"v_mfma_f32_16x16x32_bf16 v[240:243], a[144:147], v[132:135], v[240:243]\n" \
"ds_read_b128 a[136:139], v5 offset:34816\n" \
"v_cvt_pk_bf16_f32 v148, v104, v105\n" \
"v_mfma_f32_16x16x32_bf16 v[244:247], a[148:151], v[132:135], v[244:247]\n" \
"s_add_u32 m0, s49, 49152\n" \
"v_exp_f32 v106, v106\n" \
"v_mfma_f32_16x16x32_bf16 a[16:19], a[144:147], v[136:139], a[16:19]\n" \
"buffer_load_dwordx4 v6, s[40:43], s17 offen offset:1024 sc1 lds\n" \
"v_mfma_f32_16x16x32_bf16 a[20:23], a[148:151], v[136:139], a[20:23]\n" \
"v_exp_f32 v107, v107\n" \
"v_mfma_f32_16x16x32_bf16 a[48:51], a[144:147], v[140:143], a[48:51]\n" \
"ds_read_b128 a[140:143], v5 offset:35840\n" \
"v_cvt_pk_bf16_f32 v149, v106, v107\n" \
"v_mfma_f32_16x16x32_bf16 a[52:55], a[148:151], v[140:143], a[52:55]\n" \
"s_waitcnt lgkmcnt(4)\n" \
"s_add_u32 m0, s49, 49152\n" \
"ds_read_b64_tr_b16 a[210:211], v14 offset:16384\n" \
"v_mfma_f32_16x16x32_bf16 v[216:219], a[152:155], v[128:131], v[216:219]\n" \
"buffer_load_dwordx4 v6, s[40:43], s17 offen offset:2048 sc1 lds\n" \
"v_mfma_f32_16x16x32_bf16 v[220:223], a[156:159], v[128:131], v[220:223]\n" \
"v_exp_f32 v108, v108\n" \
"v_mfma_f32_16x16x32_bf16 v[248:251], a[152:155], v[132:135], v[248:251]\n" \
"ds_read_b128 a[144:147], v5 offset:36864\n" \
"v_mfma_f32_16x16x32_bf16 v[252:255], a[156:159], v[132:135], v[252:255]\n" \
"s_add_u32 m0, s49, 49152\n" \
"v_exp_f32 v109, v109\n" \
"v_mfma_f32_16x16x32_bf16 a[24:27], a[152:155], v[136:139], a[24:27]\n" \
"buffer_load_dwordx4 v6, s[40:43], s17 offen offset:3072 sc1 lds\n" \
"v_mfma_f32_16x16x32_bf16 a[28:31], a[156:159], v[136:139], a[28:31]\n" \
"v_exp_f32 v110, v110\n" \
"v_mfma_f32_16x16x32_bf16 a[56:59], a[152:155], v[140:143], a[56:59]\n" \
"ds_read_b128 a[148:151], v5 offset:37888\n" \
"v_exp_f32 v111, v111\n" \
"v_mfma_f32_16x16x32_bf16 a[60:63], a[156:159], v[140:143], a[60:63]\n" \
"s_waitcnt lgkmcnt(4)\n" \
"ds_read_b64_tr_b16 a[212:213], v15 offset:16384\n" \
"v_cvt_pk_bf16_f32 v150, v108, v109\n" \
"v_cvt_pk_bf16_f32 v151, v110, v111\n" \
"v_mfma_f32_16x16x32_bf16 v[64:67], a[128:131], v[32:35], 0\n" \
"ds_read_b64_tr_b16 a[214:215], v16 offset:16384\n" \
"v_mfma_f32_16x16x32_bf16 v[64:67], a[132:135], v[36:39], v[64:67]\n" \
"s_add_u32 s16, s16, s9\n" \
"v_exp_f32 v112, v112\n" \
"v_mfma_f32_16x16x32_bf16 v[72:75], a[128:131], v[48:51], 0\n" \
"ds_read_b128 a[152:155], v5 offset:38912\n" \
"v_mfma_f32_16x16x32_bf16 v[72:75], a[132:135], v[52:55], v[72:75]\n" \
"s_add_u32 m0, s48, 0\n" \
"v_exp_f32 v113, v113\n" \
"v_mfma_f32_16x16x32_bf16 v[80:83], a[128:131], a[64:67], 0\n" \
"buffer_load_dwordx4 v6, s[40:43], s16 offen offset:0 sc1 lds\n" \
"v_mfma_f32_16x16x32_bf16 v[80:83], a[132:135], a[68:71], v[80:83]\n" \
"ds_read_b64_tr_b16 a[216:217], v13 offset:24576\n" \
"v_cvt_pk_bf16_f32 v152, v112, v113\n" \
"v_mfma_f32_16x16x32_bf16 v[88:91], a[128:131], a[80:83], 0\n" \
"ds_read_b128 a[156:159], v5 offset:39936\n" \
"v_mfma_f32_16x16x32_bf16 v[88:91], a[132:135], a[84:87], v[88:91]\n" \
"s_waitcnt lgkmcnt(4)\n" \
"v_exp_f32 v114, v114\n" \
"s_add_u32 m0, s79, 131072\n" \
"v_mfma_f32_16x16x32_bf16 v[64:67], a[136:139], v[40:43], v[64:67]\n" \
"buffer_load_dwordx4 v12, s[28:31], s81 offen offset:0 lds\n" \
"v_mfma_f32_16x16x32_bf16 v[64:67], a[140:143], v[44:47], v[64:67]\n" \
"s_xor_b32 s49, s49, 0x10000\n" \
"v_exp_f32 v115, v115\n" \
"v_mfma_f32_16x16x32_bf16 v[72:75], a[136:139], v[56:59], v[72:75]\n" \
"ds_read_b128 a[128:131], v5 offset:49152\n" \
"v_cvt_pk_bf16_f32 v153, v114, v115\n" \
"v_mfma_f32_16x16x32_bf16 v[72:75], a[140:143], v[60:63], v[72:75]\n" \
"s_add_u32 m0, s48, 0\n" \
"v_exp_f32 v116, v116\n" \
"v_mfma_f32_16x16x32_bf16 v[80:83], a[136:139], a[72:75], v[80:83]\n" \
"buffer_load_dwordx4 v6, s[40:43], s16 offen offset:1024 sc1 lds\n" \
"v_mfma_f32_16x16x32_bf16 v[80:83], a[140:143], a[76:79], v[80:83]\n" \
"ds_read_b64_tr_b16 a[218:219], v14 offset:24576\n" \
"v_mfma_f32_16x16x32_bf16 v[88:91], a[136:139], a[88:91], v[88:91]\n" \
"ds_read_b128 a[132:135], v5 offset:50176\n" \
"v_mfma_f32_16x16x32_bf16 v[88:91], a[140:143], a[92:95], v[88:91]\n" \
"s_waitcnt lgkmcnt(4)\n" \
"v_exp_f32 v117, v117\n" \
"s_add_u32 m0, s79, 131968\n" \
"v_mfma_f32_16x16x32_bf16 v[68:71], a[144:147], v[32:35], 0\n" \
"buffer_load_dwordx4 v12, s[28:31], s81 offen offset:128 lds\n" \
"v_mfma_f32_16x16x32_bf16 v[68:71], a[148:151], v[36:39], v[68:71]\n" \
"s_mov_b32 s81, 589824\n" \
"v_exp_f32 v118, v118\n" \
"v_mfma_f32_16x16x32_bf16 v[76:79], a[144:147], v[48:51], 0\n" \
"ds_read_b128 a[136:139], v5 offset:51200\n" \
"v_cvt_pk_bf16_f32 v154, v116, v117\n" \
"v_mfma_f32_16x16x32_bf16 v[76:79], a[148:151], v[52:55], v[76:79]\n" \
"s_add_u32 m0, s48, 0\n" \
"v_exp_f32 v119, v119\n" \
"v_mfma_f32_16x16x32_bf16 v[84:87], a[144:147], a[64:67], 0\n" \
"buffer_load_dwordx4 v6, s[40:43], s16 offen offset:2048 sc1 lds\n" \
"v_mfma_f32_16x16x32_bf16 v[84:87], a[148:151], a[68:71], v[84:87]\n" \
"v_exp_f32 v120, v120\n" \
"v_mfma_f32_16x16x32_bf16 v[92:95], a[144:147], a[80:83], 0\n" \
"ds_read_b128 a[140:143], v5 offset:52224\n" \
"v_cvt_pk_bf16_f32 v155, v118, v119\n" \
"v_mfma_f32_16x16x32_bf16 v[92:95], a[148:151], a[84:87], v[92:95]\n" \
"s_waitcnt lgkmcnt(4)\n" \
"v_exp_f32 v121, v121\n" \
"v_mfma_f32_16x16x32_bf16 v[68:71], a[152:155], v[40:43], v[68:71]\n" \
"v_exp_f32 v122, v122\n" \
"v_mfma_f32_16x16x32_bf16 v[68:71], a[156:159], v[44:47], v[68:71]\n" \
"v_exp_f32 v123, v123\n" \
"v_mfma_f32_16x16x32_bf16 v[76:79], a[152:155], v[56:59], v[76:79]\n" \
"ds_read_b128 a[144:147], v5 offset:53248\n" \
"v_cvt_pk_bf16_f32 v156, v120, v121\n" \
"v_mfma_f32_16x16x32_bf16 v[76:79], a[156:159], v[60:63], v[76:79]\n" \
"s_add_u32 m0, s48, 0\n" \
"v_exp_f32 v124, v124\n" \
"v_mfma_f32_16x16x32_bf16 v[84:87], a[152:155], a[72:75], v[84:87]\n" \
"buffer_load_dwordx4 v6, s[40:43], s16 offen offset:3072 sc1 lds\n" \
"v_mfma_f32_16x16x32_bf16 v[84:87], a[156:159], a[76:79], v[84:87]\n" \
"v_exp_f32 v125, v125\n" \
"v_mfma_f32_16x16x32_bf16 v[92:95], a[152:155], a[88:91], v[92:95]\n" \
"ds_read_b128 a[148:151], v5 offset:54272\n" \
"v_cvt_pk_bf16_f32 v157, v122, v123\n" \
"v_mfma_f32_16x16x32_bf16 v[92:95], a[156:159], a[92:95], v[92:95]\n" \
"s_waitcnt lgkmcnt(4)\n" \
"v_exp_f32 v126, v126\n" \
"v_mfma_f32_16x16x32_bf16 v[160:163], v[180:183], v[144:147], v[160:163]\n" \
"s_add_u32 m0, s79, 139264\n" \
"v_exp_f32 v127, v127\n" \
"v_mfma_f32_16x16x32_bf16 v[164:167], v[180:183], v[148:151], v[164:167]\n" \
"buffer_load_dwordx4 v12, s[28:31], s81 offen offset:0 lds\n" \
"v_cvt_pk_bf16_f32 v158, v124, v125\n" \
"v_cvt_pk_bf16_f32 v159, v126, v127\n" \
"v_mfma_f32_16x16x32_bf16 v[168:171], v[180:183], v[152:155], v[168:171]\n" \
"ds_read_b64_tr_b16 a[220:221], v15 offset:24576\n" \
"v_mfma_f32_16x16x32_bf16 v[172:175], v[180:183], v[156:159], v[172:175]\n" \
"ds_read_b64_tr_b16 a[222:223], v16 offset:24576\n" \
"v_mfma_f32_16x16x32_bf16 v[192:195], a[128:131], v[144:147], v[192:195]\n" \
"s_add_u32 m0, s79, 140160\n" \
"v_exp_f32 v64, v64\n" \
"v_mfma_f32_16x16x32_bf16 v[196:199], a[132:135], v[144:147], v[196:199]\n" \
"buffer_load_dwordx4 v12, s[28:31], s81 offen offset:128 lds\n" \
"v_mfma_f32_16x16x32_bf16 v[224:227], a[128:131], v[148:151], v[224:227]\n" \
"s_mov_b32 s81, 655360\n" \
"ds_read_b128 a[152:155], v5 offset:55296\n" \
"v_mfma_f32_16x16x32_bf16 v[228:231], a[132:135], v[148:151], v[228:231]\n" \
"v_exp_f32 v65, v65\n" \
"v_mfma_f32_16x16x32_bf16 a[0:3], a[128:131], v[152:155], a[0:3]\n" \
"v_exp_f32 v66, v66\n" \
"v_mfma_f32_16x16x32_bf16 a[4:7], a[132:135], v[152:155], a[4:7]\n" \
"s_waitcnt lgkmcnt(1)\n" \
"v_exp_f32 v67, v67\n" \
"v_mfma_f32_16x16x32_bf16 a[32:35], a[128:131], v[156:159], a[32:35]\n" \
"ds_read_b128 a[156:159], v5 offset:56320\n" \
"v_cvt_pk_bf16_f32 v128, v64, v65\n" \
"v_mfma_f32_16x16x32_bf16 a[36:39], a[132:135], v[156:159], a[36:39]\n" \
"s_waitcnt vmcnt(17) lgkmcnt(4)\n" \
"v_exp_f32 v68, v68\n" \
"v_mfma_f32_16x16x32_bf16 v[200:203], a[136:139], v[144:147], v[200:203]\n" \
"s_barrier\n" \
"v_xor_b32 v5, 0x10000, v5\n" \
"v_cvt_pk_bf16_f32 v129, v66, v67\n" \
"v_mfma_f32_16x16x32_bf16 v[204:207], a[140:143], v[144:147], v[204:207]\n" \
"s_add_u32 s17, s17, s19\n" \
"v_exp_f32 v69, v69\n" \
"v_mfma_f32_16x16x32_bf16 v[232:235], a[136:139], v[148:151], v[232:235]\n" \
"ds_read_b128 a[128:131], v5 offset:0\n" \
"v_cvt_pk_bf16_f32 v130, v68, v69\n" \
"v_mfma_f32_16x16x32_bf16 v[236:239], a[140:143], v[148:151], v[236:239]\n" \
"s_add_u32 m0, s48, 16384\n" \
"v_exp_f32 v70, v70\n" \
"v_mfma_f32_16x16x32_bf16 a[8:11], a[136:139], v[152:155], a[8:11]\n" \
"buffer_load_dwordx4 v6, s[40:43], s17 offen offset:0 sc1 lds\n" \
"v_mfma_f32_16x16x32_bf16 a[12:15], a[140:143], v[152:155], a[12:15]\n" \
"v_exp_f32 v71, v71\n" \
"v_mfma_f32_16x16x32_bf16 a[40:43], a[136:139], v[156:159], a[40:43]\n" \
"ds_read_b128 a[132:135], v5 offset:1024\n" \
"v_cvt_pk_bf16_f32 v131, v70, v71\n" \
"v_mfma_f32_16x16x32_bf16 a[44:47], a[140:143], v[156:159], a[44:47]\n" \
"s_waitcnt lgkmcnt(4)\n" \
"v_exp_f32 v72, v72\n" \
"v_mfma_f32_16x16x32_bf16 v[208:211], a[144:147], v[144:147], v[208:211]\n" \
"v_exp_f32 v73, v73\n" \
"v_mfma_f32_16x16x32_bf16 v[212:215], a[148:151], v[144:147], v[212:215]\n" \
"v_mfma_f32_16x16x32_bf16 v[240:243], a[144:147], v[148:151], v[240:243]\n" \
"ds_read_b128 a[136:139], v5 offset:2048\n" \
"v_cvt_pk_bf16_f32 v132, v72, v73\n" \
"v_mfma_f32_16x16x32_bf16 v[244:247], a[148:151], v[148:151], v[244:247]\n" \
"s_add_u32 m0, s48, 16384\n" \
"v_exp_f32 v74, v74\n" \
"v_mfma_f32_16x16x32_bf16 a[16:19], a[144:147], v[152:155], a[16:19]\n" \
"buffer_load_dwordx4 v6, s[40:43], s17 offen offset:1024 sc1 lds\n" \
"v_mfma_f32_16x16x32_bf16 a[20:23], a[148:151], v[152:155], a[20:23]\n" \
"v_exp_f32 v75, v75\n" \
"v_mfma_f32_16x16x32_bf16 a[48:51], a[144:147], v[156:159], a[48:51]\n" \
"ds_read_b128 a[140:143], v5 offset:3072\n" \
"v_cvt_pk_bf16_f32 v133, v74, v75\n" \
"v_mfma_f32_16x16x32_bf16 a[52:55], a[148:151], v[156:159], a[52:55]\n" \
"s_waitcnt lgkmcnt(4)\n" \
"s_add_u32 m0, s48, 16384\n" \
"v_mfma_f32_16x16x32_bf16 v[216:219], a[152:155], v[144:147], v[216:219]\n" \
"buffer_load_dwordx4 v6, s[40:43], s17 offen offset:2048 sc1 lds\n" \
"v_mfma_f32_16x16x32_bf16 v[220:223], a[156:159], v[144:147], v[220:223]\n" \
"s_add_u32 s44, s44, 1\n" \
"v_exp_f32 v76, v76\n" \
"v_mfma_f32_16x16x32_bf16 v[248:251], a[152:155], v[148:151], v[248:251]\n" \
"ds_read_b128 a[144:147], v5 offset:4096\n" \
"v_mfma_f32_16x16x32_bf16 v[252:255], a[156:159], v[148:151], v[252:255]\n" \
"s_add_u32 m0, s48, 16384\n" \
"v_exp_f32 v77, v77\n" \
"v_mfma_f32_16x16x32_bf16 a[24:27], a[152:155], v[152:155], a[24:27]\n" \
"buffer_load_dwordx4 v6, s[40:43], s17 offen offset:3072 sc1 lds\n" \
"v_mfma_f32_16x16x32_bf16 a[28:31], a[156:159], v[152:155], a[28:31]\n" \
"s_xor_b32 s48, s48, 0x10000\n" \
"v_exp_f32 v78, v78\n" \
"v_mfma_f32_16x16x32_bf16 a[56:59], a[152:155], v[156:159], a[56:59]\n" \
"ds_read_b128 a[148:151], v5 offset:5120\n" \
"v_exp_f32 v79, v79\n" \
"v_mfma_f32_16x16x32_bf16 a[60:63], a[156:159], v[156:159], a[60:63]\n" \
"s_waitcnt lgkmcnt(4)\n" \
"v_cvt_pk_bf16_f32 v134, v76, v77\n" \
"v_cvt_pk_bf16_f32 v135, v78, v79\n" \
"v_mfma_f32_16x16x32_bf16 v[96:99], a[128:131], v[32:35], 0\n" \
"buffer_load_dwordx4 a[112:115], v188, s[36:39], s84 offen sc1\n" \
"v_mfma_f32_16x16x32_bf16 v[96:99], a[132:135], v[36:39], v[96:99]\n" \
"s_add_u32 s16, s16, s9\n" \
"v_exp_f32 v80, v80\n" \
"v_mfma_f32_16x16x32_bf16 v[104:107], a[128:131], v[48:51], 0\n" \
"ds_read_b128 a[152:155], v5 offset:6144\n" \
"v_mfma_f32_16x16x32_bf16 v[104:107], a[132:135], v[52:55], v[104:107]\n" \
"s_add_u32 m0, s49, 32768\n" \
"v_exp_f32 v81, v81\n" \
"v_mfma_f32_16x16x32_bf16 v[112:115], a[128:131], a[64:67], 0\n" \
"buffer_load_dwordx4 v6, s[40:43], s16 offen offset:0 sc1 lds\n" \
"v_mfma_f32_16x16x32_bf16 v[112:115], a[132:135], a[68:71], v[112:115]\n" \
"v_exp_f32 v82, v82\n" \
"v_mfma_f32_16x16x32_bf16 v[120:123], a[128:131], a[80:83], 0\n" \
"ds_read_b128 a[156:159], v5 offset:7168\n" \
"v_cvt_pk_bf16_f32 v136, v80, v81\n" \
"v_mfma_f32_16x16x32_bf16 v[120:123], a[132:135], a[84:87], v[120:123]\n" \
"s_waitcnt lgkmcnt(4)\n" \
"v_exp_f32 v83, v83\n" \
"s_add_u32 m0, s79, 147456\n" \
"v_mfma_f32_16x16x32_bf16 v[96:99], a[136:139], v[40:43], v[96:99]\n" \
"buffer_load_dwordx4 v12, s[28:31], s81 offen offset:0 lds\n" \
"v_mfma_f32_16x16x32_bf16 v[96:99], a[140:143], v[44:47], v[96:99]\n" \
"v_exp_f32 v84, v84\n" \
"v_mfma_f32_16x16x32_bf16 v[104:107], a[136:139], v[56:59], v[104:107]\n" \
"ds_read_b128 a[128:131], v5 offset:16384\n" \
"v_cvt_pk_bf16_f32 v137, v82, v83\n" \
"v_mfma_f32_16x16x32_bf16 v[104:107], a[140:143], v[60:63], v[104:107]\n" \
"s_add_u32 m0, s49, 32768\n" \
"v_exp_f32 v85, v85\n" \
"v_mfma_f32_16x16x32_bf16 v[112:115], a[136:139], a[72:75], v[112:115]\n" \
"buffer_load_dwordx4 v6, s[40:43], s16 offen offset:1024 sc1 lds\n" \
"v_mfma_f32_16x16x32_bf16 v[112:115], a[140:143], a[76:79], v[112:115]\n" \
"v_exp_f32 v86, v86\n" \
"v_mfma_f32_16x16x32_bf16 v[120:123], a[136:139], a[88:91], v[120:123]\n" \
"ds_read_b128 a[132:135], v5 offset:17408\n" \
"v_cvt_pk_bf16_f32 v138, v84, v85\n" \
"v_mfma_f32_16x16x32_bf16 v[120:123], a[140:143], a[92:95], v[120:123]\n" \
"s_waitcnt lgkmcnt(4)\n" \
"v_exp_f32 v87, v87\n" \
"s_add_u32 m0, s79, 148352\n" \
"v_mfma_f32_16x16x32_bf16 v[100:103], a[144:147], v[32:35], 0\n" \
"buffer_load_dwordx4 v12, s[28:31], s81 offen offset:128 lds\n" \
"v_mfma_f32_16x16x32_bf16 v[100:103], a[148:151], v[36:39], v[100:103]\n" \
"s_mov_b32 s81, 720896\n" \
"v_exp_f32 v88, v88\n" \
"v_mfma_f32_16x16x32_bf16 v[108:111], a[144:147], v[48:51], 0\n" \
"ds_read_b128 a[136:139], v5 offset:18432\n" \
"v_cvt_pk_bf16_f32 v139, v86, v87\n" \
"v_mfma_f32_16x16x32_bf16 v[108:111], a[148:151], v[52:55], v[108:111]\n" \
"s_add_u32 m0, s49, 32768\n" \
"v_exp_f32 v89, v89\n" \
"v_mfma_f32_16x16x32_bf16 v[116:119], a[144:147], a[64:67], 0\n" \
"buffer_load_dwordx4 v6, s[40:43], s16 offen offset:2048 sc1 lds\n" \
"v_mfma_f32_16x16x32_bf16 v[116:119], a[148:151], a[68:71], v[116:119]\n" \
"v_exp_f32 v90, v90\n" \
"v_mfma_f32_16x16x32_bf16 v[124:127], a[144:147], a[80:83], 0\n" \
"ds_read_b128 a[140:143], v5 offset:19456\n" \
"v_cvt_pk_bf16_f32 v140, v88, v89\n" \
"v_mfma_f32_16x16x32_bf16 v[124:127], a[148:151], a[84:87], v[124:127]\n" \
"s_waitcnt lgkmcnt(4)\n" \
"v_exp_f32 v91, v91\n" \
"s_add_u32 m0, s79, 155648\n" \
"v_mfma_f32_16x16x32_bf16 v[100:103], a[152:155], v[40:43], v[100:103]\n" \
"buffer_load_dwordx4 v12, s[28:31], s81 offen offset:0 lds\n" \
"v_mfma_f32_16x16x32_bf16 v[100:103], a[156:159], v[44:47], v[100:103]\n" \
"v_exp_f32 v92, v92\n" \
"v_mfma_f32_16x16x32_bf16 v[108:111], a[152:155], v[56:59], v[108:111]\n" \
"ds_read_b128 a[144:147], v5 offset:20480\n" \
"v_cvt_pk_bf16_f32 v141, v90, v91\n" \
"v_mfma_f32_16x16x32_bf16 v[108:111], a[156:159], v[60:63], v[108:111]\n" \
"s_add_u32 m0, s49, 32768\n" \
"v_exp_f32 v93, v93\n" \
"v_mfma_f32_16x16x32_bf16 v[116:119], a[152:155], a[72:75], v[116:119]\n" \
"buffer_load_dwordx4 v6, s[40:43], s16 offen offset:3072 sc1 lds\n" \
"v_mfma_f32_16x16x32_bf16 v[116:119], a[156:159], a[76:79], v[116:119]\n" \
"v_exp_f32 v94, v94\n" \
"v_mfma_f32_16x16x32_bf16 v[124:127], a[152:155], a[88:91], v[124:127]\n" \
"ds_read_b128 a[148:151], v5 offset:21504\n" \
"v_cvt_pk_bf16_f32 v142, v92, v93\n" \
"v_mfma_f32_16x16x32_bf16 v[124:127], a[156:159], a[92:95], v[124:127]\n" \
"s_waitcnt lgkmcnt(4)\n" \
"v_exp_f32 v95, v95\n" \
"s_add_u32 m0, s79, 156544\n" \
"v_mfma_f32_16x16x32_bf16 v[160:163], v[180:183], v[128:131], v[160:163]\n" \
"buffer_load_dwordx4 v12, s[28:31], s81 offen offset:128 lds\n" \
"v_mfma_f32_16x16x32_bf16 v[164:167], v[180:183], v[132:135], v[164:167]\n" \
"v_cvt_pk_bf16_f32 v143, v94, v95\n" \
"v_mfma_f32_16x16x32_bf16 v[168:171], v[180:183], v[136:139], v[168:171]\n" \
"buffer_store_dwordx4 v[28:31], v4, s[36:39], s80 offen offset:3072 sc1\n" \
"v_mfma_f32_16x16x32_bf16 v[172:175], v[180:183], v[140:143], v[172:175]\n" \
"v_exp_f32 v96, v96\n" \
"v_mfma_f32_16x16x32_bf16 v[192:195], a[128:131], v[128:131], v[192:195]\n" \
"buffer_load_dwordx4 a[116:119], v189, s[36:39], s84 offen sc1\n" \
"v_mfma_f32_16x16x32_bf16 v[196:199], a[132:135], v[128:131], v[196:199]\n" \
"v_bfe_u32 v28, v4, 7, 1\n" \
"v_bfe_u32 v29, v4, 4, 3\n" \
"v_mfma_f32_16x16x32_bf16 v[224:227], a[128:131], v[132:135], v[224:227]\n" \
"ds_read_b128 a[152:155], v5 offset:22528\n" \
"v_lshl_add_u32 v28, v29, 1, v28\n" \
"v_mfma_f32_16x16x32_bf16 v[228:231], a[132:135], v[132:135], v[228:231]\n" \
"v_lshrrev_b32 v29, 8, v4\n" \
"v_lshl_add_u32 v28, v29, 4, v28\n" \
"v_mfma_f32_16x16x32_bf16 a[0:3], a[128:131], v[136:139], a[0:3]\n" \
"v_exp_f32 v97, v97\n" \
"v_mfma_f32_16x16x32_bf16 a[4:7], a[132:135], v[136:139], a[4:7]\n" \
"v_exp_f32 v98, v98\n" \
"v_mfma_f32_16x16x32_bf16 a[32:35], a[128:131], v[140:143], a[32:35]\n" \
"ds_read_b128 a[156:159], v5 offset:23552\n" \
"v_lshlrev_b32 v28, 4, v28\n" \
"v_mfma_f32_16x16x32_bf16 a[36:39], a[132:135], v[140:143], a[36:39]\n" \
"s_waitcnt vmcnt(15) lgkmcnt(4)\n" \
"v_exp_f32 v99, v99\n" \
"v_mfma_f32_16x16x32_bf16 v[200:203], a[136:139], v[128:131], v[200:203]\n" \
"s_barrier\n" \
"v_exp_f32 v100, v100\n" \
"v_mfma_f32_16x16x32_bf16 v[204:207], a[140:143], v[128:131], v[204:207]\n" \
"s_add_u32 s17, s17, s19\n" \
"v_exp_f32 v101, v101\n" \
"v_mfma_f32_16x16x32_bf16 v[232:235], a[136:139], v[132:135], v[232:235]\n" \
"ds_read_b128 a[128:131], v5 offset:32768\n" \
"v_add_u32 v29, 0x10000, v28\n" \
"v_mfma_f32_16x16x32_bf16 v[236:239], a[140:143], v[132:135], v[236:239]\n" \
"s_add_u32 m0, s49, 49152\n" \
"v_exp_f32 v102, v102\n" \
"v_mfma_f32_16x16x32_bf16 a[8:11], a[136:139], v[136:139], a[8:11]\n" \
"buffer_load_dwordx4 v6, s[40:43], s17 offen offset:0 sc1 lds\n" \
"v_mfma_f32_16x16x32_bf16 a[12:15], a[140:143], v[136:139], a[12:15]\n" \
"v_exp_f32 v103, v103\n" \
"v_mfma_f32_16x16x32_bf16 a[40:43], a[136:139], v[140:143], a[40:43]\n" \
"ds_read_b128 a[132:135], v5 offset:33792\n" \
"v_add_u32 v30, 0x20000, v28\n" \
"v_mfma_f32_16x16x32_bf16 a[44:47], a[140:143], v[140:143], a[44:47]\n" \
"s_waitcnt lgkmcnt(4)\n" \
"v_exp_f32 v104, v104\n" \
"v_mfma_f32_16x16x32_bf16 v[208:211], a[144:147], v[128:131], v[208:211]\n" \
"v_exp_f32 v105, v105\n" \
"v_mfma_f32_16x16x32_bf16 v[212:215], a[148:151], v[128:131], v[212:215]\n" \
"v_mfma_f32_16x16x32_bf16 v[240:243], a[144:147], v[132:135], v[240:243]\n" \
"ds_read_b128 a[136:139], v5 offset:34816\n" \
"v_add_u32 v31, s22, v4\n" \
"v_mfma_f32_16x16x32_bf16 v[244:247], a[148:151], v[132:135], v[244:247]\n" \
"s_add_u32 m0, s49, 49152\n" \
"v_exp_f32 v106, v106\n" \
"v_mfma_f32_16x16x32_bf16 a[16:19], a[144:147], v[136:139], a[16:19]\n" \
"buffer_load_dwordx4 v6, s[40:43], s17 offen offset:1024 sc1 lds\n" \
"v_mfma_f32_16x16x32_bf16 a[20:23], a[148:151], v[136:139], a[20:23]\n" \
"v_exp_f32 v107, v107\n" \
"v_mfma_f32_16x16x32_bf16 a[48:51], a[144:147], v[140:143], a[48:51]\n" \
"ds_read_b128 a[140:143], v5 offset:35840\n" \
"v_cvt_pk_bf16_f32 v144, v96, v97\n" \
"v_mfma_f32_16x16x32_bf16 a[52:55], a[148:151], v[140:143], a[52:55]\n" \
"s_waitcnt lgkmcnt(4)\n" \
"s_add_u32 m0, s49, 49152\n" \
"ds_read_b64_tr_b16 a[224:225], v13 offset:0\n" \
"v_mfma_f32_16x16x32_bf16 v[216:219], a[152:155], v[128:131], v[216:219]\n" \
"buffer_load_dwordx4 v6, s[40:43], s17 offen offset:2048 sc1 lds\n" \
"v_mfma_f32_16x16x32_bf16 v[220:223], a[156:159], v[128:131], v[220:223]\n" \
"v_exp_f32 v108, v108\n" \
"v_mfma_f32_16x16x32_bf16 v[248:251], a[152:155], v[132:135], v[248:251]\n" \
"ds_read_b128 a[144:147], v5 offset:36864\n" \
"v_cvt_pk_bf16_f32 v145, v98, v99\n" \
"v_mfma_f32_16x16x32_bf16 v[252:255], a[156:159], v[132:135], v[252:255]\n" \
"s_add_u32 m0, s49, 49152\n" \
"v_exp_f32 v109, v109\n" \
"v_mfma_f32_16x16x32_bf16 a[24:27], a[152:155], v[136:139], a[24:27]\n" \
"buffer_load_dwordx4 v6, s[40:43], s17 offen offset:3072 sc1 lds\n" \
"v_mfma_f32_16x16x32_bf16 a[28:31], a[156:159], v[136:139], a[28:31]\n" \
"v_exp_f32 v110, v110\n" \
"v_mfma_f32_16x16x32_bf16 a[56:59], a[152:155], v[140:143], a[56:59]\n" \
"ds_read_b128 a[148:151], v5 offset:37888\n" \
"v_exp_f32 v111, v111\n" \
"v_mfma_f32_16x16x32_bf16 a[60:63], a[156:159], v[140:143], a[60:63]\n" \
"s_waitcnt lgkmcnt(4)\n" \
"ds_read_b64_tr_b16 a[226:227], v14 offset:0\n" \
"v_cvt_pk_bf16_f32 v146, v100, v101\n" \
"v_cvt_pk_bf16_f32 v147, v102, v103\n" \
"v_cvt_pk_bf16_f32 v148, v104, v105\n" \
"v_cvt_pk_bf16_f32 v149, v106, v107\n" \
"v_cvt_pk_bf16_f32 v150, v108, v109\n" \
"v_cvt_pk_bf16_f32 v151, v110, v111\n" \
"v_mfma_f32_16x16x32_bf16 v[64:67], a[128:131], v[32:35], 0\n" \
"LH19_%=:\ns_cbranch_execz LHR1_%=\n" \
"buffer_load_dwordx4 a[120:123], v188, s[36:39], s85 offen sc1\n" \
"v_mfma_f32_16x16x32_bf16 v[64:67], a[132:135], v[36:39], v[64:67]\n" \
"s_add_u32 s16, s16, s9\n" \
"ds_read_b64_tr_b16 a[228:229], v15 offset:0\n" \
"v_mfma_f32_16x16x32_bf16 v[72:75], a[128:131], v[48:51], 0\n" \
"s_add_u32 s100, s100, 262144\ns_addc_u32 s101, s101, 0\n" \
"ds_read_b128 a[152:155], v5 offset:38912\n" \
"v_mfma_f32_16x16x32_bf16 v[72:75], a[132:135], v[52:55], v[72:75]\n" \
"s_add_u32 m0, s48, 0\n" \
"ds_read_b64_tr_b16 a[230:231], v16 offset:0\n" \
"v_mfma_f32_16x16x32_bf16 v[80:83], a[128:131], a[64:67], 0\n" \
"buffer_load_dwordx4 v6, s[40:43], s16 offen offset:0 sc1 lds\n" \
"v_mfma_f32_16x16x32_bf16 v[80:83], a[132:135], a[68:71], v[80:83]\n" \
"ds_read_b64_tr_b16 a[232:233], v13 offset:8192\n" \
"v_mfma_f32_16x16x32_bf16 v[88:91], a[128:131], a[80:83], 0\n" \
"ds_read_b128 a[156:159], v5 offset:39936\n" \
"v_mfma_f32_16x16x32_bf16 v[88:91], a[132:135], a[84:87], v[88:91]\n" \
"s_waitcnt lgkmcnt(4)\n" \
"v_exp_f32 v112, v112\n" \
"v_mfma_f32_16x16x32_bf16 v[64:67], a[136:139], v[40:43], v[64:67]\n" \
"buffer_load_dwordx4 a[124:127], v189, s[36:39], s85 offen sc1\n" \
"v_mfma_f32_16x16x32_bf16 v[64:67], a[140:143], v[44:47], v[64:67]\n" \
"s_xor_b32 s49, s49, 0x10000\n" \
"v_exp_f32 v113, v113\n" \
"v_mfma_f32_16x16x32_bf16 v[72:75], a[136:139], v[56:59], v[72:75]\n" \
"ds_read_b128 a[128:131], v5 offset:49152\n" \
"v_cvt_pk_bf16_f32 v152, v112, v113\n" \
"v_mfma_f32_16x16x32_bf16 v[72:75], a[140:143], v[60:63], v[72:75]\n" \
"s_add_u32 m0, s48, 0\n" \
"v_exp_f32 v114, v114\n" \
"v_mfma_f32_16x16x32_bf16 v[80:83], a[136:139], a[72:75], v[80:83]\n" \
"buffer_load_dwordx4 v6, s[40:43], s16 offen offset:1024 sc1 lds\n" \
"v_mfma_f32_16x16x32_bf16 v[80:83], a[140:143], a[76:79], v[80:83]\n" \
"v_exp_f32 v115, v115\n" \
"v_mfma_f32_16x16x32_bf16 v[88:91], a[136:139], a[88:91], v[88:91]\n" \
"ds_read_b128 a[132:135], v5 offset:50176\n" \
"v_cvt_pk_bf16_f32 v153, v114, v115\n" \
"v_mfma_f32_16x16x32_bf16 v[88:91], a[140:143], a[92:95], v[88:91]\n" \
"s_waitcnt lgkmcnt(4)\n" \
"v_exp_f32 v116, v116\n" \
"v_mfma_f32_16x16x32_bf16 v[68:71], a[144:147], v[32:35], 0\n" \
"v_exp_f32 v117, v117\n" \
"v_mfma_f32_16x16x32_bf16 v[68:71], a[148:151], v[36:39], v[68:71]\n" \
"v_exp_f32 v118, v118\n" \
"v_mfma_f32_16x16x32_bf16 v[76:79], a[144:147], v[48:51], 0\n" \
"ds_read_b128 a[136:139], v5 offset:51200\n" \
"v_cvt_pk_bf16_f32 v154, v116, v117\n" \
"v_mfma_f32_16x16x32_bf16 v[76:79], a[148:151], v[52:55], v[76:79]\n" \
"s_add_u32 m0, s48, 0\n" \
"v_exp_f32 v119, v119\n" \
"v_mfma_f32_16x16x32_bf16 v[84:87], a[144:147], a[64:67], 0\n" \
"buffer_load_dwordx4 v6, s[40:43], s16 offen offset:2048 sc1 lds\n" \
"v_mfma_f32_16x16x32_bf16 v[84:87], a[148:151], a[68:71], v[84:87]\n" \
"v_exp_f32 v120, v120\n" \
"v_mfma_f32_16x16x32_bf16 v[92:95], a[144:147], a[80:83], 0\n" \
"ds_read_b128 a[140:143], v5 offset:52224\n" \
"v_cvt_pk_bf16_f32 v155, v118, v119\n" \
"v_mfma_f32_16x16x32_bf16 v[92:95], a[148:151], a[84:87], v[92:95]\n" \
"s_waitcnt lgkmcnt(4)\n" \
"v_exp_f32 v121, v121\n" \
"v_mfma_f32_16x16x32_bf16 v[68:71], a[152:155], v[40:43], v[68:71]\n" \
"v_exp_f32 v122, v122\n" \
"v_mfma_f32_16x16x32_bf16 v[68:71], a[156:159], v[44:47], v[68:71]\n" \
"v_exp_f32 v123, v123\n" \
"v_mfma_f32_16x16x32_bf16 v[76:79], a[152:155], v[56:59], v[76:79]\n" \
"ds_read_b128 a[144:147], v5 offset:53248\n" \
"v_cvt_pk_bf16_f32 v156, v120, v121\n" \
"v_mfma_f32_16x16x32_bf16 v[76:79], a[156:159], v[60:63], v[76:79]\n" \
"s_add_u32 m0, s48, 0\n" \
"v_exp_f32 v124, v124\n" \
"v_mfma_f32_16x16x32_bf16 v[84:87], a[152:155], a[72:75], v[84:87]\n" \
"buffer_load_dwordx4 v6, s[40:43], s16 offen offset:3072 sc1 lds\n" \
"v_mfma_f32_16x16x32_bf16 v[84:87], a[156:159], a[76:79], v[84:87]\n" \
"v_exp_f32 v125, v125\n" \
"v_mfma_f32_16x16x32_bf16 v[92:95], a[152:155], a[88:91], v[92:95]\n" \
"ds_read_b128 a[148:151], v5 offset:54272\n" \
"v_cvt_pk_bf16_f32 v157, v122, v123\n" \
"v_mfma_f32_16x16x32_bf16 v[92:95], a[156:159], a[92:95], v[92:95]\n" \
"s_waitcnt lgkmcnt(4)\n" \
"v_exp_f32 v126, v126\n" \
"v_mfma_f32_16x16x32_bf16 v[160:163], v[180:183], v[144:147], v[160:163]\n" \
"s_lshl_b32 s25, s45, 2\n" \
"v_exp_f32 v127, v127\n" \
"v_mfma_f32_16x16x32_bf16 v[164:167], v[180:183], v[148:151], v[164:167]\n" \
"buffer_store_dwordx2 v[10:11], v20, s[36:39], s12 offen sc1\n" \
"v_cvt_pk_bf16_f32 v158, v124, v125\n" \
"v_cvt_pk_bf16_f32 v159, v126, v127\n" \
"v_mfma_f32_16x16x32_bf16 v[168:171], v[180:183], v[152:155], v[168:171]\n" \
"s_andn2_b32 s25, 32, s25\n" \
"ds_read_b64_tr_b16 a[234:235], v14 offset:8192\n" \
"s_add_u32 s25, s25, %2\n" \
"v_mfma_f32_16x16x32_bf16 v[172:175], v[180:183], v[156:159], v[172:175]\n" \
"s_mov_b64 exec, 0xffff\nglobal_load_dword v1, v2, s[100:101]\ns_mov_b64 exec, -1\n" \
"v_mfma_f32_16x16x32_bf16 v[192:195], a[128:131], v[144:147], v[192:195]\n" \
"s_cmp_ge_u32 s25, 34\ns_cselect_b64 vcc, -1, 0\n" \
"ds_read_b64_tr_b16 a[236:237], v15 offset:8192\n" \
"v_mfma_f32_16x16x32_bf16 v[196:199], a[132:135], v[144:147], v[196:199]\n" \
"ds_read_b64_tr_b16 a[238:239], v16 offset:8192\n" \
"v_bfe_u32 v190, v4, 7, 2\n" \
"v_mfma_f32_16x16x32_bf16 v[224:227], a[128:131], v[148:151], v[224:227]\n" \
"ds_read_b128 a[152:155], v5 offset:55296\n" \
"v_min_u32 v190, 2, v190\n" \
"v_mfma_f32_16x16x32_bf16 v[228:231], a[132:135], v[148:151], v[228:231]\n" \
"v_exp_f32 v64, v64\n" \
"v_mfma_f32_16x16x32_bf16 a[0:3], a[128:131], v[152:155], a[0:3]\n" \
"v_exp_f32 v65, v65\n" \
"v_mfma_f32_16x16x32_bf16 a[4:7], a[132:135], v[152:155], a[4:7]\n" \
"s_waitcnt lgkmcnt(1)\n" \
"v_exp_f32 v66, v66\n" \
"v_mfma_f32_16x16x32_bf16 a[32:35], a[128:131], v[156:159], a[32:35]\n" \
"ds_read_b128 a[156:159], v5 offset:56320\n" \
"v_lshl_add_u32 v17, v190, 8, s98\n" \
"v_mfma_f32_16x16x32_bf16 a[36:39], a[132:135], v[156:159], a[36:39]\n" \
"s_waitcnt vmcnt(14) lgkmcnt(4)\n" \
"v_exp_f32 v67, v67\n" \
"v_mfma_f32_16x16x32_bf16 v[200:203], a[136:139], v[144:147], v[200:203]\n" \
"s_barrier\n" \
"v_xor_b32 v5, 0x10000, v5\n" \
"v_bfe_u32 v190, v4, 4, 3\n" \
"v_mfma_f32_16x16x32_bf16 v[204:207], a[140:143], v[144:147], v[204:207]\n" \
"s_add_u32 s17, s17, s19\n" \
"v_exp_f32 v68, v68\n" \
"v_mfma_f32_16x16x32_bf16 v[232:235], a[136:139], v[148:151], v[232:235]\n" \
"s_lshr_b32 s92, %1, 5\n" \
"ds_read_b128 a[128:131], v5 offset:0\n" \
"v_lshl_add_u32 v17, v190, 3, v17\n" \
"v_mfma_f32_16x16x32_bf16 v[236:239], a[140:143], v[148:151], v[236:239]\n" \
"s_add_u32 m0, s48, 16384\n" \
"v_exp_f32 v69, v69\n" \
"v_mfma_f32_16x16x32_bf16 a[8:11], a[136:139], v[152:155], a[8:11]\n" \
"buffer_load_dwordx4 v6, s[40:43], s17 offen offset:0 sc1 lds\n" \
"v_mfma_f32_16x16x32_bf16 a[12:15], a[140:143], v[152:155], a[12:15]\n" \
"s_lshl_b32 s92, s92, 2\n" \
"v_exp_f32 v70, v70\n" \
"v_mfma_f32_16x16x32_bf16 a[40:43], a[136:139], v[156:159], a[40:43]\n" \
"s_add_u32 s92, s92, %2\n" \
"ds_read_b128 a[132:135], v5 offset:1024\n" \
"s_lshl_b32 s92, s92, 11\n" \
"v_mfma_f32_16x16x32_bf16 a[44:47], a[140:143], v[156:159], a[44:47]\n" \
"s_waitcnt lgkmcnt(4)\n" \
"v_exp_f32 v71, v71\n" \
"v_mfma_f32_16x16x32_bf16 v[208:211], a[144:147], v[144:147], v[208:211]\n" \
"s_waitcnt vmcnt(14)\n" \
"v_exp_f32 v72, v72\n" \
"v_mfma_f32_16x16x32_bf16 v[212:215], a[148:151], v[144:147], v[212:215]\n" \
"v_exp_f32 v73, v73\n" \
"v_mfma_f32_16x16x32_bf16 v[240:243], a[144:147], v[148:151], v[240:243]\n" \
"ds_read_b128 a[136:139], v5 offset:2048\n" \
"v_and_b32 v188, 0xf0, v4\n" \
"v_mfma_f32_16x16x32_bf16 v[244:247], a[148:151], v[148:151], v[244:247]\n" \
"s_add_u32 m0, s48, 16384\n" \
"v_exp_f32 v74, v74\n" \
"v_mfma_f32_16x16x32_bf16 a[16:19], a[144:147], v[152:155], a[16:19]\n" \
"buffer_load_dwordx4 v6, s[40:43], s17 offen offset:1024 sc1 lds\n" \
"v_mfma_f32_16x16x32_bf16 a[20:23], a[148:151], v[152:155], a[20:23]\n" \
"v_exp_f32 v75, v75\n" \
"v_mfma_f32_16x16x32_bf16 a[48:51], a[144:147], v[156:159], a[48:51]\n" \
"ds_read_b128 a[140:143], v5 offset:3072\n" \
"v_lshl_add_u32 v188, v188, 3, s92\n" \
"v_mfma_f32_16x16x32_bf16 a[52:55], a[148:151], v[156:159], a[52:55]\n" \
"s_waitcnt lgkmcnt(4)\n" \
"s_add_u32 m0, s48, 16384\n" \
"ds_read_b64_tr_b16 a[240:241], v13 offset:16384\n" \
"v_mfma_f32_16x16x32_bf16 v[216:219], a[152:155], v[144:147], v[216:219]\n" \
"buffer_load_dwordx4 v6, s[40:43], s17 offen offset:2048 sc1 lds\n" \
"v_mfma_f32_16x16x32_bf16 v[220:223], a[156:159], v[144:147], v[220:223]\n" \
"s_add_u32 s44, s44, 1\n" \
"v_exp_f32 v76, v76\n" \
"v_mfma_f32_16x16x32_bf16 v[248:251], a[152:155], v[148:151], v[248:251]\n" \
"ds_read_b128 a[144:147], v5 offset:4096\n" \
"v_lshrrev_b32 v189, 8, v4\n" \
"v_mfma_f32_16x16x32_bf16 v[252:255], a[156:159], v[148:151], v[252:255]\n" \
"s_add_u32 m0, s48, 16384\n" \
"v_exp_f32 v77, v77\n" \
"v_mfma_f32_16x16x32_bf16 a[24:27], a[152:155], v[152:155], a[24:27]\n" \
"buffer_load_dwordx4 v6, s[40:43], s17 offen offset:3072 sc1 lds\n" \
"v_mfma_f32_16x16x32_bf16 a[28:31], a[156:159], v[152:155], a[28:31]\n" \
"s_xor_b32 s48, s48, 0x10000\n" \
"v_exp_f32 v78, v78\n" \
"v_mfma_f32_16x16x32_bf16 a[56:59], a[152:155], v[156:159], a[56:59]\n" \
"ds_read_b128 a[148:151], v5 offset:5120\n" \
"v_lshl_add_u32 v190, v189, 18, v188\n" \
"v_exp_f32 v79, v79\n" \
"v_mfma_f32_16x16x32_bf16 a[60:63], a[156:159], v[156:159], a[60:63]\n" \
"s_waitcnt lgkmcnt(4)\n" \
"ds_read_b64_tr_b16 a[242:243], v14 offset:16384\n" \
"v_min_u32 v189, 2, v189\n" \
"v_cvt_pk_bf16_f32 v128, v64, v65\n" \
"v_cvt_pk_bf16_f32 v129, v66, v67\n" \
"v_cvt_pk_bf16_f32 v130, v68, v69\n" \
"v_cvt_pk_bf16_f32 v131, v70, v71\n" \
"v_cvt_pk_bf16_f32 v132, v72, v73\n" \
"v_cvt_pk_bf16_f32 v133, v74, v75\n" \
"v_cvt_pk_bf16_f32 v134, v76, v77\n" \
"v_cvt_pk_bf16_f32 v135, v78, v79\n" \
"v_mfma_f32_16x16x32_bf16 v[96:99], a[128:131], v[32:35], 0\n" \
"buffer_store_dwordx2 v[10:11], v20, s[36:39], s88 offen sc1\n" \
"v_mfma_f32_16x16x32_bf16 v[96:99], a[132:135], v[36:39], v[96:99]\n" \
"s_add_u32 s16, s16, s9\n" \
"ds_read_b64_tr_b16 a[244:245], v15 offset:16384\n" \
"s_mul_i32 s92, s90, 12\n" \
"v_mfma_f32_16x16x32_bf16 v[104:107], a[128:131], v[48:51], 0\n" \
"s_add_u32 s92, s92, s89\n" \
"ds_read_b128 a[152:155], v5 offset:6144\n" \
"s_lshl_b32 s92, s92, 16\n" \
"v_mfma_f32_16x16x32_bf16 v[104:107], a[132:135], v[52:55], v[104:107]\n" \
"s_add_u32 m0, s49, 32768\n" \
"ds_read_b64_tr_b16 a[246:247], v16 offset:16384\n" \
"v_lshl_add_u32 v191, v189, 18, v188\n" \
"v_mfma_f32_16x16x32_bf16 v[112:115], a[128:131], a[64:67], 0\n" \
"buffer_load_dwordx4 v6, s[40:43], s16 offen offset:0 sc1 lds\n" \
"v_mfma_f32_16x16x32_bf16 v[112:115], a[132:135], a[68:71], v[112:115]\n" \
"s_mov_b32 s46, s23\n" \
"ds_read_b64_tr_b16 a[248:249], v13 offset:24576\n" \
"v_add_u32 v191, s92, v191\n" \
"v_mfma_f32_16x16x32_bf16 v[120:123], a[128:131], a[80:83], 0\n" \
"s_lshl_b32 s92, %2, 13\n" \
"ds_read_b128 a[156:159], v5 offset:7168\n" \
"s_add_u32 s92, s92, 131072\n" \
"v_mfma_f32_16x16x32_bf16 v[120:123], a[132:135], a[84:87], v[120:123]\n" \
"s_waitcnt lgkmcnt(4)\n" \
"v_add_u32 v186, s92, v4\n" \
"v_xor_b32 v187, 0x6000, v186\n" \
"v_mfma_f32_16x16x32_bf16 v[96:99], a[136:139], v[40:43], v[96:99]\n" \
"s_lshl_b32 s92, %2, 9\n" \
"v_exp_f32 v80, v80\n" \
"v_mfma_f32_16x16x32_bf16 v[96:99], a[140:143], v[44:47], v[96:99]\n" \
"s_add_u32 s92, s92, 65536\n" \
"v_exp_f32 v81, v81\n" \
"v_mfma_f32_16x16x32_bf16 v[104:107], a[136:139], v[56:59], v[104:107]\n" \
"ds_read_b128 a[128:131], v5 offset:16384\n" \
"v_lshrrev_b32 v184, 2, v4\n" \
"v_mfma_f32_16x16x32_bf16 v[104:107], a[140:143], v[60:63], v[104:107]\n" \
"s_add_u32 m0, s49, 32768\n" \
"v_exp_f32 v82, v82\n" \
"v_mfma_f32_16x16x32_bf16 v[112:115], a[136:139], a[72:75], v[112:115]\n" \
"buffer_load_dwordx4 v6, s[40:43], s16 offen offset:1024 sc1 lds\n" \
"v_mfma_f32_16x16x32_bf16 v[112:115], a[140:143], a[76:79], v[112:115]\n" \
"v_exp_f32 v83, v83\n" \
"v_mfma_f32_16x16x32_bf16 v[120:123], a[136:139], a[88:91], v[120:123]\n" \
"ds_read_b128 a[132:135], v5 offset:17408\n" \
"v_add_u32 v184, s92, v184\n" \
"v_mfma_f32_16x16x32_bf16 v[120:123], a[140:143], a[92:95], v[120:123]\n" \
"s_waitcnt lgkmcnt(4)\n" \
"v_exp_f32 v84, v84\n" \
"v_mfma_f32_16x16x32_bf16 v[100:103], a[144:147], v[32:35], 0\n" \
"v_exp_f32 v85, v85\n" \
"v_mfma_f32_16x16x32_bf16 v[100:103], a[148:151], v[36:39], v[100:103]\n" \
"v_exp_f32 v86, v86\n" \
"v_mfma_f32_16x16x32_bf16 v[108:111], a[144:147], v[48:51], 0\n" \
"ds_read_b128 a[136:139], v5 offset:18432\n" \
"v_xor_b32 v185, 0x600, v184\n" \
"v_mfma_f32_16x16x32_bf16 v[108:111], a[148:151], v[52:55], v[108:111]\n" \
"s_add_u32 m0, s49, 32768\n" \
"v_exp_f32 v87, v87\n" \
"v_mfma_f32_16x16x32_bf16 v[116:119], a[144:147], a[64:67], 0\n" \
"buffer_load_dwordx4 v6, s[40:43], s16 offen offset:2048 sc1 lds\n" \
"v_mfma_f32_16x16x32_bf16 v[116:119], a[148:151], a[68:71], v[116:119]\n" \
"v_exp_f32 v88, v88\n" \
"v_mfma_f32_16x16x32_bf16 v[124:127], a[144:147], a[80:83], 0\n" \
"s_add_u32 s100, s100, 262144\ns_addc_u32 s101, s101, 0\n" \
"ds_read_b128 a[140:143], v5 offset:19456\n" \
"v_mfma_f32_16x16x32_bf16 v[124:127], a[148:151], a[84:87], v[124:127]\n" \
"s_waitcnt lgkmcnt(4)\n" \
"v_exp_f32 v89, v89\n" \
"v_mfma_f32_16x16x32_bf16 v[100:103], a[152:155], v[40:43], v[100:103]\n" \
"v_exp_f32 v90, v90\n" \
"v_mfma_f32_16x16x32_bf16 v[100:103], a[156:159], v[44:47], v[100:103]\n" \
"v_exp_f32 v91, v91\n" \
"v_mfma_f32_16x16x32_bf16 v[108:111], a[152:155], v[56:59], v[108:111]\n" \
"ds_read_b128 a[144:147], v5 offset:20480\n" \
"v_cvt_pk_bf16_f32 v136, v80, v81\n" \
"v_mfma_f32_16x16x32_bf16 v[108:111], a[156:159], v[60:63], v[108:111]\n" \
"s_add_u32 m0, s49, 32768\n" \
"v_exp_f32 v92, v92\n" \
"v_mfma_f32_16x16x32_bf16 v[116:119], a[152:155], a[72:75], v[116:119]\n" \
"buffer_load_dwordx4 v6, s[40:43], s16 offen offset:3072 sc1 lds\n" \
"v_mfma_f32_16x16x32_bf16 v[116:119], a[156:159], a[76:79], v[116:119]\n" \
"v_exp_f32 v93, v93\n" \
"v_mfma_f32_16x16x32_bf16 v[124:127], a[152:155], a[88:91], v[124:127]\n" \
"ds_read_b128 a[148:151], v5 offset:21504\n" \
"v_cvt_pk_bf16_f32 v137, v82, v83\n" \
"v_mfma_f32_16x16x32_bf16 v[124:127], a[156:159], a[92:95], v[124:127]\n" \
"s_waitcnt lgkmcnt(4)\n" \
"v_exp_f32 v94, v94\n" \
"v_mfma_f32_16x16x32_bf16 v[160:163], v[180:183], v[128:131], v[160:163]\n" \
"v_exp_f32 v95, v95\n" \
"v_cvt_pk_bf16_f32 v138, v84, v85\n" \
"v_cvt_pk_bf16_f32 v139, v86, v87\n" \
"v_mfma_f32_16x16x32_bf16 v[164:167], v[180:183], v[132:135], v[164:167]\n" \
"s_mov_b64 exec, 0xffff\nglobal_load_dword v1, v2, s[100:101]\ns_mov_b64 exec, -1\n" \
"v_cvt_pk_bf16_f32 v140, v88, v89\n" \
"v_cvt_pk_bf16_f32 v141, v90, v91\n" \
"v_cvt_pk_bf16_f32 v142, v92, v93\n" \
"v_cvt_pk_bf16_f32 v143, v94, v95\n" \
"v_mfma_f32_16x16x32_bf16 v[168:171], v[180:183], v[136:139], v[168:171]\n" \
"s_add_u32 s100, s100, 262144\ns_addc_u32 s101, s101, 0\n" \
"ds_read_b64_tr_b16 a[250:251], v14 offset:24576\n" \
"v_mfma_f32_16x16x32_bf16 v[172:175], v[180:183], v[140:143], v[172:175]\n" \
"ds_read_b64_tr_b16 a[252:253], v15 offset:24576\n" \
"v_mfma_f32_16x16x32_bf16 v[192:195], a[128:131], v[128:131], v[192:195]\n" \
"ds_read_b64_tr_b16 a[254:255], v16 offset:24576\n" \
"v_mfma_f32_16x16x32_bf16 v[196:199], a[132:135], v[128:131], v[196:199]\n" \
"s_mov_b64 exec, 0xffff\nglobal_load_dword v1, v2, s[100:101]\ns_mov_b64 exec, -1\n" \
"v_mfma_f32_16x16x32_bf16 v[224:227], a[128:131], v[132:135], v[224:227]\n" \
"ds_read_b128 a[152:155], v5 offset:22528\n" \
"v_mov_b32 v2, v190\n" \
"v_mfma_f32_16x16x32_bf16 v[228:231], a[132:135], v[132:135], v[228:231]\n" \
"v_exp_f32 v96, v96\n" \
"v_mfma_f32_16x16x32_bf16 a[0:3], a[128:131], v[136:139], a[0:3]\n" \
"v_exp_f32 v97, v97\n" \
"v_mfma_f32_16x16x32_bf16 a[4:7], a[132:135], v[136:139], a[4:7]\n" \
"s_waitcnt lgkmcnt(1)\n" \
"v_exp_f32 v98, v98\n" \
"v_mfma_f32_16x16x32_bf16 a[32:35], a[128:131], v[140:143], a[32:35]\n" \
"ds_read_b128 a[156:159], v5 offset:23552\n" \
"v_cvt_pk_bf16_f32 v144, v96, v97\n" \
"v_mfma_f32_16x16x32_bf16 a[36:39], a[132:135], v[140:143], a[36:39]\n" \
"s_waitcnt vmcnt(19) lgkmcnt(4)\n" \
"v_exp_f32 v99, v99\n" \
"v_mfma_f32_16x16x32_bf16 v[200:203], a[136:139], v[128:131], v[200:203]\n" \
"s_barrier\n" \
"v_exp_f32 v100, v100\n" \
"v_mfma_f32_16x16x32_bf16 v[204:207], a[140:143], v[128:131], v[204:207]\n" \
"s_add_u32 s17, s17, s19\n" \
"v_exp_f32 v101, v101\n" \
"v_mfma_f32_16x16x32_bf16 v[232:235], a[136:139], v[132:135], v[232:235]\n" \
"ds_read_b128 a[128:131], v5 offset:32768\n" \
"v_cvt_pk_bf16_f32 v145, v98, v99\n" \
"v_mfma_f32_16x16x32_bf16 v[236:239], a[140:143], v[132:135], v[236:239]\n" \
"s_add_u32 m0, s49, 49152\n" \
"v_exp_f32 v102, v102\n" \
"v_mfma_f32_16x16x32_bf16 a[8:11], a[136:139], v[136:139], a[8:11]\n" \
"buffer_load_dwordx4 v6, s[40:43], s17 offen offset:0 sc1 lds\n" \
"v_mfma_f32_16x16x32_bf16 a[12:15], a[140:143], v[136:139], a[12:15]\n" \
"v_exp_f32 v103, v103\n" \
"v_mfma_f32_16x16x32_bf16 a[40:43], a[136:139], v[140:143], a[40:43]\n" \
"ds_read_b128 a[132:135], v5 offset:33792\n" \
"v_cvt_pk_bf16_f32 v146, v100, v101\n" \
"v_mfma_f32_16x16x32_bf16 a[44:47], a[140:143], v[140:143], a[44:47]\n" \
"s_waitcnt lgkmcnt(4)\n" \
"v_exp_f32 v104, v104\n" \
"v_mfma_f32_16x16x32_bf16 v[208:211], a[144:147], v[128:131], v[208:211]\n" \
"v_exp_f32 v105, v105\n" \
"v_mfma_f32_16x16x32_bf16 v[212:215], a[148:151], v[128:131], v[212:215]\n" \
"v_mfma_f32_16x16x32_bf16 v[240:243], a[144:147], v[132:135], v[240:243]\n" \
"ds_read_b128 a[136:139], v5 offset:34816\n" \
"v_cvt_pk_bf16_f32 v147, v102, v103\n" \
"v_mfma_f32_16x16x32_bf16 v[244:247], a[148:151], v[132:135], v[244:247]\n" \
"s_add_u32 m0, s49, 49152\n" \
"v_exp_f32 v106, v106\n" \
"v_mfma_f32_16x16x32_bf16 a[16:19], a[144:147], v[136:139], a[16:19]\n" \
"buffer_load_dwordx4 v6, s[40:43], s17 offen offset:1024 sc1 lds\n" \
"v_mfma_f32_16x16x32_bf16 a[20:23], a[148:151], v[136:139], a[20:23]\n" \
"v_exp_f32 v107, v107\n" \
"v_mfma_f32_16x16x32_bf16 a[48:51], a[144:147], v[140:143], a[48:51]\n" \
"ds_read_b128 a[140:143], v5 offset:35840\n" \
"v_cvt_pk_bf16_f32 v148, v104, v105\n" \
"v_mfma_f32_16x16x32_bf16 a[52:55], a[148:151], v[140:143], a[52:55]\n" \
"s_waitcnt lgkmcnt(4)\n" \
"s_add_u32 m0, s49, 49152\n" \
"v_cvt_pk_bf16_f32 v149, v106, v107\n" \
"v_mfma_f32_16x16x32_bf16 v[216:219], a[152:155], v[128:131], v[216:219]\n" \
"buffer_load_dwordx4 v6, s[40:43], s17 offen offset:2048 sc1 lds\n" \
"v_mfma_f32_16x16x32_bf16 v[220:223], a[156:159], v[128:131], v[220:223]\n" \
"v_exp_f32 v108, v108\n" \
"v_mfma_f32_16x16x32_bf16 v[248:251], a[152:155], v[132:135], v[248:251]\n" \
"ds_read_b128 a[144:147], v5 offset:36864\n" \
"v_mfma_f32_16x16x32_bf16 v[252:255], a[156:159], v[132:135], v[252:255]\n" \
"s_add_u32 m0, s49, 49152\n" \
"v_exp_f32 v109, v109\n" \
"v_mfma_f32_16x16x32_bf16 a[24:27], a[152:155], v[136:139], a[24:27]\n" \
"buffer_load_dwordx4 v6, s[40:43], s17 offen offset:3072 sc1 lds\n" \
"v_mfma_f32_16x16x32_bf16 a[28:31], a[156:159], v[136:139], a[28:31]\n" \
"v_exp_f32 v110, v110\n" \
"v_mfma_f32_16x16x32_bf16 a[56:59], a[152:155], v[140:143], a[56:59]\n" \
"ds_read_b128 a[148:151], v5 offset:37888\n" \
"v_exp_f32 v111, v111\n" \
"v_mfma_f32_16x16x32_bf16 a[60:63], a[156:159], v[140:143], a[60:63]\n" \
"s_waitcnt lgkmcnt(4)\n" \
"v_cvt_pk_bf16_f32 v150, v108, v109\n" \
"v_cvt_pk_bf16_f32 v151, v110, v111\n" \
"s_cbranch_vccz LQWB_%=\nds_write_b128 v21, v[32:35] offset:16384\n" \
"ds_write_b128 v21, v[36:39] offset:17408\n" \
"ds_write_b128 v21, v[40:43] offset:18432\n" \
"ds_write_b128 v21, v[44:47] offset:19456\n" \
"ds_write_b128 v21, v[48:51] offset:20480\n" \
"ds_write_b128 v21, v[52:55] offset:21504\n" \
"ds_write_b128 v21, v[56:59] offset:22528\n" \
"ds_write_b128 v21, v[60:63] offset:23552\n" \
"ds_write_b128 v21, a[64:67] offset:0\n" \
"ds_write_b128 v21, a[68:71] offset:1024\n" \
"ds_write_b128 v21, a[72:75] offset:2048\n" \
"ds_write_b128 v21, a[76:79] offset:3072\n" \
"ds_write_b128 v21, a[80:83] offset:4096\n" \
"ds_write_b128 v21, a[84:87] offset:5120\n" \
"ds_write_b128 v21, a[88:91] offset:6144\n" \
"ds_write_b128 v21, a[92:95] offset:7168\nLQWB_%=:\n" \
"s_mov_b64 exec, 0\ns_cmp_eq_u32 %2, 0\ns_cbranch_scc1 LH23_%=\ns_cmp_eq_u32 %2, 1\ns_cbranch_scc1 LH24_%=\ns_cmp_eq_u32 %2, 2\ns_cbranch_scc1 LH25_%=\ns_cmp_eq_u32 %2, 3\ns_cbranch_scc1 LH26_%=\nLHR2_%=:\ns_mov_b64 exec, -1\n" \
"s_branch LCHK_%=\n" \
"LAD2_%=:\n" \
"v_xor_b32 v8, 0x2000, v5\n" \
"ds_read_b128 v[32:35], v21 offset:0\n" \
"ds_read_b128 v[36:39], v21 offset:1024\n" \
"ds_read_b128 v[40:43], v21 offset:2048\n" \
"ds_read_b128 v[44:47], v21 offset:3072\n" \
"ds_read_b128 v[48:51], v21 offset:4096\n" \
"ds_read_b128 v[52:55], v21 offset:5120\n" \
"ds_read_b128 v[56:59], v21 offset:6144\n" \
"ds_read_b128 v[60:63], v21 offset:7168\n" \
"ds_read_b128 a[64:67], v21 offset:16384\n" \
"ds_read_b128 a[68:71], v21 offset:17408\n" \
"ds_read_b128 a[72:75], v21 offset:18432\n" \
"ds_read_b128 a[76:79], v21 offset:19456\n" \
"ds_read_b128 a[128:131], v5 offset:49152\n" \
"ds_read_b128 a[80:83], v21 offset:20480\n" \
"ds_read_b128 a[84:87], v21 offset:21504\n" \
"ds_read_b128 a[88:91], v21 offset:22528\n" \
"ds_read_b128 a[92:95], v21 offset:23552\n" \
"ds_read_b128 a[132:135], v5 offset:50176\n" \
"ds_read_b128 a[136:139], v5 offset:51200\n" \
"ds_read_b128 a[140:143], v5 offset:52224\n" \
"ds_read_b128 a[144:147], v5 offset:53248\n" \
"ds_read_b128 a[148:151], v5 offset:54272\n" \
"v_exp_f32 v112, v112\n" \
"s_nop 0\n" \
"v_exp_f32 v113, v113\n" \
"s_nop 0\n" \
"v_exp_f32 v114, v114\n" \
"s_nop 0\n" \
"v_exp_f32 v115, v115\n" \
"s_nop 0\n" \
"v_exp_f32 v120, v120\n" \
"s_nop 0\n" \
"v_exp_f32 v121, v121\n" \
"s_nop 0\n" \
"v_exp_f32 v122, v122\n" \
"s_nop 0\n" \
"v_exp_f32 v123, v123\n" \
"s_nop 0\n" \
"v_exp_f32 v124, v124\n" \
"s_nop 0\n" \
"v_exp_f32 v125, v125\n" \
"s_nop 0\n" \
"v_exp_f32 v126, v126\n" \
"s_nop 0\n" \
"v_exp_f32 v127, v127\n" \
"s_nop 0\n" \
"v_cvt_pk_bf16_f32 v152, v112, v113\n" \
"v_cvt_pk_bf16_f32 v153, v114, v115\n" \
"v_mov_b32 v154, 0\n" \
"v_mov_b32 v155, 0\n" \
"v_cvt_pk_bf16_f32 v156, v120, v121\n" \
"v_cvt_pk_bf16_f32 v157, v122, v123\n" \
"v_cvt_pk_bf16_f32 v158, v124, v125\n" \
"v_cvt_pk_bf16_f32 v159, v126, v127\n" \
"v_subrev_u32 v23, 0x1f800000, v160\n" \
"v_subrev_u32 v27, 0x1f800000, v164\n" \
"v_max_u32 v23, v23, v27\n" \
"v_cmp_le_u32 vcc, 0x40000000, v23\n" \
"s_or_b64 s[54:55], s[54:55], vcc\n" \
"s_nop 0\n" \
"v_rcp_f32 v22, v160\n" \
"s_nop 0\n" \
"v_rcp_f32 v26, v164\n" \
"s_nop 0\n" \
"s_waitcnt lgkmcnt(0)\n" \
"s_branch LS3_%=\n" \
".p2align 6\n" \
"LH20_%=:\ns_cbranch_execz LHR1_%=\n" \
"s_nop 0\n" \
"LS1_%=:\n" \
"v_mfma_f32_16x16x32_bf16 v[64:67], a[128:131], v[32:35], v[176:179]\n" \
"v_exp_f32 v112, v112\n" \
"v_mfma_f32_16x16x32_bf16 v[64:67], a[132:135], v[36:39], v[64:67]\n" \
"s_add_u32 s16, s16, s9\n" \
"v_mfma_f32_16x16x32_bf16 v[72:75], a[128:131], v[48:51], 0\n" \
"ds_read_b128 a[152:155], v5 offset:38912\n" \
"v_mfma_f32_16x16x32_bf16 v[72:75], a[132:135], v[52:55], v[72:75]\n" \
"s_add_u32 m0, s48, 0\n" \
"v_exp_f32 v113, v113\n" \
"v_mfma_f32_16x16x32_bf16 v[80:83], a[128:131], a[64:67], 0\n" \
"buffer_load_dwordx4 v6, s[40:43], s16 offen offset:0 sc1 lds\n" \
"v_mfma_f32_16x16x32_bf16 v[80:83], a[132:135], a[68:71], v[80:83]\n" \
"v_exp_f32 v114, v114\n" \
"v_mfma_f32_16x16x32_bf16 v[88:91], a[128:131], a[80:83], 0\n" \
"ds_read_b128 a[156:159], v5 offset:39936\n" \
"v_cvt_pk_bf16_f32 v152, v112, v113\n" \
"v_mfma_f32_16x16x32_bf16 v[88:91], a[132:135], a[84:87], v[88:91]\n" \
"s_waitcnt lgkmcnt(4)\n" \
"v_exp_f32 v115, v115\n" \
"v_mfma_f32_16x16x32_bf16 v[64:67], a[136:139], v[40:43], v[64:67]\n" \
"v_exp_f32 v116, v116\n" \
"v_mfma_f32_16x16x32_bf16 v[64:67], a[140:143], v[44:47], v[64:67]\n" \
"s_xor_b32 s49, s49, 0x10000\n" \
"v_cvt_pk_bf16_f32 v153, v114, v115\n" \
"v_mfma_f32_16x16x32_bf16 v[72:75], a[136:139], v[56:59], v[72:75]\n" \
"ds_read_b128 a[128:131], v5 offset:49152\n" \
"v_mfma_f32_16x16x32_bf16 v[72:75], a[140:143], v[60:63], v[72:75]\n" \
"v_exp_f32 v117, v117\n" \
"v_mfma_f32_16x16x32_bf16 v[80:83], a[136:139], a[72:75], v[80:83]\n" \
"buffer_load_dwordx4 v6, s[40:43], s16 offen offset:1024 sc1 lds\n" \
"v_mfma_f32_16x16x32_bf16 v[80:83], a[140:143], a[76:79], v[80:83]\n" \
"v_exp_f32 v118, v118\n" \
"v_mfma_f32_16x16x32_bf16 v[88:91], a[136:139], a[88:91], v[88:91]\n" \
"ds_read_b128 a[132:135], v5 offset:50176\n" \
"v_cvt_pk_bf16_f32 v154, v116, v117\n" \
"v_mfma_f32_16x16x32_bf16 v[88:91], a[140:143], a[92:95], v[88:91]\n" \
"s_waitcnt lgkmcnt(4)\n" \
"v_exp_f32 v119, v119\n" \
"v_mov_b32 v68, 0xff800000\n" \
"v_mov_b32 v69, 0xff800000\n" \
"v_exp_f32 v120, v120\n" \
"v_cvt_pk_bf16_f32 v155, v118, v119\n" \
"v_mfma_f32_16x16x32_bf16 v[76:79], a[144:147], v[48:51], v[176:179]\n" \
"ds_read_b128 a[136:139], v5 offset:51200\n" \
"v_mfma_f32_16x16x32_bf16 v[76:79], a[148:151], v[52:55], v[76:79]\n" \
"v_exp_f32 v121, v121\n" \
"v_mfma_f32_16x16x32_bf16 v[84:87], a[144:147], a[64:67], 0\n" \
"buffer_load_dwordx4 v6, s[40:43], s16 offen offset:2048 sc1 lds\n" \
"v_mfma_f32_16x16x32_bf16 v[84:87], a[148:151], a[68:71], v[84:87]\n" \
"v_exp_f32 v122, v122\n" \
"v_mfma_f32_16x16x32_bf16 v[92:95], a[144:147], a[80:83], 0\n" \
"ds_read_b128 a[140:143], v5 offset:52224\n" \
"v_cvt_pk_bf16_f32 v156, v120, v121\n" \
"v_mfma_f32_16x16x32_bf16 v[92:95], a[148:151], a[84:87], v[92:95]\n" \
"s_waitcnt lgkmcnt(4)\n" \
"v_exp_f32 v123, v123\n" \
"v_mov_b32 v70, 0xff800000\n" \
"v_mov_b32 v71, 0xff800000\n" \
"v_exp_f32 v124, v124\n" \
"v_cvt_pk_bf16_f32 v157, v122, v123\n" \
"v_mfma_f32_16x16x32_bf16 v[76:79], a[152:155], v[56:59], v[76:79]\n" \
"ds_read_b128 a[144:147], v5 offset:53248\n" \
"v_mfma_f32_16x16x32_bf16 v[76:79], a[156:159], v[60:63], v[76:79]\n" \
"v_exp_f32 v125, v125\n" \
"v_mfma_f32_16x16x32_bf16 v[84:87], a[152:155], a[72:75], v[84:87]\n" \
"buffer_load_dwordx4 v6, s[40:43], s16 offen offset:3072 sc1 lds\n" \
"v_mfma_f32_16x16x32_bf16 v[84:87], a[156:159], a[76:79], v[84:87]\n" \
"v_exp_f32 v126, v126\n" \
"v_mfma_f32_16x16x32_bf16 v[92:95], a[152:155], a[88:91], v[92:95]\n" \
"ds_read_b128 a[148:151], v5 offset:54272\n" \
"v_cvt_pk_bf16_f32 v158, v124, v125\n" \
"v_mfma_f32_16x16x32_bf16 v[92:95], a[156:159], a[92:95], v[92:95]\n" \
"s_waitcnt lgkmcnt(4)\n" \
"v_exp_f32 v127, v127\n" \
"v_mfma_f32_16x16x32_bf16 v[160:163], v[180:183], v[144:147], v[160:163]\n" \
"v_cvt_pk_bf16_f32 v159, v126, v127\n" \
"v_mfma_f32_16x16x32_bf16 v[164:167], v[180:183], v[148:151], v[164:167]\n" \
"v_mfma_f32_16x16x32_bf16 v[168:171], v[180:183], v[152:155], v[168:171]\n" \
"v_exp_f32 v64, v64\n" \
"v_mfma_f32_16x16x32_bf16 v[172:175], v[180:183], v[156:159], v[172:175]\n" \
"v_mfma_f32_16x16x32_bf16 v[192:195], a[128:131], v[144:147], v[192:195]\n" \
"v_exp_f32 v65, v65\n" \
"v_mfma_f32_16x16x32_bf16 v[196:199], a[132:135], v[144:147], v[196:199]\n" \
"v_cvt_pk_bf16_f32 v128, v64, v65\n" \
"v_mfma_f32_16x16x32_bf16 v[224:227], a[128:131], v[148:151], v[224:227]\n" \
"ds_read_b128 a[152:155], v5 offset:55296\n" \
"v_mfma_f32_16x16x32_bf16 v[228:231], a[132:135], v[148:151], v[228:231]\n" \
"v_exp_f32 v66, v66\n" \
"v_mfma_f32_16x16x32_bf16 a[0:3], a[128:131], v[152:155], a[0:3]\n" \
"v_exp_f32 v67, v67\n" \
"v_mfma_f32_16x16x32_bf16 a[4:7], a[132:135], v[152:155], a[4:7]\n" \
"v_cvt_pk_bf16_f32 v129, v66, v67\n" \
"v_mfma_f32_16x16x32_bf16 a[32:35], a[128:131], v[156:159], a[32:35]\n" \
"ds_read_b128 a[156:159], v5 offset:56320\n" \
"v_mfma_f32_16x16x32_bf16 a[36:39], a[132:135], v[156:159], a[36:39]\n" \
"s_waitcnt vmcnt(12) lgkmcnt(4)\n" \
"v_exp_f32 v68, v68\n" \
"v_mfma_f32_16x16x32_bf16 v[200:203], a[136:139], v[144:147], v[200:203]\n" \
"s_barrier\n" \
"v_xor_b32 v5, 0x10000, v5\n" \
"v_mfma_f32_16x16x32_bf16 v[204:207], a[140:143], v[144:147], v[204:207]\n" \
"s_add_u32 s17, s17, s19\n" \
"v_exp_f32 v69, v69\n" \
"v_mfma_f32_16x16x32_bf16 v[232:235], a[136:139], v[148:151], v[232:235]\n" \
"ds_read_b128 a[128:131], v5 offset:0\n" \
"v_cvt_pk_bf16_f32 v130, v68, v69\n" \
"v_mfma_f32_16x16x32_bf16 v[236:239], a[140:143], v[148:151], v[236:239]\n" \
"s_add_u32 m0, s48, 16384\n" \
"v_exp_f32 v70, v70\n" \
"v_mfma_f32_16x16x32_bf16 a[8:11], a[136:139], v[152:155], a[8:11]\n" \
"buffer_load_dwordx4 v6, s[40:43], s17 offen offset:0 sc1 lds\n" \
"v_mfma_f32_16x16x32_bf16 a[12:15], a[140:143], v[152:155], a[12:15]\n" \
"v_exp_f32 v71, v71\n" \
"v_mfma_f32_16x16x32_bf16 a[40:43], a[136:139], v[156:159], a[40:43]\n" \
"ds_read_b128 a[132:135], v5 offset:1024\n" \
"v_cvt_pk_bf16_f32 v131, v70, v71\n" \
"v_mfma_f32_16x16x32_bf16 a[44:47], a[140:143], v[156:159], a[44:47]\n" \
"s_waitcnt lgkmcnt(4)\n" \
"v_exp_f32 v72, v72\n" \
"v_mfma_f32_16x16x32_bf16 v[208:211], a[144:147], v[144:147], v[208:211]\n" \
"v_exp_f32 v73, v73\n" \
"v_mfma_f32_16x16x32_bf16 v[212:215], a[148:151], v[144:147], v[212:215]\n" \
"LH21_%=:\ns_cbranch_execz LHR1_%=\n" \
"v_cvt_pk_bf16_f32 v132, v72, v73\n" \
"v_mfma_f32_16x16x32_bf16 v[240:243], a[144:147], v[148:151], v[240:243]\n" \
"ds_read_b128 a[136:139], v5 offset:2048\n" \
"v_mfma_f32_16x16x32_bf16 v[244:247], a[148:151], v[148:151], v[244:247]\n" \
"v_exp_f32 v74, v74\n" \
"v_mfma_f32_16x16x32_bf16 a[16:19], a[144:147], v[152:155], a[16:19]\n" \
"buffer_load_dwordx4 v6, s[40:43], s17 offen offset:1024 sc1 lds\n" \
"v_mfma_f32_16x16x32_bf16 a[20:23], a[148:151], v[152:155], a[20:23]\n" \
"v_exp_f32 v75, v75\n" \
"v_mfma_f32_16x16x32_bf16 a[48:51], a[144:147], v[156:159], a[48:51]\n" \
"ds_read_b128 a[140:143], v5 offset:3072\n" \
"v_cvt_pk_bf16_f32 v133, v74, v75\n" \
"v_mfma_f32_16x16x32_bf16 a[52:55], a[148:151], v[156:159], a[52:55]\n" \
"s_waitcnt lgkmcnt(4)\n" \
"v_exp_f32 v76, v76\n" \
"v_mfma_f32_16x16x32_bf16 v[216:219], a[152:155], v[144:147], v[216:219]\n" \
"buffer_load_dwordx4 v6, s[40:43], s17 offen offset:2048 sc1 lds\n" \
"v_mfma_f32_16x16x32_bf16 v[220:223], a[156:159], v[144:147], v[220:223]\n" \
"s_add_u32 s44, s44, 1\n" \
"v_exp_f32 v77, v77\n" \
"v_mfma_f32_16x16x32_bf16 v[248:251], a[152:155], v[148:151], v[248:251]\n" \
"ds_read_b128 a[144:147], v5 offset:4096\n" \
"v_cvt_pk_bf16_f32 v134, v76, v77\n" \
"v_mfma_f32_16x16x32_bf16 v[252:255], a[156:159], v[148:151], v[252:255]\n" \
"v_exp_f32 v78, v78\n" \
"v_mfma_f32_16x16x32_bf16 a[24:27], a[152:155], v[152:155], a[24:27]\n" \
"buffer_load_dwordx4 v6, s[40:43], s17 offen offset:3072 sc1 lds\n" \
"v_mfma_f32_16x16x32_bf16 a[28:31], a[156:159], v[152:155], a[28:31]\n" \
"s_xor_b32 s48, s48, 0x10000\n" \
"v_exp_f32 v79, v79\n" \
"v_mfma_f32_16x16x32_bf16 a[56:59], a[152:155], v[156:159], a[56:59]\n" \
"ds_read_b128 a[148:151], v5 offset:5120\n" \
"v_cvt_pk_bf16_f32 v135, v78, v79\n" \
"v_mfma_f32_16x16x32_bf16 a[60:63], a[156:159], v[156:159], a[60:63]\n" \
"s_waitcnt lgkmcnt(4)\n" \
"s_cmp_ge_u32 %2, 2\n" \
"s_cbranch_scc1 LFIN_%=\n" \
"s_cmp_eq_u32 s44, 15\ns_cselect_b32 s16, s56, s16\ns_cselect_b32 s9, s57, s9\ns_cselect_b32 s17, s58, s17\ns_cselect_b32 s19, s59, s19\n" \
"v_mfma_f32_16x16x32_bf16 v[112:115], a[128:131], a[64:67], v[176:179]\n" \
"v_xor_b32 v8, 0x2000, v5\n" \
"ds_read_b128 v[32:35], v21 offset:0\n" \
"v_mfma_f32_16x16x32_bf16 v[112:115], a[132:135], a[68:71], v[112:115]\n" \
"s_add_u32 s16, s16, s9\n" \
"ds_read_b128 a[152:155], v5 offset:6144\n" \
"s_add_u32 m0, s49, 32768\n" \
"v_mfma_f32_16x16x32_bf16 v[120:123], a[128:131], a[80:83], 0\n" \
"buffer_load_dwordx4 v6, s[40:43], s16 offen offset:0 sc1 lds\n" \
"v_mfma_f32_16x16x32_bf16 v[120:123], a[132:135], a[84:87], v[120:123]\n" \
"s_waitcnt lgkmcnt(4)\n" \
"ds_read_b128 a[156:159], v5 offset:7168\n" \
"v_mfma_f32_16x16x32_bf16 v[112:115], a[136:139], a[72:75], v[112:115]\n" \
"ds_read_b128 a[128:131], v5 offset:16384\n" \
"v_mfma_f32_16x16x32_bf16 v[112:115], a[140:143], a[76:79], v[112:115]\n" \
"ds_read_b128 a[132:135], v5 offset:17408\n" \
"v_exp_f32 v80, v80\n" \
"v_mfma_f32_16x16x32_bf16 v[120:123], a[136:139], a[88:91], v[120:123]\n" \
"v_exp_f32 v81, v81\n" \
"v_mfma_f32_16x16x32_bf16 v[120:123], a[140:143], a[92:95], v[120:123]\n" \
"s_waitcnt lgkmcnt(5)\n" \
"ds_read_b128 a[136:139], v5 offset:18432\n" \
"v_exp_f32 v82, v82\n" \
"v_mfma_f32_16x16x32_bf16 v[124:127], a[144:147], a[80:83], v[176:179]\n" \
"buffer_load_dwordx4 v6, s[40:43], s16 offen offset:1024 sc1 lds\n" \
"v_exp_f32 v83, v83\n" \
"v_mfma_f32_16x16x32_bf16 v[124:127], a[148:151], a[84:87], v[124:127]\n" \
"s_waitcnt lgkmcnt(3)\n" \
"ds_read_b128 a[140:143], v5 offset:19456\n" \
"v_exp_f32 v84, v84\n" \
"v_mfma_f32_16x16x32_bf16 v[124:127], a[152:155], a[88:91], v[124:127]\n" \
"ds_read_b128 a[144:147], v5 offset:20480\n" \
"v_exp_f32 v85, v85\n" \
"v_mfma_f32_16x16x32_bf16 v[124:127], a[156:159], a[92:95], v[124:127]\n" \
"ds_read_b128 a[148:151], v5 offset:21504\n" \
"v_exp_f32 v86, v86\n" \
"ds_read_b128 a[152:155], v5 offset:22528\n" \
"v_exp_f32 v87, v87\n" \
"v_cvt_pk_bf16_f32 v136, v80, v81\n" \
"buffer_load_dwordx4 v6, s[40:43], s16 offen offset:2048 sc1 lds\n" \
"v_exp_f32 v88, v88\n" \
"ds_read_b128 a[156:159], v5 offset:23552\n" \
"v_exp_f32 v89, v89\n" \
"v_cvt_pk_bf16_f32 v137, v82, v83\n" \
"v_exp_f32 v90, v90\n" \
"v_exp_f32 v91, v91\n" \
"v_exp_f32 v92, v92\n" \
"v_exp_f32 v93, v93\n" \
"v_exp_f32 v94, v94\n" \
"v_exp_f32 v95, v95\n" \
"v_cvt_pk_bf16_f32 v138, v84, v85\n" \
"v_cvt_pk_bf16_f32 v139, v86, v87\n" \
"v_cvt_pk_bf16_f32 v140, v88, v89\n" \
"v_cvt_pk_bf16_f32 v141, v90, v91\n" \
"buffer_load_dwordx4 v6, s[40:43], s16 offen offset:3072 sc1 lds\n" \
"v_cvt_pk_bf16_f32 v142, v92, v93\n" \
"v_cvt_pk_bf16_f32 v143, v94, v95\n" \
"v_mfma_f32_16x16x32_bf16 v[160:163], v[180:183], v[128:131], v[160:163]\n" \
"v_exp_f32 v112, v112\n" \
"v_mfma_f32_16x16x32_bf16 v[164:167], v[180:183], v[132:135], v[164:167]\n" \
"v_exp_f32 v113, v113\n" \
"v_mfma_f32_16x16x32_bf16 v[168:171], v[180:183], v[136:139], v[168:171]\n" \
"v_exp_f32 v114, v114\n" \
"v_mfma_f32_16x16x32_bf16 v[172:175], v[180:183], v[140:143], v[172:175]\n" \
"s_waitcnt lgkmcnt(6)\n" \
"v_exp_f32 v115, v115\n" \
"v_mfma_f32_16x16x32_bf16 v[192:195], a[128:131], v[128:131], v[192:195]\n" \
"v_exp_f32 v120, v120\n" \
"v_mfma_f32_16x16x32_bf16 v[196:199], a[132:135], v[128:131], v[196:199]\n" \
"v_exp_f32 v121, v121\n" \
"v_mfma_f32_16x16x32_bf16 v[224:227], a[128:131], v[132:135], v[224:227]\n" \
"v_exp_f32 v122, v122\n" \
"v_mfma_f32_16x16x32_bf16 v[228:231], a[132:135], v[132:135], v[228:231]\n" \
"v_exp_f32 v123, v123\n" \
"v_mfma_f32_16x16x32_bf16 a[0:3], a[128:131], v[136:139], a[0:3]\n" \
"v_exp_f32 v124, v124\n" \
"v_mfma_f32_16x16x32_bf16 a[4:7], a[132:135], v[136:139], a[4:7]\n" \
"v_exp_f32 v125, v125\n" \
"v_mfma_f32_16x16x32_bf16 a[32:35], a[128:131], v[140:143], a[32:35]\n" \
"v_exp_f32 v126, v126\n" \
"v_mfma_f32_16x16x32_bf16 a[36:39], a[132:135], v[140:143], a[36:39]\n" \
"s_waitcnt lgkmcnt(4)\n" \
"v_exp_f32 v127, v127\n" \
"v_mfma_f32_16x16x32_bf16 v[200:203], a[136:139], v[128:131], v[200:203]\n" \
"v_cvt_pk_bf16_f32 v152, v112, v113\n" \
"v_cvt_pk_bf16_f32 v153, v114, v115\n" \
"v_mfma_f32_16x16x32_bf16 v[204:207], a[140:143], v[128:131], v[204:207]\n" \
"v_mov_b32 v154, 0\n" \
"v_mov_b32 v155, 0\n" \
"v_mfma_f32_16x16x32_bf16 v[232:235], a[136:139], v[132:135], v[232:235]\n" \
"v_cvt_pk_bf16_f32 v156, v120, v121\n" \
"ds_read_b128 v[36:39], v21 offset:1024\n" \
"v_mfma_f32_16x16x32_bf16 v[236:239], a[140:143], v[132:135], v[236:239]\n" \
"v_cvt_pk_bf16_f32 v157, v122, v123\n" \
"v_cvt_pk_bf16_f32 v158, v124, v125\n" \
"v_mfma_f32_16x16x32_bf16 a[8:11], a[136:139], v[136:139], a[8:11]\n" \
"v_cvt_pk_bf16_f32 v159, v126, v127\n" \
"ds_read_b128 v[40:43], v21 offset:2048\n" \
"v_mfma_f32_16x16x32_bf16 a[12:15], a[140:143], v[136:139], a[12:15]\n" \
"ds_read_b128 v[44:47], v21 offset:3072\n" \
"v_subrev_u32 v23, 0x1f800000, v160\n" \
"v_mfma_f32_16x16x32_bf16 a[40:43], a[136:139], v[140:143], a[40:43]\n" \
"ds_read_b128 v[48:51], v21 offset:4096\n" \
"v_subrev_u32 v27, 0x1f800000, v164\n" \
"v_mfma_f32_16x16x32_bf16 a[44:47], a[140:143], v[140:143], a[44:47]\n" \
"s_waitcnt lgkmcnt(6)\n" \
"ds_read_b128 v[52:55], v21 offset:5120\n" \
"v_max_u32 v23, v23, v27\n" \
"v_mfma_f32_16x16x32_bf16 v[208:211], a[144:147], v[128:131], v[208:211]\n" \
"ds_read_b128 v[56:59], v21 offset:6144\n" \
"v_cmp_le_u32 vcc, 0x40000000, v23\n" \
"v_mfma_f32_16x16x32_bf16 v[212:215], a[148:151], v[128:131], v[212:215]\n" \
"s_or_b64 s[54:55], s[54:55], vcc\n" \
"ds_read_b128 v[60:63], v21 offset:7168\n" \
"s_nop 0\n" \
"v_mfma_f32_16x16x32_bf16 v[240:243], a[144:147], v[132:135], v[240:243]\n" \
"ds_read_b128 a[64:67], v21 offset:16384\n" \
"v_mfma_f32_16x16x32_bf16 v[244:247], a[148:151], v[132:135], v[244:247]\n" \
"s_add_u32 s17, s17, s19\n" \
"s_add_u32 m0, s49, 49152\n" \
"ds_read_b128 a[68:71], v21 offset:17408\n" \
"v_mfma_f32_16x16x32_bf16 a[16:19], a[144:147], v[136:139], a[16:19]\n" \
"s_waitcnt vmcnt(12) lgkmcnt(15)\n" \
"ds_read_b128 a[72:75], v21 offset:18432\n" \
"v_mfma_f32_16x16x32_bf16 a[20:23], a[148:151], v[136:139], a[20:23]\n" \
"s_barrier\n" \
"ds_read_b128 a[76:79], v21 offset:19456\n" \
"v_mfma_f32_16x16x32_bf16 a[48:51], a[144:147], v[140:143], a[48:51]\n" \
"buffer_load_dwordx4 v6, s[40:43], s17 offen offset:0 sc1 lds\n" \
"v_mfma_f32_16x16x32_bf16 a[52:55], a[148:151], v[140:143], a[52:55]\n" \
"s_waitcnt lgkmcnt(11)\n" \
"ds_read_b128 a[128:131], v5 offset:49152\n" \
"ds_read_b128 a[80:83], v21 offset:20480\n" \
"ds_read_b128 a[84:87], v21 offset:21504\n" \
"ds_read_b128 a[88:91], v21 offset:22528\n" \
"ds_read_b128 a[92:95], v21 offset:23552\n" \
"v_mfma_f32_16x16x32_bf16 v[216:219], a[152:155], v[128:131], v[216:219]\n" \
"buffer_load_dwordx4 v6, s[40:43], s17 offen offset:1024 sc1 lds\n" \
"v_mfma_f32_16x16x32_bf16 v[220:223], a[156:159], v[128:131], v[220:223]\n" \
"ds_read_b128 a[132:135], v5 offset:50176\n" \
"v_mfma_f32_16x16x32_bf16 v[248:251], a[152:155], v[132:135], v[248:251]\n" \
"buffer_load_dwordx4 v6, s[40:43], s17 offen offset:2048 sc1 lds\n" \
"v_mfma_f32_16x16x32_bf16 v[252:255], a[156:159], v[132:135], v[252:255]\n" \
"ds_read_b128 a[136:139], v5 offset:51200\n" \
"v_mfma_f32_16x16x32_bf16 a[24:27], a[152:155], v[136:139], a[24:27]\n" \
"buffer_load_dwordx4 v6, s[40:43], s17 offen offset:3072 sc1 lds\n" \
"v_mfma_f32_16x16x32_bf16 a[28:31], a[156:159], v[136:139], a[28:31]\n" \
"ds_read_b128 a[140:143], v5 offset:52224\n" \
"v_mfma_f32_16x16x32_bf16 a[56:59], a[152:155], v[140:143], a[56:59]\n" \
"ds_read_b128 a[144:147], v5 offset:53248\n" \
"v_mfma_f32_16x16x32_bf16 a[60:63], a[156:159], v[140:143], a[60:63]\n" \
"ds_read_b128 a[148:151], v5 offset:54272\n" \
"v_rcp_f32 v22, v160\n" \
"v_rcp_f32 v26, v164\n" \
"LS3_%=:\n" \
"v_mfma_f32_16x16x32_bf16 v[168:171], v[180:183], v[152:155], v[168:171]\n" \
"s_add_u32 s16, s16, s9\n" \
"ds_read_b128 a[152:155], v5 offset:55296\n" \
"s_add_u32 m0, s48, 0\n" \
"v_mfma_f32_16x16x32_bf16 v[172:175], v[180:183], v[156:159], v[172:175]\n" \
"s_waitcnt lgkmcnt(5)\n" \
"ds_read_b128 a[156:159], v5 offset:56320\n" \
"v_mfma_f32_16x16x32_bf16 a[0:3], a[128:131], v[152:155], a[0:3]\n" \
"buffer_load_dwordx4 v6, s[40:43], s16 offen offset:0 sc1 lds\n" \
"v_mfma_f32_16x16x32_bf16 a[4:7], a[132:135], v[152:155], a[4:7]\n" \
"v_mul_f32 v192, v22, v192\nv_mul_f32 v193, v22, v193\n" \
"v_mfma_f32_16x16x32_bf16 a[32:35], a[128:131], v[156:159], a[32:35]\n" \
"v_mul_f32 v194, v22, v194\nv_mul_f32 v195, v22, v195\n" \
"v_mfma_f32_16x16x32_bf16 a[36:39], a[132:135], v[156:159], a[36:39]\n" \
"s_waitcnt lgkmcnt(4)\n" \
"ds_read_b128 a[128:131], v8 offset:32768\n" \
"v_mfma_f32_16x16x32_bf16 a[8:11], a[136:139], v[152:155], a[8:11]\n" \
"buffer_load_dwordx4 v6, s[40:43], s16 offen offset:1024 sc1 lds\n" \
"v_mfma_f32_16x16x32_bf16 a[12:15], a[140:143], v[152:155], a[12:15]\n" \
"ds_read_b128 a[132:135], v8 offset:33792\n" \
"v_mfma_f32_16x16x32_bf16 a[40:43], a[136:139], v[156:159], a[40:43]\n" \
"v_mul_f32 v196, v22, v196\nv_mul_f32 v197, v22, v197\n" \
"v_mfma_f32_16x16x32_bf16 a[44:47], a[140:143], v[156:159], a[44:47]\n" \
"s_waitcnt lgkmcnt(4)\n" \
"ds_read_b128 a[136:139], v8 offset:34816\n" \
"v_accvgpr_read_b32 v80, a0\n" \
"v_mfma_f32_16x16x32_bf16 a[16:19], a[144:147], v[152:155], a[16:19]\n" \
"ds_read_b128 a[140:143], v8 offset:35840\n" \
"v_accvgpr_read_b32 v81, a1\n" \
"v_mfma_f32_16x16x32_bf16 a[20:23], a[148:151], v[152:155], a[20:23]\n" \
"v_mul_f32 v198, v22, v198\nv_mul_f32 v199, v22, v199\n" \
"v_mfma_f32_16x16x32_bf16 a[48:51], a[144:147], v[156:159], a[48:51]\n" \
"buffer_load_dwordx4 v6, s[40:43], s16 offen offset:2048 sc1 lds\n" \
"v_mfma_f32_16x16x32_bf16 a[52:55], a[148:151], v[156:159], a[52:55]\n" \
"s_waitcnt lgkmcnt(4)\n" \
"ds_read_b128 a[144:147], v8 offset:36864\n" \
"v_cvt_pk_bf16_f32 v192, v192, v193\n" \
"v_mfma_f32_16x16x32_bf16 a[24:27], a[152:155], v[152:155], a[24:27]\n" \
"ds_read_b128 a[148:151], v8 offset:37888\n" \
"v_cvt_pk_bf16_f32 v193, v194, v195\n" \
"v_mfma_f32_16x16x32_bf16 a[28:31], a[156:159], v[152:155], a[28:31]\n" \
"v_cvt_pk_bf16_f32 v194, v196, v197\n" \
"v_cvt_pk_bf16_f32 v195, v198, v199\n" \
"v_mfma_f32_16x16x32_bf16 a[56:59], a[152:155], v[156:159], a[56:59]\n" \
"buffer_store_dwordx4 v[192:195], v4, s[36:39], s50 offen offset:0 sc1\n" \
"v_mfma_f32_16x16x32_bf16 a[60:63], a[156:159], v[156:159], a[60:63]\n" \
"ds_read_b128 a[152:155], v8 offset:38912\n" \
"v_accvgpr_read_b32 v82, a2\n" \
"buffer_load_dwordx4 v6, s[40:43], s16 offen offset:3072 sc1 lds\n" \
"v_mul_f32 v224, v26, v224\nv_mul_f32 v225, v26, v225\n" \
"ds_read_b128 a[156:159], v8 offset:39936\n" \
"v_pk_mul_f32 v[226:227], v[226:227], v[26:27] op_sel_hi:[1,0]\n" \
"v_pk_mul_f32 v[228:229], v[228:229], v[26:27] op_sel_hi:[1,0]\n" \
"v_pk_mul_f32 v[230:231], v[230:231], v[26:27] op_sel_hi:[1,0]\n" \
"v_cvt_pk_bf16_f32 v224, v224, v225\n" \
"v_cvt_pk_bf16_f32 v225, v226, v227\n" \
"v_cvt_pk_bf16_f32 v226, v228, v229\n" \
"v_cvt_pk_bf16_f32 v227, v230, v231\n" \
"buffer_store_dwordx4 v[224:227], v4, s[36:39], s50 offen offset:1024 sc1\n" \
"v_pk_mul_f32 v[200:201], v[200:201], v[22:23] op_sel_hi:[1,0]\n" \
"v_pk_mul_f32 v[202:203], v[202:203], v[22:23] op_sel_hi:[1,0]\n" \
"v_pk_mul_f32 v[204:205], v[204:205], v[22:23] op_sel_hi:[1,0]\n" \
"v_pk_mul_f32 v[206:207], v[206:207], v[22:23] op_sel_hi:[1,0]\n" \
"v_cvt_pk_bf16_f32 v200, v200, v201\n" \
"v_cvt_pk_bf16_f32 v201, v202, v203\n" \
"v_cvt_pk_bf16_f32 v202, v204, v205\n" \
"v_cvt_pk_bf16_f32 v203, v206, v207\n" \
"v_pk_mul_f32 v[232:233], v[232:233], v[26:27] op_sel_hi:[1,0]\n" \
"v_pk_mul_f32 v[234:235], v[234:235], v[26:27] op_sel_hi:[1,0]\n" \
"v_pk_mul_f32 v[236:237], v[236:237], v[26:27] op_sel_hi:[1,0]\n" \
"v_pk_mul_f32 v[238:239], v[238:239], v[26:27] op_sel_hi:[1,0]\n" \
"v_cvt_pk_bf16_f32 v232, v232, v233\n" \
"v_cvt_pk_bf16_f32 v233, v234, v235\n" \
"buffer_store_dwordx4 v[200:203], v4, s[36:39], s50 offen offset:2048 sc1\n" \
"v_cvt_pk_bf16_f32 v234, v236, v237\n" \
"v_cvt_pk_bf16_f32 v235, v238, v239\n" \
"v_pk_mul_f32 v[208:209], v[208:209], v[22:23] op_sel_hi:[1,0]\n" \
"v_pk_mul_f32 v[210:211], v[210:211], v[22:23] op_sel_hi:[1,0]\n" \
"v_pk_mul_f32 v[212:213], v[212:213], v[22:23] op_sel_hi:[1,0]\n" \
"v_pk_mul_f32 v[214:215], v[214:215], v[22:23] op_sel_hi:[1,0]\n" \
"v_cvt_pk_bf16_f32 v208, v208, v209\n" \
"v_cvt_pk_bf16_f32 v209, v210, v211\n" \
"v_cvt_pk_bf16_f32 v210, v212, v213\n" \
"v_cvt_pk_bf16_f32 v211, v214, v215\n" \
"v_pk_mul_f32 v[240:241], v[240:241], v[26:27] op_sel_hi:[1,0]\n" \
"v_pk_mul_f32 v[242:243], v[242:243], v[26:27] op_sel_hi:[1,0]\n" \
"v_pk_mul_f32 v[244:245], v[244:245], v[26:27] op_sel_hi:[1,0]\n" \
"v_pk_mul_f32 v[246:247], v[246:247], v[26:27] op_sel_hi:[1,0]\n" \
"buffer_store_dwordx4 v[232:235], v4, s[36:39], s50 offen offset:3072 sc1\n" \
"v_cvt_pk_bf16_f32 v240, v240, v241\n" \
"v_cvt_pk_bf16_f32 v241, v242, v243\n" \
"v_cvt_pk_bf16_f32 v242, v244, v245\n" \
"v_cvt_pk_bf16_f32 v243, v246, v247\n" \
"v_pk_mul_f32 v[216:217], v[216:217], v[22:23] op_sel_hi:[1,0]\n" \
"v_pk_mul_f32 v[218:219], v[218:219], v[22:23] op_sel_hi:[1,0]\n" \
"v_pk_mul_f32 v[220:221], v[220:221], v[22:23] op_sel_hi:[1,0]\n" \
"v_pk_mul_f32 v[222:223], v[222:223], v[22:23] op_sel_hi:[1,0]\n" \
"v_cvt_pk_bf16_f32 v216, v216, v217\n" \
"v_cvt_pk_bf16_f32 v217, v218, v219\n" \
"v_cvt_pk_bf16_f32 v218, v220, v221\n" \
"v_cvt_pk_bf16_f32 v219, v222, v223\n" \
"v_pk_mul_f32 v[248:249], v[248:249], v[26:27] op_sel_hi:[1,0]\n" \
"v_pk_mul_f32 v[250:251], v[250:251], v[26:27] op_sel_hi:[1,0]\n" \
"s_waitcnt lgkmcnt(6)\n" \
"buffer_store_dwordx4 v[208:211], v7, s[36:39], s50 offen offset:0 sc1\n" \
"v_pk_mul_f32 v[252:253], v[252:253], v[26:27] op_sel_hi:[1,0]\n" \
"v_mfma_f32_16x16x32_bf16 v[64:67], a[128:131], v[32:35], v[176:179]\n" \
"v_mul_f32 v254, v26, v254\nv_mul_f32 v255, v26, v255\n" \
"v_mfma_f32_16x16x32_bf16 v[64:67], a[132:135], v[36:39], v[64:67]\n" \
"v_cvt_pk_bf16_f32 v248, v248, v249\n" \
"v_cvt_pk_bf16_f32 v249, v250, v251\n" \
"v_mfma_f32_16x16x32_bf16 v[72:75], a[128:131], v[48:51], 0\n" \
"s_add_u32 s63, s50, 262144\n" \
"v_cvt_pk_bf16_f32 v250, v252, v253\n" \
"v_cvt_pk_bf16_f32 v251, v254, v255\n" \
"v_mfma_f32_16x16x32_bf16 v[72:75], a[132:135], v[52:55], v[72:75]\n" \
"s_waitcnt lgkmcnt(4)\n" \
"v_subrev_u32 v23, 0x1f800000, v168\n" \
"v_mfma_f32_16x16x32_bf16 v[64:67], a[136:139], v[40:43], v[64:67]\n" \
"buffer_store_dwordx4 v[240:243], v7, s[36:39], s50 offen offset:1024 sc1\n" \
"v_mfma_f32_16x16x32_bf16 v[64:67], a[140:143], v[44:47], v[64:67]\n" \
"v_subrev_u32 v27, 0x1f800000, v172\n" \
"v_max_u32 v23, v23, v27\n" \
"v_mfma_f32_16x16x32_bf16 v[72:75], a[136:139], v[56:59], v[72:75]\n" \
"v_cmp_le_u32 vcc, 0x40000000, v23\n" \
"s_nop 0\n" \
"v_mfma_f32_16x16x32_bf16 v[72:75], a[140:143], v[60:63], v[72:75]\n" \
"s_waitcnt lgkmcnt(2)\n" \
"s_or_b64 s[54:55], s[54:55], vcc\n" \
"v_accvgpr_read_b32 v83, a3\n" \
"v_mfma_f32_16x16x32_bf16 v[76:79], a[144:147], v[48:51], v[176:179]\n" \
"buffer_store_dwordx4 v[216:219], v7, s[36:39], s50 offen offset:2048 sc1\n" \
"v_mfma_f32_16x16x32_bf16 v[76:79], a[148:151], v[52:55], v[76:79]\n" \
"s_waitcnt lgkmcnt(0)\n" \
"v_rcp_f32 v188, v168\n" \
"v_mfma_f32_16x16x32_bf16 v[76:79], a[152:155], v[56:59], v[76:79]\n" \
"v_rcp_f32 v24, v172\n" \
"v_mfma_f32_16x16x32_bf16 v[76:79], a[156:159], v[60:63], v[76:79]\n" \
"v_accvgpr_read_b32 v84, a4\n" \
"v_accvgpr_read_b32 v85, a5\n" \
"buffer_store_dwordx4 v[248:251], v7, s[36:39], s50 offen offset:3072 sc1\n" \
"v_accvgpr_read_b32 v86, a6\n" \
"v_accvgpr_read_b32 v87, a7\n" \
"v_pk_mul_f32 v[80:81], v[80:81], v[188:189] op_sel_hi:[1,0]\n" \
"v_pk_mul_f32 v[82:83], v[82:83], v[188:189] op_sel_hi:[1,0]\n" \
"v_pk_mul_f32 v[84:85], v[84:85], v[188:189] op_sel_hi:[1,0]\n" \
"v_pk_mul_f32 v[86:87], v[86:87], v[188:189] op_sel_hi:[1,0]\n" \
"v_exp_f32 v64, v64\n" \
"v_exp_f32 v65, v65\n" \
"v_exp_f32 v66, v66\n" \
"v_exp_f32 v67, v67\n" \
"v_exp_f32 v72, v72\n" \
"v_exp_f32 v73, v73\n" \
"v_exp_f32 v74, v74\n" \
"v_exp_f32 v75, v75\n" \
"v_exp_f32 v76, v76\n" \
"v_exp_f32 v77, v77\n" \
"v_exp_f32 v78, v78\n" \
"v_exp_f32 v79, v79\n" \
"v_cvt_pk_bf16_f32 v128, v64, v65\n" \
"v_cvt_pk_bf16_f32 v129, v66, v67\n" \
"v_mov_b32 v130, 0\n" \
"v_mov_b32 v131, 0\n" \
"s_add_u32 s17, s17, s19\n" \
"s_add_u32 m0, s48, 16384\n" \
"v_cvt_pk_bf16_f32 v132, v72, v73\n" \
"v_cvt_pk_bf16_f32 v133, v74, v75\n" \
"s_waitcnt vmcnt(20) lgkmcnt(0)\n" \
"v_cvt_pk_bf16_f32 v134, v76, v77\n" \
"v_cvt_pk_bf16_f32 v135, v78, v79\n" \
"v_cvt_pk_bf16_f32 v80, v80, v81\n" \
"s_barrier\n" \
"v_xor_b32 v5, 0x12000, v5\n" \
"v_cvt_pk_bf16_f32 v81, v82, v83\n" \
"v_cvt_pk_bf16_f32 v82, v84, v85\n" \
"buffer_load_dwordx4 v6, s[40:43], s17 offen offset:0 sc1 lds\n" \
"v_cvt_pk_bf16_f32 v83, v86, v87\n" \
"v_accvgpr_read_b32 v88, a32\n" \
"ds_read_b128 a[128:131], v5 offset:0\n" \
"v_accvgpr_read_b32 v89, a33\n" \
"v_accvgpr_read_b32 v90, a34\n" \
"v_accvgpr_read_b32 v91, a35\n" \
"buffer_load_dwordx4 v6, s[40:43], s17 offen offset:1024 sc1 lds\n" \
"v_accvgpr_read_b32 v92, a36\n" \
"v_accvgpr_read_b32 v93, a37\n" \
"ds_read_b128 a[132:135], v5 offset:1024\n" \
"v_accvgpr_read_b32 v94, a38\n" \
"v_accvgpr_read_b32 v95, a39\n" \
"v_accvgpr_read_b32 v136, a8\n" \
"buffer_load_dwordx4 v6, s[40:43], s17 offen offset:2048 sc1 lds\n" \
"v_pk_mul_f32 v[88:89], v[88:89], v[24:25] op_sel_hi:[1,0]\n" \
"v_pk_mul_f32 v[90:91], v[90:91], v[24:25] op_sel_hi:[1,0]\n" \
"ds_read_b128 a[136:139], v5 offset:2048\n" \
"v_pk_mul_f32 v[92:93], v[92:93], v[24:25] op_sel_hi:[1,0]\n" \
"v_pk_mul_f32 v[94:95], v[94:95], v[24:25] op_sel_hi:[1,0]\n" \
"v_cvt_pk_bf16_f32 v88, v88, v89\n" \
"s_xor_b32 s48, s48, 0x10000\n" \
"buffer_load_dwordx4 v6, s[40:43], s17 offen offset:3072 sc1 lds\n" \
"s_xor_b32 s49, s49, 0x10000\n" \
"s_add_u32 s44, s44, 1\n" \
"ds_read_b128 a[140:143], v5 offset:3072\n" \
"v_cvt_pk_bf16_f32 v89, v90, v91\n" \
"v_cvt_pk_bf16_f32 v90, v92, v93\n" \
"buffer_store_dwordx4 v[80:83], v4, s[36:39], s63 offen offset:0 sc1\n" \
"v_cvt_pk_bf16_f32 v91, v94, v95\n" \
"v_accvgpr_read_b32 v137, a9\n" \
"ds_read_b128 a[144:147], v5 offset:4096\n" \
"v_accvgpr_read_b32 v138, a10\n" \
"v_accvgpr_read_b32 v139, a11\n" \
"v_accvgpr_read_b32 v140, a12\n" \
"ds_read_b128 a[148:151], v5 offset:5120\n" \
"v_accvgpr_read_b32 v141, a13\n" \
"v_accvgpr_read_b32 v142, a14\n" \
"v_accvgpr_read_b32 v143, a15\n" \
"ds_read_b128 a[152:155], v5 offset:6144\n" \
"v_pk_mul_f32 v[136:137], v[136:137], v[188:189] op_sel_hi:[1,0]\n" \
"v_pk_mul_f32 v[138:139], v[138:139], v[188:189] op_sel_hi:[1,0]\n" \
"v_pk_mul_f32 v[140:141], v[140:141], v[188:189] op_sel_hi:[1,0]\n" \
"s_cmp_eq_u32 s44, 16\n" \
"s_cbranch_scc1 LAD32_%=\n" \
"s_cmp_eq_u32 s44, 15\ns_cselect_b32 s16, s56, s16\ns_cselect_b32 s9, s57, s9\ns_cselect_b32 s17, s58, s17\ns_cselect_b32 s19, s59, s19\n" \
"buffer_store_dwordx4 v[88:91], v4, s[36:39], s63 offen offset:1024 sc1\n" \
"v_mul_f32 v142, v188, v142\nv_mul_f32 v143, v188, v143\n" \
"s_waitcnt lgkmcnt(5)\n" \
"ds_read_b128 a[156:159], v5 offset:7168\n" \
"s_add_u32 s16, s16, s9\n" \
"s_add_u32 m0, s49, 32768\n" \
"v_mfma_f32_16x16x32_bf16 v[96:99], a[128:131], v[32:35], 0\n" \
"buffer_load_dwordx4 v6, s[40:43], s16 offen offset:0 sc1 lds\n" \
"v_mfma_f32_16x16x32_bf16 v[96:99], a[132:135], v[36:39], v[96:99]\n" \
"v_cvt_pk_bf16_f32 v136, v136, v137\n" \
"v_cvt_pk_bf16_f32 v137, v138, v139\n" \
"v_mfma_f32_16x16x32_bf16 v[104:107], a[128:131], v[48:51], 0\n" \
"v_cvt_pk_bf16_f32 v138, v140, v141\n" \
"v_cvt_pk_bf16_f32 v139, v142, v143\n" \
"v_mfma_f32_16x16x32_bf16 v[104:107], a[132:135], v[52:55], v[104:107]\n" \
"buffer_store_dwordx4 v[136:139], v4, s[36:39], s63 offen offset:2048 sc1\n" \
"v_mfma_f32_16x16x32_bf16 v[112:115], a[128:131], a[64:67], v[176:179]\n" \
"v_accvgpr_read_b32 v80, a40\n" \
"v_accvgpr_read_b32 v81, a41\n" \
"v_mfma_f32_16x16x32_bf16 v[112:115], a[132:135], a[68:71], v[112:115]\n" \
"buffer_load_dwordx4 v6, s[40:43], s16 offen offset:1024 sc1 lds\n" \
"v_mfma_f32_16x16x32_bf16 v[120:123], a[128:131], a[80:83], 0\n" \
"v_accvgpr_read_b32 v82, a42\n" \
"v_accvgpr_read_b32 v83, a43\n" \
"v_mfma_f32_16x16x32_bf16 v[120:123], a[132:135], a[84:87], v[120:123]\n" \
"s_waitcnt lgkmcnt(4)\n" \
"ds_read_b128 a[128:131], v5 offset:16384\n" \
"v_accvgpr_read_b32 v84, a44\n" \
"v_mfma_f32_16x16x32_bf16 v[96:99], a[136:139], v[40:43], v[96:99]\n" \
"ds_read_b128 a[132:135], v5 offset:17408\n" \
"v_accvgpr_read_b32 v85, a45\n" \
"v_mfma_f32_16x16x32_bf16 v[96:99], a[140:143], v[44:47], v[96:99]\n" \
"v_accvgpr_read_b32 v86, a46\n" \
"v_accvgpr_read_b32 v87, a47\n" \
"v_mfma_f32_16x16x32_bf16 v[104:107], a[136:139], v[56:59], v[104:107]\n" \
"buffer_load_dwordx4 v6, s[40:43], s16 offen offset:2048 sc1 lds\n" \
"v_mfma_f32_16x16x32_bf16 v[104:107], a[140:143], v[60:63], v[104:107]\n" \
"v_mul_f32 v80, v24, v80\nv_mul_f32 v81, v24, v81\n" \
"v_mfma_f32_16x16x32_bf16 v[112:115], a[136:139], a[72:75], v[112:115]\n" \
"v_mul_f32 v82, v24, v82\nv_mul_f32 v83, v24, v83\n" \
"v_mfma_f32_16x16x32_bf16 v[112:115], a[140:143], a[76:79], v[112:115]\n" \
"v_mul_f32 v84, v24, v84\nv_mul_f32 v85, v24, v85\n" \
"v_mfma_f32_16x16x32_bf16 v[120:123], a[136:139], a[88:91], v[120:123]\n" \
"v_mul_f32 v86, v24, v86\nv_mul_f32 v87, v24, v87\n" \
"v_mfma_f32_16x16x32_bf16 v[120:123], a[140:143], a[92:95], v[120:123]\n" \
"s_waitcnt lgkmcnt(4)\n" \
"ds_read_b128 a[136:139], v5 offset:18432\n" \
"v_cvt_pk_bf16_f32 v80, v80, v81\n" \
"v_mfma_f32_16x16x32_bf16 v[100:103], a[144:147], v[32:35], 0\n" \
"buffer_load_dwordx4 v6, s[40:43], s16 offen offset:3072 sc1 lds\n" \
"v_mfma_f32_16x16x32_bf16 v[100:103], a[148:151], v[36:39], v[100:103]\n" \
"ds_read_b128 a[140:143], v5 offset:19456\n" \
"v_cvt_pk_bf16_f32 v81, v82, v83\n" \
"v_mfma_f32_16x16x32_bf16 v[108:111], a[144:147], v[48:51], 0\n" \
"v_cvt_pk_bf16_f32 v82, v84, v85\n" \
"v_cvt_pk_bf16_f32 v83, v86, v87\n" \
"v_mfma_f32_16x16x32_bf16 v[108:111], a[148:151], v[52:55], v[108:111]\n" \
"buffer_store_dwordx4 v[80:83], v4, s[36:39], s63 offen offset:3072 sc1\n" \
"v_mfma_f32_16x16x32_bf16 v[124:127], a[144:147], a[80:83], v[176:179]\n" \
"v_accvgpr_read_b32 v88, a16\n" \
"v_accvgpr_read_b32 v89, a17\n" \
"v_mfma_f32_16x16x32_bf16 v[124:127], a[148:151], a[84:87], v[124:127]\n" \
"s_waitcnt lgkmcnt(4)\n" \
"ds_read_b128 a[144:147], v5 offset:20480\n" \
"v_accvgpr_read_b32 v90, a18\n" \
"v_mfma_f32_16x16x32_bf16 v[100:103], a[152:155], v[40:43], v[100:103]\n" \
"ds_read_b128 a[148:151], v5 offset:21504\n" \
"v_accvgpr_read_b32 v91, a19\n" \
"v_mfma_f32_16x16x32_bf16 v[100:103], a[156:159], v[44:47], v[100:103]\n" \
"v_accvgpr_read_b32 v92, a20\n" \
"v_accvgpr_read_b32 v93, a21\n" \
"v_mfma_f32_16x16x32_bf16 v[108:111], a[152:155], v[56:59], v[108:111]\n" \
"v_accvgpr_read_b32 v94, a22\n" \
"v_accvgpr_read_b32 v95, a23\n" \
"v_mfma_f32_16x16x32_bf16 v[108:111], a[156:159], v[60:63], v[108:111]\n" \
"v_mul_f32 v88, v188, v88\nv_mul_f32 v89, v188, v89\n" \
"v_mfma_f32_16x16x32_bf16 v[124:127], a[152:155], a[88:91], v[124:127]\n" \
"v_mul_f32 v90, v188, v90\nv_mul_f32 v91, v188, v91\n" \
"v_mfma_f32_16x16x32_bf16 v[124:127], a[156:159], a[92:95], v[124:127]\n" \
"ds_read_b128 a[152:155], v5 offset:22528\n" \
"v_accvgpr_read_b32 v136, a48\n" \
"v_mfma_f32_16x16x32_bf16 v[160:163], v[180:183], v[128:131], 0\n" \
"ds_read_b128 a[156:159], v5 offset:23552\n" \
"v_accvgpr_read_b32 v137, a49\n" \
"v_mfma_f32_16x16x32_bf16 v[164:167], v[180:183], v[132:135], 0\n" \
"s_waitcnt lgkmcnt(6)\n" \
"v_mul_f32 v92, v188, v92\nv_mul_f32 v93, v188, v93\n" \
"v_mfma_f32_16x16x32_bf16 v[192:195], a[128:131], v[128:131], 0\n" \
"v_mul_f32 v94, v188, v94\nv_mul_f32 v95, v188, v95\n" \
"v_mfma_f32_16x16x32_bf16 v[196:199], a[132:135], v[128:131], 0\n" \
"v_exp_f32 v96, v96\n" \
"v_mfma_f32_16x16x32_bf16 v[224:227], a[128:131], v[132:135], 0\n" \
"v_exp_f32 v97, v97\n" \
"v_mfma_f32_16x16x32_bf16 v[228:231], a[132:135], v[132:135], 0\n" \
"s_waitcnt lgkmcnt(4)\n" \
"v_exp_f32 v98, v98\n" \
"v_mfma_f32_16x16x32_bf16 v[200:203], a[136:139], v[128:131], 0\n" \
"v_exp_f32 v99, v99\n" \
"v_mfma_f32_16x16x32_bf16 v[204:207], a[140:143], v[128:131], 0\n" \
"v_exp_f32 v100, v100\n" \
"v_mfma_f32_16x16x32_bf16 v[232:235], a[136:139], v[132:135], 0\n" \
"v_exp_f32 v101, v101\n" \
"v_mfma_f32_16x16x32_bf16 v[236:239], a[140:143], v[132:135], 0\n" \
"s_waitcnt lgkmcnt(2)\n" \
"v_exp_f32 v102, v102\n" \
"v_mfma_f32_16x16x32_bf16 v[208:211], a[144:147], v[128:131], 0\n" \
"v_exp_f32 v103, v103\n" \
"v_mfma_f32_16x16x32_bf16 v[212:215], a[148:151], v[128:131], 0\n" \
"v_exp_f32 v104, v104\n" \
"v_mfma_f32_16x16x32_bf16 v[240:243], a[144:147], v[132:135], 0\n" \
"v_exp_f32 v105, v105\n" \
"v_mfma_f32_16x16x32_bf16 v[244:247], a[148:151], v[132:135], 0\n" \
"s_waitcnt lgkmcnt(0)\n" \
"v_exp_f32 v106, v106\n" \
"v_mfma_f32_16x16x32_bf16 v[216:219], a[152:155], v[128:131], 0\n" \
"v_exp_f32 v107, v107\n" \
"v_mfma_f32_16x16x32_bf16 v[220:223], a[156:159], v[128:131], 0\n" \
"v_exp_f32 v108, v108\n" \
"v_mfma_f32_16x16x32_bf16 v[248:251], a[152:155], v[132:135], 0\n" \
"v_exp_f32 v109, v109\n" \
"v_mfma_f32_16x16x32_bf16 v[252:255], a[156:159], v[132:135], 0\n" \
"v_exp_f32 v110, v110\n" \
"v_exp_f32 v111, v111\n" \
"v_cvt_pk_bf16_f32 v144, v96, v97\n" \
"v_cvt_pk_bf16_f32 v145, v98, v99\n" \
"v_cvt_pk_bf16_f32 v146, v100, v101\n" \
"v_cvt_pk_bf16_f32 v147, v102, v103\n" \
"v_cvt_pk_bf16_f32 v148, v104, v105\n" \
"v_cvt_pk_bf16_f32 v149, v106, v107\n" \
"v_cvt_pk_bf16_f32 v150, v108, v109\n" \
"v_cvt_pk_bf16_f32 v151, v110, v111\n" \
"v_cvt_pk_bf16_f32 v88, v88, v89\n" \
"v_cvt_pk_bf16_f32 v89, v90, v91\n" \
"v_cvt_pk_bf16_f32 v90, v92, v93\n" \
"v_cvt_pk_bf16_f32 v91, v94, v95\n" \
"v_accvgpr_read_b32 v138, a50\n" \
"v_accvgpr_read_b32 v139, a51\n" \
"buffer_store_dwordx4 v[88:91], v7, s[36:39], s63 offen offset:0 sc1\n" \
"v_accvgpr_read_b32 v140, a52\n" \
"v_accvgpr_read_b32 v141, a53\n" \
"v_accvgpr_read_b32 v142, a54\n" \
"v_accvgpr_read_b32 v143, a55\n" \
"v_accvgpr_read_b32 v80, a24\n" \
"v_accvgpr_read_b32 v81, a25\n" \
"v_pk_mul_f32 v[136:137], v[136:137], v[24:25] op_sel_hi:[1,0]\n" \
"v_pk_mul_f32 v[138:139], v[138:139], v[24:25] op_sel_hi:[1,0]\n" \
"v_pk_mul_f32 v[140:141], v[140:141], v[24:25] op_sel_hi:[1,0]\n" \
"v_pk_mul_f32 v[142:143], v[142:143], v[24:25] op_sel_hi:[1,0]\n" \
"s_add_u32 s17, s17, s19\n" \
"s_add_u32 m0, s49, 49152\n" \
"v_cvt_pk_bf16_f32 v136, v136, v137\n" \
"v_cvt_pk_bf16_f32 v137, v138, v139\n" \
"s_waitcnt vmcnt(25) lgkmcnt(8)\n" \
"v_cvt_pk_bf16_f32 v138, v140, v141\n" \
"v_cvt_pk_bf16_f32 v139, v142, v143\n" \
"v_accvgpr_read_b32 v82, a26\n" \
"s_barrier\n" \
"buffer_store_dwordx4 v[136:139], v7, s[36:39], s63 offen offset:1024 sc1\n" \
"v_accvgpr_read_b32 v83, a27\n" \
"ds_read_b128 a[128:131], v5 offset:32768\n" \
"v_accvgpr_read_b32 v84, a28\n" \
"v_accvgpr_read_b32 v85, a29\n" \
"v_accvgpr_read_b32 v86, a30\n" \
"buffer_load_dwordx4 v6, s[40:43], s17 offen offset:0 sc1 lds\n" \
"v_accvgpr_read_b32 v87, a31\n" \
"v_accvgpr_read_b32 v88, a56\n" \
"ds_read_b128 a[132:135], v5 offset:33792\n" \
"v_pk_mul_f32 v[80:81], v[80:81], v[188:189] op_sel_hi:[1,0]\n" \
"v_pk_mul_f32 v[82:83], v[82:83], v[188:189] op_sel_hi:[1,0]\n" \
"v_pk_mul_f32 v[84:85], v[84:85], v[188:189] op_sel_hi:[1,0]\n" \
"buffer_load_dwordx4 v6, s[40:43], s17 offen offset:1024 sc1 lds\n" \
"v_pk_mul_f32 v[86:87], v[86:87], v[188:189] op_sel_hi:[1,0]\n" \
"v_cvt_pk_bf16_f32 v80, v80, v81\n" \
"ds_read_b128 a[136:139], v5 offset:34816\n" \
"v_cvt_pk_bf16_f32 v81, v82, v83\n" \
"v_cvt_pk_bf16_f32 v82, v84, v85\n" \
"v_cvt_pk_bf16_f32 v83, v86, v87\n" \
"buffer_load_dwordx4 v6, s[40:43], s17 offen offset:2048 sc1 lds\n" \
"v_accvgpr_read_b32 v89, a57\n" \
"v_accvgpr_read_b32 v90, a58\n" \
"ds_read_b128 a[140:143], v5 offset:35840\n" \
"v_accvgpr_read_b32 v91, a59\n" \
"v_accvgpr_read_b32 v92, a60\n" \
"v_accvgpr_read_b32 v93, a61\n" \
"buffer_load_dwordx4 v6, s[40:43], s17 offen offset:3072 sc1 lds\n" \
"v_accvgpr_read_b32 v94, a62\n" \
"v_accvgpr_read_b32 v95, a63\n" \
"ds_read_b128 a[144:147], v5 offset:36864\n" \
"v_pk_mul_f32 v[88:89], v[88:89], v[24:25] op_sel_hi:[1,0]\n" \
"v_pk_mul_f32 v[90:91], v[90:91], v[24:25] op_sel_hi:[1,0]\n" \
"v_pk_mul_f32 v[92:93], v[92:93], v[24:25] op_sel_hi:[1,0]\n" \
"buffer_store_dwordx4 v[80:83], v7, s[36:39], s63 offen offset:2048 sc1\n" \
"v_pk_mul_f32 v[94:95], v[94:95], v[24:25] op_sel_hi:[1,0]\n" \
"v_cvt_pk_bf16_f32 v88, v88, v89\n" \
"ds_read_b128 a[148:151], v5 offset:37888\n" \
"v_cvt_pk_bf16_f32 v89, v90, v91\n" \
"v_cvt_pk_bf16_f32 v90, v92, v93\n" \
"v_cvt_pk_bf16_f32 v91, v94, v95\n" \
"v_mov_b32 v116, 0xff800000\n" \
"v_mov_b32 v117, 0xff800000\n" \
"v_mov_b32 v118, 0xff800000\n" \
"v_mov_b32 v119, 0xff800000\n" \
"buffer_store_dwordx4 v[88:91], v7, s[36:39], s63 offen offset:3072 sc1\n" \
"s_waitcnt lgkmcnt(4)\n" \
"v_mfma_f32_16x16x32_bf16 v[64:67], a[128:131], v[32:35], 0\n" \
"LH22_%=:\ns_cbranch_execz LHR1_%=\n" \
"v_exp_f32 v112, v112\n" \
"v_mfma_f32_16x16x32_bf16 v[64:67], a[132:135], v[36:39], v[64:67]\n" \
"s_add_u32 s16, s16, s9\n" \
"v_mfma_f32_16x16x32_bf16 v[72:75], a[128:131], v[48:51], 0\n" \
"ds_read_b128 a[152:155], v5 offset:38912\n" \
"v_mfma_f32_16x16x32_bf16 v[72:75], a[132:135], v[52:55], v[72:75]\n" \
"s_add_u32 m0, s48, 0\n" \
"v_exp_f32 v113, v113\n" \
"v_mfma_f32_16x16x32_bf16 v[80:83], a[128:131], a[64:67], 0\n" \
"buffer_load_dwordx4 v6, s[40:43], s16 offen offset:0 sc1 lds\n" \
"v_mfma_f32_16x16x32_bf16 v[80:83], a[132:135], a[68:71], v[80:83]\n" \
"v_exp_f32 v114, v114\n" \
"v_mfma_f32_16x16x32_bf16 v[88:91], a[128:131], a[80:83], 0\n" \
"ds_read_b128 a[156:159], v5 offset:39936\n" \
"v_cvt_pk_bf16_f32 v152, v112, v113\n" \
"v_mfma_f32_16x16x32_bf16 v[88:91], a[132:135], a[84:87], v[88:91]\n" \
"s_waitcnt lgkmcnt(4)\n" \
"v_exp_f32 v115, v115\n" \
"v_mfma_f32_16x16x32_bf16 v[64:67], a[136:139], v[40:43], v[64:67]\n" \
"v_exp_f32 v116, v116\n" \
"v_mfma_f32_16x16x32_bf16 v[64:67], a[140:143], v[44:47], v[64:67]\n" \
"s_xor_b32 s49, s49, 0x10000\n" \
"v_cvt_pk_bf16_f32 v153, v114, v115\n" \
"v_mfma_f32_16x16x32_bf16 v[72:75], a[136:139], v[56:59], v[72:75]\n" \
"ds_read_b128 a[128:131], v5 offset:49152\n" \
"v_mfma_f32_16x16x32_bf16 v[72:75], a[140:143], v[60:63], v[72:75]\n" \
"v_exp_f32 v117, v117\n" \
"v_mfma_f32_16x16x32_bf16 v[80:83], a[136:139], a[72:75], v[80:83]\n" \
"buffer_load_dwordx4 v6, s[40:43], s16 offen offset:1024 sc1 lds\n" \
"v_mfma_f32_16x16x32_bf16 v[80:83], a[140:143], a[76:79], v[80:83]\n" \
"v_exp_f32 v118, v118\n" \
"v_mfma_f32_16x16x32_bf16 v[88:91], a[136:139], a[88:91], v[88:91]\n" \
"ds_read_b128 a[132:135], v5 offset:50176\n" \
"v_cvt_pk_bf16_f32 v154, v116, v117\n" \
"v_mfma_f32_16x16x32_bf16 v[88:91], a[140:143], a[92:95], v[88:91]\n" \
"s_waitcnt lgkmcnt(4)\n" \
"v_exp_f32 v119, v119\n" \
"v_mfma_f32_16x16x32_bf16 v[68:71], a[144:147], v[32:35], 0\n" \
"v_exp_f32 v120, v120\n" \
"v_mfma_f32_16x16x32_bf16 v[68:71], a[148:151], v[36:39], v[68:71]\n" \
"v_cvt_pk_bf16_f32 v155, v118, v119\n" \
"v_mfma_f32_16x16x32_bf16 v[76:79], a[144:147], v[48:51], 0\n" \
"ds_read_b128 a[136:139], v5 offset:51200\n" \
"v_mfma_f32_16x16x32_bf16 v[76:79], a[148:151], v[52:55], v[76:79]\n" \
"v_exp_f32 v121, v121\n" \
"v_mfma_f32_16x16x32_bf16 v[84:87], a[144:147], a[64:67], 0\n" \
"buffer_load_dwordx4 v6, s[40:43], s16 offen offset:2048 sc1 lds\n" \
"v_mfma_f32_16x16x32_bf16 v[84:87], a[148:151], a[68:71], v[84:87]\n" \
"v_exp_f32 v122, v122\n" \
"v_mfma_f32_16x16x32_bf16 v[92:95], a[144:147], a[80:83], 0\n" \
"ds_read_b128 a[140:143], v5 offset:52224\n" \
"v_cvt_pk_bf16_f32 v156, v120, v121\n" \
"v_mfma_f32_16x16x32_bf16 v[92:95], a[148:151], a[84:87], v[92:95]\n" \
"s_waitcnt lgkmcnt(4)\n" \
"v_exp_f32 v123, v123\n" \
"v_mfma_f32_16x16x32_bf16 v[68:71], a[152:155], v[40:43], v[68:71]\n" \
"v_exp_f32 v124, v124\n" \
"v_mfma_f32_16x16x32_bf16 v[68:71], a[156:159], v[44:47], v[68:71]\n" \
"v_cvt_pk_bf16_f32 v157, v122, v123\n" \
"v_mfma_f32_16x16x32_bf16 v[76:79], a[152:155], v[56:59], v[76:79]\n" \
"ds_read_b128 a[144:147], v5 offset:53248\n" \
"v_mfma_f32_16x16x32_bf16 v[76:79], a[156:159], v[60:63], v[76:79]\n" \
"v_exp_f32 v125, v125\n" \
"v_mfma_f32_16x16x32_bf16 v[84:87], a[152:155], a[72:75], v[84:87]\n" \
"buffer_load_dwordx4 v6, s[40:43], s16 offen offset:3072 sc1 lds\n" \
"v_mfma_f32_16x16x32_bf16 v[84:87], a[156:159], a[76:79], v[84:87]\n" \
"v_exp_f32 v126, v126\n" \
"v_mfma_f32_16x16x32_bf16 v[92:95], a[152:155], a[88:91], v[92:95]\n" \
"ds_read_b128 a[148:151], v5 offset:54272\n" \
"v_cvt_pk_bf16_f32 v158, v124, v125\n" \
"v_mfma_f32_16x16x32_bf16 v[92:95], a[156:159], a[92:95], v[92:95]\n" \
"s_waitcnt lgkmcnt(4)\n" \
"v_exp_f32 v127, v127\n" \
"v_mfma_f32_16x16x32_bf16 v[160:163], v[180:183], v[144:147], v[160:163]\n" \
"v_cvt_pk_bf16_f32 v159, v126, v127\n" \
"v_mfma_f32_16x16x32_bf16 v[164:167], v[180:183], v[148:151], v[164:167]\n" \
"v_mfma_f32_16x16x32_bf16 v[168:171], v[180:183], v[152:155], 0\n" \
"v_exp_f32 v64, v64\n" \
"v_mfma_f32_16x16x32_bf16 v[172:175], v[180:183], v[156:159], 0\n" \
"v_mfma_f32_16x16x32_bf16 v[192:195], a[128:131], v[144:147], v[192:195]\n" \
"v_exp_f32 v65, v65\n" \
"v_mfma_f32_16x16x32_bf16 v[196:199], a[132:135], v[144:147], v[196:199]\n" \
"v_cvt_pk_bf16_f32 v128, v64, v65\n" \
"v_mfma_f32_16x16x32_bf16 v[224:227], a[128:131], v[148:151], v[224:227]\n" \
"ds_read_b128 a[152:155], v5 offset:55296\n" \
"v_mfma_f32_16x16x32_bf16 v[228:231], a[132:135], v[148:151], v[228:231]\n" \
"v_exp_f32 v66, v66\n" \
"v_mfma_f32_16x16x32_bf16 a[0:3], a[128:131], v[152:155], 0\n" \
"v_exp_f32 v67, v67\n" \
"v_mfma_f32_16x16x32_bf16 a[4:7], a[132:135], v[152:155], 0\n" \
"v_cvt_pk_bf16_f32 v129, v66, v67\n" \
"v_mfma_f32_16x16x32_bf16 a[32:35], a[128:131], v[156:159], 0\n" \
"ds_read_b128 a[156:159], v5 offset:56320\n" \
"v_mfma_f32_16x16x32_bf16 a[36:39], a[132:135], v[156:159], 0\n" \
"s_waitcnt vmcnt(20) lgkmcnt(4)\n" \
"v_exp_f32 v68, v68\n" \
"v_mfma_f32_16x16x32_bf16 v[200:203], a[136:139], v[144:147], v[200:203]\n" \
"s_barrier\n" \
"v_xor_b32 v5, 0x10000, v5\n" \
"v_mfma_f32_16x16x32_bf16 v[204:207], a[140:143], v[144:147], v[204:207]\n" \
"s_add_u32 s17, s17, s19\n" \
"v_exp_f32 v69, v69\n" \
"v_mfma_f32_16x16x32_bf16 v[232:235], a[136:139], v[148:151], v[232:235]\n" \
"ds_read_b128 a[128:131], v5 offset:0\n" \
"v_cvt_pk_bf16_f32 v130, v68, v69\n" \
"v_mfma_f32_16x16x32_bf16 v[236:239], a[140:143], v[148:151], v[236:239]\n" \
"s_add_u32 m0, s48, 16384\n" \
"v_exp_f32 v70, v70\n" \
"v_mfma_f32_16x16x32_bf16 a[8:11], a[136:139], v[152:155], 0\n" \
"buffer_load_dwordx4 v6, s[40:43], s17 offen offset:0 sc1 lds\n" \
"v_mfma_f32_16x16x32_bf16 a[12:15], a[140:143], v[152:155], 0\n" \
"s_waitcnt vmcnt(5)\n" \
"s_cmp_lg_u64 s[54:55], 0\ns_cbranch_scc1 LFBA_%=\nLFBAR_%=:\n" \
"v_mfma_f32_16x16x32_bf16 a[40:43], a[136:139], v[156:159], 0\n" \
"ds_read_b128 a[132:135], v5 offset:1024\n" \
"v_mfma_f32_16x16x32_bf16 a[44:47], a[140:143], v[156:159], 0\n" \
"s_waitcnt lgkmcnt(4)\n" \
"v_exp_f32 v71, v71\n" \
"v_mfma_f32_16x16x32_bf16 v[208:211], a[144:147], v[144:147], v[208:211]\n" \
"v_exp_f32 v72, v72\n" \
"v_mfma_f32_16x16x32_bf16 v[212:215], a[148:151], v[144:147], v[212:215]\n" \
"v_exp_f32 v73, v73\n" \
"v_mfma_f32_16x16x32_bf16 v[240:243], a[144:147], v[148:151], v[240:243]\n" \
"ds_read_b128 a[136:139], v5 offset:2048\n" \
"v_cvt_pk_bf16_f32 v131, v70, v71\n" \
"v_mfma_f32_16x16x32_bf16 v[244:247], a[148:151], v[148:151], v[244:247]\n" \
"v_exp_f32 v74, v74\n" \
"v_mfma_f32_16x16x32_bf16 a[16:19], a[144:147], v[152:155], 0\n" \
"buffer_load_dwordx4 v6, s[40:43], s17 offen offset:1024 sc1 lds\n" \
"v_mfma_f32_16x16x32_bf16 a[20:23], a[148:151], v[152:155], 0\n" \
"v_exp_f32 v75, v75\n" \
"v_mfma_f32_16x16x32_bf16 a[48:51], a[144:147], v[156:159], 0\n" \
"ds_read_b128 a[140:143], v5 offset:3072\n" \
"v_cvt_pk_bf16_f32 v132, v72, v73\n" \
"v_mfma_f32_16x16x32_bf16 a[52:55], a[148:151], v[156:159], 0\n" \
"s_waitcnt lgkmcnt(4)\n" \
"v_exp_f32 v76, v76\n" \
"v_mfma_f32_16x16x32_bf16 v[216:219], a[152:155], v[144:147], v[216:219]\n" \
"buffer_load_dwordx4 v6, s[40:43], s17 offen offset:2048 sc1 lds\n" \
"v_mfma_f32_16x16x32_bf16 v[220:223], a[156:159], v[144:147], v[220:223]\n" \
"s_add_u32 s44, s44, 1\n" \
"v_exp_f32 v77, v77\n" \
"v_mfma_f32_16x16x32_bf16 v[248:251], a[152:155], v[148:151], v[248:251]\n" \
"ds_read_b128 a[144:147], v5 offset:4096\n" \
"v_cvt_pk_bf16_f32 v133, v74, v75\n" \
"v_mfma_f32_16x16x32_bf16 v[252:255], a[156:159], v[148:151], v[252:255]\n" \
"v_exp_f32 v78, v78\n" \
"v_mfma_f32_16x16x32_bf16 a[24:27], a[152:155], v[152:155], 0\n" \
"buffer_load_dwordx4 v6, s[40:43], s17 offen offset:3072 sc1 lds\n" \
"v_mfma_f32_16x16x32_bf16 a[28:31], a[156:159], v[152:155], 0\n" \
"s_xor_b32 s48, s48, 0x10000\n" \
"v_exp_f32 v79, v79\n" \
"v_mfma_f32_16x16x32_bf16 a[56:59], a[152:155], v[156:159], 0\n" \
"ds_read_b128 a[148:151], v5 offset:5120\n" \
"v_cvt_pk_bf16_f32 v134, v76, v77\n" \
"v_mfma_f32_16x16x32_bf16 a[60:63], a[156:159], v[156:159], 0\n" \
"s_waitcnt lgkmcnt(4)\n" \
"v_cvt_pk_bf16_f32 v135, v78, v79\n" \
"buffer_store_dwordx2 v[10:11], v9, s[36:39], s52 offen sc1\n" \
"s_mov_b32 s46, s24\n" \
"s_branch LBOT_%=\n" \
"LAD32_%=:\n" \
"v_mov_b32 v96, 0\n" \
"v_mov_b32 v97, 0\n" \
"v_mov_b32 v98, 0\n" \
"v_mov_b32 v99, 0\n" \
"buffer_store_dwordx4 v[88:91], v4, s[36:39], s63 offen offset:1024 sc1\n" \
"v_pk_mul_f32 v[142:143], v[142:143], v[188:189] op_sel_hi:[1,0]\n" \
"v_cvt_pk_bf16_f32 v136, v136, v137\n" \
"v_cvt_pk_bf16_f32 v137, v138, v139\n" \
"v_cvt_pk_bf16_f32 v138, v140, v141\n" \
"v_cvt_pk_bf16_f32 v139, v142, v143\n" \
"v_accvgpr_read_b32 v80, a40\n" \
"buffer_store_dwordx4 v[136:139], v4, s[36:39], s63 offen offset:2048 sc1\n" \
"v_accvgpr_read_b32 v81, a41\n" \
"v_accvgpr_read_b32 v82, a42\n" \
"v_accvgpr_read_b32 v83, a43\n" \
"v_accvgpr_read_b32 v84, a44\n" \
"v_accvgpr_read_b32 v85, a45\n" \
"v_accvgpr_read_b32 v86, a46\n" \
"v_accvgpr_read_b32 v87, a47\n" \
"v_accvgpr_read_b32 v88, a16\n" \
"v_accvgpr_read_b32 v89, a17\n" \
"v_accvgpr_read_b32 v90, a18\n" \
"v_pk_mul_f32 v[80:81], v[80:81], v[24:25] op_sel_hi:[1,0]\n" \
"v_pk_mul_f32 v[82:83], v[82:83], v[24:25] op_sel_hi:[1,0]\n" \
"v_pk_mul_f32 v[84:85], v[84:85], v[24:25] op_sel_hi:[1,0]\n" \
"v_pk_mul_f32 v[86:87], v[86:87], v[24:25] op_sel_hi:[1,0]\n" \
"v_cvt_pk_bf16_f32 v80, v80, v81\n" \
"v_cvt_pk_bf16_f32 v81, v82, v83\n" \
"v_cvt_pk_bf16_f32 v82, v84, v85\n" \
"v_cvt_pk_bf16_f32 v83, v86, v87\n" \
"buffer_store_dwordx4 v[80:83], v4, s[36:39], s63 offen offset:3072 sc1\n" \
"v_accvgpr_read_b32 v91, a19\n" \
"v_accvgpr_read_b32 v92, a20\n" \
"v_accvgpr_read_b32 v93, a21\n" \
"v_accvgpr_read_b32 v94, a22\n" \
"v_accvgpr_read_b32 v95, a23\n" \
"v_accvgpr_read_b32 v136, a48\n" \
"v_pk_mul_f32 v[88:89], v[88:89], v[188:189] op_sel_hi:[1,0]\n" \
"v_pk_mul_f32 v[90:91], v[90:91], v[188:189] op_sel_hi:[1,0]\n" \
"v_pk_mul_f32 v[92:93], v[92:93], v[188:189] op_sel_hi:[1,0]\n" \
"v_pk_mul_f32 v[94:95], v[94:95], v[188:189] op_sel_hi:[1,0]\n" \
"v_cvt_pk_bf16_f32 v88, v88, v89\n" \
"v_cvt_pk_bf16_f32 v89, v90, v91\n" \
"v_cvt_pk_bf16_f32 v90, v92, v93\n" \
"v_cvt_pk_bf16_f32 v91, v94, v95\n" \
"buffer_store_dwordx4 v[88:91], v7, s[36:39], s63 offen offset:0 sc1\n" \
"v_accvgpr_read_b32 v137, a49\n" \
"v_accvgpr_read_b32 v138, a50\n" \
"v_accvgpr_read_b32 v139, a51\n" \
"v_accvgpr_read_b32 v140, a52\n" \
"v_accvgpr_read_b32 v141, a53\n" \
"v_accvgpr_read_b32 v142, a54\n" \
"v_accvgpr_read_b32 v143, a55\n" \
"v_accvgpr_read_b32 v80, a24\n" \
"v_accvgpr_read_b32 v81, a25\n" \
"v_accvgpr_read_b32 v82, a26\n" \
"v_pk_mul_f32 v[136:137], v[136:137], v[24:25] op_sel_hi:[1,0]\n" \
"v_pk_mul_f32 v[138:139], v[138:139], v[24:25] op_sel_hi:[1,0]\n" \
"v_pk_mul_f32 v[140:141], v[140:141], v[24:25] op_sel_hi:[1,0]\n" \
"v_pk_mul_f32 v[142:143], v[142:143], v[24:25] op_sel_hi:[1,0]\n" \
"v_cvt_pk_bf16_f32 v136, v136, v137\n" \
"v_cvt_pk_bf16_f32 v137, v138, v139\n" \
"v_cvt_pk_bf16_f32 v138, v140, v141\n" \
"v_cvt_pk_bf16_f32 v139, v142, v143\n" \
"buffer_store_dwordx4 v[136:139], v7, s[36:39], s63 offen offset:1024 sc1\n" \
"v_accvgpr_read_b32 v83, a27\n" \
"v_accvgpr_read_b32 v84, a28\n" \
"v_accvgpr_read_b32 v85, a29\n" \
"v_accvgpr_read_b32 v86, a30\n" \
"v_accvgpr_read_b32 v87, a31\n" \
"v_accvgpr_read_b32 v88, a56\n" \
"v_pk_mul_f32 v[80:81], v[80:81], v[188:189] op_sel_hi:[1,0]\n" \
"v_pk_mul_f32 v[82:83], v[82:83], v[188:189] op_sel_hi:[1,0]\n" \
"v_pk_mul_f32 v[84:85], v[84:85], v[188:189] op_sel_hi:[1,0]\n" \
"v_pk_mul_f32 v[86:87], v[86:87], v[188:189] op_sel_hi:[1,0]\n" \
"v_cvt_pk_bf16_f32 v80, v80, v81\n" \
"v_cvt_pk_bf16_f32 v81, v82, v83\n" \
"v_cvt_pk_bf16_f32 v82, v84, v85\n" \
"v_cvt_pk_bf16_f32 v83, v86, v87\n" \
"buffer_store_dwordx4 v[80:83], v7, s[36:39], s63 offen offset:2048 sc1\n" \
"v_accvgpr_read_b32 v89, a57\n" \
"v_accvgpr_read_b32 v90, a58\n" \
"v_accvgpr_read_b32 v91, a59\n" \
"v_accvgpr_read_b32 v92, a60\n" \
"v_accvgpr_read_b32 v93, a61\n" \
"v_accvgpr_read_b32 v94, a62\n" \
"v_accvgpr_read_b32 v95, a63\n" \
"v_pk_mul_f32 v[88:89], v[88:89], v[24:25] op_sel_hi:[1,0]\n" \
"v_pk_mul_f32 v[90:91], v[90:91], v[24:25] op_sel_hi:[1,0]\n" \
"v_pk_mul_f32 v[92:93], v[92:93], v[24:25] op_sel_hi:[1,0]\n" \
"v_pk_mul_f32 v[94:95], v[94:95], v[24:25] op_sel_hi:[1,0]\n" \
"v_cvt_pk_bf16_f32 v88, v88, v89\n" \
"v_cvt_pk_bf16_f32 v89, v90, v91\n" \
"v_cvt_pk_bf16_f32 v90, v92, v93\n" \
"v_cvt_pk_bf16_f32 v91, v94, v95\n" \
"buffer_store_dwordx4 v[88:91], v7, s[36:39], s63 offen offset:3072 sc1\n" \
"v_mfma_f32_32x32x16_bf16 v[192:207], v[96:99], v[96:99], 0\n" \
"v_mfma_f32_32x32x16_bf16 v[208:223], v[96:99], v[96:99], 0\n" \
"v_mfma_f32_32x32x16_bf16 v[224:239], v[96:99], v[96:99], 0\n" \
"v_mfma_f32_32x32x16_bf16 v[240:255], v[96:99], v[96:99], 0\n" \
"v_mfma_f32_32x32x16_bf16 a[0:15], v[96:99], v[96:99], 0\n" \
"v_mfma_f32_32x32x16_bf16 a[16:31], v[96:99], v[96:99], 0\n" \
"v_mfma_f32_32x32x16_bf16 a[32:47], v[96:99], v[96:99], 0\n" \
"v_mfma_f32_32x32x16_bf16 a[48:63], v[96:99], v[96:99], 0\n" \
"v_mfma_f32_32x32x16_bf16 v[160:175], v[96:99], v[96:99], 0\n" \
"v_mov_b32 v80, 0xff800000\n" \
"v_mov_b32 v81, 0xff800000\n" \
"v_mov_b32 v82, 0xff800000\n" \
"v_mov_b32 v83, 0xff800000\n" \
"v_mov_b32 v84, 0xff800000\n" \
"v_mov_b32 v85, 0xff800000\n" \
"v_mov_b32 v86, 0xff800000\n" \
"v_mov_b32 v87, 0xff800000\n" \
"v_mov_b32 v88, 0xff800000\n" \
"v_mov_b32 v89, 0xff800000\n" \
"v_mov_b32 v90, 0xff800000\n" \
"v_mov_b32 v91, 0xff800000\n" \
"v_mov_b32 v92, 0xff800000\n" \
"v_mov_b32 v93, 0xff800000\n" \
"v_mov_b32 v94, 0xff800000\n" \
"v_mov_b32 v95, 0xff800000\n" \
"s_branch LFIN_%=\n" \
".p2align 6\n" \
"LH23_%=:\ns_cbranch_execz LH27_%=\n" \
"s_nop 0\n" \
"LDISP_%=:\n" \
"s_cmp_eq_u32 s44, 16\n" \
"s_cbranch_scc1 LFIN_%=\n" \
"s_cmp_eq_u32 s44, 15\ns_cselect_b32 s16, s56, s16\ns_cselect_b32 s9, s57, s9\ns_cselect_b32 s17, s58, s17\ns_cselect_b32 s19, s59, s19\n" \
"s_cbranch_scc0 LD14_%=\n" \
"buffer_load_dword v1, v191, s[36:39], 0 offen sc1\n" \
"s_branch LEVEN_%=\n" \
"LD14_%=:\n" \
"buffer_load_dwordx2 v[18:19], v17, s[36:39], 0 offen sc1\n" \
"s_branch LEVEN_%=\n" \
".p2align 6\n" \
"LH24_%=:\ns_cbranch_execz LH28_%=\n" \
"s_nop 0\n" \
"LFIN_%=:\n" \
"s_waitcnt lgkmcnt(6)\n" \
"ds_read_b128 a[128:131], v5 offset:16384\n" \
"ds_read_b128 a[132:135], v5 offset:17408\n" \
"s_mov_b32 s81, 786432\n" \
"ds_read_b128 a[136:139], v5 offset:18432\n" \
"ds_read_b128 a[140:143], v5 offset:19456\n" \
"s_waitcnt vmcnt(8)\n" \
"s_barrier\n" \
"ds_read_b128 a[144:147], v5 offset:20480\n" \
"ds_read_b128 a[148:151], v5 offset:21504\n" \
"v_exp_f32 v80, v80\n" \
"ds_read_b128 a[152:155], v5 offset:22528\n" \
"ds_read_b128 a[156:159], v5 offset:23552\n" \
"v_exp_f32 v81, v81\n" \
"ds_read_b128 v[96:99], v29 offset:32768\n" \
"v_exp_f32 v82, v82\n" \
"v_cvt_pk_bf16_f32 v136, v80, v81\n" \
"v_mfma_f32_16x16x32_bf16 v[160:163], v[180:183], v[128:131], v[160:163]\n" \
"s_add_u32 m0, s79, 32768\n" \
"ds_read_b128 v[100:103], v29 offset:34816\n" \
"v_exp_f32 v83, v83\n" \
"v_mfma_f32_16x16x32_bf16 v[164:167], v[180:183], v[132:135], v[164:167]\n" \
"ds_read_b128 v[104:107], v29 offset:33792\n" \
"s_waitcnt lgkmcnt(9)\n" \
"v_cvt_pk_bf16_f32 v137, v82, v83\n" \
"v_mfma_f32_16x16x32_bf16 v[192:195], a[128:131], v[128:131], v[192:195]\n" \
"ds_read_b128 v[108:111], v29 offset:35840\n" \
"v_exp_f32 v84, v84\n" \
"v_mfma_f32_16x16x32_bf16 v[196:199], a[132:135], v[128:131], v[196:199]\n" \
"ds_read_b128 v[112:115], v29 offset:36864\n" \
"v_exp_f32 v85, v85\n" \
"v_mfma_f32_16x16x32_bf16 v[224:227], a[128:131], v[132:135], v[224:227]\n" \
"ds_read_b128 v[116:119], v29 offset:38912\n" \
"v_exp_f32 v86, v86\n" \
"v_mfma_f32_16x16x32_bf16 v[228:231], a[132:135], v[132:135], v[228:231]\n" \
"ds_read_b128 v[120:123], v29 offset:37888\n" \
"s_waitcnt lgkmcnt(7)\n" \
"v_cvt_pk_bf16_f32 v138, v84, v85\n" \
"v_mfma_f32_16x16x32_bf16 v[200:203], a[136:139], v[128:131], v[200:203]\n" \
"ds_read_b128 v[124:127], v29 offset:39936\n" \
"v_exp_f32 v87, v87\n" \
"v_mfma_f32_16x16x32_bf16 v[204:207], a[140:143], v[128:131], v[204:207]\n" \
"ds_write2_b32 v184, v160, v164 offset1:64\n" \
"v_exp_f32 v88, v88\n" \
"v_mfma_f32_16x16x32_bf16 v[232:235], a[136:139], v[132:135], v[232:235]\n" \
"buffer_load_dwordx4 v12, s[28:31], s81 offen offset:0 lds\n" \
"v_mfma_f32_16x16x32_bf16 v[236:239], a[140:143], v[132:135], v[236:239]\n" \
"s_add_u32 m0, s79, 33664\n" \
"v_exp_f32 v89, v89\n" \
"v_cvt_pk_bf16_f32 v139, v86, v87\n" \
"v_mfma_f32_16x16x32_bf16 v[208:211], a[144:147], v[128:131], v[208:211]\n" \
"v_exp_f32 v90, v90\n" \
"v_cvt_pk_bf16_f32 v140, v88, v89\n" \
"v_mfma_f32_16x16x32_bf16 v[212:215], a[148:151], v[128:131], v[212:215]\n" \
"v_exp_f32 v91, v91\n" \
"v_cvt_pk_bf16_f32 v192, v192, v193\n" \
"v_mfma_f32_16x16x32_bf16 v[240:243], a[144:147], v[132:135], v[240:243]\n" \
"v_exp_f32 v92, v92\n" \
"v_cvt_pk_bf16_f32 v141, v90, v91\n" \
"v_mfma_f32_16x16x32_bf16 v[244:247], a[148:151], v[132:135], v[244:247]\n" \
"v_exp_f32 v93, v93\n" \
"v_cvt_pk_bf16_f32 v193, v194, v195\n" \
"v_mfma_f32_16x16x32_bf16 v[216:219], a[152:155], v[128:131], v[216:219]\n" \
"v_exp_f32 v94, v94\n" \
"v_cvt_pk_bf16_f32 v142, v92, v93\n" \
"v_mfma_f32_16x16x32_bf16 v[220:223], a[156:159], v[128:131], v[220:223]\n" \
"v_exp_f32 v95, v95\n" \
"v_cvt_pk_bf16_f32 v194, v196, v197\n" \
"v_mfma_f32_16x16x32_bf16 v[248:251], a[152:155], v[132:135], v[248:251]\n" \
"v_cvt_pk_bf16_f32 v143, v94, v95\n" \
"v_cvt_pk_bf16_f32 v195, v198, v199\n" \
"v_cvt_pk_bf16_f32 v224, v224, v225\n" \
"v_mfma_f32_16x16x32_bf16 v[252:255], a[156:159], v[132:135], v[252:255]\n" \
"buffer_load_dwordx4 v12, s[28:31], s81 offen offset:128 lds\n" \
"v_mfma_f32_16x16x32_bf16 v[168:171], v[180:183], v[136:139], v[168:171]\n" \
"s_mov_b32 s81, 851968\n" \
"ds_write_b128 v186, v[192:195] offset:0\n" \
"v_cvt_pk_bf16_f32 v225, v226, v227\n" \
"v_cvt_pk_bf16_f32 v226, v228, v229\n" \
"v_mfma_f32_16x16x32_bf16 v[172:175], v[180:183], v[140:143], v[172:175]\n" \
"s_add_u32 m0, s79, 40960\n" \
"v_cvt_pk_bf16_f32 v227, v230, v231\n" \
"v_cvt_pk_bf16_f32 v200, v200, v201\n" \
"v_cvt_pk_bf16_f32 v201, v202, v203\n" \
"v_mfma_f32_16x16x32_bf16 a[0:3], a[128:131], v[136:139], a[0:3]\n" \
"ds_write_b128 v186, v[224:227] offset:4096\n" \
"v_cvt_pk_bf16_f32 v202, v204, v205\n" \
"v_cvt_pk_bf16_f32 v203, v206, v207\n" \
"v_mfma_f32_16x16x32_bf16 a[4:7], a[132:135], v[136:139], a[4:7]\n" \
"buffer_load_dwordx4 v12, s[28:31], s81 offen offset:0 lds\n" \
"v_mfma_f32_16x16x32_bf16 a[32:35], a[128:131], v[140:143], a[32:35]\n" \
"s_add_u32 m0, s79, 41856\n" \
"ds_write_b128 v186, v[200:203] offset:1024\n" \
"v_cvt_pk_bf16_f32 v232, v232, v233\n" \
"v_cvt_pk_bf16_f32 v233, v234, v235\n" \
"v_mfma_f32_16x16x32_bf16 a[36:39], a[132:135], v[140:143], a[36:39]\n" \
"v_cvt_pk_bf16_f32 v234, v236, v237\n" \
"v_cvt_pk_bf16_f32 v235, v238, v239\n" \
"v_cvt_pk_bf16_f32 v208, v208, v209\n" \
"v_mfma_f32_16x16x32_bf16 a[8:11], a[136:139], v[136:139], a[8:11]\n" \
"ds_write_b128 v186, v[232:235] offset:5120\n" \
"v_cvt_pk_bf16_f32 v209, v210, v211\n" \
"v_cvt_pk_bf16_f32 v210, v212, v213\n" \
"v_mfma_f32_16x16x32_bf16 a[12:15], a[140:143], v[136:139], a[12:15]\n" \
"buffer_load_dwordx4 v12, s[28:31], s81 offen offset:128 lds\n" \
"v_mfma_f32_16x16x32_bf16 a[40:43], a[136:139], v[140:143], a[40:43]\n" \
"s_add_u32 s80, s65, 16384\n" \
"v_cvt_pk_bf16_f32 v211, v214, v215\n" \
"v_cvt_pk_bf16_f32 v240, v240, v241\n" \
"v_cvt_pk_bf16_f32 v241, v242, v243\n" \
"v_mfma_f32_16x16x32_bf16 a[44:47], a[140:143], v[140:143], a[44:47]\n" \
"s_add_u32 m0, s22, 49152\n" \
"ds_write_b128 v186, v[208:211] offset:2048\n" \
"v_cvt_pk_bf16_f32 v242, v244, v245\n" \
"v_cvt_pk_bf16_f32 v243, v246, v247\n" \
"v_mfma_f32_16x16x32_bf16 a[16:19], a[144:147], v[136:139], a[16:19]\n" \
"ds_write_b128 v186, v[240:243] offset:6144\n" \
"v_cvt_pk_bf16_f32 v216, v216, v217\n" \
"v_cvt_pk_bf16_f32 v217, v218, v219\n" \
"v_mfma_f32_16x16x32_bf16 a[20:23], a[148:151], v[136:139], a[20:23]\n" \
"buffer_load_dwordx4 v4, s[36:39], s80 offen offset:0 sc1 lds\n" \
"v_mfma_f32_16x16x32_bf16 a[48:51], a[144:147], v[140:143], a[48:51]\n" \
"v_cvt_pk_bf16_f32 v218, v220, v221\n" \
"v_cvt_pk_bf16_f32 v219, v222, v223\n" \
"v_cvt_pk_bf16_f32 v248, v248, v249\n" \
"v_mfma_f32_16x16x32_bf16 a[52:55], a[148:151], v[140:143], a[52:55]\n" \
"ds_write_b128 v186, v[216:219] offset:3072\n" \
"v_cvt_pk_bf16_f32 v249, v250, v251\n" \
"v_cvt_pk_bf16_f32 v250, v252, v253\n" \
"v_mfma_f32_16x16x32_bf16 a[24:27], a[152:155], v[136:139], a[24:27]\n" \
"v_cvt_pk_bf16_f32 v251, v254, v255\n" \
"v_and_b32 v188, 16, v4\n" \
"v_mov_b32 v189, 0x3f80\n" \
"v_mfma_f32_16x16x32_bf16 a[28:31], a[156:159], v[136:139], a[28:31]\n" \
"buffer_load_dwordx4 v4, s[36:39], s80 offen offset:1024 sc1 lds\n" \
"v_mfma_f32_16x16x32_bf16 a[56:59], a[152:155], v[140:143], a[56:59]\n" \
"s_waitcnt lgkmcnt(14)\n" \
"ds_write_b128 v186, v[248:251] offset:7168\n" \
"v_lshlrev_b32 v188, v188, v189\n" \
"v_mov_b32 v22, 0\n" \
"v_mfma_f32_16x16x32_bf16 a[60:63], a[156:159], v[140:143], a[60:63]\n" \
"s_mov_b32 s94, 0x300003\n" \
"v_mov_b32 v23, 0\n" \
"v_mov_b32 v26, 0\n" \
"v_mov_b32 v27, 0\n" \
"v_mfma_f32_16x16x32_bf16 v[32:35], a[160:163], v[96:99], 0\n" \
"s_mov_b32 s95, 0x30000300\n" \
"v_cndmask_b32 v24, 0, v188, s[94:95]\n" \
"s_mov_b32 s94, 0xc0000c\n" \
"v_mfma_f32_16x16x32_bf16 v[32:35], a[168:171], v[100:103], v[32:35]\n" \
"s_waitcnt lgkmcnt(0)\n" \
"s_mov_b32 s95, 0xc0000c00\n" \
"v_cndmask_b32 v25, 0, v188, s[94:95]\n" \
"v_mfma_f32_16x16x32_bf16 v[36:39], a[164:167], v[96:99], 0\n" \
"s_waitcnt lgkmcnt(13)\n" \
"s_barrier\n" \
"ds_read2_b32 v[190:191], v185 offset1:64\n" \
"v_mfma_f32_16x16x32_bf16 v[36:39], a[172:175], v[100:103], v[36:39]\n" \
"buffer_load_dwordx4 v4, s[36:39], s80 offen offset:2048 sc1 lds\n" \
"v_mfma_f32_16x16x32_bf16 v[40:43], a[160:163], v[104:107], 0\n" \
"ds_read_b128 v[96:99], v29 offset:40960\n" \
"v_add_u32 v176, 0xfffe0000, v13\n" \
"v_mfma_f32_16x16x32_bf16 v[40:43], a[168:171], v[108:111], v[40:43]\n" \
"ds_read_b128 v[128:131], v187 offset:0\n" \
"v_add_u32 v177, 0xfffe0000, v14\n" \
"v_mfma_f32_16x16x32_bf16 v[44:47], a[164:167], v[104:107], 0\n" \
"s_waitcnt lgkmcnt(14)\n" \
"ds_read_b128 v[100:103], v29 offset:43008\n" \
"v_add_u32 v178, 0xfffe0000, v15\n" \
"v_mfma_f32_16x16x32_bf16 v[44:47], a[172:175], v[108:111], v[44:47]\n" \
"buffer_load_dwordx4 v4, s[36:39], s80 offen offset:3072 sc1 lds\n" \
"v_mfma_f32_16x16x32_bf16 v[48:51], a[160:163], v[112:115], 0\n" \
"s_mov_b32 s81, 917504\n" \
"ds_read_b128 v[104:107], v29 offset:41984\n" \
"s_add_u32 m0, s79, 81920\n" \
"v_mfma_f32_16x16x32_bf16 v[48:51], a[168:171], v[116:119], v[48:51]\n" \
"ds_read_b128 v[132:135], v187 offset:1024\n" \
"v_add_u32 v179, 0xfffe0000, v16\n" \
"v_mfma_f32_16x16x32_bf16 v[52:55], a[164:167], v[112:115], 0\n" \
"s_waitcnt lgkmcnt(15)\n" \
"ds_read_b128 v[108:111], v29 offset:44032\n" \
"s_waitcnt vmcnt(8)\n" \
"v_mfma_f32_16x16x32_bf16 v[52:55], a[172:175], v[116:119], v[52:55]\n" \
"buffer_load_dwordx4 v12, s[28:31], s81 offen offset:0 lds\n" \
"v_mfma_f32_16x16x32_bf16 v[56:59], a[160:163], v[120:123], 0\n" \
"s_add_u32 m0, s79, 82816\n" \
"ds_read_b128 v[112:115], v29 offset:45056\n" \
"v_add_u32 v180, 0xffff0000, v13\n" \
"v_mfma_f32_16x16x32_bf16 v[56:59], a[168:171], v[124:127], v[56:59]\n" \
"ds_read_b128 v[136:139], v187 offset:2048\n" \
"v_add_u32 v181, 0xffff0000, v14\n" \
"v_mfma_f32_16x16x32_bf16 v[60:63], a[164:167], v[120:123], 0\n" \
"ds_read_b128 v[116:119], v29 offset:47104\n" \
"v_add_u32 v182, 0xffff0000, v15\n" \
"v_mfma_f32_16x16x32_bf16 v[60:63], a[172:175], v[124:127], v[60:63]\n" \
"s_waitcnt vmcnt(17) lgkmcnt(6)\n" \
"s_barrier\n" \
"ds_read_b128 v[140:143], v187 offset:3072\n" \
"v_mfma_f32_16x16x32_bf16 v[64:67], a[160:163], v[96:99], 0\n" \
"ds_read_b128 v[120:123], v29 offset:46080\n" \
"v_add_u32 v183, 0xffff0000, v16\n" \
"v_mfma_f32_16x16x32_bf16 v[64:67], a[168:171], v[100:103], v[64:67]\n" \
"buffer_load_dwordx4 v12, s[28:31], s81 offen offset:128 lds\n" \
"v_mfma_f32_16x16x32_bf16 v[68:71], a[164:167], v[96:99], 0\n" \
"s_waitcnt lgkmcnt(5)\n" \
"ds_read_b128 v[124:127], v29 offset:48128\n" \
"s_mov_b32 s81, 983040\n" \
"v_mfma_f32_16x16x32_bf16 v[68:71], a[172:175], v[100:103], v[68:71]\n" \
"s_add_u32 m0, s79, 90112\n" \
"ds_read_b128 v[144:147], v187 offset:4096\n" \
"s_waitcnt lgkmcnt(13)\n" \
"v_mfma_f32_16x16x32_bf16 v[72:75], a[160:163], v[104:107], 0\n" \
"LH25_%=:\ns_cbranch_execz LH29_%=\n" \
"ds_read_b128 v[96:99], v29 offset:49152\n" \
"v_add_f32 v190, v168, v190\n" \
"v_mfma_f32_16x16x32_bf16 v[72:75], a[168:171], v[108:111], v[72:75]\n" \
"buffer_load_dwordx4 v12, s[28:31], s81 offen offset:0 lds\n" \
"v_mfma_f32_16x16x32_bf16 v[76:79], a[164:167], v[104:107], 0\n" \
"s_add_u32 m0, s79, 91008\n" \
"ds_read_b128 v[100:103], v29 offset:51200\n" \
"v_add_f32 v191, v172, v191\n" \
"v_accvgpr_read_b32 v192, a0\n" \
"v_accvgpr_read_b32 v193, a1\n" \
"v_accvgpr_read_b32 v194, a2\n" \
"v_accvgpr_read_b32 v195, a3\n" \
"v_accvgpr_read_b32 v196, a4\n" \
"v_accvgpr_read_b32 v197, a5\n" \
"v_accvgpr_read_b32 v198, a6\n" \
"v_accvgpr_read_b32 v199, a7\n" \
"v_mfma_f32_16x16x32_bf16 v[76:79], a[172:175], v[108:111], v[76:79]\n" \
"s_waitcnt lgkmcnt(13)\n" \
"ds_read_b128 v[148:151], v187 offset:5120\n" \
"v_subrev_u32 v188, 0x1f800000, v190\n" \
"v_mfma_f32_16x16x32_bf16 v[192:195], v[24:27], v[128:131], v[192:195]\n" \
"s_waitcnt lgkmcnt(7)\n" \
"ds_read_b128 v[152:155], v187 offset:6144\n" \
"v_subrev_u32 v189, 0x1f800000, v191\n" \
"v_mfma_f32_16x16x32_bf16 v[196:199], v[22:25], v[128:131], v[196:199]\n" \
"buffer_load_dwordx4 v12, s[28:31], s81 offen offset:128 lds\n" \
"v_mfma_f32_16x16x32_bf16 v[32:35], a[176:179], v[112:115], v[32:35]\n" \
"s_add_u32 s80, s66, 16384\n" \
"ds_read_b128 v[104:107], v29 offset:50176\n" \
"s_add_u32 m0, s22, 65536\n" \
"v_mfma_f32_16x16x32_bf16 v[32:35], a[184:187], v[116:119], v[32:35]\n" \
"ds_read_b128 v[156:159], v187 offset:7168\n" \
"v_max_u32 v188, v188, v189\n" \
"v_mfma_f32_16x16x32_bf16 v[36:39], a[180:183], v[112:115], v[36:39]\n" \
"s_waitcnt lgkmcnt(7)\n" \
"ds_read_b128 v[108:111], v29 offset:52224\n" \
"v_cmp_le_u32 vcc, 0x40000000, v188\n" \
"v_mfma_f32_16x16x32_bf16 v[36:39], a[188:191], v[116:119], v[36:39]\n" \
"buffer_load_dwordx4 v4, s[36:39], s80 offen offset:0 sc1 lds\n" \
"v_mfma_f32_16x16x32_bf16 v[40:43], a[176:179], v[120:123], v[40:43]\n" \
"s_or_b64 s[4:5], s[4:5], vcc\n" \
"ds_read_b128 v[112:115], v29 offset:53248\n" \
"s_nop 0\n" \
"v_mfma_f32_16x16x32_bf16 v[40:43], a[184:187], v[124:127], v[40:43]\n" \
"v_rcp_f32 v186, v190\n" \
"v_mfma_f32_16x16x32_bf16 v[44:47], a[180:183], v[120:123], v[44:47]\n" \
"ds_read_b128 v[116:119], v29 offset:55296\n" \
"v_accvgpr_read_b32 v200, a8\n" \
"v_mfma_f32_16x16x32_bf16 v[44:47], a[188:191], v[124:127], v[44:47]\n" \
"s_waitcnt vmcnt(9) lgkmcnt(3)\n" \
"s_barrier\n" \
"v_accvgpr_read_b32 v201, a9\n" \
"v_mfma_f32_16x16x32_bf16 v[48:51], a[176:179], v[96:99], v[48:51]\n" \
"ds_read_b128 v[120:123], v29 offset:54272\n" \
"v_accvgpr_read_b32 v202, a10\n" \
"v_mfma_f32_16x16x32_bf16 v[48:51], a[184:187], v[100:103], v[48:51]\n" \
"buffer_load_dwordx4 v4, s[36:39], s80 offen offset:1024 sc1 lds\n" \
"v_mfma_f32_16x16x32_bf16 v[52:55], a[180:183], v[96:99], v[52:55]\n" \
"ds_read_b128 v[124:127], v29 offset:56320\n" \
"v_accvgpr_read_b32 v203, a11\n" \
"v_accvgpr_read_b32 v204, a12\n" \
"v_accvgpr_read_b32 v205, a13\n" \
"v_accvgpr_read_b32 v206, a14\n" \
"v_accvgpr_read_b32 v207, a15\n" \
"v_mfma_f32_16x16x32_bf16 v[52:55], a[188:191], v[100:103], v[52:55]\n" \
"s_waitcnt lgkmcnt(15)\n" \
"ds_read_b64_tr_b16 a[64:65], v176 offset:32768\n" \
"v_accvgpr_read_b32 v208, a16\n" \
"v_mfma_f32_16x16x32_bf16 v[200:203], v[24:27], v[132:135], v[200:203]\n" \
"s_waitcnt lgkmcnt(5)\n" \
"ds_read_b64_tr_b16 a[66:67], v177 offset:32768\n" \
"v_accvgpr_read_b32 v209, a17\n" \
"v_mfma_f32_16x16x32_bf16 v[204:207], v[22:25], v[132:135], v[204:207]\n" \
"buffer_load_dwordx4 v4, s[36:39], s80 offen offset:2048 sc1 lds\n" \
"v_mfma_f32_16x16x32_bf16 v[56:59], a[176:179], v[104:107], v[56:59]\n" \
"ds_read_b128 v[96:99], v29 offset:57344\n" \
"v_accvgpr_read_b32 v210, a18\n" \
"v_mfma_f32_16x16x32_bf16 v[56:59], a[184:187], v[108:111], v[56:59]\n" \
"ds_read_b64_tr_b16 a[68:69], v178 offset:32768\n" \
"v_accvgpr_read_b32 v211, a19\n" \
"v_mfma_f32_16x16x32_bf16 v[60:63], a[180:183], v[104:107], v[60:63]\n" \
"s_waitcnt lgkmcnt(6)\n" \
"ds_read_b128 v[100:103], v29 offset:59392\n" \
"v_accvgpr_read_b32 v212, a20\n" \
"v_mfma_f32_16x16x32_bf16 v[60:63], a[188:191], v[108:111], v[60:63]\n" \
"buffer_load_dwordx4 v4, s[36:39], s80 offen offset:3072 sc1 lds\n" \
"v_mfma_f32_16x16x32_bf16 v[64:67], a[176:179], v[112:115], v[64:67]\n" \
"s_mov_b32 s81, 1048576\n" \
"ds_read_b128 v[104:107], v29 offset:58368\n" \
"s_add_u32 m0, s79, 98304\n" \
"v_mfma_f32_16x16x32_bf16 v[64:67], a[184:187], v[116:119], v[64:67]\n" \
"ds_read_b64_tr_b16 a[70:71], v179 offset:32768\n" \
"v_accvgpr_read_b32 v213, a21\n" \
"v_mfma_f32_16x16x32_bf16 v[68:71], a[180:183], v[112:115], v[68:71]\n" \
"s_waitcnt lgkmcnt(7)\n" \
"ds_read_b128 v[108:111], v29 offset:60416\n" \
"v_accvgpr_read_b32 v214, a22\n" \
"v_mfma_f32_16x16x32_bf16 v[68:71], a[188:191], v[116:119], v[68:71]\n" \
"buffer_load_dwordx4 v12, s[28:31], s81 offen offset:0 lds\n" \
"v_mfma_f32_16x16x32_bf16 v[72:75], a[176:179], v[120:123], v[72:75]\n" \
"s_add_u32 m0, s79, 99200\n" \
"ds_read_b128 v[112:115], v29 offset:61440\n" \
"v_accvgpr_read_b32 v215, a23\n" \
"v_mfma_f32_16x16x32_bf16 v[72:75], a[184:187], v[124:127], v[72:75]\n" \
"ds_read_b64_tr_b16 a[72:73], v176 offset:40960\n" \
"v_accvgpr_read_b32 v216, a24\n" \
"v_mfma_f32_16x16x32_bf16 v[76:79], a[180:183], v[120:123], v[76:79]\n" \
"ds_read_b128 v[116:119], v29 offset:63488\n" \
"v_accvgpr_read_b32 v217, a25\n" \
"v_mfma_f32_16x16x32_bf16 v[76:79], a[188:191], v[124:127], v[76:79]\n" \
"s_waitcnt lgkmcnt(15)\n" \
"ds_read_b64_tr_b16 a[74:75], v177 offset:40960\n" \
"v_accvgpr_read_b32 v218, a26\n" \
"v_mfma_f32_16x16x32_bf16 v[208:211], v[24:27], v[136:139], v[208:211]\n" \
"buffer_load_dwordx4 v12, s[28:31], s81 offen offset:128 lds\n" \
"v_mfma_f32_16x16x32_bf16 v[212:215], v[22:25], v[136:139], v[212:215]\n" \
"s_waitcnt vmcnt(22) lgkmcnt(7)\n" \
"s_barrier\n" \
"s_mov_b32 s81, 1114112\n" \
"v_mfma_f32_16x16x32_bf16 v[32:35], a[192:195], v[96:99], v[32:35]\n" \
"s_add_u32 m0, s79, 106496\n" \
"ds_read_b128 v[120:123], v29 offset:62464\n" \
"v_accvgpr_read_b32 v219, a27\n" \
"v_mfma_f32_16x16x32_bf16 v[32:35], a[200:203], v[100:103], v[32:35]\n" \
"ds_read_b64_tr_b16 a[76:77], v178 offset:40960\n" \
"v_accvgpr_read_b32 v220, a28\n" \
"v_mfma_f32_16x16x32_bf16 v[36:39], a[196:199], v[96:99], v[36:39]\n" \
"s_waitcnt lgkmcnt(6)\n" \
"ds_read_b128 v[124:127], v29 offset:64512\n" \
"v_accvgpr_read_b32 v221, a29\n" \
"v_mfma_f32_16x16x32_bf16 v[36:39], a[204:207], v[100:103], v[36:39]\n" \
"buffer_load_dwordx4 v12, s[28:31], s81 offen offset:0 lds\n" \
"v_mfma_f32_16x16x32_bf16 v[40:43], a[192:195], v[104:107], v[40:43]\n" \
"s_add_u32 m0, s79, 107392\n" \
"ds_read_b128 v[96:99], v28 offset:0\n" \
"v_accvgpr_read_b32 v222, a30\n" \
"v_mfma_f32_16x16x32_bf16 v[40:43], a[200:203], v[108:111], v[40:43]\n" \
"ds_read_b64_tr_b16 a[78:79], v179 offset:40960\n" \
"v_accvgpr_read_b32 v223, a31\n" \
"v_mfma_f32_16x16x32_bf16 v[44:47], a[196:199], v[104:107], v[44:47]\n" \
"s_waitcnt lgkmcnt(6)\n" \
"ds_read_b128 v[100:103], v28 offset:2048\n" \
"v_accvgpr_read_b32 v224, a32\n" \
"v_mfma_f32_16x16x32_bf16 v[44:47], a[204:207], v[108:111], v[44:47]\n" \
"buffer_load_dwordx4 v12, s[28:31], s81 offen offset:128 lds\n" \
"v_mfma_f32_16x16x32_bf16 v[48:51], a[192:195], v[112:115], v[48:51]\n" \
"s_add_u32 s80, s64, 32768\n" \
"ds_read_b128 v[104:107], v28 offset:1024\n" \
"s_add_u32 m0, s22, 131072\n" \
"v_mfma_f32_16x16x32_bf16 v[48:51], a[200:203], v[116:119], v[48:51]\n" \
"v_rcp_f32 v187, v191\n" \
"v_mfma_f32_16x16x32_bf16 v[52:55], a[196:199], v[112:115], v[52:55]\n" \
"ds_read_b128 v[108:111], v28 offset:3072\n" \
"v_bfe_u32 v188, v4, 7, 2\n" \
"v_mfma_f32_16x16x32_bf16 v[52:55], a[204:207], v[116:119], v[52:55]\n" \
"s_waitcnt lgkmcnt(15)\n" \
"s_cmp_lg_u64 s[54:55], 0\ns_cbranch_scc1 LFBC_%=\nLFBCR_%=:\n" \
"v_mfma_f32_16x16x32_bf16 v[216:219], v[24:27], v[140:143], v[216:219]\n" \
"s_waitcnt lgkmcnt(5)\n" \
"v_min_u32 v188, 0, v188\n" \
"v_lshl_add_u32 v17, v188, 8, s10\n" \
"v_mfma_f32_16x16x32_bf16 v[220:223], v[22:25], v[140:143], v[220:223]\n" \
"buffer_store_dwordx2 v[10:11], v9, s[36:39], s52 offen sc1\n" \
"v_mfma_f32_16x16x32_bf16 v[56:59], a[192:195], v[120:123], v[56:59]\n" \
"ds_read_b128 v[112:115], v28 offset:4096\n" \
"v_bfe_u32 v188, v4, 4, 3\n" \
"v_mfma_f32_16x16x32_bf16 v[56:59], a[200:203], v[124:127], v[56:59]\n" \
"buffer_load_dwordx4 v4, s[36:39], s80 offen offset:0 sc1 lds\n" \
"v_mfma_f32_16x16x32_bf16 v[60:63], a[196:199], v[120:123], v[60:63]\n" \
"s_andn2_b32 s94, s88, 0xc0\n" \
"ds_read_b128 v[116:119], v28 offset:6144\n" \
"v_lshl_add_u32 v17, v188, 3, v17\n" \
"v_mfma_f32_16x16x32_bf16 v[60:63], a[204:207], v[124:127], v[60:63]\n" \
"s_waitcnt lgkmcnt(4)\n" \
"s_barrier\n" \
"v_and_b32 v188, 0x30, v4\n" \
"v_mfma_f32_16x16x32_bf16 v[64:67], a[192:195], v[96:99], v[64:67]\n" \
"ds_read_b128 v[120:123], v28 offset:5120\n" \
"v_mul_f32 v192, v186, v192\n" \
"v_mfma_f32_16x16x32_bf16 v[64:67], a[200:203], v[100:103], v[64:67]\n" \
"buffer_load_dwordx4 v4, s[36:39], s80 offen offset:1024 sc1 lds\n" \
"v_mfma_f32_16x16x32_bf16 v[68:71], a[196:199], v[96:99], v[68:71]\n" \
"s_waitcnt lgkmcnt(3)\n" \
"ds_read_b128 v[124:127], v28 offset:7168\n" \
"v_mul_f32 v193, v186, v193\n" \
"v_mfma_f32_16x16x32_bf16 v[68:71], a[204:207], v[100:103], v[68:71]\n" \
"s_mov_b32 exec_lo, 0\nv_lshl_add_u32 v17, v188, 2, s94\ns_mov_b32 exec_lo, -1\n" \
"v_mfma_f32_16x16x32_bf16 v[72:75], a[192:195], v[104:107], v[72:75]\n" \
"s_sub_u32 s96, s72, 109051904\n" \
"ds_read_b128 v[96:99], v28 offset:8192\n" \
"s_lshr_b32 s96, s96, 11\n" \
"v_mfma_f32_16x16x32_bf16 v[72:75], a[200:203], v[108:111], v[72:75]\n" \
"buffer_load_dwordx4 v4, s[36:39], s80 offen offset:2048 sc1 lds\n" \
"v_mfma_f32_16x16x32_bf16 v[76:79], a[196:199], v[104:107], v[76:79]\n" \
"s_mul_i32 s97, s96, 10923\n" \
"ds_read_b128 v[100:103], v28 offset:10240\n" \
"s_lshr_b32 s97, s97, 16\n" \
"v_accvgpr_read_b32 v225, a33\n" \
"v_accvgpr_read_b32 v226, a34\n" \
"v_accvgpr_read_b32 v227, a35\n" \
"v_accvgpr_read_b32 v228, a36\n" \
"v_accvgpr_read_b32 v229, a37\n" \
"v_accvgpr_read_b32 v230, a38\n" \
"v_accvgpr_read_b32 v231, a39\n" \
"v_mfma_f32_16x16x32_bf16 v[76:79], a[204:207], v[108:111], v[76:79]\n" \
"s_waitcnt lgkmcnt(15)\n" \
"s_lshl_b32 s97, s97, 1\n" \
"s_sub_u32 s96, s96, s97\n" \
"v_mfma_f32_16x16x32_bf16 v[224:227], v[24:27], v[144:147], v[224:227]\n" \
"s_waitcnt lgkmcnt(4)\n" \
"s_lshl_b32 s96, s96, 3\n" \
"s_add_u32 s23, s96, 94437376\n" \
"v_mfma_f32_16x16x32_bf16 v[228:231], v[22:25], v[144:147], v[228:231]\n" \
"buffer_load_dwordx4 v4, s[36:39], s80 offen offset:3072 sc1 lds\n" \
"v_mfma_f32_16x16x32_bf16 v[32:35], a[208:211], v[112:115], v[32:35]\n" \
"s_mov_b32 s81, 1179648\n" \
"ds_read_b128 v[104:107], v28 offset:9216\n" \
"s_add_u32 m0, s79, 147456\n" \
"v_mfma_f32_16x16x32_bf16 v[32:35], a[216:219], v[116:119], v[32:35]\n" \
"s_sub_u32 s96, s73, 109051904\n" \
"s_lshr_b32 s96, s96, 11\n" \
"s_mul_i32 s97, s96, 10923\n" \
"v_mfma_f32_16x16x32_bf16 v[36:39], a[212:215], v[112:115], v[36:39]\n" \
"s_waitcnt lgkmcnt(3)\n" \
"ds_read_b128 v[108:111], v28 offset:11264\n" \
"s_lshr_b32 s97, s97, 16\n" \
"v_mfma_f32_16x16x32_bf16 v[36:39], a[220:223], v[116:119], v[36:39]\n" \
"buffer_load_dwordx4 v12, s[28:31], s81 offen offset:0 lds\n" \
"v_mfma_f32_16x16x32_bf16 v[40:43], a[208:211], v[120:123], v[40:43]\n" \
"s_add_u32 m0, s79, 148352\n" \
"ds_read_b128 v[112:115], v28 offset:12288\n" \
"s_lshl_b32 s97, s97, 1\n" \
"v_mfma_f32_16x16x32_bf16 v[40:43], a[216:219], v[124:127], v[40:43]\n" \
"s_sub_u32 s96, s96, s97\n" \
"s_lshl_b32 s96, s96, 3\n" \
"s_add_u32 s24, s96, 94437376\n" \
"v_mfma_f32_16x16x32_bf16 v[44:47], a[212:215], v[120:123], v[44:47]\n" \
"s_sub_u32 s96, s74, 109051904\n" \
"ds_read_b128 v[116:119], v28 offset:14336\n" \
"s_lshr_b32 s96, s96, 11\n" \
"v_mfma_f32_16x16x32_bf16 v[44:47], a[220:223], v[124:127], v[44:47]\n" \
"s_waitcnt vmcnt(14) lgkmcnt(4)\n" \
"s_barrier\n" \
"s_mul_i32 s97, s96, 10923\n" \
"v_mfma_f32_16x16x32_bf16 v[48:51], a[208:211], v[96:99], v[48:51]\n" \
"s_lshr_b32 s97, s97, 16\n" \
"ds_read_b128 v[120:123], v28 offset:13312\n" \
"s_lshl_b32 s97, s97, 1\n" \
"v_mfma_f32_16x16x32_bf16 v[48:51], a[216:219], v[100:103], v[48:51]\n" \
"buffer_load_dwordx4 v12, s[28:31], s81 offen offset:128 lds\n" \
"v_mfma_f32_16x16x32_bf16 v[52:55], a[212:215], v[96:99], v[52:55]\n" \
"s_mov_b32 s81, 1245184\n" \
"ds_read_b128 v[124:127], v28 offset:15360\n" \
"s_add_u32 m0, s79, 155648\n" \
"v_accvgpr_read_b32 v232, a40\n" \
"v_accvgpr_read_b32 v233, a41\n" \
"v_accvgpr_read_b32 v234, a42\n" \
"v_accvgpr_read_b32 v235, a43\n" \
"v_accvgpr_read_b32 v236, a44\n" \
"v_accvgpr_read_b32 v237, a45\n" \
"v_accvgpr_read_b32 v238, a46\n" \
"v_accvgpr_read_b32 v239, a47\n" \
"v_mfma_f32_16x16x32_bf16 v[52:55], a[220:223], v[100:103], v[52:55]\n" \
"s_waitcnt lgkmcnt(15)\n" \
"ds_read_b64_tr_b16 a[80:81], v180 offset:16384\n" \
"s_sub_u32 s96, s96, s97\n" \
"v_mfma_f32_16x16x32_bf16 v[232:235], v[24:27], v[148:151], v[232:235]\n" \
"s_waitcnt lgkmcnt(5)\n" \
"ds_read_b64_tr_b16 a[82:83], v181 offset:16384\n" \
"s_lshl_b32 s96, s96, 3\n" \
"v_mfma_f32_16x16x32_bf16 v[236:239], v[22:25], v[148:151], v[236:239]\n" \
"buffer_load_dwordx4 v12, s[28:31], s81 offen offset:0 lds\n" \
"v_mfma_f32_16x16x32_bf16 v[56:59], a[208:211], v[104:107], v[56:59]\n" \
"s_add_u32 m0, s79, 156544\n" \
"ds_read_b128 v[96:99], v28 offset:16384\n" \
"s_add_u32 s45, s96, 94437376\n" \
"v_mfma_f32_16x16x32_bf16 v[56:59], a[216:219], v[108:111], v[56:59]\n" \
"ds_read_b64_tr_b16 a[84:85], v182 offset:16384\n" \
"v_mul_f32 v194, v186, v194\n" \
"v_mfma_f32_16x16x32_bf16 v[60:63], a[212:215], v[104:107], v[60:63]\n" \
"s_waitcnt lgkmcnt(6)\n" \
"ds_read_b128 v[100:103], v28 offset:18432\n" \
"v_mul_f32 v195, v186, v195\n" \
"v_mfma_f32_16x16x32_bf16 v[60:63], a[220:223], v[108:111], v[60:63]\n" \
"buffer_load_dwordx2 v[18:19], v17, s[36:39], 0 offen sc1\n" \
"v_mfma_f32_16x16x32_bf16 v[64:67], a[208:211], v[112:115], v[64:67]\n" \
"ds_read_b128 v[104:107], v28 offset:17408\n" \
"v_mul_f32 v196, v186, v196\n" \
"v_mfma_f32_16x16x32_bf16 v[64:67], a[216:219], v[116:119], v[64:67]\n" \
"ds_read_b64_tr_b16 a[86:87], v183 offset:16384\n" \
"v_mul_f32 v197, v186, v197\n" \
"v_mfma_f32_16x16x32_bf16 v[68:71], a[212:215], v[112:115], v[68:71]\n" \
"s_waitcnt lgkmcnt(7)\n" \
"ds_read_b128 v[108:111], v28 offset:19456\n" \
"v_mul_f32 v198, v186, v198\n" \
"v_mfma_f32_16x16x32_bf16 v[68:71], a[220:223], v[116:119], v[68:71]\n" \
"buffer_load_dwordx4 v12, s[28:31], s81 offen offset:128 lds\n" \
"v_mfma_f32_16x16x32_bf16 v[72:75], a[208:211], v[120:123], v[72:75]\n" \
"s_add_u32 s80, s65, 32768\n" \
"ds_read_b128 v[112:115], v28 offset:20480\n" \
"s_add_u32 m0, s22, 32768\n" \
"v_mfma_f32_16x16x32_bf16 v[72:75], a[216:219], v[124:127], v[72:75]\n" \
"ds_read_b64_tr_b16 a[88:89], v180 offset:24576\n" \
"v_mul_f32 v199, v186, v199\n" \
"v_mfma_f32_16x16x32_bf16 v[76:79], a[212:215], v[120:123], v[76:79]\n" \
"ds_read_b128 v[116:119], v28 offset:22528\n" \
"v_cvt_pk_bf16_f32 v192, v192, v193\n" \
"v_accvgpr_read_b32 v240, a48\n" \
"v_accvgpr_read_b32 v241, a49\n" \
"v_accvgpr_read_b32 v242, a50\n" \
"v_accvgpr_read_b32 v243, a51\n" \
"v_accvgpr_read_b32 v244, a52\n" \
"v_accvgpr_read_b32 v245, a53\n" \
"v_accvgpr_read_b32 v246, a54\n" \
"v_accvgpr_read_b32 v247, a55\n" \
"v_mfma_f32_16x16x32_bf16 v[76:79], a[220:223], v[124:127], v[76:79]\n" \
"s_waitcnt lgkmcnt(15)\n" \
"ds_read_b64_tr_b16 a[90:91], v181 offset:24576\n" \
"v_cvt_pk_bf16_f32 v193, v194, v195\n" \
"v_mfma_f32_16x16x32_bf16 v[240:243], v[24:27], v[152:155], v[240:243]\n" \
"buffer_load_dwordx4 v4, s[36:39], s80 offen offset:0 sc1 lds\n" \
"v_mfma_f32_16x16x32_bf16 v[244:247], v[22:25], v[152:155], v[244:247]\n" \
"s_waitcnt lgkmcnt(7)\n" \
"s_barrier\n" \
"ds_read_b64_tr_b16 a[92:93], v182 offset:24576\n" \
"v_mfma_f32_16x16x32_bf16 v[32:35], a[224:227], v[96:99], v[32:35]\n" \
"ds_read_b128 v[120:123], v28 offset:21504\n" \
"v_cvt_pk_bf16_f32 v194, v196, v197\n" \
"v_mfma_f32_16x16x32_bf16 v[32:35], a[232:235], v[100:103], v[32:35]\n" \
"ds_read_b64_tr_b16 a[94:95], v183 offset:24576\n" \
"v_cvt_pk_bf16_f32 v195, v198, v199\n" \
"v_mfma_f32_16x16x32_bf16 v[36:39], a[228:231], v[96:99], v[36:39]\n" \
"s_waitcnt lgkmcnt(7)\n" \
"ds_read_b128 v[124:127], v28 offset:23552\n" \
"v_mul_f32 v200, v186, v200\n" \
"v_mfma_f32_16x16x32_bf16 v[36:39], a[236:239], v[100:103], v[36:39]\n" \
"buffer_store_dwordx4 v[192:195], v4, s[36:39], s51 offen offset:0 sc1\n" \
"v_mfma_f32_16x16x32_bf16 v[40:43], a[224:227], v[104:107], v[40:43]\n" \
"ds_read_b128 v[96:99], v28 offset:24576\n" \
"v_mul_f32 v201, v186, v201\n" \
"v_mfma_f32_16x16x32_bf16 v[40:43], a[232:235], v[108:111], v[40:43]\n" \
"v_mul_f32 v202, v186, v202\n" \
"v_mul_f32 v203, v186, v203\n" \
"v_mfma_f32_16x16x32_bf16 v[44:47], a[228:231], v[104:107], v[44:47]\n" \
"s_waitcnt lgkmcnt(6)\n" \
"ds_read_b128 v[100:103], v28 offset:26624\n" \
"v_mul_f32 v204, v186, v204\n" \
"v_mfma_f32_16x16x32_bf16 v[44:47], a[236:239], v[108:111], v[44:47]\n" \
"buffer_load_dwordx4 v4, s[36:39], s80 offen offset:1024 sc1 lds\n" \
"v_mfma_f32_16x16x32_bf16 v[48:51], a[224:227], v[112:115], v[48:51]\n" \
"ds_read_b128 v[104:107], v28 offset:25600\n" \
"v_mul_f32 v205, v186, v205\n" \
"v_mfma_f32_16x16x32_bf16 v[48:51], a[232:235], v[116:119], v[48:51]\n" \
"v_mul_f32 v206, v186, v206\n" \
"v_mul_f32 v207, v186, v207\n" \
"v_mfma_f32_16x16x32_bf16 v[52:55], a[228:231], v[112:115], v[52:55]\n" \
"ds_read_b128 v[108:111], v28 offset:27648\n" \
"v_cvt_pk_bf16_f32 v200, v200, v201\n" \
"v_accvgpr_read_b32 v248, a56\n" \
"v_accvgpr_read_b32 v249, a57\n" \
"v_accvgpr_read_b32 v250, a58\n" \
"v_accvgpr_read_b32 v251, a59\n" \
"v_accvgpr_read_b32 v252, a60\n" \
"v_accvgpr_read_b32 v253, a61\n" \
"v_accvgpr_read_b32 v254, a62\n" \
"v_accvgpr_read_b32 v255, a63\n" \
"v_mfma_f32_16x16x32_bf16 v[52:55], a[236:239], v[116:119], v[52:55]\n" \
"s_waitcnt lgkmcnt(15)\n" \
"v_cvt_pk_bf16_f32 v201, v202, v203\n" \
"v_mfma_f32_16x16x32_bf16 v[248:251], v[24:27], v[156:159], v[248:251]\n" \
"s_waitcnt lgkmcnt(4)\n" \
"v_cvt_pk_bf16_f32 v202, v204, v205\n" \
"v_cvt_pk_bf16_f32 v203, v206, v207\n" \
"v_mfma_f32_16x16x32_bf16 v[252:255], v[22:25], v[156:159], v[252:255]\n" \
"buffer_store_dwordx4 v[200:203], v4, s[36:39], s51 offen offset:2048 sc1\n" \
"v_mfma_f32_16x16x32_bf16 v[56:59], a[224:227], v[120:123], v[56:59]\n" \
"ds_read_b128 v[112:115], v28 offset:28672\n" \
"v_mul_f32 v208, v186, v208\n" \
"v_mfma_f32_16x16x32_bf16 v[56:59], a[232:235], v[124:127], v[56:59]\n" \
"v_mul_f32 v209, v186, v209\n" \
"v_mul_f32 v210, v186, v210\n" \
"v_mfma_f32_16x16x32_bf16 v[60:63], a[228:231], v[120:123], v[60:63]\n" \
"ds_read_b128 v[116:119], v28 offset:30720\n" \
"s_waitcnt vmcnt(5)\n" \
"v_mfma_f32_16x16x32_bf16 v[60:63], a[236:239], v[124:127], v[60:63]\n" \
"s_waitcnt vmcnt(14) lgkmcnt(4)\n" \
"s_barrier\n" \
"v_cmp_ne_u64 vcc, s[14:15], v[18:19]\n" \
"v_mfma_f32_16x16x32_bf16 v[64:67], a[224:227], v[96:99], v[64:67]\n" \
"ds_read_b128 v[120:123], v28 offset:29696\n" \
"s_nop 0\n" \
"v_mfma_f32_16x16x32_bf16 v[64:67], a[232:235], v[100:103], v[64:67]\n" \
"buffer_load_dwordx4 v4, s[36:39], s80 offen offset:2048 sc1 lds\n" \
"v_mfma_f32_16x16x32_bf16 v[68:71], a[228:231], v[96:99], v[68:71]\n" \
"s_waitcnt lgkmcnt(3)\n" \
"ds_read_b128 v[124:127], v28 offset:31744\n" \
"s_cbranch_vccnz LPS_%=\nLPSR_%=:\n" \
"v_mfma_f32_16x16x32_bf16 v[68:71], a[236:239], v[100:103], v[68:71]\n" \
"ds_read_b64_tr_b16 a[128:129], v180 offset:32768\n" \
"v_mul_f32 v211, v186, v211\n" \
"v_mfma_f32_16x16x32_bf16 v[72:75], a[224:227], v[104:107], v[72:75]\n" \
"ds_read_b128 v[96:99], v28 offset:49152\n" \
"v_mul_f32 v212, v186, v212\n" \
"v_mfma_f32_16x16x32_bf16 v[72:75], a[232:235], v[108:111], v[72:75]\n" \
"s_mov_b64 exec, 0xffff\nbuffer_load_dword v1, v2, s[36:39], s67 offen sc1\ns_mov_b64 exec, -1\n" \
"v_mfma_f32_16x16x32_bf16 v[76:79], a[228:231], v[104:107], v[76:79]\n" \
"s_waitcnt lgkmcnt(4)\n" \
"ds_read_b128 v[100:103], v28 offset:51200\n" \
"v_mul_f32 v213, v186, v213\n" \
"v_mfma_f32_16x16x32_bf16 v[76:79], a[236:239], v[108:111], v[76:79]\n" \
"ds_read_b64_tr_b16 a[130:131], v181 offset:32768\n" \
"v_mul_f32 v214, v186, v214\n" \
"v_mfma_f32_16x16x32_bf16 v[32:35], a[240:243], v[112:115], v[32:35]\n" \
"ds_read_b128 v[104:107], v28 offset:50176\n" \
"v_mul_f32 v215, v186, v215\n" \
"v_mfma_f32_16x16x32_bf16 v[32:35], a[248:251], v[116:119], v[32:35]\n" \
"buffer_load_dwordx4 v4, s[36:39], s80 offen offset:3072 sc1 lds\n" \
"v_mfma_f32_16x16x32_bf16 v[36:39], a[244:247], v[112:115], v[36:39]\n" \
"s_waitcnt lgkmcnt(5)\n" \
"ds_read_b128 v[108:111], v28 offset:52224\n" \
"s_add_u32 s80, s67, 0\n" \
"v_mfma_f32_16x16x32_bf16 v[36:39], a[252:255], v[116:119], v[36:39]\n" \
"s_add_u32 m0, s22, 114688\n" \
"ds_read_b64_tr_b16 a[132:133], v182 offset:32768\n" \
"v_cvt_pk_bf16_f32 v208, v208, v209\n" \
"v_mfma_f32_16x16x32_bf16 v[40:43], a[240:243], v[120:123], v[40:43]\n" \
"ds_read_b128 v[112:115], v28 offset:53248\n" \
"v_cvt_pk_bf16_f32 v209, v210, v211\n" \
"v_mfma_f32_16x16x32_bf16 v[40:43], a[248:251], v[124:127], v[40:43]\n" \
"buffer_load_dwordx4 v31, s[36:39], s80 offen offset:0 sc1 lds\n" \
"v_mfma_f32_16x16x32_bf16 v[44:47], a[244:247], v[120:123], v[44:47]\n" \
"ds_read_b128 v[116:119], v28 offset:55296\n" \
"v_cvt_pk_bf16_f32 v210, v212, v213\n" \
"v_mfma_f32_16x16x32_bf16 v[44:47], a[252:255], v[124:127], v[44:47]\n" \
"s_waitcnt lgkmcnt(6)\n" \
"s_barrier\n" \
"ds_read_b64_tr_b16 a[134:135], v183 offset:32768\n" \
"v_mfma_f32_16x16x32_bf16 v[48:51], a[240:243], v[96:99], v[48:51]\n" \
"ds_read_b128 v[120:123], v28 offset:54272\n" \
"v_cvt_pk_bf16_f32 v211, v214, v215\n" \
"v_mfma_f32_16x16x32_bf16 v[48:51], a[248:251], v[100:103], v[48:51]\n" \
"buffer_store_dwordx4 v[208:211], v7, s[36:39], s51 offen offset:0 sc1\n" \
"v_mfma_f32_16x16x32_bf16 v[52:55], a[244:247], v[96:99], v[52:55]\n" \
"s_waitcnt lgkmcnt(5)\n" \
"ds_read_b128 v[124:127], v28 offset:56320\n" \
"v_mul_f32 v216, v186, v216\n" \
"v_mfma_f32_16x16x32_bf16 v[52:55], a[252:255], v[100:103], v[52:55]\n" \
"ds_read_b64_tr_b16 a[136:137], v180 offset:40960\n" \
"v_mul_f32 v217, v186, v217\n" \
"v_mfma_f32_16x16x32_bf16 v[56:59], a[240:243], v[104:107], v[56:59]\n" \
"ds_read_b128 v[96:99], v28 offset:57344\n" \
"v_mul_f32 v218, v186, v218\n" \
"v_mfma_f32_16x16x32_bf16 v[56:59], a[248:251], v[108:111], v[56:59]\n" \
"ds_read_b64_tr_b16 a[138:139], v181 offset:40960\n" \
"v_mul_f32 v219, v186, v219\n" \
"v_mfma_f32_16x16x32_bf16 v[60:63], a[244:247], v[104:107], v[60:63]\n" \
"s_waitcnt lgkmcnt(6)\n" \
"ds_read_b128 v[100:103], v28 offset:59392\n" \
"v_mul_f32 v220, v186, v220\n" \
"v_mfma_f32_16x16x32_bf16 v[60:63], a[252:255], v[108:111], v[60:63]\n" \
"buffer_load_dwordx4 v31, s[36:39], s80 offen offset:1024 sc1 lds\n" \
"v_mfma_f32_16x16x32_bf16 v[64:67], a[240:243], v[112:115], v[64:67]\n" \
"ds_read_b128 v[104:107], v28 offset:58368\n" \
"v_mul_f32 v221, v186, v221\n" \
"v_mfma_f32_16x16x32_bf16 v[64:67], a[248:251], v[116:119], v[64:67]\n" \
"ds_read_b64_tr_b16 a[140:141], v182 offset:40960\n" \
"v_mul_f32 v222, v186, v222\n" \
"v_mfma_f32_16x16x32_bf16 v[68:71], a[244:247], v[112:115], v[68:71]\n" \
"s_waitcnt lgkmcnt(6)\n" \
"ds_read_b128 v[108:111], v28 offset:60416\n" \
"ds_read_b64_tr_b16 a[142:143], v183 offset:40960\n" \
"v_mfma_f32_16x16x32_bf16 v[68:71], a[252:255], v[116:119], v[68:71]\n" \
"buffer_load_dwordx4 v31, s[36:39], s80 offen offset:2048 sc1 lds\n" \
"v_mfma_f32_16x16x32_bf16 v[72:75], a[240:243], v[120:123], v[72:75]\n" \
"ds_read_b128 v[112:115], v28 offset:61440\n" \
"v_mul_f32 v223, v186, v223\n" \
"v_mfma_f32_16x16x32_bf16 v[72:75], a[248:251], v[124:127], v[72:75]\n" \
"v_cvt_pk_bf16_f32 v216, v216, v217\n" \
"v_cvt_pk_bf16_f32 v217, v218, v219\n" \
"v_mfma_f32_16x16x32_bf16 v[76:79], a[244:247], v[120:123], v[76:79]\n" \
"ds_read_b128 v[116:119], v28 offset:63488\n" \
"v_cvt_pk_bf16_f32 v218, v220, v221\n" \
"v_mfma_f32_16x16x32_bf16 v[76:79], a[252:255], v[124:127], v[76:79]\n" \
"s_waitcnt vmcnt(25) lgkmcnt(2)\n" \
"s_barrier\n" \
"v_cvt_pk_bf16_f32 v219, v222, v223\n" \
"v_mfma_f32_16x16x32_bf16 v[32:35], a[64:67], v[96:99], v[32:35]\n" \
"ds_read_b128 v[120:123], v28 offset:62464\n" \
"v_mul_f32 v224, v187, v224\n" \
"v_mfma_f32_16x16x32_bf16 v[32:35], a[72:75], v[100:103], v[32:35]\n" \
"buffer_store_dwordx4 v[216:219], v7, s[36:39], s51 offen offset:2048 sc1\n" \
"v_mfma_f32_16x16x32_bf16 v[36:39], a[68:71], v[96:99], v[36:39]\n" \
"s_waitcnt lgkmcnt(4)\n" \
"ds_read_b128 v[124:127], v28 offset:64512\n" \
"v_mul_f32 v225, v187, v225\n" \
"v_mfma_f32_16x16x32_bf16 v[36:39], a[76:79], v[100:103], v[36:39]\n" \
"v_mul_f32 v226, v187, v226\n" \
"v_mul_f32 v227, v187, v227\n" \
"v_mfma_f32_16x16x32_bf16 v[40:43], a[64:67], v[104:107], v[40:43]\n" \
"ds_read_b128 v[96:99], v29 offset:0\n" \
"v_mul_f32 v228, v187, v228\n" \
"v_mfma_f32_16x16x32_bf16 v[40:43], a[72:75], v[108:111], v[40:43]\n" \
"v_mul_f32 v229, v187, v229\n" \
"v_mul_f32 v230, v187, v230\n" \
"v_mfma_f32_16x16x32_bf16 v[44:47], a[68:71], v[104:107], v[44:47]\n" \
"s_waitcnt lgkmcnt(3)\n" \
"ds_read_b128 v[100:103], v29 offset:2048\n" \
"v_mul_f32 v231, v187, v231\n" \
"v_mfma_f32_16x16x32_bf16 v[44:47], a[76:79], v[108:111], v[44:47]\n" \
"buffer_load_dwordx4 v31, s[36:39], s80 offen offset:3072 sc1 lds\n" \
"v_mfma_f32_16x16x32_bf16 v[48:51], a[64:67], v[112:115], v[48:51]\n" \
"s_add_u32 s80, s67, 16384\n" \
"ds_read_b128 v[104:107], v29 offset:1024\n" \
"s_add_u32 m0, s22, 0\n" \
"v_mfma_f32_16x16x32_bf16 v[48:51], a[72:75], v[116:119], v[48:51]\n" \
"v_cvt_pk_bf16_f32 v224, v224, v225\n" \
"v_cvt_pk_bf16_f32 v225, v226, v227\n" \
"v_mfma_f32_16x16x32_bf16 v[52:55], a[68:71], v[112:115], v[52:55]\n" \
"s_waitcnt lgkmcnt(3)\n" \
"ds_read_b128 v[108:111], v29 offset:3072\n" \
"v_cvt_pk_bf16_f32 v226, v228, v229\n" \
"v_mfma_f32_16x16x32_bf16 v[52:55], a[76:79], v[116:119], v[52:55]\n" \
"buffer_load_dwordx4 v31, s[36:39], s80 offen offset:0 sc1 lds\n" \
"v_mfma_f32_16x16x32_bf16 v[56:59], a[64:67], v[120:123], v[56:59]\n" \
"ds_read_b128 v[112:115], v29 offset:4096\n" \
"v_cvt_pk_bf16_f32 v227, v230, v231\n" \
"v_mfma_f32_16x16x32_bf16 v[56:59], a[72:75], v[124:127], v[56:59]\n" \
"v_mul_f32 v232, v187, v232\n" \
"v_mul_f32 v233, v187, v233\n" \
"v_mfma_f32_16x16x32_bf16 v[60:63], a[68:71], v[120:123], v[60:63]\n" \
"ds_read_b128 v[116:119], v29 offset:6144\n" \
"v_mul_f32 v234, v187, v234\n" \
"v_mfma_f32_16x16x32_bf16 v[60:63], a[76:79], v[124:127], v[60:63]\n" \
"s_waitcnt vmcnt(14) lgkmcnt(4)\n" \
"s_barrier\n" \
"v_mul_f32 v235, v187, v235\n" \
"v_mfma_f32_16x16x32_bf16 v[64:67], a[64:67], v[96:99], v[64:67]\n" \
"ds_read_b128 v[120:123], v29 offset:5120\n" \
"v_mul_f32 v236, v187, v236\n" \
"v_mfma_f32_16x16x32_bf16 v[64:67], a[72:75], v[100:103], v[64:67]\n" \
"buffer_store_dwordx4 v[224:227], v4, s[36:39], s51 offen offset:1024 sc1\n" \
"v_mfma_f32_16x16x32_bf16 v[68:71], a[68:71], v[96:99], v[68:71]\n" \
"s_waitcnt lgkmcnt(3)\n" \
"ds_read_b128 v[124:127], v29 offset:7168\n" \
"v_mul_f32 v237, v187, v237\n" \
"v_mfma_f32_16x16x32_bf16 v[68:71], a[76:79], v[100:103], v[68:71]\n" \
"ds_read_b64_tr_b16 a[144:145], v13 offset:16384\n" \
"v_mul_f32 v238, v187, v238\n" \
"v_mfma_f32_16x16x32_bf16 v[72:75], a[64:67], v[104:107], v[72:75]\n" \
"ds_read_b128 v[96:99], v29 offset:8192\n" \
"v_mul_f32 v239, v187, v239\n" \
"v_mfma_f32_16x16x32_bf16 v[72:75], a[72:75], v[108:111], v[72:75]\n" \
"ds_read_b64_tr_b16 a[146:147], v14 offset:16384\n" \
"v_cvt_pk_bf16_f32 v232, v232, v233\n" \
"v_mfma_f32_16x16x32_bf16 v[76:79], a[68:71], v[104:107], v[76:79]\n" \
"s_waitcnt lgkmcnt(5)\n" \
"ds_read_b128 v[100:103], v29 offset:10240\n" \
"v_cvt_pk_bf16_f32 v233, v234, v235\n" \
"v_mfma_f32_16x16x32_bf16 v[76:79], a[76:79], v[108:111], v[76:79]\n" \
"buffer_load_dwordx4 v31, s[36:39], s80 offen offset:1024 sc1 lds\n" \
"v_mfma_f32_16x16x32_bf16 v[32:35], a[80:83], v[112:115], v[32:35]\n" \
"ds_read_b128 v[104:107], v29 offset:9216\n" \
"v_cvt_pk_bf16_f32 v234, v236, v237\n" \
"v_mfma_f32_16x16x32_bf16 v[32:35], a[88:91], v[116:119], v[32:35]\n" \
"ds_read_b64_tr_b16 a[148:149], v15 offset:16384\n" \
"v_cvt_pk_bf16_f32 v235, v238, v239\n" \
"v_mfma_f32_16x16x32_bf16 v[36:39], a[84:87], v[112:115], v[36:39]\n" \
"s_waitcnt lgkmcnt(6)\n" \
"ds_read_b128 v[108:111], v29 offset:11264\n" \
"v_mul_f32 v240, v187, v240\n" \
"v_mfma_f32_16x16x32_bf16 v[36:39], a[92:95], v[116:119], v[36:39]\n" \
"buffer_store_dwordx4 v[232:235], v4, s[36:39], s51 offen offset:3072 sc1\n" \
"v_mfma_f32_16x16x32_bf16 v[40:43], a[80:83], v[120:123], v[40:43]\n" \
"ds_read_b128 v[112:115], v29 offset:12288\n" \
"v_mul_f32 v241, v187, v241\n" \
"v_mfma_f32_16x16x32_bf16 v[40:43], a[88:91], v[124:127], v[40:43]\n" \
"LH26_%=:\ns_cbranch_execz LHR2_%=\n" \
"ds_read_b64_tr_b16 a[150:151], v16 offset:16384\n" \
"v_mul_f32 v242, v187, v242\n" \
"v_mfma_f32_16x16x32_bf16 v[44:47], a[84:87], v[120:123], v[44:47]\n" \
"ds_read_b128 v[116:119], v29 offset:14336\n" \
"v_mul_f32 v243, v187, v243\n" \
"v_mfma_f32_16x16x32_bf16 v[44:47], a[92:95], v[124:127], v[44:47]\n" \
"s_waitcnt vmcnt(22) lgkmcnt(6)\n" \
"s_barrier\n" \
"ds_read_b64_tr_b16 a[152:153], v13 offset:24576\n" \
"v_mfma_f32_16x16x32_bf16 v[48:51], a[80:83], v[96:99], v[48:51]\n" \
"ds_read_b128 v[120:123], v29 offset:13312\n" \
"v_mul_f32 v244, v187, v244\n" \
"v_mfma_f32_16x16x32_bf16 v[48:51], a[88:91], v[100:103], v[48:51]\n" \
"ds_read_b64_tr_b16 a[154:155], v14 offset:24576\n" \
"v_mul_f32 v245, v187, v245\n" \
"v_mfma_f32_16x16x32_bf16 v[52:55], a[84:87], v[96:99], v[52:55]\n" \
"s_waitcnt lgkmcnt(6)\n" \
"ds_read_b128 v[124:127], v29 offset:15360\n" \
"v_mul_f32 v246, v187, v246\n" \
"v_mfma_f32_16x16x32_bf16 v[52:55], a[92:95], v[100:103], v[52:55]\n" \
"buffer_load_dwordx4 v31, s[36:39], s80 offen offset:2048 sc1 lds\n" \
"v_mfma_f32_16x16x32_bf16 v[56:59], a[80:83], v[104:107], v[56:59]\n" \
"ds_read_b128 v[96:99], v30 offset:0\n" \
"v_mul_f32 v247, v187, v247\n" \
"v_mfma_f32_16x16x32_bf16 v[56:59], a[88:91], v[108:111], v[56:59]\n" \
"ds_read_b64_tr_b16 a[156:157], v15 offset:24576\n" \
"v_cvt_pk_bf16_f32 v240, v240, v241\n" \
"v_mfma_f32_16x16x32_bf16 v[60:63], a[84:87], v[104:107], v[60:63]\n" \
"s_waitcnt lgkmcnt(6)\n" \
"ds_read_b128 v[100:103], v30 offset:2048\n" \
"v_cvt_pk_bf16_f32 v241, v242, v243\n" \
"v_mfma_f32_16x16x32_bf16 v[60:63], a[92:95], v[108:111], v[60:63]\n" \
"buffer_load_dwordx4 v31, s[36:39], s80 offen offset:3072 sc1 lds\n" \
"v_mfma_f32_16x16x32_bf16 v[64:67], a[80:83], v[112:115], v[64:67]\n" \
"s_add_u32 s80, s67, 32768\n" \
"ds_read_b128 v[104:107], v30 offset:1024\n" \
"s_add_u32 m0, s22, 81920\n" \
"v_mfma_f32_16x16x32_bf16 v[64:67], a[88:91], v[116:119], v[64:67]\n" \
"ds_read_b64_tr_b16 a[158:159], v16 offset:24576\n" \
"v_cvt_pk_bf16_f32 v242, v244, v245\n" \
"v_mfma_f32_16x16x32_bf16 v[68:71], a[84:87], v[112:115], v[68:71]\n" \
"s_waitcnt lgkmcnt(5)\n" \
"ds_read_b128 v[108:111], v30 offset:3072\n" \
"v_cvt_pk_bf16_f32 v243, v246, v247\n" \
"v_mfma_f32_16x16x32_bf16 v[68:71], a[92:95], v[116:119], v[68:71]\n" \
"buffer_store_dwordx4 v[240:243], v7, s[36:39], s51 offen offset:1024 sc1\n" \
"v_mfma_f32_16x16x32_bf16 v[72:75], a[80:83], v[120:123], v[72:75]\n" \
"ds_read_b128 v[112:115], v30 offset:4096\n" \
"v_mul_f32 v248, v187, v248\n" \
"v_mfma_f32_16x16x32_bf16 v[72:75], a[88:91], v[124:127], v[72:75]\n" \
"v_mul_f32 v249, v187, v249\n" \
"v_mul_f32 v250, v187, v250\n" \
"v_mfma_f32_16x16x32_bf16 v[76:79], a[84:87], v[120:123], v[76:79]\n" \
"ds_read_b128 v[116:119], v30 offset:6144\n" \
"v_mul_f32 v251, v187, v251\n" \
"v_mfma_f32_16x16x32_bf16 v[76:79], a[92:95], v[124:127], v[76:79]\n" \
"s_waitcnt vmcnt(7) lgkmcnt(3)\n" \
"s_barrier\n" \
"v_mul_f32 v252, v187, v252\n" \
"v_mfma_f32_16x16x32_bf16 v[32:35], a[128:131], v[96:99], v[32:35]\n" \
"ds_read_b128 v[120:123], v30 offset:5120\n" \
"v_mul_f32 v253, v187, v253\n" \
"v_mfma_f32_16x16x32_bf16 v[32:35], a[136:139], v[100:103], v[32:35]\n" \
"v_mul_f32 v254, v187, v254\n" \
"v_mul_f32 v255, v187, v255\n" \
"v_mfma_f32_16x16x32_bf16 v[36:39], a[132:135], v[96:99], v[36:39]\n" \
"s_waitcnt lgkmcnt(3)\n" \
"ds_read_b128 v[124:127], v30 offset:7168\n" \
"v_cvt_pk_bf16_f32 v248, v248, v249\n" \
"v_mfma_f32_16x16x32_bf16 v[36:39], a[140:143], v[100:103], v[36:39]\n" \
"buffer_load_dwordx4 v31, s[36:39], s80 offen offset:0 sc1 lds\n" \
"v_mfma_f32_16x16x32_bf16 v[40:43], a[128:131], v[104:107], v[40:43]\n" \
"ds_read_b128 v[96:99], v30 offset:8192\n" \
"v_cvt_pk_bf16_f32 v249, v250, v251\n" \
"v_mfma_f32_16x16x32_bf16 v[40:43], a[136:139], v[108:111], v[40:43]\n" \
"v_cvt_pk_bf16_f32 v250, v252, v253\n" \
"v_cvt_pk_bf16_f32 v251, v254, v255\n" \
"v_mfma_f32_16x16x32_bf16 v[44:47], a[132:135], v[104:107], v[44:47]\n" \
"s_waitcnt lgkmcnt(3)\n" \
"ds_read_b128 v[100:103], v30 offset:10240\n" \
"v_bfe_u32 v188, v4, 7, 1\n" \
"v_mfma_f32_16x16x32_bf16 v[44:47], a[140:143], v[108:111], v[44:47]\n" \
"buffer_store_dwordx4 v[248:251], v7, s[36:39], s51 offen offset:3072 sc1\n" \
"v_mfma_f32_16x16x32_bf16 v[48:51], a[128:131], v[112:115], v[48:51]\n" \
"ds_read_b128 v[104:107], v30 offset:9216\n" \
"v_bfe_u32 v189, v4, 4, 3\n" \
"v_mfma_f32_16x16x32_bf16 v[48:51], a[136:139], v[116:119], v[48:51]\n" \
"v_lshl_add_u32 v188, v189, 1, v188\n" \
"v_lshrrev_b32 v189, 8, v4\n" \
"v_mfma_f32_16x16x32_bf16 v[52:55], a[132:135], v[112:115], v[52:55]\n" \
"s_waitcnt lgkmcnt(3)\n" \
"ds_read_b128 v[108:111], v30 offset:11264\n" \
"v_lshlrev_b32 v184, 11, v188\n" \
"v_mfma_f32_16x16x32_bf16 v[52:55], a[140:143], v[116:119], v[52:55]\n" \
"v_and_b32 v188, 1, v189\n" \
"v_lshl_add_u32 v184, v188, 5, v184\n" \
"v_mfma_f32_16x16x32_bf16 v[56:59], a[128:131], v[120:123], v[56:59]\n" \
"ds_read_b128 v[112:115], v29 offset:49152\n" \
"v_lshrrev_b32 v188, 1, v189\n" \
"v_mfma_f32_16x16x32_bf16 v[56:59], a[136:139], v[124:127], v[56:59]\n" \
"v_lshl_add_u32 v184, v188, 4, v184\n" \
"v_bfe_u32 v188, v4, 7, 1\n" \
"v_mfma_f32_16x16x32_bf16 v[60:63], a[132:135], v[120:123], v[60:63]\n" \
"ds_read_b128 v[116:119], v29 offset:51200\n" \
"v_bfe_u32 v189, v4, 4, 3\n" \
"v_mfma_f32_16x16x32_bf16 v[60:63], a[140:143], v[124:127], v[60:63]\n" \
"s_waitcnt lgkmcnt(4)\n" \
"s_barrier\n" \
"v_lshl_add_u32 v188, v189, 1, v188\n" \
"v_mfma_f32_16x16x32_bf16 v[64:67], a[128:131], v[96:99], v[64:67]\n" \
"ds_read_b128 v[120:123], v29 offset:50176\n" \
"v_lshlrev_b32 v185, 3, v188\n" \
"v_mfma_f32_16x16x32_bf16 v[64:67], a[136:139], v[100:103], v[64:67]\n" \
"buffer_load_dwordx4 v31, s[36:39], s80 offen offset:1024 sc1 lds\n" \
"v_mfma_f32_16x16x32_bf16 v[68:71], a[132:135], v[96:99], v[68:71]\n" \
"s_waitcnt lgkmcnt(3)\n" \
"ds_read_b128 v[124:127], v29 offset:52224\n" \
"v_accvgpr_read_b32 v144, a96\n" \
"v_mfma_f32_16x16x32_bf16 v[68:71], a[140:143], v[100:103], v[68:71]\n" \
"v_accvgpr_read_b32 v145, a97\n" \
"v_accvgpr_read_b32 v146, a98\n" \
"v_mfma_f32_16x16x32_bf16 v[72:75], a[128:131], v[104:107], v[72:75]\n" \
"ds_read_b128 v[96:99], v29 offset:53248\n" \
"v_accvgpr_read_b32 v147, a99\n" \
"v_mfma_f32_16x16x32_bf16 v[72:75], a[136:139], v[108:111], v[72:75]\n" \
"buffer_load_dwordx4 v31, s[36:39], s80 offen offset:2048 sc1 lds\n" \
"v_mfma_f32_16x16x32_bf16 v[76:79], a[132:135], v[104:107], v[76:79]\n" \
"s_waitcnt lgkmcnt(3)\n" \
"ds_read_b128 v[100:103], v29 offset:55296\n" \
"v_accvgpr_read_b32 v148, a100\n" \
"v_mfma_f32_16x16x32_bf16 v[76:79], a[140:143], v[108:111], v[76:79]\n" \
"v_accvgpr_read_b32 v149, a101\n" \
"v_accvgpr_read_b32 v150, a102\n" \
"v_mfma_f32_16x16x32_bf16 v[80:83], a[160:163], v[112:115], 0\n" \
"ds_read_b128 v[104:107], v29 offset:54272\n" \
"v_accvgpr_read_b32 v151, a103\n" \
"v_mfma_f32_16x16x32_bf16 v[80:83], a[168:171], v[116:119], v[80:83]\n" \
"buffer_load_dwordx4 v31, s[36:39], s80 offen offset:3072 sc1 lds\n" \
"v_mfma_f32_16x16x32_bf16 v[84:87], a[164:167], v[112:115], 0\n" \
"s_waitcnt lgkmcnt(3)\n" \
"ds_read_b128 v[108:111], v29 offset:56320\n" \
"s_mov_b32 s81, 1310720\n" \
"v_mfma_f32_16x16x32_bf16 v[84:87], a[172:175], v[116:119], v[84:87]\n" \
"s_add_u32 m0, s79, 16384\n" \
"v_accvgpr_read_b32 v152, a104\n" \
"v_accvgpr_read_b32 v153, a105\n" \
"v_mfma_f32_16x16x32_bf16 v[88:91], a[160:163], v[120:123], 0\n" \
"ds_read_b128 v[112:115], v29 offset:57344\n" \
"v_accvgpr_read_b32 v154, a106\n" \
"v_mfma_f32_16x16x32_bf16 v[88:91], a[168:171], v[124:127], v[88:91]\n" \
"buffer_load_dwordx4 v12, s[28:31], s81 offen offset:0 lds\n" \
"v_mfma_f32_16x16x32_bf16 v[92:95], a[164:167], v[120:123], 0\n" \
"s_add_u32 m0, s79, 17280\n" \
"ds_read_b128 v[116:119], v29 offset:59392\n" \
"v_accvgpr_read_b32 v155, a107\n" \
"v_mfma_f32_16x16x32_bf16 v[92:95], a[172:175], v[124:127], v[92:95]\n" \
"s_waitcnt vmcnt(7) lgkmcnt(4)\n" \
"s_barrier\n" \
"v_accvgpr_read_b32 v156, a108\n" \
"v_mfma_f32_16x16x32_bf16 v[80:83], a[176:179], v[96:99], v[80:83]\n" \
"ds_read_b128 v[120:123], v29 offset:58368\n" \
"v_accvgpr_read_b32 v157, a109\n" \
"v_mfma_f32_16x16x32_bf16 v[80:83], a[184:187], v[100:103], v[80:83]\n" \
"buffer_load_dwordx4 v12, s[28:31], s81 offen offset:128 lds\n" \
"v_mfma_f32_16x16x32_bf16 v[84:87], a[180:183], v[96:99], v[84:87]\n" \
"s_waitcnt lgkmcnt(3)\n" \
"ds_read_b128 v[124:127], v29 offset:60416\n" \
"s_mov_b32 s81, 1376256\n" \
"v_mfma_f32_16x16x32_bf16 v[84:87], a[188:191], v[100:103], v[84:87]\n" \
"s_add_u32 m0, s79, 24576\n" \
"v_accvgpr_read_b32 v158, a110\n" \
"v_accvgpr_read_b32 v159, a111\n" \
"v_mfma_f32_16x16x32_bf16 v[88:91], a[176:179], v[104:107], v[88:91]\n" \
"ds_read_b128 v[96:99], v29 offset:61440\n" \
"v_accvgpr_read_b32 v128, a112\n" \
"v_mfma_f32_16x16x32_bf16 v[88:91], a[184:187], v[108:111], v[88:91]\n" \
"buffer_load_dwordx4 v12, s[28:31], s81 offen offset:0 lds\n" \
"v_mfma_f32_16x16x32_bf16 v[92:95], a[180:183], v[104:107], v[92:95]\n" \
"s_waitcnt lgkmcnt(3)\n" \
"ds_read_b128 v[100:103], v29 offset:63488\n" \
"s_add_u32 m0, s79, 25472\n" \
"v_mfma_f32_16x16x32_bf16 v[92:95], a[188:191], v[108:111], v[92:95]\n" \
"v_accvgpr_read_b32 v129, a113\n" \
"v_accvgpr_read_b32 v130, a114\n" \
"v_mfma_f32_16x16x32_bf16 v[80:83], a[192:195], v[112:115], v[80:83]\n" \
"ds_read_b128 v[104:107], v29 offset:62464\n" \
"v_accvgpr_read_b32 v131, a115\n" \
"v_mfma_f32_16x16x32_bf16 v[80:83], a[200:203], v[116:119], v[80:83]\n" \
"buffer_load_dwordx4 v12, s[28:31], s81 offen offset:128 lds\n" \
"v_mfma_f32_16x16x32_bf16 v[84:87], a[196:199], v[112:115], v[84:87]\n" \
"s_waitcnt lgkmcnt(3)\n" \
"ds_read_b128 v[108:111], v29 offset:64512\n" \
"s_mov_b32 s81, 1441792\n" \
"v_mfma_f32_16x16x32_bf16 v[84:87], a[204:207], v[116:119], v[84:87]\n" \
"s_add_u32 m0, s79, 98304\n" \
"v_accvgpr_read_b32 v132, a116\n" \
"v_accvgpr_read_b32 v133, a117\n" \
"v_mfma_f32_16x16x32_bf16 v[88:91], a[192:195], v[120:123], v[88:91]\n" \
"ds_read_b128 v[112:115], v28 offset:0\n" \
"v_accvgpr_read_b32 v134, a118\n" \
"v_mfma_f32_16x16x32_bf16 v[88:91], a[200:203], v[124:127], v[88:91]\n" \
"buffer_load_dwordx4 v12, s[28:31], s81 offen offset:0 lds\n" \
"v_mfma_f32_16x16x32_bf16 v[92:95], a[196:199], v[120:123], v[92:95]\n" \
"s_add_u32 m0, s79, 99200\n" \
"ds_read_b128 v[116:119], v28 offset:2048\n" \
"v_accvgpr_read_b32 v135, a119\n" \
"v_mfma_f32_16x16x32_bf16 v[92:95], a[204:207], v[124:127], v[92:95]\n" \
"s_waitcnt lgkmcnt(4)\n" \
"s_barrier\n" \
"v_accvgpr_read_b32 v136, a120\n" \
"v_mfma_f32_16x16x32_bf16 v[80:83], a[208:211], v[96:99], v[80:83]\n" \
"ds_read_b128 v[120:123], v28 offset:1024\n" \
"v_accvgpr_read_b32 v137, a121\n" \
"v_mfma_f32_16x16x32_bf16 v[80:83], a[216:219], v[100:103], v[80:83]\n" \
"buffer_load_dwordx4 v12, s[28:31], s81 offen offset:128 lds\n" \
"v_mfma_f32_16x16x32_bf16 v[84:87], a[212:215], v[96:99], v[84:87]\n" \
"s_waitcnt lgkmcnt(3)\n" \
"ds_read_b128 v[124:127], v28 offset:3072\n" \
"s_mov_b32 s81, 1507328\n" \
"v_mfma_f32_16x16x32_bf16 v[84:87], a[220:223], v[100:103], v[84:87]\n" \
"s_add_u32 m0, s79, 106496\n" \
"v_accvgpr_read_b32 v138, a122\n" \
"v_accvgpr_read_b32 v139, a123\n" \
"v_mfma_f32_16x16x32_bf16 v[88:91], a[208:211], v[104:107], v[88:91]\n" \
"ds_read_b128 v[96:99], v28 offset:4096\n" \
"v_accvgpr_read_b32 v140, a124\n" \
"v_mfma_f32_16x16x32_bf16 v[88:91], a[216:219], v[108:111], v[88:91]\n" \
"buffer_load_dwordx4 v12, s[28:31], s81 offen offset:0 lds\n" \
"v_mfma_f32_16x16x32_bf16 v[92:95], a[212:215], v[104:107], v[92:95]\n" \
"s_waitcnt lgkmcnt(3)\n" \
"ds_read_b128 v[100:103], v28 offset:6144\n" \
"s_add_u32 m0, s79, 107392\n" \
"v_mfma_f32_16x16x32_bf16 v[92:95], a[220:223], v[108:111], v[92:95]\n" \
"v_accvgpr_read_b32 v141, a125\n" \
"v_accvgpr_read_b32 v142, a126\n" \
"v_mfma_f32_16x16x32_bf16 v[80:83], a[224:227], v[112:115], v[80:83]\n" \
"ds_read_b128 v[104:107], v28 offset:5120\n" \
"v_accvgpr_read_b32 v143, a127\n" \
"v_mfma_f32_16x16x32_bf16 v[80:83], a[232:235], v[116:119], v[80:83]\n" \
"buffer_load_dwordx4 v12, s[28:31], s81 offen offset:128 lds\n" \
"v_mfma_f32_16x16x32_bf16 v[84:87], a[228:231], v[112:115], v[84:87]\n" \
"s_waitcnt lgkmcnt(3)\n" \
"ds_read_b128 v[108:111], v28 offset:7168\n" \
"s_add_u32 s80, s66, 32768\n" \
"v_mfma_f32_16x16x32_bf16 v[84:87], a[236:239], v[116:119], v[84:87]\n" \
"s_add_u32 m0, s22, 49152\n" \
"v_mfma_f32_16x16x32_bf16 v[88:91], a[224:227], v[120:123], v[88:91]\n" \
"ds_read_b128 v[112:115], v28 offset:8192\n" \
"v_mfma_f32_16x16x32_bf16 v[88:91], a[232:235], v[124:127], v[88:91]\n" \
"buffer_load_dwordx4 v4, s[36:39], s80 offen offset:0 sc1 lds\n" \
"v_mfma_f32_16x16x32_bf16 v[92:95], a[228:231], v[120:123], v[92:95]\n" \
"ds_read_b128 v[116:119], v28 offset:10240\n" \
"v_mfma_f32_16x16x32_bf16 v[92:95], a[236:239], v[124:127], v[92:95]\n" \
"s_waitcnt vmcnt(9) lgkmcnt(4)\n" \
"s_barrier\n" \
"v_mfma_f32_16x16x32_bf16 v[80:83], a[240:243], v[96:99], v[80:83]\n" \
"ds_read_b128 v[120:123], v28 offset:9216\n" \
"v_mfma_f32_16x16x32_bf16 v[80:83], a[248:251], v[100:103], v[80:83]\n" \
"buffer_load_dwordx4 v4, s[36:39], s80 offen offset:1024 sc1 lds\n" \
"v_mfma_f32_16x16x32_bf16 v[84:87], a[244:247], v[96:99], v[84:87]\n" \
"s_waitcnt lgkmcnt(3)\n" \
"ds_read_b128 v[124:127], v28 offset:11264\n" \
"v_mfma_f32_16x16x32_bf16 v[84:87], a[252:255], v[100:103], v[84:87]\n" \
"v_mfma_f32_16x16x32_bf16 v[88:91], a[240:243], v[104:107], v[88:91]\n" \
"ds_read_b128 v[96:99], v28 offset:12288\n" \
"v_mfma_f32_16x16x32_bf16 v[88:91], a[248:251], v[108:111], v[88:91]\n" \
"buffer_load_dwordx4 v4, s[36:39], s80 offen offset:2048 sc1 lds\n" \
"v_mfma_f32_16x16x32_bf16 v[92:95], a[244:247], v[104:107], v[92:95]\n" \
"s_waitcnt lgkmcnt(3)\n" \
"ds_read_b128 v[100:103], v28 offset:14336\n" \
"v_mfma_f32_16x16x32_bf16 v[92:95], a[252:255], v[108:111], v[92:95]\n" \
"v_mfma_f32_16x16x32_bf16 v[80:83], a[64:67], v[112:115], v[80:83]\n" \
"ds_read_b128 v[104:107], v28 offset:13312\n" \
"v_mfma_f32_16x16x32_bf16 v[80:83], a[72:75], v[116:119], v[80:83]\n" \
"buffer_load_dwordx4 v4, s[36:39], s80 offen offset:3072 sc1 lds\n" \
"v_mfma_f32_16x16x32_bf16 v[84:87], a[68:71], v[112:115], v[84:87]\n" \
"s_waitcnt lgkmcnt(3)\n" \
"ds_read_b128 v[108:111], v28 offset:15360\n" \
"s_mov_b32 s81, 1572864\n" \
"v_mfma_f32_16x16x32_bf16 v[84:87], a[76:79], v[116:119], v[84:87]\n" \
"s_add_u32 m0, s79, 65536\n" \
"v_mfma_f32_16x16x32_bf16 v[88:91], a[64:67], v[120:123], v[88:91]\n" \
"ds_read_b128 v[112:115], v29 offset:16384\n" \
"v_mfma_f32_16x16x32_bf16 v[88:91], a[72:75], v[124:127], v[88:91]\n" \
"buffer_load_dwordx4 v12, s[28:31], s81 offen offset:0 lds\n" \
"v_mfma_f32_16x16x32_bf16 v[92:95], a[68:71], v[120:123], v[92:95]\n" \
"ds_read_b128 v[116:119], v29 offset:18432\n" \
"s_waitcnt vmcnt(16)\n" \
"v_mfma_f32_16x16x32_bf16 v[92:95], a[76:79], v[124:127], v[92:95]\n" \
"s_waitcnt vmcnt(31) lgkmcnt(4)\n" \
"s_barrier\n" \
"s_add_u32 m0, s79, 66432\n" \
"v_mfma_f32_16x16x32_bf16 v[80:83], a[80:83], v[96:99], v[80:83]\n" \
"ds_read_b128 v[120:123], v29 offset:17408\n" \
"v_mfma_f32_16x16x32_bf16 v[80:83], a[88:91], v[100:103], v[80:83]\n" \
"buffer_load_dwordx4 v12, s[28:31], s81 offen offset:128 lds\n" \
"v_mfma_f32_16x16x32_bf16 v[84:87], a[84:87], v[96:99], v[84:87]\n" \
"s_waitcnt lgkmcnt(3)\n" \
"ds_read_b128 v[124:127], v29 offset:19456\n" \
"s_mov_b32 s81, 1638400\n" \
"v_mfma_f32_16x16x32_bf16 v[84:87], a[92:95], v[100:103], v[84:87]\n" \
"s_cmp_lg_u64 s[4:5], 0\ns_cbranch_scc1 LFBB_%=\nLFBBR_%=:\n" \
"s_add_u32 m0, s79, 73728\n" \
"v_mfma_f32_16x16x32_bf16 v[88:91], a[80:83], v[104:107], v[88:91]\n" \
"ds_read_b128 v[96:99], v30 offset:12288\n" \
"v_mfma_f32_16x16x32_bf16 v[88:91], a[88:91], v[108:111], v[88:91]\n" \
"buffer_store_dwordx2 v[10:11], v20, s[36:39], s53 offen sc1\n" \
"v_mfma_f32_16x16x32_bf16 v[92:95], a[84:87], v[104:107], v[92:95]\n" \
"s_waitcnt lgkmcnt(3)\n" \
"ds_read_b128 v[100:103], v30 offset:14336\n" \
"v_mfma_f32_16x16x32_bf16 v[92:95], a[92:95], v[108:111], v[92:95]\n" \
"buffer_load_dwordx4 v12, s[28:31], s81 offen offset:0 lds\n" \
"v_mfma_f32_16x16x32_bf16 v[80:83], a[128:131], v[112:115], v[80:83]\n" \
"s_add_u32 m0, s79, 74624\n" \
"ds_read_b128 v[104:107], v30 offset:13312\n" \
"v_mfma_f32_16x16x32_bf16 v[80:83], a[136:139], v[116:119], v[80:83]\n" \
"v_mfma_f32_16x16x32_bf16 v[84:87], a[132:135], v[112:115], v[84:87]\n" \
"s_waitcnt lgkmcnt(3)\n" \
"ds_read_b128 v[108:111], v30 offset:15360\n" \
"v_mfma_f32_16x16x32_bf16 v[84:87], a[140:143], v[116:119], v[84:87]\n" \
"buffer_load_dwordx4 v12, s[28:31], s81 offen offset:128 lds\n" \
"v_mfma_f32_16x16x32_bf16 v[88:91], a[128:131], v[120:123], v[88:91]\n" \
"s_add_u32 s80, s64, 49152\n" \
"ds_read_b128 v[112:115], v28 offset:32768\n" \
"s_add_u32 m0, s22, 147456\n" \
"v_mfma_f32_16x16x32_bf16 v[88:91], a[136:139], v[124:127], v[88:91]\n" \
"v_mfma_f32_16x16x32_bf16 v[92:95], a[132:135], v[120:123], v[92:95]\n" \
"ds_read_b128 v[116:119], v28 offset:34816\n" \
"v_mfma_f32_16x16x32_bf16 v[92:95], a[140:143], v[124:127], v[92:95]\n" \
"s_waitcnt vmcnt(13) lgkmcnt(4)\n" \
"s_barrier\n" \
"v_mfma_f32_16x16x32_bf16 v[32:35], a[144:147], v[96:99], v[32:35]\n" \
"ds_read_b128 v[120:123], v28 offset:33792\n" \
"v_mfma_f32_16x16x32_bf16 v[32:35], a[152:155], v[100:103], v[32:35]\n" \
"buffer_load_dwordx4 v4, s[36:39], s80 offen offset:0 sc1 lds\n" \
"v_mfma_f32_16x16x32_bf16 v[36:39], a[148:151], v[96:99], v[36:39]\n" \
"s_waitcnt lgkmcnt(3)\n" \
"ds_read_b128 v[124:127], v28 offset:35840\n" \
"v_mfma_f32_16x16x32_bf16 v[36:39], a[156:159], v[100:103], v[36:39]\n" \
"ds_read_b64_tr_b16 a[0:1], v176 offset:16384\n" \
"v_mfma_f32_16x16x32_bf16 v[40:43], a[144:147], v[104:107], v[40:43]\n" \
"ds_read_b128 v[96:99], v28 offset:36864\n" \
"v_mfma_f32_16x16x32_bf16 v[40:43], a[152:155], v[108:111], v[40:43]\n" \
"buffer_load_dwordx4 v4, s[36:39], s80 offen offset:1024 sc1 lds\n" \
"v_mfma_f32_16x16x32_bf16 v[44:47], a[148:151], v[104:107], v[44:47]\n" \
"s_waitcnt lgkmcnt(4)\n" \
"ds_read_b128 v[100:103], v28 offset:38912\n" \
"v_mfma_f32_16x16x32_bf16 v[44:47], a[156:159], v[108:111], v[44:47]\n" \
"ds_read_b64_tr_b16 a[2:3], v177 offset:16384\n" \
"v_mfma_f32_16x16x32_bf16 v[48:51], a[144:147], v[112:115], v[48:51]\n" \
"ds_read_b128 v[104:107], v28 offset:37888\n" \
"v_mfma_f32_16x16x32_bf16 v[48:51], a[152:155], v[116:119], v[48:51]\n" \
"buffer_load_dwordx4 v4, s[36:39], s80 offen offset:2048 sc1 lds\n" \
"v_mfma_f32_16x16x32_bf16 v[52:55], a[148:151], v[112:115], v[52:55]\n" \
"s_waitcnt lgkmcnt(5)\n" \
"ds_read_b128 v[108:111], v28 offset:39936\n" \
"v_mfma_f32_16x16x32_bf16 v[52:55], a[156:159], v[116:119], v[52:55]\n" \
"ds_read_b64_tr_b16 a[4:5], v178 offset:16384\n" \
"v_mfma_f32_16x16x32_bf16 v[56:59], a[144:147], v[120:123], v[56:59]\n" \
"ds_read_b128 v[112:115], v29 offset:20480\n" \
"v_mfma_f32_16x16x32_bf16 v[56:59], a[152:155], v[124:127], v[56:59]\n" \
"buffer_load_dwordx4 v4, s[36:39], s80 offen offset:3072 sc1 lds\n" \
"v_mfma_f32_16x16x32_bf16 v[60:63], a[148:151], v[120:123], v[60:63]\n" \
"s_mov_b32 s81, 1703936\n" \
"ds_read_b128 v[116:119], v29 offset:22528\n" \
"s_add_u32 m0, s79, 114688\n" \
"v_mfma_f32_16x16x32_bf16 v[60:63], a[156:159], v[124:127], v[60:63]\n" \
"s_waitcnt vmcnt(13) lgkmcnt(6)\n" \
"s_barrier\n" \
"ds_read_b64_tr_b16 a[6:7], v179 offset:16384\n" \
"v_mfma_f32_16x16x32_bf16 v[64:67], a[144:147], v[96:99], v[64:67]\n" \
"ds_read_b128 v[120:123], v29 offset:21504\n" \
"v_mfma_f32_16x16x32_bf16 v[64:67], a[152:155], v[100:103], v[64:67]\n" \
"buffer_load_dwordx4 v12, s[28:31], s81 offen offset:0 lds\n" \
"v_mfma_f32_16x16x32_bf16 v[68:71], a[148:151], v[96:99], v[68:71]\n" \
"s_waitcnt lgkmcnt(5)\n" \
"ds_read_b128 v[124:127], v29 offset:23552\n" \
"s_add_u32 m0, s79, 115584\n" \
"v_mfma_f32_16x16x32_bf16 v[68:71], a[156:159], v[100:103], v[68:71]\n" \
"ds_read_b64_tr_b16 a[8:9], v176 offset:24576\n" \
"v_mfma_f32_16x16x32_bf16 v[72:75], a[144:147], v[104:107], v[72:75]\n" \
"ds_read_b128 v[96:99], v28 offset:40960\n" \
"v_mfma_f32_16x16x32_bf16 v[72:75], a[152:155], v[108:111], v[72:75]\n" \
"buffer_load_dwordx4 v12, s[28:31], s81 offen offset:128 lds\n" \
"v_mfma_f32_16x16x32_bf16 v[76:79], a[148:151], v[104:107], v[76:79]\n" \
"s_waitcnt lgkmcnt(5)\n" \
"ds_read_b128 v[100:103], v28 offset:43008\n" \
"s_mov_b32 s81, 1769472\n" \
"v_mfma_f32_16x16x32_bf16 v[76:79], a[156:159], v[108:111], v[76:79]\n" \
"s_add_u32 m0, s79, 122880\n" \
"ds_read_b64_tr_b16 a[10:11], v177 offset:24576\n" \
"v_mfma_f32_16x16x32_bf16 v[80:83], a[144:147], v[112:115], v[80:83]\n" \
"ds_read_b128 v[104:107], v28 offset:41984\n" \
"v_mfma_f32_16x16x32_bf16 v[80:83], a[152:155], v[116:119], v[80:83]\n" \
"buffer_load_dwordx4 v12, s[28:31], s81 offen offset:0 lds\n" \
"v_mfma_f32_16x16x32_bf16 v[84:87], a[148:151], v[112:115], v[84:87]\n" \
"s_waitcnt lgkmcnt(5)\n" \
"ds_read_b128 v[108:111], v28 offset:44032\n" \
"s_add_u32 m0, s79, 123776\n" \
"ds_read_b64_tr_b16 a[12:13], v178 offset:24576\n" \
"ds_read_b64_tr_b16 a[14:15], v179 offset:24576\n" \
"v_mfma_f32_16x16x32_bf16 v[84:87], a[156:159], v[116:119], v[84:87]\n" \
"ds_read_b64_tr_b16 a[16:17], v180 offset:32768\n" \
"v_mfma_f32_16x16x32_bf16 v[88:91], a[144:147], v[120:123], v[88:91]\n" \
"ds_read_b128 v[112:115], v28 offset:45056\n" \
"v_mfma_f32_16x16x32_bf16 v[88:91], a[152:155], v[124:127], v[88:91]\n" \
"buffer_load_dwordx4 v12, s[28:31], s81 offen offset:128 lds\n" \
"v_mfma_f32_16x16x32_bf16 v[92:95], a[148:151], v[120:123], v[92:95]\n" \
"s_add_u32 s80, s67, 49152\n" \
"ds_read_b128 v[116:119], v28 offset:47104\n" \
"s_add_u32 m0, s22, 0\n" \
"v_mfma_f32_16x16x32_bf16 v[92:95], a[156:159], v[124:127], v[92:95]\n" \
"s_waitcnt vmcnt(13) lgkmcnt(3)\n" \
"s_barrier\n" \
"ds_read_b64_tr_b16 a[18:19], v181 offset:32768\n" \
"v_mfma_f32_16x16x32_bf16 v[32:35], a[0:3], v[96:99], v[32:35]\n" \
"ds_read_b128 v[120:123], v28 offset:46080\n" \
"v_mfma_f32_16x16x32_bf16 v[32:35], a[8:11], v[100:103], v[32:35]\n" \
"buffer_load_dwordx4 v31, s[36:39], s80 offen offset:0 sc1 lds\n" \
"v_mfma_f32_16x16x32_bf16 v[36:39], a[4:7], v[96:99], v[36:39]\n" \
"s_waitcnt lgkmcnt(7)\n" \
"ds_read_b128 v[124:127], v28 offset:48128\n" \
"v_mfma_f32_16x16x32_bf16 v[36:39], a[12:15], v[100:103], v[36:39]\n" \
"ds_read_b64_tr_b16 a[20:21], v182 offset:32768\n" \
"v_mfma_f32_16x16x32_bf16 v[40:43], a[0:3], v[104:107], v[40:43]\n" \
"ds_read_b128 v[96:99], v28 offset:49152\n" \
"v_mfma_f32_16x16x32_bf16 v[40:43], a[8:11], v[108:111], v[40:43]\n" \
"buffer_load_dwordx4 v31, s[36:39], s80 offen offset:1024 sc1 lds\n" \
"v_mfma_f32_16x16x32_bf16 v[44:47], a[4:7], v[104:107], v[44:47]\n" \
"s_waitcnt lgkmcnt(5)\n" \
"ds_read_b128 v[100:103], v28 offset:51200\n" \
"v_mfma_f32_16x16x32_bf16 v[44:47], a[12:15], v[108:111], v[44:47]\n" \
"ds_read_b64_tr_b16 a[22:23], v183 offset:32768\n" \
"v_mfma_f32_16x16x32_bf16 v[48:51], a[0:3], v[112:115], v[48:51]\n" \
"ds_read_b128 v[104:107], v28 offset:50176\n" \
"v_mfma_f32_16x16x32_bf16 v[48:51], a[8:11], v[116:119], v[48:51]\n" \
"buffer_load_dwordx4 v31, s[36:39], s80 offen offset:2048 sc1 lds\n" \
"v_mfma_f32_16x16x32_bf16 v[52:55], a[4:7], v[112:115], v[52:55]\n" \
"s_waitcnt lgkmcnt(5)\n" \
"ds_read_b128 v[108:111], v28 offset:52224\n" \
"ds_read_b64_tr_b16 a[24:25], v180 offset:40960\n" \
"ds_read_b64_tr_b16 a[26:27], v181 offset:40960\n" \
"ds_read_b64_tr_b16 a[28:29], v182 offset:40960\n" \
"ds_read_b64_tr_b16 a[30:31], v183 offset:40960\n" \
"v_mfma_f32_16x16x32_bf16 v[52:55], a[12:15], v[116:119], v[52:55]\n" \
"v_mfma_f32_16x16x32_bf16 v[56:59], a[0:3], v[120:123], v[56:59]\n" \
"ds_read_b128 v[112:115], v29 offset:24576\n" \
"v_mfma_f32_16x16x32_bf16 v[56:59], a[8:11], v[124:127], v[56:59]\n" \
"buffer_load_dwordx4 v31, s[36:39], s80 offen offset:3072 sc1 lds\n" \
"v_mfma_f32_16x16x32_bf16 v[60:63], a[4:7], v[120:123], v[60:63]\n" \
"s_add_u32 s80, s65, 49152\n" \
"ds_read_b128 v[116:119], v29 offset:26624\n" \
"s_add_u32 m0, s22, 131072\n" \
"v_mfma_f32_16x16x32_bf16 v[60:63], a[12:15], v[124:127], v[60:63]\n" \
"s_waitcnt lgkmcnt(2)\n" \
"s_barrier\n" \
"v_mfma_f32_16x16x32_bf16 v[64:67], a[0:3], v[96:99], v[64:67]\n" \
"ds_read_b128 v[120:123], v29 offset:25600\n" \
"v_mfma_f32_16x16x32_bf16 v[64:67], a[8:11], v[100:103], v[64:67]\n" \
"buffer_load_dwordx4 v4, s[36:39], s80 offen offset:0 sc1 lds\n" \
"v_mfma_f32_16x16x32_bf16 v[68:71], a[4:7], v[96:99], v[68:71]\n" \
"s_waitcnt lgkmcnt(7)\n" \
"ds_read_b128 v[124:127], v29 offset:27648\n" \
"v_mfma_f32_16x16x32_bf16 v[68:71], a[12:15], v[100:103], v[68:71]\n" \
"v_mfma_f32_16x16x32_bf16 v[72:75], a[0:3], v[104:107], v[72:75]\n" \
"ds_read_b128 v[96:99], v28 offset:53248\n" \
"v_mfma_f32_16x16x32_bf16 v[72:75], a[8:11], v[108:111], v[72:75]\n" \
"buffer_load_dwordx4 v4, s[36:39], s80 offen offset:1024 sc1 lds\n" \
"v_mfma_f32_16x16x32_bf16 v[76:79], a[4:7], v[104:107], v[76:79]\n" \
"s_waitcnt lgkmcnt(3)\n" \
"ds_read_b128 v[100:103], v28 offset:55296\n" \
"v_mfma_f32_16x16x32_bf16 v[76:79], a[12:15], v[108:111], v[76:79]\n" \
"v_mfma_f32_16x16x32_bf16 v[80:83], a[0:3], v[112:115], v[80:83]\n" \
"ds_read_b128 v[104:107], v28 offset:54272\n" \
"v_mfma_f32_16x16x32_bf16 v[80:83], a[8:11], v[116:119], v[80:83]\n" \
"buffer_load_dwordx4 v4, s[36:39], s80 offen offset:2048 sc1 lds\n" \
"v_mfma_f32_16x16x32_bf16 v[84:87], a[4:7], v[112:115], v[84:87]\n" \
"s_waitcnt lgkmcnt(3)\n" \
"ds_read_b128 v[108:111], v28 offset:56320\n" \
"v_bfe_u32 v186, v4, 7, 2\n" \
"v_mfma_f32_16x16x32_bf16 v[84:87], a[12:15], v[116:119], v[84:87]\n" \
"v_min_u32 v186, 3, v186\n" \
"v_lshl_add_u32 v17, v186, 8, s11\n" \
"v_mfma_f32_16x16x32_bf16 v[88:91], a[0:3], v[120:123], v[88:91]\n" \
"ds_read_b128 v[112:115], v28 offset:57344\n" \
"v_bfe_u32 v186, v4, 4, 3\n" \
"v_mfma_f32_16x16x32_bf16 v[88:91], a[8:11], v[124:127], v[88:91]\n" \
"buffer_load_dwordx4 v4, s[36:39], s80 offen offset:3072 sc1 lds\n" \
"v_mfma_f32_16x16x32_bf16 v[92:95], a[4:7], v[120:123], v[92:95]\n" \
"s_mov_b32 s81, 1835008\n" \
"ds_read_b128 v[116:119], v28 offset:59392\n" \
"v_lshl_add_u32 v17, v186, 3, v17\n" \
"v_mfma_f32_16x16x32_bf16 v[92:95], a[12:15], v[124:127], v[92:95]\n" \
"s_waitcnt vmcnt(16) lgkmcnt(4)\n" \
"s_barrier\n" \
"s_add_u32 m0, s79, 16384\n" \
"v_mfma_f32_16x16x32_bf16 v[32:35], a[16:19], v[96:99], v[32:35]\n" \
"ds_read_b128 v[120:123], v28 offset:58368\n" \
"v_mfma_f32_16x16x32_bf16 v[32:35], a[24:27], v[100:103], v[32:35]\n" \
"buffer_load_dwordx4 v12, s[28:31], s81 offen offset:0 lds\n" \
"v_mfma_f32_16x16x32_bf16 v[36:39], a[20:23], v[96:99], v[36:39]\n" \
"s_waitcnt lgkmcnt(3)\n" \
"ds_read_b128 v[124:127], v28 offset:60416\n" \
"s_add_u32 m0, s79, 17280\n" \
"v_mfma_f32_16x16x32_bf16 v[36:39], a[28:31], v[100:103], v[36:39]\n" \
"ds_read_b64_tr_b16 a[32:33], v180 offset:0\n" \
"v_mfma_f32_16x16x32_bf16 v[40:43], a[16:19], v[104:107], v[40:43]\n" \
"ds_read_b128 v[96:99], v28 offset:61440\n" \
"v_mfma_f32_16x16x32_bf16 v[40:43], a[24:27], v[108:111], v[40:43]\n" \
"buffer_load_dwordx4 v12, s[28:31], s81 offen offset:128 lds\n" \
"v_mfma_f32_16x16x32_bf16 v[44:47], a[20:23], v[104:107], v[44:47]\n" \
"s_waitcnt lgkmcnt(4)\n" \
"ds_read_b128 v[100:103], v28 offset:63488\n" \
"s_mov_b32 s81, 1900544\n" \
"v_mfma_f32_16x16x32_bf16 v[44:47], a[28:31], v[108:111], v[44:47]\n" \
"s_add_u32 m0, s79, 24576\n" \
"ds_read_b64_tr_b16 a[34:35], v181 offset:0\n" \
"v_mfma_f32_16x16x32_bf16 v[48:51], a[16:19], v[112:115], v[48:51]\n" \
"ds_read_b128 v[104:107], v28 offset:62464\n" \
"v_mfma_f32_16x16x32_bf16 v[48:51], a[24:27], v[116:119], v[48:51]\n" \
"buffer_load_dwordx2 v[18:19], v17, s[36:39], 0 offen sc1\n" \
"v_mfma_f32_16x16x32_bf16 v[52:55], a[20:23], v[112:115], v[52:55]\n" \
"s_waitcnt lgkmcnt(5)\n" \
"ds_read_b128 v[108:111], v28 offset:64512\n" \
"v_mfma_f32_16x16x32_bf16 v[52:55], a[28:31], v[116:119], v[52:55]\n" \
"ds_read_b64_tr_b16 a[36:37], v182 offset:0\n" \
"v_mfma_f32_16x16x32_bf16 v[56:59], a[16:19], v[120:123], v[56:59]\n" \
"ds_read_b128 v[112:115], v29 offset:28672\n" \
"v_mfma_f32_16x16x32_bf16 v[56:59], a[24:27], v[124:127], v[56:59]\n" \
"buffer_load_dwordx4 v12, s[28:31], s81 offen offset:0 lds\n" \
"v_mfma_f32_16x16x32_bf16 v[60:63], a[20:23], v[120:123], v[60:63]\n" \
"s_add_u32 m0, s79, 25472\n" \
"ds_read_b128 v[116:119], v29 offset:30720\n" \
"v_mfma_f32_16x16x32_bf16 v[60:63], a[28:31], v[124:127], v[60:63]\n" \
"s_waitcnt vmcnt(16) lgkmcnt(6)\n" \
"s_barrier\n" \
"ds_read_b64_tr_b16 a[38:39], v183 offset:0\n" \
"v_mfma_f32_16x16x32_bf16 v[64:67], a[16:19], v[96:99], v[64:67]\n" \
"ds_read_b128 v[120:123], v29 offset:29696\n" \
"v_mfma_f32_16x16x32_bf16 v[64:67], a[24:27], v[100:103], v[64:67]\n" \
"buffer_load_dwordx4 v12, s[28:31], s81 offen offset:128 lds\n" \
"v_mfma_f32_16x16x32_bf16 v[68:71], a[20:23], v[96:99], v[68:71]\n" \
"s_waitcnt lgkmcnt(5)\n" \
"ds_read_b128 v[124:127], v29 offset:31744\n" \
"s_mov_b32 s81, 1966080\n" \
"v_mfma_f32_16x16x32_bf16 v[68:71], a[28:31], v[100:103], v[68:71]\n" \
"s_add_u32 m0, s79, 32768\n" \
"ds_read_b64_tr_b16 a[40:41], v180 offset:8192\n" \
"v_mfma_f32_16x16x32_bf16 v[72:75], a[16:19], v[104:107], v[72:75]\n" \
"ds_read_b128 v[96:99], v30 offset:16384\n" \
"v_mfma_f32_16x16x32_bf16 v[72:75], a[24:27], v[108:111], v[72:75]\n" \
"buffer_load_dwordx4 v12, s[28:31], s81 offen offset:0 lds\n" \
"v_mfma_f32_16x16x32_bf16 v[76:79], a[20:23], v[104:107], v[76:79]\n" \
"LH27_%=:\ns_cbranch_execz LHR2_%=\n" \
"s_waitcnt lgkmcnt(5)\n" \
"ds_read_b128 v[100:103], v30 offset:18432\n" \
"s_add_u32 m0, s79, 33664\n" \
"v_mfma_f32_16x16x32_bf16 v[76:79], a[28:31], v[108:111], v[76:79]\n" \
"ds_read_b64_tr_b16 a[42:43], v181 offset:8192\n" \
"v_mfma_f32_16x16x32_bf16 v[80:83], a[16:19], v[112:115], v[80:83]\n" \
"ds_read_b128 v[104:107], v30 offset:17408\n" \
"v_mfma_f32_16x16x32_bf16 v[80:83], a[24:27], v[116:119], v[80:83]\n" \
"buffer_load_dwordx4 v12, s[28:31], s81 offen offset:128 lds\n" \
"v_mfma_f32_16x16x32_bf16 v[84:87], a[20:23], v[112:115], v[84:87]\n" \
"s_waitcnt lgkmcnt(5)\n" \
"ds_read_b128 v[108:111], v30 offset:19456\n" \
"s_mov_b32 s81, 2031616\n" \
"ds_read_b64_tr_b16 a[44:45], v182 offset:8192\n" \
"ds_read_b64_tr_b16 a[46:47], v183 offset:8192\n" \
"v_mfma_f32_16x16x32_bf16 v[84:87], a[28:31], v[116:119], v[84:87]\n" \
"s_add_u32 m0, s79, 40960\n" \
"v_mfma_f32_16x16x32_bf16 v[88:91], a[16:19], v[120:123], v[88:91]\n" \
"ds_read_b128 v[112:115], v30 offset:20480\n" \
"v_mfma_f32_16x16x32_bf16 v[88:91], a[24:27], v[124:127], v[88:91]\n" \
"buffer_load_dwordx4 v12, s[28:31], s81 offen offset:0 lds\n" \
"v_mfma_f32_16x16x32_bf16 v[92:95], a[20:23], v[120:123], v[92:95]\n" \
"s_add_u32 m0, s79, 41856\n" \
"ds_read_b128 v[116:119], v30 offset:22528\n" \
"v_mfma_f32_16x16x32_bf16 v[92:95], a[28:31], v[124:127], v[92:95]\n" \
"s_waitcnt vmcnt(12) lgkmcnt(2)\n" \
"s_barrier\n" \
"v_mfma_f32_16x16x32_bf16 v[32:35], a[32:35], v[96:99], v[32:35]\n" \
"ds_read_b128 v[120:123], v30 offset:21504\n" \
"v_mfma_f32_16x16x32_bf16 v[32:35], a[40:43], v[100:103], v[32:35]\n" \
"buffer_load_dwordx4 v12, s[28:31], s81 offen offset:128 lds\n" \
"v_mfma_f32_16x16x32_bf16 v[36:39], a[36:39], v[96:99], v[36:39]\n" \
"s_waitcnt lgkmcnt(5)\n" \
"ds_read_b128 v[124:127], v30 offset:23552\n" \
"s_add_u32 s80, s66, 49152\n" \
"v_mfma_f32_16x16x32_bf16 v[36:39], a[44:47], v[100:103], v[36:39]\n" \
"s_add_u32 m0, s22, 98304\n" \
"ds_read_b64_tr_b16 a[48:49], v180 offset:49152\n" \
"v_mfma_f32_16x16x32_bf16 v[40:43], a[32:35], v[104:107], v[40:43]\n" \
"ds_read_b128 v[96:99], v30 offset:24576\n" \
"v_mfma_f32_16x16x32_bf16 v[40:43], a[40:43], v[108:111], v[40:43]\n" \
"buffer_load_dwordx4 v4, s[36:39], s80 offen offset:0 sc1 lds\n" \
"v_mfma_f32_16x16x32_bf16 v[44:47], a[36:39], v[104:107], v[44:47]\n" \
"s_waitcnt lgkmcnt(4)\n" \
"ds_read_b128 v[100:103], v30 offset:26624\n" \
"v_mfma_f32_16x16x32_bf16 v[44:47], a[44:47], v[108:111], v[44:47]\n" \
"ds_read_b64_tr_b16 a[50:51], v181 offset:49152\n" \
"v_mfma_f32_16x16x32_bf16 v[48:51], a[32:35], v[112:115], v[48:51]\n" \
"ds_read_b128 v[104:107], v30 offset:25600\n" \
"v_mfma_f32_16x16x32_bf16 v[48:51], a[40:43], v[116:119], v[48:51]\n" \
"buffer_load_dwordx4 v4, s[36:39], s80 offen offset:1024 sc1 lds\n" \
"v_mfma_f32_16x16x32_bf16 v[52:55], a[36:39], v[112:115], v[52:55]\n" \
"s_waitcnt lgkmcnt(5)\n" \
"ds_read_b128 v[108:111], v30 offset:27648\n" \
"v_mfma_f32_16x16x32_bf16 v[52:55], a[44:47], v[116:119], v[52:55]\n" \
"ds_read_b64_tr_b16 a[52:53], v182 offset:49152\n" \
"v_mfma_f32_16x16x32_bf16 v[56:59], a[32:35], v[120:123], v[56:59]\n" \
"ds_read_b128 v[112:115], v28 offset:0\n" \
"v_mfma_f32_16x16x32_bf16 v[56:59], a[40:43], v[124:127], v[56:59]\n" \
"buffer_load_dwordx4 v4, s[36:39], s80 offen offset:2048 sc1 lds\n" \
"v_mfma_f32_16x16x32_bf16 v[60:63], a[36:39], v[120:123], v[60:63]\n" \
"ds_read_b128 v[116:119], v28 offset:2048\n" \
"v_mfma_f32_16x16x32_bf16 v[60:63], a[44:47], v[124:127], v[60:63]\n" \
"s_waitcnt vmcnt(12) lgkmcnt(6)\n" \
"s_barrier\n" \
"ds_read_b64_tr_b16 a[54:55], v183 offset:49152\n" \
"v_mfma_f32_16x16x32_bf16 v[64:67], a[32:35], v[96:99], v[64:67]\n" \
"ds_read_b128 v[120:123], v28 offset:1024\n" \
"s_waitcnt vmcnt(9)\n" \
"v_mfma_f32_16x16x32_bf16 v[64:67], a[40:43], v[100:103], v[64:67]\n" \
"buffer_load_dwordx4 v4, s[36:39], s80 offen offset:3072 sc1 lds\n" \
"v_mfma_f32_16x16x32_bf16 v[68:71], a[36:39], v[96:99], v[68:71]\n" \
"s_waitcnt lgkmcnt(5)\n" \
"ds_read_b128 v[124:127], v28 offset:3072\n" \
"v_cmp_ne_u64 vcc, s[14:15], v[18:19]\n" \
"v_mfma_f32_16x16x32_bf16 v[68:71], a[44:47], v[100:103], v[68:71]\n" \
"s_nop 0\n" \
"s_cbranch_vccnz LPL_%=\nLPLR_%=:\n" \
"v_mfma_f32_16x16x32_bf16 v[72:75], a[32:35], v[104:107], v[72:75]\n" \
"s_add_u32 s80, s68, 0\n" \
"ds_read_b128 v[96:99], v30 offset:28672\n" \
"s_add_u32 m0, s22, 49152\n" \
"v_mfma_f32_16x16x32_bf16 v[72:75], a[40:43], v[108:111], v[72:75]\n" \
"ds_read_b64_tr_b16 a[56:57], v180 offset:57344\n" \
"v_mfma_f32_16x16x32_bf16 v[76:79], a[36:39], v[104:107], v[76:79]\n" \
"s_waitcnt lgkmcnt(5)\n" \
"ds_read_b128 v[100:103], v30 offset:30720\n" \
"v_mfma_f32_16x16x32_bf16 v[76:79], a[44:47], v[108:111], v[76:79]\n" \
"buffer_load_dwordx4 v31, s[36:39], s80 offen offset:0 sc1 lds\n" \
"v_mfma_f32_16x16x32_bf16 v[80:83], a[32:35], v[112:115], v[80:83]\n" \
"ds_read_b128 v[104:107], v30 offset:29696\n" \
"v_mfma_f32_16x16x32_bf16 v[80:83], a[40:43], v[116:119], v[80:83]\n" \
"ds_read_b64_tr_b16 a[58:59], v181 offset:57344\n" \
"v_mfma_f32_16x16x32_bf16 v[84:87], a[36:39], v[112:115], v[84:87]\n" \
"s_waitcnt lgkmcnt(5)\n" \
"ds_read_b128 v[108:111], v30 offset:31744\n" \
"ds_read_b64_tr_b16 a[60:61], v182 offset:57344\n" \
"ds_read_b64_tr_b16 a[62:63], v183 offset:57344\n" \
"v_mfma_f32_16x16x32_bf16 v[84:87], a[44:47], v[116:119], v[84:87]\n" \
"buffer_load_dwordx4 v31, s[36:39], s80 offen offset:1024 sc1 lds\n" \
"v_mfma_f32_16x16x32_bf16 v[88:91], a[32:35], v[120:123], v[88:91]\n" \
"ds_read_b128 v[112:115], v30 offset:0\n" \
"v_mfma_f32_16x16x32_bf16 v[88:91], a[40:43], v[124:127], v[88:91]\n" \
"v_mfma_f32_16x16x32_bf16 v[92:95], a[36:39], v[120:123], v[92:95]\n" \
"ds_read_b128 v[116:119], v30 offset:2048\n" \
"v_mfma_f32_16x16x32_bf16 v[92:95], a[44:47], v[124:127], v[92:95]\n" \
"s_waitcnt vmcnt(10) lgkmcnt(2)\n" \
"s_barrier\n" \
"v_mfma_f32_16x16x32_bf16 v[32:35], a[48:51], v[96:99], v[32:35]\n" \
"ds_read_b128 v[120:123], v30 offset:1024\n" \
"v_mfma_f32_16x16x32_bf16 v[32:35], a[56:59], v[100:103], v[32:35]\n" \
"buffer_load_dwordx4 v31, s[36:39], s80 offen offset:2048 sc1 lds\n" \
"v_mfma_f32_16x16x32_bf16 v[36:39], a[52:55], v[96:99], v[36:39]\n" \
"s_waitcnt lgkmcnt(5)\n" \
"ds_read_b128 v[124:127], v30 offset:3072\n" \
"v_mfma_f32_16x16x32_bf16 v[36:39], a[60:63], v[100:103], v[36:39]\n" \
"ds_read_b64_tr_b16 a[96:97], v176 offset:16384\n" \
"v_mfma_f32_16x16x32_bf16 v[40:43], a[48:51], v[104:107], v[40:43]\n" \
"ds_read_b128 v[96:99], v30 offset:4096\n" \
"v_mfma_f32_16x16x32_bf16 v[40:43], a[56:59], v[108:111], v[40:43]\n" \
"buffer_load_dwordx4 v31, s[36:39], s80 offen offset:3072 sc1 lds\n" \
"v_mfma_f32_16x16x32_bf16 v[44:47], a[52:55], v[104:107], v[44:47]\n" \
"s_waitcnt lgkmcnt(4)\n" \
"ds_read_b128 v[100:103], v30 offset:6144\n" \
"s_add_u32 s80, s68, 16384\n" \
"v_mfma_f32_16x16x32_bf16 v[44:47], a[60:63], v[108:111], v[44:47]\n" \
"s_add_u32 m0, s22, 65536\n" \
"ds_read_b64_tr_b16 a[98:99], v177 offset:16384\n" \
"v_mfma_f32_16x16x32_bf16 v[48:51], a[48:51], v[112:115], v[48:51]\n" \
"ds_read_b128 v[104:107], v30 offset:5120\n" \
"v_mfma_f32_16x16x32_bf16 v[48:51], a[56:59], v[116:119], v[48:51]\n" \
"buffer_load_dwordx4 v31, s[36:39], s80 offen offset:0 sc1 lds\n" \
"v_mfma_f32_16x16x32_bf16 v[52:55], a[52:55], v[112:115], v[52:55]\n" \
"s_waitcnt lgkmcnt(5)\n" \
"ds_read_b128 v[108:111], v30 offset:7168\n" \
"v_mfma_f32_16x16x32_bf16 v[52:55], a[60:63], v[116:119], v[52:55]\n" \
"ds_read_b64_tr_b16 a[100:101], v178 offset:16384\n" \
"v_mfma_f32_16x16x32_bf16 v[56:59], a[48:51], v[120:123], v[56:59]\n" \
"ds_read_b128 v[112:115], v28 offset:4096\n" \
"v_mfma_f32_16x16x32_bf16 v[56:59], a[56:59], v[124:127], v[56:59]\n" \
"buffer_load_dwordx4 v31, s[36:39], s80 offen offset:1024 sc1 lds\n" \
"v_mfma_f32_16x16x32_bf16 v[60:63], a[52:55], v[120:123], v[60:63]\n" \
"ds_read_b128 v[116:119], v28 offset:6144\n" \
"v_mfma_f32_16x16x32_bf16 v[60:63], a[60:63], v[124:127], v[60:63]\n" \
"s_waitcnt lgkmcnt(6)\n" \
"s_barrier\n" \
"ds_read_b64_tr_b16 a[102:103], v179 offset:16384\n" \
"v_mfma_f32_16x16x32_bf16 v[64:67], a[48:51], v[96:99], v[64:67]\n" \
"ds_read_b128 v[120:123], v28 offset:5120\n" \
"v_mfma_f32_16x16x32_bf16 v[64:67], a[56:59], v[100:103], v[64:67]\n" \
"buffer_load_dwordx4 v31, s[36:39], s80 offen offset:2048 sc1 lds\n" \
"v_mfma_f32_16x16x32_bf16 v[68:71], a[52:55], v[96:99], v[68:71]\n" \
"s_waitcnt lgkmcnt(5)\n" \
"ds_read_b128 v[124:127], v28 offset:7168\n" \
"v_mfma_f32_16x16x32_bf16 v[68:71], a[60:63], v[100:103], v[68:71]\n" \
"ds_read_b64_tr_b16 a[104:105], v176 offset:24576\n" \
"v_mfma_f32_16x16x32_bf16 v[72:75], a[48:51], v[104:107], v[72:75]\n" \
"ds_read_b128 v[96:99], v30 offset:8192\n" \
"v_mfma_f32_16x16x32_bf16 v[72:75], a[56:59], v[108:111], v[72:75]\n" \
"buffer_load_dwordx4 v31, s[36:39], s80 offen offset:3072 sc1 lds\n" \
"v_mfma_f32_16x16x32_bf16 v[76:79], a[52:55], v[104:107], v[76:79]\n" \
"s_waitcnt lgkmcnt(5)\n" \
"ds_read_b128 v[100:103], v30 offset:10240\n" \
"s_add_u32 s80, s68, 32768\n" \
"v_mfma_f32_16x16x32_bf16 v[76:79], a[60:63], v[108:111], v[76:79]\n" \
"s_add_u32 m0, s22, 81920\n" \
"ds_read_b64_tr_b16 a[106:107], v177 offset:24576\n" \
"v_mfma_f32_16x16x32_bf16 v[80:83], a[48:51], v[112:115], v[80:83]\n" \
"ds_read_b128 v[104:107], v30 offset:9216\n" \
"v_mfma_f32_16x16x32_bf16 v[80:83], a[56:59], v[116:119], v[80:83]\n" \
"buffer_load_dwordx4 v31, s[36:39], s80 offen offset:0 sc1 lds\n" \
"v_mfma_f32_16x16x32_bf16 v[84:87], a[52:55], v[112:115], v[84:87]\n" \
"s_waitcnt lgkmcnt(5)\n" \
"ds_read_b128 v[108:111], v30 offset:11264\n" \
"ds_read_b64_tr_b16 a[108:109], v178 offset:24576\n" \
"ds_read_b64_tr_b16 a[110:111], v179 offset:24576\n" \
"v_mfma_f32_16x16x32_bf16 v[84:87], a[60:63], v[116:119], v[84:87]\n" \
"v_mfma_f32_16x16x32_bf16 v[88:91], a[48:51], v[120:123], v[88:91]\n" \
"ds_read_b128 v[112:115], v30 offset:12288\n" \
"v_mfma_f32_16x16x32_bf16 v[88:91], a[56:59], v[124:127], v[88:91]\n" \
"buffer_load_dwordx4 v31, s[36:39], s80 offen offset:1024 sc1 lds\n" \
"v_mfma_f32_16x16x32_bf16 v[92:95], a[52:55], v[120:123], v[92:95]\n" \
"ds_read_b128 v[116:119], v30 offset:14336\n" \
"v_mfma_f32_16x16x32_bf16 v[92:95], a[60:63], v[124:127], v[92:95]\n" \
"s_waitcnt vmcnt(10) lgkmcnt(2)\n" \
"s_barrier\n" \
"v_mfma_f32_16x16x32_bf16 v[32:35], a[96:99], v[96:99], v[32:35]\n" \
"ds_read_b128 v[120:123], v30 offset:13312\n" \
"v_mfma_f32_16x16x32_bf16 v[32:35], a[104:107], v[100:103], v[32:35]\n" \
"buffer_load_dwordx4 v31, s[36:39], s80 offen offset:2048 sc1 lds\n" \
"v_mfma_f32_16x16x32_bf16 v[36:39], a[100:103], v[96:99], v[36:39]\n" \
"s_waitcnt lgkmcnt(5)\n" \
"ds_read_b128 v[124:127], v30 offset:15360\n" \
"v_mfma_f32_16x16x32_bf16 v[36:39], a[108:111], v[100:103], v[36:39]\n" \
"ds_read_b64_tr_b16 a[112:113], v176 offset:32768\n" \
"v_mfma_f32_16x16x32_bf16 v[40:43], a[96:99], v[104:107], v[40:43]\n" \
"ds_read_b128 v[96:99], v29 offset:32768\n" \
"v_mfma_f32_16x16x32_bf16 v[40:43], a[104:107], v[108:111], v[40:43]\n" \
"buffer_load_dwordx4 v31, s[36:39], s80 offen offset:3072 sc1 lds\n" \
"v_mfma_f32_16x16x32_bf16 v[44:47], a[100:103], v[104:107], v[44:47]\n" \
"s_waitcnt lgkmcnt(4)\n" \
"ds_read_b128 v[100:103], v29 offset:34816\n" \
"s_add_u32 s80, s68, 49152\n" \
"v_mfma_f32_16x16x32_bf16 v[44:47], a[108:111], v[108:111], v[44:47]\n" \
"s_add_u32 m0, s22, 114688\n" \
"ds_read_b64_tr_b16 a[114:115], v177 offset:32768\n" \
"v_mfma_f32_16x16x32_bf16 v[48:51], a[96:99], v[112:115], v[48:51]\n" \
"ds_read_b128 v[104:107], v29 offset:33792\n" \
"v_mfma_f32_16x16x32_bf16 v[48:51], a[104:107], v[116:119], v[48:51]\n" \
"buffer_load_dwordx4 v31, s[36:39], s80 offen offset:0 sc1 lds\n" \
"v_mfma_f32_16x16x32_bf16 v[52:55], a[100:103], v[112:115], v[52:55]\n" \
"s_waitcnt lgkmcnt(5)\n" \
"ds_read_b128 v[108:111], v29 offset:35840\n" \
"v_mfma_f32_16x16x32_bf16 v[52:55], a[108:111], v[116:119], v[52:55]\n" \
"ds_read_b64_tr_b16 a[116:117], v178 offset:32768\n" \
"v_mfma_f32_16x16x32_bf16 v[56:59], a[96:99], v[120:123], v[56:59]\n" \
"ds_read_b128 v[112:115], v28 offset:8192\n" \
"v_mfma_f32_16x16x32_bf16 v[56:59], a[104:107], v[124:127], v[56:59]\n" \
"buffer_load_dwordx4 v31, s[36:39], s80 offen offset:1024 sc1 lds\n" \
"v_mfma_f32_16x16x32_bf16 v[60:63], a[100:103], v[120:123], v[60:63]\n" \
"ds_read_b128 v[116:119], v28 offset:10240\n" \
"v_mfma_f32_16x16x32_bf16 v[60:63], a[108:111], v[124:127], v[60:63]\n" \
"s_waitcnt lgkmcnt(6)\n" \
"s_barrier\n" \
"ds_read_b64_tr_b16 a[118:119], v179 offset:32768\n" \
"v_mfma_f32_16x16x32_bf16 v[64:67], a[96:99], v[96:99], v[64:67]\n" \
"ds_read_b128 v[120:123], v28 offset:9216\n" \
"v_mfma_f32_16x16x32_bf16 v[64:67], a[104:107], v[100:103], v[64:67]\n" \
"buffer_load_dwordx4 v31, s[36:39], s80 offen offset:2048 sc1 lds\n" \
"v_mfma_f32_16x16x32_bf16 v[68:71], a[100:103], v[96:99], v[68:71]\n" \
"s_waitcnt lgkmcnt(5)\n" \
"ds_read_b128 v[124:127], v28 offset:11264\n" \
"v_mfma_f32_16x16x32_bf16 v[68:71], a[108:111], v[100:103], v[68:71]\n" \
"ds_read_b64_tr_b16 a[120:121], v176 offset:40960\n" \
"v_mfma_f32_16x16x32_bf16 v[72:75], a[96:99], v[104:107], v[72:75]\n" \
"ds_read_b128 v[96:99], v29 offset:36864\n" \
"v_mfma_f32_16x16x32_bf16 v[72:75], a[104:107], v[108:111], v[72:75]\n" \
"buffer_load_dwordx4 v31, s[36:39], s80 offen offset:3072 sc1 lds\n" \
"v_mfma_f32_16x16x32_bf16 v[76:79], a[100:103], v[104:107], v[76:79]\n" \
"s_waitcnt lgkmcnt(5)\n" \
"ds_read_b128 v[100:103], v29 offset:38912\n" \
"s_add_u32 s80, s69, 0\n" \
"v_mfma_f32_16x16x32_bf16 v[76:79], a[108:111], v[108:111], v[76:79]\n" \
"s_add_u32 m0, s22, 147456\n" \
"ds_read_b64_tr_b16 a[122:123], v177 offset:40960\n" \
"v_mfma_f32_16x16x32_bf16 v[80:83], a[96:99], v[112:115], v[80:83]\n" \
"ds_read_b128 v[104:107], v29 offset:37888\n" \
"v_mfma_f32_16x16x32_bf16 v[80:83], a[104:107], v[116:119], v[80:83]\n" \
"buffer_load_dwordx4 v31, s[36:39], s80 offen offset:0 sc1 lds\n" \
"v_mfma_f32_16x16x32_bf16 v[84:87], a[100:103], v[112:115], v[84:87]\n" \
"s_waitcnt lgkmcnt(5)\n" \
"ds_read_b128 v[108:111], v29 offset:39936\n" \
"ds_read_b64_tr_b16 a[124:125], v178 offset:40960\n" \
"ds_read_b64_tr_b16 a[126:127], v179 offset:40960\n" \
"v_mfma_f32_16x16x32_bf16 v[84:87], a[108:111], v[116:119], v[84:87]\n" \
"v_mfma_f32_16x16x32_bf16 v[88:91], a[96:99], v[120:123], v[88:91]\n" \
"ds_read_b128 v[112:115], v29 offset:40960\n" \
"v_mfma_f32_16x16x32_bf16 v[88:91], a[104:107], v[124:127], v[88:91]\n" \
"buffer_load_dwordx4 v31, s[36:39], s80 offen offset:1024 sc1 lds\n" \
"v_mfma_f32_16x16x32_bf16 v[92:95], a[100:103], v[120:123], v[92:95]\n" \
"ds_read_b128 v[116:119], v29 offset:43008\n" \
"v_mfma_f32_16x16x32_bf16 v[92:95], a[108:111], v[124:127], v[92:95]\n" \
"s_waitcnt lgkmcnt(2)\n" \
"s_barrier\n" \
"v_mfma_f32_16x16x32_bf16 v[32:35], a[112:115], v[96:99], v[32:35]\n" \
"ds_read_b128 v[120:123], v29 offset:41984\n" \
"v_mfma_f32_16x16x32_bf16 v[32:35], a[120:123], v[100:103], v[32:35]\n" \
"buffer_load_dwordx2 v[176:177], v185, s[36:39], s86 offen offset:0 sc1\n" \
"v_mfma_f32_16x16x32_bf16 v[36:39], a[116:119], v[96:99], v[36:39]\n" \
"s_waitcnt lgkmcnt(5)\n" \
"ds_read_b128 v[124:127], v29 offset:44032\n" \
"v_mfma_f32_16x16x32_bf16 v[36:39], a[124:127], v[100:103], v[36:39]\n" \
"v_mfma_f32_16x16x32_bf16 v[40:43], a[112:115], v[104:107], v[40:43]\n" \
"ds_read_b128 v[96:99], v29 offset:45056\n" \
"v_mfma_f32_16x16x32_bf16 v[40:43], a[120:123], v[108:111], v[40:43]\n" \
"buffer_load_dwordx2 v[178:179], v185, s[36:39], s87 offen offset:0 sc1\n" \
"v_mfma_f32_16x16x32_bf16 v[44:47], a[116:119], v[104:107], v[44:47]\n" \
"s_waitcnt lgkmcnt(3)\n" \
"ds_read_b128 v[100:103], v29 offset:47104\n" \
"v_mfma_f32_16x16x32_bf16 v[44:47], a[124:127], v[108:111], v[44:47]\n" \
"v_mfma_f32_16x16x32_bf16 v[48:51], a[112:115], v[112:115], v[48:51]\n" \
"ds_read_b128 v[104:107], v29 offset:46080\n" \
"v_mfma_f32_16x16x32_bf16 v[48:51], a[120:123], v[116:119], v[48:51]\n" \
"buffer_load_dwordx4 v31, s[36:39], s80 offen offset:2048 sc1 lds\n" \
"v_mfma_f32_16x16x32_bf16 v[52:55], a[116:119], v[112:115], v[52:55]\n" \
"s_waitcnt lgkmcnt(3)\n" \
"ds_read_b128 v[108:111], v29 offset:48128\n" \
"v_mfma_f32_16x16x32_bf16 v[52:55], a[124:127], v[116:119], v[52:55]\n" \
"v_mfma_f32_16x16x32_bf16 v[56:59], a[112:115], v[120:123], v[56:59]\n" \
"ds_read_b128 v[112:115], v28 offset:12288\n" \
"v_mfma_f32_16x16x32_bf16 v[56:59], a[120:123], v[124:127], v[56:59]\n" \
"buffer_load_dwordx4 v31, s[36:39], s80 offen offset:3072 sc1 lds\n" \
"v_mfma_f32_16x16x32_bf16 v[60:63], a[116:119], v[120:123], v[60:63]\n" \
"s_add_u32 s80, s69, 16384\n" \
"ds_read_b128 v[116:119], v28 offset:14336\n" \
"s_add_u32 m0, s22, 16384\n" \
"v_mfma_f32_16x16x32_bf16 v[60:63], a[124:127], v[124:127], v[60:63]\n" \
"s_waitcnt vmcnt(18) lgkmcnt(4)\n" \
"s_barrier\n" \
"v_mfma_f32_16x16x32_bf16 v[64:67], a[112:115], v[96:99], v[64:67]\n" \
"ds_read_b128 v[120:123], v28 offset:13312\n" \
"v_mfma_f32_16x16x32_bf16 v[64:67], a[120:123], v[100:103], v[64:67]\n" \
"buffer_load_dwordx4 v31, s[36:39], s80 offen offset:0 sc1 lds\n" \
"v_mfma_f32_16x16x32_bf16 v[68:71], a[116:119], v[96:99], v[68:71]\n" \
"s_waitcnt lgkmcnt(3)\n" \
"ds_read_b128 v[124:127], v28 offset:15360\n" \
"v_mfma_f32_16x16x32_bf16 v[68:71], a[124:127], v[100:103], v[68:71]\n" \
"v_mfma_f32_16x16x32_bf16 v[72:75], a[112:115], v[104:107], v[72:75]\n" \
"ds_read_b128 v[96:99], v28 offset:49152\n" \
"v_mfma_f32_16x16x32_bf16 v[72:75], a[120:123], v[108:111], v[72:75]\n" \
"buffer_load_dwordx4 v31, s[36:39], s80 offen offset:1024 sc1 lds\n" \
"v_mfma_f32_16x16x32_bf16 v[76:79], a[116:119], v[104:107], v[76:79]\n" \
"s_waitcnt lgkmcnt(3)\n" \
"ds_read_b128 v[100:103], v28 offset:51200\n" \
"v_mfma_f32_16x16x32_bf16 v[76:79], a[124:127], v[108:111], v[76:79]\n" \
"v_mfma_f32_16x16x32_bf16 v[80:83], a[112:115], v[112:115], v[80:83]\n" \
"ds_read_b128 v[104:107], v28 offset:50176\n" \
"v_mfma_f32_16x16x32_bf16 v[80:83], a[120:123], v[116:119], v[80:83]\n" \
"buffer_load_dwordx4 v31, s[36:39], s80 offen offset:2048 sc1 lds\n" \
"v_mfma_f32_16x16x32_bf16 v[84:87], a[116:119], v[112:115], v[84:87]\n" \
"s_waitcnt lgkmcnt(3)\n" \
"ds_read_b128 v[108:111], v28 offset:52224\n" \
"v_mfma_f32_16x16x32_bf16 v[84:87], a[124:127], v[116:119], v[84:87]\n" \
"v_mfma_f32_16x16x32_bf16 v[88:91], a[112:115], v[120:123], v[88:91]\n" \
"ds_read_b128 v[112:115], v28 offset:53248\n" \
"v_mfma_f32_16x16x32_bf16 v[88:91], a[120:123], v[124:127], v[88:91]\n" \
"buffer_load_dwordx4 v31, s[36:39], s80 offen offset:3072 sc1 lds\n" \
"v_mfma_f32_16x16x32_bf16 v[92:95], a[116:119], v[120:123], v[92:95]\n" \
"s_add_u32 s80, s69, 32768\n" \
"ds_read_b128 v[116:119], v28 offset:55296\n" \
"s_add_u32 m0, s22, 131072\n" \
"v_mfma_f32_16x16x32_bf16 v[92:95], a[124:127], v[124:127], v[92:95]\n" \
"s_waitcnt lgkmcnt(4)\n" \
"s_barrier\n" \
"v_mfma_f32_16x16x32_bf16 v[192:195], a[160:163], v[96:99], 0\n" \
"ds_read_b128 v[120:123], v28 offset:54272\n" \
"v_mfma_f32_16x16x32_bf16 v[192:195], a[168:171], v[100:103], v[192:195]\n" \
"buffer_load_dwordx4 v31, s[36:39], s80 offen offset:0 sc1 lds\n" \
"v_mfma_f32_16x16x32_bf16 v[196:199], a[164:167], v[96:99], 0\n" \
"s_waitcnt lgkmcnt(3)\n" \
"ds_read_b128 v[124:127], v28 offset:56320\n" \
"v_cvt_pk_bf16_f32 v32, v32, v33\n" \
"v_mfma_f32_16x16x32_bf16 v[196:199], a[172:175], v[100:103], v[196:199]\n" \
"v_cvt_pk_bf16_f32 v33, v34, v35\n" \
"v_cvt_pk_bf16_f32 v34, v36, v37\n" \
"v_mfma_f32_16x16x32_bf16 v[200:203], a[160:163], v[104:107], 0\n" \
"ds_read_b128 v[96:99], v28 offset:57344\n" \
"v_cvt_pk_bf16_f32 v35, v38, v39\n" \
"v_mfma_f32_16x16x32_bf16 v[200:203], a[168:171], v[108:111], v[200:203]\n" \
"buffer_store_dwordx4 v[32:35], v4, s[36:39], s72 offen offset:0 sc1\n" \
"v_mfma_f32_16x16x32_bf16 v[204:207], a[164:167], v[104:107], 0\n" \
"s_waitcnt lgkmcnt(3)\n" \
"ds_read_b128 v[100:103], v28 offset:59392\n" \
"v_cvt_pk_bf16_f32 v40, v40, v41\n" \
"v_mfma_f32_16x16x32_bf16 v[204:207], a[172:175], v[108:111], v[204:207]\n" \
"v_cvt_pk_bf16_f32 v41, v42, v43\n" \
"v_cvt_pk_bf16_f32 v42, v44, v45\n" \
"v_mfma_f32_16x16x32_bf16 v[192:195], a[176:179], v[112:115], v[192:195]\n" \
"ds_read_b128 v[104:107], v28 offset:58368\n" \
"v_cvt_pk_bf16_f32 v43, v46, v47\n" \
"v_mfma_f32_16x16x32_bf16 v[192:195], a[184:187], v[116:119], v[192:195]\n" \
"buffer_load_dwordx4 v31, s[36:39], s80 offen offset:1024 sc1 lds\n" \
"v_mfma_f32_16x16x32_bf16 v[196:199], a[180:183], v[112:115], v[196:199]\n" \
"s_waitcnt lgkmcnt(3)\n" \
"ds_read_b128 v[108:111], v28 offset:60416\n" \
"v_mfma_f32_16x16x32_bf16 v[196:199], a[188:191], v[116:119], v[196:199]\n" \
"v_mfma_f32_16x16x32_bf16 v[200:203], a[176:179], v[120:123], v[200:203]\n" \
"ds_read_b128 v[112:115], v28 offset:61440\n" \
"v_mfma_f32_16x16x32_bf16 v[200:203], a[184:187], v[124:127], v[200:203]\n" \
"buffer_store_dwordx4 v[40:43], v4, s[36:39], s72 offen offset:1024 sc1\n" \
"v_mfma_f32_16x16x32_bf16 v[204:207], a[180:183], v[120:123], v[204:207]\n" \
"ds_read_b128 v[116:119], v28 offset:63488\n" \
"v_mfma_f32_16x16x32_bf16 v[204:207], a[188:191], v[124:127], v[204:207]\n" \
"s_waitcnt vmcnt(22) lgkmcnt(4)\n" \
"s_barrier\n" \
"v_mfma_f32_16x16x32_bf16 v[192:195], a[192:195], v[96:99], v[192:195]\n" \
"ds_read_b128 v[120:123], v28 offset:62464\n" \
"v_mfma_f32_16x16x32_bf16 v[192:195], a[200:203], v[100:103], v[192:195]\n" \
"v_mfma_f32_16x16x32_bf16 v[196:199], a[196:199], v[96:99], v[196:199]\n" \
"s_waitcnt lgkmcnt(3)\n" \
"ds_read_b128 v[124:127], v28 offset:64512\n" \
"v_mfma_f32_16x16x32_bf16 v[196:199], a[204:207], v[100:103], v[196:199]\n" \
"v_mfma_f32_16x16x32_bf16 v[200:203], a[192:195], v[104:107], v[200:203]\n" \
"ds_read_b128 v[96:99], v29 offset:0\n" \
"v_mfma_f32_16x16x32_bf16 v[200:203], a[200:203], v[108:111], v[200:203]\n" \
"buffer_load_dwordx4 v31, s[36:39], s80 offen offset:2048 sc1 lds\n" \
"v_mfma_f32_16x16x32_bf16 v[204:207], a[196:199], v[104:107], v[204:207]\n" \
"s_waitcnt lgkmcnt(3)\n" \
"ds_read_b128 v[100:103], v29 offset:2048\n" \
"v_mfma_f32_16x16x32_bf16 v[204:207], a[204:207], v[108:111], v[204:207]\n" \
"s_waitcnt vmcnt(11)\n" \
"v_mfma_f32_16x16x32_bf16 v[192:195], a[208:211], v[112:115], v[192:195]\n" \
"ds_read_b128 v[104:107], v29 offset:1024\n" \
"v_mfma_f32_16x16x32_bf16 v[192:195], a[216:219], v[116:119], v[192:195]\n" \
"buffer_load_dwordx4 v31, s[36:39], s80 offen offset:3072 sc1 lds\n" \
"v_mfma_f32_16x16x32_bf16 v[196:199], a[212:215], v[112:115], v[196:199]\n" \
"s_waitcnt lgkmcnt(3)\n" \
"ds_read_b128 v[108:111], v29 offset:3072\n" \
"s_add_u32 s80, s69, 49152\n" \
"v_mfma_f32_16x16x32_bf16 v[196:199], a[220:223], v[116:119], v[196:199]\n" \
"s_add_u32 m0, s22, 32768\n" \
"v_mfma_f32_16x16x32_bf16 v[200:203], a[208:211], v[120:123], v[200:203]\n" \
"ds_read_b128 v[112:115], v29 offset:4096\n" \
"v_mfma_f32_16x16x32_bf16 v[200:203], a[216:219], v[124:127], v[200:203]\n" \
"buffer_load_dwordx4 v31, s[36:39], s80 offen offset:0 sc1 lds\n" \
"v_mfma_f32_16x16x32_bf16 v[204:207], a[212:215], v[120:123], v[204:207]\n" \
"s_waitcnt lgkmcnt(3)\n" \
"ds_read_b128 v[116:119], v29 offset:6144\n" \
"v_mfma_f32_16x16x32_bf16 v[204:207], a[220:223], v[124:127], v[204:207]\n" \
"v_mfma_f32_16x16x32_bf16 v[192:195], a[224:227], v[96:99], v[192:195]\n" \
"ds_read_b128 v[120:123], v29 offset:5120\n" \
"v_mfma_f32_16x16x32_bf16 v[192:195], a[232:235], v[100:103], v[192:195]\n" \
"buffer_load_dwordx4 v31, s[36:39], s80 offen offset:1024 sc1 lds\n" \
"v_mfma_f32_16x16x32_bf16 v[196:199], a[228:231], v[96:99], v[196:199]\n" \
"s_waitcnt lgkmcnt(3)\n" \
"ds_read_b128 v[124:127], v29 offset:7168\n" \
"v_mfma_f32_16x16x32_bf16 v[196:199], a[236:239], v[100:103], v[196:199]\n" \
"v_mfma_f32_16x16x32_bf16 v[200:203], a[224:227], v[104:107], v[200:203]\n" \
"ds_read_b128 v[96:99], v29 offset:8192\n" \
"v_mfma_f32_16x16x32_bf16 v[200:203], a[232:235], v[108:111], v[200:203]\n" \
"buffer_load_dwordx4 v31, s[36:39], s80 offen offset:2048 sc1 lds\n" \
"v_mfma_f32_16x16x32_bf16 v[204:207], a[228:231], v[104:107], v[204:207]\n" \
"s_waitcnt lgkmcnt(3)\n" \
"ds_read_b128 v[100:103], v29 offset:10240\n" \
"v_cvt_pk_bf16_f32 v48, v48, v49\n" \
"v_mfma_f32_16x16x32_bf16 v[204:207], a[236:239], v[108:111], v[204:207]\n" \
"v_cvt_pk_bf16_f32 v49, v50, v51\n" \
"v_cvt_pk_bf16_f32 v50, v52, v53\n" \
"v_mfma_f32_16x16x32_bf16 v[192:195], a[240:243], v[112:115], v[192:195]\n" \
"ds_read_b128 v[104:107], v29 offset:9216\n" \
"v_cvt_pk_bf16_f32 v51, v54, v55\n" \
"v_mfma_f32_16x16x32_bf16 v[192:195], a[248:251], v[116:119], v[192:195]\n" \
"buffer_store_dwordx4 v[48:51], v4, s[36:39], s73 offen offset:0 sc1\n" \
"v_mfma_f32_16x16x32_bf16 v[196:199], a[244:247], v[112:115], v[196:199]\n" \
"s_waitcnt lgkmcnt(3)\n" \
"ds_read_b128 v[108:111], v29 offset:11264\n" \
"v_cvt_pk_bf16_f32 v56, v56, v57\n" \
"v_mfma_f32_16x16x32_bf16 v[196:199], a[252:255], v[116:119], v[196:199]\n" \
"v_cvt_pk_bf16_f32 v57, v58, v59\n" \
"v_cvt_pk_bf16_f32 v58, v60, v61\n" \
"v_mfma_f32_16x16x32_bf16 v[200:203], a[240:243], v[120:123], v[200:203]\n" \
"ds_read_b128 v[112:115], v29 offset:12288\n" \
"v_cvt_pk_bf16_f32 v59, v62, v63\n" \
"v_mfma_f32_16x16x32_bf16 v[200:203], a[248:251], v[124:127], v[200:203]\n" \
"v_mfma_f32_16x16x32_bf16 v[204:207], a[244:247], v[120:123], v[204:207]\n" \
"ds_read_b128 v[116:119], v29 offset:14336\n" \
"v_mfma_f32_16x16x32_bf16 v[204:207], a[252:255], v[124:127], v[204:207]\n" \
"s_waitcnt vmcnt(24) lgkmcnt(4)\n" \
"s_barrier\n" \
"v_mfma_f32_16x16x32_bf16 v[192:195], a[64:67], v[96:99], v[192:195]\n" \
"ds_read_b128 v[120:123], v29 offset:13312\n" \
"v_mfma_f32_16x16x32_bf16 v[192:195], a[72:75], v[100:103], v[192:195]\n" \
"buffer_store_dwordx4 v[56:59], v4, s[36:39], s73 offen offset:1024 sc1\n" \
"v_mfma_f32_16x16x32_bf16 v[196:199], a[68:71], v[96:99], v[196:199]\n" \
"s_waitcnt lgkmcnt(3)\n" \
"ds_read_b128 v[124:127], v29 offset:15360\n" \
"v_permlane16_swap_b32 v144, v146\n" \
"v_mfma_f32_16x16x32_bf16 v[196:199], a[76:79], v[100:103], v[196:199]\n" \
"v_permlane16_swap_b32 v145, v147\n" \
"v_sub_f32 v5, 1.0, v176\n" \
"v_mfma_f32_16x16x32_bf16 v[200:203], a[64:67], v[104:107], v[200:203]\n" \
"ds_read_b128 v[96:99], v29 offset:16384\n" \
"v_cvt_pk_bf16_f32 v5, v5, v5\n" \
"v_mfma_f32_16x16x32_bf16 v[200:203], a[72:75], v[108:111], v[200:203]\n" \
"v_and_b32 v5, 0xffff0000, v5\n" \
"v_lshlrev_b32 v32, 16, v144\n" \
"v_mfma_f32_16x16x32_bf16 v[204:207], a[68:71], v[104:107], v[204:207]\n" \
"s_waitcnt lgkmcnt(3)\n" \
"ds_read_b128 v[100:103], v29 offset:18432\n" \
"v_and_b32 v33, 0xffff0000, v144\n" \
"v_mfma_f32_16x16x32_bf16 v[204:207], a[76:79], v[108:111], v[204:207]\n" \
"v_lshlrev_b32 v188, 16, v128\n" \
"v_and_b32 v189, 0xffff0000, v128\n" \
"v_mfma_f32_16x16x32_bf16 v[192:195], a[80:83], v[112:115], v[192:195]\n" \
"ds_read_b128 v[104:107], v29 offset:17408\n" \
"v_mul_f32 v32, v176, v32\n" \
"v_mfma_f32_16x16x32_bf16 v[192:195], a[88:91], v[116:119], v[192:195]\n" \
"buffer_load_dwordx4 v31, s[36:39], s80 offen offset:3072 sc1 lds\n" \
"v_mfma_f32_16x16x32_bf16 v[196:199], a[84:87], v[112:115], v[196:199]\n" \
"s_waitcnt lgkmcnt(3)\n" \
"ds_read_b128 v[108:111], v29 offset:19456\n" \
"s_add_u32 s80, s70, 0\n" \
"v_mfma_f32_16x16x32_bf16 v[196:199], a[92:95], v[116:119], v[196:199]\n" \
"s_add_u32 m0, s22, 0\n" \
"v_mul_f32 v33, v176, v33\n" \
"v_mul_f32 v32, v188, v32\n" \
"v_mfma_f32_16x16x32_bf16 v[200:203], a[80:83], v[120:123], v[200:203]\n" \
"ds_read_b128 v[112:115], v29 offset:20480\n" \
"v_mul_f32 v33, v189, v33\n" \
"v_mfma_f32_16x16x32_bf16 v[200:203], a[88:91], v[124:127], v[200:203]\n" \
"buffer_load_dwordx4 v31, s[36:39], s80 offen offset:0 sc1 lds\n" \
"v_mfma_f32_16x16x32_bf16 v[204:207], a[84:87], v[120:123], v[204:207]\n" \
"s_waitcnt lgkmcnt(3)\n" \
"ds_read_b128 v[116:119], v29 offset:22528\n" \
"v_lshlrev_b32 v34, 16, v145\n" \
"v_mfma_f32_16x16x32_bf16 v[204:207], a[92:95], v[124:127], v[204:207]\n" \
"v_and_b32 v35, 0xffff0000, v145\n" \
"v_lshlrev_b32 v188, 16, v129\n" \
"v_mfma_f32_16x16x32_bf16 v[192:195], a[128:131], v[96:99], v[192:195]\n" \
"ds_read_b128 v[120:123], v29 offset:21504\n" \
"v_and_b32 v189, 0xffff0000, v129\n" \
"v_mfma_f32_16x16x32_bf16 v[192:195], a[136:139], v[100:103], v[192:195]\n" \
"buffer_load_dwordx4 v31, s[36:39], s80 offen offset:1024 sc1 lds\n" \
"v_mfma_f32_16x16x32_bf16 v[196:199], a[132:135], v[96:99], v[196:199]\n" \
"s_waitcnt lgkmcnt(3)\n" \
"ds_read_b128 v[124:127], v29 offset:23552\n" \
"v_mul_f32 v34, v176, v34\n" \
"v_mfma_f32_16x16x32_bf16 v[196:199], a[140:143], v[100:103], v[196:199]\n" \
"v_mul_f32 v35, v176, v35\n" \
"v_mul_f32 v34, v188, v34\n" \
"v_mfma_f32_16x16x32_bf16 v[200:203], a[128:131], v[104:107], v[200:203]\n" \
"ds_read_b128 v[96:99], v29 offset:24576\n" \
"v_mul_f32 v35, v189, v35\n" \
"v_mfma_f32_16x16x32_bf16 v[200:203], a[136:139], v[108:111], v[200:203]\n" \
"buffer_load_dwordx4 v31, s[36:39], s80 offen offset:2048 sc1 lds\n" \
"v_mfma_f32_16x16x32_bf16 v[204:207], a[132:135], v[104:107], v[204:207]\n" \
"s_waitcnt lgkmcnt(3)\n" \
"ds_read_b128 v[100:103], v29 offset:26624\n" \
"v_lshlrev_b32 v36, 16, v146\n" \
"v_mfma_f32_16x16x32_bf16 v[204:207], a[140:143], v[108:111], v[204:207]\n" \
"v_and_b32 v37, 0xffff0000, v146\n" \
"v_lshlrev_b32 v188, 16, v132\n" \
"v_mfma_f32_16x16x32_bf16 v[192:195], a[144:147], v[112:115], v[192:195]\n" \
"ds_read_b128 v[104:107], v29 offset:25600\n" \
"v_and_b32 v189, 0xffff0000, v132\n" \
"v_mfma_f32_16x16x32_bf16 v[192:195], a[152:155], v[116:119], v[192:195]\n" \
"buffer_load_dwordx4 v31, s[36:39], s80 offen offset:3072 sc1 lds\n" \
"v_mfma_f32_16x16x32_bf16 v[196:199], a[148:151], v[112:115], v[196:199]\n" \
"s_waitcnt lgkmcnt(3)\n" \
"ds_read_b128 v[108:111], v29 offset:27648\n" \
"s_add_u32 s80, s70, 16384\n" \
"v_mfma_f32_16x16x32_bf16 v[196:199], a[156:159], v[116:119], v[196:199]\n" \
"s_add_u32 m0, s22, 98304\n" \
"s_cmp_eq_u32 s89, 3\ns_cbranch_scc1 LSW_%=\nLSWR_%=:\n" \
"v_mfma_f32_16x16x32_bf16 v[200:203], a[144:147], v[120:123], v[200:203]\n" \
"ds_read_b128 v[112:115], v29 offset:28672\n" \
"v_cvt_pk_bf16_f32 v80, v80, v81\n" \
"v_mfma_f32_16x16x32_bf16 v[200:203], a[152:155], v[124:127], v[200:203]\n" \
"buffer_load_dwordx4 v31, s[36:39], s80 offen offset:0 sc1 lds\n" \
"v_mfma_f32_16x16x32_bf16 v[204:207], a[148:151], v[120:123], v[204:207]\n" \
"ds_read_b128 v[116:119], v29 offset:30720\n" \
"v_cvt_pk_bf16_f32 v81, v82, v83\n" \
"v_mfma_f32_16x16x32_bf16 v[204:207], a[156:159], v[124:127], v[204:207]\n" \
"s_waitcnt vmcnt(27) lgkmcnt(4)\n" \
"s_barrier\n" \
"v_cvt_pk_bf16_f32 v82, v84, v85\n" \
"v_mfma_f32_16x16x32_bf16 v[192:195], a[0:3], v[96:99], v[192:195]\n" \
"ds_read_b128 v[120:123], v29 offset:29696\n" \
"v_cvt_pk_bf16_f32 v83, v86, v87\n" \
"v_mfma_f32_16x16x32_bf16 v[192:195], a[8:11], v[100:103], v[192:195]\n" \
"buffer_store_dwordx4 v[80:83], v4, s[36:39], s74 offen offset:0 sc1\n" \
"v_mfma_f32_16x16x32_bf16 v[196:199], a[4:7], v[96:99], v[196:199]\n" \
"s_waitcnt lgkmcnt(3)\n" \
"ds_read_b128 v[124:127], v29 offset:31744\n" \
"v_cvt_pk_bf16_f32 v88, v88, v89\n" \
"v_mfma_f32_16x16x32_bf16 v[196:199], a[12:15], v[100:103], v[196:199]\n" \
"s_waitcnt vmcnt(14)\n" \
"v_cvt_pk_bf16_f32 v89, v90, v91\n" \
"v_cvt_pk_bf16_f32 v90, v92, v93\n" \
"v_mfma_f32_16x16x32_bf16 v[200:203], a[0:3], v[104:107], v[200:203]\n" \
"LH28_%=:\ns_cbranch_execz LHR2_%=\n" \
"ds_read_b128 v[96:99], v29 offset:49152\n" \
"v_cvt_pk_bf16_f32 v91, v94, v95\n" \
"v_mfma_f32_16x16x32_bf16 v[200:203], a[8:11], v[108:111], v[200:203]\n" \
"buffer_store_dwordx2 v[10:11], v20, s[36:39], s23 offen sc1\n" \
"v_mfma_f32_16x16x32_bf16 v[204:207], a[4:7], v[104:107], v[204:207]\n" \
"s_waitcnt lgkmcnt(3)\n" \
"ds_read_b128 v[100:103], v29 offset:51200\n" \
"v_mul_f32 v36, v176, v36\n" \
"v_mfma_f32_16x16x32_bf16 v[204:207], a[12:15], v[108:111], v[204:207]\n" \
"v_mul_f32 v37, v176, v37\n" \
"v_mul_f32 v36, v188, v36\n" \
"v_mfma_f32_16x16x32_bf16 v[192:195], a[16:19], v[112:115], v[192:195]\n" \
"ds_read_b128 v[104:107], v29 offset:50176\n" \
"v_mul_f32 v37, v189, v37\n" \
"v_mfma_f32_16x16x32_bf16 v[192:195], a[24:27], v[116:119], v[192:195]\n" \
"v_lshlrev_b32 v38, 16, v147\n" \
"v_and_b32 v39, 0xffff0000, v147\n" \
"v_mfma_f32_16x16x32_bf16 v[196:199], a[20:23], v[112:115], v[196:199]\n" \
"s_waitcnt lgkmcnt(3)\n" \
"ds_read_b128 v[108:111], v29 offset:52224\n" \
"v_lshlrev_b32 v188, 16, v133\n" \
"v_mfma_f32_16x16x32_bf16 v[196:199], a[28:31], v[116:119], v[196:199]\n" \
"buffer_store_dwordx4 v[88:91], v4, s[36:39], s74 offen offset:1024 sc1\n" \
"v_mfma_f32_16x16x32_bf16 v[200:203], a[16:19], v[120:123], v[200:203]\n" \
"ds_read_b128 v[112:115], v29 offset:53248\n" \
"v_and_b32 v189, 0xffff0000, v133\n" \
"v_mfma_f32_16x16x32_bf16 v[200:203], a[24:27], v[124:127], v[200:203]\n" \
"v_mul_f32 v38, v176, v38\n" \
"v_mul_f32 v39, v176, v39\n" \
"v_mfma_f32_16x16x32_bf16 v[204:207], a[20:23], v[120:123], v[204:207]\n" \
"s_waitcnt lgkmcnt(3)\n" \
"ds_read_b128 v[116:119], v29 offset:55296\n" \
"v_mul_f32 v38, v188, v38\n" \
"v_mfma_f32_16x16x32_bf16 v[204:207], a[28:31], v[124:127], v[204:207]\n" \
"v_mul_f32 v39, v189, v39\n" \
"v_permlane16_swap_b32 v148, v150\n" \
"v_mfma_f32_16x16x32_bf16 v[192:195], a[32:35], v[96:99], v[192:195]\n" \
"ds_read_b128 v[120:123], v29 offset:54272\n" \
"v_permlane16_swap_b32 v149, v151\n" \
"v_mfma_f32_16x16x32_bf16 v[192:195], a[40:43], v[100:103], v[192:195]\n" \
"v_sub_f32 v21, 1.0, v177\n" \
"v_cvt_pk_bf16_f32 v21, v21, v21\n" \
"v_mfma_f32_16x16x32_bf16 v[196:199], a[36:39], v[96:99], v[196:199]\n" \
"s_waitcnt lgkmcnt(3)\n" \
"ds_read_b128 v[124:127], v29 offset:56320\n" \
"v_and_b32 v21, 0xffff0000, v21\n" \
"v_mfma_f32_16x16x32_bf16 v[196:199], a[44:47], v[100:103], v[196:199]\n" \
"buffer_load_dwordx4 v31, s[36:39], s80 offen offset:1024 sc1 lds\n" \
"v_mfma_f32_16x16x32_bf16 v[200:203], a[32:35], v[104:107], v[200:203]\n" \
"ds_read_b128 v[96:99], v29 offset:57344\n" \
"v_lshlrev_b32 v40, 16, v148\n" \
"v_mfma_f32_16x16x32_bf16 v[200:203], a[40:43], v[108:111], v[200:203]\n" \
"v_and_b32 v41, 0xffff0000, v148\n" \
"v_lshlrev_b32 v188, 16, v130\n" \
"v_mfma_f32_16x16x32_bf16 v[204:207], a[36:39], v[104:107], v[204:207]\n" \
"s_waitcnt lgkmcnt(3)\n" \
"ds_read_b128 v[100:103], v29 offset:59392\n" \
"v_and_b32 v189, 0xffff0000, v130\n" \
"v_mfma_f32_16x16x32_bf16 v[204:207], a[44:47], v[108:111], v[204:207]\n" \
"buffer_load_dwordx4 v31, s[36:39], s80 offen offset:2048 sc1 lds\n" \
"v_mfma_f32_16x16x32_bf16 v[192:195], a[48:51], v[112:115], v[192:195]\n" \
"ds_read_b128 v[104:107], v29 offset:58368\n" \
"v_mul_f32 v40, v177, v40\n" \
"v_mfma_f32_16x16x32_bf16 v[192:195], a[56:59], v[116:119], v[192:195]\n" \
"v_mul_f32 v41, v177, v41\n" \
"v_mul_f32 v40, v188, v40\n" \
"v_mfma_f32_16x16x32_bf16 v[196:199], a[52:55], v[112:115], v[196:199]\n" \
"s_waitcnt lgkmcnt(3)\n" \
"ds_read_b128 v[108:111], v29 offset:60416\n" \
"v_mul_f32 v41, v189, v41\n" \
"v_mfma_f32_16x16x32_bf16 v[196:199], a[60:63], v[116:119], v[196:199]\n" \
"buffer_load_dwordx4 v31, s[36:39], s80 offen offset:3072 sc1 lds\n" \
"v_mfma_f32_16x16x32_bf16 v[200:203], a[48:51], v[120:123], v[200:203]\n" \
"s_add_u32 s80, s70, 32768\n" \
"ds_read_b128 v[112:115], v29 offset:61440\n" \
"s_add_u32 m0, s22, 49152\n" \
"v_mfma_f32_16x16x32_bf16 v[200:203], a[56:59], v[124:127], v[200:203]\n" \
"v_lshlrev_b32 v42, 16, v149\n" \
"v_and_b32 v43, 0xffff0000, v149\n" \
"v_mfma_f32_16x16x32_bf16 v[204:207], a[52:55], v[120:123], v[204:207]\n" \
"ds_read_b128 v[116:119], v29 offset:63488\n" \
"v_lshlrev_b32 v188, 16, v131\n" \
"v_mfma_f32_16x16x32_bf16 v[204:207], a[60:63], v[124:127], v[204:207]\n" \
"s_waitcnt vmcnt(27) lgkmcnt(4)\n" \
"s_barrier\n" \
"v_and_b32 v189, 0xffff0000, v131\n" \
"v_mfma_f32_16x16x32_bf16 v[192:195], a[96:99], v[96:99], v[192:195]\n" \
"ds_read_b128 v[120:123], v29 offset:62464\n" \
"v_mul_f32 v42, v177, v42\n" \
"v_mfma_f32_16x16x32_bf16 v[192:195], a[104:107], v[100:103], v[192:195]\n" \
"buffer_load_dwordx4 v31, s[36:39], s80 offen offset:0 sc1 lds\n" \
"v_mfma_f32_16x16x32_bf16 v[196:199], a[100:103], v[96:99], v[196:199]\n" \
"s_waitcnt lgkmcnt(3)\n" \
"ds_read_b128 v[124:127], v29 offset:64512\n" \
"v_mul_f32 v43, v177, v43\n" \
"v_mfma_f32_16x16x32_bf16 v[196:199], a[108:111], v[100:103], v[196:199]\n" \
"v_mul_f32 v42, v188, v42\n" \
"v_mul_f32 v43, v189, v43\n" \
"v_mfma_f32_16x16x32_bf16 v[200:203], a[96:99], v[104:107], v[200:203]\n" \
"ds_read_b128 v[96:99], v30 offset:16384\n" \
"v_lshlrev_b32 v44, 16, v150\n" \
"v_mfma_f32_16x16x32_bf16 v[200:203], a[104:107], v[108:111], v[200:203]\n" \
"buffer_load_dwordx4 v31, s[36:39], s80 offen offset:1024 sc1 lds\n" \
"v_mfma_f32_16x16x32_bf16 v[204:207], a[100:103], v[104:107], v[204:207]\n" \
"s_waitcnt lgkmcnt(3)\n" \
"ds_read_b128 v[100:103], v30 offset:18432\n" \
"v_and_b32 v45, 0xffff0000, v150\n" \
"v_mfma_f32_16x16x32_bf16 v[204:207], a[108:111], v[108:111], v[204:207]\n" \
"s_waitcnt vmcnt(14)\n" \
"v_lshlrev_b32 v188, 16, v134\n" \
"v_and_b32 v189, 0xffff0000, v134\n" \
"v_mfma_f32_16x16x32_bf16 v[192:195], a[112:115], v[112:115], v[192:195]\n" \
"ds_read_b128 v[104:107], v30 offset:17408\n" \
"v_mul_f32 v44, v177, v44\n" \
"v_mfma_f32_16x16x32_bf16 v[192:195], a[120:123], v[116:119], v[192:195]\n" \
"buffer_store_dwordx2 v[10:11], v20, s[36:39], s24 offen sc1\n" \
"v_mfma_f32_16x16x32_bf16 v[196:199], a[116:119], v[112:115], v[196:199]\n" \
"s_waitcnt lgkmcnt(3)\n" \
"ds_read_b128 v[108:111], v30 offset:19456\n" \
"v_mul_f32 v45, v177, v45\n" \
"v_mfma_f32_16x16x32_bf16 v[196:199], a[124:127], v[116:119], v[196:199]\n" \
"buffer_load_dwordx4 v31, s[36:39], s80 offen offset:2048 sc1 lds\n" \
"v_mfma_f32_16x16x32_bf16 v[200:203], a[112:115], v[120:123], v[200:203]\n" \
"ds_read_b128 v[112:115], v30 offset:20480\n" \
"v_mul_f32 v44, v188, v44\n" \
"v_mfma_f32_16x16x32_bf16 v[200:203], a[120:123], v[124:127], v[200:203]\n" \
"v_mul_f32 v45, v189, v45\n" \
"v_lshlrev_b32 v46, 16, v151\n" \
"v_mfma_f32_16x16x32_bf16 v[204:207], a[116:119], v[120:123], v[204:207]\n" \
"s_waitcnt lgkmcnt(3)\n" \
"ds_read_b128 v[116:119], v30 offset:22528\n" \
"v_and_b32 v47, 0xffff0000, v151\n" \
"v_mfma_f32_16x16x32_bf16 v[204:207], a[124:127], v[124:127], v[204:207]\n" \
"buffer_load_dwordx4 v31, s[36:39], s80 offen offset:3072 sc1 lds\n" \
"v_mfma_f32_16x16x32_bf16 v[208:211], a[160:163], v[96:99], 0\n" \
"s_add_u32 s80, s70, 49152\n" \
"ds_read_b128 v[120:123], v30 offset:21504\n" \
"s_add_u32 m0, s22, 65536\n" \
"v_mfma_f32_16x16x32_bf16 v[208:211], a[168:171], v[100:103], v[208:211]\n" \
"v_lshlrev_b32 v188, 16, v135\n" \
"v_and_b32 v189, 0xffff0000, v135\n" \
"v_mfma_f32_16x16x32_bf16 v[212:215], a[164:167], v[96:99], 0\n" \
"s_waitcnt lgkmcnt(3)\n" \
"ds_read_b128 v[124:127], v30 offset:23552\n" \
"v_cvt_pk_bf16_f32 v192, v192, v193\n" \
"v_mfma_f32_16x16x32_bf16 v[212:215], a[172:175], v[100:103], v[212:215]\n" \
"v_cvt_pk_bf16_f32 v193, v194, v195\n" \
"v_cvt_pk_bf16_f32 v194, v196, v197\n" \
"v_mfma_f32_16x16x32_bf16 v[216:219], a[160:163], v[104:107], 0\n" \
"ds_read_b128 v[96:99], v30 offset:24576\n" \
"v_cvt_pk_bf16_f32 v195, v198, v199\n" \
"v_mfma_f32_16x16x32_bf16 v[216:219], a[168:171], v[108:111], v[216:219]\n" \
"buffer_store_dwordx4 v[192:195], v4, s[36:39], s75 offen offset:0 sc1\n" \
"v_mfma_f32_16x16x32_bf16 v[220:223], a[164:167], v[104:107], 0\n" \
"s_waitcnt lgkmcnt(3)\n" \
"ds_read_b128 v[100:103], v30 offset:26624\n" \
"v_cvt_pk_bf16_f32 v200, v200, v201\n" \
"v_mfma_f32_16x16x32_bf16 v[220:223], a[172:175], v[108:111], v[220:223]\n" \
"v_cvt_pk_bf16_f32 v201, v202, v203\n" \
"v_cvt_pk_bf16_f32 v202, v204, v205\n" \
"v_mfma_f32_16x16x32_bf16 v[208:211], a[176:179], v[112:115], v[208:211]\n" \
"ds_read_b128 v[104:107], v30 offset:25600\n" \
"v_cvt_pk_bf16_f32 v203, v206, v207\n" \
"v_mfma_f32_16x16x32_bf16 v[208:211], a[184:187], v[116:119], v[208:211]\n" \
"buffer_load_dwordx4 v31, s[36:39], s80 offen offset:0 sc1 lds\n" \
"v_mfma_f32_16x16x32_bf16 v[212:215], a[180:183], v[112:115], v[212:215]\n" \
"s_waitcnt lgkmcnt(3)\n" \
"ds_read_b128 v[108:111], v30 offset:27648\n" \
"v_mul_f32 v46, v177, v46\n" \
"v_mfma_f32_16x16x32_bf16 v[212:215], a[188:191], v[116:119], v[212:215]\n" \
"v_mul_f32 v47, v177, v47\n" \
"v_mul_f32 v46, v188, v46\n" \
"v_mfma_f32_16x16x32_bf16 v[216:219], a[176:179], v[120:123], v[216:219]\n" \
"ds_read_b128 v[112:115], v30 offset:28672\n" \
"v_mul_f32 v47, v189, v47\n" \
"v_mfma_f32_16x16x32_bf16 v[216:219], a[184:187], v[124:127], v[216:219]\n" \
"buffer_store_dwordx4 v[200:203], v4, s[36:39], s75 offen offset:1024 sc1\n" \
"v_mfma_f32_16x16x32_bf16 v[220:223], a[180:183], v[120:123], v[220:223]\n" \
"ds_read_b128 v[116:119], v30 offset:30720\n" \
"v_permlane16_swap_b32 v152, v154\n" \
"v_mfma_f32_16x16x32_bf16 v[220:223], a[188:191], v[124:127], v[220:223]\n" \
"s_waitcnt vmcnt(31) lgkmcnt(4)\n" \
"s_barrier\n" \
"v_permlane16_swap_b32 v153, v155\n" \
"v_mfma_f32_16x16x32_bf16 v[208:211], a[192:195], v[96:99], v[208:211]\n" \
"ds_read_b128 v[120:123], v30 offset:29696\n" \
"v_sub_f32 v6, 1.0, v178\n" \
"v_mfma_f32_16x16x32_bf16 v[208:211], a[200:203], v[100:103], v[208:211]\n" \
"v_cvt_pk_bf16_f32 v6, v6, v6\n" \
"v_and_b32 v6, 0xffff0000, v6\n" \
"v_mfma_f32_16x16x32_bf16 v[212:215], a[196:199], v[96:99], v[212:215]\n" \
"s_waitcnt lgkmcnt(3)\n" \
"ds_read_b128 v[124:127], v30 offset:31744\n" \
"v_lshlrev_b32 v48, 16, v152\n" \
"v_mfma_f32_16x16x32_bf16 v[212:215], a[204:207], v[100:103], v[212:215]\n" \
"v_and_b32 v49, 0xffff0000, v152\n" \
"v_lshlrev_b32 v188, 16, v136\n" \
"v_mfma_f32_16x16x32_bf16 v[216:219], a[192:195], v[104:107], v[216:219]\n" \
"ds_read_b128 v[96:99], v28 offset:16384\n" \
"v_and_b32 v189, 0xffff0000, v136\n" \
"v_mfma_f32_16x16x32_bf16 v[216:219], a[200:203], v[108:111], v[216:219]\n" \
"buffer_load_dwordx4 v31, s[36:39], s80 offen offset:1024 sc1 lds\n" \
"v_mfma_f32_16x16x32_bf16 v[220:223], a[196:199], v[104:107], v[220:223]\n" \
"s_waitcnt lgkmcnt(3)\n" \
"ds_read_b128 v[100:103], v28 offset:18432\n" \
"v_mul_f32 v48, v178, v48\n" \
"v_mfma_f32_16x16x32_bf16 v[220:223], a[204:207], v[108:111], v[220:223]\n" \
"v_mul_f32 v49, v178, v49\n" \
"v_mul_f32 v48, v188, v48\n" \
"v_mfma_f32_16x16x32_bf16 v[208:211], a[208:211], v[112:115], v[208:211]\n" \
"ds_read_b128 v[104:107], v28 offset:17408\n" \
"v_mul_f32 v49, v189, v49\n" \
"v_mfma_f32_16x16x32_bf16 v[208:211], a[216:219], v[116:119], v[208:211]\n" \
"buffer_load_dwordx4 v31, s[36:39], s80 offen offset:2048 sc1 lds\n" \
"v_mfma_f32_16x16x32_bf16 v[212:215], a[212:215], v[112:115], v[212:215]\n" \
"s_waitcnt lgkmcnt(3)\n" \
"ds_read_b128 v[108:111], v28 offset:19456\n" \
"v_lshlrev_b32 v50, 16, v153\n" \
"v_mfma_f32_16x16x32_bf16 v[212:215], a[220:223], v[116:119], v[212:215]\n" \
"v_and_b32 v51, 0xffff0000, v153\n" \
"v_lshlrev_b32 v188, 16, v137\n" \
"v_mfma_f32_16x16x32_bf16 v[216:219], a[208:211], v[120:123], v[216:219]\n" \
"ds_read_b128 v[112:115], v28 offset:20480\n" \
"v_and_b32 v189, 0xffff0000, v137\n" \
"v_mfma_f32_16x16x32_bf16 v[216:219], a[216:219], v[124:127], v[216:219]\n" \
"buffer_load_dwordx4 v31, s[36:39], s80 offen offset:3072 sc1 lds\n" \
"v_mfma_f32_16x16x32_bf16 v[220:223], a[212:215], v[120:123], v[220:223]\n" \
"s_waitcnt lgkmcnt(3)\n" \
"ds_read_b128 v[116:119], v28 offset:22528\n" \
"v_mul_f32 v50, v178, v50\n" \
"v_mfma_f32_16x16x32_bf16 v[220:223], a[220:223], v[124:127], v[220:223]\n" \
"s_add_u32 s80, s71, 0\n" \
"s_add_u32 m0, s22, 81920\n" \
"v_mul_f32 v51, v178, v51\n" \
"v_mfma_f32_16x16x32_bf16 v[208:211], a[224:227], v[96:99], v[208:211]\n" \
"ds_read_b128 v[120:123], v28 offset:21504\n" \
"v_mul_f32 v50, v188, v50\n" \
"v_mfma_f32_16x16x32_bf16 v[208:211], a[232:235], v[100:103], v[208:211]\n" \
"s_waitcnt vmcnt(14)\n" \
"v_mul_f32 v51, v189, v51\n" \
"v_lshlrev_b32 v52, 16, v154\n" \
"v_mfma_f32_16x16x32_bf16 v[212:215], a[228:231], v[96:99], v[212:215]\n" \
"s_waitcnt lgkmcnt(3)\n" \
"ds_read_b128 v[124:127], v28 offset:23552\n" \
"v_and_b32 v53, 0xffff0000, v154\n" \
"v_mfma_f32_16x16x32_bf16 v[212:215], a[236:239], v[100:103], v[212:215]\n" \
"buffer_store_dwordx2 v[10:11], v20, s[36:39], s45 offen sc1\n" \
"v_mfma_f32_16x16x32_bf16 v[216:219], a[224:227], v[104:107], v[216:219]\n" \
"ds_read_b128 v[96:99], v28 offset:24576\n" \
"v_lshlrev_b32 v188, 16, v140\n" \
"v_mfma_f32_16x16x32_bf16 v[216:219], a[232:235], v[108:111], v[216:219]\n" \
"v_and_b32 v189, 0xffff0000, v140\n" \
"v_mul_f32 v52, v178, v52\n" \
"v_mfma_f32_16x16x32_bf16 v[220:223], a[228:231], v[104:107], v[220:223]\n" \
"s_waitcnt lgkmcnt(3)\n" \
"ds_read_b128 v[100:103], v28 offset:26624\n" \
"v_mul_f32 v53, v178, v53\n" \
"v_mfma_f32_16x16x32_bf16 v[220:223], a[236:239], v[108:111], v[220:223]\n" \
"buffer_load_dwordx4 v31, s[36:39], s80 offen offset:0 sc1 lds\n" \
"v_mfma_f32_16x16x32_bf16 v[208:211], a[240:243], v[112:115], v[208:211]\n" \
"ds_read_b128 v[104:107], v28 offset:25600\n" \
"v_mul_f32 v52, v188, v52\n" \
"v_mfma_f32_16x16x32_bf16 v[208:211], a[248:251], v[116:119], v[208:211]\n" \
"v_mul_f32 v53, v189, v53\n" \
"v_lshlrev_b32 v54, 16, v155\n" \
"v_mfma_f32_16x16x32_bf16 v[212:215], a[244:247], v[112:115], v[212:215]\n" \
"s_waitcnt lgkmcnt(3)\n" \
"ds_read_b128 v[108:111], v28 offset:27648\n" \
"v_and_b32 v55, 0xffff0000, v155\n" \
"v_mfma_f32_16x16x32_bf16 v[212:215], a[252:255], v[116:119], v[212:215]\n" \
"buffer_load_dwordx4 v31, s[36:39], s80 offen offset:1024 sc1 lds\n" \
"v_mfma_f32_16x16x32_bf16 v[216:219], a[240:243], v[120:123], v[216:219]\n" \
"ds_read_b128 v[112:115], v28 offset:28672\n" \
"v_lshlrev_b32 v188, 16, v141\n" \
"v_mfma_f32_16x16x32_bf16 v[216:219], a[248:251], v[124:127], v[216:219]\n" \
"v_and_b32 v189, 0xffff0000, v141\n" \
"v_mul_f32 v54, v178, v54\n" \
"v_mfma_f32_16x16x32_bf16 v[220:223], a[244:247], v[120:123], v[220:223]\n" \
"ds_read_b128 v[116:119], v28 offset:30720\n" \
"v_mul_f32 v55, v178, v55\n" \
"v_mfma_f32_16x16x32_bf16 v[220:223], a[252:255], v[124:127], v[220:223]\n" \
"s_waitcnt vmcnt(31) lgkmcnt(4)\n" \
"s_barrier\n" \
"v_mul_f32 v54, v188, v54\n" \
"v_mfma_f32_16x16x32_bf16 v[208:211], a[64:67], v[96:99], v[208:211]\n" \
"ds_read_b128 v[120:123], v28 offset:29696\n" \
"v_mul_f32 v55, v189, v55\n" \
"v_mfma_f32_16x16x32_bf16 v[208:211], a[72:75], v[100:103], v[208:211]\n" \
"buffer_load_dwordx4 v31, s[36:39], s80 offen offset:2048 sc1 lds\n" \
"v_mfma_f32_16x16x32_bf16 v[212:215], a[68:71], v[96:99], v[212:215]\n" \
"s_waitcnt lgkmcnt(3)\n" \
"ds_read_b128 v[124:127], v28 offset:31744\n" \
"v_permlane16_swap_b32 v156, v158\n" \
"v_mfma_f32_16x16x32_bf16 v[212:215], a[76:79], v[100:103], v[212:215]\n" \
"v_permlane16_swap_b32 v157, v159\n" \
"v_sub_f32 v8, 1.0, v179\n" \
"v_mfma_f32_16x16x32_bf16 v[216:219], a[64:67], v[104:107], v[216:219]\n" \
"ds_read_b128 v[96:99], v30 offset:0\n" \
"v_cvt_pk_bf16_f32 v8, v8, v8\n" \
"v_mfma_f32_16x16x32_bf16 v[216:219], a[72:75], v[108:111], v[216:219]\n" \
"buffer_load_dwordx4 v31, s[36:39], s80 offen offset:3072 sc1 lds\n" \
"v_mfma_f32_16x16x32_bf16 v[220:223], a[68:71], v[104:107], v[220:223]\n" \
"s_waitcnt lgkmcnt(3)\n" \
"ds_read_b128 v[100:103], v30 offset:2048\n" \
"v_and_b32 v8, 0xffff0000, v8\n" \
"v_mfma_f32_16x16x32_bf16 v[220:223], a[76:79], v[108:111], v[220:223]\n" \
"v_lshlrev_b32 v56, 16, v156\n" \
"v_and_b32 v57, 0xffff0000, v156\n" \
"v_mfma_f32_16x16x32_bf16 v[208:211], a[80:83], v[112:115], v[208:211]\n" \
"ds_read_b128 v[104:107], v30 offset:1024\n" \
"v_lshlrev_b32 v188, 16, v138\n" \
"v_mfma_f32_16x16x32_bf16 v[208:211], a[88:91], v[116:119], v[208:211]\n" \
"v_and_b32 v189, 0xffff0000, v138\n" \
"v_mul_f32 v56, v179, v56\n" \
"v_mfma_f32_16x16x32_bf16 v[212:215], a[84:87], v[112:115], v[212:215]\n" \
"s_waitcnt lgkmcnt(3)\n" \
"ds_read_b128 v[108:111], v30 offset:3072\n" \
"v_mul_f32 v57, v179, v57\n" \
"v_mfma_f32_16x16x32_bf16 v[212:215], a[92:95], v[116:119], v[212:215]\n" \
"v_mul_f32 v56, v188, v56\n" \
"v_mul_f32 v57, v189, v57\n" \
"v_mfma_f32_16x16x32_bf16 v[216:219], a[80:83], v[120:123], v[216:219]\n" \
"ds_read_b128 v[112:115], v30 offset:4096\n" \
"v_lshlrev_b32 v58, 16, v157\n" \
"v_mfma_f32_16x16x32_bf16 v[216:219], a[88:91], v[124:127], v[216:219]\n" \
"v_and_b32 v59, 0xffff0000, v157\n" \
"v_lshlrev_b32 v188, 16, v139\n" \
"v_mfma_f32_16x16x32_bf16 v[220:223], a[84:87], v[120:123], v[220:223]\n" \
"s_waitcnt lgkmcnt(3)\n" \
"ds_read_b128 v[116:119], v30 offset:6144\n" \
"v_and_b32 v189, 0xffff0000, v139\n" \
"v_mfma_f32_16x16x32_bf16 v[220:223], a[92:95], v[124:127], v[220:223]\n" \
"v_mul_f32 v58, v179, v58\n" \
"v_mul_f32 v59, v179, v59\n" \
"v_mfma_f32_16x16x32_bf16 v[208:211], a[128:131], v[96:99], v[208:211]\n" \
"ds_read_b128 v[120:123], v30 offset:5120\n" \
"v_mul_f32 v58, v188, v58\n" \
"v_mfma_f32_16x16x32_bf16 v[208:211], a[136:139], v[100:103], v[208:211]\n" \
"v_mul_f32 v59, v189, v59\n" \
"v_lshlrev_b32 v60, 16, v158\n" \
"v_mfma_f32_16x16x32_bf16 v[212:215], a[132:135], v[96:99], v[212:215]\n" \
"s_waitcnt lgkmcnt(3)\n" \
"ds_read_b128 v[124:127], v30 offset:7168\n" \
"v_and_b32 v61, 0xffff0000, v158\n" \
"v_mfma_f32_16x16x32_bf16 v[212:215], a[140:143], v[100:103], v[212:215]\n" \
"v_lshlrev_b32 v188, 16, v142\n" \
"v_and_b32 v189, 0xffff0000, v142\n" \
"v_mfma_f32_16x16x32_bf16 v[216:219], a[128:131], v[104:107], v[216:219]\n" \
"ds_read_b128 v[96:99], v30 offset:8192\n" \
"v_mul_f32 v60, v179, v60\n" \
"v_mfma_f32_16x16x32_bf16 v[216:219], a[136:139], v[108:111], v[216:219]\n" \
"v_mul_f32 v61, v179, v61\n" \
"v_mul_f32 v60, v188, v60\n" \
"v_mfma_f32_16x16x32_bf16 v[220:223], a[132:135], v[104:107], v[220:223]\n" \
"s_waitcnt lgkmcnt(3)\n" \
"ds_read_b128 v[100:103], v30 offset:10240\n" \
"v_mul_f32 v61, v189, v61\n" \
"v_mfma_f32_16x16x32_bf16 v[220:223], a[140:143], v[108:111], v[220:223]\n" \
"v_lshlrev_b32 v62, 16, v159\n" \
"v_and_b32 v63, 0xffff0000, v159\n" \
"v_mfma_f32_16x16x32_bf16 v[208:211], a[144:147], v[112:115], v[208:211]\n" \
"ds_read_b128 v[104:107], v30 offset:9216\n" \
"v_lshlrev_b32 v188, 16, v143\n" \
"v_mfma_f32_16x16x32_bf16 v[208:211], a[152:155], v[116:119], v[208:211]\n" \
"v_and_b32 v189, 0xffff0000, v143\n" \
"v_mul_f32 v62, v179, v62\n" \
"v_mfma_f32_16x16x32_bf16 v[212:215], a[148:151], v[112:115], v[212:215]\n" \
"s_waitcnt lgkmcnt(3)\n" \
"ds_read_b128 v[108:111], v30 offset:11264\n" \
"v_mul_f32 v63, v179, v63\n" \
"v_mfma_f32_16x16x32_bf16 v[212:215], a[156:159], v[116:119], v[212:215]\n" \
"s_add_u32 s80, s71, 16384\n" \
"s_add_u32 m0, s22, 114688\n" \
"v_mul_f32 v62, v188, v62\n" \
"v_mfma_f32_16x16x32_bf16 v[216:219], a[144:147], v[120:123], v[216:219]\n" \
"ds_read_b128 v[112:115], v30 offset:12288\n" \
"v_mul_f32 v63, v189, v63\n" \
"v_mfma_f32_16x16x32_bf16 v[216:219], a[152:155], v[124:127], v[216:219]\n" \
"v_mfma_f32_16x16x32_bf16 v[220:223], a[148:151], v[120:123], v[220:223]\n" \
"ds_read_b128 v[116:119], v30 offset:14336\n" \
"v_mfma_f32_16x16x32_bf16 v[220:223], a[156:159], v[124:127], v[220:223]\n" \
"s_waitcnt vmcnt(27) lgkmcnt(4)\n" \
"s_barrier\n" \
"v_mfma_f32_16x16x32_bf16 v[208:211], a[0:3], v[96:99], v[208:211]\n" \
"ds_read_b128 v[120:123], v30 offset:13312\n" \
"v_mfma_f32_16x16x32_bf16 v[208:211], a[8:11], v[100:103], v[208:211]\n" \
"v_mfma_f32_16x16x32_bf16 v[212:215], a[4:7], v[96:99], v[212:215]\n" \
"s_waitcnt lgkmcnt(3)\n" \
"ds_read_b128 v[124:127], v30 offset:15360\n" \
"v_mfma_f32_16x16x32_bf16 v[212:215], a[12:15], v[100:103], v[212:215]\n" \
"buffer_load_dwordx4 v31, s[36:39], s80 offen offset:0 sc1 lds\n" \
"v_mfma_f32_16x16x32_bf16 v[216:219], a[0:3], v[104:107], v[216:219]\n" \
"ds_read_b128 v[96:99], v28 offset:32768\n" \
"v_mfma_f32_16x16x32_bf16 v[216:219], a[8:11], v[108:111], v[216:219]\n" \
"v_mfma_f32_16x16x32_bf16 v[220:223], a[4:7], v[104:107], v[220:223]\n" \
"s_waitcnt lgkmcnt(3)\n" \
"ds_read_b128 v[100:103], v28 offset:34816\n" \
"v_mfma_f32_16x16x32_bf16 v[220:223], a[12:15], v[108:111], v[220:223]\n" \
"buffer_load_dwordx4 v31, s[36:39], s80 offen offset:1024 sc1 lds\n" \
"v_mfma_f32_16x16x32_bf16 v[208:211], a[16:19], v[112:115], v[208:211]\n" \
"ds_read_b128 v[104:107], v28 offset:33792\n" \
"v_mfma_f32_16x16x32_bf16 v[208:211], a[24:27], v[116:119], v[208:211]\n" \
"v_mfma_f32_16x16x32_bf16 v[212:215], a[20:23], v[112:115], v[212:215]\n" \
"s_waitcnt lgkmcnt(3)\n" \
"ds_read_b128 v[108:111], v28 offset:35840\n" \
"v_mfma_f32_16x16x32_bf16 v[212:215], a[28:31], v[116:119], v[212:215]\n" \
"buffer_load_dwordx4 v31, s[36:39], s80 offen offset:2048 sc1 lds\n" \
"v_mfma_f32_16x16x32_bf16 v[216:219], a[16:19], v[120:123], v[216:219]\n" \
"ds_read_b128 v[112:115], v28 offset:36864\n" \
"v_mfma_f32_16x16x32_bf16 v[216:219], a[24:27], v[124:127], v[216:219]\n" \
"v_mfma_f32_16x16x32_bf16 v[220:223], a[20:23], v[120:123], v[220:223]\n" \
"s_waitcnt lgkmcnt(3)\n" \
"ds_read_b128 v[116:119], v28 offset:38912\n" \
"v_mfma_f32_16x16x32_bf16 v[220:223], a[28:31], v[124:127], v[220:223]\n" \
"buffer_load_dwordx4 v31, s[36:39], s80 offen offset:3072 sc1 lds\n" \
"v_mfma_f32_16x16x32_bf16 v[208:211], a[32:35], v[96:99], v[208:211]\n" \
"ds_read_b128 v[120:123], v28 offset:37888\n" \
"v_mfma_f32_16x16x32_bf16 v[208:211], a[40:43], v[100:103], v[208:211]\n" \
"v_mfma_f32_16x16x32_bf16 v[212:215], a[36:39], v[96:99], v[212:215]\n" \
"s_waitcnt lgkmcnt(3)\n" \
"ds_read_b128 v[124:127], v28 offset:39936\n" \
"v_mfma_f32_16x16x32_bf16 v[212:215], a[44:47], v[100:103], v[212:215]\n" \
"v_mfma_f32_16x16x32_bf16 v[216:219], a[32:35], v[104:107], v[216:219]\n" \
"ds_read_b128 v[96:99], v28 offset:40960\n" \
"v_mfma_f32_16x16x32_bf16 v[216:219], a[40:43], v[108:111], v[216:219]\n" \
"v_mfma_f32_16x16x32_bf16 v[220:223], a[36:39], v[104:107], v[220:223]\n" \
"s_waitcnt lgkmcnt(3)\n" \
"ds_read_b128 v[100:103], v28 offset:43008\n" \
"v_mfma_f32_16x16x32_bf16 v[220:223], a[44:47], v[108:111], v[220:223]\n" \
"v_mfma_f32_16x16x32_bf16 v[208:211], a[48:51], v[112:115], v[208:211]\n" \
"ds_read_b128 v[104:107], v28 offset:41984\n" \
"v_mfma_f32_16x16x32_bf16 v[208:211], a[56:59], v[116:119], v[208:211]\n" \
"v_mfma_f32_16x16x32_bf16 v[212:215], a[52:55], v[112:115], v[212:215]\n" \
"s_waitcnt lgkmcnt(3)\n" \
"ds_read_b128 v[108:111], v28 offset:44032\n" \
"v_mfma_f32_16x16x32_bf16 v[212:215], a[60:63], v[116:119], v[212:215]\n" \
"s_add_u32 s80, s71, 32768\n" \
"s_add_u32 m0, s22, 147456\n" \
"v_mfma_f32_16x16x32_bf16 v[216:219], a[48:51], v[120:123], v[216:219]\n" \
"s_sub_u32 s96, s88, 94404608\n" \
"ds_read_b128 v[112:115], v28 offset:45056\n" \
"s_lshl_b32 s96, s96, 2\n" \
"v_mfma_f32_16x16x32_bf16 v[216:219], a[56:59], v[124:127], v[216:219]\n" \
"s_add_u32 s96, s96, 94437376\n" \
"v_bfe_u32 v17, v4, 4, 2\n" \
"v_min_u32 v17, 2, v17\n" \
"v_mfma_f32_16x16x32_bf16 v[220:223], a[52:55], v[120:123], v[220:223]\n" \
"ds_read_b128 v[116:119], v28 offset:47104\n" \
"v_lshl_add_u32 v17, v17, 3, s96\n" \
"v_mfma_f32_16x16x32_bf16 v[220:223], a[60:63], v[124:127], v[220:223]\n" \
"s_waitcnt vmcnt(27) lgkmcnt(4)\n" \
"s_barrier\n" \
"v_mfma_f32_16x16x32_bf16 v[208:211], a[96:99], v[96:99], v[208:211]\n" \
"ds_read_b128 v[120:123], v28 offset:46080\n" \
"v_mfma_f32_16x16x32_bf16 v[208:211], a[104:107], v[100:103], v[208:211]\n" \
"v_mfma_f32_16x16x32_bf16 v[212:215], a[100:103], v[96:99], v[212:215]\n" \
"s_waitcnt lgkmcnt(3)\n" \
"ds_read_b128 v[124:127], v28 offset:48128\n" \
"v_mfma_f32_16x16x32_bf16 v[212:215], a[108:111], v[100:103], v[212:215]\n" \
"buffer_load_dwordx4 v31, s[36:39], s80 offen offset:0 sc1 lds\n" \
"v_mfma_f32_16x16x32_bf16 v[216:219], a[96:99], v[104:107], v[216:219]\n" \
"ds_read_b128 v[96:99], v28 offset:0\n" \
"v_mfma_f32_16x16x32_bf16 v[216:219], a[104:107], v[108:111], v[216:219]\n" \
"v_mfma_f32_16x16x32_bf16 v[220:223], a[100:103], v[104:107], v[220:223]\n" \
"s_waitcnt lgkmcnt(3)\n" \
"ds_read_b128 v[100:103], v28 offset:2048\n" \
"v_mfma_f32_16x16x32_bf16 v[220:223], a[108:111], v[108:111], v[220:223]\n" \
"buffer_load_dwordx4 v31, s[36:39], s80 offen offset:1024 sc1 lds\n" \
"v_mfma_f32_16x16x32_bf16 v[208:211], a[112:115], v[112:115], v[208:211]\n" \
"ds_read_b128 v[104:107], v28 offset:1024\n" \
"v_mfma_f32_16x16x32_bf16 v[208:211], a[120:123], v[116:119], v[208:211]\n" \
"v_mfma_f32_16x16x32_bf16 v[212:215], a[116:119], v[112:115], v[212:215]\n" \
"s_waitcnt lgkmcnt(3)\n" \
"ds_read_b128 v[108:111], v28 offset:3072\n" \
"v_mfma_f32_16x16x32_bf16 v[212:215], a[124:127], v[116:119], v[212:215]\n" \
"buffer_load_dwordx2 v[18:19], v17, s[36:39], 0 offen sc1\n" \
"v_mfma_f32_16x16x32_bf16 v[216:219], a[112:115], v[120:123], v[216:219]\n" \
"ds_read_b128 v[112:115], v28 offset:4096\n" \
"v_mfma_f32_16x16x32_bf16 v[216:219], a[120:123], v[124:127], v[216:219]\n" \
"v_mfma_f32_16x16x32_bf16 v[220:223], a[116:119], v[120:123], v[220:223]\n" \
"s_waitcnt lgkmcnt(3)\n" \
"ds_read_b128 v[116:119], v28 offset:6144\n" \
"v_mfma_f32_16x16x32_bf16 v[220:223], a[124:127], v[124:127], v[220:223]\n" \
"buffer_load_dwordx4 v31, s[36:39], s80 offen offset:2048 sc1 lds\n" \
"v_mfma_f32_16x16x32_bf16 v[192:195], a[160:163], v[96:99], 0\n" \
"ds_read_b128 v[120:123], v28 offset:5120\n" \
"v_mfma_f32_16x16x32_bf16 v[192:195], a[168:171], v[100:103], v[192:195]\n" \
"v_mfma_f32_16x16x32_bf16 v[196:199], a[164:167], v[96:99], 0\n" \
"s_waitcnt lgkmcnt(3)\n" \
"ds_read_b128 v[124:127], v28 offset:7168\n" \
"v_cvt_pk_bf16_f32 v208, v208, v209\n" \
"v_mfma_f32_16x16x32_bf16 v[196:199], a[172:175], v[100:103], v[196:199]\n" \
"buffer_load_dwordx4 v31, s[36:39], s80 offen offset:3072 sc1 lds\n" \
"v_mfma_f32_16x16x32_bf16 v[200:203], a[160:163], v[104:107], 0\n" \
"ds_read_b128 v[96:99], v28 offset:8192\n" \
"v_cvt_pk_bf16_f32 v209, v210, v211\n" \
"v_mfma_f32_16x16x32_bf16 v[200:203], a[168:171], v[108:111], v[200:203]\n" \
"v_cvt_pk_bf16_f32 v210, v212, v213\n" \
"v_cvt_pk_bf16_f32 v211, v214, v215\n" \
"v_mfma_f32_16x16x32_bf16 v[204:207], a[164:167], v[104:107], 0\n" \
"s_waitcnt lgkmcnt(3)\n" \
"ds_read_b128 v[100:103], v28 offset:10240\n" \
"v_cvt_pk_bf16_f32 v216, v216, v217\n" \
"v_mfma_f32_16x16x32_bf16 v[204:207], a[172:175], v[108:111], v[204:207]\n" \
"buffer_store_dwordx4 v[208:211], v4, s[36:39], s76 offen offset:0 sc1\n" \
"v_mfma_f32_16x16x32_bf16 v[192:195], a[176:179], v[112:115], v[192:195]\n" \
"ds_read_b128 v[104:107], v28 offset:9216\n" \
"v_cvt_pk_bf16_f32 v217, v218, v219\n" \
"v_mfma_f32_16x16x32_bf16 v[192:195], a[184:187], v[116:119], v[192:195]\n" \
"v_cvt_pk_bf16_f32 v218, v220, v221\n" \
"v_cvt_pk_bf16_f32 v219, v222, v223\n" \
"v_mfma_f32_16x16x32_bf16 v[196:199], a[180:183], v[112:115], v[196:199]\n" \
"s_waitcnt lgkmcnt(3)\n" \
"ds_read_b128 v[108:111], v28 offset:11264\n" \
"v_mfma_f32_16x16x32_bf16 v[196:199], a[188:191], v[116:119], v[196:199]\n" \
"s_add_u32 s80, s71, 49152\n" \
"s_add_u32 m0, s22, 16384\n" \
"v_mfma_f32_16x16x32_bf16 v[200:203], a[176:179], v[120:123], v[200:203]\n" \
"ds_read_b128 v[112:115], v28 offset:12288\n" \
"v_mfma_f32_16x16x32_bf16 v[200:203], a[184:187], v[124:127], v[200:203]\n" \
"v_mfma_f32_16x16x32_bf16 v[204:207], a[180:183], v[120:123], v[204:207]\n" \
"ds_read_b128 v[116:119], v28 offset:14336\n" \
"v_mfma_f32_16x16x32_bf16 v[204:207], a[188:191], v[124:127], v[204:207]\n" \
"s_waitcnt vmcnt(26) lgkmcnt(4)\n" \
"s_barrier\n" \
"v_mfma_f32_16x16x32_bf16 v[192:195], a[192:195], v[96:99], v[192:195]\n" \
"ds_read_b128 v[120:123], v28 offset:13312\n" \
"v_mfma_f32_16x16x32_bf16 v[192:195], a[200:203], v[100:103], v[192:195]\n" \
"buffer_store_dwordx4 v[216:219], v4, s[36:39], s76 offen offset:1024 sc1\n" \
"v_mfma_f32_16x16x32_bf16 v[196:199], a[196:199], v[96:99], v[196:199]\n" \
"s_waitcnt lgkmcnt(3)\n" \
"ds_read_b128 v[124:127], v28 offset:15360\n" \
"v_mfma_f32_16x16x32_bf16 v[196:199], a[204:207], v[100:103], v[196:199]\n" \
"v_mfma_f32_16x16x32_bf16 v[200:203], a[192:195], v[104:107], v[200:203]\n" \
"ds_read_b128 v[96:99], v29 offset:32768\n" \
"v_mfma_f32_16x16x32_bf16 v[200:203], a[200:203], v[108:111], v[200:203]\n" \
"buffer_load_dwordx4 v31, s[36:39], s80 offen offset:0 sc1 lds\n" \
"v_mfma_f32_16x16x32_bf16 v[204:207], a[196:199], v[104:107], v[204:207]\n" \
"s_waitcnt lgkmcnt(3)\n" \
"ds_read_b128 v[100:103], v29 offset:34816\n" \
"v_mfma_f32_16x16x32_bf16 v[204:207], a[204:207], v[108:111], v[204:207]\n" \
"v_mfma_f32_16x16x32_bf16 v[192:195], a[208:211], v[112:115], v[192:195]\n" \
"ds_read_b128 v[104:107], v29 offset:33792\n" \
"v_mfma_f32_16x16x32_bf16 v[192:195], a[216:219], v[116:119], v[192:195]\n" \
"buffer_load_dwordx4 v31, s[36:39], s80 offen offset:1024 sc1 lds\n" \
"v_mfma_f32_16x16x32_bf16 v[196:199], a[212:215], v[112:115], v[196:199]\n" \
"s_waitcnt lgkmcnt(3)\n" \
"ds_read_b128 v[108:111], v29 offset:35840\n" \
"v_mfma_f32_16x16x32_bf16 v[196:199], a[220:223], v[116:119], v[196:199]\n" \
"v_mfma_f32_16x16x32_bf16 v[200:203], a[208:211], v[120:123], v[200:203]\n" \
"ds_read_b128 v[112:115], v29 offset:36864\n" \
"v_mfma_f32_16x16x32_bf16 v[200:203], a[216:219], v[124:127], v[200:203]\n" \
"buffer_load_dwordx4 v31, s[36:39], s80 offen offset:2048 sc1 lds\n" \
"v_mfma_f32_16x16x32_bf16 v[204:207], a[212:215], v[120:123], v[204:207]\n" \
"s_waitcnt lgkmcnt(3)\n" \
"ds_read_b128 v[116:119], v29 offset:38912\n" \
"v_mfma_f32_16x16x32_bf16 v[204:207], a[220:223], v[124:127], v[204:207]\n" \
"v_mfma_f32_16x16x32_bf16 v[192:195], a[224:227], v[96:99], v[192:195]\n" \
"ds_read_b128 v[120:123], v29 offset:37888\n" \
"v_mfma_f32_16x16x32_bf16 v[192:195], a[232:235], v[100:103], v[192:195]\n" \
"buffer_load_dwordx4 v31, s[36:39], s80 offen offset:3072 sc1 lds\n" \
"v_mfma_f32_16x16x32_bf16 v[196:199], a[228:231], v[96:99], v[196:199]\n" \
"s_waitcnt lgkmcnt(3)\n" \
"ds_read_b128 v[124:127], v29 offset:39936\n" \
"v_mfma_f32_16x16x32_bf16 v[196:199], a[236:239], v[100:103], v[196:199]\n" \
"v_mfma_f32_16x16x32_bf16 v[200:203], a[224:227], v[104:107], v[200:203]\n" \
"ds_read_b128 v[96:99], v29 offset:40960\n" \
"v_mfma_f32_16x16x32_bf16 v[200:203], a[232:235], v[108:111], v[200:203]\n" \
"v_mfma_f32_16x16x32_bf16 v[204:207], a[228:231], v[104:107], v[204:207]\n" \
"s_waitcnt lgkmcnt(3)\n" \
"ds_read_b128 v[100:103], v29 offset:43008\n" \
"s_waitcnt vmcnt(8)\n" \
"v_mfma_f32_16x16x32_bf16 v[204:207], a[236:239], v[108:111], v[204:207]\n" \
"v_cmp_ne_u64 vcc, s[14:15], v[18:19]\n" \
"s_nop 0\n" \
"v_mfma_f32_16x16x32_bf16 v[192:195], a[240:243], v[112:115], v[192:195]\n" \
"ds_read_b128 v[104:107], v29 offset:41984\n" \
"s_cbranch_vccnz LPE_%=\nLPER_%=:\n" \
"v_mfma_f32_16x16x32_bf16 v[192:195], a[248:251], v[116:119], v[192:195]\n" \
"s_add_u32 s97, s78, 0\n" \
"v_mfma_f32_16x16x32_bf16 v[196:199], a[244:247], v[112:115], v[196:199]\n" \
"s_waitcnt lgkmcnt(3)\n" \
"ds_read_b128 v[108:111], v29 offset:44032\n" \
"v_mfma_f32_16x16x32_bf16 v[196:199], a[252:255], v[116:119], v[196:199]\n" \
"buffer_load_dwordx4 v[128:131], v4, s[36:39], s97 offen offset:0 sc1\n" \
"v_mfma_f32_16x16x32_bf16 v[200:203], a[240:243], v[120:123], v[200:203]\n" \
"ds_read_b128 v[112:115], v29 offset:45056\n" \
"v_mfma_f32_16x16x32_bf16 v[200:203], a[248:251], v[124:127], v[200:203]\n" \
"v_mfma_f32_16x16x32_bf16 v[204:207], a[244:247], v[120:123], v[204:207]\n" \
"ds_read_b128 v[116:119], v29 offset:47104\n" \
"v_mfma_f32_16x16x32_bf16 v[204:207], a[252:255], v[124:127], v[204:207]\n" \
"LH29_%=:\ns_cbranch_execz LHR2_%=\n" \
"s_waitcnt vmcnt(27) lgkmcnt(4)\n" \
"s_barrier\n" \
"v_mfma_f32_16x16x32_bf16 v[192:195], a[64:67], v[96:99], v[192:195]\n" \
"ds_read_b128 v[120:123], v29 offset:46080\n" \
"v_mfma_f32_16x16x32_bf16 v[192:195], a[72:75], v[100:103], v[192:195]\n" \
"buffer_load_dwordx4 v[132:135], v4, s[36:39], s97 offen offset:1024 sc1\n" \
"v_mfma_f32_16x16x32_bf16 v[196:199], a[68:71], v[96:99], v[196:199]\n" \
"s_waitcnt lgkmcnt(3)\n" \
"ds_read_b128 v[124:127], v29 offset:48128\n" \
"v_mfma_f32_16x16x32_bf16 v[196:199], a[76:79], v[100:103], v[196:199]\n" \
"v_mfma_f32_16x16x32_bf16 v[200:203], a[64:67], v[104:107], v[200:203]\n" \
"ds_read_b128 v[96:99], v28 offset:49152\n" \
"v_mfma_f32_16x16x32_bf16 v[200:203], a[72:75], v[108:111], v[200:203]\n" \
"buffer_load_dwordx4 v[136:139], v4, s[36:39], s97 offen offset:2048 sc1\n" \
"v_mfma_f32_16x16x32_bf16 v[204:207], a[68:71], v[104:107], v[204:207]\n" \
"s_waitcnt lgkmcnt(3)\n" \
"ds_read_b128 v[100:103], v28 offset:51200\n" \
"v_mfma_f32_16x16x32_bf16 v[204:207], a[76:79], v[108:111], v[204:207]\n" \
"v_mfma_f32_16x16x32_bf16 v[192:195], a[80:83], v[112:115], v[192:195]\n" \
"ds_read_b128 v[104:107], v28 offset:50176\n" \
"v_mfma_f32_16x16x32_bf16 v[192:195], a[88:91], v[116:119], v[192:195]\n" \
"buffer_load_dwordx4 v[140:143], v4, s[36:39], s97 offen offset:3072 sc1\n" \
"v_mfma_f32_16x16x32_bf16 v[196:199], a[84:87], v[112:115], v[196:199]\n" \
"s_waitcnt lgkmcnt(3)\n" \
"ds_read_b128 v[108:111], v28 offset:52224\n" \
"s_add_u32 s97, s78, 4096\n" \
"v_mfma_f32_16x16x32_bf16 v[196:199], a[92:95], v[116:119], v[196:199]\n" \
"v_mfma_f32_16x16x32_bf16 v[200:203], a[80:83], v[120:123], v[200:203]\n" \
"ds_read_b128 v[112:115], v28 offset:53248\n" \
"v_mfma_f32_16x16x32_bf16 v[200:203], a[88:91], v[124:127], v[200:203]\n" \
"buffer_load_dwordx4 v[160:163], v4, s[36:39], s97 offen offset:0 sc1\n" \
"v_mfma_f32_16x16x32_bf16 v[204:207], a[84:87], v[120:123], v[204:207]\n" \
"s_waitcnt lgkmcnt(3)\n" \
"ds_read_b128 v[116:119], v28 offset:55296\n" \
"v_mfma_f32_16x16x32_bf16 v[204:207], a[92:95], v[124:127], v[204:207]\n" \
"v_mfma_f32_16x16x32_bf16 v[192:195], a[128:131], v[96:99], v[192:195]\n" \
"ds_read_b128 v[120:123], v28 offset:54272\n" \
"v_mfma_f32_16x16x32_bf16 v[192:195], a[136:139], v[100:103], v[192:195]\n" \
"buffer_load_dwordx4 v[164:167], v4, s[36:39], s97 offen offset:1024 sc1\n" \
"v_mfma_f32_16x16x32_bf16 v[196:199], a[132:135], v[96:99], v[196:199]\n" \
"s_waitcnt lgkmcnt(3)\n" \
"ds_read_b128 v[124:127], v28 offset:56320\n" \
"v_mfma_f32_16x16x32_bf16 v[196:199], a[140:143], v[100:103], v[196:199]\n" \
"v_mfma_f32_16x16x32_bf16 v[200:203], a[128:131], v[104:107], v[200:203]\n" \
"ds_read_b128 v[96:99], v28 offset:57344\n" \
"v_mfma_f32_16x16x32_bf16 v[200:203], a[136:139], v[108:111], v[200:203]\n" \
"v_mfma_f32_16x16x32_bf16 v[204:207], a[132:135], v[104:107], v[204:207]\n" \
"s_waitcnt lgkmcnt(3)\n" \
"ds_read_b128 v[100:103], v28 offset:59392\n" \
"v_mfma_f32_16x16x32_bf16 v[204:207], a[140:143], v[108:111], v[204:207]\n" \
"v_mfma_f32_16x16x32_bf16 v[192:195], a[144:147], v[112:115], v[192:195]\n" \
"ds_read_b128 v[104:107], v28 offset:58368\n" \
"v_mfma_f32_16x16x32_bf16 v[192:195], a[152:155], v[116:119], v[192:195]\n" \
"v_mfma_f32_16x16x32_bf16 v[196:199], a[148:151], v[112:115], v[196:199]\n" \
"s_waitcnt lgkmcnt(3)\n" \
"ds_read_b128 v[108:111], v28 offset:60416\n" \
"v_mfma_f32_16x16x32_bf16 v[196:199], a[156:159], v[116:119], v[196:199]\n" \
"v_mfma_f32_16x16x32_bf16 v[200:203], a[144:147], v[120:123], v[200:203]\n" \
"ds_read_b128 v[112:115], v28 offset:61440\n" \
"v_mfma_f32_16x16x32_bf16 v[200:203], a[152:155], v[124:127], v[200:203]\n" \
"v_mfma_f32_16x16x32_bf16 v[204:207], a[148:151], v[120:123], v[204:207]\n" \
"ds_read_b128 v[116:119], v28 offset:63488\n" \
"v_mfma_f32_16x16x32_bf16 v[204:207], a[156:159], v[124:127], v[204:207]\n" \
"s_waitcnt vmcnt(26) lgkmcnt(4)\n" \
"s_barrier\n" \
"v_mfma_f32_16x16x32_bf16 v[192:195], a[0:3], v[96:99], v[192:195]\n" \
"ds_read_b128 v[120:123], v28 offset:62464\n" \
"v_mfma_f32_16x16x32_bf16 v[192:195], a[8:11], v[100:103], v[192:195]\n" \
"v_mfma_f32_16x16x32_bf16 v[196:199], a[4:7], v[96:99], v[196:199]\n" \
"s_waitcnt lgkmcnt(3)\n" \
"ds_read_b128 v[124:127], v28 offset:64512\n" \
"v_mfma_f32_16x16x32_bf16 v[196:199], a[12:15], v[100:103], v[196:199]\n" \
"v_mfma_f32_16x16x32_bf16 v[200:203], a[0:3], v[104:107], v[200:203]\n" \
"ds_read_b128 v[96:99], v29 offset:0\n" \
"v_mfma_f32_16x16x32_bf16 v[200:203], a[8:11], v[108:111], v[200:203]\n" \
"v_mfma_f32_16x16x32_bf16 v[204:207], a[4:7], v[104:107], v[204:207]\n" \
"s_waitcnt lgkmcnt(3)\n" \
"ds_read_b128 v[100:103], v29 offset:2048\n" \
"v_mfma_f32_16x16x32_bf16 v[204:207], a[12:15], v[108:111], v[204:207]\n" \
"v_mfma_f32_16x16x32_bf16 v[192:195], a[16:19], v[112:115], v[192:195]\n" \
"ds_read_b128 v[104:107], v29 offset:1024\n" \
"v_mfma_f32_16x16x32_bf16 v[192:195], a[24:27], v[116:119], v[192:195]\n" \
"v_mfma_f32_16x16x32_bf16 v[196:199], a[20:23], v[112:115], v[196:199]\n" \
"s_waitcnt lgkmcnt(3)\n" \
"ds_read_b128 v[108:111], v29 offset:3072\n" \
"v_mfma_f32_16x16x32_bf16 v[196:199], a[28:31], v[116:119], v[196:199]\n" \
"v_mfma_f32_16x16x32_bf16 v[200:203], a[16:19], v[120:123], v[200:203]\n" \
"ds_read_b128 v[112:115], v29 offset:4096\n" \
"v_mfma_f32_16x16x32_bf16 v[200:203], a[24:27], v[124:127], v[200:203]\n" \
"v_mfma_f32_16x16x32_bf16 v[204:207], a[20:23], v[120:123], v[204:207]\n" \
"s_waitcnt lgkmcnt(3)\n" \
"ds_read_b128 v[116:119], v29 offset:6144\n" \
"v_mfma_f32_16x16x32_bf16 v[204:207], a[28:31], v[124:127], v[204:207]\n" \
"v_mfma_f32_16x16x32_bf16 v[192:195], a[32:35], v[96:99], v[192:195]\n" \
"ds_read_b128 v[120:123], v29 offset:5120\n" \
"v_mfma_f32_16x16x32_bf16 v[192:195], a[40:43], v[100:103], v[192:195]\n" \
"v_mfma_f32_16x16x32_bf16 v[196:199], a[36:39], v[96:99], v[196:199]\n" \
"s_waitcnt lgkmcnt(3)\n" \
"ds_read_b128 v[124:127], v29 offset:7168\n" \
"v_mfma_f32_16x16x32_bf16 v[196:199], a[44:47], v[100:103], v[196:199]\n" \
"v_mfma_f32_16x16x32_bf16 v[200:203], a[32:35], v[104:107], v[200:203]\n" \
"ds_read_b128 v[96:99], v29 offset:8192\n" \
"v_mfma_f32_16x16x32_bf16 v[200:203], a[40:43], v[108:111], v[200:203]\n" \
"v_mfma_f32_16x16x32_bf16 v[204:207], a[36:39], v[104:107], v[204:207]\n" \
"s_waitcnt lgkmcnt(3)\n" \
"ds_read_b128 v[100:103], v29 offset:10240\n" \
"v_mfma_f32_16x16x32_bf16 v[204:207], a[44:47], v[108:111], v[204:207]\n" \
"v_mfma_f32_16x16x32_bf16 v[192:195], a[48:51], v[112:115], v[192:195]\n" \
"ds_read_b128 v[104:107], v29 offset:9216\n" \
"v_mfma_f32_16x16x32_bf16 v[192:195], a[56:59], v[116:119], v[192:195]\n" \
"v_mfma_f32_16x16x32_bf16 v[196:199], a[52:55], v[112:115], v[196:199]\n" \
"s_waitcnt lgkmcnt(3)\n" \
"ds_read_b128 v[108:111], v29 offset:11264\n" \
"v_mfma_f32_16x16x32_bf16 v[196:199], a[60:63], v[116:119], v[196:199]\n" \
"v_mfma_f32_16x16x32_bf16 v[200:203], a[48:51], v[120:123], v[200:203]\n" \
"ds_read_b128 v[112:115], v29 offset:12288\n" \
"v_mfma_f32_16x16x32_bf16 v[200:203], a[56:59], v[124:127], v[200:203]\n" \
"v_mfma_f32_16x16x32_bf16 v[204:207], a[52:55], v[120:123], v[204:207]\n" \
"ds_read_b128 v[116:119], v29 offset:14336\n" \
"v_mfma_f32_16x16x32_bf16 v[204:207], a[60:63], v[124:127], v[204:207]\n" \
"s_waitcnt vmcnt(21) lgkmcnt(4)\n" \
"s_barrier\n" \
"v_mfma_f32_16x16x32_bf16 v[192:195], a[96:99], v[96:99], v[192:195]\n" \
"ds_read_b128 v[120:123], v29 offset:13312\n" \
"v_mfma_f32_16x16x32_bf16 v[192:195], a[104:107], v[100:103], v[192:195]\n" \
"v_mfma_f32_16x16x32_bf16 v[196:199], a[100:103], v[96:99], v[196:199]\n" \
"s_waitcnt lgkmcnt(3)\n" \
"ds_read_b128 v[124:127], v29 offset:15360\n" \
"v_mfma_f32_16x16x32_bf16 v[196:199], a[108:111], v[100:103], v[196:199]\n" \
"v_mfma_f32_16x16x32_bf16 v[200:203], a[96:99], v[104:107], v[200:203]\n" \
"ds_read_b128 v[96:99], v29 offset:16384\n" \
"v_mfma_f32_16x16x32_bf16 v[200:203], a[104:107], v[108:111], v[200:203]\n" \
"v_mfma_f32_16x16x32_bf16 v[204:207], a[100:103], v[104:107], v[204:207]\n" \
"s_waitcnt lgkmcnt(3)\n" \
"ds_read_b128 v[100:103], v29 offset:18432\n" \
"v_mfma_f32_16x16x32_bf16 v[204:207], a[108:111], v[108:111], v[204:207]\n" \
"v_mfma_f32_16x16x32_bf16 v[192:195], a[112:115], v[112:115], v[192:195]\n" \
"ds_read_b128 v[104:107], v29 offset:17408\n" \
"v_mfma_f32_16x16x32_bf16 v[192:195], a[120:123], v[116:119], v[192:195]\n" \
"v_mfma_f32_16x16x32_bf16 v[196:199], a[116:119], v[112:115], v[196:199]\n" \
"s_waitcnt lgkmcnt(3)\n" \
"ds_read_b128 v[108:111], v29 offset:19456\n" \
"v_mfma_f32_16x16x32_bf16 v[196:199], a[124:127], v[116:119], v[196:199]\n" \
"v_mfma_f32_16x16x32_bf16 v[200:203], a[112:115], v[120:123], v[200:203]\n" \
"ds_read_b128 v[112:115], v29 offset:20480\n" \
"v_mfma_f32_16x16x32_bf16 v[200:203], a[120:123], v[124:127], v[200:203]\n" \
"v_mfma_f32_16x16x32_bf16 v[204:207], a[116:119], v[120:123], v[204:207]\n" \
"ds_read_b128 v[116:119], v29 offset:22528\n" \
"v_mfma_f32_16x16x32_bf16 v[204:207], a[124:127], v[124:127], v[204:207]\n" \
"s_waitcnt vmcnt(0)\n" \
"v_mfma_f32_16x16x32_bf16 v[64:67], v[24:27], v[128:131], v[64:67]\n" \
"v_mfma_f32_16x16x32_bf16 v[68:71], v[22:25], v[128:131], v[68:71]\n" \
"v_mfma_f32_16x16x32_bf16 v[72:75], v[24:27], v[132:135], v[72:75]\n" \
"v_cvt_pk_bf16_f32 v192, v192, v193\n" \
"v_cvt_pk_bf16_f32 v193, v194, v195\n" \
"v_mfma_f32_16x16x32_bf16 v[76:79], v[22:25], v[132:135], v[76:79]\n" \
"v_cvt_pk_bf16_f32 v194, v196, v197\n" \
"v_cvt_pk_bf16_f32 v195, v198, v199\n" \
"v_mfma_f32_16x16x32_bf16 v[64:67], v[24:27], v[136:139], v[64:67]\n" \
"buffer_store_dwordx4 v[192:195], v4, s[36:39], s77 offen offset:0 sc1\n" \
"v_mfma_f32_16x16x32_bf16 v[68:71], v[22:25], v[136:139], v[68:71]\n" \
"v_cvt_pk_bf16_f32 v200, v200, v201\n" \
"v_cvt_pk_bf16_f32 v201, v202, v203\n" \
"v_mfma_f32_16x16x32_bf16 v[72:75], v[24:27], v[140:143], v[72:75]\n" \
"v_cvt_pk_bf16_f32 v202, v204, v205\n" \
"v_cvt_pk_bf16_f32 v203, v206, v207\n" \
"v_mfma_f32_16x16x32_bf16 v[76:79], v[22:25], v[140:143], v[76:79]\n" \
"v_mfma_f32_16x16x32_bf16 v[64:67], v[24:27], v[160:163], v[64:67]\n" \
"v_mfma_f32_16x16x32_bf16 v[68:71], v[22:25], v[160:163], v[68:71]\n" \
"v_mfma_f32_16x16x32_bf16 v[72:75], v[24:27], v[164:167], v[72:75]\n" \
"s_waitcnt lgkmcnt(4)\n" \
"v_mfma_f32_16x16x32_bf16 v[76:79], v[22:25], v[164:167], v[76:79]\n" \
"v_mfma_f32_16x16x32_bf16 v[240:243], a[160:163], v[96:99], 0\n" \
"ds_read_b128 v[120:123], v29 offset:21504\n" \
"v_mfma_f32_16x16x32_bf16 v[240:243], a[168:171], v[100:103], v[240:243]\n" \
"buffer_store_dwordx4 v[200:203], v4, s[36:39], s77 offen offset:1024 sc1\n" \
"v_mfma_f32_16x16x32_bf16 v[244:247], a[164:167], v[96:99], 0\n" \
"s_waitcnt lgkmcnt(3)\n" \
"ds_read_b128 v[124:127], v29 offset:23552\n" \
"v_fmac_f32 v32, v5, v64\n" \
"v_mfma_f32_16x16x32_bf16 v[244:247], a[172:175], v[100:103], v[244:247]\n" \
"v_fmac_f32 v33, v5, v65\n" \
"v_cvt_pk_bf16_f32 v80, v32, v33\n" \
"v_mfma_f32_16x16x32_bf16 v[248:251], a[160:163], v[104:107], 0\n" \
"ds_read_b128 v[96:99], v29 offset:24576\n" \
"v_fmac_f32 v34, v5, v66\n" \
"v_mfma_f32_16x16x32_bf16 v[248:251], a[168:171], v[108:111], v[248:251]\n" \
"v_fmac_f32 v35, v5, v67\n" \
"v_cvt_pk_bf16_f32 v81, v34, v35\n" \
"v_mfma_f32_16x16x32_bf16 v[252:255], a[164:167], v[104:107], 0\n" \
"s_waitcnt lgkmcnt(3)\n" \
"ds_read_b128 v[100:103], v29 offset:26624\n" \
"v_fmac_f32 v36, v5, v68\n" \
"v_mfma_f32_16x16x32_bf16 v[252:255], a[172:175], v[108:111], v[252:255]\n" \
"v_fmac_f32 v37, v5, v69\n" \
"v_cvt_pk_bf16_f32 v82, v36, v37\n" \
"v_mfma_f32_16x16x32_bf16 v[240:243], a[176:179], v[112:115], v[240:243]\n" \
"ds_read_b128 v[104:107], v29 offset:25600\n" \
"v_fmac_f32 v38, v5, v70\n" \
"v_mfma_f32_16x16x32_bf16 v[240:243], a[184:187], v[116:119], v[240:243]\n" \
"s_add_u32 s92, s82, 0\n" \
"v_fmac_f32 v39, v5, v71\n" \
"v_cvt_pk_bf16_f32 v83, v38, v39\n" \
"v_mfma_f32_16x16x32_bf16 v[244:247], a[180:183], v[112:115], v[244:247]\n" \
"s_waitcnt lgkmcnt(3)\n" \
"ds_read_b128 v[108:111], v29 offset:27648\n" \
"s_nop 0\n" \
"v_mfma_f32_16x16x32_bf16 v[244:247], a[188:191], v[116:119], v[244:247]\n" \
"v_permlane16_swap_b32 v80, v82\n" \
"v_permlane16_swap_b32 v81, v83\n" \
"v_mfma_f32_16x16x32_bf16 v[248:251], a[176:179], v[120:123], v[248:251]\n" \
"ds_read_b128 v[112:115], v29 offset:28672\n" \
"v_fmac_f32 v40, v21, v72\n" \
"v_mfma_f32_16x16x32_bf16 v[248:251], a[184:187], v[124:127], v[248:251]\n" \
"v_fmac_f32 v41, v21, v73\n" \
"v_cvt_pk_bf16_f32 v84, v40, v41\n" \
"v_mfma_f32_16x16x32_bf16 v[252:255], a[180:183], v[120:123], v[252:255]\n" \
"ds_read_b128 v[116:119], v29 offset:30720\n" \
"v_fmac_f32 v42, v21, v74\n" \
"v_mfma_f32_16x16x32_bf16 v[252:255], a[188:191], v[124:127], v[252:255]\n" \
"s_waitcnt vmcnt(19) lgkmcnt(4)\n" \
"s_barrier\n" \
"v_fmac_f32 v43, v21, v75\n" \
"v_mfma_f32_16x16x32_bf16 v[240:243], a[192:195], v[96:99], v[240:243]\n" \
"ds_read_b128 v[120:123], v29 offset:29696\n" \
"v_cvt_pk_bf16_f32 v85, v42, v43\n" \
"v_mfma_f32_16x16x32_bf16 v[240:243], a[200:203], v[100:103], v[240:243]\n" \
"v_fmac_f32 v44, v21, v76\n" \
"v_fmac_f32 v45, v21, v77\n" \
"v_mfma_f32_16x16x32_bf16 v[244:247], a[196:199], v[96:99], v[244:247]\n" \
"s_waitcnt lgkmcnt(3)\n" \
"ds_read_b128 v[124:127], v29 offset:31744\n" \
"v_cvt_pk_bf16_f32 v86, v44, v45\n" \
"v_mfma_f32_16x16x32_bf16 v[244:247], a[204:207], v[100:103], v[244:247]\n" \
"v_fmac_f32 v46, v21, v78\n" \
"v_fmac_f32 v47, v21, v79\n" \
"v_mfma_f32_16x16x32_bf16 v[248:251], a[192:195], v[104:107], v[248:251]\n" \
"s_add_u32 s97, s78, 4096\n" \
"ds_read_b128 v[96:99], v29 offset:49152\n" \
"v_cvt_pk_bf16_f32 v87, v46, v47\n" \
"v_mfma_f32_16x16x32_bf16 v[248:251], a[200:203], v[108:111], v[248:251]\n" \
"buffer_load_dwordx4 v[168:171], v4, s[36:39], s97 offen offset:2048 sc1\n" \
"v_mfma_f32_16x16x32_bf16 v[252:255], a[196:199], v[104:107], v[252:255]\n" \
"s_waitcnt lgkmcnt(3)\n" \
"ds_read_b128 v[100:103], v29 offset:51200\n" \
"s_add_u32 s93, s82, 32768\n" \
"v_mfma_f32_16x16x32_bf16 v[252:255], a[204:207], v[108:111], v[252:255]\n" \
"s_nop 0\n" \
"v_permlane16_swap_b32 v84, v86\n" \
"v_mfma_f32_16x16x32_bf16 v[240:243], a[208:211], v[112:115], v[240:243]\n" \
"ds_read_b128 v[104:107], v29 offset:50176\n" \
"v_permlane16_swap_b32 v85, v87\n" \
"v_mfma_f32_16x16x32_bf16 v[240:243], a[216:219], v[116:119], v[240:243]\n" \
"buffer_load_dwordx4 v[172:175], v4, s[36:39], s97 offen offset:3072 sc1\n" \
"v_mfma_f32_16x16x32_bf16 v[244:247], a[212:215], v[112:115], v[244:247]\n" \
"s_waitcnt lgkmcnt(3)\n" \
"ds_read_b128 v[108:111], v29 offset:52224\n" \
"s_add_u32 s97, s78, 8192\n" \
"v_mfma_f32_16x16x32_bf16 v[244:247], a[220:223], v[116:119], v[244:247]\n" \
"v_mfma_f32_16x16x32_bf16 v[248:251], a[208:211], v[120:123], v[248:251]\n" \
"ds_read_b128 v[112:115], v29 offset:53248\n" \
"v_mfma_f32_16x16x32_bf16 v[248:251], a[216:219], v[124:127], v[248:251]\n" \
"buffer_load_dwordx4 v[224:227], v4, s[36:39], s97 offen offset:0 sc1\n" \
"v_mfma_f32_16x16x32_bf16 v[252:255], a[212:215], v[120:123], v[252:255]\n" \
"s_waitcnt lgkmcnt(3)\n" \
"ds_read_b128 v[116:119], v29 offset:55296\n" \
"v_mfma_f32_16x16x32_bf16 v[252:255], a[220:223], v[124:127], v[252:255]\n" \
"v_mfma_f32_16x16x32_bf16 v[240:243], a[224:227], v[96:99], v[240:243]\n" \
"ds_read_b128 v[120:123], v29 offset:54272\n" \
"v_mfma_f32_16x16x32_bf16 v[240:243], a[232:235], v[100:103], v[240:243]\n" \
"buffer_load_dwordx4 v[228:231], v4, s[36:39], s97 offen offset:1024 sc1\n" \
"v_mfma_f32_16x16x32_bf16 v[244:247], a[228:231], v[96:99], v[244:247]\n" \
"s_waitcnt lgkmcnt(3)\n" \
"ds_read_b128 v[124:127], v29 offset:56320\n" \
"v_mfma_f32_16x16x32_bf16 v[244:247], a[236:239], v[100:103], v[244:247]\n" \
"v_mfma_f32_16x16x32_bf16 v[248:251], a[224:227], v[104:107], v[248:251]\n" \
"ds_read_b128 v[96:99], v29 offset:57344\n" \
"v_mfma_f32_16x16x32_bf16 v[248:251], a[232:235], v[108:111], v[248:251]\n" \
"v_mfma_f32_16x16x32_bf16 v[252:255], a[228:231], v[104:107], v[252:255]\n" \
"s_waitcnt lgkmcnt(3)\n" \
"ds_read_b128 v[100:103], v29 offset:59392\n" \
"v_mfma_f32_16x16x32_bf16 v[252:255], a[236:239], v[108:111], v[252:255]\n" \
"v_mfma_f32_16x16x32_bf16 v[240:243], a[240:243], v[112:115], v[240:243]\n" \
"ds_read_b128 v[104:107], v29 offset:58368\n" \
"v_mfma_f32_16x16x32_bf16 v[240:243], a[248:251], v[116:119], v[240:243]\n" \
"v_mfma_f32_16x16x32_bf16 v[244:247], a[244:247], v[112:115], v[244:247]\n" \
"s_waitcnt lgkmcnt(3)\n" \
"ds_read_b128 v[108:111], v29 offset:60416\n" \
"v_mfma_f32_16x16x32_bf16 v[244:247], a[252:255], v[116:119], v[244:247]\n" \
"v_mfma_f32_16x16x32_bf16 v[248:251], a[240:243], v[120:123], v[248:251]\n" \
"ds_read_b128 v[112:115], v29 offset:61440\n" \
"v_mfma_f32_16x16x32_bf16 v[248:251], a[248:251], v[124:127], v[248:251]\n" \
"v_mfma_f32_16x16x32_bf16 v[252:255], a[244:247], v[120:123], v[252:255]\n" \
"ds_read_b128 v[116:119], v29 offset:63488\n" \
"v_mfma_f32_16x16x32_bf16 v[252:255], a[252:255], v[124:127], v[252:255]\n" \
"s_waitcnt vmcnt(18) lgkmcnt(4)\n" \
"s_barrier\n" \
"v_mfma_f32_16x16x32_bf16 v[240:243], a[64:67], v[96:99], v[240:243]\n" \
"ds_read_b128 v[120:123], v29 offset:62464\n" \
"v_mfma_f32_16x16x32_bf16 v[240:243], a[72:75], v[100:103], v[240:243]\n" \
"v_mfma_f32_16x16x32_bf16 v[244:247], a[68:71], v[96:99], v[244:247]\n" \
"s_waitcnt lgkmcnt(3)\n" \
"ds_read_b128 v[124:127], v29 offset:64512\n" \
"v_mfma_f32_16x16x32_bf16 v[244:247], a[76:79], v[100:103], v[244:247]\n" \
"v_mfma_f32_16x16x32_bf16 v[248:251], a[64:67], v[104:107], v[248:251]\n" \
"ds_read_b128 v[96:99], v30 offset:16384\n" \
"v_mfma_f32_16x16x32_bf16 v[248:251], a[72:75], v[108:111], v[248:251]\n" \
"v_mfma_f32_16x16x32_bf16 v[252:255], a[68:71], v[104:107], v[252:255]\n" \
"s_waitcnt lgkmcnt(3)\n" \
"ds_read_b128 v[100:103], v30 offset:18432\n" \
"v_mfma_f32_16x16x32_bf16 v[252:255], a[76:79], v[108:111], v[252:255]\n" \
"v_mfma_f32_16x16x32_bf16 v[240:243], a[80:83], v[112:115], v[240:243]\n" \
"ds_read_b128 v[104:107], v30 offset:17408\n" \
"v_mfma_f32_16x16x32_bf16 v[240:243], a[88:91], v[116:119], v[240:243]\n" \
"v_mfma_f32_16x16x32_bf16 v[244:247], a[84:87], v[112:115], v[244:247]\n" \
"s_waitcnt lgkmcnt(3)\n" \
"ds_read_b128 v[108:111], v30 offset:19456\n" \
"v_mfma_f32_16x16x32_bf16 v[244:247], a[92:95], v[116:119], v[244:247]\n" \
"v_mfma_f32_16x16x32_bf16 v[248:251], a[80:83], v[120:123], v[248:251]\n" \
"ds_read_b128 v[112:115], v30 offset:20480\n" \
"v_mfma_f32_16x16x32_bf16 v[248:251], a[88:91], v[124:127], v[248:251]\n" \
"v_mfma_f32_16x16x32_bf16 v[252:255], a[84:87], v[120:123], v[252:255]\n" \
"s_waitcnt lgkmcnt(3)\n" \
"ds_read_b128 v[116:119], v30 offset:22528\n" \
"v_mfma_f32_16x16x32_bf16 v[252:255], a[92:95], v[124:127], v[252:255]\n" \
"v_mfma_f32_16x16x32_bf16 v[240:243], a[128:131], v[96:99], v[240:243]\n" \
"ds_read_b128 v[120:123], v30 offset:21504\n" \
"v_mfma_f32_16x16x32_bf16 v[240:243], a[136:139], v[100:103], v[240:243]\n" \
"v_mfma_f32_16x16x32_bf16 v[244:247], a[132:135], v[96:99], v[244:247]\n" \
"s_waitcnt lgkmcnt(3)\n" \
"ds_read_b128 v[124:127], v30 offset:23552\n" \
"v_mfma_f32_16x16x32_bf16 v[244:247], a[140:143], v[100:103], v[244:247]\n" \
"v_mfma_f32_16x16x32_bf16 v[248:251], a[128:131], v[104:107], v[248:251]\n" \
"ds_read_b128 v[96:99], v30 offset:24576\n" \
"v_mfma_f32_16x16x32_bf16 v[248:251], a[136:139], v[108:111], v[248:251]\n" \
"v_mfma_f32_16x16x32_bf16 v[252:255], a[132:135], v[104:107], v[252:255]\n" \
"s_waitcnt lgkmcnt(3)\n" \
"ds_read_b128 v[100:103], v30 offset:26624\n" \
"v_mfma_f32_16x16x32_bf16 v[252:255], a[140:143], v[108:111], v[252:255]\n" \
"v_mfma_f32_16x16x32_bf16 v[240:243], a[144:147], v[112:115], v[240:243]\n" \
"ds_read_b128 v[104:107], v30 offset:25600\n" \
"v_mfma_f32_16x16x32_bf16 v[240:243], a[152:155], v[116:119], v[240:243]\n" \
"v_mfma_f32_16x16x32_bf16 v[244:247], a[148:151], v[112:115], v[244:247]\n" \
"s_waitcnt lgkmcnt(3)\n" \
"ds_read_b128 v[108:111], v30 offset:27648\n" \
"v_mfma_f32_16x16x32_bf16 v[244:247], a[156:159], v[116:119], v[244:247]\n" \
"v_mfma_f32_16x16x32_bf16 v[248:251], a[144:147], v[120:123], v[248:251]\n" \
"ds_read_b128 v[112:115], v30 offset:28672\n" \
"v_mfma_f32_16x16x32_bf16 v[248:251], a[152:155], v[124:127], v[248:251]\n" \
"v_mfma_f32_16x16x32_bf16 v[252:255], a[148:151], v[120:123], v[252:255]\n" \
"ds_read_b128 v[116:119], v30 offset:30720\n" \
"v_mfma_f32_16x16x32_bf16 v[252:255], a[156:159], v[124:127], v[252:255]\n" \
"s_waitcnt vmcnt(12) lgkmcnt(4)\n" \
"s_barrier\n" \
"v_mfma_f32_16x16x32_bf16 v[240:243], a[0:3], v[96:99], v[240:243]\n" \
"ds_read_b128 v[120:123], v30 offset:29696\n" \
"v_mfma_f32_16x16x32_bf16 v[240:243], a[8:11], v[100:103], v[240:243]\n" \
"v_mfma_f32_16x16x32_bf16 v[244:247], a[4:7], v[96:99], v[244:247]\n" \
"s_waitcnt lgkmcnt(3)\n" \
"ds_read_b128 v[124:127], v30 offset:31744\n" \
"v_mfma_f32_16x16x32_bf16 v[244:247], a[12:15], v[100:103], v[244:247]\n" \
"v_mfma_f32_16x16x32_bf16 v[248:251], a[0:3], v[104:107], v[248:251]\n" \
"ds_read_b128 v[96:99], v28 offset:16384\n" \
"v_mfma_f32_16x16x32_bf16 v[248:251], a[8:11], v[108:111], v[248:251]\n" \
"v_mfma_f32_16x16x32_bf16 v[252:255], a[4:7], v[104:107], v[252:255]\n" \
"s_waitcnt lgkmcnt(3)\n" \
"ds_read_b128 v[100:103], v28 offset:18432\n" \
"v_mfma_f32_16x16x32_bf16 v[252:255], a[12:15], v[108:111], v[252:255]\n" \
"v_mfma_f32_16x16x32_bf16 v[240:243], a[16:19], v[112:115], v[240:243]\n" \
"ds_read_b128 v[104:107], v28 offset:17408\n" \
"v_mfma_f32_16x16x32_bf16 v[240:243], a[24:27], v[116:119], v[240:243]\n" \
"v_mfma_f32_16x16x32_bf16 v[244:247], a[20:23], v[112:115], v[244:247]\n" \
"s_waitcnt lgkmcnt(3)\n" \
"ds_read_b128 v[108:111], v28 offset:19456\n" \
"s_add_u32 s97, s78, 8192\n" \
"v_mfma_f32_16x16x32_bf16 v[244:247], a[28:31], v[116:119], v[244:247]\n" \
"buffer_load_dwordx4 v[232:235], v4, s[36:39], s97 offen offset:2048 sc1\n" \
"v_mfma_f32_16x16x32_bf16 v[248:251], a[16:19], v[120:123], v[248:251]\n" \
"ds_read_b128 v[112:115], v28 offset:20480\n" \
"s_waitcnt vmcnt(1)\n" \
"v_mfma_f32_16x16x32_bf16 v[248:251], a[24:27], v[124:127], v[248:251]\n" \
"v_max_u32 v18, v168, v169\n" \
"v_max3_u32 v18, v18, v170, v171\n" \
"v_mfma_f32_16x16x32_bf16 v[252:255], a[20:23], v[120:123], v[252:255]\n" \
"s_waitcnt lgkmcnt(3)\n" \
"ds_read_b128 v[116:119], v28 offset:22528\n" \
"v_max3_u32 v18, v18, v172, v173\n" \
"v_mfma_f32_16x16x32_bf16 v[252:255], a[28:31], v[124:127], v[252:255]\n" \
"buffer_load_dwordx4 v[236:239], v4, s[36:39], s97 offen offset:3072 sc1\n" \
"v_mfma_f32_16x16x32_bf16 v[240:243], a[32:35], v[96:99], v[240:243]\n" \
"ds_read_b128 v[120:123], v28 offset:21504\n" \
"v_max3_u32 v18, v18, v174, v175\n" \
"v_mfma_f32_16x16x32_bf16 v[240:243], a[40:43], v[100:103], v[240:243]\n" \
"buffer_store_dwordx4 v[80:83], v184, s[32:35], s92 offen\n" \
"v_mfma_f32_16x16x32_bf16 v[244:247], a[36:39], v[96:99], v[244:247]\n" \
"s_waitcnt lgkmcnt(3)\n" \
"ds_read_b128 v[124:127], v28 offset:23552\n" \
"v_max3_u32 v18, v18, v224, v225\n" \
"v_mfma_f32_16x16x32_bf16 v[244:247], a[44:47], v[100:103], v[244:247]\n" \
"v_max3_u32 v18, v18, v226, v227\n" \
"v_max3_u32 v18, v18, v228, v229\n" \
"v_mfma_f32_16x16x32_bf16 v[248:251], a[32:35], v[104:107], v[248:251]\n" \
"ds_read_b128 v[96:99], v28 offset:24576\n" \
"v_max3_u32 v18, v18, v230, v231\n" \
"v_mfma_f32_16x16x32_bf16 v[248:251], a[40:43], v[108:111], v[248:251]\n" \
"v_cmp_eq_u32 vcc, -1, v18\n" \
"s_cbranch_vccnz LIM_%=\nLIMR_%=:\n" \
"v_mfma_f32_16x16x32_bf16 v[252:255], a[36:39], v[104:107], v[252:255]\n" \
"s_waitcnt lgkmcnt(3)\n" \
"ds_read_b128 v[100:103], v28 offset:26624\n" \
"v_mfma_f32_16x16x32_bf16 v[252:255], a[44:47], v[108:111], v[252:255]\n" \
"v_mfma_f32_16x16x32_bf16 v[240:243], a[48:51], v[112:115], v[240:243]\n" \
"ds_read_b128 v[104:107], v28 offset:25600\n" \
"v_mfma_f32_16x16x32_bf16 v[240:243], a[56:59], v[116:119], v[240:243]\n" \
"buffer_store_dwordx4 v[84:87], v184, s[32:35], s93 offen\n" \
"v_mfma_f32_16x16x32_bf16 v[244:247], a[52:55], v[112:115], v[244:247]\n" \
"s_waitcnt lgkmcnt(3)\n" \
"ds_read_b128 v[108:111], v28 offset:27648\n" \
"v_mfma_f32_16x16x32_bf16 v[244:247], a[60:63], v[116:119], v[244:247]\n" \
"v_mfma_f32_16x16x32_bf16 v[248:251], a[48:51], v[120:123], v[248:251]\n" \
"ds_read_b128 v[112:115], v28 offset:28672\n" \
"v_mfma_f32_16x16x32_bf16 v[248:251], a[56:59], v[124:127], v[248:251]\n" \
"v_mfma_f32_16x16x32_bf16 v[252:255], a[52:55], v[120:123], v[252:255]\n" \
"ds_read_b128 v[116:119], v28 offset:30720\n" \
"v_mfma_f32_16x16x32_bf16 v[252:255], a[60:63], v[124:127], v[252:255]\n" \
"s_waitcnt lgkmcnt(4)\n" \
"s_barrier\n" \
"v_mfma_f32_16x16x32_bf16 v[240:243], a[96:99], v[96:99], v[240:243]\n" \
"ds_read_b128 v[120:123], v28 offset:29696\n" \
"v_mfma_f32_16x16x32_bf16 v[240:243], a[104:107], v[100:103], v[240:243]\n" \
"v_mfma_f32_16x16x32_bf16 v[244:247], a[100:103], v[96:99], v[244:247]\n" \
"s_waitcnt lgkmcnt(3)\n" \
"ds_read_b128 v[124:127], v28 offset:31744\n" \
"v_mfma_f32_16x16x32_bf16 v[244:247], a[108:111], v[100:103], v[244:247]\n" \
"v_mfma_f32_16x16x32_bf16 v[248:251], a[96:99], v[104:107], v[248:251]\n" \
"v_mfma_f32_16x16x32_bf16 v[248:251], a[104:107], v[108:111], v[248:251]\n" \
"v_mfma_f32_16x16x32_bf16 v[252:255], a[100:103], v[104:107], v[252:255]\n" \
"s_waitcnt lgkmcnt(2)\n" \
"v_mfma_f32_16x16x32_bf16 v[252:255], a[108:111], v[108:111], v[252:255]\n" \
"v_mfma_f32_16x16x32_bf16 v[240:243], a[112:115], v[112:115], v[240:243]\n" \
"v_mfma_f32_16x16x32_bf16 v[240:243], a[120:123], v[116:119], v[240:243]\n" \
"v_mfma_f32_16x16x32_bf16 v[244:247], a[116:119], v[112:115], v[244:247]\n" \
"s_waitcnt lgkmcnt(0)\n" \
"v_mfma_f32_16x16x32_bf16 v[244:247], a[124:127], v[116:119], v[244:247]\n" \
"v_mfma_f32_16x16x32_bf16 v[248:251], a[112:115], v[120:123], v[248:251]\n" \
"v_mfma_f32_16x16x32_bf16 v[248:251], a[120:123], v[124:127], v[248:251]\n" \
"v_mfma_f32_16x16x32_bf16 v[252:255], a[116:119], v[120:123], v[252:255]\n" \
"v_mfma_f32_16x16x32_bf16 v[252:255], a[124:127], v[124:127], v[252:255]\n" \
"v_mfma_f32_16x16x32_bf16 v[240:243], v[24:27], v[168:171], v[240:243]\n" \
"v_mfma_f32_16x16x32_bf16 v[244:247], v[22:25], v[168:171], v[244:247]\n" \
"v_mfma_f32_16x16x32_bf16 v[248:251], v[24:27], v[172:175], v[248:251]\n" \
"v_mfma_f32_16x16x32_bf16 v[252:255], v[22:25], v[172:175], v[252:255]\n" \
"v_mfma_f32_16x16x32_bf16 v[240:243], v[24:27], v[224:227], v[240:243]\n" \
"v_mfma_f32_16x16x32_bf16 v[244:247], v[22:25], v[224:227], v[244:247]\n" \
"v_mfma_f32_16x16x32_bf16 v[248:251], v[24:27], v[228:231], v[248:251]\n" \
"v_mfma_f32_16x16x32_bf16 v[252:255], v[22:25], v[228:231], v[252:255]\n" \
"s_mov_b32 s92, s83\n" \
"s_add_u32 s93, s83, 32768\n" \
"s_waitcnt vmcnt(2)\n" \
"v_max_u32 v18, v232, v233\n" \
"v_max3_u32 v18, v18, v234, v235\n" \
"v_max3_u32 v18, v18, v236, v237\n" \
"v_max3_u32 v18, v18, v238, v239\n" \
"v_cmp_eq_u32 vcc, -1, v18\n" \
"s_cbranch_vccnz LIN_%=\nLINR_%=:\n" \
"v_mfma_f32_16x16x32_bf16 v[240:243], v[24:27], v[232:235], v[240:243]\n" \
"v_mfma_f32_16x16x32_bf16 v[244:247], v[22:25], v[232:235], v[244:247]\n" \
"v_mfma_f32_16x16x32_bf16 v[248:251], v[24:27], v[236:239], v[248:251]\n" \
"v_mfma_f32_16x16x32_bf16 v[252:255], v[22:25], v[236:239], v[252:255]\n" \
"s_nop 3\n" \
"v_pk_fma_f32 v[240:241], v[240:241], v[6:7], v[48:49] op_sel_hi:[1,0,1]\n" \
"v_pk_fma_f32 v[242:243], v[242:243], v[6:7], v[50:51] op_sel_hi:[1,0,1]\n" \
"v_pk_fma_f32 v[244:245], v[244:245], v[6:7], v[52:53] op_sel_hi:[1,0,1]\n" \
"v_pk_fma_f32 v[246:247], v[246:247], v[6:7], v[54:55] op_sel_hi:[1,0,1]\n" \
"v_cvt_pk_bf16_f32 v80, v240, v241\n" \
"v_cvt_pk_bf16_f32 v81, v242, v243\n" \
"v_cvt_pk_bf16_f32 v82, v244, v245\n" \
"v_cvt_pk_bf16_f32 v83, v246, v247\n" \
"s_nop 1\n" \
"v_permlane16_swap_b32 v80, v82\n" \
"v_permlane16_swap_b32 v81, v83\n" \
"buffer_store_dwordx4 v[80:83], v184, s[32:35], s92 offen\n" \
"v_pk_fma_f32 v[248:249], v[248:249], v[8:9], v[56:57] op_sel_hi:[1,0,1]\n" \
"v_pk_fma_f32 v[250:251], v[250:251], v[8:9], v[58:59] op_sel_hi:[1,0,1]\n" \
"v_pk_fma_f32 v[252:253], v[252:253], v[8:9], v[60:61] op_sel_hi:[1,0,1]\n" \
"v_pk_fma_f32 v[254:255], v[254:255], v[8:9], v[62:63] op_sel_hi:[1,0,1]\n" \
"v_cvt_pk_bf16_f32 v84, v248, v249\n" \
"v_cvt_pk_bf16_f32 v85, v250, v251\n" \
"v_cvt_pk_bf16_f32 v86, v252, v253\n" \
"v_cvt_pk_bf16_f32 v87, v254, v255\n" \
"s_nop 1\n" \
"v_permlane16_swap_b32 v84, v86\n" \
"v_permlane16_swap_b32 v85, v87\n" \
"buffer_store_dwordx4 v[84:87], v184, s[32:35], s93 offen\n" \
"s_branch LEND_%=\n" \
"LPUK_%=:\n" \
"s_mov_b32 s25, 0\n" \
"LPUKL_%=:\n" \
"s_sleep 2\n" \
"buffer_load_dwordx2 v[58:59], v57, s[36:39], 0 offen sc1\n" \
"s_add_u32 s25, s25, 1\n" \
"s_waitcnt vmcnt(0)\n" \
"v_cmp_ne_u64 vcc, s[14:15], v[58:59]\n" \
"s_cmp_gt_u32 s25, 262144\n" \
"s_cbranch_scc1 LPUKR_%=\n" \
"s_cbranch_vccnz LPUKL_%=\n" \
"s_branch LPUKR_%=\n" \
"LPUV_%=:\n" \
"s_mov_b32 s25, 0\n" \
"LPUVL_%=:\n" \
"s_sleep 2\n" \
"buffer_load_dwordx2 v[58:59], v57, s[36:39], 0 offen sc1\n" \
"s_add_u32 s25, s25, 1\n" \
"s_waitcnt vmcnt(0)\n" \
"v_cmp_ne_u64 vcc, s[14:15], v[58:59]\n" \
"s_cmp_gt_u32 s25, 262144\n" \
"s_cbranch_scc1 LPUVR_%=\n" \
"s_cbranch_vccnz LPUVL_%=\n" \
"s_branch LPUVR_%=\n" \
"LPK_%=:\n" \
"s_mov_b32 s25, 0\n" \
"LPKL_%=:\n" \
"s_sleep 2\n" \
"buffer_load_dwordx2 v[58:59], v57, s[36:39], 0 offen sc1\n" \
"s_add_u32 s25, s25, 1\n" \
"s_waitcnt vmcnt(0)\n" \
"v_cmp_ne_u64 vcc, s[14:15], v[58:59]\n" \
"s_cmp_gt_u32 s25, 262144\n" \
"s_cbranch_scc1 LPKR_%=\n" \
"s_cbranch_vccnz LPKL_%=\n" \
"s_branch LPKR_%=\n" \
"LSW_%=:\n" \
"v_swap_b32 v64, v80\n" \
"v_swap_b32 v65, v81\n" \
"v_swap_b32 v66, v82\n" \
"v_swap_b32 v67, v83\n" \
"v_swap_b32 v68, v84\n" \
"v_swap_b32 v69, v85\n" \
"v_swap_b32 v70, v86\n" \
"v_swap_b32 v71, v87\n" \
"v_swap_b32 v72, v88\n" \
"v_swap_b32 v73, v89\n" \
"v_swap_b32 v74, v90\n" \
"v_swap_b32 v75, v91\n" \
"v_swap_b32 v76, v92\n" \
"v_swap_b32 v77, v93\n" \
"v_swap_b32 v78, v94\n" \
"v_swap_b32 v79, v95\n" \
"s_branch LSWR_%=\n" \
"LIM_%=:\n" \
"s_mov_b32 s25, 0\n" \
"LIML_%=:\n" \
"s_sleep 1\n" \
"s_add_u32 s98, s78, 4096\n" \
"buffer_load_dwordx4 v[168:171], v4, s[36:39], s98 offen offset:2048 sc1\n" \
"buffer_load_dwordx4 v[172:175], v4, s[36:39], s98 offen offset:3072 sc1\n" \
"s_add_u32 s98, s78, 8192\n" \
"buffer_load_dwordx4 v[224:227], v4, s[36:39], s98 offen offset:0 sc1\n" \
"buffer_load_dwordx4 v[228:231], v4, s[36:39], s98 offen offset:1024 sc1\n" \
"s_add_u32 s25, s25, 1\n" \
"s_waitcnt vmcnt(0)\n" \
"v_max_u32 v18, v168, v169\n" \
"v_max3_u32 v18, v18, v170, v171\n" \
"v_max3_u32 v18, v18, v172, v173\n" \
"v_max3_u32 v18, v18, v174, v175\n" \
"v_max3_u32 v18, v18, v224, v225\n" \
"v_max3_u32 v18, v18, v226, v227\n" \
"v_max3_u32 v18, v18, v228, v229\n" \
"v_max3_u32 v18, v18, v230, v231\n" \
"v_cmp_eq_u32 vcc, -1, v18\n" \
"s_cmp_gt_u32 s25, 262144\n" \
"s_cbranch_scc1 LIMR_%=\n" \
"s_cbranch_vccnz LIML_%=\n" \
"s_branch LIMR_%=\n" \
"LIN_%=:\n" \
"s_mov_b32 s25, 0\n" \
"LINL_%=:\n" \
"s_sleep 1\n" \
"s_add_u32 s98, s78, 8192\n" \
"buffer_load_dwordx4 v[232:235], v4, s[36:39], s98 offen offset:2048 sc1\n" \
"buffer_load_dwordx4 v[236:239], v4, s[36:39], s98 offen offset:3072 sc1\n" \
"s_add_u32 s25, s25, 1\n" \
"s_waitcnt vmcnt(0)\n" \
"v_max_u32 v18, v232, v233\n" \
"v_max3_u32 v18, v18, v234, v235\n" \
"v_max3_u32 v18, v18, v236, v237\n" \
"v_max3_u32 v18, v18, v238, v239\n" \
"v_cmp_eq_u32 vcc, -1, v18\n" \
"s_cmp_gt_u32 s25, 262144\n" \
"s_cbranch_scc1 LINR_%=\n" \
"s_cbranch_vccnz LINL_%=\n" \
"s_branch LINR_%=\n" \
"LPZ_%=:\n" \
"s_mov_b32 s25, 0\n" \
"LPZL_%=:\n" \
"s_sleep 2\n" \
"buffer_load_dwordx2 v[18:19], v17, s[36:39], 0 offen sc1\n" \
"s_add_u32 s25, s25, 1\n" \
"s_waitcnt vmcnt(0)\n" \
"v_cmp_ne_u64 vcc, s[14:15], v[18:19]\n" \
"s_cmp_gt_u32 s25, 262144\n" \
"s_cbranch_scc1 LPZR_%=\n" \
"s_cbranch_vccnz LPZL_%=\n" \
"s_branch LPZR_%=\n" \
"LPS_%=:\n" \
"s_mov_b32 s25, 0\n" \
"LPSL_%=:\n" \
"s_sleep 2\n" \
"buffer_load_dwordx2 v[18:19], v17, s[36:39], 0 offen sc1\n" \
"s_add_u32 s25, s25, 1\n" \
"s_waitcnt vmcnt(0)\n" \
"v_cmp_ne_u64 vcc, s[14:15], v[18:19]\n" \
"s_cmp_gt_u32 s25, 262144\n" \
"s_cbranch_scc1 LPSR_%=\n" \
"s_cbranch_vccnz LPSL_%=\n" \
"s_branch LPSR_%=\n" \
"LPL_%=:\n" \
"s_mov_b32 s25, 0\n" \
"LPLL_%=:\n" \
"s_sleep 2\n" \
"buffer_load_dwordx2 v[18:19], v17, s[36:39], 0 offen sc1\n" \
"s_add_u32 s25, s25, 1\n" \
"s_waitcnt vmcnt(0)\n" \
"v_cmp_ne_u64 vcc, s[14:15], v[18:19]\n" \
"s_cmp_gt_u32 s25, 262144\n" \
"s_cbranch_scc1 LPLR_%=\n" \
"s_cbranch_vccnz LPLL_%=\n" \
"s_branch LPLR_%=\n" \
"LPE_%=:\n" \
"s_mov_b32 s25, 0\n" \
"LPEL_%=:\n" \
"s_sleep 2\n" \
"buffer_load_dwordx2 v[18:19], v17, s[36:39], 0 offen sc1\n" \
"s_add_u32 s25, s25, 1\n" \
"s_waitcnt vmcnt(0)\n" \
"v_cmp_ne_u64 vcc, s[14:15], v[18:19]\n" \
"s_cmp_gt_u32 s25, 262144\n" \
"s_cbranch_scc1 LPER_%=\n" \
"s_cbranch_vccnz LPEL_%=\n" \
"s_branch LPER_%=\n" \
"LPE0_%=:\n" \
"s_mov_b32 s25, 0\n" \
"LPE0L_%=:\n" \
"s_sleep 2\n" \
"buffer_load_dwordx2 v[18:19], v17, s[36:39], 0 offen sc1\n" \
"s_add_u32 s25, s25, 1\n" \
"s_waitcnt vmcnt(0)\n" \
"v_cmp_ne_u64 vcc, s[14:15], v[18:19]\n" \
"s_cmp_gt_u32 s25, 262144\n" \
"s_cbranch_scc1 LPE0R_%=\n" \
"s_cbranch_vccnz LPE0L_%=\n" \
"s_branch LPE0R_%=\n" \
"LFBA_%=:\n" \
"s_mov_b32 s99, 0\n" \
"s_branch LFB_%=\n" \
"LFBC_%=:\n" \
"s_mov_b32 s99, 2\n" \
"s_branch LFB_%=\n" \
"LFBB_%=:\n" \
"s_mov_b32 s99, 1\n" \
"LFB_%=:\n" \
"s_waitcnt vmcnt(0) lgkmcnt(0)\n" \
"s_nop 15\n" \
"s_lshl_b32 s25, %1, 2\n" \
"s_add_u32 s25, s25, %2\n" \
"s_lshl_b32 s25, s25, 13\n" \
"s_add_u32 s25, s25, 16777216\n" \
"buffer_store_dwordx4 v[64:67], v4, s[36:39], s25 offen offset:0\n" \
"buffer_store_dwordx4 v[68:71], v4, s[36:39], s25 offen offset:1024\n" \
"buffer_store_dwordx4 v[72:75], v4, s[36:39], s25 offen offset:2048\n" \
"buffer_store_dwordx4 v[76:79], v4, s[36:39], s25 offen offset:3072\n" \
"buffer_store_dwordx4 v[80:83], v7, s[36:39], s25 offen offset:0\n" \
"buffer_store_dwordx4 v[84:87], v7, s[36:39], s25 offen offset:1024\n" \
"buffer_store_dwordx4 v[88:91], v7, s[36:39], s25 offen offset:2048\n" \
"buffer_store_dwordx4 v[92:95], v7, s[36:39], s25 offen offset:3072\n" \
"s_add_u32 s25, s25, 8388608\n" \
"buffer_store_dwordx4 v[96:99], v4, s[36:39], s25 offen offset:0\n" \
"buffer_store_dwordx4 v[100:103], v4, s[36:39], s25 offen offset:1024\n" \
"buffer_store_dwordx4 v[104:107], v4, s[36:39], s25 offen offset:2048\n" \
"buffer_store_dwordx4 v[108:111], v4, s[36:39], s25 offen offset:3072\n" \
"buffer_store_dwordx4 v[112:115], v7, s[36:39], s25 offen offset:0\n" \
"buffer_store_dwordx4 v[116:119], v7, s[36:39], s25 offen offset:1024\n" \
"buffer_store_dwordx4 v[120:123], v7, s[36:39], s25 offen offset:2048\n" \
"buffer_store_dwordx4 v[124:127], v7, s[36:39], s25 offen offset:3072\n" \
"s_add_u32 s25, s25, 58720256\n" \
"buffer_store_dwordx4 v[128:131], v4, s[36:39], s25 offen offset:0\n" \
"buffer_store_dwordx4 v[132:135], v4, s[36:39], s25 offen offset:1024\n" \
"buffer_store_dwordx4 v[136:139], v4, s[36:39], s25 offen offset:2048\n" \
"buffer_store_dwordx4 v[140:143], v4, s[36:39], s25 offen offset:3072\n" \
"buffer_store_dwordx4 v[144:147], v7, s[36:39], s25 offen offset:0\n" \
"buffer_store_dwordx4 v[148:151], v7, s[36:39], s25 offen offset:1024\n" \
"buffer_store_dwordx4 v[152:155], v7, s[36:39], s25 offen offset:2048\n" \
"buffer_store_dwordx4 v[156:159], v7, s[36:39], s25 offen offset:3072\n" \
"s_add_u32 s25, s25, 37748736\n" \
"buffer_store_dwordx4 v[160:163], v4, s[36:39], s25 offen offset:0\n" \
"buffer_store_dwordx4 v[164:167], v4, s[36:39], s25 offen offset:1024\n" \
"buffer_store_dwordx4 v[168:171], v4, s[36:39], s25 offen offset:2048\n" \
"buffer_store_dwordx4 v[172:175], v4, s[36:39], s25 offen offset:3072\n" \
"s_waitcnt vmcnt(0)\n" \
"v_writelane_b32 v172, vcc_lo, 0\n" \
"v_writelane_b32 v172, vcc_hi, 1\n" \
"v_writelane_b32 v172, s64, 2\n" \
"v_writelane_b32 v172, s65, 3\n" \
"v_writelane_b32 v172, s66, 4\n" \
"v_writelane_b32 v172, s67, 5\n" \
"v_writelane_b32 v172, s68, 6\n" \
"v_writelane_b32 v172, s69, 7\n" \
"v_writelane_b32 v172, s70, 8\n" \
"v_writelane_b32 v172, s71, 9\n" \
"v_writelane_b32 v172, s72, 10\n" \
"v_writelane_b32 v172, s73, 11\n" \
"s_cmp_eq_u32 s99, 1\n" \
"s_cselect_b32 s64, s51, s50\n" \
"s_cselect_b32 s65, 2, 4\n" \
"s_cselect_b64 s[54:55], s[54:55], 0\n" \
"s_lshr_b32 s64, s64, 18\n" \
"s_lshl_b32 s64, s64, 5\n" \
"v_mov_b32 v160, 0x3f803f80\n" \
"v_mov_b32 v161, 0x3f803f80\n" \
"v_mov_b32 v162, 0x3f803f80\n" \
"v_mov_b32 v163, 0x3f803f80\n" \
"LFG_%=:\n" \
"s_mov_b32 s69, 0\n" \
"v_bfe_u32 v167, v4, 4, 4\n" \
"v_lshrrev_b32 v168, 8, v4\n" \
"v_add_u32 v165, s64, v167\n" \
"s_lshl_b32 s67, s6, 8\n" \
"v_lshl_add_u32 v128, v167, 1, s67\n" \
"v_lshl_add_u32 v128, v168, 16, v128\n" \
"v_add_u32 v129, 0x2000, v128\n" \
"v_add_u32 v130, 0x2000, v129\n" \
"v_add_u32 v131, 0x2000, v130\n" \
"v_add_u32 v132, 0x2000, v131\n" \
"v_add_u32 v133, 0x2000, v132\n" \
"v_add_u32 v134, 0x2000, v133\n" \
"v_add_u32 v135, 0x2000, v134\n" \
"LFQ_%=:\n" \
"s_load_dwordx2 s[70:71], %0, 0x0\n" \
"s_load_dwordx2 s[72:73], %0, 0x38\n" \
"v_lshlrev_b32 v166, 11, v165\n" \
"v_lshl_add_u32 v166, v168, 4, v166\n" \
"s_mov_b32 s66, 0\n" \
"v_mov_b32 v136, 0\n" \
"v_mov_b32 v137, 0\n" \
"v_mov_b32 v138, 0\n" \
"v_mov_b32 v139, 0\n" \
"v_mov_b32 v140, 0\n" \
"v_mov_b32 v141, 0\n" \
"v_mov_b32 v142, 0\n" \
"v_mov_b32 v143, 0\n" \
"s_waitcnt lgkmcnt(0)\n" \
"LFK_%=:\n" \
"global_load_dwordx4 v[112:115], v166, s[70:71]\n" \
"global_load_ushort v96, v128, s[72:73] offset:0\n" \
"global_load_ushort v97, v129, s[72:73] offset:0\n" \
"global_load_ushort v98, v130, s[72:73] offset:0\n" \
"global_load_ushort v99, v131, s[72:73] offset:0\n" \
"global_load_ushort v100, v132, s[72:73] offset:0\n" \
"global_load_ushort v101, v133, s[72:73] offset:0\n" \
"global_load_ushort v102, v134, s[72:73] offset:0\n" \
"global_load_ushort v103, v135, s[72:73] offset:0\n" \
"global_load_ushort v104, v128, s[72:73] offset:32\n" \
"global_load_ushort v105, v129, s[72:73] offset:32\n" \
"global_load_ushort v106, v130, s[72:73] offset:32\n" \
"global_load_ushort v107, v131, s[72:73] offset:32\n" \
"global_load_ushort v108, v132, s[72:73] offset:32\n" \
"global_load_ushort v109, v133, s[72:73] offset:32\n" \
"global_load_ushort v110, v134, s[72:73] offset:32\n" \
"global_load_ushort v111, v135, s[72:73] offset:32\n" \
"v_add_u32 v166, 64, v166\n" \
"s_add_u32 s72, s72, 262144\ns_addc_u32 s73, s73, 0\n" \
"s_add_u32 s66, s66, 1\n" \
"s_waitcnt vmcnt(0)\n" \
"v_lshl_or_b32 v116, v97, 16, v96\n" \
"v_lshl_or_b32 v117, v99, 16, v98\n" \
"v_lshl_or_b32 v118, v101, 16, v100\n" \
"v_lshl_or_b32 v119, v103, 16, v102\n" \
"v_lshl_or_b32 v120, v105, 16, v104\n" \
"v_lshl_or_b32 v121, v107, 16, v106\n" \
"v_lshl_or_b32 v122, v109, 16, v108\n" \
"v_lshl_or_b32 v123, v111, 16, v110\n" \
"s_nop 1\n" \
"v_mfma_f32_16x16x32_bf16 v[136:139], v[116:119], v[112:115], v[136:139]\n" \
"v_mfma_f32_16x16x32_bf16 v[140:143], v[120:123], v[112:115], v[140:143]\n" \
"s_cmp_lt_u32 s66, 32\n" \
"s_cbranch_scc1 LFK_%=\n" \
"s_nop 7\n" \
"v_mov_b32 v144, v148\n" \
"v_mov_b32 v145, v149\n" \
"v_mov_b32 v146, v150\n" \
"v_mov_b32 v147, v151\n" \
"v_mov_b32 v148, v152\n" \
"v_mov_b32 v149, v153\n" \
"v_mov_b32 v150, v154\n" \
"v_mov_b32 v151, v155\n" \
"v_mov_b32 v152, v156\n" \
"v_mov_b32 v153, v157\n" \
"v_mov_b32 v154, v158\n" \
"v_mov_b32 v155, v159\n" \
"v_cvt_pk_bf16_f32 v156, v136, v137\n" \
"v_cvt_pk_bf16_f32 v157, v138, v139\n" \
"v_cvt_pk_bf16_f32 v158, v140, v141\n" \
"v_cvt_pk_bf16_f32 v159, v142, v143\n" \
"v_add_u32 v128, 64, v128\n" \
"v_add_u32 v129, 64, v129\n" \
"v_add_u32 v130, 64, v130\n" \
"v_add_u32 v131, 64, v131\n" \
"v_add_u32 v132, 64, v132\n" \
"v_add_u32 v133, 64, v133\n" \
"v_add_u32 v134, 64, v134\n" \
"v_add_u32 v135, 64, v135\n" \
"s_add_u32 s69, s69, 1\n" \
"s_cmp_lt_u32 s69, 4\n" \
"s_cbranch_scc1 LFQ_%=\n" \
"v_mov_b32 v64, 0\n" \
"v_mov_b32 v65, 0\n" \
"v_mov_b32 v66, 0\n" \
"v_mov_b32 v67, 0\n" \
"v_mov_b32 v68, 0\n" \
"v_mov_b32 v69, 0\n" \
"v_mov_b32 v70, 0\n" \
"v_mov_b32 v71, 0\n" \
"v_mov_b32 v72, 0\n" \
"v_mov_b32 v73, 0\n" \
"v_mov_b32 v74, 0\n" \
"v_mov_b32 v75, 0\n" \
"v_mov_b32 v76, 0\n" \
"v_mov_b32 v77, 0\n" \
"v_mov_b32 v78, 0\n" \
"v_mov_b32 v79, 0\n" \
"v_mov_b32 v80, 0\n" \
"v_mov_b32 v81, 0\n" \
"v_mov_b32 v82, 0\n" \
"v_mov_b32 v83, 0\n" \
"v_mov_b32 v84, 0\n" \
"v_mov_b32 v85, 0\n" \
"v_mov_b32 v86, 0\n" \
"v_mov_b32 v87, 0\n" \
"v_mov_b32 v88, 0\n" \
"v_mov_b32 v89, 0\n" \
"v_mov_b32 v90, 0\n" \
"v_mov_b32 v91, 0\n" \
"v_mov_b32 v92, 0\n" \
"v_mov_b32 v93, 0\n" \
"v_mov_b32 v94, 0\n" \
"v_mov_b32 v95, 0\n" \
"v_mov_b32 v132, 0\n" \
"v_mov_b32 v133, 0\n" \
"v_mov_b32 v134, 0\n" \
"v_mov_b32 v135, 0\n" \
"v_mov_b32 v164, 0xff800000\n" \
"v_mov_b32 v169, 0xff800000\n" \
"s_mov_b32 s69, 0\n" \
"s_lshr_b32 s68, s64, 5\n" \
"LFP_%=:\n" \
"s_mov_b32 s66, 0\n" \
"s_lshl_b32 s67, s6, 20\n" \
"s_add_u32 s67, s67, 33554432\n" \
"v_lshlrev_b32 v166, 2, v168\n" \
"v_sub_u32 v166, v165, v166\n" \
"LFT_%=:\n" \
"buffer_load_dwordx4 v[96:99], v4, s[36:39], s67 offen offset:0 sc1\n" \
"buffer_load_dwordx4 v[100:103], v4, s[36:39], s67 offen offset:1024 sc1\n" \
"buffer_load_dwordx4 v[104:107], v4, s[36:39], s67 offen offset:2048 sc1\n" \
"buffer_load_dwordx4 v[108:111], v4, s[36:39], s67 offen offset:3072 sc1\n" \
"buffer_load_dwordx4 v[112:115], v7, s[36:39], s67 offen offset:0 sc1\n" \
"buffer_load_dwordx4 v[116:119], v7, s[36:39], s67 offen offset:1024 sc1\n" \
"buffer_load_dwordx4 v[120:123], v7, s[36:39], s67 offen offset:2048 sc1\n" \
"buffer_load_dwordx4 v[124:127], v7, s[36:39], s67 offen offset:3072 sc1\n" \
"s_add_u32 s67, s67, 8192\n" \
"s_waitcnt vmcnt(0)\n" \
"v_mfma_f32_16x16x32_bf16 v[136:139], v[96:99], v[144:147], 0\n" \
"v_mfma_f32_16x16x32_bf16 v[136:139], v[100:103], v[148:151], v[136:139]\n" \
"v_mfma_f32_16x16x32_bf16 v[136:139], v[104:107], v[152:155], v[136:139]\n" \
"v_mfma_f32_16x16x32_bf16 v[136:139], v[108:111], v[156:159], v[136:139]\n" \
"v_mfma_f32_16x16x32_bf16 v[140:143], v[112:115], v[144:147], 0\n" \
"v_mfma_f32_16x16x32_bf16 v[140:143], v[116:119], v[148:151], v[140:143]\n" \
"v_mfma_f32_16x16x32_bf16 v[140:143], v[120:123], v[152:155], v[140:143]\n" \
"v_mfma_f32_16x16x32_bf16 v[140:143], v[124:127], v[156:159], v[140:143]\n" \
"s_nop 7\n" \
"v_cmp_gt_i32 vcc, 0, v166\n" \
"s_nop 1\n" \
"v_cndmask_b32 v136, v136, v169, vcc\n" \
"v_cmp_gt_i32 vcc, 1, v166\n" \
"s_nop 1\n" \
"v_cndmask_b32 v137, v137, v169, vcc\n" \
"v_cmp_gt_i32 vcc, 2, v166\n" \
"s_nop 1\n" \
"v_cndmask_b32 v138, v138, v169, vcc\n" \
"v_cmp_gt_i32 vcc, 3, v166\n" \
"s_nop 1\n" \
"v_cndmask_b32 v139, v139, v169, vcc\n" \
"v_cmp_gt_i32 vcc, 16, v166\n" \
"s_nop 1\n" \
"v_cndmask_b32 v140, v140, v169, vcc\n" \
"v_cmp_gt_i32 vcc, 17, v166\n" \
"s_nop 1\n" \
"v_cndmask_b32 v141, v141, v169, vcc\n" \
"v_cmp_gt_i32 vcc, 18, v166\n" \
"s_nop 1\n" \
"v_cndmask_b32 v142, v142, v169, vcc\n" \
"v_cmp_gt_i32 vcc, 19, v166\n" \
"s_nop 1\n" \
"v_cndmask_b32 v143, v143, v169, vcc\n" \
"s_cmp_lg_u32 s69, 0\n" \
"s_cbranch_scc1 LF2_%=\n" \
"v_max3_f32 v164, v164, v136, v137\n" \
"v_max3_f32 v164, v164, v138, v139\n" \
"v_max3_f32 v164, v164, v140, v141\n" \
"v_max3_f32 v164, v164, v142, v143\n" \
"s_branch LFN_%=\n" \
"LF2_%=:\n" \
"buffer_load_dwordx4 v[96:99], v4, s[36:39], s67 offen offset:0 sc1\n" \
"buffer_load_dwordx4 v[100:103], v4, s[36:39], s67 offen offset:1024 sc1\n" \
"buffer_load_dwordx4 v[104:107], v4, s[36:39], s67 offen offset:2048 sc1\n" \
"buffer_load_dwordx4 v[108:111], v4, s[36:39], s67 offen offset:3072 sc1\n" \
"buffer_load_dwordx4 v[112:115], v7, s[36:39], s67 offen offset:0 sc1\n" \
"buffer_load_dwordx4 v[116:119], v7, s[36:39], s67 offen offset:1024 sc1\n" \
"buffer_load_dwordx4 v[120:123], v7, s[36:39], s67 offen offset:2048 sc1\n" \
"buffer_load_dwordx4 v[124:127], v7, s[36:39], s67 offen offset:3072 sc1\n" \
"v_sub_f32 v136, v136, v164\n" \
"v_sub_f32 v137, v137, v164\n" \
"v_sub_f32 v138, v138, v164\n" \
"v_sub_f32 v139, v139, v164\n" \
"v_sub_f32 v140, v140, v164\n" \
"v_sub_f32 v141, v141, v164\n" \
"v_sub_f32 v142, v142, v164\n" \
"v_sub_f32 v143, v143, v164\n" \
"v_exp_f32 v136, v136\n" \
"v_exp_f32 v137, v137\n" \
"v_exp_f32 v138, v138\n" \
"v_exp_f32 v139, v139\n" \
"v_exp_f32 v140, v140\n" \
"v_exp_f32 v141, v141\n" \
"v_exp_f32 v142, v142\n" \
"v_exp_f32 v143, v143\n" \
"s_nop 0\n" \
"v_cvt_pk_bf16_f32 v128, v136, v137\n" \
"v_cvt_pk_bf16_f32 v129, v138, v139\n" \
"v_cvt_pk_bf16_f32 v130, v140, v141\n" \
"v_cvt_pk_bf16_f32 v131, v142, v143\n" \
"s_waitcnt vmcnt(0)\n" \
"s_nop 1\n" \
"v_mfma_f32_16x16x32_bf16 v[132:135], v[160:163], v[128:131], v[132:135]\n" \
"v_mfma_f32_16x16x32_bf16 v[64:67], v[96:99], v[128:131], v[64:67]\n" \
"v_mfma_f32_16x16x32_bf16 v[68:71], v[100:103], v[128:131], v[68:71]\n" \
"v_mfma_f32_16x16x32_bf16 v[72:75], v[104:107], v[128:131], v[72:75]\n" \
"v_mfma_f32_16x16x32_bf16 v[76:79], v[108:111], v[128:131], v[76:79]\n" \
"v_mfma_f32_16x16x32_bf16 v[80:83], v[112:115], v[128:131], v[80:83]\n" \
"v_mfma_f32_16x16x32_bf16 v[84:87], v[116:119], v[128:131], v[84:87]\n" \
"v_mfma_f32_16x16x32_bf16 v[88:91], v[120:123], v[128:131], v[88:91]\n" \
"v_mfma_f32_16x16x32_bf16 v[92:95], v[124:127], v[128:131], v[92:95]\n" \
"LFN_%=:\n" \
"s_add_u32 s67, s67, 8192\n" \
"v_subrev_u32 v166, 32, v166\n" \
"s_add_u32 s66, s66, 1\n" \
"s_cmp_le_u32 s66, s68\n" \
"s_cbranch_scc1 LFT_%=\n" \
"v_mov_b32 v170, v164\n" \
"s_nop 1\n" \
"v_permlane32_swap_b32 v164, v170\n" \
"s_nop 0\n" \
"v_max_f32 v164, v164, v170\n" \
"v_mov_b32 v170, v164\n" \
"s_nop 1\n" \
"v_permlane16_swap_b32 v164, v170\n" \
"s_nop 0\n" \
"v_max_f32 v164, v164, v170\n" \
"s_add_u32 s69, s69, 1\n" \
"s_cmp_lt_u32 s69, 2\n" \
"s_cbranch_scc1 LFP_%=\n" \
"s_nop 7\n" \
"v_rcp_f32 v170, v132\n" \
"s_lshl_b32 s67, s68, 5\n" \
"s_add_u32 s67, s67, s6\n" \
"s_lshl_b32 s67, s67, 13\n" \
"s_and_b32 s66, s64, 16\n" \
"s_lshl_b32 s66, s66, 6\n" \
"s_add_u32 s67, s67, s66\n" \
"v_mul_f32 v64, v170, v64\n" \
"v_mul_f32 v65, v170, v65\n" \
"v_mul_f32 v66, v170, v66\n" \
"v_mul_f32 v67, v170, v67\n" \
"v_mul_f32 v68, v170, v68\n" \
"v_mul_f32 v69, v170, v69\n" \
"v_mul_f32 v70, v170, v70\n" \
"v_mul_f32 v71, v170, v71\n" \
"v_mul_f32 v72, v170, v72\n" \
"v_mul_f32 v73, v170, v73\n" \
"v_mul_f32 v74, v170, v74\n" \
"v_mul_f32 v75, v170, v75\n" \
"v_mul_f32 v76, v170, v76\n" \
"v_mul_f32 v77, v170, v77\n" \
"v_mul_f32 v78, v170, v78\n" \
"v_mul_f32 v79, v170, v79\n" \
"v_mul_f32 v80, v170, v80\n" \
"v_mul_f32 v81, v170, v81\n" \
"v_mul_f32 v82, v170, v82\n" \
"v_mul_f32 v83, v170, v83\n" \
"v_mul_f32 v84, v170, v84\n" \
"v_mul_f32 v85, v170, v85\n" \
"v_mul_f32 v86, v170, v86\n" \
"v_mul_f32 v87, v170, v87\n" \
"v_mul_f32 v88, v170, v88\n" \
"v_mul_f32 v89, v170, v89\n" \
"v_mul_f32 v90, v170, v90\n" \
"v_mul_f32 v91, v170, v91\n" \
"v_mul_f32 v92, v170, v92\n" \
"v_mul_f32 v93, v170, v93\n" \
"v_mul_f32 v94, v170, v94\n" \
"v_mul_f32 v95, v170, v95\n" \
"v_cvt_pk_bf16_f32 v64, v64, v65\n" \
"v_cvt_pk_bf16_f32 v65, v66, v67\n" \
"v_cvt_pk_bf16_f32 v66, v68, v69\n" \
"v_cvt_pk_bf16_f32 v67, v70, v71\n" \
"v_cvt_pk_bf16_f32 v68, v72, v73\n" \
"v_cvt_pk_bf16_f32 v69, v74, v75\n" \
"v_cvt_pk_bf16_f32 v70, v76, v77\n" \
"v_cvt_pk_bf16_f32 v71, v78, v79\n" \
"v_cvt_pk_bf16_f32 v72, v80, v81\n" \
"v_cvt_pk_bf16_f32 v73, v82, v83\n" \
"v_cvt_pk_bf16_f32 v74, v84, v85\n" \
"v_cvt_pk_bf16_f32 v75, v86, v87\n" \
"v_cvt_pk_bf16_f32 v76, v88, v89\n" \
"v_cvt_pk_bf16_f32 v77, v90, v91\n" \
"v_cvt_pk_bf16_f32 v78, v92, v93\n" \
"v_cvt_pk_bf16_f32 v79, v94, v95\n" \
"buffer_store_dwordx4 v[64:67], v4, s[36:39], s67 offen offset:0 sc1\n" \
"buffer_store_dwordx4 v[68:71], v4, s[36:39], s67 offen offset:2048 sc1\n" \
"buffer_store_dwordx4 v[72:75], v7, s[36:39], s67 offen offset:0 sc1\n" \
"buffer_store_dwordx4 v[76:79], v7, s[36:39], s67 offen offset:2048 sc1\n" \
"s_waitcnt vmcnt(0)\n" \
"s_add_u32 s64, s64, 16\n" \
"s_sub_u32 s65, s65, 1\n" \
"s_cmp_lg_u32 s65, 0\n" \
"s_cbranch_scc1 LFG_%=\n" \
"v_readlane_b32 vcc_lo, v172, 0\n" \
"v_readlane_b32 vcc_hi, v172, 1\n" \
"v_readlane_b32 s64, v172, 2\n" \
"v_readlane_b32 s65, v172, 3\n" \
"v_readlane_b32 s66, v172, 4\n" \
"v_readlane_b32 s67, v172, 5\n" \
"v_readlane_b32 s68, v172, 6\n" \
"v_readlane_b32 s69, v172, 7\n" \
"v_readlane_b32 s70, v172, 8\n" \
"v_readlane_b32 s71, v172, 9\n" \
"v_readlane_b32 s72, v172, 10\n" \
"v_readlane_b32 s73, v172, 11\n" \
"s_lshl_b32 s25, %1, 2\n" \
"s_add_u32 s25, s25, %2\n" \
"s_lshl_b32 s25, s25, 13\n" \
"s_add_u32 s25, s25, 16777216\n" \
"buffer_load_dwordx4 v[64:67], v4, s[36:39], s25 offen offset:0\n" \
"buffer_load_dwordx4 v[68:71], v4, s[36:39], s25 offen offset:1024\n" \
"buffer_load_dwordx4 v[72:75], v4, s[36:39], s25 offen offset:2048\n" \
"buffer_load_dwordx4 v[76:79], v4, s[36:39], s25 offen offset:3072\n" \
"buffer_load_dwordx4 v[80:83], v7, s[36:39], s25 offen offset:0\n" \
"buffer_load_dwordx4 v[84:87], v7, s[36:39], s25 offen offset:1024\n" \
"buffer_load_dwordx4 v[88:91], v7, s[36:39], s25 offen offset:2048\n" \
"buffer_load_dwordx4 v[92:95], v7, s[36:39], s25 offen offset:3072\n" \
"s_add_u32 s25, s25, 8388608\n" \
"buffer_load_dwordx4 v[96:99], v4, s[36:39], s25 offen offset:0\n" \
"buffer_load_dwordx4 v[100:103], v4, s[36:39], s25 offen offset:1024\n" \
"buffer_load_dwordx4 v[104:107], v4, s[36:39], s25 offen offset:2048\n" \
"buffer_load_dwordx4 v[108:111], v4, s[36:39], s25 offen offset:3072\n" \
"buffer_load_dwordx4 v[112:115], v7, s[36:39], s25 offen offset:0\n" \
"buffer_load_dwordx4 v[116:119], v7, s[36:39], s25 offen offset:1024\n" \
"buffer_load_dwordx4 v[120:123], v7, s[36:39], s25 offen offset:2048\n" \
"buffer_load_dwordx4 v[124:127], v7, s[36:39], s25 offen offset:3072\n" \
"s_add_u32 s25, s25, 58720256\n" \
"buffer_load_dwordx4 v[128:131], v4, s[36:39], s25 offen offset:0\n" \
"buffer_load_dwordx4 v[132:135], v4, s[36:39], s25 offen offset:1024\n" \
"buffer_load_dwordx4 v[136:139], v4, s[36:39], s25 offen offset:2048\n" \
"buffer_load_dwordx4 v[140:143], v4, s[36:39], s25 offen offset:3072\n" \
"buffer_load_dwordx4 v[144:147], v7, s[36:39], s25 offen offset:0\n" \
"buffer_load_dwordx4 v[148:151], v7, s[36:39], s25 offen offset:1024\n" \
"buffer_load_dwordx4 v[152:155], v7, s[36:39], s25 offen offset:2048\n" \
"buffer_load_dwordx4 v[156:159], v7, s[36:39], s25 offen offset:3072\n" \
"s_add_u32 s25, s25, 37748736\n" \
"buffer_load_dwordx4 v[160:163], v4, s[36:39], s25 offen offset:0\n" \
"buffer_load_dwordx4 v[164:167], v4, s[36:39], s25 offen offset:1024\n" \
"buffer_load_dwordx4 v[168:171], v4, s[36:39], s25 offen offset:2048\n" \
"buffer_load_dwordx4 v[172:175], v4, s[36:39], s25 offen offset:3072\n" \
"s_waitcnt vmcnt(0)\n" \
"s_cmp_eq_u32 s99, 0\n" \
"s_cbranch_scc1 LFBAR_%=\n" \
"s_cmp_eq_u32 s99, 1\n" \
"s_cbranch_scc1 LFBBR_%=\n" \
"s_branch LFBCR_%=\n" \
"LEND_%=:\n" \
"s_waitcnt vmcnt(0) lgkmcnt(0)\n" \
""
#define QKV_ASM_CLOBBERS "v1", "v2", "v3", "v4", "v5", "v6", "v7", "v8", "v9", "v10", "v11", "v12", "v13", "v14", "v15", "v16", "v17", "v18", "v19", "v20", "v21", "v22", "v23", "v24", "v25", "v26", "v27", "v28", "v29", "v30", "v31", "v32", "v33", "v34", "v35", "v36", "v37", "v38", "v39", "v40", "v41", "v42", "v43", "v44", "v45", "v46", "v47", "v48", "v49", "v50", "v51", "v52", "v53", "v54", "v55", "v56", "v57", "v58", "v59", "v60", "v61", "v62", "v63", "v64", "v65", "v66", "v67", "v68", "v69", "v70", "v71", "v72", "v73", "v74", "v75", "v76", "v77", "v78", "v79", "v80", "v81", "v82", "v83", "v84", "v85", "v86", "v87", "v88", "v89", "v90", "v91", "v92", "v93", "v94", "v95", "v96", "v97", "v98", "v99", "v100", "v101", "v102", "v103", "v104", "v105", "v106", "v107", "v108", "v109", "v110", "v111", "v112", "v113", "v114", "v115", "v116", "v117", "v118", "v119", "v120", "v121", "v122", "v123", "v124", "v125", "v126", "v127", "v128", "v129", "v130", "v131", "v132", "v133", "v134", "v135", "v136", "v137", "v138", "v139", "v140", "v141", "v142", "v143", "v144", "v145", "v146", "v147", "v148", "v149", "v150", "v151", "v152", "v153", "v154", "v155", "v156", "v157", "v158", "v159", "v160", "v161", "v162", "v163", "v164", "v165", "v166", "v167", "v168", "v169", "v170", "v171", "v172", "v173", "v174", "v175", "v176", "v177", "v178", "v179", "v180", "v181", "v182", "v183", "v184", "v185", "v186", "v187", "v188", "v189", "v190", "v191", "v192", "v193", "v194", "v195", "v196", "v197", "v198", "v199", "v200", "v201", "v202", "v203", "v204", "v205", "v206", "v207", "v208", "v209", "v210", "v211", "v212", "v213", "v214", "v215", "v216", "v217", "v218", "v219", "v220", "v221", "v222", "v223", "v224", "v225", "v226", "v227", "v228", "v229", "v230", "v231", "v232", "v233", "v234", "v235", "v236", "v237", "v238", "v239", "v240", "v241", "v242", "v243", "v244", "v245", "v246", "v247", "v248", "v249", "v250", "v251", "v252", "v253", "v254", "v255", "a0", "a1", "a2", "a3", "a4", "a5", "a6", "a7", "a8", "a9", "a10", "a11", "a12", "a13", "a14", "a15", "a16", "a17", "a18", "a19", "a20", "a21", "a22", "a23", "a24", "a25", "a26", "a27", "a28", "a29", "a30", "a31", "a32", "a33", "a34", "a35", "a36", "a37", "a38", "a39", "a40", "a41", "a42", "a43", "a44", "a45", "a46", "a47", "a48", "a49", "a50", "a51", "a52", "a53", "a54", "a55", "a56", "a57", "a58", "a59", "a60", "a61", "a62", "a63", "a64", "a65", "a66", "a67", "a68", "a69", "a70", "a71", "a72", "a73", "a74", "a75", "a76", "a77", "a78", "a79", "a80", "a81", "a82", "a83", "a84", "a85", "a86", "a87", "a88", "a89", "a90", "a91", "a92", "a93", "a94", "a95", "a96", "a97", "a98", "a99", "a100", "a101", "a102", "a103", "a104", "a105", "a106", "a107", "a108", "a109", "a110", "a111", "a112", "a113", "a114", "a115", "a116", "a117", "a118", "a119", "a120", "a121", "a122", "a123", "a124", "a125", "a126", "a127", "a128", "a129", "a130", "a131", "a132", "a133", "a134", "a135", "a136", "a137", "a138", "a139", "a140", "a141", "a142", "a143", "a144", "a145", "a146", "a147", "a148", "a149", "a150", "a151", "a152", "a153", "a154", "a155", "a156", "a157", "a158", "a159", "a160", "a161", "a162", "a163", "a164", "a165", "a166", "a167", "a168", "a169", "a170", "a171", "a172", "a173", "a174", "a175", "a176", "a177", "a178", "a179", "a180", "a181", "a182", "a183", "a184", "a185", "a186", "a187", "a188", "a189", "a190", "a191", "a192", "a193", "a194", "a195", "a196", "a197", "a198", "a199", "a200", "a201", "a202", "a203", "a204", "a205", "a206", "a207", "a208", "a209", "a210", "a211", "a212", "a213", "a214", "a215", "a216", "a217", "a218", "a219", "a220", "a221", "a222", "a223", "a224", "a225", "a226", "a227", "a228", "a229", "a230", "a231", "a232", "a233", "a234", "a235", "a236", "a237", "a238", "a239", "a240", "a241", "a242", "a243", "a244", "a245", "a246", "a247", "a248", "a249", "a250", "a251", "a252", "a253", "a254", "a255", "s4", "s5", "s6", "s7", "s8", "s9", "s10", "s11", "s12", "s13", "s14", "s15", "s16", "s17", "s18", "s19", "s20", "s21", "s22", "s23", "s24", "s25", "s26", "s27", "s28", "s29", "s30", "s31", "s32", "s33", "s34", "s35", "s36", "s37", "s38", "s39", "s40", "s41", "s42", "s43", "s44", "s45", "s46", "s47", "s48", "s49", "s50", "s51", "s52", "s53", "s54", "s55", "s56", "s57", "s58", "s59", "s60", "s61", "s62", "s63", "s64", "s65", "s66", "s67", "s68", "s69", "s70", "s71", "s72", "s73", "s74", "s75", "s76", "s77", "s78", "s79", "s80", "s81", "s82", "s83", "s84", "s85", "s86", "s87", "s88", "s89", "s90", "s91", "s92", "s93", "s94", "s95", "s96", "s97", "s98", "s99", "s100", "s101", "vcc", "scc", "memory"
constexpr int Q_LDS = 163840;
__global__ __launch_bounds__(256, 1) void fused_kernel(const void* x, const void* k, const void* v, const void* shk, const void* ssmk, const void* ssmD, const void* ckv,
                                                       const void* wq, const void* wuk, const void* wuv, const void* wo, const void* gw, void* out, void* ws) {
    const int w = __builtin_amdgcn_readfirstlane(threadIdx.x >> 6);
    const unsigned long long ka = (unsigned long long)__builtin_amdgcn_kernarg_segment_ptr();
    asm volatile(QKV_ASM_TEXT :: "s"(ka), "s"(blockIdx.x), "s"(w) : QKV_ASM_CLOBBERS);
}

extern "C" void kernel_launch(void* const* d_in, const int* in_sizes, int n_in, void* d_out, int out_size, void* d_ws, size_t ws_size,
                              hipStream_t stream) {
    (void)hipFuncSetAttribute((const void*)fused_kernel, hipFuncAttributeMaxDynamicSharedMemorySize, Q_LDS);
    fused_kernel<<<256, 256, Q_LDS, stream>>>(d_in[0], d_in[1], d_in[2], d_in[3], d_in[4], d_in[5], d_in[7], d_in[8], d_in[9], d_in[10], d_in[11], d_in[12], d_out, d_ws);
}
```

```cpp
#include <hip/hip_runtime.h>
#include <stdint.h>
#define QKV_ASM_TEXT \
"s_nop 0\n" \
"s_load_dwordx16 s[64:79], %0, 0x0\n" \
"s_load_dwordx8 s[80:87], %0, 0x40\n" \
"s_load_dwordx4 s[88:91], %0, 0x60\n" \
"v_mbcnt_lo_u32_b32 v23, -1, 0\n" \
"v_mbcnt_hi_u32_b32 v23, -1, v23\n" \
"s_bfe_u32 s58, %1, 0x30003\n" \
"s_and_b32 s59, %1, 7\n" \
"s_lshl_b32 s59, s59, 1\n" \
"s_bfe_u32 s60, %1, 0x10006\n" \
"s_add_u32 s59, s59, s60\n" \
"v_lshlrev_b32 v4, 4, v23\n" \
"s_getpc_b64 s[40:41]\nLCTBEG_%=:\n" \
"s_add_u32 s40, s40, LBEG_%=-LCTBEG_%=\ns_addc_u32 s41, s41, 0\n" \
"s_and_b32 s41, s41, 0xffff\n" \
"s_mov_b32 s42, LEND_%=-LBEG_%=\n" \
"s_mov_b32 s43, 0x00020000\n" \
"s_lshr_b32 s62, %1, 3\n" \
"s_lshl_b32 s62, s62, 2\n" \
"s_add_u32 s62, s62, %2\n" \
"s_and_b32 s62, s62, 31\n" \
"s_lshl_b32 s62, s62, 13\n" \
"v_lshlrev_b32 v64, 3, v4\n" \
"buffer_load_dword v64, v64, s[40:43], s62 offen\n" \
"s_lshl_b32 s60, %2, 2\n" \
"v_lshrrev_b32 v24, 4, v23\n" \
"v_add_u32 v25, s60, v24\n" \
"v_xor_b32 v25, v25, v23\n" \
"v_and_b32 v25, 7, v25\n" \
"s_lshl_b32 s61, %2, 3\n" \
"v_lshrrev_b32 v26, 3, v23\n" \
"v_add_u32 v26, s61, v26\n" \
"v_lshlrev_b32 v26, 10, v26\n" \
"v_lshl_add_u32 v13, v25, 4, v26\n" \
"v_add_u32 v63, v13, v26\n" \
"v_add_u32 v14, 0x8000, v13\n" \
"v_add_u32 v15, 0x8000, v14\n" \
"v_add_u32 v16, 0x8000, v15\n" \
"v_add_u32 v17, 0x8000, v16\n" \
"v_add_u32 v18, 0x8000, v17\n" \
"v_add_u32 v19, 0x8000, v18\n" \
"v_add_u32 v20, 0x8000, v19\n" \
"v_bfe_u32 v25, v23, 2, 3\n" \
"v_add_u32 v25, s61, v25\n" \
"v_lshlrev_b32 v25, 13, v25\n" \
"s_lshl_b32 s61, %2, 1\n" \
"v_and_b32 v26, 1, v24\n" \
"v_add_u32 v26, s61, v26\n" \
"v_xor_b32 v26, v26, v23\n" \
"v_and_b32 v26, 3, v26\n" \
"v_lshrrev_b32 v27, 5, v23\n" \
"v_lshl_add_u32 v26, v27, 2, v26\n" \
"v_lshl_add_u32 v21, v26, 4, v25\n" \
"s_lshl_b32 s50, %2, 10\n" \
"s_add_u32 s50, s50, 65536\n" \
"s_lshl_b32 s22, %2, 12\n" \
"s_lshl_b32 s51, %2, 11\n" \
"s_mov_b32 s31, 0x00020000\n" \
"s_mov_b32 s35, 0x00020000\n" \
"s_mov_b32 s30, 262144\n" \
"s_lshl_b32 s60, s58, 18\n" \
"s_lshl_b32 s61, s59, 9\n" \
"s_sub_u32 s34, 4194304, s61\n" \
"s_mov_b32 s46, s60\n" \
"s_mov_b32 s47, s61\n" \
"s_lshl_b32 s57, s59, 1\n" \
"s_and_b32 s60, %2, 1\n" \
"s_add_u32 s57, s57, s60\n" \
"s_lshl_b32 s57, s57, 20\n" \
"s_lshl_b32 s60, s58, 17\n" \
"s_add_u32 s57, s57, s60\n" \
"s_lshr_b32 s60, %2, 1\n" \
"s_lshl_b32 s60, s60, 16\n" \
"s_add_u32 s57, s57, s60\n" \
"s_add_u32 s57, s57, 33554432\n" \
"s_lshl_b32 s24, s59, 7\n" \
"s_lshl_b32 s60, s58, 3\n" \
"s_add_u32 s24, s24, s60\n" \
"s_lshl_b32 s60, %2, 27\n" \
"s_add_u32 s24, s24, s60\n" \
"s_and_b32 s23, %1, 7\n" \
"s_lshl_b32 s23, s23, 8\n" \
"s_add_u32 s23, s23, s60\n" \
"s_and_b32 s60, %1, 0xf8\n" \
"s_add_u32 s23, s23, s60\n" \
"s_add_u32 s23, s23, 94380032\n" \
"s_cmp_lt_u32 %1, 128\n" \
"s_cselect_b32 s60, 0, 8192\n" \
"s_cselect_b32 s61, 0, 64\n" \
"s_cselect_b32 s62, 14, 14\n" \
"s_cselect_b32 s63, 10, 11\n" \
"s_cselect_b32 s98, 12, 10\n" \
"s_cselect_b32 s20, 0x3e0293ee, 1.0\n" \
"s_add_u32 s57, s57, s60\n" \
"s_add_u32 s24, s24, s61\n" \
"s_add_u32 s24, s24, 94375936\n" \
"s_lshl_b32 s92, 1, s62\n" \
"s_lshl_b32 s93, 1, s63\n" \
"s_lshl_b32 s98, 1, s98\n" \
"s_and_b32 s6, %1, 7\n" \
"s_lshl_b32 s8, s6, 5\n" \
"s_lshl_b32 s6, s6, 2\n" \
"s_lshr_b32 s58, %1, 6\n" \
"s_add_u32 s6, s6, s58\n" \
"s_lshr_b32 s58, %1, 3\n" \
"s_add_u32 s8, s8, s58\n" \
"s_and_b32 s7, s58, 7\n" \
"s_lshl_b32 s58, s6, 8\n" \
"s_mov_b32 s94, s58\n" \
"s_sub_u32 s49, 8388608, s58\n" \
"s_waitcnt lgkmcnt(0)\n" \
"s_cmp_lt_u32 %1, 128\n" \
"s_cselect_b64 s[32:33], s[80:81], s[82:83]\n" \
"s_add_u32 s28, s76, s46\ns_addc_u32 s29, s77, 0\n" \
"s_and_b32 s29, s29, 0xffff\n" \
"s_add_u32 s32, s32, s47\ns_addc_u32 s33, s33, 0\n" \
"s_and_b32 s33, s33, 0xffff\n" \
"s_mov_b32 s99, 94371840\n" \
"s_load_dwordx2 s[14:15], s[90:91], s99\n" \
"s_mov_b32 s46, 0\n" \
"s_add_u32 m0, s50, 65536\n" \
"s_mov_b64 s[36:37], s[90:91]\n" \
"buffer_load_dwordx4 v13, s[28:31], s46 offen lds\n" \
"s_and_b32 s37, s37, 0xffff\n" \
"s_mov_b32 s38, 134217728\n" \
"s_mov_b32 s39, 0x00020000\n" \
"v_lshrrev_b32 v23, 4, v4\n" \
"s_add_u32 m0, s50, 69632\n" \
"v_and_b32 v24, 15, v23\n" \
"buffer_load_dwordx4 v14, s[28:31], s46 offen lds\n" \
"v_lshrrev_b32 v25, 4, v23\n" \
"s_lshr_b32 s58, %2, 1\n" \
"s_lshl_b32 s58, s58, 14\n" \
"s_and_b32 s54, %2, 1\n" \
"s_add_u32 m0, s50, 73728\n" \
"s_lshl_b32 s54, s54, 13\n" \
"buffer_load_dwordx4 v15, s[28:31], s46 offen lds\n" \
"s_add_u32 s58, s58, 65536\n" \
"v_lshrrev_b32 v26, 1, v24\n" \
"v_xor_b32 v26, v26, v25\n" \
"v_lshlrev_b32 v26, 4, v26\n" \
"s_add_u32 m0, s50, 77824\n" \
"v_lshl_add_u32 v26, v24, 7, v26\n" \
"buffer_load_dwordx4 v16, s[28:31], s46 offen lds\n" \
"v_add_u32 v5, s58, v26\n" \
"v_xor_b32 v6, 64, v5\n" \
"v_add_u32 v7, 0x10000, v5\n" \
"v_add_u32 v8, 0x10000, v6\n" \
"s_mov_b32 s47, 0\n" \
"s_add_u32 m0, s22, 0\n" \
"s_and_b32 s58, %2, 1\n" \
"buffer_load_dwordx4 v21, s[32:35], s47 offen offset:0 nt lds\n" \
"s_lshl_b32 s58, s58, 11\n" \
"v_lshrrev_b32 v26, 2, v24\n" \
"v_lshlrev_b32 v26, 6, v26\n" \
"v_and_b32 v27, 1, v23\n" \
"s_mov_b32 s46, 0\n" \
"s_add_u32 m0, s50, 81920\n" \
"v_lshl_add_u32 v26, v27, 3, v26\n" \
"buffer_load_dwordx4 v17, s[28:31], s46 offen lds\n" \
"v_bfe_u32 v27, v23, 1, 1\n" \
"v_lshlrev_b32 v28, 1, v25\n" \
"v_xor_b32 v27, v27, v28\n" \
"v_and_b32 v27, 3, v27\n" \
"s_add_u32 m0, s22, 1792\n" \
"v_lshl_add_u32 v26, v27, 4, v26\n" \
"buffer_load_dwordx4 v21, s[32:35], s47 offen offset:256 nt lds\n" \
"v_lshl_add_u32 v9, v25, 12, v26\n" \
"v_add_u32 v9, s58, v9\n" \
"v_lshl_add_u32 v62, v25, 11, s58\n" \
"v_xor_b32 v10, 0x110, v9\n" \
"s_add_u32 m0, s50, 86016\n" \
"v_xor_b32 v11, 32, v9\n" \
"buffer_load_dwordx4 v18, s[28:31], s46 offen lds\n" \
"v_xor_b32 v12, 0x130, v9\n" \
"s_and_b32 s62, %1, 7\n" \
"s_lshl_b32 s62, s62, 7\n" \
"s_lshl_b32 s63, %2, 6\n" \
"s_mov_b32 s47, 262144\n" \
"s_add_u32 m0, s22, 1024\n" \
"s_and_b32 s63, s63, 127\n" \
"buffer_load_dwordx4 v21, s[32:35], s47 offen offset:0 nt lds\n" \
"s_add_u32 s62, s62, s63\n" \
"v_bfe_u32 v24, v4, 4, 4\n" \
"v_lshl_add_u32 v24, v24, 2, s62\n" \
"s_lshr_b32 s62, %2, 1\n" \
"s_add_u32 m0, s50, 90112\n" \
"s_lshl_b32 s62, s62, 2\n" \
"buffer_load_dwordx4 v19, s[28:31], s46 offen lds\n" \
"s_and_b32 s63, %1, 0xf8\n" \
"s_add_u32 s63, s63, s62\n" \
"v_lshrrev_b32 v25, 8, v4\n" \
"v_lshrrev_b32 v23, 7, v4\n" \
"s_add_u32 m0, s22, 2816\n" \
"v_add_u32 v25, s63, v25\n" \
"buffer_load_dwordx4 v21, s[32:35], s47 offen offset:256 nt lds\n" \
"v_lshlrev_b32 v26, 14, v25\n" \
"v_lshl_add_u32 v59, v24, 1, v26\n" \
"v_add_u32 v54, 0xffffe800, v59\n" \
"v_add_u32 v55, 0xfffff800, v59\n" \
"s_add_u32 m0, s50, 94208\n" \
"v_lshlrev_b32 v60, 12, v24\n" \
"buffer_load_dwordx4 v20, s[28:31], s46 offen lds\n" \
"v_lshl_add_u32 v60, v25, 4, v60\n" \
"v_lshlrev_b32 v27, 3, v24\n" \
"s_lshl_b32 s62, %1, 2\n" \
"s_add_u32 s62, s62, %2\n" \
"s_mov_b32 s47, 0\n" \
"s_add_u32 m0, s22, 16256\n" \
"s_lshl_b32 s53, s62, 12\n" \
"buffer_load_dwordx4 v21, s[32:35], s47 offen offset:128 nt lds\n" \
"s_and_b32 s63, s62, 7\n" \
"s_lshl_b32 s63, s63, 4\n" \
"s_add_u32 s63, s63, 71303168\n" \
"v_and_b32 v61, 4, v23\n" \
"s_add_u32 m0, s22, 18048\n" \
"v_lshl_add_u32 v61, v61, 1, s63\n" \
"buffer_load_dwordx4 v21, s[32:35], s47 offen offset:384 nt lds\n" \
"s_bfe_u32 s63, s62, 0x10003\n" \
"s_lshl_b32 s63, s63, 2\n" \
"s_lshr_b32 s62, s62, 4\n" \
"s_lshl_b32 s62, s62, 7\n" \
"s_mov_b32 s47, 262144\n" \
"s_add_u32 m0, s22, 17280\n" \
"s_add_u32 s62, s62, s63\n" \
"buffer_load_dwordx4 v21, s[32:35], s47 offen offset:128 nt lds\n" \
"v_add_u32 v61, s62, v61\n" \
"v_lshlrev_b32 v2, 3, v4\n" \
"v_and_b32 v1, 0x380, v2\n" \
"v_lshrrev_b32 v23, 7, v4\n" \
"s_add_u32 m0, s22, 19072\n" \
"v_lshl_add_u32 v1, v23, 13, v1\n" \
"buffer_load_dwordx4 v21, s[32:35], s47 offen offset:384 nt lds\n" \
"s_lshr_b32 s58, %1, 3\n" \
"s_lshl_b32 s58, s58, 2\n" \
"s_add_u32 s58, s58, %2\n" \
"s_and_b32 s60, %1, 7\n" \
"s_mov_b32 s47, 524288\n" \
"s_add_u32 m0, s22, 32768\n" \
"s_lshl_b32 s60, s60, 10\n" \
"buffer_load_dwordx4 v21, s[32:35], s47 offen offset:0 nt lds\n" \
"s_lshl_b32 s59, s58, 14\n" \
"s_add_u32 s59, s59, s60\n" \
"s_mov_b64 s[4:5], s[90:91]\n" \
"s_lshl_b32 s95, s58, 11\n" \
"s_add_u32 m0, s22, 34560\n" \
"s_lshl_b32 s96, s60, 8\n" \
"buffer_load_dwordx4 v21, s[32:35], s47 offen offset:256 nt lds\n" \
"s_add_u32 s95, s95, s96\n" \
"s_add_u32 s96, s76, s95\ns_addc_u32 s97, s77, 0\n" \
"s_add_u32 s76, s78, s59\ns_addc_u32 s77, s79, 0\n" \
"s_and_b32 s59, s59, 1048575\n" \
"s_mov_b32 s47, 786432\n" \
"s_add_u32 m0, s22, 33792\n" \
"s_cmp_lt_u32 s58, 64\ns_cselect_b64 s[84:85], s[80:81], s[82:83]\n" \
"buffer_load_dwordx4 v21, s[32:35], s47 offen offset:0 nt lds\n" \
"s_add_u32 s84, s84, s59\ns_addc_u32 s85, s85, 0\n" \
"s_lshl_b32 s59, s8, 2\n" \
"s_add_u32 s59, s59, %2\n" \
"s_lshl_b32 s59, s59, 12\n" \
"s_add_u32 m0, s22, 35584\n" \
"s_add_u32 s88, s72, s59\ns_addc_u32 s89, s73, 0\n" \
"buffer_load_dwordx4 v21, s[32:35], s47 offen offset:256 nt lds\n" \
"s_mov_b64 s[4:5], s[90:91]\n" \
"s_mov_b64 s[10:11], s[72:73]\n" \
"s_mov_b64 s[12:13], s[74:75]\n" \
"s_add_u32 s44, s78, s94\ns_addc_u32 s45, s79, 0\n" \
"s_mov_b32 s46, 128\n" \
"s_add_u32 m0, s50, 0\n" \
"s_mov_b64 s[40:41], s[66:67]\n" \
"buffer_load_dwordx4 v13, s[28:31], s46 offen lds\n" \
"s_mov_b64 s[80:81], s[68:69]\n" \
"s_and_b32 s81, s81, 0xffff\n" \
"s_mov_b32 s82, 4194304\n" \
"s_mov_b32 s83, 0x00020000\n" \
"s_add_u32 m0, s50, 4096\n" \
"s_and_b32 s45, s45, 0xffff\n" \
"buffer_load_dwordx4 v14, s[28:31], s46 offen lds\n" \
"s_and_b32 s41, s41, 0xffff\n" \
"s_mov_b32 s42, 4194304\n" \
"s_mov_b32 s43, 0x00020000\n" \
"s_mov_b64 exec, 0xffff\nglobal_load_dword v22, v1, s[84:85]\ns_mov_b64 exec, -1\n" \
"s_mov_b64 exec, 0xffff\nglobal_load_dword v22, v2, s[96:97]\ns_mov_b64 exec, -1\n" \
"s_cmp_lt_u32 %1, 128\ns_cbranch_scc0 LHV_%=\ns_mov_b64 exec, 0\ns_cmp_eq_u32 %2, 0\ns_cbranch_scc1 LH0_%=\ns_cmp_eq_u32 %2, 1\ns_cbranch_scc1 LH1_%=\ns_cmp_eq_u32 %2, 2\ns_cbranch_scc1 LH2_%=\ns_cmp_eq_u32 %2, 3\ns_cbranch_scc1 LH3_%=\nLHV_%=:\ns_mov_b64 exec, 0\ns_cmp_eq_u32 %2, 0\ns_cbranch_scc1 LH4_%=\ns_cmp_eq_u32 %2, 1\ns_cbranch_scc1 LH5_%=\ns_cmp_eq_u32 %2, 2\ns_cbranch_scc1 LH6_%=\ns_cmp_eq_u32 %2, 3\ns_cbranch_scc1 LH7_%=\nLHR0_%=:\ns_mov_b64 exec, -1\n" \
"s_waitcnt vmcnt(8)\n" \
"s_barrier\n" \
"s_add_u32 m0, s50, 8192\n" \
"ds_read_b64_tr_b16 a[64:65], v9 offset:0\n" \
"ds_read_b64_tr_b16 a[66:67], v10 offset:0\n" \
"ds_read_b64_tr_b16 a[68:69], v9 offset:1024\n" \
"ds_read_b64_tr_b16 a[70:71], v10 offset:1024\n" \
"ds_read_b128 a[0:3], v7 offset:0\n" \
"ds_read_b128 a[4:7], v8 offset:0\n" \
"buffer_load_dwordx4 v15, s[28:31], s46 offen lds\n" \
"s_add_u32 m0, s50, 12288\n" \
"ds_read_b128 a[8:11], v7 offset:2048\n" \
"ds_read_b128 a[12:15], v8 offset:2048\n" \
"ds_read_b128 a[16:19], v7 offset:4096\n" \
"ds_read_b128 a[20:23], v8 offset:4096\n" \
"ds_read_b128 a[24:27], v7 offset:6144\n" \
"ds_read_b128 a[28:31], v8 offset:6144\n" \
"buffer_load_dwordx4 v16, s[28:31], s46 offen lds\n" \
"s_mov_b32 s46, 128\n" \
"s_add_u32 m0, s50, 16384\n" \
"ds_read_b128 a[32:35], v7 offset:8192\n" \
"ds_read_b128 a[36:39], v8 offset:8192\n" \
"ds_read_b128 a[40:43], v7 offset:10240\n" \
"ds_read_b128 a[44:47], v8 offset:10240\n" \
"ds_read_b128 a[48:51], v7 offset:12288\n" \
"ds_read_b128 a[52:55], v8 offset:12288\n" \
"buffer_load_dwordx4 v17, s[28:31], s46 offen lds\n" \
"s_add_u32 m0, s50, 20480\n" \
"ds_read_b128 a[56:59], v7 offset:14336\n" \
"ds_read_b128 a[60:63], v8 offset:14336\n" \
"ds_read_b64_tr_b16 a[72:73], v11 offset:0\n" \
"ds_read_b64_tr_b16 a[74:75], v12 offset:0\n" \
"ds_read_b64_tr_b16 a[76:77], v11 offset:1024\n" \
"ds_read_b64_tr_b16 a[78:79], v12 offset:1024\n" \
"buffer_load_dwordx4 v18, s[28:31], s46 offen lds\n" \
"s_add_u32 m0, s50, 24576\n" \
"ds_read_b64_tr_b16 a[80:81], v9 offset:512\n" \
"ds_read_b64_tr_b16 a[82:83], v10 offset:512\n" \
"ds_read_b64_tr_b16 a[84:85], v9 offset:1536\n" \
"ds_read_b64_tr_b16 a[86:87], v10 offset:1536\n" \
"ds_read_b64_tr_b16 a[88:89], v11 offset:512\n" \
"ds_read_b64_tr_b16 a[90:91], v12 offset:512\n" \
"buffer_load_dwordx4 v19, s[28:31], s46 offen lds\n" \
"s_add_u32 m0, s50, 28672\n" \
"ds_read_b64_tr_b16 a[92:93], v11 offset:1536\n" \
"ds_read_b64_tr_b16 a[94:95], v12 offset:1536\n" \
"buffer_load_dwordx4 v20, s[28:31], s46 offen lds\n" \
"s_waitcnt lgkmcnt(0)\n" \
"s_branch LBEG_%=\n" \
".p2align 12\n" \
"LBEG_%=:\n" \
"s_cmp_lt_u32 %1, 128\n" \
"s_cbranch_scc0 LAV_%=\n" \
"v_mfma_f32_16x16x32_bf16 v[64:67], a[64:67], a[0:3], 0\n" \
"s_mov_b32 s47, 524288\n" \
"s_add_u32 m0, s22, 49024\n" \
"v_mfma_f32_16x16x32_bf16 v[64:67], a[68:71], a[4:7], v[64:67]\n" \
"buffer_load_dwordx4 v21, s[32:35], s47 offen offset:128 nt lds\n" \
"v_mfma_f32_16x16x32_bf16 v[72:75], a[64:67], a[8:11], 0\n" \
"s_add_u32 s14, s14, 1\ns_addc_u32 s15, s15, 0\n" \
"v_mfma_f32_16x16x32_bf16 v[72:75], a[68:71], a[12:15], v[72:75]\n" \
"v_mfma_f32_16x16x32_bf16 v[80:83], a[64:67], a[16:19], 0\n" \
"s_add_u32 m0, s22, 50816\n" \
"ds_read_b64_tr_b16 a[96:97], v9 offset:16384\n" \
"v_mfma_f32_16x16x32_bf16 v[80:83], a[68:71], a[20:23], v[80:83]\n" \
"buffer_load_dwordx4 v21, s[32:35], s47 offen offset:384 nt lds\n" \
"v_mfma_f32_16x16x32_bf16 v[88:91], a[64:67], a[24:27], 0\n" \
"ds_read_b64_tr_b16 a[98:99], v10 offset:16384\n" \
"v_mfma_f32_16x16x32_bf16 v[88:91], a[68:71], a[28:31], v[88:91]\n" \
"v_mfma_f32_16x16x32_bf16 v[96:99], a[64:67], a[32:35], 0\n" \
"s_mov_b32 s47, 786432\n" \
"s_add_u32 m0, s22, 50048\n" \
"ds_read_b64_tr_b16 a[100:101], v9 offset:17408\n" \
"v_mfma_f32_16x16x32_bf16 v[96:99], a[68:71], a[36:39], v[96:99]\n" \
"buffer_load_dwordx4 v21, s[32:35], s47 offen offset:128 nt lds\n" \
"v_mfma_f32_16x16x32_bf16 v[104:107], a[64:67], a[40:43], 0\n" \
"ds_read_b64_tr_b16 a[102:103], v10 offset:17408\n" \
"v_mfma_f32_16x16x32_bf16 v[104:107], a[68:71], a[44:47], v[104:107]\n" \
"v_mfma_f32_16x16x32_bf16 v[112:115], a[64:67], a[48:51], 0\n" \
"s_add_u32 m0, s22, 51840\n" \
"ds_read_b64_tr_b16 a[104:105], v11 offset:16384\n" \
"v_mfma_f32_16x16x32_bf16 v[112:115], a[68:71], a[52:55], v[112:115]\n" \
"buffer_load_dwordx4 v21, s[32:35], s47 offen offset:384 nt lds\n" \
"v_mfma_f32_16x16x32_bf16 v[120:123], a[64:67], a[56:59], 0\n" \
"ds_read_b64_tr_b16 a[106:107], v12 offset:16384\n" \
"v_mfma_f32_16x16x32_bf16 v[120:123], a[68:71], a[60:63], v[120:123]\n" \
"v_mfma_f32_16x16x32_bf16 v[68:71], a[72:75], a[0:3], 0\n" \
"s_mov_b32 s46, 256\n" \
"s_add_u32 m0, s50, 32768\n" \
"ds_read_b64_tr_b16 a[108:109], v11 offset:17408\n" \
"v_mfma_f32_16x16x32_bf16 v[68:71], a[76:79], a[4:7], v[68:71]\n" \
"buffer_load_dwordx4 v13, s[28:31], s46 offen lds\n" \
"v_mfma_f32_16x16x32_bf16 v[76:79], a[72:75], a[8:11], 0\n" \
"ds_read_b64_tr_b16 a[110:111], v12 offset:17408\n" \
"v_mfma_f32_16x16x32_bf16 v[76:79], a[76:79], a[12:15], v[76:79]\n" \
"v_mfma_f32_16x16x32_bf16 v[84:87], a[72:75], a[16:19], 0\n" \
"s_add_u32 m0, s50, 36864\n" \
"ds_read_b64_tr_b16 a[112:113], v9 offset:16896\n" \
"v_mfma_f32_16x16x32_bf16 v[84:87], a[76:79], a[20:23], v[84:87]\n" \
"buffer_load_dwordx4 v14, s[28:31], s46 offen lds\n" \
"v_mfma_f32_16x16x32_bf16 v[92:95], a[72:75], a[24:27], 0\n" \
"ds_read_b64_tr_b16 a[114:115], v10 offset:16896\n" \
"v_mfma_f32_16x16x32_bf16 v[92:95], a[76:79], a[28:31], v[92:95]\n" \
"v_mfma_f32_16x16x32_bf16 v[100:103], a[72:75], a[32:35], 0\n" \
"s_add_u32 m0, s50, 40960\n" \
"ds_read_b64_tr_b16 a[116:117], v9 offset:17920\n" \
"v_mfma_f32_16x16x32_bf16 v[100:103], a[76:79], a[36:39], v[100:103]\n" \
"buffer_load_dwordx4 v15, s[28:31], s46 offen lds\n" \
"v_mfma_f32_16x16x32_bf16 v[108:111], a[72:75], a[40:43], 0\n" \
"ds_read_b64_tr_b16 a[118:119], v10 offset:17920\n" \
"v_mfma_f32_16x16x32_bf16 v[108:111], a[76:79], a[44:47], v[108:111]\n" \
"v_mfma_f32_16x16x32_bf16 v[116:119], a[72:75], a[48:51], 0\n" \
"s_add_u32 m0, s50, 45056\n" \
"ds_read_b64_tr_b16 a[120:121], v11 offset:16896\n" \
"v_mfma_f32_16x16x32_bf16 v[116:119], a[76:79], a[52:55], v[116:119]\n" \
"buffer_load_dwordx4 v16, s[28:31], s46 offen lds\n" \
"v_mfma_f32_16x16x32_bf16 v[124:127], a[72:75], a[56:59], 0\n" \
"ds_read_b64_tr_b16 a[122:123], v12 offset:16896\n" \
"v_mfma_f32_16x16x32_bf16 v[124:127], a[76:79], a[60:63], v[124:127]\n" \
"v_mfma_f32_16x16x32_bf16 v[128:131], a[80:83], a[0:3], 0\n" \
"s_mov_b32 s46, 256\n" \
"s_add_u32 m0, s50, 49152\n" \
"ds_read_b64_tr_b16 a[124:125], v11 offset:17920\n" \
"v_mfma_f32_16x16x32_bf16 v[128:131], a[84:87], a[4:7], v[128:131]\n" \
"buffer_load_dwordx4 v17, s[28:31], s46 offen lds\n" \
"v_mfma_f32_16x16x32_bf16 v[136:139], a[80:83], a[8:11], 0\n" \
"ds_read_b64_tr_b16 a[126:127], v12 offset:17920\n" \
"v_mfma_f32_16x16x32_bf16 v[136:139], a[84:87], a[12:15], v[136:139]\n" \
"v_mfma_f32_16x16x32_bf16 v[144:147], a[80:83], a[16:19], 0\n" \
"s_add_u32 m0, s50, 53248\n" \
"v_mfma_f32_16x16x32_bf16 v[144:147], a[84:87], a[20:23], v[144:147]\n" \
"buffer_load_dwordx4 v18, s[28:31], s46 offen lds\n" \
"v_mfma_f32_16x16x32_bf16 v[152:155], a[80:83], a[24:27], 0\n" \
"v_mfma_f32_16x16x32_bf16 v[152:155], a[84:87], a[28:31], v[152:155]\n" \
"v_mfma_f32_16x16x32_bf16 v[160:163], a[80:83], a[32:35], 0\n" \
"s_add_u32 m0, s50, 57344\n" \
"v_mfma_f32_16x16x32_bf16 v[160:163], a[84:87], a[36:39], v[160:163]\n" \
"buffer_load_dwordx4 v19, s[28:31], s46 offen lds\n" \
"v_mfma_f32_16x16x32_bf16 v[168:171], a[80:83], a[40:43], 0\n" \
"v_mfma_f32_16x16x32_bf16 v[168:171], a[84:87], a[44:47], v[168:171]\n" \
"s_waitcnt vmcnt(21) lgkmcnt(0)\n" \
"v_mfma_f32_16x16x32_bf16 v[176:179], a[80:83], a[48:51], 0\n" \
"s_add_u32 m0, s50, 61440\n" \
"s_barrier\n" \
"v_mfma_f32_16x16x32_bf16 v[176:179], a[84:87], a[52:55], v[176:179]\n" \
"buffer_load_dwordx4 v20, s[28:31], s46 offen lds\n" \
"v_mfma_f32_16x16x32_bf16 v[184:187], a[80:83], a[56:59], 0\n" \
"v_mfma_f32_16x16x32_bf16 v[184:187], a[84:87], a[60:63], v[184:187]\n" \
"v_mfma_f32_16x16x32_bf16 v[132:135], a[88:91], a[0:3], 0\n" \
"s_mov_b32 s47, 1048576\n" \
"s_add_u32 m0, s22, 0\n" \
"v_mfma_f32_16x16x32_bf16 v[132:135], a[92:95], a[4:7], v[132:135]\n" \
"buffer_load_dwordx4 v21, s[32:35], s47 offen offset:0 lds\n" \
"v_mfma_f32_16x16x32_bf16 v[140:143], a[88:91], a[8:11], 0\n" \
"v_mfma_f32_16x16x32_bf16 v[140:143], a[92:95], a[12:15], v[140:143]\n" \
"v_mfma_f32_16x16x32_bf16 v[148:151], a[88:91], a[16:19], 0\n" \
"s_add_u32 m0, s22, 1792\n" \
"v_mfma_f32_16x16x32_bf16 v[148:151], a[92:95], a[20:23], v[148:151]\n" \
"buffer_load_dwordx4 v21, s[32:35], s47 offen offset:256 lds\n" \
"v_mfma_f32_16x16x32_bf16 v[156:159], a[88:91], a[24:27], 0\n" \
"v_mfma_f32_16x16x32_bf16 v[156:159], a[92:95], a[28:31], v[156:159]\n" \
"v_mfma_f32_16x16x32_bf16 v[164:167], a[88:91], a[32:35], 0\n" \
"s_mov_b32 s47, 1310720\n" \
"s_add_u32 m0, s22, 1024\n" \
"v_mfma_f32_16x16x32_bf16 v[164:167], a[92:95], a[36:39], v[164:167]\n" \
"buffer_load_dwordx4 v21, s[32:35], s47 offen offset:0 lds\n" \
"v_mfma_f32_16x16x32_bf16 v[172:175], a[88:91], a[40:43], 0\n" \
"v_mfma_f32_16x16x32_bf16 v[172:175], a[92:95], a[44:47], v[172:175]\n" \
"v_mfma_f32_16x16x32_bf16 v[180:183], a[88:91], a[48:51], 0\n" \
"s_add_u32 m0, s22, 2816\n" \
"v_mfma_f32_16x16x32_bf16 v[180:183], a[92:95], a[52:55], v[180:183]\n" \
"buffer_load_dwordx4 v21, s[32:35], s47 offen offset:256 lds\n" \
"v_mfma_f32_16x16x32_bf16 v[188:191], a[88:91], a[56:59], 0\n" \
"v_mfma_f32_16x16x32_bf16 v[188:191], a[92:95], a[60:63], v[188:191]\n" \
"v_mfma_f32_16x16x32_bf16 v[192:195], a[96:99], a[0:3], 0\n" \
"s_mov_b32 s47, 1048576\n" \
"s_add_u32 m0, s22, 16256\n" \
"v_mfma_f32_16x16x32_bf16 v[192:195], a[100:103], a[4:7], v[192:195]\n" \
"buffer_load_dwordx4 v21, s[32:35], s47 offen offset:128 lds\n" \
"v_mfma_f32_16x16x32_bf16 v[200:203], a[96:99], a[8:11], 0\n" \
"v_mfma_f32_16x16x32_bf16 v[200:203], a[100:103], a[12:15], v[200:203]\n" \
"v_mfma_f32_16x16x32_bf16 v[208:211], a[96:99], a[16:19], 0\n" \
"s_mov_b32 s46, 384\n" \
"s_add_u32 m0, s50, 65536\n" \
"ds_read_b64_tr_b16 a[64:65], v9 offset:32768\n" \
"v_mfma_f32_16x16x32_bf16 v[208:211], a[100:103], a[20:23], v[208:211]\n" \
"buffer_load_dwordx4 v13, s[28:31], s46 offen lds\n" \
"v_mfma_f32_16x16x32_bf16 v[216:219], a[96:99], a[24:27], 0\n" \
"ds_read_b64_tr_b16 a[66:67], v10 offset:32768\n" \
"v_mfma_f32_16x16x32_bf16 v[216:219], a[100:103], a[28:31], v[216:219]\n" \
"v_mfma_f32_16x16x32_bf16 v[224:227], a[96:99], a[32:35], 0\n" \
"s_add_u32 m0, s22, 18048\n" \
"ds_read_b64_tr_b16 a[68:69], v9 offset:33792\n" \
"v_mfma_f32_16x16x32_bf16 v[224:227], a[100:103], a[36:39], v[224:227]\n" \
"buffer_load_dwordx4 v21, s[32:35], s47 offen offset:384 lds\n" \
"v_mfma_f32_16x16x32_bf16 v[232:235], a[96:99], a[40:43], 0\n" \
"ds_read_b64_tr_b16 a[70:71], v10 offset:33792\n" \
"v_mfma_f32_16x16x32_bf16 v[232:235], a[100:103], a[44:47], v[232:235]\n" \
"v_mfma_f32_16x16x32_bf16 v[240:243], a[96:99], a[48:51], 0\n" \
"s_add_u32 m0, s50, 69632\n" \
"ds_read_b64_tr_b16 a[72:73], v11 offset:32768\n" \
"v_mfma_f32_16x16x32_bf16 v[240:243], a[100:103], a[52:55], v[240:243]\n" \
"buffer_load_dwordx4 v14, s[28:31], s46 offen lds\n" \
"v_mfma_f32_16x16x32_bf16 v[248:251], a[96:99], a[56:59], 0\n" \
"ds_read_b64_tr_b16 a[74:75], v12 offset:32768\n" \
"v_mfma_f32_16x16x32_bf16 v[248:251], a[100:103], a[60:63], v[248:251]\n" \
"v_mfma_f32_16x16x32_bf16 v[196:199], a[104:107], a[0:3], 0\n" \
"s_mov_b32 s47, 1310720\n" \
"s_add_u32 m0, s22, 17280\n" \
"ds_read_b64_tr_b16 a[76:77], v11 offset:33792\n" \
"v_mfma_f32_16x16x32_bf16 v[196:199], a[108:111], a[4:7], v[196:199]\n" \
"buffer_load_dwordx4 v21, s[32:35], s47 offen offset:128 lds\n" \
"v_mfma_f32_16x16x32_bf16 v[204:207], a[104:107], a[8:11], 0\n" \
"ds_read_b64_tr_b16 a[78:79], v12 offset:33792\n" \
"v_mfma_f32_16x16x32_bf16 v[204:207], a[108:111], a[12:15], v[204:207]\n" \
"v_mfma_f32_16x16x32_bf16 v[212:215], a[104:107], a[16:19], 0\n" \
"s_add_u32 m0, s50, 73728\n" \
"ds_read_b64_tr_b16 a[80:81], v9 offset:33280\n" \
"v_mfma_f32_16x16x32_bf16 v[212:215], a[108:111], a[20:23], v[212:215]\n" \
"buffer_load_dwordx4 v15, s[28:31], s46 offen lds\n" \
"v_mfma_f32_16x16x32_bf16 v[220:223], a[104:107], a[24:27], 0\n" \
"ds_read_b64_tr_b16 a[82:83], v10 offset:33280\n" \
"v_mfma_f32_16x16x32_bf16 v[220:223], a[108:111], a[28:31], v[220:223]\n" \
"v_mfma_f32_16x16x32_bf16 v[228:231], a[104:107], a[32:35], 0\n" \
"s_add_u32 m0, s22, 19072\n" \
"ds_read_b64_tr_b16 a[84:85], v9 offset:34304\n" \
"v_mfma_f32_16x16x32_bf16 v[228:231], a[108:111], a[36:39], v[228:231]\n" \
"buffer_load_dwordx4 v21, s[32:35], s47 offen offset:384 lds\n" \
"v_mfma_f32_16x16x32_bf16 v[236:239], a[104:107], a[40:43], 0\n" \
"ds_read_b64_tr_b16 a[86:87], v10 offset:34304\n" \
"v_mfma_f32_16x16x32_bf16 v[236:239], a[108:111], a[44:47], v[236:239]\n" \
"v_mfma_f32_16x16x32_bf16 v[244:247], a[104:107], a[48:51], 0\n" \
"s_add_u32 m0, s50, 77824\n" \
"ds_read_b64_tr_b16 a[88:89], v11 offset:33280\n" \
"v_mfma_f32_16x16x32_bf16 v[244:247], a[108:111], a[52:55], v[244:247]\n" \
"buffer_load_dwordx4 v16, s[28:31], s46 offen lds\n" \
"v_mfma_f32_16x16x32_bf16 v[252:255], a[104:107], a[56:59], 0\n" \
"ds_read_b64_tr_b16 a[90:91], v12 offset:33280\n" \
"v_mfma_f32_16x16x32_bf16 v[252:255], a[108:111], a[60:63], v[252:255]\n" \
"v_mfma_f32_16x16x32_bf16 a[128:131], a[112:115], a[0:3], 0\n" \
"ds_read_b64_tr_b16 a[92:93], v11 offset:34304\n" \
"v_mfma_f32_16x16x32_bf16 a[128:131], a[116:119], a[4:7], a[128:131]\n" \
"global_load_dwordx4 a[230:233], v27, s[70:71] offset:0\n" \
"v_mfma_f32_16x16x32_bf16 a[136:139], a[112:115], a[8:11], 0\n" \
"ds_read_b64_tr_b16 a[94:95], v12 offset:34304\n" \
"v_mfma_f32_16x16x32_bf16 a[136:139], a[116:119], a[12:15], a[136:139]\n" \
"v_mfma_f32_16x16x32_bf16 a[144:147], a[112:115], a[16:19], 0\n" \
"v_mfma_f32_16x16x32_bf16 a[144:147], a[116:119], a[20:23], a[144:147]\n" \
"global_load_dwordx4 a[234:237], v27, s[70:71] offset:16\n" \
"v_mfma_f32_16x16x32_bf16 a[152:155], a[112:115], a[24:27], 0\n" \
"v_mfma_f32_16x16x32_bf16 a[152:155], a[116:119], a[28:31], a[152:155]\n" \
"v_mfma_f32_16x16x32_bf16 a[160:163], a[112:115], a[32:35], 0\n" \
"v_mfma_f32_16x16x32_bf16 a[160:163], a[116:119], a[36:39], a[160:163]\n" \
"buffer_load_dwordx2 a[192:193], v54, s[40:43], 0 offen offset:0 nt\n" \
"v_mfma_f32_16x16x32_bf16 a[168:171], a[112:115], a[40:43], 0\n" \
"v_mfma_f32_16x16x32_bf16 a[168:171], a[116:119], a[44:47], a[168:171]\n" \
"s_waitcnt vmcnt(23) lgkmcnt(0)\n" \
"v_mfma_f32_16x16x32_bf16 a[176:179], a[112:115], a[48:51], 0\n" \
"s_barrier\n" \
"v_mfma_f32_16x16x32_bf16 a[176:179], a[116:119], a[52:55], a[176:179]\n" \
"buffer_load_dwordx2 a[194:195], v54, s[40:43], 0 offen offset:2048 nt\n" \
"v_mfma_f32_16x16x32_bf16 a[184:187], a[112:115], a[56:59], 0\n" \
"v_mfma_f32_16x16x32_bf16 a[184:187], a[116:119], a[60:63], a[184:187]\n" \
"v_mfma_f32_16x16x32_bf16 a[132:135], a[120:123], a[0:3], 0\n" \
"v_mfma_f32_16x16x32_bf16 a[132:135], a[124:127], a[4:7], a[132:135]\n" \
"buffer_load_dwordx2 a[196:197], v55, s[40:43], 0 offen offset:0 nt\n" \
"v_mfma_f32_16x16x32_bf16 a[140:143], a[120:123], a[8:11], 0\n" \
"s_add_u32 s84, s84, 1048576\ns_addc_u32 s85, s85, 0\n" \
"ds_read_b128 a[0:3], v5 offset:0\n" \
"v_mfma_f32_16x16x32_bf16 a[140:143], a[124:127], a[12:15], a[140:143]\n" \
"s_mov_b32 s52, 0\n" \
"ds_read_b128 a[4:7], v6 offset:0\n" \
"v_mfma_f32_16x16x32_bf16 a[148:151], a[120:123], a[16:19], 0\n" \
"ds_read_b128 a[8:11], v5 offset:2048\n" \
"v_mfma_f32_16x16x32_bf16 a[148:151], a[124:127], a[20:23], a[148:151]\n" \
"ds_read_b128 a[12:15], v6 offset:2048\n" \
"v_mfma_f32_16x16x32_bf16 a[156:159], a[120:123], a[24:27], 0\n" \
"ds_read_b128 a[16:19], v5 offset:4096\n" \
"v_mfma_f32_16x16x32_bf16 a[156:159], a[124:127], a[28:31], a[156:159]\n" \
"ds_read_b128 a[20:23], v6 offset:4096\n" \
"v_mfma_f32_16x16x32_bf16 a[164:167], a[120:123], a[32:35], 0\n" \
"ds_read_b128 a[24:27], v5 offset:6144\n" \
"v_mfma_f32_16x16x32_bf16 a[164:167], a[124:127], a[36:39], a[164:167]\n" \
"ds_read_b128 a[28:31], v6 offset:6144\n" \
"v_mfma_f32_16x16x32_bf16 a[172:175], a[120:123], a[40:43], 0\n" \
"ds_read_b128 a[32:35], v5 offset:8192\n" \
"v_mfma_f32_16x16x32_bf16 a[172:175], a[124:127], a[44:47], a[172:175]\n" \
"ds_read_b128 a[36:39], v6 offset:8192\n" \
"v_mfma_f32_16x16x32_bf16 a[180:183], a[120:123], a[48:51], 0\n" \
"ds_read_b128 a[40:43], v5 offset:10240\n" \
"v_mfma_f32_16x16x32_bf16 a[180:183], a[124:127], a[52:55], a[180:183]\n" \
"ds_read_b128 a[44:47], v6 offset:10240\n" \
"v_mfma_f32_16x16x32_bf16 a[188:191], a[120:123], a[56:59], 0\n" \
"ds_read_b128 a[48:51], v5 offset:12288\n" \
"v_mfma_f32_16x16x32_bf16 a[188:191], a[124:127], a[60:63], a[188:191]\n" \
"s_waitcnt lgkmcnt(9)\n" \
"ds_read_b128 a[52:55], v6 offset:12288\n" \
"v_mfma_f32_16x16x32_bf16 v[64:67], a[64:67], a[0:3], v[64:67]\n" \
"s_mov_b32 s47, 1572864\n" \
"s_add_u32 m0, s22, 32768\n" \
"ds_read_b128 a[56:59], v5 offset:14336\n" \
"v_mfma_f32_16x16x32_bf16 v[64:67], a[68:71], a[4:7], v[64:67]\n" \
"buffer_load_dwordx4 v21, s[32:35], s47 offen offset:0 lds\n" \
"v_mfma_f32_16x16x32_bf16 v[72:75], a[64:67], a[8:11], v[72:75]\n" \
"s_waitcnt lgkmcnt(7)\n" \
"ds_read_b128 a[60:63], v6 offset:14336\n" \
"v_mfma_f32_16x16x32_bf16 v[72:75], a[68:71], a[12:15], v[72:75]\n" \
"v_mfma_f32_16x16x32_bf16 v[80:83], a[64:67], a[16:19], v[80:83]\n" \
"ds_read_b64_tr_b16 a[96:97], v9 offset:49152\n" \
"v_mfma_f32_16x16x32_bf16 v[80:83], a[68:71], a[20:23], v[80:83]\n" \
"s_mov_b64 exec, 0xffff\nglobal_load_dword v22, v1, s[84:85]\ns_mov_b64 exec, -1\n" \
"v_mfma_f32_16x16x32_bf16 v[88:91], a[64:67], a[24:27], v[88:91]\n" \
"s_waitcnt lgkmcnt(5)\n" \
"ds_read_b64_tr_b16 a[98:99], v10 offset:49152\n" \
"v_mfma_f32_16x16x32_bf16 v[88:91], a[68:71], a[28:31], v[88:91]\n" \
"v_mfma_f32_16x16x32_bf16 v[96:99], a[64:67], a[32:35], v[96:99]\n" \
"s_mov_b32 s46, 384\n" \
"s_add_u32 m0, s50, 81920\n" \
"ds_read_b64_tr_b16 a[100:101], v9 offset:50176\n" \
"v_mfma_f32_16x16x32_bf16 v[96:99], a[68:71], a[36:39], v[96:99]\n" \
"buffer_load_dwordx4 v17, s[28:31], s46 offen lds\n" \
"v_mfma_f32_16x16x32_bf16 v[104:107], a[64:67], a[40:43], v[104:107]\n" \
"s_waitcnt lgkmcnt(3)\n" \
"ds_read_b64_tr_b16 a[102:103], v10 offset:50176\n" \
"v_mfma_f32_16x16x32_bf16 v[104:107], a[68:71], a[44:47], v[104:107]\n" \
"v_mfma_f32_16x16x32_bf16 v[112:115], a[64:67], a[48:51], v[112:115]\n" \
"ds_read_b64_tr_b16 a[104:105], v11 offset:49152\n" \
"v_mfma_f32_16x16x32_bf16 v[112:115], a[68:71], a[52:55], v[112:115]\n" \
"buffer_load_dwordx2 a[198:199], v59, s[40:43], s52 offen offset:0 nt\n" \
"v_mfma_f32_16x16x32_bf16 v[120:123], a[64:67], a[56:59], v[120:123]\n" \
"ds_read_b64_tr_b16 a[106:107], v12 offset:49152\n" \
"v_mfma_f32_16x16x32_bf16 v[120:123], a[68:71], a[60:63], v[120:123]\n" \
"v_mfma_f32_16x16x32_bf16 v[68:71], a[72:75], a[0:3], v[68:71]\n" \
"s_add_u32 m0, s22, 34560\n" \
"ds_read_b64_tr_b16 a[108:109], v11 offset:50176\n" \
"v_mfma_f32_16x16x32_bf16 v[68:71], a[76:79], a[4:7], v[68:71]\n" \
"buffer_load_dwordx4 v21, s[32:35], s47 offen offset:256 lds\n" \
"v_mfma_f32_16x16x32_bf16 v[76:79], a[72:75], a[8:11], v[76:79]\n" \
"ds_read_b64_tr_b16 a[110:111], v12 offset:50176\n" \
"v_mfma_f32_16x16x32_bf16 v[76:79], a[76:79], a[12:15], v[76:79]\n" \
"v_mfma_f32_16x16x32_bf16 v[84:87], a[72:75], a[16:19], v[84:87]\n" \
"ds_read_b64_tr_b16 a[112:113], v9 offset:49664\n" \
"v_mfma_f32_16x16x32_bf16 v[84:87], a[76:79], a[20:23], v[84:87]\n" \
"buffer_load_dwordx2 a[214:215], v59, s[80:83], s52 offen offset:0 nt\n" \
"v_mfma_f32_16x16x32_bf16 v[92:95], a[72:75], a[24:27], v[92:95]\n" \
"ds_read_b64_tr_b16 a[114:115], v10 offset:49664\n" \
"v_mfma_f32_16x16x32_bf16 v[92:95], a[76:79], a[28:31], v[92:95]\n" \
"v_mfma_f32_16x16x32_bf16 v[100:103], a[72:75], a[32:35], v[100:103]\n" \
"s_add_u32 m0, s50, 86016\n" \
"ds_read_b64_tr_b16 a[116:117], v9 offset:50688\n" \
"v_mfma_f32_16x16x32_bf16 v[100:103], a[76:79], a[36:39], v[100:103]\n" \
"buffer_load_dwordx4 v18, s[28:31], s46 offen lds\n" \
"v_mfma_f32_16x16x32_bf16 v[108:111], a[72:75], a[40:43], v[108:111]\n" \
"ds_read_b64_tr_b16 a[118:119], v10 offset:50688\n" \
"v_mfma_f32_16x16x32_bf16 v[108:111], a[76:79], a[44:47], v[108:111]\n" \
"v_mfma_f32_16x16x32_bf16 v[116:119], a[72:75], a[48:51], v[116:119]\n" \
"ds_read_b64_tr_b16 a[120:121], v11 offset:49664\n" \
"v_mfma_f32_16x16x32_bf16 v[116:119], a[76:79], a[52:55], v[116:119]\n" \
"buffer_load_dwordx2 a[200:201], v59, s[40:43], s52 offen offset:2048 nt\n" \
"v_mfma_f32_16x16x32_bf16 v[124:127], a[72:75], a[56:59], v[124:127]\n" \
"ds_read_b64_tr_b16 a[122:123], v12 offset:49664\n" \
"v_mfma_f32_16x16x32_bf16 v[124:127], a[76:79], a[60:63], v[124:127]\n" \
"v_mfma_f32_16x16x32_bf16 v[128:131], a[80:83], a[0:3], v[128:131]\n" \
"s_mov_b32 s47, 1835008\n" \
"s_add_u32 m0, s22, 33792\n" \
"ds_read_b64_tr_b16 a[124:125], v11 offset:50688\n" \
"v_mfma_f32_16x16x32_bf16 v[128:131], a[84:87], a[4:7], v[128:131]\n" \
"buffer_load_dwordx4 v21, s[32:35], s47 offen offset:0 lds\n" \
"v_mfma_f32_16x16x32_bf16 v[136:139], a[80:83], a[8:11], v[136:139]\n" \
"ds_read_b64_tr_b16 a[126:127], v12 offset:50688\n" \
"v_mfma_f32_16x16x32_bf16 v[136:139], a[84:87], a[12:15], v[136:139]\n" \
"v_mfma_f32_16x16x32_bf16 v[144:147], a[80:83], a[16:19], v[144:147]\n" \
"v_mfma_f32_16x16x32_bf16 v[144:147], a[84:87], a[20:23], v[144:147]\n" \
"buffer_load_dwordx2 a[216:217], v59, s[80:83], s52 offen offset:2048 nt\n" \
"v_mfma_f32_16x16x32_bf16 v[152:155], a[80:83], a[24:27], v[152:155]\n" \
"s_mov_b32 s52, 4096\n" \
"v_mfma_f32_16x16x32_bf16 v[152:155], a[84:87], a[28:31], v[152:155]\n" \
"v_mfma_f32_16x16x32_bf16 v[160:163], a[80:83], a[32:35], v[160:163]\n" \
"s_add_u32 m0, s50, 90112\n" \
"v_mfma_f32_16x16x32_bf16 v[160:163], a[84:87], a[36:39], v[160:163]\n" \
"buffer_load_dwordx4 v19, s[28:31], s46 offen lds\n" \
"v_mfma_f32_16x16x32_bf16 v[168:171], a[80:83], a[40:43], v[168:171]\n" \
"v_mfma_f32_16x16x32_bf16 v[168:171], a[84:87], a[44:47], v[168:171]\n" \
"s_waitcnt vmcnt(24) lgkmcnt(0)\n" \
"v_mfma_f32_16x16x32_bf16 v[176:179], a[80:83], a[48:51], v[176:179]\n" \
"s_barrier\n" \
"v_mfma_f32_16x16x32_bf16 v[176:179], a[84:87], a[52:55], v[176:179]\n" \
"buffer_load_dwordx2 a[202:203], v59, s[40:43], s52 offen offset:0 nt\n" \
"v_mfma_f32_16x16x32_bf16 v[184:187], a[80:83], a[56:59], v[184:187]\n" \
"v_mfma_f32_16x16x32_bf16 v[184:187], a[84:87], a[60:63], v[184:187]\n" \
"v_mfma_f32_16x16x32_bf16 v[132:135], a[88:91], a[0:3], v[132:135]\n" \
"s_add_u32 m0, s22, 35584\n" \
"v_mfma_f32_16x16x32_bf16 v[132:135], a[92:95], a[4:7], v[132:135]\n" \
"buffer_load_dwordx4 v21, s[32:35], s47 offen offset:256 lds\n" \
"v_mfma_f32_16x16x32_bf16 v[140:143], a[88:91], a[8:11], v[140:143]\n" \
"v_mfma_f32_16x16x32_bf16 v[140:143], a[92:95], a[12:15], v[140:143]\n" \
"v_mfma_f32_16x16x32_bf16 v[148:151], a[88:91], a[16:19], v[148:151]\n" \
"v_mfma_f32_16x16x32_bf16 v[148:151], a[92:95], a[20:23], v[148:151]\n" \
"buffer_load_dwordx2 a[218:219], v59, s[80:83], s52 offen offset:0 nt\n" \
"v_mfma_f32_16x16x32_bf16 v[156:159], a[88:91], a[24:27], v[156:159]\n" \
"v_mfma_f32_16x16x32_bf16 v[156:159], a[92:95], a[28:31], v[156:159]\n" \
"v_mfma_f32_16x16x32_bf16 v[164:167], a[88:91], a[32:35], v[164:167]\n" \
"s_add_u32 m0, s50, 94208\n" \
"v_mfma_f32_16x16x32_bf16 v[164:167], a[92:95], a[36:39], v[164:167]\n" \
"buffer_load_dwordx4 v20, s[28:31], s46 offen lds\n" \
"v_mfma_f32_16x16x32_bf16 v[172:175], a[88:91], a[40:43], v[172:175]\n" \
"v_mfma_f32_16x16x32_bf16 v[172:175], a[92:95], a[44:47], v[172:175]\n" \
"v_mfma_f32_16x16x32_bf16 v[180:183], a[88:91], a[48:51], v[180:183]\n" \
"v_mfma_f32_16x16x32_bf16 v[180:183], a[92:95], a[52:55], v[180:183]\n" \
"buffer_load_dwordx2 a[204:205], v59, s[40:43], s52 offen offset:2048 nt\n" \
"v_mfma_f32_16x16x32_bf16 v[188:191], a[88:91], a[56:59], v[188:191]\n" \
"v_mfma_f32_16x16x32_bf16 v[188:191], a[92:95], a[60:63], v[188:191]\n" \
"v_mfma_f32_16x16x32_bf16 v[192:195], a[96:99], a[0:3], v[192:195]\n" \
"s_mov_b32 s47, 1572864\n" \
"s_add_u32 m0, s22, 49024\n" \
"v_mfma_f32_16x16x32_bf16 v[192:195], a[100:103], a[4:7], v[192:195]\n" \
"buffer_load_dwordx4 v21, s[32:35], s47 offen offset:128 lds\n" \
"v_mfma_f32_16x16x32_bf16 v[200:203], a[96:99], a[8:11], v[200:203]\n" \
"v_mfma_f32_16x16x32_bf16 v[200:203], a[100:103], a[12:15], v[200:203]\n" \
"v_mfma_f32_16x16x32_bf16 v[208:211], a[96:99], a[16:19], v[208:211]\n" \
"s_mov_b32 s46, 512\n" \
"s_add_u32 m0, s50, 0\n" \
"ds_read_b64_tr_b16 a[64:65], v9 offset:0\n" \
"v_mfma_f32_16x16x32_bf16 v[208:211], a[100:103], a[20:23], v[208:211]\n" \
"buffer_load_dwordx4 v13, s[28:31], s46 offen lds\n" \
"v_mfma_f32_16x16x32_bf16 v[216:219], a[96:99], a[24:27], v[216:219]\n" \
"ds_read_b64_tr_b16 a[66:67], v10 offset:0\n" \
"v_mfma_f32_16x16x32_bf16 v[216:219], a[100:103], a[28:31], v[216:219]\n" \
"s_waitcnt vmcnt(12)\n" \
"v_accvgpr_read_b32 v43, a230\n" \
"v_lshlrev_b32 v42, 16, v43\n" \
"v_mfma_f32_16x16x32_bf16 v[224:227], a[96:99], a[32:35], v[224:227]\n" \
"ds_read_b64_tr_b16 a[68:69], v9 offset:1024\n" \
"v_and_b32 v43, 0xffff0000, v43\n" \
"v_mfma_f32_16x16x32_bf16 v[224:227], a[100:103], a[36:39], v[224:227]\n" \
"buffer_load_dwordx2 a[220:221], v59, s[80:83], s52 offen offset:2048 nt\n" \
"v_mfma_f32_16x16x32_bf16 v[232:235], a[96:99], a[40:43], v[232:235]\n" \
"s_mov_b32 s52, 8192\n" \
"ds_read_b64_tr_b16 a[70:71], v10 offset:1024\n" \
"v_accvgpr_read_b32 v45, a231\n" \
"v_mfma_f32_16x16x32_bf16 v[232:235], a[100:103], a[44:47], v[232:235]\n" \
"v_lshlrev_b32 v44, 16, v45\n" \
"v_and_b32 v45, 0xffff0000, v45\n" \
"v_mfma_f32_16x16x32_bf16 v[240:243], a[96:99], a[48:51], v[240:243]\n" \
"s_add_u32 m0, s22, 50816\n" \
"ds_read_b64_tr_b16 a[72:73], v11 offset:0\n" \
"v_accvgpr_read_b32 v47, a232\n" \
"v_mfma_f32_16x16x32_bf16 v[240:243], a[100:103], a[52:55], v[240:243]\n" \
"buffer_load_dwordx4 v21, s[32:35], s47 offen offset:384 lds\n" \
"v_mfma_f32_16x16x32_bf16 v[248:251], a[96:99], a[56:59], v[248:251]\n" \
"ds_read_b64_tr_b16 a[74:75], v12 offset:0\n" \
"v_lshlrev_b32 v46, 16, v47\n" \
"v_mfma_f32_16x16x32_bf16 v[248:251], a[100:103], a[60:63], v[248:251]\n" \
"v_and_b32 v47, 0xffff0000, v47\n" \
"v_accvgpr_read_b32 v49, a233\n" \
"v_mfma_f32_16x16x32_bf16 v[196:199], a[104:107], a[0:3], v[196:199]\n" \
"s_add_u32 m0, s50, 4096\n" \
"ds_read_b64_tr_b16 a[76:77], v11 offset:1024\n" \
"v_lshlrev_b32 v48, 16, v49\n" \
"v_mfma_f32_16x16x32_bf16 v[196:199], a[108:111], a[4:7], v[196:199]\n" \
"buffer_load_dwordx4 v14, s[28:31], s46 offen lds\n" \
"v_mfma_f32_16x16x32_bf16 v[204:207], a[104:107], a[8:11], v[204:207]\n" \
"ds_read_b64_tr_b16 a[78:79], v12 offset:1024\n" \
"v_and_b32 v49, 0xffff0000, v49\n" \
"v_mfma_f32_16x16x32_bf16 v[204:207], a[108:111], a[12:15], v[204:207]\n" \
"v_accvgpr_read_b32 v36, a192\n" \
"v_lshlrev_b32 v30, 16, v36\n" \
"v_mfma_f32_16x16x32_bf16 v[212:215], a[104:107], a[16:19], v[212:215]\n" \
"ds_read_b64_tr_b16 a[80:81], v9 offset:512\n" \
"v_and_b32 v36, 0xffff0000, v36\n" \
"v_mfma_f32_16x16x32_bf16 v[212:215], a[108:111], a[20:23], v[212:215]\n" \
"buffer_load_dwordx2 a[206:207], v59, s[40:43], s52 offen offset:0 nt\n" \
"v_mfma_f32_16x16x32_bf16 v[220:223], a[104:107], a[24:27], v[220:223]\n" \
"ds_read_b64_tr_b16 a[82:83], v10 offset:512\n" \
"v_accvgpr_read_b32 v37, a194\n" \
"v_mfma_f32_16x16x32_bf16 v[220:223], a[108:111], a[28:31], v[220:223]\n" \
"v_lshlrev_b32 v31, 16, v37\n" \
"v_and_b32 v37, 0xffff0000, v37\n" \
"v_mfma_f32_16x16x32_bf16 v[228:231], a[104:107], a[32:35], v[228:231]\n" \
"s_mov_b32 s47, 1835008\n" \
"s_add_u32 m0, s22, 50048\n" \
"ds_read_b64_tr_b16 a[84:85], v9 offset:1536\n" \
"v_mfma_f32_16x16x32_bf16 v[228:231], a[108:111], a[36:39], v[228:231]\n" \
"buffer_load_dwordx4 v21, s[32:35], s47 offen offset:128 lds\n" \
"v_mfma_f32_16x16x32_bf16 v[236:239], a[104:107], a[40:43], v[236:239]\n" \
"ds_read_b64_tr_b16 a[86:87], v10 offset:1536\n" \
"v_accvgpr_read_b32 v38, a196\n" \
"v_mfma_f32_16x16x32_bf16 v[236:239], a[108:111], a[44:47], v[236:239]\n" \
"v_lshlrev_b32 v32, 16, v38\n" \
"v_and_b32 v38, 0xffff0000, v38\n" \
"v_mfma_f32_16x16x32_bf16 v[244:247], a[104:107], a[48:51], v[244:247]\n" \
"s_add_u32 m0, s50, 8192\n" \
"ds_read_b64_tr_b16 a[88:89], v11 offset:512\n" \
"v_accvgpr_read_b32 v39, a198\n" \
"v_mfma_f32_16x16x32_bf16 v[244:247], a[108:111], a[52:55], v[244:247]\n" \
"buffer_load_dwordx4 v15, s[28:31], s46 offen lds\n" \
"v_mfma_f32_16x16x32_bf16 v[252:255], a[104:107], a[56:59], v[252:255]\n" \
"ds_read_b64_tr_b16 a[90:91], v12 offset:512\n" \
"v_lshlrev_b32 v33, 16, v39\n" \
"v_mfma_f32_16x16x32_bf16 v[252:255], a[108:111], a[60:63], v[252:255]\n" \
"v_and_b32 v39, 0xffff0000, v39\n" \
"v_mul_f32 v25, v30, v45\n" \
"v_mfma_f32_16x16x32_bf16 a[128:131], a[112:115], a[0:3], a[128:131]\n" \
"ds_read_b64_tr_b16 a[92:93], v11 offset:1536\n" \
"v_mul_f32 v26, v36, v49\n" \
"v_mfma_f32_16x16x32_bf16 a[128:131], a[116:119], a[4:7], a[128:131]\n" \
"buffer_load_dwordx2 a[222:223], v59, s[80:83], s52 offen offset:0 nt\n" \
"v_mfma_f32_16x16x32_bf16 a[136:139], a[112:115], a[8:11], a[136:139]\n" \
"ds_read_b64_tr_b16 a[94:95], v12 offset:1536\n" \
"v_fmac_f32 v25, v31, v44\n" \
"v_mfma_f32_16x16x32_bf16 a[136:139], a[116:119], a[12:15], a[136:139]\n" \
"v_fmac_f32 v26, v37, v48\n" \
"v_fmac_f32 v25, v32, v43\n" \
"v_mfma_f32_16x16x32_bf16 a[144:147], a[112:115], a[16:19], a[144:147]\n" \
"s_add_u32 m0, s22, 51840\n" \
"v_fmac_f32 v26, v38, v47\n" \
"v_fmac_f32 v25, v33, v42\n" \
"v_mfma_f32_16x16x32_bf16 a[144:147], a[116:119], a[20:23], a[144:147]\n" \
"buffer_load_dwordx4 v21, s[32:35], s47 offen offset:384 lds\n" \
"v_mfma_f32_16x16x32_bf16 a[152:155], a[112:115], a[24:27], a[152:155]\n" \
"v_fmac_f32 v26, v39, v46\n" \
"v_accvgpr_read_b32 v28, a214\n" \
"v_mfma_f32_16x16x32_bf16 a[152:155], a[116:119], a[28:31], a[152:155]\n" \
"v_lshlrev_b32 v27, 16, v28\n" \
"v_and_b32 v28, 0xffff0000, v28\n" \
"v_mfma_f32_16x16x32_bf16 a[160:163], a[112:115], a[32:35], a[160:163]\n" \
"v_mul_f32 v29, v25, v27\n" \
"v_mfma_f32_16x16x32_bf16 a[160:163], a[116:119], a[36:39], a[160:163]\n" \
"buffer_load_dwordx2 a[208:209], v59, s[40:43], s52 offen offset:2048 nt\n" \
"v_mfma_f32_16x16x32_bf16 a[168:171], a[112:115], a[40:43], a[168:171]\n" \
"s_waitcnt vmcnt(17)\n" \
"v_mul_f32 v58, v26, v28\n" \
"v_accvgpr_read_b32 v40, a200\n" \
"v_mfma_f32_16x16x32_bf16 a[168:171], a[116:119], a[44:47], a[168:171]\n" \
"s_waitcnt vmcnt(33) lgkmcnt(0)\n" \
"v_lshlrev_b32 v34, 16, v40\n" \
"v_and_b32 v40, 0xffff0000, v40\n" \
"v_mfma_f32_16x16x32_bf16 a[176:179], a[112:115], a[48:51], a[176:179]\n" \
"s_add_u32 m0, s50, 12288\n" \
"s_barrier\n" \
"v_mul_f32 v25, v31, v45\n" \
"v_mfma_f32_16x16x32_bf16 a[176:179], a[116:119], a[52:55], a[176:179]\n" \
"buffer_load_dwordx4 v16, s[28:31], s46 offen lds\n" \
"v_mfma_f32_16x16x32_bf16 a[184:187], a[112:115], a[56:59], a[184:187]\n" \
"v_mul_f32 v26, v37, v49\n" \
"v_fmac_f32 v25, v32, v44\n" \
"v_mfma_f32_16x16x32_bf16 a[184:187], a[116:119], a[60:63], a[184:187]\n" \
"v_fmac_f32 v26, v38, v48\n" \
"v_fmac_f32 v25, v33, v43\n" \
"v_mfma_f32_16x16x32_bf16 a[132:135], a[120:123], a[0:3], a[132:135]\n" \
"v_fmac_f32 v26, v39, v47\n" \
"v_mfma_f32_16x16x32_bf16 a[132:135], a[124:127], a[4:7], a[132:135]\n" \
"buffer_load_dwordx2 a[224:225], v59, s[80:83], s52 offen offset:2048 nt\n" \
"v_mfma_f32_16x16x32_bf16 a[140:143], a[120:123], a[8:11], a[140:143]\n" \
"s_mov_b32 s52, 12288\n" \
"ds_read_b128 a[0:3], v5 offset:32768\n" \
"v_fmac_f32 v25, v34, v42\n" \
"v_mfma_f32_16x16x32_bf16 a[140:143], a[124:127], a[12:15], a[140:143]\n" \
"ds_read_b128 a[4:7], v6 offset:32768\n" \
"v_fmac_f32 v26, v40, v46\n" \
"v_mfma_f32_16x16x32_bf16 a[148:151], a[120:123], a[16:19], a[148:151]\n" \
"ds_read_b128 a[8:11], v5 offset:34816\n" \
"v_accvgpr_read_b32 v28, a216\n" \
"v_mfma_f32_16x16x32_bf16 a[148:151], a[124:127], a[20:23], a[148:151]\n" \
"ds_read_b128 a[12:15], v6 offset:34816\n" \
"v_lshlrev_b32 v27, 16, v28\n" \
"v_mfma_f32_16x16x32_bf16 a[156:159], a[120:123], a[24:27], a[156:159]\n" \
"ds_read_b128 a[16:19], v5 offset:36864\n" \
"v_and_b32 v28, 0xffff0000, v28\n" \
"v_mfma_f32_16x16x32_bf16 a[156:159], a[124:127], a[28:31], a[156:159]\n" \
"ds_read_b128 a[20:23], v6 offset:36864\n" \
"v_mul_f32 v25, v25, v27\n" \
"v_mfma_f32_16x16x32_bf16 a[164:167], a[120:123], a[32:35], a[164:167]\n" \
"ds_read_b128 a[24:27], v5 offset:38912\n" \
"v_mul_f32 v26, v26, v28\n" \
"v_mfma_f32_16x16x32_bf16 a[164:167], a[124:127], a[36:39], a[164:167]\n" \
"ds_read_b128 a[28:31], v6 offset:38912\n" \
"v_cvt_pk_bf16_f32 v53, v25, v29\n" \
"v_mfma_f32_16x16x32_bf16 a[172:175], a[120:123], a[40:43], a[172:175]\n" \
"ds_read_b128 a[32:35], v5 offset:40960\n" \
"v_cvt_pk_bf16_f32 v57, v26, v58\n" \
"v_mfma_f32_16x16x32_bf16 a[172:175], a[124:127], a[44:47], a[172:175]\n" \
"ds_read_b128 a[36:39], v6 offset:40960\n" \
"s_waitcnt vmcnt(15)\n" \
"v_mfma_f32_16x16x32_bf16 a[180:183], a[120:123], a[48:51], a[180:183]\n" \
"ds_read_b128 a[40:43], v5 offset:43008\n" \
"v_accvgpr_read_b32 v41, a202\n" \
"v_mfma_f32_16x16x32_bf16 a[180:183], a[124:127], a[52:55], a[180:183]\n" \
"LH0_%=:\ns_cbranch_execz LH8_%=\n" \
"ds_read_b128 a[44:47], v6 offset:43008\n" \
"v_lshlrev_b32 v35, 16, v41\n" \
"v_mfma_f32_16x16x32_bf16 a[188:191], a[120:123], a[56:59], a[188:191]\n" \
"ds_read_b128 a[48:51], v5 offset:45056\n" \
"v_and_b32 v41, 0xffff0000, v41\n" \
"v_mfma_f32_16x16x32_bf16 a[188:191], a[124:127], a[60:63], a[188:191]\n" \
"s_waitcnt lgkmcnt(9)\n" \
"ds_read_b128 a[52:55], v6 offset:45056\n" \
"v_mul_f32 v25, v32, v45\n" \
"v_mfma_f32_16x16x32_bf16 v[64:67], a[64:67], a[0:3], v[64:67]\n" \
"s_mov_b32 s47, 2097152\n" \
"s_add_u32 m0, s22, 0\n" \
"ds_read_b128 a[56:59], v5 offset:47104\n" \
"v_mfma_f32_16x16x32_bf16 v[64:67], a[68:71], a[4:7], v[64:67]\n" \
"buffer_load_dwordx4 v21, s[32:35], s47 offen offset:0 lds\n" \
"v_mfma_f32_16x16x32_bf16 v[72:75], a[64:67], a[8:11], v[72:75]\n" \
"s_waitcnt lgkmcnt(7)\n" \
"ds_read_b128 a[60:63], v6 offset:47104\n" \
"v_mul_f32 v26, v38, v49\n" \
"v_mfma_f32_16x16x32_bf16 v[72:75], a[68:71], a[12:15], v[72:75]\n" \
"v_fmac_f32 v25, v33, v44\n" \
"v_fmac_f32 v26, v39, v48\n" \
"v_mfma_f32_16x16x32_bf16 v[80:83], a[64:67], a[16:19], v[80:83]\n" \
"ds_read_b64_tr_b16 a[96:97], v9 offset:16384\n" \
"v_fmac_f32 v25, v34, v43\n" \
"v_mfma_f32_16x16x32_bf16 v[80:83], a[68:71], a[20:23], v[80:83]\n" \
"buffer_load_dwordx2 a[210:211], v59, s[40:43], s52 offen offset:0 nt\n" \
"v_mfma_f32_16x16x32_bf16 v[88:91], a[64:67], a[24:27], v[88:91]\n" \
"s_waitcnt lgkmcnt(5)\n" \
"ds_read_b64_tr_b16 a[98:99], v10 offset:16384\n" \
"v_fmac_f32 v26, v40, v47\n" \
"v_mfma_f32_16x16x32_bf16 v[88:91], a[68:71], a[28:31], v[88:91]\n" \
"v_fmac_f32 v25, v35, v42\n" \
"v_fmac_f32 v26, v41, v46\n" \
"v_mfma_f32_16x16x32_bf16 v[96:99], a[64:67], a[32:35], v[96:99]\n" \
"s_mov_b32 s46, 512\n" \
"s_add_u32 m0, s50, 16384\n" \
"ds_read_b64_tr_b16 a[100:101], v9 offset:17408\n" \
"v_mfma_f32_16x16x32_bf16 v[96:99], a[68:71], a[36:39], v[96:99]\n" \
"buffer_load_dwordx4 v17, s[28:31], s46 offen lds\n" \
"v_mfma_f32_16x16x32_bf16 v[104:107], a[64:67], a[40:43], v[104:107]\n" \
"s_waitcnt lgkmcnt(3)\n" \
"ds_read_b64_tr_b16 a[102:103], v10 offset:17408\n" \
"v_accvgpr_read_b32 v28, a218\n" \
"v_mfma_f32_16x16x32_bf16 v[104:107], a[68:71], a[44:47], v[104:107]\n" \
"v_lshlrev_b32 v27, 16, v28\n" \
"v_and_b32 v28, 0xffff0000, v28\n" \
"v_mfma_f32_16x16x32_bf16 v[112:115], a[64:67], a[48:51], v[112:115]\n" \
"ds_read_b64_tr_b16 a[104:105], v11 offset:16384\n" \
"v_mul_f32 v29, v25, v27\n" \
"v_mfma_f32_16x16x32_bf16 v[112:115], a[68:71], a[52:55], v[112:115]\n" \
"buffer_load_dwordx2 a[226:227], v59, s[80:83], s52 offen offset:0 nt\n" \
"v_mfma_f32_16x16x32_bf16 v[120:123], a[64:67], a[56:59], v[120:123]\n" \
"ds_read_b64_tr_b16 a[106:107], v12 offset:16384\n" \
"v_mul_f32 v58, v26, v28\n" \
"v_mfma_f32_16x16x32_bf16 v[120:123], a[68:71], a[60:63], v[120:123]\n" \
"s_waitcnt vmcnt(14)\n" \
"v_accvgpr_read_b32 v36, a204\n" \
"v_lshlrev_b32 v30, 16, v36\n" \
"v_mfma_f32_16x16x32_bf16 v[68:71], a[72:75], a[0:3], v[68:71]\n" \
"s_add_u32 m0, s22, 1792\n" \
"ds_read_b64_tr_b16 a[108:109], v11 offset:17408\n" \
"v_and_b32 v36, 0xffff0000, v36\n" \
"v_mfma_f32_16x16x32_bf16 v[68:71], a[76:79], a[4:7], v[68:71]\n" \
"buffer_load_dwordx4 v21, s[32:35], s47 offen offset:256 lds\n" \
"v_mfma_f32_16x16x32_bf16 v[76:79], a[72:75], a[8:11], v[76:79]\n" \
"ds_read_b64_tr_b16 a[110:111], v12 offset:17408\n" \
"v_mul_f32 v25, v33, v45\n" \
"v_mfma_f32_16x16x32_bf16 v[76:79], a[76:79], a[12:15], v[76:79]\n" \
"v_mul_f32 v26, v39, v49\n" \
"v_fmac_f32 v25, v34, v44\n" \
"v_mfma_f32_16x16x32_bf16 v[84:87], a[72:75], a[16:19], v[84:87]\n" \
"ds_read_b64_tr_b16 a[112:113], v9 offset:16896\n" \
"v_fmac_f32 v26, v40, v48\n" \
"v_mfma_f32_16x16x32_bf16 v[84:87], a[76:79], a[20:23], v[84:87]\n" \
"buffer_load_dwordx2 a[212:213], v59, s[40:43], s52 offen offset:2048 nt\n" \
"v_mfma_f32_16x16x32_bf16 v[92:95], a[72:75], a[24:27], v[92:95]\n" \
"ds_read_b64_tr_b16 a[114:115], v10 offset:16896\n" \
"v_fmac_f32 v25, v35, v43\n" \
"v_mfma_f32_16x16x32_bf16 v[92:95], a[76:79], a[28:31], v[92:95]\n" \
"v_fmac_f32 v26, v41, v47\n" \
"v_fmac_f32 v25, v30, v42\n" \
"v_mfma_f32_16x16x32_bf16 v[100:103], a[72:75], a[32:35], v[100:103]\n" \
"s_add_u32 m0, s50, 20480\n" \
"ds_read_b64_tr_b16 a[116:117], v9 offset:17920\n" \
"v_fmac_f32 v26, v36, v46\n" \
"v_mfma_f32_16x16x32_bf16 v[100:103], a[76:79], a[36:39], v[100:103]\n" \
"buffer_load_dwordx4 v18, s[28:31], s46 offen lds\n" \
"v_mfma_f32_16x16x32_bf16 v[108:111], a[72:75], a[40:43], v[108:111]\n" \
"ds_read_b64_tr_b16 a[118:119], v10 offset:17920\n" \
"v_accvgpr_read_b32 v28, a220\n" \
"v_mfma_f32_16x16x32_bf16 v[108:111], a[76:79], a[44:47], v[108:111]\n" \
"v_lshlrev_b32 v27, 16, v28\n" \
"v_and_b32 v28, 0xffff0000, v28\n" \
"v_mfma_f32_16x16x32_bf16 v[116:119], a[72:75], a[48:51], v[116:119]\n" \
"ds_read_b64_tr_b16 a[120:121], v11 offset:16896\n" \
"v_mul_f32 v25, v25, v27\n" \
"v_mfma_f32_16x16x32_bf16 v[116:119], a[76:79], a[52:55], v[116:119]\n" \
"buffer_load_dwordx2 a[228:229], v59, s[80:83], s52 offen offset:2048 nt\n" \
"v_mfma_f32_16x16x32_bf16 v[124:127], a[72:75], a[56:59], v[124:127]\n" \
"ds_read_b64_tr_b16 a[122:123], v12 offset:16896\n" \
"v_mul_f32 v26, v26, v28\n" \
"v_mfma_f32_16x16x32_bf16 v[124:127], a[76:79], a[60:63], v[124:127]\n" \
"s_waitcnt vmcnt(12)\n" \
"v_cvt_pk_bf16_f32 v52, v25, v29\n" \
"v_cvt_pk_bf16_f32 v56, v26, v58\n" \
"v_mfma_f32_16x16x32_bf16 v[128:131], a[80:83], a[0:3], v[128:131]\n" \
"s_mov_b32 s47, 2359296\n" \
"s_add_u32 m0, s22, 1024\n" \
"ds_read_b64_tr_b16 a[124:125], v11 offset:17920\n" \
"v_mfma_f32_16x16x32_bf16 v[128:131], a[84:87], a[4:7], v[128:131]\n" \
"buffer_load_dwordx4 v21, s[32:35], s47 offen offset:0 lds\n" \
"v_mfma_f32_16x16x32_bf16 v[136:139], a[80:83], a[8:11], v[136:139]\n" \
"ds_read_b64_tr_b16 a[126:127], v12 offset:17920\n" \
"v_accvgpr_read_b32 v37, a206\n" \
"v_mfma_f32_16x16x32_bf16 v[136:139], a[84:87], a[12:15], v[136:139]\n" \
"v_lshlrev_b32 v31, 16, v37\n" \
"v_and_b32 v37, 0xffff0000, v37\n" \
"v_mfma_f32_16x16x32_bf16 v[144:147], a[80:83], a[16:19], v[144:147]\n" \
"v_mul_f32 v25, v34, v45\n" \
"v_mul_f32 v26, v40, v49\n" \
"v_mfma_f32_16x16x32_bf16 v[144:147], a[84:87], a[20:23], v[144:147]\n" \
"v_fmac_f32 v25, v35, v44\n" \
"v_mfma_f32_16x16x32_bf16 v[152:155], a[80:83], a[24:27], v[152:155]\n" \
"v_fmac_f32 v26, v41, v48\n" \
"v_fmac_f32 v25, v30, v43\n" \
"v_mfma_f32_16x16x32_bf16 v[152:155], a[84:87], a[28:31], v[152:155]\n" \
"v_fmac_f32 v26, v36, v47\n" \
"v_fmac_f32 v25, v31, v42\n" \
"v_mfma_f32_16x16x32_bf16 v[160:163], a[80:83], a[32:35], v[160:163]\n" \
"s_add_u32 m0, s50, 24576\n" \
"v_fmac_f32 v26, v37, v46\n" \
"v_mfma_f32_16x16x32_bf16 v[160:163], a[84:87], a[36:39], v[160:163]\n" \
"buffer_load_dwordx4 v19, s[28:31], s46 offen lds\n" \
"v_mfma_f32_16x16x32_bf16 v[168:171], a[80:83], a[40:43], v[168:171]\n" \
"v_accvgpr_read_b32 v28, a222\n" \
"v_lshlrev_b32 v27, 16, v28\n" \
"v_mfma_f32_16x16x32_bf16 v[168:171], a[84:87], a[44:47], v[168:171]\n" \
"s_waitcnt vmcnt(26) lgkmcnt(0)\n" \
"v_and_b32 v28, 0xffff0000, v28\n" \
"v_mul_f32 v29, v25, v27\n" \
"v_mfma_f32_16x16x32_bf16 v[176:179], a[80:83], a[48:51], v[176:179]\n" \
"s_barrier\n" \
"v_mul_f32 v58, v26, v28\n" \
"s_waitcnt vmcnt(10)\n" \
"v_mfma_f32_16x16x32_bf16 v[176:179], a[84:87], a[52:55], v[176:179]\n" \
"v_accvgpr_read_b32 v38, a208\n" \
"v_lshlrev_b32 v32, 16, v38\n" \
"v_mfma_f32_16x16x32_bf16 v[184:187], a[80:83], a[56:59], v[184:187]\n" \
"v_and_b32 v38, 0xffff0000, v38\n" \
"v_mul_f32 v25, v35, v45\n" \
"v_mfma_f32_16x16x32_bf16 v[184:187], a[84:87], a[60:63], v[184:187]\n" \
"v_mul_f32 v26, v41, v49\n" \
"v_mfma_f32_16x16x32_bf16 v[132:135], a[88:91], a[0:3], v[132:135]\n" \
"s_add_u32 m0, s22, 2816\n" \
"v_fmac_f32 v25, v30, v44\n" \
"v_fmac_f32 v26, v36, v48\n" \
"v_mfma_f32_16x16x32_bf16 v[132:135], a[92:95], a[4:7], v[132:135]\n" \
"buffer_load_dwordx4 v21, s[32:35], s47 offen offset:256 lds\n" \
"v_mfma_f32_16x16x32_bf16 v[140:143], a[88:91], a[8:11], v[140:143]\n" \
"s_add_u32 s84, s84, 1048576\ns_addc_u32 s85, s85, 0\n" \
"v_fmac_f32 v25, v31, v43\n" \
"v_mfma_f32_16x16x32_bf16 v[140:143], a[92:95], a[12:15], v[140:143]\n" \
"v_fmac_f32 v26, v37, v47\n" \
"v_fmac_f32 v25, v32, v42\n" \
"v_mfma_f32_16x16x32_bf16 v[148:151], a[88:91], a[16:19], v[148:151]\n" \
"v_fmac_f32 v26, v38, v46\n" \
"v_accvgpr_read_b32 v28, a224\n" \
"v_mfma_f32_16x16x32_bf16 v[148:151], a[92:95], a[20:23], v[148:151]\n" \
"v_lshlrev_b32 v27, 16, v28\n" \
"v_mfma_f32_16x16x32_bf16 v[156:159], a[88:91], a[24:27], v[156:159]\n" \
"v_and_b32 v28, 0xffff0000, v28\n" \
"v_mul_f32 v25, v25, v27\n" \
"v_mfma_f32_16x16x32_bf16 v[156:159], a[92:95], a[28:31], v[156:159]\n" \
"v_mul_f32 v26, v26, v28\n" \
"v_cvt_pk_bf16_f32 v51, v25, v29\n" \
"v_mfma_f32_16x16x32_bf16 v[164:167], a[88:91], a[32:35], v[164:167]\n" \
"s_add_u32 m0, s50, 28672\n" \
"v_cvt_pk_bf16_f32 v55, v26, v58\n" \
"v_mfma_f32_16x16x32_bf16 v[164:167], a[92:95], a[36:39], v[164:167]\n" \
"buffer_load_dwordx4 v20, s[28:31], s46 offen lds\n" \
"v_mfma_f32_16x16x32_bf16 v[172:175], a[88:91], a[40:43], v[172:175]\n" \
"v_mfma_f32_16x16x32_bf16 v[172:175], a[92:95], a[44:47], v[172:175]\n" \
"v_mfma_f32_16x16x32_bf16 v[180:183], a[88:91], a[48:51], v[180:183]\n" \
"v_mfma_f32_16x16x32_bf16 v[180:183], a[92:95], a[52:55], v[180:183]\n" \
"s_mov_b64 exec, 0xffff\nglobal_load_dword v22, v1, s[84:85]\ns_mov_b64 exec, -1\n" \
"v_mfma_f32_16x16x32_bf16 v[188:191], a[88:91], a[56:59], v[188:191]\n" \
"v_mfma_f32_16x16x32_bf16 v[188:191], a[92:95], a[60:63], v[188:191]\n" \
"s_waitcnt vmcnt(9)\n" \
"v_accvgpr_read_b32 v39, a210\n" \
"v_lshlrev_b32 v33, 16, v39\n" \
"v_mfma_f32_16x16x32_bf16 v[192:195], a[96:99], a[0:3], v[192:195]\n" \
"s_mov_b32 s47, 2097152\n" \
"s_add_u32 m0, s22, 16256\n" \
"v_and_b32 v39, 0xffff0000, v39\n" \
"v_mfma_f32_16x16x32_bf16 v[192:195], a[100:103], a[4:7], v[192:195]\n" \
"buffer_load_dwordx4 v21, s[32:35], s47 offen offset:128 lds\n" \
"v_mfma_f32_16x16x32_bf16 v[200:203], a[96:99], a[8:11], v[200:203]\n" \
"v_mul_f32 v25, v30, v45\n" \
"v_mul_f32 v26, v36, v49\n" \
"v_mfma_f32_16x16x32_bf16 v[200:203], a[100:103], a[12:15], v[200:203]\n" \
"v_fmac_f32 v25, v31, v44\n" \
"v_fmac_f32 v26, v37, v48\n" \
"v_mfma_f32_16x16x32_bf16 v[208:211], a[96:99], a[16:19], v[208:211]\n" \
"s_mov_b32 s46, 640\n" \
"s_add_u32 m0, s50, 32768\n" \
"ds_read_b64_tr_b16 a[64:65], v9 offset:32768\n" \
"v_mfma_f32_16x16x32_bf16 v[208:211], a[100:103], a[20:23], v[208:211]\n" \
"buffer_load_dwordx4 v13, s[28:31], s46 offen lds\n" \
"v_mfma_f32_16x16x32_bf16 v[216:219], a[96:99], a[24:27], v[216:219]\n" \
"ds_read_b64_tr_b16 a[66:67], v10 offset:32768\n" \
"v_fmac_f32 v25, v32, v43\n" \
"v_mfma_f32_16x16x32_bf16 v[216:219], a[100:103], a[28:31], v[216:219]\n" \
"v_fmac_f32 v26, v38, v47\n" \
"v_fmac_f32 v25, v33, v42\n" \
"v_mfma_f32_16x16x32_bf16 v[224:227], a[96:99], a[32:35], v[224:227]\n" \
"ds_read_b64_tr_b16 a[68:69], v9 offset:33792\n" \
"v_fmac_f32 v26, v39, v46\n" \
"v_mfma_f32_16x16x32_bf16 v[224:227], a[100:103], a[36:39], v[224:227]\n" \
"v_accvgpr_read_b32 v28, a226\n" \
"v_lshlrev_b32 v27, 16, v28\n" \
"v_mfma_f32_16x16x32_bf16 v[232:235], a[96:99], a[40:43], v[232:235]\n" \
"ds_read_b64_tr_b16 a[70:71], v10 offset:33792\n" \
"v_and_b32 v28, 0xffff0000, v28\n" \
"v_mfma_f32_16x16x32_bf16 v[232:235], a[100:103], a[44:47], v[232:235]\n" \
"v_mul_f32 v29, v25, v27\n" \
"v_mul_f32 v58, v26, v28\n" \
"v_mfma_f32_16x16x32_bf16 v[240:243], a[96:99], a[48:51], v[240:243]\n" \
"s_add_u32 m0, s22, 18048\n" \
"ds_read_b64_tr_b16 a[72:73], v11 offset:32768\n" \
"v_mfma_f32_16x16x32_bf16 v[240:243], a[100:103], a[52:55], v[240:243]\n" \
"buffer_load_dwordx4 v21, s[32:35], s47 offen offset:384 lds\n" \
"v_mfma_f32_16x16x32_bf16 v[248:251], a[96:99], a[56:59], v[248:251]\n" \
"ds_read_b64_tr_b16 a[74:75], v12 offset:32768\n" \
"v_mfma_f32_16x16x32_bf16 v[248:251], a[100:103], a[60:63], v[248:251]\n" \
"s_waitcnt vmcnt(8)\n" \
"v_accvgpr_read_b32 v40, a212\n" \
"v_lshlrev_b32 v34, 16, v40\n" \
"v_mfma_f32_16x16x32_bf16 v[196:199], a[104:107], a[0:3], v[196:199]\n" \
"s_add_u32 m0, s50, 36864\n" \
"ds_read_b64_tr_b16 a[76:77], v11 offset:33792\n" \
"v_and_b32 v40, 0xffff0000, v40\n" \
"v_mfma_f32_16x16x32_bf16 v[196:199], a[108:111], a[4:7], v[196:199]\n" \
"buffer_load_dwordx4 v14, s[28:31], s46 offen lds\n" \
"v_mfma_f32_16x16x32_bf16 v[204:207], a[104:107], a[8:11], v[204:207]\n" \
"ds_read_b64_tr_b16 a[78:79], v12 offset:33792\n" \
"v_mul_f32 v25, v31, v45\n" \
"v_mfma_f32_16x16x32_bf16 v[204:207], a[108:111], a[12:15], v[204:207]\n" \
"v_mul_f32 v26, v37, v49\n" \
"v_fmac_f32 v25, v32, v44\n" \
"v_mfma_f32_16x16x32_bf16 v[212:215], a[104:107], a[16:19], v[212:215]\n" \
"ds_read_b64_tr_b16 a[80:81], v9 offset:33280\n" \
"v_fmac_f32 v26, v38, v48\n" \
"v_mfma_f32_16x16x32_bf16 v[212:215], a[108:111], a[20:23], v[212:215]\n" \
"v_fmac_f32 v25, v33, v43\n" \
"v_fmac_f32 v26, v39, v47\n" \
"v_mfma_f32_16x16x32_bf16 v[220:223], a[104:107], a[24:27], v[220:223]\n" \
"ds_read_b64_tr_b16 a[82:83], v10 offset:33280\n" \
"v_fmac_f32 v25, v34, v42\n" \
"v_mfma_f32_16x16x32_bf16 v[220:223], a[108:111], a[28:31], v[220:223]\n" \
"v_fmac_f32 v26, v40, v46\n" \
"v_accvgpr_read_b32 v28, a228\n" \
"v_mfma_f32_16x16x32_bf16 v[228:231], a[104:107], a[32:35], v[228:231]\n" \
"s_mov_b32 s47, 2359296\n" \
"s_add_u32 m0, s22, 17280\n" \
"ds_read_b64_tr_b16 a[84:85], v9 offset:34304\n" \
"v_mfma_f32_16x16x32_bf16 v[228:231], a[108:111], a[36:39], v[228:231]\n" \
"buffer_load_dwordx4 v21, s[32:35], s47 offen offset:128 lds\n" \
"v_mfma_f32_16x16x32_bf16 v[236:239], a[104:107], a[40:43], v[236:239]\n" \
"ds_read_b64_tr_b16 a[86:87], v10 offset:34304\n" \
"v_lshlrev_b32 v27, 16, v28\n" \
"v_mfma_f32_16x16x32_bf16 v[236:239], a[108:111], a[44:47], v[236:239]\n" \
"v_and_b32 v28, 0xffff0000, v28\n" \
"v_mul_f32 v25, v25, v27\n" \
"v_mfma_f32_16x16x32_bf16 v[244:247], a[104:107], a[48:51], v[244:247]\n" \
"s_add_u32 m0, s50, 40960\n" \
"ds_read_b64_tr_b16 a[88:89], v11 offset:33280\n" \
"v_mul_f32 v26, v26, v28\n" \
"v_mfma_f32_16x16x32_bf16 v[244:247], a[108:111], a[52:55], v[244:247]\n" \
"buffer_load_dwordx4 v15, s[28:31], s46 offen lds\n" \
"v_mfma_f32_16x16x32_bf16 v[252:255], a[104:107], a[56:59], v[252:255]\n" \
"ds_read_b64_tr_b16 a[90:91], v12 offset:33280\n" \
"v_cvt_pk_bf16_f32 v50, v25, v29\n" \
"v_mfma_f32_16x16x32_bf16 v[252:255], a[108:111], a[60:63], v[252:255]\n" \
"s_mov_b32 s94, 79691776\n" \
"v_cvt_pk_bf16_f32 v54, v26, v58\n" \
"v_accvgpr_read_b32 v43, a234\n" \
"v_mfma_f32_16x16x32_bf16 a[128:131], a[112:115], a[0:3], a[128:131]\n" \
"ds_read_b64_tr_b16 a[92:93], v11 offset:34304\n" \
"v_lshlrev_b32 v42, 16, v43\n" \
"v_mfma_f32_16x16x32_bf16 a[128:131], a[116:119], a[4:7], a[128:131]\n" \
"buffer_store_dwordx4 v[50:53], v60, s[36:39], s94 offen offset:0 sc1\n" \
"v_mfma_f32_16x16x32_bf16 a[136:139], a[112:115], a[8:11], a[136:139]\n" \
"s_mov_b32 s95, 79695872\n" \
"ds_read_b64_tr_b16 a[94:95], v12 offset:34304\n" \
"v_and_b32 v43, 0xffff0000, v43\n" \
"v_mfma_f32_16x16x32_bf16 a[136:139], a[116:119], a[12:15], a[136:139]\n" \
"v_accvgpr_read_b32 v45, a235\n" \
"v_lshlrev_b32 v44, 16, v45\n" \
"v_mfma_f32_16x16x32_bf16 a[144:147], a[112:115], a[16:19], a[144:147]\n" \
"s_add_u32 m0, s22, 19072\n" \
"v_and_b32 v45, 0xffff0000, v45\n" \
"v_accvgpr_read_b32 v47, a236\n" \
"v_mfma_f32_16x16x32_bf16 a[144:147], a[116:119], a[20:23], a[144:147]\n" \
"buffer_load_dwordx4 v21, s[32:35], s47 offen offset:384 lds\n" \
"v_mfma_f32_16x16x32_bf16 a[152:155], a[112:115], a[24:27], a[152:155]\n" \
"v_lshlrev_b32 v46, 16, v47\n" \
"v_and_b32 v47, 0xffff0000, v47\n" \
"v_mfma_f32_16x16x32_bf16 a[152:155], a[116:119], a[28:31], a[152:155]\n" \
"v_accvgpr_read_b32 v49, a237\n" \
"v_lshlrev_b32 v48, 16, v49\n" \
"v_mfma_f32_16x16x32_bf16 a[160:163], a[112:115], a[32:35], a[160:163]\n" \
"v_and_b32 v49, 0xffff0000, v49\n" \
"v_mfma_f32_16x16x32_bf16 a[160:163], a[116:119], a[36:39], a[160:163]\n" \
"v_accvgpr_read_b32 v36, a193\n" \
"v_lshlrev_b32 v30, 16, v36\n" \
"v_mfma_f32_16x16x32_bf16 a[168:171], a[112:115], a[40:43], a[168:171]\n" \
"v_and_b32 v36, 0xffff0000, v36\n" \
"v_accvgpr_read_b32 v37, a195\n" \
"v_mfma_f32_16x16x32_bf16 a[168:171], a[116:119], a[44:47], a[168:171]\n" \
"s_waitcnt vmcnt(24) lgkmcnt(0)\n" \
"v_lshlrev_b32 v31, 16, v37\n" \
"v_mfma_f32_16x16x32_bf16 a[176:179], a[112:115], a[48:51], a[176:179]\n" \
"s_add_u32 m0, s50, 45056\n" \
"s_barrier\n" \
"v_and_b32 v37, 0xffff0000, v37\n" \
"v_mfma_f32_16x16x32_bf16 a[176:179], a[116:119], a[52:55], a[176:179]\n" \
"buffer_load_dwordx4 v16, s[28:31], s46 offen lds\n" \
"v_mfma_f32_16x16x32_bf16 a[184:187], a[112:115], a[56:59], a[184:187]\n" \
"v_accvgpr_read_b32 v38, a197\n" \
"v_lshlrev_b32 v32, 16, v38\n" \
"v_mfma_f32_16x16x32_bf16 a[184:187], a[116:119], a[60:63], a[184:187]\n" \
"buffer_store_dwordx4 v[54:57], v60, s[36:39], s95 offen offset:0 sc1\n" \
"v_mfma_f32_16x16x32_bf16 a[132:135], a[120:123], a[0:3], a[132:135]\n" \
"v_and_b32 v38, 0xffff0000, v38\n" \
"v_accvgpr_read_b32 v39, a199\n" \
"v_mfma_f32_16x16x32_bf16 a[132:135], a[124:127], a[4:7], a[132:135]\n" \
"v_lshlrev_b32 v33, 16, v39\n" \
"v_and_b32 v39, 0xffff0000, v39\n" \
"v_mfma_f32_16x16x32_bf16 a[140:143], a[120:123], a[8:11], a[140:143]\n" \
"ds_read_b128 a[0:3], v7 offset:0\n" \
"v_mul_f32 v25, v30, v45\n" \
"v_mfma_f32_16x16x32_bf16 a[140:143], a[124:127], a[12:15], a[140:143]\n" \
"ds_read_b128 a[4:7], v8 offset:0\n" \
"v_mul_f32 v26, v36, v49\n" \
"v_mfma_f32_16x16x32_bf16 a[148:151], a[120:123], a[16:19], a[148:151]\n" \
"ds_read_b128 a[8:11], v7 offset:2048\n" \
"v_fmac_f32 v25, v31, v44\n" \
"v_mfma_f32_16x16x32_bf16 a[148:151], a[124:127], a[20:23], a[148:151]\n" \
"ds_read_b128 a[12:15], v8 offset:2048\n" \
"v_fmac_f32 v26, v37, v48\n" \
"v_mfma_f32_16x16x32_bf16 a[156:159], a[120:123], a[24:27], a[156:159]\n" \
"ds_read_b128 a[16:19], v7 offset:4096\n" \
"v_fmac_f32 v25, v32, v43\n" \
"v_mfma_f32_16x16x32_bf16 a[156:159], a[124:127], a[28:31], a[156:159]\n" \
"ds_read_b128 a[20:23], v8 offset:4096\n" \
"v_fmac_f32 v26, v38, v47\n" \
"v_mfma_f32_16x16x32_bf16 a[164:167], a[120:123], a[32:35], a[164:167]\n" \
"ds_read_b128 a[24:27], v7 offset:6144\n" \
"v_fmac_f32 v25, v33, v42\n" \
"v_mfma_f32_16x16x32_bf16 a[164:167], a[124:127], a[36:39], a[164:167]\n" \
"ds_read_b128 a[28:31], v8 offset:6144\n" \
"v_fmac_f32 v26, v39, v46\n" \
"v_mfma_f32_16x16x32_bf16 a[172:175], a[120:123], a[40:43], a[172:175]\n" \
"ds_read_b128 a[32:35], v7 offset:8192\n" \
"v_accvgpr_read_b32 v28, a215\n" \
"v_mfma_f32_16x16x32_bf16 a[172:175], a[124:127], a[44:47], a[172:175]\n" \
"ds_read_b128 a[36:39], v8 offset:8192\n" \
"v_lshlrev_b32 v27, 16, v28\n" \
"v_mfma_f32_16x16x32_bf16 a[180:183], a[120:123], a[48:51], a[180:183]\n" \
"ds_read_b128 a[40:43], v7 offset:10240\n" \
"v_and_b32 v28, 0xffff0000, v28\n" \
"v_mfma_f32_16x16x32_bf16 a[180:183], a[124:127], a[52:55], a[180:183]\n" \
"ds_read_b128 a[44:47], v8 offset:10240\n" \
"v_mul_f32 v29, v25, v27\n" \
"v_mfma_f32_16x16x32_bf16 a[188:191], a[120:123], a[56:59], a[188:191]\n" \
"ds_read_b128 a[48:51], v7 offset:12288\n" \
"v_mul_f32 v58, v26, v28\n" \
"v_mfma_f32_16x16x32_bf16 a[188:191], a[124:127], a[60:63], a[188:191]\n" \
"s_waitcnt lgkmcnt(9)\n" \
"ds_read_b128 a[52:55], v8 offset:12288\n" \
"v_accvgpr_read_b32 v40, a201\n" \
"v_mfma_f32_16x16x32_bf16 v[64:67], a[64:67], a[0:3], v[64:67]\n" \
"s_mov_b32 s47, 2621440\n" \
"s_add_u32 m0, s22, 32768\n" \
"ds_read_b128 a[56:59], v7 offset:14336\n" \
"v_mfma_f32_16x16x32_bf16 v[64:67], a[68:71], a[4:7], v[64:67]\n" \
"buffer_load_dwordx4 v21, s[32:35], s47 offen offset:0 lds\n" \
"v_mfma_f32_16x16x32_bf16 v[72:75], a[64:67], a[8:11], v[72:75]\n" \
"s_waitcnt lgkmcnt(7)\n" \
"ds_read_b128 a[60:63], v8 offset:14336\n" \
"v_lshlrev_b32 v34, 16, v40\n" \
"v_mfma_f32_16x16x32_bf16 v[72:75], a[68:71], a[12:15], v[72:75]\n" \
"v_and_b32 v40, 0xffff0000, v40\n" \
"v_mul_f32 v25, v31, v45\n" \
"v_mfma_f32_16x16x32_bf16 v[80:83], a[64:67], a[16:19], v[80:83]\n" \
"s_mov_b32 s46, 640\n" \
"s_add_u32 m0, s50, 49152\n" \
"ds_read_b64_tr_b16 a[96:97], v9 offset:49152\n" \
"v_mfma_f32_16x16x32_bf16 v[80:83], a[68:71], a[20:23], v[80:83]\n" \
"buffer_load_dwordx4 v17, s[28:31], s46 offen lds\n" \
"v_mfma_f32_16x16x32_bf16 v[88:91], a[64:67], a[24:27], v[88:91]\n" \
"s_waitcnt lgkmcnt(5)\n" \
"ds_read_b64_tr_b16 a[98:99], v10 offset:49152\n" \
"v_mul_f32 v26, v37, v49\n" \
"v_mfma_f32_16x16x32_bf16 v[88:91], a[68:71], a[28:31], v[88:91]\n" \
"v_fmac_f32 v25, v32, v44\n" \
"v_fmac_f32 v26, v38, v48\n" \
"v_mfma_f32_16x16x32_bf16 v[96:99], a[64:67], a[32:35], v[96:99]\n" \
"s_add_u32 m0, s22, 34560\n" \
"ds_read_b64_tr_b16 a[100:101], v9 offset:50176\n" \
"v_fmac_f32 v25, v33, v43\n" \
"v_mfma_f32_16x16x32_bf16 v[96:99], a[68:71], a[36:39], v[96:99]\n" \
"buffer_load_dwordx4 v21, s[32:35], s47 offen offset:256 lds\n" \
"v_mfma_f32_16x16x32_bf16 v[104:107], a[64:67], a[40:43], v[104:107]\n" \
"s_waitcnt lgkmcnt(3)\n" \
"ds_read_b64_tr_b16 a[102:103], v10 offset:50176\n" \
"v_fmac_f32 v26, v39, v47\n" \
"v_mfma_f32_16x16x32_bf16 v[104:107], a[68:71], a[44:47], v[104:107]\n" \
"v_fmac_f32 v25, v34, v42\n" \
"v_fmac_f32 v26, v40, v46\n" \
"v_mfma_f32_16x16x32_bf16 v[112:115], a[64:67], a[48:51], v[112:115]\n" \
"s_add_u32 m0, s50, 53248\n" \
"ds_read_b64_tr_b16 a[104:105], v11 offset:49152\n" \
"v_accvgpr_read_b32 v28, a217\n" \
"v_mfma_f32_16x16x32_bf16 v[112:115], a[68:71], a[52:55], v[112:115]\n" \
"buffer_load_dwordx4 v18, s[28:31], s46 offen lds\n" \
"v_mfma_f32_16x16x32_bf16 v[120:123], a[64:67], a[56:59], v[120:123]\n" \
"ds_read_b64_tr_b16 a[106:107], v12 offset:49152\n" \
"v_lshlrev_b32 v27, 16, v28\n" \
"v_mfma_f32_16x16x32_bf16 v[120:123], a[68:71], a[60:63], v[120:123]\n" \
"v_and_b32 v28, 0xffff0000, v28\n" \
"v_mul_f32 v25, v25, v27\n" \
"v_mfma_f32_16x16x32_bf16 v[68:71], a[72:75], a[0:3], v[68:71]\n" \
"s_mov_b32 s47, 2883584\n" \
"s_add_u32 m0, s22, 33792\n" \
"ds_read_b64_tr_b16 a[108:109], v11 offset:50176\n" \
"v_mfma_f32_16x16x32_bf16 v[68:71], a[76:79], a[4:7], v[68:71]\n" \
"buffer_load_dwordx4 v21, s[32:35], s47 offen offset:0 lds\n" \
"v_mfma_f32_16x16x32_bf16 v[76:79], a[72:75], a[8:11], v[76:79]\n" \
"ds_read_b64_tr_b16 a[110:111], v12 offset:50176\n" \
"v_mul_f32 v26, v26, v28\n" \
"v_mfma_f32_16x16x32_bf16 v[76:79], a[76:79], a[12:15], v[76:79]\n" \
"v_cvt_pk_bf16_f32 v53, v25, v29\n" \
"v_cvt_pk_bf16_f32 v57, v26, v58\n" \
"v_mfma_f32_16x16x32_bf16 v[84:87], a[72:75], a[16:19], v[84:87]\n" \
"s_add_u32 m0, s50, 57344\n" \
"ds_read_b64_tr_b16 a[112:113], v9 offset:49664\n" \
"v_accvgpr_read_b32 v41, a203\n" \
"v_mfma_f32_16x16x32_bf16 v[84:87], a[76:79], a[20:23], v[84:87]\n" \
"buffer_load_dwordx4 v19, s[28:31], s46 offen lds\n" \
"v_mfma_f32_16x16x32_bf16 v[92:95], a[72:75], a[24:27], v[92:95]\n" \
"ds_read_b64_tr_b16 a[114:115], v10 offset:49664\n" \
"v_lshlrev_b32 v35, 16, v41\n" \
"v_mfma_f32_16x16x32_bf16 v[92:95], a[76:79], a[28:31], v[92:95]\n" \
"v_and_b32 v41, 0xffff0000, v41\n" \
"v_mul_f32 v25, v32, v45\n" \
"v_mfma_f32_16x16x32_bf16 v[100:103], a[72:75], a[32:35], v[100:103]\n" \
"s_add_u32 m0, s22, 35584\n" \
"ds_read_b64_tr_b16 a[116:117], v9 offset:50688\n" \
"v_mul_f32 v26, v38, v49\n" \
"v_mfma_f32_16x16x32_bf16 v[100:103], a[76:79], a[36:39], v[100:103]\n" \
"buffer_load_dwordx4 v21, s[32:35], s47 offen offset:256 lds\n" \
"v_mfma_f32_16x16x32_bf16 v[108:111], a[72:75], a[40:43], v[108:111]\n" \
"s_add_u32 s84, s84, 1048576\ns_addc_u32 s85, s85, 0\n" \
"ds_read_b64_tr_b16 a[118:119], v10 offset:50688\n" \
"v_mfma_f32_16x16x32_bf16 v[108:111], a[76:79], a[44:47], v[108:111]\n" \
"v_fmac_f32 v25, v33, v44\n" \
"v_fmac_f32 v26, v39, v48\n" \
"v_mfma_f32_16x16x32_bf16 v[116:119], a[72:75], a[48:51], v[116:119]\n" \
"s_add_u32 m0, s50, 61440\n" \
"ds_read_b64_tr_b16 a[120:121], v11 offset:49664\n" \
"v_fmac_f32 v25, v34, v43\n" \
"v_mfma_f32_16x16x32_bf16 v[116:119], a[76:79], a[52:55], v[116:119]\n" \
"buffer_load_dwordx4 v20, s[28:31], s46 offen lds\n" \
"v_mfma_f32_16x16x32_bf16 v[124:127], a[72:75], a[56:59], v[124:127]\n" \
"ds_read_b64_tr_b16 a[122:123], v12 offset:49664\n" \
"v_fmac_f32 v26, v40, v47\n" \
"v_mfma_f32_16x16x32_bf16 v[124:127], a[76:79], a[60:63], v[124:127]\n" \
"v_fmac_f32 v25, v35, v42\n" \
"v_fmac_f32 v26, v41, v46\n" \
"v_mfma_f32_16x16x32_bf16 v[128:131], a[80:83], a[0:3], v[128:131]\n" \
"ds_read_b64_tr_b16 a[124:125], v11 offset:50688\n" \
"v_accvgpr_read_b32 v28, a219\n" \
"v_mfma_f32_16x16x32_bf16 v[128:131], a[84:87], a[4:7], v[128:131]\n" \
"s_mov_b64 exec, 0xffff\nglobal_load_dword v22, v1, s[84:85]\ns_mov_b64 exec, -1\n" \
"v_mfma_f32_16x16x32_bf16 v[136:139], a[80:83], a[8:11], v[136:139]\n" \
"ds_read_b64_tr_b16 a[126:127], v12 offset:50688\n" \
"v_lshlrev_b32 v27, 16, v28\n" \
"v_mfma_f32_16x16x32_bf16 v[136:139], a[84:87], a[12:15], v[136:139]\n" \
"v_and_b32 v28, 0xffff0000, v28\n" \
"v_mul_f32 v29, v25, v27\n" \
"v_mfma_f32_16x16x32_bf16 v[144:147], a[80:83], a[16:19], v[144:147]\n" \
"v_mul_f32 v58, v26, v28\n" \
"v_accvgpr_read_b32 v36, a205\n" \
"v_mfma_f32_16x16x32_bf16 v[144:147], a[84:87], a[20:23], v[144:147]\n" \
"s_mov_b64 exec, 0xffff\nglobal_load_dword v22, v1, s[76:77]\ns_mov_b64 exec, -1\n" \
"v_mfma_f32_16x16x32_bf16 v[152:155], a[80:83], a[24:27], v[152:155]\n" \
"v_lshlrev_b32 v30, 16, v36\n" \
"v_and_b32 v36, 0xffff0000, v36\n" \
"v_mfma_f32_16x16x32_bf16 v[152:155], a[84:87], a[28:31], v[152:155]\n" \
"v_mul_f32 v25, v33, v45\n" \
"v_mul_f32 v26, v39, v49\n" \
"v_mfma_f32_16x16x32_bf16 v[160:163], a[80:83], a[32:35], v[160:163]\n" \
"v_fmac_f32 v25, v34, v44\n" \
"v_mfma_f32_16x16x32_bf16 v[160:163], a[84:87], a[36:39], v[160:163]\n" \
"v_fmac_f32 v26, v40, v48\n" \
"v_fmac_f32 v25, v35, v43\n" \
"v_mfma_f32_16x16x32_bf16 v[168:171], a[80:83], a[40:43], v[168:171]\n" \
"v_fmac_f32 v26, v41, v47\n" \
"v_fmac_f32 v25, v30, v42\n" \
"v_mfma_f32_16x16x32_bf16 v[168:171], a[84:87], a[44:47], v[168:171]\n" \
"s_waitcnt vmcnt(22) lgkmcnt(0)\n" \
"v_fmac_f32 v26, v36, v46\n" \
"v_mfma_f32_16x16x32_bf16 v[176:179], a[80:83], a[48:51], v[176:179]\n" \
"s_barrier\n" \
"v_accvgpr_read_b32 v28, a221\n" \
"v_lshlrev_b32 v27, 16, v28\n" \
"v_mfma_f32_16x16x32_bf16 v[176:179], a[84:87], a[52:55], v[176:179]\n" \
"v_and_b32 v28, 0xffff0000, v28\n" \
"v_mul_f32 v25, v25, v27\n" \
"v_mfma_f32_16x16x32_bf16 v[184:187], a[80:83], a[56:59], v[184:187]\n" \
"v_mul_f32 v26, v26, v28\n" \
"v_mfma_f32_16x16x32_bf16 v[184:187], a[84:87], a[60:63], v[184:187]\n" \
"v_cvt_pk_bf16_f32 v52, v25, v29\n" \
"v_cvt_pk_bf16_f32 v56, v26, v58\n" \
"v_mfma_f32_16x16x32_bf16 v[132:135], a[88:91], a[0:3], v[132:135]\n" \
"v_accvgpr_read_b32 v37, a207\n" \
"v_lshlrev_b32 v31, 16, v37\n" \
"v_mfma_f32_16x16x32_bf16 v[132:135], a[92:95], a[4:7], v[132:135]\n" \
"v_and_b32 v37, 0xffff0000, v37\n" \
"v_mfma_f32_16x16x32_bf16 v[140:143], a[88:91], a[8:11], v[140:143]\n" \
"v_mul_f32 v25, v34, v45\n" \
"v_mul_f32 v26, v40, v49\n" \
"v_mfma_f32_16x16x32_bf16 v[140:143], a[92:95], a[12:15], v[140:143]\n" \
"v_fmac_f32 v25, v35, v44\n" \
"v_fmac_f32 v26, v41, v48\n" \
"v_mfma_f32_16x16x32_bf16 v[148:151], a[88:91], a[16:19], v[148:151]\n" \
"v_fmac_f32 v25, v30, v43\n" \
"v_mfma_f32_16x16x32_bf16 v[148:151], a[92:95], a[20:23], v[148:151]\n" \
"v_fmac_f32 v26, v36, v47\n" \
"v_fmac_f32 v25, v31, v42\n" \
"v_mfma_f32_16x16x32_bf16 v[156:159], a[88:91], a[24:27], v[156:159]\n" \
"v_fmac_f32 v26, v37, v46\n" \
"v_accvgpr_read_b32 v28, a223\n" \
"v_mfma_f32_16x16x32_bf16 v[156:159], a[92:95], a[28:31], v[156:159]\n" \
"v_lshlrev_b32 v27, 16, v28\n" \
"v_mfma_f32_16x16x32_bf16 v[164:167], a[88:91], a[32:35], v[164:167]\n" \
"v_and_b32 v28, 0xffff0000, v28\n" \
"v_mul_f32 v29, v25, v27\n" \
"v_mfma_f32_16x16x32_bf16 v[164:167], a[92:95], a[36:39], v[164:167]\n" \
"v_mul_f32 v58, v26, v28\n" \
"v_accvgpr_read_b32 v38, a209\n" \
"v_mfma_f32_16x16x32_bf16 v[172:175], a[88:91], a[40:43], v[172:175]\n" \
"v_lshlrev_b32 v32, 16, v38\n" \
"v_mfma_f32_16x16x32_bf16 v[172:175], a[92:95], a[44:47], v[172:175]\n" \
"v_and_b32 v38, 0xffff0000, v38\n" \
"v_mul_f32 v25, v35, v45\n" \
"v_mfma_f32_16x16x32_bf16 v[180:183], a[88:91], a[48:51], v[180:183]\n" \
"v_mul_f32 v26, v41, v49\n" \
"v_fmac_f32 v25, v30, v44\n" \
"v_mfma_f32_16x16x32_bf16 v[180:183], a[92:95], a[52:55], v[180:183]\n" \
"v_fmac_f32 v26, v36, v48\n" \
"v_mfma_f32_16x16x32_bf16 v[188:191], a[88:91], a[56:59], v[188:191]\n" \
"v_fmac_f32 v25, v31, v43\n" \
"v_fmac_f32 v26, v37, v47\n" \
"v_mfma_f32_16x16x32_bf16 v[188:191], a[92:95], a[60:63], v[188:191]\n" \
"v_fmac_f32 v25, v32, v42\n" \
"v_fmac_f32 v26, v38, v46\n" \
"v_mfma_f32_16x16x32_bf16 v[192:195], a[96:99], a[0:3], v[192:195]\n" \
"v_accvgpr_read_b32 v28, a225\n" \
"v_mfma_f32_16x16x32_bf16 v[192:195], a[100:103], a[4:7], v[192:195]\n" \
"v_lshlrev_b32 v27, 16, v28\n" \
"v_and_b32 v28, 0xffff0000, v28\n" \
"v_mfma_f32_16x16x32_bf16 v[200:203], a[96:99], a[8:11], v[200:203]\n" \
"v_mul_f32 v25, v25, v27\n" \
"v_mul_f32 v26, v26, v28\n" \
"v_mfma_f32_16x16x32_bf16 v[200:203], a[100:103], a[12:15], v[200:203]\n" \
"v_cvt_pk_bf16_f32 v51, v25, v29\n" \
"v_mfma_f32_16x16x32_bf16 v[208:211], a[96:99], a[16:19], v[208:211]\n" \
"ds_read_b64_tr_b16 a[64:65], v9 offset:0\n" \
"v_cvt_pk_bf16_f32 v55, v26, v58\n" \
"v_mfma_f32_16x16x32_bf16 v[208:211], a[100:103], a[20:23], v[208:211]\n" \
"v_accvgpr_read_b32 v39, a211\n" \
"v_lshlrev_b32 v33, 16, v39\n" \
"v_mfma_f32_16x16x32_bf16 v[216:219], a[96:99], a[24:27], v[216:219]\n" \
"ds_read_b64_tr_b16 a[66:67], v10 offset:0\n" \
"v_and_b32 v39, 0xffff0000, v39\n" \
"v_mfma_f32_16x16x32_bf16 v[216:219], a[100:103], a[28:31], v[216:219]\n" \
"v_mul_f32 v25, v30, v45\n" \
"v_mul_f32 v26, v36, v49\n" \
"v_mfma_f32_16x16x32_bf16 v[224:227], a[96:99], a[32:35], v[224:227]\n" \
"LH1_%=:\ns_cbranch_execz LH9_%=\n" \
"ds_read_b64_tr_b16 a[68:69], v9 offset:1024\n" \
"v_fmac_f32 v25, v31, v44\n" \
"v_mfma_f32_16x16x32_bf16 v[224:227], a[100:103], a[36:39], v[224:227]\n" \
"v_fmac_f32 v26, v37, v48\n" \
"v_fmac_f32 v25, v32, v43\n" \
"v_mfma_f32_16x16x32_bf16 v[232:235], a[96:99], a[40:43], v[232:235]\n" \
"ds_read_b64_tr_b16 a[70:71], v10 offset:1024\n" \
"v_fmac_f32 v26, v38, v47\n" \
"v_mfma_f32_16x16x32_bf16 v[232:235], a[100:103], a[44:47], v[232:235]\n" \
"v_fmac_f32 v25, v33, v42\n" \
"v_fmac_f32 v26, v39, v46\n" \
"v_mfma_f32_16x16x32_bf16 v[240:243], a[96:99], a[48:51], v[240:243]\n" \
"ds_read_b64_tr_b16 a[72:73], v11 offset:0\n" \
"v_accvgpr_read_b32 v28, a227\n" \
"v_mfma_f32_16x16x32_bf16 v[240:243], a[100:103], a[52:55], v[240:243]\n" \
"v_lshlrev_b32 v27, 16, v28\n" \
"v_and_b32 v28, 0xffff0000, v28\n" \
"v_mfma_f32_16x16x32_bf16 v[248:251], a[96:99], a[56:59], v[248:251]\n" \
"ds_read_b64_tr_b16 a[74:75], v12 offset:0\n" \
"v_mul_f32 v29, v25, v27\n" \
"v_mfma_f32_16x16x32_bf16 v[248:251], a[100:103], a[60:63], v[248:251]\n" \
"v_mul_f32 v58, v26, v28\n" \
"v_accvgpr_read_b32 v40, a213\n" \
"v_mfma_f32_16x16x32_bf16 v[196:199], a[104:107], a[0:3], v[196:199]\n" \
"ds_read_b64_tr_b16 a[76:77], v11 offset:1024\n" \
"v_lshlrev_b32 v34, 16, v40\n" \
"v_mfma_f32_16x16x32_bf16 v[196:199], a[108:111], a[4:7], v[196:199]\n" \
"v_and_b32 v40, 0xffff0000, v40\n" \
"v_mul_f32 v25, v31, v45\n" \
"v_mfma_f32_16x16x32_bf16 v[204:207], a[104:107], a[8:11], v[204:207]\n" \
"ds_read_b64_tr_b16 a[78:79], v12 offset:1024\n" \
"v_mul_f32 v26, v37, v49\n" \
"v_mfma_f32_16x16x32_bf16 v[204:207], a[108:111], a[12:15], v[204:207]\n" \
"v_fmac_f32 v25, v32, v44\n" \
"v_fmac_f32 v26, v38, v48\n" \
"v_mfma_f32_16x16x32_bf16 v[212:215], a[104:107], a[16:19], v[212:215]\n" \
"ds_read_b64_tr_b16 a[80:81], v9 offset:512\n" \
"v_fmac_f32 v25, v33, v43\n" \
"v_mfma_f32_16x16x32_bf16 v[212:215], a[108:111], a[20:23], v[212:215]\n" \
"v_fmac_f32 v26, v39, v47\n" \
"v_fmac_f32 v25, v34, v42\n" \
"v_mfma_f32_16x16x32_bf16 v[220:223], a[104:107], a[24:27], v[220:223]\n" \
"ds_read_b64_tr_b16 a[82:83], v10 offset:512\n" \
"v_fmac_f32 v26, v40, v46\n" \
"v_mfma_f32_16x16x32_bf16 v[220:223], a[108:111], a[28:31], v[220:223]\n" \
"v_accvgpr_read_b32 v28, a229\n" \
"v_lshlrev_b32 v27, 16, v28\n" \
"v_mfma_f32_16x16x32_bf16 v[228:231], a[104:107], a[32:35], v[228:231]\n" \
"ds_read_b64_tr_b16 a[84:85], v9 offset:1536\n" \
"v_and_b32 v28, 0xffff0000, v28\n" \
"v_mfma_f32_16x16x32_bf16 v[228:231], a[108:111], a[36:39], v[228:231]\n" \
"v_mul_f32 v25, v25, v27\n" \
"v_mul_f32 v26, v26, v28\n" \
"v_mfma_f32_16x16x32_bf16 v[236:239], a[104:107], a[40:43], v[236:239]\n" \
"ds_read_b64_tr_b16 a[86:87], v10 offset:1536\n" \
"v_cvt_pk_bf16_f32 v50, v25, v29\n" \
"v_mfma_f32_16x16x32_bf16 v[236:239], a[108:111], a[44:47], v[236:239]\n" \
"s_mov_b32 s94, 79699968\n" \
"v_cvt_pk_bf16_f32 v54, v26, v58\n" \
"v_mfma_f32_16x16x32_bf16 v[244:247], a[104:107], a[48:51], v[244:247]\n" \
"ds_read_b64_tr_b16 a[88:89], v11 offset:512\n" \
"v_mfma_f32_16x16x32_bf16 v[244:247], a[108:111], a[52:55], v[244:247]\n" \
"buffer_store_dwordx4 v[50:53], v60, s[36:39], s94 offen offset:0 sc1\n" \
"v_mfma_f32_16x16x32_bf16 v[252:255], a[104:107], a[56:59], v[252:255]\n" \
"s_mov_b32 s95, 79704064\n" \
"ds_read_b64_tr_b16 a[90:91], v12 offset:512\n" \
"v_mfma_f32_16x16x32_bf16 v[252:255], a[108:111], a[60:63], v[252:255]\n" \
"v_mfma_f32_16x16x32_bf16 a[128:131], a[112:115], a[0:3], a[128:131]\n" \
"ds_read_b64_tr_b16 a[92:93], v11 offset:1536\n" \
"v_mfma_f32_16x16x32_bf16 a[128:131], a[116:119], a[4:7], a[128:131]\n" \
"v_mfma_f32_16x16x32_bf16 a[136:139], a[112:115], a[8:11], a[136:139]\n" \
"ds_read_b64_tr_b16 a[94:95], v12 offset:1536\n" \
"v_mfma_f32_16x16x32_bf16 a[136:139], a[116:119], a[12:15], a[136:139]\n" \
"v_mfma_f32_16x16x32_bf16 a[144:147], a[112:115], a[16:19], a[144:147]\n" \
"v_mfma_f32_16x16x32_bf16 a[144:147], a[116:119], a[20:23], a[144:147]\n" \
"v_mfma_f32_16x16x32_bf16 a[152:155], a[112:115], a[24:27], a[152:155]\n" \
"v_mfma_f32_16x16x32_bf16 a[152:155], a[116:119], a[28:31], a[152:155]\n" \
"v_mfma_f32_16x16x32_bf16 a[160:163], a[112:115], a[32:35], a[160:163]\n" \
"v_mfma_f32_16x16x32_bf16 a[160:163], a[116:119], a[36:39], a[160:163]\n" \
"v_mfma_f32_16x16x32_bf16 a[168:171], a[112:115], a[40:43], a[168:171]\n" \
"v_mfma_f32_16x16x32_bf16 a[168:171], a[116:119], a[44:47], a[168:171]\n" \
"s_waitcnt vmcnt(13) lgkmcnt(0)\n" \
"v_mfma_f32_16x16x32_bf16 a[176:179], a[112:115], a[48:51], a[176:179]\n" \
"s_barrier\n" \
"v_mfma_f32_16x16x32_bf16 a[176:179], a[116:119], a[52:55], a[176:179]\n" \
"buffer_store_dwordx4 v[54:57], v60, s[36:39], s95 offen offset:0 sc1\n" \
"v_mfma_f32_16x16x32_bf16 a[184:187], a[112:115], a[56:59], a[184:187]\n" \
"s_lshl_b32 s62, s8, 2\n" \
"s_add_u32 s62, s62, %2\n" \
"s_lshl_b32 s63, s62, 12\n" \
"v_mfma_f32_16x16x32_bf16 a[184:187], a[116:119], a[60:63], a[184:187]\n" \
"s_add_u32 s68, s10, s63\ns_addc_u32 s69, s11, 0\n" \
"s_and_b32 s69, s69, 0xffff\n" \
"v_mfma_f32_16x16x32_bf16 a[132:135], a[120:123], a[0:3], a[132:135]\n" \
"s_mov_b32 s70, 4096\n" \
"s_mov_b32 s71, 0x00020000\n" \
"s_add_u32 s63, s63, 79691776\n" \
"v_mfma_f32_16x16x32_bf16 a[132:135], a[124:127], a[4:7], a[132:135]\n" \
"s_add_u32 s72, s4, s63\ns_addc_u32 s73, s5, 0\n" \
"s_and_b32 s73, s73, 0xffff\n" \
"v_mfma_f32_16x16x32_bf16 a[140:143], a[120:123], a[8:11], a[140:143]\n" \
"s_mov_b32 s74, 4096\n" \
"ds_read_b128 a[0:3], v5 offset:0\n" \
"s_mov_b32 s75, 0x00020000\n" \
"v_mfma_f32_16x16x32_bf16 a[140:143], a[124:127], a[12:15], a[140:143]\n" \
"ds_read_b128 a[4:7], v6 offset:0\n" \
"v_add_u32 v23, -32, v4\n" \
"v_mfma_f32_16x16x32_bf16 a[148:151], a[120:123], a[16:19], a[148:151]\n" \
"ds_read_b128 a[8:11], v5 offset:2048\n" \
"v_add_u32 v24, 992, v4\n" \
"v_mfma_f32_16x16x32_bf16 a[148:151], a[124:127], a[20:23], a[148:151]\n" \
"ds_read_b128 a[12:15], v6 offset:2048\n" \
"v_add_u32 v25, -16, v4\n" \
"v_mfma_f32_16x16x32_bf16 a[156:159], a[120:123], a[24:27], a[156:159]\n" \
"ds_read_b128 a[16:19], v5 offset:4096\n" \
"v_add_u32 v26, -512, v4\n" \
"v_mfma_f32_16x16x32_bf16 a[156:159], a[124:127], a[28:31], a[156:159]\n" \
"s_lshr_b32 s63, %2, 1\n" \
"ds_read_b128 a[20:23], v6 offset:4096\n" \
"v_add_u32 v27, 512, v4\n" \
"v_mfma_f32_16x16x32_bf16 a[164:167], a[120:123], a[32:35], a[164:167]\n" \
"s_lshl_b32 s63, s63, 16\n" \
"ds_read_b128 a[24:27], v5 offset:6144\n" \
"s_lshl_b32 s56, %2, 14\n" \
"v_mfma_f32_16x16x32_bf16 a[164:167], a[124:127], a[36:39], a[164:167]\n" \
"s_add_u32 s56, s56, s63\n" \
"ds_read_b128 a[28:31], v6 offset:6144\n" \
"v_add_u32 v55, s56, v4\n" \
"v_mfma_f32_16x16x32_bf16 a[172:175], a[120:123], a[40:43], a[172:175]\n" \
"s_lshl_b32 s62, s62, 1\n" \
"ds_read_b128 a[32:35], v5 offset:8192\n" \
"v_mov_b32 v28, 0\n" \
"v_mfma_f32_16x16x32_bf16 a[172:175], a[124:127], a[44:47], a[172:175]\n" \
"ds_read_b128 a[36:39], v6 offset:8192\n" \
"v_mov_b32 v29, s62\n" \
"v_mfma_f32_16x16x32_bf16 a[180:183], a[120:123], a[48:51], a[180:183]\n" \
"s_and_b32 s62, %1, 7\n" \
"ds_read_b128 a[40:43], v5 offset:10240\n" \
"v_lshlrev_b32 v56, 23, v4\n" \
"v_mfma_f32_16x16x32_bf16 a[180:183], a[124:127], a[52:55], a[180:183]\n" \
"s_lshl_b32 s62, s62, 8\n" \
"ds_read_b128 a[44:47], v6 offset:10240\n" \
"s_add_u32 s62, s62, 94380032\n" \
"v_mfma_f32_16x16x32_bf16 a[188:191], a[120:123], a[56:59], a[188:191]\n" \
"ds_read_b128 a[48:51], v5 offset:12288\n" \
"v_bfe_u32 v57, v4, 4, 5\n" \
"v_mfma_f32_16x16x32_bf16 a[188:191], a[124:127], a[60:63], a[188:191]\n" \
"s_waitcnt lgkmcnt(9)\n" \
"ds_read_b128 a[52:55], v6 offset:12288\n" \
"v_lshl_add_u32 v57, v57, 3, s62\n" \
"v_mfma_f32_16x16x32_bf16 v[64:67], a[64:67], a[0:3], v[64:67]\n" \
"s_mov_b32 s47, 2621440\n" \
"s_add_u32 m0, s22, 49024\n" \
"ds_read_b128 a[56:59], v5 offset:14336\n" \
"v_mfma_f32_16x16x32_bf16 v[64:67], a[68:71], a[4:7], v[64:67]\n" \
"buffer_load_dwordx4 v21, s[32:35], s47 offen offset:128 lds\n" \
"v_mfma_f32_16x16x32_bf16 v[72:75], a[64:67], a[8:11], v[72:75]\n" \
"s_waitcnt lgkmcnt(7)\n" \
"ds_read_b128 a[60:63], v6 offset:14336\n" \
"v_mfma_f32_16x16x32_bf16 v[72:75], a[68:71], a[12:15], v[72:75]\n" \
"v_mfma_f32_16x16x32_bf16 v[80:83], a[64:67], a[16:19], v[80:83]\n" \
"s_add_u32 m0, s22, 50816\n" \
"ds_read_b64_tr_b16 a[96:97], v9 offset:16384\n" \
"v_mfma_f32_16x16x32_bf16 v[80:83], a[68:71], a[20:23], v[80:83]\n" \
"buffer_load_dwordx4 v21, s[32:35], s47 offen offset:384 lds\n" \
"v_mfma_f32_16x16x32_bf16 v[88:91], a[64:67], a[24:27], v[88:91]\n" \
"s_waitcnt lgkmcnt(5)\n" \
"ds_read_b64_tr_b16 a[98:99], v10 offset:16384\n" \
"v_mfma_f32_16x16x32_bf16 v[88:91], a[68:71], a[28:31], v[88:91]\n" \
"v_mfma_f32_16x16x32_bf16 v[96:99], a[64:67], a[32:35], v[96:99]\n" \
"s_mov_b32 s47, 2883584\n" \
"s_add_u32 m0, s22, 50048\n" \
"ds_read_b64_tr_b16 a[100:101], v9 offset:17408\n" \
"v_mfma_f32_16x16x32_bf16 v[96:99], a[68:71], a[36:39], v[96:99]\n" \
"buffer_load_dwordx4 v21, s[32:35], s47 offen offset:128 lds\n" \
"v_mfma_f32_16x16x32_bf16 v[104:107], a[64:67], a[40:43], v[104:107]\n" \
"s_waitcnt lgkmcnt(3)\n" \
"ds_read_b64_tr_b16 a[102:103], v10 offset:17408\n" \
"v_mfma_f32_16x16x32_bf16 v[104:107], a[68:71], a[44:47], v[104:107]\n" \
"v_mfma_f32_16x16x32_bf16 v[112:115], a[64:67], a[48:51], v[112:115]\n" \
"s_add_u32 m0, s22, 51840\n" \
"ds_read_b64_tr_b16 a[104:105], v11 offset:16384\n" \
"v_mfma_f32_16x16x32_bf16 v[112:115], a[68:71], a[52:55], v[112:115]\n" \
"buffer_load_dwordx4 v21, s[32:35], s47 offen offset:384 lds\n" \
"v_mfma_f32_16x16x32_bf16 v[120:123], a[64:67], a[56:59], v[120:123]\n" \
"ds_read_b64_tr_b16 a[106:107], v12 offset:16384\n" \
"v_mfma_f32_16x16x32_bf16 v[120:123], a[68:71], a[60:63], v[120:123]\n" \
"v_mfma_f32_16x16x32_bf16 v[68:71], a[72:75], a[0:3], v[68:71]\n" \
"s_mov_b32 s46, 768\n" \
"s_add_u32 m0, s50, 65536\n" \
"ds_read_b64_tr_b16 a[108:109], v11 offset:17408\n" \
"v_mfma_f32_16x16x32_bf16 v[68:71], a[76:79], a[4:7], v[68:71]\n" \
"buffer_load_dwordx4 v13, s[28:31], s46 offen lds\n" \
"v_mfma_f32_16x16x32_bf16 v[76:79], a[72:75], a[8:11], v[76:79]\n" \
"ds_read_b64_tr_b16 a[110:111], v12 offset:17408\n" \
"v_mfma_f32_16x16x32_bf16 v[76:79], a[76:79], a[12:15], v[76:79]\n" \
"v_mfma_f32_16x16x32_bf16 v[84:87], a[72:75], a[16:19], v[84:87]\n" \
"s_add_u32 m0, s50, 69632\n" \
"ds_read_b64_tr_b16 a[112:113], v9 offset:16896\n" \
"v_mfma_f32_16x16x32_bf16 v[84:87], a[76:79], a[20:23], v[84:87]\n" \
"buffer_load_dwordx4 v14, s[28:31], s46 offen lds\n" \
"v_mfma_f32_16x16x32_bf16 v[92:95], a[72:75], a[24:27], v[92:95]\n" \
"ds_read_b64_tr_b16 a[114:115], v10 offset:16896\n" \
"v_mfma_f32_16x16x32_bf16 v[92:95], a[76:79], a[28:31], v[92:95]\n" \
"v_mfma_f32_16x16x32_bf16 v[100:103], a[72:75], a[32:35], v[100:103]\n" \
"s_add_u32 m0, s50, 73728\n" \
"ds_read_b64_tr_b16 a[116:117], v9 offset:17920\n" \
"v_mfma_f32_16x16x32_bf16 v[100:103], a[76:79], a[36:39], v[100:103]\n" \
"buffer_load_dwordx4 v15, s[28:31], s46 offen lds\n" \
"v_mfma_f32_16x16x32_bf16 v[108:111], a[72:75], a[40:43], v[108:111]\n" \
"ds_read_b64_tr_b16 a[118:119], v10 offset:17920\n" \
"v_mfma_f32_16x16x32_bf16 v[108:111], a[76:79], a[44:47], v[108:111]\n" \
"v_mfma_f32_16x16x32_bf16 v[116:119], a[72:75], a[48:51], v[116:119]\n" \
"s_add_u32 m0, s50, 77824\n" \
"ds_read_b64_tr_b16 a[120:121], v11 offset:16896\n" \
"v_mfma_f32_16x16x32_bf16 v[116:119], a[76:79], a[52:55], v[116:119]\n" \
"buffer_load_dwordx4 v16, s[28:31], s46 offen lds\n" \
"v_mfma_f32_16x16x32_bf16 v[124:127], a[72:75], a[56:59], v[124:127]\n" \
"ds_read_b64_tr_b16 a[122:123], v12 offset:16896\n" \
"v_mfma_f32_16x16x32_bf16 v[124:127], a[76:79], a[60:63], v[124:127]\n" \
"v_mfma_f32_16x16x32_bf16 v[128:131], a[80:83], a[0:3], v[128:131]\n" \
"s_mov_b32 s46, 768\n" \
"s_add_u32 m0, s50, 81920\n" \
"ds_read_b64_tr_b16 a[124:125], v11 offset:17920\n" \
"v_mfma_f32_16x16x32_bf16 v[128:131], a[84:87], a[4:7], v[128:131]\n" \
"buffer_load_dwordx4 v17, s[28:31], s46 offen lds\n" \
"v_mfma_f32_16x16x32_bf16 v[136:139], a[80:83], a[8:11], v[136:139]\n" \
"ds_read_b64_tr_b16 a[126:127], v12 offset:17920\n" \
"v_mfma_f32_16x16x32_bf16 v[136:139], a[84:87], a[12:15], v[136:139]\n" \
"v_mfma_f32_16x16x32_bf16 v[144:147], a[80:83], a[16:19], v[144:147]\n" \
"s_add_u32 m0, s50, 86016\n" \
"v_mfma_f32_16x16x32_bf16 v[144:147], a[84:87], a[20:23], v[144:147]\n" \
"buffer_load_dwordx4 v18, s[28:31], s46 offen lds\n" \
"v_mfma_f32_16x16x32_bf16 v[152:155], a[80:83], a[24:27], v[152:155]\n" \
"v_mfma_f32_16x16x32_bf16 v[152:155], a[84:87], a[28:31], v[152:155]\n" \
"v_mfma_f32_16x16x32_bf16 v[160:163], a[80:83], a[32:35], v[160:163]\n" \
"s_add_u32 m0, s50, 90112\n" \
"v_mfma_f32_16x16x32_bf16 v[160:163], a[84:87], a[36:39], v[160:163]\n" \
"buffer_load_dwordx4 v19, s[28:31], s46 offen lds\n" \
"v_mfma_f32_16x16x32_bf16 v[168:171], a[80:83], a[40:43], v[168:171]\n" \
"v_mfma_f32_16x16x32_bf16 v[168:171], a[84:87], a[44:47], v[168:171]\n" \
"s_waitcnt vmcnt(16) lgkmcnt(0)\n" \
"v_mfma_f32_16x16x32_bf16 v[176:179], a[80:83], a[48:51], v[176:179]\n" \
"s_add_u32 m0, s50, 94208\n" \
"s_barrier\n" \
"v_mfma_f32_16x16x32_bf16 v[176:179], a[84:87], a[52:55], v[176:179]\n" \
"buffer_load_dwordx4 v20, s[28:31], s46 offen lds\n" \
"v_mfma_f32_16x16x32_bf16 v[184:187], a[80:83], a[56:59], v[184:187]\n" \
"v_mfma_f32_16x16x32_bf16 v[184:187], a[84:87], a[60:63], v[184:187]\n" \
"v_mfma_f32_16x16x32_bf16 v[132:135], a[88:91], a[0:3], v[132:135]\n" \
"s_mov_b32 s47, 3145728\n" \
"s_add_u32 m0, s22, 0\n" \
"v_mfma_f32_16x16x32_bf16 v[132:135], a[92:95], a[4:7], v[132:135]\n" \
"buffer_load_dwordx4 v21, s[32:35], s47 offen offset:0 lds\n" \
"v_mfma_f32_16x16x32_bf16 v[140:143], a[88:91], a[8:11], v[140:143]\n" \
"v_mfma_f32_16x16x32_bf16 v[140:143], a[92:95], a[12:15], v[140:143]\n" \
"v_mfma_f32_16x16x32_bf16 v[148:151], a[88:91], a[16:19], v[148:151]\n" \
"s_add_u32 m0, s22, 1792\n" \
"v_mfma_f32_16x16x32_bf16 v[148:151], a[92:95], a[20:23], v[148:151]\n" \
"buffer_load_dwordx4 v21, s[32:35], s47 offen offset:256 lds\n" \
"v_mfma_f32_16x16x32_bf16 v[156:159], a[88:91], a[24:27], v[156:159]\n" \
"v_mfma_f32_16x16x32_bf16 v[156:159], a[92:95], a[28:31], v[156:159]\n" \
"v_mfma_f32_16x16x32_bf16 v[164:167], a[88:91], a[32:35], v[164:167]\n" \
"s_mov_b32 s47, 3407872\n" \
"s_add_u32 m0, s22, 1024\n" \
"v_mfma_f32_16x16x32_bf16 v[164:167], a[92:95], a[36:39], v[164:167]\n" \
"buffer_load_dwordx4 v21, s[32:35], s47 offen offset:0 lds\n" \
"v_mfma_f32_16x16x32_bf16 v[172:175], a[88:91], a[40:43], v[172:175]\n" \
"s_add_u32 s90, s64, s53\ns_addc_u32 s91, s65, 0\n" \
"v_mfma_f32_16x16x32_bf16 v[172:175], a[92:95], a[44:47], v[172:175]\n" \
"v_mfma_f32_16x16x32_bf16 v[180:183], a[88:91], a[48:51], v[180:183]\n" \
"s_add_u32 m0, s22, 2816\n" \
"v_mfma_f32_16x16x32_bf16 v[180:183], a[92:95], a[52:55], v[180:183]\n" \
"buffer_load_dwordx4 v21, s[32:35], s47 offen offset:256 lds\n" \
"v_mfma_f32_16x16x32_bf16 v[188:191], a[88:91], a[56:59], v[188:191]\n" \
"v_mfma_f32_16x16x32_bf16 v[188:191], a[92:95], a[60:63], v[188:191]\n" \
"v_mfma_f32_16x16x32_bf16 v[192:195], a[96:99], a[0:3], v[192:195]\n" \
"v_mfma_f32_16x16x32_bf16 v[192:195], a[100:103], a[4:7], v[192:195]\n" \
"global_load_dwordx4 a[240:243], v4, s[90:91] offset:0\n" \
"v_mfma_f32_16x16x32_bf16 v[200:203], a[96:99], a[8:11], v[200:203]\n" \
"v_mfma_f32_16x16x32_bf16 v[200:203], a[100:103], a[12:15], v[200:203]\n" \
"v_mfma_f32_16x16x32_bf16 v[208:211], a[96:99], a[16:19], v[208:211]\n" \
"ds_read_b64_tr_b16 a[64:65], v9 offset:32768\n" \
"v_mfma_f32_16x16x32_bf16 v[208:211], a[100:103], a[20:23], v[208:211]\n" \
"global_load_dwordx4 a[244:247], v4, s[90:91] offset:1024\n" \
"v_mfma_f32_16x16x32_bf16 v[216:219], a[96:99], a[24:27], v[216:219]\n" \
"ds_read_b64_tr_b16 a[66:67], v10 offset:32768\n" \
"v_mfma_f32_16x16x32_bf16 v[216:219], a[100:103], a[28:31], v[216:219]\n" \
"v_mfma_f32_16x16x32_bf16 v[224:227], a[96:99], a[32:35], v[224:227]\n" \
"ds_read_b64_tr_b16 a[68:69], v9 offset:33792\n" \
"v_mfma_f32_16x16x32_bf16 v[224:227], a[100:103], a[36:39], v[224:227]\n" \
"global_load_dwordx4 a[248:251], v4, s[90:91] offset:2048\n" \
"v_mfma_f32_16x16x32_bf16 v[232:235], a[96:99], a[40:43], v[232:235]\n" \
"ds_read_b64_tr_b16 a[70:71], v10 offset:33792\n" \
"v_mfma_f32_16x16x32_bf16 v[232:235], a[100:103], a[44:47], v[232:235]\n" \
"v_mfma_f32_16x16x32_bf16 v[240:243], a[96:99], a[48:51], v[240:243]\n" \
"ds_read_b64_tr_b16 a[72:73], v11 offset:32768\n" \
"v_mfma_f32_16x16x32_bf16 v[240:243], a[100:103], a[52:55], v[240:243]\n" \
"global_load_dwordx4 a[252:255], v4, s[90:91] offset:3072\n" \
"v_mfma_f32_16x16x32_bf16 v[248:251], a[96:99], a[56:59], v[248:251]\n" \
"ds_read_b64_tr_b16 a[74:75], v12 offset:32768\n" \
"v_mfma_f32_16x16x32_bf16 v[248:251], a[100:103], a[60:63], v[248:251]\n" \
"v_mfma_f32_16x16x32_bf16 v[196:199], a[104:107], a[0:3], v[196:199]\n" \
"ds_read_b64_tr_b16 a[76:77], v11 offset:33792\n" \
"v_mfma_f32_16x16x32_bf16 v[196:199], a[108:111], a[4:7], v[196:199]\n" \
"v_mfma_f32_16x16x32_bf16 v[204:207], a[104:107], a[8:11], v[204:207]\n" \
"ds_read_b64_tr_b16 a[78:79], v12 offset:33792\n" \
"v_mfma_f32_16x16x32_bf16 v[204:207], a[108:111], a[12:15], v[204:207]\n" \
"v_mfma_f32_16x16x32_bf16 v[212:215], a[104:107], a[16:19], v[212:215]\n" \
"ds_read_b64_tr_b16 a[80:81], v9 offset:33280\n" \
"v_mfma_f32_16x16x32_bf16 v[212:215], a[108:111], a[20:23], v[212:215]\n" \
"v_mfma_f32_16x16x32_bf16 v[220:223], a[104:107], a[24:27], v[220:223]\n" \
"ds_read_b64_tr_b16 a[82:83], v10 offset:33280\n" \
"v_mfma_f32_16x16x32_bf16 v[220:223], a[108:111], a[28:31], v[220:223]\n" \
"v_mfma_f32_16x16x32_bf16 v[228:231], a[104:107], a[32:35], v[228:231]\n" \
"ds_read_b64_tr_b16 a[84:85], v9 offset:34304\n" \
"v_mfma_f32_16x16x32_bf16 v[228:231], a[108:111], a[36:39], v[228:231]\n" \
"v_mfma_f32_16x16x32_bf16 v[236:239], a[104:107], a[40:43], v[236:239]\n" \
"ds_read_b64_tr_b16 a[86:87], v10 offset:34304\n" \
"v_mfma_f32_16x16x32_bf16 v[236:239], a[108:111], a[44:47], v[236:239]\n" \
"v_mfma_f32_16x16x32_bf16 v[244:247], a[104:107], a[48:51], v[244:247]\n" \
"ds_read_b64_tr_b16 a[88:89], v11 offset:33280\n" \
"v_mfma_f32_16x16x32_bf16 v[244:247], a[108:111], a[52:55], v[244:247]\n" \
"v_mfma_f32_16x16x32_bf16 v[252:255], a[104:107], a[56:59], v[252:255]\n" \
"ds_read_b64_tr_b16 a[90:91], v12 offset:33280\n" \
"v_mfma_f32_16x16x32_bf16 v[252:255], a[108:111], a[60:63], v[252:255]\n" \
"v_mfma_f32_16x16x32_bf16 a[128:131], a[112:115], a[0:3], a[128:131]\n" \
"ds_read_b64_tr_b16 a[92:93], v11 offset:34304\n" \
"v_mfma_f32_16x16x32_bf16 a[128:131], a[116:119], a[4:7], a[128:131]\n" \
"v_mfma_f32_16x16x32_bf16 a[136:139], a[112:115], a[8:11], a[136:139]\n" \
"ds_read_b64_tr_b16 a[94:95], v12 offset:34304\n" \
"v_mfma_f32_16x16x32_bf16 a[136:139], a[116:119], a[12:15], a[136:139]\n" \
"v_mfma_f32_16x16x32_bf16 a[144:147], a[112:115], a[16:19], a[144:147]\n" \
"v_mfma_f32_16x16x32_bf16 a[144:147], a[116:119], a[20:23], a[144:147]\n" \
"v_mfma_f32_16x16x32_bf16 a[152:155], a[112:115], a[24:27], a[152:155]\n" \
"v_mfma_f32_16x16x32_bf16 a[152:155], a[116:119], a[28:31], a[152:155]\n" \
"global_load_dwordx4 a[192:195], v4, s[86:87] offset:0\n" \
"v_mfma_f32_16x16x32_bf16 a[160:163], a[112:115], a[32:35], a[160:163]\n" \
"v_mov_b32 v58, s14\n" \
"v_mfma_f32_16x16x32_bf16 a[160:163], a[116:119], a[36:39], a[160:163]\n" \
"v_mov_b32 v59, s15\n" \
"v_mfma_f32_16x16x32_bf16 a[168:171], a[112:115], a[40:43], a[168:171]\n" \
"v_mfma_f32_16x16x32_bf16 a[168:171], a[116:119], a[44:47], a[168:171]\n" \
"s_waitcnt vmcnt(17) lgkmcnt(0)\n" \
"v_mfma_f32_16x16x32_bf16 a[176:179], a[112:115], a[48:51], a[176:179]\n" \
"s_barrier\n" \
"v_mfma_f32_16x16x32_bf16 a[176:179], a[116:119], a[52:55], a[176:179]\n" \
"v_mfma_f32_16x16x32_bf16 a[184:187], a[112:115], a[56:59], a[184:187]\n" \
"v_mfma_f32_16x16x32_bf16 a[184:187], a[116:119], a[60:63], a[184:187]\n" \
"buffer_store_dwordx2 v[58:59], v56, s[36:39], s23 offen sc1\n" \
"v_mfma_f32_16x16x32_bf16 a[132:135], a[120:123], a[0:3], a[132:135]\n" \
"v_mfma_f32_16x16x32_bf16 a[132:135], a[124:127], a[4:7], a[132:135]\n" \
"v_mfma_f32_16x16x32_bf16 a[140:143], a[120:123], a[8:11], a[140:143]\n" \
"ds_read_b128 a[0:3], v5 offset:32768\n" \
"v_mfma_f32_16x16x32_bf16 a[140:143], a[124:127], a[12:15], a[140:143]\n" \
"ds_read_b128 a[4:7], v6 offset:32768\n" \
"v_mfma_f32_16x16x32_bf16 a[148:151], a[120:123], a[16:19], a[148:151]\n" \
"ds_read_b128 a[8:11], v5 offset:34816\n" \
"v_mfma_f32_16x16x32_bf16 a[148:151], a[124:127], a[20:23], a[148:151]\n" \
"ds_read_b128 a[12:15], v6 offset:34816\n" \
"v_mfma_f32_16x16x32_bf16 a[156:159], a[120:123], a[24:27], a[156:159]\n" \
"ds_read_b128 a[16:19], v5 offset:36864\n" \
"v_mfma_f32_16x16x32_bf16 a[156:159], a[124:127], a[28:31], a[156:159]\n" \
"ds_read_b128 a[20:23], v6 offset:36864\n" \
"v_mfma_f32_16x16x32_bf16 a[164:167], a[120:123], a[32:35], a[164:167]\n" \
"ds_read_b128 a[24:27], v5 offset:38912\n" \
"v_mfma_f32_16x16x32_bf16 a[164:167], a[124:127], a[36:39], a[164:167]\n" \
"ds_read_b128 a[28:31], v6 offset:38912\n" \
"v_mfma_f32_16x16x32_bf16 a[172:175], a[120:123], a[40:43], a[172:175]\n" \
"ds_read_b128 a[32:35], v5 offset:40960\n" \
"v_mfma_f32_16x16x32_bf16 a[172:175], a[124:127], a[44:47], a[172:175]\n" \
"ds_read_b128 a[36:39], v6 offset:40960\n" \
"v_mfma_f32_16x16x32_bf16 a[180:183], a[120:123], a[48:51], a[180:183]\n" \
"ds_read_b128 a[40:43], v5 offset:43008\n" \
"v_mfma_f32_16x16x32_bf16 a[180:183], a[124:127], a[52:55], a[180:183]\n" \
"ds_read_b128 a[44:47], v6 offset:43008\n" \
"v_mfma_f32_16x16x32_bf16 a[188:191], a[120:123], a[56:59], a[188:191]\n" \
"ds_read_b128 a[48:51], v5 offset:45056\n" \
"v_mfma_f32_16x16x32_bf16 a[188:191], a[124:127], a[60:63], a[188:191]\n" \
"s_waitcnt lgkmcnt(9)\n" \
"ds_read_b128 a[52:55], v6 offset:45056\n" \
"v_mfma_f32_16x16x32_bf16 v[64:67], a[64:67], a[0:3], v[64:67]\n" \
"s_mov_b32 s47, 3145728\n" \
"s_add_u32 m0, s22, 16256\n" \
"ds_read_b128 a[56:59], v5 offset:47104\n" \
"v_mfma_f32_16x16x32_bf16 v[64:67], a[68:71], a[4:7], v[64:67]\n" \
"buffer_load_dwordx4 v21, s[32:35], s47 offen offset:128 lds\n" \
"v_mfma_f32_16x16x32_bf16 v[72:75], a[64:67], a[8:11], v[72:75]\n" \
"s_waitcnt lgkmcnt(7)\n" \
"ds_read_b128 a[60:63], v6 offset:47104\n" \
"v_mfma_f32_16x16x32_bf16 v[72:75], a[68:71], a[12:15], v[72:75]\n" \
"v_mfma_f32_16x16x32_bf16 v[80:83], a[64:67], a[16:19], v[80:83]\n" \
"s_add_u32 m0, s22, 18048\n" \
"ds_read_b64_tr_b16 a[96:97], v9 offset:49152\n" \
"v_mfma_f32_16x16x32_bf16 v[80:83], a[68:71], a[20:23], v[80:83]\n" \
"buffer_load_dwordx4 v21, s[32:35], s47 offen offset:384 lds\n" \
"v_mfma_f32_16x16x32_bf16 v[88:91], a[64:67], a[24:27], v[88:91]\n" \
"s_waitcnt lgkmcnt(5)\n" \
"ds_read_b64_tr_b16 a[98:99], v10 offset:49152\n" \
"v_mfma_f32_16x16x32_bf16 v[88:91], a[68:71], a[28:31], v[88:91]\n" \
"v_mfma_f32_16x16x32_bf16 v[96:99], a[64:67], a[32:35], v[96:99]\n" \
"s_mov_b32 s47, 3407872\n" \
"s_add_u32 m0, s22, 17280\n" \
"ds_read_b64_tr_b16 a[100:101], v9 offset:50176\n" \
"v_mfma_f32_16x16x32_bf16 v[96:99], a[68:71], a[36:39], v[96:99]\n" \
"buffer_load_dwordx4 v21, s[32:35], s47 offen offset:128 lds\n" \
"v_mfma_f32_16x16x32_bf16 v[104:107], a[64:67], a[40:43], v[104:107]\n" \
"s_waitcnt lgkmcnt(3)\n" \
"ds_read_b64_tr_b16 a[102:103], v10 offset:50176\n" \
"v_mfma_f32_16x16x32_bf16 v[104:107], a[68:71], a[44:47], v[104:107]\n" \
"v_mfma_f32_16x16x32_bf16 v[112:115], a[64:67], a[48:51], v[112:115]\n" \
"s_add_u32 m0, s22, 19072\n" \
"ds_read_b64_tr_b16 a[104:105], v11 offset:49152\n" \
"v_mfma_f32_16x16x32_bf16 v[112:115], a[68:71], a[52:55], v[112:115]\n" \
"buffer_load_dwordx4 v21, s[32:35], s47 offen offset:384 lds\n" \
"v_mfma_f32_16x16x32_bf16 v[120:123], a[64:67], a[56:59], v[120:123]\n" \
"ds_read_b64_tr_b16 a[106:107], v12 offset:49152\n" \
"v_mfma_f32_16x16x32_bf16 v[120:123], a[68:71], a[60:63], v[120:123]\n" \
"v_mfma_f32_16x16x32_bf16 v[68:71], a[72:75], a[0:3], v[68:71]\n" \
"s_mov_b32 s46, 896\n" \
"s_add_u32 m0, s50, 0\n" \
"ds_read_b64_tr_b16 a[108:109], v11 offset:50176\n" \
"v_mfma_f32_16x16x32_bf16 v[68:71], a[76:79], a[4:7], v[68:71]\n" \
"buffer_load_dwordx4 v13, s[28:31], s46 offen lds\n" \
"v_mfma_f32_16x16x32_bf16 v[76:79], a[72:75], a[8:11], v[76:79]\n" \
"ds_read_b64_tr_b16 a[110:111], v12 offset:50176\n" \
"v_mfma_f32_16x16x32_bf16 v[76:79], a[76:79], a[12:15], v[76:79]\n" \
"v_mfma_f32_16x16x32_bf16 v[84:87], a[72:75], a[16:19], v[84:87]\n" \
"s_add_u32 m0, s50, 4096\n" \
"ds_read_b64_tr_b16 a[112:113], v9 offset:49664\n" \
"v_mfma_f32_16x16x32_bf16 v[84:87], a[76:79], a[20:23], v[84:87]\n" \
"buffer_load_dwordx4 v14, s[28:31], s46 offen lds\n" \
"v_mfma_f32_16x16x32_bf16 v[92:95], a[72:75], a[24:27], v[92:95]\n" \
"ds_read_b64_tr_b16 a[114:115], v10 offset:49664\n" \
"v_mfma_f32_16x16x32_bf16 v[92:95], a[76:79], a[28:31], v[92:95]\n" \
"v_mfma_f32_16x16x32_bf16 v[100:103], a[72:75], a[32:35], v[100:103]\n" \
"s_add_u32 m0, s50, 8192\n" \
"ds_read_b64_tr_b16 a[116:117], v9 offset:50688\n" \
"v_mfma_f32_16x16x32_bf16 v[100:103], a[76:79], a[36:39], v[100:103]\n" \
"buffer_load_dwordx4 v15, s[28:31], s46 offen lds\n" \
"v_mfma_f32_16x16x32_bf16 v[108:111], a[72:75], a[40:43], v[108:111]\n" \
"ds_read_b64_tr_b16 a[118:119], v10 offset:50688\n" \
"v_mfma_f32_16x16x32_bf16 v[108:111], a[76:79], a[44:47], v[108:111]\n" \
"v_mfma_f32_16x16x32_bf16 v[116:119], a[72:75], a[48:51], v[116:119]\n" \
"s_add_u32 m0, s50, 12288\n" \
"ds_read_b64_tr_b16 a[120:121], v11 offset:49664\n" \
"v_mfma_f32_16x16x32_bf16 v[116:119], a[76:79], a[52:55], v[116:119]\n" \
"buffer_load_dwordx4 v16, s[28:31], s46 offen lds\n" \
"v_mfma_f32_16x16x32_bf16 v[124:127], a[72:75], a[56:59], v[124:127]\n" \
"ds_read_b64_tr_b16 a[122:123], v12 offset:49664\n" \
"v_mfma_f32_16x16x32_bf16 v[124:127], a[76:79], a[60:63], v[124:127]\n" \
"v_mfma_f32_16x16x32_bf16 v[128:131], a[80:83], a[0:3], v[128:131]\n" \
"s_mov_b32 s46, 896\n" \
"s_add_u32 m0, s50, 16384\n" \
"ds_read_b64_tr_b16 a[124:125], v11 offset:50688\n" \
"v_mfma_f32_16x16x32_bf16 v[128:131], a[84:87], a[4:7], v[128:131]\n" \
"buffer_load_dwordx4 v17, s[28:31], s46 offen lds\n" \
"v_mfma_f32_16x16x32_bf16 v[136:139], a[80:83], a[8:11], v[136:139]\n" \
"ds_read_b64_tr_b16 a[126:127], v12 offset:50688\n" \
"v_mfma_f32_16x16x32_bf16 v[136:139], a[84:87], a[12:15], v[136:139]\n" \
"v_mfma_f32_16x16x32_bf16 v[144:147], a[80:83], a[16:19], v[144:147]\n" \
"s_add_u32 m0, s50, 20480\n" \
"v_mfma_f32_16x16x32_bf16 v[144:147], a[84:87], a[20:23], v[144:147]\n" \
"buffer_load_dwordx4 v18, s[28:31], s46 offen lds\n" \
"v_mfma_f32_16x16x32_bf16 v[152:155], a[80:83], a[24:27], v[152:155]\n" \
"v_mfma_f32_16x16x32_bf16 v[152:155], a[84:87], a[28:31], v[152:155]\n" \
"v_mfma_f32_16x16x32_bf16 v[160:163], a[80:83], a[32:35], v[160:163]\n" \
"s_add_u32 m0, s50, 24576\n" \
"v_mfma_f32_16x16x32_bf16 v[160:163], a[84:87], a[36:39], v[160:163]\n" \
"buffer_load_dwordx4 v19, s[28:31], s46 offen lds\n" \
"v_mfma_f32_16x16x32_bf16 v[168:171], a[80:83], a[40:43], v[168:171]\n" \
"v_mfma_f32_16x16x32_bf16 v[168:171], a[84:87], a[44:47], v[168:171]\n" \
"s_waitcnt vmcnt(17) lgkmcnt(0)\n" \
"v_mfma_f32_16x16x32_bf16 v[176:179], a[80:83], a[48:51], v[176:179]\n" \
"s_add_u32 m0, s50, 28672\n" \
"s_barrier\n" \
"v_mfma_f32_16x16x32_bf16 v[176:179], a[84:87], a[52:55], v[176:179]\n" \
"buffer_load_dwordx4 v20, s[28:31], s46 offen lds\n" \
"v_mfma_f32_16x16x32_bf16 v[184:187], a[80:83], a[56:59], v[184:187]\n" \
"v_mfma_f32_16x16x32_bf16 v[184:187], a[84:87], a[60:63], v[184:187]\n" \
"v_mfma_f32_16x16x32_bf16 v[132:135], a[88:91], a[0:3], v[132:135]\n" \
"s_mov_b32 s47, 3670016\n" \
"s_add_u32 m0, s22, 32768\n" \
"v_mfma_f32_16x16x32_bf16 v[132:135], a[92:95], a[4:7], v[132:135]\n" \
"buffer_load_dwordx4 v21, s[32:35], s47 offen offset:0 lds\n" \
"v_mfma_f32_16x16x32_bf16 v[140:143], a[88:91], a[8:11], v[140:143]\n" \
"v_mfma_f32_16x16x32_bf16 v[140:143], a[92:95], a[12:15], v[140:143]\n" \
"v_mfma_f32_16x16x32_bf16 v[148:151], a[88:91], a[16:19], v[148:151]\n" \
"s_add_u32 m0, s22, 34560\n" \
"v_mfma_f32_16x16x32_bf16 v[148:151], a[92:95], a[20:23], v[148:151]\n" \
"buffer_load_dwordx4 v21, s[32:35], s47 offen offset:256 lds\n" \
"v_mfma_f32_16x16x32_bf16 v[156:159], a[88:91], a[24:27], v[156:159]\n" \
"v_mfma_f32_16x16x32_bf16 v[156:159], a[92:95], a[28:31], v[156:159]\n" \
"v_mfma_f32_16x16x32_bf16 v[164:167], a[88:91], a[32:35], v[164:167]\n" \
"s_mov_b32 s47, 3932160\n" \
"s_add_u32 m0, s22, 33792\n" \
"v_mfma_f32_16x16x32_bf16 v[164:167], a[92:95], a[36:39], v[164:167]\n" \
"buffer_load_dwordx4 v21, s[32:35], s47 offen offset:0 lds\n" \
"v_mfma_f32_16x16x32_bf16 v[172:175], a[88:91], a[40:43], v[172:175]\n" \
"v_mfma_f32_16x16x32_bf16 v[172:175], a[92:95], a[44:47], v[172:175]\n" \
"v_mfma_f32_16x16x32_bf16 v[180:183], a[88:91], a[48:51], v[180:183]\n" \
"s_add_u32 m0, s22, 35584\n" \
"v_mfma_f32_16x16x32_bf16 v[180:183], a[92:95], a[52:55], v[180:183]\n" \
"buffer_load_dwordx4 v21, s[32:35], s47 offen offset:256 lds\n" \
"v_mfma_f32_16x16x32_bf16 v[188:191], a[88:91], a[56:59], v[188:191]\n" \
"v_mfma_f32_16x16x32_bf16 v[188:191], a[92:95], a[60:63], v[188:191]\n" \
"v_mfma_f32_16x16x32_bf16 v[192:195], a[96:99], a[0:3], v[192:195]\n" \
"v_mfma_f32_16x16x32_bf16 v[192:195], a[100:103], a[4:7], v[192:195]\n" \
"buffer_load_dwordx4 v[30:33], v23, s[68:71], 0 offen offset:0 nt\n" \
"v_mfma_f32_16x16x32_bf16 v[200:203], a[96:99], a[8:11], v[200:203]\n" \
"s_mov_b64 s[28:29], s[64:65]\n" \
"v_mfma_f32_16x16x32_bf16 v[200:203], a[100:103], a[12:15], v[200:203]\n" \
"v_mfma_f32_16x16x32_bf16 v[208:211], a[96:99], a[16:19], v[208:211]\n" \
"ds_read_b64_tr_b16 a[64:65], v9 offset:0\n" \
"v_mfma_f32_16x16x32_bf16 v[208:211], a[100:103], a[20:23], v[208:211]\n" \
"buffer_load_dwordx4 v[34:37], v24, s[68:71], 0 offen offset:0 nt\n" \
"v_mfma_f32_16x16x32_bf16 v[216:219], a[96:99], a[24:27], v[216:219]\n" \
"s_and_b32 s29, s29, 0xffff\n" \
"ds_read_b64_tr_b16 a[66:67], v10 offset:0\n" \
"v_mfma_f32_16x16x32_bf16 v[216:219], a[100:103], a[28:31], v[216:219]\n" \
"v_mfma_f32_16x16x32_bf16 v[224:227], a[96:99], a[32:35], v[224:227]\n" \
"ds_read_b64_tr_b16 a[68:69], v9 offset:1024\n" \
"v_mfma_f32_16x16x32_bf16 v[224:227], a[100:103], a[36:39], v[224:227]\n" \
"buffer_load_dwordx4 v[38:41], v24, s[68:71], 0 offen offset:1024 nt\n" \
"v_mfma_f32_16x16x32_bf16 v[232:235], a[96:99], a[40:43], v[232:235]\n" \
"s_mov_b32 s30, 4194304\n" \
"ds_read_b64_tr_b16 a[70:71], v10 offset:1024\n" \
"v_mfma_f32_16x16x32_bf16 v[232:235], a[100:103], a[44:47], v[232:235]\n" \
"v_mfma_f32_16x16x32_bf16 v[240:243], a[96:99], a[48:51], v[240:243]\n" \
"ds_read_b64_tr_b16 a[72:73], v11 offset:0\n" \
"v_mfma_f32_16x16x32_bf16 v[240:243], a[100:103], a[52:55], v[240:243]\n" \
"buffer_load_dwordx4 v[42:45], v24, s[68:71], 0 offen offset:2048 nt\n" \
"v_mfma_f32_16x16x32_bf16 v[248:251], a[96:99], a[56:59], v[248:251]\n" \
"s_lshl_b32 s60, s7, 18\n" \
"ds_read_b64_tr_b16 a[74:75], v12 offset:0\n" \
"v_mfma_f32_16x16x32_bf16 v[248:251], a[100:103], a[60:63], v[248:251]\n" \
"v_mfma_f32_16x16x32_bf16 v[196:199], a[104:107], a[0:3], v[196:199]\n" \
"ds_read_b64_tr_b16 a[76:77], v11 offset:1024\n" \
"v_mfma_f32_16x16x32_bf16 v[196:199], a[108:111], a[4:7], v[196:199]\n" \
"buffer_load_dwordx4 v[46:49], v24, s[68:71], 0 offen offset:3072 nt\n" \
"v_mfma_f32_16x16x32_bf16 v[204:207], a[104:107], a[8:11], v[204:207]\n" \
"LH2_%=:\ns_cbranch_execz LH10_%=\n" \
"s_sub_u32 s61, 3932160, s60\n" \
"ds_read_b64_tr_b16 a[78:79], v12 offset:1024\n" \
"v_mfma_f32_16x16x32_bf16 v[204:207], a[108:111], a[12:15], v[204:207]\n" \
"v_mfma_f32_16x16x32_bf16 v[212:215], a[104:107], a[16:19], v[212:215]\n" \
"ds_read_b64_tr_b16 a[80:81], v9 offset:512\n" \
"v_mfma_f32_16x16x32_bf16 v[212:215], a[108:111], a[20:23], v[212:215]\n" \
"buffer_load_dword v50, v25, s[68:71], 0 offen offset:0\n" \
"v_mfma_f32_16x16x32_bf16 v[220:223], a[104:107], a[24:27], v[220:223]\n" \
"ds_read_b64_tr_b16 a[82:83], v10 offset:512\n" \
"v_add_u32 v13, s60, v63\n" \
"v_mfma_f32_16x16x32_bf16 v[220:223], a[108:111], a[28:31], v[220:223]\n" \
"v_mfma_f32_16x16x32_bf16 v[228:231], a[104:107], a[32:35], v[228:231]\n" \
"ds_read_b64_tr_b16 a[84:85], v9 offset:1536\n" \
"v_mfma_f32_16x16x32_bf16 v[228:231], a[108:111], a[36:39], v[228:231]\n" \
"buffer_load_dword v51, v24, s[68:71], 0 offen offset:16\n" \
"v_mfma_f32_16x16x32_bf16 v[236:239], a[104:107], a[40:43], v[236:239]\n" \
"ds_read_b64_tr_b16 a[86:87], v10 offset:1536\n" \
"v_add_u32 v17, s61, v63\n" \
"v_mfma_f32_16x16x32_bf16 v[236:239], a[108:111], a[44:47], v[236:239]\n" \
"v_mfma_f32_16x16x32_bf16 v[244:247], a[104:107], a[48:51], v[244:247]\n" \
"ds_read_b64_tr_b16 a[88:89], v11 offset:512\n" \
"v_mfma_f32_16x16x32_bf16 v[244:247], a[108:111], a[52:55], v[244:247]\n" \
"buffer_load_dword v52, v24, s[68:71], 0 offen offset:1040\n" \
"v_mfma_f32_16x16x32_bf16 v[252:255], a[104:107], a[56:59], v[252:255]\n" \
"ds_read_b64_tr_b16 a[90:91], v12 offset:512\n" \
"v_add_u32 v14, 0x10000, v13\n" \
"v_mfma_f32_16x16x32_bf16 v[252:255], a[108:111], a[60:63], v[252:255]\n" \
"v_mfma_f32_16x16x32_bf16 a[128:131], a[112:115], a[0:3], a[128:131]\n" \
"ds_read_b64_tr_b16 a[92:93], v11 offset:1536\n" \
"v_mfma_f32_16x16x32_bf16 a[128:131], a[116:119], a[4:7], a[128:131]\n" \
"buffer_load_dword v53, v24, s[68:71], 0 offen offset:2064\n" \
"v_mfma_f32_16x16x32_bf16 a[136:139], a[112:115], a[8:11], a[136:139]\n" \
"ds_read_b64_tr_b16 a[94:95], v12 offset:1536\n" \
"v_add_u32 v18, 0x10000, v17\n" \
"v_mfma_f32_16x16x32_bf16 a[136:139], a[116:119], a[12:15], a[136:139]\n" \
"v_mfma_f32_16x16x32_bf16 a[144:147], a[112:115], a[16:19], a[144:147]\n" \
"v_mfma_f32_16x16x32_bf16 a[144:147], a[116:119], a[20:23], a[144:147]\n" \
"buffer_load_dword v54, v24, s[68:71], 0 offen offset:3088\n" \
"v_mfma_f32_16x16x32_bf16 a[152:155], a[112:115], a[24:27], a[152:155]\n" \
"v_add_u32 v15, 0x10000, v14\n" \
"v_mfma_f32_16x16x32_bf16 a[152:155], a[116:119], a[28:31], a[152:155]\n" \
"v_mfma_f32_16x16x32_bf16 a[160:163], a[112:115], a[32:35], a[160:163]\n" \
"v_mfma_f32_16x16x32_bf16 a[160:163], a[116:119], a[36:39], a[160:163]\n" \
"global_load_short_d16_hi v28, v29, s[12:13]\n" \
"v_mfma_f32_16x16x32_bf16 a[168:171], a[112:115], a[40:43], a[168:171]\n" \
"v_add_u32 v19, 0x10000, v18\n" \
"v_mfma_f32_16x16x32_bf16 a[168:171], a[116:119], a[44:47], a[168:171]\n" \
"s_waitcnt vmcnt(23) lgkmcnt(0)\n" \
"v_mfma_f32_16x16x32_bf16 a[176:179], a[112:115], a[48:51], a[176:179]\n" \
"s_barrier\n" \
"v_mfma_f32_16x16x32_bf16 a[176:179], a[116:119], a[52:55], a[176:179]\n" \
"global_load_dwordx4 a[196:199], v4, s[86:87] offset:1024\n" \
"v_mfma_f32_16x16x32_bf16 a[184:187], a[112:115], a[56:59], a[184:187]\n" \
"v_add_u32 v16, 0x10000, v15\n" \
"v_mfma_f32_16x16x32_bf16 a[184:187], a[116:119], a[60:63], a[184:187]\n" \
"v_mfma_f32_16x16x32_bf16 a[132:135], a[120:123], a[0:3], a[132:135]\n" \
"v_mfma_f32_16x16x32_bf16 a[132:135], a[124:127], a[4:7], a[132:135]\n" \
"v_mfma_f32_16x16x32_bf16 a[140:143], a[120:123], a[8:11], a[140:143]\n" \
"ds_read_b128 a[0:3], v7 offset:0\n" \
"v_add_u32 v20, 0x10000, v19\n" \
"v_mfma_f32_16x16x32_bf16 a[140:143], a[124:127], a[12:15], a[140:143]\n" \
"ds_read_b128 a[4:7], v8 offset:0\n" \
"v_mfma_f32_16x16x32_bf16 a[148:151], a[120:123], a[16:19], a[148:151]\n" \
"ds_read_b128 a[8:11], v7 offset:2048\n" \
"v_mfma_f32_16x16x32_bf16 a[148:151], a[124:127], a[20:23], a[148:151]\n" \
"ds_read_b128 a[12:15], v8 offset:2048\n" \
"v_mfma_f32_16x16x32_bf16 a[156:159], a[120:123], a[24:27], a[156:159]\n" \
"ds_read_b128 a[16:19], v7 offset:4096\n" \
"v_mfma_f32_16x16x32_bf16 a[156:159], a[124:127], a[28:31], a[156:159]\n" \
"ds_read_b128 a[20:23], v8 offset:4096\n" \
"v_mfma_f32_16x16x32_bf16 a[164:167], a[120:123], a[32:35], a[164:167]\n" \
"ds_read_b128 a[24:27], v7 offset:6144\n" \
"v_mfma_f32_16x16x32_bf16 a[164:167], a[124:127], a[36:39], a[164:167]\n" \
"ds_read_b128 a[28:31], v8 offset:6144\n" \
"v_mfma_f32_16x16x32_bf16 a[172:175], a[120:123], a[40:43], a[172:175]\n" \
"ds_read_b128 a[32:35], v7 offset:8192\n" \
"v_mfma_f32_16x16x32_bf16 a[172:175], a[124:127], a[44:47], a[172:175]\n" \
"ds_read_b128 a[36:39], v8 offset:8192\n" \
"v_mfma_f32_16x16x32_bf16 a[180:183], a[120:123], a[48:51], a[180:183]\n" \
"ds_read_b128 a[40:43], v7 offset:10240\n" \
"v_mfma_f32_16x16x32_bf16 a[180:183], a[124:127], a[52:55], a[180:183]\n" \
"ds_read_b128 a[44:47], v8 offset:10240\n" \
"v_mfma_f32_16x16x32_bf16 a[188:191], a[120:123], a[56:59], a[188:191]\n" \
"ds_read_b128 a[48:51], v7 offset:12288\n" \
"v_mfma_f32_16x16x32_bf16 a[188:191], a[124:127], a[60:63], a[188:191]\n" \
"s_waitcnt lgkmcnt(9)\n" \
"ds_read_b128 a[52:55], v8 offset:12288\n" \
"v_mfma_f32_16x16x32_bf16 v[64:67], a[64:67], a[0:3], v[64:67]\n" \
"ds_read_b128 a[56:59], v7 offset:14336\n" \
"v_mfma_f32_16x16x32_bf16 v[64:67], a[68:71], a[4:7], v[64:67]\n" \
"buffer_load_dwordx2 v[58:59], v57, s[36:39], 0 offen sc1\n" \
"v_mfma_f32_16x16x32_bf16 v[72:75], a[64:67], a[8:11], v[72:75]\n" \
"s_waitcnt lgkmcnt(7)\n" \
"ds_read_b128 a[60:63], v8 offset:14336\n" \
"v_mfma_f32_16x16x32_bf16 v[72:75], a[68:71], a[12:15], v[72:75]\n" \
"v_mfma_f32_16x16x32_bf16 v[80:83], a[64:67], a[16:19], v[80:83]\n" \
"s_mov_b32 s47, 3670016\n" \
"s_add_u32 m0, s22, 49024\n" \
"ds_read_b64_tr_b16 a[96:97], v9 offset:16384\n" \
"v_mfma_f32_16x16x32_bf16 v[80:83], a[68:71], a[20:23], v[80:83]\n" \
"buffer_load_dwordx4 v21, s[32:35], s47 offen offset:128 lds\n" \
"v_mfma_f32_16x16x32_bf16 v[88:91], a[64:67], a[24:27], v[88:91]\n" \
"s_waitcnt lgkmcnt(5)\n" \
"ds_read_b64_tr_b16 a[98:99], v10 offset:16384\n" \
"v_mfma_f32_16x16x32_bf16 v[88:91], a[68:71], a[28:31], v[88:91]\n" \
"v_mfma_f32_16x16x32_bf16 v[96:99], a[64:67], a[32:35], v[96:99]\n" \
"s_add_u32 m0, s22, 50816\n" \
"ds_read_b64_tr_b16 a[100:101], v9 offset:17408\n" \
"v_mfma_f32_16x16x32_bf16 v[96:99], a[68:71], a[36:39], v[96:99]\n" \
"buffer_load_dwordx4 v21, s[32:35], s47 offen offset:384 lds\n" \
"v_mfma_f32_16x16x32_bf16 v[104:107], a[64:67], a[40:43], v[104:107]\n" \
"s_waitcnt lgkmcnt(3)\n" \
"ds_read_b64_tr_b16 a[102:103], v10 offset:17408\n" \
"v_mfma_f32_16x16x32_bf16 v[104:107], a[68:71], a[44:47], v[104:107]\n" \
"v_mfma_f32_16x16x32_bf16 v[112:115], a[64:67], a[48:51], v[112:115]\n" \
"s_mov_b32 s47, 3932160\n" \
"s_add_u32 m0, s22, 50048\n" \
"ds_read_b64_tr_b16 a[104:105], v11 offset:16384\n" \
"v_mfma_f32_16x16x32_bf16 v[112:115], a[68:71], a[52:55], v[112:115]\n" \
"buffer_load_dwordx4 v21, s[32:35], s47 offen offset:128 lds\n" \
"v_mfma_f32_16x16x32_bf16 v[120:123], a[64:67], a[56:59], v[120:123]\n" \
"ds_read_b64_tr_b16 a[106:107], v12 offset:16384\n" \
"v_mfma_f32_16x16x32_bf16 v[120:123], a[68:71], a[60:63], v[120:123]\n" \
"s_add_u32 s76, s76, 2097152\ns_addc_u32 s77, s77, 0\n" \
"v_mfma_f32_16x16x32_bf16 v[68:71], a[72:75], a[0:3], v[68:71]\n" \
"s_add_u32 m0, s22, 51840\n" \
"ds_read_b64_tr_b16 a[108:109], v11 offset:17408\n" \
"v_mfma_f32_16x16x32_bf16 v[68:71], a[76:79], a[4:7], v[68:71]\n" \
"buffer_load_dwordx4 v21, s[32:35], s47 offen offset:384 lds\n" \
"v_mfma_f32_16x16x32_bf16 v[76:79], a[72:75], a[8:11], v[76:79]\n" \
"ds_read_b64_tr_b16 a[110:111], v12 offset:17408\n" \
"v_mfma_f32_16x16x32_bf16 v[76:79], a[76:79], a[12:15], v[76:79]\n" \
"v_mfma_f32_16x16x32_bf16 v[84:87], a[72:75], a[16:19], v[84:87]\n" \
"ds_read_b64_tr_b16 a[112:113], v9 offset:16896\n" \
"v_mfma_f32_16x16x32_bf16 v[84:87], a[76:79], a[20:23], v[84:87]\n" \
"v_mfma_f32_16x16x32_bf16 v[92:95], a[72:75], a[24:27], v[92:95]\n" \
"ds_read_b64_tr_b16 a[114:115], v10 offset:16896\n" \
"v_mfma_f32_16x16x32_bf16 v[92:95], a[76:79], a[28:31], v[92:95]\n" \
"s_mov_b64 exec, 0xffff\nglobal_load_dword v22, v1, s[76:77]\ns_mov_b64 exec, -1\n" \
"v_mfma_f32_16x16x32_bf16 v[100:103], a[72:75], a[32:35], v[100:103]\n" \
"s_add_u32 s76, s76, 2097152\ns_addc_u32 s77, s77, 0\n" \
"ds_read_b64_tr_b16 a[116:117], v9 offset:17920\n" \
"v_mfma_f32_16x16x32_bf16 v[100:103], a[76:79], a[36:39], v[100:103]\n" \
"v_mfma_f32_16x16x32_bf16 v[108:111], a[72:75], a[40:43], v[108:111]\n" \
"ds_read_b64_tr_b16 a[118:119], v10 offset:17920\n" \
"v_mfma_f32_16x16x32_bf16 v[108:111], a[76:79], a[44:47], v[108:111]\n" \
"s_mov_b64 exec, 0xffff\nglobal_load_dword v22, v1, s[76:77]\ns_mov_b64 exec, -1\n" \
"v_mfma_f32_16x16x32_bf16 v[116:119], a[72:75], a[48:51], v[116:119]\n" \
"ds_read_b64_tr_b16 a[120:121], v11 offset:16896\n" \
"v_mfma_f32_16x16x32_bf16 v[116:119], a[76:79], a[52:55], v[116:119]\n" \
"v_mfma_f32_16x16x32_bf16 v[124:127], a[72:75], a[56:59], v[124:127]\n" \
"ds_read_b64_tr_b16 a[122:123], v12 offset:16896\n" \
"v_mfma_f32_16x16x32_bf16 v[124:127], a[76:79], a[60:63], v[124:127]\n" \
"v_mfma_f32_16x16x32_bf16 v[128:131], a[80:83], a[0:3], v[128:131]\n" \
"ds_read_b64_tr_b16 a[124:125], v11 offset:17920\n" \
"v_mfma_f32_16x16x32_bf16 v[128:131], a[84:87], a[4:7], v[128:131]\n" \
"v_mfma_f32_16x16x32_bf16 v[136:139], a[80:83], a[8:11], v[136:139]\n" \
"ds_read_b64_tr_b16 a[126:127], v12 offset:17920\n" \
"v_mfma_f32_16x16x32_bf16 v[136:139], a[84:87], a[12:15], v[136:139]\n" \
"v_mfma_f32_16x16x32_bf16 v[144:147], a[80:83], a[16:19], v[144:147]\n" \
"v_mfma_f32_16x16x32_bf16 v[144:147], a[84:87], a[20:23], v[144:147]\n" \
"v_mfma_f32_16x16x32_bf16 v[152:155], a[80:83], a[24:27], v[152:155]\n" \
"v_mfma_f32_16x16x32_bf16 v[152:155], a[84:87], a[28:31], v[152:155]\n" \
"v_mfma_f32_16x16x32_bf16 v[160:163], a[80:83], a[32:35], v[160:163]\n" \
"v_mfma_f32_16x16x32_bf16 v[160:163], a[84:87], a[36:39], v[160:163]\n" \
"v_mfma_f32_16x16x32_bf16 v[168:171], a[80:83], a[40:43], v[168:171]\n" \
"v_mfma_f32_16x16x32_bf16 v[168:171], a[84:87], a[44:47], v[168:171]\n" \
"s_waitcnt vmcnt(19) lgkmcnt(0)\n" \
"v_mfma_f32_16x16x32_bf16 v[176:179], a[80:83], a[48:51], v[176:179]\n" \
"s_barrier\n" \
"v_mfma_f32_16x16x32_bf16 v[176:179], a[84:87], a[52:55], v[176:179]\n" \
"v_mfma_f32_16x16x32_bf16 v[184:187], a[80:83], a[56:59], v[184:187]\n" \
"v_mfma_f32_16x16x32_bf16 v[184:187], a[84:87], a[60:63], v[184:187]\n" \
"v_mfma_f32_16x16x32_bf16 v[132:135], a[88:91], a[0:3], v[132:135]\n" \
"v_mfma_f32_16x16x32_bf16 v[132:135], a[92:95], a[4:7], v[132:135]\n" \
"v_mfma_f32_16x16x32_bf16 v[140:143], a[88:91], a[8:11], v[140:143]\n" \
"v_mfma_f32_16x16x32_bf16 v[140:143], a[92:95], a[12:15], v[140:143]\n" \
"v_mfma_f32_16x16x32_bf16 v[148:151], a[88:91], a[16:19], v[148:151]\n" \
"v_mfma_f32_16x16x32_bf16 v[148:151], a[92:95], a[20:23], v[148:151]\n" \
"v_mfma_f32_16x16x32_bf16 v[156:159], a[88:91], a[24:27], v[156:159]\n" \
"v_mfma_f32_16x16x32_bf16 v[156:159], a[92:95], a[28:31], v[156:159]\n" \
"v_mfma_f32_16x16x32_bf16 v[164:167], a[88:91], a[32:35], v[164:167]\n" \
"v_mfma_f32_16x16x32_bf16 v[164:167], a[92:95], a[36:39], v[164:167]\n" \
"v_mfma_f32_16x16x32_bf16 v[172:175], a[88:91], a[40:43], v[172:175]\n" \
"v_mfma_f32_16x16x32_bf16 v[172:175], a[92:95], a[44:47], v[172:175]\n" \
"s_waitcnt vmcnt(6)\n" \
"v_mfma_f32_16x16x32_bf16 v[180:183], a[88:91], a[48:51], v[180:183]\n" \
"v_cmp_ne_u64 vcc, s[14:15], v[58:59]\n" \
"v_mfma_f32_16x16x32_bf16 v[180:183], a[92:95], a[52:55], v[180:183]\n" \
"v_mfma_f32_16x16x32_bf16 v[188:191], a[88:91], a[56:59], v[188:191]\n" \
"s_cbranch_vccnz LPUK_%=\nLPUKR_%=:\n" \
"v_mfma_f32_16x16x32_bf16 v[188:191], a[92:95], a[60:63], v[188:191]\n" \
"v_mfma_f32_16x16x32_bf16 v[192:195], a[96:99], a[0:3], v[192:195]\n" \
"s_add_u32 m0, s56, 10304\n" \
"v_mfma_f32_16x16x32_bf16 v[192:195], a[100:103], a[4:7], v[192:195]\n" \
"buffer_load_dwordx4 v26, s[72:75], 0 offen offset:0 sc1 lds\n" \
"v_mfma_f32_16x16x32_bf16 v[200:203], a[96:99], a[8:11], v[200:203]\n" \
"s_mov_b64 s[32:33], s[44:45]\n" \
"v_mfma_f32_16x16x32_bf16 v[200:203], a[100:103], a[12:15], v[200:203]\n" \
"v_mfma_f32_16x16x32_bf16 v[208:211], a[96:99], a[16:19], v[208:211]\n" \
"s_add_u32 m0, s56, 11328\n" \
"ds_read_b64_tr_b16 a[64:65], v9 offset:32768\n" \
"s_waitcnt vmcnt(9)\n" \
"v_mfma_f32_16x16x32_bf16 v[208:211], a[100:103], a[20:23], v[208:211]\n" \
"buffer_load_dwordx4 v27, s[72:75], 0 offen offset:0 sc1 lds\n" \
"v_mfma_f32_16x16x32_bf16 v[216:219], a[96:99], a[24:27], v[216:219]\n" \
"s_mov_b32 s34, s49\n" \
"ds_read_b64_tr_b16 a[66:67], v10 offset:32768\n" \
"v_alignbyte_b32 v30, v31, v30, 2\n" \
"v_mfma_f32_16x16x32_bf16 v[216:219], a[100:103], a[28:31], v[216:219]\n" \
"v_alignbyte_b32 v31, v32, v31, 2\n" \
"v_alignbyte_b32 v32, v33, v32, 2\n" \
"v_mfma_f32_16x16x32_bf16 v[224:227], a[96:99], a[32:35], v[224:227]\n" \
"s_add_u32 m0, s56, 11328\n" \
"ds_read_b64_tr_b16 a[68:69], v9 offset:33792\n" \
"v_alignbyte_b32 v33, v50, v33, 2\n" \
"v_mfma_f32_16x16x32_bf16 v[224:227], a[100:103], a[36:39], v[224:227]\n" \
"buffer_load_dwordx4 v27, s[72:75], 0 offen offset:1024 sc1 lds\n" \
"v_mfma_f32_16x16x32_bf16 v[232:235], a[96:99], a[40:43], v[232:235]\n" \
"ds_read_b64_tr_b16 a[70:71], v10 offset:33792\n" \
"v_mfma_f32_16x16x32_bf16 v[232:235], a[100:103], a[44:47], v[232:235]\n" \
"ds_write_b128 v55, v[30:33] offset:5184\n" \
"v_alignbyte_b32 v34, v35, v34, 2\n" \
"v_mfma_f32_16x16x32_bf16 v[240:243], a[96:99], a[48:51], v[240:243]\n" \
"s_add_u32 m0, s56, 11328\n" \
"ds_read_b64_tr_b16 a[72:73], v11 offset:32768\n" \
"v_alignbyte_b32 v35, v36, v35, 2\n" \
"v_mfma_f32_16x16x32_bf16 v[240:243], a[100:103], a[52:55], v[240:243]\n" \
"buffer_load_dwordx4 v27, s[72:75], 0 offen offset:2048 sc1 lds\n" \
"v_mfma_f32_16x16x32_bf16 v[248:251], a[96:99], a[56:59], v[248:251]\n" \
"ds_read_b64_tr_b16 a[74:75], v12 offset:32768\n" \
"v_alignbyte_b32 v36, v37, v36, 2\n" \
"v_mfma_f32_16x16x32_bf16 v[248:251], a[100:103], a[60:63], v[248:251]\n" \
"v_alignbyte_b32 v37, v51, v37, 2\n" \
"ds_write_b128 v55, v[34:37] offset:6208\n" \
"v_mfma_f32_16x16x32_bf16 v[196:199], a[104:107], a[0:3], v[196:199]\n" \
"s_add_u32 m0, s56, 11328\n" \
"ds_read_b64_tr_b16 a[76:77], v11 offset:33792\n" \
"v_alignbyte_b32 v38, v39, v38, 2\n" \
"v_mfma_f32_16x16x32_bf16 v[196:199], a[108:111], a[4:7], v[196:199]\n" \
"buffer_load_dwordx4 v27, s[72:75], 0 offen offset:3072 sc1 lds\n" \
"v_mfma_f32_16x16x32_bf16 v[204:207], a[104:107], a[8:11], v[204:207]\n" \
"ds_read_b64_tr_b16 a[78:79], v12 offset:33792\n" \
"v_alignbyte_b32 v39, v40, v39, 2\n" \
"v_mfma_f32_16x16x32_bf16 v[204:207], a[108:111], a[12:15], v[204:207]\n" \
"v_alignbyte_b32 v40, v41, v40, 2\n" \
"v_alignbyte_b32 v41, v52, v41, 2\n" \
"v_mfma_f32_16x16x32_bf16 v[212:215], a[104:107], a[16:19], v[212:215]\n" \
"s_add_u32 m0, s56, 0\n" \
"ds_read_b64_tr_b16 a[80:81], v9 offset:33280\n" \
"v_mfma_f32_16x16x32_bf16 v[212:215], a[108:111], a[20:23], v[212:215]\n" \
"buffer_load_dwordx4 v23, s[68:71], 0 offen offset:0 lds\n" \
"v_mfma_f32_16x16x32_bf16 v[220:223], a[104:107], a[24:27], v[220:223]\n" \
"ds_read_b64_tr_b16 a[82:83], v10 offset:33280\n" \
"v_mfma_f32_16x16x32_bf16 v[220:223], a[108:111], a[28:31], v[220:223]\n" \
"ds_write_b128 v55, v[38:41] offset:7232\n" \
"v_alignbyte_b32 v42, v43, v42, 2\n" \
"v_mfma_f32_16x16x32_bf16 v[228:231], a[104:107], a[32:35], v[228:231]\n" \
"s_add_u32 m0, s56, 1024\n" \
"ds_read_b64_tr_b16 a[84:85], v9 offset:34304\n" \
"v_alignbyte_b32 v43, v44, v43, 2\n" \
"v_mfma_f32_16x16x32_bf16 v[228:231], a[108:111], a[36:39], v[228:231]\n" \
"buffer_load_dwordx4 v24, s[68:71], 0 offen offset:0 lds\n" \
"v_mfma_f32_16x16x32_bf16 v[236:239], a[104:107], a[40:43], v[236:239]\n" \
"ds_read_b64_tr_b16 a[86:87], v10 offset:34304\n" \
"v_alignbyte_b32 v44, v45, v44, 2\n" \
"v_mfma_f32_16x16x32_bf16 v[236:239], a[108:111], a[44:47], v[236:239]\n" \
"v_alignbyte_b32 v45, v53, v45, 2\n" \
"ds_write_b128 v55, v[42:45] offset:8256\n" \
"v_mfma_f32_16x16x32_bf16 v[244:247], a[104:107], a[48:51], v[244:247]\n" \
"s_add_u32 m0, s56, 1024\n" \
"ds_read_b64_tr_b16 a[88:89], v11 offset:33280\n" \
"v_alignbyte_b32 v46, v47, v46, 2\n" \
"v_mfma_f32_16x16x32_bf16 v[244:247], a[108:111], a[52:55], v[244:247]\n" \
"buffer_load_dwordx4 v24, s[68:71], 0 offen offset:1024 lds\n" \
"v_mfma_f32_16x16x32_bf16 v[252:255], a[104:107], a[56:59], v[252:255]\n" \
"ds_read_b64_tr_b16 a[90:91], v12 offset:33280\n" \
"v_alignbyte_b32 v47, v48, v47, 2\n" \
"v_mfma_f32_16x16x32_bf16 v[252:255], a[108:111], a[60:63], v[252:255]\n" \
"v_alignbyte_b32 v48, v49, v48, 2\n" \
"v_alignbyte_b32 v49, v54, v49, 2\n" \
"v_mfma_f32_16x16x32_bf16 a[128:131], a[112:115], a[0:3], a[128:131]\n" \
"s_add_u32 m0, s56, 1024\n" \
"ds_read_b64_tr_b16 a[92:93], v11 offset:34304\n" \
"v_mfma_f32_16x16x32_bf16 a[128:131], a[116:119], a[4:7], a[128:131]\n" \
"buffer_load_dwordx4 v24, s[68:71], 0 offen offset:2048 lds\n" \
"v_mfma_f32_16x16x32_bf16 a[136:139], a[112:115], a[8:11], a[136:139]\n" \
"ds_read_b64_tr_b16 a[94:95], v12 offset:34304\n" \
"v_mfma_f32_16x16x32_bf16 a[136:139], a[116:119], a[12:15], a[136:139]\n" \
"ds_write_b128 v55, v[46:49] offset:9280\n" \
"v_lshrrev_b32 v30, 4, v4\n" \
"v_mfma_f32_16x16x32_bf16 a[144:147], a[112:115], a[16:19], a[144:147]\n" \
"s_add_u32 m0, s56, 1024\n" \
"v_and_b32 v31, 15, v30\n" \
"v_lshrrev_b32 v32, 4, v30\n" \
"v_mfma_f32_16x16x32_bf16 a[144:147], a[116:119], a[20:23], a[144:147]\n" \
"buffer_load_dwordx4 v24, s[68:71], 0 offen offset:3072 lds\n" \
"v_mfma_f32_16x16x32_bf16 a[152:155], a[112:115], a[24:27], a[152:155]\n" \
"v_add_u32 v33, 1, v31\n" \
"v_and_b32 v34, 1, v33\n" \
"v_mfma_f32_16x16x32_bf16 a[152:155], a[116:119], a[28:31], a[152:155]\n" \
"v_and_b32 v33, 30, v33\n" \
"v_mul_u32_u24 v34, 5184, v34\n" \
"v_mfma_f32_16x16x32_bf16 a[160:163], a[112:115], a[32:35], a[160:163]\n" \
"s_mov_b32 s46, 0\n" \
"s_add_u32 m0, s50, 65536\n" \
"v_lshl_add_u32 v33, v33, 1, v34\n" \
"v_mfma_f32_16x16x32_bf16 a[160:163], a[116:119], a[36:39], a[160:163]\n" \
"buffer_load_dwordx4 v13, s[28:31], s46 offen lds\n" \
"v_mfma_f32_16x16x32_bf16 a[168:171], a[112:115], a[40:43], a[168:171]\n" \
"v_lshl_add_u32 v33, v32, 4, v33\n" \
"v_add_u32 v46, s56, v33\n" \
"v_mfma_f32_16x16x32_bf16 a[168:171], a[116:119], a[44:47], a[168:171]\n" \
"s_waitcnt vmcnt(13) lgkmcnt(0)\n" \
"v_add_u32 v47, 1024, v46\n" \
"v_add_u32 v48, 2048, v46\n" \
"v_mfma_f32_16x16x32_bf16 a[176:179], a[112:115], a[48:51], a[176:179]\n" \
"s_add_u32 m0, s50, 69632\n" \
"s_barrier\n" \
"v_add_u32 v49, 3072, v46\n" \
"v_mfma_f32_16x16x32_bf16 a[176:179], a[116:119], a[52:55], a[176:179]\n" \
"buffer_load_dwordx4 v14, s[28:31], s46 offen lds\n" \
"v_mfma_f32_16x16x32_bf16 a[184:187], a[112:115], a[56:59], a[184:187]\n" \
"v_lshlrev_b32 v33, 1, v31\n" \
"v_sub_u32 v33, v33, v32\n" \
"v_mfma_f32_16x16x32_bf16 a[184:187], a[116:119], a[60:63], a[184:187]\n" \
"v_lshlrev_b32 v33, 4, v33\n" \
"v_add_u32 v33, 10320, v33\n" \
"v_mfma_f32_16x16x32_bf16 a[132:135], a[120:123], a[0:3], a[132:135]\n" \
"s_add_u32 m0, s50, 73728\n" \
"v_add_u32 v50, s56, v33\n" \
"v_mfma_f32_16x16x32_bf16 a[132:135], a[124:127], a[4:7], a[132:135]\n" \
"buffer_load_dwordx4 v15, s[28:31], s46 offen lds\n" \
"v_mfma_f32_16x16x32_bf16 a[140:143], a[120:123], a[8:11], a[140:143]\n" \
"ds_read_b128 a[0:3], v5 offset:0\n" \
"v_lshlrev_b32 v33, 5, v31\n" \
"v_mfma_f32_16x16x32_bf16 a[140:143], a[124:127], a[12:15], a[140:143]\n" \
"ds_read_b128 a[4:7], v6 offset:0\n" \
"v_lshrrev_b32 v55, 2, v30\n" \
"v_mfma_f32_16x16x32_bf16 a[148:151], a[120:123], a[16:19], a[148:151]\n" \
"ds_read_b128 a[8:11], v5 offset:2048\n" \
"v_and_b32 v55, 8, v55\n" \
"v_mfma_f32_16x16x32_bf16 a[148:151], a[124:127], a[20:23], a[148:151]\n" \
"ds_read_b128 a[12:15], v6 offset:2048\n" \
"v_xor_b32 v55, v30, v55\n" \
"v_mfma_f32_16x16x32_bf16 a[156:159], a[120:123], a[24:27], a[156:159]\n" \
"ds_read_b128 a[16:19], v5 offset:4096\n" \
"v_lshl_add_u32 v55, v55, 3, s22\n" \
"v_mfma_f32_16x16x32_bf16 a[156:159], a[124:127], a[28:31], a[156:159]\n" \
"ds_read_b128 a[20:23], v6 offset:4096\n" \
"v_add_u32 v33, s56, v33\n" \
"v_mfma_f32_16x16x32_bf16 a[164:167], a[120:123], a[32:35], a[164:167]\n" \
"ds_read_b128 a[24:27], v5 offset:6144\n" \
"v_xor_b32 v32, 1, v32\n" \
"v_mfma_f32_16x16x32_bf16 a[164:167], a[124:127], a[36:39], a[164:167]\n" \
"ds_read_b128 a[28:31], v6 offset:6144\n" \
"v_lshl_add_u32 v51, v32, 3, v33\n" \
"v_mfma_f32_16x16x32_bf16 a[172:175], a[120:123], a[40:43], a[172:175]\n" \
"ds_read_b128 a[32:35], v5 offset:8192\n" \
"v_mfma_f32_16x16x32_bf16 a[172:175], a[124:127], a[44:47], a[172:175]\n" \
"ds_read_b128 a[36:39], v6 offset:8192\n" \
"v_mfma_f32_16x16x32_bf16 a[180:183], a[120:123], a[48:51], a[180:183]\n" \
"ds_read_b128 a[40:43], v5 offset:10240\n" \
"v_mfma_f32_16x16x32_bf16 a[180:183], a[124:127], a[52:55], a[180:183]\n" \
"ds_read_b128 a[44:47], v6 offset:10240\n" \
"v_mfma_f32_16x16x32_bf16 a[188:191], a[120:123], a[56:59], a[188:191]\n" \
"ds_read_b128 a[48:51], v5 offset:12288\n" \
"v_mfma_f32_16x16x32_bf16 a[188:191], a[124:127], a[60:63], a[188:191]\n" \
"s_waitcnt lgkmcnt(9)\n" \
"ds_read_b128 a[52:55], v6 offset:12288\n" \
"v_mfma_f32_16x16x32_bf16 v[64:67], a[64:67], a[0:3], v[64:67]\n" \
"s_add_u32 m0, s50, 77824\n" \
"ds_read_b128 a[56:59], v5 offset:14336\n" \
"v_mfma_f32_16x16x32_bf16 v[64:67], a[68:71], a[4:7], v[64:67]\n" \
"buffer_load_dwordx4 v16, s[28:31], s46 offen lds\n" \
"v_mfma_f32_16x16x32_bf16 v[72:75], a[64:67], a[8:11], v[72:75]\n" \
"s_waitcnt lgkmcnt(7)\n" \
"ds_read_b128 a[60:63], v6 offset:14336\n" \
"v_mfma_f32_16x16x32_bf16 v[72:75], a[68:71], a[12:15], v[72:75]\n" \
"v_mfma_f32_16x16x32_bf16 v[80:83], a[64:67], a[16:19], v[80:83]\n" \
"s_mov_b32 s46, 0\n" \
"s_add_u32 m0, s50, 81920\n" \
"ds_read_b64_tr_b16 a[96:97], v9 offset:49152\n" \
"v_mfma_f32_16x16x32_bf16 v[80:83], a[68:71], a[20:23], v[80:83]\n" \
"buffer_load_dwordx4 v17, s[28:31], s46 offen lds\n" \
"v_mfma_f32_16x16x32_bf16 v[88:91], a[64:67], a[24:27], v[88:91]\n" \
"s_waitcnt lgkmcnt(5)\n" \
"ds_read_b64_tr_b16 a[98:99], v10 offset:49152\n" \
"v_mfma_f32_16x16x32_bf16 v[88:91], a[68:71], a[28:31], v[88:91]\n" \
"v_mfma_f32_16x16x32_bf16 v[96:99], a[64:67], a[32:35], v[96:99]\n" \
"s_add_u32 m0, s50, 86016\n" \
"ds_read_b64_tr_b16 a[100:101], v9 offset:50176\n" \
"v_mfma_f32_16x16x32_bf16 v[96:99], a[68:71], a[36:39], v[96:99]\n" \
"buffer_load_dwordx4 v18, s[28:31], s46 offen lds\n" \
"v_mfma_f32_16x16x32_bf16 v[104:107], a[64:67], a[40:43], v[104:107]\n" \
"s_waitcnt lgkmcnt(3)\n" \
"ds_read_b64_tr_b16 a[102:103], v10 offset:50176\n" \
"v_mfma_f32_16x16x32_bf16 v[104:107], a[68:71], a[44:47], v[104:107]\n" \
"v_mfma_f32_16x16x32_bf16 v[112:115], a[64:67], a[48:51], v[112:115]\n" \
"s_add_u32 m0, s50, 90112\n" \
"ds_read_b64_tr_b16 a[104:105], v11 offset:49152\n" \
"v_mfma_f32_16x16x32_bf16 v[112:115], a[68:71], a[52:55], v[112:115]\n" \
"buffer_load_dwordx4 v19, s[28:31], s46 offen lds\n" \
"v_mfma_f32_16x16x32_bf16 v[120:123], a[64:67], a[56:59], v[120:123]\n" \
"ds_read_b64_tr_b16 a[106:107], v12 offset:49152\n" \
"v_mfma_f32_16x16x32_bf16 v[120:123], a[68:71], a[60:63], v[120:123]\n" \
"v_mfma_f32_16x16x32_bf16 v[68:71], a[72:75], a[0:3], v[68:71]\n" \
"s_add_u32 m0, s50, 94208\n" \
"ds_read_b64_tr_b16 a[108:109], v11 offset:50176\n" \
"v_mfma_f32_16x16x32_bf16 v[68:71], a[76:79], a[4:7], v[68:71]\n" \
"buffer_load_dwordx4 v20, s[28:31], s46 offen lds\n" \
"v_mfma_f32_16x16x32_bf16 v[76:79], a[72:75], a[8:11], v[76:79]\n" \
"ds_read_b64_tr_b16 a[110:111], v12 offset:50176\n" \
"v_mfma_f32_16x16x32_bf16 v[76:79], a[76:79], a[12:15], v[76:79]\n" \
"v_mfma_f32_16x16x32_bf16 v[84:87], a[72:75], a[16:19], v[84:87]\n" \
"s_mov_b32 s47, 0\n" \
"s_add_u32 m0, s51, 32768\n" \
"ds_read_b64_tr_b16 a[112:113], v9 offset:49664\n" \
"v_mfma_f32_16x16x32_bf16 v[84:87], a[76:79], a[20:23], v[84:87]\n" \
"buffer_load_dwordx4 v21, s[32:35], s47 offen offset:0 lds\n" \
"v_mfma_f32_16x16x32_bf16 v[92:95], a[72:75], a[24:27], v[92:95]\n" \
"ds_read_b64_tr_b16 a[114:115], v10 offset:49664\n" \
"v_mfma_f32_16x16x32_bf16 v[92:95], a[76:79], a[28:31], v[92:95]\n" \
"v_mfma_f32_16x16x32_bf16 v[100:103], a[72:75], a[32:35], v[100:103]\n" \
"s_add_u32 m0, s51, 33664\n" \
"ds_read_b64_tr_b16 a[116:117], v9 offset:50688\n" \
"v_mfma_f32_16x16x32_bf16 v[100:103], a[76:79], a[36:39], v[100:103]\n" \
"buffer_load_dwordx4 v21, s[32:35], s47 offen offset:128 lds\n" \
"v_mfma_f32_16x16x32_bf16 v[108:111], a[72:75], a[40:43], v[108:111]\n" \
"ds_read_b64_tr_b16 a[118:119], v10 offset:50688\n" \
"v_mfma_f32_16x16x32_bf16 v[108:111], a[76:79], a[44:47], v[108:111]\n" \
"v_mfma_f32_16x16x32_bf16 v[116:119], a[72:75], a[48:51], v[116:119]\n" \
"s_mov_b32 s47, 262144\n" \
"s_add_u32 m0, s51, 40960\n" \
"ds_read_b64_tr_b16 a[120:121], v11 offset:49664\n" \
"v_mfma_f32_16x16x32_bf16 v[116:119], a[76:79], a[52:55], v[116:119]\n" \
"buffer_load_dwordx4 v21, s[32:35], s47 offen offset:0 lds\n" \
"v_mfma_f32_16x16x32_bf16 v[124:127], a[72:75], a[56:59], v[124:127]\n" \
"ds_read_b64_tr_b16 a[122:123], v12 offset:49664\n" \
"v_mfma_f32_16x16x32_bf16 v[124:127], a[76:79], a[60:63], v[124:127]\n" \
"v_mfma_f32_16x16x32_bf16 v[128:131], a[80:83], a[0:3], v[128:131]\n" \
"s_add_u32 m0, s51, 41856\n" \
"ds_read_b64_tr_b16 a[124:125], v11 offset:50688\n" \
"v_mfma_f32_16x16x32_bf16 v[128:131], a[84:87], a[4:7], v[128:131]\n" \
"buffer_load_dwordx4 v21, s[32:35], s47 offen offset:128 lds\n" \
"v_mfma_f32_16x16x32_bf16 v[136:139], a[80:83], a[8:11], v[136:139]\n" \
"s_mov_b32 s96, s57\n" \
"ds_read_b64_tr_b16 a[126:127], v12 offset:50688\n" \
"v_mul_f32 v64, s20, v64\n" \
"v_mfma_f32_16x16x32_bf16 v[136:139], a[84:87], a[12:15], v[136:139]\n" \
"v_mul_f32 v65, s20, v65\n" \
"v_mul_f32 v66, s20, v66\n" \
"v_mfma_f32_16x16x32_bf16 v[144:147], a[80:83], a[16:19], v[144:147]\n" \
"v_mul_f32 v67, s20, v67\n" \
"v_cvt_pk_bf16_f32 v30, v64, v65\n" \
"v_mfma_f32_16x16x32_bf16 v[144:147], a[84:87], a[20:23], v[144:147]\n" \
"v_cvt_pk_bf16_f32 v31, v66, v67\n" \
"v_mfma_f32_16x16x32_bf16 v[152:155], a[80:83], a[24:27], v[152:155]\n" \
"v_mul_f32 v68, s20, v68\n" \
"v_mul_f32 v69, s20, v69\n" \
"v_mfma_f32_16x16x32_bf16 v[152:155], a[84:87], a[28:31], v[152:155]\n" \
"v_mul_f32 v70, s20, v70\n" \
"v_mul_f32 v71, s20, v71\n" \
"v_mfma_f32_16x16x32_bf16 v[160:163], a[80:83], a[32:35], v[160:163]\n" \
"v_cvt_pk_bf16_f32 v32, v68, v69\n" \
"v_mfma_f32_16x16x32_bf16 v[160:163], a[84:87], a[36:39], v[160:163]\n" \
"v_cvt_pk_bf16_f32 v33, v70, v71\n" \
"v_mul_f32 v72, s20, v72\n" \
"v_mfma_f32_16x16x32_bf16 v[168:171], a[80:83], a[40:43], v[168:171]\n" \
"buffer_store_dwordx4 v[30:33], v4, s[36:39], s96 offen offset:0 sc1\n" \
"v_mfma_f32_16x16x32_bf16 v[168:171], a[84:87], a[44:47], v[168:171]\n" \
"s_waitcnt lgkmcnt(0)\n" \
"s_add_u32 s97, s96, s98\n" \
"v_mul_f32 v73, s20, v73\n" \
"v_mfma_f32_16x16x32_bf16 v[176:179], a[80:83], a[48:51], v[176:179]\n" \
"s_barrier\n" \
"v_mul_f32 v74, s20, v74\n" \
"v_mul_f32 v75, s20, v75\n" \
"v_mfma_f32_16x16x32_bf16 v[176:179], a[84:87], a[52:55], v[176:179]\n" \
"v_cvt_pk_bf16_f32 v34, v72, v73\n" \
"v_cvt_pk_bf16_f32 v35, v74, v75\n" \
"v_mfma_f32_16x16x32_bf16 v[184:187], a[80:83], a[56:59], v[184:187]\n" \
"v_mul_f32 v76, s20, v76\n" \
"v_mfma_f32_16x16x32_bf16 v[184:187], a[84:87], a[60:63], v[184:187]\n" \
"v_add_u32 v5, s54, v5\n" \
"v_mul_f32 v77, s20, v77\n" \
"v_mfma_f32_16x16x32_bf16 v[132:135], a[88:91], a[0:3], v[132:135]\n" \
"v_mul_f32 v78, s20, v78\n" \
"v_mul_f32 v79, s20, v79\n" \
"v_mfma_f32_16x16x32_bf16 v[132:135], a[92:95], a[4:7], v[132:135]\n" \
"v_cvt_pk_bf16_f32 v36, v76, v77\n" \
"v_mfma_f32_16x16x32_bf16 v[140:143], a[88:91], a[8:11], v[140:143]\n" \
"v_add_u32 v6, s54, v6\n" \
"v_cvt_pk_bf16_f32 v37, v78, v79\n" \
"v_mfma_f32_16x16x32_bf16 v[140:143], a[92:95], a[12:15], v[140:143]\n" \
"v_add_u32 v7, s54, v7\n" \
"v_mul_f32 v80, s20, v80\n" \
"v_mfma_f32_16x16x32_bf16 v[148:151], a[88:91], a[16:19], v[148:151]\n" \
"v_mul_f32 v81, s20, v81\n" \
"v_mfma_f32_16x16x32_bf16 v[148:151], a[92:95], a[20:23], v[148:151]\n" \
"v_mul_f32 v82, s20, v82\n" \
"v_mul_f32 v83, s20, v83\n" \
"v_mfma_f32_16x16x32_bf16 v[156:159], a[88:91], a[24:27], v[156:159]\n" \
"v_add_u32 v8, s54, v8\n" \
"v_cvt_pk_bf16_f32 v38, v80, v81\n" \
"v_mfma_f32_16x16x32_bf16 v[156:159], a[92:95], a[28:31], v[156:159]\n" \
"v_sub_u32 v9, v9, v62\n" \
"v_mfma_f32_16x16x32_bf16 v[164:167], a[88:91], a[32:35], v[164:167]\n" \
"buffer_store_dwordx4 v[34:37], v4, s[36:39], s97 offen offset:0 sc1\n" \
"v_mfma_f32_16x16x32_bf16 v[164:167], a[92:95], a[36:39], v[164:167]\n" \
"s_add_u32 s96, s96, s92\n" \
"v_cvt_pk_bf16_f32 v39, v82, v83\n" \
"v_mul_f32 v84, s20, v84\n" \
"v_mfma_f32_16x16x32_bf16 v[172:175], a[88:91], a[40:43], v[172:175]\n" \
"v_sub_u32 v10, v10, v62\n" \
"v_mul_f32 v85, s20, v85\n" \
"v_mfma_f32_16x16x32_bf16 v[172:175], a[92:95], a[44:47], v[172:175]\n" \
"v_sub_u32 v11, v11, v62\n" \
"v_mfma_f32_16x16x32_bf16 v[180:183], a[88:91], a[48:51], v[180:183]\n" \
"v_mul_f32 v86, s20, v86\n" \
"v_mul_f32 v87, s20, v87\n" \
"v_mfma_f32_16x16x32_bf16 v[180:183], a[92:95], a[52:55], v[180:183]\n" \
"v_cvt_pk_bf16_f32 v40, v84, v85\n" \
"v_cvt_pk_bf16_f32 v41, v86, v87\n" \
"v_mfma_f32_16x16x32_bf16 v[188:191], a[88:91], a[56:59], v[188:191]\n" \
"v_sub_u32 v12, v12, v62\n" \
"v_mfma_f32_16x16x32_bf16 v[188:191], a[92:95], a[60:63], v[188:191]\n" \
"v_mul_f32 v88, s20, v88\n" \
"v_mul_f32 v89, s20, v89\n" \
"v_mfma_f32_16x16x32_bf16 v[192:195], a[96:99], a[0:3], v[192:195]\n" \
"s_mov_b32 s46, 128\n" \
"s_add_u32 m0, s50, 0\n" \
"s_waitcnt vmcnt(14) lgkmcnt(0)\n" \
"v_mfma_f32_16x16x32_bf16 v[192:195], a[100:103], a[4:7], v[192:195]\n" \
"buffer_load_dwordx4 v13, s[28:31], s46 offen lds\n" \
"v_mfma_f32_16x16x32_bf16 v[200:203], a[96:99], a[8:11], v[200:203]\n" \
"ds_read_b128 a[200:203], v50 offset:512\n" \
"v_mul_f32 v90, s20, v90\n" \
"v_mfma_f32_16x16x32_bf16 v[200:203], a[100:103], a[12:15], v[200:203]\n" \
"v_mul_f32 v91, s20, v91\n" \
"v_cvt_pk_bf16_f32 v42, v88, v89\n" \
"v_mfma_f32_16x16x32_bf16 v[208:211], a[96:99], a[16:19], v[208:211]\n" \
"ds_read2_b32 a[64:65], v46 offset0:0 offset1:1\n" \
"v_cvt_pk_bf16_f32 v43, v90, v91\n" \
"v_mfma_f32_16x16x32_bf16 v[208:211], a[100:103], a[20:23], v[208:211]\n" \
"v_mul_f32 v92, s20, v92\n" \
"v_mul_f32 v93, s20, v93\n" \
"v_mfma_f32_16x16x32_bf16 v[216:219], a[96:99], a[24:27], v[216:219]\n" \
"ds_read2_b32 a[66:67], v46 offset0:2 offset1:3\n" \
"v_mul_f32 v94, s20, v94\n" \
"v_mfma_f32_16x16x32_bf16 v[216:219], a[100:103], a[28:31], v[216:219]\n" \
"LH3_%=:\ns_cbranch_execz LH11_%=\n" \
"buffer_store_dwordx4 v[38:41], v4, s[36:39], s96 offen offset:0 sc1\n" \
"v_mfma_f32_16x16x32_bf16 v[224:227], a[96:99], a[32:35], v[224:227]\n" \
"s_add_u32 m0, s50, 4096\n" \
"ds_read_b128 a[204:207], v50 offset:1024\n" \
"s_add_u32 s97, s96, s98\n" \
"v_mfma_f32_16x16x32_bf16 v[224:227], a[100:103], a[36:39], v[224:227]\n" \
"buffer_load_dwordx4 v14, s[28:31], s46 offen lds\n" \
"v_mfma_f32_16x16x32_bf16 v[232:235], a[96:99], a[40:43], v[232:235]\n" \
"ds_read2_b32 a[68:69], v46 offset0:128 offset1:129\n" \
"v_mul_f32 v95, s20, v95\n" \
"v_mfma_f32_16x16x32_bf16 v[232:235], a[100:103], a[44:47], v[232:235]\n" \
"v_cvt_pk_bf16_f32 v44, v92, v93\n" \
"v_cvt_pk_bf16_f32 v45, v94, v95\n" \
"v_mfma_f32_16x16x32_bf16 v[240:243], a[96:99], a[48:51], v[240:243]\n" \
"ds_read2_b32 a[70:71], v46 offset0:130 offset1:131\n" \
"v_mul_f32 v96, s20, v96\n" \
"v_mfma_f32_16x16x32_bf16 v[240:243], a[100:103], a[52:55], v[240:243]\n" \
"v_mul_f32 v97, s20, v97\n" \
"v_mul_f32 v98, s20, v98\n" \
"v_mfma_f32_16x16x32_bf16 v[248:251], a[96:99], a[56:59], v[248:251]\n" \
"ds_read_b128 a[208:211], v50 offset:1536\n" \
"v_mul_f32 v99, s20, v99\n" \
"v_mfma_f32_16x16x32_bf16 v[248:251], a[100:103], a[60:63], v[248:251]\n" \
"v_cvt_pk_bf16_f32 v30, v96, v97\n" \
"v_cvt_pk_bf16_f32 v31, v98, v99\n" \
"v_mfma_f32_16x16x32_bf16 v[196:199], a[104:107], a[0:3], v[196:199]\n" \
"s_add_u32 m0, s50, 8192\n" \
"ds_read2_b32 a[72:73], v47 offset0:0 offset1:1\n" \
"v_mul_f32 v100, s20, v100\n" \
"v_mfma_f32_16x16x32_bf16 v[196:199], a[108:111], a[4:7], v[196:199]\n" \
"buffer_load_dwordx4 v15, s[28:31], s46 offen lds\n" \
"v_mfma_f32_16x16x32_bf16 v[204:207], a[104:107], a[8:11], v[204:207]\n" \
"ds_read2_b32 a[74:75], v47 offset0:2 offset1:3\n" \
"v_mul_f32 v101, s20, v101\n" \
"v_mfma_f32_16x16x32_bf16 v[204:207], a[108:111], a[12:15], v[204:207]\n" \
"v_mul_f32 v102, s20, v102\n" \
"v_mul_f32 v103, s20, v103\n" \
"v_mfma_f32_16x16x32_bf16 v[212:215], a[104:107], a[16:19], v[212:215]\n" \
"ds_read_b128 a[212:215], v50 offset:2048\n" \
"v_cvt_pk_bf16_f32 v32, v100, v101\n" \
"v_mfma_f32_16x16x32_bf16 v[212:215], a[108:111], a[20:23], v[212:215]\n" \
"buffer_store_dwordx4 v[42:45], v4, s[36:39], s97 offen offset:0 sc1\n" \
"v_mfma_f32_16x16x32_bf16 v[220:223], a[104:107], a[24:27], v[220:223]\n" \
"s_add_u32 s96, s96, s92\n" \
"ds_read2_b32 a[76:77], v47 offset0:128 offset1:129\n" \
"v_cvt_pk_bf16_f32 v33, v102, v103\n" \
"v_mfma_f32_16x16x32_bf16 v[220:223], a[108:111], a[28:31], v[220:223]\n" \
"v_mul_f32 v104, s20, v104\n" \
"v_mul_f32 v105, s20, v105\n" \
"v_mfma_f32_16x16x32_bf16 v[228:231], a[104:107], a[32:35], v[228:231]\n" \
"s_add_u32 m0, s50, 12288\n" \
"ds_read2_b32 a[78:79], v47 offset0:130 offset1:131\n" \
"v_mul_f32 v106, s20, v106\n" \
"v_mfma_f32_16x16x32_bf16 v[228:231], a[108:111], a[36:39], v[228:231]\n" \
"buffer_load_dwordx4 v16, s[28:31], s46 offen lds\n" \
"v_mfma_f32_16x16x32_bf16 v[236:239], a[104:107], a[40:43], v[236:239]\n" \
"ds_read_b128 a[216:219], v50 offset:2560\n" \
"v_mul_f32 v107, s20, v107\n" \
"v_mfma_f32_16x16x32_bf16 v[236:239], a[108:111], a[44:47], v[236:239]\n" \
"v_cvt_pk_bf16_f32 v34, v104, v105\n" \
"v_cvt_pk_bf16_f32 v35, v106, v107\n" \
"v_mfma_f32_16x16x32_bf16 v[244:247], a[104:107], a[48:51], v[244:247]\n" \
"ds_read2_b32 a[80:81], v48 offset0:0 offset1:1\n" \
"v_mul_f32 v108, s20, v108\n" \
"v_mfma_f32_16x16x32_bf16 v[244:247], a[108:111], a[52:55], v[244:247]\n" \
"v_mul_f32 v109, s20, v109\n" \
"v_mul_f32 v110, s20, v110\n" \
"v_mfma_f32_16x16x32_bf16 v[252:255], a[104:107], a[56:59], v[252:255]\n" \
"ds_read2_b32 a[82:83], v48 offset0:2 offset1:3\n" \
"v_mul_f32 v111, s20, v111\n" \
"v_mfma_f32_16x16x32_bf16 v[252:255], a[108:111], a[60:63], v[252:255]\n" \
"v_cvt_pk_bf16_f32 v36, v108, v109\n" \
"v_cvt_pk_bf16_f32 v37, v110, v111\n" \
"v_mfma_f32_16x16x32_bf16 a[128:131], a[112:115], a[0:3], a[128:131]\n" \
"s_mov_b32 s46, 128\n" \
"s_add_u32 m0, s50, 16384\n" \
"ds_read_b128 a[220:223], v50 offset:3072\n" \
"v_mfma_f32_16x16x32_bf16 a[128:131], a[116:119], a[4:7], a[128:131]\n" \
"buffer_load_dwordx4 v17, s[28:31], s46 offen lds\n" \
"v_mfma_f32_16x16x32_bf16 a[136:139], a[112:115], a[8:11], a[136:139]\n" \
"ds_read2_b32 a[84:85], v48 offset0:128 offset1:129\n" \
"v_mul_f32 v112, s20, v112\n" \
"v_mfma_f32_16x16x32_bf16 a[136:139], a[116:119], a[12:15], a[136:139]\n" \
"buffer_store_dwordx4 v[30:33], v4, s[36:39], s96 offen offset:0 sc1\n" \
"v_mfma_f32_16x16x32_bf16 a[144:147], a[112:115], a[16:19], a[144:147]\n" \
"s_add_u32 s97, s96, s98\n" \
"ds_read2_b32 a[86:87], v48 offset0:130 offset1:131\n" \
"v_mul_f32 v113, s20, v113\n" \
"v_mfma_f32_16x16x32_bf16 a[144:147], a[116:119], a[20:23], a[144:147]\n" \
"v_mul_f32 v114, s20, v114\n" \
"v_mul_f32 v115, s20, v115\n" \
"v_mfma_f32_16x16x32_bf16 a[152:155], a[112:115], a[24:27], a[152:155]\n" \
"ds_read_b128 a[224:227], v50 offset:3584\n" \
"v_cvt_pk_bf16_f32 v38, v112, v113\n" \
"v_mfma_f32_16x16x32_bf16 a[152:155], a[116:119], a[28:31], a[152:155]\n" \
"v_cvt_pk_bf16_f32 v39, v114, v115\n" \
"v_mul_f32 v116, s20, v116\n" \
"v_mfma_f32_16x16x32_bf16 a[160:163], a[112:115], a[32:35], a[160:163]\n" \
"s_add_u32 m0, s50, 20480\n" \
"ds_read2_b32 a[88:89], v49 offset0:0 offset1:1\n" \
"v_mul_f32 v117, s20, v117\n" \
"v_mfma_f32_16x16x32_bf16 a[160:163], a[116:119], a[36:39], a[160:163]\n" \
"buffer_load_dwordx4 v18, s[28:31], s46 offen lds\n" \
"v_mfma_f32_16x16x32_bf16 a[168:171], a[112:115], a[40:43], a[168:171]\n" \
"ds_read2_b32 a[90:91], v49 offset0:2 offset1:3\n" \
"v_mul_f32 v118, s20, v118\n" \
"v_mfma_f32_16x16x32_bf16 a[168:171], a[116:119], a[44:47], a[168:171]\n" \
"v_mul_f32 v119, s20, v119\n" \
"v_cvt_pk_bf16_f32 v40, v116, v117\n" \
"v_mfma_f32_16x16x32_bf16 a[176:179], a[112:115], a[48:51], a[176:179]\n" \
"ds_read_b128 a[228:231], v50 offset:4096\n" \
"v_cvt_pk_bf16_f32 v41, v118, v119\n" \
"v_mfma_f32_16x16x32_bf16 a[176:179], a[116:119], a[52:55], a[176:179]\n" \
"v_mul_f32 v120, s20, v120\n" \
"v_mul_f32 v121, s20, v121\n" \
"v_mfma_f32_16x16x32_bf16 a[184:187], a[112:115], a[56:59], a[184:187]\n" \
"ds_read2_b32 a[92:93], v49 offset0:128 offset1:129\n" \
"v_mul_f32 v122, s20, v122\n" \
"v_mfma_f32_16x16x32_bf16 a[184:187], a[116:119], a[60:63], a[184:187]\n" \
"v_mul_f32 v123, s20, v123\n" \
"v_cvt_pk_bf16_f32 v42, v120, v121\n" \
"v_mfma_f32_16x16x32_bf16 a[132:135], a[120:123], a[0:3], a[132:135]\n" \
"s_add_u32 m0, s50, 24576\n" \
"ds_read2_b32 a[94:95], v49 offset0:130 offset1:131\n" \
"v_cvt_pk_bf16_f32 v43, v122, v123\n" \
"v_mfma_f32_16x16x32_bf16 a[132:135], a[124:127], a[4:7], a[132:135]\n" \
"buffer_load_dwordx4 v19, s[28:31], s46 offen lds\n" \
"v_mfma_f32_16x16x32_bf16 a[140:143], a[120:123], a[8:11], a[140:143]\n" \
"v_mul_f32 v124, s20, v124\n" \
"v_mul_f32 v125, s20, v125\n" \
"v_mfma_f32_16x16x32_bf16 a[140:143], a[124:127], a[12:15], a[140:143]\n" \
"buffer_store_dwordx4 v[34:37], v4, s[36:39], s97 offen offset:0 sc1\n" \
"v_mfma_f32_16x16x32_bf16 a[148:151], a[120:123], a[16:19], a[148:151]\n" \
"s_add_u32 s96, s96, s92\n" \
"v_mul_f32 v126, s20, v126\n" \
"v_mul_f32 v127, s20, v127\n" \
"v_mfma_f32_16x16x32_bf16 a[148:151], a[124:127], a[20:23], a[148:151]\n" \
"v_cvt_pk_bf16_f32 v44, v124, v125\n" \
"v_cvt_pk_bf16_f32 v45, v126, v127\n" \
"v_mfma_f32_16x16x32_bf16 a[156:159], a[120:123], a[24:27], a[156:159]\n" \
"v_mul_f32 v128, s20, v128\n" \
"v_mfma_f32_16x16x32_bf16 a[156:159], a[124:127], a[28:31], a[156:159]\n" \
"v_mul_f32 v129, s20, v129\n" \
"v_mul_f32 v130, s20, v130\n" \
"v_mfma_f32_16x16x32_bf16 a[164:167], a[120:123], a[32:35], a[164:167]\n" \
"s_add_u32 m0, s50, 28672\n" \
"v_mul_f32 v131, s20, v131\n" \
"v_cvt_pk_bf16_f32 v30, v128, v129\n" \
"v_mfma_f32_16x16x32_bf16 a[164:167], a[124:127], a[36:39], a[164:167]\n" \
"buffer_load_dwordx4 v20, s[28:31], s46 offen lds\n" \
"v_mfma_f32_16x16x32_bf16 a[172:175], a[120:123], a[40:43], a[172:175]\n" \
"v_cvt_pk_bf16_f32 v31, v130, v131\n" \
"v_mul_f32 v132, s20, v132\n" \
"v_mfma_f32_16x16x32_bf16 a[172:175], a[124:127], a[44:47], a[172:175]\n" \
"v_mul_f32 v133, s20, v133\n" \
"v_mul_f32 v134, s20, v134\n" \
"v_mfma_f32_16x16x32_bf16 a[180:183], a[120:123], a[48:51], a[180:183]\n" \
"v_mul_f32 v135, s20, v135\n" \
"v_mfma_f32_16x16x32_bf16 a[180:183], a[124:127], a[52:55], a[180:183]\n" \
"v_cvt_pk_bf16_f32 v32, v132, v133\n" \
"v_cvt_pk_bf16_f32 v33, v134, v135\n" \
"v_mfma_f32_16x16x32_bf16 a[188:191], a[120:123], a[56:59], a[188:191]\n" \
"s_waitcnt lgkmcnt(0)\n" \
"v_mul_f32 v136, s20, v136\n" \
"v_mul_f32 v137, s20, v137\n" \
"v_mfma_f32_16x16x32_bf16 a[188:191], a[124:127], a[60:63], a[188:191]\n" \
"v_mul_f32 v138, s20, v138\n" \
"s_branch LC_%=\n" \
"LAV_%=:\n" \
"v_mfma_f32_16x16x32_bf16 v[64:67], a[0:3], a[64:67], 0\n" \
"s_mov_b32 s47, 524288\n" \
"s_add_u32 m0, s22, 49024\n" \
"v_mfma_f32_16x16x32_bf16 v[64:67], a[4:7], a[68:71], v[64:67]\n" \
"buffer_load_dwordx4 v21, s[32:35], s47 offen offset:128 nt lds\n" \
"v_mfma_f32_16x16x32_bf16 v[68:71], a[8:11], a[64:67], 0\n" \
"s_add_u32 s14, s14, 1\ns_addc_u32 s15, s15, 0\n" \
"v_mfma_f32_16x16x32_bf16 v[68:71], a[12:15], a[68:71], v[68:71]\n" \
"v_mfma_f32_16x16x32_bf16 v[80:83], a[16:19], a[64:67], 0\n" \
"s_add_u32 m0, s22, 50816\n" \
"ds_read_b64_tr_b16 a[96:97], v9 offset:16384\n" \
"v_mfma_f32_16x16x32_bf16 v[80:83], a[20:23], a[68:71], v[80:83]\n" \
"buffer_load_dwordx4 v21, s[32:35], s47 offen offset:384 nt lds\n" \
"v_mfma_f32_16x16x32_bf16 v[84:87], a[24:27], a[64:67], 0\n" \
"ds_read_b64_tr_b16 a[98:99], v10 offset:16384\n" \
"v_mfma_f32_16x16x32_bf16 v[84:87], a[28:31], a[68:71], v[84:87]\n" \
"v_mfma_f32_16x16x32_bf16 v[96:99], a[32:35], a[64:67], 0\n" \
"s_mov_b32 s47, 786432\n" \
"s_add_u32 m0, s22, 50048\n" \
"ds_read_b64_tr_b16 a[100:101], v9 offset:17408\n" \
"v_mfma_f32_16x16x32_bf16 v[96:99], a[36:39], a[68:71], v[96:99]\n" \
"buffer_load_dwordx4 v21, s[32:35], s47 offen offset:128 nt lds\n" \
"v_mfma_f32_16x16x32_bf16 v[100:103], a[40:43], a[64:67], 0\n" \
"ds_read_b64_tr_b16 a[102:103], v10 offset:17408\n" \
"v_mfma_f32_16x16x32_bf16 v[100:103], a[44:47], a[68:71], v[100:103]\n" \
"v_mfma_f32_16x16x32_bf16 v[112:115], a[48:51], a[64:67], 0\n" \
"s_add_u32 m0, s22, 51840\n" \
"ds_read_b64_tr_b16 a[104:105], v11 offset:16384\n" \
"v_mfma_f32_16x16x32_bf16 v[112:115], a[52:55], a[68:71], v[112:115]\n" \
"buffer_load_dwordx4 v21, s[32:35], s47 offen offset:384 nt lds\n" \
"v_mfma_f32_16x16x32_bf16 v[116:119], a[56:59], a[64:67], 0\n" \
"ds_read_b64_tr_b16 a[106:107], v12 offset:16384\n" \
"v_mfma_f32_16x16x32_bf16 v[116:119], a[60:63], a[68:71], v[116:119]\n" \
"v_mfma_f32_16x16x32_bf16 v[72:75], a[0:3], a[72:75], 0\n" \
"s_mov_b32 s46, 256\n" \
"s_add_u32 m0, s50, 32768\n" \
"ds_read_b64_tr_b16 a[108:109], v11 offset:17408\n" \
"v_mfma_f32_16x16x32_bf16 v[72:75], a[4:7], a[76:79], v[72:75]\n" \
"buffer_load_dwordx4 v13, s[28:31], s46 offen lds\n" \
"v_mfma_f32_16x16x32_bf16 v[76:79], a[8:11], a[72:75], 0\n" \
"ds_read_b64_tr_b16 a[110:111], v12 offset:17408\n" \
"v_mfma_f32_16x16x32_bf16 v[76:79], a[12:15], a[76:79], v[76:79]\n" \
"v_mfma_f32_16x16x32_bf16 v[88:91], a[16:19], a[72:75], 0\n" \
"s_add_u32 m0, s50, 36864\n" \
"ds_read_b64_tr_b16 a[112:113], v9 offset:16896\n" \
"v_mfma_f32_16x16x32_bf16 v[88:91], a[20:23], a[76:79], v[88:91]\n" \
"buffer_load_dwordx4 v14, s[28:31], s46 offen lds\n" \
"v_mfma_f32_16x16x32_bf16 v[92:95], a[24:27], a[72:75], 0\n" \
"ds_read_b64_tr_b16 a[114:115], v10 offset:16896\n" \
"v_mfma_f32_16x16x32_bf16 v[92:95], a[28:31], a[76:79], v[92:95]\n" \
"v_mfma_f32_16x16x32_bf16 v[104:107], a[32:35], a[72:75], 0\n" \
"s_add_u32 m0, s50, 40960\n" \
"ds_read_b64_tr_b16 a[116:117], v9 offset:17920\n" \
"v_mfma_f32_16x16x32_bf16 v[104:107], a[36:39], a[76:79], v[104:107]\n" \
"buffer_load_dwordx4 v15, s[28:31], s46 offen lds\n" \
"v_mfma_f32_16x16x32_bf16 v[108:111], a[40:43], a[72:75], 0\n" \
"ds_read_b64_tr_b16 a[118:119], v10 offset:17920\n" \
"v_mfma_f32_16x16x32_bf16 v[108:111], a[44:47], a[76:79], v[108:111]\n" \
"v_mfma_f32_16x16x32_bf16 v[120:123], a[48:51], a[72:75], 0\n" \
"s_add_u32 m0, s50, 45056\n" \
"ds_read_b64_tr_b16 a[120:121], v11 offset:16896\n" \
"v_mfma_f32_16x16x32_bf16 v[120:123], a[52:55], a[76:79], v[120:123]\n" \
"buffer_load_dwordx4 v16, s[28:31], s46 offen lds\n" \
"v_mfma_f32_16x16x32_bf16 v[124:127], a[56:59], a[72:75], 0\n" \
"ds_read_b64_tr_b16 a[122:123], v12 offset:16896\n" \
"v_mfma_f32_16x16x32_bf16 v[124:127], a[60:63], a[76:79], v[124:127]\n" \
"v_mfma_f32_16x16x32_bf16 v[128:131], a[0:3], a[80:83], 0\n" \
"s_mov_b32 s46, 256\n" \
"s_add_u32 m0, s50, 49152\n" \
"ds_read_b64_tr_b16 a[124:125], v11 offset:17920\n" \
"v_mfma_f32_16x16x32_bf16 v[128:131], a[4:7], a[84:87], v[128:131]\n" \
"buffer_load_dwordx4 v17, s[28:31], s46 offen lds\n" \
"v_mfma_f32_16x16x32_bf16 v[132:135], a[8:11], a[80:83], 0\n" \
"ds_read_b64_tr_b16 a[126:127], v12 offset:17920\n" \
"v_mfma_f32_16x16x32_bf16 v[132:135], a[12:15], a[84:87], v[132:135]\n" \
"v_mfma_f32_16x16x32_bf16 v[144:147], a[16:19], a[80:83], 0\n" \
"s_add_u32 m0, s50, 53248\n" \
"v_mfma_f32_16x16x32_bf16 v[144:147], a[20:23], a[84:87], v[144:147]\n" \
"buffer_load_dwordx4 v18, s[28:31], s46 offen lds\n" \
"v_mfma_f32_16x16x32_bf16 v[148:151], a[24:27], a[80:83], 0\n" \
"v_mfma_f32_16x16x32_bf16 v[148:151], a[28:31], a[84:87], v[148:151]\n" \
"v_mfma_f32_16x16x32_bf16 v[160:163], a[32:35], a[80:83], 0\n" \
"s_add_u32 m0, s50, 57344\n" \
"v_mfma_f32_16x16x32_bf16 v[160:163], a[36:39], a[84:87], v[160:163]\n" \
"buffer_load_dwordx4 v19, s[28:31], s46 offen lds\n" \
"v_mfma_f32_16x16x32_bf16 v[164:167], a[40:43], a[80:83], 0\n" \
"v_mfma_f32_16x16x32_bf16 v[164:167], a[44:47], a[84:87], v[164:167]\n" \
"s_waitcnt vmcnt(21) lgkmcnt(0)\n" \
"v_mfma_f32_16x16x32_bf16 v[176:179], a[48:51], a[80:83], 0\n" \
"s_add_u32 m0, s50, 61440\n" \
"s_barrier\n" \
"v_mfma_f32_16x16x32_bf16 v[176:179], a[52:55], a[84:87], v[176:179]\n" \
"buffer_load_dwordx4 v20, s[28:31], s46 offen lds\n" \
"v_mfma_f32_16x16x32_bf16 v[180:183], a[56:59], a[80:83], 0\n" \
"v_mfma_f32_16x16x32_bf16 v[180:183], a[60:63], a[84:87], v[180:183]\n" \
"v_mfma_f32_16x16x32_bf16 v[136:139], a[0:3], a[88:91], 0\n" \
"s_mov_b32 s47, 1048576\n" \
"s_add_u32 m0, s22, 0\n" \
"v_mfma_f32_16x16x32_bf16 v[136:139], a[4:7], a[92:95], v[136:139]\n" \
"buffer_load_dwordx4 v21, s[32:35], s47 offen offset:0 lds\n" \
"v_mfma_f32_16x16x32_bf16 v[140:143], a[8:11], a[88:91], 0\n" \
"v_mfma_f32_16x16x32_bf16 v[140:143], a[12:15], a[92:95], v[140:143]\n" \
"v_mfma_f32_16x16x32_bf16 v[152:155], a[16:19], a[88:91], 0\n" \
"s_add_u32 m0, s22, 1792\n" \
"v_mfma_f32_16x16x32_bf16 v[152:155], a[20:23], a[92:95], v[152:155]\n" \
"buffer_load_dwordx4 v21, s[32:35], s47 offen offset:256 lds\n" \
"v_mfma_f32_16x16x32_bf16 v[156:159], a[24:27], a[88:91], 0\n" \
"v_mfma_f32_16x16x32_bf16 v[156:159], a[28:31], a[92:95], v[156:159]\n" \
"v_mfma_f32_16x16x32_bf16 v[168:171], a[32:35], a[88:91], 0\n" \
"s_mov_b32 s47, 1310720\n" \
"s_add_u32 m0, s22, 1024\n" \
"v_mfma_f32_16x16x32_bf16 v[168:171], a[36:39], a[92:95], v[168:171]\n" \
"buffer_load_dwordx4 v21, s[32:35], s47 offen offset:0 lds\n" \
"v_mfma_f32_16x16x32_bf16 v[172:175], a[40:43], a[88:91], 0\n" \
"v_mfma_f32_16x16x32_bf16 v[172:175], a[44:47], a[92:95], v[172:175]\n" \
"v_mfma_f32_16x16x32_bf16 v[184:187], a[48:51], a[88:91], 0\n" \
"s_add_u32 m0, s22, 2816\n" \
"v_mfma_f32_16x16x32_bf16 v[184:187], a[52:55], a[92:95], v[184:187]\n" \
"buffer_load_dwordx4 v21, s[32:35], s47 offen offset:256 lds\n" \
"v_mfma_f32_16x16x32_bf16 v[188:191], a[56:59], a[88:91], 0\n" \
"v_mfma_f32_16x16x32_bf16 v[188:191], a[60:63], a[92:95], v[188:191]\n" \
"v_mfma_f32_16x16x32_bf16 v[192:195], a[0:3], a[96:99], 0\n" \
"s_mov_b32 s47, 1048576\n" \
"s_add_u32 m0, s22, 16256\n" \
"v_mfma_f32_16x16x32_bf16 v[192:195], a[4:7], a[100:103], v[192:195]\n" \
"buffer_load_dwordx4 v21, s[32:35], s47 offen offset:128 lds\n" \
"v_mfma_f32_16x16x32_bf16 v[196:199], a[8:11], a[96:99], 0\n" \
"v_mfma_f32_16x16x32_bf16 v[196:199], a[12:15], a[100:103], v[196:199]\n" \
"v_mfma_f32_16x16x32_bf16 v[208:211], a[16:19], a[96:99], 0\n" \
"s_mov_b32 s46, 384\n" \
"s_add_u32 m0, s50, 65536\n" \
"ds_read_b64_tr_b16 a[64:65], v9 offset:32768\n" \
"v_mfma_f32_16x16x32_bf16 v[208:211], a[20:23], a[100:103], v[208:211]\n" \
"buffer_load_dwordx4 v13, s[28:31], s46 offen lds\n" \
"v_mfma_f32_16x16x32_bf16 v[212:215], a[24:27], a[96:99], 0\n" \
"ds_read_b64_tr_b16 a[66:67], v10 offset:32768\n" \
"v_mfma_f32_16x16x32_bf16 v[212:215], a[28:31], a[100:103], v[212:215]\n" \
"v_mfma_f32_16x16x32_bf16 v[224:227], a[32:35], a[96:99], 0\n" \
"s_add_u32 m0, s22, 18048\n" \
"ds_read_b64_tr_b16 a[68:69], v9 offset:33792\n" \
"v_mfma_f32_16x16x32_bf16 v[224:227], a[36:39], a[100:103], v[224:227]\n" \
"buffer_load_dwordx4 v21, s[32:35], s47 offen offset:384 lds\n" \
"v_mfma_f32_16x16x32_bf16 v[228:231], a[40:43], a[96:99], 0\n" \
"ds_read_b64_tr_b16 a[70:71], v10 offset:33792\n" \
"v_mfma_f32_16x16x32_bf16 v[228:231], a[44:47], a[100:103], v[228:231]\n" \
"v_mfma_f32_16x16x32_bf16 v[240:243], a[48:51], a[96:99], 0\n" \
"s_add_u32 m0, s50, 69632\n" \
"ds_read_b64_tr_b16 a[72:73], v11 offset:32768\n" \
"v_mfma_f32_16x16x32_bf16 v[240:243], a[52:55], a[100:103], v[240:243]\n" \
"buffer_load_dwordx4 v14, s[28:31], s46 offen lds\n" \
"v_mfma_f32_16x16x32_bf16 v[244:247], a[56:59], a[96:99], 0\n" \
"ds_read_b64_tr_b16 a[74:75], v12 offset:32768\n" \
"v_mfma_f32_16x16x32_bf16 v[244:247], a[60:63], a[100:103], v[244:247]\n" \
"v_mfma_f32_16x16x32_bf16 v[200:203], a[0:3], a[104:107], 0\n" \
"s_mov_b32 s47, 1310720\n" \
"s_add_u32 m0, s22, 17280\n" \
"ds_read_b64_tr_b16 a[76:77], v11 offset:33792\n" \
"v_mfma_f32_16x16x32_bf16 v[200:203], a[4:7], a[108:111], v[200:203]\n" \
"buffer_load_dwordx4 v21, s[32:35], s47 offen offset:128 lds\n" \
"v_mfma_f32_16x16x32_bf16 v[204:207], a[8:11], a[104:107], 0\n" \
"ds_read_b64_tr_b16 a[78:79], v12 offset:33792\n" \
"v_mfma_f32_16x16x32_bf16 v[204:207], a[12:15], a[108:111], v[204:207]\n" \
"v_mfma_f32_16x16x32_bf16 v[216:219], a[16:19], a[104:107], 0\n" \
"s_add_u32 m0, s50, 73728\n" \
"ds_read_b64_tr_b16 a[80:81], v9 offset:33280\n" \
"v_mfma_f32_16x16x32_bf16 v[216:219], a[20:23], a[108:111], v[216:219]\n" \
"buffer_load_dwordx4 v15, s[28:31], s46 offen lds\n" \
"v_mfma_f32_16x16x32_bf16 v[220:223], a[24:27], a[104:107], 0\n" \
"ds_read_b64_tr_b16 a[82:83], v10 offset:33280\n" \
"v_mfma_f32_16x16x32_bf16 v[220:223], a[28:31], a[108:111], v[220:223]\n" \
"v_mfma_f32_16x16x32_bf16 v[232:235], a[32:35], a[104:107], 0\n" \
"s_add_u32 m0, s22, 19072\n" \
"ds_read_b64_tr_b16 a[84:85], v9 offset:34304\n" \
"v_mfma_f32_16x16x32_bf16 v[232:235], a[36:39], a[108:111], v[232:235]\n" \
"buffer_load_dwordx4 v21, s[32:35], s47 offen offset:384 lds\n" \
"v_mfma_f32_16x16x32_bf16 v[236:239], a[40:43], a[104:107], 0\n" \
"ds_read_b64_tr_b16 a[86:87], v10 offset:34304\n" \
"v_mfma_f32_16x16x32_bf16 v[236:239], a[44:47], a[108:111], v[236:239]\n" \
"v_mfma_f32_16x16x32_bf16 v[248:251], a[48:51], a[104:107], 0\n" \
"s_add_u32 m0, s50, 77824\n" \
"ds_read_b64_tr_b16 a[88:89], v11 offset:33280\n" \
"v_mfma_f32_16x16x32_bf16 v[248:251], a[52:55], a[108:111], v[248:251]\n" \
"buffer_load_dwordx4 v16, s[28:31], s46 offen lds\n" \
"v_mfma_f32_16x16x32_bf16 v[252:255], a[56:59], a[104:107], 0\n" \
"ds_read_b64_tr_b16 a[90:91], v12 offset:33280\n" \
"v_mfma_f32_16x16x32_bf16 v[252:255], a[60:63], a[108:111], v[252:255]\n" \
"v_mfma_f32_16x16x32_bf16 a[128:131], a[0:3], a[112:115], 0\n" \
"ds_read_b64_tr_b16 a[92:93], v11 offset:34304\n" \
"v_mfma_f32_16x16x32_bf16 a[128:131], a[4:7], a[116:119], a[128:131]\n" \
"global_load_dwordx4 a[230:233], v27, s[70:71] offset:0\n" \
"v_mfma_f32_16x16x32_bf16 a[132:135], a[8:11], a[112:115], 0\n" \
"ds_read_b64_tr_b16 a[94:95], v12 offset:34304\n" \
"v_mfma_f32_16x16x32_bf16 a[132:135], a[12:15], a[116:119], a[132:135]\n" \
"v_mfma_f32_16x16x32_bf16 a[144:147], a[16:19], a[112:115], 0\n" \
"v_mfma_f32_16x16x32_bf16 a[144:147], a[20:23], a[116:119], a[144:147]\n" \
"global_load_dwordx4 a[234:237], v27, s[70:71] offset:16\n" \
"v_mfma_f32_16x16x32_bf16 a[148:151], a[24:27], a[112:115], 0\n" \
"v_mfma_f32_16x16x32_bf16 a[148:151], a[28:31], a[116:119], a[148:151]\n" \
"v_mfma_f32_16x16x32_bf16 a[160:163], a[32:35], a[112:115], 0\n" \
"v_mfma_f32_16x16x32_bf16 a[160:163], a[36:39], a[116:119], a[160:163]\n" \
"buffer_load_dwordx2 a[192:193], v54, s[40:43], 0 offen offset:0 nt\n" \
"v_mfma_f32_16x16x32_bf16 a[164:167], a[40:43], a[112:115], 0\n" \
"v_mfma_f32_16x16x32_bf16 a[164:167], a[44:47], a[116:119], a[164:167]\n" \
"s_waitcnt vmcnt(23) lgkmcnt(0)\n" \
"v_mfma_f32_16x16x32_bf16 a[176:179], a[48:51], a[112:115], 0\n" \
"s_barrier\n" \
"v_mfma_f32_16x16x32_bf16 a[176:179], a[52:55], a[116:119], a[176:179]\n" \
"buffer_load_dwordx2 a[194:195], v54, s[40:43], 0 offen offset:2048 nt\n" \
"v_mfma_f32_16x16x32_bf16 a[180:183], a[56:59], a[112:115], 0\n" \
"v_mfma_f32_16x16x32_bf16 a[180:183], a[60:63], a[116:119], a[180:183]\n" \
"v_mfma_f32_16x16x32_bf16 a[136:139], a[0:3], a[120:123], 0\n" \
"v_mfma_f32_16x16x32_bf16 a[136:139], a[4:7], a[124:127], a[136:139]\n" \
"buffer_load_dwordx2 a[196:197], v55, s[40:43], 0 offen offset:0 nt\n" \
"v_mfma_f32_16x16x32_bf16 a[140:143], a[8:11], a[120:123], 0\n" \
"s_add_u32 s84, s84, 1048576\ns_addc_u32 s85, s85, 0\n" \
"ds_read_b128 a[0:3], v5 offset:0\n" \
"v_mfma_f32_16x16x32_bf16 a[140:143], a[12:15], a[124:127], a[140:143]\n" \
"s_mov_b32 s52, 0\n" \
"ds_read_b128 a[4:7], v6 offset:0\n" \
"v_mfma_f32_16x16x32_bf16 a[152:155], a[16:19], a[120:123], 0\n" \
"ds_read_b128 a[8:11], v5 offset:2048\n" \
"v_mfma_f32_16x16x32_bf16 a[152:155], a[20:23], a[124:127], a[152:155]\n" \
"ds_read_b128 a[12:15], v6 offset:2048\n" \
"v_mfma_f32_16x16x32_bf16 a[156:159], a[24:27], a[120:123], 0\n" \
"ds_read_b128 a[16:19], v5 offset:4096\n" \
"v_mfma_f32_16x16x32_bf16 a[156:159], a[28:31], a[124:127], a[156:159]\n" \
"ds_read_b128 a[20:23], v6 offset:4096\n" \
"v_mfma_f32_16x16x32_bf16 a[168:171], a[32:35], a[120:123], 0\n" \
"ds_read_b128 a[24:27], v5 offset:6144\n" \
"v_mfma_f32_16x16x32_bf16 a[168:171], a[36:39], a[124:127], a[168:171]\n" \
"ds_read_b128 a[28:31], v6 offset:6144\n" \
"v_mfma_f32_16x16x32_bf16 a[172:175], a[40:43], a[120:123], 0\n" \
"ds_read_b128 a[32:35], v5 offset:8192\n" \
"v_mfma_f32_16x16x32_bf16 a[172:175], a[44:47], a[124:127], a[172:175]\n" \
"ds_read_b128 a[36:39], v6 offset:8192\n" \
"v_mfma_f32_16x16x32_bf16 a[184:187], a[48:51], a[120:123], 0\n" \
"ds_read_b128 a[40:43], v5 offset:10240\n" \
"v_mfma_f32_16x16x32_bf16 a[184:187], a[52:55], a[124:127], a[184:187]\n" \
"ds_read_b128 a[44:47], v6 offset:10240\n" \
"v_mfma_f32_16x16x32_bf16 a[188:191], a[56:59], a[120:123], 0\n" \
"ds_read_b128 a[48:51], v5 offset:12288\n" \
"v_mfma_f32_16x16x32_bf16 a[188:191], a[60:63], a[124:127], a[188:191]\n" \
"s_waitcnt lgkmcnt(9)\n" \
"ds_read_b128 a[52:55], v6 offset:12288\n" \
"v_mfma_f32_16x16x32_bf16 v[64:67], a[0:3], a[64:67], v[64:67]\n" \
"s_mov_b32 s47, 1572864\n" \
"s_add_u32 m0, s22, 32768\n" \
"ds_read_b128 a[56:59], v5 offset:14336\n" \
"v_mfma_f32_16x16x32_bf16 v[64:67], a[4:7], a[68:71], v[64:67]\n" \
"buffer_load_dwordx4 v21, s[32:35], s47 offen offset:0 lds\n" \
"v_mfma_f32_16x16x32_bf16 v[68:71], a[8:11], a[64:67], v[68:71]\n" \
"s_waitcnt lgkmcnt(7)\n" \
"ds_read_b128 a[60:63], v6 offset:14336\n" \
"v_mfma_f32_16x16x32_bf16 v[68:71], a[12:15], a[68:71], v[68:71]\n" \
"v_mfma_f32_16x16x32_bf16 v[80:83], a[16:19], a[64:67], v[80:83]\n" \
"ds_read_b64_tr_b16 a[96:97], v9 offset:49152\n" \
"v_mfma_f32_16x16x32_bf16 v[80:83], a[20:23], a[68:71], v[80:83]\n" \
"s_mov_b64 exec, 0xffff\nglobal_load_dword v22, v1, s[84:85]\ns_mov_b64 exec, -1\n" \
"v_mfma_f32_16x16x32_bf16 v[84:87], a[24:27], a[64:67], v[84:87]\n" \
"s_waitcnt lgkmcnt(5)\n" \
"ds_read_b64_tr_b16 a[98:99], v10 offset:49152\n" \
"v_mfma_f32_16x16x32_bf16 v[84:87], a[28:31], a[68:71], v[84:87]\n" \
"v_mfma_f32_16x16x32_bf16 v[96:99], a[32:35], a[64:67], v[96:99]\n" \
"s_mov_b32 s46, 384\n" \
"s_add_u32 m0, s50, 81920\n" \
"ds_read_b64_tr_b16 a[100:101], v9 offset:50176\n" \
"v_mfma_f32_16x16x32_bf16 v[96:99], a[36:39], a[68:71], v[96:99]\n" \
"buffer_load_dwordx4 v17, s[28:31], s46 offen lds\n" \
"v_mfma_f32_16x16x32_bf16 v[100:103], a[40:43], a[64:67], v[100:103]\n" \
"s_waitcnt lgkmcnt(3)\n" \
"ds_read_b64_tr_b16 a[102:103], v10 offset:50176\n" \
"v_mfma_f32_16x16x32_bf16 v[100:103], a[44:47], a[68:71], v[100:103]\n" \
"v_mfma_f32_16x16x32_bf16 v[112:115], a[48:51], a[64:67], v[112:115]\n" \
"ds_read_b64_tr_b16 a[104:105], v11 offset:49152\n" \
"v_mfma_f32_16x16x32_bf16 v[112:115], a[52:55], a[68:71], v[112:115]\n" \
"buffer_load_dwordx2 a[198:199], v59, s[40:43], s52 offen offset:0 nt\n" \
"v_mfma_f32_16x16x32_bf16 v[116:119], a[56:59], a[64:67], v[116:119]\n" \
"ds_read_b64_tr_b16 a[106:107], v12 offset:49152\n" \
"v_mfma_f32_16x16x32_bf16 v[116:119], a[60:63], a[68:71], v[116:119]\n" \
"v_mfma_f32_16x16x32_bf16 v[72:75], a[0:3], a[72:75], v[72:75]\n" \
"s_add_u32 m0, s22, 34560\n" \
"ds_read_b64_tr_b16 a[108:109], v11 offset:50176\n" \
"v_mfma_f32_16x16x32_bf16 v[72:75], a[4:7], a[76:79], v[72:75]\n" \
"buffer_load_dwordx4 v21, s[32:35], s47 offen offset:256 lds\n" \
"v_mfma_f32_16x16x32_bf16 v[76:79], a[8:11], a[72:75], v[76:79]\n" \
"ds_read_b64_tr_b16 a[110:111], v12 offset:50176\n" \
"v_mfma_f32_16x16x32_bf16 v[76:79], a[12:15], a[76:79], v[76:79]\n" \
"v_mfma_f32_16x16x32_bf16 v[88:91], a[16:19], a[72:75], v[88:91]\n" \
"ds_read_b64_tr_b16 a[112:113], v9 offset:49664\n" \
"v_mfma_f32_16x16x32_bf16 v[88:91], a[20:23], a[76:79], v[88:91]\n" \
"buffer_load_dwordx2 a[214:215], v59, s[80:83], s52 offen offset:0 nt\n" \
"v_mfma_f32_16x16x32_bf16 v[92:95], a[24:27], a[72:75], v[92:95]\n" \
"ds_read_b64_tr_b16 a[114:115], v10 offset:49664\n" \
"v_mfma_f32_16x16x32_bf16 v[92:95], a[28:31], a[76:79], v[92:95]\n" \
"v_mfma_f32_16x16x32_bf16 v[104:107], a[32:35], a[72:75], v[104:107]\n" \
"s_add_u32 m0, s50, 86016\n" \
"ds_read_b64_tr_b16 a[116:117], v9 offset:50688\n" \
"v_mfma_f32_16x16x32_bf16 v[104:107], a[36:39], a[76:79], v[104:107]\n" \
"buffer_load_dwordx4 v18, s[28:31], s46 offen lds\n" \
"v_mfma_f32_16x16x32_bf16 v[108:111], a[40:43], a[72:75], v[108:111]\n" \
"ds_read_b64_tr_b16 a[118:119], v10 offset:50688\n" \
"v_mfma_f32_16x16x32_bf16 v[108:111], a[44:47], a[76:79], v[108:111]\n" \
"v_mfma_f32_16x16x32_bf16 v[120:123], a[48:51], a[72:75], v[120:123]\n" \
"ds_read_b64_tr_b16 a[120:121], v11 offset:49664\n" \
"v_mfma_f32_16x16x32_bf16 v[120:123], a[52:55], a[76:79], v[120:123]\n" \
"buffer_load_dwordx2 a[200:201], v59, s[40:43], s52 offen offset:2048 nt\n" \
"v_mfma_f32_16x16x32_bf16 v[124:127], a[56:59], a[72:75], v[124:127]\n" \
"ds_read_b64_tr_b16 a[122:123], v12 offset:49664\n" \
"v_mfma_f32_16x16x32_bf16 v[124:127], a[60:63], a[76:79], v[124:127]\n" \
"v_mfma_f32_16x16x32_bf16 v[128:131], a[0:3], a[80:83], v[128:131]\n" \
"s_mov_b32 s47, 1835008\n" \
"s_add_u32 m0, s22, 33792\n" \
"ds_read_b64_tr_b16 a[124:125], v11 offset:50688\n" \
"v_mfma_f32_16x16x32_bf16 v[128:131], a[4:7], a[84:87], v[128:131]\n" \
"buffer_load_dwordx4 v21, s[32:35], s47 offen offset:0 lds\n" \
"v_mfma_f32_16x16x32_bf16 v[132:135], a[8:11], a[80:83], v[132:135]\n" \
"ds_read_b64_tr_b16 a[126:127], v12 offset:50688\n" \
"v_mfma_f32_16x16x32_bf16 v[132:135], a[12:15], a[84:87], v[132:135]\n" \
"v_mfma_f32_16x16x32_bf16 v[144:147], a[16:19], a[80:83], v[144:147]\n" \
"v_mfma_f32_16x16x32_bf16 v[144:147], a[20:23], a[84:87], v[144:147]\n" \
"buffer_load_dwordx2 a[216:217], v59, s[80:83], s52 offen offset:2048 nt\n" \
"v_mfma_f32_16x16x32_bf16 v[148:151], a[24:27], a[80:83], v[148:151]\n" \
"s_mov_b32 s52, 4096\n" \
"v_mfma_f32_16x16x32_bf16 v[148:151], a[28:31], a[84:87], v[148:151]\n" \
"v_mfma_f32_16x16x32_bf16 v[160:163], a[32:35], a[80:83], v[160:163]\n" \
"s_add_u32 m0, s50, 90112\n" \
"v_mfma_f32_16x16x32_bf16 v[160:163], a[36:39], a[84:87], v[160:163]\n" \
"buffer_load_dwordx4 v19, s[28:31], s46 offen lds\n" \
"v_mfma_f32_16x16x32_bf16 v[164:167], a[40:43], a[80:83], v[164:167]\n" \
"v_mfma_f32_16x16x32_bf16 v[164:167], a[44:47], a[84:87], v[164:167]\n" \
"s_waitcnt vmcnt(24) lgkmcnt(0)\n" \
"v_mfma_f32_16x16x32_bf16 v[176:179], a[48:51], a[80:83], v[176:179]\n" \
"s_barrier\n" \
"v_mfma_f32_16x16x32_bf16 v[176:179], a[52:55], a[84:87], v[176:179]\n" \
"buffer_load_dwordx2 a[202:203], v59, s[40:43], s52 offen offset:0 nt\n" \
"v_mfma_f32_16x16x32_bf16 v[180:183], a[56:59], a[80:83], v[180:183]\n" \
"v_mfma_f32_16x16x32_bf16 v[180:183], a[60:63], a[84:87], v[180:183]\n" \
"v_mfma_f32_16x16x32_bf16 v[136:139], a[0:3], a[88:91], v[136:139]\n" \
"s_add_u32 m0, s22, 35584\n" \
"v_mfma_f32_16x16x32_bf16 v[136:139], a[4:7], a[92:95], v[136:139]\n" \
"buffer_load_dwordx4 v21, s[32:35], s47 offen offset:256 lds\n" \
"v_mfma_f32_16x16x32_bf16 v[140:143], a[8:11], a[88:91], v[140:143]\n" \
"v_mfma_f32_16x16x32_bf16 v[140:143], a[12:15], a[92:95], v[140:143]\n" \
"v_mfma_f32_16x16x32_bf16 v[152:155], a[16:19], a[88:91], v[152:155]\n" \
"v_mfma_f32_16x16x32_bf16 v[152:155], a[20:23], a[92:95], v[152:155]\n" \
"buffer_load_dwordx2 a[218:219], v59, s[80:83], s52 offen offset:0 nt\n" \
"v_mfma_f32_16x16x32_bf16 v[156:159], a[24:27], a[88:91], v[156:159]\n" \
"v_mfma_f32_16x16x32_bf16 v[156:159], a[28:31], a[92:95], v[156:159]\n" \
"v_mfma_f32_16x16x32_bf16 v[168:171], a[32:35], a[88:91], v[168:171]\n" \
"s_add_u32 m0, s50, 94208\n" \
"v_mfma_f32_16x16x32_bf16 v[168:171], a[36:39], a[92:95], v[168:171]\n" \
"buffer_load_dwordx4 v20, s[28:31], s46 offen lds\n" \
"v_mfma_f32_16x16x32_bf16 v[172:175], a[40:43], a[88:91], v[172:175]\n" \
"v_mfma_f32_16x16x32_bf16 v[172:175], a[44:47], a[92:95], v[172:175]\n" \
"v_mfma_f32_16x16x32_bf16 v[184:187], a[48:51], a[88:91], v[184:187]\n" \
"v_mfma_f32_16x16x32_bf16 v[184:187], a[52:55], a[92:95], v[184:187]\n" \
"buffer_load_dwordx2 a[204:205], v59, s[40:43], s52 offen offset:2048 nt\n" \
"v_mfma_f32_16x16x32_bf16 v[188:191], a[56:59], a[88:91], v[188:191]\n" \
"v_mfma_f32_16x16x32_bf16 v[188:191], a[60:63], a[92:95], v[188:191]\n" \
"v_mfma_f32_16x16x32_bf16 v[192:195], a[0:3], a[96:99], v[192:195]\n" \
"s_mov_b32 s47, 1572864\n" \
"s_add_u32 m0, s22, 49024\n" \
"v_mfma_f32_16x16x32_bf16 v[192:195], a[4:7], a[100:103], v[192:195]\n" \
"buffer_load_dwordx4 v21, s[32:35], s47 offen offset:128 lds\n" \
"v_mfma_f32_16x16x32_bf16 v[196:199], a[8:11], a[96:99], v[196:199]\n" \
"v_mfma_f32_16x16x32_bf16 v[196:199], a[12:15], a[100:103], v[196:199]\n" \
"v_mfma_f32_16x16x32_bf16 v[208:211], a[16:19], a[96:99], v[208:211]\n" \
"s_mov_b32 s46, 512\n" \
"s_add_u32 m0, s50, 0\n" \
"ds_read_b64_tr_b16 a[64:65], v9 offset:0\n" \
"v_mfma_f32_16x16x32_bf16 v[208:211], a[20:23], a[100:103], v[208:211]\n" \
"buffer_load_dwordx4 v13, s[28:31], s46 offen lds\n" \
"v_mfma_f32_16x16x32_bf16 v[212:215], a[24:27], a[96:99], v[212:215]\n" \
"LH4_%=:\ns_cbranch_execz LH8_%=\n" \
"ds_read_b64_tr_b16 a[66:67], v10 offset:0\n" \
"v_mfma_f32_16x16x32_bf16 v[212:215], a[28:31], a[100:103], v[212:215]\n" \
"s_waitcnt vmcnt(12)\n" \
"v_accvgpr_read_b32 v43, a230\n" \
"v_lshlrev_b32 v42, 16, v43\n" \
"v_mfma_f32_16x16x32_bf16 v[224:227], a[32:35], a[96:99], v[224:227]\n" \
"ds_read_b64_tr_b16 a[68:69], v9 offset:1024\n" \
"v_and_b32 v43, 0xffff0000, v43\n" \
"v_mfma_f32_16x16x32_bf16 v[224:227], a[36:39], a[100:103], v[224:227]\n" \
"buffer_load_dwordx2 a[220:221], v59, s[80:83], s52 offen offset:2048 nt\n" \
"v_mfma_f32_16x16x32_bf16 v[228:231], a[40:43], a[96:99], v[228:231]\n" \
"s_mov_b32 s52, 8192\n" \
"ds_read_b64_tr_b16 a[70:71], v10 offset:1024\n" \
"v_accvgpr_read_b32 v45, a231\n" \
"v_mfma_f32_16x16x32_bf16 v[228:231], a[44:47], a[100:103], v[228:231]\n" \
"v_lshlrev_b32 v44, 16, v45\n" \
"v_and_b32 v45, 0xffff0000, v45\n" \
"v_mfma_f32_16x16x32_bf16 v[240:243], a[48:51], a[96:99], v[240:243]\n" \
"s_add_u32 m0, s22, 50816\n" \
"ds_read_b64_tr_b16 a[72:73], v11 offset:0\n" \
"v_accvgpr_read_b32 v47, a232\n" \
"v_mfma_f32_16x16x32_bf16 v[240:243], a[52:55], a[100:103], v[240:243]\n" \
"buffer_load_dwordx4 v21, s[32:35], s47 offen offset:384 lds\n" \
"v_mfma_f32_16x16x32_bf16 v[244:247], a[56:59], a[96:99], v[244:247]\n" \
"ds_read_b64_tr_b16 a[74:75], v12 offset:0\n" \
"v_lshlrev_b32 v46, 16, v47\n" \
"v_mfma_f32_16x16x32_bf16 v[244:247], a[60:63], a[100:103], v[244:247]\n" \
"v_and_b32 v47, 0xffff0000, v47\n" \
"v_accvgpr_read_b32 v49, a233\n" \
"v_mfma_f32_16x16x32_bf16 v[200:203], a[0:3], a[104:107], v[200:203]\n" \
"s_add_u32 m0, s50, 4096\n" \
"ds_read_b64_tr_b16 a[76:77], v11 offset:1024\n" \
"v_lshlrev_b32 v48, 16, v49\n" \
"v_mfma_f32_16x16x32_bf16 v[200:203], a[4:7], a[108:111], v[200:203]\n" \
"buffer_load_dwordx4 v14, s[28:31], s46 offen lds\n" \
"v_mfma_f32_16x16x32_bf16 v[204:207], a[8:11], a[104:107], v[204:207]\n" \
"ds_read_b64_tr_b16 a[78:79], v12 offset:1024\n" \
"v_and_b32 v49, 0xffff0000, v49\n" \
"v_mfma_f32_16x16x32_bf16 v[204:207], a[12:15], a[108:111], v[204:207]\n" \
"v_accvgpr_read_b32 v36, a192\n" \
"v_lshlrev_b32 v30, 16, v36\n" \
"v_mfma_f32_16x16x32_bf16 v[216:219], a[16:19], a[104:107], v[216:219]\n" \
"ds_read_b64_tr_b16 a[80:81], v9 offset:512\n" \
"v_and_b32 v36, 0xffff0000, v36\n" \
"v_mfma_f32_16x16x32_bf16 v[216:219], a[20:23], a[108:111], v[216:219]\n" \
"buffer_load_dwordx2 a[206:207], v59, s[40:43], s52 offen offset:0 nt\n" \
"v_mfma_f32_16x16x32_bf16 v[220:223], a[24:27], a[104:107], v[220:223]\n" \
"ds_read_b64_tr_b16 a[82:83], v10 offset:512\n" \
"v_accvgpr_read_b32 v37, a194\n" \
"v_mfma_f32_16x16x32_bf16 v[220:223], a[28:31], a[108:111], v[220:223]\n" \
"v_lshlrev_b32 v31, 16, v37\n" \
"v_and_b32 v37, 0xffff0000, v37\n" \
"v_mfma_f32_16x16x32_bf16 v[232:235], a[32:35], a[104:107], v[232:235]\n" \
"s_mov_b32 s47, 1835008\n" \
"s_add_u32 m0, s22, 50048\n" \
"ds_read_b64_tr_b16 a[84:85], v9 offset:1536\n" \
"v_mfma_f32_16x16x32_bf16 v[232:235], a[36:39], a[108:111], v[232:235]\n" \
"buffer_load_dwordx4 v21, s[32:35], s47 offen offset:128 lds\n" \
"v_mfma_f32_16x16x32_bf16 v[236:239], a[40:43], a[104:107], v[236:239]\n" \
"ds_read_b64_tr_b16 a[86:87], v10 offset:1536\n" \
"v_accvgpr_read_b32 v38, a196\n" \
"v_mfma_f32_16x16x32_bf16 v[236:239], a[44:47], a[108:111], v[236:239]\n" \
"v_lshlrev_b32 v32, 16, v38\n" \
"v_and_b32 v38, 0xffff0000, v38\n" \
"v_mfma_f32_16x16x32_bf16 v[248:251], a[48:51], a[104:107], v[248:251]\n" \
"s_add_u32 m0, s50, 8192\n" \
"ds_read_b64_tr_b16 a[88:89], v11 offset:512\n" \
"v_accvgpr_read_b32 v39, a198\n" \
"v_mfma_f32_16x16x32_bf16 v[248:251], a[52:55], a[108:111], v[248:251]\n" \
"buffer_load_dwordx4 v15, s[28:31], s46 offen lds\n" \
"v_mfma_f32_16x16x32_bf16 v[252:255], a[56:59], a[104:107], v[252:255]\n" \
"ds_read_b64_tr_b16 a[90:91], v12 offset:512\n" \
"v_lshlrev_b32 v33, 16, v39\n" \
"v_mfma_f32_16x16x32_bf16 v[252:255], a[60:63], a[108:111], v[252:255]\n" \
"v_and_b32 v39, 0xffff0000, v39\n" \
"v_mul_f32 v25, v30, v45\n" \
"v_mfma_f32_16x16x32_bf16 a[128:131], a[0:3], a[112:115], a[128:131]\n" \
"ds_read_b64_tr_b16 a[92:93], v11 offset:1536\n" \
"v_mul_f32 v26, v36, v49\n" \
"v_mfma_f32_16x16x32_bf16 a[128:131], a[4:7], a[116:119], a[128:131]\n" \
"buffer_load_dwordx2 a[222:223], v59, s[80:83], s52 offen offset:0 nt\n" \
"v_mfma_f32_16x16x32_bf16 a[132:135], a[8:11], a[112:115], a[132:135]\n" \
"ds_read_b64_tr_b16 a[94:95], v12 offset:1536\n" \
"v_fmac_f32 v25, v31, v44\n" \
"v_mfma_f32_16x16x32_bf16 a[132:135], a[12:15], a[116:119], a[132:135]\n" \
"v_fmac_f32 v26, v37, v48\n" \
"v_fmac_f32 v25, v32, v43\n" \
"v_mfma_f32_16x16x32_bf16 a[144:147], a[16:19], a[112:115], a[144:147]\n" \
"s_add_u32 m0, s22, 51840\n" \
"v_fmac_f32 v26, v38, v47\n" \
"v_fmac_f32 v25, v33, v42\n" \
"v_mfma_f32_16x16x32_bf16 a[144:147], a[20:23], a[116:119], a[144:147]\n" \
"buffer_load_dwordx4 v21, s[32:35], s47 offen offset:384 lds\n" \
"v_mfma_f32_16x16x32_bf16 a[148:151], a[24:27], a[112:115], a[148:151]\n" \
"v_fmac_f32 v26, v39, v46\n" \
"v_accvgpr_read_b32 v28, a214\n" \
"v_mfma_f32_16x16x32_bf16 a[148:151], a[28:31], a[116:119], a[148:151]\n" \
"v_lshlrev_b32 v27, 16, v28\n" \
"v_and_b32 v28, 0xffff0000, v28\n" \
"v_mfma_f32_16x16x32_bf16 a[160:163], a[32:35], a[112:115], a[160:163]\n" \
"v_mul_f32 v29, v25, v27\n" \
"v_mfma_f32_16x16x32_bf16 a[160:163], a[36:39], a[116:119], a[160:163]\n" \
"buffer_load_dwordx2 a[208:209], v59, s[40:43], s52 offen offset:2048 nt\n" \
"v_mfma_f32_16x16x32_bf16 a[164:167], a[40:43], a[112:115], a[164:167]\n" \
"s_waitcnt vmcnt(17)\n" \
"v_mul_f32 v58, v26, v28\n" \
"v_accvgpr_read_b32 v40, a200\n" \
"v_mfma_f32_16x16x32_bf16 a[164:167], a[44:47], a[116:119], a[164:167]\n" \
"s_waitcnt vmcnt(33) lgkmcnt(0)\n" \
"v_lshlrev_b32 v34, 16, v40\n" \
"v_and_b32 v40, 0xffff0000, v40\n" \
"v_mfma_f32_16x16x32_bf16 a[176:179], a[48:51], a[112:115], a[176:179]\n" \
"s_add_u32 m0, s50, 12288\n" \
"s_barrier\n" \
"v_mul_f32 v25, v31, v45\n" \
"v_mfma_f32_16x16x32_bf16 a[176:179], a[52:55], a[116:119], a[176:179]\n" \
"buffer_load_dwordx4 v16, s[28:31], s46 offen lds\n" \
"v_mfma_f32_16x16x32_bf16 a[180:183], a[56:59], a[112:115], a[180:183]\n" \
"v_mul_f32 v26, v37, v49\n" \
"v_fmac_f32 v25, v32, v44\n" \
"v_mfma_f32_16x16x32_bf16 a[180:183], a[60:63], a[116:119], a[180:183]\n" \
"v_fmac_f32 v26, v38, v48\n" \
"v_fmac_f32 v25, v33, v43\n" \
"v_mfma_f32_16x16x32_bf16 a[136:139], a[0:3], a[120:123], a[136:139]\n" \
"v_fmac_f32 v26, v39, v47\n" \
"v_mfma_f32_16x16x32_bf16 a[136:139], a[4:7], a[124:127], a[136:139]\n" \
"buffer_load_dwordx2 a[224:225], v59, s[80:83], s52 offen offset:2048 nt\n" \
"v_mfma_f32_16x16x32_bf16 a[140:143], a[8:11], a[120:123], a[140:143]\n" \
"s_mov_b32 s52, 12288\n" \
"ds_read_b128 a[0:3], v5 offset:32768\n" \
"v_fmac_f32 v25, v34, v42\n" \
"v_mfma_f32_16x16x32_bf16 a[140:143], a[12:15], a[124:127], a[140:143]\n" \
"ds_read_b128 a[4:7], v6 offset:32768\n" \
"v_fmac_f32 v26, v40, v46\n" \
"v_mfma_f32_16x16x32_bf16 a[152:155], a[16:19], a[120:123], a[152:155]\n" \
"ds_read_b128 a[8:11], v5 offset:34816\n" \
"v_accvgpr_read_b32 v28, a216\n" \
"v_mfma_f32_16x16x32_bf16 a[152:155], a[20:23], a[124:127], a[152:155]\n" \
"ds_read_b128 a[12:15], v6 offset:34816\n" \
"v_lshlrev_b32 v27, 16, v28\n" \
"v_mfma_f32_16x16x32_bf16 a[156:159], a[24:27], a[120:123], a[156:159]\n" \
"ds_read_b128 a[16:19], v5 offset:36864\n" \
"v_and_b32 v28, 0xffff0000, v28\n" \
"v_mfma_f32_16x16x32_bf16 a[156:159], a[28:31], a[124:127], a[156:159]\n" \
"ds_read_b128 a[20:23], v6 offset:36864\n" \
"v_mul_f32 v25, v25, v27\n" \
"v_mfma_f32_16x16x32_bf16 a[168:171], a[32:35], a[120:123], a[168:171]\n" \
"ds_read_b128 a[24:27], v5 offset:38912\n" \
"v_mul_f32 v26, v26, v28\n" \
"v_mfma_f32_16x16x32_bf16 a[168:171], a[36:39], a[124:127], a[168:171]\n" \
"ds_read_b128 a[28:31], v6 offset:38912\n" \
"v_cvt_pk_bf16_f32 v53, v25, v29\n" \
"v_mfma_f32_16x16x32_bf16 a[172:175], a[40:43], a[120:123], a[172:175]\n" \
"ds_read_b128 a[32:35], v5 offset:40960\n" \
"v_cvt_pk_bf16_f32 v57, v26, v58\n" \
"v_mfma_f32_16x16x32_bf16 a[172:175], a[44:47], a[124:127], a[172:175]\n" \
"ds_read_b128 a[36:39], v6 offset:40960\n" \
"s_waitcnt vmcnt(15)\n" \
"v_mfma_f32_16x16x32_bf16 a[184:187], a[48:51], a[120:123], a[184:187]\n" \
"ds_read_b128 a[40:43], v5 offset:43008\n" \
"v_accvgpr_read_b32 v41, a202\n" \
"v_mfma_f32_16x16x32_bf16 a[184:187], a[52:55], a[124:127], a[184:187]\n" \
"ds_read_b128 a[44:47], v6 offset:43008\n" \
"v_lshlrev_b32 v35, 16, v41\n" \
"v_mfma_f32_16x16x32_bf16 a[188:191], a[56:59], a[120:123], a[188:191]\n" \
"ds_read_b128 a[48:51], v5 offset:45056\n" \
"v_and_b32 v41, 0xffff0000, v41\n" \
"v_mfma_f32_16x16x32_bf16 a[188:191], a[60:63], a[124:127], a[188:191]\n" \
"s_waitcnt lgkmcnt(9)\n" \
"ds_read_b128 a[52:55], v6 offset:45056\n" \
"v_mul_f32 v25, v32, v45\n" \
"v_mfma_f32_16x16x32_bf16 v[64:67], a[0:3], a[64:67], v[64:67]\n" \
"s_mov_b32 s47, 2097152\n" \
"s_add_u32 m0, s22, 0\n" \
"ds_read_b128 a[56:59], v5 offset:47104\n" \
"v_mfma_f32_16x16x32_bf16 v[64:67], a[4:7], a[68:71], v[64:67]\n" \
"buffer_load_dwordx4 v21, s[32:35], s47 offen offset:0 lds\n" \
"v_mfma_f32_16x16x32_bf16 v[68:71], a[8:11], a[64:67], v[68:71]\n" \
"s_waitcnt lgkmcnt(7)\n" \
"ds_read_b128 a[60:63], v6 offset:47104\n" \
"v_mul_f32 v26, v38, v49\n" \
"v_mfma_f32_16x16x32_bf16 v[68:71], a[12:15], a[68:71], v[68:71]\n" \
"v_fmac_f32 v25, v33, v44\n" \
"v_fmac_f32 v26, v39, v48\n" \
"v_mfma_f32_16x16x32_bf16 v[80:83], a[16:19], a[64:67], v[80:83]\n" \
"ds_read_b64_tr_b16 a[96:97], v9 offset:16384\n" \
"v_fmac_f32 v25, v34, v43\n" \
"v_mfma_f32_16x16x32_bf16 v[80:83], a[20:23], a[68:71], v[80:83]\n" \
"buffer_load_dwordx2 a[210:211], v59, s[40:43], s52 offen offset:0 nt\n" \
"v_mfma_f32_16x16x32_bf16 v[84:87], a[24:27], a[64:67], v[84:87]\n" \
"s_waitcnt lgkmcnt(5)\n" \
"ds_read_b64_tr_b16 a[98:99], v10 offset:16384\n" \
"v_fmac_f32 v26, v40, v47\n" \
"v_mfma_f32_16x16x32_bf16 v[84:87], a[28:31], a[68:71], v[84:87]\n" \
"v_fmac_f32 v25, v35, v42\n" \
"v_fmac_f32 v26, v41, v46\n" \
"v_mfma_f32_16x16x32_bf16 v[96:99], a[32:35], a[64:67], v[96:99]\n" \
"s_mov_b32 s46, 512\n" \
"s_add_u32 m0, s50, 16384\n" \
"ds_read_b64_tr_b16 a[100:101], v9 offset:17408\n" \
"v_mfma_f32_16x16x32_bf16 v[96:99], a[36:39], a[68:71], v[96:99]\n" \
"buffer_load_dwordx4 v17, s[28:31], s46 offen lds\n" \
"v_mfma_f32_16x16x32_bf16 v[100:103], a[40:43], a[64:67], v[100:103]\n" \
"s_waitcnt lgkmcnt(3)\n" \
"ds_read_b64_tr_b16 a[102:103], v10 offset:17408\n" \
"v_accvgpr_read_b32 v28, a218\n" \
"v_mfma_f32_16x16x32_bf16 v[100:103], a[44:47], a[68:71], v[100:103]\n" \
"v_lshlrev_b32 v27, 16, v28\n" \
"v_and_b32 v28, 0xffff0000, v28\n" \
"v_mfma_f32_16x16x32_bf16 v[112:115], a[48:51], a[64:67], v[112:115]\n" \
"ds_read_b64_tr_b16 a[104:105], v11 offset:16384\n" \
"v_mul_f32 v29, v25, v27\n" \
"v_mfma_f32_16x16x32_bf16 v[112:115], a[52:55], a[68:71], v[112:115]\n" \
"buffer_load_dwordx2 a[226:227], v59, s[80:83], s52 offen offset:0 nt\n" \
"v_mfma_f32_16x16x32_bf16 v[116:119], a[56:59], a[64:67], v[116:119]\n" \
"ds_read_b64_tr_b16 a[106:107], v12 offset:16384\n" \
"v_mul_f32 v58, v26, v28\n" \
"v_mfma_f32_16x16x32_bf16 v[116:119], a[60:63], a[68:71], v[116:119]\n" \
"s_waitcnt vmcnt(14)\n" \
"v_accvgpr_read_b32 v36, a204\n" \
"v_lshlrev_b32 v30, 16, v36\n" \
"v_mfma_f32_16x16x32_bf16 v[72:75], a[0:3], a[72:75], v[72:75]\n" \
"s_add_u32 m0, s22, 1792\n" \
"ds_read_b64_tr_b16 a[108:109], v11 offset:17408\n" \
"v_and_b32 v36, 0xffff0000, v36\n" \
"v_mfma_f32_16x16x32_bf16 v[72:75], a[4:7], a[76:79], v[72:75]\n" \
"buffer_load_dwordx4 v21, s[32:35], s47 offen offset:256 lds\n" \
"v_mfma_f32_16x16x32_bf16 v[76:79], a[8:11], a[72:75], v[76:79]\n" \
"ds_read_b64_tr_b16 a[110:111], v12 offset:17408\n" \
"v_mul_f32 v25, v33, v45\n" \
"v_mfma_f32_16x16x32_bf16 v[76:79], a[12:15], a[76:79], v[76:79]\n" \
"v_mul_f32 v26, v39, v49\n" \
"v_fmac_f32 v25, v34, v44\n" \
"v_mfma_f32_16x16x32_bf16 v[88:91], a[16:19], a[72:75], v[88:91]\n" \
"ds_read_b64_tr_b16 a[112:113], v9 offset:16896\n" \
"v_fmac_f32 v26, v40, v48\n" \
"v_mfma_f32_16x16x32_bf16 v[88:91], a[20:23], a[76:79], v[88:91]\n" \
"buffer_load_dwordx2 a[212:213], v59, s[40:43], s52 offen offset:2048 nt\n" \
"v_mfma_f32_16x16x32_bf16 v[92:95], a[24:27], a[72:75], v[92:95]\n" \
"ds_read_b64_tr_b16 a[114:115], v10 offset:16896\n" \
"v_fmac_f32 v25, v35, v43\n" \
"v_mfma_f32_16x16x32_bf16 v[92:95], a[28:31], a[76:79], v[92:95]\n" \
"v_fmac_f32 v26, v41, v47\n" \
"v_fmac_f32 v25, v30, v42\n" \
"v_mfma_f32_16x16x32_bf16 v[104:107], a[32:35], a[72:75], v[104:107]\n" \
"s_add_u32 m0, s50, 20480\n" \
"ds_read_b64_tr_b16 a[116:117], v9 offset:17920\n" \
"v_fmac_f32 v26, v36, v46\n" \
"v_mfma_f32_16x16x32_bf16 v[104:107], a[36:39], a[76:79], v[104:107]\n" \
"buffer_load_dwordx4 v18, s[28:31], s46 offen lds\n" \
"v_mfma_f32_16x16x32_bf16 v[108:111], a[40:43], a[72:75], v[108:111]\n" \
"ds_read_b64_tr_b16 a[118:119], v10 offset:17920\n" \
"v_accvgpr_read_b32 v28, a220\n" \
"v_mfma_f32_16x16x32_bf16 v[108:111], a[44:47], a[76:79], v[108:111]\n" \
"v_lshlrev_b32 v27, 16, v28\n" \
"v_and_b32 v28, 0xffff0000, v28\n" \
"v_mfma_f32_16x16x32_bf16 v[120:123], a[48:51], a[72:75], v[120:123]\n" \
"ds_read_b64_tr_b16 a[120:121], v11 offset:16896\n" \
"v_mul_f32 v25, v25, v27\n" \
"v_mfma_f32_16x16x32_bf16 v[120:123], a[52:55], a[76:79], v[120:123]\n" \
"buffer_load_dwordx2 a[228:229], v59, s[80:83], s52 offen offset:2048 nt\n" \
"v_mfma_f32_16x16x32_bf16 v[124:127], a[56:59], a[72:75], v[124:127]\n" \
"ds_read_b64_tr_b16 a[122:123], v12 offset:16896\n" \
"v_mul_f32 v26, v26, v28\n" \
"v_mfma_f32_16x16x32_bf16 v[124:127], a[60:63], a[76:79], v[124:127]\n" \
"s_waitcnt vmcnt(12)\n" \
"v_cvt_pk_bf16_f32 v52, v25, v29\n" \
"v_cvt_pk_bf16_f32 v56, v26, v58\n" \
"v_mfma_f32_16x16x32_bf16 v[128:131], a[0:3], a[80:83], v[128:131]\n" \
"s_mov_b32 s47, 2359296\n" \
"s_add_u32 m0, s22, 1024\n" \
"ds_read_b64_tr_b16 a[124:125], v11 offset:17920\n" \
"v_mfma_f32_16x16x32_bf16 v[128:131], a[4:7], a[84:87], v[128:131]\n" \
"buffer_load_dwordx4 v21, s[32:35], s47 offen offset:0 lds\n" \
"v_mfma_f32_16x16x32_bf16 v[132:135], a[8:11], a[80:83], v[132:135]\n" \
"ds_read_b64_tr_b16 a[126:127], v12 offset:17920\n" \
"v_accvgpr_read_b32 v37, a206\n" \
"v_mfma_f32_16x16x32_bf16 v[132:135], a[12:15], a[84:87], v[132:135]\n" \
"v_lshlrev_b32 v31, 16, v37\n" \
"v_and_b32 v37, 0xffff0000, v37\n" \
"v_mfma_f32_16x16x32_bf16 v[144:147], a[16:19], a[80:83], v[144:147]\n" \
"v_mul_f32 v25, v34, v45\n" \
"v_mul_f32 v26, v40, v49\n" \
"v_mfma_f32_16x16x32_bf16 v[144:147], a[20:23], a[84:87], v[144:147]\n" \
"v_fmac_f32 v25, v35, v44\n" \
"v_mfma_f32_16x16x32_bf16 v[148:151], a[24:27], a[80:83], v[148:151]\n" \
"v_fmac_f32 v26, v41, v48\n" \
"v_fmac_f32 v25, v30, v43\n" \
"v_mfma_f32_16x16x32_bf16 v[148:151], a[28:31], a[84:87], v[148:151]\n" \
"v_fmac_f32 v26, v36, v47\n" \
"v_fmac_f32 v25, v31, v42\n" \
"v_mfma_f32_16x16x32_bf16 v[160:163], a[32:35], a[80:83], v[160:163]\n" \
"s_add_u32 m0, s50, 24576\n" \
"v_fmac_f32 v26, v37, v46\n" \
"v_mfma_f32_16x16x32_bf16 v[160:163], a[36:39], a[84:87], v[160:163]\n" \
"buffer_load_dwordx4 v19, s[28:31], s46 offen lds\n" \
"v_mfma_f32_16x16x32_bf16 v[164:167], a[40:43], a[80:83], v[164:167]\n" \
"v_accvgpr_read_b32 v28, a222\n" \
"v_lshlrev_b32 v27, 16, v28\n" \
"v_mfma_f32_16x16x32_bf16 v[164:167], a[44:47], a[84:87], v[164:167]\n" \
"s_waitcnt vmcnt(26) lgkmcnt(0)\n" \
"v_and_b32 v28, 0xffff0000, v28\n" \
"v_mul_f32 v29, v25, v27\n" \
"v_mfma_f32_16x16x32_bf16 v[176:179], a[48:51], a[80:83], v[176:179]\n" \
"s_barrier\n" \
"v_mul_f32 v58, v26, v28\n" \
"s_waitcnt vmcnt(10)\n" \
"v_mfma_f32_16x16x32_bf16 v[176:179], a[52:55], a[84:87], v[176:179]\n" \
"v_accvgpr_read_b32 v38, a208\n" \
"v_lshlrev_b32 v32, 16, v38\n" \
"v_mfma_f32_16x16x32_bf16 v[180:183], a[56:59], a[80:83], v[180:183]\n" \
"v_and_b32 v38, 0xffff0000, v38\n" \
"v_mul_f32 v25, v35, v45\n" \
"v_mfma_f32_16x16x32_bf16 v[180:183], a[60:63], a[84:87], v[180:183]\n" \
"v_mul_f32 v26, v41, v49\n" \
"v_mfma_f32_16x16x32_bf16 v[136:139], a[0:3], a[88:91], v[136:139]\n" \
"s_add_u32 m0, s22, 2816\n" \
"v_fmac_f32 v25, v30, v44\n" \
"v_fmac_f32 v26, v36, v48\n" \
"v_mfma_f32_16x16x32_bf16 v[136:139], a[4:7], a[92:95], v[136:139]\n" \
"buffer_load_dwordx4 v21, s[32:35], s47 offen offset:256 lds\n" \
"v_mfma_f32_16x16x32_bf16 v[140:143], a[8:11], a[88:91], v[140:143]\n" \
"s_add_u32 s84, s84, 1048576\ns_addc_u32 s85, s85, 0\n" \
"v_fmac_f32 v25, v31, v43\n" \
"v_mfma_f32_16x16x32_bf16 v[140:143], a[12:15], a[92:95], v[140:143]\n" \
"v_fmac_f32 v26, v37, v47\n" \
"v_fmac_f32 v25, v32, v42\n" \
"v_mfma_f32_16x16x32_bf16 v[152:155], a[16:19], a[88:91], v[152:155]\n" \
"v_fmac_f32 v26, v38, v46\n" \
"v_accvgpr_read_b32 v28, a224\n" \
"v_mfma_f32_16x16x32_bf16 v[152:155], a[20:23], a[92:95], v[152:155]\n" \
"v_lshlrev_b32 v27, 16, v28\n" \
"v_mfma_f32_16x16x32_bf16 v[156:159], a[24:27], a[88:91], v[156:159]\n" \
"v_and_b32 v28, 0xffff0000, v28\n" \
"v_mul_f32 v25, v25, v27\n" \
"v_mfma_f32_16x16x32_bf16 v[156:159], a[28:31], a[92:95], v[156:159]\n" \
"v_mul_f32 v26, v26, v28\n" \
"v_cvt_pk_bf16_f32 v51, v25, v29\n" \
"v_mfma_f32_16x16x32_bf16 v[168:171], a[32:35], a[88:91], v[168:171]\n" \
"s_add_u32 m0, s50, 28672\n" \
"v_cvt_pk_bf16_f32 v55, v26, v58\n" \
"v_mfma_f32_16x16x32_bf16 v[168:171], a[36:39], a[92:95], v[168:171]\n" \
"buffer_load_dwordx4 v20, s[28:31], s46 offen lds\n" \
"v_mfma_f32_16x16x32_bf16 v[172:175], a[40:43], a[88:91], v[172:175]\n" \
"v_mfma_f32_16x16x32_bf16 v[172:175], a[44:47], a[92:95], v[172:175]\n" \
"v_mfma_f32_16x16x32_bf16 v[184:187], a[48:51], a[88:91], v[184:187]\n" \
"v_mfma_f32_16x16x32_bf16 v[184:187], a[52:55], a[92:95], v[184:187]\n" \
"s_mov_b64 exec, 0xffff\nglobal_load_dword v22, v1, s[84:85]\ns_mov_b64 exec, -1\n" \
"v_mfma_f32_16x16x32_bf16 v[188:191], a[56:59], a[88:91], v[188:191]\n" \
"v_mfma_f32_16x16x32_bf16 v[188:191], a[60:63], a[92:95], v[188:191]\n" \
"s_waitcnt vmcnt(9)\n" \
"v_accvgpr_read_b32 v39, a210\n" \
"v_lshlrev_b32 v33, 16, v39\n" \
"v_mfma_f32_16x16x32_bf16 v[192:195], a[0:3], a[96:99], v[192:195]\n" \
"s_mov_b32 s47, 2097152\n" \
"s_add_u32 m0, s22, 16256\n" \
"v_and_b32 v39, 0xffff0000, v39\n" \
"v_mfma_f32_16x16x32_bf16 v[192:195], a[4:7], a[100:103], v[192:195]\n" \
"buffer_load_dwordx4 v21, s[32:35], s47 offen offset:128 lds\n" \
"v_mfma_f32_16x16x32_bf16 v[196:199], a[8:11], a[96:99], v[196:199]\n" \
"v_mul_f32 v25, v30, v45\n" \
"v_mul_f32 v26, v36, v49\n" \
"v_mfma_f32_16x16x32_bf16 v[196:199], a[12:15], a[100:103], v[196:199]\n" \
"v_fmac_f32 v25, v31, v44\n" \
"v_fmac_f32 v26, v37, v48\n" \
"v_mfma_f32_16x16x32_bf16 v[208:211], a[16:19], a[96:99], v[208:211]\n" \
"s_mov_b32 s46, 640\n" \
"s_add_u32 m0, s50, 32768\n" \
"ds_read_b64_tr_b16 a[64:65], v9 offset:32768\n" \
"v_mfma_f32_16x16x32_bf16 v[208:211], a[20:23], a[100:103], v[208:211]\n" \
"buffer_load_dwordx4 v13, s[28:31], s46 offen lds\n" \
"v_mfma_f32_16x16x32_bf16 v[212:215], a[24:27], a[96:99], v[212:215]\n" \
"ds_read_b64_tr_b16 a[66:67], v10 offset:32768\n" \
"v_fmac_f32 v25, v32, v43\n" \
"v_mfma_f32_16x16x32_bf16 v[212:215], a[28:31], a[100:103], v[212:215]\n" \
"v_fmac_f32 v26, v38, v47\n" \
"v_fmac_f32 v25, v33, v42\n" \
"v_mfma_f32_16x16x32_bf16 v[224:227], a[32:35], a[96:99], v[224:227]\n" \
"ds_read_b64_tr_b16 a[68:69], v9 offset:33792\n" \
"v_fmac_f32 v26, v39, v46\n" \
"v_mfma_f32_16x16x32_bf16 v[224:227], a[36:39], a[100:103], v[224:227]\n" \
"v_accvgpr_read_b32 v28, a226\n" \
"v_lshlrev_b32 v27, 16, v28\n" \
"v_mfma_f32_16x16x32_bf16 v[228:231], a[40:43], a[96:99], v[228:231]\n" \
"ds_read_b64_tr_b16 a[70:71], v10 offset:33792\n" \
"v_and_b32 v28, 0xffff0000, v28\n" \
"v_mfma_f32_16x16x32_bf16 v[228:231], a[44:47], a[100:103], v[228:231]\n" \
"v_mul_f32 v29, v25, v27\n" \
"v_mul_f32 v58, v26, v28\n" \
"v_mfma_f32_16x16x32_bf16 v[240:243], a[48:51], a[96:99], v[240:243]\n" \
"s_add_u32 m0, s22, 18048\n" \
"ds_read_b64_tr_b16 a[72:73], v11 offset:32768\n" \
"v_mfma_f32_16x16x32_bf16 v[240:243], a[52:55], a[100:103], v[240:243]\n" \
"buffer_load_dwordx4 v21, s[32:35], s47 offen offset:384 lds\n" \
"v_mfma_f32_16x16x32_bf16 v[244:247], a[56:59], a[96:99], v[244:247]\n" \
"ds_read_b64_tr_b16 a[74:75], v12 offset:32768\n" \
"v_mfma_f32_16x16x32_bf16 v[244:247], a[60:63], a[100:103], v[244:247]\n" \
"s_waitcnt vmcnt(8)\n" \
"v_accvgpr_read_b32 v40, a212\n" \
"v_lshlrev_b32 v34, 16, v40\n" \
"v_mfma_f32_16x16x32_bf16 v[200:203], a[0:3], a[104:107], v[200:203]\n" \
"s_add_u32 m0, s50, 36864\n" \
"ds_read_b64_tr_b16 a[76:77], v11 offset:33792\n" \
"v_and_b32 v40, 0xffff0000, v40\n" \
"v_mfma_f32_16x16x32_bf16 v[200:203], a[4:7], a[108:111], v[200:203]\n" \
"buffer_load_dwordx4 v14, s[28:31], s46 offen lds\n" \
"v_mfma_f32_16x16x32_bf16 v[204:207], a[8:11], a[104:107], v[204:207]\n" \
"ds_read_b64_tr_b16 a[78:79], v12 offset:33792\n" \
"v_mul_f32 v25, v31, v45\n" \
"v_mfma_f32_16x16x32_bf16 v[204:207], a[12:15], a[108:111], v[204:207]\n" \
"v_mul_f32 v26, v37, v49\n" \
"v_fmac_f32 v25, v32, v44\n" \
"v_mfma_f32_16x16x32_bf16 v[216:219], a[16:19], a[104:107], v[216:219]\n" \
"ds_read_b64_tr_b16 a[80:81], v9 offset:33280\n" \
"v_fmac_f32 v26, v38, v48\n" \
"v_mfma_f32_16x16x32_bf16 v[216:219], a[20:23], a[108:111], v[216:219]\n" \
"v_fmac_f32 v25, v33, v43\n" \
"v_fmac_f32 v26, v39, v47\n" \
"v_mfma_f32_16x16x32_bf16 v[220:223], a[24:27], a[104:107], v[220:223]\n" \
"ds_read_b64_tr_b16 a[82:83], v10 offset:33280\n" \
"v_fmac_f32 v25, v34, v42\n" \
"v_mfma_f32_16x16x32_bf16 v[220:223], a[28:31], a[108:111], v[220:223]\n" \
"v_fmac_f32 v26, v40, v46\n" \
"v_accvgpr_read_b32 v28, a228\n" \
"v_mfma_f32_16x16x32_bf16 v[232:235], a[32:35], a[104:107], v[232:235]\n" \
"s_mov_b32 s47, 2359296\n" \
"s_add_u32 m0, s22, 17280\n" \
"ds_read_b64_tr_b16 a[84:85], v9 offset:34304\n" \
"v_mfma_f32_16x16x32_bf16 v[232:235], a[36:39], a[108:111], v[232:235]\n" \
"buffer_load_dwordx4 v21, s[32:35], s47 offen offset:128 lds\n" \
"v_mfma_f32_16x16x32_bf16 v[236:239], a[40:43], a[104:107], v[236:239]\n" \
"ds_read_b64_tr_b16 a[86:87], v10 offset:34304\n" \
"v_lshlrev_b32 v27, 16, v28\n" \
"v_mfma_f32_16x16x32_bf16 v[236:239], a[44:47], a[108:111], v[236:239]\n" \
"v_and_b32 v28, 0xffff0000, v28\n" \
"v_mul_f32 v25, v25, v27\n" \
"v_mfma_f32_16x16x32_bf16 v[248:251], a[48:51], a[104:107], v[248:251]\n" \
"s_add_u32 m0, s50, 40960\n" \
"ds_read_b64_tr_b16 a[88:89], v11 offset:33280\n" \
"v_mul_f32 v26, v26, v28\n" \
"v_mfma_f32_16x16x32_bf16 v[248:251], a[52:55], a[108:111], v[248:251]\n" \
"buffer_load_dwordx4 v15, s[28:31], s46 offen lds\n" \
"v_mfma_f32_16x16x32_bf16 v[252:255], a[56:59], a[104:107], v[252:255]\n" \
"ds_read_b64_tr_b16 a[90:91], v12 offset:33280\n" \
"v_cvt_pk_bf16_f32 v50, v25, v29\n" \
"v_mfma_f32_16x16x32_bf16 v[252:255], a[60:63], a[108:111], v[252:255]\n" \
"s_mov_b32 s94, 79691776\n" \
"v_cvt_pk_bf16_f32 v54, v26, v58\n" \
"v_accvgpr_read_b32 v43, a234\n" \
"v_mfma_f32_16x16x32_bf16 a[128:131], a[0:3], a[112:115], a[128:131]\n" \
"ds_read_b64_tr_b16 a[92:93], v11 offset:34304\n" \
"v_lshlrev_b32 v42, 16, v43\n" \
"v_mfma_f32_16x16x32_bf16 a[128:131], a[4:7], a[116:119], a[128:131]\n" \
"buffer_store_dwordx4 v[50:53], v60, s[36:39], s94 offen offset:0 sc1\n" \
"v_mfma_f32_16x16x32_bf16 a[132:135], a[8:11], a[112:115], a[132:135]\n" \
"s_mov_b32 s95, 79695872\n" \
"ds_read_b64_tr_b16 a[94:95], v12 offset:34304\n" \
"v_and_b32 v43, 0xffff0000, v43\n" \
"v_mfma_f32_16x16x32_bf16 a[132:135], a[12:15], a[116:119], a[132:135]\n" \
"v_accvgpr_read_b32 v45, a235\n" \
"v_lshlrev_b32 v44, 16, v45\n" \
"v_mfma_f32_16x16x32_bf16 a[144:147], a[16:19], a[112:115], a[144:147]\n" \
"s_add_u32 m0, s22, 19072\n" \
"v_and_b32 v45, 0xffff0000, v45\n" \
"v_accvgpr_read_b32 v47, a236\n" \
"v_mfma_f32_16x16x32_bf16 a[144:147], a[20:23], a[116:119], a[144:147]\n" \
"buffer_load_dwordx4 v21, s[32:35], s47 offen offset:384 lds\n" \
"v_mfma_f32_16x16x32_bf16 a[148:151], a[24:27], a[112:115], a[148:151]\n" \
"v_lshlrev_b32 v46, 16, v47\n" \
"v_and_b32 v47, 0xffff0000, v47\n" \
"v_mfma_f32_16x16x32_bf16 a[148:151], a[28:31], a[116:119], a[148:151]\n" \
"v_accvgpr_read_b32 v49, a237\n" \
"v_lshlrev_b32 v48, 16, v49\n" \
"v_mfma_f32_16x16x32_bf16 a[160:163], a[32:35], a[112:115], a[160:163]\n" \
"v_and_b32 v49, 0xffff0000, v49\n" \
"v_mfma_f32_16x16x32_bf16 a[160:163], a[36:39], a[116:119], a[160:163]\n" \
"v_accvgpr_read_b32 v36, a193\n" \
"v_lshlrev_b32 v30, 16, v36\n" \
"v_mfma_f32_16x16x32_bf16 a[164:167], a[40:43], a[112:115], a[164:167]\n" \
"v_and_b32 v36, 0xffff0000, v36\n" \
"v_accvgpr_read_b32 v37, a195\n" \
"v_mfma_f32_16x16x32_bf16 a[164:167], a[44:47], a[116:119], a[164:167]\n" \
"s_waitcnt vmcnt(24) lgkmcnt(0)\n" \
"v_lshlrev_b32 v31, 16, v37\n" \
"v_mfma_f32_16x16x32_bf16 a[176:179], a[48:51], a[112:115], a[176:179]\n" \
"s_add_u32 m0, s50, 45056\n" \
"s_barrier\n" \
"v_and_b32 v37, 0xffff0000, v37\n" \
"v_mfma_f32_16x16x32_bf16 a[176:179], a[52:55], a[116:119], a[176:179]\n" \
"buffer_load_dwordx4 v16, s[28:31], s46 offen lds\n" \
"v_mfma_f32_16x16x32_bf16 a[180:183], a[56:59], a[112:115], a[180:183]\n" \
"v_accvgpr_read_b32 v38, a197\n" \
"v_lshlrev_b32 v32, 16, v38\n" \
"v_mfma_f32_16x16x32_bf16 a[180:183], a[60:63], a[116:119], a[180:183]\n" \
"buffer_store_dwordx4 v[54:57], v60, s[36:39], s95 offen offset:0 sc1\n" \
"v_mfma_f32_16x16x32_bf16 a[136:139], a[0:3], a[120:123], a[136:139]\n" \
"v_and_b32 v38, 0xffff0000, v38\n" \
"v_accvgpr_read_b32 v39, a199\n" \
"v_mfma_f32_16x16x32_bf16 a[136:139], a[4:7], a[124:127], a[136:139]\n" \
"v_lshlrev_b32 v33, 16, v39\n" \
"v_and_b32 v39, 0xffff0000, v39\n" \
"v_mfma_f32_16x16x32_bf16 a[140:143], a[8:11], a[120:123], a[140:143]\n" \
"ds_read_b128 a[0:3], v7 offset:0\n" \
"v_mul_f32 v25, v30, v45\n" \
"v_mfma_f32_16x16x32_bf16 a[140:143], a[12:15], a[124:127], a[140:143]\n" \
"ds_read_b128 a[4:7], v8 offset:0\n" \
"v_mul_f32 v26, v36, v49\n" \
"v_mfma_f32_16x16x32_bf16 a[152:155], a[16:19], a[120:123], a[152:155]\n" \
"ds_read_b128 a[8:11], v7 offset:2048\n" \
"v_fmac_f32 v25, v31, v44\n" \
"v_mfma_f32_16x16x32_bf16 a[152:155], a[20:23], a[124:127], a[152:155]\n" \
"ds_read_b128 a[12:15], v8 offset:2048\n" \
"v_fmac_f32 v26, v37, v48\n" \
"v_mfma_f32_16x16x32_bf16 a[156:159], a[24:27], a[120:123], a[156:159]\n" \
"ds_read_b128 a[16:19], v7 offset:4096\n" \
"v_fmac_f32 v25, v32, v43\n" \
"v_mfma_f32_16x16x32_bf16 a[156:159], a[28:31], a[124:127], a[156:159]\n" \
"ds_read_b128 a[20:23], v8 offset:4096\n" \
"v_fmac_f32 v26, v38, v47\n" \
"v_mfma_f32_16x16x32_bf16 a[168:171], a[32:35], a[120:123], a[168:171]\n" \
"ds_read_b128 a[24:27], v7 offset:6144\n" \
"v_fmac_f32 v25, v33, v42\n" \
"v_mfma_f32_16x16x32_bf16 a[168:171], a[36:39], a[124:127], a[168:171]\n" \
"ds_read_b128 a[28:31], v8 offset:6144\n" \
"v_fmac_f32 v26, v39, v46\n" \
"v_mfma_f32_16x16x32_bf16 a[172:175], a[40:43], a[120:123], a[172:175]\n" \
"ds_read_b128 a[32:35], v7 offset:8192\n" \
"v_accvgpr_read_b32 v28, a215\n" \
"v_mfma_f32_16x16x32_bf16 a[172:175], a[44:47], a[124:127], a[172:175]\n" \
"ds_read_b128 a[36:39], v8 offset:8192\n" \
"v_lshlrev_b32 v27, 16, v28\n" \
"v_mfma_f32_16x16x32_bf16 a[184:187], a[48:51], a[120:123], a[184:187]\n" \
"ds_read_b128 a[40:43], v7 offset:10240\n" \
"v_and_b32 v28, 0xffff0000, v28\n" \
"v_mfma_f32_16x16x32_bf16 a[184:187], a[52:55], a[124:127], a[184:187]\n" \
"ds_read_b128 a[44:47], v8 offset:10240\n" \
"v_mul_f32 v29, v25, v27\n" \
"v_mfma_f32_16x16x32_bf16 a[188:191], a[56:59], a[120:123], a[188:191]\n" \
"ds_read_b128 a[48:51], v7 offset:12288\n" \
"v_mul_f32 v58, v26, v28\n" \
"v_mfma_f32_16x16x32_bf16 a[188:191], a[60:63], a[124:127], a[188:191]\n" \
"s_waitcnt lgkmcnt(9)\n" \
"ds_read_b128 a[52:55], v8 offset:12288\n" \
"v_accvgpr_read_b32 v40, a201\n" \
"v_mfma_f32_16x16x32_bf16 v[64:67], a[0:3], a[64:67], v[64:67]\n" \
"s_mov_b32 s47, 2621440\n" \
"s_add_u32 m0, s22, 32768\n" \
"ds_read_b128 a[56:59], v7 offset:14336\n" \
"v_mfma_f32_16x16x32_bf16 v[64:67], a[4:7], a[68:71], v[64:67]\n" \
"buffer_load_dwordx4 v21, s[32:35], s47 offen offset:0 lds\n" \
"v_mfma_f32_16x16x32_bf16 v[68:71], a[8:11], a[64:67], v[68:71]\n" \
"s_waitcnt lgkmcnt(7)\n" \
"ds_read_b128 a[60:63], v8 offset:14336\n" \
"v_lshlrev_b32 v34, 16, v40\n" \
"v_mfma_f32_16x16x32_bf16 v[68:71], a[12:15], a[68:71], v[68:71]\n" \
"v_and_b32 v40, 0xffff0000, v40\n" \
"v_mul_f32 v25, v31, v45\n" \
"v_mfma_f32_16x16x32_bf16 v[80:83], a[16:19], a[64:67], v[80:83]\n" \
"s_mov_b32 s46, 640\n" \
"s_add_u32 m0, s50, 49152\n" \
"ds_read_b64_tr_b16 a[96:97], v9 offset:49152\n" \
"v_mfma_f32_16x16x32_bf16 v[80:83], a[20:23], a[68:71], v[80:83]\n" \
"buffer_load_dwordx4 v17, s[28:31], s46 offen lds\n" \
"v_mfma_f32_16x16x32_bf16 v[84:87], a[24:27], a[64:67], v[84:87]\n" \
"s_waitcnt lgkmcnt(5)\n" \
"ds_read_b64_tr_b16 a[98:99], v10 offset:49152\n" \
"v_mul_f32 v26, v37, v49\n" \
"v_mfma_f32_16x16x32_bf16 v[84:87], a[28:31], a[68:71], v[84:87]\n" \
"v_fmac_f32 v25, v32, v44\n" \
"v_fmac_f32 v26, v38, v48\n" \
"v_mfma_f32_16x16x32_bf16 v[96:99], a[32:35], a[64:67], v[96:99]\n" \
"s_add_u32 m0, s22, 34560\n" \
"ds_read_b64_tr_b16 a[100:101], v9 offset:50176\n" \
"v_fmac_f32 v25, v33, v43\n" \
"v_mfma_f32_16x16x32_bf16 v[96:99], a[36:39], a[68:71], v[96:99]\n" \
"buffer_load_dwordx4 v21, s[32:35], s47 offen offset:256 lds\n" \
"v_mfma_f32_16x16x32_bf16 v[100:103], a[40:43], a[64:67], v[100:103]\n" \
"s_waitcnt lgkmcnt(3)\n" \
"ds_read_b64_tr_b16 a[102:103], v10 offset:50176\n" \
"v_fmac_f32 v26, v39, v47\n" \
"v_mfma_f32_16x16x32_bf16 v[100:103], a[44:47], a[68:71], v[100:103]\n" \
"v_fmac_f32 v25, v34, v42\n" \
"v_fmac_f32 v26, v40, v46\n" \
"v_mfma_f32_16x16x32_bf16 v[112:115], a[48:51], a[64:67], v[112:115]\n" \
"s_add_u32 m0, s50, 53248\n" \
"ds_read_b64_tr_b16 a[104:105], v11 offset:49152\n" \
"v_accvgpr_read_b32 v28, a217\n" \
"v_mfma_f32_16x16x32_bf16 v[112:115], a[52:55], a[68:71], v[112:115]\n" \
"LH5_%=:\ns_cbranch_execz LH9_%=\n" \
"buffer_load_dwordx4 v18, s[28:31], s46 offen lds\n" \
"v_mfma_f32_16x16x32_bf16 v[116:119], a[56:59], a[64:67], v[116:119]\n" \
"ds_read_b64_tr_b16 a[106:107], v12 offset:49152\n" \
"v_lshlrev_b32 v27, 16, v28\n" \
"v_mfma_f32_16x16x32_bf16 v[116:119], a[60:63], a[68:71], v[116:119]\n" \
"v_and_b32 v28, 0xffff0000, v28\n" \
"v_mul_f32 v25, v25, v27\n" \
"v_mfma_f32_16x16x32_bf16 v[72:75], a[0:3], a[72:75], v[72:75]\n" \
"s_mov_b32 s47, 2883584\n" \
"s_add_u32 m0, s22, 33792\n" \
"ds_read_b64_tr_b16 a[108:109], v11 offset:50176\n" \
"v_mfma_f32_16x16x32_bf16 v[72:75], a[4:7], a[76:79], v[72:75]\n" \
"buffer_load_dwordx4 v21, s[32:35], s47 offen offset:0 lds\n" \
"v_mfma_f32_16x16x32_bf16 v[76:79], a[8:11], a[72:75], v[76:79]\n" \
"ds_read_b64_tr_b16 a[110:111], v12 offset:50176\n" \
"v_mul_f32 v26, v26, v28\n" \
"v_mfma_f32_16x16x32_bf16 v[76:79], a[12:15], a[76:79], v[76:79]\n" \
"v_cvt_pk_bf16_f32 v53, v25, v29\n" \
"v_cvt_pk_bf16_f32 v57, v26, v58\n" \
"v_mfma_f32_16x16x32_bf16 v[88:91], a[16:19], a[72:75], v[88:91]\n" \
"s_add_u32 m0, s50, 57344\n" \
"ds_read_b64_tr_b16 a[112:113], v9 offset:49664\n" \
"v_accvgpr_read_b32 v41, a203\n" \
"v_mfma_f32_16x16x32_bf16 v[88:91], a[20:23], a[76:79], v[88:91]\n" \
"buffer_load_dwordx4 v19, s[28:31], s46 offen lds\n" \
"v_mfma_f32_16x16x32_bf16 v[92:95], a[24:27], a[72:75], v[92:95]\n" \
"ds_read_b64_tr_b16 a[114:115], v10 offset:49664\n" \
"v_lshlrev_b32 v35, 16, v41\n" \
"v_mfma_f32_16x16x32_bf16 v[92:95], a[28:31], a[76:79], v[92:95]\n" \
"v_and_b32 v41, 0xffff0000, v41\n" \
"v_mul_f32 v25, v32, v45\n" \
"v_mfma_f32_16x16x32_bf16 v[104:107], a[32:35], a[72:75], v[104:107]\n" \
"s_add_u32 m0, s22, 35584\n" \
"ds_read_b64_tr_b16 a[116:117], v9 offset:50688\n" \
"v_mul_f32 v26, v38, v49\n" \
"v_mfma_f32_16x16x32_bf16 v[104:107], a[36:39], a[76:79], v[104:107]\n" \
"buffer_load_dwordx4 v21, s[32:35], s47 offen offset:256 lds\n" \
"v_mfma_f32_16x16x32_bf16 v[108:111], a[40:43], a[72:75], v[108:111]\n" \
"s_add_u32 s84, s84, 1048576\ns_addc_u32 s85, s85, 0\n" \
"ds_read_b64_tr_b16 a[118:119], v10 offset:50688\n" \
"v_mfma_f32_16x16x32_bf16 v[108:111], a[44:47], a[76:79], v[108:111]\n" \
"v_fmac_f32 v25, v33, v44\n" \
"v_fmac_f32 v26, v39, v48\n" \
"v_mfma_f32_16x16x32_bf16 v[120:123], a[48:51], a[72:75], v[120:123]\n" \
"s_add_u32 m0, s50, 61440\n" \
"ds_read_b64_tr_b16 a[120:121], v11 offset:49664\n" \
"v_fmac_f32 v25, v34, v43\n" \
"v_mfma_f32_16x16x32_bf16 v[120:123], a[52:55], a[76:79], v[120:123]\n" \
"buffer_load_dwordx4 v20, s[28:31], s46 offen lds\n" \
"v_mfma_f32_16x16x32_bf16 v[124:127], a[56:59], a[72:75], v[124:127]\n" \
"ds_read_b64_tr_b16 a[122:123], v12 offset:49664\n" \
"v_fmac_f32 v26, v40, v47\n" \
"v_mfma_f32_16x16x32_bf16 v[124:127], a[60:63], a[76:79], v[124:127]\n" \
"v_fmac_f32 v25, v35, v42\n" \
"v_fmac_f32 v26, v41, v46\n" \
"v_mfma_f32_16x16x32_bf16 v[128:131], a[0:3], a[80:83], v[128:131]\n" \
"ds_read_b64_tr_b16 a[124:125], v11 offset:50688\n" \
"v_accvgpr_read_b32 v28, a219\n" \
"v_mfma_f32_16x16x32_bf16 v[128:131], a[4:7], a[84:87], v[128:131]\n" \
"s_mov_b64 exec, 0xffff\nglobal_load_dword v22, v1, s[84:85]\ns_mov_b64 exec, -1\n" \
"v_mfma_f32_16x16x32_bf16 v[132:135], a[8:11], a[80:83], v[132:135]\n" \
"ds_read_b64_tr_b16 a[126:127], v12 offset:50688\n" \
"v_lshlrev_b32 v27, 16, v28\n" \
"v_mfma_f32_16x16x32_bf16 v[132:135], a[12:15], a[84:87], v[132:135]\n" \
"v_and_b32 v28, 0xffff0000, v28\n" \
"v_mul_f32 v29, v25, v27\n" \
"v_mfma_f32_16x16x32_bf16 v[144:147], a[16:19], a[80:83], v[144:147]\n" \
"v_mul_f32 v58, v26, v28\n" \
"v_accvgpr_read_b32 v36, a205\n" \
"v_mfma_f32_16x16x32_bf16 v[144:147], a[20:23], a[84:87], v[144:147]\n" \
"s_mov_b64 exec, 0xffff\nglobal_load_dword v22, v1, s[76:77]\ns_mov_b64 exec, -1\n" \
"v_mfma_f32_16x16x32_bf16 v[148:151], a[24:27], a[80:83], v[148:151]\n" \
"v_lshlrev_b32 v30, 16, v36\n" \
"v_and_b32 v36, 0xffff0000, v36\n" \
"v_mfma_f32_16x16x32_bf16 v[148:151], a[28:31], a[84:87], v[148:151]\n" \
"v_mul_f32 v25, v33, v45\n" \
"v_mul_f32 v26, v39, v49\n" \
"v_mfma_f32_16x16x32_bf16 v[160:163], a[32:35], a[80:83], v[160:163]\n" \
"v_fmac_f32 v25, v34, v44\n" \
"v_mfma_f32_16x16x32_bf16 v[160:163], a[36:39], a[84:87], v[160:163]\n" \
"v_fmac_f32 v26, v40, v48\n" \
"v_fmac_f32 v25, v35, v43\n" \
"v_mfma_f32_16x16x32_bf16 v[164:167], a[40:43], a[80:83], v[164:167]\n" \
"v_fmac_f32 v26, v41, v47\n" \
"v_fmac_f32 v25, v30, v42\n" \
"v_mfma_f32_16x16x32_bf16 v[164:167], a[44:47], a[84:87], v[164:167]\n" \
"s_waitcnt vmcnt(22) lgkmcnt(0)\n" \
"v_fmac_f32 v26, v36, v46\n" \
"v_mfma_f32_16x16x32_bf16 v[176:179], a[48:51], a[80:83], v[176:179]\n" \
"s_barrier\n" \
"v_accvgpr_read_b32 v28, a221\n" \
"v_lshlrev_b32 v27, 16, v28\n" \
"v_mfma_f32_16x16x32_bf16 v[176:179], a[52:55], a[84:87], v[176:179]\n" \
"v_and_b32 v28, 0xffff0000, v28\n" \
"v_mul_f32 v25, v25, v27\n" \
"v_mfma_f32_16x16x32_bf16 v[180:183], a[56:59], a[80:83], v[180:183]\n" \
"v_mul_f32 v26, v26, v28\n" \
"v_mfma_f32_16x16x32_bf16 v[180:183], a[60:63], a[84:87], v[180:183]\n" \
"v_cvt_pk_bf16_f32 v52, v25, v29\n" \
"v_cvt_pk_bf16_f32 v56, v26, v58\n" \
"v_mfma_f32_16x16x32_bf16 v[136:139], a[0:3], a[88:91], v[136:139]\n" \
"v_accvgpr_read_b32 v37, a207\n" \
"v_lshlrev_b32 v31, 16, v37\n" \
"v_mfma_f32_16x16x32_bf16 v[136:139], a[4:7], a[92:95], v[136:139]\n" \
"v_and_b32 v37, 0xffff0000, v37\n" \
"v_mfma_f32_16x16x32_bf16 v[140:143], a[8:11], a[88:91], v[140:143]\n" \
"v_mul_f32 v25, v34, v45\n" \
"v_mul_f32 v26, v40, v49\n" \
"v_mfma_f32_16x16x32_bf16 v[140:143], a[12:15], a[92:95], v[140:143]\n" \
"v_fmac_f32 v25, v35, v44\n" \
"v_fmac_f32 v26, v41, v48\n" \
"v_mfma_f32_16x16x32_bf16 v[152:155], a[16:19], a[88:91], v[152:155]\n" \
"v_fmac_f32 v25, v30, v43\n" \
"v_mfma_f32_16x16x32_bf16 v[152:155], a[20:23], a[92:95], v[152:155]\n" \
"v_fmac_f32 v26, v36, v47\n" \
"v_fmac_f32 v25, v31, v42\n" \
"v_mfma_f32_16x16x32_bf16 v[156:159], a[24:27], a[88:91], v[156:159]\n" \
"v_fmac_f32 v26, v37, v46\n" \
"v_accvgpr_read_b32 v28, a223\n" \
"v_mfma_f32_16x16x32_bf16 v[156:159], a[28:31], a[92:95], v[156:159]\n" \
"v_lshlrev_b32 v27, 16, v28\n" \
"v_mfma_f32_16x16x32_bf16 v[168:171], a[32:35], a[88:91], v[168:171]\n" \
"v_and_b32 v28, 0xffff0000, v28\n" \
"v_mul_f32 v29, v25, v27\n" \
"v_mfma_f32_16x16x32_bf16 v[168:171], a[36:39], a[92:95], v[168:171]\n" \
"v_mul_f32 v58, v26, v28\n" \
"v_accvgpr_read_b32 v38, a209\n" \
"v_mfma_f32_16x16x32_bf16 v[172:175], a[40:43], a[88:91], v[172:175]\n" \
"v_lshlrev_b32 v32, 16, v38\n" \
"v_mfma_f32_16x16x32_bf16 v[172:175], a[44:47], a[92:95], v[172:175]\n" \
"v_and_b32 v38, 0xffff0000, v38\n" \
"v_mul_f32 v25, v35, v45\n" \
"v_mfma_f32_16x16x32_bf16 v[184:187], a[48:51], a[88:91], v[184:187]\n" \
"v_mul_f32 v26, v41, v49\n" \
"v_fmac_f32 v25, v30, v44\n" \
"v_mfma_f32_16x16x32_bf16 v[184:187], a[52:55], a[92:95], v[184:187]\n" \
"v_fmac_f32 v26, v36, v48\n" \
"v_mfma_f32_16x16x32_bf16 v[188:191], a[56:59], a[88:91], v[188:191]\n" \
"v_fmac_f32 v25, v31, v43\n" \
"v_fmac_f32 v26, v37, v47\n" \
"v_mfma_f32_16x16x32_bf16 v[188:191], a[60:63], a[92:95], v[188:191]\n" \
"v_fmac_f32 v25, v32, v42\n" \
"v_fmac_f32 v26, v38, v46\n" \
"v_mfma_f32_16x16x32_bf16 v[192:195], a[0:3], a[96:99], v[192:195]\n" \
"v_accvgpr_read_b32 v28, a225\n" \
"v_mfma_f32_16x16x32_bf16 v[192:195], a[4:7], a[100:103], v[192:195]\n" \
"v_lshlrev_b32 v27, 16, v28\n" \
"v_and_b32 v28, 0xffff0000, v28\n" \
"v_mfma_f32_16x16x32_bf16 v[196:199], a[8:11], a[96:99], v[196:199]\n" \
"v_mul_f32 v25, v25, v27\n" \
"v_mul_f32 v26, v26, v28\n" \
"v_mfma_f32_16x16x32_bf16 v[196:199], a[12:15], a[100:103], v[196:199]\n" \
"v_cvt_pk_bf16_f32 v51, v25, v29\n" \
"v_mfma_f32_16x16x32_bf16 v[208:211], a[16:19], a[96:99], v[208:211]\n" \
"ds_read_b64_tr_b16 a[64:65], v9 offset:0\n" \
"v_cvt_pk_bf16_f32 v55, v26, v58\n" \
"v_mfma_f32_16x16x32_bf16 v[208:211], a[20:23], a[100:103], v[208:211]\n" \
"v_accvgpr_read_b32 v39, a211\n" \
"v_lshlrev_b32 v33, 16, v39\n" \
"v_mfma_f32_16x16x32_bf16 v[212:215], a[24:27], a[96:99], v[212:215]\n" \
"ds_read_b64_tr_b16 a[66:67], v10 offset:0\n" \
"v_and_b32 v39, 0xffff0000, v39\n" \
"v_mfma_f32_16x16x32_bf16 v[212:215], a[28:31], a[100:103], v[212:215]\n" \
"v_mul_f32 v25, v30, v45\n" \
"v_mul_f32 v26, v36, v49\n" \
"v_mfma_f32_16x16x32_bf16 v[224:227], a[32:35], a[96:99], v[224:227]\n" \
"ds_read_b64_tr_b16 a[68:69], v9 offset:1024\n" \
"v_fmac_f32 v25, v31, v44\n" \
"v_mfma_f32_16x16x32_bf16 v[224:227], a[36:39], a[100:103], v[224:227]\n" \
"v_fmac_f32 v26, v37, v48\n" \
"v_fmac_f32 v25, v32, v43\n" \
"v_mfma_f32_16x16x32_bf16 v[228:231], a[40:43], a[96:99], v[228:231]\n" \
"ds_read_b64_tr_b16 a[70:71], v10 offset:1024\n" \
"v_fmac_f32 v26, v38, v47\n" \
"v_mfma_f32_16x16x32_bf16 v[228:231], a[44:47], a[100:103], v[228:231]\n" \
"v_fmac_f32 v25, v33, v42\n" \
"v_fmac_f32 v26, v39, v46\n" \
"v_mfma_f32_16x16x32_bf16 v[240:243], a[48:51], a[96:99], v[240:243]\n" \
"ds_read_b64_tr_b16 a[72:73], v11 offset:0\n" \
"v_accvgpr_read_b32 v28, a227\n" \
"v_mfma_f32_16x16x32_bf16 v[240:243], a[52:55], a[100:103], v[240:243]\n" \
"v_lshlrev_b32 v27, 16, v28\n" \
"v_and_b32 v28, 0xffff0000, v28\n" \
"v_mfma_f32_16x16x32_bf16 v[244:247], a[56:59], a[96:99], v[244:247]\n" \
"ds_read_b64_tr_b16 a[74:75], v12 offset:0\n" \
"v_mul_f32 v29, v25, v27\n" \
"v_mfma_f32_16x16x32_bf16 v[244:247], a[60:63], a[100:103], v[244:247]\n" \
"v_mul_f32 v58, v26, v28\n" \
"v_accvgpr_read_b32 v40, a213\n" \
"v_mfma_f32_16x16x32_bf16 v[200:203], a[0:3], a[104:107], v[200:203]\n" \
"ds_read_b64_tr_b16 a[76:77], v11 offset:1024\n" \
"v_lshlrev_b32 v34, 16, v40\n" \
"v_mfma_f32_16x16x32_bf16 v[200:203], a[4:7], a[108:111], v[200:203]\n" \
"v_and_b32 v40, 0xffff0000, v40\n" \
"v_mul_f32 v25, v31, v45\n" \
"v_mfma_f32_16x16x32_bf16 v[204:207], a[8:11], a[104:107], v[204:207]\n" \
"ds_read_b64_tr_b16 a[78:79], v12 offset:1024\n" \
"v_mul_f32 v26, v37, v49\n" \
"v_mfma_f32_16x16x32_bf16 v[204:207], a[12:15], a[108:111], v[204:207]\n" \
"v_fmac_f32 v25, v32, v44\n" \
"v_fmac_f32 v26, v38, v48\n" \
"v_mfma_f32_16x16x32_bf16 v[216:219], a[16:19], a[104:107], v[216:219]\n" \
"ds_read_b64_tr_b16 a[80:81], v9 offset:512\n" \
"v_fmac_f32 v25, v33, v43\n" \
"v_mfma_f32_16x16x32_bf16 v[216:219], a[20:23], a[108:111], v[216:219]\n" \
"v_fmac_f32 v26, v39, v47\n" \
"v_fmac_f32 v25, v34, v42\n" \
"v_mfma_f32_16x16x32_bf16 v[220:223], a[24:27], a[104:107], v[220:223]\n" \
"ds_read_b64_tr_b16 a[82:83], v10 offset:512\n" \
"v_fmac_f32 v26, v40, v46\n" \
"v_mfma_f32_16x16x32_bf16 v[220:223], a[28:31], a[108:111], v[220:223]\n" \
"v_accvgpr_read_b32 v28, a229\n" \
"v_lshlrev_b32 v27, 16, v28\n" \
"v_mfma_f32_16x16x32_bf16 v[232:235], a[32:35], a[104:107], v[232:235]\n" \
"ds_read_b64_tr_b16 a[84:85], v9 offset:1536\n" \
"v_and_b32 v28, 0xffff0000, v28\n" \
"v_mfma_f32_16x16x32_bf16 v[232:235], a[36:39], a[108:111], v[232:235]\n" \
"v_mul_f32 v25, v25, v27\n" \
"v_mul_f32 v26, v26, v28\n" \
"v_mfma_f32_16x16x32_bf16 v[236:239], a[40:43], a[104:107], v[236:239]\n" \
"ds_read_b64_tr_b16 a[86:87], v10 offset:1536\n" \
"v_cvt_pk_bf16_f32 v50, v25, v29\n" \
"v_mfma_f32_16x16x32_bf16 v[236:239], a[44:47], a[108:111], v[236:239]\n" \
"s_mov_b32 s94, 79699968\n" \
"v_cvt_pk_bf16_f32 v54, v26, v58\n" \
"v_mfma_f32_16x16x32_bf16 v[248:251], a[48:51], a[104:107], v[248:251]\n" \
"ds_read_b64_tr_b16 a[88:89], v11 offset:512\n" \
"v_mfma_f32_16x16x32_bf16 v[248:251], a[52:55], a[108:111], v[248:251]\n" \
"buffer_store_dwordx4 v[50:53], v60, s[36:39], s94 offen offset:0 sc1\n" \
"v_mfma_f32_16x16x32_bf16 v[252:255], a[56:59], a[104:107], v[252:255]\n" \
"s_mov_b32 s95, 79704064\n" \
"ds_read_b64_tr_b16 a[90:91], v12 offset:512\n" \
"v_mfma_f32_16x16x32_bf16 v[252:255], a[60:63], a[108:111], v[252:255]\n" \
"v_mfma_f32_16x16x32_bf16 a[128:131], a[0:3], a[112:115], a[128:131]\n" \
"ds_read_b64_tr_b16 a[92:93], v11 offset:1536\n" \
"v_mfma_f32_16x16x32_bf16 a[128:131], a[4:7], a[116:119], a[128:131]\n" \
"v_mfma_f32_16x16x32_bf16 a[132:135], a[8:11], a[112:115], a[132:135]\n" \
"ds_read_b64_tr_b16 a[94:95], v12 offset:1536\n" \
"v_mfma_f32_16x16x32_bf16 a[132:135], a[12:15], a[116:119], a[132:135]\n" \
"v_mfma_f32_16x16x32_bf16 a[144:147], a[16:19], a[112:115], a[144:147]\n" \
"v_mfma_f32_16x16x32_bf16 a[144:147], a[20:23], a[116:119], a[144:147]\n" \
"v_mfma_f32_16x16x32_bf16 a[148:151], a[24:27], a[112:115], a[148:151]\n" \
"v_mfma_f32_16x16x32_bf16 a[148:151], a[28:31], a[116:119], a[148:151]\n" \
"v_mfma_f32_16x16x32_bf16 a[160:163], a[32:35], a[112:115], a[160:163]\n" \
"v_mfma_f32_16x16x32_bf16 a[160:163], a[36:39], a[116:119], a[160:163]\n" \
"v_mfma_f32_16x16x32_bf16 a[164:167], a[40:43], a[112:115], a[164:167]\n" \
"v_mfma_f32_16x16x32_bf16 a[164:167], a[44:47], a[116:119], a[164:167]\n" \
"s_waitcnt vmcnt(13) lgkmcnt(0)\n" \
"v_mfma_f32_16x16x32_bf16 a[176:179], a[48:51], a[112:115], a[176:179]\n" \
"s_barrier\n" \
"v_mfma_f32_16x16x32_bf16 a[176:179], a[52:55], a[116:119], a[176:179]\n" \
"buffer_store_dwordx4 v[54:57], v60, s[36:39], s95 offen offset:0 sc1\n" \
"v_mfma_f32_16x16x32_bf16 a[180:183], a[56:59], a[112:115], a[180:183]\n" \
"s_lshl_b32 s62, s8, 2\n" \
"s_add_u32 s62, s62, %2\n" \
"s_lshl_b32 s63, s62, 12\n" \
"v_mfma_f32_16x16x32_bf16 a[180:183], a[60:63], a[116:119], a[180:183]\n" \
"s_add_u32 s68, s10, s63\ns_addc_u32 s69, s11, 0\n" \
"s_and_b32 s69, s69, 0xffff\n" \
"v_mfma_f32_16x16x32_bf16 a[136:139], a[0:3], a[120:123], a[136:139]\n" \
"s_mov_b32 s70, 4096\n" \
"s_mov_b32 s71, 0x00020000\n" \
"s_add_u32 s63, s63, 79691776\n" \
"v_mfma_f32_16x16x32_bf16 a[136:139], a[4:7], a[124:127], a[136:139]\n" \
"s_add_u32 s72, s4, s63\ns_addc_u32 s73, s5, 0\n" \
"s_and_b32 s73, s73, 0xffff\n" \
"v_mfma_f32_16x16x32_bf16 a[140:143], a[8:11], a[120:123], a[140:143]\n" \
"s_mov_b32 s74, 4096\n" \
"ds_read_b128 a[0:3], v5 offset:0\n" \
"s_mov_b32 s75, 0x00020000\n" \
"v_mfma_f32_16x16x32_bf16 a[140:143], a[12:15], a[124:127], a[140:143]\n" \
"ds_read_b128 a[4:7], v6 offset:0\n" \
"v_add_u32 v23, -32, v4\n" \
"v_mfma_f32_16x16x32_bf16 a[152:155], a[16:19], a[120:123], a[152:155]\n" \
"ds_read_b128 a[8:11], v5 offset:2048\n" \
"v_add_u32 v24, 992, v4\n" \
"v_mfma_f32_16x16x32_bf16 a[152:155], a[20:23], a[124:127], a[152:155]\n" \
"ds_read_b128 a[12:15], v6 offset:2048\n" \
"v_add_u32 v25, -16, v4\n" \
"v_mfma_f32_16x16x32_bf16 a[156:159], a[24:27], a[120:123], a[156:159]\n" \
"ds_read_b128 a[16:19], v5 offset:4096\n" \
"v_add_u32 v26, -512, v4\n" \
"v_mfma_f32_16x16x32_bf16 a[156:159], a[28:31], a[124:127], a[156:159]\n" \
"s_lshr_b32 s63, %2, 1\n" \
"ds_read_b128 a[20:23], v6 offset:4096\n" \
"v_add_u32 v27, 512, v4\n" \
"v_mfma_f32_16x16x32_bf16 a[168:171], a[32:35], a[120:123], a[168:171]\n" \
"s_lshl_b32 s63, s63, 16\n" \
"ds_read_b128 a[24:27], v5 offset:6144\n" \
"s_lshl_b32 s56, %2, 14\n" \
"v_mfma_f32_16x16x32_bf16 a[168:171], a[36:39], a[124:127], a[168:171]\n" \
"s_add_u32 s56, s56, s63\n" \
"ds_read_b128 a[28:31], v6 offset:6144\n" \
"v_add_u32 v55, s56, v4\n" \
"v_mfma_f32_16x16x32_bf16 a[172:175], a[40:43], a[120:123], a[172:175]\n" \
"s_lshl_b32 s62, s62, 1\n" \
"ds_read_b128 a[32:35], v5 offset:8192\n" \
"v_mov_b32 v28, 0\n" \
"v_mfma_f32_16x16x32_bf16 a[172:175], a[44:47], a[124:127], a[172:175]\n" \
"ds_read_b128 a[36:39], v6 offset:8192\n" \
"v_mov_b32 v29, s62\n" \
"v_mfma_f32_16x16x32_bf16 a[184:187], a[48:51], a[120:123], a[184:187]\n" \
"s_and_b32 s62, %1, 7\n" \
"ds_read_b128 a[40:43], v5 offset:10240\n" \
"v_lshlrev_b32 v56, 23, v4\n" \
"v_mfma_f32_16x16x32_bf16 a[184:187], a[52:55], a[124:127], a[184:187]\n" \
"s_lshl_b32 s62, s62, 8\n" \
"ds_read_b128 a[44:47], v6 offset:10240\n" \
"s_add_u32 s62, s62, 94380032\n" \
"v_mfma_f32_16x16x32_bf16 a[188:191], a[56:59], a[120:123], a[188:191]\n" \
"ds_read_b128 a[48:51], v5 offset:12288\n" \
"v_bfe_u32 v57, v4, 4, 5\n" \
"v_mfma_f32_16x16x32_bf16 a[188:191], a[60:63], a[124:127], a[188:191]\n" \
"s_waitcnt lgkmcnt(9)\n" \
"ds_read_b128 a[52:55], v6 offset:12288\n" \
"v_lshl_add_u32 v57, v57, 3, s62\n" \
"v_mfma_f32_16x16x32_bf16 v[64:67], a[0:3], a[64:67], v[64:67]\n" \
"s_mov_b32 s47, 2621440\n" \
"s_add_u32 m0, s22, 49024\n" \
"ds_read_b128 a[56:59], v5 offset:14336\n" \
"v_mfma_f32_16x16x32_bf16 v[64:67], a[4:7], a[68:71], v[64:67]\n" \
"buffer_load_dwordx4 v21, s[32:35], s47 offen offset:128 lds\n" \
"v_mfma_f32_16x16x32_bf16 v[68:71], a[8:11], a[64:67], v[68:71]\n" \
"s_waitcnt lgkmcnt(7)\n" \
"ds_read_b128 a[60:63], v6 offset:14336\n" \
"v_mfma_f32_16x16x32_bf16 v[68:71], a[12:15], a[68:71], v[68:71]\n" \
"v_mfma_f32_16x16x32_bf16 v[80:83], a[16:19], a[64:67], v[80:83]\n" \
"s_add_u32 m0, s22, 50816\n" \
"ds_read_b64_tr_b16 a[96:97], v9 offset:16384\n" \
"v_mfma_f32_16x16x32_bf16 v[80:83], a[20:23], a[68:71], v[80:83]\n" \
"buffer_load_dwordx4 v21, s[32:35], s47 offen offset:384 lds\n" \
"v_mfma_f32_16x16x32_bf16 v[84:87], a[24:27], a[64:67], v[84:87]\n" \
"s_waitcnt lgkmcnt(5)\n" \
"ds_read_b64_tr_b16 a[98:99], v10 offset:16384\n" \
"v_mfma_f32_16x16x32_bf16 v[84:87], a[28:31], a[68:71], v[84:87]\n" \
"v_mfma_f32_16x16x32_bf16 v[96:99], a[32:35], a[64:67], v[96:99]\n" \
"s_mov_b32 s47, 2883584\n" \
"s_add_u32 m0, s22, 50048\n" \
"ds_read_b64_tr_b16 a[100:101], v9 offset:17408\n" \
"v_mfma_f32_16x16x32_bf16 v[96:99], a[36:39], a[68:71], v[96:99]\n" \
"buffer_load_dwordx4 v21, s[32:35], s47 offen offset:128 lds\n" \
"v_mfma_f32_16x16x32_bf16 v[100:103], a[40:43], a[64:67], v[100:103]\n" \
"s_waitcnt lgkmcnt(3)\n" \
"ds_read_b64_tr_b16 a[102:103], v10 offset:17408\n" \
"v_mfma_f32_16x16x32_bf16 v[100:103], a[44:47], a[68:71], v[100:103]\n" \
"v_mfma_f32_16x16x32_bf16 v[112:115], a[48:51], a[64:67], v[112:115]\n" \
"s_add_u32 m0, s22, 51840\n" \
"ds_read_b64_tr_b16 a[104:105], v11 offset:16384\n" \
"v_mfma_f32_16x16x32_bf16 v[112:115], a[52:55], a[68:71], v[112:115]\n" \
"buffer_load_dwordx4 v21, s[32:35], s47 offen offset:384 lds\n" \
"v_mfma_f32_16x16x32_bf16 v[116:119], a[56:59], a[64:67], v[116:119]\n" \
"ds_read_b64_tr_b16 a[106:107], v12 offset:16384\n" \
"v_mfma_f32_16x16x32_bf16 v[116:119], a[60:63], a[68:71], v[116:119]\n" \
"v_mfma_f32_16x16x32_bf16 v[72:75], a[0:3], a[72:75], v[72:75]\n" \
"s_mov_b32 s46, 768\n" \
"s_add_u32 m0, s50, 65536\n" \
"ds_read_b64_tr_b16 a[108:109], v11 offset:17408\n" \
"v_mfma_f32_16x16x32_bf16 v[72:75], a[4:7], a[76:79], v[72:75]\n" \
"buffer_load_dwordx4 v13, s[28:31], s46 offen lds\n" \
"v_mfma_f32_16x16x32_bf16 v[76:79], a[8:11], a[72:75], v[76:79]\n" \
"ds_read_b64_tr_b16 a[110:111], v12 offset:17408\n" \
"v_mfma_f32_16x16x32_bf16 v[76:79], a[12:15], a[76:79], v[76:79]\n" \
"v_mfma_f32_16x16x32_bf16 v[88:91], a[16:19], a[72:75], v[88:91]\n" \
"s_add_u32 m0, s50, 69632\n" \
"ds_read_b64_tr_b16 a[112:113], v9 offset:16896\n" \
"v_mfma_f32_16x16x32_bf16 v[88:91], a[20:23], a[76:79], v[88:91]\n" \
"buffer_load_dwordx4 v14, s[28:31], s46 offen lds\n" \
"v_mfma_f32_16x16x32_bf16 v[92:95], a[24:27], a[72:75], v[92:95]\n" \
"ds_read_b64_tr_b16 a[114:115], v10 offset:16896\n" \
"v_mfma_f32_16x16x32_bf16 v[92:95], a[28:31], a[76:79], v[92:95]\n" \
"v_mfma_f32_16x16x32_bf16 v[104:107], a[32:35], a[72:75], v[104:107]\n" \
"s_add_u32 m0, s50, 73728\n" \
"ds_read_b64_tr_b16 a[116:117], v9 offset:17920\n" \
"v_mfma_f32_16x16x32_bf16 v[104:107], a[36:39], a[76:79], v[104:107]\n" \
"buffer_load_dwordx4 v15, s[28:31], s46 offen lds\n" \
"v_mfma_f32_16x16x32_bf16 v[108:111], a[40:43], a[72:75], v[108:111]\n" \
"ds_read_b64_tr_b16 a[118:119], v10 offset:17920\n" \
"v_mfma_f32_16x16x32_bf16 v[108:111], a[44:47], a[76:79], v[108:111]\n" \
"v_mfma_f32_16x16x32_bf16 v[120:123], a[48:51], a[72:75], v[120:123]\n" \
"s_add_u32 m0, s50, 77824\n" \
"ds_read_b64_tr_b16 a[120:121], v11 offset:16896\n" \
"v_mfma_f32_16x16x32_bf16 v[120:123], a[52:55], a[76:79], v[120:123]\n" \
"buffer_load_dwordx4 v16, s[28:31], s46 offen lds\n" \
"v_mfma_f32_16x16x32_bf16 v[124:127], a[56:59], a[72:75], v[124:127]\n" \
"ds_read_b64_tr_b16 a[122:123], v12 offset:16896\n" \
"v_mfma_f32_16x16x32_bf16 v[124:127], a[60:63], a[76:79], v[124:127]\n" \
"v_mfma_f32_16x16x32_bf16 v[128:131], a[0:3], a[80:83], v[128:131]\n" \
"s_mov_b32 s46, 768\n" \
"s_add_u32 m0, s50, 81920\n" \
"ds_read_b64_tr_b16 a[124:125], v11 offset:17920\n" \
"v_mfma_f32_16x16x32_bf16 v[128:131], a[4:7], a[84:87], v[128:131]\n" \
"buffer_load_dwordx4 v17, s[28:31], s46 offen lds\n" \
"v_mfma_f32_16x16x32_bf16 v[132:135], a[8:11], a[80:83], v[132:135]\n" \
"ds_read_b64_tr_b16 a[126:127], v12 offset:17920\n" \
"v_mfma_f32_16x16x32_bf16 v[132:135], a[12:15], a[84:87], v[132:135]\n" \
"v_mfma_f32_16x16x32_bf16 v[144:147], a[16:19], a[80:83], v[144:147]\n" \
"s_add_u32 m0, s50, 86016\n" \
"v_mfma_f32_16x16x32_bf16 v[144:147], a[20:23], a[84:87], v[144:147]\n" \
"buffer_load_dwordx4 v18, s[28:31], s46 offen lds\n" \
"v_mfma_f32_16x16x32_bf16 v[148:151], a[24:27], a[80:83], v[148:151]\n" \
"v_mfma_f32_16x16x32_bf16 v[148:151], a[28:31], a[84:87], v[148:151]\n" \
"v_mfma_f32_16x16x32_bf16 v[160:163], a[32:35], a[80:83], v[160:163]\n" \
"s_add_u32 m0, s50, 90112\n" \
"v_mfma_f32_16x16x32_bf16 v[160:163], a[36:39], a[84:87], v[160:163]\n" \
"buffer_load_dwordx4 v19, s[28:31], s46 offen lds\n" \
"v_mfma_f32_16x16x32_bf16 v[164:167], a[40:43], a[80:83], v[164:167]\n" \
"v_mfma_f32_16x16x32_bf16 v[164:167], a[44:47], a[84:87], v[164:167]\n" \
"s_waitcnt vmcnt(16) lgkmcnt(0)\n" \
"v_mfma_f32_16x16x32_bf16 v[176:179], a[48:51], a[80:83], v[176:179]\n" \
"s_add_u32 m0, s50, 94208\n" \
"s_barrier\n" \
"v_mfma_f32_16x16x32_bf16 v[176:179], a[52:55], a[84:87], v[176:179]\n" \
"buffer_load_dwordx4 v20, s[28:31], s46 offen lds\n" \
"v_mfma_f32_16x16x32_bf16 v[180:183], a[56:59], a[80:83], v[180:183]\n" \
"v_mfma_f32_16x16x32_bf16 v[180:183], a[60:63], a[84:87], v[180:183]\n" \
"v_mfma_f32_16x16x32_bf16 v[136:139], a[0:3], a[88:91], v[136:139]\n" \
"s_mov_b32 s47, 3145728\n" \
"s_add_u32 m0, s22, 0\n" \
"v_mfma_f32_16x16x32_bf16 v[136:139], a[4:7], a[92:95], v[136:139]\n" \
"buffer_load_dwordx4 v21, s[32:35], s47 offen offset:0 lds\n" \
"v_mfma_f32_16x16x32_bf16 v[140:143], a[8:11], a[88:91], v[140:143]\n" \
"v_mfma_f32_16x16x32_bf16 v[140:143], a[12:15], a[92:95], v[140:143]\n" \
"v_mfma_f32_16x16x32_bf16 v[152:155], a[16:19], a[88:91], v[152:155]\n" \
"s_add_u32 m0, s22, 1792\n" \
"v_mfma_f32_16x16x32_bf16 v[152:155], a[20:23], a[92:95], v[152:155]\n" \
"buffer_load_dwordx4 v21, s[32:35], s47 offen offset:256 lds\n" \
"v_mfma_f32_16x16x32_bf16 v[156:159], a[24:27], a[88:91], v[156:159]\n" \
"v_mfma_f32_16x16x32_bf16 v[156:159], a[28:31], a[92:95], v[156:159]\n" \
"v_mfma_f32_16x16x32_bf16 v[168:171], a[32:35], a[88:91], v[168:171]\n" \
"s_mov_b32 s47, 3407872\n" \
"s_add_u32 m0, s22, 1024\n" \
"v_mfma_f32_16x16x32_bf16 v[168:171], a[36:39], a[92:95], v[168:171]\n" \
"buffer_load_dwordx4 v21, s[32:35], s47 offen offset:0 lds\n" \
"v_mfma_f32_16x16x32_bf16 v[172:175], a[40:43], a[88:91], v[172:175]\n" \
"s_add_u32 s90, s64, s53\ns_addc_u32 s91, s65, 0\n" \
"v_mfma_f32_16x16x32_bf16 v[172:175], a[44:47], a[92:95], v[172:175]\n" \
"v_mfma_f32_16x16x32_bf16 v[184:187], a[48:51], a[88:91], v[184:187]\n" \
"s_add_u32 m0, s22, 2816\n" \
"v_mfma_f32_16x16x32_bf16 v[184:187], a[52:55], a[92:95], v[184:187]\n" \
"buffer_load_dwordx4 v21, s[32:35], s47 offen offset:256 lds\n" \
"v_mfma_f32_16x16x32_bf16 v[188:191], a[56:59], a[88:91], v[188:191]\n" \
"v_mfma_f32_16x16x32_bf16 v[188:191], a[60:63], a[92:95], v[188:191]\n" \
"v_mfma_f32_16x16x32_bf16 v[192:195], a[0:3], a[96:99], v[192:195]\n" \
"v_mfma_f32_16x16x32_bf16 v[192:195], a[4:7], a[100:103], v[192:195]\n" \
"global_load_dwordx4 a[240:243], v4, s[90:91] offset:0\n" \
"v_mfma_f32_16x16x32_bf16 v[196:199], a[8:11], a[96:99], v[196:199]\n" \
"v_mfma_f32_16x16x32_bf16 v[196:199], a[12:15], a[100:103], v[196:199]\n" \
"v_mfma_f32_16x16x32_bf16 v[208:211], a[16:19], a[96:99], v[208:211]\n" \
"ds_read_b64_tr_b16 a[64:65], v9 offset:32768\n" \
"v_mfma_f32_16x16x32_bf16 v[208:211], a[20:23], a[100:103], v[208:211]\n" \
"global_load_dwordx4 a[244:247], v4, s[90:91] offset:1024\n" \
"v_mfma_f32_16x16x32_bf16 v[212:215], a[24:27], a[96:99], v[212:215]\n" \
"ds_read_b64_tr_b16 a[66:67], v10 offset:32768\n" \
"v_mfma_f32_16x16x32_bf16 v[212:215], a[28:31], a[100:103], v[212:215]\n" \
"v_mfma_f32_16x16x32_bf16 v[224:227], a[32:35], a[96:99], v[224:227]\n" \
"ds_read_b64_tr_b16 a[68:69], v9 offset:33792\n" \
"v_mfma_f32_16x16x32_bf16 v[224:227], a[36:39], a[100:103], v[224:227]\n" \
"global_load_dwordx4 a[248:251], v4, s[90:91] offset:2048\n" \
"v_mfma_f32_16x16x32_bf16 v[228:231], a[40:43], a[96:99], v[228:231]\n" \
"ds_read_b64_tr_b16 a[70:71], v10 offset:33792\n" \
"v_mfma_f32_16x16x32_bf16 v[228:231], a[44:47], a[100:103], v[228:231]\n" \
"v_mfma_f32_16x16x32_bf16 v[240:243], a[48:51], a[96:99], v[240:243]\n" \
"ds_read_b64_tr_b16 a[72:73], v11 offset:32768\n" \
"v_mfma_f32_16x16x32_bf16 v[240:243], a[52:55], a[100:103], v[240:243]\n" \
"global_load_dwordx4 a[252:255], v4, s[90:91] offset:3072\n" \
"v_mfma_f32_16x16x32_bf16 v[244:247], a[56:59], a[96:99], v[244:247]\n" \
"ds_read_b64_tr_b16 a[74:75], v12 offset:32768\n" \
"v_mfma_f32_16x16x32_bf16 v[244:247], a[60:63], a[100:103], v[244:247]\n" \
"v_mfma_f32_16x16x32_bf16 v[200:203], a[0:3], a[104:107], v[200:203]\n" \
"ds_read_b64_tr_b16 a[76:77], v11 offset:33792\n" \
"v_mfma_f32_16x16x32_bf16 v[200:203], a[4:7], a[108:111], v[200:203]\n" \
"v_mfma_f32_16x16x32_bf16 v[204:207], a[8:11], a[104:107], v[204:207]\n" \
"ds_read_b64_tr_b16 a[78:79], v12 offset:33792\n" \
"v_mfma_f32_16x16x32_bf16 v[204:207], a[12:15], a[108:111], v[204:207]\n" \
"v_mfma_f32_16x16x32_bf16 v[216:219], a[16:19], a[104:107], v[216:219]\n" \
"ds_read_b64_tr_b16 a[80:81], v9 offset:33280\n" \
"v_mfma_f32_16x16x32_bf16 v[216:219], a[20:23], a[108:111], v[216:219]\n" \
"v_mfma_f32_16x16x32_bf16 v[220:223], a[24:27], a[104:107], v[220:223]\n" \
"ds_read_b64_tr_b16 a[82:83], v10 offset:33280\n" \
"v_mfma_f32_16x16x32_bf16 v[220:223], a[28:31], a[108:111], v[220:223]\n" \
"v_mfma_f32_16x16x32_bf16 v[232:235], a[32:35], a[104:107], v[232:235]\n" \
"ds_read_b64_tr_b16 a[84:85], v9 offset:34304\n" \
"v_mfma_f32_16x16x32_bf16 v[232:235], a[36:39], a[108:111], v[232:235]\n" \
"v_mfma_f32_16x16x32_bf16 v[236:239], a[40:43], a[104:107], v[236:239]\n" \
"ds_read_b64_tr_b16 a[86:87], v10 offset:34304\n" \
"v_mfma_f32_16x16x32_bf16 v[236:239], a[44:47], a[108:111], v[236:239]\n" \
"v_mfma_f32_16x16x32_bf16 v[248:251], a[48:51], a[104:107], v[248:251]\n" \
"ds_read_b64_tr_b16 a[88:89], v11 offset:33280\n" \
"v_mfma_f32_16x16x32_bf16 v[248:251], a[52:55], a[108:111], v[248:251]\n" \
"v_mfma_f32_16x16x32_bf16 v[252:255], a[56:59], a[104:107], v[252:255]\n" \
"ds_read_b64_tr_b16 a[90:91], v12 offset:33280\n" \
"v_mfma_f32_16x16x32_bf16 v[252:255], a[60:63], a[108:111], v[252:255]\n" \
"v_mfma_f32_16x16x32_bf16 a[128:131], a[0:3], a[112:115], a[128:131]\n" \
"ds_read_b64_tr_b16 a[92:93], v11 offset:34304\n" \
"v_mfma_f32_16x16x32_bf16 a[128:131], a[4:7], a[116:119], a[128:131]\n" \
"v_mfma_f32_16x16x32_bf16 a[132:135], a[8:11], a[112:115], a[132:135]\n" \
"ds_read_b64_tr_b16 a[94:95], v12 offset:34304\n" \
"v_mfma_f32_16x16x32_bf16 a[132:135], a[12:15], a[116:119], a[132:135]\n" \
"v_mfma_f32_16x16x32_bf16 a[144:147], a[16:19], a[112:115], a[144:147]\n" \
"v_mfma_f32_16x16x32_bf16 a[144:147], a[20:23], a[116:119], a[144:147]\n" \
"v_mfma_f32_16x16x32_bf16 a[148:151], a[24:27], a[112:115], a[148:151]\n" \
"v_mfma_f32_16x16x32_bf16 a[148:151], a[28:31], a[116:119], a[148:151]\n" \
"global_load_dwordx4 a[192:195], v4, s[86:87] offset:0\n" \
"v_mfma_f32_16x16x32_bf16 a[160:163], a[32:35], a[112:115], a[160:163]\n" \
"v_mov_b32 v58, s14\n" \
"v_mfma_f32_16x16x32_bf16 a[160:163], a[36:39], a[116:119], a[160:163]\n" \
"v_mov_b32 v59, s15\n" \
"v_mfma_f32_16x16x32_bf16 a[164:167], a[40:43], a[112:115], a[164:167]\n" \
"v_mfma_f32_16x16x32_bf16 a[164:167], a[44:47], a[116:119], a[164:167]\n" \
"s_waitcnt vmcnt(17) lgkmcnt(0)\n" \
"v_mfma_f32_16x16x32_bf16 a[176:179], a[48:51], a[112:115], a[176:179]\n" \
"s_barrier\n" \
"v_mfma_f32_16x16x32_bf16 a[176:179], a[52:55], a[116:119], a[176:179]\n" \
"v_mfma_f32_16x16x32_bf16 a[180:183], a[56:59], a[112:115], a[180:183]\n" \
"v_mfma_f32_16x16x32_bf16 a[180:183], a[60:63], a[116:119], a[180:183]\n" \
"buffer_store_dwordx2 v[58:59], v56, s[36:39], s23 offen sc1\n" \
"v_mfma_f32_16x16x32_bf16 a[136:139], a[0:3], a[120:123], a[136:139]\n" \
"v_mfma_f32_16x16x32_bf16 a[136:139], a[4:7], a[124:127], a[136:139]\n" \
"v_mfma_f32_16x16x32_bf16 a[140:143], a[8:11], a[120:123], a[140:143]\n" \
"ds_read_b128 a[0:3], v5 offset:32768\n" \
"v_mfma_f32_16x16x32_bf16 a[140:143], a[12:15], a[124:127], a[140:143]\n" \
"ds_read_b128 a[4:7], v6 offset:32768\n" \
"v_mfma_f32_16x16x32_bf16 a[152:155], a[16:19], a[120:123], a[152:155]\n" \
"ds_read_b128 a[8:11], v5 offset:34816\n" \
"v_mfma_f32_16x16x32_bf16 a[152:155], a[20:23], a[124:127], a[152:155]\n" \
"ds_read_b128 a[12:15], v6 offset:34816\n" \
"v_mfma_f32_16x16x32_bf16 a[156:159], a[24:27], a[120:123], a[156:159]\n" \
"ds_read_b128 a[16:19], v5 offset:36864\n" \
"v_mfma_f32_16x16x32_bf16 a[156:159], a[28:31], a[124:127], a[156:159]\n" \
"ds_read_b128 a[20:23], v6 offset:36864\n" \
"v_mfma_f32_16x16x32_bf16 a[168:171], a[32:35], a[120:123], a[168:171]\n" \
"ds_read_b128 a[24:27], v5 offset:38912\n" \
"v_mfma_f32_16x16x32_bf16 a[168:171], a[36:39], a[124:127], a[168:171]\n" \
"ds_read_b128 a[28:31], v6 offset:38912\n" \
"v_mfma_f32_16x16x32_bf16 a[172:175], a[40:43], a[120:123], a[172:175]\n" \
"ds_read_b128 a[32:35], v5 offset:40960\n" \
"v_mfma_f32_16x16x32_bf16 a[172:175], a[44:47], a[124:127], a[172:175]\n" \
"ds_read_b128 a[36:39], v6 offset:40960\n" \
"v_mfma_f32_16x16x32_bf16 a[184:187], a[48:51], a[120:123], a[184:187]\n" \
"ds_read_b128 a[40:43], v5 offset:43008\n" \
"v_mfma_f32_16x16x32_bf16 a[184:187], a[52:55], a[124:127], a[184:187]\n" \
"ds_read_b128 a[44:47], v6 offset:43008\n" \
"v_mfma_f32_16x16x32_bf16 a[188:191], a[56:59], a[120:123], a[188:191]\n" \
"ds_read_b128 a[48:51], v5 offset:45056\n" \
"v_mfma_f32_16x16x32_bf16 a[188:191], a[60:63], a[124:127], a[188:191]\n" \
"s_waitcnt lgkmcnt(9)\n" \
"ds_read_b128 a[52:55], v6 offset:45056\n" \
"v_mfma_f32_16x16x32_bf16 v[64:67], a[0:3], a[64:67], v[64:67]\n" \
"s_mov_b32 s47, 3145728\n" \
"s_add_u32 m0, s22, 16256\n" \
"ds_read_b128 a[56:59], v5 offset:47104\n" \
"v_mfma_f32_16x16x32_bf16 v[64:67], a[4:7], a[68:71], v[64:67]\n" \
"buffer_load_dwordx4 v21, s[32:35], s47 offen offset:128 lds\n" \
"v_mfma_f32_16x16x32_bf16 v[68:71], a[8:11], a[64:67], v[68:71]\n" \
"s_waitcnt lgkmcnt(7)\n" \
"ds_read_b128 a[60:63], v6 offset:47104\n" \
"v_mfma_f32_16x16x32_bf16 v[68:71], a[12:15], a[68:71], v[68:71]\n" \
"v_mfma_f32_16x16x32_bf16 v[80:83], a[16:19], a[64:67], v[80:83]\n" \
"s_add_u32 m0, s22, 18048\n" \
"ds_read_b64_tr_b16 a[96:97], v9 offset:49152\n" \
"v_mfma_f32_16x16x32_bf16 v[80:83], a[20:23], a[68:71], v[80:83]\n" \
"LH6_%=:\ns_cbranch_execz LH10_%=\n" \
"buffer_load_dwordx4 v21, s[32:35], s47 offen offset:384 lds\n" \
"v_mfma_f32_16x16x32_bf16 v[84:87], a[24:27], a[64:67], v[84:87]\n" \
"s_waitcnt lgkmcnt(5)\n" \
"ds_read_b64_tr_b16 a[98:99], v10 offset:49152\n" \
"v_mfma_f32_16x16x32_bf16 v[84:87], a[28:31], a[68:71], v[84:87]\n" \
"v_mfma_f32_16x16x32_bf16 v[96:99], a[32:35], a[64:67], v[96:99]\n" \
"s_mov_b32 s47, 3407872\n" \
"s_add_u32 m0, s22, 17280\n" \
"ds_read_b64_tr_b16 a[100:101], v9 offset:50176\n" \
"v_mfma_f32_16x16x32_bf16 v[96:99], a[36:39], a[68:71], v[96:99]\n" \
"buffer_load_dwordx4 v21, s[32:35], s47 offen offset:128 lds\n" \
"v_mfma_f32_16x16x32_bf16 v[100:103], a[40:43], a[64:67], v[100:103]\n" \
"s_waitcnt lgkmcnt(3)\n" \
"ds_read_b64_tr_b16 a[102:103], v10 offset:50176\n" \
"v_mfma_f32_16x16x32_bf16 v[100:103], a[44:47], a[68:71], v[100:103]\n" \
"v_mfma_f32_16x16x32_bf16 v[112:115], a[48:51], a[64:67], v[112:115]\n" \
"s_add_u32 m0, s22, 19072\n" \
"ds_read_b64_tr_b16 a[104:105], v11 offset:49152\n" \
"v_mfma_f32_16x16x32_bf16 v[112:115], a[52:55], a[68:71], v[112:115]\n" \
"buffer_load_dwordx4 v21, s[32:35], s47 offen offset:384 lds\n" \
"v_mfma_f32_16x16x32_bf16 v[116:119], a[56:59], a[64:67], v[116:119]\n" \
"ds_read_b64_tr_b16 a[106:107], v12 offset:49152\n" \
"v_mfma_f32_16x16x32_bf16 v[116:119], a[60:63], a[68:71], v[116:119]\n" \
"v_mfma_f32_16x16x32_bf16 v[72:75], a[0:3], a[72:75], v[72:75]\n" \
"s_mov_b32 s46, 896\n" \
"s_add_u32 m0, s50, 0\n" \
"ds_read_b64_tr_b16 a[108:109], v11 offset:50176\n" \
"v_mfma_f32_16x16x32_bf16 v[72:75], a[4:7], a[76:79], v[72:75]\n" \
"buffer_load_dwordx4 v13, s[28:31], s46 offen lds\n" \
"v_mfma_f32_16x16x32_bf16 v[76:79], a[8:11], a[72:75], v[76:79]\n" \
"ds_read_b64_tr_b16 a[110:111], v12 offset:50176\n" \
"v_mfma_f32_16x16x32_bf16 v[76:79], a[12:15], a[76:79], v[76:79]\n" \
"v_mfma_f32_16x16x32_bf16 v[88:91], a[16:19], a[72:75], v[88:91]\n" \
"s_add_u32 m0, s50, 4096\n" \
"ds_read_b64_tr_b16 a[112:113], v9 offset:49664\n" \
"v_mfma_f32_16x16x32_bf16 v[88:91], a[20:23], a[76:79], v[88:91]\n" \
"buffer_load_dwordx4 v14, s[28:31], s46 offen lds\n" \
"v_mfma_f32_16x16x32_bf16 v[92:95], a[24:27], a[72:75], v[92:95]\n" \
"ds_read_b64_tr_b16 a[114:115], v10 offset:49664\n" \
"v_mfma_f32_16x16x32_bf16 v[92:95], a[28:31], a[76:79], v[92:95]\n" \
"v_mfma_f32_16x16x32_bf16 v[104:107], a[32:35], a[72:75], v[104:107]\n" \
"s_add_u32 m0, s50, 8192\n" \
"ds_read_b64_tr_b16 a[116:117], v9 offset:50688\n" \
"v_mfma_f32_16x16x32_bf16 v[104:107], a[36:39], a[76:79], v[104:107]\n" \
"buffer_load_dwordx4 v15, s[28:31], s46 offen lds\n" \
"v_mfma_f32_16x16x32_bf16 v[108:111], a[40:43], a[72:75], v[108:111]\n" \
"ds_read_b64_tr_b16 a[118:119], v10 offset:50688\n" \
"v_mfma_f32_16x16x32_bf16 v[108:111], a[44:47], a[76:79], v[108:111]\n" \
"v_mfma_f32_16x16x32_bf16 v[120:123], a[48:51], a[72:75], v[120:123]\n" \
"s_add_u32 m0, s50, 12288\n" \
"ds_read_b64_tr_b16 a[120:121], v11 offset:49664\n" \
"v_mfma_f32_16x16x32_bf16 v[120:123], a[52:55], a[76:79], v[120:123]\n" \
"buffer_load_dwordx4 v16, s[28:31], s46 offen lds\n" \
"v_mfma_f32_16x16x32_bf16 v[124:127], a[56:59], a[72:75], v[124:127]\n" \
"ds_read_b64_tr_b16 a[122:123], v12 offset:49664\n" \
"v_mfma_f32_16x16x32_bf16 v[124:127], a[60:63], a[76:79], v[124:127]\n" \
"v_mfma_f32_16x16x32_bf16 v[128:131], a[0:3], a[80:83], v[128:131]\n" \
"s_mov_b32 s46, 896\n" \
"s_add_u32 m0, s50, 16384\n" \
"ds_read_b64_tr_b16 a[124:125], v11 offset:50688\n" \
"v_mfma_f32_16x16x32_bf16 v[128:131], a[4:7], a[84:87], v[128:131]\n" \
"buffer_load_dwordx4 v17, s[28:31], s46 offen lds\n" \
"v_mfma_f32_16x16x32_bf16 v[132:135], a[8:11], a[80:83], v[132:135]\n" \
"ds_read_b64_tr_b16 a[126:127], v12 offset:50688\n" \
"v_mfma_f32_16x16x32_bf16 v[132:135], a[12:15], a[84:87], v[132:135]\n" \
"v_mfma_f32_16x16x32_bf16 v[144:147], a[16:19], a[80:83], v[144:147]\n" \
"s_add_u32 m0, s50, 20480\n" \
"v_mfma_f32_16x16x32_bf16 v[144:147], a[20:23], a[84:87], v[144:147]\n" \
"buffer_load_dwordx4 v18, s[28:31], s46 offen lds\n" \
"v_mfma_f32_16x16x32_bf16 v[148:151], a[24:27], a[80:83], v[148:151]\n" \
"v_mfma_f32_16x16x32_bf16 v[148:151], a[28:31], a[84:87], v[148:151]\n" \
"v_mfma_f32_16x16x32_bf16 v[160:163], a[32:35], a[80:83], v[160:163]\n" \
"s_add_u32 m0, s50, 24576\n" \
"v_mfma_f32_16x16x32_bf16 v[160:163], a[36:39], a[84:87], v[160:163]\n" \
"buffer_load_dwordx4 v19, s[28:31], s46 offen lds\n" \
"v_mfma_f32_16x16x32_bf16 v[164:167], a[40:43], a[80:83], v[164:167]\n" \
"v_mfma_f32_16x16x32_bf16 v[164:167], a[44:47], a[84:87], v[164:167]\n" \
"s_waitcnt vmcnt(17) lgkmcnt(0)\n" \
"v_mfma_f32_16x16x32_bf16 v[176:179], a[48:51], a[80:83], v[176:179]\n" \
"s_add_u32 m0, s50, 28672\n" \
"s_barrier\n" \
"v_mfma_f32_16x16x32_bf16 v[176:179], a[52:55], a[84:87], v[176:179]\n" \
"buffer_load_dwordx4 v20, s[28:31], s46 offen lds\n" \
"v_mfma_f32_16x16x32_bf16 v[180:183], a[56:59], a[80:83], v[180:183]\n" \
"v_mfma_f32_16x16x32_bf16 v[180:183], a[60:63], a[84:87], v[180:183]\n" \
"v_mfma_f32_16x16x32_bf16 v[136:139], a[0:3], a[88:91], v[136:139]\n" \
"s_mov_b32 s47, 3670016\n" \
"s_add_u32 m0, s22, 32768\n" \
"v_mfma_f32_16x16x32_bf16 v[136:139], a[4:7], a[92:95], v[136:139]\n" \
"buffer_load_dwordx4 v21, s[32:35], s47 offen offset:0 lds\n" \
"v_mfma_f32_16x16x32_bf16 v[140:143], a[8:11], a[88:91], v[140:143]\n" \
"v_mfma_f32_16x16x32_bf16 v[140:143], a[12:15], a[92:95], v[140:143]\n" \
"v_mfma_f32_16x16x32_bf16 v[152:155], a[16:19], a[88:91], v[152:155]\n" \
"s_add_u32 m0, s22, 34560\n" \
"v_mfma_f32_16x16x32_bf16 v[152:155], a[20:23], a[92:95], v[152:155]\n" \
"buffer_load_dwordx4 v21, s[32:35], s47 offen offset:256 lds\n" \
"v_mfma_f32_16x16x32_bf16 v[156:159], a[24:27], a[88:91], v[156:159]\n" \
"v_mfma_f32_16x16x32_bf16 v[156:159], a[28:31], a[92:95], v[156:159]\n" \
"v_mfma_f32_16x16x32_bf16 v[168:171], a[32:35], a[88:91], v[168:171]\n" \
"s_mov_b32 s47, 3932160\n" \
"s_add_u32 m0, s22, 33792\n" \
"v_mfma_f32_16x16x32_bf16 v[168:171], a[36:39], a[92:95], v[168:171]\n" \
"buffer_load_dwordx4 v21, s[32:35], s47 offen offset:0 lds\n" \
"v_mfma_f32_16x16x32_bf16 v[172:175], a[40:43], a[88:91], v[172:175]\n" \
"v_mfma_f32_16x16x32_bf16 v[172:175], a[44:47], a[92:95], v[172:175]\n" \
"v_mfma_f32_16x16x32_bf16 v[184:187], a[48:51], a[88:91], v[184:187]\n" \
"s_add_u32 m0, s22, 35584\n" \
"v_mfma_f32_16x16x32_bf16 v[184:187], a[52:55], a[92:95], v[184:187]\n" \
"buffer_load_dwordx4 v21, s[32:35], s47 offen offset:256 lds\n" \
"v_mfma_f32_16x16x32_bf16 v[188:191], a[56:59], a[88:91], v[188:191]\n" \
"v_mfma_f32_16x16x32_bf16 v[188:191], a[60:63], a[92:95], v[188:191]\n" \
"v_mfma_f32_16x16x32_bf16 v[192:195], a[0:3], a[96:99], v[192:195]\n" \
"v_mfma_f32_16x16x32_bf16 v[192:195], a[4:7], a[100:103], v[192:195]\n" \
"buffer_load_dwordx4 v[30:33], v23, s[68:71], 0 offen offset:0 nt\n" \
"v_mfma_f32_16x16x32_bf16 v[196:199], a[8:11], a[96:99], v[196:199]\n" \
"s_mov_b64 s[28:29], s[64:65]\n" \
"v_mfma_f32_16x16x32_bf16 v[196:199], a[12:15], a[100:103], v[196:199]\n" \
"v_mfma_f32_16x16x32_bf16 v[208:211], a[16:19], a[96:99], v[208:211]\n" \
"ds_read_b64_tr_b16 a[64:65], v9 offset:0\n" \
"v_mfma_f32_16x16x32_bf16 v[208:211], a[20:23], a[100:103], v[208:211]\n" \
"buffer_load_dwordx4 v[34:37], v24, s[68:71], 0 offen offset:0 nt\n" \
"v_mfma_f32_16x16x32_bf16 v[212:215], a[24:27], a[96:99], v[212:215]\n" \
"s_and_b32 s29, s29, 0xffff\n" \
"ds_read_b64_tr_b16 a[66:67], v10 offset:0\n" \
"v_mfma_f32_16x16x32_bf16 v[212:215], a[28:31], a[100:103], v[212:215]\n" \
"v_mfma_f32_16x16x32_bf16 v[224:227], a[32:35], a[96:99], v[224:227]\n" \
"ds_read_b64_tr_b16 a[68:69], v9 offset:1024\n" \
"v_mfma_f32_16x16x32_bf16 v[224:227], a[36:39], a[100:103], v[224:227]\n" \
"buffer_load_dwordx4 v[38:41], v24, s[68:71], 0 offen offset:1024 nt\n" \
"v_mfma_f32_16x16x32_bf16 v[228:231], a[40:43], a[96:99], v[228:231]\n" \
"s_mov_b32 s30, 4194304\n" \
"ds_read_b64_tr_b16 a[70:71], v10 offset:1024\n" \
"v_mfma_f32_16x16x32_bf16 v[228:231], a[44:47], a[100:103], v[228:231]\n" \
"v_mfma_f32_16x16x32_bf16 v[240:243], a[48:51], a[96:99], v[240:243]\n" \
"ds_read_b64_tr_b16 a[72:73], v11 offset:0\n" \
"v_mfma_f32_16x16x32_bf16 v[240:243], a[52:55], a[100:103], v[240:243]\n" \
"buffer_load_dwordx4 v[42:45], v24, s[68:71], 0 offen offset:2048 nt\n" \
"v_mfma_f32_16x16x32_bf16 v[244:247], a[56:59], a[96:99], v[244:247]\n" \
"s_lshl_b32 s60, s7, 18\n" \
"ds_read_b64_tr_b16 a[74:75], v12 offset:0\n" \
"v_mfma_f32_16x16x32_bf16 v[244:247], a[60:63], a[100:103], v[244:247]\n" \
"v_mfma_f32_16x16x32_bf16 v[200:203], a[0:3], a[104:107], v[200:203]\n" \
"ds_read_b64_tr_b16 a[76:77], v11 offset:1024\n" \
"v_mfma_f32_16x16x32_bf16 v[200:203], a[4:7], a[108:111], v[200:203]\n" \
"buffer_load_dwordx4 v[46:49], v24, s[68:71], 0 offen offset:3072 nt\n" \
"v_mfma_f32_16x16x32_bf16 v[204:207], a[8:11], a[104:107], v[204:207]\n" \
"s_sub_u32 s61, 3932160, s60\n" \
"ds_read_b64_tr_b16 a[78:79], v12 offset:1024\n" \
"v_mfma_f32_16x16x32_bf16 v[204:207], a[12:15], a[108:111], v[204:207]\n" \
"v_mfma_f32_16x16x32_bf16 v[216:219], a[16:19], a[104:107], v[216:219]\n" \
"ds_read_b64_tr_b16 a[80:81], v9 offset:512\n" \
"v_mfma_f32_16x16x32_bf16 v[216:219], a[20:23], a[108:111], v[216:219]\n" \
"buffer_load_dword v50, v25, s[68:71], 0 offen offset:0\n" \
"v_mfma_f32_16x16x32_bf16 v[220:223], a[24:27], a[104:107], v[220:223]\n" \
"ds_read_b64_tr_b16 a[82:83], v10 offset:512\n" \
"v_add_u32 v13, s60, v63\n" \
"v_mfma_f32_16x16x32_bf16 v[220:223], a[28:31], a[108:111], v[220:223]\n" \
"v_mfma_f32_16x16x32_bf16 v[232:235], a[32:35], a[104:107], v[232:235]\n" \
"ds_read_b64_tr_b16 a[84:85], v9 offset:1536\n" \
"v_mfma_f32_16x16x32_bf16 v[232:235], a[36:39], a[108:111], v[232:235]\n" \
"buffer_load_dword v51, v24, s[68:71], 0 offen offset:16\n" \
"v_mfma_f32_16x16x32_bf16 v[236:239], a[40:43], a[104:107], v[236:239]\n" \
"ds_read_b64_tr_b16 a[86:87], v10 offset:1536\n" \
"v_add_u32 v17, s61, v63\n" \
"v_mfma_f32_16x16x32_bf16 v[236:239], a[44:47], a[108:111], v[236:239]\n" \
"v_mfma_f32_16x16x32_bf16 v[248:251], a[48:51], a[104:107], v[248:251]\n" \
"ds_read_b64_tr_b16 a[88:89], v11 offset:512\n" \
"v_mfma_f32_16x16x32_bf16 v[248:251], a[52:55], a[108:111], v[248:251]\n" \
"buffer_load_dword v52, v24, s[68:71], 0 offen offset:1040\n" \
"v_mfma_f32_16x16x32_bf16 v[252:255], a[56:59], a[104:107], v[252:255]\n" \
"ds_read_b64_tr_b16 a[90:91], v12 offset:512\n" \
"v_add_u32 v14, 0x10000, v13\n" \
"v_mfma_f32_16x16x32_bf16 v[252:255], a[60:63], a[108:111], v[252:255]\n" \
"v_mfma_f32_16x16x32_bf16 a[128:131], a[0:3], a[112:115], a[128:131]\n" \
"ds_read_b64_tr_b16 a[92:93], v11 offset:1536\n" \
"v_mfma_f32_16x16x32_bf16 a[128:131], a[4:7], a[116:119], a[128:131]\n" \
"buffer_load_dword v53, v24, s[68:71], 0 offen offset:2064\n" \
"v_mfma_f32_16x16x32_bf16 a[132:135], a[8:11], a[112:115], a[132:135]\n" \
"ds_read_b64_tr_b16 a[94:95], v12 offset:1536\n" \
"v_add_u32 v18, 0x10000, v17\n" \
"v_mfma_f32_16x16x32_bf16 a[132:135], a[12:15], a[116:119], a[132:135]\n" \
"v_mfma_f32_16x16x32_bf16 a[144:147], a[16:19], a[112:115], a[144:147]\n" \
"v_mfma_f32_16x16x32_bf16 a[144:147], a[20:23], a[116:119], a[144:147]\n" \
"buffer_load_dword v54, v24, s[68:71], 0 offen offset:3088\n" \
"v_mfma_f32_16x16x32_bf16 a[148:151], a[24:27], a[112:115], a[148:151]\n" \
"v_add_u32 v15, 0x10000, v14\n" \
"v_mfma_f32_16x16x32_bf16 a[148:151], a[28:31], a[116:119], a[148:151]\n" \
"v_mfma_f32_16x16x32_bf16 a[160:163], a[32:35], a[112:115], a[160:163]\n" \
"v_mfma_f32_16x16x32_bf16 a[160:163], a[36:39], a[116:119], a[160:163]\n" \
"global_load_short_d16_hi v28, v29, s[12:13]\n" \
"v_mfma_f32_16x16x32_bf16 a[164:167], a[40:43], a[112:115], a[164:167]\n" \
"v_add_u32 v19, 0x10000, v18\n" \
"v_mfma_f32_16x16x32_bf16 a[164:167], a[44:47], a[116:119], a[164:167]\n" \
"s_waitcnt vmcnt(23) lgkmcnt(0)\n" \
"v_mfma_f32_16x16x32_bf16 a[176:179], a[48:51], a[112:115], a[176:179]\n" \
"s_barrier\n" \
"v_mfma_f32_16x16x32_bf16 a[176:179], a[52:55], a[116:119], a[176:179]\n" \
"global_load_dwordx4 a[196:199], v4, s[86:87] offset:1024\n" \
"v_mfma_f32_16x16x32_bf16 a[180:183], a[56:59], a[112:115], a[180:183]\n" \
"v_add_u32 v16, 0x10000, v15\n" \
"v_mfma_f32_16x16x32_bf16 a[180:183], a[60:63], a[116:119], a[180:183]\n" \
"v_mfma_f32_16x16x32_bf16 a[136:139], a[0:3], a[120:123], a[136:139]\n" \
"v_mfma_f32_16x16x32_bf16 a[136:139], a[4:7], a[124:127], a[136:139]\n" \
"v_mfma_f32_16x16x32_bf16 a[140:143], a[8:11], a[120:123], a[140:143]\n" \
"ds_read_b128 a[0:3], v7 offset:0\n" \
"v_add_u32 v20, 0x10000, v19\n" \
"v_mfma_f32_16x16x32_bf16 a[140:143], a[12:15], a[124:127], a[140:143]\n" \
"ds_read_b128 a[4:7], v8 offset:0\n" \
"v_mfma_f32_16x16x32_bf16 a[152:155], a[16:19], a[120:123], a[152:155]\n" \
"ds_read_b128 a[8:11], v7 offset:2048\n" \
"v_mfma_f32_16x16x32_bf16 a[152:155], a[20:23], a[124:127], a[152:155]\n" \
"ds_read_b128 a[12:15], v8 offset:2048\n" \
"v_mfma_f32_16x16x32_bf16 a[156:159], a[24:27], a[120:123], a[156:159]\n" \
"ds_read_b128 a[16:19], v7 offset:4096\n" \
"v_mfma_f32_16x16x32_bf16 a[156:159], a[28:31], a[124:127], a[156:159]\n" \
"ds_read_b128 a[20:23], v8 offset:4096\n" \
"v_mfma_f32_16x16x32_bf16 a[168:171], a[32:35], a[120:123], a[168:171]\n" \
"ds_read_b128 a[24:27], v7 offset:6144\n" \
"v_mfma_f32_16x16x32_bf16 a[168:171], a[36:39], a[124:127], a[168:171]\n" \
"ds_read_b128 a[28:31], v8 offset:6144\n" \
"v_mfma_f32_16x16x32_bf16 a[172:175], a[40:43], a[120:123], a[172:175]\n" \
"ds_read_b128 a[32:35], v7 offset:8192\n" \
"v_mfma_f32_16x16x32_bf16 a[172:175], a[44:47], a[124:127], a[172:175]\n" \
"ds_read_b128 a[36:39], v8 offset:8192\n" \
"v_mfma_f32_16x16x32_bf16 a[184:187], a[48:51], a[120:123], a[184:187]\n" \
"ds_read_b128 a[40:43], v7 offset:10240\n" \
"v_mfma_f32_16x16x32_bf16 a[184:187], a[52:55], a[124:127], a[184:187]\n" \
"ds_read_b128 a[44:47], v8 offset:10240\n" \
"v_mfma_f32_16x16x32_bf16 a[188:191], a[56:59], a[120:123], a[188:191]\n" \
"ds_read_b128 a[48:51], v7 offset:12288\n" \
"v_mfma_f32_16x16x32_bf16 a[188:191], a[60:63], a[124:127], a[188:191]\n" \
"s_waitcnt lgkmcnt(9)\n" \
"ds_read_b128 a[52:55], v8 offset:12288\n" \
"v_mfma_f32_16x16x32_bf16 v[64:67], a[0:3], a[64:67], v[64:67]\n" \
"ds_read_b128 a[56:59], v7 offset:14336\n" \
"v_mfma_f32_16x16x32_bf16 v[64:67], a[4:7], a[68:71], v[64:67]\n" \
"buffer_load_dwordx2 v[58:59], v57, s[36:39], 0 offen sc1\n" \
"v_mfma_f32_16x16x32_bf16 v[68:71], a[8:11], a[64:67], v[68:71]\n" \
"s_waitcnt lgkmcnt(7)\n" \
"ds_read_b128 a[60:63], v8 offset:14336\n" \
"v_mfma_f32_16x16x32_bf16 v[68:71], a[12:15], a[68:71], v[68:71]\n" \
"v_mfma_f32_16x16x32_bf16 v[80:83], a[16:19], a[64:67], v[80:83]\n" \
"s_mov_b32 s47, 3670016\n" \
"s_add_u32 m0, s22, 49024\n" \
"ds_read_b64_tr_b16 a[96:97], v9 offset:16384\n" \
"v_mfma_f32_16x16x32_bf16 v[80:83], a[20:23], a[68:71], v[80:83]\n" \
"buffer_load_dwordx4 v21, s[32:35], s47 offen offset:128 lds\n" \
"v_mfma_f32_16x16x32_bf16 v[84:87], a[24:27], a[64:67], v[84:87]\n" \
"s_waitcnt lgkmcnt(5)\n" \
"ds_read_b64_tr_b16 a[98:99], v10 offset:16384\n" \
"v_mfma_f32_16x16x32_bf16 v[84:87], a[28:31], a[68:71], v[84:87]\n" \
"v_mfma_f32_16x16x32_bf16 v[96:99], a[32:35], a[64:67], v[96:99]\n" \
"s_add_u32 m0, s22, 50816\n" \
"ds_read_b64_tr_b16 a[100:101], v9 offset:17408\n" \
"v_mfma_f32_16x16x32_bf16 v[96:99], a[36:39], a[68:71], v[96:99]\n" \
"buffer_load_dwordx4 v21, s[32:35], s47 offen offset:384 lds\n" \
"v_mfma_f32_16x16x32_bf16 v[100:103], a[40:43], a[64:67], v[100:103]\n" \
"s_waitcnt lgkmcnt(3)\n" \
"ds_read_b64_tr_b16 a[102:103], v10 offset:17408\n" \
"v_mfma_f32_16x16x32_bf16 v[100:103], a[44:47], a[68:71], v[100:103]\n" \
"v_mfma_f32_16x16x32_bf16 v[112:115], a[48:51], a[64:67], v[112:115]\n" \
"s_mov_b32 s47, 3932160\n" \
"s_add_u32 m0, s22, 50048\n" \
"ds_read_b64_tr_b16 a[104:105], v11 offset:16384\n" \
"v_mfma_f32_16x16x32_bf16 v[112:115], a[52:55], a[68:71], v[112:115]\n" \
"buffer_load_dwordx4 v21, s[32:35], s47 offen offset:128 lds\n" \
"v_mfma_f32_16x16x32_bf16 v[116:119], a[56:59], a[64:67], v[116:119]\n" \
"ds_read_b64_tr_b16 a[106:107], v12 offset:16384\n" \
"v_mfma_f32_16x16x32_bf16 v[116:119], a[60:63], a[68:71], v[116:119]\n" \
"s_add_u32 s76, s76, 2097152\ns_addc_u32 s77, s77, 0\n" \
"v_mfma_f32_16x16x32_bf16 v[72:75], a[0:3], a[72:75], v[72:75]\n" \
"s_add_u32 m0, s22, 51840\n" \
"ds_read_b64_tr_b16 a[108:109], v11 offset:17408\n" \
"v_mfma_f32_16x16x32_bf16 v[72:75], a[4:7], a[76:79], v[72:75]\n" \
"buffer_load_dwordx4 v21, s[32:35], s47 offen offset:384 lds\n" \
"v_mfma_f32_16x16x32_bf16 v[76:79], a[8:11], a[72:75], v[76:79]\n" \
"ds_read_b64_tr_b16 a[110:111], v12 offset:17408\n" \
"v_mfma_f32_16x16x32_bf16 v[76:79], a[12:15], a[76:79], v[76:79]\n" \
"v_mfma_f32_16x16x32_bf16 v[88:91], a[16:19], a[72:75], v[88:91]\n" \
"ds_read_b64_tr_b16 a[112:113], v9 offset:16896\n" \
"v_mfma_f32_16x16x32_bf16 v[88:91], a[20:23], a[76:79], v[88:91]\n" \
"v_mfma_f32_16x16x32_bf16 v[92:95], a[24:27], a[72:75], v[92:95]\n" \
"ds_read_b64_tr_b16 a[114:115], v10 offset:16896\n" \
"v_mfma_f32_16x16x32_bf16 v[92:95], a[28:31], a[76:79], v[92:95]\n" \
"s_mov_b64 exec, 0xffff\nglobal_load_dword v22, v1, s[76:77]\ns_mov_b64 exec, -1\n" \
"v_mfma_f32_16x16x32_bf16 v[104:107], a[32:35], a[72:75], v[104:107]\n" \
"s_add_u32 s76, s76, 2097152\ns_addc_u32 s77, s77, 0\n" \
"ds_read_b64_tr_b16 a[116:117], v9 offset:17920\n" \
"v_mfma_f32_16x16x32_bf16 v[104:107], a[36:39], a[76:79], v[104:107]\n" \
"v_mfma_f32_16x16x32_bf16 v[108:111], a[40:43], a[72:75], v[108:111]\n" \
"ds_read_b64_tr_b16 a[118:119], v10 offset:17920\n" \
"v_mfma_f32_16x16x32_bf16 v[108:111], a[44:47], a[76:79], v[108:111]\n" \
"s_mov_b64 exec, 0xffff\nglobal_load_dword v22, v1, s[76:77]\ns_mov_b64 exec, -1\n" \
"v_mfma_f32_16x16x32_bf16 v[120:123], a[48:51], a[72:75], v[120:123]\n" \
"ds_read_b64_tr_b16 a[120:121], v11 offset:16896\n" \
"v_mfma_f32_16x16x32_bf16 v[120:123], a[52:55], a[76:79], v[120:123]\n" \
"v_mfma_f32_16x16x32_bf16 v[124:127], a[56:59], a[72:75], v[124:127]\n" \
"ds_read_b64_tr_b16 a[122:123], v12 offset:16896\n" \
"v_mfma_f32_16x16x32_bf16 v[124:127], a[60:63], a[76:79], v[124:127]\n" \
"v_mfma_f32_16x16x32_bf16 v[128:131], a[0:3], a[80:83], v[128:131]\n" \
"ds_read_b64_tr_b16 a[124:125], v11 offset:17920\n" \
"v_mfma_f32_16x16x32_bf16 v[128:131], a[4:7], a[84:87], v[128:131]\n" \
"v_mfma_f32_16x16x32_bf16 v[132:135], a[8:11], a[80:83], v[132:135]\n" \
"ds_read_b64_tr_b16 a[126:127], v12 offset:17920\n" \
"v_mfma_f32_16x16x32_bf16 v[132:135], a[12:15], a[84:87], v[132:135]\n" \
"v_mfma_f32_16x16x32_bf16 v[144:147], a[16:19], a[80:83], v[144:147]\n" \
"v_mfma_f32_16x16x32_bf16 v[144:147], a[20:23], a[84:87], v[144:147]\n" \
"v_mfma_f32_16x16x32_bf16 v[148:151], a[24:27], a[80:83], v[148:151]\n" \
"v_mfma_f32_16x16x32_bf16 v[148:151], a[28:31], a[84:87], v[148:151]\n" \
"v_mfma_f32_16x16x32_bf16 v[160:163], a[32:35], a[80:83], v[160:163]\n" \
"v_mfma_f32_16x16x32_bf16 v[160:163], a[36:39], a[84:87], v[160:163]\n" \
"v_mfma_f32_16x16x32_bf16 v[164:167], a[40:43], a[80:83], v[164:167]\n" \
"v_mfma_f32_16x16x32_bf16 v[164:167], a[44:47], a[84:87], v[164:167]\n" \
"s_waitcnt vmcnt(19) lgkmcnt(0)\n" \
"v_mfma_f32_16x16x32_bf16 v[176:179], a[48:51], a[80:83], v[176:179]\n" \
"s_barrier\n" \
"v_mfma_f32_16x16x32_bf16 v[176:179], a[52:55], a[84:87], v[176:179]\n" \
"v_mfma_f32_16x16x32_bf16 v[180:183], a[56:59], a[80:83], v[180:183]\n" \
"v_mfma_f32_16x16x32_bf16 v[180:183], a[60:63], a[84:87], v[180:183]\n" \
"v_mfma_f32_16x16x32_bf16 v[136:139], a[0:3], a[88:91], v[136:139]\n" \
"v_mfma_f32_16x16x32_bf16 v[136:139], a[4:7], a[92:95], v[136:139]\n" \
"v_mfma_f32_16x16x32_bf16 v[140:143], a[8:11], a[88:91], v[140:143]\n" \
"v_mfma_f32_16x16x32_bf16 v[140:143], a[12:15], a[92:95], v[140:143]\n" \
"v_mfma_f32_16x16x32_bf16 v[152:155], a[16:19], a[88:91], v[152:155]\n" \
"v_mfma_f32_16x16x32_bf16 v[152:155], a[20:23], a[92:95], v[152:155]\n" \
"v_mfma_f32_16x16x32_bf16 v[156:159], a[24:27], a[88:91], v[156:159]\n" \
"v_mfma_f32_16x16x32_bf16 v[156:159], a[28:31], a[92:95], v[156:159]\n" \
"v_mfma_f32_16x16x32_bf16 v[168:171], a[32:35], a[88:91], v[168:171]\n" \
"v_mfma_f32_16x16x32_bf16 v[168:171], a[36:39], a[92:95], v[168:171]\n" \
"v_mfma_f32_16x16x32_bf16 v[172:175], a[40:43], a[88:91], v[172:175]\n" \
"v_mfma_f32_16x16x32_bf16 v[172:175], a[44:47], a[92:95], v[172:175]\n" \
"s_waitcnt vmcnt(6)\n" \
"v_mfma_f32_16x16x32_bf16 v[184:187], a[48:51], a[88:91], v[184:187]\n" \
"v_cmp_ne_u64 vcc, s[14:15], v[58:59]\n" \
"v_mfma_f32_16x16x32_bf16 v[184:187], a[52:55], a[92:95], v[184:187]\n" \
"v_mfma_f32_16x16x32_bf16 v[188:191], a[56:59], a[88:91], v[188:191]\n" \
"s_cbranch_vccnz LPUV_%=\nLPUVR_%=:\n" \
"v_mfma_f32_16x16x32_bf16 v[188:191], a[60:63], a[92:95], v[188:191]\n" \
"v_mfma_f32_16x16x32_bf16 v[192:195], a[0:3], a[96:99], v[192:195]\n" \
"s_add_u32 m0, s56, 10304\n" \
"v_mfma_f32_16x16x32_bf16 v[192:195], a[4:7], a[100:103], v[192:195]\n" \
"buffer_load_dwordx4 v26, s[72:75], 0 offen offset:0 sc1 lds\n" \
"v_mfma_f32_16x16x32_bf16 v[196:199], a[8:11], a[96:99], v[196:199]\n" \
"s_mov_b64 s[32:33], s[44:45]\n" \
"v_mfma_f32_16x16x32_bf16 v[196:199], a[12:15], a[100:103], v[196:199]\n" \
"v_mfma_f32_16x16x32_bf16 v[208:211], a[16:19], a[96:99], v[208:211]\n" \
"s_add_u32 m0, s56, 11328\n" \
"ds_read_b64_tr_b16 a[64:65], v9 offset:32768\n" \
"s_waitcnt vmcnt(9)\n" \
"v_mfma_f32_16x16x32_bf16 v[208:211], a[20:23], a[100:103], v[208:211]\n" \
"buffer_load_dwordx4 v27, s[72:75], 0 offen offset:0 sc1 lds\n" \
"v_mfma_f32_16x16x32_bf16 v[212:215], a[24:27], a[96:99], v[212:215]\n" \
"s_mov_b32 s34, s49\n" \
"ds_read_b64_tr_b16 a[66:67], v10 offset:32768\n" \
"v_alignbyte_b32 v30, v31, v30, 2\n" \
"v_mfma_f32_16x16x32_bf16 v[212:215], a[28:31], a[100:103], v[212:215]\n" \
"v_alignbyte_b32 v31, v32, v31, 2\n" \
"v_alignbyte_b32 v32, v33, v32, 2\n" \
"v_mfma_f32_16x16x32_bf16 v[224:227], a[32:35], a[96:99], v[224:227]\n" \
"s_add_u32 m0, s56, 11328\n" \
"ds_read_b64_tr_b16 a[68:69], v9 offset:33792\n" \
"v_alignbyte_b32 v33, v50, v33, 2\n" \
"v_mfma_f32_16x16x32_bf16 v[224:227], a[36:39], a[100:103], v[224:227]\n" \
"buffer_load_dwordx4 v27, s[72:75], 0 offen offset:1024 sc1 lds\n" \
"v_mfma_f32_16x16x32_bf16 v[228:231], a[40:43], a[96:99], v[228:231]\n" \
"ds_read_b64_tr_b16 a[70:71], v10 offset:33792\n" \
"v_mfma_f32_16x16x32_bf16 v[228:231], a[44:47], a[100:103], v[228:231]\n" \
"ds_write_b128 v55, v[30:33] offset:5184\n" \
"v_alignbyte_b32 v34, v35, v34, 2\n" \
"v_mfma_f32_16x16x32_bf16 v[240:243], a[48:51], a[96:99], v[240:243]\n" \
"s_add_u32 m0, s56, 11328\n" \
"ds_read_b64_tr_b16 a[72:73], v11 offset:32768\n" \
"v_alignbyte_b32 v35, v36, v35, 2\n" \
"v_mfma_f32_16x16x32_bf16 v[240:243], a[52:55], a[100:103], v[240:243]\n" \
"buffer_load_dwordx4 v27, s[72:75], 0 offen offset:2048 sc1 lds\n" \
"v_mfma_f32_16x16x32_bf16 v[244:247], a[56:59], a[96:99], v[244:247]\n" \
"ds_read_b64_tr_b16 a[74:75], v12 offset:32768\n" \
"v_alignbyte_b32 v36, v37, v36, 2\n" \
"v_mfma_f32_16x16x32_bf16 v[244:247], a[60:63], a[100:103], v[244:247]\n" \
"v_alignbyte_b32 v37, v51, v37, 2\n" \
"ds_write_b128 v55, v[34:37] offset:6208\n" \
"v_mfma_f32_16x16x32_bf16 v[200:203], a[0:3], a[104:107], v[200:203]\n" \
"s_add_u32 m0, s56, 11328\n" \
"ds_read_b64_tr_b16 a[76:77], v11 offset:33792\n" \
"v_alignbyte_b32 v38, v39, v38, 2\n" \
"v_mfma_f32_16x16x32_bf16 v[200:203], a[4:7], a[108:111], v[200:203]\n" \
"buffer_load_dwordx4 v27, s[72:75], 0 offen offset:3072 sc1 lds\n" \
"v_mfma_f32_16x16x32_bf16 v[204:207], a[8:11], a[104:107], v[204:207]\n" \
"ds_read_b64_tr_b16 a[78:79], v12 offset:33792\n" \
"v_alignbyte_b32 v39, v40, v39, 2\n" \
"v_mfma_f32_16x16x32_bf16 v[204:207], a[12:15], a[108:111], v[204:207]\n" \
"v_alignbyte_b32 v40, v41, v40, 2\n" \
"v_alignbyte_b32 v41, v52, v41, 2\n" \
"v_mfma_f32_16x16x32_bf16 v[216:219], a[16:19], a[104:107], v[216:219]\n" \
"s_add_u32 m0, s56, 0\n" \
"ds_read_b64_tr_b16 a[80:81], v9 offset:33280\n" \
"v_mfma_f32_16x16x32_bf16 v[216:219], a[20:23], a[108:111], v[216:219]\n" \
"buffer_load_dwordx4 v23, s[68:71], 0 offen offset:0 lds\n" \
"v_mfma_f32_16x16x32_bf16 v[220:223], a[24:27], a[104:107], v[220:223]\n" \
"ds_read_b64_tr_b16 a[82:83], v10 offset:33280\n" \
"v_mfma_f32_16x16x32_bf16 v[220:223], a[28:31], a[108:111], v[220:223]\n" \
"ds_write_b128 v55, v[38:41] offset:7232\n" \
"v_alignbyte_b32 v42, v43, v42, 2\n" \
"v_mfma_f32_16x16x32_bf16 v[232:235], a[32:35], a[104:107], v[232:235]\n" \
"s_add_u32 m0, s56, 1024\n" \
"ds_read_b64_tr_b16 a[84:85], v9 offset:34304\n" \
"v_alignbyte_b32 v43, v44, v43, 2\n" \
"v_mfma_f32_16x16x32_bf16 v[232:235], a[36:39], a[108:111], v[232:235]\n" \
"buffer_load_dwordx4 v24, s[68:71], 0 offen offset:0 lds\n" \
"v_mfma_f32_16x16x32_bf16 v[236:239], a[40:43], a[104:107], v[236:239]\n" \
"ds_read_b64_tr_b16 a[86:87], v10 offset:34304\n" \
"v_alignbyte_b32 v44, v45, v44, 2\n" \
"v_mfma_f32_16x16x32_bf16 v[236:239], a[44:47], a[108:111], v[236:239]\n" \
"v_alignbyte_b32 v45, v53, v45, 2\n" \
"ds_write_b128 v55, v[42:45] offset:8256\n" \
"v_mfma_f32_16x16x32_bf16 v[248:251], a[48:51], a[104:107], v[248:251]\n" \
"s_add_u32 m0, s56, 1024\n" \
"ds_read_b64_tr_b16 a[88:89], v11 offset:33280\n" \
"v_alignbyte_b32 v46, v47, v46, 2\n" \
"v_mfma_f32_16x16x32_bf16 v[248:251], a[52:55], a[108:111], v[248:251]\n" \
"buffer_load_dwordx4 v24, s[68:71], 0 offen offset:1024 lds\n" \
"v_mfma_f32_16x16x32_bf16 v[252:255], a[56:59], a[104:107], v[252:255]\n" \
"ds_read_b64_tr_b16 a[90:91], v12 offset:33280\n" \
"v_alignbyte_b32 v47, v48, v47, 2\n" \
"v_mfma_f32_16x16x32_bf16 v[252:255], a[60:63], a[108:111], v[252:255]\n" \
"v_alignbyte_b32 v48, v49, v48, 2\n" \
"v_alignbyte_b32 v49, v54, v49, 2\n" \
"v_mfma_f32_16x16x32_bf16 a[128:131], a[0:3], a[112:115], a[128:131]\n" \
"s_add_u32 m0, s56, 1024\n" \
"ds_read_b64_tr_b16 a[92:93], v11 offset:34304\n" \
"v_mfma_f32_16x16x32_bf16 a[128:131], a[4:7], a[116:119], a[128:131]\n" \
"buffer_load_dwordx4 v24, s[68:71], 0 offen offset:2048 lds\n" \
"v_mfma_f32_16x16x32_bf16 a[132:135], a[8:11], a[112:115], a[132:135]\n" \
"ds_read_b64_tr_b16 a[94:95], v12 offset:34304\n" \
"v_mfma_f32_16x16x32_bf16 a[132:135], a[12:15], a[116:119], a[132:135]\n" \
"ds_write_b128 v55, v[46:49] offset:9280\n" \
"v_lshrrev_b32 v30, 4, v4\n" \
"v_mfma_f32_16x16x32_bf16 a[144:147], a[16:19], a[112:115], a[144:147]\n" \
"s_add_u32 m0, s56, 1024\n" \
"v_and_b32 v31, 15, v30\n" \
"v_lshrrev_b32 v32, 4, v30\n" \
"v_mfma_f32_16x16x32_bf16 a[144:147], a[20:23], a[116:119], a[144:147]\n" \
"buffer_load_dwordx4 v24, s[68:71], 0 offen offset:3072 lds\n" \
"v_mfma_f32_16x16x32_bf16 a[148:151], a[24:27], a[112:115], a[148:151]\n" \
"v_add_u32 v33, 1, v31\n" \
"v_and_b32 v34, 1, v33\n" \
"v_mfma_f32_16x16x32_bf16 a[148:151], a[28:31], a[116:119], a[148:151]\n" \
"v_and_b32 v33, 30, v33\n" \
"v_mul_u32_u24 v34, 5184, v34\n" \
"v_mfma_f32_16x16x32_bf16 a[160:163], a[32:35], a[112:115], a[160:163]\n" \
"s_mov_b32 s46, 0\n" \
"s_add_u32 m0, s50, 65536\n" \
"v_lshl_add_u32 v33, v33, 1, v34\n" \
"v_mfma_f32_16x16x32_bf16 a[160:163], a[36:39], a[116:119], a[160:163]\n" \
"buffer_load_dwordx4 v13, s[28:31], s46 offen lds\n" \
"v_mfma_f32_16x16x32_bf16 a[164:167], a[40:43], a[112:115], a[164:167]\n" \
"v_lshl_add_u32 v33, v32, 4, v33\n" \
"v_add_u32 v46, s56, v33\n" \
"v_mfma_f32_16x16x32_bf16 a[164:167], a[44:47], a[116:119], a[164:167]\n" \
"s_waitcnt vmcnt(13) lgkmcnt(0)\n" \
"v_add_u32 v47, 1024, v46\n" \
"v_add_u32 v48, 2048, v46\n" \
"v_mfma_f32_16x16x32_bf16 a[176:179], a[48:51], a[112:115], a[176:179]\n" \
"s_add_u32 m0, s50, 69632\n" \
"s_barrier\n" \
"v_add_u32 v49, 3072, v46\n" \
"v_mfma_f32_16x16x32_bf16 a[176:179], a[52:55], a[116:119], a[176:179]\n" \
"buffer_load_dwordx4 v14, s[28:31], s46 offen lds\n" \
"v_mfma_f32_16x16x32_bf16 a[180:183], a[56:59], a[112:115], a[180:183]\n" \
"v_lshlrev_b32 v33, 1, v31\n" \
"v_sub_u32 v33, v33, v32\n" \
"v_mfma_f32_16x16x32_bf16 a[180:183], a[60:63], a[116:119], a[180:183]\n" \
"v_lshlrev_b32 v33, 4, v33\n" \
"v_add_u32 v33, 10320, v33\n" \
"v_mfma_f32_16x16x32_bf16 a[136:139], a[0:3], a[120:123], a[136:139]\n" \
"s_add_u32 m0, s50, 73728\n" \
"v_add_u32 v50, s56, v33\n" \
"v_mfma_f32_16x16x32_bf16 a[136:139], a[4:7], a[124:127], a[136:139]\n" \
"buffer_load_dwordx4 v15, s[28:31], s46 offen lds\n" \
"v_mfma_f32_16x16x32_bf16 a[140:143], a[8:11], a[120:123], a[140:143]\n" \
"ds_read_b128 a[0:3], v5 offset:0\n" \
"v_lshlrev_b32 v33, 5, v31\n" \
"v_mfma_f32_16x16x32_bf16 a[140:143], a[12:15], a[124:127], a[140:143]\n" \
"ds_read_b128 a[4:7], v6 offset:0\n" \
"v_lshrrev_b32 v55, 2, v30\n" \
"v_mfma_f32_16x16x32_bf16 a[152:155], a[16:19], a[120:123], a[152:155]\n" \
"ds_read_b128 a[8:11], v5 offset:2048\n" \
"v_and_b32 v55, 8, v55\n" \
"v_mfma_f32_16x16x32_bf16 a[152:155], a[20:23], a[124:127], a[152:155]\n" \
"ds_read_b128 a[12:15], v6 offset:2048\n" \
"v_xor_b32 v55, v30, v55\n" \
"v_mfma_f32_16x16x32_bf16 a[156:159], a[24:27], a[120:123], a[156:159]\n" \
"ds_read_b128 a[16:19], v5 offset:4096\n" \
"v_lshl_add_u32 v55, v55, 3, s22\n" \
"v_mfma_f32_16x16x32_bf16 a[156:159], a[28:31], a[124:127], a[156:159]\n" \
"ds_read_b128 a[20:23], v6 offset:4096\n" \
"v_add_u32 v33, s56, v33\n" \
"v_mfma_f32_16x16x32_bf16 a[168:171], a[32:35], a[120:123], a[168:171]\n" \
"ds_read_b128 a[24:27], v5 offset:6144\n" \
"v_xor_b32 v32, 1, v32\n" \
"v_mfma_f32_16x16x32_bf16 a[168:171], a[36:39], a[124:127], a[168:171]\n" \
"ds_read_b128 a[28:31], v6 offset:6144\n" \
"v_lshl_add_u32 v51, v32, 3, v33\n" \
"v_mfma_f32_16x16x32_bf16 a[172:175], a[40:43], a[120:123], a[172:175]\n" \
"ds_read_b128 a[32:35], v5 offset:8192\n" \
"v_mfma_f32_16x16x32_bf16 a[172:175], a[44:47], a[124:127], a[172:175]\n" \
"ds_read_b128 a[36:39], v6 offset:8192\n" \
"v_mfma_f32_16x16x32_bf16 a[184:187], a[48:51], a[120:123], a[184:187]\n" \
"ds_read_b128 a[40:43], v5 offset:10240\n" \
"v_mfma_f32_16x16x32_bf16 a[184:187], a[52:55], a[124:127], a[184:187]\n" \
"ds_read_b128 a[44:47], v6 offset:10240\n" \
"v_mfma_f32_16x16x32_bf16 a[188:191], a[56:59], a[120:123], a[188:191]\n" \
"ds_read_b128 a[48:51], v5 offset:12288\n" \
"v_mfma_f32_16x16x32_bf16 a[188:191], a[60:63], a[124:127], a[188:191]\n" \
"s_waitcnt lgkmcnt(9)\n" \
"ds_read_b128 a[52:55], v6 offset:12288\n" \
"v_mfma_f32_16x16x32_bf16 v[64:67], a[0:3], a[64:67], v[64:67]\n" \
"s_add_u32 m0, s50, 77824\n" \
"ds_read_b128 a[56:59], v5 offset:14336\n" \
"v_mfma_f32_16x16x32_bf16 v[64:67], a[4:7], a[68:71], v[64:67]\n" \
"buffer_load_dwordx4 v16, s[28:31], s46 offen lds\n" \
"v_mfma_f32_16x16x32_bf16 v[68:71], a[8:11], a[64:67], v[68:71]\n" \
"s_waitcnt lgkmcnt(7)\n" \
"ds_read_b128 a[60:63], v6 offset:14336\n" \
"v_mfma_f32_16x16x32_bf16 v[68:71], a[12:15], a[68:71], v[68:71]\n" \
"v_mfma_f32_16x16x32_bf16 v[80:83], a[16:19], a[64:67], v[80:83]\n" \
"s_mov_b32 s46, 0\n" \
"s_add_u32 m0, s50, 81920\n" \
"ds_read_b64_tr_b16 a[96:97], v9 offset:49152\n" \
"v_mfma_f32_16x16x32_bf16 v[80:83], a[20:23], a[68:71], v[80:83]\n" \
"LH7_%=:\ns_cbranch_execz LH11_%=\n" \
"buffer_load_dwordx4 v17, s[28:31], s46 offen lds\n" \
"v_mfma_f32_16x16x32_bf16 v[84:87], a[24:27], a[64:67], v[84:87]\n" \
"s_waitcnt lgkmcnt(5)\n" \
"ds_read_b64_tr_b16 a[98:99], v10 offset:49152\n" \
"v_mfma_f32_16x16x32_bf16 v[84:87], a[28:31], a[68:71], v[84:87]\n" \
"v_mfma_f32_16x16x32_bf16 v[96:99], a[32:35], a[64:67], v[96:99]\n" \
"s_add_u32 m0, s50, 86016\n" \
"ds_read_b64_tr_b16 a[100:101], v9 offset:50176\n" \
"v_mfma_f32_16x16x32_bf16 v[96:99], a[36:39], a[68:71], v[96:99]\n" \
"buffer_load_dwordx4 v18, s[28:31], s46 offen lds\n" \
"v_mfma_f32_16x16x32_bf16 v[100:103], a[40:43], a[64:67], v[100:103]\n" \
"s_waitcnt lgkmcnt(3)\n" \
"ds_read_b64_tr_b16 a[102:103], v10 offset:50176\n" \
"v_mfma_f32_16x16x32_bf16 v[100:103], a[44:47], a[68:71], v[100:103]\n" \
"v_mfma_f32_16x16x32_bf16 v[112:115], a[48:51], a[64:67], v[112:115]\n" \
"s_add_u32 m0, s50, 90112\n" \
"ds_read_b64_tr_b16 a[104:105], v11 offset:49152\n" \
"v_mfma_f32_16x16x32_bf16 v[112:115], a[52:55], a[68:71], v[112:115]\n" \
"buffer_load_dwordx4 v19, s[28:31], s46 offen lds\n" \
"v_mfma_f32_16x16x32_bf16 v[116:119], a[56:59], a[64:67], v[116:119]\n" \
"ds_read_b64_tr_b16 a[106:107], v12 offset:49152\n" \
"v_mfma_f32_16x16x32_bf16 v[116:119], a[60:63], a[68:71], v[116:119]\n" \
"v_mfma_f32_16x16x32_bf16 v[72:75], a[0:3], a[72:75], v[72:75]\n" \
"s_add_u32 m0, s50, 94208\n" \
"ds_read_b64_tr_b16 a[108:109], v11 offset:50176\n" \
"v_mfma_f32_16x16x32_bf16 v[72:75], a[4:7], a[76:79], v[72:75]\n" \
"buffer_load_dwordx4 v20, s[28:31], s46 offen lds\n" \
"v_mfma_f32_16x16x32_bf16 v[76:79], a[8:11], a[72:75], v[76:79]\n" \
"ds_read_b64_tr_b16 a[110:111], v12 offset:50176\n" \
"v_mfma_f32_16x16x32_bf16 v[76:79], a[12:15], a[76:79], v[76:79]\n" \
"v_mfma_f32_16x16x32_bf16 v[88:91], a[16:19], a[72:75], v[88:91]\n" \
"s_mov_b32 s47, 0\n" \
"s_add_u32 m0, s51, 32768\n" \
"ds_read_b64_tr_b16 a[112:113], v9 offset:49664\n" \
"v_mfma_f32_16x16x32_bf16 v[88:91], a[20:23], a[76:79], v[88:91]\n" \
"buffer_load_dwordx4 v21, s[32:35], s47 offen offset:0 lds\n" \
"v_mfma_f32_16x16x32_bf16 v[92:95], a[24:27], a[72:75], v[92:95]\n" \
"ds_read_b64_tr_b16 a[114:115], v10 offset:49664\n" \
"v_mfma_f32_16x16x32_bf16 v[92:95], a[28:31], a[76:79], v[92:95]\n" \
"v_mfma_f32_16x16x32_bf16 v[104:107], a[32:35], a[72:75], v[104:107]\n" \
"s_add_u32 m0, s51, 33664\n" \
"ds_read_b64_tr_b16 a[116:117], v9 offset:50688\n" \
"v_mfma_f32_16x16x32_bf16 v[104:107], a[36:39], a[76:79], v[104:107]\n" \
"buffer_load_dwordx4 v21, s[32:35], s47 offen offset:128 lds\n" \
"v_mfma_f32_16x16x32_bf16 v[108:111], a[40:43], a[72:75], v[108:111]\n" \
"ds_read_b64_tr_b16 a[118:119], v10 offset:50688\n" \
"v_mfma_f32_16x16x32_bf16 v[108:111], a[44:47], a[76:79], v[108:111]\n" \
"v_mfma_f32_16x16x32_bf16 v[120:123], a[48:51], a[72:75], v[120:123]\n" \
"s_mov_b32 s47, 262144\n" \
"s_add_u32 m0, s51, 40960\n" \
"ds_read_b64_tr_b16 a[120:121], v11 offset:49664\n" \
"v_mfma_f32_16x16x32_bf16 v[120:123], a[52:55], a[76:79], v[120:123]\n" \
"buffer_load_dwordx4 v21, s[32:35], s47 offen offset:0 lds\n" \
"v_mfma_f32_16x16x32_bf16 v[124:127], a[56:59], a[72:75], v[124:127]\n" \
"ds_read_b64_tr_b16 a[122:123], v12 offset:49664\n" \
"v_mfma_f32_16x16x32_bf16 v[124:127], a[60:63], a[76:79], v[124:127]\n" \
"v_mfma_f32_16x16x32_bf16 v[128:131], a[0:3], a[80:83], v[128:131]\n" \
"s_add_u32 m0, s51, 41856\n" \
"ds_read_b64_tr_b16 a[124:125], v11 offset:50688\n" \
"v_mfma_f32_16x16x32_bf16 v[128:131], a[4:7], a[84:87], v[128:131]\n" \
"buffer_load_dwordx4 v21, s[32:35], s47 offen offset:128 lds\n" \
"v_mfma_f32_16x16x32_bf16 v[132:135], a[8:11], a[80:83], v[132:135]\n" \
"s_mov_b32 s96, s57\n" \
"ds_read_b64_tr_b16 a[126:127], v12 offset:50688\n" \
"v_mul_f32 v64, s20, v64\n" \
"v_mfma_f32_16x16x32_bf16 v[132:135], a[12:15], a[84:87], v[132:135]\n" \
"v_mul_f32 v65, s20, v65\n" \
"v_mul_f32 v66, s20, v66\n" \
"v_mfma_f32_16x16x32_bf16 v[144:147], a[16:19], a[80:83], v[144:147]\n" \
"v_mul_f32 v67, s20, v67\n" \
"v_cvt_pk_bf16_f32 v30, v64, v65\n" \
"v_mfma_f32_16x16x32_bf16 v[144:147], a[20:23], a[84:87], v[144:147]\n" \
"v_cvt_pk_bf16_f32 v31, v66, v67\n" \
"v_mfma_f32_16x16x32_bf16 v[148:151], a[24:27], a[80:83], v[148:151]\n" \
"v_mul_f32 v68, s20, v68\n" \
"v_mul_f32 v69, s20, v69\n" \
"v_mfma_f32_16x16x32_bf16 v[148:151], a[28:31], a[84:87], v[148:151]\n" \
"v_mul_f32 v70, s20, v70\n" \
"v_mul_f32 v71, s20, v71\n" \
"v_mfma_f32_16x16x32_bf16 v[160:163], a[32:35], a[80:83], v[160:163]\n" \
"v_cvt_pk_bf16_f32 v32, v68, v69\n" \
"v_mfma_f32_16x16x32_bf16 v[160:163], a[36:39], a[84:87], v[160:163]\n" \
"v_cvt_pk_bf16_f32 v33, v70, v71\n" \
"v_mul_f32 v72, s20, v72\n" \
"v_mfma_f32_16x16x32_bf16 v[164:167], a[40:43], a[80:83], v[164:167]\n" \
"buffer_store_dwordx4 v[30:33], v4, s[36:39], s96 offen offset:0 sc1\n" \
"v_mfma_f32_16x16x32_bf16 v[164:167], a[44:47], a[84:87], v[164:167]\n" \
"s_waitcnt lgkmcnt(0)\n" \
"s_add_u32 s97, s96, s98\n" \
"v_mul_f32 v73, s20, v73\n" \
"v_mfma_f32_16x16x32_bf16 v[176:179], a[48:51], a[80:83], v[176:179]\n" \
"s_barrier\n" \
"v_mul_f32 v74, s20, v74\n" \
"v_mul_f32 v75, s20, v75\n" \
"v_mfma_f32_16x16x32_bf16 v[176:179], a[52:55], a[84:87], v[176:179]\n" \
"v_cvt_pk_bf16_f32 v34, v72, v73\n" \
"v_cvt_pk_bf16_f32 v35, v74, v75\n" \
"v_mfma_f32_16x16x32_bf16 v[180:183], a[56:59], a[80:83], v[180:183]\n" \
"v_mul_f32 v76, s20, v76\n" \
"v_mfma_f32_16x16x32_bf16 v[180:183], a[60:63], a[84:87], v[180:183]\n" \
"v_add_u32 v5, s54, v5\n" \
"v_mul_f32 v77, s20, v77\n" \
"v_mfma_f32_16x16x32_bf16 v[136:139], a[0:3], a[88:91], v[136:139]\n" \
"v_mul_f32 v78, s20, v78\n" \
"v_mul_f32 v79, s20, v79\n" \
"v_mfma_f32_16x16x32_bf16 v[136:139], a[4:7], a[92:95], v[136:139]\n" \
"v_cvt_pk_bf16_f32 v36, v76, v77\n" \
"v_mfma_f32_16x16x32_bf16 v[140:143], a[8:11], a[88:91], v[140:143]\n" \
"v_add_u32 v6, s54, v6\n" \
"v_cvt_pk_bf16_f32 v37, v78, v79\n" \
"v_mfma_f32_16x16x32_bf16 v[140:143], a[12:15], a[92:95], v[140:143]\n" \
"v_add_u32 v7, s54, v7\n" \
"v_mul_f32 v80, s20, v80\n" \
"v_mfma_f32_16x16x32_bf16 v[152:155], a[16:19], a[88:91], v[152:155]\n" \
"v_mul_f32 v81, s20, v81\n" \
"v_mfma_f32_16x16x32_bf16 v[152:155], a[20:23], a[92:95], v[152:155]\n" \
"v_mul_f32 v82, s20, v82\n" \
"v_mul_f32 v83, s20, v83\n" \
"v_mfma_f32_16x16x32_bf16 v[156:159], a[24:27], a[88:91], v[156:159]\n" \
"v_add_u32 v8, s54, v8\n" \
"v_cvt_pk_bf16_f32 v38, v80, v81\n" \
"v_mfma_f32_16x16x32_bf16 v[156:159], a[28:31], a[92:95], v[156:159]\n" \
"v_sub_u32 v9, v9, v62\n" \
"v_mfma_f32_16x16x32_bf16 v[168:171], a[32:35], a[88:91], v[168:171]\n" \
"buffer_store_dwordx4 v[34:37], v4, s[36:39], s97 offen offset:0 sc1\n" \
"v_mfma_f32_16x16x32_bf16 v[168:171], a[36:39], a[92:95], v[168:171]\n" \
"s_add_u32 s96, s96, s92\n" \
"v_cvt_pk_bf16_f32 v39, v82, v83\n" \
"v_mul_f32 v84, s20, v84\n" \
"v_mfma_f32_16x16x32_bf16 v[172:175], a[40:43], a[88:91], v[172:175]\n" \
"v_sub_u32 v10, v10, v62\n" \
"v_mul_f32 v85, s20, v85\n" \
"v_mfma_f32_16x16x32_bf16 v[172:175], a[44:47], a[92:95], v[172:175]\n" \
"v_sub_u32 v11, v11, v62\n" \
"v_mfma_f32_16x16x32_bf16 v[184:187], a[48:51], a[88:91], v[184:187]\n" \
"v_mul_f32 v86, s20, v86\n" \
"v_mul_f32 v87, s20, v87\n" \
"v_mfma_f32_16x16x32_bf16 v[184:187], a[52:55], a[92:95], v[184:187]\n" \
"v_cvt_pk_bf16_f32 v40, v84, v85\n" \
"v_cvt_pk_bf16_f32 v41, v86, v87\n" \
"v_mfma_f32_16x16x32_bf16 v[188:191], a[56:59], a[88:91], v[188:191]\n" \
"v_sub_u32 v12, v12, v62\n" \
"v_mfma_f32_16x16x32_bf16 v[188:191], a[60:63], a[92:95], v[188:191]\n" \
"v_mul_f32 v88, s20, v88\n" \
"v_mul_f32 v89, s20, v89\n" \
"v_mfma_f32_16x16x32_bf16 v[192:195], a[0:3], a[96:99], v[192:195]\n" \
"s_mov_b32 s46, 128\n" \
"s_add_u32 m0, s50, 0\n" \
"s_waitcnt vmcnt(14) lgkmcnt(0)\n" \
"v_mfma_f32_16x16x32_bf16 v[192:195], a[4:7], a[100:103], v[192:195]\n" \
"buffer_load_dwordx4 v13, s[28:31], s46 offen lds\n" \
"v_mfma_f32_16x16x32_bf16 v[196:199], a[8:11], a[96:99], v[196:199]\n" \
"ds_read_b128 a[200:203], v50 offset:512\n" \
"v_mul_f32 v90, s20, v90\n" \
"v_mfma_f32_16x16x32_bf16 v[196:199], a[12:15], a[100:103], v[196:199]\n" \
"v_mul_f32 v91, s20, v91\n" \
"v_cvt_pk_bf16_f32 v42, v88, v89\n" \
"v_mfma_f32_16x16x32_bf16 v[208:211], a[16:19], a[96:99], v[208:211]\n" \
"ds_read2_b32 a[64:65], v46 offset0:0 offset1:1\n" \
"v_cvt_pk_bf16_f32 v43, v90, v91\n" \
"v_mfma_f32_16x16x32_bf16 v[208:211], a[20:23], a[100:103], v[208:211]\n" \
"v_mul_f32 v92, s20, v92\n" \
"v_mul_f32 v93, s20, v93\n" \
"v_mfma_f32_16x16x32_bf16 v[212:215], a[24:27], a[96:99], v[212:215]\n" \
"ds_read2_b32 a[66:67], v46 offset0:2 offset1:3\n" \
"v_mul_f32 v94, s20, v94\n" \
"v_mfma_f32_16x16x32_bf16 v[212:215], a[28:31], a[100:103], v[212:215]\n" \
"buffer_store_dwordx4 v[38:41], v4, s[36:39], s96 offen offset:0 sc1\n" \
"v_mfma_f32_16x16x32_bf16 v[224:227], a[32:35], a[96:99], v[224:227]\n" \
"s_add_u32 m0, s50, 4096\n" \
"ds_read_b128 a[204:207], v50 offset:1024\n" \
"s_add_u32 s97, s96, s98\n" \
"v_mfma_f32_16x16x32_bf16 v[224:227], a[36:39], a[100:103], v[224:227]\n" \
"buffer_load_dwordx4 v14, s[28:31], s46 offen lds\n" \
"v_mfma_f32_16x16x32_bf16 v[228:231], a[40:43], a[96:99], v[228:231]\n" \
"ds_read2_b32 a[68:69], v46 offset0:128 offset1:129\n" \
"v_mul_f32 v95, s20, v95\n" \
"v_mfma_f32_16x16x32_bf16 v[228:231], a[44:47], a[100:103], v[228:231]\n" \
"v_cvt_pk_bf16_f32 v44, v92, v93\n" \
"v_cvt_pk_bf16_f32 v45, v94, v95\n" \
"v_mfma_f32_16x16x32_bf16 v[240:243], a[48:51], a[96:99], v[240:243]\n" \
"ds_read2_b32 a[70:71], v46 offset0:130 offset1:131\n" \
"v_mul_f32 v96, s20, v96\n" \
"v_mfma_f32_16x16x32_bf16 v[240:243], a[52:55], a[100:103], v[240:243]\n" \
"v_mul_f32 v97, s20, v97\n" \
"v_mul_f32 v98, s20, v98\n" \
"v_mfma_f32_16x16x32_bf16 v[244:247], a[56:59], a[96:99], v[244:247]\n" \
"ds_read_b128 a[208:211], v50 offset:1536\n" \
"v_mul_f32 v99, s20, v99\n" \
"v_mfma_f32_16x16x32_bf16 v[244:247], a[60:63], a[100:103], v[244:247]\n" \
"v_cvt_pk_bf16_f32 v30, v96, v97\n" \
"v_cvt_pk_bf16_f32 v31, v98, v99\n" \
"v_mfma_f32_16x16x32_bf16 v[200:203], a[0:3], a[104:107], v[200:203]\n" \
"s_add_u32 m0, s50, 8192\n" \
"ds_read2_b32 a[72:73], v47 offset0:0 offset1:1\n" \
"v_mul_f32 v100, s20, v100\n" \
"v_mfma_f32_16x16x32_bf16 v[200:203], a[4:7], a[108:111], v[200:203]\n" \
"buffer_load_dwordx4 v15, s[28:31], s46 offen lds\n" \
"v_mfma_f32_16x16x32_bf16 v[204:207], a[8:11], a[104:107], v[204:207]\n" \
"ds_read2_b32 a[74:75], v47 offset0:2 offset1:3\n" \
"v_mul_f32 v101, s20, v101\n" \
"v_mfma_f32_16x16x32_bf16 v[204:207], a[12:15], a[108:111], v[204:207]\n" \
"v_mul_f32 v102, s20, v102\n" \
"v_mul_f32 v103, s20, v103\n" \
"v_mfma_f32_16x16x32_bf16 v[216:219], a[16:19], a[104:107], v[216:219]\n" \
"ds_read_b128 a[212:215], v50 offset:2048\n" \
"v_cvt_pk_bf16_f32 v32, v100, v101\n" \
"v_mfma_f32_16x16x32_bf16 v[216:219], a[20:23], a[108:111], v[216:219]\n" \
"buffer_store_dwordx4 v[42:45], v4, s[36:39], s97 offen offset:0 sc1\n" \
"v_mfma_f32_16x16x32_bf16 v[220:223], a[24:27], a[104:107], v[220:223]\n" \
"s_add_u32 s96, s96, s92\n" \
"ds_read2_b32 a[76:77], v47 offset0:128 offset1:129\n" \
"v_cvt_pk_bf16_f32 v33, v102, v103\n" \
"v_mfma_f32_16x16x32_bf16 v[220:223], a[28:31], a[108:111], v[220:223]\n" \
"v_mul_f32 v104, s20, v104\n" \
"v_mul_f32 v105, s20, v105\n" \
"v_mfma_f32_16x16x32_bf16 v[232:235], a[32:35], a[104:107], v[232:235]\n" \
"s_add_u32 m0, s50, 12288\n" \
"ds_read2_b32 a[78:79], v47 offset0:130 offset1:131\n" \
"v_mul_f32 v106, s20, v106\n" \
"v_mfma_f32_16x16x32_bf16 v[232:235], a[36:39], a[108:111], v[232:235]\n" \
"buffer_load_dwordx4 v16, s[28:31], s46 offen lds\n" \
"v_mfma_f32_16x16x32_bf16 v[236:239], a[40:43], a[104:107], v[236:239]\n" \
"ds_read_b128 a[216:219], v50 offset:2560\n" \
"v_mul_f32 v107, s20, v107\n" \
"v_mfma_f32_16x16x32_bf16 v[236:239], a[44:47], a[108:111], v[236:239]\n" \
"v_cvt_pk_bf16_f32 v34, v104, v105\n" \
"v_cvt_pk_bf16_f32 v35, v106, v107\n" \
"v_mfma_f32_16x16x32_bf16 v[248:251], a[48:51], a[104:107], v[248:251]\n" \
"ds_read2_b32 a[80:81], v48 offset0:0 offset1:1\n" \
"v_mul_f32 v108, s20, v108\n" \
"v_mfma_f32_16x16x32_bf16 v[248:251], a[52:55], a[108:111], v[248:251]\n" \
"v_mul_f32 v109, s20, v109\n" \
"v_mul_f32 v110, s20, v110\n" \
"v_mfma_f32_16x16x32_bf16 v[252:255], a[56:59], a[104:107], v[252:255]\n" \
"ds_read2_b32 a[82:83], v48 offset0:2 offset1:3\n" \
"v_mul_f32 v111, s20, v111\n" \
"v_mfma_f32_16x16x32_bf16 v[252:255], a[60:63], a[108:111], v[252:255]\n" \
"v_cvt_pk_bf16_f32 v36, v108, v109\n" \
"v_cvt_pk_bf16_f32 v37, v110, v111\n" \
"v_mfma_f32_16x16x32_bf16 a[128:131], a[0:3], a[112:115], a[128:131]\n" \
"s_mov_b32 s46, 128\n" \
"s_add_u32 m0, s50, 16384\n" \
"ds_read_b128 a[220:223], v50 offset:3072\n" \
"v_mfma_f32_16x16x32_bf16 a[128:131], a[4:7], a[116:119], a[128:131]\n" \
"buffer_load_dwordx4 v17, s[28:31], s46 offen lds\n" \
"v_mfma_f32_16x16x32_bf16 a[132:135], a[8:11], a[112:115], a[132:135]\n" \
"ds_read2_b32 a[84:85], v48 offset0:128 offset1:129\n" \
"v_mul_f32 v112, s20, v112\n" \
"v_mfma_f32_16x16x32_bf16 a[132:135], a[12:15], a[116:119], a[132:135]\n" \
"buffer_store_dwordx4 v[30:33], v4, s[36:39], s96 offen offset:0 sc1\n" \
"v_mfma_f32_16x16x32_bf16 a[144:147], a[16:19], a[112:115], a[144:147]\n" \
"s_add_u32 s97, s96, s98\n" \
"ds_read2_b32 a[86:87], v48 offset0:130 offset1:131\n" \
"v_mul_f32 v113, s20, v113\n" \
"v_mfma_f32_16x16x32_bf16 a[144:147], a[20:23], a[116:119], a[144:147]\n" \
"v_mul_f32 v114, s20, v114\n" \
"v_mul_f32 v115, s20, v115\n" \
"v_mfma_f32_16x16x32_bf16 a[148:151], a[24:27], a[112:115], a[148:151]\n" \
"ds_read_b128 a[224:227], v50 offset:3584\n" \
"v_cvt_pk_bf16_f32 v38, v112, v113\n" \
"v_mfma_f32_16x16x32_bf16 a[148:151], a[28:31], a[116:119], a[148:151]\n" \
"v_cvt_pk_bf16_f32 v39, v114, v115\n" \
"v_mul_f32 v116, s20, v116\n" \
"v_mfma_f32_16x16x32_bf16 a[160:163], a[32:35], a[112:115], a[160:163]\n" \
"s_add_u32 m0, s50, 20480\n" \
"ds_read2_b32 a[88:89], v49 offset0:0 offset1:1\n" \
"v_mul_f32 v117, s20, v117\n" \
"v_mfma_f32_16x16x32_bf16 a[160:163], a[36:39], a[116:119], a[160:163]\n" \
"buffer_load_dwordx4 v18, s[28:31], s46 offen lds\n" \
"v_mfma_f32_16x16x32_bf16 a[164:167], a[40:43], a[112:115], a[164:167]\n" \
"ds_read2_b32 a[90:91], v49 offset0:2 offset1:3\n" \
"v_mul_f32 v118, s20, v118\n" \
"v_mfma_f32_16x16x32_bf16 a[164:167], a[44:47], a[116:119], a[164:167]\n" \
"v_mul_f32 v119, s20, v119\n" \
"v_cvt_pk_bf16_f32 v40, v116, v117\n" \
"v_mfma_f32_16x16x32_bf16 a[176:179], a[48:51], a[112:115], a[176:179]\n" \
"ds_read_b128 a[228:231], v50 offset:4096\n" \
"v_cvt_pk_bf16_f32 v41, v118, v119\n" \
"v_mfma_f32_16x16x32_bf16 a[176:179], a[52:55], a[116:119], a[176:179]\n" \
"v_mul_f32 v120, s20, v120\n" \
"v_mul_f32 v121, s20, v121\n" \
"v_mfma_f32_16x16x32_bf16 a[180:183], a[56:59], a[112:115], a[180:183]\n" \
"ds_read2_b32 a[92:93], v49 offset0:128 offset1:129\n" \
"v_mul_f32 v122, s20, v122\n" \
"v_mfma_f32_16x16x32_bf16 a[180:183], a[60:63], a[116:119], a[180:183]\n" \
"v_mul_f32 v123, s20, v123\n" \
"v_cvt_pk_bf16_f32 v42, v120, v121\n" \
"v_mfma_f32_16x16x32_bf16 a[136:139], a[0:3], a[120:123], a[136:139]\n" \
"s_add_u32 m0, s50, 24576\n" \
"ds_read2_b32 a[94:95], v49 offset0:130 offset1:131\n" \
"v_cvt_pk_bf16_f32 v43, v122, v123\n" \
"v_mfma_f32_16x16x32_bf16 a[136:139], a[4:7], a[124:127], a[136:139]\n" \
"buffer_load_dwordx4 v19, s[28:31], s46 offen lds\n" \
"v_mfma_f32_16x16x32_bf16 a[140:143], a[8:11], a[120:123], a[140:143]\n" \
"v_mul_f32 v124, s20, v124\n" \
"v_mul_f32 v125, s20, v125\n" \
"v_mfma_f32_16x16x32_bf16 a[140:143], a[12:15], a[124:127], a[140:143]\n" \
"buffer_store_dwordx4 v[34:37], v4, s[36:39], s97 offen offset:0 sc1\n" \
"v_mfma_f32_16x16x32_bf16 a[152:155], a[16:19], a[120:123], a[152:155]\n" \
"s_add_u32 s96, s96, s92\n" \
"v_mul_f32 v126, s20, v126\n" \
"v_mul_f32 v127, s20, v127\n" \
"v_mfma_f32_16x16x32_bf16 a[152:155], a[20:23], a[124:127], a[152:155]\n" \
"v_cvt_pk_bf16_f32 v44, v124, v125\n" \
"v_cvt_pk_bf16_f32 v45, v126, v127\n" \
"v_mfma_f32_16x16x32_bf16 a[156:159], a[24:27], a[120:123], a[156:159]\n" \
"v_mul_f32 v128, s20, v128\n" \
"v_mfma_f32_16x16x32_bf16 a[156:159], a[28:31], a[124:127], a[156:159]\n" \
"v_mul_f32 v129, s20, v129\n" \
"v_mul_f32 v130, s20, v130\n" \
"v_mfma_f32_16x16x32_bf16 a[168:171], a[32:35], a[120:123], a[168:171]\n" \
"s_add_u32 m0, s50, 28672\n" \
"v_mul_f32 v131, s20, v131\n" \
"v_cvt_pk_bf16_f32 v30, v128, v129\n" \
"v_mfma_f32_16x16x32_bf16 a[168:171], a[36:39], a[124:127], a[168:171]\n" \
"buffer_load_dwordx4 v20, s[28:31], s46 offen lds\n" \
"v_mfma_f32_16x16x32_bf16 a[172:175], a[40:43], a[120:123], a[172:175]\n" \
"v_cvt_pk_bf16_f32 v31, v130, v131\n" \
"v_mul_f32 v132, s20, v132\n" \
"v_mfma_f32_16x16x32_bf16 a[172:175], a[44:47], a[124:127], a[172:175]\n" \
"v_mul_f32 v133, s20, v133\n" \
"v_mul_f32 v134, s20, v134\n" \
"v_mfma_f32_16x16x32_bf16 a[184:187], a[48:51], a[120:123], a[184:187]\n" \
"v_mul_f32 v135, s20, v135\n" \
"v_mfma_f32_16x16x32_bf16 a[184:187], a[52:55], a[124:127], a[184:187]\n" \
"v_cvt_pk_bf16_f32 v32, v132, v133\n" \
"v_cvt_pk_bf16_f32 v33, v134, v135\n" \
"v_mfma_f32_16x16x32_bf16 a[188:191], a[56:59], a[120:123], a[188:191]\n" \
"s_waitcnt lgkmcnt(0)\n" \
"v_mul_f32 v136, s20, v136\n" \
"v_mul_f32 v137, s20, v137\n" \
"v_mfma_f32_16x16x32_bf16 a[188:191], a[60:63], a[124:127], a[188:191]\n" \
"v_mul_f32 v138, s20, v138\n" \
"LC_%=:\n" \
"v_mfma_f32_16x16x32_bf16 v[64:67], a[64:67], a[200:203], 0\n" \
"ds_read_b128 a[32:35], v50 offset:448\n" \
"v_mul_f32 v139, s20, v139\n" \
"v_mfma_f32_16x16x32_bf16 v[68:71], a[64:67], a[204:207], 0\n" \
"buffer_store_dwordx4 v[38:41], v4, s[36:39], s96 offen offset:0 sc1\n" \
"v_mfma_f32_16x16x32_bf16 v[68:71], a[68:71], a[200:203], v[68:71]\n" \
"s_add_u32 s97, s96, s98\n" \
"ds_read2_b32 a[0:1], v46 offset0:16 offset1:17\n" \
"v_cvt_pk_bf16_f32 v34, v136, v137\n" \
"v_mfma_f32_16x16x32_bf16 v[72:75], a[64:67], a[208:211], 0\n" \
"ds_read2_b32 a[2:3], v46 offset0:18 offset1:19\n" \
"v_cvt_pk_bf16_f32 v35, v138, v139\n" \
"v_mfma_f32_16x16x32_bf16 v[72:75], a[68:71], a[204:207], v[72:75]\n" \
"s_mov_b32 s47, 524288\n" \
"s_add_u32 m0, s51, 49152\n" \
"ds_read_b128 a[36:39], v50 offset:960\n" \
"v_mfma_f32_16x16x32_bf16 v[72:75], a[72:75], a[200:203], v[72:75]\n" \
"buffer_load_dwordx4 v21, s[32:35], s47 offen offset:0 lds\n" \
"v_mfma_f32_16x16x32_bf16 v[76:79], a[64:67], a[212:215], 0\n" \
"ds_read2_b32 a[4:5], v46 offset0:144 offset1:145\n" \
"v_mul_f32 v140, s20, v140\n" \
"v_mfma_f32_16x16x32_bf16 v[76:79], a[68:71], a[208:211], v[76:79]\n" \
"ds_read2_b32 a[6:7], v46 offset0:146 offset1:147\n" \
"v_mul_f32 v141, s20, v141\n" \
"v_mfma_f32_16x16x32_bf16 v[76:79], a[72:75], a[204:207], v[76:79]\n" \
"ds_read_b128 a[40:43], v50 offset:1472\n" \
"v_mul_f32 v142, s20, v142\n" \
"v_mfma_f32_16x16x32_bf16 v[76:79], a[76:79], a[200:203], v[76:79]\n" \
"v_mul_f32 v143, s20, v143\n" \
"v_cvt_pk_bf16_f32 v36, v140, v141\n" \
"v_mfma_f32_16x16x32_bf16 v[80:83], a[64:67], a[216:219], 0\n" \
"ds_read2_b32 a[8:9], v47 offset0:16 offset1:17\n" \
"v_cvt_pk_bf16_f32 v37, v142, v143\n" \
"v_mfma_f32_16x16x32_bf16 v[80:83], a[68:71], a[212:215], v[80:83]\n" \
"ds_read2_b32 a[10:11], v47 offset0:18 offset1:19\n" \
"v_mul_f32 v144, s20, v144\n" \
"v_mfma_f32_16x16x32_bf16 v[80:83], a[72:75], a[208:211], v[80:83]\n" \
"s_add_u32 m0, s51, 50048\n" \
"ds_read_b128 a[44:47], v50 offset:1984\n" \
"v_mul_f32 v145, s20, v145\n" \
"v_mfma_f32_16x16x32_bf16 v[80:83], a[76:79], a[204:207], v[80:83]\n" \
"buffer_load_dwordx4 v21, s[32:35], s47 offen offset:128 lds\n" \
"v_mfma_f32_16x16x32_bf16 v[80:83], a[80:83], a[200:203], v[80:83]\n" \
"ds_read2_b32 a[12:13], v47 offset0:144 offset1:145\n" \
"v_mul_f32 v146, s20, v146\n" \
"v_mfma_f32_16x16x32_bf16 v[84:87], a[64:67], a[220:223], 0\n" \
"ds_read2_b32 a[14:15], v47 offset0:146 offset1:147\n" \
"v_mul_f32 v147, s20, v147\n" \
"v_mfma_f32_16x16x32_bf16 v[84:87], a[68:71], a[216:219], v[84:87]\n" \
"ds_read_b128 a[48:51], v50 offset:2496\n" \
"v_cvt_pk_bf16_f32 v38, v144, v145\n" \
"v_mfma_f32_16x16x32_bf16 v[84:87], a[72:75], a[212:215], v[84:87]\n" \
"buffer_store_dwordx4 v[42:45], v4, s[36:39], s97 offen offset:0 sc1\n" \
"v_mfma_f32_16x16x32_bf16 v[84:87], a[76:79], a[208:211], v[84:87]\n" \
"s_add_u32 s57, s57, s93\n" \
"ds_read2_b32 a[16:17], v48 offset0:16 offset1:17\n" \
"s_mov_b32 s96, s57\n" \
"v_mfma_f32_16x16x32_bf16 v[84:87], a[80:83], a[204:207], v[84:87]\n" \
"ds_read2_b32 a[18:19], v48 offset0:18 offset1:19\n" \
"v_cvt_pk_bf16_f32 v39, v146, v147\n" \
"v_mfma_f32_16x16x32_bf16 v[84:87], a[84:87], a[200:203], v[84:87]\n" \
"s_mov_b32 s47, 786432\n" \
"s_add_u32 m0, s51, 57344\n" \
"ds_read_b128 a[52:55], v50 offset:3008\n" \
"v_mfma_f32_16x16x32_bf16 v[88:91], a[64:67], a[224:227], 0\n" \
"buffer_load_dwordx4 v21, s[32:35], s47 offen offset:0 lds\n" \
"v_mfma_f32_16x16x32_bf16 v[88:91], a[68:71], a[220:223], v[88:91]\n" \
"ds_read2_b32 a[20:21], v48 offset0:144 offset1:145\n" \
"v_mul_f32 v148, s20, v148\n" \
"v_mfma_f32_16x16x32_bf16 v[88:91], a[72:75], a[216:219], v[88:91]\n" \
"ds_read2_b32 a[22:23], v48 offset0:146 offset1:147\n" \
"v_mul_f32 v149, s20, v149\n" \
"v_mfma_f32_16x16x32_bf16 v[88:91], a[76:79], a[212:215], v[88:91]\n" \
"ds_read_b128 a[56:59], v50 offset:3520\n" \
"v_mul_f32 v150, s20, v150\n" \
"v_mfma_f32_16x16x32_bf16 v[88:91], a[80:83], a[208:211], v[88:91]\n" \
"v_mul_f32 v151, s20, v151\n" \
"v_cvt_pk_bf16_f32 v40, v148, v149\n" \
"v_mfma_f32_16x16x32_bf16 v[88:91], a[84:87], a[204:207], v[88:91]\n" \
"ds_read2_b32 a[24:25], v49 offset0:16 offset1:17\n" \
"v_cvt_pk_bf16_f32 v41, v150, v151\n" \
"v_mfma_f32_16x16x32_bf16 v[88:91], a[88:91], a[200:203], v[88:91]\n" \
"ds_read2_b32 a[26:27], v49 offset0:18 offset1:19\n" \
"v_mul_f32 v152, s20, v152\n" \
"v_mfma_f32_16x16x32_bf16 v[92:95], a[64:67], a[228:231], 0\n" \
"s_add_u32 m0, s51, 58240\n" \
"ds_read_b128 a[60:63], v50 offset:4032\n" \
"v_mul_f32 v153, s20, v153\n" \
"v_mfma_f32_16x16x32_bf16 v[92:95], a[68:71], a[224:227], v[92:95]\n" \
"buffer_load_dwordx4 v21, s[32:35], s47 offen offset:128 lds\n" \
"v_mfma_f32_16x16x32_bf16 v[92:95], a[72:75], a[220:223], v[92:95]\n" \
"ds_read2_b32 a[28:29], v49 offset0:144 offset1:145\n" \
"v_mul_f32 v154, s20, v154\n" \
"v_mfma_f32_16x16x32_bf16 v[92:95], a[76:79], a[216:219], v[92:95]\n" \
"ds_read2_b32 a[30:31], v49 offset0:146 offset1:147\n" \
"v_mul_f32 v155, s20, v155\n" \
"v_mfma_f32_16x16x32_bf16 v[92:95], a[80:83], a[212:215], v[92:95]\n" \
"buffer_store_dwordx4 v[30:33], v4, s[36:39], s96 offen offset:0 sc1\n" \
"v_mfma_f32_16x16x32_bf16 v[92:95], a[84:87], a[208:211], v[92:95]\n" \
"s_add_u32 s97, s96, s98\n" \
"v_cvt_pk_bf16_f32 v42, v152, v153\n" \
"v_cvt_pk_bf16_f32 v43, v154, v155\n" \
"v_mfma_f32_16x16x32_bf16 v[92:95], a[88:91], a[204:207], v[92:95]\n" \
"v_mul_f32 v156, s20, v156\n" \
"v_mul_f32 v157, s20, v157\n" \
"v_mfma_f32_16x16x32_bf16 v[92:95], a[92:95], a[200:203], v[92:95]\n" \
"s_waitcnt lgkmcnt(0)\n" \
"v_mul_f32 v158, s20, v158\n" \
"v_mfma_f32_16x16x32_bf16 v[64:67], a[0:3], a[32:35], v[64:67]\n" \
"ds_read_b128 a[200:203], v50 offset:384\n" \
"v_mul_f32 v159, s20, v159\n" \
"v_mfma_f32_16x16x32_bf16 v[68:71], a[0:3], a[36:39], v[68:71]\n" \
"v_cvt_pk_bf16_f32 v44, v156, v157\n" \
"v_cvt_pk_bf16_f32 v45, v158, v159\n" \
"v_mfma_f32_16x16x32_bf16 v[68:71], a[4:7], a[32:35], v[68:71]\n" \
"ds_read2_b32 a[64:65], v46 offset0:32 offset1:33\n" \
"v_mul_f32 v160, s20, v160\n" \
"v_mfma_f32_16x16x32_bf16 v[72:75], a[0:3], a[40:43], v[72:75]\n" \
"ds_read2_b32 a[66:67], v46 offset0:34 offset1:35\n" \
"v_mul_f32 v161, s20, v161\n" \
"v_mfma_f32_16x16x32_bf16 v[72:75], a[4:7], a[36:39], v[72:75]\n" \
"ds_read_b128 a[204:207], v50 offset:896\n" \
"v_mul_f32 v162, s20, v162\n" \
"v_mfma_f32_16x16x32_bf16 v[72:75], a[8:11], a[32:35], v[72:75]\n" \
"v_mul_f32 v163, s20, v163\n" \
"v_cvt_pk_bf16_f32 v30, v160, v161\n" \
"v_mfma_f32_16x16x32_bf16 v[76:79], a[0:3], a[44:47], v[76:79]\n" \
"ds_read2_b32 a[68:69], v46 offset0:160 offset1:161\n" \
"v_cvt_pk_bf16_f32 v31, v162, v163\n" \
"v_mfma_f32_16x16x32_bf16 v[76:79], a[4:7], a[40:43], v[76:79]\n" \
"ds_read2_b32 a[70:71], v46 offset0:162 offset1:163\n" \
"v_mul_f32 v164, s20, v164\n" \
"v_mfma_f32_16x16x32_bf16 v[76:79], a[8:11], a[36:39], v[76:79]\n" \
"ds_read_b128 a[208:211], v50 offset:1408\n" \
"v_mul_f32 v165, s20, v165\n" \
"v_mfma_f32_16x16x32_bf16 v[76:79], a[12:15], a[32:35], v[76:79]\n" \
"buffer_store_dwordx4 v[34:37], v4, s[36:39], s97 offen offset:0 sc1\n" \
"v_mfma_f32_16x16x32_bf16 v[80:83], a[0:3], a[48:51], v[80:83]\n" \
"s_add_u32 s96, s96, s92\n" \
"ds_read2_b32 a[72:73], v47 offset0:32 offset1:33\n" \
"v_mul_f32 v166, s20, v166\n" \
"v_mfma_f32_16x16x32_bf16 v[80:83], a[4:7], a[44:47], v[80:83]\n" \
"ds_read2_b32 a[74:75], v47 offset0:34 offset1:35\n" \
"v_mul_f32 v167, s20, v167\n" \
"v_mfma_f32_16x16x32_bf16 v[80:83], a[8:11], a[40:43], v[80:83]\n" \
"ds_read_b128 a[212:215], v50 offset:1920\n" \
"v_cvt_pk_bf16_f32 v32, v164, v165\n" \
"v_mfma_f32_16x16x32_bf16 v[80:83], a[12:15], a[36:39], v[80:83]\n" \
"v_cvt_pk_bf16_f32 v33, v166, v167\n" \
"v_mul_f32 v168, s20, v168\n" \
"v_mfma_f32_16x16x32_bf16 v[80:83], a[16:19], a[32:35], v[80:83]\n" \
"ds_read2_b32 a[76:77], v47 offset0:160 offset1:161\n" \
"v_mul_f32 v169, s20, v169\n" \
"v_mfma_f32_16x16x32_bf16 v[84:87], a[0:3], a[52:55], v[84:87]\n" \
"ds_read2_b32 a[78:79], v47 offset0:162 offset1:163\n" \
"v_mul_f32 v170, s20, v170\n" \
"v_mfma_f32_16x16x32_bf16 v[84:87], a[4:7], a[48:51], v[84:87]\n" \
"ds_read_b128 a[216:219], v50 offset:2432\n" \
"v_mul_f32 v171, s20, v171\n" \
"v_mfma_f32_16x16x32_bf16 v[84:87], a[8:11], a[44:47], v[84:87]\n" \
"v_cvt_pk_bf16_f32 v34, v168, v169\n" \
"v_cvt_pk_bf16_f32 v35, v170, v171\n" \
"v_mfma_f32_16x16x32_bf16 v[84:87], a[12:15], a[40:43], v[84:87]\n" \
"ds_read2_b32 a[80:81], v48 offset0:32 offset1:33\n" \
"v_mul_f32 v172, s20, v172\n" \
"v_mfma_f32_16x16x32_bf16 v[84:87], a[16:19], a[36:39], v[84:87]\n" \
"ds_read2_b32 a[82:83], v48 offset0:34 offset1:35\n" \
"v_mul_f32 v173, s20, v173\n" \
"v_mfma_f32_16x16x32_bf16 v[84:87], a[20:23], a[32:35], v[84:87]\n" \
"ds_read_b128 a[220:223], v50 offset:2944\n" \
"v_mul_f32 v174, s20, v174\n" \
"v_mfma_f32_16x16x32_bf16 v[88:91], a[0:3], a[56:59], v[88:91]\n" \
"buffer_store_dwordx4 v[38:41], v4, s[36:39], s96 offen offset:0 sc1\n" \
"v_mfma_f32_16x16x32_bf16 v[88:91], a[4:7], a[52:55], v[88:91]\n" \
"s_add_u32 s97, s96, s98\n" \
"ds_read2_b32 a[84:85], v48 offset0:160 offset1:161\n" \
"v_mul_f32 v175, s20, v175\n" \
"v_mfma_f32_16x16x32_bf16 v[88:91], a[8:11], a[48:51], v[88:91]\n" \
"ds_read2_b32 a[86:87], v48 offset0:162 offset1:163\n" \
"v_cvt_pk_bf16_f32 v36, v172, v173\n" \
"v_mfma_f32_16x16x32_bf16 v[88:91], a[12:15], a[44:47], v[88:91]\n" \
"ds_read_b128 a[224:227], v50 offset:3456\n" \
"v_cvt_pk_bf16_f32 v37, v174, v175\n" \
"v_mfma_f32_16x16x32_bf16 v[88:91], a[16:19], a[40:43], v[88:91]\n" \
"v_mul_f32 v176, s20, v176\n" \
"v_mul_f32 v177, s20, v177\n" \
"v_mfma_f32_16x16x32_bf16 v[88:91], a[20:23], a[36:39], v[88:91]\n" \
"ds_read2_b32 a[88:89], v49 offset0:32 offset1:33\n" \
"v_mul_f32 v178, s20, v178\n" \
"v_mfma_f32_16x16x32_bf16 v[88:91], a[24:27], a[32:35], v[88:91]\n" \
"ds_read2_b32 a[90:91], v49 offset0:34 offset1:35\n" \
"v_mul_f32 v179, s20, v179\n" \
"v_mfma_f32_16x16x32_bf16 v[92:95], a[0:3], a[60:63], v[92:95]\n" \
"ds_read_b128 a[228:231], v50 offset:3968\n" \
"v_cvt_pk_bf16_f32 v38, v176, v177\n" \
"v_mfma_f32_16x16x32_bf16 v[92:95], a[4:7], a[56:59], v[92:95]\n" \
"v_cvt_pk_bf16_f32 v39, v178, v179\n" \
"v_mul_f32 v180, s20, v180\n" \
"v_mfma_f32_16x16x32_bf16 v[92:95], a[8:11], a[52:55], v[92:95]\n" \
"ds_read2_b32 a[92:93], v49 offset0:160 offset1:161\n" \
"v_mul_f32 v181, s20, v181\n" \
"v_mfma_f32_16x16x32_bf16 v[92:95], a[12:15], a[48:51], v[92:95]\n" \
"ds_read2_b32 a[94:95], v49 offset0:162 offset1:163\n" \
"v_mul_f32 v182, s20, v182\n" \
"v_mfma_f32_16x16x32_bf16 v[92:95], a[16:19], a[44:47], v[92:95]\n" \
"buffer_store_dwordx4 v[42:45], v4, s[36:39], s97 offen offset:0 sc1\n" \
"v_mfma_f32_16x16x32_bf16 v[92:95], a[20:23], a[40:43], v[92:95]\n" \
"s_add_u32 s96, s96, s92\n" \
"v_mul_f32 v183, s20, v183\n" \
"v_cvt_pk_bf16_f32 v40, v180, v181\n" \
"v_mfma_f32_16x16x32_bf16 v[92:95], a[24:27], a[36:39], v[92:95]\n" \
"v_cvt_pk_bf16_f32 v41, v182, v183\n" \
"v_mul_f32 v184, s20, v184\n" \
"v_mfma_f32_16x16x32_bf16 v[92:95], a[28:31], a[32:35], v[92:95]\n" \
"s_waitcnt lgkmcnt(0)\n" \
"v_mul_f32 v185, s20, v185\n" \
"v_mfma_f32_16x16x32_bf16 v[64:67], a[64:67], a[200:203], v[64:67]\n" \
"ds_read_b128 a[32:35], v50 offset:320\n" \
"v_mul_f32 v186, s20, v186\n" \
"v_mfma_f32_16x16x32_bf16 v[68:71], a[64:67], a[204:207], v[68:71]\n" \
"v_mul_f32 v187, s20, v187\n" \
"v_cvt_pk_bf16_f32 v42, v184, v185\n" \
"v_mfma_f32_16x16x32_bf16 v[68:71], a[68:71], a[200:203], v[68:71]\n" \
"ds_read2_b32 a[0:1], v46 offset0:48 offset1:49\n" \
"v_cvt_pk_bf16_f32 v43, v186, v187\n" \
"v_mfma_f32_16x16x32_bf16 v[72:75], a[64:67], a[208:211], v[72:75]\n" \
"ds_read2_b32 a[2:3], v46 offset0:50 offset1:51\n" \
"v_mul_f32 v188, s20, v188\n" \
"v_mfma_f32_16x16x32_bf16 v[72:75], a[68:71], a[204:207], v[72:75]\n" \
"ds_read_b128 a[36:39], v50 offset:832\n" \
"v_mul_f32 v189, s20, v189\n" \
"v_mfma_f32_16x16x32_bf16 v[72:75], a[72:75], a[200:203], v[72:75]\n" \
"v_mul_f32 v190, s20, v190\n" \
"v_mul_f32 v191, s20, v191\n" \
"v_mfma_f32_16x16x32_bf16 v[76:79], a[64:67], a[212:215], v[76:79]\n" \
"ds_read2_b32 a[4:5], v46 offset0:176 offset1:177\n" \
"v_cvt_pk_bf16_f32 v44, v188, v189\n" \
"v_mfma_f32_16x16x32_bf16 v[76:79], a[68:71], a[208:211], v[76:79]\n" \
"ds_read2_b32 a[6:7], v46 offset0:178 offset1:179\n" \
"v_cvt_pk_bf16_f32 v45, v190, v191\n" \
"v_mfma_f32_16x16x32_bf16 v[76:79], a[72:75], a[204:207], v[76:79]\n" \
"ds_read_b128 a[40:43], v50 offset:1344\n" \
"v_mul_f32 v192, s20, v192\n" \
"v_mfma_f32_16x16x32_bf16 v[76:79], a[76:79], a[200:203], v[76:79]\n" \
"buffer_store_dwordx4 v[30:33], v4, s[36:39], s96 offen offset:0 sc1\n" \
"v_mfma_f32_16x16x32_bf16 v[80:83], a[64:67], a[216:219], v[80:83]\n" \
"s_add_u32 s97, s96, s98\n" \
"ds_read2_b32 a[8:9], v47 offset0:48 offset1:49\n" \
"v_mul_f32 v193, s20, v193\n" \
"v_mfma_f32_16x16x32_bf16 v[80:83], a[68:71], a[212:215], v[80:83]\n" \
"ds_read2_b32 a[10:11], v47 offset0:50 offset1:51\n" \
"v_mul_f32 v194, s20, v194\n" \
"v_mfma_f32_16x16x32_bf16 v[80:83], a[72:75], a[208:211], v[80:83]\n" \
"ds_read_b128 a[44:47], v50 offset:1856\n" \
"v_mul_f32 v195, s20, v195\n" \
"v_mfma_f32_16x16x32_bf16 v[80:83], a[76:79], a[204:207], v[80:83]\n" \
"v_cvt_pk_bf16_f32 v30, v192, v193\n" \
"v_cvt_pk_bf16_f32 v31, v194, v195\n" \
"v_mfma_f32_16x16x32_bf16 v[80:83], a[80:83], a[200:203], v[80:83]\n" \
"ds_read2_b32 a[12:13], v47 offset0:176 offset1:177\n" \
"v_mul_f32 v196, s20, v196\n" \
"v_mfma_f32_16x16x32_bf16 v[84:87], a[64:67], a[220:223], v[84:87]\n" \
"LH8_%=:\ns_cbranch_execz LH12_%=\n" \
"ds_read2_b32 a[14:15], v47 offset0:178 offset1:179\n" \
"v_mul_f32 v197, s20, v197\n" \
"v_mfma_f32_16x16x32_bf16 v[84:87], a[68:71], a[216:219], v[84:87]\n" \
"ds_read_b128 a[48:51], v50 offset:2368\n" \
"v_mul_f32 v198, s20, v198\n" \
"v_mfma_f32_16x16x32_bf16 v[84:87], a[72:75], a[212:215], v[84:87]\n" \
"v_mul_f32 v199, s20, v199\n" \
"v_cvt_pk_bf16_f32 v32, v196, v197\n" \
"v_mfma_f32_16x16x32_bf16 v[84:87], a[76:79], a[208:211], v[84:87]\n" \
"ds_read2_b32 a[16:17], v48 offset0:48 offset1:49\n" \
"v_cvt_pk_bf16_f32 v33, v198, v199\n" \
"v_mfma_f32_16x16x32_bf16 v[84:87], a[80:83], a[204:207], v[84:87]\n" \
"ds_read2_b32 a[18:19], v48 offset0:50 offset1:51\n" \
"v_mul_f32 v200, s20, v200\n" \
"v_mfma_f32_16x16x32_bf16 v[84:87], a[84:87], a[200:203], v[84:87]\n" \
"ds_read_b128 a[52:55], v50 offset:2880\n" \
"v_mul_f32 v201, s20, v201\n" \
"v_mfma_f32_16x16x32_bf16 v[88:91], a[64:67], a[224:227], v[88:91]\n" \
"buffer_store_dwordx4 v[34:37], v4, s[36:39], s97 offen offset:0 sc1\n" \
"v_mfma_f32_16x16x32_bf16 v[88:91], a[68:71], a[220:223], v[88:91]\n" \
"s_add_u32 s96, s96, s92\n" \
"ds_read2_b32 a[20:21], v48 offset0:176 offset1:177\n" \
"v_mul_f32 v202, s20, v202\n" \
"v_mfma_f32_16x16x32_bf16 v[88:91], a[72:75], a[216:219], v[88:91]\n" \
"ds_read2_b32 a[22:23], v48 offset0:178 offset1:179\n" \
"v_mul_f32 v203, s20, v203\n" \
"v_mfma_f32_16x16x32_bf16 v[88:91], a[76:79], a[212:215], v[88:91]\n" \
"ds_read_b128 a[56:59], v50 offset:3392\n" \
"v_cvt_pk_bf16_f32 v34, v200, v201\n" \
"v_mfma_f32_16x16x32_bf16 v[88:91], a[80:83], a[208:211], v[88:91]\n" \
"v_cvt_pk_bf16_f32 v35, v202, v203\n" \
"v_mul_f32 v204, s20, v204\n" \
"v_mfma_f32_16x16x32_bf16 v[88:91], a[84:87], a[204:207], v[88:91]\n" \
"ds_read2_b32 a[24:25], v49 offset0:48 offset1:49\n" \
"v_mul_f32 v205, s20, v205\n" \
"v_mfma_f32_16x16x32_bf16 v[88:91], a[88:91], a[200:203], v[88:91]\n" \
"ds_read2_b32 a[26:27], v49 offset0:50 offset1:51\n" \
"v_mul_f32 v206, s20, v206\n" \
"v_mfma_f32_16x16x32_bf16 v[92:95], a[64:67], a[228:231], v[92:95]\n" \
"ds_read_b128 a[60:63], v50 offset:3904\n" \
"v_mul_f32 v207, s20, v207\n" \
"v_mfma_f32_16x16x32_bf16 v[92:95], a[68:71], a[224:227], v[92:95]\n" \
"v_cvt_pk_bf16_f32 v36, v204, v205\n" \
"v_cvt_pk_bf16_f32 v37, v206, v207\n" \
"v_mfma_f32_16x16x32_bf16 v[92:95], a[72:75], a[220:223], v[92:95]\n" \
"ds_read2_b32 a[28:29], v49 offset0:176 offset1:177\n" \
"v_mul_f32 v208, s20, v208\n" \
"v_mfma_f32_16x16x32_bf16 v[92:95], a[76:79], a[216:219], v[92:95]\n" \
"ds_read2_b32 a[30:31], v49 offset0:178 offset1:179\n" \
"v_mul_f32 v209, s20, v209\n" \
"v_mfma_f32_16x16x32_bf16 v[92:95], a[80:83], a[212:215], v[92:95]\n" \
"buffer_store_dwordx4 v[38:41], v4, s[36:39], s96 offen offset:0 sc1\n" \
"v_mfma_f32_16x16x32_bf16 v[92:95], a[84:87], a[208:211], v[92:95]\n" \
"s_add_u32 s97, s96, s98\n" \
"v_mul_f32 v210, s20, v210\n" \
"v_mul_f32 v211, s20, v211\n" \
"v_mfma_f32_16x16x32_bf16 v[92:95], a[88:91], a[204:207], v[92:95]\n" \
"v_cvt_pk_bf16_f32 v38, v208, v209\n" \
"v_cvt_pk_bf16_f32 v39, v210, v211\n" \
"v_mfma_f32_16x16x32_bf16 v[92:95], a[92:95], a[200:203], v[92:95]\n" \
"s_waitcnt lgkmcnt(0)\n" \
"v_mul_f32 v212, s20, v212\n" \
"v_mfma_f32_16x16x32_bf16 v[64:67], a[0:3], a[32:35], v[64:67]\n" \
"ds_read_b128 a[200:203], v50 offset:256\n" \
"v_mul_f32 v213, s20, v213\n" \
"v_mfma_f32_16x16x32_bf16 v[68:71], a[0:3], a[36:39], v[68:71]\n" \
"v_mul_f32 v214, s20, v214\n" \
"v_mul_f32 v215, s20, v215\n" \
"v_mfma_f32_16x16x32_bf16 v[68:71], a[4:7], a[32:35], v[68:71]\n" \
"ds_read2_b32 a[64:65], v46 offset0:64 offset1:65\n" \
"v_cvt_pk_bf16_f32 v40, v212, v213\n" \
"v_mfma_f32_16x16x32_bf16 v[72:75], a[0:3], a[40:43], v[72:75]\n" \
"ds_read2_b32 a[66:67], v46 offset0:66 offset1:67\n" \
"v_cvt_pk_bf16_f32 v41, v214, v215\n" \
"v_mfma_f32_16x16x32_bf16 v[72:75], a[4:7], a[36:39], v[72:75]\n" \
"ds_read_b128 a[204:207], v50 offset:768\n" \
"v_mul_f32 v216, s20, v216\n" \
"v_mfma_f32_16x16x32_bf16 v[72:75], a[8:11], a[32:35], v[72:75]\n" \
"v_mul_f32 v217, s20, v217\n" \
"v_mul_f32 v218, s20, v218\n" \
"v_mfma_f32_16x16x32_bf16 v[76:79], a[0:3], a[44:47], v[76:79]\n" \
"ds_read2_b32 a[68:69], v46 offset0:192 offset1:193\n" \
"v_mul_f32 v219, s20, v219\n" \
"v_mfma_f32_16x16x32_bf16 v[76:79], a[4:7], a[40:43], v[76:79]\n" \
"ds_read2_b32 a[70:71], v46 offset0:194 offset1:195\n" \
"v_mfma_f32_16x16x32_bf16 v[76:79], a[8:11], a[36:39], v[76:79]\n" \
"ds_read_b128 a[208:211], v50 offset:1280\n" \
"v_mfma_f32_16x16x32_bf16 v[76:79], a[12:15], a[32:35], v[76:79]\n" \
"buffer_store_dwordx4 v[42:45], v4, s[36:39], s97 offen offset:0 sc1\n" \
"v_mfma_f32_16x16x32_bf16 v[80:83], a[0:3], a[48:51], v[80:83]\n" \
"s_add_u32 s57, s57, s93\n" \
"ds_read2_b32 a[72:73], v47 offset0:64 offset1:65\n" \
"s_mov_b32 s96, s57\n" \
"v_mfma_f32_16x16x32_bf16 v[80:83], a[4:7], a[44:47], v[80:83]\n" \
"ds_read2_b32 a[74:75], v47 offset0:66 offset1:67\n" \
"v_cvt_pk_bf16_f32 v42, v216, v217\n" \
"v_mfma_f32_16x16x32_bf16 v[80:83], a[8:11], a[40:43], v[80:83]\n" \
"ds_read_b128 a[212:215], v50 offset:1792\n" \
"v_cvt_pk_bf16_f32 v43, v218, v219\n" \
"v_mfma_f32_16x16x32_bf16 v[80:83], a[12:15], a[36:39], v[80:83]\n" \
"v_mul_f32 v220, s20, v220\n" \
"v_mul_f32 v221, s20, v221\n" \
"v_mfma_f32_16x16x32_bf16 v[80:83], a[16:19], a[32:35], v[80:83]\n" \
"ds_read2_b32 a[76:77], v47 offset0:192 offset1:193\n" \
"v_mul_f32 v222, s20, v222\n" \
"v_mfma_f32_16x16x32_bf16 v[84:87], a[0:3], a[52:55], v[84:87]\n" \
"ds_read2_b32 a[78:79], v47 offset0:194 offset1:195\n" \
"v_mul_f32 v223, s20, v223\n" \
"v_mfma_f32_16x16x32_bf16 v[84:87], a[4:7], a[48:51], v[84:87]\n" \
"ds_read_b128 a[216:219], v50 offset:2304\n" \
"v_cvt_pk_bf16_f32 v44, v220, v221\n" \
"v_mfma_f32_16x16x32_bf16 v[84:87], a[8:11], a[44:47], v[84:87]\n" \
"v_cvt_pk_bf16_f32 v45, v222, v223\n" \
"v_mul_f32 v224, s20, v224\n" \
"v_mfma_f32_16x16x32_bf16 v[84:87], a[12:15], a[40:43], v[84:87]\n" \
"ds_read2_b32 a[80:81], v48 offset0:64 offset1:65\n" \
"v_mul_f32 v225, s20, v225\n" \
"v_mfma_f32_16x16x32_bf16 v[84:87], a[16:19], a[36:39], v[84:87]\n" \
"ds_read2_b32 a[82:83], v48 offset0:66 offset1:67\n" \
"v_mul_f32 v226, s20, v226\n" \
"v_mfma_f32_16x16x32_bf16 v[84:87], a[20:23], a[32:35], v[84:87]\n" \
"ds_read_b128 a[220:223], v50 offset:2816\n" \
"v_mul_f32 v227, s20, v227\n" \
"v_mfma_f32_16x16x32_bf16 v[88:91], a[0:3], a[56:59], v[88:91]\n" \
"buffer_store_dwordx4 v[30:33], v4, s[36:39], s96 offen offset:0 sc1\n" \
"v_mfma_f32_16x16x32_bf16 v[88:91], a[4:7], a[52:55], v[88:91]\n" \
"s_add_u32 s97, s96, s98\n" \
"ds_read2_b32 a[84:85], v48 offset0:192 offset1:193\n" \
"v_cvt_pk_bf16_f32 v30, v224, v225\n" \
"v_mfma_f32_16x16x32_bf16 v[88:91], a[8:11], a[48:51], v[88:91]\n" \
"ds_read2_b32 a[86:87], v48 offset0:194 offset1:195\n" \
"v_cvt_pk_bf16_f32 v31, v226, v227\n" \
"v_mfma_f32_16x16x32_bf16 v[88:91], a[12:15], a[44:47], v[88:91]\n" \
"ds_read_b128 a[224:227], v50 offset:3328\n" \
"v_mul_f32 v228, s20, v228\n" \
"v_mfma_f32_16x16x32_bf16 v[88:91], a[16:19], a[40:43], v[88:91]\n" \
"v_mul_f32 v229, s20, v229\n" \
"v_mul_f32 v230, s20, v230\n" \
"v_mfma_f32_16x16x32_bf16 v[88:91], a[20:23], a[36:39], v[88:91]\n" \
"ds_read2_b32 a[88:89], v49 offset0:64 offset1:65\n" \
"v_mul_f32 v231, s20, v231\n" \
"v_mfma_f32_16x16x32_bf16 v[88:91], a[24:27], a[32:35], v[88:91]\n" \
"ds_read2_b32 a[90:91], v49 offset0:66 offset1:67\n" \
"v_cvt_pk_bf16_f32 v32, v228, v229\n" \
"v_mfma_f32_16x16x32_bf16 v[92:95], a[0:3], a[60:63], v[92:95]\n" \
"ds_read_b128 a[228:231], v50 offset:3840\n" \
"v_cvt_pk_bf16_f32 v33, v230, v231\n" \
"v_mfma_f32_16x16x32_bf16 v[92:95], a[4:7], a[56:59], v[92:95]\n" \
"v_mul_f32 v232, s20, v232\n" \
"v_mul_f32 v233, s20, v233\n" \
"v_mfma_f32_16x16x32_bf16 v[92:95], a[8:11], a[52:55], v[92:95]\n" \
"ds_read2_b32 a[92:93], v49 offset0:192 offset1:193\n" \
"v_mul_f32 v234, s20, v234\n" \
"v_mfma_f32_16x16x32_bf16 v[92:95], a[12:15], a[48:51], v[92:95]\n" \
"ds_read2_b32 a[94:95], v49 offset0:194 offset1:195\n" \
"v_mul_f32 v235, s20, v235\n" \
"v_mfma_f32_16x16x32_bf16 v[92:95], a[16:19], a[44:47], v[92:95]\n" \
"buffer_store_dwordx4 v[34:37], v4, s[36:39], s97 offen offset:0 sc1\n" \
"v_mfma_f32_16x16x32_bf16 v[92:95], a[20:23], a[40:43], v[92:95]\n" \
"s_add_u32 s96, s96, s92\n" \
"v_cvt_pk_bf16_f32 v34, v232, v233\n" \
"v_cvt_pk_bf16_f32 v35, v234, v235\n" \
"v_mfma_f32_16x16x32_bf16 v[92:95], a[24:27], a[36:39], v[92:95]\n" \
"v_mul_f32 v236, s20, v236\n" \
"v_mul_f32 v237, s20, v237\n" \
"v_mfma_f32_16x16x32_bf16 v[92:95], a[28:31], a[32:35], v[92:95]\n" \
"s_waitcnt lgkmcnt(0)\n" \
"v_mul_f32 v238, s20, v238\n" \
"v_mfma_f32_16x16x32_bf16 v[64:67], a[64:67], a[200:203], v[64:67]\n" \
"ds_read_b128 a[32:35], v50 offset:192\n" \
"v_mul_f32 v239, s20, v239\n" \
"v_mfma_f32_16x16x32_bf16 v[68:71], a[64:67], a[204:207], v[68:71]\n" \
"v_cvt_pk_bf16_f32 v36, v236, v237\n" \
"v_cvt_pk_bf16_f32 v37, v238, v239\n" \
"v_mfma_f32_16x16x32_bf16 v[68:71], a[68:71], a[200:203], v[68:71]\n" \
"ds_read2_b32 a[0:1], v46 offset0:80 offset1:81\n" \
"v_mul_f32 v240, s20, v240\n" \
"v_mfma_f32_16x16x32_bf16 v[72:75], a[64:67], a[208:211], v[72:75]\n" \
"ds_read2_b32 a[2:3], v46 offset0:82 offset1:83\n" \
"v_mul_f32 v241, s20, v241\n" \
"v_mfma_f32_16x16x32_bf16 v[72:75], a[68:71], a[204:207], v[72:75]\n" \
"ds_read_b128 a[36:39], v50 offset:704\n" \
"v_mul_f32 v242, s20, v242\n" \
"v_mfma_f32_16x16x32_bf16 v[72:75], a[72:75], a[200:203], v[72:75]\n" \
"v_mul_f32 v243, s20, v243\n" \
"v_mfma_f32_16x16x32_bf16 v[76:79], a[64:67], a[212:215], v[76:79]\n" \
"ds_read2_b32 a[4:5], v46 offset0:208 offset1:209\n" \
"v_mfma_f32_16x16x32_bf16 v[76:79], a[68:71], a[208:211], v[76:79]\n" \
"ds_read2_b32 a[6:7], v46 offset0:210 offset1:211\n" \
"v_mfma_f32_16x16x32_bf16 v[76:79], a[72:75], a[204:207], v[76:79]\n" \
"ds_read_b128 a[40:43], v50 offset:1216\n" \
"v_mfma_f32_16x16x32_bf16 v[76:79], a[76:79], a[200:203], v[76:79]\n" \
"buffer_store_dwordx4 v[38:41], v4, s[36:39], s96 offen offset:0 sc1\n" \
"v_mfma_f32_16x16x32_bf16 v[80:83], a[64:67], a[216:219], v[80:83]\n" \
"s_add_u32 s97, s96, s98\n" \
"ds_read2_b32 a[8:9], v47 offset0:80 offset1:81\n" \
"v_cvt_pk_bf16_f32 v38, v240, v241\n" \
"v_mfma_f32_16x16x32_bf16 v[80:83], a[68:71], a[212:215], v[80:83]\n" \
"ds_read2_b32 a[10:11], v47 offset0:82 offset1:83\n" \
"v_cvt_pk_bf16_f32 v39, v242, v243\n" \
"v_mfma_f32_16x16x32_bf16 v[80:83], a[72:75], a[208:211], v[80:83]\n" \
"ds_read_b128 a[44:47], v50 offset:1728\n" \
"v_mul_f32 v244, s20, v244\n" \
"v_mfma_f32_16x16x32_bf16 v[80:83], a[76:79], a[204:207], v[80:83]\n" \
"v_mul_f32 v245, s20, v245\n" \
"v_mul_f32 v246, s20, v246\n" \
"v_mfma_f32_16x16x32_bf16 v[80:83], a[80:83], a[200:203], v[80:83]\n" \
"ds_read2_b32 a[12:13], v47 offset0:208 offset1:209\n" \
"v_mul_f32 v247, s20, v247\n" \
"v_mfma_f32_16x16x32_bf16 v[84:87], a[64:67], a[220:223], v[84:87]\n" \
"ds_read2_b32 a[14:15], v47 offset0:210 offset1:211\n" \
"v_cvt_pk_bf16_f32 v40, v244, v245\n" \
"v_mfma_f32_16x16x32_bf16 v[84:87], a[68:71], a[216:219], v[84:87]\n" \
"ds_read_b128 a[48:51], v50 offset:2240\n" \
"v_cvt_pk_bf16_f32 v41, v246, v247\n" \
"v_mfma_f32_16x16x32_bf16 v[84:87], a[72:75], a[212:215], v[84:87]\n" \
"v_mul_f32 v248, s20, v248\n" \
"v_mul_f32 v249, s20, v249\n" \
"v_mfma_f32_16x16x32_bf16 v[84:87], a[76:79], a[208:211], v[84:87]\n" \
"ds_read2_b32 a[16:17], v48 offset0:80 offset1:81\n" \
"v_mul_f32 v250, s20, v250\n" \
"v_mfma_f32_16x16x32_bf16 v[84:87], a[80:83], a[204:207], v[84:87]\n" \
"ds_read2_b32 a[18:19], v48 offset0:82 offset1:83\n" \
"v_mul_f32 v251, s20, v251\n" \
"v_mfma_f32_16x16x32_bf16 v[84:87], a[84:87], a[200:203], v[84:87]\n" \
"ds_read_b128 a[52:55], v50 offset:2752\n" \
"v_mfma_f32_16x16x32_bf16 v[88:91], a[64:67], a[224:227], v[88:91]\n" \
"buffer_store_dwordx4 v[42:45], v4, s[36:39], s97 offen offset:0 sc1\n" \
"v_mfma_f32_16x16x32_bf16 v[88:91], a[68:71], a[220:223], v[88:91]\n" \
"s_add_u32 s96, s96, s92\n" \
"ds_read2_b32 a[20:21], v48 offset0:208 offset1:209\n" \
"v_cvt_pk_bf16_f32 v42, v248, v249\n" \
"v_mfma_f32_16x16x32_bf16 v[88:91], a[72:75], a[216:219], v[88:91]\n" \
"ds_read2_b32 a[22:23], v48 offset0:210 offset1:211\n" \
"v_cvt_pk_bf16_f32 v43, v250, v251\n" \
"v_mfma_f32_16x16x32_bf16 v[88:91], a[76:79], a[212:215], v[88:91]\n" \
"ds_read_b128 a[56:59], v50 offset:3264\n" \
"v_mul_f32 v252, s20, v252\n" \
"v_mfma_f32_16x16x32_bf16 v[88:91], a[80:83], a[208:211], v[88:91]\n" \
"v_mul_f32 v253, s20, v253\n" \
"v_mul_f32 v254, s20, v254\n" \
"v_mfma_f32_16x16x32_bf16 v[88:91], a[84:87], a[204:207], v[88:91]\n" \
"ds_read2_b32 a[24:25], v49 offset0:80 offset1:81\n" \
"v_mul_f32 v255, s20, v255\n" \
"v_mfma_f32_16x16x32_bf16 v[88:91], a[88:91], a[200:203], v[88:91]\n" \
"ds_read2_b32 a[26:27], v49 offset0:82 offset1:83\n" \
"v_cvt_pk_bf16_f32 v44, v252, v253\n" \
"v_mfma_f32_16x16x32_bf16 v[92:95], a[64:67], a[228:231], v[92:95]\n" \
"ds_read_b128 a[60:63], v50 offset:3776\n" \
"v_cvt_pk_bf16_f32 v45, v254, v255\n" \
"v_mfma_f32_16x16x32_bf16 v[92:95], a[68:71], a[224:227], v[92:95]\n" \
"v_accvgpr_read_b32 v248, a128\n" \
"v_accvgpr_read_b32 v249, a129\n" \
"v_mfma_f32_16x16x32_bf16 v[92:95], a[72:75], a[220:223], v[92:95]\n" \
"ds_read2_b32 a[28:29], v49 offset0:208 offset1:209\n" \
"v_accvgpr_read_b32 v250, a130\n" \
"v_mfma_f32_16x16x32_bf16 v[92:95], a[76:79], a[216:219], v[92:95]\n" \
"ds_read2_b32 a[30:31], v49 offset0:210 offset1:211\n" \
"v_accvgpr_read_b32 v251, a131\n" \
"v_mfma_f32_16x16x32_bf16 v[92:95], a[80:83], a[212:215], v[92:95]\n" \
"buffer_store_dwordx4 v[30:33], v4, s[36:39], s96 offen offset:0 sc1\n" \
"v_mfma_f32_16x16x32_bf16 v[92:95], a[84:87], a[208:211], v[92:95]\n" \
"s_add_u32 s97, s96, s98\n" \
"v_mul_f32 v248, s20, v248\n" \
"v_mul_f32 v249, s20, v249\n" \
"v_mfma_f32_16x16x32_bf16 v[92:95], a[88:91], a[204:207], v[92:95]\n" \
"v_mul_f32 v250, s20, v250\n" \
"v_mul_f32 v251, s20, v251\n" \
"v_mfma_f32_16x16x32_bf16 v[92:95], a[92:95], a[200:203], v[92:95]\n" \
"s_waitcnt lgkmcnt(0)\n" \
"v_cvt_pk_bf16_f32 v30, v248, v249\n" \
"v_mfma_f32_16x16x32_bf16 v[64:67], a[0:3], a[32:35], v[64:67]\n" \
"ds_read_b128 a[200:203], v50 offset:128\n" \
"v_cvt_pk_bf16_f32 v31, v250, v251\n" \
"v_mfma_f32_16x16x32_bf16 v[68:71], a[0:3], a[36:39], v[68:71]\n" \
"v_accvgpr_read_b32 v252, a132\n" \
"v_accvgpr_read_b32 v253, a133\n" \
"v_mfma_f32_16x16x32_bf16 v[68:71], a[4:7], a[32:35], v[68:71]\n" \
"ds_read2_b32 a[64:65], v46 offset0:96 offset1:97\n" \
"v_accvgpr_read_b32 v254, a134\n" \
"v_mfma_f32_16x16x32_bf16 v[72:75], a[0:3], a[40:43], v[72:75]\n" \
"ds_read2_b32 a[66:67], v46 offset0:98 offset1:99\n" \
"v_accvgpr_read_b32 v255, a135\n" \
"v_mfma_f32_16x16x32_bf16 v[72:75], a[4:7], a[36:39], v[72:75]\n" \
"ds_read_b128 a[204:207], v50 offset:640\n" \
"v_mul_f32 v252, s20, v252\n" \
"v_mfma_f32_16x16x32_bf16 v[72:75], a[8:11], a[32:35], v[72:75]\n" \
"v_mul_f32 v253, s20, v253\n" \
"v_mul_f32 v254, s20, v254\n" \
"v_mfma_f32_16x16x32_bf16 v[76:79], a[0:3], a[44:47], v[76:79]\n" \
"ds_read2_b32 a[68:69], v46 offset0:224 offset1:225\n" \
"v_mul_f32 v255, s20, v255\n" \
"v_mfma_f32_16x16x32_bf16 v[76:79], a[4:7], a[40:43], v[76:79]\n" \
"ds_read2_b32 a[70:71], v46 offset0:226 offset1:227\n" \
"v_cvt_pk_bf16_f32 v32, v252, v253\n" \
"v_mfma_f32_16x16x32_bf16 v[76:79], a[8:11], a[36:39], v[76:79]\n" \
"ds_read_b128 a[208:211], v50 offset:1152\n" \
"v_cvt_pk_bf16_f32 v33, v254, v255\n" \
"v_mfma_f32_16x16x32_bf16 v[76:79], a[12:15], a[32:35], v[76:79]\n" \
"buffer_store_dwordx4 v[34:37], v4, s[36:39], s97 offen offset:0 sc1\n" \
"v_mfma_f32_16x16x32_bf16 v[80:83], a[0:3], a[48:51], v[80:83]\n" \
"s_add_u32 s96, s96, s92\n" \
"ds_read2_b32 a[72:73], v47 offset0:96 offset1:97\n" \
"v_accvgpr_read_b32 v248, a136\n" \
"v_mfma_f32_16x16x32_bf16 v[80:83], a[4:7], a[44:47], v[80:83]\n" \
"ds_read2_b32 a[74:75], v47 offset0:98 offset1:99\n" \
"v_accvgpr_read_b32 v249, a137\n" \
"v_mfma_f32_16x16x32_bf16 v[80:83], a[8:11], a[40:43], v[80:83]\n" \
"ds_read_b128 a[212:215], v50 offset:1664\n" \
"v_accvgpr_read_b32 v250, a138\n" \
"v_mfma_f32_16x16x32_bf16 v[80:83], a[12:15], a[36:39], v[80:83]\n" \
"v_accvgpr_read_b32 v251, a139\n" \
"v_mul_f32 v248, s20, v248\n" \
"v_mfma_f32_16x16x32_bf16 v[80:83], a[16:19], a[32:35], v[80:83]\n" \
"ds_read2_b32 a[76:77], v47 offset0:224 offset1:225\n" \
"v_mul_f32 v249, s20, v249\n" \
"v_mfma_f32_16x16x32_bf16 v[84:87], a[0:3], a[52:55], v[84:87]\n" \
"ds_read2_b32 a[78:79], v47 offset0:226 offset1:227\n" \
"v_mul_f32 v250, s20, v250\n" \
"v_mfma_f32_16x16x32_bf16 v[84:87], a[4:7], a[48:51], v[84:87]\n" \
"ds_read_b128 a[216:219], v50 offset:2176\n" \
"v_mul_f32 v251, s20, v251\n" \
"v_mfma_f32_16x16x32_bf16 v[84:87], a[8:11], a[44:47], v[84:87]\n" \
"v_cvt_pk_bf16_f32 v34, v248, v249\n" \
"v_cvt_pk_bf16_f32 v35, v250, v251\n" \
"v_mfma_f32_16x16x32_bf16 v[84:87], a[12:15], a[40:43], v[84:87]\n" \
"ds_read2_b32 a[80:81], v48 offset0:96 offset1:97\n" \
"v_accvgpr_read_b32 v252, a140\n" \
"v_mfma_f32_16x16x32_bf16 v[84:87], a[16:19], a[36:39], v[84:87]\n" \
"ds_read2_b32 a[82:83], v48 offset0:98 offset1:99\n" \
"v_accvgpr_read_b32 v253, a141\n" \
"v_mfma_f32_16x16x32_bf16 v[84:87], a[20:23], a[32:35], v[84:87]\n" \
"ds_read_b128 a[220:223], v50 offset:2688\n" \
"v_accvgpr_read_b32 v254, a142\n" \
"v_mfma_f32_16x16x32_bf16 v[88:91], a[0:3], a[56:59], v[88:91]\n" \
"buffer_store_dwordx4 v[38:41], v4, s[36:39], s96 offen offset:0 sc1\n" \
"v_mfma_f32_16x16x32_bf16 v[88:91], a[4:7], a[52:55], v[88:91]\n" \
"s_add_u32 s97, s96, s98\n" \
"ds_read2_b32 a[84:85], v48 offset0:224 offset1:225\n" \
"v_accvgpr_read_b32 v255, a143\n" \
"v_mfma_f32_16x16x32_bf16 v[88:91], a[8:11], a[48:51], v[88:91]\n" \
"ds_read2_b32 a[86:87], v48 offset0:226 offset1:227\n" \
"v_mul_f32 v252, s20, v252\n" \
"v_mfma_f32_16x16x32_bf16 v[88:91], a[12:15], a[44:47], v[88:91]\n" \
"ds_read_b128 a[224:227], v50 offset:3200\n" \
"v_mul_f32 v253, s20, v253\n" \
"v_mfma_f32_16x16x32_bf16 v[88:91], a[16:19], a[40:43], v[88:91]\n" \
"v_mul_f32 v254, s20, v254\n" \
"v_mul_f32 v255, s20, v255\n" \
"v_mfma_f32_16x16x32_bf16 v[88:91], a[20:23], a[36:39], v[88:91]\n" \
"ds_read2_b32 a[88:89], v49 offset0:96 offset1:97\n" \
"v_cvt_pk_bf16_f32 v36, v252, v253\n" \
"v_mfma_f32_16x16x32_bf16 v[88:91], a[24:27], a[32:35], v[88:91]\n" \
"ds_read2_b32 a[90:91], v49 offset0:98 offset1:99\n" \
"v_cvt_pk_bf16_f32 v37, v254, v255\n" \
"v_mfma_f32_16x16x32_bf16 v[92:95], a[0:3], a[60:63], v[92:95]\n" \
"ds_read_b128 a[228:231], v50 offset:3712\n" \
"v_accvgpr_read_b32 v248, a144\n" \
"v_mfma_f32_16x16x32_bf16 v[92:95], a[4:7], a[56:59], v[92:95]\n" \
"v_accvgpr_read_b32 v249, a145\n" \
"v_accvgpr_read_b32 v250, a146\n" \
"v_mfma_f32_16x16x32_bf16 v[92:95], a[8:11], a[52:55], v[92:95]\n" \
"ds_read2_b32 a[92:93], v49 offset0:224 offset1:225\n" \
"v_accvgpr_read_b32 v251, a147\n" \
"v_mfma_f32_16x16x32_bf16 v[92:95], a[12:15], a[48:51], v[92:95]\n" \
"ds_read2_b32 a[94:95], v49 offset0:226 offset1:227\n" \
"v_mul_f32 v248, s20, v248\n" \
"v_mfma_f32_16x16x32_bf16 v[92:95], a[16:19], a[44:47], v[92:95]\n" \
"buffer_store_dwordx4 v[42:45], v4, s[36:39], s97 offen offset:0 sc1\n" \
"v_mfma_f32_16x16x32_bf16 v[92:95], a[20:23], a[40:43], v[92:95]\n" \
"s_add_u32 s57, s57, s93\n" \
"s_mov_b32 s96, s57\n" \
"v_mul_f32 v249, s20, v249\n" \
"v_mfma_f32_16x16x32_bf16 v[92:95], a[24:27], a[36:39], v[92:95]\n" \
"v_mul_f32 v250, s20, v250\n" \
"v_mul_f32 v251, s20, v251\n" \
"v_mfma_f32_16x16x32_bf16 v[92:95], a[28:31], a[32:35], v[92:95]\n" \
"s_waitcnt lgkmcnt(0)\n" \
"v_cvt_pk_bf16_f32 v38, v248, v249\n" \
"v_cvt_pk_bf16_f32 v39, v250, v251\n" \
"v_mfma_f32_16x16x32_bf16 v[64:67], a[64:67], a[200:203], v[64:67]\n" \
"ds_read_b128 a[32:35], v50 offset:64\n" \
"v_accvgpr_read_b32 v252, a148\n" \
"v_mfma_f32_16x16x32_bf16 v[68:71], a[64:67], a[204:207], v[68:71]\n" \
"v_accvgpr_read_b32 v253, a149\n" \
"v_accvgpr_read_b32 v254, a150\n" \
"v_mfma_f32_16x16x32_bf16 v[68:71], a[68:71], a[200:203], v[68:71]\n" \
"ds_read2_b32 a[0:1], v46 offset0:112 offset1:113\n" \
"v_accvgpr_read_b32 v255, a151\n" \
"v_mfma_f32_16x16x32_bf16 v[72:75], a[64:67], a[208:211], v[72:75]\n" \
"ds_read2_b32 a[2:3], v46 offset0:114 offset1:115\n" \
"v_mul_f32 v252, s20, v252\n" \
"v_mfma_f32_16x16x32_bf16 v[72:75], a[68:71], a[204:207], v[72:75]\n" \
"ds_read_b128 a[36:39], v50 offset:576\n" \
"v_mul_f32 v253, s20, v253\n" \
"v_mfma_f32_16x16x32_bf16 v[72:75], a[72:75], a[200:203], v[72:75]\n" \
"v_mul_f32 v254, s20, v254\n" \
"v_mul_f32 v255, s20, v255\n" \
"v_mfma_f32_16x16x32_bf16 v[76:79], a[64:67], a[212:215], v[76:79]\n" \
"ds_read2_b32 a[4:5], v46 offset0:240 offset1:241\n" \
"v_cvt_pk_bf16_f32 v40, v252, v253\n" \
"v_mfma_f32_16x16x32_bf16 v[76:79], a[68:71], a[208:211], v[76:79]\n" \
"ds_read2_b32 a[6:7], v46 offset0:242 offset1:243\n" \
"v_cvt_pk_bf16_f32 v41, v254, v255\n" \
"v_mfma_f32_16x16x32_bf16 v[76:79], a[72:75], a[204:207], v[76:79]\n" \
"ds_read_b128 a[40:43], v50 offset:1088\n" \
"v_accvgpr_read_b32 v248, a152\n" \
"v_mfma_f32_16x16x32_bf16 v[76:79], a[76:79], a[200:203], v[76:79]\n" \
"buffer_store_dwordx4 v[30:33], v4, s[36:39], s96 offen offset:0 sc1\n" \
"v_mfma_f32_16x16x32_bf16 v[80:83], a[64:67], a[216:219], v[80:83]\n" \
"s_add_u32 s97, s96, s98\n" \
"ds_read2_b32 a[8:9], v47 offset0:112 offset1:113\n" \
"v_accvgpr_read_b32 v249, a153\n" \
"v_mfma_f32_16x16x32_bf16 v[80:83], a[68:71], a[212:215], v[80:83]\n" \
"ds_read2_b32 a[10:11], v47 offset0:114 offset1:115\n" \
"v_accvgpr_read_b32 v250, a154\n" \
"v_mfma_f32_16x16x32_bf16 v[80:83], a[72:75], a[208:211], v[80:83]\n" \
"ds_read_b128 a[44:47], v50 offset:1600\n" \
"v_accvgpr_read_b32 v251, a155\n" \
"v_mfma_f32_16x16x32_bf16 v[80:83], a[76:79], a[204:207], v[80:83]\n" \
"v_mul_f32 v248, s20, v248\n" \
"v_mul_f32 v249, s20, v249\n" \
"v_mfma_f32_16x16x32_bf16 v[80:83], a[80:83], a[200:203], v[80:83]\n" \
"ds_read2_b32 a[12:13], v47 offset0:240 offset1:241\n" \
"v_mul_f32 v250, s20, v250\n" \
"v_mfma_f32_16x16x32_bf16 v[84:87], a[64:67], a[220:223], v[84:87]\n" \
"ds_read2_b32 a[14:15], v47 offset0:242 offset1:243\n" \
"v_mul_f32 v251, s20, v251\n" \
"v_mfma_f32_16x16x32_bf16 v[84:87], a[68:71], a[216:219], v[84:87]\n" \
"ds_read_b128 a[48:51], v50 offset:2112\n" \
"v_cvt_pk_bf16_f32 v42, v248, v249\n" \
"v_mfma_f32_16x16x32_bf16 v[84:87], a[72:75], a[212:215], v[84:87]\n" \
"v_cvt_pk_bf16_f32 v43, v250, v251\n" \
"v_accvgpr_read_b32 v252, a156\n" \
"v_mfma_f32_16x16x32_bf16 v[84:87], a[76:79], a[208:211], v[84:87]\n" \
"ds_read2_b32 a[16:17], v48 offset0:112 offset1:113\n" \
"v_accvgpr_read_b32 v253, a157\n" \
"v_mfma_f32_16x16x32_bf16 v[84:87], a[80:83], a[204:207], v[84:87]\n" \
"ds_read2_b32 a[18:19], v48 offset0:114 offset1:115\n" \
"v_accvgpr_read_b32 v254, a158\n" \
"v_mfma_f32_16x16x32_bf16 v[84:87], a[84:87], a[200:203], v[84:87]\n" \
"ds_read_b128 a[52:55], v50 offset:2624\n" \
"v_accvgpr_read_b32 v255, a159\n" \
"v_mfma_f32_16x16x32_bf16 v[88:91], a[64:67], a[224:227], v[88:91]\n" \
"buffer_store_dwordx4 v[34:37], v4, s[36:39], s97 offen offset:0 sc1\n" \
"v_mfma_f32_16x16x32_bf16 v[88:91], a[68:71], a[220:223], v[88:91]\n" \
"s_add_u32 s96, s96, s92\n" \
"ds_read2_b32 a[20:21], v48 offset0:240 offset1:241\n" \
"v_mul_f32 v252, s20, v252\n" \
"v_mfma_f32_16x16x32_bf16 v[88:91], a[72:75], a[216:219], v[88:91]\n" \
"ds_read2_b32 a[22:23], v48 offset0:242 offset1:243\n" \
"v_mul_f32 v253, s20, v253\n" \
"v_mfma_f32_16x16x32_bf16 v[88:91], a[76:79], a[212:215], v[88:91]\n" \
"ds_read_b128 a[56:59], v50 offset:3136\n" \
"v_mul_f32 v254, s20, v254\n" \
"v_mfma_f32_16x16x32_bf16 v[88:91], a[80:83], a[208:211], v[88:91]\n" \
"v_mul_f32 v255, s20, v255\n" \
"v_cvt_pk_bf16_f32 v44, v252, v253\n" \
"v_mfma_f32_16x16x32_bf16 v[88:91], a[84:87], a[204:207], v[88:91]\n" \
"ds_read2_b32 a[24:25], v49 offset0:112 offset1:113\n" \
"v_cvt_pk_bf16_f32 v45, v254, v255\n" \
"v_mfma_f32_16x16x32_bf16 v[88:91], a[88:91], a[200:203], v[88:91]\n" \
"ds_read2_b32 a[26:27], v49 offset0:114 offset1:115\n" \
"v_accvgpr_read_b32 v248, a160\n" \
"v_mfma_f32_16x16x32_bf16 v[92:95], a[64:67], a[228:231], v[92:95]\n" \
"ds_read_b128 a[60:63], v50 offset:3648\n" \
"v_accvgpr_read_b32 v249, a161\n" \
"v_mfma_f32_16x16x32_bf16 v[92:95], a[68:71], a[224:227], v[92:95]\n" \
"v_accvgpr_read_b32 v250, a162\n" \
"v_accvgpr_read_b32 v251, a163\n" \
"v_mfma_f32_16x16x32_bf16 v[92:95], a[72:75], a[220:223], v[92:95]\n" \
"ds_read2_b32 a[28:29], v49 offset0:240 offset1:241\n" \
"v_mul_f32 v248, s20, v248\n" \
"v_mfma_f32_16x16x32_bf16 v[92:95], a[76:79], a[216:219], v[92:95]\n" \
"ds_read2_b32 a[30:31], v49 offset0:242 offset1:243\n" \
"v_mul_f32 v249, s20, v249\n" \
"v_mfma_f32_16x16x32_bf16 v[92:95], a[80:83], a[212:215], v[92:95]\n" \
"buffer_store_dwordx4 v[38:41], v4, s[36:39], s96 offen offset:0 sc1\n" \
"v_mfma_f32_16x16x32_bf16 v[92:95], a[84:87], a[208:211], v[92:95]\n" \
"s_add_u32 s97, s96, s98\n" \
"v_mul_f32 v250, s20, v250\n" \
"v_mul_f32 v251, s20, v251\n" \
"v_mfma_f32_16x16x32_bf16 v[92:95], a[88:91], a[204:207], v[92:95]\n" \
"v_cvt_pk_bf16_f32 v30, v248, v249\n" \
"v_cvt_pk_bf16_f32 v31, v250, v251\n" \
"v_mfma_f32_16x16x32_bf16 v[92:95], a[92:95], a[200:203], v[92:95]\n" \
"s_waitcnt lgkmcnt(0)\n" \
"v_accvgpr_read_b32 v252, a164\n" \
"v_mfma_f32_16x16x32_bf16 v[64:67], a[0:3], a[32:35], v[64:67]\n" \
"s_lshl_b32 s59, %2, 10\n" \
"ds_read_b64 v[192:193], v51 offset:10816\n" \
"v_and_b32 v243, 0x70, v4\n" \
"v_mfma_f32_16x16x32_bf16 v[68:71], a[0:3], a[36:39], v[68:71]\n" \
"ds_read_b64 v[194:195], v51 offset:11328\n" \
"v_lshrrev_b32 v240, 7, v4\n" \
"v_mfma_f32_16x16x32_bf16 v[68:71], a[4:7], a[32:35], v[68:71]\n" \
"ds_read_b64 v[196:197], v51 offset:11840\n" \
"v_lshl_add_u32 v240, v240, 9, v243\n" \
"v_mfma_f32_16x16x32_bf16 v[72:75], a[0:3], a[40:43], v[72:75]\n" \
"ds_read_b64 v[198:199], v51 offset:12352\n" \
"v_add_u32 v240, s22, v240\n" \
"v_mfma_f32_16x16x32_bf16 v[72:75], a[4:7], a[36:39], v[72:75]\n" \
"ds_read_b64 v[200:201], v51 offset:12864\n" \
"v_and_b32 v243, 0x30, v4\n" \
"v_mfma_f32_16x16x32_bf16 v[72:75], a[8:11], a[32:35], v[72:75]\n" \
"ds_read_b64 v[202:203], v51 offset:13376\n" \
"v_lshl_add_u32 v241, v243, 3, s59\n" \
"v_mfma_f32_16x16x32_bf16 v[76:79], a[0:3], a[44:47], v[76:79]\n" \
"ds_read_b64 v[204:205], v51 offset:13888\n" \
"v_and_b32 v243, 0x200, v4\n" \
"v_mfma_f32_16x16x32_bf16 v[76:79], a[4:7], a[40:43], v[76:79]\n" \
"ds_read_b64 v[206:207], v51 offset:14400\n" \
"v_add_u32 v241, v243, v241\n" \
"v_mfma_f32_16x16x32_bf16 v[76:79], a[8:11], a[36:39], v[76:79]\n" \
"buffer_store_dwordx4 v[42:45], v4, s[36:39], s97 offen offset:0 sc1\n" \
"v_mfma_f32_16x16x32_bf16 v[76:79], a[12:15], a[32:35], v[76:79]\n" \
"s_add_u32 s96, s96, s92\n" \
"v_bfe_u32 v243, v4, 6, 3\n" \
"v_lshl_add_u32 v241, v243, 4, v241\n" \
"v_mfma_f32_16x16x32_bf16 v[80:83], a[0:3], a[48:51], v[80:83]\n" \
"v_and_b32 v243, 0x20, v4\n" \
"v_lshlrev_b32 v243, 1, v243\n" \
"v_mfma_f32_16x16x32_bf16 v[80:83], a[4:7], a[44:47], v[80:83]\n" \
"s_waitcnt vmcnt(40) lgkmcnt(0)\n" \
"v_xor_b32 v241, v243, v241\n" \
"s_lshl_b32 s59, s8, 14\n" \
"v_mfma_f32_16x16x32_bf16 v[80:83], a[8:11], a[40:43], v[80:83]\n" \
"s_barrier\n" \
"ds_read_b64_tr_b16 a[64:65], v9 offset:33280\n" \
"v_and_b32 v208, 0xffff0000, v193\n" \
"v_mfma_f32_16x16x32_bf16 v[80:83], a[12:15], a[36:39], v[80:83]\n" \
"s_add_u32 s59, s59, 67108864\n" \
"ds_read_b64_tr_b16 a[66:67], v10 offset:33280\n" \
"v_lshlrev_b32 v209, 16, v193\n" \
"v_mfma_f32_16x16x32_bf16 v[80:83], a[16:19], a[32:35], v[80:83]\n" \
"s_mov_b32 s94, 0x05040100\n" \
"ds_read_b64_tr_b16 a[68:69], v9 offset:41472\n" \
"v_and_b32 v210, 0xffff0000, v192\n" \
"v_mfma_f32_16x16x32_bf16 v[84:87], a[0:3], a[52:55], v[84:87]\n" \
"s_mov_b32 s95, 0x07060302\n" \
"ds_read_b64_tr_b16 a[70:71], v10 offset:41472\n" \
"v_lshlrev_b32 v211, 16, v192\n" \
"v_mfma_f32_16x16x32_bf16 v[84:87], a[4:7], a[48:51], v[84:87]\n" \
"s_mov_b32 s46, 256\n" \
"s_add_u32 m0, s50, 32768\n" \
"ds_read_b128 a[200:203], v7 offset:0\n" \
"v_mfma_f32_16x16x32_bf16 v[84:87], a[8:11], a[44:47], v[84:87]\n" \
"buffer_load_dwordx4 v13, s[28:31], s46 offen lds\n" \
"v_mfma_f32_16x16x32_bf16 v[84:87], a[12:15], a[40:43], v[84:87]\n" \
"ds_read_b128 a[204:207], v8 offset:0\n" \
"v_fma_f32 v208, v208, v28, v64\n" \
"v_mfma_f32_16x16x32_bf16 v[84:87], a[16:19], a[36:39], v[84:87]\n" \
"ds_read_b128 a[208:211], v7 offset:2048\n" \
"v_fma_f32 v209, v209, v28, v65\n" \
"v_mfma_f32_16x16x32_bf16 v[84:87], a[20:23], a[32:35], v[84:87]\n" \
"ds_read_b128 a[212:215], v8 offset:2048\n" \
"v_fma_f32 v210, v210, v28, v66\n" \
"v_mfma_f32_16x16x32_bf16 v[88:91], a[0:3], a[56:59], v[88:91]\n" \
"ds_read_b128 a[216:219], v7 offset:4096\n" \
"v_fma_f32 v211, v211, v28, v67\n" \
"v_mfma_f32_16x16x32_bf16 v[88:91], a[4:7], a[52:55], v[88:91]\n" \
"s_add_u32 m0, s50, 36864\n" \
"ds_read_b128 a[220:223], v8 offset:4096\n" \
"v_and_b32 v212, 0xffff0000, v195\n" \
"v_mfma_f32_16x16x32_bf16 v[88:91], a[8:11], a[48:51], v[88:91]\n" \
"buffer_load_dwordx4 v14, s[28:31], s46 offen lds\n" \
"v_mfma_f32_16x16x32_bf16 v[88:91], a[12:15], a[44:47], v[88:91]\n" \
"ds_read_b128 a[224:227], v7 offset:6144\n" \
"v_lshlrev_b32 v213, 16, v195\n" \
"v_mfma_f32_16x16x32_bf16 v[88:91], a[16:19], a[40:43], v[88:91]\n" \
"ds_read_b128 a[228:231], v8 offset:6144\n" \
"v_and_b32 v214, 0xffff0000, v194\n" \
"v_mfma_f32_16x16x32_bf16 v[88:91], a[20:23], a[36:39], v[88:91]\n" \
"ds_read_b64_tr_b16 a[72:73], v11 offset:33280\n" \
"v_lshlrev_b32 v215, 16, v194\n" \
"v_mfma_f32_16x16x32_bf16 v[88:91], a[24:27], a[32:35], v[88:91]\n" \
"LH9_%=:\ns_cbranch_execz LH13_%=\n" \
"ds_read_b64_tr_b16 a[74:75], v12 offset:33280\n" \
"v_fma_f32 v212, v212, v28, v68\n" \
"v_mfma_f32_16x16x32_bf16 v[92:95], a[0:3], a[60:63], v[92:95]\n" \
"s_add_u32 m0, s50, 40960\n" \
"ds_read_b64_tr_b16 a[76:77], v11 offset:41472\n" \
"v_fma_f32 v213, v213, v28, v69\n" \
"v_mfma_f32_16x16x32_bf16 v[92:95], a[4:7], a[56:59], v[92:95]\n" \
"buffer_load_dwordx4 v15, s[28:31], s46 offen lds\n" \
"v_mfma_f32_16x16x32_bf16 v[92:95], a[8:11], a[52:55], v[92:95]\n" \
"ds_read_b64_tr_b16 a[78:79], v12 offset:41472\n" \
"v_fma_f32 v214, v214, v28, v70\n" \
"v_mfma_f32_16x16x32_bf16 v[92:95], a[12:15], a[48:51], v[92:95]\n" \
"ds_read_b64_tr_b16 a[80:81], v9 offset:33792\n" \
"v_fma_f32 v215, v215, v28, v71\n" \
"v_mfma_f32_16x16x32_bf16 v[92:95], a[16:19], a[44:47], v[92:95]\n" \
"ds_read_b64_tr_b16 a[82:83], v10 offset:33792\n" \
"v_cvt_pk_bf16_f32 v192, v208, v209\n" \
"v_mfma_f32_16x16x32_bf16 v[92:95], a[20:23], a[40:43], v[92:95]\n" \
"ds_read_b64_tr_b16 a[84:85], v9 offset:41984\n" \
"v_cvt_pk_bf16_f32 v193, v210, v211\n" \
"v_mfma_f32_16x16x32_bf16 v[92:95], a[24:27], a[36:39], v[92:95]\n" \
"ds_read_b64_tr_b16 a[86:87], v10 offset:41984\n" \
"v_cvt_pk_bf16_f32 v194, v212, v213\n" \
"v_mfma_f32_16x16x32_bf16 v[92:95], a[28:31], a[32:35], v[92:95]\n" \
"s_waitcnt lgkmcnt(8)\n" \
"v_cvt_pk_bf16_f32 v195, v214, v215\n" \
"v_and_b32 v216, 0xffff0000, v197\n" \
"s_branch LHR3_%=\ns_nop 0\ns_nop 0\ns_nop 0\ns_nop 0\ns_nop 0\ns_nop 0\ns_nop 0\ns_nop 0\ns_nop 0\nLHR3_%=:\n" \
"v_mfma_f32_16x16x32_bf16 v[96:99], a[64:67], a[200:203], 0\n" \
"s_add_u32 m0, s50, 45056\n" \
"ds_read_b64_tr_b16 a[88:89], v11 offset:33792\n" \
"v_lshlrev_b32 v217, 16, v197\n" \
"v_mfma_f32_16x16x32_bf16 v[96:99], a[68:71], a[204:207], v[96:99]\n" \
"buffer_load_dwordx4 v16, s[28:31], s46 offen lds\n" \
"v_mfma_f32_16x16x32_bf16 v[104:107], a[64:67], a[208:211], 0\n" \
"ds_read_b64_tr_b16 a[90:91], v12 offset:33792\n" \
"v_and_b32 v218, 0xffff0000, v196\n" \
"v_mfma_f32_16x16x32_bf16 v[104:107], a[68:71], a[212:215], v[104:107]\n" \
"ds_read_b64_tr_b16 a[92:93], v11 offset:41984\n" \
"v_lshlrev_b32 v219, 16, v196\n" \
"v_mfma_f32_16x16x32_bf16 v[112:115], a[64:67], a[216:219], 0\n" \
"s_mov_b32 s46, 256\n" \
"s_add_u32 m0, s50, 49152\n" \
"ds_read_b64_tr_b16 a[94:95], v12 offset:41984\n" \
"v_mfma_f32_16x16x32_bf16 v[112:115], a[68:71], a[220:223], v[112:115]\n" \
"buffer_load_dwordx4 v17, s[28:31], s46 offen lds\n" \
"v_mfma_f32_16x16x32_bf16 v[120:123], a[64:67], a[224:227], 0\n" \
"s_waitcnt lgkmcnt(8)\n" \
"ds_read_b64_tr_b16 a[96:97], v9 offset:34304\n" \
"v_fma_f32 v216, v216, v28, v72\n" \
"v_mfma_f32_16x16x32_bf16 v[120:123], a[68:71], a[228:231], v[120:123]\n" \
"ds_read_b64_tr_b16 a[98:99], v10 offset:34304\n" \
"v_fma_f32 v217, v217, v28, v73\n" \
"v_mfma_f32_16x16x32_bf16 v[100:103], a[72:75], a[200:203], 0\n" \
"s_add_u32 m0, s50, 53248\n" \
"ds_read_b64_tr_b16 a[100:101], v9 offset:42496\n" \
"v_fma_f32 v218, v218, v28, v74\n" \
"v_mfma_f32_16x16x32_bf16 v[100:103], a[76:79], a[204:207], v[100:103]\n" \
"buffer_load_dwordx4 v18, s[28:31], s46 offen lds\n" \
"v_mfma_f32_16x16x32_bf16 v[108:111], a[72:75], a[208:211], 0\n" \
"ds_read_b64_tr_b16 a[102:103], v10 offset:42496\n" \
"v_fma_f32 v219, v219, v28, v75\n" \
"v_mfma_f32_16x16x32_bf16 v[108:111], a[76:79], a[212:215], v[108:111]\n" \
"ds_read_b64_tr_b16 a[104:105], v11 offset:34304\n" \
"v_and_b32 v220, 0xffff0000, v199\n" \
"v_mfma_f32_16x16x32_bf16 v[116:119], a[72:75], a[216:219], 0\n" \
"s_add_u32 m0, s50, 57344\n" \
"ds_read_b64_tr_b16 a[106:107], v12 offset:34304\n" \
"v_lshlrev_b32 v221, 16, v199\n" \
"v_mfma_f32_16x16x32_bf16 v[116:119], a[76:79], a[220:223], v[116:119]\n" \
"buffer_load_dwordx4 v19, s[28:31], s46 offen lds\n" \
"v_mfma_f32_16x16x32_bf16 v[124:127], a[72:75], a[224:227], 0\n" \
"s_waitcnt lgkmcnt(10)\n" \
"ds_read_b64_tr_b16 a[108:109], v11 offset:42496\n" \
"v_and_b32 v222, 0xffff0000, v198\n" \
"v_mfma_f32_16x16x32_bf16 v[124:127], a[76:79], a[228:231], v[124:127]\n" \
"ds_read_b64_tr_b16 a[110:111], v12 offset:42496\n" \
"v_lshlrev_b32 v223, 16, v198\n" \
"v_mfma_f32_16x16x32_bf16 v[128:131], a[80:83], a[200:203], 0\n" \
"s_add_u32 m0, s50, 61440\n" \
"ds_read_b64_tr_b16 a[64:65], v9 offset:32768\n" \
"v_fma_f32 v220, v220, v28, v76\n" \
"v_mfma_f32_16x16x32_bf16 v[128:131], a[84:87], a[204:207], v[128:131]\n" \
"buffer_load_dwordx4 v20, s[28:31], s46 offen lds\n" \
"v_mfma_f32_16x16x32_bf16 v[136:139], a[80:83], a[208:211], 0\n" \
"ds_read_b64_tr_b16 a[66:67], v10 offset:32768\n" \
"v_fma_f32 v221, v221, v28, v77\n" \
"v_mfma_f32_16x16x32_bf16 v[136:139], a[84:87], a[212:215], v[136:139]\n" \
"ds_read_b64_tr_b16 a[68:69], v9 offset:40960\n" \
"v_fma_f32 v222, v222, v28, v78\n" \
"v_mfma_f32_16x16x32_bf16 v[144:147], a[80:83], a[216:219], 0\n" \
"s_mov_b32 s47, 1048576\n" \
"s_add_u32 m0, s51, 0\n" \
"ds_read_b64_tr_b16 a[70:71], v10 offset:40960\n" \
"v_mfma_f32_16x16x32_bf16 v[144:147], a[84:87], a[220:223], v[144:147]\n" \
"buffer_load_dwordx4 v21, s[32:35], s47 offen offset:0 lds\n" \
"v_mfma_f32_16x16x32_bf16 v[152:155], a[80:83], a[224:227], 0\n" \
"s_waitcnt lgkmcnt(12)\n" \
"ds_read_b64_tr_b16 a[72:73], v11 offset:32768\n" \
"v_fma_f32 v223, v223, v28, v79\n" \
"v_mfma_f32_16x16x32_bf16 v[152:155], a[84:87], a[228:231], v[152:155]\n" \
"ds_read_b64_tr_b16 a[74:75], v12 offset:32768\n" \
"v_and_b32 v224, 0xffff0000, v201\n" \
"v_mfma_f32_16x16x32_bf16 v[132:135], a[88:91], a[200:203], 0\n" \
"s_add_u32 m0, s51, 896\n" \
"ds_read_b64_tr_b16 a[76:77], v11 offset:40960\n" \
"v_lshlrev_b32 v225, 16, v201\n" \
"v_mfma_f32_16x16x32_bf16 v[132:135], a[92:95], a[204:207], v[132:135]\n" \
"buffer_load_dwordx4 v21, s[32:35], s47 offen offset:128 lds\n" \
"v_mfma_f32_16x16x32_bf16 v[140:143], a[88:91], a[208:211], 0\n" \
"ds_read_b64_tr_b16 a[78:79], v12 offset:40960\n" \
"v_and_b32 v226, 0xffff0000, v200\n" \
"v_mfma_f32_16x16x32_bf16 v[140:143], a[92:95], a[212:215], v[140:143]\n" \
"ds_write2st64_b64 v55, v[192:193], v[194:195] offset0:32 offset1:33\n" \
"v_lshlrev_b32 v227, 16, v200\n" \
"v_mfma_f32_16x16x32_bf16 v[148:151], a[88:91], a[216:219], 0\n" \
"s_mov_b32 s47, 1310720\n" \
"s_add_u32 m0, s51, 8192\n" \
"v_fma_f32 v224, v224, v28, v80\n" \
"v_mfma_f32_16x16x32_bf16 v[148:151], a[92:95], a[220:223], v[148:151]\n" \
"buffer_load_dwordx4 v21, s[32:35], s47 offen offset:0 lds\n" \
"v_mfma_f32_16x16x32_bf16 v[156:159], a[88:91], a[224:227], 0\n" \
"s_waitcnt vmcnt(31) lgkmcnt(0)\n" \
"v_fma_f32 v225, v225, v28, v81\n" \
"v_fma_f32 v226, v226, v28, v82\n" \
"v_mfma_f32_16x16x32_bf16 v[156:159], a[92:95], a[228:231], v[156:159]\n" \
"s_barrier\n" \
"v_fma_f32 v227, v227, v28, v83\n" \
"v_and_b32 v228, 0xffff0000, v203\n" \
"v_mfma_f32_16x16x32_bf16 v[160:163], a[96:99], a[200:203], 0\n" \
"s_add_u32 m0, s51, 9088\n" \
"ds_read_b64_tr_b16 a[80:81], v9 offset:49664\n" \
"v_lshlrev_b32 v229, 16, v203\n" \
"v_mfma_f32_16x16x32_bf16 v[160:163], a[100:103], a[204:207], v[160:163]\n" \
"buffer_load_dwordx4 v21, s[32:35], s47 offen offset:128 lds\n" \
"v_mfma_f32_16x16x32_bf16 v[168:171], a[96:99], a[208:211], 0\n" \
"ds_read_b64_tr_b16 a[82:83], v10 offset:49664\n" \
"v_and_b32 v230, 0xffff0000, v202\n" \
"v_mfma_f32_16x16x32_bf16 v[168:171], a[100:103], a[212:215], v[168:171]\n" \
"ds_read_b64_tr_b16 a[84:85], v9 offset:57856\n" \
"v_lshlrev_b32 v231, 16, v202\n" \
"v_mfma_f32_16x16x32_bf16 v[176:179], a[96:99], a[216:219], 0\n" \
"ds_read_b64_tr_b16 a[86:87], v10 offset:57856\n" \
"v_fma_f32 v228, v228, v28, v84\n" \
"v_mfma_f32_16x16x32_bf16 v[176:179], a[100:103], a[220:223], v[176:179]\n" \
"s_mov_b32 s46, 384\n" \
"s_add_u32 m0, s50, 65536\n" \
"ds_read_b128 a[0:3], v5 offset:0\n" \
"v_mfma_f32_16x16x32_bf16 v[184:187], a[96:99], a[224:227], 0\n" \
"buffer_load_dwordx4 v13, s[28:31], s46 offen lds\n" \
"v_mfma_f32_16x16x32_bf16 v[184:187], a[100:103], a[228:231], v[184:187]\n" \
"ds_read_b128 a[4:7], v6 offset:0\n" \
"v_fma_f32 v229, v229, v28, v85\n" \
"v_mfma_f32_16x16x32_bf16 v[164:167], a[104:107], a[200:203], 0\n" \
"ds_read_b128 a[8:11], v5 offset:2048\n" \
"v_fma_f32 v230, v230, v28, v86\n" \
"v_mfma_f32_16x16x32_bf16 v[164:167], a[108:111], a[204:207], v[164:167]\n" \
"ds_read_b128 a[12:15], v6 offset:2048\n" \
"v_fma_f32 v231, v231, v28, v87\n" \
"v_mfma_f32_16x16x32_bf16 v[172:175], a[104:107], a[208:211], 0\n" \
"s_add_u32 m0, s50, 69632\n" \
"ds_read_b128 a[16:19], v5 offset:4096\n" \
"v_and_b32 v232, 0xffff0000, v205\n" \
"v_mfma_f32_16x16x32_bf16 v[172:175], a[108:111], a[212:215], v[172:175]\n" \
"buffer_load_dwordx4 v14, s[28:31], s46 offen lds\n" \
"v_lshlrev_b32 v233, 16, v205\n" \
"v_and_b32 v234, 0xffff0000, v204\n" \
"v_lshlrev_b32 v235, 16, v204\n" \
"v_fma_f32 v232, v232, v28, v88\n" \
"v_fma_f32 v233, v233, v28, v89\n" \
"v_fma_f32 v234, v234, v28, v90\n" \
"v_fma_f32 v235, v235, v28, v91\n" \
"v_and_b32 v236, 0xffff0000, v207\n" \
"v_lshlrev_b32 v237, 16, v207\n" \
"v_and_b32 v238, 0xffff0000, v206\n" \
"v_lshlrev_b32 v239, 16, v206\n" \
"v_fma_f32 v236, v236, v28, v92\n" \
"v_fma_f32 v237, v237, v28, v93\n" \
"v_fma_f32 v238, v238, v28, v94\n" \
"v_fma_f32 v239, v239, v28, v95\n" \
"v_mfma_f32_16x16x32_bf16 v[180:183], a[104:107], a[216:219], 0\n" \
"ds_read_b128 a[20:23], v6 offset:4096\n" \
"v_cvt_pk_bf16_f32 v196, v216, v217\n" \
"v_mfma_f32_16x16x32_bf16 v[180:183], a[108:111], a[220:223], v[180:183]\n" \
"ds_read_b128 a[24:27], v5 offset:6144\n" \
"v_cvt_pk_bf16_f32 v197, v218, v219\n" \
"v_cvt_pk_bf16_f32 v198, v220, v221\n" \
"v_cvt_pk_bf16_f32 v199, v222, v223\n" \
"v_mfma_f32_16x16x32_bf16 v[188:191], a[104:107], a[224:227], 0\n" \
"ds_write2st64_b64 v55, v[196:197], v[198:199] offset0:34 offset1:35\n" \
"v_cvt_pk_bf16_f32 v200, v224, v225\n" \
"v_mfma_f32_16x16x32_bf16 v[188:191], a[108:111], a[228:231], v[188:191]\n" \
"s_add_u32 m0, s50, 73728\n" \
"ds_read_b128 a[28:31], v6 offset:6144\n" \
"v_cvt_pk_bf16_f32 v201, v226, v227\n" \
"v_mfma_f32_16x16x32_bf16 v[64:67], a[64:67], a[200:203], 0\n" \
"buffer_load_dwordx4 v15, s[28:31], s46 offen lds\n" \
"v_mfma_f32_16x16x32_bf16 v[64:67], a[68:71], a[204:207], v[64:67]\n" \
"ds_read_b64_tr_b16 a[88:89], v11 offset:49664\n" \
"v_cvt_pk_bf16_f32 v202, v228, v229\n" \
"v_mfma_f32_16x16x32_bf16 v[72:75], a[64:67], a[208:211], 0\n" \
"ds_read_b64_tr_b16 a[90:91], v12 offset:49664\n" \
"v_cvt_pk_bf16_f32 v203, v230, v231\n" \
"v_mfma_f32_16x16x32_bf16 v[72:75], a[68:71], a[212:215], v[72:75]\n" \
"ds_read_b64_tr_b16 a[92:93], v11 offset:57856\n" \
"v_cvt_pk_bf16_f32 v204, v232, v233\n" \
"v_mfma_f32_16x16x32_bf16 v[80:83], a[64:67], a[216:219], 0\n" \
"s_add_u32 m0, s50, 77824\n" \
"ds_write2st64_b64 v55, v[200:201], v[202:203] offset0:36 offset1:37\n" \
"v_cvt_pk_bf16_f32 v205, v234, v235\n" \
"v_mfma_f32_16x16x32_bf16 v[80:83], a[68:71], a[220:223], v[80:83]\n" \
"buffer_load_dwordx4 v16, s[28:31], s46 offen lds\n" \
"v_mfma_f32_16x16x32_bf16 v[88:91], a[64:67], a[224:227], 0\n" \
"ds_read_b64_tr_b16 a[94:95], v12 offset:57856\n" \
"v_cvt_pk_bf16_f32 v206, v236, v237\n" \
"v_mfma_f32_16x16x32_bf16 v[88:91], a[68:71], a[228:231], v[88:91]\n" \
"ds_read_b64_tr_b16 a[96:97], v9 offset:50176\n" \
"v_cvt_pk_bf16_f32 v207, v238, v239\n" \
"v_mfma_f32_16x16x32_bf16 v[68:71], a[72:75], a[200:203], 0\n" \
"ds_read_b64_tr_b16 a[98:99], v10 offset:50176\n" \
"v_accvgpr_read_b32 v253, a165\n" \
"v_mfma_f32_16x16x32_bf16 v[68:71], a[76:79], a[204:207], v[68:71]\n" \
"ds_read_b64_tr_b16 a[100:101], v9 offset:58368\n" \
"v_accvgpr_read_b32 v254, a166\n" \
"v_mfma_f32_16x16x32_bf16 v[76:79], a[72:75], a[208:211], 0\n" \
"ds_write2st64_b64 v55, v[204:205], v[206:207] offset0:38 offset1:39\n" \
"v_accvgpr_read_b32 v255, a167\n" \
"v_mfma_f32_16x16x32_bf16 v[76:79], a[76:79], a[212:215], v[76:79]\n" \
"ds_read_b64_tr_b16 a[102:103], v10 offset:58368\n" \
"v_mul_f32 v252, s20, v252\n" \
"v_mfma_f32_16x16x32_bf16 v[84:87], a[72:75], a[216:219], 0\n" \
"ds_read_b64_tr_b16 a[104:105], v11 offset:50176\n" \
"v_mul_f32 v253, s20, v253\n" \
"v_mfma_f32_16x16x32_bf16 v[84:87], a[76:79], a[220:223], v[84:87]\n" \
"ds_read_b64_tr_b16 a[106:107], v12 offset:50176\n" \
"v_mul_f32 v254, s20, v254\n" \
"v_mfma_f32_16x16x32_bf16 v[92:95], a[72:75], a[224:227], 0\n" \
"ds_read_b64_tr_b16 a[108:109], v11 offset:58368\n" \
"v_mul_f32 v255, s20, v255\n" \
"v_mfma_f32_16x16x32_bf16 v[92:95], a[76:79], a[228:231], v[92:95]\n" \
"s_waitcnt lgkmcnt(13)\n" \
"ds_read_b64_tr_b16 a[110:111], v12 offset:58368\n" \
"s_waitcnt lgkmcnt(5)\n" \
"s_barrier\n" \
"v_mfma_f32_16x16x32_bf16 v[96:99], a[80:83], a[0:3], v[96:99]\n" \
"s_mov_b32 s46, 384\n" \
"s_add_u32 m0, s50, 81920\n" \
"ds_read_b64_tr_b16 a[64:65], v9 offset:50688\n" \
"v_mfma_f32_16x16x32_bf16 v[96:99], a[84:87], a[4:7], v[96:99]\n" \
"buffer_load_dwordx4 v17, s[28:31], s46 offen lds\n" \
"v_mfma_f32_16x16x32_bf16 v[104:107], a[80:83], a[8:11], v[104:107]\n" \
"ds_read_b64_tr_b16 a[66:67], v10 offset:50688\n" \
"v_cvt_pk_bf16_f32 v32, v252, v253\n" \
"v_mfma_f32_16x16x32_bf16 v[104:107], a[84:87], a[12:15], v[104:107]\n" \
"ds_read_b64_tr_b16 a[68:69], v9 offset:58880\n" \
"v_cvt_pk_bf16_f32 v33, v254, v255\n" \
"v_mfma_f32_16x16x32_bf16 v[112:115], a[80:83], a[16:19], v[112:115]\n" \
"s_add_u32 m0, s50, 86016\n" \
"ds_read_b64_tr_b16 a[70:71], v10 offset:58880\n" \
"v_accvgpr_read_b32 v248, a168\n" \
"v_mfma_f32_16x16x32_bf16 v[112:115], a[84:87], a[20:23], v[112:115]\n" \
"buffer_load_dwordx4 v18, s[28:31], s46 offen lds\n" \
"v_mfma_f32_16x16x32_bf16 v[120:123], a[80:83], a[24:27], v[120:123]\n" \
"s_waitcnt lgkmcnt(13)\n" \
"ds_read_b64_tr_b16 a[72:73], v11 offset:50688\n" \
"v_accvgpr_read_b32 v249, a169\n" \
"v_mfma_f32_16x16x32_bf16 v[120:123], a[84:87], a[28:31], v[120:123]\n" \
"ds_read_b64_tr_b16 a[74:75], v12 offset:50688\n" \
"v_accvgpr_read_b32 v250, a170\n" \
"v_mfma_f32_16x16x32_bf16 v[100:103], a[88:91], a[0:3], v[100:103]\n" \
"s_add_u32 m0, s50, 90112\n" \
"ds_read_b64_tr_b16 a[76:77], v11 offset:58880\n" \
"v_accvgpr_read_b32 v251, a171\n" \
"v_mfma_f32_16x16x32_bf16 v[100:103], a[92:95], a[4:7], v[100:103]\n" \
"buffer_load_dwordx4 v19, s[28:31], s46 offen lds\n" \
"v_mfma_f32_16x16x32_bf16 v[108:111], a[88:91], a[8:11], v[108:111]\n" \
"ds_read_b64_tr_b16 a[78:79], v12 offset:58880\n" \
"v_mul_f32 v248, s20, v248\n" \
"v_mfma_f32_16x16x32_bf16 v[108:111], a[92:95], a[12:15], v[108:111]\n" \
"ds_read_b64_tr_b16 a[80:81], v9 offset:49152\n" \
"v_mul_f32 v249, s20, v249\n" \
"v_mfma_f32_16x16x32_bf16 v[116:119], a[88:91], a[16:19], v[116:119]\n" \
"s_add_u32 m0, s50, 94208\n" \
"ds_read_b64_tr_b16 a[82:83], v10 offset:49152\n" \
"v_mul_f32 v250, s20, v250\n" \
"v_mfma_f32_16x16x32_bf16 v[116:119], a[92:95], a[20:23], v[116:119]\n" \
"buffer_load_dwordx4 v20, s[28:31], s46 offen lds\n" \
"v_mfma_f32_16x16x32_bf16 v[124:127], a[88:91], a[24:27], v[124:127]\n" \
"s_waitcnt lgkmcnt(14)\n" \
"ds_read_b64_tr_b16 a[84:85], v9 offset:57344\n" \
"v_mul_f32 v251, s20, v251\n" \
"v_mfma_f32_16x16x32_bf16 v[124:127], a[92:95], a[28:31], v[124:127]\n" \
"ds_read_b64_tr_b16 a[86:87], v10 offset:57344\n" \
"v_cvt_pk_bf16_f32 v34, v248, v249\n" \
"v_mfma_f32_16x16x32_bf16 v[128:131], a[96:99], a[0:3], v[128:131]\n" \
"s_mov_b32 s47, 2097152\n" \
"s_add_u32 m0, s51, 32768\n" \
"ds_read_b64_tr_b16 a[88:89], v11 offset:49152\n" \
"v_mfma_f32_16x16x32_bf16 v[128:131], a[100:103], a[4:7], v[128:131]\n" \
"buffer_load_dwordx4 v21, s[32:35], s47 offen offset:0 lds\n" \
"v_mfma_f32_16x16x32_bf16 v[136:139], a[96:99], a[8:11], v[136:139]\n" \
"ds_read_b64_tr_b16 a[90:91], v12 offset:49152\n" \
"v_cvt_pk_bf16_f32 v35, v250, v251\n" \
"v_mfma_f32_16x16x32_bf16 v[136:139], a[100:103], a[12:15], v[136:139]\n" \
"ds_read_b64_tr_b16 a[92:93], v11 offset:57344\n" \
"v_accvgpr_read_b32 v252, a172\n" \
"v_mfma_f32_16x16x32_bf16 v[144:147], a[96:99], a[16:19], v[144:147]\n" \
"s_add_u32 m0, s51, 33664\n" \
"ds_read_b64_tr_b16 a[94:95], v12 offset:57344\n" \
"v_accvgpr_read_b32 v253, a173\n" \
"v_mfma_f32_16x16x32_bf16 v[144:147], a[100:103], a[20:23], v[144:147]\n" \
"buffer_load_dwordx4 v21, s[32:35], s47 offen offset:128 lds\n" \
"v_mfma_f32_16x16x32_bf16 v[152:155], a[96:99], a[24:27], v[152:155]\n" \
"s_waitcnt lgkmcnt(15)\n" \
"ds_read_b128 v[208:211], v241 offset:16384\n" \
"v_accvgpr_read_b32 v254, a174\n" \
"v_mfma_f32_16x16x32_bf16 v[152:155], a[100:103], a[28:31], v[152:155]\n" \
"buffer_store_dwordx4 v[30:33], v4, s[36:39], s96 offen offset:0 sc1\n" \
"v_mfma_f32_16x16x32_bf16 v[132:135], a[104:107], a[0:3], v[132:135]\n" \
"s_mov_b32 s47, 2359296\n" \
"s_add_u32 m0, s51, 40960\n" \
"s_add_u32 s97, s96, s98\n" \
"v_mfma_f32_16x16x32_bf16 v[132:135], a[108:111], a[4:7], v[132:135]\n" \
"buffer_load_dwordx4 v21, s[32:35], s47 offen offset:0 lds\n" \
"v_mfma_f32_16x16x32_bf16 v[140:143], a[104:107], a[8:11], v[140:143]\n" \
"ds_read_b128 v[212:215], v241 offset:20480\n" \
"v_accvgpr_read_b32 v255, a175\n" \
"v_mfma_f32_16x16x32_bf16 v[140:143], a[108:111], a[12:15], v[140:143]\n" \
"ds_read_b128 v[216:219], v241 offset:24576\n" \
"ds_read_b128 v[220:223], v241 offset:28672\n" \
"v_mfma_f32_16x16x32_bf16 v[148:151], a[104:107], a[16:19], v[148:151]\n" \
"s_add_u32 m0, s51, 41856\n" \
"v_mul_f32 v252, s20, v252\n" \
"v_mul_f32 v253, s20, v253\n" \
"v_mfma_f32_16x16x32_bf16 v[148:151], a[108:111], a[20:23], v[148:151]\n" \
"buffer_load_dwordx4 v21, s[32:35], s47 offen offset:128 lds\n" \
"v_mfma_f32_16x16x32_bf16 v[156:159], a[104:107], a[24:27], v[156:159]\n" \
"s_waitcnt vmcnt(13) lgkmcnt(0)\n" \
"v_mul_f32 v254, s20, v254\n" \
"v_mul_f32 v255, s20, v255\n" \
"v_mfma_f32_16x16x32_bf16 v[156:159], a[108:111], a[28:31], v[156:159]\n" \
"s_barrier\n" \
"v_cvt_pk_bf16_f32 v36, v252, v253\n" \
"v_cvt_pk_bf16_f32 v37, v254, v255\n" \
"v_mfma_f32_16x16x32_bf16 v[160:163], a[64:67], a[0:3], v[160:163]\n" \
"s_mov_b32 s47, 1572864\n" \
"s_add_u32 m0, s51, 16384\n" \
"ds_read_b64_tr_b16 a[96:97], v9 offset:512\n" \
"v_mfma_f32_16x16x32_bf16 v[160:163], a[68:71], a[4:7], v[160:163]\n" \
"buffer_load_dwordx4 v21, s[32:35], s47 offen offset:0 lds\n" \
"v_mfma_f32_16x16x32_bf16 v[168:171], a[64:67], a[8:11], v[168:171]\n" \
"ds_read_b64_tr_b16 a[98:99], v10 offset:512\n" \
"v_perm_b32 v224, v212, v208, s94\n" \
"v_mfma_f32_16x16x32_bf16 v[168:171], a[68:71], a[12:15], v[168:171]\n" \
"ds_read_b64_tr_b16 a[100:101], v9 offset:8704\n" \
"v_perm_b32 v225, v220, v216, s94\n" \
"v_mfma_f32_16x16x32_bf16 v[176:179], a[64:67], a[16:19], v[176:179]\n" \
"ds_read_b64_tr_b16 a[102:103], v10 offset:8704\n" \
"v_perm_b32 v226, v214, v210, s94\n" \
"v_mfma_f32_16x16x32_bf16 v[176:179], a[68:71], a[20:23], v[176:179]\n" \
"s_add_u32 m0, s51, 17280\n" \
"ds_read_b128 a[200:203], v5 offset:32768\n" \
"v_perm_b32 v227, v222, v218, s94\n" \
"v_mfma_f32_16x16x32_bf16 v[184:187], a[64:67], a[24:27], v[184:187]\n" \
"buffer_load_dwordx4 v21, s[32:35], s47 offen offset:128 lds\n" \
"v_mfma_f32_16x16x32_bf16 v[184:187], a[68:71], a[28:31], v[184:187]\n" \
"ds_read_b128 a[204:207], v6 offset:32768\n" \
"v_perm_b32 v228, v212, v208, s95\n" \
"v_mfma_f32_16x16x32_bf16 v[164:167], a[72:75], a[0:3], v[164:167]\n" \
"ds_read_b128 a[208:211], v5 offset:34816\n" \
"v_perm_b32 v229, v220, v216, s95\n" \
"v_mfma_f32_16x16x32_bf16 v[164:167], a[76:79], a[4:7], v[164:167]\n" \
"ds_read_b128 a[212:215], v6 offset:34816\n" \
"v_perm_b32 v230, v214, v210, s95\n" \
"v_mfma_f32_16x16x32_bf16 v[172:175], a[72:75], a[8:11], v[172:175]\n" \
"s_mov_b32 s47, 1835008\n" \
"s_add_u32 m0, s51, 24576\n" \
"ds_read_b128 a[216:219], v5 offset:36864\n" \
"v_mfma_f32_16x16x32_bf16 v[172:175], a[76:79], a[12:15], v[172:175]\n" \
"buffer_load_dwordx4 v21, s[32:35], s47 offen offset:0 lds\n" \
"v_mfma_f32_16x16x32_bf16 v[180:183], a[72:75], a[16:19], v[180:183]\n" \
"ds_read_b128 a[220:223], v6 offset:36864\n" \
"v_perm_b32 v231, v222, v218, s95\n" \
"v_mfma_f32_16x16x32_bf16 v[180:183], a[76:79], a[20:23], v[180:183]\n" \
"ds_read_b128 a[224:227], v5 offset:38912\n" \
"v_perm_b32 v232, v213, v209, s94\n" \
"v_mfma_f32_16x16x32_bf16 v[188:191], a[72:75], a[24:27], v[188:191]\n" \
"ds_read_b128 a[228:231], v6 offset:38912\n" \
"v_perm_b32 v233, v221, v217, s94\n" \
"v_mfma_f32_16x16x32_bf16 v[188:191], a[76:79], a[28:31], v[188:191]\n" \
"s_add_u32 m0, s51, 25472\n" \
"ds_read_b64_tr_b16 a[104:105], v11 offset:512\n" \
"v_perm_b32 v234, v215, v211, s94\n" \
"v_mfma_f32_16x16x32_bf16 v[64:67], a[80:83], a[0:3], v[64:67]\n" \
"buffer_load_dwordx4 v21, s[32:35], s47 offen offset:128 lds\n" \
"v_mfma_f32_16x16x32_bf16 v[64:67], a[84:87], a[4:7], v[64:67]\n" \
"ds_read_b64_tr_b16 a[106:107], v12 offset:512\n" \
"v_perm_b32 v235, v223, v219, s94\n" \
"v_mfma_f32_16x16x32_bf16 v[72:75], a[80:83], a[8:11], v[72:75]\n" \
"ds_read_b64_tr_b16 a[108:109], v11 offset:8704\n" \
"v_perm_b32 v236, v213, v209, s95\n" \
"v_mfma_f32_16x16x32_bf16 v[72:75], a[84:87], a[12:15], v[72:75]\n" \
"ds_read_b64_tr_b16 a[110:111], v12 offset:8704\n" \
"v_perm_b32 v237, v221, v217, s95\n" \
"v_mfma_f32_16x16x32_bf16 v[80:83], a[80:83], a[16:19], v[80:83]\n" \
"s_mov_b32 s46, 512\n" \
"s_add_u32 m0, s50, 0\n" \
"ds_read_b64_tr_b16 a[64:65], v9 offset:1024\n" \
"v_mfma_f32_16x16x32_bf16 v[80:83], a[84:87], a[20:23], v[80:83]\n" \
"buffer_load_dwordx4 v13, s[28:31], s46 offen lds\n" \
"v_mfma_f32_16x16x32_bf16 v[88:91], a[80:83], a[24:27], v[88:91]\n" \
"ds_read_b64_tr_b16 a[66:67], v10 offset:1024\n" \
"v_perm_b32 v238, v215, v211, s95\n" \
"v_mfma_f32_16x16x32_bf16 v[88:91], a[84:87], a[28:31], v[88:91]\n" \
"ds_read_b64_tr_b16 a[68:69], v9 offset:9216\n" \
"v_perm_b32 v239, v223, v219, s95\n" \
"v_mfma_f32_16x16x32_bf16 v[68:71], a[88:91], a[0:3], v[68:71]\n" \
"ds_read_b64_tr_b16 a[70:71], v10 offset:9216\n" \
"v_accvgpr_read_b32 v248, a176\n" \
"v_mfma_f32_16x16x32_bf16 v[68:71], a[92:95], a[4:7], v[68:71]\n" \
"s_add_u32 m0, s50, 4096\n" \
"ds_read_b64_tr_b16 a[72:73], v11 offset:1024\n" \
"v_accvgpr_read_b32 v249, a177\n" \
"v_mfma_f32_16x16x32_bf16 v[76:79], a[88:91], a[8:11], v[76:79]\n" \
"buffer_load_dwordx4 v14, s[28:31], s46 offen lds\n" \
"v_mfma_f32_16x16x32_bf16 v[76:79], a[92:95], a[12:15], v[76:79]\n" \
"ds_read_b64_tr_b16 a[74:75], v12 offset:1024\n" \
"v_accvgpr_read_b32 v250, a178\n" \
"v_mfma_f32_16x16x32_bf16 v[84:87], a[88:91], a[16:19], v[84:87]\n" \
"ds_read_b64_tr_b16 a[76:77], v11 offset:9216\n" \
"v_accvgpr_read_b32 v251, a179\n" \
"v_mfma_f32_16x16x32_bf16 v[84:87], a[92:95], a[20:23], v[84:87]\n" \
"ds_read_b64_tr_b16 a[78:79], v12 offset:9216\n" \
"v_mul_f32 v248, s20, v248\n" \
"v_mfma_f32_16x16x32_bf16 v[92:95], a[88:91], a[24:27], v[92:95]\n" \
"buffer_store_dwordx4 v[224:227], v240, s[36:39], s59 offen offset:0 sc1\n" \
"v_mfma_f32_16x16x32_bf16 v[92:95], a[92:95], a[28:31], v[92:95]\n" \
"s_waitcnt lgkmcnt(12)\n" \
"v_mul_f32 v249, s20, v249\n" \
"v_mul_f32 v250, s20, v250\n" \
"v_mfma_f32_16x16x32_bf16 v[96:99], a[96:99], a[200:203], v[96:99]\n" \
"s_add_u32 m0, s50, 8192\n" \
"ds_read_b64_tr_b16 a[80:81], v9 offset:1536\n" \
"v_mul_f32 v251, s20, v251\n" \
"v_mfma_f32_16x16x32_bf16 v[96:99], a[100:103], a[204:207], v[96:99]\n" \
"buffer_load_dwordx4 v15, s[28:31], s46 offen lds\n" \
"v_mfma_f32_16x16x32_bf16 v[104:107], a[96:99], a[208:211], v[104:107]\n" \
"ds_read_b64_tr_b16 a[82:83], v10 offset:1536\n" \
"v_cvt_pk_bf16_f32 v38, v248, v249\n" \
"v_mfma_f32_16x16x32_bf16 v[104:107], a[100:103], a[212:215], v[104:107]\n" \
"ds_read_b64_tr_b16 a[84:85], v9 offset:9728\n" \
"v_cvt_pk_bf16_f32 v39, v250, v251\n" \
"v_mfma_f32_16x16x32_bf16 v[112:115], a[96:99], a[216:219], v[112:115]\n" \
"s_add_u32 m0, s50, 12288\n" \
"ds_read_b64_tr_b16 a[86:87], v10 offset:9728\n" \
"v_accvgpr_read_b32 v252, a180\n" \
"v_mfma_f32_16x16x32_bf16 v[112:115], a[100:103], a[220:223], v[112:115]\n" \
"buffer_load_dwordx4 v16, s[28:31], s46 offen lds\n" \
"v_mfma_f32_16x16x32_bf16 v[120:123], a[96:99], a[224:227], v[120:123]\n" \
"s_waitcnt lgkmcnt(12)\n" \
"ds_read_b64_tr_b16 a[88:89], v11 offset:1536\n" \
"v_accvgpr_read_b32 v253, a181\n" \
"v_mfma_f32_16x16x32_bf16 v[120:123], a[100:103], a[228:231], v[120:123]\n" \
"ds_read_b64_tr_b16 a[90:91], v12 offset:1536\n" \
"v_accvgpr_read_b32 v254, a182\n" \
"v_mfma_f32_16x16x32_bf16 v[100:103], a[104:107], a[200:203], v[100:103]\n" \
"s_mov_b32 s46, 512\n" \
"s_add_u32 m0, s50, 16384\n" \
"ds_read_b64_tr_b16 a[92:93], v11 offset:9728\n" \
"v_mfma_f32_16x16x32_bf16 v[100:103], a[108:111], a[204:207], v[100:103]\n" \
"buffer_load_dwordx4 v17, s[28:31], s46 offen lds\n" \
"v_mfma_f32_16x16x32_bf16 v[108:111], a[104:107], a[208:211], v[108:111]\n" \
"ds_read_b64_tr_b16 a[94:95], v12 offset:9728\n" \
"v_accvgpr_read_b32 v255, a183\n" \
"v_mfma_f32_16x16x32_bf16 v[108:111], a[108:111], a[212:215], v[108:111]\n" \
"ds_read_b64_tr_b16 a[96:97], v9 offset:0\n" \
"v_mul_f32 v252, s20, v252\n" \
"v_mfma_f32_16x16x32_bf16 v[116:119], a[104:107], a[216:219], v[116:119]\n" \
"s_add_u32 m0, s50, 20480\n" \
"ds_read_b64_tr_b16 a[98:99], v10 offset:0\n" \
"v_mul_f32 v253, s20, v253\n" \
"v_mfma_f32_16x16x32_bf16 v[116:119], a[108:111], a[220:223], v[116:119]\n" \
"buffer_load_dwordx4 v18, s[28:31], s46 offen lds\n" \
"v_mfma_f32_16x16x32_bf16 v[124:127], a[104:107], a[224:227], v[124:127]\n" \
"s_waitcnt lgkmcnt(14)\n" \
"ds_read_b64_tr_b16 a[100:101], v9 offset:8192\n" \
"v_mul_f32 v254, s20, v254\n" \
"v_mfma_f32_16x16x32_bf16 v[124:127], a[108:111], a[228:231], v[124:127]\n" \
"ds_read_b64_tr_b16 a[102:103], v10 offset:8192\n" \
"v_mul_f32 v255, s20, v255\n" \
"v_mfma_f32_16x16x32_bf16 v[128:131], a[64:67], a[200:203], v[128:131]\n" \
"s_add_u32 m0, s50, 24576\n" \
"ds_read_b64_tr_b16 a[104:105], v11 offset:0\n" \
"v_cvt_pk_bf16_f32 v40, v252, v253\n" \
"v_mfma_f32_16x16x32_bf16 v[128:131], a[68:71], a[204:207], v[128:131]\n" \
"buffer_load_dwordx4 v19, s[28:31], s46 offen lds\n" \
"v_mfma_f32_16x16x32_bf16 v[136:139], a[64:67], a[208:211], v[136:139]\n" \
"ds_read_b64_tr_b16 a[106:107], v12 offset:0\n" \
"v_cvt_pk_bf16_f32 v41, v254, v255\n" \
"v_mfma_f32_16x16x32_bf16 v[136:139], a[68:71], a[212:215], v[136:139]\n" \
"ds_read_b64_tr_b16 a[108:109], v11 offset:8192\n" \
"v_accvgpr_read_b32 v248, a184\n" \
"v_mfma_f32_16x16x32_bf16 v[144:147], a[64:67], a[216:219], v[144:147]\n" \
"s_add_u32 m0, s50, 28672\n" \
"ds_read_b64_tr_b16 a[110:111], v12 offset:8192\n" \
"v_accvgpr_read_b32 v249, a185\n" \
"v_mfma_f32_16x16x32_bf16 v[144:147], a[68:71], a[220:223], v[144:147]\n" \
"buffer_load_dwordx4 v20, s[28:31], s46 offen lds\n" \
"v_mfma_f32_16x16x32_bf16 v[152:155], a[64:67], a[224:227], v[152:155]\n" \
"s_waitcnt lgkmcnt(15)\n" \
"v_accvgpr_read_b32 v250, a186\n" \
"v_accvgpr_read_b32 v251, a187\n" \
"v_mfma_f32_16x16x32_bf16 v[152:155], a[68:71], a[228:231], v[152:155]\n" \
"v_mul_f32 v248, s20, v248\n" \
"v_mul_f32 v249, s20, v249\n" \
"v_mfma_f32_16x16x32_bf16 v[132:135], a[72:75], a[200:203], v[132:135]\n" \
"s_mov_b32 s47, 2621440\n" \
"s_add_u32 m0, s51, 49152\n" \
"v_mul_f32 v250, s20, v250\n" \
"v_mfma_f32_16x16x32_bf16 v[132:135], a[76:79], a[204:207], v[132:135]\n" \
"buffer_load_dwordx4 v21, s[32:35], s47 offen offset:0 lds\n" \
"v_mfma_f32_16x16x32_bf16 v[140:143], a[72:75], a[208:211], v[140:143]\n" \
"v_mul_f32 v251, s20, v251\n" \
"v_cvt_pk_bf16_f32 v42, v248, v249\n" \
"v_mfma_f32_16x16x32_bf16 v[140:143], a[76:79], a[212:215], v[140:143]\n" \
"v_cvt_pk_bf16_f32 v43, v250, v251\n" \
"v_accvgpr_read_b32 v252, a188\n" \
"v_mfma_f32_16x16x32_bf16 v[148:151], a[72:75], a[216:219], v[148:151]\n" \
"s_add_u32 m0, s51, 50048\n" \
"v_accvgpr_read_b32 v253, a189\n" \
"v_mfma_f32_16x16x32_bf16 v[148:151], a[76:79], a[220:223], v[148:151]\n" \
"buffer_load_dwordx4 v21, s[32:35], s47 offen offset:128 lds\n" \
"v_mfma_f32_16x16x32_bf16 v[156:159], a[72:75], a[224:227], v[156:159]\n" \
"s_waitcnt vmcnt(11) lgkmcnt(0)\n" \
"v_accvgpr_read_b32 v254, a190\n" \
"v_accvgpr_read_b32 v255, a191\n" \
"v_mfma_f32_16x16x32_bf16 v[156:159], a[76:79], a[228:231], v[156:159]\n" \
"LH10_%=:\ns_cbranch_execz LH14_%=\n" \
"s_barrier\n" \
"v_mul_f32 v252, s20, v252\n" \
"v_mul_f32 v253, s20, v253\n" \
"v_mfma_f32_16x16x32_bf16 v[160:163], a[80:83], a[200:203], v[160:163]\n" \
"s_mov_b32 s47, 2883584\n" \
"s_add_u32 m0, s51, 57344\n" \
"ds_read_b64_tr_b16 a[64:65], v9 offset:16896\n" \
"v_mfma_f32_16x16x32_bf16 v[160:163], a[84:87], a[204:207], v[160:163]\n" \
"buffer_load_dwordx4 v21, s[32:35], s47 offen offset:0 lds\n" \
"v_mfma_f32_16x16x32_bf16 v[168:171], a[80:83], a[208:211], v[168:171]\n" \
"ds_read_b64_tr_b16 a[66:67], v10 offset:16896\n" \
"v_mul_f32 v254, s20, v254\n" \
"v_mfma_f32_16x16x32_bf16 v[168:171], a[84:87], a[212:215], v[168:171]\n" \
"ds_read_b64_tr_b16 a[68:69], v9 offset:25088\n" \
"v_mul_f32 v255, s20, v255\n" \
"v_mfma_f32_16x16x32_bf16 v[176:179], a[80:83], a[216:219], v[176:179]\n" \
"ds_read_b64_tr_b16 a[70:71], v10 offset:25088\n" \
"v_cvt_pk_bf16_f32 v44, v252, v253\n" \
"v_mfma_f32_16x16x32_bf16 v[176:179], a[84:87], a[220:223], v[176:179]\n" \
"s_add_u32 m0, s51, 58240\n" \
"ds_read_b128 a[0:3], v7 offset:0\n" \
"v_cvt_pk_bf16_f32 v45, v254, v255\n" \
"v_mfma_f32_16x16x32_bf16 v[184:187], a[80:83], a[224:227], v[184:187]\n" \
"buffer_load_dwordx4 v21, s[32:35], s47 offen offset:128 lds\n" \
"v_mfma_f32_16x16x32_bf16 v[184:187], a[84:87], a[228:231], v[184:187]\n" \
"ds_read_b128 a[4:7], v8 offset:0\n" \
"v_mfma_f32_16x16x32_bf16 v[164:167], a[88:91], a[200:203], v[164:167]\n" \
"ds_read_b128 a[8:11], v7 offset:2048\n" \
"v_mfma_f32_16x16x32_bf16 v[164:167], a[92:95], a[204:207], v[164:167]\n" \
"ds_read_b128 a[12:15], v8 offset:2048\n" \
"v_mfma_f32_16x16x32_bf16 v[172:175], a[88:91], a[208:211], v[172:175]\n" \
"s_mov_b32 s46, 640\n" \
"s_add_u32 m0, s50, 32768\n" \
"ds_read_b128 a[16:19], v7 offset:4096\n" \
"v_mfma_f32_16x16x32_bf16 v[172:175], a[92:95], a[212:215], v[172:175]\n" \
"buffer_load_dwordx4 v13, s[28:31], s46 offen lds\n" \
"v_mfma_f32_16x16x32_bf16 v[180:183], a[88:91], a[216:219], v[180:183]\n" \
"ds_read_b128 a[20:23], v8 offset:4096\n" \
"v_mfma_f32_16x16x32_bf16 v[180:183], a[92:95], a[220:223], v[180:183]\n" \
"ds_read_b128 a[24:27], v7 offset:6144\n" \
"v_mfma_f32_16x16x32_bf16 v[188:191], a[88:91], a[224:227], v[188:191]\n" \
"ds_read_b128 a[28:31], v8 offset:6144\n" \
"v_mfma_f32_16x16x32_bf16 v[188:191], a[92:95], a[228:231], v[188:191]\n" \
"s_add_u32 m0, s50, 36864\n" \
"ds_read_b64_tr_b16 a[72:73], v11 offset:16896\n" \
"v_mfma_f32_16x16x32_bf16 v[64:67], a[96:99], a[200:203], v[64:67]\n" \
"buffer_load_dwordx4 v14, s[28:31], s46 offen lds\n" \
"v_mfma_f32_16x16x32_bf16 v[64:67], a[100:103], a[204:207], v[64:67]\n" \
"ds_read_b64_tr_b16 a[74:75], v12 offset:16896\n" \
"v_mfma_f32_16x16x32_bf16 v[72:75], a[96:99], a[208:211], v[72:75]\n" \
"ds_read_b64_tr_b16 a[76:77], v11 offset:25088\n" \
"v_mfma_f32_16x16x32_bf16 v[72:75], a[100:103], a[212:215], v[72:75]\n" \
"ds_read_b64_tr_b16 a[78:79], v12 offset:25088\n" \
"v_mfma_f32_16x16x32_bf16 v[80:83], a[96:99], a[216:219], v[80:83]\n" \
"s_add_u32 m0, s50, 40960\n" \
"ds_read_b64_tr_b16 a[80:81], v9 offset:17408\n" \
"v_mfma_f32_16x16x32_bf16 v[80:83], a[100:103], a[220:223], v[80:83]\n" \
"buffer_load_dwordx4 v15, s[28:31], s46 offen lds\n" \
"v_mfma_f32_16x16x32_bf16 v[88:91], a[96:99], a[224:227], v[88:91]\n" \
"ds_read_b64_tr_b16 a[82:83], v10 offset:17408\n" \
"v_mfma_f32_16x16x32_bf16 v[88:91], a[100:103], a[228:231], v[88:91]\n" \
"ds_read_b64_tr_b16 a[84:85], v9 offset:25600\n" \
"v_mfma_f32_16x16x32_bf16 v[68:71], a[104:107], a[200:203], v[68:71]\n" \
"ds_read_b64_tr_b16 a[86:87], v10 offset:25600\n" \
"v_mfma_f32_16x16x32_bf16 v[68:71], a[108:111], a[204:207], v[68:71]\n" \
"s_add_u32 m0, s50, 45056\n" \
"ds_read_b64_tr_b16 a[88:89], v11 offset:17408\n" \
"v_mfma_f32_16x16x32_bf16 v[76:79], a[104:107], a[208:211], v[76:79]\n" \
"buffer_load_dwordx4 v16, s[28:31], s46 offen lds\n" \
"v_mfma_f32_16x16x32_bf16 v[76:79], a[108:111], a[212:215], v[76:79]\n" \
"ds_read_b64_tr_b16 a[90:91], v12 offset:17408\n" \
"v_mfma_f32_16x16x32_bf16 v[84:87], a[104:107], a[216:219], v[84:87]\n" \
"ds_read_b64_tr_b16 a[92:93], v11 offset:25600\n" \
"v_mfma_f32_16x16x32_bf16 v[84:87], a[108:111], a[220:223], v[84:87]\n" \
"ds_read_b64_tr_b16 a[94:95], v12 offset:25600\n" \
"v_mfma_f32_16x16x32_bf16 v[92:95], a[104:107], a[224:227], v[92:95]\n" \
"buffer_store_dwordx4 v[228:231], v240, s[36:39], s59 offen offset:128 sc1\n" \
"v_mfma_f32_16x16x32_bf16 v[92:95], a[108:111], a[228:231], v[92:95]\n" \
"s_waitcnt lgkmcnt(12)\n" \
"v_mfma_f32_16x16x32_bf16 v[96:99], a[64:67], a[0:3], v[96:99]\n" \
"s_mov_b32 s46, 640\n" \
"s_add_u32 m0, s50, 49152\n" \
"ds_read_b64_tr_b16 a[96:97], v9 offset:17920\n" \
"v_mfma_f32_16x16x32_bf16 v[96:99], a[68:71], a[4:7], v[96:99]\n" \
"buffer_load_dwordx4 v17, s[28:31], s46 offen lds\n" \
"v_mfma_f32_16x16x32_bf16 v[104:107], a[64:67], a[8:11], v[104:107]\n" \
"ds_read_b64_tr_b16 a[98:99], v10 offset:17920\n" \
"s_waitcnt vmcnt(63)\n" \
"v_mfma_f32_16x16x32_bf16 v[104:107], a[68:71], a[12:15], v[104:107]\n" \
"ds_read_b64_tr_b16 a[100:101], v9 offset:26112\n" \
"s_load_dwordx2 s[100:101], %0, 0x50\n" \
"v_mfma_f32_16x16x32_bf16 v[112:115], a[64:67], a[16:19], v[112:115]\n" \
"s_add_u32 m0, s50, 53248\n" \
"ds_read_b64_tr_b16 a[102:103], v10 offset:26112\n" \
"s_lshr_b32 s64, %1, 3\n" \
"v_mfma_f32_16x16x32_bf16 v[112:115], a[68:71], a[20:23], v[112:115]\n" \
"buffer_load_dwordx4 v18, s[28:31], s46 offen lds\n" \
"v_mfma_f32_16x16x32_bf16 v[120:123], a[64:67], a[24:27], v[120:123]\n" \
"s_waitcnt lgkmcnt(12)\n" \
"ds_read_b64_tr_b16 a[104:105], v11 offset:17920\n" \
"s_lshl_b32 s64, s64, 2\n" \
"v_mfma_f32_16x16x32_bf16 v[120:123], a[68:71], a[28:31], v[120:123]\n" \
"s_add_u32 s64, s64, %2\n" \
"v_mfma_f32_16x16x32_bf16 v[24:27], a[240:243], a[192:195], 0\n" \
"v_mfma_f32_16x16x32_bf16 v[100:103], a[72:75], a[0:3], v[100:103]\n" \
"s_add_u32 m0, s50, 57344\n" \
"ds_read_b64_tr_b16 a[106:107], v12 offset:17920\n" \
"s_lshl_b32 s64, s64, 11\n" \
"v_mfma_f32_16x16x32_bf16 v[100:103], a[76:79], a[4:7], v[100:103]\n" \
"buffer_load_dwordx4 v19, s[28:31], s46 offen lds\n" \
"v_mfma_f32_16x16x32_bf16 v[108:111], a[72:75], a[8:11], v[108:111]\n" \
"s_bfe_u32 s65, %1, 0x20001\n" \
"ds_read_b64_tr_b16 a[108:109], v11 offset:26112\n" \
"s_lshl_b32 s65, s65, 21\n" \
"v_mfma_f32_16x16x32_bf16 v[108:111], a[76:79], a[12:15], v[108:111]\n" \
"s_add_u32 s64, s64, s65\n" \
"ds_read_b64_tr_b16 a[110:111], v12 offset:26112\n" \
"v_mfma_f32_16x16x32_bf16 v[116:119], a[72:75], a[16:19], v[116:119]\n" \
"s_add_u32 m0, s50, 61440\n" \
"ds_read_b64_tr_b16 a[64:65], v9 offset:16384\n" \
"v_mfma_f32_16x16x32_bf16 v[116:119], a[76:79], a[20:23], v[116:119]\n" \
"buffer_load_dwordx4 v20, s[28:31], s46 offen lds\n" \
"v_mfma_f32_16x16x32_bf16 v[124:127], a[72:75], a[24:27], v[124:127]\n" \
"s_waitcnt lgkmcnt(13)\n" \
"ds_read_b64_tr_b16 a[66:67], v10 offset:16384\n" \
"v_mfma_f32_16x16x32_bf16 v[124:127], a[76:79], a[28:31], v[124:127]\n" \
"ds_read_b64_tr_b16 a[68:69], v9 offset:24576\n" \
"v_mfma_f32_16x16x32_bf16 v[128:131], a[80:83], a[0:3], v[128:131]\n" \
"s_mov_b32 s47, 3145728\n" \
"s_add_u32 m0, s51, 0\n" \
"ds_read_b64_tr_b16 a[70:71], v10 offset:24576\n" \
"v_mfma_f32_16x16x32_bf16 v[128:131], a[84:87], a[4:7], v[128:131]\n" \
"buffer_load_dwordx4 v21, s[32:35], s47 offen offset:0 lds\n" \
"v_mfma_f32_16x16x32_bf16 v[136:139], a[80:83], a[8:11], v[136:139]\n" \
"ds_read_b64_tr_b16 a[72:73], v11 offset:16384\n" \
"v_mfma_f32_16x16x32_bf16 v[136:139], a[84:87], a[12:15], v[136:139]\n" \
"s_mov_b32 s66, 0x100001\n" \
"v_mfma_f32_16x16x32_bf16 v[24:27], a[244:247], a[196:199], v[24:27]\n" \
"v_mfma_f32_16x16x32_bf16 v[144:147], a[80:83], a[16:19], v[144:147]\n" \
"s_add_u32 m0, s51, 896\n" \
"ds_read_b64_tr_b16 a[74:75], v12 offset:16384\n" \
"s_mov_b32 s67, 0x10000100\n" \
"v_mfma_f32_16x16x32_bf16 v[144:147], a[84:87], a[20:23], v[144:147]\n" \
"buffer_load_dwordx4 v21, s[32:35], s47 offen offset:128 lds\n" \
"v_mfma_f32_16x16x32_bf16 v[152:155], a[80:83], a[24:27], v[152:155]\n" \
"s_waitcnt lgkmcnt(14)\n" \
"ds_read_b64_tr_b16 a[76:77], v11 offset:24576\n" \
"v_cndmask_b32 v23, 0, v24, s[66:67]\n" \
"v_mfma_f32_16x16x32_bf16 v[152:155], a[84:87], a[28:31], v[152:155]\n" \
"s_mov_b32 s66, 0x200002\n" \
"ds_read_b64_tr_b16 a[78:79], v12 offset:24576\n" \
"s_mov_b32 s67, 0x20000200\n" \
"v_mfma_f32_16x16x32_bf16 v[132:135], a[88:91], a[0:3], v[132:135]\n" \
"s_mov_b32 s47, 3407872\n" \
"s_add_u32 m0, s51, 8192\n" \
"v_cndmask_b32 v23, v23, v25, s[66:67]\n" \
"v_mfma_f32_16x16x32_bf16 v[132:135], a[92:95], a[4:7], v[132:135]\n" \
"buffer_load_dwordx4 v21, s[32:35], s47 offen offset:0 lds\n" \
"v_mfma_f32_16x16x32_bf16 v[140:143], a[88:91], a[8:11], v[140:143]\n" \
"s_mov_b32 s66, 0x400004\n" \
"s_mov_b32 s67, 0x40000400\n" \
"v_cndmask_b32 v23, v23, v26, s[66:67]\n" \
"v_mfma_f32_16x16x32_bf16 v[140:143], a[92:95], a[12:15], v[140:143]\n" \
"s_mov_b32 s66, 0x800008\n" \
"s_mov_b32 s67, 0x80000800\n" \
"v_cndmask_b32 v23, v23, v27, s[66:67]\n" \
"v_mfma_f32_16x16x32_bf16 v[148:151], a[88:91], a[16:19], v[148:151]\n" \
"s_add_u32 m0, s51, 9088\n" \
"v_mfma_f32_16x16x32_bf16 v[24:27], a[248:251], a[192:195], 0\n" \
"v_mfma_f32_16x16x32_bf16 v[148:151], a[92:95], a[20:23], v[148:151]\n" \
"buffer_load_dwordx4 v21, s[32:35], s47 offen offset:128 lds\n" \
"v_mfma_f32_16x16x32_bf16 v[156:159], a[88:91], a[24:27], v[156:159]\n" \
"s_waitcnt vmcnt(17) lgkmcnt(0)\n" \
"v_mfma_f32_16x16x32_bf16 v[24:27], a[252:255], a[196:199], v[24:27]\n" \
"v_mfma_f32_16x16x32_bf16 v[156:159], a[92:95], a[28:31], v[156:159]\n" \
"s_barrier\n" \
"s_mov_b32 s66, 0x100001\n" \
"s_mov_b32 s67, 0x10000100\n" \
"v_mfma_f32_16x16x32_bf16 v[160:163], a[96:99], a[0:3], v[160:163]\n" \
"ds_read_b64_tr_b16 a[80:81], v9 offset:33280\n" \
"v_mfma_f32_16x16x32_bf16 v[160:163], a[100:103], a[4:7], v[160:163]\n" \
"ds_read_b64_tr_b16 a[82:83], v10 offset:33280\n" \
"v_cndmask_b32 v54, 0, v24, s[66:67]\n" \
"v_mfma_f32_16x16x32_bf16 v[168:171], a[96:99], a[8:11], v[168:171]\n" \
"s_mov_b32 s46, 768\n" \
"s_add_u32 m0, s50, 65536\n" \
"ds_read_b64_tr_b16 a[84:85], v9 offset:41472\n" \
"v_mfma_f32_16x16x32_bf16 v[168:171], a[100:103], a[12:15], v[168:171]\n" \
"buffer_load_dwordx4 v13, s[28:31], s46 offen lds\n" \
"v_mfma_f32_16x16x32_bf16 v[176:179], a[96:99], a[16:19], v[176:179]\n" \
"s_mov_b32 s66, 0x200002\n" \
"ds_read_b64_tr_b16 a[86:87], v10 offset:41472\n" \
"s_mov_b32 s67, 0x20000200\n" \
"v_mfma_f32_16x16x32_bf16 v[176:179], a[100:103], a[20:23], v[176:179]\n" \
"ds_read_b128 a[200:203], v5 offset:0\n" \
"v_cndmask_b32 v54, v54, v25, s[66:67]\n" \
"v_mfma_f32_16x16x32_bf16 v[184:187], a[96:99], a[24:27], v[184:187]\n" \
"s_mov_b32 s66, 0x400004\n" \
"ds_read_b128 a[204:207], v6 offset:0\n" \
"s_mov_b32 s67, 0x40000400\n" \
"v_mfma_f32_16x16x32_bf16 v[184:187], a[100:103], a[28:31], v[184:187]\n" \
"ds_read_b128 a[208:211], v5 offset:2048\n" \
"v_cndmask_b32 v54, v54, v26, s[66:67]\n" \
"v_mfma_f32_16x16x32_bf16 v[164:167], a[104:107], a[0:3], v[164:167]\n" \
"s_mov_b32 s66, 0x800008\n" \
"ds_read_b128 a[212:215], v6 offset:2048\n" \
"s_mov_b32 s67, 0x80000800\n" \
"v_mfma_f32_16x16x32_bf16 v[164:167], a[108:111], a[4:7], v[164:167]\n" \
"ds_read_b128 a[216:219], v5 offset:4096\n" \
"v_cndmask_b32 v54, v54, v27, s[66:67]\n" \
"v_mfma_f32_16x16x32_bf16 v[172:175], a[104:107], a[8:11], v[172:175]\n" \
"s_add_u32 m0, s50, 69632\n" \
"ds_read_b128 a[220:223], v6 offset:4096\n" \
"v_mfma_f32_16x16x32_bf16 v[172:175], a[108:111], a[12:15], v[172:175]\n" \
"buffer_load_dwordx4 v14, s[28:31], s46 offen lds\n" \
"v_mfma_f32_16x16x32_bf16 v[180:183], a[104:107], a[16:19], v[180:183]\n" \
"ds_read_b128 a[224:227], v5 offset:6144\n" \
"v_permlane32_swap_b32 v23, v54\n" \
"v_mfma_f32_16x16x32_bf16 v[180:183], a[108:111], a[20:23], v[180:183]\n" \
"ds_read_b128 a[228:231], v6 offset:6144\n" \
"v_add_f32 v23, v23, v54\n" \
"v_mfma_f32_16x16x32_bf16 v[188:191], a[104:107], a[24:27], v[188:191]\n" \
"ds_read_b64_tr_b16 a[88:89], v11 offset:33280\n" \
"v_mov_b32 v54, v23\n" \
"v_mfma_f32_16x16x32_bf16 v[188:191], a[108:111], a[28:31], v[188:191]\n" \
"ds_read_b64_tr_b16 a[90:91], v12 offset:33280\n" \
"v_mfma_f32_16x16x32_bf16 v[64:67], a[64:67], a[0:3], v[64:67]\n" \
"ds_read_b64_tr_b16 a[92:93], v11 offset:41472\n" \
"v_permlane16_swap_b32 v23, v54\n" \
"v_mfma_f32_16x16x32_bf16 v[64:67], a[68:71], a[4:7], v[64:67]\n" \
"ds_read_b64_tr_b16 a[94:95], v12 offset:41472\n" \
"v_add_f32 v23, v23, v54\n" \
"v_mfma_f32_16x16x32_bf16 v[72:75], a[64:67], a[8:11], v[72:75]\n" \
"s_add_u32 m0, s50, 73728\n" \
"ds_read_b64_tr_b16 a[96:97], v9 offset:33792\n" \
"v_mfma_f32_16x16x32_bf16 v[72:75], a[68:71], a[12:15], v[72:75]\n" \
"buffer_load_dwordx4 v15, s[28:31], s46 offen lds\n" \
"v_mfma_f32_16x16x32_bf16 v[80:83], a[64:67], a[16:19], v[80:83]\n" \
"ds_read_b64_tr_b16 a[98:99], v10 offset:33792\n" \
"v_add_f32 v23, v23, v23 row_ror:8 row_mask:0xf bank_mask:0xf\n" \
"v_mfma_f32_16x16x32_bf16 v[80:83], a[68:71], a[20:23], v[80:83]\n" \
"ds_read_b64_tr_b16 a[100:101], v9 offset:41984\n" \
"v_mfma_f32_16x16x32_bf16 v[88:91], a[64:67], a[24:27], v[88:91]\n" \
"ds_read_b64_tr_b16 a[102:103], v10 offset:41984\n" \
"v_add_f32 v23, v23, v23 row_ror:4 row_mask:0xf bank_mask:0xf\n" \
"v_mfma_f32_16x16x32_bf16 v[88:91], a[68:71], a[28:31], v[88:91]\n" \
"ds_read_b64_tr_b16 a[104:105], v11 offset:33792\n" \
"v_mfma_f32_16x16x32_bf16 v[68:71], a[72:75], a[0:3], v[68:71]\n" \
"ds_read_b64_tr_b16 a[106:107], v12 offset:33792\n" \
"v_add_f32 v23, v23, v23 row_ror:2 row_mask:0xf bank_mask:0xf\n" \
"v_mfma_f32_16x16x32_bf16 v[68:71], a[76:79], a[4:7], v[68:71]\n" \
"ds_read_b64_tr_b16 a[108:109], v11 offset:41984\n" \
"v_mfma_f32_16x16x32_bf16 v[76:79], a[72:75], a[8:11], v[76:79]\n" \
"s_add_u32 m0, s50, 77824\n" \
"ds_read_b64_tr_b16 a[110:111], v12 offset:41984\n" \
"v_add_f32 v23, v23, v23 row_ror:1 row_mask:0xf bank_mask:0xf\n" \
"v_mfma_f32_16x16x32_bf16 v[76:79], a[76:79], a[12:15], v[76:79]\n" \
"buffer_load_dwordx4 v16, s[28:31], s46 offen lds\n" \
"v_mfma_f32_16x16x32_bf16 v[84:87], a[72:75], a[16:19], v[84:87]\n" \
"v_cvt_pk_bf16_f32 v23, v23, v23\n" \
"v_and_b32 v23, 0xffff0000, v23\n" \
"v_mfma_f32_16x16x32_bf16 v[84:87], a[76:79], a[20:23], v[84:87]\n" \
"buffer_store_dwordx4 v[232:235], v240, s[36:39], s59 offen offset:256 sc1\n" \
"v_mfma_f32_16x16x32_bf16 v[92:95], a[72:75], a[24:27], v[92:95]\n" \
"v_mul_f32 v23, 0xbfb8aa3b, v23\n" \
"v_mfma_f32_16x16x32_bf16 v[92:95], a[76:79], a[28:31], v[92:95]\n" \
"s_waitcnt lgkmcnt(12)\n" \
"v_exp_f32 v23, v23\n" \
"v_mfma_f32_16x16x32_bf16 v[96:99], a[80:83], a[200:203], v[96:99]\n" \
"s_mov_b32 s46, 768\n" \
"s_add_u32 m0, s50, 81920\n" \
"ds_read_b64_tr_b16 a[64:65], v9 offset:34304\n" \
"v_mfma_f32_16x16x32_bf16 v[96:99], a[84:87], a[204:207], v[96:99]\n" \
"buffer_load_dwordx4 v17, s[28:31], s46 offen lds\n" \
"v_mfma_f32_16x16x32_bf16 v[104:107], a[80:83], a[208:211], v[104:107]\n" \
"ds_read_b64_tr_b16 a[66:67], v10 offset:34304\n" \
"v_add_f32 v23, 1.0, v23\n" \
"v_mfma_f32_16x16x32_bf16 v[104:107], a[84:87], a[212:215], v[104:107]\n" \
"s_add_u32 s100, s100, s64\ns_addc_u32 s101, s101, 0\n" \
"ds_read_b64_tr_b16 a[68:69], v9 offset:42496\n" \
"v_mfma_f32_16x16x32_bf16 v[112:115], a[80:83], a[216:219], v[112:115]\n" \
"s_add_u32 m0, s50, 86016\n" \
"ds_read_b64_tr_b16 a[70:71], v10 offset:42496\n" \
"v_mfma_f32_16x16x32_bf16 v[112:115], a[84:87], a[220:223], v[112:115]\n" \
"buffer_load_dwordx4 v18, s[28:31], s46 offen lds\n" \
"v_mfma_f32_16x16x32_bf16 v[120:123], a[80:83], a[224:227], v[120:123]\n" \
"s_waitcnt lgkmcnt(12)\n" \
"ds_read_b64_tr_b16 a[72:73], v11 offset:34304\n" \
"v_mfma_f32_16x16x32_bf16 v[120:123], a[84:87], a[228:231], v[120:123]\n" \
"v_rcp_f32 v23, v23\n" \
"v_mfma_f32_16x16x32_bf16 v[100:103], a[88:91], a[200:203], v[100:103]\n" \
"s_add_u32 m0, s50, 90112\n" \
"ds_read_b64_tr_b16 a[74:75], v12 offset:34304\n" \
"v_mfma_f32_16x16x32_bf16 v[100:103], a[92:95], a[204:207], v[100:103]\n" \
"buffer_load_dwordx4 v19, s[28:31], s46 offen lds\n" \
"v_mfma_f32_16x16x32_bf16 v[108:111], a[88:91], a[208:211], v[108:111]\n" \
"ds_read_b64_tr_b16 a[76:77], v11 offset:42496\n" \
"v_cvt_pk_bf16_f32 v23, v23, v23\n" \
"v_mfma_f32_16x16x32_bf16 v[108:111], a[92:95], a[212:215], v[108:111]\n" \
"ds_read_b64_tr_b16 a[78:79], v12 offset:42496\n" \
"v_and_b32 v23, 0xffff0000, v23\n" \
"v_mfma_f32_16x16x32_bf16 v[116:119], a[88:91], a[216:219], v[116:119]\n" \
"s_add_u32 m0, s50, 94208\n" \
"ds_read_b64_tr_b16 a[80:81], v9 offset:32768\n" \
"v_mfma_f32_16x16x32_bf16 v[116:119], a[92:95], a[220:223], v[116:119]\n" \
"buffer_load_dwordx4 v20, s[28:31], s46 offen lds\n" \
"v_mfma_f32_16x16x32_bf16 v[124:127], a[88:91], a[224:227], v[124:127]\n" \
"s_waitcnt lgkmcnt(13)\n" \
"ds_read_b64_tr_b16 a[82:83], v10 offset:32768\n" \
"v_mfma_f32_16x16x32_bf16 v[124:127], a[92:95], a[228:231], v[124:127]\n" \
"ds_read_b64_tr_b16 a[84:85], v9 offset:40960\n" \
"v_mfma_f32_16x16x32_bf16 v[128:131], a[96:99], a[200:203], v[128:131]\n" \
"s_mov_b32 s47, 3670016\n" \
"s_add_u32 m0, s51, 16384\n" \
"ds_read_b64_tr_b16 a[86:87], v10 offset:40960\n" \
"v_mfma_f32_16x16x32_bf16 v[128:131], a[100:103], a[204:207], v[128:131]\n" \
"buffer_load_dwordx4 v21, s[32:35], s47 offen offset:0 lds\n" \
"v_mfma_f32_16x16x32_bf16 v[136:139], a[96:99], a[208:211], v[136:139]\n" \
"ds_read_b64_tr_b16 a[88:89], v11 offset:32768\n" \
"v_mfma_f32_16x16x32_bf16 v[136:139], a[100:103], a[212:215], v[136:139]\n" \
"buffer_store_dword v23, v61, s[36:39], 0 offen sc1\n" \
"v_mfma_f32_16x16x32_bf16 v[144:147], a[96:99], a[216:219], v[144:147]\n" \
"s_add_u32 m0, s51, 17280\n" \
"ds_read_b64_tr_b16 a[90:91], v12 offset:32768\n" \
"v_mfma_f32_16x16x32_bf16 v[144:147], a[100:103], a[220:223], v[144:147]\n" \
"buffer_load_dwordx4 v21, s[32:35], s47 offen offset:128 lds\n" \
"v_mfma_f32_16x16x32_bf16 v[152:155], a[96:99], a[224:227], v[152:155]\n" \
"s_waitcnt lgkmcnt(14)\n" \
"ds_read_b64_tr_b16 a[92:93], v11 offset:40960\n" \
"v_mfma_f32_16x16x32_bf16 v[152:155], a[100:103], a[228:231], v[152:155]\n" \
"ds_read_b64_tr_b16 a[94:95], v12 offset:40960\n" \
"v_mfma_f32_16x16x32_bf16 v[132:135], a[104:107], a[200:203], v[132:135]\n" \
"s_mov_b32 s47, 3932160\n" \
"s_add_u32 m0, s51, 24576\n" \
"v_mfma_f32_16x16x32_bf16 v[132:135], a[108:111], a[204:207], v[132:135]\n" \
"buffer_load_dwordx4 v21, s[32:35], s47 offen offset:0 lds\n" \
"v_mfma_f32_16x16x32_bf16 v[140:143], a[104:107], a[208:211], v[140:143]\n" \
"v_mfma_f32_16x16x32_bf16 v[140:143], a[108:111], a[212:215], v[140:143]\n" \
"buffer_store_dwordx4 v[236:239], v240, s[36:39], s59 offen offset:384 sc1\n" \
"v_mfma_f32_16x16x32_bf16 v[148:151], a[104:107], a[216:219], v[148:151]\n" \
"s_add_u32 m0, s51, 25472\n" \
"s_mov_b64 s[40:41], s[4:5]\n" \
"s_and_b32 s41, s41, 0xffff\n" \
"v_mfma_f32_16x16x32_bf16 v[148:151], a[108:111], a[220:223], v[148:151]\n" \
"buffer_load_dwordx4 v21, s[32:35], s47 offen offset:128 lds\n" \
"v_mfma_f32_16x16x32_bf16 v[156:159], a[104:107], a[224:227], v[156:159]\n" \
"s_waitcnt vmcnt(19) lgkmcnt(0)\n" \
"s_lshl_b32 s62, s6, 20\n" \
"s_add_u32 s16, s62, 33554432\n" \
"v_mfma_f32_16x16x32_bf16 v[156:159], a[108:111], a[228:231], v[156:159]\n" \
"s_barrier\n" \
"s_lshl_b32 s63, s7, 16\n" \
"s_sub_u32 s63, 1048576, s63\n" \
"v_mfma_f32_16x16x32_bf16 v[160:163], a[64:67], a[200:203], v[160:163]\n" \
"s_add_u32 s42, s16, s63\n" \
"ds_read_b64_tr_b16 a[96:97], v9 offset:49664\n" \
"v_mfma_f32_16x16x32_bf16 v[160:163], a[68:71], a[204:207], v[160:163]\n" \
"ds_read_b64_tr_b16 a[98:99], v10 offset:49664\n" \
"v_mfma_f32_16x16x32_bf16 v[168:171], a[64:67], a[208:211], v[168:171]\n" \
"s_mov_b32 s46, 896\n" \
"s_add_u32 m0, s50, 0\n" \
"ds_read_b64_tr_b16 a[100:101], v9 offset:57856\n" \
"v_mfma_f32_16x16x32_bf16 v[168:171], a[68:71], a[212:215], v[168:171]\n" \
"buffer_load_dwordx4 v13, s[28:31], s46 offen lds\n" \
"v_mfma_f32_16x16x32_bf16 v[176:179], a[64:67], a[216:219], v[176:179]\n" \
"ds_read_b64_tr_b16 a[102:103], v10 offset:57856\n" \
"v_mfma_f32_16x16x32_bf16 v[176:179], a[68:71], a[220:223], v[176:179]\n" \
"ds_read_b128 a[0:3], v5 offset:32768\n" \
"v_mfma_f32_16x16x32_bf16 v[184:187], a[64:67], a[224:227], v[184:187]\n" \
"ds_read_b128 a[4:7], v6 offset:32768\n" \
"v_mfma_f32_16x16x32_bf16 v[184:187], a[68:71], a[228:231], v[184:187]\n" \
"ds_read_b128 a[8:11], v5 offset:34816\n" \
"v_mfma_f32_16x16x32_bf16 v[164:167], a[72:75], a[200:203], v[164:167]\n" \
"ds_read_b128 a[12:15], v6 offset:34816\n" \
"v_mfma_f32_16x16x32_bf16 v[164:167], a[76:79], a[204:207], v[164:167]\n" \
"ds_read_b128 a[16:19], v5 offset:36864\n" \
"v_mfma_f32_16x16x32_bf16 v[172:175], a[72:75], a[208:211], v[172:175]\n" \
"s_add_u32 m0, s50, 4096\n" \
"ds_read_b128 a[20:23], v6 offset:36864\n" \
"v_mfma_f32_16x16x32_bf16 v[172:175], a[76:79], a[212:215], v[172:175]\n" \
"buffer_load_dwordx4 v14, s[28:31], s46 offen lds\n" \
"v_mfma_f32_16x16x32_bf16 v[180:183], a[72:75], a[216:219], v[180:183]\n" \
"ds_read_b128 a[24:27], v5 offset:38912\n" \
"v_mfma_f32_16x16x32_bf16 v[180:183], a[76:79], a[220:223], v[180:183]\n" \
"ds_read_b128 a[28:31], v6 offset:38912\n" \
"v_mfma_f32_16x16x32_bf16 v[188:191], a[72:75], a[224:227], v[188:191]\n" \
"ds_read_b64_tr_b16 a[104:105], v11 offset:49664\n" \
"v_mfma_f32_16x16x32_bf16 v[188:191], a[76:79], a[228:231], v[188:191]\n" \
"ds_read_b64_tr_b16 a[106:107], v12 offset:49664\n" \
"v_mfma_f32_16x16x32_bf16 v[64:67], a[80:83], a[200:203], v[64:67]\n" \
"ds_read_b64_tr_b16 a[108:109], v11 offset:57856\n" \
"v_mfma_f32_16x16x32_bf16 v[64:67], a[84:87], a[204:207], v[64:67]\n" \
"ds_read_b64_tr_b16 a[110:111], v12 offset:57856\n" \
"v_mfma_f32_16x16x32_bf16 v[72:75], a[80:83], a[208:211], v[72:75]\n" \
"s_add_u32 m0, s50, 8192\n" \
"ds_read_b64_tr_b16 a[64:65], v9 offset:50176\n" \
"v_mfma_f32_16x16x32_bf16 v[72:75], a[84:87], a[212:215], v[72:75]\n" \
"buffer_load_dwordx4 v15, s[28:31], s46 offen lds\n" \
"v_mfma_f32_16x16x32_bf16 v[80:83], a[80:83], a[216:219], v[80:83]\n" \
"ds_read_b64_tr_b16 a[66:67], v10 offset:50176\n" \
"v_mfma_f32_16x16x32_bf16 v[80:83], a[84:87], a[220:223], v[80:83]\n" \
"ds_read_b64_tr_b16 a[68:69], v9 offset:58368\n" \
"v_mfma_f32_16x16x32_bf16 v[88:91], a[80:83], a[224:227], v[88:91]\n" \
"ds_read_b64_tr_b16 a[70:71], v10 offset:58368\n" \
"v_mfma_f32_16x16x32_bf16 v[88:91], a[84:87], a[228:231], v[88:91]\n" \
"ds_read_b64_tr_b16 a[72:73], v11 offset:50176\n" \
"v_mfma_f32_16x16x32_bf16 v[68:71], a[88:91], a[200:203], v[68:71]\n" \
"ds_read_b64_tr_b16 a[74:75], v12 offset:50176\n" \
"v_mfma_f32_16x16x32_bf16 v[68:71], a[92:95], a[204:207], v[68:71]\n" \
"ds_read_b64_tr_b16 a[76:77], v11 offset:58368\n" \
"v_mfma_f32_16x16x32_bf16 v[76:79], a[88:91], a[208:211], v[76:79]\n" \
"s_add_u32 m0, s50, 12288\n" \
"ds_read_b64_tr_b16 a[78:79], v12 offset:58368\n" \
"v_mfma_f32_16x16x32_bf16 v[76:79], a[92:95], a[212:215], v[76:79]\n" \
"buffer_load_dwordx4 v16, s[28:31], s46 offen lds\n" \
"v_mfma_f32_16x16x32_bf16 v[84:87], a[88:91], a[216:219], v[84:87]\n" \
"s_cmp_lt_u32 %2, 2\ns_cselect_b32 s62, 1064960, 0\ns_cselect_b32 s9, -1, 1\n" \
"v_mfma_f32_16x16x32_bf16 v[84:87], a[92:95], a[220:223], v[84:87]\n" \
"buffer_store_dwordx4 v[34:37], v4, s[36:39], s97 offen offset:0 sc1\n" \
"v_mfma_f32_16x16x32_bf16 v[92:95], a[88:91], a[224:227], v[92:95]\n" \
"s_add_u32 s96, s96, s92\n" \
"s_add_u32 s16, s16, s62\n" \
"s_add_u32 s17, s16, 8192\n" \
"v_mfma_f32_16x16x32_bf16 v[92:95], a[92:95], a[228:231], v[92:95]\n" \
"s_waitcnt lgkmcnt(12)\n" \
"s_lshl_b32 s9, s9, 14\n" \
"s_mov_b32 s19, s9\n" \
"v_mfma_f32_16x16x32_bf16 v[96:99], a[96:99], a[0:3], v[96:99]\n" \
"s_mov_b32 s46, 896\n" \
"s_add_u32 m0, s50, 16384\n" \
"ds_read_b64_tr_b16 a[80:81], v9 offset:50688\n" \
"v_mfma_f32_16x16x32_bf16 v[96:99], a[100:103], a[4:7], v[96:99]\n" \
"buffer_load_dwordx4 v17, s[28:31], s46 offen lds\n" \
"v_mfma_f32_16x16x32_bf16 v[104:107], a[96:99], a[8:11], v[104:107]\n" \
"s_and_b32 s63, %2, 1\n" \
"ds_read_b64_tr_b16 a[82:83], v10 offset:50688\n" \
"s_lshl_b32 s63, s63, 12\n" \
"v_mfma_f32_16x16x32_bf16 v[104:107], a[100:103], a[12:15], v[104:107]\n" \
"s_lshr_b32 s62, s6, 1\n" \
"ds_read_b64_tr_b16 a[84:85], v9 offset:58880\n" \
"v_add_u32 v60, s63, v4\n" \
"v_mfma_f32_16x16x32_bf16 v[112:115], a[96:99], a[16:19], v[112:115]\n" \
"s_add_u32 m0, s50, 20480\n" \
"ds_read_b64_tr_b16 a[86:87], v10 offset:58880\n" \
"s_lshl_b32 s62, s62, 7\n" \
"v_mfma_f32_16x16x32_bf16 v[112:115], a[100:103], a[20:23], v[112:115]\n" \
"buffer_load_dwordx4 v18, s[28:31], s46 offen lds\n" \
"v_mfma_f32_16x16x32_bf16 v[120:123], a[96:99], a[24:27], v[120:123]\n" \
"s_waitcnt lgkmcnt(12)\n" \
"ds_read_b64_tr_b16 a[88:89], v11 offset:50688\n" \
"s_add_u32 s62, s62, 94375936\n" \
"v_mfma_f32_16x16x32_bf16 v[120:123], a[100:103], a[28:31], v[120:123]\n" \
"v_bfe_u32 v57, v4, 4, 4\n" \
"v_lshl_add_u32 v57, v57, 3, s62\n" \
"v_mfma_f32_16x16x32_bf16 v[100:103], a[104:107], a[0:3], v[100:103]\n" \
"s_add_u32 m0, s50, 24576\n" \
"ds_read_b64_tr_b16 a[90:91], v12 offset:50688\n" \
"v_mfma_f32_16x16x32_bf16 v[100:103], a[108:111], a[4:7], v[100:103]\n" \
"buffer_load_dwordx4 v19, s[28:31], s46 offen lds\n" \
"v_mfma_f32_16x16x32_bf16 v[108:111], a[104:107], a[8:11], v[108:111]\n" \
"ds_read_b64_tr_b16 a[92:93], v11 offset:58880\n" \
"v_mfma_f32_16x16x32_bf16 v[108:111], a[108:111], a[12:15], v[108:111]\n" \
"ds_read_b64_tr_b16 a[94:95], v12 offset:58880\n" \
"v_mfma_f32_16x16x32_bf16 v[116:119], a[104:107], a[16:19], v[116:119]\n" \
"s_add_u32 m0, s50, 28672\n" \
"ds_read_b64_tr_b16 a[96:97], v9 offset:49152\n" \
"v_mfma_f32_16x16x32_bf16 v[116:119], a[108:111], a[20:23], v[116:119]\n" \
"buffer_load_dwordx4 v20, s[28:31], s46 offen lds\n" \
"v_mfma_f32_16x16x32_bf16 v[124:127], a[104:107], a[24:27], v[124:127]\n" \
"s_waitcnt lgkmcnt(13)\n" \
"ds_read_b64_tr_b16 a[98:99], v10 offset:49152\n" \
"v_mfma_f32_16x16x32_bf16 v[124:127], a[108:111], a[28:31], v[124:127]\n" \
"ds_read_b64_tr_b16 a[100:101], v9 offset:57344\n" \
"v_mfma_f32_16x16x32_bf16 v[128:131], a[64:67], a[0:3], v[128:131]\n" \
"s_mov_b32 s47, 4194304\n" \
"s_add_u32 m0, s51, 32768\n" \
"ds_read_b64_tr_b16 a[102:103], v10 offset:57344\n" \
"v_mfma_f32_16x16x32_bf16 v[128:131], a[68:71], a[4:7], v[128:131]\n" \
"buffer_load_dwordx4 v21, s[32:35], s47 offen offset:0 lds\n" \
"v_mfma_f32_16x16x32_bf16 v[136:139], a[64:67], a[8:11], v[136:139]\n" \
"ds_read_b64_tr_b16 a[104:105], v11 offset:49152\n" \
"v_mfma_f32_16x16x32_bf16 v[136:139], a[68:71], a[12:15], v[136:139]\n" \
"v_mfma_f32_16x16x32_bf16 v[144:147], a[64:67], a[16:19], v[144:147]\n" \
"s_add_u32 m0, s51, 33664\n" \
"ds_read_b64_tr_b16 a[106:107], v12 offset:49152\n" \
"v_mfma_f32_16x16x32_bf16 v[144:147], a[68:71], a[20:23], v[144:147]\n" \
"buffer_load_dwordx4 v21, s[32:35], s47 offen offset:128 lds\n" \
"v_mfma_f32_16x16x32_bf16 v[152:155], a[64:67], a[24:27], v[152:155]\n" \
"s_waitcnt lgkmcnt(14)\n" \
"ds_read_b64_tr_b16 a[108:109], v11 offset:57344\n" \
"v_mfma_f32_16x16x32_bf16 v[152:155], a[68:71], a[28:31], v[152:155]\n" \
"ds_read_b64_tr_b16 a[110:111], v12 offset:57344\n" \
"v_mfma_f32_16x16x32_bf16 v[132:135], a[72:75], a[0:3], v[132:135]\n" \
"s_mov_b32 s47, 4456448\n" \
"s_add_u32 m0, s51, 40960\n" \
"v_mfma_f32_16x16x32_bf16 v[132:135], a[76:79], a[4:7], v[132:135]\n" \
"buffer_load_dwordx4 v21, s[32:35], s47 offen offset:0 lds\n" \
"v_mfma_f32_16x16x32_bf16 v[140:143], a[72:75], a[8:11], v[140:143]\n" \
"v_mfma_f32_16x16x32_bf16 v[140:143], a[76:79], a[12:15], v[140:143]\n" \
"buffer_store_dwordx4 v[38:41], v4, s[36:39], s96 offen offset:0 sc1\n" \
"v_mfma_f32_16x16x32_bf16 v[148:151], a[72:75], a[16:19], v[148:151]\n" \
"s_add_u32 m0, s51, 41856\n" \
"s_add_u32 s97, s96, s98\n" \
"v_mfma_f32_16x16x32_bf16 v[148:151], a[76:79], a[20:23], v[148:151]\n" \
"buffer_load_dwordx4 v21, s[32:35], s47 offen offset:128 lds\n" \
"v_mfma_f32_16x16x32_bf16 v[156:159], a[72:75], a[24:27], v[156:159]\n" \
"s_waitcnt vmcnt(20) lgkmcnt(0)\n" \
"v_mfma_f32_16x16x32_bf16 v[156:159], a[76:79], a[28:31], v[156:159]\n" \
"s_barrier\n" \
"v_mfma_f32_16x16x32_bf16 v[160:163], a[80:83], a[0:3], v[160:163]\n" \
"ds_read_b64_tr_b16 a[64:65], v9 offset:512\n" \
"v_mfma_f32_16x16x32_bf16 v[160:163], a[84:87], a[4:7], v[160:163]\n" \
"ds_read_b64_tr_b16 a[66:67], v10 offset:512\n" \
"v_mfma_f32_16x16x32_bf16 v[168:171], a[80:83], a[8:11], v[168:171]\n" \
"s_mov_b32 s46, 1024\n" \
"s_add_u32 m0, s50, 32768\n" \
"ds_read_b64_tr_b16 a[68:69], v9 offset:8704\n" \
"v_mfma_f32_16x16x32_bf16 v[168:171], a[84:87], a[12:15], v[168:171]\n" \
"buffer_load_dwordx4 v13, s[28:31], s46 offen lds\n" \
"v_mfma_f32_16x16x32_bf16 v[176:179], a[80:83], a[16:19], v[176:179]\n" \
"ds_read_b64_tr_b16 a[70:71], v10 offset:8704\n" \
"v_mfma_f32_16x16x32_bf16 v[176:179], a[84:87], a[20:23], v[176:179]\n" \
"ds_read_b128 a[200:203], v7 offset:0\n" \
"v_mfma_f32_16x16x32_bf16 v[184:187], a[80:83], a[24:27], v[184:187]\n" \
"ds_read_b128 a[204:207], v8 offset:0\n" \
"v_mfma_f32_16x16x32_bf16 v[184:187], a[84:87], a[28:31], v[184:187]\n" \
"ds_read_b128 a[208:211], v7 offset:2048\n" \
"v_mfma_f32_16x16x32_bf16 v[164:167], a[88:91], a[0:3], v[164:167]\n" \
"ds_read_b128 a[212:215], v8 offset:2048\n" \
"v_mfma_f32_16x16x32_bf16 v[164:167], a[92:95], a[4:7], v[164:167]\n" \
"ds_read_b128 a[216:219], v7 offset:4096\n" \
"v_mfma_f32_16x16x32_bf16 v[172:175], a[88:91], a[8:11], v[172:175]\n" \
"s_add_u32 m0, s50, 36864\n" \
"ds_read_b128 a[220:223], v8 offset:4096\n" \
"v_mfma_f32_16x16x32_bf16 v[172:175], a[92:95], a[12:15], v[172:175]\n" \
"buffer_load_dwordx4 v14, s[28:31], s46 offen lds\n" \
"v_mfma_f32_16x16x32_bf16 v[180:183], a[88:91], a[16:19], v[180:183]\n" \
"LH11_%=:\ns_cbranch_execz LH15_%=\n" \
"ds_read_b128 a[224:227], v7 offset:6144\n" \
"v_mfma_f32_16x16x32_bf16 v[180:183], a[92:95], a[20:23], v[180:183]\n" \
"ds_read_b128 a[228:231], v8 offset:6144\n" \
"v_mfma_f32_16x16x32_bf16 v[188:191], a[88:91], a[24:27], v[188:191]\n" \
"ds_read_b64_tr_b16 a[72:73], v11 offset:512\n" \
"v_mfma_f32_16x16x32_bf16 v[188:191], a[92:95], a[28:31], v[188:191]\n" \
"ds_read_b64_tr_b16 a[74:75], v12 offset:512\n" \
"v_mfma_f32_16x16x32_bf16 v[64:67], a[96:99], a[0:3], v[64:67]\n" \
"ds_read_b64_tr_b16 a[76:77], v11 offset:8704\n" \
"v_mfma_f32_16x16x32_bf16 v[64:67], a[100:103], a[4:7], v[64:67]\n" \
"ds_read_b64_tr_b16 a[78:79], v12 offset:8704\n" \
"v_mfma_f32_16x16x32_bf16 v[72:75], a[96:99], a[8:11], v[72:75]\n" \
"s_add_u32 m0, s50, 40960\n" \
"ds_read_b64_tr_b16 a[80:81], v9 offset:1024\n" \
"v_mfma_f32_16x16x32_bf16 v[72:75], a[100:103], a[12:15], v[72:75]\n" \
"buffer_load_dwordx4 v15, s[28:31], s46 offen lds\n" \
"v_mfma_f32_16x16x32_bf16 v[80:83], a[96:99], a[16:19], v[80:83]\n" \
"ds_read_b64_tr_b16 a[82:83], v10 offset:1024\n" \
"v_mfma_f32_16x16x32_bf16 v[80:83], a[100:103], a[20:23], v[80:83]\n" \
"ds_read_b64_tr_b16 a[84:85], v9 offset:9216\n" \
"v_mfma_f32_16x16x32_bf16 v[88:91], a[96:99], a[24:27], v[88:91]\n" \
"ds_read_b64_tr_b16 a[86:87], v10 offset:9216\n" \
"v_mfma_f32_16x16x32_bf16 v[88:91], a[100:103], a[28:31], v[88:91]\n" \
"ds_read_b64_tr_b16 a[88:89], v11 offset:1024\n" \
"v_mfma_f32_16x16x32_bf16 v[68:71], a[104:107], a[0:3], v[68:71]\n" \
"ds_read_b64_tr_b16 a[90:91], v12 offset:1024\n" \
"v_mfma_f32_16x16x32_bf16 v[68:71], a[108:111], a[4:7], v[68:71]\n" \
"ds_read_b64_tr_b16 a[92:93], v11 offset:9216\n" \
"v_mfma_f32_16x16x32_bf16 v[76:79], a[104:107], a[8:11], v[76:79]\n" \
"s_add_u32 m0, s50, 45056\n" \
"ds_read_b64_tr_b16 a[94:95], v12 offset:9216\n" \
"v_mfma_f32_16x16x32_bf16 v[76:79], a[108:111], a[12:15], v[76:79]\n" \
"buffer_load_dwordx4 v16, s[28:31], s46 offen lds\n" \
"v_mfma_f32_16x16x32_bf16 v[84:87], a[104:107], a[16:19], v[84:87]\n" \
"v_mfma_f32_16x16x32_bf16 v[84:87], a[108:111], a[20:23], v[84:87]\n" \
"buffer_store_dwordx4 v[42:45], v4, s[36:39], s97 offen offset:0 sc1\n" \
"v_mfma_f32_16x16x32_bf16 v[92:95], a[104:107], a[24:27], v[92:95]\n" \
"v_mfma_f32_16x16x32_bf16 v[92:95], a[108:111], a[28:31], v[92:95]\n" \
"s_waitcnt lgkmcnt(12)\n" \
"v_mfma_f32_16x16x32_bf16 v[96:99], a[64:67], a[200:203], v[96:99]\n" \
"s_mov_b32 s46, 1024\n" \
"s_add_u32 m0, s50, 49152\n" \
"ds_read_b64_tr_b16 a[96:97], v9 offset:1536\n" \
"v_mfma_f32_16x16x32_bf16 v[96:99], a[68:71], a[204:207], v[96:99]\n" \
"buffer_load_dwordx4 v17, s[28:31], s46 offen lds\n" \
"v_mfma_f32_16x16x32_bf16 v[104:107], a[64:67], a[208:211], v[104:107]\n" \
"ds_read_b64_tr_b16 a[98:99], v10 offset:1536\n" \
"v_mfma_f32_16x16x32_bf16 v[104:107], a[68:71], a[212:215], v[104:107]\n" \
"ds_read_b64_tr_b16 a[100:101], v9 offset:9728\n" \
"v_mfma_f32_16x16x32_bf16 v[112:115], a[64:67], a[216:219], v[112:115]\n" \
"s_add_u32 m0, s50, 53248\n" \
"ds_read_b64_tr_b16 a[102:103], v10 offset:9728\n" \
"v_mfma_f32_16x16x32_bf16 v[112:115], a[68:71], a[220:223], v[112:115]\n" \
"buffer_load_dwordx4 v18, s[28:31], s46 offen lds\n" \
"v_mfma_f32_16x16x32_bf16 v[120:123], a[64:67], a[224:227], v[120:123]\n" \
"s_waitcnt lgkmcnt(12)\n" \
"ds_read_b64_tr_b16 a[104:105], v11 offset:1536\n" \
"v_mfma_f32_16x16x32_bf16 v[120:123], a[68:71], a[228:231], v[120:123]\n" \
"v_mfma_f32_16x16x32_bf16 v[100:103], a[72:75], a[200:203], v[100:103]\n" \
"s_add_u32 m0, s50, 57344\n" \
"ds_read_b64_tr_b16 a[106:107], v12 offset:1536\n" \
"v_mfma_f32_16x16x32_bf16 v[100:103], a[76:79], a[204:207], v[100:103]\n" \
"buffer_load_dwordx4 v19, s[28:31], s46 offen lds\n" \
"v_mfma_f32_16x16x32_bf16 v[108:111], a[72:75], a[208:211], v[108:111]\n" \
"ds_read_b64_tr_b16 a[108:109], v11 offset:9728\n" \
"v_mfma_f32_16x16x32_bf16 v[108:111], a[76:79], a[212:215], v[108:111]\n" \
"ds_read_b64_tr_b16 a[110:111], v12 offset:9728\n" \
"v_mfma_f32_16x16x32_bf16 v[116:119], a[72:75], a[216:219], v[116:119]\n" \
"s_add_u32 m0, s50, 61440\n" \
"ds_read_b64_tr_b16 a[64:65], v9 offset:0\n" \
"v_mfma_f32_16x16x32_bf16 v[116:119], a[76:79], a[220:223], v[116:119]\n" \
"buffer_load_dwordx4 v20, s[28:31], s46 offen lds\n" \
"v_mfma_f32_16x16x32_bf16 v[124:127], a[72:75], a[224:227], v[124:127]\n" \
"s_waitcnt lgkmcnt(13)\n" \
"ds_read_b64_tr_b16 a[66:67], v10 offset:0\n" \
"v_mfma_f32_16x16x32_bf16 v[124:127], a[76:79], a[228:231], v[124:127]\n" \
"ds_read_b64_tr_b16 a[68:69], v9 offset:8192\n" \
"v_mfma_f32_16x16x32_bf16 v[128:131], a[80:83], a[200:203], v[128:131]\n" \
"s_mov_b32 s47, 4718592\n" \
"s_add_u32 m0, s51, 49152\n" \
"ds_read_b64_tr_b16 a[70:71], v10 offset:8192\n" \
"v_mfma_f32_16x16x32_bf16 v[128:131], a[84:87], a[204:207], v[128:131]\n" \
"buffer_load_dwordx4 v21, s[32:35], s47 offen offset:0 lds\n" \
"v_mfma_f32_16x16x32_bf16 v[136:139], a[80:83], a[208:211], v[136:139]\n" \
"ds_read_b64_tr_b16 a[72:73], v11 offset:0\n" \
"v_mfma_f32_16x16x32_bf16 v[136:139], a[84:87], a[212:215], v[136:139]\n" \
"v_mfma_f32_16x16x32_bf16 v[144:147], a[80:83], a[216:219], v[144:147]\n" \
"s_add_u32 m0, s51, 50048\n" \
"ds_read_b64_tr_b16 a[74:75], v12 offset:0\n" \
"v_mfma_f32_16x16x32_bf16 v[144:147], a[84:87], a[220:223], v[144:147]\n" \
"buffer_load_dwordx4 v21, s[32:35], s47 offen offset:128 lds\n" \
"v_mfma_f32_16x16x32_bf16 v[152:155], a[80:83], a[224:227], v[152:155]\n" \
"s_waitcnt lgkmcnt(14)\n" \
"ds_read_b64_tr_b16 a[76:77], v11 offset:8192\n" \
"v_mfma_f32_16x16x32_bf16 v[152:155], a[84:87], a[228:231], v[152:155]\n" \
"ds_read_b64_tr_b16 a[78:79], v12 offset:8192\n" \
"v_mfma_f32_16x16x32_bf16 v[132:135], a[88:91], a[200:203], v[132:135]\n" \
"s_mov_b32 s47, 4980736\n" \
"s_add_u32 m0, s51, 57344\n" \
"v_mfma_f32_16x16x32_bf16 v[132:135], a[92:95], a[204:207], v[132:135]\n" \
"buffer_load_dwordx4 v21, s[32:35], s47 offen offset:0 lds\n" \
"v_mfma_f32_16x16x32_bf16 v[140:143], a[88:91], a[208:211], v[140:143]\n" \
"s_add_u32 s76, s76, 2097152\ns_addc_u32 s77, s77, 0\n" \
"v_mfma_f32_16x16x32_bf16 v[140:143], a[92:95], a[212:215], v[140:143]\n" \
"v_mfma_f32_16x16x32_bf16 v[148:151], a[88:91], a[216:219], v[148:151]\n" \
"s_add_u32 m0, s51, 58240\n" \
"v_mfma_f32_16x16x32_bf16 v[148:151], a[92:95], a[220:223], v[148:151]\n" \
"buffer_load_dwordx4 v21, s[32:35], s47 offen offset:128 lds\n" \
"v_mfma_f32_16x16x32_bf16 v[156:159], a[88:91], a[224:227], v[156:159]\n" \
"s_waitcnt vmcnt(18) lgkmcnt(0)\n" \
"v_mfma_f32_16x16x32_bf16 v[156:159], a[92:95], a[228:231], v[156:159]\n" \
"s_barrier\n" \
"v_mfma_f32_16x16x32_bf16 v[160:163], a[96:99], a[200:203], v[160:163]\n" \
"ds_read_b64_tr_b16 a[80:81], v9 offset:16896\n" \
"v_mfma_f32_16x16x32_bf16 v[160:163], a[100:103], a[204:207], v[160:163]\n" \
"ds_read_b64_tr_b16 a[82:83], v10 offset:16896\n" \
"v_mfma_f32_16x16x32_bf16 v[168:171], a[96:99], a[208:211], v[168:171]\n" \
"s_mov_b32 s46, 1152\n" \
"s_add_u32 m0, s50, 65536\n" \
"ds_read_b64_tr_b16 a[84:85], v9 offset:25088\n" \
"v_mfma_f32_16x16x32_bf16 v[168:171], a[100:103], a[212:215], v[168:171]\n" \
"buffer_load_dwordx4 v13, s[28:31], s46 offen lds\n" \
"v_mfma_f32_16x16x32_bf16 v[176:179], a[96:99], a[216:219], v[176:179]\n" \
"ds_read_b64_tr_b16 a[86:87], v10 offset:25088\n" \
"v_mfma_f32_16x16x32_bf16 v[176:179], a[100:103], a[220:223], v[176:179]\n" \
"ds_read_b128 a[0:3], v5 offset:0\n" \
"v_mfma_f32_16x16x32_bf16 v[184:187], a[96:99], a[224:227], v[184:187]\n" \
"ds_read_b128 a[4:7], v6 offset:0\n" \
"v_mfma_f32_16x16x32_bf16 v[184:187], a[100:103], a[228:231], v[184:187]\n" \
"ds_read_b128 a[8:11], v5 offset:2048\n" \
"v_mfma_f32_16x16x32_bf16 v[164:167], a[104:107], a[200:203], v[164:167]\n" \
"ds_read_b128 a[12:15], v6 offset:2048\n" \
"v_mfma_f32_16x16x32_bf16 v[164:167], a[108:111], a[204:207], v[164:167]\n" \
"ds_read_b128 a[16:19], v5 offset:4096\n" \
"v_mfma_f32_16x16x32_bf16 v[172:175], a[104:107], a[208:211], v[172:175]\n" \
"s_add_u32 m0, s50, 69632\n" \
"ds_read_b128 a[20:23], v6 offset:4096\n" \
"v_mfma_f32_16x16x32_bf16 v[172:175], a[108:111], a[212:215], v[172:175]\n" \
"buffer_load_dwordx4 v14, s[28:31], s46 offen lds\n" \
"v_mfma_f32_16x16x32_bf16 v[180:183], a[104:107], a[216:219], v[180:183]\n" \
"ds_read_b128 a[24:27], v5 offset:6144\n" \
"v_mfma_f32_16x16x32_bf16 v[180:183], a[108:111], a[220:223], v[180:183]\n" \
"ds_read_b128 a[28:31], v6 offset:6144\n" \
"v_mfma_f32_16x16x32_bf16 v[188:191], a[104:107], a[224:227], v[188:191]\n" \
"ds_read_b64_tr_b16 a[88:89], v11 offset:16896\n" \
"v_mfma_f32_16x16x32_bf16 v[188:191], a[108:111], a[228:231], v[188:191]\n" \
"ds_read_b64_tr_b16 a[90:91], v12 offset:16896\n" \
"v_mfma_f32_16x16x32_bf16 v[64:67], a[64:67], a[200:203], v[64:67]\n" \
"ds_read_b64_tr_b16 a[92:93], v11 offset:25088\n" \
"v_mfma_f32_16x16x32_bf16 v[64:67], a[68:71], a[204:207], v[64:67]\n" \
"ds_read_b64_tr_b16 a[94:95], v12 offset:25088\n" \
"v_mfma_f32_16x16x32_bf16 v[72:75], a[64:67], a[208:211], v[72:75]\n" \
"s_add_u32 m0, s50, 73728\n" \
"ds_read_b64_tr_b16 a[96:97], v9 offset:17408\n" \
"v_mfma_f32_16x16x32_bf16 v[72:75], a[68:71], a[212:215], v[72:75]\n" \
"buffer_load_dwordx4 v15, s[28:31], s46 offen lds\n" \
"v_mfma_f32_16x16x32_bf16 v[80:83], a[64:67], a[216:219], v[80:83]\n" \
"ds_read_b64_tr_b16 a[98:99], v10 offset:17408\n" \
"v_mfma_f32_16x16x32_bf16 v[80:83], a[68:71], a[220:223], v[80:83]\n" \
"ds_read_b64_tr_b16 a[100:101], v9 offset:25600\n" \
"v_mfma_f32_16x16x32_bf16 v[88:91], a[64:67], a[224:227], v[88:91]\n" \
"ds_read_b64_tr_b16 a[102:103], v10 offset:25600\n" \
"v_mfma_f32_16x16x32_bf16 v[88:91], a[68:71], a[228:231], v[88:91]\n" \
"ds_read_b64_tr_b16 a[104:105], v11 offset:17408\n" \
"v_mfma_f32_16x16x32_bf16 v[68:71], a[72:75], a[200:203], v[68:71]\n" \
"ds_read_b64_tr_b16 a[106:107], v12 offset:17408\n" \
"v_mfma_f32_16x16x32_bf16 v[68:71], a[76:79], a[204:207], v[68:71]\n" \
"ds_read_b64_tr_b16 a[108:109], v11 offset:25600\n" \
"v_mfma_f32_16x16x32_bf16 v[76:79], a[72:75], a[208:211], v[76:79]\n" \
"s_add_u32 m0, s50, 77824\n" \
"ds_read_b64_tr_b16 a[110:111], v12 offset:25600\n" \
"v_mfma_f32_16x16x32_bf16 v[76:79], a[76:79], a[212:215], v[76:79]\n" \
"buffer_load_dwordx4 v16, s[28:31], s46 offen lds\n" \
"v_mfma_f32_16x16x32_bf16 v[84:87], a[72:75], a[216:219], v[84:87]\n" \
"v_mfma_f32_16x16x32_bf16 v[84:87], a[76:79], a[220:223], v[84:87]\n" \
"s_mov_b64 exec, 0xffff\nglobal_load_dword v22, v1, s[76:77]\ns_mov_b64 exec, -1\n" \
"v_mfma_f32_16x16x32_bf16 v[92:95], a[72:75], a[224:227], v[92:95]\n" \
"v_mfma_f32_16x16x32_bf16 v[92:95], a[76:79], a[228:231], v[92:95]\n" \
"s_waitcnt lgkmcnt(12)\n" \
"v_mfma_f32_16x16x32_bf16 v[96:99], a[80:83], a[0:3], v[96:99]\n" \
"s_mov_b32 s46, 1152\n" \
"s_add_u32 m0, s50, 81920\n" \
"ds_read_b64_tr_b16 a[64:65], v9 offset:17920\n" \
"v_mfma_f32_16x16x32_bf16 v[96:99], a[84:87], a[4:7], v[96:99]\n" \
"buffer_load_dwordx4 v17, s[28:31], s46 offen lds\n" \
"v_mfma_f32_16x16x32_bf16 v[104:107], a[80:83], a[8:11], v[104:107]\n" \
"ds_read_b64_tr_b16 a[66:67], v10 offset:17920\n" \
"v_mfma_f32_16x16x32_bf16 v[104:107], a[84:87], a[12:15], v[104:107]\n" \
"ds_read_b64_tr_b16 a[68:69], v9 offset:26112\n" \
"v_mfma_f32_16x16x32_bf16 v[112:115], a[80:83], a[16:19], v[112:115]\n" \
"s_add_u32 m0, s50, 86016\n" \
"ds_read_b64_tr_b16 a[70:71], v10 offset:26112\n" \
"v_mfma_f32_16x16x32_bf16 v[112:115], a[84:87], a[20:23], v[112:115]\n" \
"buffer_load_dwordx4 v18, s[28:31], s46 offen lds\n" \
"v_mfma_f32_16x16x32_bf16 v[120:123], a[80:83], a[24:27], v[120:123]\n" \
"s_waitcnt lgkmcnt(12)\n" \
"ds_read_b64_tr_b16 a[72:73], v11 offset:17920\n" \
"v_mfma_f32_16x16x32_bf16 v[120:123], a[84:87], a[28:31], v[120:123]\n" \
"v_mfma_f32_16x16x32_bf16 v[100:103], a[88:91], a[0:3], v[100:103]\n" \
"s_add_u32 m0, s50, 90112\n" \
"ds_read_b64_tr_b16 a[74:75], v12 offset:17920\n" \
"v_mfma_f32_16x16x32_bf16 v[100:103], a[92:95], a[4:7], v[100:103]\n" \
"buffer_load_dwordx4 v19, s[28:31], s46 offen lds\n" \
"v_mfma_f32_16x16x32_bf16 v[108:111], a[88:91], a[8:11], v[108:111]\n" \
"ds_read_b64_tr_b16 a[76:77], v11 offset:26112\n" \
"v_mfma_f32_16x16x32_bf16 v[108:111], a[92:95], a[12:15], v[108:111]\n" \
"ds_read_b64_tr_b16 a[78:79], v12 offset:26112\n" \
"v_mfma_f32_16x16x32_bf16 v[116:119], a[88:91], a[16:19], v[116:119]\n" \
"s_add_u32 m0, s50, 94208\n" \
"ds_read_b64_tr_b16 a[80:81], v9 offset:16384\n" \
"v_mfma_f32_16x16x32_bf16 v[116:119], a[92:95], a[20:23], v[116:119]\n" \
"buffer_load_dwordx4 v20, s[28:31], s46 offen lds\n" \
"v_mfma_f32_16x16x32_bf16 v[124:127], a[88:91], a[24:27], v[124:127]\n" \
"s_waitcnt lgkmcnt(13)\n" \
"ds_read_b64_tr_b16 a[82:83], v10 offset:16384\n" \
"v_mfma_f32_16x16x32_bf16 v[124:127], a[92:95], a[28:31], v[124:127]\n" \
"ds_read_b64_tr_b16 a[84:85], v9 offset:24576\n" \
"v_mfma_f32_16x16x32_bf16 v[128:131], a[96:99], a[0:3], v[128:131]\n" \
"s_mov_b32 s47, 5242880\n" \
"s_add_u32 m0, s51, 0\n" \
"ds_read_b64_tr_b16 a[86:87], v10 offset:24576\n" \
"v_mfma_f32_16x16x32_bf16 v[128:131], a[100:103], a[4:7], v[128:131]\n" \
"buffer_load_dwordx4 v21, s[32:35], s47 offen offset:0 lds\n" \
"v_mfma_f32_16x16x32_bf16 v[136:139], a[96:99], a[8:11], v[136:139]\n" \
"ds_read_b64_tr_b16 a[88:89], v11 offset:16384\n" \
"v_mfma_f32_16x16x32_bf16 v[136:139], a[100:103], a[12:15], v[136:139]\n" \
"v_mfma_f32_16x16x32_bf16 v[144:147], a[96:99], a[16:19], v[144:147]\n" \
"s_add_u32 m0, s51, 896\n" \
"ds_read_b64_tr_b16 a[90:91], v12 offset:16384\n" \
"v_mfma_f32_16x16x32_bf16 v[144:147], a[100:103], a[20:23], v[144:147]\n" \
"buffer_load_dwordx4 v21, s[32:35], s47 offen offset:128 lds\n" \
"v_mfma_f32_16x16x32_bf16 v[152:155], a[96:99], a[24:27], v[152:155]\n" \
"s_waitcnt lgkmcnt(14)\n" \
"ds_read_b64_tr_b16 a[92:93], v11 offset:24576\n" \
"v_mfma_f32_16x16x32_bf16 v[152:155], a[100:103], a[28:31], v[152:155]\n" \
"ds_read_b64_tr_b16 a[94:95], v12 offset:24576\n" \
"v_mfma_f32_16x16x32_bf16 v[132:135], a[104:107], a[0:3], v[132:135]\n" \
"s_mov_b32 s47, 5505024\n" \
"s_add_u32 m0, s51, 8192\n" \
"v_mfma_f32_16x16x32_bf16 v[132:135], a[108:111], a[4:7], v[132:135]\n" \
"buffer_load_dwordx4 v21, s[32:35], s47 offen offset:0 lds\n" \
"v_mfma_f32_16x16x32_bf16 v[140:143], a[104:107], a[8:11], v[140:143]\n" \
"v_mfma_f32_16x16x32_bf16 v[140:143], a[108:111], a[12:15], v[140:143]\n" \
"v_mfma_f32_16x16x32_bf16 v[148:151], a[104:107], a[16:19], v[148:151]\n" \
"s_add_u32 m0, s51, 9088\n" \
"v_mfma_f32_16x16x32_bf16 v[148:151], a[108:111], a[20:23], v[148:151]\n" \
"buffer_load_dwordx4 v21, s[32:35], s47 offen offset:128 lds\n" \
"v_mfma_f32_16x16x32_bf16 v[156:159], a[104:107], a[24:27], v[156:159]\n" \
"s_waitcnt vmcnt(17) lgkmcnt(0)\n" \
"v_mfma_f32_16x16x32_bf16 v[156:159], a[108:111], a[28:31], v[156:159]\n" \
"s_barrier\n" \
"v_mfma_f32_16x16x32_bf16 v[160:163], a[64:67], a[0:3], v[160:163]\n" \
"ds_read_b64_tr_b16 a[96:97], v9 offset:33280\n" \
"v_mfma_f32_16x16x32_bf16 v[160:163], a[68:71], a[4:7], v[160:163]\n" \
"ds_read_b64_tr_b16 a[98:99], v10 offset:33280\n" \
"v_mfma_f32_16x16x32_bf16 v[168:171], a[64:67], a[8:11], v[168:171]\n" \
"s_mov_b32 s46, 1280\n" \
"s_add_u32 m0, s50, 0\n" \
"ds_read_b64_tr_b16 a[100:101], v9 offset:41472\n" \
"v_mfma_f32_16x16x32_bf16 v[168:171], a[68:71], a[12:15], v[168:171]\n" \
"buffer_load_dwordx4 v13, s[28:31], s46 offen lds\n" \
"v_mfma_f32_16x16x32_bf16 v[176:179], a[64:67], a[16:19], v[176:179]\n" \
"ds_read_b64_tr_b16 a[102:103], v10 offset:41472\n" \
"v_mfma_f32_16x16x32_bf16 v[176:179], a[68:71], a[20:23], v[176:179]\n" \
"ds_read_b128 a[200:203], v5 offset:32768\n" \
"v_mfma_f32_16x16x32_bf16 v[184:187], a[64:67], a[24:27], v[184:187]\n" \
"ds_read_b128 a[204:207], v6 offset:32768\n" \
"v_mfma_f32_16x16x32_bf16 v[184:187], a[68:71], a[28:31], v[184:187]\n" \
"ds_read_b128 a[208:211], v5 offset:34816\n" \
"v_mfma_f32_16x16x32_bf16 v[164:167], a[72:75], a[0:3], v[164:167]\n" \
"ds_read_b128 a[212:215], v6 offset:34816\n" \
"v_mfma_f32_16x16x32_bf16 v[164:167], a[76:79], a[4:7], v[164:167]\n" \
"ds_read_b128 a[216:219], v5 offset:36864\n" \
"v_mfma_f32_16x16x32_bf16 v[172:175], a[72:75], a[8:11], v[172:175]\n" \
"s_add_u32 m0, s50, 4096\n" \
"ds_read_b128 a[220:223], v6 offset:36864\n" \
"v_mfma_f32_16x16x32_bf16 v[172:175], a[76:79], a[12:15], v[172:175]\n" \
"buffer_load_dwordx4 v14, s[28:31], s46 offen lds\n" \
"v_mfma_f32_16x16x32_bf16 v[180:183], a[72:75], a[16:19], v[180:183]\n" \
"ds_read_b128 a[224:227], v5 offset:38912\n" \
"v_mfma_f32_16x16x32_bf16 v[180:183], a[76:79], a[20:23], v[180:183]\n" \
"ds_read_b128 a[228:231], v6 offset:38912\n" \
"v_mfma_f32_16x16x32_bf16 v[188:191], a[72:75], a[24:27], v[188:191]\n" \
"ds_read_b64_tr_b16 a[104:105], v11 offset:33280\n" \
"v_mfma_f32_16x16x32_bf16 v[188:191], a[76:79], a[28:31], v[188:191]\n" \
"ds_read_b64_tr_b16 a[106:107], v12 offset:33280\n" \
"v_mfma_f32_16x16x32_bf16 v[64:67], a[80:83], a[0:3], v[64:67]\n" \
"ds_read_b64_tr_b16 a[108:109], v11 offset:41472\n" \
"v_mfma_f32_16x16x32_bf16 v[64:67], a[84:87], a[4:7], v[64:67]\n" \
"ds_read_b64_tr_b16 a[110:111], v12 offset:41472\n" \
"v_mfma_f32_16x16x32_bf16 v[72:75], a[80:83], a[8:11], v[72:75]\n" \
"s_add_u32 m0, s50, 8192\n" \
"ds_read_b64_tr_b16 a[64:65], v9 offset:33792\n" \
"v_mfma_f32_16x16x32_bf16 v[72:75], a[84:87], a[12:15], v[72:75]\n" \
"buffer_load_dwordx4 v15, s[28:31], s46 offen lds\n" \
"v_mfma_f32_16x16x32_bf16 v[80:83], a[80:83], a[16:19], v[80:83]\n" \
"ds_read_b64_tr_b16 a[66:67], v10 offset:33792\n" \
"v_mfma_f32_16x16x32_bf16 v[80:83], a[84:87], a[20:23], v[80:83]\n" \
"ds_read_b64_tr_b16 a[68:69], v9 offset:41984\n" \
"v_mfma_f32_16x16x32_bf16 v[88:91], a[80:83], a[24:27], v[88:91]\n" \
"ds_read_b64_tr_b16 a[70:71], v10 offset:41984\n" \
"v_mfma_f32_16x16x32_bf16 v[88:91], a[84:87], a[28:31], v[88:91]\n" \
"ds_read_b64_tr_b16 a[72:73], v11 offset:33792\n" \
"v_mfma_f32_16x16x32_bf16 v[68:71], a[88:91], a[0:3], v[68:71]\n" \
"ds_read_b64_tr_b16 a[74:75], v12 offset:33792\n" \
"v_mfma_f32_16x16x32_bf16 v[68:71], a[92:95], a[4:7], v[68:71]\n" \
"ds_read_b64_tr_b16 a[76:77], v11 offset:41984\n" \
"v_mfma_f32_16x16x32_bf16 v[76:79], a[88:91], a[8:11], v[76:79]\n" \
"s_add_u32 m0, s50, 12288\n" \
"ds_read_b64_tr_b16 a[78:79], v12 offset:41984\n" \
"v_mfma_f32_16x16x32_bf16 v[76:79], a[92:95], a[12:15], v[76:79]\n" \
"buffer_load_dwordx4 v16, s[28:31], s46 offen lds\n" \
"v_mfma_f32_16x16x32_bf16 v[84:87], a[88:91], a[16:19], v[84:87]\n" \
"v_mfma_f32_16x16x32_bf16 v[84:87], a[92:95], a[20:23], v[84:87]\n" \
"v_mfma_f32_16x16x32_bf16 v[92:95], a[88:91], a[24:27], v[92:95]\n" \
"v_mfma_f32_16x16x32_bf16 v[92:95], a[92:95], a[28:31], v[92:95]\n" \
"s_waitcnt lgkmcnt(12)\n" \
"v_mfma_f32_16x16x32_bf16 v[96:99], a[96:99], a[200:203], v[96:99]\n" \
"s_mov_b32 s46, 1280\n" \
"s_add_u32 m0, s50, 16384\n" \
"ds_read_b64_tr_b16 a[80:81], v9 offset:34304\n" \
"v_mfma_f32_16x16x32_bf16 v[96:99], a[100:103], a[204:207], v[96:99]\n" \
"buffer_load_dwordx4 v17, s[28:31], s46 offen lds\n" \
"v_mfma_f32_16x16x32_bf16 v[104:107], a[96:99], a[208:211], v[104:107]\n" \
"ds_read_b64_tr_b16 a[82:83], v10 offset:34304\n" \
"v_mfma_f32_16x16x32_bf16 v[104:107], a[100:103], a[212:215], v[104:107]\n" \
"ds_read_b64_tr_b16 a[84:85], v9 offset:42496\n" \
"v_mfma_f32_16x16x32_bf16 v[112:115], a[96:99], a[216:219], v[112:115]\n" \
"s_add_u32 m0, s50, 20480\n" \
"ds_read_b64_tr_b16 a[86:87], v10 offset:42496\n" \
"v_mfma_f32_16x16x32_bf16 v[112:115], a[100:103], a[220:223], v[112:115]\n" \
"buffer_load_dwordx4 v18, s[28:31], s46 offen lds\n" \
"v_mfma_f32_16x16x32_bf16 v[120:123], a[96:99], a[224:227], v[120:123]\n" \
"s_waitcnt lgkmcnt(12)\n" \
"ds_read_b64_tr_b16 a[88:89], v11 offset:34304\n" \
"v_mfma_f32_16x16x32_bf16 v[120:123], a[100:103], a[228:231], v[120:123]\n" \
"v_mfma_f32_16x16x32_bf16 v[100:103], a[104:107], a[200:203], v[100:103]\n" \
"s_add_u32 m0, s50, 24576\n" \
"ds_read_b64_tr_b16 a[90:91], v12 offset:34304\n" \
"v_mfma_f32_16x16x32_bf16 v[100:103], a[108:111], a[204:207], v[100:103]\n" \
"buffer_load_dwordx4 v19, s[28:31], s46 offen lds\n" \
"v_mfma_f32_16x16x32_bf16 v[108:111], a[104:107], a[208:211], v[108:111]\n" \
"ds_read_b64_tr_b16 a[92:93], v11 offset:42496\n" \
"v_mfma_f32_16x16x32_bf16 v[108:111], a[108:111], a[212:215], v[108:111]\n" \
"ds_read_b64_tr_b16 a[94:95], v12 offset:42496\n" \
"v_mfma_f32_16x16x32_bf16 v[116:119], a[104:107], a[216:219], v[116:119]\n" \
"s_add_u32 m0, s50, 28672\n" \
"ds_read_b64_tr_b16 a[96:97], v9 offset:32768\n" \
"v_mfma_f32_16x16x32_bf16 v[116:119], a[108:111], a[220:223], v[116:119]\n" \
"buffer_load_dwordx4 v20, s[28:31], s46 offen lds\n" \
"v_mfma_f32_16x16x32_bf16 v[124:127], a[104:107], a[224:227], v[124:127]\n" \
"s_waitcnt lgkmcnt(13)\n" \
"ds_read_b64_tr_b16 a[98:99], v10 offset:32768\n" \
"v_mfma_f32_16x16x32_bf16 v[124:127], a[108:111], a[228:231], v[124:127]\n" \
"ds_read_b64_tr_b16 a[100:101], v9 offset:40960\n" \
"v_mfma_f32_16x16x32_bf16 v[128:131], a[64:67], a[200:203], v[128:131]\n" \
"s_mov_b32 s47, 5767168\n" \
"s_add_u32 m0, s51, 16384\n" \
"ds_read_b64_tr_b16 a[102:103], v10 offset:40960\n" \
"v_mfma_f32_16x16x32_bf16 v[128:131], a[68:71], a[204:207], v[128:131]\n" \
"buffer_load_dwordx4 v21, s[32:35], s47 offen offset:0 lds\n" \
"v_mfma_f32_16x16x32_bf16 v[136:139], a[64:67], a[208:211], v[136:139]\n" \
"ds_read_b64_tr_b16 a[104:105], v11 offset:32768\n" \
"v_mfma_f32_16x16x32_bf16 v[136:139], a[68:71], a[212:215], v[136:139]\n" \
"v_mfma_f32_16x16x32_bf16 v[144:147], a[64:67], a[216:219], v[144:147]\n" \
"s_add_u32 m0, s51, 17280\n" \
"ds_read_b64_tr_b16 a[106:107], v12 offset:32768\n" \
"v_mfma_f32_16x16x32_bf16 v[144:147], a[68:71], a[220:223], v[144:147]\n" \
"buffer_load_dwordx4 v21, s[32:35], s47 offen offset:128 lds\n" \
"v_mfma_f32_16x16x32_bf16 v[152:155], a[64:67], a[224:227], v[152:155]\n" \
"s_waitcnt lgkmcnt(14)\n" \
"ds_read_b64_tr_b16 a[108:109], v11 offset:40960\n" \
"v_mfma_f32_16x16x32_bf16 v[152:155], a[68:71], a[228:231], v[152:155]\n" \
"ds_read_b64_tr_b16 a[110:111], v12 offset:40960\n" \
"v_mfma_f32_16x16x32_bf16 v[132:135], a[72:75], a[200:203], v[132:135]\n" \
"s_mov_b32 s47, 6029312\n" \
"s_add_u32 m0, s51, 24576\n" \
"v_mfma_f32_16x16x32_bf16 v[132:135], a[76:79], a[204:207], v[132:135]\n" \
"buffer_load_dwordx4 v21, s[32:35], s47 offen offset:0 lds\n" \
"v_mfma_f32_16x16x32_bf16 v[140:143], a[72:75], a[208:211], v[140:143]\n" \
"v_mfma_f32_16x16x32_bf16 v[140:143], a[76:79], a[212:215], v[140:143]\n" \
"v_mfma_f32_16x16x32_bf16 v[148:151], a[72:75], a[216:219], v[148:151]\n" \
"s_add_u32 m0, s51, 25472\n" \
"v_mfma_f32_16x16x32_bf16 v[148:151], a[76:79], a[220:223], v[148:151]\n" \
"buffer_load_dwordx4 v21, s[32:35], s47 offen offset:128 lds\n" \
"v_mfma_f32_16x16x32_bf16 v[156:159], a[72:75], a[224:227], v[156:159]\n" \
"s_waitcnt vmcnt(16) lgkmcnt(0)\n" \
"v_mfma_f32_16x16x32_bf16 v[156:159], a[76:79], a[228:231], v[156:159]\n" \
"s_barrier\n" \
"v_mfma_f32_16x16x32_bf16 v[160:163], a[80:83], a[200:203], v[160:163]\n" \
"ds_read_b64_tr_b16 a[64:65], v9 offset:49664\n" \
"v_mfma_f32_16x16x32_bf16 v[160:163], a[84:87], a[204:207], v[160:163]\n" \
"ds_read_b64_tr_b16 a[66:67], v10 offset:49664\n" \
"v_mfma_f32_16x16x32_bf16 v[168:171], a[80:83], a[208:211], v[168:171]\n" \
"s_mov_b32 s46, 1408\n" \
"s_add_u32 m0, s50, 32768\n" \
"ds_read_b64_tr_b16 a[68:69], v9 offset:57856\n" \
"v_mfma_f32_16x16x32_bf16 v[168:171], a[84:87], a[212:215], v[168:171]\n" \
"buffer_load_dwordx4 v13, s[28:31], s46 offen lds\n" \
"v_mfma_f32_16x16x32_bf16 v[176:179], a[80:83], a[216:219], v[176:179]\n" \
"ds_read_b64_tr_b16 a[70:71], v10 offset:57856\n" \
"v_mfma_f32_16x16x32_bf16 v[176:179], a[84:87], a[220:223], v[176:179]\n" \
"ds_read_b128 a[0:3], v7 offset:0\n" \
"v_mfma_f32_16x16x32_bf16 v[184:187], a[80:83], a[224:227], v[184:187]\n" \
"ds_read_b128 a[4:7], v8 offset:0\n" \
"v_mfma_f32_16x16x32_bf16 v[184:187], a[84:87], a[228:231], v[184:187]\n" \
"ds_read_b128 a[8:11], v7 offset:2048\n" \
"v_mfma_f32_16x16x32_bf16 v[164:167], a[88:91], a[200:203], v[164:167]\n" \
"ds_read_b128 a[12:15], v8 offset:2048\n" \
"v_mfma_f32_16x16x32_bf16 v[164:167], a[92:95], a[204:207], v[164:167]\n" \
"ds_read_b128 a[16:19], v7 offset:4096\n" \
"v_mfma_f32_16x16x32_bf16 v[172:175], a[88:91], a[208:211], v[172:175]\n" \
"s_add_u32 m0, s50, 36864\n" \
"ds_read_b128 a[20:23], v8 offset:4096\n" \
"v_mfma_f32_16x16x32_bf16 v[172:175], a[92:95], a[212:215], v[172:175]\n" \
"buffer_load_dwordx4 v14, s[28:31], s46 offen lds\n" \
"v_mfma_f32_16x16x32_bf16 v[180:183], a[88:91], a[216:219], v[180:183]\n" \
"ds_read_b128 a[24:27], v7 offset:6144\n" \
"v_mfma_f32_16x16x32_bf16 v[180:183], a[92:95], a[220:223], v[180:183]\n" \
"ds_read_b128 a[28:31], v8 offset:6144\n" \
"v_mfma_f32_16x16x32_bf16 v[188:191], a[88:91], a[224:227], v[188:191]\n" \
"ds_read_b64_tr_b16 a[72:73], v11 offset:49664\n" \
"v_mfma_f32_16x16x32_bf16 v[188:191], a[92:95], a[228:231], v[188:191]\n" \
"ds_read_b64_tr_b16 a[74:75], v12 offset:49664\n" \
"v_mfma_f32_16x16x32_bf16 v[64:67], a[96:99], a[200:203], v[64:67]\n" \
"ds_read_b64_tr_b16 a[76:77], v11 offset:57856\n" \
"v_mfma_f32_16x16x32_bf16 v[64:67], a[100:103], a[204:207], v[64:67]\n" \
"ds_read_b64_tr_b16 a[78:79], v12 offset:57856\n" \
"v_mfma_f32_16x16x32_bf16 v[72:75], a[96:99], a[208:211], v[72:75]\n" \
"s_add_u32 m0, s50, 40960\n" \
"ds_read_b64_tr_b16 a[80:81], v9 offset:50176\n" \
"v_mfma_f32_16x16x32_bf16 v[72:75], a[100:103], a[212:215], v[72:75]\n" \
"buffer_load_dwordx4 v15, s[28:31], s46 offen lds\n" \
"v_mfma_f32_16x16x32_bf16 v[80:83], a[96:99], a[216:219], v[80:83]\n" \
"ds_read_b64_tr_b16 a[82:83], v10 offset:50176\n" \
"v_mfma_f32_16x16x32_bf16 v[80:83], a[100:103], a[220:223], v[80:83]\n" \
"ds_read_b64_tr_b16 a[84:85], v9 offset:58368\n" \
"v_mfma_f32_16x16x32_bf16 v[88:91], a[96:99], a[224:227], v[88:91]\n" \
"ds_read_b64_tr_b16 a[86:87], v10 offset:58368\n" \
"v_mfma_f32_16x16x32_bf16 v[88:91], a[100:103], a[228:231], v[88:91]\n" \
"ds_read_b64_tr_b16 a[88:89], v11 offset:50176\n" \
"v_mfma_f32_16x16x32_bf16 v[68:71], a[104:107], a[200:203], v[68:71]\n" \
"ds_read_b64_tr_b16 a[90:91], v12 offset:50176\n" \
"v_mfma_f32_16x16x32_bf16 v[68:71], a[108:111], a[204:207], v[68:71]\n" \
"ds_read_b64_tr_b16 a[92:93], v11 offset:58368\n" \
"v_mfma_f32_16x16x32_bf16 v[76:79], a[104:107], a[208:211], v[76:79]\n" \
"s_add_u32 m0, s50, 45056\n" \
"ds_read_b64_tr_b16 a[94:95], v12 offset:58368\n" \
"v_mfma_f32_16x16x32_bf16 v[76:79], a[108:111], a[212:215], v[76:79]\n" \
"buffer_load_dwordx4 v16, s[28:31], s46 offen lds\n" \
"v_mfma_f32_16x16x32_bf16 v[84:87], a[104:107], a[216:219], v[84:87]\n" \
"v_mfma_f32_16x16x32_bf16 v[84:87], a[108:111], a[220:223], v[84:87]\n" \
"v_mfma_f32_16x16x32_bf16 v[92:95], a[104:107], a[224:227], v[92:95]\n" \
"v_mfma_f32_16x16x32_bf16 v[92:95], a[108:111], a[228:231], v[92:95]\n" \
"s_waitcnt lgkmcnt(12)\n" \
"v_mfma_f32_16x16x32_bf16 v[96:99], a[64:67], a[0:3], v[96:99]\n" \
"s_mov_b32 s46, 1408\n" \
"s_add_u32 m0, s50, 49152\n" \
"ds_read_b64_tr_b16 a[96:97], v9 offset:50688\n" \
"v_mfma_f32_16x16x32_bf16 v[96:99], a[68:71], a[4:7], v[96:99]\n" \
"buffer_load_dwordx4 v17, s[28:31], s46 offen lds\n" \
"v_mfma_f32_16x16x32_bf16 v[104:107], a[64:67], a[8:11], v[104:107]\n" \
"s_bfe_u32 s89, %1, 0x20001\n" \
"ds_read_b64_tr_b16 a[98:99], v10 offset:50688\n" \
"s_and_b32 s90, %1, 1\n" \
"v_mfma_f32_16x16x32_bf16 v[104:107], a[68:71], a[12:15], v[104:107]\n" \
"s_lshl_b32 s90, s90, 2\n" \
"ds_read_b64_tr_b16 a[100:101], v9 offset:58880\n" \
"s_bfe_u32 s92, %1, 0x20003\n" \
"v_mfma_f32_16x16x32_bf16 v[112:115], a[64:67], a[16:19], v[112:115]\n" \
"s_add_u32 m0, s50, 53248\n" \
"ds_read_b64_tr_b16 a[102:103], v10 offset:58880\n" \
"s_add_u32 s90, s90, s92\n" \
"v_mfma_f32_16x16x32_bf16 v[112:115], a[68:71], a[20:23], v[112:115]\n" \
"buffer_load_dwordx4 v18, s[28:31], s46 offen lds\n" \
"v_mfma_f32_16x16x32_bf16 v[120:123], a[64:67], a[24:27], v[120:123]\n" \
"s_waitcnt lgkmcnt(12)\n" \
"ds_read_b64_tr_b16 a[104:105], v11 offset:50688\n" \
"s_lshl_b32 s92, %1, 2\n" \
"v_mfma_f32_16x16x32_bf16 v[120:123], a[68:71], a[28:31], v[120:123]\n" \
"s_mov_b64 exec, 0xffff\nglobal_load_dword v1, v2, s[100:101]\ns_mov_b64 exec, -1\n" \
"v_mfma_f32_16x16x32_bf16 v[100:103], a[72:75], a[0:3], v[100:103]\n" \
"s_add_u32 m0, s50, 57344\n" \
"ds_read_b64_tr_b16 a[106:107], v12 offset:50688\n" \
"s_add_u32 s92, s92, %2\n" \
"v_mfma_f32_16x16x32_bf16 v[100:103], a[76:79], a[4:7], v[100:103]\n" \
"buffer_load_dwordx4 v19, s[28:31], s46 offen lds\n" \
"v_mfma_f32_16x16x32_bf16 v[108:111], a[72:75], a[8:11], v[108:111]\n" \
"s_mul_i32 s93, s92, 12288\n" \
"ds_read_b64_tr_b16 a[108:109], v11 offset:58880\n" \
"s_add_u32 s78, s93, 109051904\n" \
"v_mfma_f32_16x16x32_bf16 v[108:111], a[76:79], a[12:15], v[108:111]\n" \
"s_lshl_b32 s88, s92, 3\n" \
"ds_read_b64_tr_b16 a[110:111], v12 offset:58880\n" \
"s_add_u32 s88, s88, 94404608\n" \
"v_mfma_f32_16x16x32_bf16 v[116:119], a[72:75], a[16:19], v[116:119]\n" \
"s_add_u32 m0, s50, 61440\n" \
"ds_read_b64_tr_b16 a[64:65], v9 offset:49152\n" \
"v_mfma_f32_16x16x32_bf16 v[116:119], a[76:79], a[20:23], v[116:119]\n" \
"buffer_load_dwordx4 v20, s[28:31], s46 offen lds\n" \
"v_mfma_f32_16x16x32_bf16 v[124:127], a[72:75], a[24:27], v[124:127]\n" \
"s_waitcnt lgkmcnt(13)\n" \
"ds_read_b64_tr_b16 a[66:67], v10 offset:49152\n" \
"v_mfma_f32_16x16x32_bf16 v[124:127], a[76:79], a[28:31], v[124:127]\n" \
"s_cmp_eq_u32 s92, 0\ns_cselect_b32 s12, 94371840, -1\n" \
"ds_read_b64_tr_b16 a[68:69], v9 offset:57344\n" \
"v_mfma_f32_16x16x32_bf16 v[128:131], a[80:83], a[0:3], v[128:131]\n" \
"s_mov_b32 s47, 6291456\n" \
"s_add_u32 m0, s51, 32768\n" \
"ds_read_b64_tr_b16 a[70:71], v10 offset:57344\n" \
"v_mfma_f32_16x16x32_bf16 v[128:131], a[84:87], a[4:7], v[128:131]\n" \
"buffer_load_dwordx4 v21, s[32:35], s47 offen offset:0 lds\n" \
"v_mfma_f32_16x16x32_bf16 v[136:139], a[80:83], a[8:11], v[136:139]\n" \
"s_mul_i32 s80, s90, 12\n" \
"ds_read_b64_tr_b16 a[72:73], v11 offset:49152\n" \
"s_add_u32 s80, s80, s89\n" \
"v_mfma_f32_16x16x32_bf16 v[136:139], a[84:87], a[12:15], v[136:139]\n" \
"s_lshl_b32 s80, s80, 16\n" \
"s_add_u32 s92, %2, 0\n" \
"s_mul_i32 s93, s92, 43\n" \
"v_mfma_f32_16x16x32_bf16 v[144:147], a[80:83], a[16:19], v[144:147]\n" \
"s_add_u32 m0, s51, 33664\n" \
"ds_read_b64_tr_b16 a[74:75], v12 offset:49152\n" \
"s_lshr_b32 s93, s93, 7\n" \
"v_mfma_f32_16x16x32_bf16 v[144:147], a[84:87], a[20:23], v[144:147]\n" \
"LH12_%=:\ns_cbranch_execz LHR0_%=\n" \
"buffer_load_dwordx4 v21, s[32:35], s47 offen offset:128 lds\n" \
"v_mfma_f32_16x16x32_bf16 v[152:155], a[80:83], a[24:27], v[152:155]\n" \
"s_waitcnt lgkmcnt(14)\n" \
"ds_read_b64_tr_b16 a[76:77], v11 offset:57344\n" \
"s_lshl_b32 s93, s93, 12\n" \
"v_mfma_f32_16x16x32_bf16 v[152:155], a[84:87], a[28:31], v[152:155]\n" \
"s_add_u32 s64, s80, s93\n" \
"ds_read_b64_tr_b16 a[78:79], v12 offset:57344\n" \
"s_add_u32 s92, s92, s89\n" \
"v_mfma_f32_16x16x32_bf16 v[132:135], a[88:91], a[0:3], v[132:135]\n" \
"s_mov_b32 s47, 6553600\n" \
"s_add_u32 m0, s51, 40960\n" \
"s_add_u32 s92, s92, 1\n" \
"v_mfma_f32_16x16x32_bf16 v[132:135], a[92:95], a[4:7], v[132:135]\n" \
"buffer_load_dwordx4 v21, s[32:35], s47 offen offset:0 lds\n" \
"v_mfma_f32_16x16x32_bf16 v[140:143], a[88:91], a[8:11], v[140:143]\n" \
"s_mul_i32 s93, s92, 43\n" \
"s_lshr_b32 s93, s93, 7\n" \
"s_mul_i32 s93, s93, 3\n" \
"v_mfma_f32_16x16x32_bf16 v[140:143], a[92:95], a[12:15], v[140:143]\n" \
"s_sub_u32 s93, s92, s93\n" \
"s_lshl_b32 s93, s93, 18\n" \
"s_add_u32 s64, s64, s93\n" \
"v_mfma_f32_16x16x32_bf16 v[148:151], a[88:91], a[16:19], v[148:151]\n" \
"s_add_u32 m0, s51, 41856\n" \
"s_add_u32 s92, %2, 4\n" \
"s_mul_i32 s93, s92, 43\n" \
"v_mfma_f32_16x16x32_bf16 v[148:151], a[92:95], a[20:23], v[148:151]\n" \
"buffer_load_dwordx4 v21, s[32:35], s47 offen offset:128 lds\n" \
"v_mfma_f32_16x16x32_bf16 v[156:159], a[88:91], a[24:27], v[156:159]\n" \
"s_waitcnt vmcnt(17) lgkmcnt(0)\n" \
"s_lshr_b32 s93, s93, 7\n" \
"s_lshl_b32 s93, s93, 12\n" \
"v_mfma_f32_16x16x32_bf16 v[156:159], a[92:95], a[28:31], v[156:159]\n" \
"s_barrier\n" \
"s_add_u32 s65, s80, s93\n" \
"s_add_u32 s92, s92, s89\n" \
"v_mfma_f32_16x16x32_bf16 v[160:163], a[96:99], a[0:3], v[160:163]\n" \
"s_add_u32 s92, s92, 1\n" \
"ds_read_b64_tr_b16 a[80:81], v9 offset:512\n" \
"s_mul_i32 s93, s92, 43\n" \
"v_mfma_f32_16x16x32_bf16 v[160:163], a[100:103], a[4:7], v[160:163]\n" \
"s_lshr_b32 s93, s93, 7\n" \
"ds_read_b64_tr_b16 a[82:83], v10 offset:512\n" \
"s_mul_i32 s93, s93, 3\n" \
"v_mfma_f32_16x16x32_bf16 v[168:171], a[96:99], a[8:11], v[168:171]\n" \
"s_mov_b32 s46, 1536\n" \
"s_add_u32 m0, s50, 65536\n" \
"ds_read_b64_tr_b16 a[84:85], v9 offset:8704\n" \
"v_mov_b32 v58, s14\n" \
"v_mfma_f32_16x16x32_bf16 v[168:171], a[100:103], a[12:15], v[168:171]\n" \
"buffer_load_dwordx4 v13, s[28:31], s46 offen lds\n" \
"v_mov_b32 v59, s15\n" \
"v_mfma_f32_16x16x32_bf16 v[176:179], a[96:99], a[16:19], v[176:179]\n" \
"s_sub_u32 s93, s92, s93\n" \
"ds_read_b64_tr_b16 a[86:87], v10 offset:8704\n" \
"s_lshl_b32 s93, s93, 18\n" \
"v_mfma_f32_16x16x32_bf16 v[176:179], a[100:103], a[20:23], v[176:179]\n" \
"s_add_u32 s65, s65, s93\n" \
"ds_read_b128 a[200:203], v5 offset:0\n" \
"s_add_u32 s92, %2, 8\n" \
"v_mfma_f32_16x16x32_bf16 v[184:187], a[96:99], a[24:27], v[184:187]\n" \
"s_mul_i32 s93, s92, 43\n" \
"ds_read_b128 a[204:207], v6 offset:0\n" \
"s_lshr_b32 s93, s93, 7\n" \
"v_mfma_f32_16x16x32_bf16 v[184:187], a[100:103], a[28:31], v[184:187]\n" \
"s_lshl_b32 s93, s93, 12\n" \
"ds_read_b128 a[208:211], v5 offset:2048\n" \
"s_add_u32 s66, s80, s93\n" \
"v_mfma_f32_16x16x32_bf16 v[164:167], a[104:107], a[0:3], v[164:167]\n" \
"s_add_u32 s92, s92, s89\n" \
"ds_read_b128 a[212:215], v6 offset:2048\n" \
"s_add_u32 s92, s92, 1\n" \
"v_mfma_f32_16x16x32_bf16 v[164:167], a[108:111], a[4:7], v[164:167]\n" \
"s_mul_i32 s93, s92, 43\n" \
"ds_read_b128 a[216:219], v5 offset:4096\n" \
"s_lshr_b32 s93, s93, 7\n" \
"v_mfma_f32_16x16x32_bf16 v[172:175], a[104:107], a[8:11], v[172:175]\n" \
"s_add_u32 m0, s50, 69632\n" \
"ds_read_b128 a[220:223], v6 offset:4096\n" \
"s_mul_i32 s93, s93, 3\n" \
"v_mfma_f32_16x16x32_bf16 v[172:175], a[108:111], a[12:15], v[172:175]\n" \
"buffer_load_dwordx4 v14, s[28:31], s46 offen lds\n" \
"v_mfma_f32_16x16x32_bf16 v[180:183], a[104:107], a[16:19], v[180:183]\n" \
"s_sub_u32 s93, s92, s93\n" \
"ds_read_b128 a[224:227], v5 offset:6144\n" \
"s_lshl_b32 s93, s93, 18\n" \
"v_mfma_f32_16x16x32_bf16 v[180:183], a[108:111], a[20:23], v[180:183]\n" \
"s_add_u32 s66, s66, s93\n" \
"ds_read_b128 a[228:231], v6 offset:6144\n" \
"s_and_b32 s92, %2, 1\n" \
"v_mfma_f32_16x16x32_bf16 v[188:191], a[104:107], a[24:27], v[188:191]\n" \
"s_lshl_b32 s92, s92, 12\n" \
"ds_read_b64_tr_b16 a[88:89], v11 offset:512\n" \
"s_sub_u32 s57, s66, s64\n" \
"v_mfma_f32_16x16x32_bf16 v[188:191], a[108:111], a[28:31], v[188:191]\n" \
"s_sub_u32 s56, s64, s57\n" \
"ds_read_b64_tr_b16 a[90:91], v12 offset:512\n" \
"s_sub_u32 s56, s56, s92\n" \
"v_mfma_f32_16x16x32_bf16 v[64:67], a[64:67], a[0:3], v[64:67]\n" \
"s_sub_u32 s59, s64, s65\n" \
"ds_read_b64_tr_b16 a[92:93], v11 offset:8704\n" \
"s_add_u32 s59, s59, 16384\n" \
"v_mfma_f32_16x16x32_bf16 v[64:67], a[68:71], a[4:7], v[64:67]\n" \
"s_sub_u32 s58, s65, s59\n" \
"ds_read_b64_tr_b16 a[94:95], v12 offset:8704\n" \
"s_sub_u32 s58, s58, s92\n" \
"v_mfma_f32_16x16x32_bf16 v[72:75], a[64:67], a[8:11], v[72:75]\n" \
"s_add_u32 m0, s50, 73728\n" \
"ds_read_b64_tr_b16 a[96:97], v9 offset:1024\n" \
"s_lshl_b32 s92, s90, 18\n" \
"v_mfma_f32_16x16x32_bf16 v[72:75], a[68:71], a[12:15], v[72:75]\n" \
"buffer_load_dwordx4 v15, s[28:31], s46 offen lds\n" \
"v_mfma_f32_16x16x32_bf16 v[80:83], a[64:67], a[16:19], v[80:83]\n" \
"s_lshl_b32 s93, s89, 16\n" \
"ds_read_b64_tr_b16 a[98:99], v10 offset:1024\n" \
"s_add_u32 s67, s92, s93\n" \
"v_mfma_f32_16x16x32_bf16 v[80:83], a[68:71], a[20:23], v[80:83]\n" \
"s_add_u32 s67, s67, 6291456\n" \
"ds_read_b64_tr_b16 a[100:101], v9 offset:9216\n" \
"s_lshl_b32 s92, s90, 20\n" \
"v_mfma_f32_16x16x32_bf16 v[88:91], a[64:67], a[24:27], v[88:91]\n" \
"s_add_u32 s94, s92, s93\n" \
"ds_read_b64_tr_b16 a[102:103], v10 offset:9216\n" \
"s_add_u32 s94, s94, 8388608\n" \
"v_mfma_f32_16x16x32_bf16 v[88:91], a[68:71], a[28:31], v[88:91]\n" \
"ds_read_b64_tr_b16 a[104:105], v11 offset:1024\n" \
"buffer_store_dwordx2 v[58:59], v56, s[36:39], s24 offen sc1\n" \
"v_mfma_f32_16x16x32_bf16 v[68:71], a[72:75], a[0:3], v[68:71]\n" \
"s_add_u32 s95, s89, 1\n" \
"ds_read_b64_tr_b16 a[106:107], v12 offset:1024\n" \
"s_and_b32 s95, s95, 3\n" \
"v_mfma_f32_16x16x32_bf16 v[68:71], a[76:79], a[4:7], v[68:71]\n" \
"s_lshl_b32 s92, s95, 18\n" \
"ds_read_b64_tr_b16 a[108:109], v11 offset:9216\n" \
"s_add_u32 s68, s94, s92\n" \
"v_mfma_f32_16x16x32_bf16 v[76:79], a[72:75], a[8:11], v[76:79]\n" \
"s_add_u32 m0, s50, 77824\n" \
"ds_read_b64_tr_b16 a[110:111], v12 offset:9216\n" \
"s_sub_u32 s92, s95, s89\n" \
"v_mfma_f32_16x16x32_bf16 v[76:79], a[76:79], a[12:15], v[76:79]\n" \
"buffer_load_dwordx4 v16, s[28:31], s46 offen lds\n" \
"v_mfma_f32_16x16x32_bf16 v[84:87], a[72:75], a[16:19], v[84:87]\n" \
"s_mul_i32 s93, s92, 98304\n" \
"s_add_u32 s75, s78, s93\n" \
"s_add_u32 s75, s75, 6144\n" \
"v_mfma_f32_16x16x32_bf16 v[84:87], a[76:79], a[20:23], v[84:87]\n" \
"s_add_u32 s95, s89, 2\n" \
"s_and_b32 s95, s95, 3\n" \
"s_lshl_b32 s92, s95, 18\n" \
"v_mfma_f32_16x16x32_bf16 v[92:95], a[72:75], a[24:27], v[92:95]\n" \
"s_add_u32 s69, s94, s92\n" \
"s_sub_u32 s92, s95, s89\n" \
"s_mul_i32 s93, s92, 98304\n" \
"v_mfma_f32_16x16x32_bf16 v[92:95], a[76:79], a[28:31], v[92:95]\n" \
"s_waitcnt lgkmcnt(12)\n" \
"s_add_u32 s76, s78, s93\n" \
"s_add_u32 s76, s76, 8192\n" \
"v_mfma_f32_16x16x32_bf16 v[96:99], a[80:83], a[200:203], v[96:99]\n" \
"s_mov_b32 s46, 1536\n" \
"s_add_u32 m0, s50, 81920\n" \
"ds_read_b64_tr_b16 a[64:65], v9 offset:1536\n" \
"v_mfma_f32_16x16x32_bf16 v[96:99], a[84:87], a[204:207], v[96:99]\n" \
"buffer_load_dwordx4 v17, s[28:31], s46 offen lds\n" \
"v_mfma_f32_16x16x32_bf16 v[104:107], a[80:83], a[208:211], v[104:107]\n" \
"s_add_u32 s95, s89, 3\n" \
"ds_read_b64_tr_b16 a[66:67], v10 offset:1536\n" \
"s_and_b32 s95, s95, 3\n" \
"v_mfma_f32_16x16x32_bf16 v[104:107], a[84:87], a[212:215], v[104:107]\n" \
"s_lshl_b32 s92, s95, 18\n" \
"ds_read_b64_tr_b16 a[68:69], v9 offset:9728\n" \
"s_add_u32 s70, s94, s92\n" \
"v_mfma_f32_16x16x32_bf16 v[112:115], a[80:83], a[216:219], v[112:115]\n" \
"s_add_u32 m0, s50, 86016\n" \
"ds_read_b64_tr_b16 a[70:71], v10 offset:9728\n" \
"s_sub_u32 s92, s95, s89\n" \
"v_mfma_f32_16x16x32_bf16 v[112:115], a[84:87], a[220:223], v[112:115]\n" \
"buffer_load_dwordx4 v18, s[28:31], s46 offen lds\n" \
"v_mfma_f32_16x16x32_bf16 v[120:123], a[80:83], a[224:227], v[120:123]\n" \
"s_waitcnt lgkmcnt(12)\n" \
"ds_read_b64_tr_b16 a[72:73], v11 offset:1536\n" \
"s_mul_i32 s93, s92, 98304\n" \
"v_mfma_f32_16x16x32_bf16 v[120:123], a[84:87], a[228:231], v[120:123]\n" \
"s_add_u32 s77, s78, s93\n" \
"s_add_u32 s77, s77, 10240\n" \
"s_add_u32 s95, s89, 4\n" \
"v_mfma_f32_16x16x32_bf16 v[100:103], a[88:91], a[200:203], v[100:103]\n" \
"s_add_u32 m0, s50, 90112\n" \
"ds_read_b64_tr_b16 a[74:75], v12 offset:1536\n" \
"s_and_b32 s95, s95, 3\n" \
"v_mfma_f32_16x16x32_bf16 v[100:103], a[92:95], a[204:207], v[100:103]\n" \
"buffer_load_dwordx4 v19, s[28:31], s46 offen lds\n" \
"v_mfma_f32_16x16x32_bf16 v[108:111], a[88:91], a[208:211], v[108:111]\n" \
"s_lshl_b32 s92, s95, 18\n" \
"ds_read_b64_tr_b16 a[76:77], v11 offset:9728\n" \
"s_add_u32 s71, s94, s92\n" \
"v_mfma_f32_16x16x32_bf16 v[108:111], a[92:95], a[212:215], v[108:111]\n" \
"s_add_u32 s92, s89, 1\n" \
"ds_read_b64_tr_b16 a[78:79], v12 offset:9728\n" \
"s_mul_i32 s95, s92, 43\n" \
"v_mfma_f32_16x16x32_bf16 v[116:119], a[88:91], a[216:219], v[116:119]\n" \
"s_add_u32 m0, s50, 94208\n" \
"ds_read_b64_tr_b16 a[80:81], v9 offset:0\n" \
"s_lshr_b32 s95, s95, 7\n" \
"v_mfma_f32_16x16x32_bf16 v[116:119], a[92:95], a[220:223], v[116:119]\n" \
"buffer_load_dwordx4 v20, s[28:31], s46 offen lds\n" \
"v_mfma_f32_16x16x32_bf16 v[124:127], a[88:91], a[224:227], v[124:127]\n" \
"s_waitcnt lgkmcnt(13)\n" \
"ds_read_b64_tr_b16 a[82:83], v10 offset:0\n" \
"s_mul_i32 s95, s95, 3\n" \
"v_mfma_f32_16x16x32_bf16 v[124:127], a[92:95], a[228:231], v[124:127]\n" \
"s_sub_u32 s95, s92, s95\n" \
"ds_read_b64_tr_b16 a[84:85], v9 offset:8192\n" \
"s_sub_u32 s92, s95, s89\n" \
"v_mfma_f32_16x16x32_bf16 v[128:131], a[96:99], a[200:203], v[128:131]\n" \
"s_mov_b32 s47, 6815744\n" \
"s_add_u32 m0, s51, 49152\n" \
"ds_read_b64_tr_b16 a[86:87], v10 offset:8192\n" \
"v_mfma_f32_16x16x32_bf16 v[128:131], a[100:103], a[204:207], v[128:131]\n" \
"buffer_load_dwordx4 v21, s[32:35], s47 offen offset:0 lds\n" \
"v_mfma_f32_16x16x32_bf16 v[136:139], a[96:99], a[208:211], v[136:139]\n" \
"s_mul_i32 s93, s92, 98304\n" \
"ds_read_b64_tr_b16 a[88:89], v11 offset:0\n" \
"s_add_u32 s72, s78, s93\n" \
"v_mfma_f32_16x16x32_bf16 v[136:139], a[100:103], a[212:215], v[136:139]\n" \
"s_sub_u32 s92, s92, 1\n" \
"s_and_b32 s92, s92, 3\n" \
"s_lshl_b32 s92, s92, 11\n" \
"v_mfma_f32_16x16x32_bf16 v[144:147], a[96:99], a[216:219], v[144:147]\n" \
"s_add_u32 m0, s51, 50048\n" \
"ds_read_b64_tr_b16 a[90:91], v12 offset:0\n" \
"s_add_u32 s72, s72, s92\n" \
"v_mfma_f32_16x16x32_bf16 v[144:147], a[100:103], a[220:223], v[144:147]\n" \
"buffer_load_dwordx4 v21, s[32:35], s47 offen offset:128 lds\n" \
"v_mfma_f32_16x16x32_bf16 v[152:155], a[96:99], a[224:227], v[152:155]\n" \
"s_waitcnt lgkmcnt(14)\n" \
"ds_read_b64_tr_b16 a[92:93], v11 offset:8192\n" \
"s_add_u32 s92, s89, 2\n" \
"v_mfma_f32_16x16x32_bf16 v[152:155], a[100:103], a[228:231], v[152:155]\n" \
"s_mul_i32 s95, s92, 43\n" \
"ds_read_b64_tr_b16 a[94:95], v12 offset:8192\n" \
"s_lshr_b32 s95, s95, 7\n" \
"v_mfma_f32_16x16x32_bf16 v[132:135], a[104:107], a[200:203], v[132:135]\n" \
"s_mov_b32 s47, 7077888\n" \
"s_add_u32 m0, s51, 57344\n" \
"s_mul_i32 s95, s95, 3\n" \
"v_mfma_f32_16x16x32_bf16 v[132:135], a[108:111], a[204:207], v[132:135]\n" \
"buffer_load_dwordx4 v21, s[32:35], s47 offen offset:0 lds\n" \
"v_mfma_f32_16x16x32_bf16 v[140:143], a[104:107], a[208:211], v[140:143]\n" \
"s_sub_u32 s95, s92, s95\n" \
"s_sub_u32 s92, s95, s89\n" \
"s_mul_i32 s93, s92, 98304\n" \
"v_mfma_f32_16x16x32_bf16 v[140:143], a[108:111], a[212:215], v[140:143]\n" \
"s_add_u32 s73, s78, s93\n" \
"s_sub_u32 s92, s92, 1\n" \
"s_and_b32 s92, s92, 3\n" \
"v_mfma_f32_16x16x32_bf16 v[148:151], a[104:107], a[216:219], v[148:151]\n" \
"s_add_u32 m0, s51, 58240\n" \
"s_lshl_b32 s92, s92, 11\n" \
"s_add_u32 s73, s73, s92\n" \
"v_mfma_f32_16x16x32_bf16 v[148:151], a[108:111], a[220:223], v[148:151]\n" \
"buffer_load_dwordx4 v21, s[32:35], s47 offen offset:128 lds\n" \
"v_mfma_f32_16x16x32_bf16 v[156:159], a[104:107], a[224:227], v[156:159]\n" \
"s_waitcnt vmcnt(17) lgkmcnt(0)\n" \
"s_cmp_lt_u32 s89, 3\ns_cselect_b32 s95, 3, 0\n" \
"v_mfma_f32_16x16x32_bf16 v[156:159], a[108:111], a[228:231], v[156:159]\n" \
"s_barrier\n" \
"s_sub_u32 s92, s95, s89\n" \
"s_mul_i32 s93, s92, 98304\n" \
"v_mfma_f32_16x16x32_bf16 v[160:163], a[64:67], a[200:203], v[160:163]\n" \
"s_add_u32 s74, s78, s93\n" \
"ds_read_b64_tr_b16 a[96:97], v9 offset:16896\n" \
"s_sub_u32 s92, s92, 1\n" \
"v_mfma_f32_16x16x32_bf16 v[160:163], a[68:71], a[204:207], v[160:163]\n" \
"s_and_b32 s92, s92, 3\n" \
"ds_read_b64_tr_b16 a[98:99], v10 offset:16896\n" \
"s_lshl_b32 s92, s92, 11\n" \
"v_mfma_f32_16x16x32_bf16 v[168:171], a[64:67], a[208:211], v[168:171]\n" \
"s_mov_b32 s46, 1664\n" \
"s_add_u32 m0, s50, 0\n" \
"ds_read_b64_tr_b16 a[100:101], v9 offset:25088\n" \
"v_mfma_f32_16x16x32_bf16 v[168:171], a[68:71], a[212:215], v[168:171]\n" \
"buffer_load_dwordx4 v13, s[28:31], s46 offen lds\n" \
"v_mfma_f32_16x16x32_bf16 v[176:179], a[64:67], a[216:219], v[176:179]\n" \
"s_add_u32 s74, s74, s92\n" \
"ds_read_b64_tr_b16 a[102:103], v10 offset:25088\n" \
"s_mul_i32 s96, s90, 3\n" \
"v_mfma_f32_16x16x32_bf16 v[176:179], a[68:71], a[220:223], v[176:179]\n" \
"s_add_u32 s96, s96, s89\n" \
"ds_read_b128 a[0:3], v5 offset:32768\n" \
"s_add_u32 s92, s90, 24\n" \
"v_mfma_f32_16x16x32_bf16 v[184:187], a[64:67], a[224:227], v[184:187]\n" \
"s_cmp_lt_u32 s89, 3\ns_cselect_b32 s96, s96, s92\n" \
"ds_read_b128 a[4:7], v6 offset:32768\n" \
"v_mfma_f32_16x16x32_bf16 v[184:187], a[68:71], a[228:231], v[184:187]\n" \
"s_lshl_b32 s96, s96, 5\n" \
"ds_read_b128 a[8:11], v5 offset:34816\n" \
"s_lshl_b32 s97, s90, 2\n" \
"v_mfma_f32_16x16x32_bf16 v[164:167], a[72:75], a[200:203], v[164:167]\n" \
"s_add_u32 s97, s97, s89\n" \
"ds_read_b128 a[12:15], v6 offset:34816\n" \
"s_add_u32 s97, s97, 32\n" \
"v_mfma_f32_16x16x32_bf16 v[164:167], a[76:79], a[204:207], v[164:167]\n" \
"s_lshl_b32 s97, s97, 5\n" \
"ds_read_b128 a[16:19], v5 offset:36864\n" \
"s_lshr_b32 s92, %1, 5\n" \
"v_mfma_f32_16x16x32_bf16 v[172:175], a[72:75], a[208:211], v[172:175]\n" \
"s_add_u32 m0, s50, 4096\n" \
"ds_read_b128 a[20:23], v6 offset:36864\n" \
"s_lshl_b32 s92, s92, 2\n" \
"v_mfma_f32_16x16x32_bf16 v[172:175], a[76:79], a[212:215], v[172:175]\n" \
"buffer_load_dwordx4 v14, s[28:31], s46 offen lds\n" \
"v_mfma_f32_16x16x32_bf16 v[180:183], a[72:75], a[216:219], v[180:183]\n" \
"s_add_u32 s92, s92, %2\n" \
"ds_read_b128 a[24:27], v5 offset:38912\n" \
"s_lshl_b32 s93, s92, 6\n" \
"v_mfma_f32_16x16x32_bf16 v[180:183], a[76:79], a[220:223], v[180:183]\n" \
"s_lshl_b32 s92, s92, 17\n" \
"ds_read_b128 a[28:31], v6 offset:38912\n" \
"s_add_u32 s92, s92, 67108864\n" \
"v_mfma_f32_16x16x32_bf16 v[188:191], a[72:75], a[224:227], v[188:191]\n" \
"s_lshl_b32 s82, s96, 11\n" \
"ds_read_b64_tr_b16 a[104:105], v11 offset:16896\n" \
"s_add_u32 s82, s82, s93\n" \
"v_mfma_f32_16x16x32_bf16 v[188:191], a[76:79], a[228:231], v[188:191]\n" \
"s_lshl_b32 s86, s96, 2\n" \
"ds_read_b64_tr_b16 a[106:107], v12 offset:16896\n" \
"s_add_u32 s86, s86, 71303168\n" \
"v_mfma_f32_16x16x32_bf16 v[64:67], a[80:83], a[200:203], v[64:67]\n" \
"s_lshl_b32 s84, s96, 3\n" \
"ds_read_b64_tr_b16 a[108:109], v11 offset:25088\n" \
"s_add_u32 s84, s84, s92\n" \
"v_mfma_f32_16x16x32_bf16 v[64:67], a[84:87], a[204:207], v[64:67]\n" \
"s_and_b32 s96, s96, 32\n" \
"ds_read_b64_tr_b16 a[110:111], v12 offset:25088\n" \
"s_mul_i32 s96, s96, 6\n" \
"v_mfma_f32_16x16x32_bf16 v[72:75], a[80:83], a[208:211], v[72:75]\n" \
"s_add_u32 m0, s50, 8192\n" \
"ds_read_b64_tr_b16 a[64:65], v9 offset:17408\n" \
"s_sub_u32 s84, s84, s96\n" \
"v_mfma_f32_16x16x32_bf16 v[72:75], a[84:87], a[212:215], v[72:75]\n" \
"buffer_load_dwordx4 v15, s[28:31], s46 offen lds\n" \
"v_mfma_f32_16x16x32_bf16 v[80:83], a[80:83], a[216:219], v[80:83]\n" \
"s_lshl_b32 s83, s97, 11\n" \
"ds_read_b64_tr_b16 a[66:67], v10 offset:17408\n" \
"s_add_u32 s83, s83, s93\n" \
"v_mfma_f32_16x16x32_bf16 v[80:83], a[84:87], a[220:223], v[80:83]\n" \
"s_lshl_b32 s87, s97, 2\n" \
"ds_read_b64_tr_b16 a[68:69], v9 offset:25600\n" \
"s_add_u32 s87, s87, 71303168\n" \
"v_mfma_f32_16x16x32_bf16 v[88:91], a[80:83], a[224:227], v[88:91]\n" \
"s_lshl_b32 s85, s97, 3\n" \
"ds_read_b64_tr_b16 a[70:71], v10 offset:25600\n" \
"s_add_u32 s85, s85, s92\n" \
"v_mfma_f32_16x16x32_bf16 v[88:91], a[84:87], a[228:231], v[88:91]\n" \
"s_and_b32 s97, s97, 32\n" \
"ds_read_b64_tr_b16 a[72:73], v11 offset:17408\n" \
"s_mul_i32 s97, s97, 6\n" \
"v_mfma_f32_16x16x32_bf16 v[68:71], a[88:91], a[200:203], v[68:71]\n" \
"s_sub_u32 s85, s85, s97\n" \
"ds_read_b64_tr_b16 a[74:75], v12 offset:17408\n" \
"s_lshl_b32 s93, s89, 6\n" \
"v_mfma_f32_16x16x32_bf16 v[68:71], a[92:95], a[204:207], v[68:71]\n" \
"s_add_u32 s93, s93, 94388224\n" \
"ds_read_b64_tr_b16 a[76:77], v11 offset:25600\n" \
"s_lshl_b32 s92, s90, 8\n" \
"v_mfma_f32_16x16x32_bf16 v[76:79], a[88:91], a[208:211], v[76:79]\n" \
"s_add_u32 m0, s50, 12288\n" \
"ds_read_b64_tr_b16 a[78:79], v12 offset:25600\n" \
"s_add_u32 s10, s93, s92\n" \
"v_mfma_f32_16x16x32_bf16 v[76:79], a[92:95], a[212:215], v[76:79]\n" \
"buffer_load_dwordx4 v16, s[28:31], s46 offen lds\n" \
"v_mfma_f32_16x16x32_bf16 v[84:87], a[88:91], a[216:219], v[84:87]\n" \
"s_add_u32 s10, s10, 6144\n" \
"s_lshl_b32 s92, s90, 10\n" \
"s_add_u32 s11, s93, s92\n" \
"v_mfma_f32_16x16x32_bf16 v[84:87], a[92:95], a[220:223], v[84:87]\n" \
"s_add_u32 s11, s11, 8192\n" \
"s_mul_i32 s92, s90, 768\n" \
"s_add_u32 s98, s93, s92\n" \
"v_mfma_f32_16x16x32_bf16 v[92:95], a[88:91], a[224:227], v[92:95]\n" \
"v_mfma_f32_16x16x32_bf16 v[92:95], a[92:95], a[228:231], v[92:95]\n" \
"s_waitcnt lgkmcnt(12)\n" \
"v_mfma_f32_16x16x32_bf16 v[96:99], a[96:99], a[0:3], v[96:99]\n" \
"s_mov_b32 s46, 1664\n" \
"s_add_u32 m0, s50, 16384\n" \
"ds_read_b64_tr_b16 a[80:81], v9 offset:17920\n" \
"v_mfma_f32_16x16x32_bf16 v[96:99], a[100:103], a[4:7], v[96:99]\n" \
"buffer_load_dwordx4 v17, s[28:31], s46 offen lds\n" \
"v_mfma_f32_16x16x32_bf16 v[104:107], a[96:99], a[8:11], v[104:107]\n" \
"ds_read_b64_tr_b16 a[82:83], v10 offset:17920\n" \
"v_mfma_f32_16x16x32_bf16 v[104:107], a[100:103], a[12:15], v[104:107]\n" \
"ds_read_b64_tr_b16 a[84:85], v9 offset:26112\n" \
"v_mfma_f32_16x16x32_bf16 v[112:115], a[96:99], a[16:19], v[112:115]\n" \
"s_add_u32 m0, s50, 20480\n" \
"ds_read_b64_tr_b16 a[86:87], v10 offset:26112\n" \
"v_mfma_f32_16x16x32_bf16 v[112:115], a[100:103], a[20:23], v[112:115]\n" \
"buffer_load_dwordx4 v18, s[28:31], s46 offen lds\n" \
"v_mfma_f32_16x16x32_bf16 v[120:123], a[96:99], a[24:27], v[120:123]\n" \
"s_waitcnt lgkmcnt(12)\n" \
"ds_read_b64_tr_b16 a[88:89], v11 offset:17920\n" \
"v_mfma_f32_16x16x32_bf16 v[120:123], a[100:103], a[28:31], v[120:123]\n" \
"v_mfma_f32_16x16x32_bf16 v[100:103], a[104:107], a[0:3], v[100:103]\n" \
"s_add_u32 m0, s50, 24576\n" \
"ds_read_b64_tr_b16 a[90:91], v12 offset:17920\n" \
"v_mfma_f32_16x16x32_bf16 v[100:103], a[108:111], a[4:7], v[100:103]\n" \
"buffer_load_dwordx4 v19, s[28:31], s46 offen lds\n" \
"v_mfma_f32_16x16x32_bf16 v[108:111], a[104:107], a[8:11], v[108:111]\n" \
"ds_read_b64_tr_b16 a[92:93], v11 offset:26112\n" \
"v_mfma_f32_16x16x32_bf16 v[108:111], a[108:111], a[12:15], v[108:111]\n" \
"ds_read_b64_tr_b16 a[94:95], v12 offset:26112\n" \
"v_mfma_f32_16x16x32_bf16 v[116:119], a[104:107], a[16:19], v[116:119]\n" \
"s_add_u32 m0, s50, 28672\n" \
"ds_read_b64_tr_b16 a[96:97], v9 offset:16384\n" \
"v_mfma_f32_16x16x32_bf16 v[116:119], a[108:111], a[20:23], v[116:119]\n" \
"buffer_load_dwordx4 v20, s[28:31], s46 offen lds\n" \
"v_mfma_f32_16x16x32_bf16 v[124:127], a[104:107], a[24:27], v[124:127]\n" \
"s_waitcnt lgkmcnt(13)\n" \
"ds_read_b64_tr_b16 a[98:99], v10 offset:16384\n" \
"v_mfma_f32_16x16x32_bf16 v[124:127], a[108:111], a[28:31], v[124:127]\n" \
"ds_read_b64_tr_b16 a[100:101], v9 offset:24576\n" \
"v_mfma_f32_16x16x32_bf16 v[128:131], a[64:67], a[0:3], v[128:131]\n" \
"s_mov_b32 s47, 7340032\n" \
"s_add_u32 m0, s51, 0\n" \
"ds_read_b64_tr_b16 a[102:103], v10 offset:24576\n" \
"v_mfma_f32_16x16x32_bf16 v[128:131], a[68:71], a[4:7], v[128:131]\n" \
"buffer_load_dwordx4 v21, s[32:35], s47 offen offset:0 lds\n" \
"v_mfma_f32_16x16x32_bf16 v[136:139], a[64:67], a[8:11], v[136:139]\n" \
"ds_read_b64_tr_b16 a[104:105], v11 offset:16384\n" \
"v_mfma_f32_16x16x32_bf16 v[136:139], a[68:71], a[12:15], v[136:139]\n" \
"v_mfma_f32_16x16x32_bf16 v[144:147], a[64:67], a[16:19], v[144:147]\n" \
"s_add_u32 m0, s51, 896\n" \
"ds_read_b64_tr_b16 a[106:107], v12 offset:16384\n" \
"v_mfma_f32_16x16x32_bf16 v[144:147], a[68:71], a[20:23], v[144:147]\n" \
"buffer_load_dwordx4 v21, s[32:35], s47 offen offset:128 lds\n" \
"v_mfma_f32_16x16x32_bf16 v[152:155], a[64:67], a[24:27], v[152:155]\n" \
"s_waitcnt lgkmcnt(14)\n" \
"ds_read_b64_tr_b16 a[108:109], v11 offset:24576\n" \
"v_mfma_f32_16x16x32_bf16 v[152:155], a[68:71], a[28:31], v[152:155]\n" \
"ds_read_b64_tr_b16 a[110:111], v12 offset:24576\n" \
"v_mfma_f32_16x16x32_bf16 v[132:135], a[72:75], a[0:3], v[132:135]\n" \
"s_mov_b32 s47, 7602176\n" \
"s_add_u32 m0, s51, 8192\n" \
"v_mfma_f32_16x16x32_bf16 v[132:135], a[76:79], a[4:7], v[132:135]\n" \
"buffer_load_dwordx4 v21, s[32:35], s47 offen offset:0 lds\n" \
"v_mfma_f32_16x16x32_bf16 v[140:143], a[72:75], a[8:11], v[140:143]\n" \
"v_mfma_f32_16x16x32_bf16 v[140:143], a[76:79], a[12:15], v[140:143]\n" \
"v_mfma_f32_16x16x32_bf16 v[148:151], a[72:75], a[16:19], v[148:151]\n" \
"s_add_u32 m0, s51, 9088\n" \
"v_mfma_f32_16x16x32_bf16 v[148:151], a[76:79], a[20:23], v[148:151]\n" \
"buffer_load_dwordx4 v21, s[32:35], s47 offen offset:128 lds\n" \
"v_mfma_f32_16x16x32_bf16 v[156:159], a[72:75], a[24:27], v[156:159]\n" \
"s_waitcnt vmcnt(16) lgkmcnt(0)\n" \
"v_mfma_f32_16x16x32_bf16 v[156:159], a[76:79], a[28:31], v[156:159]\n" \
"s_barrier\n" \
"v_mfma_f32_16x16x32_bf16 v[160:163], a[80:83], a[0:3], v[160:163]\n" \
"ds_read_b64_tr_b16 a[64:65], v9 offset:33280\n" \
"v_mfma_f32_16x16x32_bf16 v[160:163], a[84:87], a[4:7], v[160:163]\n" \
"ds_read_b64_tr_b16 a[66:67], v10 offset:33280\n" \
"v_mfma_f32_16x16x32_bf16 v[168:171], a[80:83], a[8:11], v[168:171]\n" \
"s_mov_b32 s46, 1792\n" \
"s_add_u32 m0, s50, 32768\n" \
"ds_read_b64_tr_b16 a[68:69], v9 offset:41472\n" \
"v_mfma_f32_16x16x32_bf16 v[168:171], a[84:87], a[12:15], v[168:171]\n" \
"buffer_load_dwordx4 v13, s[28:31], s46 offen lds\n" \
"v_mfma_f32_16x16x32_bf16 v[176:179], a[80:83], a[16:19], v[176:179]\n" \
"ds_read_b64_tr_b16 a[70:71], v10 offset:41472\n" \
"v_mfma_f32_16x16x32_bf16 v[176:179], a[84:87], a[20:23], v[176:179]\n" \
"ds_read_b128 a[200:203], v7 offset:0\n" \
"v_mfma_f32_16x16x32_bf16 v[184:187], a[80:83], a[24:27], v[184:187]\n" \
"ds_read_b128 a[204:207], v8 offset:0\n" \
"v_mfma_f32_16x16x32_bf16 v[184:187], a[84:87], a[28:31], v[184:187]\n" \
"ds_read_b128 a[208:211], v7 offset:2048\n" \
"v_mfma_f32_16x16x32_bf16 v[164:167], a[88:91], a[0:3], v[164:167]\n" \
"ds_read_b128 a[212:215], v8 offset:2048\n" \
"v_mfma_f32_16x16x32_bf16 v[164:167], a[92:95], a[4:7], v[164:167]\n" \
"ds_read_b128 a[216:219], v7 offset:4096\n" \
"v_mfma_f32_16x16x32_bf16 v[172:175], a[88:91], a[8:11], v[172:175]\n" \
"s_add_u32 m0, s50, 36864\n" \
"ds_read_b128 a[220:223], v8 offset:4096\n" \
"v_mfma_f32_16x16x32_bf16 v[172:175], a[92:95], a[12:15], v[172:175]\n" \
"buffer_load_dwordx4 v14, s[28:31], s46 offen lds\n" \
"v_mfma_f32_16x16x32_bf16 v[180:183], a[88:91], a[16:19], v[180:183]\n" \
"ds_read_b128 a[224:227], v7 offset:6144\n" \
"v_mfma_f32_16x16x32_bf16 v[180:183], a[92:95], a[20:23], v[180:183]\n" \
"ds_read_b128 a[228:231], v8 offset:6144\n" \
"v_mfma_f32_16x16x32_bf16 v[188:191], a[88:91], a[24:27], v[188:191]\n" \
"ds_read_b64_tr_b16 a[72:73], v11 offset:33280\n" \
"v_mfma_f32_16x16x32_bf16 v[188:191], a[92:95], a[28:31], v[188:191]\n" \
"ds_read_b64_tr_b16 a[74:75], v12 offset:33280\n" \
"v_mfma_f32_16x16x32_bf16 v[64:67], a[96:99], a[0:3], v[64:67]\n" \
"ds_read_b64_tr_b16 a[76:77], v11 offset:41472\n" \
"v_mfma_f32_16x16x32_bf16 v[64:67], a[100:103], a[4:7], v[64:67]\n" \
"ds_read_b64_tr_b16 a[78:79], v12 offset:41472\n" \
"v_mfma_f32_16x16x32_bf16 v[72:75], a[96:99], a[8:11], v[72:75]\n" \
"s_add_u32 m0, s50, 40960\n" \
"ds_read_b64_tr_b16 a[80:81], v9 offset:33792\n" \
"v_mfma_f32_16x16x32_bf16 v[72:75], a[100:103], a[12:15], v[72:75]\n" \
"buffer_load_dwordx4 v15, s[28:31], s46 offen lds\n" \
"v_mfma_f32_16x16x32_bf16 v[80:83], a[96:99], a[16:19], v[80:83]\n" \
"ds_read_b64_tr_b16 a[82:83], v10 offset:33792\n" \
"v_mfma_f32_16x16x32_bf16 v[80:83], a[100:103], a[20:23], v[80:83]\n" \
"ds_read_b64_tr_b16 a[84:85], v9 offset:41984\n" \
"v_mfma_f32_16x16x32_bf16 v[88:91], a[96:99], a[24:27], v[88:91]\n" \
"ds_read_b64_tr_b16 a[86:87], v10 offset:41984\n" \
"v_mfma_f32_16x16x32_bf16 v[88:91], a[100:103], a[28:31], v[88:91]\n" \
"ds_read_b64_tr_b16 a[88:89], v11 offset:33792\n" \
"v_mfma_f32_16x16x32_bf16 v[68:71], a[104:107], a[0:3], v[68:71]\n" \
"ds_read_b64_tr_b16 a[90:91], v12 offset:33792\n" \
"v_mfma_f32_16x16x32_bf16 v[68:71], a[108:111], a[4:7], v[68:71]\n" \
"ds_read_b64_tr_b16 a[92:93], v11 offset:41984\n" \
"v_mfma_f32_16x16x32_bf16 v[76:79], a[104:107], a[8:11], v[76:79]\n" \
"s_add_u32 m0, s50, 45056\n" \
"ds_read_b64_tr_b16 a[94:95], v12 offset:41984\n" \
"v_mfma_f32_16x16x32_bf16 v[76:79], a[108:111], a[12:15], v[76:79]\n" \
"buffer_load_dwordx4 v16, s[28:31], s46 offen lds\n" \
"v_mfma_f32_16x16x32_bf16 v[84:87], a[104:107], a[16:19], v[84:87]\n" \
"v_mfma_f32_16x16x32_bf16 v[84:87], a[108:111], a[20:23], v[84:87]\n" \
"v_mfma_f32_16x16x32_bf16 v[92:95], a[104:107], a[24:27], v[92:95]\n" \
"v_mfma_f32_16x16x32_bf16 v[92:95], a[108:111], a[28:31], v[92:95]\n" \
"s_waitcnt lgkmcnt(12)\n" \
"v_mfma_f32_16x16x32_bf16 v[96:99], a[64:67], a[200:203], v[96:99]\n" \
"s_mov_b32 s46, 1792\n" \
"s_add_u32 m0, s50, 49152\n" \
"ds_read_b64_tr_b16 a[96:97], v9 offset:34304\n" \
"v_mfma_f32_16x16x32_bf16 v[96:99], a[68:71], a[204:207], v[96:99]\n" \
"buffer_load_dwordx4 v17, s[28:31], s46 offen lds\n" \
"v_mfma_f32_16x16x32_bf16 v[104:107], a[64:67], a[208:211], v[104:107]\n" \
"ds_read_b64_tr_b16 a[98:99], v10 offset:34304\n" \
"v_mfma_f32_16x16x32_bf16 v[104:107], a[68:71], a[212:215], v[104:107]\n" \
"ds_read_b64_tr_b16 a[100:101], v9 offset:42496\n" \
"v_mfma_f32_16x16x32_bf16 v[112:115], a[64:67], a[216:219], v[112:115]\n" \
"s_add_u32 m0, s50, 53248\n" \
"ds_read_b64_tr_b16 a[102:103], v10 offset:42496\n" \
"v_mfma_f32_16x16x32_bf16 v[112:115], a[68:71], a[220:223], v[112:115]\n" \
"buffer_load_dwordx4 v18, s[28:31], s46 offen lds\n" \
"v_mfma_f32_16x16x32_bf16 v[120:123], a[64:67], a[224:227], v[120:123]\n" \
"s_waitcnt lgkmcnt(12)\n" \
"ds_read_b64_tr_b16 a[104:105], v11 offset:34304\n" \
"v_mfma_f32_16x16x32_bf16 v[120:123], a[68:71], a[228:231], v[120:123]\n" \
"v_mfma_f32_16x16x32_bf16 v[100:103], a[72:75], a[200:203], v[100:103]\n" \
"s_add_u32 m0, s50, 57344\n" \
"ds_read_b64_tr_b16 a[106:107], v12 offset:34304\n" \
"v_mfma_f32_16x16x32_bf16 v[100:103], a[76:79], a[204:207], v[100:103]\n" \
"buffer_load_dwordx4 v19, s[28:31], s46 offen lds\n" \
"v_mfma_f32_16x16x32_bf16 v[108:111], a[72:75], a[208:211], v[108:111]\n" \
"ds_read_b64_tr_b16 a[108:109], v11 offset:42496\n" \
"v_mfma_f32_16x16x32_bf16 v[108:111], a[76:79], a[212:215], v[108:111]\n" \
"ds_read_b64_tr_b16 a[110:111], v12 offset:42496\n" \
"v_mfma_f32_16x16x32_bf16 v[116:119], a[72:75], a[216:219], v[116:119]\n" \
"s_add_u32 m0, s50, 61440\n" \
"ds_read_b64_tr_b16 a[64:65], v9 offset:32768\n" \
"v_mfma_f32_16x16x32_bf16 v[116:119], a[76:79], a[220:223], v[116:119]\n" \
"buffer_load_dwordx4 v20, s[28:31], s46 offen lds\n" \
"v_mfma_f32_16x16x32_bf16 v[124:127], a[72:75], a[224:227], v[124:127]\n" \
"s_waitcnt lgkmcnt(13)\n" \
"ds_read_b64_tr_b16 a[66:67], v10 offset:32768\n" \
"v_mfma_f32_16x16x32_bf16 v[124:127], a[76:79], a[228:231], v[124:127]\n" \
"ds_read_b64_tr_b16 a[68:69], v9 offset:40960\n" \
"v_mfma_f32_16x16x32_bf16 v[128:131], a[80:83], a[200:203], v[128:131]\n" \
"s_mov_b32 s47, 7864320\n" \
"s_add_u32 m0, s51, 16384\n" \
"ds_read_b64_tr_b16 a[70:71], v10 offset:40960\n" \
"v_mfma_f32_16x16x32_bf16 v[128:131], a[84:87], a[204:207], v[128:131]\n" \
"buffer_load_dwordx4 v21, s[32:35], s47 offen offset:0 lds\n" \
"v_mfma_f32_16x16x32_bf16 v[136:139], a[80:83], a[208:211], v[136:139]\n" \
"ds_read_b64_tr_b16 a[72:73], v11 offset:32768\n" \
"v_mfma_f32_16x16x32_bf16 v[136:139], a[84:87], a[212:215], v[136:139]\n" \
"v_mfma_f32_16x16x32_bf16 v[144:147], a[80:83], a[216:219], v[144:147]\n" \
"s_add_u32 m0, s51, 17280\n" \
"ds_read_b64_tr_b16 a[74:75], v12 offset:32768\n" \
"v_mfma_f32_16x16x32_bf16 v[144:147], a[84:87], a[220:223], v[144:147]\n" \
"buffer_load_dwordx4 v21, s[32:35], s47 offen offset:128 lds\n" \
"v_mfma_f32_16x16x32_bf16 v[152:155], a[80:83], a[224:227], v[152:155]\n" \
"s_waitcnt lgkmcnt(14)\n" \
"ds_read_b64_tr_b16 a[76:77], v11 offset:40960\n" \
"v_mfma_f32_16x16x32_bf16 v[152:155], a[84:87], a[228:231], v[152:155]\n" \
"ds_read_b64_tr_b16 a[78:79], v12 offset:40960\n" \
"v_mfma_f32_16x16x32_bf16 v[132:135], a[88:91], a[200:203], v[132:135]\n" \
"s_mov_b32 s47, 8126464\n" \
"s_add_u32 m0, s51, 24576\n" \
"v_mfma_f32_16x16x32_bf16 v[132:135], a[92:95], a[204:207], v[132:135]\n" \
"buffer_load_dwordx4 v21, s[32:35], s47 offen offset:0 lds\n" \
"v_mfma_f32_16x16x32_bf16 v[140:143], a[88:91], a[208:211], v[140:143]\n" \
"v_mfma_f32_16x16x32_bf16 v[140:143], a[92:95], a[212:215], v[140:143]\n" \
"v_mfma_f32_16x16x32_bf16 v[148:151], a[88:91], a[216:219], v[148:151]\n" \
"s_add_u32 m0, s51, 25472\n" \
"v_mfma_f32_16x16x32_bf16 v[148:151], a[92:95], a[220:223], v[148:151]\n" \
"buffer_load_dwordx4 v21, s[32:35], s47 offen offset:128 lds\n" \
"v_mfma_f32_16x16x32_bf16 v[156:159], a[88:91], a[224:227], v[156:159]\n" \
"LH13_%=:\ns_cbranch_execz LHR0_%=\n" \
"s_waitcnt vmcnt(16) lgkmcnt(0)\n" \
"v_mfma_f32_16x16x32_bf16 v[156:159], a[92:95], a[228:231], v[156:159]\n" \
"s_barrier\n" \
"v_mfma_f32_16x16x32_bf16 v[160:163], a[96:99], a[200:203], v[160:163]\n" \
"ds_read_b64_tr_b16 a[80:81], v9 offset:49664\n" \
"v_mfma_f32_16x16x32_bf16 v[160:163], a[100:103], a[204:207], v[160:163]\n" \
"ds_read_b64_tr_b16 a[82:83], v10 offset:49664\n" \
"v_mfma_f32_16x16x32_bf16 v[168:171], a[96:99], a[208:211], v[168:171]\n" \
"s_mov_b32 s46, 1920\n" \
"s_add_u32 m0, s50, 65536\n" \
"ds_read_b64_tr_b16 a[84:85], v9 offset:57856\n" \
"v_mfma_f32_16x16x32_bf16 v[168:171], a[100:103], a[212:215], v[168:171]\n" \
"buffer_load_dwordx4 v13, s[28:31], s46 offen lds\n" \
"v_mfma_f32_16x16x32_bf16 v[176:179], a[96:99], a[216:219], v[176:179]\n" \
"ds_read_b64_tr_b16 a[86:87], v10 offset:57856\n" \
"v_mfma_f32_16x16x32_bf16 v[176:179], a[100:103], a[220:223], v[176:179]\n" \
"ds_read_b128 a[0:3], v5 offset:0\n" \
"v_mfma_f32_16x16x32_bf16 v[184:187], a[96:99], a[224:227], v[184:187]\n" \
"ds_read_b128 a[4:7], v6 offset:0\n" \
"v_mfma_f32_16x16x32_bf16 v[184:187], a[100:103], a[228:231], v[184:187]\n" \
"ds_read_b128 a[8:11], v5 offset:2048\n" \
"v_mfma_f32_16x16x32_bf16 v[164:167], a[104:107], a[200:203], v[164:167]\n" \
"ds_read_b128 a[12:15], v6 offset:2048\n" \
"v_mfma_f32_16x16x32_bf16 v[164:167], a[108:111], a[204:207], v[164:167]\n" \
"ds_read_b128 a[16:19], v5 offset:4096\n" \
"v_mfma_f32_16x16x32_bf16 v[172:175], a[104:107], a[208:211], v[172:175]\n" \
"s_add_u32 m0, s50, 69632\n" \
"ds_read_b128 a[20:23], v6 offset:4096\n" \
"v_mfma_f32_16x16x32_bf16 v[172:175], a[108:111], a[212:215], v[172:175]\n" \
"buffer_load_dwordx4 v14, s[28:31], s46 offen lds\n" \
"v_mfma_f32_16x16x32_bf16 v[180:183], a[104:107], a[216:219], v[180:183]\n" \
"ds_read_b128 a[24:27], v5 offset:6144\n" \
"v_mfma_f32_16x16x32_bf16 v[180:183], a[108:111], a[220:223], v[180:183]\n" \
"ds_read_b128 a[28:31], v6 offset:6144\n" \
"v_mfma_f32_16x16x32_bf16 v[188:191], a[104:107], a[224:227], v[188:191]\n" \
"ds_read_b64_tr_b16 a[88:89], v11 offset:49664\n" \
"v_mfma_f32_16x16x32_bf16 v[188:191], a[108:111], a[228:231], v[188:191]\n" \
"ds_read_b64_tr_b16 a[90:91], v12 offset:49664\n" \
"v_mfma_f32_16x16x32_bf16 v[64:67], a[64:67], a[200:203], v[64:67]\n" \
"ds_read_b64_tr_b16 a[92:93], v11 offset:57856\n" \
"v_mfma_f32_16x16x32_bf16 v[64:67], a[68:71], a[204:207], v[64:67]\n" \
"ds_read_b64_tr_b16 a[94:95], v12 offset:57856\n" \
"v_mfma_f32_16x16x32_bf16 v[72:75], a[64:67], a[208:211], v[72:75]\n" \
"s_add_u32 m0, s50, 73728\n" \
"ds_read_b64_tr_b16 a[96:97], v9 offset:50176\n" \
"v_mfma_f32_16x16x32_bf16 v[72:75], a[68:71], a[212:215], v[72:75]\n" \
"buffer_load_dwordx4 v15, s[28:31], s46 offen lds\n" \
"v_mfma_f32_16x16x32_bf16 v[80:83], a[64:67], a[216:219], v[80:83]\n" \
"ds_read_b64_tr_b16 a[98:99], v10 offset:50176\n" \
"v_mfma_f32_16x16x32_bf16 v[80:83], a[68:71], a[220:223], v[80:83]\n" \
"ds_read_b64_tr_b16 a[100:101], v9 offset:58368\n" \
"v_mfma_f32_16x16x32_bf16 v[88:91], a[64:67], a[224:227], v[88:91]\n" \
"ds_read_b64_tr_b16 a[102:103], v10 offset:58368\n" \
"v_mfma_f32_16x16x32_bf16 v[88:91], a[68:71], a[228:231], v[88:91]\n" \
"ds_read_b64_tr_b16 a[104:105], v11 offset:50176\n" \
"v_mfma_f32_16x16x32_bf16 v[68:71], a[72:75], a[200:203], v[68:71]\n" \
"ds_read_b64_tr_b16 a[106:107], v12 offset:50176\n" \
"v_mfma_f32_16x16x32_bf16 v[68:71], a[76:79], a[204:207], v[68:71]\n" \
"ds_read_b64_tr_b16 a[108:109], v11 offset:58368\n" \
"v_mfma_f32_16x16x32_bf16 v[76:79], a[72:75], a[208:211], v[76:79]\n" \
"s_add_u32 m0, s50, 77824\n" \
"ds_read_b64_tr_b16 a[110:111], v12 offset:58368\n" \
"v_mfma_f32_16x16x32_bf16 v[76:79], a[76:79], a[212:215], v[76:79]\n" \
"buffer_load_dwordx4 v16, s[28:31], s46 offen lds\n" \
"v_mfma_f32_16x16x32_bf16 v[84:87], a[72:75], a[216:219], v[84:87]\n" \
"v_mfma_f32_16x16x32_bf16 v[84:87], a[76:79], a[220:223], v[84:87]\n" \
"v_mfma_f32_16x16x32_bf16 v[92:95], a[72:75], a[224:227], v[92:95]\n" \
"v_mfma_f32_16x16x32_bf16 v[92:95], a[76:79], a[228:231], v[92:95]\n" \
"s_waitcnt lgkmcnt(12)\n" \
"v_mfma_f32_16x16x32_bf16 v[96:99], a[80:83], a[0:3], v[96:99]\n" \
"ds_read_b64_tr_b16 a[64:65], v9 offset:50688\n" \
"v_mfma_f32_16x16x32_bf16 v[96:99], a[84:87], a[4:7], v[96:99]\n" \
"buffer_load_dwordx2 v[58:59], v57, s[36:39], 0 offen sc1\n" \
"v_mfma_f32_16x16x32_bf16 v[104:107], a[80:83], a[8:11], v[104:107]\n" \
"ds_read_b64_tr_b16 a[66:67], v10 offset:50688\n" \
"v_mfma_f32_16x16x32_bf16 v[104:107], a[84:87], a[12:15], v[104:107]\n" \
"ds_read_b64_tr_b16 a[68:69], v9 offset:58880\n" \
"v_mfma_f32_16x16x32_bf16 v[112:115], a[80:83], a[16:19], v[112:115]\n" \
"s_mov_b32 s46, 1920\n" \
"s_add_u32 m0, s50, 81920\n" \
"ds_read_b64_tr_b16 a[70:71], v10 offset:58880\n" \
"v_mfma_f32_16x16x32_bf16 v[112:115], a[84:87], a[20:23], v[112:115]\n" \
"buffer_load_dwordx4 v17, s[28:31], s46 offen lds\n" \
"v_mfma_f32_16x16x32_bf16 v[120:123], a[80:83], a[24:27], v[120:123]\n" \
"s_waitcnt lgkmcnt(12)\n" \
"ds_read_b64_tr_b16 a[72:73], v11 offset:50688\n" \
"v_mfma_f32_16x16x32_bf16 v[120:123], a[84:87], a[28:31], v[120:123]\n" \
"ds_read_b64_tr_b16 a[74:75], v12 offset:50688\n" \
"v_mfma_f32_16x16x32_bf16 v[100:103], a[88:91], a[0:3], v[100:103]\n" \
"s_add_u32 m0, s50, 86016\n" \
"ds_read_b64_tr_b16 a[76:77], v11 offset:58880\n" \
"v_mfma_f32_16x16x32_bf16 v[100:103], a[92:95], a[4:7], v[100:103]\n" \
"buffer_load_dwordx4 v18, s[28:31], s46 offen lds\n" \
"v_mfma_f32_16x16x32_bf16 v[108:111], a[88:91], a[8:11], v[108:111]\n" \
"ds_read_b64_tr_b16 a[78:79], v12 offset:58880\n" \
"v_mfma_f32_16x16x32_bf16 v[108:111], a[92:95], a[12:15], v[108:111]\n" \
"ds_read_b64_tr_b16 a[80:81], v9 offset:49152\n" \
"v_mfma_f32_16x16x32_bf16 v[116:119], a[88:91], a[16:19], v[116:119]\n" \
"s_add_u32 m0, s50, 90112\n" \
"ds_read_b64_tr_b16 a[82:83], v10 offset:49152\n" \
"v_mfma_f32_16x16x32_bf16 v[116:119], a[92:95], a[20:23], v[116:119]\n" \
"buffer_load_dwordx4 v19, s[28:31], s46 offen lds\n" \
"v_mfma_f32_16x16x32_bf16 v[124:127], a[88:91], a[24:27], v[124:127]\n" \
"s_waitcnt lgkmcnt(14)\n" \
"ds_read_b64_tr_b16 a[84:85], v9 offset:57344\n" \
"v_mfma_f32_16x16x32_bf16 v[124:127], a[92:95], a[28:31], v[124:127]\n" \
"ds_read_b64_tr_b16 a[86:87], v10 offset:57344\n" \
"v_mfma_f32_16x16x32_bf16 v[128:131], a[96:99], a[0:3], v[128:131]\n" \
"s_add_u32 m0, s50, 94208\n" \
"ds_read_b64_tr_b16 a[88:89], v11 offset:49152\n" \
"v_mfma_f32_16x16x32_bf16 v[128:131], a[100:103], a[4:7], v[128:131]\n" \
"buffer_load_dwordx4 v20, s[28:31], s46 offen lds\n" \
"v_mfma_f32_16x16x32_bf16 v[136:139], a[96:99], a[8:11], v[136:139]\n" \
"ds_read_b64_tr_b16 a[90:91], v12 offset:49152\n" \
"v_mfma_f32_16x16x32_bf16 v[136:139], a[100:103], a[12:15], v[136:139]\n" \
"s_add_u32 s100, s100, 262144\ns_addc_u32 s101, s101, 0\n" \
"ds_read_b64_tr_b16 a[92:93], v11 offset:57344\n" \
"v_mfma_f32_16x16x32_bf16 v[144:147], a[96:99], a[16:19], v[144:147]\n" \
"s_mov_b32 s44, 0\n" \
"ds_read_b64_tr_b16 a[94:95], v12 offset:57344\n" \
"s_mov_b64 s[54:55], 0\n" \
"v_mfma_f32_16x16x32_bf16 v[144:147], a[100:103], a[20:23], v[144:147]\n" \
"s_mov_b64 exec, 0xffff\nglobal_load_dword v1, v2, s[100:101]\ns_mov_b64 exec, -1\n" \
"v_mfma_f32_16x16x32_bf16 v[152:155], a[96:99], a[24:27], v[152:155]\n" \
"s_waitcnt lgkmcnt(15)\n" \
"s_add_u32 s100, s100, 262144\ns_addc_u32 s101, s101, 0\n" \
"v_mfma_f32_16x16x32_bf16 v[152:155], a[100:103], a[28:31], v[152:155]\n" \
"s_mov_b64 exec, 0xffff\nglobal_load_dword v1, v2, s[100:101]\ns_mov_b64 exec, -1\n" \
"v_mfma_f32_16x16x32_bf16 v[132:135], a[104:107], a[0:3], v[132:135]\n" \
"s_load_dwordx2 s[28:29], %0, 0x50\n" \
"s_load_dwordx2 s[32:33], %0, 0x60\n" \
"s_lshl_b32 s79, %2, 11\n" \
"v_mfma_f32_16x16x32_bf16 v[132:135], a[108:111], a[4:7], v[132:135]\n" \
"s_lshl_b32 s20, s89, 21\n" \
"s_lshr_b32 s21, %1, 5\n" \
"s_lshl_b32 s21, s21, 8\n" \
"v_mfma_f32_16x16x32_bf16 v[140:143], a[104:107], a[8:11], v[140:143]\n" \
"s_add_u32 s18, s20, s21\n" \
"s_sub_u32 s30, 8388608, s18\n" \
"s_lshl_b32 s20, %2, 2\n" \
"v_mfma_f32_16x16x32_bf16 v[140:143], a[108:111], a[12:15], v[140:143]\n" \
"s_lshl_b32 s21, %2, 1\n" \
"v_bfe_u32 v29, v4, 6, 2\n" \
"v_add_u32 v29, s20, v29\n" \
"v_mfma_f32_16x16x32_bf16 v[148:151], a[104:107], a[16:19], v[148:151]\n" \
"s_waitcnt vmcnt(6)\n" \
"v_bfe_u32 v31, v4, 8, 1\n" \
"v_lshl_add_u32 v29, v31, 4, v29\n" \
"v_mfma_f32_16x16x32_bf16 v[148:151], a[108:111], a[20:23], v[148:151]\n" \
"s_mov_b32 s49, s22\n" \
"v_cmp_ne_u64 vcc, s[14:15], v[58:59]\n" \
"s_add_u32 s48, s22, 0x10000\n" \
"v_mfma_f32_16x16x32_bf16 v[156:159], a[104:107], a[24:27], v[156:159]\n" \
"s_waitcnt lgkmcnt(0)\n" \
"v_lshlrev_b32 v29, 11, v29\n" \
"v_add_u32 v31, s21, v31\n" \
"v_mfma_f32_16x16x32_bf16 v[156:159], a[108:111], a[28:31], v[156:159]\n" \
"s_cbranch_vccnz LPK_%=\nLPKR_%=:\n" \
"s_barrier\n" \
"v_lshrrev_b32 v28, 4, v4\n" \
"v_mfma_f32_16x16x32_bf16 v[160:163], a[64:67], a[0:3], v[160:163]\n" \
"s_lshl_b32 s60, s7, 1\n" \
"ds_read_b64_tr_b16 a[96:97], v9 offset:512\n" \
"v_xor_b32 v31, v31, v28\n" \
"v_mfma_f32_16x16x32_bf16 v[160:163], a[68:71], a[4:7], v[160:163]\n" \
"s_add_u32 s60, s60, %2\n" \
"ds_read_b64_tr_b16 a[98:99], v10 offset:512\n" \
"v_and_b32 v31, 3, v31\n" \
"v_mfma_f32_16x16x32_bf16 v[168:171], a[64:67], a[8:11], v[168:171]\n" \
"s_sub_u32 s46, s60, 1\n" \
"ds_read_b64_tr_b16 a[100:101], v9 offset:8704\n" \
"v_lshrrev_b32 v28, 9, v4\n" \
"v_mfma_f32_16x16x32_bf16 v[168:171], a[68:71], a[12:15], v[168:171]\n" \
"s_xor_b32 s62, %2, 3\n" \
"ds_read_b64_tr_b16 a[102:103], v10 offset:8704\n" \
"v_lshl_add_u32 v31, v28, 2, v31\n" \
"v_mfma_f32_16x16x32_bf16 v[176:179], a[64:67], a[16:19], v[176:179]\n" \
"s_lshl_b32 s20, %2, 9\n" \
"ds_read_b128 a[200:203], v5 offset:32768\n" \
"v_lshl_add_u32 v30, v31, 4, v29\n" \
"v_mfma_f32_16x16x32_bf16 v[176:179], a[68:71], a[20:23], v[176:179]\n" \
"s_add_u32 s20, s20, 0x20000\n" \
"ds_read_b128 a[204:207], v6 offset:32768\n" \
"v_add_u32 v13, s20, v9\n" \
"v_mfma_f32_16x16x32_bf16 v[184:187], a[64:67], a[24:27], v[184:187]\n" \
"ds_read_b128 a[208:211], v5 offset:34816\n" \
"v_add_u32 v14, s20, v10\n" \
"v_mfma_f32_16x16x32_bf16 v[184:187], a[68:71], a[28:31], v[184:187]\n" \
"ds_read_b128 a[212:215], v6 offset:34816\n" \
"v_add_u32 v15, s20, v11\n" \
"v_mfma_f32_16x16x32_bf16 v[164:167], a[72:75], a[0:3], v[164:167]\n" \
"ds_read_b128 a[216:219], v5 offset:36864\n" \
"v_add_u32 v16, s20, v12\n" \
"v_mfma_f32_16x16x32_bf16 v[164:167], a[76:79], a[4:7], v[164:167]\n" \
"ds_read_b128 a[220:223], v6 offset:36864\n" \
"v_lshlrev_b32 v20, 23, v4\n" \
"v_mfma_f32_16x16x32_bf16 v[172:175], a[72:75], a[8:11], v[172:175]\n" \
"ds_read_b128 a[224:227], v5 offset:38912\n" \
"v_mfma_f32_16x16x32_bf16 v[172:175], a[76:79], a[12:15], v[172:175]\n" \
"ds_read_b128 a[228:231], v6 offset:38912\n" \
"v_mfma_f32_16x16x32_bf16 v[180:183], a[72:75], a[16:19], v[180:183]\n" \
"ds_read_b64_tr_b16 a[104:105], v11 offset:512\n" \
"v_mfma_f32_16x16x32_bf16 v[180:183], a[76:79], a[20:23], v[180:183]\n" \
"ds_read_b64_tr_b16 a[106:107], v12 offset:512\n" \
"v_mfma_f32_16x16x32_bf16 v[188:191], a[72:75], a[24:27], v[188:191]\n" \
"ds_read_b64_tr_b16 a[108:109], v11 offset:8704\n" \
"v_mfma_f32_16x16x32_bf16 v[188:191], a[76:79], a[28:31], v[188:191]\n" \
"ds_read_b64_tr_b16 a[110:111], v12 offset:8704\n" \
"v_mfma_f32_16x16x32_bf16 v[64:67], a[80:83], a[0:3], v[64:67]\n" \
"ds_read_b64_tr_b16 a[64:65], v9 offset:1024\n" \
"v_mfma_f32_16x16x32_bf16 v[64:67], a[84:87], a[4:7], v[64:67]\n" \
"ds_read_b64_tr_b16 a[66:67], v10 offset:1024\n" \
"v_mfma_f32_16x16x32_bf16 v[72:75], a[80:83], a[8:11], v[72:75]\n" \
"ds_read_b64_tr_b16 a[68:69], v9 offset:9216\n" \
"v_mfma_f32_16x16x32_bf16 v[72:75], a[84:87], a[12:15], v[72:75]\n" \
"ds_read_b64_tr_b16 a[70:71], v10 offset:9216\n" \
"v_mfma_f32_16x16x32_bf16 v[80:83], a[80:83], a[16:19], v[80:83]\n" \
"ds_read_b64_tr_b16 a[72:73], v11 offset:1024\n" \
"v_mfma_f32_16x16x32_bf16 v[80:83], a[84:87], a[20:23], v[80:83]\n" \
"ds_read_b64_tr_b16 a[74:75], v12 offset:1024\n" \
"v_mfma_f32_16x16x32_bf16 v[88:91], a[80:83], a[24:27], v[88:91]\n" \
"ds_read_b64_tr_b16 a[76:77], v11 offset:9216\n" \
"v_mfma_f32_16x16x32_bf16 v[88:91], a[84:87], a[28:31], v[88:91]\n" \
"ds_read_b64_tr_b16 a[78:79], v12 offset:9216\n" \
"v_mfma_f32_16x16x32_bf16 v[68:71], a[88:91], a[0:3], v[68:71]\n" \
"s_cmp_lt_u32 %2, 2\ns_cselect_b32 s46, s46, 14\ns_cselect_b32 s62, s62, %2\ns_cselect_b32 s61, 56, 57\n" \
"v_mfma_f32_16x16x32_bf16 v[68:71], a[92:95], a[4:7], v[68:71]\n" \
"s_cmp_lt_u32 s7, 3\ns_cselect_b32 s45, 8, 0\n" \
"v_mfma_f32_16x16x32_bf16 v[76:79], a[88:91], a[8:11], v[76:79]\n" \
"s_cmp_lt_i32 s46, s45\ns_cselect_b32 s23, 99, s46\ns_cselect_b32 s24, s45, 99\n" \
"v_mfma_f32_16x16x32_bf16 v[76:79], a[92:95], a[12:15], v[76:79]\n" \
"s_min_i32 s46, s46, s45\n" \
"s_lshl_b32 s60, s60, 6\n" \
"s_add_u32 s60, s60, s6\n" \
"v_mfma_f32_16x16x32_bf16 v[84:87], a[88:91], a[16:19], v[84:87]\n" \
"s_lshl_b32 s50, s60, 13\n" \
"s_lshl_b32 s52, s60, 3\n" \
"s_add_u32 s52, s52, 94388224\n" \
"v_mfma_f32_16x16x32_bf16 v[84:87], a[92:95], a[20:23], v[84:87]\n" \
"s_lshr_b32 s60, %2, 1\n" \
"s_lshl_b32 s60, s60, 28\n" \
"s_add_u32 s52, s52, s60\n" \
"v_mfma_f32_16x16x32_bf16 v[92:95], a[88:91], a[24:27], v[92:95]\n" \
"s_lshl_b32 s62, s62, 1\n" \
"s_add_u32 s61, s61, s62\n" \
"s_lshl_b32 s60, s7, 2\n" \
"v_mfma_f32_16x16x32_bf16 v[92:95], a[92:95], a[28:31], v[92:95]\n" \
"s_waitcnt lgkmcnt(12)\n" \
"s_sub_u32 s61, s61, s60\n" \
"s_lshl_b32 s61, s61, 5\n" \
"v_mfma_f32_16x16x32_bf16 v[96:99], a[96:99], a[200:203], v[96:99]\n" \
"s_add_u32 m0, s22, 32768\n" \
"ds_read_b64_tr_b16 a[80:81], v9 offset:1536\n" \
"s_add_u32 s61, s61, s6\n" \
"v_mfma_f32_16x16x32_bf16 v[96:99], a[100:103], a[204:207], v[96:99]\n" \
"buffer_load_dwordx4 v60, s[40:43], s16 offen offset:0 sc1 lds\n" \
"v_mfma_f32_16x16x32_bf16 v[104:107], a[96:99], a[208:211], v[104:107]\n" \
"s_lshl_b32 s51, s61, 13\n" \
"ds_read_b64_tr_b16 a[82:83], v10 offset:1536\n" \
"s_lshl_b32 s53, s61, 3\n" \
"v_mfma_f32_16x16x32_bf16 v[104:107], a[100:103], a[212:215], v[104:107]\n" \
"s_add_u32 s53, s53, 94388224\n" \
"ds_read_b64_tr_b16 a[84:85], v9 offset:9728\n" \
"s_and_b32 s62, s62, 2\n" \
"v_mfma_f32_16x16x32_bf16 v[112:115], a[96:99], a[216:219], v[112:115]\n" \
"s_lshl_b32 s62, s62, 12\n" \
"ds_read_b64_tr_b16 a[86:87], v10 offset:9728\n" \
"s_add_u32 s62, s62, 131072\n" \
"v_mfma_f32_16x16x32_bf16 v[112:115], a[100:103], a[220:223], v[112:115]\n" \
"buffer_load_dwordx4 v60, s[40:43], s16 offen offset:1024 sc1 lds\n" \
"v_mfma_f32_16x16x32_bf16 v[120:123], a[96:99], a[224:227], v[120:123]\n" \
"s_waitcnt lgkmcnt(12)\n" \
"ds_read_b64_tr_b16 a[88:89], v11 offset:1536\n" \
"v_add_u32 v21, s62, v4\n" \
"v_mfma_f32_16x16x32_bf16 v[120:123], a[100:103], a[228:231], v[120:123]\n" \
"s_or_b32 s60, s7, %2\n" \
"ds_read_b64_tr_b16 a[90:91], v12 offset:1536\n" \
"v_mfma_f32_16x16x32_bf16 v[100:103], a[104:107], a[200:203], v[100:103]\n" \
"s_cmp_eq_u32 s60, 0\ns_cselect_b32 s60, 0xff800000, 0\n" \
"ds_read_b64_tr_b16 a[92:93], v11 offset:9728\n" \
"v_mfma_f32_16x16x32_bf16 v[100:103], a[108:111], a[204:207], v[100:103]\n" \
"buffer_load_dwordx4 v60, s[40:43], s16 offen offset:2048 sc1 lds\n" \
"v_mfma_f32_16x16x32_bf16 v[108:111], a[104:107], a[208:211], v[108:111]\n" \
"ds_read_b64_tr_b16 a[94:95], v12 offset:9728\n" \
"v_lshrrev_b32 v22, 6, v4\n" \
"v_mfma_f32_16x16x32_bf16 v[108:111], a[108:111], a[212:215], v[108:111]\n" \
"ds_read_b64_tr_b16 a[96:97], v9 offset:0\n" \
"v_and_b32 v22, 12, v22\n" \
"v_mfma_f32_16x16x32_bf16 v[116:119], a[104:107], a[216:219], v[116:119]\n" \
"ds_read_b64_tr_b16 a[98:99], v10 offset:0\n" \
"v_bfe_u32 v23, v4, 4, 4\n" \
"v_mfma_f32_16x16x32_bf16 v[116:119], a[108:111], a[220:223], v[116:119]\n" \
"buffer_load_dwordx4 v60, s[40:43], s16 offen offset:3072 sc1 lds\n" \
"v_mfma_f32_16x16x32_bf16 v[124:127], a[104:107], a[224:227], v[124:127]\n" \
"s_waitcnt lgkmcnt(14)\n" \
"ds_read_b64_tr_b16 a[100:101], v9 offset:8192\n" \
"v_sub_u32 v22, v23, v22\n" \
"v_mfma_f32_16x16x32_bf16 v[124:127], a[108:111], a[228:231], v[124:127]\n" \
"ds_read_b64_tr_b16 a[102:103], v10 offset:8192\n" \
"v_subrev_u32 v240, 0, v22\n" \
"v_mfma_f32_16x16x32_bf16 v[128:131], a[64:67], a[200:203], v[128:131]\n" \
"s_add_u32 s16, s16, s9\n" \
"s_add_u32 m0, s22, 65536\n" \
"ds_read_b64_tr_b16 a[104:105], v11 offset:0\n" \
"v_mfma_f32_16x16x32_bf16 v[128:131], a[68:71], a[204:207], v[128:131]\n" \
"buffer_load_dwordx4 v60, s[40:43], s16 offen offset:0 sc1 lds\n" \
"v_mfma_f32_16x16x32_bf16 v[136:139], a[64:67], a[208:211], v[136:139]\n" \
"ds_read_b64_tr_b16 a[106:107], v12 offset:0\n" \
"v_ashrrev_i32 v240, 31, v240\n" \
"v_mfma_f32_16x16x32_bf16 v[136:139], a[68:71], a[212:215], v[136:139]\n" \
"ds_read_b64_tr_b16 a[108:109], v11 offset:8192\n" \
"v_and_b32 v240, 0xff800000, v240\n" \
"v_mfma_f32_16x16x32_bf16 v[144:147], a[64:67], a[216:219], v[144:147]\n" \
"ds_read_b64_tr_b16 a[110:111], v12 offset:8192\n" \
"v_and_b32 v24, s60, v240\n" \
"v_mfma_f32_16x16x32_bf16 v[144:147], a[68:71], a[220:223], v[144:147]\n" \
"buffer_load_dwordx4 v60, s[40:43], s16 offen offset:1024 sc1 lds\n" \
"v_mfma_f32_16x16x32_bf16 v[152:155], a[64:67], a[224:227], v[152:155]\n" \
"s_waitcnt lgkmcnt(15)\n" \
"v_mov_b32 v244, s60\n" \
"v_subrev_u32 v241, 1, v22\n" \
"v_mfma_f32_16x16x32_bf16 v[152:155], a[68:71], a[228:231], v[152:155]\n" \
"v_ashrrev_i32 v241, 31, v241\n" \
"v_and_b32 v241, 0xff800000, v241\n" \
"v_mfma_f32_16x16x32_bf16 v[132:135], a[72:75], a[200:203], v[132:135]\n" \
"v_and_b32 v25, s60, v241\n" \
"v_mfma_f32_16x16x32_bf16 v[132:135], a[76:79], a[204:207], v[132:135]\n" \
"buffer_load_dwordx4 v60, s[40:43], s16 offen offset:2048 sc1 lds\n" \
"v_mfma_f32_16x16x32_bf16 v[140:143], a[72:75], a[208:211], v[140:143]\n" \
"v_mov_b32 v245, s60\n" \
"v_subrev_u32 v242, 2, v22\n" \
"v_mfma_f32_16x16x32_bf16 v[140:143], a[76:79], a[212:215], v[140:143]\n" \
"v_ashrrev_i32 v242, 31, v242\n" \
"v_and_b32 v242, 0xff800000, v242\n" \
"v_mfma_f32_16x16x32_bf16 v[148:151], a[72:75], a[216:219], v[148:151]\n" \
"v_and_b32 v26, s60, v242\n" \
"v_mfma_f32_16x16x32_bf16 v[148:151], a[76:79], a[220:223], v[148:151]\n" \
"buffer_load_dwordx4 v60, s[40:43], s16 offen offset:3072 sc1 lds\n" \
"v_mfma_f32_16x16x32_bf16 v[156:159], a[72:75], a[224:227], v[156:159]\n" \
"s_waitcnt vmcnt(10) lgkmcnt(0)\n" \
"v_mov_b32 v246, s60\n" \
"v_subrev_u32 v243, 3, v22\n" \
"v_mfma_f32_16x16x32_bf16 v[156:159], a[76:79], a[228:231], v[156:159]\n" \
"s_barrier\n" \
"v_ashrrev_i32 v243, 31, v243\n" \
"v_and_b32 v243, 0xff800000, v243\n" \
"v_mfma_f32_16x16x32_bf16 v[160:163], a[80:83], a[200:203], v[160:163]\n" \
"s_add_u32 m0, s22, 81920\n" \
"ds_read_b64_tr_b16 a[64:65], v9 offset:16384\n" \
"v_and_b32 v27, s60, v243\n" \
"v_mfma_f32_16x16x32_bf16 v[160:163], a[84:87], a[204:207], v[160:163]\n" \
"buffer_load_dwordx4 v60, s[40:43], s17 offen offset:0 sc1 lds\n" \
"v_mfma_f32_16x16x32_bf16 v[168:171], a[80:83], a[208:211], v[168:171]\n" \
"s_add_u32 s28, s28, s18\ns_addc_u32 s29, s29, 0\n" \
"ds_read_b64_tr_b16 a[66:67], v10 offset:16384\n" \
"v_mfma_f32_16x16x32_bf16 v[168:171], a[84:87], a[212:215], v[168:171]\n" \
"s_and_b32 s29, s29, 0xffff\n" \
"ds_read_b64_tr_b16 a[68:69], v9 offset:24576\n" \
"s_and_b32 s33, s33, 0xffff\n" \
"v_mfma_f32_16x16x32_bf16 v[176:179], a[80:83], a[216:219], v[176:179]\n" \
"s_mov_b32 s34, 4194304\n" \
"ds_read_b64_tr_b16 a[70:71], v10 offset:24576\n" \
"s_mov_b32 s35, 0x00020000\n" \
"v_mfma_f32_16x16x32_bf16 v[176:179], a[84:87], a[220:223], v[176:179]\n" \
"buffer_load_dwordx4 v60, s[40:43], s17 offen offset:1024 sc1 lds\n" \
"v_mfma_f32_16x16x32_bf16 v[184:187], a[80:83], a[224:227], v[184:187]\n" \
"ds_read_b128 a[0:3], v7 offset:0\n" \
"v_mov_b32 v247, s60\n" \
"v_mfma_f32_16x16x32_bf16 v[184:187], a[84:87], a[228:231], v[184:187]\n" \
"ds_read_b128 a[4:7], v8 offset:0\n" \
"s_load_dwordx2 s[60:61], %0, 0x0\n" \
"v_mfma_f32_16x16x32_bf16 v[164:167], a[88:91], a[200:203], v[164:167]\n" \
"ds_read_b128 a[8:11], v7 offset:2048\n" \
"v_mfma_f32_16x16x32_bf16 v[164:167], a[92:95], a[204:207], v[164:167]\n" \
"buffer_load_dwordx4 v60, s[40:43], s17 offen offset:2048 sc1 lds\n" \
"v_mfma_f32_16x16x32_bf16 v[172:175], a[88:91], a[208:211], v[172:175]\n" \
"ds_read_b128 a[12:15], v8 offset:2048\n" \
"v_mfma_f32_16x16x32_bf16 v[172:175], a[92:95], a[212:215], v[172:175]\n" \
"ds_read_b128 a[16:19], v7 offset:4096\n" \
"v_mfma_f32_16x16x32_bf16 v[180:183], a[88:91], a[216:219], v[180:183]\n" \
"ds_read_b128 a[20:23], v8 offset:4096\n" \
"v_mfma_f32_16x16x32_bf16 v[180:183], a[92:95], a[220:223], v[180:183]\n" \
"buffer_load_dwordx4 v60, s[40:43], s17 offen offset:3072 sc1 lds\n" \
"v_mfma_f32_16x16x32_bf16 v[188:191], a[88:91], a[224:227], v[188:191]\n" \
"ds_read_b128 a[24:27], v7 offset:6144\n" \
"v_mfma_f32_16x16x32_bf16 v[188:191], a[92:95], a[228:231], v[188:191]\n" \
"ds_read_b128 a[28:31], v8 offset:6144\n" \
"v_mfma_f32_16x16x32_bf16 v[64:67], a[96:99], a[200:203], v[64:67]\n" \
"s_add_u32 s16, s16, s9\n" \
"s_add_u32 m0, s22, 98304\n" \
"ds_read_b64_tr_b16 a[72:73], v11 offset:16384\n" \
"v_mfma_f32_16x16x32_bf16 v[64:67], a[100:103], a[204:207], v[64:67]\n" \
"buffer_load_dwordx4 v60, s[40:43], s16 offen offset:0 sc1 lds\n" \
"v_mfma_f32_16x16x32_bf16 v[72:75], a[96:99], a[208:211], v[72:75]\n" \
"ds_read_b64_tr_b16 a[74:75], v12 offset:16384\n" \
"v_mfma_f32_16x16x32_bf16 v[72:75], a[100:103], a[212:215], v[72:75]\n" \
"ds_read_b64_tr_b16 a[76:77], v11 offset:24576\n" \
"v_mfma_f32_16x16x32_bf16 v[80:83], a[96:99], a[216:219], v[80:83]\n" \
"ds_read_b64_tr_b16 a[78:79], v12 offset:24576\n" \
"v_mfma_f32_16x16x32_bf16 v[80:83], a[100:103], a[220:223], v[80:83]\n" \
"buffer_load_dwordx4 v60, s[40:43], s16 offen offset:1024 sc1 lds\n" \
"v_mfma_f32_16x16x32_bf16 v[88:91], a[96:99], a[224:227], v[88:91]\n" \
"ds_read_b64_tr_b16 a[80:81], v9 offset:16896\n" \
"v_mfma_f32_16x16x32_bf16 v[88:91], a[100:103], a[228:231], v[88:91]\n" \
"ds_read_b64_tr_b16 a[82:83], v10 offset:16896\n" \
"v_mfma_f32_16x16x32_bf16 v[68:71], a[104:107], a[200:203], v[68:71]\n" \
"ds_read_b64_tr_b16 a[84:85], v9 offset:25088\n" \
"v_mfma_f32_16x16x32_bf16 v[68:71], a[108:111], a[204:207], v[68:71]\n" \
"buffer_load_dwordx4 v60, s[40:43], s16 offen offset:2048 sc1 lds\n" \
"v_mfma_f32_16x16x32_bf16 v[76:79], a[104:107], a[208:211], v[76:79]\n" \
"ds_read_b64_tr_b16 a[86:87], v10 offset:25088\n" \
"v_mfma_f32_16x16x32_bf16 v[76:79], a[108:111], a[212:215], v[76:79]\n" \
"ds_read_b64_tr_b16 a[88:89], v11 offset:16896\n" \
"v_mfma_f32_16x16x32_bf16 v[84:87], a[104:107], a[216:219], v[84:87]\n" \
"ds_read_b64_tr_b16 a[90:91], v12 offset:16896\n" \
"v_mfma_f32_16x16x32_bf16 v[84:87], a[108:111], a[220:223], v[84:87]\n" \
"buffer_load_dwordx4 v60, s[40:43], s16 offen offset:3072 sc1 lds\n" \
"v_mfma_f32_16x16x32_bf16 v[92:95], a[104:107], a[224:227], v[92:95]\n" \
"ds_read_b64_tr_b16 a[92:93], v11 offset:25088\n" \
"v_mfma_f32_16x16x32_bf16 v[92:95], a[108:111], a[228:231], v[92:95]\n" \
"s_waitcnt lgkmcnt(11)\n" \
"ds_read_b64_tr_b16 a[94:95], v12 offset:25088\n" \
"s_mov_b64 exec, 0\ns_cmp_eq_u32 %2, 0\ns_cbranch_scc1 LH16_%=\ns_cmp_eq_u32 %2, 1\ns_cbranch_scc1 LH17_%=\ns_cmp_eq_u32 %2, 2\ns_cbranch_scc1 LH18_%=\ns_cmp_eq_u32 %2, 3\ns_cbranch_scc1 LH19_%=\nLHR1_%=:\ns_mov_b64 exec, -1\n" \
"v_mfma_f32_16x16x32_bf16 v[64:67], a[64:67], a[0:3], v[64:67]\n" \
"ds_read_b64_tr_b16 a[96:97], v9 offset:17408\n" \
"v_mfma_f32_16x16x32_bf16 v[64:67], a[68:71], a[4:7], v[64:67]\n" \
"ds_read_b64_tr_b16 a[98:99], v10 offset:17408\n" \
"v_mfma_f32_16x16x32_bf16 v[72:75], a[64:67], a[8:11], v[72:75]\n" \
"ds_read_b64_tr_b16 a[100:101], v9 offset:25600\n" \
"v_mfma_f32_16x16x32_bf16 v[72:75], a[68:71], a[12:15], v[72:75]\n" \
"ds_read_b64_tr_b16 a[102:103], v10 offset:25600\n" \
"v_mfma_f32_16x16x32_bf16 v[80:83], a[64:67], a[16:19], v[80:83]\n" \
"s_add_u32 s17, s17, s19\n" \
"s_add_u32 m0, s22, 114688\n" \
"ds_read_b64_tr_b16 a[104:105], v11 offset:17408\n" \
"v_mfma_f32_16x16x32_bf16 v[80:83], a[68:71], a[20:23], v[80:83]\n" \
"buffer_load_dwordx4 v60, s[40:43], s17 offen offset:0 sc1 lds\n" \
"v_mfma_f32_16x16x32_bf16 v[88:91], a[64:67], a[24:27], v[88:91]\n" \
"s_waitcnt lgkmcnt(13)\n" \
"ds_read_b64_tr_b16 a[106:107], v12 offset:17408\n" \
"v_mfma_f32_16x16x32_bf16 v[88:91], a[68:71], a[28:31], v[88:91]\n" \
"ds_read_b64_tr_b16 a[108:109], v11 offset:25600\n" \
"v_mfma_f32_16x16x32_bf16 v[68:71], a[72:75], a[0:3], v[68:71]\n" \
"ds_read_b64_tr_b16 a[110:111], v12 offset:25600\n" \
"v_mfma_f32_16x16x32_bf16 v[68:71], a[76:79], a[4:7], v[68:71]\n" \
"ds_read_b64_tr_b16 a[64:65], v9 offset:17920\n" \
"v_mfma_f32_16x16x32_bf16 v[76:79], a[72:75], a[8:11], v[76:79]\n" \
"ds_read_b64_tr_b16 a[66:67], v10 offset:17920\n" \
"v_mfma_f32_16x16x32_bf16 v[76:79], a[76:79], a[12:15], v[76:79]\n" \
"buffer_load_dwordx4 v60, s[40:43], s17 offen offset:1024 sc1 lds\n" \
"v_mfma_f32_16x16x32_bf16 v[84:87], a[72:75], a[16:19], v[84:87]\n" \
"ds_read_b64_tr_b16 a[68:69], v9 offset:26112\n" \
"v_cvt_pk_bf16_f32 v32, v64, v65\n" \
"v_mfma_f32_16x16x32_bf16 v[84:87], a[76:79], a[20:23], v[84:87]\n" \
"ds_read_b64_tr_b16 a[70:71], v10 offset:26112\n" \
"v_cvt_pk_bf16_f32 v33, v66, v67\n" \
"v_mfma_f32_16x16x32_bf16 v[92:95], a[72:75], a[24:27], v[92:95]\n" \
"s_waitcnt lgkmcnt(15)\n" \
"v_cvt_pk_bf16_f32 v34, v68, v69\n" \
"v_cvt_pk_bf16_f32 v35, v70, v71\n" \
"v_mfma_f32_16x16x32_bf16 v[92:95], a[76:79], a[28:31], v[92:95]\n" \
"v_cvt_pk_bf16_f32 v48, v72, v73\n" \
"v_cvt_pk_bf16_f32 v49, v74, v75\n" \
"v_mfma_f32_16x16x32_bf16 v[96:99], a[80:83], a[0:3], v[96:99]\n" \
"s_lshl_b32 s25, s45, 2\n" \
"ds_read_b64_tr_b16 a[72:73], v11 offset:17920\n" \
"s_and_b32 s25, s25, 32\n" \
"v_mfma_f32_16x16x32_bf16 v[96:99], a[84:87], a[4:7], v[96:99]\n" \
"buffer_load_dwordx4 v60, s[40:43], s17 offen offset:2048 sc1 lds\n" \
"v_mfma_f32_16x16x32_bf16 v[104:107], a[80:83], a[8:11], v[104:107]\n" \
"s_add_u32 s25, s25, %2\n" \
"ds_read_b64_tr_b16 a[74:75], v12 offset:17920\n" \
"v_cvt_pk_bf16_f32 v50, v76, v77\n" \
"v_mfma_f32_16x16x32_bf16 v[104:107], a[84:87], a[12:15], v[104:107]\n" \
"s_cmp_ge_u32 s25, 34\ns_cselect_b64 vcc, -1, 0\n" \
"ds_read_b64_tr_b16 a[76:77], v11 offset:26112\n" \
"v_mfma_f32_16x16x32_bf16 v[112:115], a[80:83], a[16:19], v[112:115]\n" \
"ds_read_b64_tr_b16 a[78:79], v12 offset:26112\n" \
"v_cvt_pk_bf16_f32 v51, v78, v79\n" \
"v_mfma_f32_16x16x32_bf16 v[112:115], a[84:87], a[20:23], v[112:115]\n" \
"buffer_load_dwordx4 v60, s[40:43], s17 offen offset:3072 sc1 lds\n" \
"v_mfma_f32_16x16x32_bf16 v[120:123], a[80:83], a[24:27], v[120:123]\n" \
"s_waitcnt lgkmcnt(15)\n" \
"v_cvt_pk_bf16_f32 v192, v80, v81\n" \
"v_cvt_pk_bf16_f32 v193, v82, v83\n" \
"v_mfma_f32_16x16x32_bf16 v[120:123], a[84:87], a[28:31], v[120:123]\n" \
"v_cvt_pk_bf16_f32 v194, v84, v85\n" \
"v_cvt_pk_bf16_f32 v195, v86, v87\n" \
"v_mfma_f32_16x16x32_bf16 v[100:103], a[88:91], a[0:3], v[100:103]\n" \
"v_cvt_pk_bf16_f32 v208, v88, v89\n" \
"v_mfma_f32_16x16x32_bf16 v[100:103], a[92:95], a[4:7], v[100:103]\n" \
"s_waitcnt vmcnt(16) lgkmcnt(0)\n" \
"v_cvt_pk_bf16_f32 v209, v90, v91\n" \
"v_cvt_pk_bf16_f32 v210, v92, v93\n" \
"v_mfma_f32_16x16x32_bf16 v[108:111], a[88:91], a[8:11], v[108:111]\n" \
"s_barrier\n" \
"v_cvt_pk_bf16_f32 v211, v94, v95\n" \
"v_mfma_f32_16x16x32_bf16 v[108:111], a[92:95], a[12:15], v[108:111]\n" \
"v_add_u32 v5, 0x2000, v4\n" \
"v_mov_b32 v6, v60\n" \
"v_mfma_f32_16x16x32_bf16 v[116:119], a[88:91], a[16:19], v[116:119]\n" \
"s_add_u32 s16, s16, s9\n" \
"v_add_u32 v7, 0x1000, v4\n" \
"s_add_u32 m0, s49, 0\n" \
"v_mfma_f32_16x16x32_bf16 v[116:119], a[92:95], a[20:23], v[116:119]\n" \
"buffer_load_dwordx4 v6, s[40:43], s16 offen offset:0 sc1 lds\n" \
"v_mfma_f32_16x16x32_bf16 v[124:127], a[88:91], a[24:27], v[124:127]\n" \
"ds_read_b128 a[128:131], v5 offset:32768\n" \
"v_cvt_pk_bf16_f32 v36, v96, v97\n" \
"v_mfma_f32_16x16x32_bf16 v[124:127], a[92:95], a[28:31], v[124:127]\n" \
"s_cbranch_vccz LQW1_%=\nds_write_b128 v21, v[32:35] offset:16384\nLQW1_%=:\n" \
"v_mfma_f32_16x16x32_bf16 v[128:131], a[96:99], a[0:3], v[128:131]\n" \
"s_cbranch_vccz LQW2_%=\nds_write_b128 v21, v[48:51] offset:20480\nLQW2_%=:\n" \
"v_mfma_f32_16x16x32_bf16 v[128:131], a[100:103], a[4:7], v[128:131]\n" \
"buffer_load_dwordx4 v6, s[40:43], s16 offen offset:1024 sc1 lds\n" \
"v_mfma_f32_16x16x32_bf16 v[136:139], a[96:99], a[8:11], v[136:139]\n" \
"ds_read_b128 a[132:135], v5 offset:33792\n" \
"v_cvt_pk_bf16_f32 v37, v98, v99\n" \
"v_mfma_f32_16x16x32_bf16 v[136:139], a[100:103], a[12:15], v[136:139]\n" \
"s_cbranch_vccz LQW3_%=\nds_write_b128 v21, v[192:195] offset:0\nLQW3_%=:\n" \
"v_mfma_f32_16x16x32_bf16 v[144:147], a[96:99], a[16:19], v[144:147]\n" \
"v_cvt_pk_bf16_f32 v38, v100, v101\n" \
"v_cvt_pk_bf16_f32 v39, v102, v103\n" \
"v_mfma_f32_16x16x32_bf16 v[144:147], a[100:103], a[20:23], v[144:147]\n" \
"buffer_load_dwordx4 v6, s[40:43], s16 offen offset:2048 sc1 lds\n" \
"v_mfma_f32_16x16x32_bf16 v[152:155], a[96:99], a[24:27], v[152:155]\n" \
"ds_read_b128 a[144:147], v5 offset:36864\n" \
"v_cvt_pk_bf16_f32 v52, v104, v105\n" \
"v_mfma_f32_16x16x32_bf16 v[152:155], a[100:103], a[28:31], v[152:155]\n" \
"s_cbranch_vccz LQW4_%=\nds_write_b128 v21, v[208:211] offset:4096\nLQW4_%=:\n" \
"v_mfma_f32_16x16x32_bf16 v[132:135], a[104:107], a[0:3], v[132:135]\n" \
"s_cbranch_vccz LQW5_%=\nds_write_b128 v21, v[36:39] offset:17408\nLQW5_%=:\n" \
"v_mfma_f32_16x16x32_bf16 v[132:135], a[108:111], a[4:7], v[132:135]\n" \
"buffer_load_dwordx4 v6, s[40:43], s16 offen offset:3072 sc1 lds\n" \
"v_mfma_f32_16x16x32_bf16 v[140:143], a[104:107], a[8:11], v[140:143]\n" \
"s_add_u32 s17, s17, s19\n" \
"ds_read_b128 a[148:151], v5 offset:37888\n" \
"s_add_u32 m0, s49, 16384\n" \
"v_mfma_f32_16x16x32_bf16 v[140:143], a[108:111], a[12:15], v[140:143]\n" \
"v_cvt_pk_bf16_f32 v53, v106, v107\n" \
"v_cvt_pk_bf16_f32 v54, v108, v109\n" \
"v_mfma_f32_16x16x32_bf16 v[148:151], a[104:107], a[16:19], v[148:151]\n" \
"v_cvt_pk_bf16_f32 v55, v110, v111\n" \
"v_cvt_pk_bf16_f32 v196, v112, v113\n" \
"v_mfma_f32_16x16x32_bf16 v[148:151], a[108:111], a[20:23], v[148:151]\n" \
"buffer_load_dwordx4 v6, s[40:43], s17 offen offset:0 sc1 lds\n" \
"v_mfma_f32_16x16x32_bf16 v[156:159], a[104:107], a[24:27], v[156:159]\n" \
"LH14_%=:\ns_cbranch_execz LHR0_%=\n" \
"ds_read_b128 a[136:139], v5 offset:34816\n" \
"v_cvt_pk_bf16_f32 v197, v114, v115\n" \
"v_mfma_f32_16x16x32_bf16 v[156:159], a[108:111], a[28:31], v[156:159]\n" \
"s_cbranch_vccz LQW6_%=\nds_write_b128 v21, v[52:55] offset:21504\nLQW6_%=:\n" \
"v_mfma_f32_16x16x32_bf16 v[160:163], a[64:67], a[0:3], v[160:163]\n" \
"v_cvt_pk_bf16_f32 v198, v116, v117\n" \
"v_cvt_pk_bf16_f32 v199, v118, v119\n" \
"v_mfma_f32_16x16x32_bf16 v[160:163], a[68:71], a[4:7], v[160:163]\n" \
"buffer_load_dwordx4 v6, s[40:43], s17 offen offset:1024 sc1 lds\n" \
"v_mfma_f32_16x16x32_bf16 v[168:171], a[64:67], a[8:11], v[168:171]\n" \
"ds_read_b128 a[140:143], v5 offset:35840\n" \
"v_cvt_pk_bf16_f32 v212, v120, v121\n" \
"v_mfma_f32_16x16x32_bf16 v[168:171], a[68:71], a[12:15], v[168:171]\n" \
"s_cbranch_vccz LQW7_%=\nds_write_b128 v21, v[196:199] offset:1024\nLQW7_%=:\n" \
"v_mfma_f32_16x16x32_bf16 v[176:179], a[64:67], a[16:19], v[176:179]\n" \
"v_cvt_pk_bf16_f32 v213, v122, v123\n" \
"v_cvt_pk_bf16_f32 v214, v124, v125\n" \
"v_mfma_f32_16x16x32_bf16 v[176:179], a[68:71], a[20:23], v[176:179]\n" \
"buffer_load_dwordx4 v6, s[40:43], s17 offen offset:2048 sc1 lds\n" \
"v_mfma_f32_16x16x32_bf16 v[184:187], a[64:67], a[24:27], v[184:187]\n" \
"ds_read_b128 a[152:155], v5 offset:38912\n" \
"v_cvt_pk_bf16_f32 v215, v126, v127\n" \
"v_mfma_f32_16x16x32_bf16 v[184:187], a[68:71], a[28:31], v[184:187]\n" \
"s_cbranch_vccz LQW8_%=\nds_write_b128 v21, v[212:215] offset:5120\nLQW8_%=:\n" \
"v_mfma_f32_16x16x32_bf16 v[164:167], a[72:75], a[0:3], v[164:167]\n" \
"v_cvt_pk_bf16_f32 v40, v128, v129\n" \
"v_cvt_pk_bf16_f32 v41, v130, v131\n" \
"v_mfma_f32_16x16x32_bf16 v[164:167], a[76:79], a[4:7], v[164:167]\n" \
"buffer_load_dwordx4 v6, s[40:43], s17 offen offset:3072 sc1 lds\n" \
"v_mfma_f32_16x16x32_bf16 v[172:175], a[72:75], a[8:11], v[172:175]\n" \
"ds_read_b128 a[156:159], v5 offset:39936\n" \
"v_cvt_pk_bf16_f32 v42, v132, v133\n" \
"v_mfma_f32_16x16x32_bf16 v[172:175], a[76:79], a[12:15], v[172:175]\n" \
"v_cvt_pk_bf16_f32 v43, v134, v135\n" \
"v_cvt_pk_bf16_f32 v56, v136, v137\n" \
"v_mfma_f32_16x16x32_bf16 v[180:183], a[72:75], a[16:19], v[180:183]\n" \
"s_cbranch_vccz LQW9_%=\nds_write_b128 v21, v[40:43] offset:18432\nLQW9_%=:\n" \
"v_mfma_f32_16x16x32_bf16 v[180:183], a[76:79], a[20:23], v[180:183]\n" \
"v_cvt_pk_bf16_f32 v57, v138, v139\n" \
"v_cvt_pk_bf16_f32 v58, v140, v141\n" \
"v_mfma_f32_16x16x32_bf16 v[188:191], a[72:75], a[24:27], v[188:191]\n" \
"v_xor_b32 v5, 0x10000, v5\n" \
"v_cvt_pk_bf16_f32 v59, v142, v143\n" \
"v_mfma_f32_16x16x32_bf16 v[188:191], a[76:79], a[28:31], v[188:191]\n" \
"s_waitcnt lgkmcnt(0)\n" \
"s_cbranch_vccz LQW10_%=\nds_write_b128 v21, v[56:59] offset:22528\nLQW10_%=:\n" \
"v_mfma_f32_16x16x32_bf16 v[64:67], a[128:131], v[32:35], v[24:27]\n" \
"v_cvt_pk_bf16_f32 v200, v144, v145\n" \
"v_cvt_pk_bf16_f32 v201, v146, v147\n" \
"v_mfma_f32_16x16x32_bf16 v[64:67], a[132:135], v[36:39], v[64:67]\n" \
"s_waitcnt vmcnt(16)\n" \
"v_cvt_pk_bf16_f32 v202, v148, v149\n" \
"v_cvt_pk_bf16_f32 v203, v150, v151\n" \
"v_mfma_f32_16x16x32_bf16 v[72:75], a[128:131], v[48:51], 0\n" \
"s_barrier\n" \
"s_cbranch_vccz LQW11_%=\nds_write_b128 v21, v[200:203] offset:2048\nLQW11_%=:\n" \
"v_mfma_f32_16x16x32_bf16 v[72:75], a[132:135], v[52:55], v[72:75]\n" \
"v_cvt_pk_bf16_f32 v216, v152, v153\n" \
"v_cvt_pk_bf16_f32 v217, v154, v155\n" \
"v_mfma_f32_16x16x32_bf16 v[80:83], a[128:131], v[192:195], 0\n" \
"v_cvt_pk_bf16_f32 v218, v156, v157\n" \
"v_cvt_pk_bf16_f32 v219, v158, v159\n" \
"v_mfma_f32_16x16x32_bf16 v[80:83], a[132:135], v[196:199], v[80:83]\n" \
"s_cbranch_vccz LQW12_%=\nds_write_b128 v21, v[216:219] offset:6144\nLQW12_%=:\n" \
"v_mfma_f32_16x16x32_bf16 v[88:91], a[128:131], v[208:211], 0\n" \
"v_cvt_pk_bf16_f32 v44, v160, v161\n" \
"v_cvt_pk_bf16_f32 v45, v162, v163\n" \
"v_mfma_f32_16x16x32_bf16 v[88:91], a[132:135], v[212:215], v[88:91]\n" \
"ds_read_b128 a[128:131], v5 offset:0\n" \
"v_cvt_pk_bf16_f32 v46, v164, v165\n" \
"v_mfma_f32_16x16x32_bf16 v[68:71], a[144:147], v[32:35], v[244:247]\n" \
"ds_read_b128 a[132:135], v5 offset:1024\n" \
"v_cvt_pk_bf16_f32 v47, v166, v167\n" \
"v_mfma_f32_16x16x32_bf16 v[68:71], a[148:151], v[36:39], v[68:71]\n" \
"s_cbranch_vccz LQW13_%=\nds_write_b128 v21, v[44:47] offset:19456\nLQW13_%=:\n" \
"v_mfma_f32_16x16x32_bf16 v[76:79], a[144:147], v[48:51], v[24:27]\n" \
"v_cvt_pk_bf16_f32 v60, v168, v169\n" \
"v_cvt_pk_bf16_f32 v61, v170, v171\n" \
"v_mfma_f32_16x16x32_bf16 v[76:79], a[148:151], v[52:55], v[76:79]\n" \
"v_cvt_pk_bf16_f32 v62, v172, v173\n" \
"v_cvt_pk_bf16_f32 v63, v174, v175\n" \
"v_mfma_f32_16x16x32_bf16 v[84:87], a[144:147], v[192:195], 0\n" \
"s_cbranch_vccz LQW14_%=\nds_write_b128 v21, v[60:63] offset:23552\nLQW14_%=:\n" \
"v_mfma_f32_16x16x32_bf16 v[84:87], a[148:151], v[196:199], v[84:87]\n" \
"v_cvt_pk_bf16_f32 v204, v176, v177\n" \
"v_cvt_pk_bf16_f32 v205, v178, v179\n" \
"v_mfma_f32_16x16x32_bf16 v[92:95], a[144:147], v[208:211], 0\n" \
"v_cvt_pk_bf16_f32 v206, v180, v181\n" \
"v_cvt_pk_bf16_f32 v207, v182, v183\n" \
"v_mfma_f32_16x16x32_bf16 v[92:95], a[148:151], v[212:215], v[92:95]\n" \
"ds_read_b128 a[144:147], v5 offset:4096\n" \
"v_cvt_pk_bf16_f32 v220, v184, v185\n" \
"v_mfma_f32_16x16x32_bf16 v[64:67], a[136:139], v[40:43], v[64:67]\n" \
"ds_read_b128 a[148:151], v5 offset:5120\n" \
"v_cvt_pk_bf16_f32 v221, v186, v187\n" \
"v_mfma_f32_16x16x32_bf16 v[64:67], a[140:143], v[44:47], v[64:67]\n" \
"s_cbranch_vccz LQW15_%=\nds_write_b128 v21, v[204:207] offset:3072\nLQW15_%=:\n" \
"v_mfma_f32_16x16x32_bf16 v[72:75], a[136:139], v[56:59], v[72:75]\n" \
"v_cvt_pk_bf16_f32 v222, v188, v189\n" \
"v_cvt_pk_bf16_f32 v223, v190, v191\n" \
"v_mfma_f32_16x16x32_bf16 v[72:75], a[140:143], v[60:63], v[72:75]\n" \
"s_cbranch_vccz LQW16_%=\nds_write_b128 v21, v[220:223] offset:7168\nLQW16_%=:\n" \
"v_mfma_f32_16x16x32_bf16 v[80:83], a[136:139], v[200:203], v[80:83]\n" \
"v_exp_f32 v64, v64\n" \
"v_mfma_f32_16x16x32_bf16 v[80:83], a[140:143], v[204:207], v[80:83]\n" \
"v_exp_f32 v65, v65\n" \
"v_mfma_f32_16x16x32_bf16 v[88:91], a[136:139], v[216:219], v[88:91]\n" \
"v_exp_f32 v66, v66\n" \
"v_mfma_f32_16x16x32_bf16 v[88:91], a[140:143], v[220:223], v[88:91]\n" \
"ds_read_b128 a[136:139], v5 offset:2048\n" \
"v_accvgpr_write_b32 a64, v192\n" \
"v_mfma_f32_16x16x32_bf16 v[68:71], a[152:155], v[40:43], v[68:71]\n" \
"ds_read_b128 a[140:143], v5 offset:3072\n" \
"v_accvgpr_write_b32 a65, v193\n" \
"v_mfma_f32_16x16x32_bf16 v[68:71], a[156:159], v[44:47], v[68:71]\n" \
"v_exp_f32 v67, v67\n" \
"v_mfma_f32_16x16x32_bf16 v[76:79], a[152:155], v[56:59], v[76:79]\n" \
"v_exp_f32 v72, v72\n" \
"v_mfma_f32_16x16x32_bf16 v[76:79], a[156:159], v[60:63], v[76:79]\n" \
"v_exp_f32 v73, v73\n" \
"v_mfma_f32_16x16x32_bf16 v[84:87], a[152:155], v[200:203], v[84:87]\n" \
"v_exp_f32 v74, v74\n" \
"v_mfma_f32_16x16x32_bf16 v[84:87], a[156:159], v[204:207], v[84:87]\n" \
"v_exp_f32 v75, v75\n" \
"v_mfma_f32_16x16x32_bf16 v[92:95], a[152:155], v[216:219], v[92:95]\n" \
"v_exp_f32 v80, v80\n" \
"v_mfma_f32_16x16x32_bf16 v[92:95], a[156:159], v[220:223], v[92:95]\n" \
"s_waitcnt lgkmcnt(0)\n" \
"v_exp_f32 v81, v81\n" \
"v_mfma_f32_16x16x32_bf16 v[96:99], a[128:131], v[32:35], v[244:247]\n" \
"v_exp_f32 v82, v82\n" \
"v_mfma_f32_16x16x32_bf16 v[96:99], a[132:135], v[36:39], v[96:99]\n" \
"s_add_u32 s16, s16, s9\n" \
"v_exp_f32 v83, v83\n" \
"v_mfma_f32_16x16x32_bf16 v[104:107], a[128:131], v[48:51], v[244:247]\n" \
"ds_read_b128 a[152:155], v5 offset:6144\n" \
"v_cvt_pk_bf16_f32 v128, v64, v65\n" \
"v_mfma_f32_16x16x32_bf16 v[104:107], a[132:135], v[52:55], v[104:107]\n" \
"s_add_u32 m0, s49, 32768\n" \
"v_exp_f32 v88, v88\n" \
"v_mfma_f32_16x16x32_bf16 v[112:115], a[128:131], v[192:195], v[24:27]\n" \
"buffer_load_dwordx4 v6, s[40:43], s16 offen offset:0 sc1 lds\n" \
"v_mfma_f32_16x16x32_bf16 v[112:115], a[132:135], v[196:199], v[112:115]\n" \
"v_exp_f32 v89, v89\n" \
"v_mfma_f32_16x16x32_bf16 v[120:123], a[128:131], v[208:211], 0\n" \
"ds_read_b128 a[156:159], v5 offset:7168\n" \
"v_cvt_pk_bf16_f32 v129, v66, v67\n" \
"v_mfma_f32_16x16x32_bf16 v[120:123], a[132:135], v[212:215], v[120:123]\n" \
"s_waitcnt lgkmcnt(4)\n" \
"v_exp_f32 v90, v90\n" \
"v_mfma_f32_16x16x32_bf16 v[96:99], a[136:139], v[40:43], v[96:99]\n" \
"v_exp_f32 v91, v91\n" \
"v_mfma_f32_16x16x32_bf16 v[96:99], a[140:143], v[44:47], v[96:99]\n" \
"v_exp_f32 v68, v68\n" \
"v_mfma_f32_16x16x32_bf16 v[104:107], a[136:139], v[56:59], v[104:107]\n" \
"ds_read_b128 a[128:131], v5 offset:16384\n" \
"v_cvt_pk_bf16_f32 v132, v72, v73\n" \
"v_mfma_f32_16x16x32_bf16 v[104:107], a[140:143], v[60:63], v[104:107]\n" \
"v_exp_f32 v69, v69\n" \
"v_mfma_f32_16x16x32_bf16 v[112:115], a[136:139], v[200:203], v[112:115]\n" \
"buffer_load_dwordx4 v6, s[40:43], s16 offen offset:1024 sc1 lds\n" \
"v_mfma_f32_16x16x32_bf16 v[112:115], a[140:143], v[204:207], v[112:115]\n" \
"v_exp_f32 v70, v70\n" \
"v_mfma_f32_16x16x32_bf16 v[120:123], a[136:139], v[216:219], v[120:123]\n" \
"ds_read_b128 a[132:135], v5 offset:17408\n" \
"v_cvt_pk_bf16_f32 v133, v74, v75\n" \
"v_mfma_f32_16x16x32_bf16 v[120:123], a[140:143], v[220:223], v[120:123]\n" \
"s_waitcnt lgkmcnt(4)\n" \
"v_exp_f32 v71, v71\n" \
"v_mfma_f32_16x16x32_bf16 v[100:103], a[144:147], v[32:35], v[244:247]\n" \
"v_exp_f32 v76, v76\n" \
"v_mfma_f32_16x16x32_bf16 v[100:103], a[148:151], v[36:39], v[100:103]\n" \
"v_exp_f32 v77, v77\n" \
"v_mfma_f32_16x16x32_bf16 v[108:111], a[144:147], v[48:51], v[244:247]\n" \
"ds_read_b128 a[136:139], v5 offset:18432\n" \
"v_cvt_pk_bf16_f32 v136, v80, v81\n" \
"v_mfma_f32_16x16x32_bf16 v[108:111], a[148:151], v[52:55], v[108:111]\n" \
"v_exp_f32 v78, v78\n" \
"v_accvgpr_write_b32 a66, v194\n" \
"v_accvgpr_write_b32 a67, v195\n" \
"v_mfma_f32_16x16x32_bf16 v[116:119], a[144:147], v[192:195], v[244:247]\n" \
"buffer_load_dwordx4 v6, s[40:43], s16 offen offset:2048 sc1 lds\n" \
"v_accvgpr_write_b32 a80, v208\n" \
"v_accvgpr_write_b32 a81, v209\n" \
"v_mfma_f32_16x16x32_bf16 v[116:119], a[148:151], v[196:199], v[116:119]\n" \
"v_exp_f32 v79, v79\n" \
"v_accvgpr_write_b32 a82, v210\n" \
"v_accvgpr_write_b32 a83, v211\n" \
"v_mfma_f32_16x16x32_bf16 v[124:127], a[144:147], v[208:211], v[24:27]\n" \
"ds_read_b128 a[140:143], v5 offset:19456\n" \
"v_cvt_pk_bf16_f32 v137, v82, v83\n" \
"v_accvgpr_write_b32 a68, v196\n" \
"v_accvgpr_write_b32 a69, v197\n" \
"v_mfma_f32_16x16x32_bf16 v[124:127], a[148:151], v[212:215], v[124:127]\n" \
"s_waitcnt lgkmcnt(4)\n" \
"v_exp_f32 v84, v84\n" \
"v_accvgpr_write_b32 a70, v198\n" \
"v_accvgpr_write_b32 a71, v199\n" \
"v_mfma_f32_16x16x32_bf16 v[100:103], a[152:155], v[40:43], v[100:103]\n" \
"v_exp_f32 v85, v85\n" \
"v_accvgpr_write_b32 a84, v212\n" \
"v_accvgpr_write_b32 a85, v213\n" \
"v_mfma_f32_16x16x32_bf16 v[100:103], a[156:159], v[44:47], v[100:103]\n" \
"v_exp_f32 v86, v86\n" \
"v_accvgpr_write_b32 a86, v214\n" \
"v_accvgpr_write_b32 a87, v215\n" \
"v_mfma_f32_16x16x32_bf16 v[108:111], a[152:155], v[56:59], v[108:111]\n" \
"ds_read_b128 a[144:147], v5 offset:20480\n" \
"v_cvt_pk_bf16_f32 v140, v88, v89\n" \
"v_accvgpr_write_b32 a72, v200\n" \
"v_accvgpr_write_b32 a73, v201\n" \
"v_mfma_f32_16x16x32_bf16 v[108:111], a[156:159], v[60:63], v[108:111]\n" \
"v_exp_f32 v87, v87\n" \
"v_accvgpr_write_b32 a74, v202\n" \
"v_accvgpr_write_b32 a75, v203\n" \
"v_mov_b32 v180, 0x3f803f80\n" \
"v_mov_b32 v181, 0x3f803f80\n" \
"v_mov_b32 v182, 0x3f803f80\n" \
"v_mov_b32 v183, 0x3f803f80\n" \
"v_mfma_f32_16x16x32_bf16 v[116:119], a[152:155], v[200:203], v[116:119]\n" \
"buffer_load_dwordx4 v6, s[40:43], s16 offen offset:3072 sc1 lds\n" \
"v_accvgpr_write_b32 a88, v216\n" \
"v_accvgpr_write_b32 a89, v217\n" \
"v_mfma_f32_16x16x32_bf16 v[116:119], a[156:159], v[204:207], v[116:119]\n" \
"v_exp_f32 v92, v92\n" \
"v_accvgpr_write_b32 a90, v218\n" \
"v_accvgpr_write_b32 a91, v219\n" \
"v_mov_b32 v176, v240\n" \
"v_mov_b32 v177, v241\n" \
"v_mov_b32 v178, v242\n" \
"v_mov_b32 v179, v243\n" \
"v_mfma_f32_16x16x32_bf16 v[124:127], a[152:155], v[216:219], v[124:127]\n" \
"ds_read_b128 a[148:151], v5 offset:21504\n" \
"v_exp_f32 v93, v93\n" \
"v_cvt_pk_bf16_f32 v141, v90, v91\n" \
"v_cvt_pk_bf16_f32 v130, v68, v69\n" \
"v_cvt_pk_bf16_f32 v131, v70, v71\n" \
"v_accvgpr_write_b32 a76, v204\n" \
"v_accvgpr_write_b32 a77, v205\n" \
"v_mfma_f32_16x16x32_bf16 v[124:127], a[156:159], v[220:223], v[124:127]\n" \
"s_waitcnt lgkmcnt(4)\n" \
"v_exp_f32 v94, v94\n" \
"v_cvt_pk_bf16_f32 v134, v76, v77\n" \
"v_cvt_pk_bf16_f32 v135, v78, v79\n" \
"v_accvgpr_write_b32 a78, v206\n" \
"v_accvgpr_write_b32 a79, v207\n" \
"v_mfma_f32_16x16x32_bf16 v[160:163], v[180:183], v[128:131], 0\n" \
"v_exp_f32 v95, v95\n" \
"v_cvt_pk_bf16_f32 v138, v84, v85\n" \
"v_cvt_pk_bf16_f32 v139, v86, v87\n" \
"v_accvgpr_write_b32 a92, v220\n" \
"v_accvgpr_write_b32 a93, v221\n" \
"v_mfma_f32_16x16x32_bf16 v[164:167], v[180:183], v[132:135], 0\n" \
"v_cvt_pk_bf16_f32 v142, v92, v93\n" \
"v_cvt_pk_bf16_f32 v143, v94, v95\n" \
"v_accvgpr_write_b32 a94, v222\n" \
"v_accvgpr_write_b32 a95, v223\n" \
"v_mfma_f32_16x16x32_bf16 v[168:171], v[180:183], v[136:139], 0\n" \
"v_exp_f32 v96, v96\n" \
"v_mfma_f32_16x16x32_bf16 v[172:175], v[180:183], v[140:143], 0\n" \
"v_lshrrev_b32 v9, 5, v4\n" \
"v_lshlrev_b32 v9, 27, v9\n" \
"v_mfma_f32_16x16x32_bf16 v[192:195], a[128:131], v[128:131], 0\n" \
"v_exp_f32 v97, v97\n" \
"v_mfma_f32_16x16x32_bf16 v[196:199], a[132:135], v[128:131], 0\n" \
"v_cvt_pk_bf16_f32 v144, v96, v97\n" \
"v_lshl_add_u32 v9, v4, 4, v9\n" \
"v_mfma_f32_16x16x32_bf16 v[224:227], a[128:131], v[132:135], 0\n" \
"ds_read_b128 a[152:155], v5 offset:22528\n" \
"v_mov_b32 v10, s14\n" \
"v_mfma_f32_16x16x32_bf16 v[228:231], a[132:135], v[132:135], 0\n" \
"v_exp_f32 v98, v98\n" \
"v_mfma_f32_16x16x32_bf16 a[0:3], a[128:131], v[136:139], 0\n" \
"v_exp_f32 v99, v99\n" \
"v_mfma_f32_16x16x32_bf16 a[4:7], a[132:135], v[136:139], 0\n" \
"v_cvt_pk_bf16_f32 v145, v98, v99\n" \
"v_mov_b32 v11, s15\n" \
"v_mfma_f32_16x16x32_bf16 a[32:35], a[128:131], v[140:143], 0\n" \
"ds_read_b128 a[156:159], v5 offset:23552\n" \
"s_waitcnt lgkmcnt(12)\n" \
"v_mfma_f32_16x16x32_bf16 a[36:39], a[132:135], v[140:143], 0\n" \
"s_waitcnt vmcnt(12) lgkmcnt(4)\n" \
"v_exp_f32 v100, v100\n" \
"v_mfma_f32_16x16x32_bf16 v[200:203], a[136:139], v[128:131], 0\n" \
"s_barrier\n" \
"v_exp_f32 v101, v101\n" \
"v_mfma_f32_16x16x32_bf16 v[204:207], a[140:143], v[128:131], 0\n" \
"s_add_u32 s17, s17, s19\n" \
"v_cvt_pk_bf16_f32 v146, v100, v101\n" \
"v_mfma_f32_16x16x32_bf16 v[232:235], a[136:139], v[132:135], 0\n" \
"ds_read_b128 a[128:131], v5 offset:32768\n" \
"v_mfma_f32_16x16x32_bf16 v[236:239], a[140:143], v[132:135], 0\n" \
"s_add_u32 m0, s49, 49152\n" \
"v_exp_f32 v102, v102\n" \
"v_mfma_f32_16x16x32_bf16 a[8:11], a[136:139], v[136:139], 0\n" \
"buffer_load_dwordx4 v6, s[40:43], s17 offen offset:0 sc1 lds\n" \
"v_mfma_f32_16x16x32_bf16 a[12:15], a[140:143], v[136:139], 0\n" \
"v_exp_f32 v103, v103\n" \
"v_mfma_f32_16x16x32_bf16 a[40:43], a[136:139], v[140:143], 0\n" \
"ds_read_b128 a[132:135], v5 offset:33792\n" \
"v_cvt_pk_bf16_f32 v147, v102, v103\n" \
"v_mfma_f32_16x16x32_bf16 a[44:47], a[140:143], v[140:143], 0\n" \
"s_waitcnt lgkmcnt(4)\n" \
"v_exp_f32 v104, v104\n" \
"v_mfma_f32_16x16x32_bf16 v[208:211], a[144:147], v[128:131], 0\n" \
"v_exp_f32 v105, v105\n" \
"v_mfma_f32_16x16x32_bf16 v[212:215], a[148:151], v[128:131], 0\n" \
"v_cvt_pk_bf16_f32 v148, v104, v105\n" \
"v_mfma_f32_16x16x32_bf16 v[240:243], a[144:147], v[132:135], 0\n" \
"ds_read_b128 a[136:139], v5 offset:34816\n" \
"v_mfma_f32_16x16x32_bf16 v[244:247], a[148:151], v[132:135], 0\n" \
"v_exp_f32 v106, v106\n" \
"v_mfma_f32_16x16x32_bf16 a[16:19], a[144:147], v[136:139], 0\n" \
"buffer_load_dwordx4 v6, s[40:43], s17 offen offset:1024 sc1 lds\n" \
"v_mfma_f32_16x16x32_bf16 a[20:23], a[148:151], v[136:139], 0\n" \
"v_exp_f32 v107, v107\n" \
"v_mfma_f32_16x16x32_bf16 a[48:51], a[144:147], v[140:143], 0\n" \
"ds_read_b128 a[140:143], v5 offset:35840\n" \
"v_cvt_pk_bf16_f32 v149, v106, v107\n" \
"v_mfma_f32_16x16x32_bf16 a[52:55], a[148:151], v[140:143], 0\n" \
"s_waitcnt lgkmcnt(4)\n" \
"v_exp_f32 v108, v108\n" \
"v_mfma_f32_16x16x32_bf16 v[216:219], a[152:155], v[128:131], 0\n" \
"buffer_load_dwordx4 v6, s[40:43], s17 offen offset:2048 sc1 lds\n" \
"v_mfma_f32_16x16x32_bf16 v[220:223], a[156:159], v[128:131], 0\n" \
"v_exp_f32 v109, v109\n" \
"v_mfma_f32_16x16x32_bf16 v[248:251], a[152:155], v[132:135], 0\n" \
"ds_read_b128 a[144:147], v5 offset:36864\n" \
"v_cvt_pk_bf16_f32 v150, v108, v109\n" \
"v_mfma_f32_16x16x32_bf16 v[252:255], a[156:159], v[132:135], 0\n" \
"v_exp_f32 v110, v110\n" \
"v_mfma_f32_16x16x32_bf16 a[24:27], a[152:155], v[136:139], 0\n" \
"buffer_load_dwordx4 v6, s[40:43], s17 offen offset:3072 sc1 lds\n" \
"v_mfma_f32_16x16x32_bf16 a[28:31], a[156:159], v[136:139], 0\n" \
"v_exp_f32 v111, v111\n" \
"v_mfma_f32_16x16x32_bf16 a[56:59], a[152:155], v[140:143], 0\n" \
"ds_read_b128 a[148:151], v5 offset:37888\n" \
"v_cvt_pk_bf16_f32 v151, v110, v111\n" \
"v_mfma_f32_16x16x32_bf16 a[60:63], a[156:159], v[140:143], 0\n" \
"s_waitcnt lgkmcnt(4)\n" \
"s_cmp_lt_i32 s46, 0\n" \
"s_cbranch_scc1 LAD2_%=\n" \
"LCHK_%=:\n" \
"s_cmp_eq_u32 s44, s46\n" \
"s_cbranch_scc1 LMID_%=\n" \
"s_branch LODD_%=\n" \
".p2align 12\n" \
"LH15_%=:\ns_cbranch_execz LHR0_%=\n" \
"s_nop 0\n" \
"LODD_%=:\n" \
"v_mfma_f32_16x16x32_bf16 v[64:67], a[128:131], v[32:35], 0\n" \
"v_exp_f32 v112, v112\n" \
"v_mfma_f32_16x16x32_bf16 v[64:67], a[132:135], v[36:39], v[64:67]\n" \
"s_add_u32 s16, s16, s9\n" \
"v_mfma_f32_16x16x32_bf16 v[72:75], a[128:131], v[48:51], 0\n" \
"ds_read_b128 a[152:155], v5 offset:38912\n" \
"v_mfma_f32_16x16x32_bf16 v[72:75], a[132:135], v[52:55], v[72:75]\n" \
"s_add_u32 m0, s48, 0\n" \
"v_exp_f32 v113, v113\n" \
"v_mfma_f32_16x16x32_bf16 v[80:83], a[128:131], a[64:67], 0\n" \
"buffer_load_dwordx4 v6, s[40:43], s16 offen offset:0 sc1 lds\n" \
"v_mfma_f32_16x16x32_bf16 v[80:83], a[132:135], a[68:71], v[80:83]\n" \
"v_exp_f32 v114, v114\n" \
"v_mfma_f32_16x16x32_bf16 v[88:91], a[128:131], a[80:83], 0\n" \
"ds_read_b128 a[156:159], v5 offset:39936\n" \
"v_cvt_pk_bf16_f32 v152, v112, v113\n" \
"v_mfma_f32_16x16x32_bf16 v[88:91], a[132:135], a[84:87], v[88:91]\n" \
"s_waitcnt lgkmcnt(4)\n" \
"v_exp_f32 v115, v115\n" \
"v_mfma_f32_16x16x32_bf16 v[64:67], a[136:139], v[40:43], v[64:67]\n" \
"v_exp_f32 v116, v116\n" \
"v_mfma_f32_16x16x32_bf16 v[64:67], a[140:143], v[44:47], v[64:67]\n" \
"s_xor_b32 s49, s49, 0x10000\n" \
"v_cvt_pk_bf16_f32 v153, v114, v115\n" \
"v_mfma_f32_16x16x32_bf16 v[72:75], a[136:139], v[56:59], v[72:75]\n" \
"ds_read_b128 a[128:131], v5 offset:49152\n" \
"v_mfma_f32_16x16x32_bf16 v[72:75], a[140:143], v[60:63], v[72:75]\n" \
"v_exp_f32 v117, v117\n" \
"v_mfma_f32_16x16x32_bf16 v[80:83], a[136:139], a[72:75], v[80:83]\n" \
"buffer_load_dwordx4 v6, s[40:43], s16 offen offset:1024 sc1 lds\n" \
"v_mfma_f32_16x16x32_bf16 v[80:83], a[140:143], a[76:79], v[80:83]\n" \
"v_exp_f32 v118, v118\n" \
"v_mfma_f32_16x16x32_bf16 v[88:91], a[136:139], a[88:91], v[88:91]\n" \
"ds_read_b128 a[132:135], v5 offset:50176\n" \
"v_cvt_pk_bf16_f32 v154, v116, v117\n" \
"v_mfma_f32_16x16x32_bf16 v[88:91], a[140:143], a[92:95], v[88:91]\n" \
"s_waitcnt lgkmcnt(4)\n" \
"v_exp_f32 v119, v119\n" \
"v_mfma_f32_16x16x32_bf16 v[68:71], a[144:147], v[32:35], 0\n" \
"v_exp_f32 v120, v120\n" \
"v_mfma_f32_16x16x32_bf16 v[68:71], a[148:151], v[36:39], v[68:71]\n" \
"v_cvt_pk_bf16_f32 v155, v118, v119\n" \
"v_mfma_f32_16x16x32_bf16 v[76:79], a[144:147], v[48:51], 0\n" \
"ds_read_b128 a[136:139], v5 offset:51200\n" \
"v_mfma_f32_16x16x32_bf16 v[76:79], a[148:151], v[52:55], v[76:79]\n" \
"v_exp_f32 v121, v121\n" \
"v_mfma_f32_16x16x32_bf16 v[84:87], a[144:147], a[64:67], 0\n" \
"buffer_load_dwordx4 v6, s[40:43], s16 offen offset:2048 sc1 lds\n" \
"v_mfma_f32_16x16x32_bf16 v[84:87], a[148:151], a[68:71], v[84:87]\n" \
"v_exp_f32 v122, v122\n" \
"v_mfma_f32_16x16x32_bf16 v[92:95], a[144:147], a[80:83], 0\n" \
"ds_read_b128 a[140:143], v5 offset:52224\n" \
"v_cvt_pk_bf16_f32 v156, v120, v121\n" \
"v_mfma_f32_16x16x32_bf16 v[92:95], a[148:151], a[84:87], v[92:95]\n" \
"s_waitcnt lgkmcnt(4)\n" \
"v_exp_f32 v123, v123\n" \
"v_mfma_f32_16x16x32_bf16 v[68:71], a[152:155], v[40:43], v[68:71]\n" \
"v_exp_f32 v124, v124\n" \
"v_mfma_f32_16x16x32_bf16 v[68:71], a[156:159], v[44:47], v[68:71]\n" \
"v_cvt_pk_bf16_f32 v157, v122, v123\n" \
"v_mfma_f32_16x16x32_bf16 v[76:79], a[152:155], v[56:59], v[76:79]\n" \
"ds_read_b128 a[144:147], v5 offset:53248\n" \
"v_mfma_f32_16x16x32_bf16 v[76:79], a[156:159], v[60:63], v[76:79]\n" \
"v_exp_f32 v125, v125\n" \
"v_mfma_f32_16x16x32_bf16 v[84:87], a[152:155], a[72:75], v[84:87]\n" \
"buffer_load_dwordx4 v6, s[40:43], s16 offen offset:3072 sc1 lds\n" \
"v_mfma_f32_16x16x32_bf16 v[84:87], a[156:159], a[76:79], v[84:87]\n" \
"v_exp_f32 v126, v126\n" \
"v_mfma_f32_16x16x32_bf16 v[92:95], a[152:155], a[88:91], v[92:95]\n" \
"ds_read_b128 a[148:151], v5 offset:54272\n" \
"v_cvt_pk_bf16_f32 v158, v124, v125\n" \
"v_mfma_f32_16x16x32_bf16 v[92:95], a[156:159], a[92:95], v[92:95]\n" \
"s_waitcnt lgkmcnt(4)\n" \
"v_exp_f32 v127, v127\n" \
"v_mfma_f32_16x16x32_bf16 v[160:163], v[180:183], v[144:147], v[160:163]\n" \
"v_cvt_pk_bf16_f32 v159, v126, v127\n" \
"v_mfma_f32_16x16x32_bf16 v[164:167], v[180:183], v[148:151], v[164:167]\n" \
"v_mfma_f32_16x16x32_bf16 v[168:171], v[180:183], v[152:155], v[168:171]\n" \
"v_exp_f32 v64, v64\n" \
"v_mfma_f32_16x16x32_bf16 v[172:175], v[180:183], v[156:159], v[172:175]\n" \
"v_mfma_f32_16x16x32_bf16 v[192:195], a[128:131], v[144:147], v[192:195]\n" \
"v_exp_f32 v65, v65\n" \
"v_mfma_f32_16x16x32_bf16 v[196:199], a[132:135], v[144:147], v[196:199]\n" \
"v_cvt_pk_bf16_f32 v128, v64, v65\n" \
"v_mfma_f32_16x16x32_bf16 v[224:227], a[128:131], v[148:151], v[224:227]\n" \
"ds_read_b128 a[152:155], v5 offset:55296\n" \
"v_mfma_f32_16x16x32_bf16 v[228:231], a[132:135], v[148:151], v[228:231]\n" \
"v_exp_f32 v66, v66\n" \
"v_mfma_f32_16x16x32_bf16 a[0:3], a[128:131], v[152:155], a[0:3]\n" \
"v_exp_f32 v67, v67\n" \
"v_mfma_f32_16x16x32_bf16 a[4:7], a[132:135], v[152:155], a[4:7]\n" \
"v_cvt_pk_bf16_f32 v129, v66, v67\n" \
"v_mfma_f32_16x16x32_bf16 a[32:35], a[128:131], v[156:159], a[32:35]\n" \
"ds_read_b128 a[156:159], v5 offset:56320\n" \
"v_mfma_f32_16x16x32_bf16 a[36:39], a[132:135], v[156:159], a[36:39]\n" \
"s_waitcnt vmcnt(12) lgkmcnt(4)\n" \
"v_exp_f32 v68, v68\n" \
"v_mfma_f32_16x16x32_bf16 v[200:203], a[136:139], v[144:147], v[200:203]\n" \
"s_barrier\n" \
"v_xor_b32 v5, 0x10000, v5\n" \
"v_mfma_f32_16x16x32_bf16 v[204:207], a[140:143], v[144:147], v[204:207]\n" \
"s_add_u32 s17, s17, s19\n" \
"v_exp_f32 v69, v69\n" \
"v_mfma_f32_16x16x32_bf16 v[232:235], a[136:139], v[148:151], v[232:235]\n" \
"ds_read_b128 a[128:131], v5 offset:0\n" \
"v_cvt_pk_bf16_f32 v130, v68, v69\n" \
"v_mfma_f32_16x16x32_bf16 v[236:239], a[140:143], v[148:151], v[236:239]\n" \
"s_add_u32 m0, s48, 16384\n" \
"v_exp_f32 v70, v70\n" \
"v_mfma_f32_16x16x32_bf16 a[8:11], a[136:139], v[152:155], a[8:11]\n" \
"buffer_load_dwordx4 v6, s[40:43], s17 offen offset:0 sc1 lds\n" \
"v_mfma_f32_16x16x32_bf16 a[12:15], a[140:143], v[152:155], a[12:15]\n" \
"v_exp_f32 v71, v71\n" \
"v_mfma_f32_16x16x32_bf16 a[40:43], a[136:139], v[156:159], a[40:43]\n" \
"ds_read_b128 a[132:135], v5 offset:1024\n" \
"v_cvt_pk_bf16_f32 v131, v70, v71\n" \
"v_mfma_f32_16x16x32_bf16 a[44:47], a[140:143], v[156:159], a[44:47]\n" \
"s_waitcnt lgkmcnt(4)\n" \
"v_exp_f32 v72, v72\n" \
"v_mfma_f32_16x16x32_bf16 v[208:211], a[144:147], v[144:147], v[208:211]\n" \
"v_exp_f32 v73, v73\n" \
"v_mfma_f32_16x16x32_bf16 v[212:215], a[148:151], v[144:147], v[212:215]\n" \
"v_cvt_pk_bf16_f32 v132, v72, v73\n" \
"v_mfma_f32_16x16x32_bf16 v[240:243], a[144:147], v[148:151], v[240:243]\n" \
"ds_read_b128 a[136:139], v5 offset:2048\n" \
"v_mfma_f32_16x16x32_bf16 v[244:247], a[148:151], v[148:151], v[244:247]\n" \
"v_exp_f32 v74, v74\n" \
"v_mfma_f32_16x16x32_bf16 a[16:19], a[144:147], v[152:155], a[16:19]\n" \
"buffer_load_dwordx4 v6, s[40:43], s17 offen offset:1024 sc1 lds\n" \
"v_mfma_f32_16x16x32_bf16 a[20:23], a[148:151], v[152:155], a[20:23]\n" \
"v_exp_f32 v75, v75\n" \
"v_mfma_f32_16x16x32_bf16 a[48:51], a[144:147], v[156:159], a[48:51]\n" \
"ds_read_b128 a[140:143], v5 offset:3072\n" \
"v_cvt_pk_bf16_f32 v133, v74, v75\n" \
"v_mfma_f32_16x16x32_bf16 a[52:55], a[148:151], v[156:159], a[52:55]\n" \
"s_waitcnt lgkmcnt(4)\n" \
"v_exp_f32 v76, v76\n" \
"v_mfma_f32_16x16x32_bf16 v[216:219], a[152:155], v[144:147], v[216:219]\n" \
"buffer_load_dwordx4 v6, s[40:43], s17 offen offset:2048 sc1 lds\n" \
"v_mfma_f32_16x16x32_bf16 v[220:223], a[156:159], v[144:147], v[220:223]\n" \
"s_add_u32 s44, s44, 1\n" \
"v_exp_f32 v77, v77\n" \
"v_mfma_f32_16x16x32_bf16 v[248:251], a[152:155], v[148:151], v[248:251]\n" \
"ds_read_b128 a[144:147], v5 offset:4096\n" \
"v_cvt_pk_bf16_f32 v134, v76, v77\n" \
"v_mfma_f32_16x16x32_bf16 v[252:255], a[156:159], v[148:151], v[252:255]\n" \
"v_exp_f32 v78, v78\n" \
"v_mfma_f32_16x16x32_bf16 a[24:27], a[152:155], v[152:155], a[24:27]\n" \
"buffer_load_dwordx4 v6, s[40:43], s17 offen offset:3072 sc1 lds\n" \
"v_mfma_f32_16x16x32_bf16 a[28:31], a[156:159], v[152:155], a[28:31]\n" \
"s_xor_b32 s48, s48, 0x10000\n" \
"v_exp_f32 v79, v79\n" \
"v_mfma_f32_16x16x32_bf16 a[56:59], a[152:155], v[156:159], a[56:59]\n" \
"ds_read_b128 a[148:151], v5 offset:5120\n" \
"v_cvt_pk_bf16_f32 v135, v78, v79\n" \
"v_mfma_f32_16x16x32_bf16 a[60:63], a[156:159], v[156:159], a[60:63]\n" \
"s_waitcnt lgkmcnt(4)\n" \
"LBOT_%=:\n" \
"s_cmp_ge_u32 s44, 14\n" \
"s_cbranch_scc1 LDISP_%=\n" \
"LEVEN_%=:\n" \
"v_mfma_f32_16x16x32_bf16 v[96:99], a[128:131], v[32:35], 0\n" \
"v_exp_f32 v80, v80\n" \
"v_mfma_f32_16x16x32_bf16 v[96:99], a[132:135], v[36:39], v[96:99]\n" \
"s_add_u32 s16, s16, s9\n" \
"v_mfma_f32_16x16x32_bf16 v[104:107], a[128:131], v[48:51], 0\n" \
"ds_read_b128 a[152:155], v5 offset:6144\n" \
"v_mfma_f32_16x16x32_bf16 v[104:107], a[132:135], v[52:55], v[104:107]\n" \
"s_add_u32 m0, s49, 32768\n" \
"v_exp_f32 v81, v81\n" \
"v_mfma_f32_16x16x32_bf16 v[112:115], a[128:131], a[64:67], 0\n" \
"buffer_load_dwordx4 v6, s[40:43], s16 offen offset:0 sc1 lds\n" \
"v_mfma_f32_16x16x32_bf16 v[112:115], a[132:135], a[68:71], v[112:115]\n" \
"v_exp_f32 v82, v82\n" \
"v_mfma_f32_16x16x32_bf16 v[120:123], a[128:131], a[80:83], 0\n" \
"ds_read_b128 a[156:159], v5 offset:7168\n" \
"v_cvt_pk_bf16_f32 v136, v80, v81\n" \
"v_mfma_f32_16x16x32_bf16 v[120:123], a[132:135], a[84:87], v[120:123]\n" \
"s_waitcnt lgkmcnt(4)\n" \
"v_exp_f32 v83, v83\n" \
"v_mfma_f32_16x16x32_bf16 v[96:99], a[136:139], v[40:43], v[96:99]\n" \
"v_exp_f32 v84, v84\n" \
"v_mfma_f32_16x16x32_bf16 v[96:99], a[140:143], v[44:47], v[96:99]\n" \
"v_cvt_pk_bf16_f32 v137, v82, v83\n" \
"v_mfma_f32_16x16x32_bf16 v[104:107], a[136:139], v[56:59], v[104:107]\n" \
"ds_read_b128 a[128:131], v5 offset:16384\n" \
"v_mfma_f32_16x16x32_bf16 v[104:107], a[140:143], v[60:63], v[104:107]\n" \
"v_exp_f32 v85, v85\n" \
"v_mfma_f32_16x16x32_bf16 v[112:115], a[136:139], a[72:75], v[112:115]\n" \
"buffer_load_dwordx4 v6, s[40:43], s16 offen offset:1024 sc1 lds\n" \
"v_mfma_f32_16x16x32_bf16 v[112:115], a[140:143], a[76:79], v[112:115]\n" \
"v_exp_f32 v86, v86\n" \
"v_mfma_f32_16x16x32_bf16 v[120:123], a[136:139], a[88:91], v[120:123]\n" \
"ds_read_b128 a[132:135], v5 offset:17408\n" \
"v_cvt_pk_bf16_f32 v138, v84, v85\n" \
"v_mfma_f32_16x16x32_bf16 v[120:123], a[140:143], a[92:95], v[120:123]\n" \
"s_waitcnt lgkmcnt(4)\n" \
"v_exp_f32 v87, v87\n" \
"v_mfma_f32_16x16x32_bf16 v[100:103], a[144:147], v[32:35], 0\n" \
"v_exp_f32 v88, v88\n" \
"v_mfma_f32_16x16x32_bf16 v[100:103], a[148:151], v[36:39], v[100:103]\n" \
"v_cvt_pk_bf16_f32 v139, v86, v87\n" \
"v_mfma_f32_16x16x32_bf16 v[108:111], a[144:147], v[48:51], 0\n" \
"ds_read_b128 a[136:139], v5 offset:18432\n" \
"v_mfma_f32_16x16x32_bf16 v[108:111], a[148:151], v[52:55], v[108:111]\n" \
"v_exp_f32 v89, v89\n" \
"v_mfma_f32_16x16x32_bf16 v[116:119], a[144:147], a[64:67], 0\n" \
"buffer_load_dwordx4 v6, s[40:43], s16 offen offset:2048 sc1 lds\n" \
"v_mfma_f32_16x16x32_bf16 v[116:119], a[148:151], a[68:71], v[116:119]\n" \
"v_exp_f32 v90, v90\n" \
"v_mfma_f32_16x16x32_bf16 v[124:127], a[144:147], a[80:83], 0\n" \
"ds_read_b128 a[140:143], v5 offset:19456\n" \
"v_cvt_pk_bf16_f32 v140, v88, v89\n" \
"v_mfma_f32_16x16x32_bf16 v[124:127], a[148:151], a[84:87], v[124:127]\n" \
"s_waitcnt lgkmcnt(4)\n" \
"v_exp_f32 v91, v91\n" \
"v_mfma_f32_16x16x32_bf16 v[100:103], a[152:155], v[40:43], v[100:103]\n" \
"v_exp_f32 v92, v92\n" \
"v_mfma_f32_16x16x32_bf16 v[100:103], a[156:159], v[44:47], v[100:103]\n" \
"v_cvt_pk_bf16_f32 v141, v90, v91\n" \
"v_mfma_f32_16x16x32_bf16 v[108:111], a[152:155], v[56:59], v[108:111]\n" \
"ds_read_b128 a[144:147], v5 offset:20480\n" \
"v_mfma_f32_16x16x32_bf16 v[108:111], a[156:159], v[60:63], v[108:111]\n" \
"v_exp_f32 v93, v93\n" \
"v_mfma_f32_16x16x32_bf16 v[116:119], a[152:155], a[72:75], v[116:119]\n" \
"buffer_load_dwordx4 v6, s[40:43], s16 offen offset:3072 sc1 lds\n" \
"v_mfma_f32_16x16x32_bf16 v[116:119], a[156:159], a[76:79], v[116:119]\n" \
"v_exp_f32 v94, v94\n" \
"v_mfma_f32_16x16x32_bf16 v[124:127], a[152:155], a[88:91], v[124:127]\n" \
"ds_read_b128 a[148:151], v5 offset:21504\n" \
"v_cvt_pk_bf16_f32 v142, v92, v93\n" \
"v_mfma_f32_16x16x32_bf16 v[124:127], a[156:159], a[92:95], v[124:127]\n" \
"s_waitcnt lgkmcnt(4)\n" \
"v_exp_f32 v95, v95\n" \
"v_mfma_f32_16x16x32_bf16 v[160:163], v[180:183], v[128:131], v[160:163]\n" \
"v_cvt_pk_bf16_f32 v143, v94, v95\n" \
"v_mfma_f32_16x16x32_bf16 v[164:167], v[180:183], v[132:135], v[164:167]\n" \
"v_mfma_f32_16x16x32_bf16 v[168:171], v[180:183], v[136:139], v[168:171]\n" \
"v_exp_f32 v96, v96\n" \
"v_mfma_f32_16x16x32_bf16 v[172:175], v[180:183], v[140:143], v[172:175]\n" \
"v_mfma_f32_16x16x32_bf16 v[192:195], a[128:131], v[128:131], v[192:195]\n" \
"v_exp_f32 v97, v97\n" \
"v_mfma_f32_16x16x32_bf16 v[196:199], a[132:135], v[128:131], v[196:199]\n" \
"v_cvt_pk_bf16_f32 v144, v96, v97\n" \
"v_mfma_f32_16x16x32_bf16 v[224:227], a[128:131], v[132:135], v[224:227]\n" \
"ds_read_b128 a[152:155], v5 offset:22528\n" \
"v_mfma_f32_16x16x32_bf16 v[228:231], a[132:135], v[132:135], v[228:231]\n" \
"v_exp_f32 v98, v98\n" \
"v_mfma_f32_16x16x32_bf16 a[0:3], a[128:131], v[136:139], a[0:3]\n" \
"v_exp_f32 v99, v99\n" \
"v_mfma_f32_16x16x32_bf16 a[4:7], a[132:135], v[136:139], a[4:7]\n" \
"v_cvt_pk_bf16_f32 v145, v98, v99\n" \
"v_mfma_f32_16x16x32_bf16 a[32:35], a[128:131], v[140:143], a[32:35]\n" \
"ds_read_b128 a[156:159], v5 offset:23552\n" \
"v_mfma_f32_16x16x32_bf16 a[36:39], a[132:135], v[140:143], a[36:39]\n" \
"s_waitcnt vmcnt(12) lgkmcnt(4)\n" \
"v_exp_f32 v100, v100\n" \
"v_mfma_f32_16x16x32_bf16 v[200:203], a[136:139], v[128:131], v[200:203]\n" \
"s_barrier\n" \
"v_exp_f32 v101, v101\n" \
"v_mfma_f32_16x16x32_bf16 v[204:207], a[140:143], v[128:131], v[204:207]\n" \
"s_add_u32 s17, s17, s19\n" \
"v_cvt_pk_bf16_f32 v146, v100, v101\n" \
"v_mfma_f32_16x16x32_bf16 v[232:235], a[136:139], v[132:135], v[232:235]\n" \
"ds_read_b128 a[128:131], v5 offset:32768\n" \
"v_mfma_f32_16x16x32_bf16 v[236:239], a[140:143], v[132:135], v[236:239]\n" \
"s_add_u32 m0, s49, 49152\n" \
"v_exp_f32 v102, v102\n" \
"v_mfma_f32_16x16x32_bf16 a[8:11], a[136:139], v[136:139], a[8:11]\n" \
"buffer_load_dwordx4 v6, s[40:43], s17 offen offset:0 sc1 lds\n" \
"v_mfma_f32_16x16x32_bf16 a[12:15], a[140:143], v[136:139], a[12:15]\n" \
"v_exp_f32 v103, v103\n" \
"v_mfma_f32_16x16x32_bf16 a[40:43], a[136:139], v[140:143], a[40:43]\n" \
"ds_read_b128 a[132:135], v5 offset:33792\n" \
"v_cvt_pk_bf16_f32 v147, v102, v103\n" \
"v_mfma_f32_16x16x32_bf16 a[44:47], a[140:143], v[140:143], a[44:47]\n" \
"s_waitcnt lgkmcnt(4)\n" \
"v_exp_f32 v104, v104\n" \
"v_mfma_f32_16x16x32_bf16 v[208:211], a[144:147], v[128:131], v[208:211]\n" \
"v_exp_f32 v105, v105\n" \
"v_mfma_f32_16x16x32_bf16 v[212:215], a[148:151], v[128:131], v[212:215]\n" \
"v_cvt_pk_bf16_f32 v148, v104, v105\n" \
"v_mfma_f32_16x16x32_bf16 v[240:243], a[144:147], v[132:135], v[240:243]\n" \
"ds_read_b128 a[136:139], v5 offset:34816\n" \
"v_mfma_f32_16x16x32_bf16 v[244:247], a[148:151], v[132:135], v[244:247]\n" \
"v_exp_f32 v106, v106\n" \
"v_mfma_f32_16x16x32_bf16 a[16:19], a[144:147], v[136:139], a[16:19]\n" \
"buffer_load_dwordx4 v6, s[40:43], s17 offen offset:1024 sc1 lds\n" \
"v_mfma_f32_16x16x32_bf16 a[20:23], a[148:151], v[136:139], a[20:23]\n" \
"v_exp_f32 v107, v107\n" \
"v_mfma_f32_16x16x32_bf16 a[48:51], a[144:147], v[140:143], a[48:51]\n" \
"ds_read_b128 a[140:143], v5 offset:35840\n" \
"v_cvt_pk_bf16_f32 v149, v106, v107\n" \
"v_mfma_f32_16x16x32_bf16 a[52:55], a[148:151], v[140:143], a[52:55]\n" \
"s_waitcnt lgkmcnt(4)\n" \
"v_exp_f32 v108, v108\n" \
"v_mfma_f32_16x16x32_bf16 v[216:219], a[152:155], v[128:131], v[216:219]\n" \
"buffer_load_dwordx4 v6, s[40:43], s17 offen offset:2048 sc1 lds\n" \
"v_mfma_f32_16x16x32_bf16 v[220:223], a[156:159], v[128:131], v[220:223]\n" \
"v_exp_f32 v109, v109\n" \
"v_mfma_f32_16x16x32_bf16 v[248:251], a[152:155], v[132:135], v[248:251]\n" \
"ds_read_b128 a[144:147], v5 offset:36864\n" \
"v_cvt_pk_bf16_f32 v150, v108, v109\n" \
"v_mfma_f32_16x16x32_bf16 v[252:255], a[156:159], v[132:135], v[252:255]\n" \
"v_exp_f32 v110, v110\n" \
"v_mfma_f32_16x16x32_bf16 a[24:27], a[152:155], v[136:139], a[24:27]\n" \
"buffer_load_dwordx4 v6, s[40:43], s17 offen offset:3072 sc1 lds\n" \
"v_mfma_f32_16x16x32_bf16 a[28:31], a[156:159], v[136:139], a[28:31]\n" \
"v_exp_f32 v111, v111\n" \
"v_mfma_f32_16x16x32_bf16 a[56:59], a[152:155], v[140:143], a[56:59]\n" \
"ds_read_b128 a[148:151], v5 offset:37888\n" \
"v_cvt_pk_bf16_f32 v151, v110, v111\n" \
"v_mfma_f32_16x16x32_bf16 a[60:63], a[156:159], v[140:143], a[60:63]\n" \
"s_waitcnt lgkmcnt(4)\n" \
"s_cmp_lg_u32 s44, s46\n" \
"s_cbranch_scc1 LODD_%=\n" \
"LMID_%=:\n" \
"s_cmp_eq_u32 s44, s45\n" \
"s_cbranch_scc1 LWS_%=\n" \
"s_cmp_lt_u32 %2, 2\n" \
"s_cbranch_scc1 LS1_%=\n" \
"s_cmp_eq_u32 s44, 15\n" \
"s_cbranch_scc1 LS1_%=\n" \
"s_lshl_b32 s46, s7, 2\n" \
"s_add_u32 s46, s46, %2\n" \
"s_waitcnt vmcnt(8)\n" \
"v_cmp_ne_u64 vcc, s[14:15], v[18:19]\n" \
"s_cmp_eq_u32 s46, 30\ns_cselect_b32 s46, 15, 99\n" \
"s_cbranch_vccnz LPE0_%=\nLPE0R_%=:\n" \
"s_branch LODD_%=\n" \
".p2align 6\n" \
"LH16_%=:\ns_cbranch_execz LH20_%=\n" \
"s_nop 0\n" \
"LWS_%=:\n" \
"v_mfma_f32_16x16x32_bf16 v[64:67], a[128:131], v[32:35], 0\n" \
"s_mov_b32 s81, 0\n" \
"v_exp_f32 v112, v112\n" \
"v_mfma_f32_16x16x32_bf16 v[64:67], a[132:135], v[36:39], v[64:67]\n" \
"s_add_u32 s16, s16, s9\n" \
"v_mov_b32 v12, v30\n" \
"v_mov_b32 v28, -1\n" \
"v_mfma_f32_16x16x32_bf16 v[72:75], a[128:131], v[48:51], 0\n" \
"ds_read_b128 a[152:155], v5 offset:38912\n" \
"v_mov_b32 v29, -1\n" \
"v_mfma_f32_16x16x32_bf16 v[72:75], a[132:135], v[52:55], v[72:75]\n" \
"s_add_u32 m0, s48, 0\n" \
"v_exp_f32 v113, v113\n" \
"v_mfma_f32_16x16x32_bf16 v[80:83], a[128:131], a[64:67], 0\n" \
"buffer_load_dwordx4 v6, s[40:43], s16 offen offset:0 sc1 lds\n" \
"v_mfma_f32_16x16x32_bf16 v[80:83], a[132:135], a[68:71], v[80:83]\n" \
"v_exp_f32 v114, v114\n" \
"v_mfma_f32_16x16x32_bf16 v[88:91], a[128:131], a[80:83], 0\n" \
"ds_read_b128 a[156:159], v5 offset:39936\n" \
"v_cvt_pk_bf16_f32 v152, v112, v113\n" \
"v_mfma_f32_16x16x32_bf16 v[88:91], a[132:135], a[84:87], v[88:91]\n" \
"s_waitcnt lgkmcnt(4)\n" \
"v_exp_f32 v115, v115\n" \
"s_add_u32 m0, s79, 131072\n" \
"v_mfma_f32_16x16x32_bf16 v[64:67], a[136:139], v[40:43], v[64:67]\n" \
"buffer_load_dwordx4 v12, s[28:31], s81 offen offset:0 lds\n" \
"v_mfma_f32_16x16x32_bf16 v[64:67], a[140:143], v[44:47], v[64:67]\n" \
"s_xor_b32 s49, s49, 0x10000\n" \
"v_exp_f32 v116, v116\n" \
"v_mfma_f32_16x16x32_bf16 v[72:75], a[136:139], v[56:59], v[72:75]\n" \
"ds_read_b128 a[128:131], v5 offset:49152\n" \
"v_cvt_pk_bf16_f32 v153, v114, v115\n" \
"v_mfma_f32_16x16x32_bf16 v[72:75], a[140:143], v[60:63], v[72:75]\n" \
"s_add_u32 m0, s48, 0\n" \
"v_exp_f32 v117, v117\n" \
"v_mfma_f32_16x16x32_bf16 v[80:83], a[136:139], a[72:75], v[80:83]\n" \
"buffer_load_dwordx4 v6, s[40:43], s16 offen offset:1024 sc1 lds\n" \
"v_mfma_f32_16x16x32_bf16 v[80:83], a[140:143], a[76:79], v[80:83]\n" \
"v_exp_f32 v118, v118\n" \
"v_mfma_f32_16x16x32_bf16 v[88:91], a[136:139], a[88:91], v[88:91]\n" \
"ds_read_b128 a[132:135], v5 offset:50176\n" \
"v_cvt_pk_bf16_f32 v154, v116, v117\n" \
"v_mfma_f32_16x16x32_bf16 v[88:91], a[140:143], a[92:95], v[88:91]\n" \
"s_waitcnt lgkmcnt(4)\n" \
"v_exp_f32 v119, v119\n" \
"s_add_u32 m0, s79, 131968\n" \
"v_mfma_f32_16x16x32_bf16 v[68:71], a[144:147], v[32:35], 0\n" \
"buffer_load_dwordx4 v12, s[28:31], s81 offen offset:128 lds\n" \
"v_mfma_f32_16x16x32_bf16 v[68:71], a[148:151], v[36:39], v[68:71]\n" \
"s_mov_b32 s81, 65536\n" \
"v_exp_f32 v120, v120\n" \
"v_mfma_f32_16x16x32_bf16 v[76:79], a[144:147], v[48:51], 0\n" \
"ds_read_b128 a[136:139], v5 offset:51200\n" \
"v_cvt_pk_bf16_f32 v155, v118, v119\n" \
"v_mfma_f32_16x16x32_bf16 v[76:79], a[148:151], v[52:55], v[76:79]\n" \
"s_add_u32 m0, s48, 0\n" \
"v_exp_f32 v121, v121\n" \
"v_mfma_f32_16x16x32_bf16 v[84:87], a[144:147], a[64:67], 0\n" \
"buffer_load_dwordx4 v6, s[40:43], s16 offen offset:2048 sc1 lds\n" \
"v_mfma_f32_16x16x32_bf16 v[84:87], a[148:151], a[68:71], v[84:87]\n" \
"v_exp_f32 v122, v122\n" \
"v_mfma_f32_16x16x32_bf16 v[92:95], a[144:147], a[80:83], 0\n" \
"ds_read_b128 a[140:143], v5 offset:52224\n" \
"v_cvt_pk_bf16_f32 v156, v120, v121\n" \
"v_mfma_f32_16x16x32_bf16 v[92:95], a[148:151], a[84:87], v[92:95]\n" \
"s_waitcnt lgkmcnt(4)\n" \
"v_exp_f32 v123, v123\n" \
"s_add_u32 m0, s79, 139264\n" \
"v_mfma_f32_16x16x32_bf16 v[68:71], a[152:155], v[40:43], v[68:71]\n" \
"buffer_load_dwordx4 v12, s[28:31], s81 offen offset:0 lds\n" \
"v_mfma_f32_16x16x32_bf16 v[68:71], a[156:159], v[44:47], v[68:71]\n" \
"v_exp_f32 v124, v124\n" \
"v_mfma_f32_16x16x32_bf16 v[76:79], a[152:155], v[56:59], v[76:79]\n" \
"ds_read_b128 a[144:147], v5 offset:53248\n" \
"v_cvt_pk_bf16_f32 v157, v122, v123\n" \
"v_mfma_f32_16x16x32_bf16 v[76:79], a[156:159], v[60:63], v[76:79]\n" \
"s_add_u32 m0, s48, 0\n" \
"v_exp_f32 v125, v125\n" \
"v_mfma_f32_16x16x32_bf16 v[84:87], a[152:155], a[72:75], v[84:87]\n" \
"buffer_load_dwordx4 v6, s[40:43], s16 offen offset:3072 sc1 lds\n" \
"v_mfma_f32_16x16x32_bf16 v[84:87], a[156:159], a[76:79], v[84:87]\n" \
"v_exp_f32 v126, v126\n" \
"v_mfma_f32_16x16x32_bf16 v[92:95], a[152:155], a[88:91], v[92:95]\n" \
"ds_read_b128 a[148:151], v5 offset:54272\n" \
"v_cvt_pk_bf16_f32 v158, v124, v125\n" \
"v_mfma_f32_16x16x32_bf16 v[92:95], a[156:159], a[92:95], v[92:95]\n" \
"s_waitcnt lgkmcnt(4)\n" \
"v_exp_f32 v127, v127\n" \
"s_add_u32 m0, s79, 140160\n" \
"v_mfma_f32_16x16x32_bf16 v[160:163], v[180:183], v[144:147], v[160:163]\n" \
"buffer_load_dwordx4 v12, s[28:31], s81 offen offset:128 lds\n" \
"v_mfma_f32_16x16x32_bf16 v[164:167], v[180:183], v[148:151], v[164:167]\n" \
"s_mov_b32 s81, 131072\n" \
"v_cvt_pk_bf16_f32 v159, v126, v127\n" \
"v_mov_b32 v30, -1\n" \
"v_mfma_f32_16x16x32_bf16 v[168:171], v[180:183], v[152:155], v[168:171]\n" \
"v_exp_f32 v64, v64\n" \
"v_mfma_f32_16x16x32_bf16 v[172:175], v[180:183], v[156:159], v[172:175]\n" \
"s_add_u32 s80, s78, 4096\n" \
"v_mov_b32 v31, -1\n" \
"v_bfe_u32 v189, v4, 7, 1\n" \
"v_mfma_f32_16x16x32_bf16 v[192:195], a[128:131], v[144:147], v[192:195]\n" \
"buffer_store_dwordx4 v[28:31], v4, s[36:39], s80 offen offset:2048 sc1\n" \
"v_mfma_f32_16x16x32_bf16 v[196:199], a[132:135], v[144:147], v[196:199]\n" \
"v_bfe_u32 v190, v4, 4, 3\n" \
"v_lshl_add_u32 v189, v190, 1, v189\n" \
"v_mfma_f32_16x16x32_bf16 v[224:227], a[128:131], v[148:151], v[224:227]\n" \
"ds_read_b128 a[152:155], v5 offset:55296\n" \
"v_lshrrev_b32 v190, 8, v4\n" \
"v_mfma_f32_16x16x32_bf16 v[228:231], a[132:135], v[148:151], v[228:231]\n" \
"v_exp_f32 v65, v65\n" \
"v_mfma_f32_16x16x32_bf16 a[0:3], a[128:131], v[152:155], a[0:3]\n" \
"v_exp_f32 v66, v66\n" \
"v_mfma_f32_16x16x32_bf16 a[4:7], a[132:135], v[152:155], a[4:7]\n" \
"v_exp_f32 v67, v67\n" \
"v_mfma_f32_16x16x32_bf16 a[32:35], a[128:131], v[156:159], a[32:35]\n" \
"ds_read_b128 a[156:159], v5 offset:56320\n" \
"v_lshlrev_b32 v188, 11, v189\n" \
"v_mfma_f32_16x16x32_bf16 a[36:39], a[132:135], v[156:159], a[36:39]\n" \
"s_waitcnt vmcnt(17) lgkmcnt(4)\n" \
"v_exp_f32 v68, v68\n" \
"v_mfma_f32_16x16x32_bf16 v[200:203], a[136:139], v[144:147], v[200:203]\n" \
"s_barrier\n" \
"v_xor_b32 v5, 0x10000, v5\n" \
"v_and_b32 v189, 1, v190\n" \
"v_mfma_f32_16x16x32_bf16 v[204:207], a[140:143], v[144:147], v[204:207]\n" \
"s_add_u32 s17, s17, s19\n" \
"v_exp_f32 v69, v69\n" \
"v_mfma_f32_16x16x32_bf16 v[232:235], a[136:139], v[148:151], v[232:235]\n" \
"ds_read_b128 a[128:131], v5 offset:0\n" \
"v_lshl_add_u32 v188, v189, 5, v188\n" \
"v_mfma_f32_16x16x32_bf16 v[236:239], a[140:143], v[148:151], v[236:239]\n" \
"s_add_u32 m0, s48, 16384\n" \
"v_exp_f32 v70, v70\n" \
"v_mfma_f32_16x16x32_bf16 a[8:11], a[136:139], v[152:155], a[8:11]\n" \
"buffer_load_dwordx4 v6, s[40:43], s17 offen offset:0 sc1 lds\n" \
"v_mfma_f32_16x16x32_bf16 a[12:15], a[140:143], v[152:155], a[12:15]\n" \
"v_exp_f32 v71, v71\n" \
"v_mfma_f32_16x16x32_bf16 a[40:43], a[136:139], v[156:159], a[40:43]\n" \
"ds_read_b128 a[132:135], v5 offset:1024\n" \
"v_lshrrev_b32 v189, 1, v190\n" \
"v_mfma_f32_16x16x32_bf16 a[44:47], a[140:143], v[156:159], a[44:47]\n" \
"s_waitcnt lgkmcnt(4)\n" \
"v_exp_f32 v72, v72\n" \
"v_mfma_f32_16x16x32_bf16 v[208:211], a[144:147], v[144:147], v[208:211]\n" \
"v_exp_f32 v73, v73\n" \
"v_mfma_f32_16x16x32_bf16 v[212:215], a[148:151], v[144:147], v[212:215]\n" \
"v_mfma_f32_16x16x32_bf16 v[240:243], a[144:147], v[148:151], v[240:243]\n" \
"ds_read_b128 a[136:139], v5 offset:2048\n" \
"v_lshl_add_u32 v188, v189, 4, v188\n" \
"v_mfma_f32_16x16x32_bf16 v[244:247], a[148:151], v[148:151], v[244:247]\n" \
"s_add_u32 m0, s48, 16384\n" \
"v_exp_f32 v74, v74\n" \
"v_mfma_f32_16x16x32_bf16 a[16:19], a[144:147], v[152:155], a[16:19]\n" \
"buffer_load_dwordx4 v6, s[40:43], s17 offen offset:1024 sc1 lds\n" \
"v_mfma_f32_16x16x32_bf16 a[20:23], a[148:151], v[152:155], a[20:23]\n" \
"v_exp_f32 v75, v75\n" \
"v_mfma_f32_16x16x32_bf16 a[48:51], a[144:147], v[156:159], a[48:51]\n" \
"ds_read_b128 a[140:143], v5 offset:3072\n" \
"v_add_u32 v190, s83, v188\n" \
"v_mfma_f32_16x16x32_bf16 a[52:55], a[148:151], v[156:159], a[52:55]\n" \
"s_waitcnt lgkmcnt(4)\n" \
"s_add_u32 m0, s48, 16384\n" \
"v_add_u32 v188, s82, v188\n" \
"v_mfma_f32_16x16x32_bf16 v[216:219], a[152:155], v[144:147], v[216:219]\n" \
"buffer_load_dwordx4 v6, s[40:43], s17 offen offset:2048 sc1 lds\n" \
"v_mfma_f32_16x16x32_bf16 v[220:223], a[156:159], v[144:147], v[220:223]\n" \
"s_add_u32 s44, s44, 1\n" \
"v_exp_f32 v76, v76\n" \
"v_mfma_f32_16x16x32_bf16 v[248:251], a[152:155], v[148:151], v[248:251]\n" \
"ds_read_b128 a[144:147], v5 offset:4096\n" \
"v_add_u32 v189, 0x8000, v188\n" \
"v_mfma_f32_16x16x32_bf16 v[252:255], a[156:159], v[148:151], v[252:255]\n" \
"s_add_u32 m0, s48, 16384\n" \
"v_exp_f32 v77, v77\n" \
"v_mfma_f32_16x16x32_bf16 a[24:27], a[152:155], v[152:155], a[24:27]\n" \
"buffer_load_dwordx4 v6, s[40:43], s17 offen offset:3072 sc1 lds\n" \
"v_mfma_f32_16x16x32_bf16 a[28:31], a[156:159], v[152:155], a[28:31]\n" \
"s_xor_b32 s48, s48, 0x10000\n" \
"v_exp_f32 v78, v78\n" \
"v_mfma_f32_16x16x32_bf16 a[56:59], a[152:155], v[156:159], a[56:59]\n" \
"ds_read_b128 a[148:151], v5 offset:5120\n" \
"v_add_u32 v191, 0x8000, v190\n" \
"v_exp_f32 v79, v79\n" \
"v_mfma_f32_16x16x32_bf16 a[60:63], a[156:159], v[156:159], a[60:63]\n" \
"s_waitcnt lgkmcnt(4)\n" \
"v_cvt_pk_bf16_f32 v128, v64, v65\n" \
"v_cvt_pk_bf16_f32 v129, v66, v67\n" \
"v_cvt_pk_bf16_f32 v130, v68, v69\n" \
"v_cvt_pk_bf16_f32 v131, v70, v71\n" \
"v_cvt_pk_bf16_f32 v132, v72, v73\n" \
"v_cvt_pk_bf16_f32 v133, v74, v75\n" \
"v_cvt_pk_bf16_f32 v134, v76, v77\n" \
"v_cvt_pk_bf16_f32 v135, v78, v79\n" \
"v_mfma_f32_16x16x32_bf16 v[96:99], a[128:131], v[32:35], 0\n" \
"global_load_dwordx4 a[96:99], v188, s[60:61]\n" \
"v_mfma_f32_16x16x32_bf16 v[96:99], a[132:135], v[36:39], v[96:99]\n" \
"s_add_u32 s16, s16, s9\n" \
"v_exp_f32 v80, v80\n" \
"v_mfma_f32_16x16x32_bf16 v[104:107], a[128:131], v[48:51], 0\n" \
"ds_read_b128 a[152:155], v5 offset:6144\n" \
"v_mfma_f32_16x16x32_bf16 v[104:107], a[132:135], v[52:55], v[104:107]\n" \
"s_add_u32 m0, s49, 32768\n" \
"v_exp_f32 v81, v81\n" \
"v_mfma_f32_16x16x32_bf16 v[112:115], a[128:131], a[64:67], 0\n" \
"buffer_load_dwordx4 v6, s[40:43], s16 offen offset:0 sc1 lds\n" \
"v_mfma_f32_16x16x32_bf16 v[112:115], a[132:135], a[68:71], v[112:115]\n" \
"v_exp_f32 v82, v82\n" \
"v_mfma_f32_16x16x32_bf16 v[120:123], a[128:131], a[80:83], 0\n" \
"ds_read_b128 a[156:159], v5 offset:7168\n" \
"v_cvt_pk_bf16_f32 v136, v80, v81\n" \
"v_mfma_f32_16x16x32_bf16 v[120:123], a[132:135], a[84:87], v[120:123]\n" \
"s_waitcnt lgkmcnt(4)\n" \
"v_exp_f32 v83, v83\n" \
"s_add_u32 m0, s79, 147456\n" \
"v_mfma_f32_16x16x32_bf16 v[96:99], a[136:139], v[40:43], v[96:99]\n" \
"buffer_load_dwordx4 v12, s[28:31], s81 offen offset:0 lds\n" \
"v_mfma_f32_16x16x32_bf16 v[96:99], a[140:143], v[44:47], v[96:99]\n" \
"v_exp_f32 v84, v84\n" \
"v_mfma_f32_16x16x32_bf16 v[104:107], a[136:139], v[56:59], v[104:107]\n" \
"ds_read_b128 a[128:131], v5 offset:16384\n" \
"v_cvt_pk_bf16_f32 v137, v82, v83\n" \
"v_mfma_f32_16x16x32_bf16 v[104:107], a[140:143], v[60:63], v[104:107]\n" \
"s_add_u32 m0, s49, 32768\n" \
"v_exp_f32 v85, v85\n" \
"v_mfma_f32_16x16x32_bf16 v[112:115], a[136:139], a[72:75], v[112:115]\n" \
"buffer_load_dwordx4 v6, s[40:43], s16 offen offset:1024 sc1 lds\n" \
"v_mfma_f32_16x16x32_bf16 v[112:115], a[140:143], a[76:79], v[112:115]\n" \
"v_exp_f32 v86, v86\n" \
"v_mfma_f32_16x16x32_bf16 v[120:123], a[136:139], a[88:91], v[120:123]\n" \
"ds_read_b128 a[132:135], v5 offset:17408\n" \
"v_cvt_pk_bf16_f32 v138, v84, v85\n" \
"v_mfma_f32_16x16x32_bf16 v[120:123], a[140:143], a[92:95], v[120:123]\n" \
"s_waitcnt lgkmcnt(4)\n" \
"v_exp_f32 v87, v87\n" \
"s_add_u32 m0, s79, 148352\n" \
"v_mfma_f32_16x16x32_bf16 v[100:103], a[144:147], v[32:35], 0\n" \
"buffer_load_dwordx4 v12, s[28:31], s81 offen offset:128 lds\n" \
"v_mfma_f32_16x16x32_bf16 v[100:103], a[148:151], v[36:39], v[100:103]\n" \
"s_mov_b32 s81, 196608\n" \
"v_exp_f32 v88, v88\n" \
"v_mfma_f32_16x16x32_bf16 v[108:111], a[144:147], v[48:51], 0\n" \
"ds_read_b128 a[136:139], v5 offset:18432\n" \
"v_cvt_pk_bf16_f32 v139, v86, v87\n" \
"v_mfma_f32_16x16x32_bf16 v[108:111], a[148:151], v[52:55], v[108:111]\n" \
"s_add_u32 m0, s49, 32768\n" \
"v_exp_f32 v89, v89\n" \
"v_mfma_f32_16x16x32_bf16 v[116:119], a[144:147], a[64:67], 0\n" \
"buffer_load_dwordx4 v6, s[40:43], s16 offen offset:2048 sc1 lds\n" \
"v_mfma_f32_16x16x32_bf16 v[116:119], a[148:151], a[68:71], v[116:119]\n" \
"v_exp_f32 v90, v90\n" \
"v_mfma_f32_16x16x32_bf16 v[124:127], a[144:147], a[80:83], 0\n" \
"ds_read_b128 a[140:143], v5 offset:19456\n" \
"v_cvt_pk_bf16_f32 v140, v88, v89\n" \
"v_mfma_f32_16x16x32_bf16 v[124:127], a[148:151], a[84:87], v[124:127]\n" \
"s_waitcnt lgkmcnt(4)\n" \
"v_exp_f32 v91, v91\n" \
"s_add_u32 m0, s79, 155648\n" \
"v_mfma_f32_16x16x32_bf16 v[100:103], a[152:155], v[40:43], v[100:103]\n" \
"buffer_load_dwordx4 v12, s[28:31], s81 offen offset:0 lds\n" \
"v_mfma_f32_16x16x32_bf16 v[100:103], a[156:159], v[44:47], v[100:103]\n" \
"v_exp_f32 v92, v92\n" \
"v_mfma_f32_16x16x32_bf16 v[108:111], a[152:155], v[56:59], v[108:111]\n" \
"ds_read_b128 a[144:147], v5 offset:20480\n" \
"v_cvt_pk_bf16_f32 v141, v90, v91\n" \
"v_mfma_f32_16x16x32_bf16 v[108:111], a[156:159], v[60:63], v[108:111]\n" \
"s_add_u32 m0, s49, 32768\n" \
"v_exp_f32 v93, v93\n" \
"v_mfma_f32_16x16x32_bf16 v[116:119], a[152:155], a[72:75], v[116:119]\n" \
"buffer_load_dwordx4 v6, s[40:43], s16 offen offset:3072 sc1 lds\n" \
"v_mfma_f32_16x16x32_bf16 v[116:119], a[156:159], a[76:79], v[116:119]\n" \
"LH17_%=:\ns_cbranch_execz LH21_%=\n" \
"v_exp_f32 v94, v94\n" \
"v_mfma_f32_16x16x32_bf16 v[124:127], a[152:155], a[88:91], v[124:127]\n" \
"ds_read_b128 a[148:151], v5 offset:21504\n" \
"v_cvt_pk_bf16_f32 v142, v92, v93\n" \
"v_mfma_f32_16x16x32_bf16 v[124:127], a[156:159], a[92:95], v[124:127]\n" \
"s_waitcnt lgkmcnt(4)\n" \
"v_exp_f32 v95, v95\n" \
"s_add_u32 m0, s79, 156544\n" \
"v_mfma_f32_16x16x32_bf16 v[160:163], v[180:183], v[128:131], v[160:163]\n" \
"buffer_load_dwordx4 v12, s[28:31], s81 offen offset:128 lds\n" \
"v_mfma_f32_16x16x32_bf16 v[164:167], v[180:183], v[132:135], v[164:167]\n" \
"s_mov_b32 s81, 262144\n" \
"v_cvt_pk_bf16_f32 v143, v94, v95\n" \
"v_mfma_f32_16x16x32_bf16 v[168:171], v[180:183], v[136:139], v[168:171]\n" \
"buffer_store_dwordx4 v[28:31], v4, s[36:39], s80 offen offset:3072 sc1\n" \
"v_mfma_f32_16x16x32_bf16 v[172:175], v[180:183], v[140:143], v[172:175]\n" \
"s_add_u32 s80, s78, 8192\n" \
"v_exp_f32 v96, v96\n" \
"v_mfma_f32_16x16x32_bf16 v[192:195], a[128:131], v[128:131], v[192:195]\n" \
"global_load_dwordx4 a[100:103], v189, s[60:61]\n" \
"v_mfma_f32_16x16x32_bf16 v[196:199], a[132:135], v[128:131], v[196:199]\n" \
"v_exp_f32 v97, v97\n" \
"v_mfma_f32_16x16x32_bf16 v[224:227], a[128:131], v[132:135], v[224:227]\n" \
"ds_read_b128 a[152:155], v5 offset:22528\n" \
"v_cvt_pk_bf16_f32 v144, v96, v97\n" \
"v_mfma_f32_16x16x32_bf16 v[228:231], a[132:135], v[132:135], v[228:231]\n" \
"buffer_store_dwordx4 v[28:31], v4, s[36:39], s80 offen offset:0 sc1\n" \
"v_mfma_f32_16x16x32_bf16 a[0:3], a[128:131], v[136:139], a[0:3]\n" \
"v_exp_f32 v98, v98\n" \
"v_mfma_f32_16x16x32_bf16 a[4:7], a[132:135], v[136:139], a[4:7]\n" \
"global_load_dwordx4 a[104:107], v190, s[60:61]\n" \
"v_mfma_f32_16x16x32_bf16 a[32:35], a[128:131], v[140:143], a[32:35]\n" \
"ds_read_b128 a[156:159], v5 offset:23552\n" \
"v_mfma_f32_16x16x32_bf16 a[36:39], a[132:135], v[140:143], a[36:39]\n" \
"s_waitcnt vmcnt(18) lgkmcnt(4)\n" \
"v_exp_f32 v99, v99\n" \
"v_mfma_f32_16x16x32_bf16 v[200:203], a[136:139], v[128:131], v[200:203]\n" \
"s_barrier\n" \
"v_exp_f32 v100, v100\n" \
"v_mfma_f32_16x16x32_bf16 v[204:207], a[140:143], v[128:131], v[204:207]\n" \
"s_add_u32 s17, s17, s19\n" \
"v_exp_f32 v101, v101\n" \
"v_mfma_f32_16x16x32_bf16 v[232:235], a[136:139], v[132:135], v[232:235]\n" \
"ds_read_b128 a[128:131], v5 offset:32768\n" \
"v_cvt_pk_bf16_f32 v145, v98, v99\n" \
"v_mfma_f32_16x16x32_bf16 v[236:239], a[140:143], v[132:135], v[236:239]\n" \
"s_add_u32 m0, s49, 49152\n" \
"v_exp_f32 v102, v102\n" \
"v_mfma_f32_16x16x32_bf16 a[8:11], a[136:139], v[136:139], a[8:11]\n" \
"buffer_load_dwordx4 v6, s[40:43], s17 offen offset:0 sc1 lds\n" \
"v_mfma_f32_16x16x32_bf16 a[12:15], a[140:143], v[136:139], a[12:15]\n" \
"v_exp_f32 v103, v103\n" \
"v_mfma_f32_16x16x32_bf16 a[40:43], a[136:139], v[140:143], a[40:43]\n" \
"ds_read_b128 a[132:135], v5 offset:33792\n" \
"v_cvt_pk_bf16_f32 v146, v100, v101\n" \
"v_mfma_f32_16x16x32_bf16 a[44:47], a[140:143], v[140:143], a[44:47]\n" \
"s_waitcnt lgkmcnt(4)\n" \
"v_exp_f32 v104, v104\n" \
"v_mfma_f32_16x16x32_bf16 v[208:211], a[144:147], v[128:131], v[208:211]\n" \
"v_exp_f32 v105, v105\n" \
"v_mfma_f32_16x16x32_bf16 v[212:215], a[148:151], v[128:131], v[212:215]\n" \
"v_mfma_f32_16x16x32_bf16 v[240:243], a[144:147], v[132:135], v[240:243]\n" \
"ds_read_b128 a[136:139], v5 offset:34816\n" \
"v_cvt_pk_bf16_f32 v147, v102, v103\n" \
"v_mfma_f32_16x16x32_bf16 v[244:247], a[148:151], v[132:135], v[244:247]\n" \
"s_add_u32 m0, s49, 49152\n" \
"v_exp_f32 v106, v106\n" \
"v_mfma_f32_16x16x32_bf16 a[16:19], a[144:147], v[136:139], a[16:19]\n" \
"buffer_load_dwordx4 v6, s[40:43], s17 offen offset:1024 sc1 lds\n" \
"v_mfma_f32_16x16x32_bf16 a[20:23], a[148:151], v[136:139], a[20:23]\n" \
"v_exp_f32 v107, v107\n" \
"v_mfma_f32_16x16x32_bf16 a[48:51], a[144:147], v[140:143], a[48:51]\n" \
"ds_read_b128 a[140:143], v5 offset:35840\n" \
"v_cvt_pk_bf16_f32 v148, v104, v105\n" \
"v_mfma_f32_16x16x32_bf16 a[52:55], a[148:151], v[140:143], a[52:55]\n" \
"s_waitcnt lgkmcnt(4)\n" \
"s_add_u32 m0, s49, 49152\n" \
"ds_read_b64_tr_b16 a[160:161], v13 offset:0\n" \
"v_mfma_f32_16x16x32_bf16 v[216:219], a[152:155], v[128:131], v[216:219]\n" \
"buffer_load_dwordx4 v6, s[40:43], s17 offen offset:2048 sc1 lds\n" \
"v_mfma_f32_16x16x32_bf16 v[220:223], a[156:159], v[128:131], v[220:223]\n" \
"v_exp_f32 v108, v108\n" \
"v_mfma_f32_16x16x32_bf16 v[248:251], a[152:155], v[132:135], v[248:251]\n" \
"ds_read_b128 a[144:147], v5 offset:36864\n" \
"v_cvt_pk_bf16_f32 v149, v106, v107\n" \
"v_mfma_f32_16x16x32_bf16 v[252:255], a[156:159], v[132:135], v[252:255]\n" \
"s_add_u32 m0, s49, 49152\n" \
"v_exp_f32 v109, v109\n" \
"v_mfma_f32_16x16x32_bf16 a[24:27], a[152:155], v[136:139], a[24:27]\n" \
"buffer_load_dwordx4 v6, s[40:43], s17 offen offset:3072 sc1 lds\n" \
"v_mfma_f32_16x16x32_bf16 a[28:31], a[156:159], v[136:139], a[28:31]\n" \
"v_exp_f32 v110, v110\n" \
"v_mfma_f32_16x16x32_bf16 a[56:59], a[152:155], v[140:143], a[56:59]\n" \
"ds_read_b128 a[148:151], v5 offset:37888\n" \
"v_exp_f32 v111, v111\n" \
"v_mfma_f32_16x16x32_bf16 a[60:63], a[156:159], v[140:143], a[60:63]\n" \
"s_waitcnt lgkmcnt(4)\n" \
"ds_read_b64_tr_b16 a[162:163], v14 offset:0\n" \
"v_cvt_pk_bf16_f32 v150, v108, v109\n" \
"v_cvt_pk_bf16_f32 v151, v110, v111\n" \
"v_mfma_f32_16x16x32_bf16 v[64:67], a[128:131], v[32:35], 0\n" \
"global_load_dwordx4 a[108:111], v191, s[60:61]\n" \
"v_mfma_f32_16x16x32_bf16 v[64:67], a[132:135], v[36:39], v[64:67]\n" \
"s_add_u32 s16, s16, s9\n" \
"ds_read_b64_tr_b16 a[164:165], v15 offset:0\n" \
"s_lshl_b32 s92, %2, 9\n" \
"v_mfma_f32_16x16x32_bf16 v[72:75], a[128:131], v[48:51], 0\n" \
"s_add_u32 s92, s92, 94375936\n" \
"ds_read_b128 a[152:155], v5 offset:38912\n" \
"v_lshrrev_b32 v17, 1, v4\n" \
"v_mfma_f32_16x16x32_bf16 v[72:75], a[132:135], v[52:55], v[72:75]\n" \
"s_add_u32 m0, s48, 0\n" \
"ds_read_b64_tr_b16 a[166:167], v16 offset:0\n" \
"v_add_u32 v17, s92, v17\n" \
"v_mfma_f32_16x16x32_bf16 v[80:83], a[128:131], a[64:67], 0\n" \
"buffer_load_dwordx4 v6, s[40:43], s16 offen offset:0 sc1 lds\n" \
"v_mfma_f32_16x16x32_bf16 v[80:83], a[132:135], a[68:71], v[80:83]\n" \
"s_mov_b64 s[4:5], 0\n" \
"ds_read_b64_tr_b16 a[168:169], v13 offset:8192\n" \
"v_bfe_u32 v188, v4, 5, 2\n" \
"v_mfma_f32_16x16x32_bf16 v[88:91], a[128:131], a[80:83], 0\n" \
"ds_read_b128 a[156:159], v5 offset:39936\n" \
"v_and_b32 v189, 0x10, v4\n" \
"v_mfma_f32_16x16x32_bf16 v[88:91], a[132:135], a[84:87], v[88:91]\n" \
"s_waitcnt lgkmcnt(4)\n" \
"v_add_u32 v188, v189, v188\n" \
"v_lshrrev_b32 v189, 8, v4\n" \
"v_mfma_f32_16x16x32_bf16 v[64:67], a[136:139], v[40:43], v[64:67]\n" \
"v_exp_f32 v112, v112\n" \
"v_mfma_f32_16x16x32_bf16 v[64:67], a[140:143], v[44:47], v[64:67]\n" \
"s_xor_b32 s49, s49, 0x10000\n" \
"v_exp_f32 v113, v113\n" \
"v_mfma_f32_16x16x32_bf16 v[72:75], a[136:139], v[56:59], v[72:75]\n" \
"ds_read_b128 a[128:131], v5 offset:49152\n" \
"v_lshl_add_u32 v188, v189, 10, v188\n" \
"v_mfma_f32_16x16x32_bf16 v[72:75], a[140:143], v[60:63], v[72:75]\n" \
"s_add_u32 m0, s48, 0\n" \
"v_exp_f32 v114, v114\n" \
"v_mfma_f32_16x16x32_bf16 v[80:83], a[136:139], a[72:75], v[80:83]\n" \
"buffer_load_dwordx4 v6, s[40:43], s16 offen offset:1024 sc1 lds\n" \
"v_mfma_f32_16x16x32_bf16 v[80:83], a[140:143], a[76:79], v[80:83]\n" \
"v_exp_f32 v115, v115\n" \
"v_mfma_f32_16x16x32_bf16 v[88:91], a[136:139], a[88:91], v[88:91]\n" \
"ds_read_b128 a[132:135], v5 offset:50176\n" \
"v_and_b32 v189, 0x80, v4\n" \
"v_mfma_f32_16x16x32_bf16 v[88:91], a[140:143], a[92:95], v[88:91]\n" \
"s_waitcnt lgkmcnt(4)\n" \
"v_exp_f32 v116, v116\n" \
"v_mfma_f32_16x16x32_bf16 v[68:71], a[144:147], v[32:35], 0\n" \
"v_exp_f32 v117, v117\n" \
"v_mfma_f32_16x16x32_bf16 v[68:71], a[148:151], v[36:39], v[68:71]\n" \
"v_exp_f32 v118, v118\n" \
"v_mfma_f32_16x16x32_bf16 v[76:79], a[144:147], v[48:51], 0\n" \
"ds_read_b128 a[136:139], v5 offset:51200\n" \
"v_lshl_add_u32 v188, v188, 4, v189\n" \
"v_mfma_f32_16x16x32_bf16 v[76:79], a[148:151], v[52:55], v[76:79]\n" \
"s_add_u32 m0, s48, 0\n" \
"v_exp_f32 v119, v119\n" \
"v_mfma_f32_16x16x32_bf16 v[84:87], a[144:147], a[64:67], 0\n" \
"buffer_load_dwordx4 v6, s[40:43], s16 offen offset:2048 sc1 lds\n" \
"v_mfma_f32_16x16x32_bf16 v[84:87], a[148:151], a[68:71], v[84:87]\n" \
"v_exp_f32 v120, v120\n" \
"v_mfma_f32_16x16x32_bf16 v[92:95], a[144:147], a[80:83], 0\n" \
"ds_read_b128 a[140:143], v5 offset:52224\n" \
"v_add_u32 v189, 0x10000, v188\n" \
"v_mfma_f32_16x16x32_bf16 v[92:95], a[148:151], a[84:87], v[92:95]\n" \
"s_waitcnt lgkmcnt(4)\n" \
"v_exp_f32 v121, v121\n" \
"v_mfma_f32_16x16x32_bf16 v[68:71], a[152:155], v[40:43], v[68:71]\n" \
"v_exp_f32 v122, v122\n" \
"v_mfma_f32_16x16x32_bf16 v[68:71], a[156:159], v[44:47], v[68:71]\n" \
"v_exp_f32 v123, v123\n" \
"v_mfma_f32_16x16x32_bf16 v[76:79], a[152:155], v[56:59], v[76:79]\n" \
"s_add_u32 s100, s100, 262144\ns_addc_u32 s101, s101, 0\n" \
"ds_read_b128 a[144:147], v5 offset:53248\n" \
"v_mfma_f32_16x16x32_bf16 v[76:79], a[156:159], v[60:63], v[76:79]\n" \
"s_add_u32 m0, s48, 0\n" \
"v_exp_f32 v124, v124\n" \
"v_mfma_f32_16x16x32_bf16 v[84:87], a[152:155], a[72:75], v[84:87]\n" \
"buffer_load_dwordx4 v6, s[40:43], s16 offen offset:3072 sc1 lds\n" \
"v_mfma_f32_16x16x32_bf16 v[84:87], a[156:159], a[76:79], v[84:87]\n" \
"v_exp_f32 v125, v125\n" \
"v_mfma_f32_16x16x32_bf16 v[92:95], a[152:155], a[88:91], v[92:95]\n" \
"ds_read_b128 a[148:151], v5 offset:54272\n" \
"v_cvt_pk_bf16_f32 v152, v112, v113\n" \
"v_mfma_f32_16x16x32_bf16 v[92:95], a[156:159], a[92:95], v[92:95]\n" \
"s_waitcnt lgkmcnt(4)\n" \
"v_exp_f32 v126, v126\n" \
"v_mfma_f32_16x16x32_bf16 v[160:163], v[180:183], v[144:147], v[160:163]\n" \
"v_exp_f32 v127, v127\n" \
"v_cvt_pk_bf16_f32 v153, v114, v115\n" \
"v_cvt_pk_bf16_f32 v154, v116, v117\n" \
"v_cvt_pk_bf16_f32 v155, v118, v119\n" \
"v_mfma_f32_16x16x32_bf16 v[164:167], v[180:183], v[148:151], v[164:167]\n" \
"s_mov_b64 exec, 0xffff\nglobal_load_dword v1, v2, s[100:101]\ns_mov_b64 exec, -1\n" \
"v_cvt_pk_bf16_f32 v156, v120, v121\n" \
"v_cvt_pk_bf16_f32 v157, v122, v123\n" \
"v_cvt_pk_bf16_f32 v158, v124, v125\n" \
"v_cvt_pk_bf16_f32 v159, v126, v127\n" \
"v_mfma_f32_16x16x32_bf16 v[168:171], v[180:183], v[152:155], v[168:171]\n" \
"ds_read_b64_tr_b16 a[170:171], v14 offset:8192\n" \
"v_mfma_f32_16x16x32_bf16 v[172:175], v[180:183], v[156:159], v[172:175]\n" \
"ds_read_b64_tr_b16 a[172:173], v15 offset:8192\n" \
"v_mfma_f32_16x16x32_bf16 v[192:195], a[128:131], v[144:147], v[192:195]\n" \
"ds_read_b64_tr_b16 a[174:175], v16 offset:8192\n" \
"v_mfma_f32_16x16x32_bf16 v[196:199], a[132:135], v[144:147], v[196:199]\n" \
"v_exp_f32 v64, v64\n" \
"v_mfma_f32_16x16x32_bf16 v[224:227], a[128:131], v[148:151], v[224:227]\n" \
"ds_read_b128 a[152:155], v5 offset:55296\n" \
"v_mfma_f32_16x16x32_bf16 v[228:231], a[132:135], v[148:151], v[228:231]\n" \
"v_exp_f32 v65, v65\n" \
"v_mfma_f32_16x16x32_bf16 a[0:3], a[128:131], v[152:155], a[0:3]\n" \
"v_exp_f32 v66, v66\n" \
"v_mfma_f32_16x16x32_bf16 a[4:7], a[132:135], v[152:155], a[4:7]\n" \
"s_waitcnt lgkmcnt(1)\n" \
"v_exp_f32 v67, v67\n" \
"v_mfma_f32_16x16x32_bf16 a[32:35], a[128:131], v[156:159], a[32:35]\n" \
"ds_read_b128 a[156:159], v5 offset:56320\n" \
"v_cvt_pk_bf16_f32 v128, v64, v65\n" \
"v_mfma_f32_16x16x32_bf16 a[36:39], a[132:135], v[156:159], a[36:39]\n" \
"s_waitcnt vmcnt(14) lgkmcnt(4)\n" \
"v_exp_f32 v68, v68\n" \
"v_mfma_f32_16x16x32_bf16 v[200:203], a[136:139], v[144:147], v[200:203]\n" \
"s_barrier\n" \
"v_xor_b32 v5, 0x10000, v5\n" \
"v_cvt_pk_bf16_f32 v129, v66, v67\n" \
"v_mfma_f32_16x16x32_bf16 v[204:207], a[140:143], v[144:147], v[204:207]\n" \
"s_add_u32 s17, s17, s19\n" \
"v_exp_f32 v69, v69\n" \
"v_mfma_f32_16x16x32_bf16 v[232:235], a[136:139], v[148:151], v[232:235]\n" \
"ds_read_b128 a[128:131], v5 offset:0\n" \
"v_cvt_pk_bf16_f32 v130, v68, v69\n" \
"v_mfma_f32_16x16x32_bf16 v[236:239], a[140:143], v[148:151], v[236:239]\n" \
"s_add_u32 m0, s48, 16384\n" \
"v_exp_f32 v70, v70\n" \
"v_mfma_f32_16x16x32_bf16 a[8:11], a[136:139], v[152:155], a[8:11]\n" \
"buffer_load_dwordx4 v6, s[40:43], s17 offen offset:0 sc1 lds\n" \
"v_mfma_f32_16x16x32_bf16 a[12:15], a[140:143], v[152:155], a[12:15]\n" \
"v_exp_f32 v71, v71\n" \
"v_mfma_f32_16x16x32_bf16 a[40:43], a[136:139], v[156:159], a[40:43]\n" \
"ds_read_b128 a[132:135], v5 offset:1024\n" \
"v_cvt_pk_bf16_f32 v131, v70, v71\n" \
"v_mfma_f32_16x16x32_bf16 a[44:47], a[140:143], v[156:159], a[44:47]\n" \
"s_waitcnt lgkmcnt(4)\n" \
"v_exp_f32 v72, v72\n" \
"v_mfma_f32_16x16x32_bf16 v[208:211], a[144:147], v[144:147], v[208:211]\n" \
"v_exp_f32 v73, v73\n" \
"v_mfma_f32_16x16x32_bf16 v[212:215], a[148:151], v[144:147], v[212:215]\n" \
"v_mfma_f32_16x16x32_bf16 v[240:243], a[144:147], v[148:151], v[240:243]\n" \
"ds_read_b128 a[136:139], v5 offset:2048\n" \
"v_cvt_pk_bf16_f32 v132, v72, v73\n" \
"v_mfma_f32_16x16x32_bf16 v[244:247], a[148:151], v[148:151], v[244:247]\n" \
"s_add_u32 m0, s48, 16384\n" \
"v_exp_f32 v74, v74\n" \
"v_mfma_f32_16x16x32_bf16 a[16:19], a[144:147], v[152:155], a[16:19]\n" \
"buffer_load_dwordx4 v6, s[40:43], s17 offen offset:1024 sc1 lds\n" \
"v_mfma_f32_16x16x32_bf16 a[20:23], a[148:151], v[152:155], a[20:23]\n" \
"v_exp_f32 v75, v75\n" \
"v_mfma_f32_16x16x32_bf16 a[48:51], a[144:147], v[156:159], a[48:51]\n" \
"ds_read_b128 a[140:143], v5 offset:3072\n" \
"v_cvt_pk_bf16_f32 v133, v74, v75\n" \
"v_mfma_f32_16x16x32_bf16 a[52:55], a[148:151], v[156:159], a[52:55]\n" \
"s_waitcnt lgkmcnt(4)\n" \
"s_add_u32 m0, s48, 16384\n" \
"ds_read_b64_tr_b16 a[176:177], v13 offset:16384\n" \
"v_mfma_f32_16x16x32_bf16 v[216:219], a[152:155], v[144:147], v[216:219]\n" \
"buffer_load_dwordx4 v6, s[40:43], s17 offen offset:2048 sc1 lds\n" \
"v_mfma_f32_16x16x32_bf16 v[220:223], a[156:159], v[144:147], v[220:223]\n" \
"s_add_u32 s44, s44, 1\n" \
"v_exp_f32 v76, v76\n" \
"v_mfma_f32_16x16x32_bf16 v[248:251], a[152:155], v[148:151], v[248:251]\n" \
"ds_read_b128 a[144:147], v5 offset:4096\n" \
"v_mfma_f32_16x16x32_bf16 v[252:255], a[156:159], v[148:151], v[252:255]\n" \
"s_add_u32 m0, s48, 16384\n" \
"v_exp_f32 v77, v77\n" \
"v_mfma_f32_16x16x32_bf16 a[24:27], a[152:155], v[152:155], a[24:27]\n" \
"buffer_load_dwordx4 v6, s[40:43], s17 offen offset:3072 sc1 lds\n" \
"v_mfma_f32_16x16x32_bf16 a[28:31], a[156:159], v[152:155], a[28:31]\n" \
"s_xor_b32 s48, s48, 0x10000\n" \
"v_exp_f32 v78, v78\n" \
"v_mfma_f32_16x16x32_bf16 a[56:59], a[152:155], v[156:159], a[56:59]\n" \
"ds_read_b128 a[148:151], v5 offset:5120\n" \
"v_exp_f32 v79, v79\n" \
"v_mfma_f32_16x16x32_bf16 a[60:63], a[156:159], v[156:159], a[60:63]\n" \
"s_waitcnt lgkmcnt(4)\n" \
"ds_read_b64_tr_b16 a[178:179], v14 offset:16384\n" \
"v_cvt_pk_bf16_f32 v134, v76, v77\n" \
"v_cvt_pk_bf16_f32 v135, v78, v79\n" \
"v_mfma_f32_16x16x32_bf16 v[96:99], a[128:131], v[32:35], 0\n" \
"ds_read_b64_tr_b16 a[180:181], v15 offset:16384\n" \
"v_mfma_f32_16x16x32_bf16 v[96:99], a[132:135], v[36:39], v[96:99]\n" \
"s_add_u32 s16, s16, s9\n" \
"ds_read_b64_tr_b16 a[182:183], v16 offset:16384\n" \
"v_mfma_f32_16x16x32_bf16 v[104:107], a[128:131], v[48:51], 0\n" \
"ds_read_b128 a[152:155], v5 offset:6144\n" \
"v_mfma_f32_16x16x32_bf16 v[104:107], a[132:135], v[52:55], v[104:107]\n" \
"s_add_u32 m0, s49, 32768\n" \
"v_exp_f32 v80, v80\n" \
"v_mfma_f32_16x16x32_bf16 v[112:115], a[128:131], a[64:67], 0\n" \
"buffer_load_dwordx4 v6, s[40:43], s16 offen offset:0 sc1 lds\n" \
"v_mfma_f32_16x16x32_bf16 v[112:115], a[132:135], a[68:71], v[112:115]\n" \
"ds_read_b64_tr_b16 a[184:185], v13 offset:24576\n" \
"v_mfma_f32_16x16x32_bf16 v[120:123], a[128:131], a[80:83], 0\n" \
"ds_read_b128 a[156:159], v5 offset:7168\n" \
"v_mfma_f32_16x16x32_bf16 v[120:123], a[132:135], a[84:87], v[120:123]\n" \
"s_waitcnt lgkmcnt(4)\n" \
"v_exp_f32 v81, v81\n" \
"s_add_u32 m0, s79, 131072\n" \
"v_mfma_f32_16x16x32_bf16 v[96:99], a[136:139], v[40:43], v[96:99]\n" \
"buffer_load_dwordx4 v12, s[28:31], s81 offen offset:0 lds\n" \
"v_mfma_f32_16x16x32_bf16 v[96:99], a[140:143], v[44:47], v[96:99]\n" \
"v_exp_f32 v82, v82\n" \
"v_mfma_f32_16x16x32_bf16 v[104:107], a[136:139], v[56:59], v[104:107]\n" \
"ds_read_b128 a[128:131], v5 offset:16384\n" \
"v_cvt_pk_bf16_f32 v136, v80, v81\n" \
"v_mfma_f32_16x16x32_bf16 v[104:107], a[140:143], v[60:63], v[104:107]\n" \
"s_add_u32 m0, s49, 32768\n" \
"v_exp_f32 v83, v83\n" \
"v_mfma_f32_16x16x32_bf16 v[112:115], a[136:139], a[72:75], v[112:115]\n" \
"buffer_load_dwordx4 v6, s[40:43], s16 offen offset:1024 sc1 lds\n" \
"v_mfma_f32_16x16x32_bf16 v[112:115], a[140:143], a[76:79], v[112:115]\n" \
"ds_read_b64_tr_b16 a[186:187], v14 offset:24576\n" \
"v_cvt_pk_bf16_f32 v137, v82, v83\n" \
"v_mfma_f32_16x16x32_bf16 v[120:123], a[136:139], a[88:91], v[120:123]\n" \
"ds_read_b128 a[132:135], v5 offset:17408\n" \
"v_mfma_f32_16x16x32_bf16 v[120:123], a[140:143], a[92:95], v[120:123]\n" \
"s_waitcnt lgkmcnt(4)\n" \
"v_exp_f32 v84, v84\n" \
"v_mfma_f32_16x16x32_bf16 v[100:103], a[144:147], v[32:35], 0\n" \
"v_exp_f32 v85, v85\n" \
"v_mfma_f32_16x16x32_bf16 v[100:103], a[148:151], v[36:39], v[100:103]\n" \
"v_exp_f32 v86, v86\n" \
"v_mfma_f32_16x16x32_bf16 v[108:111], a[144:147], v[48:51], 0\n" \
"ds_read_b128 a[136:139], v5 offset:18432\n" \
"v_cvt_pk_bf16_f32 v138, v84, v85\n" \
"v_mfma_f32_16x16x32_bf16 v[108:111], a[148:151], v[52:55], v[108:111]\n" \
"s_add_u32 m0, s49, 32768\n" \
"v_exp_f32 v87, v87\n" \
"v_mfma_f32_16x16x32_bf16 v[116:119], a[144:147], a[64:67], 0\n" \
"buffer_load_dwordx4 v6, s[40:43], s16 offen offset:2048 sc1 lds\n" \
"v_mfma_f32_16x16x32_bf16 v[116:119], a[148:151], a[68:71], v[116:119]\n" \
"v_exp_f32 v88, v88\n" \
"v_mfma_f32_16x16x32_bf16 v[124:127], a[144:147], a[80:83], 0\n" \
"ds_read_b128 a[140:143], v5 offset:19456\n" \
"v_cvt_pk_bf16_f32 v139, v86, v87\n" \
"v_mfma_f32_16x16x32_bf16 v[124:127], a[148:151], a[84:87], v[124:127]\n" \
"s_waitcnt lgkmcnt(4)\n" \
"v_exp_f32 v89, v89\n" \
"v_mfma_f32_16x16x32_bf16 v[100:103], a[152:155], v[40:43], v[100:103]\n" \
"v_exp_f32 v90, v90\n" \
"v_mfma_f32_16x16x32_bf16 v[100:103], a[156:159], v[44:47], v[100:103]\n" \
"v_exp_f32 v91, v91\n" \
"v_mfma_f32_16x16x32_bf16 v[108:111], a[152:155], v[56:59], v[108:111]\n" \
"ds_read_b128 a[144:147], v5 offset:20480\n" \
"v_cvt_pk_bf16_f32 v140, v88, v89\n" \
"v_mfma_f32_16x16x32_bf16 v[108:111], a[156:159], v[60:63], v[108:111]\n" \
"s_add_u32 m0, s49, 32768\n" \
"v_exp_f32 v92, v92\n" \
"v_mfma_f32_16x16x32_bf16 v[116:119], a[152:155], a[72:75], v[116:119]\n" \
"buffer_load_dwordx4 v6, s[40:43], s16 offen offset:3072 sc1 lds\n" \
"v_mfma_f32_16x16x32_bf16 v[116:119], a[156:159], a[76:79], v[116:119]\n" \
"v_exp_f32 v93, v93\n" \
"v_mfma_f32_16x16x32_bf16 v[124:127], a[152:155], a[88:91], v[124:127]\n" \
"ds_read_b128 a[148:151], v5 offset:21504\n" \
"v_cvt_pk_bf16_f32 v141, v90, v91\n" \
"v_mfma_f32_16x16x32_bf16 v[124:127], a[156:159], a[92:95], v[124:127]\n" \
"s_waitcnt lgkmcnt(4)\n" \
"v_exp_f32 v94, v94\n" \
"v_mfma_f32_16x16x32_bf16 v[160:163], v[180:183], v[128:131], v[160:163]\n" \
"s_add_u32 m0, s79, 131968\n" \
"v_exp_f32 v95, v95\n" \
"v_mfma_f32_16x16x32_bf16 v[164:167], v[180:183], v[132:135], v[164:167]\n" \
"buffer_load_dwordx4 v12, s[28:31], s81 offen offset:128 lds\n" \
"v_cvt_pk_bf16_f32 v142, v92, v93\n" \
"v_cvt_pk_bf16_f32 v143, v94, v95\n" \
"v_mfma_f32_16x16x32_bf16 v[168:171], v[180:183], v[136:139], v[168:171]\n" \
"s_mov_b32 s81, 327680\n" \
"ds_read_b64_tr_b16 a[188:189], v15 offset:24576\n" \
"v_mfma_f32_16x16x32_bf16 v[172:175], v[180:183], v[140:143], v[172:175]\n" \
"ds_read_b64_tr_b16 a[190:191], v16 offset:24576\n" \
"v_mfma_f32_16x16x32_bf16 v[192:195], a[128:131], v[128:131], v[192:195]\n" \
"s_add_u32 m0, s79, 139264\n" \
"v_exp_f32 v96, v96\n" \
"v_mfma_f32_16x16x32_bf16 v[196:199], a[132:135], v[128:131], v[196:199]\n" \
"buffer_load_dwordx4 v12, s[28:31], s81 offen offset:0 lds\n" \
"v_mfma_f32_16x16x32_bf16 v[224:227], a[128:131], v[132:135], v[224:227]\n" \
"ds_read_b128 a[152:155], v5 offset:22528\n" \
"v_mfma_f32_16x16x32_bf16 v[228:231], a[132:135], v[132:135], v[228:231]\n" \
"v_exp_f32 v97, v97\n" \
"v_mfma_f32_16x16x32_bf16 a[0:3], a[128:131], v[136:139], a[0:3]\n" \
"v_exp_f32 v98, v98\n" \
"v_mfma_f32_16x16x32_bf16 a[4:7], a[132:135], v[136:139], a[4:7]\n" \
"s_waitcnt lgkmcnt(1)\n" \
"v_exp_f32 v99, v99\n" \
"v_mfma_f32_16x16x32_bf16 a[32:35], a[128:131], v[140:143], a[32:35]\n" \
"ds_read_b128 a[156:159], v5 offset:23552\n" \
"v_cvt_pk_bf16_f32 v144, v96, v97\n" \
"v_mfma_f32_16x16x32_bf16 a[36:39], a[132:135], v[140:143], a[36:39]\n" \
"s_waitcnt vmcnt(17) lgkmcnt(4)\n" \
"v_exp_f32 v100, v100\n" \
"v_mfma_f32_16x16x32_bf16 v[200:203], a[136:139], v[128:131], v[200:203]\n" \
"s_barrier\n" \
"v_exp_f32 v101, v101\n" \
"v_mfma_f32_16x16x32_bf16 v[204:207], a[140:143], v[128:131], v[204:207]\n" \
"s_add_u32 s17, s17, s19\n" \
"v_cvt_pk_bf16_f32 v145, v98, v99\n" \
"v_cvt_pk_bf16_f32 v146, v100, v101\n" \
"v_mfma_f32_16x16x32_bf16 v[232:235], a[136:139], v[132:135], v[232:235]\n" \
"ds_read_b128 a[128:131], v5 offset:32768\n" \
"v_mfma_f32_16x16x32_bf16 v[236:239], a[140:143], v[132:135], v[236:239]\n" \
"s_add_u32 m0, s49, 49152\n" \
"v_exp_f32 v102, v102\n" \
"v_mfma_f32_16x16x32_bf16 a[8:11], a[136:139], v[136:139], a[8:11]\n" \
"buffer_load_dwordx4 v6, s[40:43], s17 offen offset:0 sc1 lds\n" \
"v_mfma_f32_16x16x32_bf16 a[12:15], a[140:143], v[136:139], a[12:15]\n" \
"v_exp_f32 v103, v103\n" \
"v_mfma_f32_16x16x32_bf16 a[40:43], a[136:139], v[140:143], a[40:43]\n" \
"ds_read_b128 a[132:135], v5 offset:33792\n" \
"v_cvt_pk_bf16_f32 v147, v102, v103\n" \
"v_mfma_f32_16x16x32_bf16 a[44:47], a[140:143], v[140:143], a[44:47]\n" \
"s_waitcnt lgkmcnt(4)\n" \
"v_exp_f32 v104, v104\n" \
"v_mfma_f32_16x16x32_bf16 v[208:211], a[144:147], v[128:131], v[208:211]\n" \
"v_exp_f32 v105, v105\n" \
"v_mfma_f32_16x16x32_bf16 v[212:215], a[148:151], v[128:131], v[212:215]\n" \
"v_mfma_f32_16x16x32_bf16 v[240:243], a[144:147], v[132:135], v[240:243]\n" \
"ds_read_b128 a[136:139], v5 offset:34816\n" \
"v_cvt_pk_bf16_f32 v148, v104, v105\n" \
"v_mfma_f32_16x16x32_bf16 v[244:247], a[148:151], v[132:135], v[244:247]\n" \
"s_add_u32 m0, s49, 49152\n" \
"v_exp_f32 v106, v106\n" \
"v_mfma_f32_16x16x32_bf16 a[16:19], a[144:147], v[136:139], a[16:19]\n" \
"buffer_load_dwordx4 v6, s[40:43], s17 offen offset:1024 sc1 lds\n" \
"v_mfma_f32_16x16x32_bf16 a[20:23], a[148:151], v[136:139], a[20:23]\n" \
"v_exp_f32 v107, v107\n" \
"v_mfma_f32_16x16x32_bf16 a[48:51], a[144:147], v[140:143], a[48:51]\n" \
"ds_read_b128 a[140:143], v5 offset:35840\n" \
"v_cvt_pk_bf16_f32 v149, v106, v107\n" \
"v_mfma_f32_16x16x32_bf16 a[52:55], a[148:151], v[140:143], a[52:55]\n" \
"s_waitcnt lgkmcnt(4)\n" \
"s_add_u32 m0, s49, 49152\n" \
"v_mfma_f32_16x16x32_bf16 v[216:219], a[152:155], v[128:131], v[216:219]\n" \
"buffer_load_dwordx4 v6, s[40:43], s17 offen offset:2048 sc1 lds\n" \
"v_mfma_f32_16x16x32_bf16 v[220:223], a[156:159], v[128:131], v[220:223]\n" \
"v_exp_f32 v108, v108\n" \
"v_mfma_f32_16x16x32_bf16 v[248:251], a[152:155], v[132:135], v[248:251]\n" \
"ds_read_b128 a[144:147], v5 offset:36864\n" \
"v_mfma_f32_16x16x32_bf16 v[252:255], a[156:159], v[132:135], v[252:255]\n" \
"s_add_u32 m0, s49, 49152\n" \
"v_exp_f32 v109, v109\n" \
"v_mfma_f32_16x16x32_bf16 a[24:27], a[152:155], v[136:139], a[24:27]\n" \
"buffer_load_dwordx4 v6, s[40:43], s17 offen offset:3072 sc1 lds\n" \
"v_mfma_f32_16x16x32_bf16 a[28:31], a[156:159], v[136:139], a[28:31]\n" \
"v_exp_f32 v110, v110\n" \
"v_mfma_f32_16x16x32_bf16 a[56:59], a[152:155], v[140:143], a[56:59]\n" \
"ds_read_b128 a[148:151], v5 offset:37888\n" \
"v_exp_f32 v111, v111\n" \
"v_mfma_f32_16x16x32_bf16 a[60:63], a[156:159], v[140:143], a[60:63]\n" \
"s_waitcnt lgkmcnt(4)\n" \
"v_cvt_pk_bf16_f32 v150, v108, v109\n" \
"v_cvt_pk_bf16_f32 v151, v110, v111\n" \
"v_mfma_f32_16x16x32_bf16 v[64:67], a[128:131], v[32:35], 0\n" \
"buffer_load_dwordx2 v[18:19], v17, s[36:39], 0 offen sc1\n" \
"v_mfma_f32_16x16x32_bf16 v[64:67], a[132:135], v[36:39], v[64:67]\n" \
"s_add_u32 s16, s16, s9\n" \
"v_exp_f32 v112, v112\n" \
"v_mfma_f32_16x16x32_bf16 v[72:75], a[128:131], v[48:51], 0\n" \
"ds_read_b128 a[152:155], v5 offset:38912\n" \
"v_mfma_f32_16x16x32_bf16 v[72:75], a[132:135], v[52:55], v[72:75]\n" \
"s_add_u32 m0, s48, 0\n" \
"v_exp_f32 v113, v113\n" \
"v_mfma_f32_16x16x32_bf16 v[80:83], a[128:131], a[64:67], 0\n" \
"buffer_load_dwordx4 v6, s[40:43], s16 offen offset:0 sc1 lds\n" \
"v_mfma_f32_16x16x32_bf16 v[80:83], a[132:135], a[68:71], v[80:83]\n" \
"v_exp_f32 v114, v114\n" \
"v_mfma_f32_16x16x32_bf16 v[88:91], a[128:131], a[80:83], 0\n" \
"ds_read_b128 a[156:159], v5 offset:39936\n" \
"v_cvt_pk_bf16_f32 v152, v112, v113\n" \
"v_mfma_f32_16x16x32_bf16 v[88:91], a[132:135], a[84:87], v[88:91]\n" \
"s_waitcnt lgkmcnt(4)\n" \
"v_exp_f32 v115, v115\n" \
"s_add_u32 m0, s79, 140160\n" \
"v_mfma_f32_16x16x32_bf16 v[64:67], a[136:139], v[40:43], v[64:67]\n" \
"buffer_load_dwordx4 v12, s[28:31], s81 offen offset:128 lds\n" \
"v_mfma_f32_16x16x32_bf16 v[64:67], a[140:143], v[44:47], v[64:67]\n" \
"s_xor_b32 s49, s49, 0x10000\n" \
"s_mov_b32 s81, 393216\n" \
"v_cvt_pk_bf16_f32 v153, v114, v115\n" \
"v_mfma_f32_16x16x32_bf16 v[72:75], a[136:139], v[56:59], v[72:75]\n" \
"ds_read_b128 a[128:131], v5 offset:49152\n" \
"v_mfma_f32_16x16x32_bf16 v[72:75], a[140:143], v[60:63], v[72:75]\n" \
"s_add_u32 m0, s48, 0\n" \
"v_exp_f32 v116, v116\n" \
"v_mfma_f32_16x16x32_bf16 v[80:83], a[136:139], a[72:75], v[80:83]\n" \
"buffer_load_dwordx4 v6, s[40:43], s16 offen offset:1024 sc1 lds\n" \
"v_mfma_f32_16x16x32_bf16 v[80:83], a[140:143], a[76:79], v[80:83]\n" \
"v_exp_f32 v117, v117\n" \
"v_mfma_f32_16x16x32_bf16 v[88:91], a[136:139], a[88:91], v[88:91]\n" \
"ds_read_b128 a[132:135], v5 offset:50176\n" \
"v_cvt_pk_bf16_f32 v154, v116, v117\n" \
"v_mfma_f32_16x16x32_bf16 v[88:91], a[140:143], a[92:95], v[88:91]\n" \
"s_waitcnt lgkmcnt(4)\n" \
"v_exp_f32 v118, v118\n" \
"s_add_u32 m0, s79, 147456\n" \
"v_mfma_f32_16x16x32_bf16 v[68:71], a[144:147], v[32:35], 0\n" \
"buffer_load_dwordx4 v12, s[28:31], s81 offen offset:0 lds\n" \
"v_mfma_f32_16x16x32_bf16 v[68:71], a[148:151], v[36:39], v[68:71]\n" \
"v_exp_f32 v119, v119\n" \
"v_mfma_f32_16x16x32_bf16 v[76:79], a[144:147], v[48:51], 0\n" \
"ds_read_b128 a[136:139], v5 offset:51200\n" \
"v_cvt_pk_bf16_f32 v155, v118, v119\n" \
"v_mfma_f32_16x16x32_bf16 v[76:79], a[148:151], v[52:55], v[76:79]\n" \
"s_add_u32 m0, s48, 0\n" \
"v_exp_f32 v120, v120\n" \
"v_mfma_f32_16x16x32_bf16 v[84:87], a[144:147], a[64:67], 0\n" \
"buffer_load_dwordx4 v6, s[40:43], s16 offen offset:2048 sc1 lds\n" \
"v_mfma_f32_16x16x32_bf16 v[84:87], a[148:151], a[68:71], v[84:87]\n" \
"v_exp_f32 v121, v121\n" \
"v_mfma_f32_16x16x32_bf16 v[92:95], a[144:147], a[80:83], 0\n" \
"ds_read_b128 a[140:143], v5 offset:52224\n" \
"v_cvt_pk_bf16_f32 v156, v120, v121\n" \
"v_mfma_f32_16x16x32_bf16 v[92:95], a[148:151], a[84:87], v[92:95]\n" \
"s_waitcnt lgkmcnt(4)\n" \
"v_exp_f32 v122, v122\n" \
"s_add_u32 m0, s79, 148352\n" \
"v_mfma_f32_16x16x32_bf16 v[68:71], a[152:155], v[40:43], v[68:71]\n" \
"buffer_load_dwordx4 v12, s[28:31], s81 offen offset:128 lds\n" \
"v_mfma_f32_16x16x32_bf16 v[68:71], a[156:159], v[44:47], v[68:71]\n" \
"s_mov_b32 s81, 458752\n" \
"v_exp_f32 v123, v123\n" \
"v_mfma_f32_16x16x32_bf16 v[76:79], a[152:155], v[56:59], v[76:79]\n" \
"ds_read_b128 a[144:147], v5 offset:53248\n" \
"v_cvt_pk_bf16_f32 v157, v122, v123\n" \
"v_mfma_f32_16x16x32_bf16 v[76:79], a[156:159], v[60:63], v[76:79]\n" \
"s_add_u32 m0, s48, 0\n" \
"v_exp_f32 v124, v124\n" \
"v_mfma_f32_16x16x32_bf16 v[84:87], a[152:155], a[72:75], v[84:87]\n" \
"buffer_load_dwordx4 v6, s[40:43], s16 offen offset:3072 sc1 lds\n" \
"v_mfma_f32_16x16x32_bf16 v[84:87], a[156:159], a[76:79], v[84:87]\n" \
"v_exp_f32 v125, v125\n" \
"v_mfma_f32_16x16x32_bf16 v[92:95], a[152:155], a[88:91], v[92:95]\n" \
"ds_read_b128 a[148:151], v5 offset:54272\n" \
"v_cvt_pk_bf16_f32 v158, v124, v125\n" \
"v_mfma_f32_16x16x32_bf16 v[92:95], a[156:159], a[92:95], v[92:95]\n" \
"s_waitcnt lgkmcnt(4)\n" \
"v_exp_f32 v126, v126\n" \
"v_mfma_f32_16x16x32_bf16 v[160:163], v[180:183], v[144:147], v[160:163]\n" \
"s_add_u32 m0, s79, 155648\n" \
"v_exp_f32 v127, v127\n" \
"v_mfma_f32_16x16x32_bf16 v[164:167], v[180:183], v[148:151], v[164:167]\n" \
"buffer_load_dwordx4 v12, s[28:31], s81 offen offset:0 lds\n" \
"v_cvt_pk_bf16_f32 v159, v126, v127\n" \
"v_mfma_f32_16x16x32_bf16 v[168:171], v[180:183], v[152:155], v[168:171]\n" \
"v_exp_f32 v64, v64\n" \
"v_mfma_f32_16x16x32_bf16 v[172:175], v[180:183], v[156:159], v[172:175]\n" \
"buffer_store_dwordx4 v[28:31], v4, s[36:39], s80 offen offset:1024 sc1\n" \
"v_mfma_f32_16x16x32_bf16 v[192:195], a[128:131], v[144:147], v[192:195]\n" \
"s_add_u32 m0, s79, 156544\n" \
"v_exp_f32 v65, v65\n" \
"v_mfma_f32_16x16x32_bf16 v[196:199], a[132:135], v[144:147], v[196:199]\n" \
"buffer_load_dwordx4 v12, s[28:31], s81 offen offset:128 lds\n" \
"v_mfma_f32_16x16x32_bf16 v[224:227], a[128:131], v[148:151], v[224:227]\n" \
"s_mov_b32 s81, 524288\n" \
"ds_read_b128 a[152:155], v5 offset:55296\n" \
"v_cvt_pk_bf16_f32 v128, v64, v65\n" \
"v_mfma_f32_16x16x32_bf16 v[228:231], a[132:135], v[148:151], v[228:231]\n" \
"buffer_store_dwordx4 v[28:31], v4, s[36:39], s80 offen offset:2048 sc1\n" \
"v_mfma_f32_16x16x32_bf16 a[0:3], a[128:131], v[152:155], a[0:3]\n" \
"v_exp_f32 v66, v66\n" \
"v_mfma_f32_16x16x32_bf16 a[4:7], a[132:135], v[152:155], a[4:7]\n" \
"v_exp_f32 v67, v67\n" \
"v_mfma_f32_16x16x32_bf16 a[32:35], a[128:131], v[156:159], a[32:35]\n" \
"ds_read_b128 a[156:159], v5 offset:56320\n" \
"v_cvt_pk_bf16_f32 v129, v66, v67\n" \
"v_mfma_f32_16x16x32_bf16 a[36:39], a[132:135], v[156:159], a[36:39]\n" \
"s_waitcnt vmcnt(9) lgkmcnt(4)\n" \
"v_exp_f32 v68, v68\n" \
"v_mfma_f32_16x16x32_bf16 v[200:203], a[136:139], v[144:147], v[200:203]\n" \
"s_barrier\n" \
"v_xor_b32 v5, 0x10000, v5\n" \
"v_mfma_f32_16x16x32_bf16 v[204:207], a[140:143], v[144:147], v[204:207]\n" \
"s_add_u32 s17, s17, s19\n" \
"v_exp_f32 v69, v69\n" \
"v_mfma_f32_16x16x32_bf16 v[232:235], a[136:139], v[148:151], v[232:235]\n" \
"ds_read_b128 a[128:131], v5 offset:0\n" \
"v_cvt_pk_bf16_f32 v130, v68, v69\n" \
"v_mfma_f32_16x16x32_bf16 v[236:239], a[140:143], v[148:151], v[236:239]\n" \
"s_add_u32 m0, s48, 16384\n" \
"v_exp_f32 v70, v70\n" \
"v_mfma_f32_16x16x32_bf16 a[8:11], a[136:139], v[152:155], a[8:11]\n" \
"LH18_%=:\ns_cbranch_execz LH22_%=\n" \
"buffer_load_dwordx4 v6, s[40:43], s17 offen offset:0 sc1 lds\n" \
"v_mfma_f32_16x16x32_bf16 a[12:15], a[140:143], v[152:155], a[12:15]\n" \
"v_exp_f32 v71, v71\n" \
"v_mfma_f32_16x16x32_bf16 a[40:43], a[136:139], v[156:159], a[40:43]\n" \
"ds_read_b128 a[132:135], v5 offset:1024\n" \
"v_cvt_pk_bf16_f32 v131, v70, v71\n" \
"v_mfma_f32_16x16x32_bf16 a[44:47], a[140:143], v[156:159], a[44:47]\n" \
"s_waitcnt lgkmcnt(4)\n" \
"v_exp_f32 v72, v72\n" \
"v_mfma_f32_16x16x32_bf16 v[208:211], a[144:147], v[144:147], v[208:211]\n" \
"v_exp_f32 v73, v73\n" \
"v_mfma_f32_16x16x32_bf16 v[212:215], a[148:151], v[144:147], v[212:215]\n" \
"v_mfma_f32_16x16x32_bf16 v[240:243], a[144:147], v[148:151], v[240:243]\n" \
"ds_read_b128 a[136:139], v5 offset:2048\n" \
"v_cvt_pk_bf16_f32 v132, v72, v73\n" \
"v_mfma_f32_16x16x32_bf16 v[244:247], a[148:151], v[148:151], v[244:247]\n" \
"s_add_u32 m0, s48, 16384\n" \
"v_exp_f32 v74, v74\n" \
"v_mfma_f32_16x16x32_bf16 a[16:19], a[144:147], v[152:155], a[16:19]\n" \
"buffer_load_dwordx4 v6, s[40:43], s17 offen offset:1024 sc1 lds\n" \
"v_mfma_f32_16x16x32_bf16 a[20:23], a[148:151], v[152:155], a[20:23]\n" \
"v_exp_f32 v75, v75\n" \
"v_mfma_f32_16x16x32_bf16 a[48:51], a[144:147], v[156:159], a[48:51]\n" \
"ds_read_b128 a[140:143], v5 offset:3072\n" \
"v_cvt_pk_bf16_f32 v133, v74, v75\n" \
"v_mfma_f32_16x16x32_bf16 a[52:55], a[148:151], v[156:159], a[52:55]\n" \
"s_waitcnt lgkmcnt(4)\n" \
"s_add_u32 m0, s48, 16384\n" \
"ds_read_b64_tr_b16 a[192:193], v13 offset:0\n" \
"v_mfma_f32_16x16x32_bf16 v[216:219], a[152:155], v[144:147], v[216:219]\n" \
"buffer_load_dwordx4 v6, s[40:43], s17 offen offset:2048 sc1 lds\n" \
"v_mfma_f32_16x16x32_bf16 v[220:223], a[156:159], v[144:147], v[220:223]\n" \
"s_add_u32 s44, s44, 1\n" \
"v_exp_f32 v76, v76\n" \
"v_mfma_f32_16x16x32_bf16 v[248:251], a[152:155], v[148:151], v[248:251]\n" \
"ds_read_b128 a[144:147], v5 offset:4096\n" \
"v_mfma_f32_16x16x32_bf16 v[252:255], a[156:159], v[148:151], v[252:255]\n" \
"s_add_u32 m0, s48, 16384\n" \
"v_exp_f32 v77, v77\n" \
"v_mfma_f32_16x16x32_bf16 a[24:27], a[152:155], v[152:155], a[24:27]\n" \
"buffer_load_dwordx4 v6, s[40:43], s17 offen offset:3072 sc1 lds\n" \
"v_mfma_f32_16x16x32_bf16 a[28:31], a[156:159], v[152:155], a[28:31]\n" \
"s_xor_b32 s48, s48, 0x10000\n" \
"v_exp_f32 v78, v78\n" \
"v_mfma_f32_16x16x32_bf16 a[56:59], a[152:155], v[156:159], a[56:59]\n" \
"ds_read_b128 a[148:151], v5 offset:5120\n" \
"v_exp_f32 v79, v79\n" \
"v_mfma_f32_16x16x32_bf16 a[60:63], a[156:159], v[156:159], a[60:63]\n" \
"s_waitcnt lgkmcnt(4)\n" \
"ds_read_b64_tr_b16 a[194:195], v14 offset:0\n" \
"v_cvt_pk_bf16_f32 v134, v76, v77\n" \
"v_cvt_pk_bf16_f32 v135, v78, v79\n" \
"v_mfma_f32_16x16x32_bf16 v[96:99], a[128:131], v[32:35], 0\n" \
"s_add_u32 s100, s100, 262144\ns_addc_u32 s101, s101, 0\n" \
"ds_read_b64_tr_b16 a[196:197], v15 offset:0\n" \
"v_mfma_f32_16x16x32_bf16 v[96:99], a[132:135], v[36:39], v[96:99]\n" \
"s_add_u32 s16, s16, s9\n" \
"ds_read_b64_tr_b16 a[198:199], v16 offset:0\n" \
"v_mfma_f32_16x16x32_bf16 v[104:107], a[128:131], v[48:51], 0\n" \
"ds_read_b128 a[152:155], v5 offset:6144\n" \
"v_mfma_f32_16x16x32_bf16 v[104:107], a[132:135], v[52:55], v[104:107]\n" \
"s_add_u32 m0, s49, 32768\n" \
"v_exp_f32 v80, v80\n" \
"v_mfma_f32_16x16x32_bf16 v[112:115], a[128:131], a[64:67], 0\n" \
"buffer_load_dwordx4 v6, s[40:43], s16 offen offset:0 sc1 lds\n" \
"v_mfma_f32_16x16x32_bf16 v[112:115], a[132:135], a[68:71], v[112:115]\n" \
"ds_read_b64_tr_b16 a[200:201], v13 offset:8192\n" \
"v_mfma_f32_16x16x32_bf16 v[120:123], a[128:131], a[80:83], 0\n" \
"ds_read_b128 a[156:159], v5 offset:7168\n" \
"v_mfma_f32_16x16x32_bf16 v[120:123], a[132:135], a[84:87], v[120:123]\n" \
"s_waitcnt lgkmcnt(4)\n" \
"v_exp_f32 v81, v81\n" \
"v_mfma_f32_16x16x32_bf16 v[96:99], a[136:139], v[40:43], v[96:99]\n" \
"s_mov_b64 exec, 0xffff\nglobal_load_dword v1, v2, s[100:101]\ns_mov_b64 exec, -1\n" \
"v_mfma_f32_16x16x32_bf16 v[96:99], a[140:143], v[44:47], v[96:99]\n" \
"v_exp_f32 v82, v82\n" \
"v_mfma_f32_16x16x32_bf16 v[104:107], a[136:139], v[56:59], v[104:107]\n" \
"ds_read_b128 a[128:131], v5 offset:16384\n" \
"v_cvt_pk_bf16_f32 v136, v80, v81\n" \
"v_mfma_f32_16x16x32_bf16 v[104:107], a[140:143], v[60:63], v[104:107]\n" \
"s_add_u32 m0, s49, 32768\n" \
"v_exp_f32 v83, v83\n" \
"v_mfma_f32_16x16x32_bf16 v[112:115], a[136:139], a[72:75], v[112:115]\n" \
"buffer_load_dwordx4 v6, s[40:43], s16 offen offset:1024 sc1 lds\n" \
"v_mfma_f32_16x16x32_bf16 v[112:115], a[140:143], a[76:79], v[112:115]\n" \
"ds_read_b64_tr_b16 a[202:203], v14 offset:8192\n" \
"v_cvt_pk_bf16_f32 v137, v82, v83\n" \
"v_mfma_f32_16x16x32_bf16 v[120:123], a[136:139], a[88:91], v[120:123]\n" \
"ds_read_b128 a[132:135], v5 offset:17408\n" \
"v_mfma_f32_16x16x32_bf16 v[120:123], a[140:143], a[92:95], v[120:123]\n" \
"s_waitcnt lgkmcnt(4)\n" \
"v_exp_f32 v84, v84\n" \
"v_mfma_f32_16x16x32_bf16 v[100:103], a[144:147], v[32:35], 0\n" \
"v_exp_f32 v85, v85\n" \
"v_mfma_f32_16x16x32_bf16 v[100:103], a[148:151], v[36:39], v[100:103]\n" \
"v_exp_f32 v86, v86\n" \
"v_mfma_f32_16x16x32_bf16 v[108:111], a[144:147], v[48:51], 0\n" \
"ds_read_b128 a[136:139], v5 offset:18432\n" \
"v_cvt_pk_bf16_f32 v138, v84, v85\n" \
"v_mfma_f32_16x16x32_bf16 v[108:111], a[148:151], v[52:55], v[108:111]\n" \
"s_add_u32 m0, s49, 32768\n" \
"v_exp_f32 v87, v87\n" \
"v_mfma_f32_16x16x32_bf16 v[116:119], a[144:147], a[64:67], 0\n" \
"buffer_load_dwordx4 v6, s[40:43], s16 offen offset:2048 sc1 lds\n" \
"v_mfma_f32_16x16x32_bf16 v[116:119], a[148:151], a[68:71], v[116:119]\n" \
"v_exp_f32 v88, v88\n" \
"v_mfma_f32_16x16x32_bf16 v[124:127], a[144:147], a[80:83], 0\n" \
"ds_read_b128 a[140:143], v5 offset:19456\n" \
"v_cvt_pk_bf16_f32 v139, v86, v87\n" \
"v_mfma_f32_16x16x32_bf16 v[124:127], a[148:151], a[84:87], v[124:127]\n" \
"s_waitcnt lgkmcnt(4)\n" \
"v_exp_f32 v89, v89\n" \
"v_mfma_f32_16x16x32_bf16 v[100:103], a[152:155], v[40:43], v[100:103]\n" \
"v_exp_f32 v90, v90\n" \
"v_mfma_f32_16x16x32_bf16 v[100:103], a[156:159], v[44:47], v[100:103]\n" \
"v_exp_f32 v91, v91\n" \
"v_mfma_f32_16x16x32_bf16 v[108:111], a[152:155], v[56:59], v[108:111]\n" \
"ds_read_b128 a[144:147], v5 offset:20480\n" \
"v_cvt_pk_bf16_f32 v140, v88, v89\n" \
"v_mfma_f32_16x16x32_bf16 v[108:111], a[156:159], v[60:63], v[108:111]\n" \
"s_add_u32 m0, s49, 32768\n" \
"v_exp_f32 v92, v92\n" \
"v_mfma_f32_16x16x32_bf16 v[116:119], a[152:155], a[72:75], v[116:119]\n" \
"buffer_load_dwordx4 v6, s[40:43], s16 offen offset:3072 sc1 lds\n" \
"v_mfma_f32_16x16x32_bf16 v[116:119], a[156:159], a[76:79], v[116:119]\n" \
"v_exp_f32 v93, v93\n" \
"v_mfma_f32_16x16x32_bf16 v[124:127], a[152:155], a[88:91], v[124:127]\n" \
"ds_read_b128 a[148:151], v5 offset:21504\n" \
"v_cvt_pk_bf16_f32 v141, v90, v91\n" \
"v_mfma_f32_16x16x32_bf16 v[124:127], a[156:159], a[92:95], v[124:127]\n" \
"s_waitcnt lgkmcnt(4)\n" \
"v_exp_f32 v94, v94\n" \
"v_mfma_f32_16x16x32_bf16 v[160:163], v[180:183], v[128:131], v[160:163]\n" \
"s_waitcnt vmcnt(20)\n" \
"v_exp_f32 v95, v95\n" \
"v_mfma_f32_16x16x32_bf16 v[164:167], v[180:183], v[132:135], v[164:167]\n" \
"ds_read_b64_tr_b16 a[204:205], v15 offset:8192\n" \
"v_cmp_ne_u64 vcc, s[14:15], v[18:19]\n" \
"v_cvt_pk_bf16_f32 v142, v92, v93\n" \
"v_cvt_pk_bf16_f32 v143, v94, v95\n" \
"v_mfma_f32_16x16x32_bf16 v[168:171], v[180:183], v[136:139], v[168:171]\n" \
"ds_read_b64_tr_b16 a[206:207], v16 offset:8192\n" \
"s_nop 0\n" \
"v_mfma_f32_16x16x32_bf16 v[172:175], v[180:183], v[140:143], v[172:175]\n" \
"s_cbranch_vccnz LPZ_%=\nLPZR_%=:\n" \
"v_exp_f32 v96, v96\n" \
"v_mfma_f32_16x16x32_bf16 v[192:195], a[128:131], v[128:131], v[192:195]\n" \
"v_exp_f32 v97, v97\n" \
"v_mfma_f32_16x16x32_bf16 v[196:199], a[132:135], v[128:131], v[196:199]\n" \
"v_cvt_pk_bf16_f32 v144, v96, v97\n" \
"v_mfma_f32_16x16x32_bf16 v[224:227], a[128:131], v[132:135], v[224:227]\n" \
"ds_read_b128 a[152:155], v5 offset:22528\n" \
"v_mfma_f32_16x16x32_bf16 v[228:231], a[132:135], v[132:135], v[228:231]\n" \
"v_exp_f32 v98, v98\n" \
"v_mfma_f32_16x16x32_bf16 a[0:3], a[128:131], v[136:139], a[0:3]\n" \
"v_exp_f32 v99, v99\n" \
"v_mfma_f32_16x16x32_bf16 a[4:7], a[132:135], v[136:139], a[4:7]\n" \
"s_waitcnt lgkmcnt(1)\n" \
"v_cvt_pk_bf16_f32 v145, v98, v99\n" \
"v_mfma_f32_16x16x32_bf16 a[32:35], a[128:131], v[140:143], a[32:35]\n" \
"ds_read_b128 a[156:159], v5 offset:23552\n" \
"v_mfma_f32_16x16x32_bf16 a[36:39], a[132:135], v[140:143], a[36:39]\n" \
"s_waitcnt vmcnt(10) lgkmcnt(4)\n" \
"v_exp_f32 v100, v100\n" \
"v_mfma_f32_16x16x32_bf16 v[200:203], a[136:139], v[128:131], v[200:203]\n" \
"s_barrier\n" \
"v_exp_f32 v101, v101\n" \
"v_mfma_f32_16x16x32_bf16 v[204:207], a[140:143], v[128:131], v[204:207]\n" \
"s_add_u32 s17, s17, s19\n" \
"ds_read_b64_tr_b16 a[208:209], v13 offset:16384\n" \
"v_cvt_pk_bf16_f32 v146, v100, v101\n" \
"v_mfma_f32_16x16x32_bf16 v[232:235], a[136:139], v[132:135], v[232:235]\n" \
"ds_read_b128 a[128:131], v5 offset:32768\n" \
"v_mfma_f32_16x16x32_bf16 v[236:239], a[140:143], v[132:135], v[236:239]\n" \
"s_add_u32 m0, s49, 49152\n" \
"v_exp_f32 v102, v102\n" \
"v_mfma_f32_16x16x32_bf16 a[8:11], a[136:139], v[136:139], a[8:11]\n" \
"buffer_load_dwordx4 v6, s[40:43], s17 offen offset:0 sc1 lds\n" \
"v_mfma_f32_16x16x32_bf16 a[12:15], a[140:143], v[136:139], a[12:15]\n" \
"v_exp_f32 v103, v103\n" \
"v_mfma_f32_16x16x32_bf16 a[40:43], a[136:139], v[140:143], a[40:43]\n" \
"ds_read_b128 a[132:135], v5 offset:33792\n" \
"v_cvt_pk_bf16_f32 v147, v102, v103\n" \
"v_mfma_f32_16x16x32_bf16 a[44:47], a[140:143], v[140:143], a[44:47]\n" \
"s_waitcnt lgkmcnt(4)\n" \
"v_exp_f32 v104, v104\n" \
"v_mfma_f32_16x16x32_bf16 v[208:211], a[144:147], v[128:131], v[208:211]\n" \
"v_exp_f32 v105, v105\n" \
"v_mfma_f32_16x16x32_bf16 v[212:215], a[148:151], v[128:131], v[212:215]\n" \
"v_mfma_f32_16x16x32_bf16 v[240:243], a[144:147], v[132:135], v[240:243]\n" \
"ds_read_b128 a[136:139], v5 offset:34816\n" \
"v_cvt_pk_bf16_f32 v148, v104, v105\n" \
"v_mfma_f32_16x16x32_bf16 v[244:247], a[148:151], v[132:135], v[244:247]\n" \
"s_add_u32 m0, s49, 49152\n" \
"v_exp_f32 v106, v106\n" \
"v_mfma_f32_16x16x32_bf16 a[16:19], a[144:147], v[136:139], a[16:19]\n" \
"buffer_load_dwordx4 v6, s[40:43], s17 offen offset:1024 sc1 lds\n" \
"v_mfma_f32_16x16x32_bf16 a[20:23], a[148:151], v[136:139], a[20:23]\n" \
"v_exp_f32 v107, v107\n" \
"v_mfma_f32_16x16x32_bf16 a[48:51], a[144:147], v[140:143], a[48:51]\n" \
"ds_read_b128 a[140:143], v5 offset:35840\n" \
"v_cvt_pk_bf16_f32 v149, v106, v107\n" \
"v_mfma_f32_16x16x32_bf16 a[52:55], a[148:151], v[140:143], a[52:55]\n" \
"s_waitcnt lgkmcnt(4)\n" \
"s_add_u32 m0, s49, 49152\n" \
"ds_read_b64_tr_b16 a[210:211], v14 offset:16384\n" \
"v_mfma_f32_16x16x32_bf16 v[216:219], a[152:155], v[128:131], v[216:219]\n" \
"buffer_load_dwordx4 v6, s[40:43], s17 offen offset:2048 sc1 lds\n" \
"v_mfma_f32_16x16x32_bf16 v[220:223], a[156:159], v[128:131], v[220:223]\n" \
"v_exp_f32 v108, v108\n" \
"v_mfma_f32_16x16x32_bf16 v[248:251], a[152:155], v[132:135], v[248:251]\n" \
"ds_read_b128 a[144:147], v5 offset:36864\n" \
"v_mfma_f32_16x16x32_bf16 v[252:255], a[156:159], v[132:135], v[252:255]\n" \
"s_add_u32 m0, s49, 49152\n" \
"v_exp_f32 v109, v109\n" \
"v_mfma_f32_16x16x32_bf16 a[24:27], a[152:155], v[136:139], a[24:27]\n" \
"buffer_load_dwordx4 v6, s[40:43], s17 offen offset:3072 sc1 lds\n" \
"v_mfma_f32_16x16x32_bf16 a[28:31], a[156:159], v[136:139], a[28:31]\n" \
"v_exp_f32 v110, v110\n" \
"v_mfma_f32_16x16x32_bf16 a[56:59], a[152:155], v[140:143], a[56:59]\n" \
"ds_read_b128 a[148:151], v5 offset:37888\n" \
"v_exp_f32 v111, v111\n" \
"v_mfma_f32_16x16x32_bf16 a[60:63], a[156:159], v[140:143], a[60:63]\n" \
"s_waitcnt lgkmcnt(4)\n" \
"ds_read_b64_tr_b16 a[212:213], v15 offset:16384\n" \
"v_cvt_pk_bf16_f32 v150, v108, v109\n" \
"v_cvt_pk_bf16_f32 v151, v110, v111\n" \
"v_mfma_f32_16x16x32_bf16 v[64:67], a[128:131], v[32:35], 0\n" \
"ds_read_b64_tr_b16 a[214:215], v16 offset:16384\n" \
"v_mfma_f32_16x16x32_bf16 v[64:67], a[132:135], v[36:39], v[64:67]\n" \
"s_add_u32 s16, s16, s9\n" \
"v_exp_f32 v112, v112\n" \
"v_mfma_f32_16x16x32_bf16 v[72:75], a[128:131], v[48:51], 0\n" \
"ds_read_b128 a[152:155], v5 offset:38912\n" \
"v_mfma_f32_16x16x32_bf16 v[72:75], a[132:135], v[52:55], v[72:75]\n" \
"s_add_u32 m0, s48, 0\n" \
"v_exp_f32 v113, v113\n" \
"v_mfma_f32_16x16x32_bf16 v[80:83], a[128:131], a[64:67], 0\n" \
"buffer_load_dwordx4 v6, s[40:43], s16 offen offset:0 sc1 lds\n" \
"v_mfma_f32_16x16x32_bf16 v[80:83], a[132:135], a[68:71], v[80:83]\n" \
"ds_read_b64_tr_b16 a[216:217], v13 offset:24576\n" \
"v_cvt_pk_bf16_f32 v152, v112, v113\n" \
"v_mfma_f32_16x16x32_bf16 v[88:91], a[128:131], a[80:83], 0\n" \
"ds_read_b128 a[156:159], v5 offset:39936\n" \
"v_mfma_f32_16x16x32_bf16 v[88:91], a[132:135], a[84:87], v[88:91]\n" \
"s_waitcnt lgkmcnt(4)\n" \
"v_exp_f32 v114, v114\n" \
"s_add_u32 m0, s79, 131072\n" \
"v_mfma_f32_16x16x32_bf16 v[64:67], a[136:139], v[40:43], v[64:67]\n" \
"buffer_load_dwordx4 v12, s[28:31], s81 offen offset:0 lds\n" \
"v_mfma_f32_16x16x32_bf16 v[64:67], a[140:143], v[44:47], v[64:67]\n" \
"s_xor_b32 s49, s49, 0x10000\n" \
"v_exp_f32 v115, v115\n" \
"v_mfma_f32_16x16x32_bf16 v[72:75], a[136:139], v[56:59], v[72:75]\n" \
"ds_read_b128 a[128:131], v5 offset:49152\n" \
"v_cvt_pk_bf16_f32 v153, v114, v115\n" \
"v_mfma_f32_16x16x32_bf16 v[72:75], a[140:143], v[60:63], v[72:75]\n" \
"s_add_u32 m0, s48, 0\n" \
"v_exp_f32 v116, v116\n" \
"v_mfma_f32_16x16x32_bf16 v[80:83], a[136:139], a[72:75], v[80:83]\n" \
"buffer_load_dwordx4 v6, s[40:43], s16 offen offset:1024 sc1 lds\n" \
"v_mfma_f32_16x16x32_bf16 v[80:83], a[140:143], a[76:79], v[80:83]\n" \
"ds_read_b64_tr_b16 a[218:219], v14 offset:24576\n" \
"v_mfma_f32_16x16x32_bf16 v[88:91], a[136:139], a[88:91], v[88:91]\n" \
"ds_read_b128 a[132:135], v5 offset:50176\n" \
"v_mfma_f32_16x16x32_bf16 v[88:91], a[140:143], a[92:95], v[88:91]\n" \
"s_waitcnt lgkmcnt(4)\n" \
"v_exp_f32 v117, v117\n" \
"s_add_u32 m0, s79, 131968\n" \
"v_mfma_f32_16x16x32_bf16 v[68:71], a[144:147], v[32:35], 0\n" \
"buffer_load_dwordx4 v12, s[28:31], s81 offen offset:128 lds\n" \
"v_mfma_f32_16x16x32_bf16 v[68:71], a[148:151], v[36:39], v[68:71]\n" \
"s_mov_b32 s81, 589824\n" \
"v_exp_f32 v118, v118\n" \
"v_mfma_f32_16x16x32_bf16 v[76:79], a[144:147], v[48:51], 0\n" \
"ds_read_b128 a[136:139], v5 offset:51200\n" \
"v_cvt_pk_bf16_f32 v154, v116, v117\n" \
"v_mfma_f32_16x16x32_bf16 v[76:79], a[148:151], v[52:55], v[76:79]\n" \
"s_add_u32 m0, s48, 0\n" \
"v_exp_f32 v119, v119\n" \
"v_mfma_f32_16x16x32_bf16 v[84:87], a[144:147], a[64:67], 0\n" \
"buffer_load_dwordx4 v6, s[40:43], s16 offen offset:2048 sc1 lds\n" \
"v_mfma_f32_16x16x32_bf16 v[84:87], a[148:151], a[68:71], v[84:87]\n" \
"v_exp_f32 v120, v120\n" \
"v_mfma_f32_16x16x32_bf16 v[92:95], a[144:147], a[80:83], 0\n" \
"ds_read_b128 a[140:143], v5 offset:52224\n" \
"v_cvt_pk_bf16_f32 v155, v118, v119\n" \
"v_mfma_f32_16x16x32_bf16 v[92:95], a[148:151], a[84:87], v[92:95]\n" \
"s_waitcnt lgkmcnt(4)\n" \
"v_exp_f32 v121, v121\n" \
"v_mfma_f32_16x16x32_bf16 v[68:71], a[152:155], v[40:43], v[68:71]\n" \
"v_exp_f32 v122, v122\n" \
"v_mfma_f32_16x16x32_bf16 v[68:71], a[156:159], v[44:47], v[68:71]\n" \
"v_exp_f32 v123, v123\n" \
"v_mfma_f32_16x16x32_bf16 v[76:79], a[152:155], v[56:59], v[76:79]\n" \
"ds_read_b128 a[144:147], v5 offset:53248\n" \
"v_cvt_pk_bf16_f32 v156, v120, v121\n" \
"v_mfma_f32_16x16x32_bf16 v[76:79], a[156:159], v[60:63], v[76:79]\n" \
"s_add_u32 m0, s48, 0\n" \
"v_exp_f32 v124, v124\n" \
"v_mfma_f32_16x16x32_bf16 v[84:87], a[152:155], a[72:75], v[84:87]\n" \
"buffer_load_dwordx4 v6, s[40:43], s16 offen offset:3072 sc1 lds\n" \
"v_mfma_f32_16x16x32_bf16 v[84:87], a[156:159], a[76:79], v[84:87]\n" \
"v_exp_f32 v125, v125\n" \
"v_mfma_f32_16x16x32_bf16 v[92:95], a[152:155], a[88:91], v[92:95]\n" \
"ds_read_b128 a[148:151], v5 offset:54272\n" \
"v_cvt_pk_bf16_f32 v157, v122, v123\n" \
"v_mfma_f32_16x16x32_bf16 v[92:95], a[156:159], a[92:95], v[92:95]\n" \
"s_waitcnt lgkmcnt(4)\n" \
"v_exp_f32 v126, v126\n" \
"v_mfma_f32_16x16x32_bf16 v[160:163], v[180:183], v[144:147], v[160:163]\n" \
"s_add_u32 m0, s79, 139264\n" \
"v_exp_f32 v127, v127\n" \
"v_mfma_f32_16x16x32_bf16 v[164:167], v[180:183], v[148:151], v[164:167]\n" \
"buffer_load_dwordx4 v12, s[28:31], s81 offen offset:0 lds\n" \
"v_cvt_pk_bf16_f32 v158, v124, v125\n" \
"v_cvt_pk_bf16_f32 v159, v126, v127\n" \
"v_mfma_f32_16x16x32_bf16 v[168:171], v[180:183], v[152:155], v[168:171]\n" \
"ds_read_b64_tr_b16 a[220:221], v15 offset:24576\n" \
"v_mfma_f32_16x16x32_bf16 v[172:175], v[180:183], v[156:159], v[172:175]\n" \
"ds_read_b64_tr_b16 a[222:223], v16 offset:24576\n" \
"v_mfma_f32_16x16x32_bf16 v[192:195], a[128:131], v[144:147], v[192:195]\n" \
"s_add_u32 m0, s79, 140160\n" \
"v_exp_f32 v64, v64\n" \
"v_mfma_f32_16x16x32_bf16 v[196:199], a[132:135], v[144:147], v[196:199]\n" \
"buffer_load_dwordx4 v12, s[28:31], s81 offen offset:128 lds\n" \
"v_mfma_f32_16x16x32_bf16 v[224:227], a[128:131], v[148:151], v[224:227]\n" \
"s_mov_b32 s81, 655360\n" \
"ds_read_b128 a[152:155], v5 offset:55296\n" \
"v_mfma_f32_16x16x32_bf16 v[228:231], a[132:135], v[148:151], v[228:231]\n" \
"v_exp_f32 v65, v65\n" \
"v_mfma_f32_16x16x32_bf16 a[0:3], a[128:131], v[152:155], a[0:3]\n" \
"v_exp_f32 v66, v66\n" \
"v_mfma_f32_16x16x32_bf16 a[4:7], a[132:135], v[152:155], a[4:7]\n" \
"s_waitcnt lgkmcnt(1)\n" \
"v_exp_f32 v67, v67\n" \
"v_mfma_f32_16x16x32_bf16 a[32:35], a[128:131], v[156:159], a[32:35]\n" \
"ds_read_b128 a[156:159], v5 offset:56320\n" \
"v_cvt_pk_bf16_f32 v128, v64, v65\n" \
"v_mfma_f32_16x16x32_bf16 a[36:39], a[132:135], v[156:159], a[36:39]\n" \
"s_waitcnt vmcnt(17) lgkmcnt(4)\n" \
"v_exp_f32 v68, v68\n" \
"v_mfma_f32_16x16x32_bf16 v[200:203], a[136:139], v[144:147], v[200:203]\n" \
"s_barrier\n" \
"v_xor_b32 v5, 0x10000, v5\n" \
"v_cvt_pk_bf16_f32 v129, v66, v67\n" \
"v_mfma_f32_16x16x32_bf16 v[204:207], a[140:143], v[144:147], v[204:207]\n" \
"s_add_u32 s17, s17, s19\n" \
"v_exp_f32 v69, v69\n" \
"v_mfma_f32_16x16x32_bf16 v[232:235], a[136:139], v[148:151], v[232:235]\n" \
"ds_read_b128 a[128:131], v5 offset:0\n" \
"v_cvt_pk_bf16_f32 v130, v68, v69\n" \
"v_mfma_f32_16x16x32_bf16 v[236:239], a[140:143], v[148:151], v[236:239]\n" \
"s_add_u32 m0, s48, 16384\n" \
"v_exp_f32 v70, v70\n" \
"v_mfma_f32_16x16x32_bf16 a[8:11], a[136:139], v[152:155], a[8:11]\n" \
"buffer_load_dwordx4 v6, s[40:43], s17 offen offset:0 sc1 lds\n" \
"v_mfma_f32_16x16x32_bf16 a[12:15], a[140:143], v[152:155], a[12:15]\n" \
"v_exp_f32 v71, v71\n" \
"v_mfma_f32_16x16x32_bf16 a[40:43], a[136:139], v[156:159], a[40:43]\n" \
"ds_read_b128 a[132:135], v5 offset:1024\n" \
"v_cvt_pk_bf16_f32 v131, v70, v71\n" \
"v_mfma_f32_16x16x32_bf16 a[44:47], a[140:143], v[156:159], a[44:47]\n" \
"s_waitcnt lgkmcnt(4)\n" \
"v_exp_f32 v72, v72\n" \
"v_mfma_f32_16x16x32_bf16 v[208:211], a[144:147], v[144:147], v[208:211]\n" \
"v_exp_f32 v73, v73\n" \
"v_mfma_f32_16x16x32_bf16 v[212:215], a[148:151], v[144:147], v[212:215]\n" \
"v_mfma_f32_16x16x32_bf16 v[240:243], a[144:147], v[148:151], v[240:243]\n" \
"ds_read_b128 a[136:139], v5 offset:2048\n" \
"v_cvt_pk_bf16_f32 v132, v72, v73\n" \
"v_mfma_f32_16x16x32_bf16 v[244:247], a[148:151], v[148:151], v[244:247]\n" \
"s_add_u32 m0, s48, 16384\n" \
"v_exp_f32 v74, v74\n" \
"v_mfma_f32_16x16x32_bf16 a[16:19], a[144:147], v[152:155], a[16:19]\n" \
"buffer_load_dwordx4 v6, s[40:43], s17 offen offset:1024 sc1 lds\n" \
"v_mfma_f32_16x16x32_bf16 a[20:23], a[148:151], v[152:155], a[20:23]\n" \
"v_exp_f32 v75, v75\n" \
"v_mfma_f32_16x16x32_bf16 a[48:51], a[144:147], v[156:159], a[48:51]\n" \
"ds_read_b128 a[140:143], v5 offset:3072\n" \
"v_cvt_pk_bf16_f32 v133, v74, v75\n" \
"v_mfma_f32_16x16x32_bf16 a[52:55], a[148:151], v[156:159], a[52:55]\n" \
"s_waitcnt lgkmcnt(4)\n" \
"s_add_u32 m0, s48, 16384\n" \
"v_mfma_f32_16x16x32_bf16 v[216:219], a[152:155], v[144:147], v[216:219]\n" \
"buffer_load_dwordx4 v6, s[40:43], s17 offen offset:2048 sc1 lds\n" \
"v_mfma_f32_16x16x32_bf16 v[220:223], a[156:159], v[144:147], v[220:223]\n" \
"s_add_u32 s44, s44, 1\n" \
"v_exp_f32 v76, v76\n" \
"v_mfma_f32_16x16x32_bf16 v[248:251], a[152:155], v[148:151], v[248:251]\n" \
"ds_read_b128 a[144:147], v5 offset:4096\n" \
"v_mfma_f32_16x16x32_bf16 v[252:255], a[156:159], v[148:151], v[252:255]\n" \
"s_add_u32 m0, s48, 16384\n" \
"v_exp_f32 v77, v77\n" \
"v_mfma_f32_16x16x32_bf16 a[24:27], a[152:155], v[152:155], a[24:27]\n" \
"buffer_load_dwordx4 v6, s[40:43], s17 offen offset:3072 sc1 lds\n" \
"v_mfma_f32_16x16x32_bf16 a[28:31], a[156:159], v[152:155], a[28:31]\n" \
"s_xor_b32 s48, s48, 0x10000\n" \
"v_exp_f32 v78, v78\n" \
"v_mfma_f32_16x16x32_bf16 a[56:59], a[152:155], v[156:159], a[56:59]\n" \
"ds_read_b128 a[148:151], v5 offset:5120\n" \
"v_exp_f32 v79, v79\n" \
"v_mfma_f32_16x16x32_bf16 a[60:63], a[156:159], v[156:159], a[60:63]\n" \
"s_waitcnt lgkmcnt(4)\n" \
"v_cvt_pk_bf16_f32 v134, v76, v77\n" \
"v_cvt_pk_bf16_f32 v135, v78, v79\n" \
"v_mfma_f32_16x16x32_bf16 v[96:99], a[128:131], v[32:35], 0\n" \
"buffer_load_dwordx4 a[112:115], v188, s[36:39], s84 offen sc1\n" \
"v_mfma_f32_16x16x32_bf16 v[96:99], a[132:135], v[36:39], v[96:99]\n" \
"s_add_u32 s16, s16, s9\n" \
"v_exp_f32 v80, v80\n" \
"v_mfma_f32_16x16x32_bf16 v[104:107], a[128:131], v[48:51], 0\n" \
"ds_read_b128 a[152:155], v5 offset:6144\n" \
"v_mfma_f32_16x16x32_bf16 v[104:107], a[132:135], v[52:55], v[104:107]\n" \
"s_add_u32 m0, s49, 32768\n" \
"v_exp_f32 v81, v81\n" \
"v_mfma_f32_16x16x32_bf16 v[112:115], a[128:131], a[64:67], 0\n" \
"buffer_load_dwordx4 v6, s[40:43], s16 offen offset:0 sc1 lds\n" \
"v_mfma_f32_16x16x32_bf16 v[112:115], a[132:135], a[68:71], v[112:115]\n" \
"v_exp_f32 v82, v82\n" \
"v_mfma_f32_16x16x32_bf16 v[120:123], a[128:131], a[80:83], 0\n" \
"ds_read_b128 a[156:159], v5 offset:7168\n" \
"v_cvt_pk_bf16_f32 v136, v80, v81\n" \
"v_mfma_f32_16x16x32_bf16 v[120:123], a[132:135], a[84:87], v[120:123]\n" \
"s_waitcnt lgkmcnt(4)\n" \
"v_exp_f32 v83, v83\n" \
"s_add_u32 m0, s79, 147456\n" \
"v_mfma_f32_16x16x32_bf16 v[96:99], a[136:139], v[40:43], v[96:99]\n" \
"buffer_load_dwordx4 v12, s[28:31], s81 offen offset:0 lds\n" \
"v_mfma_f32_16x16x32_bf16 v[96:99], a[140:143], v[44:47], v[96:99]\n" \
"v_exp_f32 v84, v84\n" \
"v_mfma_f32_16x16x32_bf16 v[104:107], a[136:139], v[56:59], v[104:107]\n" \
"ds_read_b128 a[128:131], v5 offset:16384\n" \
"v_cvt_pk_bf16_f32 v137, v82, v83\n" \
"v_mfma_f32_16x16x32_bf16 v[104:107], a[140:143], v[60:63], v[104:107]\n" \
"s_add_u32 m0, s49, 32768\n" \
"v_exp_f32 v85, v85\n" \
"v_mfma_f32_16x16x32_bf16 v[112:115], a[136:139], a[72:75], v[112:115]\n" \
"buffer_load_dwordx4 v6, s[40:43], s16 offen offset:1024 sc1 lds\n" \
"v_mfma_f32_16x16x32_bf16 v[112:115], a[140:143], a[76:79], v[112:115]\n" \
"v_exp_f32 v86, v86\n" \
"v_mfma_f32_16x16x32_bf16 v[120:123], a[136:139], a[88:91], v[120:123]\n" \
"ds_read_b128 a[132:135], v5 offset:17408\n" \
"v_cvt_pk_bf16_f32 v138, v84, v85\n" \
"v_mfma_f32_16x16x32_bf16 v[120:123], a[140:143], a[92:95], v[120:123]\n" \
"s_waitcnt lgkmcnt(4)\n" \
"v_exp_f32 v87, v87\n" \
"s_add_u32 m0, s79, 148352\n" \
"v_mfma_f32_16x16x32_bf16 v[100:103], a[144:147], v[32:35], 0\n" \
"buffer_load_dwordx4 v12, s[28:31], s81 offen offset:128 lds\n" \
"v_mfma_f32_16x16x32_bf16 v[100:103], a[148:151], v[36:39], v[100:103]\n" \
"s_mov_b32 s81, 720896\n" \
"v_exp_f32 v88, v88\n" \
"v_mfma_f32_16x16x32_bf16 v[108:111], a[144:147], v[48:51], 0\n" \
"ds_read_b128 a[136:139], v5 offset:18432\n" \
"v_cvt_pk_bf16_f32 v139, v86, v87\n" \
"v_mfma_f32_16x16x32_bf16 v[108:111], a[148:151], v[52:55], v[108:111]\n" \
"s_add_u32 m0, s49, 32768\n" \
"v_exp_f32 v89, v89\n" \
"v_mfma_f32_16x16x32_bf16 v[116:119], a[144:147], a[64:67], 0\n" \
"buffer_load_dwordx4 v6, s[40:43], s16 offen offset:2048 sc1 lds\n" \
"v_mfma_f32_16x16x32_bf16 v[116:119], a[148:151], a[68:71], v[116:119]\n" \
"v_exp_f32 v90, v90\n" \
"v_mfma_f32_16x16x32_bf16 v[124:127], a[144:147], a[80:83], 0\n" \
"ds_read_b128 a[140:143], v5 offset:19456\n" \
"v_cvt_pk_bf16_f32 v140, v88, v89\n" \
"v_mfma_f32_16x16x32_bf16 v[124:127], a[148:151], a[84:87], v[124:127]\n" \
"s_waitcnt lgkmcnt(4)\n" \
"v_exp_f32 v91, v91\n" \
"s_add_u32 m0, s79, 155648\n" \
"v_mfma_f32_16x16x32_bf16 v[100:103], a[152:155], v[40:43], v[100:103]\n" \
"buffer_load_dwordx4 v12, s[28:31], s81 offen offset:0 lds\n" \
"v_mfma_f32_16x16x32_bf16 v[100:103], a[156:159], v[44:47], v[100:103]\n" \
"v_exp_f32 v92, v92\n" \
"v_mfma_f32_16x16x32_bf16 v[108:111], a[152:155], v[56:59], v[108:111]\n" \
"ds_read_b128 a[144:147], v5 offset:20480\n" \
"v_cvt_pk_bf16_f32 v141, v90, v91\n" \
"v_mfma_f32_16x16x32_bf16 v[108:111], a[156:159], v[60:63], v[108:111]\n" \
"s_add_u32 m0, s49, 32768\n" \
"v_exp_f32 v93, v93\n" \
"v_mfma_f32_16x16x32_bf16 v[116:119], a[152:155], a[72:75], v[116:119]\n" \
"buffer_load_dwordx4 v6, s[40:43], s16 offen offset:3072 sc1 lds\n" \
"v_mfma_f32_16x16x32_bf16 v[116:119], a[156:159], a[76:79], v[116:119]\n" \
"v_exp_f32 v94, v94\n" \
"v_mfma_f32_16x16x32_bf16 v[124:127], a[152:155], a[88:91], v[124:127]\n" \
"ds_read_b128 a[148:151], v5 offset:21504\n" \
"v_cvt_pk_bf16_f32 v142, v92, v93\n" \
"v_mfma_f32_16x16x32_bf16 v[124:127], a[156:159], a[92:95], v[124:127]\n" \
"s_waitcnt lgkmcnt(4)\n" \
"v_exp_f32 v95, v95\n" \
"s_add_u32 m0, s79, 156544\n" \
"v_mfma_f32_16x16x32_bf16 v[160:163], v[180:183], v[128:131], v[160:163]\n" \
"buffer_load_dwordx4 v12, s[28:31], s81 offen offset:128 lds\n" \
"v_mfma_f32_16x16x32_bf16 v[164:167], v[180:183], v[132:135], v[164:167]\n" \
"v_cvt_pk_bf16_f32 v143, v94, v95\n" \
"v_mfma_f32_16x16x32_bf16 v[168:171], v[180:183], v[136:139], v[168:171]\n" \
"buffer_store_dwordx4 v[28:31], v4, s[36:39], s80 offen offset:3072 sc1\n" \
"v_mfma_f32_16x16x32_bf16 v[172:175], v[180:183], v[140:143], v[172:175]\n" \
"v_exp_f32 v96, v96\n" \
"v_mfma_f32_16x16x32_bf16 v[192:195], a[128:131], v[128:131], v[192:195]\n" \
"buffer_load_dwordx4 a[116:119], v189, s[36:39], s84 offen sc1\n" \
"v_mfma_f32_16x16x32_bf16 v[196:199], a[132:135], v[128:131], v[196:199]\n" \
"v_bfe_u32 v28, v4, 7, 1\n" \
"v_bfe_u32 v29, v4, 4, 3\n" \
"v_mfma_f32_16x16x32_bf16 v[224:227], a[128:131], v[132:135], v[224:227]\n" \
"ds_read_b128 a[152:155], v5 offset:22528\n" \
"v_lshl_add_u32 v28, v29, 1, v28\n" \
"v_mfma_f32_16x16x32_bf16 v[228:231], a[132:135], v[132:135], v[228:231]\n" \
"v_lshrrev_b32 v29, 8, v4\n" \
"v_lshl_add_u32 v28, v29, 4, v28\n" \
"v_mfma_f32_16x16x32_bf16 a[0:3], a[128:131], v[136:139], a[0:3]\n" \
"v_exp_f32 v97, v97\n" \
"v_mfma_f32_16x16x32_bf16 a[4:7], a[132:135], v[136:139], a[4:7]\n" \
"v_exp_f32 v98, v98\n" \
"v_mfma_f32_16x16x32_bf16 a[32:35], a[128:131], v[140:143], a[32:35]\n" \
"ds_read_b128 a[156:159], v5 offset:23552\n" \
"v_lshlrev_b32 v28, 4, v28\n" \
"v_mfma_f32_16x16x32_bf16 a[36:39], a[132:135], v[140:143], a[36:39]\n" \
"s_waitcnt vmcnt(15) lgkmcnt(4)\n" \
"v_exp_f32 v99, v99\n" \
"v_mfma_f32_16x16x32_bf16 v[200:203], a[136:139], v[128:131], v[200:203]\n" \
"s_barrier\n" \
"v_exp_f32 v100, v100\n" \
"v_mfma_f32_16x16x32_bf16 v[204:207], a[140:143], v[128:131], v[204:207]\n" \
"s_add_u32 s17, s17, s19\n" \
"v_exp_f32 v101, v101\n" \
"v_mfma_f32_16x16x32_bf16 v[232:235], a[136:139], v[132:135], v[232:235]\n" \
"ds_read_b128 a[128:131], v5 offset:32768\n" \
"v_add_u32 v29, 0x10000, v28\n" \
"v_mfma_f32_16x16x32_bf16 v[236:239], a[140:143], v[132:135], v[236:239]\n" \
"s_add_u32 m0, s49, 49152\n" \
"v_exp_f32 v102, v102\n" \
"v_mfma_f32_16x16x32_bf16 a[8:11], a[136:139], v[136:139], a[8:11]\n" \
"buffer_load_dwordx4 v6, s[40:43], s17 offen offset:0 sc1 lds\n" \
"v_mfma_f32_16x16x32_bf16 a[12:15], a[140:143], v[136:139], a[12:15]\n" \
"v_exp_f32 v103, v103\n" \
"v_mfma_f32_16x16x32_bf16 a[40:43], a[136:139], v[140:143], a[40:43]\n" \
"ds_read_b128 a[132:135], v5 offset:33792\n" \
"v_add_u32 v30, 0x20000, v28\n" \
"v_mfma_f32_16x16x32_bf16 a[44:47], a[140:143], v[140:143], a[44:47]\n" \
"s_waitcnt lgkmcnt(4)\n" \
"v_exp_f32 v104, v104\n" \
"v_mfma_f32_16x16x32_bf16 v[208:211], a[144:147], v[128:131], v[208:211]\n" \
"v_exp_f32 v105, v105\n" \
"v_mfma_f32_16x16x32_bf16 v[212:215], a[148:151], v[128:131], v[212:215]\n" \
"v_mfma_f32_16x16x32_bf16 v[240:243], a[144:147], v[132:135], v[240:243]\n" \
"ds_read_b128 a[136:139], v5 offset:34816\n" \
"v_add_u32 v31, s22, v4\n" \
"v_mfma_f32_16x16x32_bf16 v[244:247], a[148:151], v[132:135], v[244:247]\n" \
"s_add_u32 m0, s49, 49152\n" \
"v_exp_f32 v106, v106\n" \
"v_mfma_f32_16x16x32_bf16 a[16:19], a[144:147], v[136:139], a[16:19]\n" \
"buffer_load_dwordx4 v6, s[40:43], s17 offen offset:1024 sc1 lds\n" \
"v_mfma_f32_16x16x32_bf16 a[20:23], a[148:151], v[136:139], a[20:23]\n" \
"v_exp_f32 v107, v107\n" \
"v_mfma_f32_16x16x32_bf16 a[48:51], a[144:147], v[140:143], a[48:51]\n" \
"ds_read_b128 a[140:143], v5 offset:35840\n" \
"v_cvt_pk_bf16_f32 v144, v96, v97\n" \
"v_mfma_f32_16x16x32_bf16 a[52:55], a[148:151], v[140:143], a[52:55]\n" \
"s_waitcnt lgkmcnt(4)\n" \
"s_add_u32 m0, s49, 49152\n" \
"ds_read_b64_tr_b16 a[224:225], v13 offset:0\n" \
"v_mfma_f32_16x16x32_bf16 v[216:219], a[152:155], v[128:131], v[216:219]\n" \
"buffer_load_dwordx4 v6, s[40:43], s17 offen offset:2048 sc1 lds\n" \
"v_mfma_f32_16x16x32_bf16 v[220:223], a[156:159], v[128:131], v[220:223]\n" \
"v_exp_f32 v108, v108\n" \
"v_mfma_f32_16x16x32_bf16 v[248:251], a[152:155], v[132:135], v[248:251]\n" \
"ds_read_b128 a[144:147], v5 offset:36864\n" \
"v_cvt_pk_bf16_f32 v145, v98, v99\n" \
"v_mfma_f32_16x16x32_bf16 v[252:255], a[156:159], v[132:135], v[252:255]\n" \
"s_add_u32 m0, s49, 49152\n" \
"v_exp_f32 v109, v109\n" \
"v_mfma_f32_16x16x32_bf16 a[24:27], a[152:155], v[136:139], a[24:27]\n" \
"buffer_load_dwordx4 v6, s[40:43], s17 offen offset:3072 sc1 lds\n" \
"v_mfma_f32_16x16x32_bf16 a[28:31], a[156:159], v[136:139], a[28:31]\n" \
"v_exp_f32 v110, v110\n" \
"v_mfma_f32_16x16x32_bf16 a[56:59], a[152:155], v[140:143], a[56:59]\n" \
"ds_read_b128 a[148:151], v5 offset:37888\n" \
"v_exp_f32 v111, v111\n" \
"v_mfma_f32_16x16x32_bf16 a[60:63], a[156:159], v[140:143], a[60:63]\n" \
"s_waitcnt lgkmcnt(4)\n" \
"ds_read_b64_tr_b16 a[226:227], v14 offset:0\n" \
"v_cvt_pk_bf16_f32 v146, v100, v101\n" \
"v_cvt_pk_bf16_f32 v147, v102, v103\n" \
"v_cvt_pk_bf16_f32 v148, v104, v105\n" \
"v_cvt_pk_bf16_f32 v149, v106, v107\n" \
"v_cvt_pk_bf16_f32 v150, v108, v109\n" \
"v_cvt_pk_bf16_f32 v151, v110, v111\n" \
"v_mfma_f32_16x16x32_bf16 v[64:67], a[128:131], v[32:35], 0\n" \
"LH19_%=:\ns_cbranch_execz LHR1_%=\n" \
"buffer_load_dwordx4 a[120:123], v188, s[36:39], s85 offen sc1\n" \
"v_mfma_f32_16x16x32_bf16 v[64:67], a[132:135], v[36:39], v[64:67]\n" \
"s_add_u32 s16, s16, s9\n" \
"ds_read_b64_tr_b16 a[228:229], v15 offset:0\n" \
"v_mfma_f32_16x16x32_bf16 v[72:75], a[128:131], v[48:51], 0\n" \
"s_add_u32 s100, s100, 262144\ns_addc_u32 s101, s101, 0\n" \
"ds_read_b128 a[152:155], v5 offset:38912\n" \
"v_mfma_f32_16x16x32_bf16 v[72:75], a[132:135], v[52:55], v[72:75]\n" \
"s_add_u32 m0, s48, 0\n" \
"ds_read_b64_tr_b16 a[230:231], v16 offset:0\n" \
"v_mfma_f32_16x16x32_bf16 v[80:83], a[128:131], a[64:67], 0\n" \
"buffer_load_dwordx4 v6, s[40:43], s16 offen offset:0 sc1 lds\n" \
"v_mfma_f32_16x16x32_bf16 v[80:83], a[132:135], a[68:71], v[80:83]\n" \
"ds_read_b64_tr_b16 a[232:233], v13 offset:8192\n" \
"v_mfma_f32_16x16x32_bf16 v[88:91], a[128:131], a[80:83], 0\n" \
"ds_read_b128 a[156:159], v5 offset:39936\n" \
"v_mfma_f32_16x16x32_bf16 v[88:91], a[132:135], a[84:87], v[88:91]\n" \
"s_waitcnt lgkmcnt(4)\n" \
"v_exp_f32 v112, v112\n" \
"v_mfma_f32_16x16x32_bf16 v[64:67], a[136:139], v[40:43], v[64:67]\n" \
"buffer_load_dwordx4 a[124:127], v189, s[36:39], s85 offen sc1\n" \
"v_mfma_f32_16x16x32_bf16 v[64:67], a[140:143], v[44:47], v[64:67]\n" \
"s_xor_b32 s49, s49, 0x10000\n" \
"v_exp_f32 v113, v113\n" \
"v_mfma_f32_16x16x32_bf16 v[72:75], a[136:139], v[56:59], v[72:75]\n" \
"ds_read_b128 a[128:131], v5 offset:49152\n" \
"v_cvt_pk_bf16_f32 v152, v112, v113\n" \
"v_mfma_f32_16x16x32_bf16 v[72:75], a[140:143], v[60:63], v[72:75]\n" \
"s_add_u32 m0, s48, 0\n" \
"v_exp_f32 v114, v114\n" \
"v_mfma_f32_16x16x32_bf16 v[80:83], a[136:139], a[72:75], v[80:83]\n" \
"buffer_load_dwordx4 v6, s[40:43], s16 offen offset:1024 sc1 lds\n" \
"v_mfma_f32_16x16x32_bf16 v[80:83], a[140:143], a[76:79], v[80:83]\n" \
"v_exp_f32 v115, v115\n" \
"v_mfma_f32_16x16x32_bf16 v[88:91], a[136:139], a[88:91], v[88:91]\n" \
"ds_read_b128 a[132:135], v5 offset:50176\n" \
"v_cvt_pk_bf16_f32 v153, v114, v115\n" \
"v_mfma_f32_16x16x32_bf16 v[88:91], a[140:143], a[92:95], v[88:91]\n" \
"s_waitcnt lgkmcnt(4)\n" \
"v_exp_f32 v116, v116\n" \
"v_mfma_f32_16x16x32_bf16 v[68:71], a[144:147], v[32:35], 0\n" \
"v_exp_f32 v117, v117\n" \
"v_mfma_f32_16x16x32_bf16 v[68:71], a[148:151], v[36:39], v[68:71]\n" \
"v_exp_f32 v118, v118\n" \
"v_mfma_f32_16x16x32_bf16 v[76:79], a[144:147], v[48:51], 0\n" \
"ds_read_b128 a[136:139], v5 offset:51200\n" \
"v_cvt_pk_bf16_f32 v154, v116, v117\n" \
"v_mfma_f32_16x16x32_bf16 v[76:79], a[148:151], v[52:55], v[76:79]\n" \
"s_add_u32 m0, s48, 0\n" \
"v_exp_f32 v119, v119\n" \
"v_mfma_f32_16x16x32_bf16 v[84:87], a[144:147], a[64:67], 0\n" \
"buffer_load_dwordx4 v6, s[40:43], s16 offen offset:2048 sc1 lds\n" \
"v_mfma_f32_16x16x32_bf16 v[84:87], a[148:151], a[68:71], v[84:87]\n" \
"v_exp_f32 v120, v120\n" \
"v_mfma_f32_16x16x32_bf16 v[92:95], a[144:147], a[80:83], 0\n" \
"ds_read_b128 a[140:143], v5 offset:52224\n" \
"v_cvt_pk_bf16_f32 v155, v118, v119\n" \
"v_mfma_f32_16x16x32_bf16 v[92:95], a[148:151], a[84:87], v[92:95]\n" \
"s_waitcnt lgkmcnt(4)\n" \
"v_exp_f32 v121, v121\n" \
"v_mfma_f32_16x16x32_bf16 v[68:71], a[152:155], v[40:43], v[68:71]\n" \
"v_exp_f32 v122, v122\n" \
"v_mfma_f32_16x16x32_bf16 v[68:71], a[156:159], v[44:47], v[68:71]\n" \
"v_exp_f32 v123, v123\n" \
"v_mfma_f32_16x16x32_bf16 v[76:79], a[152:155], v[56:59], v[76:79]\n" \
"ds_read_b128 a[144:147], v5 offset:53248\n" \
"v_cvt_pk_bf16_f32 v156, v120, v121\n" \
"v_mfma_f32_16x16x32_bf16 v[76:79], a[156:159], v[60:63], v[76:79]\n" \
"s_add_u32 m0, s48, 0\n" \
"v_exp_f32 v124, v124\n" \
"v_mfma_f32_16x16x32_bf16 v[84:87], a[152:155], a[72:75], v[84:87]\n" \
"buffer_load_dwordx4 v6, s[40:43], s16 offen offset:3072 sc1 lds\n" \
"v_mfma_f32_16x16x32_bf16 v[84:87], a[156:159], a[76:79], v[84:87]\n" \
"v_exp_f32 v125, v125\n" \
"v_mfma_f32_16x16x32_bf16 v[92:95], a[152:155], a[88:91], v[92:95]\n" \
"ds_read_b128 a[148:151], v5 offset:54272\n" \
"v_cvt_pk_bf16_f32 v157, v122, v123\n" \
"v_mfma_f32_16x16x32_bf16 v[92:95], a[156:159], a[92:95], v[92:95]\n" \
"s_waitcnt lgkmcnt(4)\n" \
"v_exp_f32 v126, v126\n" \
"v_mfma_f32_16x16x32_bf16 v[160:163], v[180:183], v[144:147], v[160:163]\n" \
"s_lshl_b32 s25, s45, 2\n" \
"v_exp_f32 v127, v127\n" \
"v_mfma_f32_16x16x32_bf16 v[164:167], v[180:183], v[148:151], v[164:167]\n" \
"buffer_store_dwordx2 v[10:11], v20, s[36:39], s12 offen sc1\n" \
"v_cvt_pk_bf16_f32 v158, v124, v125\n" \
"v_cvt_pk_bf16_f32 v159, v126, v127\n" \
"v_mfma_f32_16x16x32_bf16 v[168:171], v[180:183], v[152:155], v[168:171]\n" \
"s_andn2_b32 s25, 32, s25\n" \
"ds_read_b64_tr_b16 a[234:235], v14 offset:8192\n" \
"s_add_u32 s25, s25, %2\n" \
"v_mfma_f32_16x16x32_bf16 v[172:175], v[180:183], v[156:159], v[172:175]\n" \
"s_mov_b64 exec, 0xffff\nglobal_load_dword v1, v2, s[100:101]\ns_mov_b64 exec, -1\n" \
"v_mfma_f32_16x16x32_bf16 v[192:195], a[128:131], v[144:147], v[192:195]\n" \
"s_cmp_ge_u32 s25, 34\ns_cselect_b64 vcc, -1, 0\n" \
"ds_read_b64_tr_b16 a[236:237], v15 offset:8192\n" \
"v_mfma_f32_16x16x32_bf16 v[196:199], a[132:135], v[144:147], v[196:199]\n" \
"ds_read_b64_tr_b16 a[238:239], v16 offset:8192\n" \
"v_bfe_u32 v190, v4, 7, 2\n" \
"v_mfma_f32_16x16x32_bf16 v[224:227], a[128:131], v[148:151], v[224:227]\n" \
"ds_read_b128 a[152:155], v5 offset:55296\n" \
"v_min_u32 v190, 2, v190\n" \
"v_mfma_f32_16x16x32_bf16 v[228:231], a[132:135], v[148:151], v[228:231]\n" \
"v_exp_f32 v64, v64\n" \
"v_mfma_f32_16x16x32_bf16 a[0:3], a[128:131], v[152:155], a[0:3]\n" \
"v_exp_f32 v65, v65\n" \
"v_mfma_f32_16x16x32_bf16 a[4:7], a[132:135], v[152:155], a[4:7]\n" \
"s_waitcnt lgkmcnt(1)\n" \
"v_exp_f32 v66, v66\n" \
"v_mfma_f32_16x16x32_bf16 a[32:35], a[128:131], v[156:159], a[32:35]\n" \
"ds_read_b128 a[156:159], v5 offset:56320\n" \
"v_lshl_add_u32 v17, v190, 8, s98\n" \
"v_mfma_f32_16x16x32_bf16 a[36:39], a[132:135], v[156:159], a[36:39]\n" \
"s_waitcnt vmcnt(14) lgkmcnt(4)\n" \
"v_exp_f32 v67, v67\n" \
"v_mfma_f32_16x16x32_bf16 v[200:203], a[136:139], v[144:147], v[200:203]\n" \
"s_barrier\n" \
"v_xor_b32 v5, 0x10000, v5\n" \
"v_bfe_u32 v190, v4, 4, 3\n" \
"v_mfma_f32_16x16x32_bf16 v[204:207], a[140:143], v[144:147], v[204:207]\n" \
"s_add_u32 s17, s17, s19\n" \
"v_exp_f32 v68, v68\n" \
"v_mfma_f32_16x16x32_bf16 v[232:235], a[136:139], v[148:151], v[232:235]\n" \
"s_lshr_b32 s92, %1, 5\n" \
"ds_read_b128 a[128:131], v5 offset:0\n" \
"v_lshl_add_u32 v17, v190, 3, v17\n" \
"v_mfma_f32_16x16x32_bf16 v[236:239], a[140:143], v[148:151], v[236:239]\n" \
"s_add_u32 m0, s48, 16384\n" \
"v_exp_f32 v69, v69\n" \
"v_mfma_f32_16x16x32_bf16 a[8:11], a[136:139], v[152:155], a[8:11]\n" \
"buffer_load_dwordx4 v6, s[40:43], s17 offen offset:0 sc1 lds\n" \
"v_mfma_f32_16x16x32_bf16 a[12:15], a[140:143], v[152:155], a[12:15]\n" \
"s_lshl_b32 s92, s92, 2\n" \
"v_exp_f32 v70, v70\n" \
"v_mfma_f32_16x16x32_bf16 a[40:43], a[136:139], v[156:159], a[40:43]\n" \
"s_add_u32 s92, s92, %2\n" \
"ds_read_b128 a[132:135], v5 offset:1024\n" \
"s_lshl_b32 s92, s92, 11\n" \
"v_mfma_f32_16x16x32_bf16 a[44:47], a[140:143], v[156:159], a[44:47]\n" \
"s_waitcnt lgkmcnt(4)\n" \
"v_exp_f32 v71, v71\n" \
"v_mfma_f32_16x16x32_bf16 v[208:211], a[144:147], v[144:147], v[208:211]\n" \
"s_waitcnt vmcnt(14)\n" \
"v_exp_f32 v72, v72\n" \
"v_mfma_f32_16x16x32_bf16 v[212:215], a[148:151], v[144:147], v[212:215]\n" \
"v_exp_f32 v73, v73\n" \
"v_mfma_f32_16x16x32_bf16 v[240:243], a[144:147], v[148:151], v[240:243]\n" \
"ds_read_b128 a[136:139], v5 offset:2048\n" \
"v_and_b32 v188, 0xf0, v4\n" \
"v_mfma_f32_16x16x32_bf16 v[244:247], a[148:151], v[148:151], v[244:247]\n" \
"s_add_u32 m0, s48, 16384\n" \
"v_exp_f32 v74, v74\n" \
"v_mfma_f32_16x16x32_bf16 a[16:19], a[144:147], v[152:155], a[16:19]\n" \
"buffer_load_dwordx4 v6, s[40:43], s17 offen offset:1024 sc1 lds\n" \
"v_mfma_f32_16x16x32_bf16 a[20:23], a[148:151], v[152:155], a[20:23]\n" \
"v_exp_f32 v75, v75\n" \
"v_mfma_f32_16x16x32_bf16 a[48:51], a[144:147], v[156:159], a[48:51]\n" \
"ds_read_b128 a[140:143], v5 offset:3072\n" \
"v_lshl_add_u32 v188, v188, 3, s92\n" \
"v_mfma_f32_16x16x32_bf16 a[52:55], a[148:151], v[156:159], a[52:55]\n" \
"s_waitcnt lgkmcnt(4)\n" \
"s_add_u32 m0, s48, 16384\n" \
"ds_read_b64_tr_b16 a[240:241], v13 offset:16384\n" \
"v_mfma_f32_16x16x32_bf16 v[216:219], a[152:155], v[144:147], v[216:219]\n" \
"buffer_load_dwordx4 v6, s[40:43], s17 offen offset:2048 sc1 lds\n" \
"v_mfma_f32_16x16x32_bf16 v[220:223], a[156:159], v[144:147], v[220:223]\n" \
"s_add_u32 s44, s44, 1\n" \
"v_exp_f32 v76, v76\n" \
"v_mfma_f32_16x16x32_bf16 v[248:251], a[152:155], v[148:151], v[248:251]\n" \
"ds_read_b128 a[144:147], v5 offset:4096\n" \
"v_lshrrev_b32 v189, 8, v4\n" \
"v_mfma_f32_16x16x32_bf16 v[252:255], a[156:159], v[148:151], v[252:255]\n" \
"s_add_u32 m0, s48, 16384\n" \
"v_exp_f32 v77, v77\n" \
"v_mfma_f32_16x16x32_bf16 a[24:27], a[152:155], v[152:155], a[24:27]\n" \
"buffer_load_dwordx4 v6, s[40:43], s17 offen offset:3072 sc1 lds\n" \
"v_mfma_f32_16x16x32_bf16 a[28:31], a[156:159], v[152:155], a[28:31]\n" \
"s_xor_b32 s48, s48, 0x10000\n" \
"v_exp_f32 v78, v78\n" \
"v_mfma_f32_16x16x32_bf16 a[56:59], a[152:155], v[156:159], a[56:59]\n" \
"ds_read_b128 a[148:151], v5 offset:5120\n" \
"v_lshl_add_u32 v190, v189, 18, v188\n" \
"v_exp_f32 v79, v79\n" \
"v_mfma_f32_16x16x32_bf16 a[60:63], a[156:159], v[156:159], a[60:63]\n" \
"s_waitcnt lgkmcnt(4)\n" \
"ds_read_b64_tr_b16 a[242:243], v14 offset:16384\n" \
"v_min_u32 v189, 2, v189\n" \
"v_cvt_pk_bf16_f32 v128, v64, v65\n" \
"v_cvt_pk_bf16_f32 v129, v66, v67\n" \
"v_cvt_pk_bf16_f32 v130, v68, v69\n" \
"v_cvt_pk_bf16_f32 v131, v70, v71\n" \
"v_cvt_pk_bf16_f32 v132, v72, v73\n" \
"v_cvt_pk_bf16_f32 v133, v74, v75\n" \
"v_cvt_pk_bf16_f32 v134, v76, v77\n" \
"v_cvt_pk_bf16_f32 v135, v78, v79\n" \
"v_mfma_f32_16x16x32_bf16 v[96:99], a[128:131], v[32:35], 0\n" \
"buffer_store_dwordx2 v[10:11], v20, s[36:39], s88 offen sc1\n" \
"v_mfma_f32_16x16x32_bf16 v[96:99], a[132:135], v[36:39], v[96:99]\n" \
"s_add_u32 s16, s16, s9\n" \
"ds_read_b64_tr_b16 a[244:245], v15 offset:16384\n" \
"s_mul_i32 s92, s90, 12\n" \
"v_mfma_f32_16x16x32_bf16 v[104:107], a[128:131], v[48:51], 0\n" \
"s_add_u32 s92, s92, s89\n" \
"ds_read_b128 a[152:155], v5 offset:6144\n" \
"s_lshl_b32 s92, s92, 16\n" \
"v_mfma_f32_16x16x32_bf16 v[104:107], a[132:135], v[52:55], v[104:107]\n" \
"s_add_u32 m0, s49, 32768\n" \
"ds_read_b64_tr_b16 a[246:247], v16 offset:16384\n" \
"v_lshl_add_u32 v191, v189, 18, v188\n" \
"v_mfma_f32_16x16x32_bf16 v[112:115], a[128:131], a[64:67], 0\n" \
"buffer_load_dwordx4 v6, s[40:43], s16 offen offset:0 sc1 lds\n" \
"v_mfma_f32_16x16x32_bf16 v[112:115], a[132:135], a[68:71], v[112:115]\n" \
"s_mov_b32 s46, s23\n" \
"ds_read_b64_tr_b16 a[248:249], v13 offset:24576\n" \
"v_add_u32 v191, s92, v191\n" \
"v_mfma_f32_16x16x32_bf16 v[120:123], a[128:131], a[80:83], 0\n" \
"s_lshl_b32 s92, %2, 13\n" \
"ds_read_b128 a[156:159], v5 offset:7168\n" \
"s_add_u32 s92, s92, 131072\n" \
"v_mfma_f32_16x16x32_bf16 v[120:123], a[132:135], a[84:87], v[120:123]\n" \
"s_waitcnt lgkmcnt(4)\n" \
"v_add_u32 v186, s92, v4\n" \
"v_xor_b32 v187, 0x6000, v186\n" \
"v_mfma_f32_16x16x32_bf16 v[96:99], a[136:139], v[40:43], v[96:99]\n" \
"s_lshl_b32 s92, %2, 9\n" \
"v_exp_f32 v80, v80\n" \
"v_mfma_f32_16x16x32_bf16 v[96:99], a[140:143], v[44:47], v[96:99]\n" \
"s_add_u32 s92, s92, 65536\n" \
"v_exp_f32 v81, v81\n" \
"v_mfma_f32_16x16x32_bf16 v[104:107], a[136:139], v[56:59], v[104:107]\n" \
"ds_read_b128 a[128:131], v5 offset:16384\n" \
"v_lshrrev_b32 v184, 2, v4\n" \
"v_mfma_f32_16x16x32_bf16 v[104:107], a[140:143], v[60:63], v[104:107]\n" \
"s_add_u32 m0, s49, 32768\n" \
"v_exp_f32 v82, v82\n" \
"v_mfma_f32_16x16x32_bf16 v[112:115], a[136:139], a[72:75], v[112:115]\n" \
"buffer_load_dwordx4 v6, s[40:43], s16 offen offset:1024 sc1 lds\n" \
"v_mfma_f32_16x16x32_bf16 v[112:115], a[140:143], a[76:79], v[112:115]\n" \
"v_exp_f32 v83, v83\n" \
"v_mfma_f32_16x16x32_bf16 v[120:123], a[136:139], a[88:91], v[120:123]\n" \
"ds_read_b128 a[132:135], v5 offset:17408\n" \
"v_add_u32 v184, s92, v184\n" \
"v_mfma_f32_16x16x32_bf16 v[120:123], a[140:143], a[92:95], v[120:123]\n" \
"s_waitcnt lgkmcnt(4)\n" \
"v_exp_f32 v84, v84\n" \
"v_mfma_f32_16x16x32_bf16 v[100:103], a[144:147], v[32:35], 0\n" \
"v_exp_f32 v85, v85\n" \
"v_mfma_f32_16x16x32_bf16 v[100:103], a[148:151], v[36:39], v[100:103]\n" \
"v_exp_f32 v86, v86\n" \
"v_mfma_f32_16x16x32_bf16 v[108:111], a[144:147], v[48:51], 0\n" \
"ds_read_b128 a[136:139], v5 offset:18432\n" \
"v_xor_b32 v185, 0x600, v184\n" \
"v_mfma_f32_16x16x32_bf16 v[108:111], a[148:151], v[52:55], v[108:111]\n" \
"s_add_u32 m0, s49, 32768\n" \
"v_exp_f32 v87, v87\n" \
"v_mfma_f32_16x16x32_bf16 v[116:119], a[144:147], a[64:67], 0\n" \
"buffer_load_dwordx4 v6, s[40:43], s16 offen offset:2048 sc1 lds\n" \
"v_mfma_f32_16x16x32_bf16 v[116:119], a[148:151], a[68:71], v[116:119]\n" \
"v_exp_f32 v88, v88\n" \
"v_mfma_f32_16x16x32_bf16 v[124:127], a[144:147], a[80:83], 0\n" \
"s_add_u32 s100, s100, 262144\ns_addc_u32 s101, s101, 0\n" \
"ds_read_b128 a[140:143], v5 offset:19456\n" \
"v_mfma_f32_16x16x32_bf16 v[124:127], a[148:151], a[84:87], v[124:127]\n" \
"s_waitcnt lgkmcnt(4)\n" \
"v_exp_f32 v89, v89\n" \
"v_mfma_f32_16x16x32_bf16 v[100:103], a[152:155], v[40:43], v[100:103]\n" \
"v_exp_f32 v90, v90\n" \
"v_mfma_f32_16x16x32_bf16 v[100:103], a[156:159], v[44:47], v[100:103]\n" \
"v_exp_f32 v91, v91\n" \
"v_mfma_f32_16x16x32_bf16 v[108:111], a[152:155], v[56:59], v[108:111]\n" \
"ds_read_b128 a[144:147], v5 offset:20480\n" \
"v_cvt_pk_bf16_f32 v136, v80, v81\n" \
"v_mfma_f32_16x16x32_bf16 v[108:111], a[156:159], v[60:63], v[108:111]\n" \
"s_add_u32 m0, s49, 32768\n" \
"v_exp_f32 v92, v92\n" \
"v_mfma_f32_16x16x32_bf16 v[116:119], a[152:155], a[72:75], v[116:119]\n" \
"buffer_load_dwordx4 v6, s[40:43], s16 offen offset:3072 sc1 lds\n" \
"v_mfma_f32_16x16x32_bf16 v[116:119], a[156:159], a[76:79], v[116:119]\n" \
"v_exp_f32 v93, v93\n" \
"v_mfma_f32_16x16x32_bf16 v[124:127], a[152:155], a[88:91], v[124:127]\n" \
"ds_read_b128 a[148:151], v5 offset:21504\n" \
"v_cvt_pk_bf16_f32 v137, v82, v83\n" \
"v_mfma_f32_16x16x32_bf16 v[124:127], a[156:159], a[92:95], v[124:127]\n" \
"s_waitcnt lgkmcnt(4)\n" \
"v_exp_f32 v94, v94\n" \
"v_mfma_f32_16x16x32_bf16 v[160:163], v[180:183], v[128:131], v[160:163]\n" \
"v_exp_f32 v95, v95\n" \
"v_cvt_pk_bf16_f32 v138, v84, v85\n" \
"v_cvt_pk_bf16_f32 v139, v86, v87\n" \
"v_mfma_f32_16x16x32_bf16 v[164:167], v[180:183], v[132:135], v[164:167]\n" \
"s_mov_b64 exec, 0xffff\nglobal_load_dword v1, v2, s[100:101]\ns_mov_b64 exec, -1\n" \
"v_cvt_pk_bf16_f32 v140, v88, v89\n" \
"v_cvt_pk_bf16_f32 v141, v90, v91\n" \
"v_cvt_pk_bf16_f32 v142, v92, v93\n" \
"v_cvt_pk_bf16_f32 v143, v94, v95\n" \
"v_mfma_f32_16x16x32_bf16 v[168:171], v[180:183], v[136:139], v[168:171]\n" \
"s_add_u32 s100, s100, 262144\ns_addc_u32 s101, s101, 0\n" \
"ds_read_b64_tr_b16 a[250:251], v14 offset:24576\n" \
"v_mfma_f32_16x16x32_bf16 v[172:175], v[180:183], v[140:143], v[172:175]\n" \
"ds_read_b64_tr_b16 a[252:253], v15 offset:24576\n" \
"v_mfma_f32_16x16x32_bf16 v[192:195], a[128:131], v[128:131], v[192:195]\n" \
"ds_read_b64_tr_b16 a[254:255], v16 offset:24576\n" \
"v_mfma_f32_16x16x32_bf16 v[196:199], a[132:135], v[128:131], v[196:199]\n" \
"s_mov_b64 exec, 0xffff\nglobal_load_dword v1, v2, s[100:101]\ns_mov_b64 exec, -1\n" \
"v_mfma_f32_16x16x32_bf16 v[224:227], a[128:131], v[132:135], v[224:227]\n" \
"ds_read_b128 a[152:155], v5 offset:22528\n" \
"v_mov_b32 v2, v190\n" \
"v_mfma_f32_16x16x32_bf16 v[228:231], a[132:135], v[132:135], v[228:231]\n" \
"v_exp_f32 v96, v96\n" \
"v_mfma_f32_16x16x32_bf16 a[0:3], a[128:131], v[136:139], a[0:3]\n" \
"v_exp_f32 v97, v97\n" \
"v_mfma_f32_16x16x32_bf16 a[4:7], a[132:135], v[136:139], a[4:7]\n" \
"s_waitcnt lgkmcnt(1)\n" \
"v_exp_f32 v98, v98\n" \
"v_mfma_f32_16x16x32_bf16 a[32:35], a[128:131], v[140:143], a[32:35]\n" \
"ds_read_b128 a[156:159], v5 offset:23552\n" \
"v_cvt_pk_bf16_f32 v144, v96, v97\n" \
"v_mfma_f32_16x16x32_bf16 a[36:39], a[132:135], v[140:143], a[36:39]\n" \
"s_waitcnt vmcnt(19) lgkmcnt(4)\n" \
"v_exp_f32 v99, v99\n" \
"v_mfma_f32_16x16x32_bf16 v[200:203], a[136:139], v[128:131], v[200:203]\n" \
"s_barrier\n" \
"v_exp_f32 v100, v100\n" \
"v_mfma_f32_16x16x32_bf16 v[204:207], a[140:143], v[128:131], v[204:207]\n" \
"s_add_u32 s17, s17, s19\n" \
"v_exp_f32 v101, v101\n" \
"v_mfma_f32_16x16x32_bf16 v[232:235], a[136:139], v[132:135], v[232:235]\n" \
"ds_read_b128 a[128:131], v5 offset:32768\n" \
"v_cvt_pk_bf16_f32 v145, v98, v99\n" \
"v_mfma_f32_16x16x32_bf16 v[236:239], a[140:143], v[132:135], v[236:239]\n" \
"s_add_u32 m0, s49, 49152\n" \
"v_exp_f32 v102, v102\n" \
"v_mfma_f32_16x16x32_bf16 a[8:11], a[136:139], v[136:139], a[8:11]\n" \
"buffer_load_dwordx4 v6, s[40:43], s17 offen offset:0 sc1 lds\n" \
"v_mfma_f32_16x16x32_bf16 a[12:15], a[140:143], v[136:139], a[12:15]\n" \
"v_exp_f32 v103, v103\n" \
"v_mfma_f32_16x16x32_bf16 a[40:43], a[136:139], v[140:143], a[40:43]\n" \
"ds_read_b128 a[132:135], v5 offset:33792\n" \
"v_cvt_pk_bf16_f32 v146, v100, v101\n" \
"v_mfma_f32_16x16x32_bf16 a[44:47], a[140:143], v[140:143], a[44:47]\n" \
"s_waitcnt lgkmcnt(4)\n" \
"v_exp_f32 v104, v104\n" \
"v_mfma_f32_16x16x32_bf16 v[208:211], a[144:147], v[128:131], v[208:211]\n" \
"v_exp_f32 v105, v105\n" \
"v_mfma_f32_16x16x32_bf16 v[212:215], a[148:151], v[128:131], v[212:215]\n" \
"v_mfma_f32_16x16x32_bf16 v[240:243], a[144:147], v[132:135], v[240:243]\n" \
"ds_read_b128 a[136:139], v5 offset:34816\n" \
"v_cvt_pk_bf16_f32 v147, v102, v103\n" \
"v_mfma_f32_16x16x32_bf16 v[244:247], a[148:151], v[132:135], v[244:247]\n" \
"s_add_u32 m0, s49, 49152\n" \
"v_exp_f32 v106, v106\n" \
"v_mfma_f32_16x16x32_bf16 a[16:19], a[144:147], v[136:139], a[16:19]\n" \
"buffer_load_dwordx4 v6, s[40:43], s17 offen offset:1024 sc1 lds\n" \
"v_mfma_f32_16x16x32_bf16 a[20:23], a[148:151], v[136:139], a[20:23]\n" \
"v_exp_f32 v107, v107\n" \
"v_mfma_f32_16x16x32_bf16 a[48:51], a[144:147], v[140:143], a[48:51]\n" \
"ds_read_b128 a[140:143], v5 offset:35840\n" \
"v_cvt_pk_bf16_f32 v148, v104, v105\n" \
"v_mfma_f32_16x16x32_bf16 a[52:55], a[148:151], v[140:143], a[52:55]\n" \
"s_waitcnt lgkmcnt(4)\n" \
"s_add_u32 m0, s49, 49152\n" \
"v_cvt_pk_bf16_f32 v149, v106, v107\n" \
"v_mfma_f32_16x16x32_bf16 v[216:219], a[152:155], v[128:131], v[216:219]\n" \
"buffer_load_dwordx4 v6, s[40:43], s17 offen offset:2048 sc1 lds\n" \
"v_mfma_f32_16x16x32_bf16 v[220:223], a[156:159], v[128:131], v[220:223]\n" \
"v_exp_f32 v108, v108\n" \
"v_mfma_f32_16x16x32_bf16 v[248:251], a[152:155], v[132:135], v[248:251]\n" \
"ds_read_b128 a[144:147], v5 offset:36864\n" \
"v_mfma_f32_16x16x32_bf16 v[252:255], a[156:159], v[132:135], v[252:255]\n" \
"s_add_u32 m0, s49, 49152\n" \
"v_exp_f32 v109, v109\n" \
"v_mfma_f32_16x16x32_bf16 a[24:27], a[152:155], v[136:139], a[24:27]\n" \
"buffer_load_dwordx4 v6, s[40:43], s17 offen offset:3072 sc1 lds\n" \
"v_mfma_f32_16x16x32_bf16 a[28:31], a[156:159], v[136:139], a[28:31]\n" \
"v_exp_f32 v110, v110\n" \
"v_mfma_f32_16x16x32_bf16 a[56:59], a[152:155], v[140:143], a[56:59]\n" \
"ds_read_b128 a[148:151], v5 offset:37888\n" \
"v_exp_f32 v111, v111\n" \
"v_mfma_f32_16x16x32_bf16 a[60:63], a[156:159], v[140:143], a[60:63]\n" \
"s_waitcnt lgkmcnt(4)\n" \
"v_cvt_pk_bf16_f32 v150, v108, v109\n" \
"v_cvt_pk_bf16_f32 v151, v110, v111\n" \
"s_cbranch_vccz LQWB_%=\nds_write_b128 v21, v[32:35] offset:16384\n" \
"ds_write_b128 v21, v[36:39] offset:17408\n" \
"ds_write_b128 v21, v[40:43] offset:18432\n" \
"ds_write_b128 v21, v[44:47] offset:19456\n" \
"ds_write_b128 v21, v[48:51] offset:20480\n" \
"ds_write_b128 v21, v[52:55] offset:21504\n" \
"ds_write_b128 v21, v[56:59] offset:22528\n" \
"ds_write_b128 v21, v[60:63] offset:23552\n" \
"ds_write_b128 v21, a[64:67] offset:0\n" \
"ds_write_b128 v21, a[68:71] offset:1024\n" \
"ds_write_b128 v21, a[72:75] offset:2048\n" \
"ds_write_b128 v21, a[76:79] offset:3072\n" \
"ds_write_b128 v21, a[80:83] offset:4096\n" \
"ds_write_b128 v21, a[84:87] offset:5120\n" \
"ds_write_b128 v21, a[88:91] offset:6144\n" \
"ds_write_b128 v21, a[92:95] offset:7168\nLQWB_%=:\n" \
"s_mov_b64 exec, 0\ns_cmp_eq_u32 %2, 0\ns_cbranch_scc1 LH23_%=\ns_cmp_eq_u32 %2, 1\ns_cbranch_scc1 LH24_%=\ns_cmp_eq_u32 %2, 2\ns_cbranch_scc1 LH25_%=\ns_cmp_eq_u32 %2, 3\ns_cbranch_scc1 LH26_%=\nLHR2_%=:\ns_mov_b64 exec, -1\n" \
"s_branch LCHK_%=\n" \
"LAD2_%=:\n" \
"v_xor_b32 v8, 0x2000, v5\n" \
"ds_read_b128 v[32:35], v21 offset:0\n" \
"ds_read_b128 v[36:39], v21 offset:1024\n" \
"ds_read_b128 v[40:43], v21 offset:2048\n" \
"ds_read_b128 v[44:47], v21 offset:3072\n" \
"ds_read_b128 v[48:51], v21 offset:4096\n" \
"ds_read_b128 v[52:55], v21 offset:5120\n" \
"ds_read_b128 v[56:59], v21 offset:6144\n" \
"ds_read_b128 v[60:63], v21 offset:7168\n" \
"ds_read_b128 a[64:67], v21 offset:16384\n" \
"ds_read_b128 a[68:71], v21 offset:17408\n" \
"ds_read_b128 a[72:75], v21 offset:18432\n" \
"ds_read_b128 a[76:79], v21 offset:19456\n" \
"ds_read_b128 a[128:131], v5 offset:49152\n" \
"ds_read_b128 a[80:83], v21 offset:20480\n" \
"ds_read_b128 a[84:87], v21 offset:21504\n" \
"ds_read_b128 a[88:91], v21 offset:22528\n" \
"ds_read_b128 a[92:95], v21 offset:23552\n" \
"ds_read_b128 a[132:135], v5 offset:50176\n" \
"ds_read_b128 a[136:139], v5 offset:51200\n" \
"ds_read_b128 a[140:143], v5 offset:52224\n" \
"ds_read_b128 a[144:147], v5 offset:53248\n" \
"ds_read_b128 a[148:151], v5 offset:54272\n" \
"v_exp_f32 v112, v112\n" \
"s_nop 0\n" \
"v_exp_f32 v113, v113\n" \
"s_nop 0\n" \
"v_exp_f32 v114, v114\n" \
"s_nop 0\n" \
"v_exp_f32 v115, v115\n" \
"s_nop 0\n" \
"v_exp_f32 v120, v120\n" \
"s_nop 0\n" \
"v_exp_f32 v121, v121\n" \
"s_nop 0\n" \
"v_exp_f32 v122, v122\n" \
"s_nop 0\n" \
"v_exp_f32 v123, v123\n" \
"s_nop 0\n" \
"v_exp_f32 v124, v124\n" \
"s_nop 0\n" \
"v_exp_f32 v125, v125\n" \
"s_nop 0\n" \
"v_exp_f32 v126, v126\n" \
"s_nop 0\n" \
"v_exp_f32 v127, v127\n" \
"s_nop 0\n" \
"v_cvt_pk_bf16_f32 v152, v112, v113\n" \
"v_cvt_pk_bf16_f32 v153, v114, v115\n" \
"v_mov_b32 v154, 0\n" \
"v_mov_b32 v155, 0\n" \
"v_cvt_pk_bf16_f32 v156, v120, v121\n" \
"v_cvt_pk_bf16_f32 v157, v122, v123\n" \
"v_cvt_pk_bf16_f32 v158, v124, v125\n" \
"v_cvt_pk_bf16_f32 v159, v126, v127\n" \
"v_subrev_u32 v23, 0x1f800000, v160\n" \
"v_subrev_u32 v27, 0x1f800000, v164\n" \
"v_max_u32 v23, v23, v27\n" \
"v_cmp_le_u32 vcc, 0x40000000, v23\n" \
"s_or_b64 s[54:55], s[54:55], vcc\n" \
"s_nop 0\n" \
"v_rcp_f32 v22, v160\n" \
"s_nop 0\n" \
"v_rcp_f32 v26, v164\n" \
"s_nop 0\n" \
"s_waitcnt lgkmcnt(0)\n" \
"s_branch LS3_%=\n" \
".p2align 6\n" \
"LH20_%=:\ns_cbranch_execz LHR1_%=\n" \
"s_nop 0\n" \
"LS1_%=:\n" \
"v_mfma_f32_16x16x32_bf16 v[64:67], a[128:131], v[32:35], v[176:179]\n" \
"v_exp_f32 v112, v112\n" \
"v_mfma_f32_16x16x32_bf16 v[64:67], a[132:135], v[36:39], v[64:67]\n" \
"s_add_u32 s16, s16, s9\n" \
"v_mfma_f32_16x16x32_bf16 v[72:75], a[128:131], v[48:51], 0\n" \
"ds_read_b128 a[152:155], v5 offset:38912\n" \
"v_mfma_f32_16x16x32_bf16 v[72:75], a[132:135], v[52:55], v[72:75]\n" \
"s_add_u32 m0, s48, 0\n" \
"v_exp_f32 v113, v113\n" \
"v_mfma_f32_16x16x32_bf16 v[80:83], a[128:131], a[64:67], 0\n" \
"buffer_load_dwordx4 v6, s[40:43], s16 offen offset:0 sc1 lds\n" \
"v_mfma_f32_16x16x32_bf16 v[80:83], a[132:135], a[68:71], v[80:83]\n" \
"v_exp_f32 v114, v114\n" \
"v_mfma_f32_16x16x32_bf16 v[88:91], a[128:131], a[80:83], 0\n" \
"ds_read_b128 a[156:159], v5 offset:39936\n" \
"v_cvt_pk_bf16_f32 v152, v112, v113\n" \
"v_mfma_f32_16x16x32_bf16 v[88:91], a[132:135], a[84:87], v[88:91]\n" \
"s_waitcnt lgkmcnt(4)\n" \
"v_exp_f32 v115, v115\n" \
"v_mfma_f32_16x16x32_bf16 v[64:67], a[136:139], v[40:43], v[64:67]\n" \
"v_exp_f32 v116, v116\n" \
"v_mfma_f32_16x16x32_bf16 v[64:67], a[140:143], v[44:47], v[64:67]\n" \
"s_xor_b32 s49, s49, 0x10000\n" \
"v_cvt_pk_bf16_f32 v153, v114, v115\n" \
"v_mfma_f32_16x16x32_bf16 v[72:75], a[136:139], v[56:59], v[72:75]\n" \
"ds_read_b128 a[128:131], v5 offset:49152\n" \
"v_mfma_f32_16x16x32_bf16 v[72:75], a[140:143], v[60:63], v[72:75]\n" \
"v_exp_f32 v117, v117\n" \
"v_mfma_f32_16x16x32_bf16 v[80:83], a[136:139], a[72:75], v[80:83]\n" \
"buffer_load_dwordx4 v6, s[40:43], s16 offen offset:1024 sc1 lds\n" \
"v_mfma_f32_16x16x32_bf16 v[80:83], a[140:143], a[76:79], v[80:83]\n" \
"v_exp_f32 v118, v118\n" \
"v_mfma_f32_16x16x32_bf16 v[88:91], a[136:139], a[88:91], v[88:91]\n" \
"ds_read_b128 a[132:135], v5 offset:50176\n" \
"v_cvt_pk_bf16_f32 v154, v116, v117\n" \
"v_mfma_f32_16x16x32_bf16 v[88:91], a[140:143], a[92:95], v[88:91]\n" \
"s_waitcnt lgkmcnt(4)\n" \
"v_exp_f32 v119, v119\n" \
"v_mov_b32 v68, 0xff800000\n" \
"v_mov_b32 v69, 0xff800000\n" \
"v_exp_f32 v120, v120\n" \
"v_cvt_pk_bf16_f32 v155, v118, v119\n" \
"v_mfma_f32_16x16x32_bf16 v[76:79], a[144:147], v[48:51], v[176:179]\n" \
"ds_read_b128 a[136:139], v5 offset:51200\n" \
"v_mfma_f32_16x16x32_bf16 v[76:79], a[148:151], v[52:55], v[76:79]\n" \
"v_exp_f32 v121, v121\n" \
"v_mfma_f32_16x16x32_bf16 v[84:87], a[144:147], a[64:67], 0\n" \
"buffer_load_dwordx4 v6, s[40:43], s16 offen offset:2048 sc1 lds\n" \
"v_mfma_f32_16x16x32_bf16 v[84:87], a[148:151], a[68:71], v[84:87]\n" \
"v_exp_f32 v122, v122\n" \
"v_mfma_f32_16x16x32_bf16 v[92:95], a[144:147], a[80:83], 0\n" \
"ds_read_b128 a[140:143], v5 offset:52224\n" \
"v_cvt_pk_bf16_f32 v156, v120, v121\n" \
"v_mfma_f32_16x16x32_bf16 v[92:95], a[148:151], a[84:87], v[92:95]\n" \
"s_waitcnt lgkmcnt(4)\n" \
"v_exp_f32 v123, v123\n" \
"v_mov_b32 v70, 0xff800000\n" \
"v_mov_b32 v71, 0xff800000\n" \
"v_exp_f32 v124, v124\n" \
"v_cvt_pk_bf16_f32 v157, v122, v123\n" \
"v_mfma_f32_16x16x32_bf16 v[76:79], a[152:155], v[56:59], v[76:79]\n" \
"ds_read_b128 a[144:147], v5 offset:53248\n" \
"v_mfma_f32_16x16x32_bf16 v[76:79], a[156:159], v[60:63], v[76:79]\n" \
"v_exp_f32 v125, v125\n" \
"v_mfma_f32_16x16x32_bf16 v[84:87], a[152:155], a[72:75], v[84:87]\n" \
"buffer_load_dwordx4 v6, s[40:43], s16 offen offset:3072 sc1 lds\n" \
"v_mfma_f32_16x16x32_bf16 v[84:87], a[156:159], a[76:79], v[84:87]\n" \
"v_exp_f32 v126, v126\n" \
"v_mfma_f32_16x16x32_bf16 v[92:95], a[152:155], a[88:91], v[92:95]\n" \
"ds_read_b128 a[148:151], v5 offset:54272\n" \
"v_cvt_pk_bf16_f32 v158, v124, v125\n" \
"v_mfma_f32_16x16x32_bf16 v[92:95], a[156:159], a[92:95], v[92:95]\n" \
"s_waitcnt lgkmcnt(4)\n" \
"v_exp_f32 v127, v127\n" \
"v_mfma_f32_16x16x32_bf16 v[160:163], v[180:183], v[144:147], v[160:163]\n" \
"v_cvt_pk_bf16_f32 v159, v126, v127\n" \
"v_mfma_f32_16x16x32_bf16 v[164:167], v[180:183], v[148:151], v[164:167]\n" \
"v_mfma_f32_16x16x32_bf16 v[168:171], v[180:183], v[152:155], v[168:171]\n" \
"v_exp_f32 v64, v64\n" \
"v_mfma_f32_16x16x32_bf16 v[172:175], v[180:183], v[156:159], v[172:175]\n" \
"v_mfma_f32_16x16x32_bf16 v[192:195], a[128:131], v[144:147], v[192:195]\n" \
"v_exp_f32 v65, v65\n" \
"v_mfma_f32_16x16x32_bf16 v[196:199], a[132:135], v[144:147], v[196:199]\n" \
"v_cvt_pk_bf16_f32 v128, v64, v65\n" \
"v_mfma_f32_16x16x32_bf16 v[224:227], a[128:131], v[148:151], v[224:227]\n" \
"ds_read_b128 a[152:155], v5 offset:55296\n" \
"v_mfma_f32_16x16x32_bf16 v[228:231], a[132:135], v[148:151], v[228:231]\n" \
"v_exp_f32 v66, v66\n" \
"v_mfma_f32_16x16x32_bf16 a[0:3], a[128:131], v[152:155], a[0:3]\n" \
"v_exp_f32 v67, v67\n" \
"v_mfma_f32_16x16x32_bf16 a[4:7], a[132:135], v[152:155], a[4:7]\n" \
"v_cvt_pk_bf16_f32 v129, v66, v67\n" \
"v_mfma_f32_16x16x32_bf16 a[32:35], a[128:131], v[156:159], a[32:35]\n" \
"ds_read_b128 a[156:159], v5 offset:56320\n" \
"v_mfma_f32_16x16x32_bf16 a[36:39], a[132:135], v[156:159], a[36:39]\n" \
"s_waitcnt vmcnt(12) lgkmcnt(4)\n" \
"v_exp_f32 v68, v68\n" \
"v_mfma_f32_16x16x32_bf16 v[200:203], a[136:139], v[144:147], v[200:203]\n" \
"s_barrier\n" \
"v_xor_b32 v5, 0x10000, v5\n" \
"v_mfma_f32_16x16x32_bf16 v[204:207], a[140:143], v[144:147], v[204:207]\n" \
"s_add_u32 s17, s17, s19\n" \
"v_exp_f32 v69, v69\n" \
"v_mfma_f32_16x16x32_bf16 v[232:235], a[136:139], v[148:151], v[232:235]\n" \
"ds_read_b128 a[128:131], v5 offset:0\n" \
"v_cvt_pk_bf16_f32 v130, v68, v69\n" \
"v_mfma_f32_16x16x32_bf16 v[236:239], a[140:143], v[148:151], v[236:239]\n" \
"s_add_u32 m0, s48, 16384\n" \
"v_exp_f32 v70, v70\n" \
"v_mfma_f32_16x16x32_bf16 a[8:11], a[136:139], v[152:155], a[8:11]\n" \
"buffer_load_dwordx4 v6, s[40:43], s17 offen offset:0 sc1 lds\n" \
"v_mfma_f32_16x16x32_bf16 a[12:15], a[140:143], v[152:155], a[12:15]\n" \
"v_exp_f32 v71, v71\n" \
"v_mfma_f32_16x16x32_bf16 a[40:43], a[136:139], v[156:159], a[40:43]\n" \
"ds_read_b128 a[132:135], v5 offset:1024\n" \
"v_cvt_pk_bf16_f32 v131, v70, v71\n" \
"v_mfma_f32_16x16x32_bf16 a[44:47], a[140:143], v[156:159], a[44:47]\n" \
"s_waitcnt lgkmcnt(4)\n" \
"v_exp_f32 v72, v72\n" \
"v_mfma_f32_16x16x32_bf16 v[208:211], a[144:147], v[144:147], v[208:211]\n" \
"v_exp_f32 v73, v73\n" \
"v_mfma_f32_16x16x32_bf16 v[212:215], a[148:151], v[144:147], v[212:215]\n" \
"LH21_%=:\ns_cbranch_execz LHR1_%=\n" \
"v_cvt_pk_bf16_f32 v132, v72, v73\n" \
"v_mfma_f32_16x16x32_bf16 v[240:243], a[144:147], v[148:151], v[240:243]\n" \
"ds_read_b128 a[136:139], v5 offset:2048\n" \
"v_mfma_f32_16x16x32_bf16 v[244:247], a[148:151], v[148:151], v[244:247]\n" \
"v_exp_f32 v74, v74\n" \
"v_mfma_f32_16x16x32_bf16 a[16:19], a[144:147], v[152:155], a[16:19]\n" \
"buffer_load_dwordx4 v6, s[40:43], s17 offen offset:1024 sc1 lds\n" \
"v_mfma_f32_16x16x32_bf16 a[20:23], a[148:151], v[152:155], a[20:23]\n" \
"v_exp_f32 v75, v75\n" \
"v_mfma_f32_16x16x32_bf16 a[48:51], a[144:147], v[156:159], a[48:51]\n" \
"ds_read_b128 a[140:143], v5 offset:3072\n" \
"v_cvt_pk_bf16_f32 v133, v74, v75\n" \
"v_mfma_f32_16x16x32_bf16 a[52:55], a[148:151], v[156:159], a[52:55]\n" \
"s_waitcnt lgkmcnt(4)\n" \
"v_exp_f32 v76, v76\n" \
"v_mfma_f32_16x16x32_bf16 v[216:219], a[152:155], v[144:147], v[216:219]\n" \
"buffer_load_dwordx4 v6, s[40:43], s17 offen offset:2048 sc1 lds\n" \
"v_mfma_f32_16x16x32_bf16 v[220:223], a[156:159], v[144:147], v[220:223]\n" \
"s_add_u32 s44, s44, 1\n" \
"v_exp_f32 v77, v77\n" \
"v_mfma_f32_16x16x32_bf16 v[248:251], a[152:155], v[148:151], v[248:251]\n" \
"ds_read_b128 a[144:147], v5 offset:4096\n" \
"v_cvt_pk_bf16_f32 v134, v76, v77\n" \
"v_mfma_f32_16x16x32_bf16 v[252:255], a[156:159], v[148:151], v[252:255]\n" \
"v_exp_f32 v78, v78\n" \
"v_mfma_f32_16x16x32_bf16 a[24:27], a[152:155], v[152:155], a[24:27]\n" \
"buffer_load_dwordx4 v6, s[40:43], s17 offen offset:3072 sc1 lds\n" \
"v_mfma_f32_16x16x32_bf16 a[28:31], a[156:159], v[152:155], a[28:31]\n" \
"s_xor_b32 s48, s48, 0x10000\n" \
"v_exp_f32 v79, v79\n" \
"v_mfma_f32_16x16x32_bf16 a[56:59], a[152:155], v[156:159], a[56:59]\n" \
"ds_read_b128 a[148:151], v5 offset:5120\n" \
"v_cvt_pk_bf16_f32 v135, v78, v79\n" \
"v_mfma_f32_16x16x32_bf16 a[60:63], a[156:159], v[156:159], a[60:63]\n" \
"s_waitcnt lgkmcnt(4)\n" \
"s_cmp_ge_u32 %2, 2\n" \
"s_cbranch_scc1 LFIN_%=\n" \
"s_cmp_eq_u32 s44, 15\ns_cselect_b32 s16, s56, s16\ns_cselect_b32 s9, s57, s9\ns_cselect_b32 s17, s58, s17\ns_cselect_b32 s19, s59, s19\n" \
"v_mfma_f32_16x16x32_bf16 v[112:115], a[128:131], a[64:67], v[176:179]\n" \
"v_xor_b32 v8, 0x2000, v5\n" \
"ds_read_b128 v[32:35], v21 offset:0\n" \
"v_mfma_f32_16x16x32_bf16 v[112:115], a[132:135], a[68:71], v[112:115]\n" \
"s_add_u32 s16, s16, s9\n" \
"ds_read_b128 a[152:155], v5 offset:6144\n" \
"s_add_u32 m0, s49, 32768\n" \
"v_mfma_f32_16x16x32_bf16 v[120:123], a[128:131], a[80:83], 0\n" \
"buffer_load_dwordx4 v6, s[40:43], s16 offen offset:0 sc1 lds\n" \
"v_mfma_f32_16x16x32_bf16 v[120:123], a[132:135], a[84:87], v[120:123]\n" \
"s_waitcnt lgkmcnt(4)\n" \
"ds_read_b128 a[156:159], v5 offset:7168\n" \
"v_mfma_f32_16x16x32_bf16 v[112:115], a[136:139], a[72:75], v[112:115]\n" \
"ds_read_b128 a[128:131], v5 offset:16384\n" \
"v_mfma_f32_16x16x32_bf16 v[112:115], a[140:143], a[76:79], v[112:115]\n" \
"ds_read_b128 a[132:135], v5 offset:17408\n" \
"v_exp_f32 v80, v80\n" \
"v_mfma_f32_16x16x32_bf16 v[120:123], a[136:139], a[88:91], v[120:123]\n" \
"v_exp_f32 v81, v81\n" \
"v_mfma_f32_16x16x32_bf16 v[120:123], a[140:143], a[92:95], v[120:123]\n" \
"s_waitcnt lgkmcnt(5)\n" \
"ds_read_b128 a[136:139], v5 offset:18432\n" \
"v_exp_f32 v82, v82\n" \
"v_mfma_f32_16x16x32_bf16 v[124:127], a[144:147], a[80:83], v[176:179]\n" \
"buffer_load_dwordx4 v6, s[40:43], s16 offen offset:1024 sc1 lds\n" \
"v_exp_f32 v83, v83\n" \
"v_mfma_f32_16x16x32_bf16 v[124:127], a[148:151], a[84:87], v[124:127]\n" \
"s_waitcnt lgkmcnt(3)\n" \
"ds_read_b128 a[140:143], v5 offset:19456\n" \
"v_exp_f32 v84, v84\n" \
"v_mfma_f32_16x16x32_bf16 v[124:127], a[152:155], a[88:91], v[124:127]\n" \
"ds_read_b128 a[144:147], v5 offset:20480\n" \
"v_exp_f32 v85, v85\n" \
"v_mfma_f32_16x16x32_bf16 v[124:127], a[156:159], a[92:95], v[124:127]\n" \
"ds_read_b128 a[148:151], v5 offset:21504\n" \
"v_exp_f32 v86, v86\n" \
"ds_read_b128 a[152:155], v5 offset:22528\n" \
"v_exp_f32 v87, v87\n" \
"v_cvt_pk_bf16_f32 v136, v80, v81\n" \
"buffer_load_dwordx4 v6, s[40:43], s16 offen offset:2048 sc1 lds\n" \
"v_exp_f32 v88, v88\n" \
"ds_read_b128 a[156:159], v5 offset:23552\n" \
"v_exp_f32 v89, v89\n" \
"v_cvt_pk_bf16_f32 v137, v82, v83\n" \
"v_exp_f32 v90, v90\n" \
"v_exp_f32 v91, v91\n" \
"v_exp_f32 v92, v92\n" \
"v_exp_f32 v93, v93\n" \
"v_exp_f32 v94, v94\n" \
"v_exp_f32 v95, v95\n" \
"v_cvt_pk_bf16_f32 v138, v84, v85\n" \
"v_cvt_pk_bf16_f32 v139, v86, v87\n" \
"v_cvt_pk_bf16_f32 v140, v88, v89\n" \
"v_cvt_pk_bf16_f32 v141, v90, v91\n" \
"buffer_load_dwordx4 v6, s[40:43], s16 offen offset:3072 sc1 lds\n" \
"v_cvt_pk_bf16_f32 v142, v92, v93\n" \
"v_cvt_pk_bf16_f32 v143, v94, v95\n" \
"v_mfma_f32_16x16x32_bf16 v[160:163], v[180:183], v[128:131], v[160:163]\n" \
"v_exp_f32 v112, v112\n" \
"v_mfma_f32_16x16x32_bf16 v[164:167], v[180:183], v[132:135], v[164:167]\n" \
"v_exp_f32 v113, v113\n" \
"v_mfma_f32_16x16x32_bf16 v[168:171], v[180:183], v[136:139], v[168:171]\n" \
"v_exp_f32 v114, v114\n" \
"v_mfma_f32_16x16x32_bf16 v[172:175], v[180:183], v[140:143], v[172:175]\n" \
"s_waitcnt lgkmcnt(6)\n" \
"v_exp_f32 v115, v115\n" \
"v_mfma_f32_16x16x32_bf16 v[192:195], a[128:131], v[128:131], v[192:195]\n" \
"v_exp_f32 v120, v120\n" \
"v_mfma_f32_16x16x32_bf16 v[196:199], a[132:135], v[128:131], v[196:199]\n" \
"v_exp_f32 v121, v121\n" \
"v_mfma_f32_16x16x32_bf16 v[224:227], a[128:131], v[132:135], v[224:227]\n" \
"v_exp_f32 v122, v122\n" \
"v_mfma_f32_16x16x32_bf16 v[228:231], a[132:135], v[132:135], v[228:231]\n" \
"v_exp_f32 v123, v123\n" \
"v_mfma_f32_16x16x32_bf16 a[0:3], a[128:131], v[136:139], a[0:3]\n" \
"v_exp_f32 v124, v124\n" \
"v_mfma_f32_16x16x32_bf16 a[4:7], a[132:135], v[136:139], a[4:7]\n" \
"v_exp_f32 v125, v125\n" \
"v_mfma_f32_16x16x32_bf16 a[32:35], a[128:131], v[140:143], a[32:35]\n" \
"v_exp_f32 v126, v126\n" \
"v_mfma_f32_16x16x32_bf16 a[36:39], a[132:135], v[140:143], a[36:39]\n" \
"s_waitcnt lgkmcnt(4)\n" \
"v_exp_f32 v127, v127\n" \
"v_mfma_f32_16x16x32_bf16 v[200:203], a[136:139], v[128:131], v[200:203]\n" \
"v_cvt_pk_bf16_f32 v152, v112, v113\n" \
"v_cvt_pk_bf16_f32 v153, v114, v115\n" \
"v_mfma_f32_16x16x32_bf16 v[204:207], a[140:143], v[128:131], v[204:207]\n" \
"v_mov_b32 v154, 0\n" \
"v_mov_b32 v155, 0\n" \
"v_mfma_f32_16x16x32_bf16 v[232:235], a[136:139], v[132:135], v[232:235]\n" \
"v_cvt_pk_bf16_f32 v156, v120, v121\n" \
"ds_read_b128 v[36:39], v21 offset:1024\n" \
"v_mfma_f32_16x16x32_bf16 v[236:239], a[140:143], v[132:135], v[236:239]\n" \
"v_cvt_pk_bf16_f32 v157, v122, v123\n" \
"v_cvt_pk_bf16_f32 v158, v124, v125\n" \
"v_mfma_f32_16x16x32_bf16 a[8:11], a[136:139], v[136:139], a[8:11]\n" \
"v_cvt_pk_bf16_f32 v159, v126, v127\n" \
"ds_read_b128 v[40:43], v21 offset:2048\n" \
"v_mfma_f32_16x16x32_bf16 a[12:15], a[140:143], v[136:139], a[12:15]\n" \
"ds_read_b128 v[44:47], v21 offset:3072\n" \
"v_subrev_u32 v23, 0x1f800000, v160\n" \
"v_mfma_f32_16x16x32_bf16 a[40:43], a[136:139], v[140:143], a[40:43]\n" \
"ds_read_b128 v[48:51], v21 offset:4096\n" \
"v_subrev_u32 v27, 0x1f800000, v164\n" \
"v_mfma_f32_16x16x32_bf16 a[44:47], a[140:143], v[140:143], a[44:47]\n" \
"s_waitcnt lgkmcnt(6)\n" \
"ds_read_b128 v[52:55], v21 offset:5120\n" \
"v_max_u32 v23, v23, v27\n" \
"v_mfma_f32_16x16x32_bf16 v[208:211], a[144:147], v[128:131], v[208:211]\n" \
"ds_read_b128 v[56:59], v21 offset:6144\n" \
"v_cmp_le_u32 vcc, 0x40000000, v23\n" \
"v_mfma_f32_16x16x32_bf16 v[212:215], a[148:151], v[128:131], v[212:215]\n" \
"s_or_b64 s[54:55], s[54:55], vcc\n" \
"ds_read_b128 v[60:63], v21 offset:7168\n" \
"s_nop 0\n" \
"v_mfma_f32_16x16x32_bf16 v[240:243], a[144:147], v[132:135], v[240:243]\n" \
"ds_read_b128 a[64:67], v21 offset:16384\n" \
"v_mfma_f32_16x16x32_bf16 v[244:247], a[148:151], v[132:135], v[244:247]\n" \
"s_add_u32 s17, s17, s19\n" \
"s_add_u32 m0, s49, 49152\n" \
"ds_read_b128 a[68:71], v21 offset:17408\n" \
"v_mfma_f32_16x16x32_bf16 a[16:19], a[144:147], v[136:139], a[16:19]\n" \
"s_waitcnt vmcnt(12) lgkmcnt(15)\n" \
"ds_read_b128 a[72:75], v21 offset:18432\n" \
"v_mfma_f32_16x16x32_bf16 a[20:23], a[148:151], v[136:139], a[20:23]\n" \
"s_barrier\n" \
"ds_read_b128 a[76:79], v21 offset:19456\n" \
"v_mfma_f32_16x16x32_bf16 a[48:51], a[144:147], v[140:143], a[48:51]\n" \
"buffer_load_dwordx4 v6, s[40:43], s17 offen offset:0 sc1 lds\n" \
"v_mfma_f32_16x16x32_bf16 a[52:55], a[148:151], v[140:143], a[52:55]\n" \
"s_waitcnt lgkmcnt(11)\n" \
"ds_read_b128 a[128:131], v5 offset:49152\n" \
"ds_read_b128 a[80:83], v21 offset:20480\n" \
"ds_read_b128 a[84:87], v21 offset:21504\n" \
"ds_read_b128 a[88:91], v21 offset:22528\n" \
"ds_read_b128 a[92:95], v21 offset:23552\n" \
"v_mfma_f32_16x16x32_bf16 v[216:219], a[152:155], v[128:131], v[216:219]\n" \
"buffer_load_dwordx4 v6, s[40:43], s17 offen offset:1024 sc1 lds\n" \
"v_mfma_f32_16x16x32_bf16 v[220:223], a[156:159], v[128:131], v[220:223]\n" \
"ds_read_b128 a[132:135], v5 offset:50176\n" \
"v_mfma_f32_16x16x32_bf16 v[248:251], a[152:155], v[132:135], v[248:251]\n" \
"buffer_load_dwordx4 v6, s[40:43], s17 offen offset:2048 sc1 lds\n" \
"v_mfma_f32_16x16x32_bf16 v[252:255], a[156:159], v[132:135], v[252:255]\n" \
"ds_read_b128 a[136:139], v5 offset:51200\n" \
"v_mfma_f32_16x16x32_bf16 a[24:27], a[152:155], v[136:139], a[24:27]\n" \
"buffer_load_dwordx4 v6, s[40:43], s17 offen offset:3072 sc1 lds\n" \
"v_mfma_f32_16x16x32_bf16 a[28:31], a[156:159], v[136:139], a[28:31]\n" \
"ds_read_b128 a[140:143], v5 offset:52224\n" \
"v_mfma_f32_16x16x32_bf16 a[56:59], a[152:155], v[140:143], a[56:59]\n" \
"ds_read_b128 a[144:147], v5 offset:53248\n" \
"v_mfma_f32_16x16x32_bf16 a[60:63], a[156:159], v[140:143], a[60:63]\n" \
"ds_read_b128 a[148:151], v5 offset:54272\n" \
"v_rcp_f32 v22, v160\n" \
"v_rcp_f32 v26, v164\n" \
"LS3_%=:\n" \
"v_mfma_f32_16x16x32_bf16 v[168:171], v[180:183], v[152:155], v[168:171]\n" \
"s_add_u32 s16, s16, s9\n" \
"ds_read_b128 a[152:155], v5 offset:55296\n" \
"s_add_u32 m0, s48, 0\n" \
"v_mfma_f32_16x16x32_bf16 v[172:175], v[180:183], v[156:159], v[172:175]\n" \
"s_waitcnt lgkmcnt(5)\n" \
"ds_read_b128 a[156:159], v5 offset:56320\n" \
"v_mfma_f32_16x16x32_bf16 a[0:3], a[128:131], v[152:155], a[0:3]\n" \
"buffer_load_dwordx4 v6, s[40:43], s16 offen offset:0 sc1 lds\n" \
"v_mfma_f32_16x16x32_bf16 a[4:7], a[132:135], v[152:155], a[4:7]\n" \
"v_mul_f32 v192, v22, v192\nv_mul_f32 v193, v22, v193\n" \
"v_mfma_f32_16x16x32_bf16 a[32:35], a[128:131], v[156:159], a[32:35]\n" \
"v_mul_f32 v194, v22, v194\nv_mul_f32 v195, v22, v195\n" \
"v_mfma_f32_16x16x32_bf16 a[36:39], a[132:135], v[156:159], a[36:39]\n" \
"s_waitcnt lgkmcnt(4)\n" \
"ds_read_b128 a[128:131], v8 offset:32768\n" \
"v_mfma_f32_16x16x32_bf16 a[8:11], a[136:139], v[152:155], a[8:11]\n" \
"buffer_load_dwordx4 v6, s[40:43], s16 offen offset:1024 sc1 lds\n" \
"v_mfma_f32_16x16x32_bf16 a[12:15], a[140:143], v[152:155], a[12:15]\n" \
"ds_read_b128 a[132:135], v8 offset:33792\n" \
"v_mfma_f32_16x16x32_bf16 a[40:43], a[136:139], v[156:159], a[40:43]\n" \
"v_mul_f32 v196, v22, v196\nv_mul_f32 v197, v22, v197\n" \
"v_mfma_f32_16x16x32_bf16 a[44:47], a[140:143], v[156:159], a[44:47]\n" \
"s_waitcnt lgkmcnt(4)\n" \
"ds_read_b128 a[136:139], v8 offset:34816\n" \
"v_accvgpr_read_b32 v80, a0\n" \
"v_mfma_f32_16x16x32_bf16 a[16:19], a[144:147], v[152:155], a[16:19]\n" \
"ds_read_b128 a[140:143], v8 offset:35840\n" \
"v_accvgpr_read_b32 v81, a1\n" \
"v_mfma_f32_16x16x32_bf16 a[20:23], a[148:151], v[152:155], a[20:23]\n" \
"v_mul_f32 v198, v22, v198\nv_mul_f32 v199, v22, v199\n" \
"v_mfma_f32_16x16x32_bf16 a[48:51], a[144:147], v[156:159], a[48:51]\n" \
"buffer_load_dwordx4 v6, s[40:43], s16 offen offset:2048 sc1 lds\n" \
"v_mfma_f32_16x16x32_bf16 a[52:55], a[148:151], v[156:159], a[52:55]\n" \
"s_waitcnt lgkmcnt(4)\n" \
"ds_read_b128 a[144:147], v8 offset:36864\n" \
"v_cvt_pk_bf16_f32 v192, v192, v193\n" \
"v_mfma_f32_16x16x32_bf16 a[24:27], a[152:155], v[152:155], a[24:27]\n" \
"ds_read_b128 a[148:151], v8 offset:37888\n" \
"v_cvt_pk_bf16_f32 v193, v194, v195\n" \
"v_mfma_f32_16x16x32_bf16 a[28:31], a[156:159], v[152:155], a[28:31]\n" \
"v_cvt_pk_bf16_f32 v194, v196, v197\n" \
"v_cvt_pk_bf16_f32 v195, v198, v199\n" \
"v_mfma_f32_16x16x32_bf16 a[56:59], a[152:155], v[156:159], a[56:59]\n" \
"buffer_store_dwordx4 v[192:195], v4, s[36:39], s50 offen offset:0 sc1\n" \
"v_mfma_f32_16x16x32_bf16 a[60:63], a[156:159], v[156:159], a[60:63]\n" \
"ds_read_b128 a[152:155], v8 offset:38912\n" \
"v_accvgpr_read_b32 v82, a2\n" \
"buffer_load_dwordx4 v6, s[40:43], s16 offen offset:3072 sc1 lds\n" \
"v_mul_f32 v224, v26, v224\nv_mul_f32 v225, v26, v225\n" \
"ds_read_b128 a[156:159], v8 offset:39936\n" \
"v_pk_mul_f32 v[226:227], v[226:227], v[26:27] op_sel_hi:[1,0]\n" \
"v_pk_mul_f32 v[228:229], v[228:229], v[26:27] op_sel_hi:[1,0]\n" \
"v_pk_mul_f32 v[230:231], v[230:231], v[26:27] op_sel_hi:[1,0]\n" \
"v_cvt_pk_bf16_f32 v224, v224, v225\n" \
"v_cvt_pk_bf16_f32 v225, v226, v227\n" \
"v_cvt_pk_bf16_f32 v226, v228, v229\n" \
"v_cvt_pk_bf16_f32 v227, v230, v231\n" \
"buffer_store_dwordx4 v[224:227], v4, s[36:39], s50 offen offset:1024 sc1\n" \
"v_pk_mul_f32 v[200:201], v[200:201], v[22:23] op_sel_hi:[1,0]\n" \
"v_pk_mul_f32 v[202:203], v[202:203], v[22:23] op_sel_hi:[1,0]\n" \
"v_pk_mul_f32 v[204:205], v[204:205], v[22:23] op_sel_hi:[1,0]\n" \
"v_pk_mul_f32 v[206:207], v[206:207], v[22:23] op_sel_hi:[1,0]\n" \
"v_cvt_pk_bf16_f32 v200, v200, v201\n" \
"v_cvt_pk_bf16_f32 v201, v202, v203\n" \
"v_cvt_pk_bf16_f32 v202, v204, v205\n" \
"v_cvt_pk_bf16_f32 v203, v206, v207\n" \
"v_pk_mul_f32 v[232:233], v[232:233], v[26:27] op_sel_hi:[1,0]\n" \
"v_pk_mul_f32 v[234:235], v[234:235], v[26:27] op_sel_hi:[1,0]\n" \
"v_pk_mul_f32 v[236:237], v[236:237], v[26:27] op_sel_hi:[1,0]\n" \
"v_pk_mul_f32 v[238:239], v[238:239], v[26:27] op_sel_hi:[1,0]\n" \
"v_cvt_pk_bf16_f32 v232, v232, v233\n" \
"v_cvt_pk_bf16_f32 v233, v234, v235\n" \
"buffer_store_dwordx4 v[200:203], v4, s[36:39], s50 offen offset:2048 sc1\n" \
"v_cvt_pk_bf16_f32 v234, v236, v237\n" \
"v_cvt_pk_bf16_f32 v235, v238, v239\n" \
"v_pk_mul_f32 v[208:209], v[208:209], v[22:23] op_sel_hi:[1,0]\n" \
"v_pk_mul_f32 v[210:211], v[210:211], v[22:23] op_sel_hi:[1,0]\n" \
"v_pk_mul_f32 v[212:213], v[212:213], v[22:23] op_sel_hi:[1,0]\n" \
"v_pk_mul_f32 v[214:215], v[214:215], v[22:23] op_sel_hi:[1,0]\n" \
"v_cvt_pk_bf16_f32 v208, v208, v209\n" \
"v_cvt_pk_bf16_f32 v209, v210, v211\n" \
"v_cvt_pk_bf16_f32 v210, v212, v213\n" \
"v_cvt_pk_bf16_f32 v211, v214, v215\n" \
"v_pk_mul_f32 v[240:241], v[240:241], v[26:27] op_sel_hi:[1,0]\n" \
"v_pk_mul_f32 v[242:243], v[242:243], v[26:27] op_sel_hi:[1,0]\n" \
"v_pk_mul_f32 v[244:245], v[244:245], v[26:27] op_sel_hi:[1,0]\n" \
"v_pk_mul_f32 v[246:247], v[246:247], v[26:27] op_sel_hi:[1,0]\n" \
"buffer_store_dwordx4 v[232:235], v4, s[36:39], s50 offen offset:3072 sc1\n" \
"v_cvt_pk_bf16_f32 v240, v240, v241\n" \
"v_cvt_pk_bf16_f32 v241, v242, v243\n" \
"v_cvt_pk_bf16_f32 v242, v244, v245\n" \
"v_cvt_pk_bf16_f32 v243, v246, v247\n" \
"v_pk_mul_f32 v[216:217], v[216:217], v[22:23] op_sel_hi:[1,0]\n" \
"v_pk_mul_f32 v[218:219], v[218:219], v[22:23] op_sel_hi:[1,0]\n" \
"v_pk_mul_f32 v[220:221], v[220:221], v[22:23] op_sel_hi:[1,0]\n" \
"v_pk_mul_f32 v[222:223], v[222:223], v[22:23] op_sel_hi:[1,0]\n" \
"v_cvt_pk_bf16_f32 v216, v216, v217\n" \
"v_cvt_pk_bf16_f32 v217, v218, v219\n" \
"v_cvt_pk_bf16_f32 v218, v220, v221\n" \
"v_cvt_pk_bf16_f32 v219, v222, v223\n" \
"v_pk_mul_f32 v[248:249], v[248:249], v[26:27] op_sel_hi:[1,0]\n" \
"v_pk_mul_f32 v[250:251], v[250:251], v[26:27] op_sel_hi:[1,0]\n" \
"s_waitcnt lgkmcnt(6)\n" \
"buffer_store_dwordx4 v[208:211], v7, s[36:39], s50 offen offset:0 sc1\n" \
"v_pk_mul_f32 v[252:253], v[252:253], v[26:27] op_sel_hi:[1,0]\n" \
"v_mfma_f32_16x16x32_bf16 v[64:67], a[128:131], v[32:35], v[176:179]\n" \
"v_mul_f32 v254, v26, v254\nv_mul_f32 v255, v26, v255\n" \
"v_mfma_f32_16x16x32_bf16 v[64:67], a[132:135], v[36:39], v[64:67]\n" \
"v_cvt_pk_bf16_f32 v248, v248, v249\n" \
"v_cvt_pk_bf16_f32 v249, v250, v251\n" \
"v_mfma_f32_16x16x32_bf16 v[72:75], a[128:131], v[48:51], 0\n" \
"s_add_u32 s63, s50, 262144\n" \
"v_cvt_pk_bf16_f32 v250, v252, v253\n" \
"v_cvt_pk_bf16_f32 v251, v254, v255\n" \
"v_mfma_f32_16x16x32_bf16 v[72:75], a[132:135], v[52:55], v[72:75]\n" \
"s_waitcnt lgkmcnt(4)\n" \
"v_subrev_u32 v23, 0x1f800000, v168\n" \
"v_mfma_f32_16x16x32_bf16 v[64:67], a[136:139], v[40:43], v[64:67]\n" \
"buffer_store_dwordx4 v[240:243], v7, s[36:39], s50 offen offset:1024 sc1\n" \
"v_mfma_f32_16x16x32_bf16 v[64:67], a[140:143], v[44:47], v[64:67]\n" \
"v_subrev_u32 v27, 0x1f800000, v172\n" \
"v_max_u32 v23, v23, v27\n" \
"v_mfma_f32_16x16x32_bf16 v[72:75], a[136:139], v[56:59], v[72:75]\n" \
"v_cmp_le_u32 vcc, 0x40000000, v23\n" \
"s_nop 0\n" \
"v_mfma_f32_16x16x32_bf16 v[72:75], a[140:143], v[60:63], v[72:75]\n" \
"s_waitcnt lgkmcnt(2)\n" \
"s_or_b64 s[54:55], s[54:55], vcc\n" \
"v_accvgpr_read_b32 v83, a3\n" \
"v_mfma_f32_16x16x32_bf16 v[76:79], a[144:147], v[48:51], v[176:179]\n" \
"buffer_store_dwordx4 v[216:219], v7, s[36:39], s50 offen offset:2048 sc1\n" \
"v_mfma_f32_16x16x32_bf16 v[76:79], a[148:151], v[52:55], v[76:79]\n" \
"s_waitcnt lgkmcnt(0)\n" \
"v_rcp_f32 v188, v168\n" \
"v_mfma_f32_16x16x32_bf16 v[76:79], a[152:155], v[56:59], v[76:79]\n" \
"v_rcp_f32 v24, v172\n" \
"v_mfma_f32_16x16x32_bf16 v[76:79], a[156:159], v[60:63], v[76:79]\n" \
"v_accvgpr_read_b32 v84, a4\n" \
"v_accvgpr_read_b32 v85, a5\n" \
"buffer_store_dwordx4 v[248:251], v7, s[36:39], s50 offen offset:3072 sc1\n" \
"v_accvgpr_read_b32 v86, a6\n" \
"v_accvgpr_read_b32 v87, a7\n" \
"v_pk_mul_f32 v[80:81], v[80:81], v[188:189] op_sel_hi:[1,0]\n" \
"v_pk_mul_f32 v[82:83], v[82:83], v[188:189] op_sel_hi:[1,0]\n" \
"v_pk_mul_f32 v[84:85], v[84:85], v[188:189] op_sel_hi:[1,0]\n" \
"v_pk_mul_f32 v[86:87], v[86:87], v[188:189] op_sel_hi:[1,0]\n" \
"v_exp_f32 v64, v64\n" \
"v_exp_f32 v65, v65\n" \
"v_exp_f32 v66, v66\n" \
"v_exp_f32 v67, v67\n" \
"v_exp_f32 v72, v72\n" \
"v_exp_f32 v73, v73\n" \
"v_exp_f32 v74, v74\n" \
"v_exp_f32 v75, v75\n" \
"v_exp_f32 v76, v76\n" \
"v_exp_f32 v77, v77\n" \
"v_exp_f32 v78, v78\n" \
"v_exp_f32 v79, v79\n" \
"v_cvt_pk_bf16_f32 v128, v64, v65\n" \
"v_cvt_pk_bf16_f32 v129, v66, v67\n" \
"v_mov_b32 v130, 0\n" \
"v_mov_b32 v131, 0\n" \
"s_add_u32 s17, s17, s19\n" \
"s_add_u32 m0, s48, 16384\n" \
"v_cvt_pk_bf16_f32 v132, v72, v73\n" \
"v_cvt_pk_bf16_f32 v133, v74, v75\n" \
"s_waitcnt vmcnt(20) lgkmcnt(0)\n" \
"v_cvt_pk_bf16_f32 v134, v76, v77\n" \
"v_cvt_pk_bf16_f32 v135, v78, v79\n" \
"v_cvt_pk_bf16_f32 v80, v80, v81\n" \
"s_barrier\n" \
"v_xor_b32 v5, 0x12000, v5\n" \
"v_cvt_pk_bf16_f32 v81, v82, v83\n" \
"v_cvt_pk_bf16_f32 v82, v84, v85\n" \
"buffer_load_dwordx4 v6, s[40:43], s17 offen offset:0 sc1 lds\n" \
"v_cvt_pk_bf16_f32 v83, v86, v87\n" \
"v_accvgpr_read_b32 v88, a32\n" \
"ds_read_b128 a[128:131], v5 offset:0\n" \
"v_accvgpr_read_b32 v89, a33\n" \
"v_accvgpr_read_b32 v90, a34\n" \
"v_accvgpr_read_b32 v91, a35\n" \
"buffer_load_dwordx4 v6, s[40:43], s17 offen offset:1024 sc1 lds\n" \
"v_accvgpr_read_b32 v92, a36\n" \
"v_accvgpr_read_b32 v93, a37\n" \
"ds_read_b128 a[132:135], v5 offset:1024\n" \
"v_accvgpr_read_b32 v94, a38\n" \
"v_accvgpr_read_b32 v95, a39\n" \
"v_accvgpr_read_b32 v136, a8\n" \
"buffer_load_dwordx4 v6, s[40:43], s17 offen offset:2048 sc1 lds\n" \
"v_pk_mul_f32 v[88:89], v[88:89], v[24:25] op_sel_hi:[1,0]\n" \
"v_pk_mul_f32 v[90:91], v[90:91], v[24:25] op_sel_hi:[1,0]\n" \
"ds_read_b128 a[136:139], v5 offset:2048\n" \
"v_pk_mul_f32 v[92:93], v[92:93], v[24:25] op_sel_hi:[1,0]\n" \
"v_pk_mul_f32 v[94:95], v[94:95], v[24:25] op_sel_hi:[1,0]\n" \
"v_cvt_pk_bf16_f32 v88, v88, v89\n" \
"s_xor_b32 s48, s48, 0x10000\n" \
"buffer_load_dwordx4 v6, s[40:43], s17 offen offset:3072 sc1 lds\n" \
"s_xor_b32 s49, s49, 0x10000\n" \
"s_add_u32 s44, s44, 1\n" \
"ds_read_b128 a[140:143], v5 offset:3072\n" \
"v_cvt_pk_bf16_f32 v89, v90, v91\n" \
"v_cvt_pk_bf16_f32 v90, v92, v93\n" \
"buffer_store_dwordx4 v[80:83], v4, s[36:39], s63 offen offset:0 sc1\n" \
"v_cvt_pk_bf16_f32 v91, v94, v95\n" \
"v_accvgpr_read_b32 v137, a9\n" \
"ds_read_b128 a[144:147], v5 offset:4096\n" \
"v_accvgpr_read_b32 v138, a10\n" \
"v_accvgpr_read_b32 v139, a11\n" \
"v_accvgpr_read_b32 v140, a12\n" \
"ds_read_b128 a[148:151], v5 offset:5120\n" \
"v_accvgpr_read_b32 v141, a13\n" \
"v_accvgpr_read_b32 v142, a14\n" \
"v_accvgpr_read_b32 v143, a15\n" \
"ds_read_b128 a[152:155], v5 offset:6144\n" \
"v_pk_mul_f32 v[136:137], v[136:137], v[188:189] op_sel_hi:[1,0]\n" \
"v_pk_mul_f32 v[138:139], v[138:139], v[188:189] op_sel_hi:[1,0]\n" \
"v_pk_mul_f32 v[140:141], v[140:141], v[188:189] op_sel_hi:[1,0]\n" \
"s_cmp_eq_u32 s44, 16\n" \
"s_cbranch_scc1 LAD32_%=\n" \
"s_cmp_eq_u32 s44, 15\ns_cselect_b32 s16, s56, s16\ns_cselect_b32 s9, s57, s9\ns_cselect_b32 s17, s58, s17\ns_cselect_b32 s19, s59, s19\n" \
"buffer_store_dwordx4 v[88:91], v4, s[36:39], s63 offen offset:1024 sc1\n" \
"v_mul_f32 v142, v188, v142\nv_mul_f32 v143, v188, v143\n" \
"s_waitcnt lgkmcnt(5)\n" \
"ds_read_b128 a[156:159], v5 offset:7168\n" \
"s_add_u32 s16, s16, s9\n" \
"s_add_u32 m0, s49, 32768\n" \
"v_mfma_f32_16x16x32_bf16 v[96:99], a[128:131], v[32:35], 0\n" \
"buffer_load_dwordx4 v6, s[40:43], s16 offen offset:0 sc1 lds\n" \
"v_mfma_f32_16x16x32_bf16 v[96:99], a[132:135], v[36:39], v[96:99]\n" \
"v_cvt_pk_bf16_f32 v136, v136, v137\n" \
"v_cvt_pk_bf16_f32 v137, v138, v139\n" \
"v_mfma_f32_16x16x32_bf16 v[104:107], a[128:131], v[48:51], 0\n" \
"v_cvt_pk_bf16_f32 v138, v140, v141\n" \
"v_cvt_pk_bf16_f32 v139, v142, v143\n" \
"v_mfma_f32_16x16x32_bf16 v[104:107], a[132:135], v[52:55], v[104:107]\n" \
"buffer_store_dwordx4 v[136:139], v4, s[36:39], s63 offen offset:2048 sc1\n" \
"v_mfma_f32_16x16x32_bf16 v[112:115], a[128:131], a[64:67], v[176:179]\n" \
"v_accvgpr_read_b32 v80, a40\n" \
"v_accvgpr_read_b32 v81, a41\n" \
"v_mfma_f32_16x16x32_bf16 v[112:115], a[132:135], a[68:71], v[112:115]\n" \
"buffer_load_dwordx4 v6, s[40:43], s16 offen offset:1024 sc1 lds\n" \
"v_mfma_f32_16x16x32_bf16 v[120:123], a[128:131], a[80:83], 0\n" \
"v_accvgpr_read_b32 v82, a42\n" \
"v_accvgpr_read_b32 v83, a43\n" \
"v_mfma_f32_16x16x32_bf16 v[120:123], a[132:135], a[84:87], v[120:123]\n" \
"s_waitcnt lgkmcnt(4)\n" \
"ds_read_b128 a[128:131], v5 offset:16384\n" \
"v_accvgpr_read_b32 v84, a44\n" \
"v_mfma_f32_16x16x32_bf16 v[96:99], a[136:139], v[40:43], v[96:99]\n" \
"ds_read_b128 a[132:135], v5 offset:17408\n" \
"v_accvgpr_read_b32 v85, a45\n" \
"v_mfma_f32_16x16x32_bf16 v[96:99], a[140:143], v[44:47], v[96:99]\n" \
"v_accvgpr_read_b32 v86, a46\n" \
"v_accvgpr_read_b32 v87, a47\n" \
"v_mfma_f32_16x16x32_bf16 v[104:107], a[136:139], v[56:59], v[104:107]\n" \
"buffer_load_dwordx4 v6, s[40:43], s16 offen offset:2048 sc1 lds\n" \
"v_mfma_f32_16x16x32_bf16 v[104:107], a[140:143], v[60:63], v[104:107]\n" \
"v_mul_f32 v80, v24, v80\nv_mul_f32 v81, v24, v81\n" \
"v_mfma_f32_16x16x32_bf16 v[112:115], a[136:139], a[72:75], v[112:115]\n" \
"v_mul_f32 v82, v24, v82\nv_mul_f32 v83, v24, v83\n" \
"v_mfma_f32_16x16x32_bf16 v[112:115], a[140:143], a[76:79], v[112:115]\n" \
"v_mul_f32 v84, v24, v84\nv_mul_f32 v85, v24, v85\n" \
"v_mfma_f32_16x16x32_bf16 v[120:123], a[136:139], a[88:91], v[120:123]\n" \
"v_mul_f32 v86, v24, v86\nv_mul_f32 v87, v24, v87\n" \
"v_mfma_f32_16x16x32_bf16 v[120:123], a[140:143], a[92:95], v[120:123]\n" \
"s_waitcnt lgkmcnt(4)\n" \
"ds_read_b128 a[136:139], v5 offset:18432\n" \
"v_cvt_pk_bf16_f32 v80, v80, v81\n" \
"v_mfma_f32_16x16x32_bf16 v[100:103], a[144:147], v[32:35], 0\n" \
"buffer_load_dwordx4 v6, s[40:43], s16 offen offset:3072 sc1 lds\n" \
"v_mfma_f32_16x16x32_bf16 v[100:103], a[148:151], v[36:39], v[100:103]\n" \
"ds_read_b128 a[140:143], v5 offset:19456\n" \
"v_cvt_pk_bf16_f32 v81, v82, v83\n" \
"v_mfma_f32_16x16x32_bf16 v[108:111], a[144:147], v[48:51], 0\n" \
"v_cvt_pk_bf16_f32 v82, v84, v85\n" \
"v_cvt_pk_bf16_f32 v83, v86, v87\n" \
"v_mfma_f32_16x16x32_bf16 v[108:111], a[148:151], v[52:55], v[108:111]\n" \
"buffer_store_dwordx4 v[80:83], v4, s[36:39], s63 offen offset:3072 sc1\n" \
"v_mfma_f32_16x16x32_bf16 v[124:127], a[144:147], a[80:83], v[176:179]\n" \
"v_accvgpr_read_b32 v88, a16\n" \
"v_accvgpr_read_b32 v89, a17\n" \
"v_mfma_f32_16x16x32_bf16 v[124:127], a[148:151], a[84:87], v[124:127]\n" \
"s_waitcnt lgkmcnt(4)\n" \
"ds_read_b128 a[144:147], v5 offset:20480\n" \
"v_accvgpr_read_b32 v90, a18\n" \
"v_mfma_f32_16x16x32_bf16 v[100:103], a[152:155], v[40:43], v[100:103]\n" \
"ds_read_b128 a[148:151], v5 offset:21504\n" \
"v_accvgpr_read_b32 v91, a19\n" \
"v_mfma_f32_16x16x32_bf16 v[100:103], a[156:159], v[44:47], v[100:103]\n" \
"v_accvgpr_read_b32 v92, a20\n" \
"v_accvgpr_read_b32 v93, a21\n" \
"v_mfma_f32_16x16x32_bf16 v[108:111], a[152:155], v[56:59], v[108:111]\n" \
"v_accvgpr_read_b32 v94, a22\n" \
"v_accvgpr_read_b32 v95, a23\n" \
"v_mfma_f32_16x16x32_bf16 v[108:111], a[156:159], v[60:63], v[108:111]\n" \
"v_mul_f32 v88, v188, v88\nv_mul_f32 v89, v188, v89\n" \
"v_mfma_f32_16x16x32_bf16 v[124:127], a[152:155], a[88:91], v[124:127]\n" \
"v_mul_f32 v90, v188, v90\nv_mul_f32 v91, v188, v91\n" \
"v_mfma_f32_16x16x32_bf16 v[124:127], a[156:159], a[92:95], v[124:127]\n" \
"ds_read_b128 a[152:155], v5 offset:22528\n" \
"v_accvgpr_read_b32 v136, a48\n" \
"v_mfma_f32_16x16x32_bf16 v[160:163], v[180:183], v[128:131], 0\n" \
"ds_read_b128 a[156:159], v5 offset:23552\n" \
"v_accvgpr_read_b32 v137, a49\n" \
"v_mfma_f32_16x16x32_bf16 v[164:167], v[180:183], v[132:135], 0\n" \
"s_waitcnt lgkmcnt(6)\n" \
"v_mul_f32 v92, v188, v92\nv_mul_f32 v93, v188, v93\n" \
"v_mfma_f32_16x16x32_bf16 v[192:195], a[128:131], v[128:131], 0\n" \
"v_mul_f32 v94, v188, v94\nv_mul_f32 v95, v188, v95\n" \
"v_mfma_f32_16x16x32_bf16 v[196:199], a[132:135], v[128:131], 0\n" \
"v_exp_f32 v96, v96\n" \
"v_mfma_f32_16x16x32_bf16 v[224:227], a[128:131], v[132:135], 0\n" \
"v_exp_f32 v97, v97\n" \
"v_mfma_f32_16x16x32_bf16 v[228:231], a[132:135], v[132:135], 0\n" \
"s_waitcnt lgkmcnt(4)\n" \
"v_exp_f32 v98, v98\n" \
"v_mfma_f32_16x16x32_bf16 v[200:203], a[136:139], v[128:131], 0\n" \
"v_exp_f32 v99, v99\n" \
"v_mfma_f32_16x16x32_bf16 v[204:207], a[140:143], v[128:131], 0\n" \
"v_exp_f32 v100, v100\n" \
"v_mfma_f32_16x16x32_bf16 v[232:235], a[136:139], v[132:135], 0\n" \
"v_exp_f32 v101, v101\n" \
"v_mfma_f32_16x16x32_bf16 v[236:239], a[140:143], v[132:135], 0\n" \
"s_waitcnt lgkmcnt(2)\n" \
"v_exp_f32 v102, v102\n" \
"v_mfma_f32_16x16x32_bf16 v[208:211], a[144:147], v[128:131], 0\n" \
"v_exp_f32 v103, v103\n" \
"v_mfma_f32_16x16x32_bf16 v[212:215], a[148:151], v[128:131], 0\n" \
"v_exp_f32 v104, v104\n" \
"v_mfma_f32_16x16x32_bf16 v[240:243], a[144:147], v[132:135], 0\n" \
"v_exp_f32 v105, v105\n" \
"v_mfma_f32_16x16x32_bf16 v[244:247], a[148:151], v[132:135], 0\n" \
"s_waitcnt lgkmcnt(0)\n" \
"v_exp_f32 v106, v106\n" \
"v_mfma_f32_16x16x32_bf16 v[216:219], a[152:155], v[128:131], 0\n" \
"v_exp_f32 v107, v107\n" \
"v_mfma_f32_16x16x32_bf16 v[220:223], a[156:159], v[128:131], 0\n" \
"v_exp_f32 v108, v108\n" \
"v_mfma_f32_16x16x32_bf16 v[248:251], a[152:155], v[132:135], 0\n" \
"v_exp_f32 v109, v109\n" \
"v_mfma_f32_16x16x32_bf16 v[252:255], a[156:159], v[132:135], 0\n" \
"v_exp_f32 v110, v110\n" \
"v_exp_f32 v111, v111\n" \
"v_cvt_pk_bf16_f32 v144, v96, v97\n" \
"v_cvt_pk_bf16_f32 v145, v98, v99\n" \
"v_cvt_pk_bf16_f32 v146, v100, v101\n" \
"v_cvt_pk_bf16_f32 v147, v102, v103\n" \
"v_cvt_pk_bf16_f32 v148, v104, v105\n" \
"v_cvt_pk_bf16_f32 v149, v106, v107\n" \
"v_cvt_pk_bf16_f32 v150, v108, v109\n" \
"v_cvt_pk_bf16_f32 v151, v110, v111\n" \
"v_cvt_pk_bf16_f32 v88, v88, v89\n" \
"v_cvt_pk_bf16_f32 v89, v90, v91\n" \
"v_cvt_pk_bf16_f32 v90, v92, v93\n" \
"v_cvt_pk_bf16_f32 v91, v94, v95\n" \
"v_accvgpr_read_b32 v138, a50\n" \
"v_accvgpr_read_b32 v139, a51\n" \
"buffer_store_dwordx4 v[88:91], v7, s[36:39], s63 offen offset:0 sc1\n" \
"v_accvgpr_read_b32 v140, a52\n" \
"v_accvgpr_read_b32 v141, a53\n" \
"v_accvgpr_read_b32 v142, a54\n" \
"v_accvgpr_read_b32 v143, a55\n" \
"v_accvgpr_read_b32 v80, a24\n" \
"v_accvgpr_read_b32 v81, a25\n" \
"v_pk_mul_f32 v[136:137], v[136:137], v[24:25] op_sel_hi:[1,0]\n" \
"v_pk_mul_f32 v[138:139], v[138:139], v[24:25] op_sel_hi:[1,0]\n" \
"v_pk_mul_f32 v[140:141], v[140:141], v[24:25] op_sel_hi:[1,0]\n" \
"v_pk_mul_f32 v[142:143], v[142:143], v[24:25] op_sel_hi:[1,0]\n" \
"s_add_u32 s17, s17, s19\n" \
"s_add_u32 m0, s49, 49152\n" \
"v_cvt_pk_bf16_f32 v136, v136, v137\n" \
"v_cvt_pk_bf16_f32 v137, v138, v139\n" \
"s_waitcnt vmcnt(25) lgkmcnt(8)\n" \
"v_cvt_pk_bf16_f32 v138, v140, v141\n" \
"v_cvt_pk_bf16_f32 v139, v142, v143\n" \
"v_accvgpr_read_b32 v82, a26\n" \
"s_barrier\n" \
"buffer_store_dwordx4 v[136:139], v7, s[36:39], s63 offen offset:1024 sc1\n" \
"v_accvgpr_read_b32 v83, a27\n" \
"ds_read_b128 a[128:131], v5 offset:32768\n" \
"v_accvgpr_read_b32 v84, a28\n" \
"v_accvgpr_read_b32 v85, a29\n" \
"v_accvgpr_read_b32 v86, a30\n" \
"buffer_load_dwordx4 v6, s[40:43], s17 offen offset:0 sc1 lds\n" \
"v_accvgpr_read_b32 v87, a31\n" \
"v_accvgpr_read_b32 v88, a56\n" \
"ds_read_b128 a[132:135], v5 offset:33792\n" \
"v_pk_mul_f32 v[80:81], v[80:81], v[188:189] op_sel_hi:[1,0]\n" \
"v_pk_mul_f32 v[82:83], v[82:83], v[188:189] op_sel_hi:[1,0]\n" \
"v_pk_mul_f32 v[84:85], v[84:85], v[188:189] op_sel_hi:[1,0]\n" \
"buffer_load_dwordx4 v6, s[40:43], s17 offen offset:1024 sc1 lds\n" \
"v_pk_mul_f32 v[86:87], v[86:87], v[188:189] op_sel_hi:[1,0]\n" \
"v_cvt_pk_bf16_f32 v80, v80, v81\n" \
"ds_read_b128 a[136:139], v5 offset:34816\n" \
"v_cvt_pk_bf16_f32 v81, v82, v83\n" \
"v_cvt_pk_bf16_f32 v82, v84, v85\n" \
"v_cvt_pk_bf16_f32 v83, v86, v87\n" \
"buffer_load_dwordx4 v6, s[40:43], s17 offen offset:2048 sc1 lds\n" \
"v_accvgpr_read_b32 v89, a57\n" \
"v_accvgpr_read_b32 v90, a58\n" \
"ds_read_b128 a[140:143], v5 offset:35840\n" \
"v_accvgpr_read_b32 v91, a59\n" \
"v_accvgpr_read_b32 v92, a60\n" \
"v_accvgpr_read_b32 v93, a61\n" \
"buffer_load_dwordx4 v6, s[40:43], s17 offen offset:3072 sc1 lds\n" \
"v_accvgpr_read_b32 v94, a62\n" \
"v_accvgpr_read_b32 v95, a63\n" \
"ds_read_b128 a[144:147], v5 offset:36864\n" \
"v_pk_mul_f32 v[88:89], v[88:89], v[24:25] op_sel_hi:[1,0]\n" \
"v_pk_mul_f32 v[90:91], v[90:91], v[24:25] op_sel_hi:[1,0]\n" \
"v_pk_mul_f32 v[92:93], v[92:93], v[24:25] op_sel_hi:[1,0]\n" \
"buffer_store_dwordx4 v[80:83], v7, s[36:39], s63 offen offset:2048 sc1\n" \
"v_pk_mul_f32 v[94:95], v[94:95], v[24:25] op_sel_hi:[1,0]\n" \
"v_cvt_pk_bf16_f32 v88, v88, v89\n" \
"ds_read_b128 a[148:151], v5 offset:37888\n" \
"v_cvt_pk_bf16_f32 v89, v90, v91\n" \
"v_cvt_pk_bf16_f32 v90, v92, v93\n" \
"v_cvt_pk_bf16_f32 v91, v94, v95\n" \
"v_mov_b32 v116, 0xff800000\n" \
"v_mov_b32 v117, 0xff800000\n" \
"v_mov_b32 v118, 0xff800000\n" \
"v_mov_b32 v119, 0xff800000\n" \
"buffer_store_dwordx4 v[88:91], v7, s[36:39], s63 offen offset:3072 sc1\n" \
"s_waitcnt lgkmcnt(4)\n" \
"v_mfma_f32_16x16x32_bf16 v[64:67], a[128:131], v[32:35], 0\n" \
"LH22_%=:\ns_cbranch_execz LHR1_%=\n" \
"v_exp_f32 v112, v112\n" \
"v_mfma_f32_16x16x32_bf16 v[64:67], a[132:135], v[36:39], v[64:67]\n" \
"s_add_u32 s16, s16, s9\n" \
"v_mfma_f32_16x16x32_bf16 v[72:75], a[128:131], v[48:51], 0\n" \
"ds_read_b128 a[152:155], v5 offset:38912\n" \
"v_mfma_f32_16x16x32_bf16 v[72:75], a[132:135], v[52:55], v[72:75]\n" \
"s_add_u32 m0, s48, 0\n" \
"v_exp_f32 v113, v113\n" \
"v_mfma_f32_16x16x32_bf16 v[80:83], a[128:131], a[64:67], 0\n" \
"buffer_load_dwordx4 v6, s[40:43], s16 offen offset:0 sc1 lds\n" \
"v_mfma_f32_16x16x32_bf16 v[80:83], a[132:135], a[68:71], v[80:83]\n" \
"v_exp_f32 v114, v114\n" \
"v_mfma_f32_16x16x32_bf16 v[88:91], a[128:131], a[80:83], 0\n" \
"ds_read_b128 a[156:159], v5 offset:39936\n" \
"v_cvt_pk_bf16_f32 v152, v112, v113\n" \
"v_mfma_f32_16x16x32_bf16 v[88:91], a[132:135], a[84:87], v[88:91]\n" \
"s_waitcnt lgkmcnt(4)\n" \
"v_exp_f32 v115, v115\n" \
"v_mfma_f32_16x16x32_bf16 v[64:67], a[136:139], v[40:43], v[64:67]\n" \
"v_exp_f32 v116, v116\n" \
"v_mfma_f32_16x16x32_bf16 v[64:67], a[140:143], v[44:47], v[64:67]\n" \
"s_xor_b32 s49, s49, 0x10000\n" \
"v_cvt_pk_bf16_f32 v153, v114, v115\n" \
"v_mfma_f32_16x16x32_bf16 v[72:75], a[136:139], v[56:59], v[72:75]\n" \
"ds_read_b128 a[128:131], v5 offset:49152\n" \
"v_mfma_f32_16x16x32_bf16 v[72:75], a[140:143], v[60:63], v[72:75]\n" \
"v_exp_f32 v117, v117\n" \
"v_mfma_f32_16x16x32_bf16 v[80:83], a[136:139], a[72:75], v[80:83]\n" \
"buffer_load_dwordx4 v6, s[40:43], s16 offen offset:1024 sc1 lds\n" \
"v_mfma_f32_16x16x32_bf16 v[80:83], a[140:143], a[76:79], v[80:83]\n" \
"v_exp_f32 v118, v118\n" \
"v_mfma_f32_16x16x32_bf16 v[88:91], a[136:139], a[88:91], v[88:91]\n" \
"ds_read_b128 a[132:135], v5 offset:50176\n" \
"v_cvt_pk_bf16_f32 v154, v116, v117\n" \
"v_mfma_f32_16x16x32_bf16 v[88:91], a[140:143], a[92:95], v[88:91]\n" \
"s_waitcnt lgkmcnt(4)\n" \
"v_exp_f32 v119, v119\n" \
"v_mfma_f32_16x16x32_bf16 v[68:71], a[144:147], v[32:35], 0\n" \
"v_exp_f32 v120, v120\n" \
"v_mfma_f32_16x16x32_bf16 v[68:71], a[148:151], v[36:39], v[68:71]\n" \
"v_cvt_pk_bf16_f32 v155, v118, v119\n" \
"v_mfma_f32_16x16x32_bf16 v[76:79], a[144:147], v[48:51], 0\n" \
"ds_read_b128 a[136:139], v5 offset:51200\n" \
"v_mfma_f32_16x16x32_bf16 v[76:79], a[148:151], v[52:55], v[76:79]\n" \
"v_exp_f32 v121, v121\n" \
"v_mfma_f32_16x16x32_bf16 v[84:87], a[144:147], a[64:67], 0\n" \
"buffer_load_dwordx4 v6, s[40:43], s16 offen offset:2048 sc1 lds\n" \
"v_mfma_f32_16x16x32_bf16 v[84:87], a[148:151], a[68:71], v[84:87]\n" \
"v_exp_f32 v122, v122\n" \
"v_mfma_f32_16x16x32_bf16 v[92:95], a[144:147], a[80:83], 0\n" \
"ds_read_b128 a[140:143], v5 offset:52224\n" \
"v_cvt_pk_bf16_f32 v156, v120, v121\n" \
"v_mfma_f32_16x16x32_bf16 v[92:95], a[148:151], a[84:87], v[92:95]\n" \
"s_waitcnt lgkmcnt(4)\n" \
"v_exp_f32 v123, v123\n" \
"v_mfma_f32_16x16x32_bf16 v[68:71], a[152:155], v[40:43], v[68:71]\n" \
"v_exp_f32 v124, v124\n" \
"v_mfma_f32_16x16x32_bf16 v[68:71], a[156:159], v[44:47], v[68:71]\n" \
"v_cvt_pk_bf16_f32 v157, v122, v123\n" \
"v_mfma_f32_16x16x32_bf16 v[76:79], a[152:155], v[56:59], v[76:79]\n" \
"ds_read_b128 a[144:147], v5 offset:53248\n" \
"v_mfma_f32_16x16x32_bf16 v[76:79], a[156:159], v[60:63], v[76:79]\n" \
"v_exp_f32 v125, v125\n" \
"v_mfma_f32_16x16x32_bf16 v[84:87], a[152:155], a[72:75], v[84:87]\n" \
"buffer_load_dwordx4 v6, s[40:43], s16 offen offset:3072 sc1 lds\n" \
"v_mfma_f32_16x16x32_bf16 v[84:87], a[156:159], a[76:79], v[84:87]\n" \
"v_exp_f32 v126, v126\n" \
"v_mfma_f32_16x16x32_bf16 v[92:95], a[152:155], a[88:91], v[92:95]\n" \
"ds_read_b128 a[148:151], v5 offset:54272\n" \
"v_cvt_pk_bf16_f32 v158, v124, v125\n" \
"v_mfma_f32_16x16x32_bf16 v[92:95], a[156:159], a[92:95], v[92:95]\n" \
"s_waitcnt lgkmcnt(4)\n" \
"v_exp_f32 v127, v127\n" \
"v_mfma_f32_16x16x32_bf16 v[160:163], v[180:183], v[144:147], v[160:163]\n" \
"v_cvt_pk_bf16_f32 v159, v126, v127\n" \
"v_mfma_f32_16x16x32_bf16 v[164:167], v[180:183], v[148:151], v[164:167]\n" \
"v_mfma_f32_16x16x32_bf16 v[168:171], v[180:183], v[152:155], 0\n" \
"v_exp_f32 v64, v64\n" \
"v_mfma_f32_16x16x32_bf16 v[172:175], v[180:183], v[156:159], 0\n" \
"v_mfma_f32_16x16x32_bf16 v[192:195], a[128:131], v[144:147], v[192:195]\n" \
"v_exp_f32 v65, v65\n" \
"v_mfma_f32_16x16x32_bf16 v[196:199], a[132:135], v[144:147], v[196:199]\n" \
"v_cvt_pk_bf16_f32 v128, v64, v65\n" \
"v_mfma_f32_16x16x32_bf16 v[224:227], a[128:131], v[148:151], v[224:227]\n" \
"ds_read_b128 a[152:155], v5 offset:55296\n" \
"v_mfma_f32_16x16x32_bf16 v[228:231], a[132:135], v[148:151], v[228:231]\n" \
"v_exp_f32 v66, v66\n" \
"v_mfma_f32_16x16x32_bf16 a[0:3], a[128:131], v[152:155], 0\n" \
"v_exp_f32 v67, v67\n" \
"v_mfma_f32_16x16x32_bf16 a[4:7], a[132:135], v[152:155], 0\n" \
"v_cvt_pk_bf16_f32 v129, v66, v67\n" \
"v_mfma_f32_16x16x32_bf16 a[32:35], a[128:131], v[156:159], 0\n" \
"ds_read_b128 a[156:159], v5 offset:56320\n" \
"v_mfma_f32_16x16x32_bf16 a[36:39], a[132:135], v[156:159], 0\n" \
"s_waitcnt vmcnt(20) lgkmcnt(4)\n" \
"v_exp_f32 v68, v68\n" \
"v_mfma_f32_16x16x32_bf16 v[200:203], a[136:139], v[144:147], v[200:203]\n" \
"s_barrier\n" \
"v_xor_b32 v5, 0x10000, v5\n" \
"v_mfma_f32_16x16x32_bf16 v[204:207], a[140:143], v[144:147], v[204:207]\n" \
"s_add_u32 s17, s17, s19\n" \
"v_exp_f32 v69, v69\n" \
"v_mfma_f32_16x16x32_bf16 v[232:235], a[136:139], v[148:151], v[232:235]\n" \
"ds_read_b128 a[128:131], v5 offset:0\n" \
"v_cvt_pk_bf16_f32 v130, v68, v69\n" \
"v_mfma_f32_16x16x32_bf16 v[236:239], a[140:143], v[148:151], v[236:239]\n" \
"s_add_u32 m0, s48, 16384\n" \
"v_exp_f32 v70, v70\n" \
"v_mfma_f32_16x16x32_bf16 a[8:11], a[136:139], v[152:155], 0\n" \
"buffer_load_dwordx4 v6, s[40:43], s17 offen offset:0 sc1 lds\n" \
"v_mfma_f32_16x16x32_bf16 a[12:15], a[140:143], v[152:155], 0\n" \
"s_waitcnt vmcnt(5)\n" \
"s_cmp_lg_u64 s[54:55], 0\ns_cbranch_scc1 LFBA_%=\nLFBAR_%=:\n" \
"v_mfma_f32_16x16x32_bf16 a[40:43], a[136:139], v[156:159], 0\n" \
"ds_read_b128 a[132:135], v5 offset:1024\n" \
"v_mfma_f32_16x16x32_bf16 a[44:47], a[140:143], v[156:159], 0\n" \
"s_waitcnt lgkmcnt(4)\n" \
"v_exp_f32 v71, v71\n" \
"v_mfma_f32_16x16x32_bf16 v[208:211], a[144:147], v[144:147], v[208:211]\n" \
"v_exp_f32 v72, v72\n" \
"v_mfma_f32_16x16x32_bf16 v[212:215], a[148:151], v[144:147], v[212:215]\n" \
"v_exp_f32 v73, v73\n" \
"v_mfma_f32_16x16x32_bf16 v[240:243], a[144:147], v[148:151], v[240:243]\n" \
"ds_read_b128 a[136:139], v5 offset:2048\n" \
"v_cvt_pk_bf16_f32 v131, v70, v71\n" \
"v_mfma_f32_16x16x32_bf16 v[244:247], a[148:151], v[148:151], v[244:247]\n" \
"v_exp_f32 v74, v74\n" \
"v_mfma_f32_16x16x32_bf16 a[16:19], a[144:147], v[152:155], 0\n" \
"buffer_load_dwordx4 v6, s[40:43], s17 offen offset:1024 sc1 lds\n" \
"v_mfma_f32_16x16x32_bf16 a[20:23], a[148:151], v[152:155], 0\n" \
"v_exp_f32 v75, v75\n" \
"v_mfma_f32_16x16x32_bf16 a[48:51], a[144:147], v[156:159], 0\n" \
"ds_read_b128 a[140:143], v5 offset:3072\n" \
"v_cvt_pk_bf16_f32 v132, v72, v73\n" \
"v_mfma_f32_16x16x32_bf16 a[52:55], a[148:151], v[156:159], 0\n" \
"s_waitcnt lgkmcnt(4)\n" \
"v_exp_f32 v76, v76\n" \
"v_mfma_f32_16x16x32_bf16 v[216:219], a[152:155], v[144:147], v[216:219]\n" \
"buffer_load_dwordx4 v6, s[40:43], s17 offen offset:2048 sc1 lds\n" \
"v_mfma_f32_16x16x32_bf16 v[220:223], a[156:159], v[144:147], v[220:223]\n" \
"s_add_u32 s44, s44, 1\n" \
"v_exp_f32 v77, v77\n" \
"v_mfma_f32_16x16x32_bf16 v[248:251], a[152:155], v[148:151], v[248:251]\n" \
"ds_read_b128 a[144:147], v5 offset:4096\n" \
"v_cvt_pk_bf16_f32 v133, v74, v75\n" \
"v_mfma_f32_16x16x32_bf16 v[252:255], a[156:159], v[148:151], v[252:255]\n" \
"v_exp_f32 v78, v78\n" \
"v_mfma_f32_16x16x32_bf16 a[24:27], a[152:155], v[152:155], 0\n" \
"buffer_load_dwordx4 v6, s[40:43], s17 offen offset:3072 sc1 lds\n" \
"v_mfma_f32_16x16x32_bf16 a[28:31], a[156:159], v[152:155], 0\n" \
"s_xor_b32 s48, s48, 0x10000\n" \
"v_exp_f32 v79, v79\n" \
"v_mfma_f32_16x16x32_bf16 a[56:59], a[152:155], v[156:159], 0\n" \
"ds_read_b128 a[148:151], v5 offset:5120\n" \
"v_cvt_pk_bf16_f32 v134, v76, v77\n" \
"v_mfma_f32_16x16x32_bf16 a[60:63], a[156:159], v[156:159], 0\n" \
"s_waitcnt lgkmcnt(4)\n" \
"v_cvt_pk_bf16_f32 v135, v78, v79\n" \
"buffer_store_dwordx2 v[10:11], v9, s[36:39], s52 offen sc1\n" \
"s_mov_b32 s46, s24\n" \
"s_branch LBOT_%=\n" \
"LAD32_%=:\n" \
"v_mov_b32 v96, 0\n" \
"v_mov_b32 v97, 0\n" \
"v_mov_b32 v98, 0\n" \
"v_mov_b32 v99, 0\n" \
"buffer_store_dwordx4 v[88:91], v4, s[36:39], s63 offen offset:1024 sc1\n" \
"v_pk_mul_f32 v[142:143], v[142:143], v[188:189] op_sel_hi:[1,0]\n" \
"v_cvt_pk_bf16_f32 v136, v136, v137\n" \
"v_cvt_pk_bf16_f32 v137, v138, v139\n" \
"v_cvt_pk_bf16_f32 v138, v140, v141\n" \
"v_cvt_pk_bf16_f32 v139, v142, v143\n" \
"v_accvgpr_read_b32 v80, a40\n" \
"buffer_store_dwordx4 v[136:139], v4, s[36:39], s63 offen offset:2048 sc1\n" \
"v_accvgpr_read_b32 v81, a41\n" \
"v_accvgpr_read_b32 v82, a42\n" \
"v_accvgpr_read_b32 v83, a43\n" \
"v_accvgpr_read_b32 v84, a44\n" \
"v_accvgpr_read_b32 v85, a45\n" \
"v_accvgpr_read_b32 v86, a46\n" \
"v_accvgpr_read_b32 v87, a47\n" \
"v_accvgpr_read_b32 v88, a16\n" \
"v_accvgpr_read_b32 v89, a17\n" \
"v_accvgpr_read_b32 v90, a18\n" \
"v_pk_mul_f32 v[80:81], v[80:81], v[24:25] op_sel_hi:[1,0]\n" \
"v_pk_mul_f32 v[82:83], v[82:83], v[24:25] op_sel_hi:[1,0]\n" \
"v_pk_mul_f32 v[84:85], v[84:85], v[24:25] op_sel_hi:[1,0]\n" \
"v_pk_mul_f32 v[86:87], v[86:87], v[24:25] op_sel_hi:[1,0]\n" \
"v_cvt_pk_bf16_f32 v80, v80, v81\n" \
"v_cvt_pk_bf16_f32 v81, v82, v83\n" \
"v_cvt_pk_bf16_f32 v82, v84, v85\n" \
"v_cvt_pk_bf16_f32 v83, v86, v87\n" \
"buffer_store_dwordx4 v[80:83], v4, s[36:39], s63 offen offset:3072 sc1\n" \
"v_accvgpr_read_b32 v91, a19\n" \
"v_accvgpr_read_b32 v92, a20\n" \
"v_accvgpr_read_b32 v93, a21\n" \
"v_accvgpr_read_b32 v94, a22\n" \
"v_accvgpr_read_b32 v95, a23\n" \
"v_accvgpr_read_b32 v136, a48\n" \
"v_pk_mul_f32 v[88:89], v[88:89], v[188:189] op_sel_hi:[1,0]\n" \
"v_pk_mul_f32 v[90:91], v[90:91], v[188:189] op_sel_hi:[1,0]\n" \
"v_pk_mul_f32 v[92:93], v[92:93], v[188:189] op_sel_hi:[1,0]\n" \
"v_pk_mul_f32 v[94:95], v[94:95], v[188:189] op_sel_hi:[1,0]\n" \
"v_cvt_pk_bf16_f32 v88, v88, v89\n" \
"v_cvt_pk_bf16_f32 v89, v90, v91\n" \
"v_cvt_pk_bf16_f32 v90, v92, v93\n" \
"v_cvt_pk_bf16_f32 v91, v94, v95\n" \
"buffer_store_dwordx4 v[88:91], v7, s[36:39], s63 offen offset:0 sc1\n" \
"v_accvgpr_read_b32 v137, a49\n" \
"v_accvgpr_read_b32 v138, a50\n" \
"v_accvgpr_read_b32 v139, a51\n" \
"v_accvgpr_read_b32 v140, a52\n" \
"v_accvgpr_read_b32 v141, a53\n" \
"v_accvgpr_read_b32 v142, a54\n" \
"v_accvgpr_read_b32 v143, a55\n" \
"v_accvgpr_read_b32 v80, a24\n" \
"v_accvgpr_read_b32 v81, a25\n" \
"v_accvgpr_read_b32 v82, a26\n" \
"v_pk_mul_f32 v[136:137], v[136:137], v[24:25] op_sel_hi:[1,0]\n" \
"v_pk_mul_f32 v[138:139], v[138:139], v[24:25] op_sel_hi:[1,0]\n" \
"v_pk_mul_f32 v[140:141], v[140:141], v[24:25] op_sel_hi:[1,0]\n" \
"v_pk_mul_f32 v[142:143], v[142:143], v[24:25] op_sel_hi:[1,0]\n" \
"v_cvt_pk_bf16_f32 v136, v136, v137\n" \
"v_cvt_pk_bf16_f32 v137, v138, v139\n" \
"v_cvt_pk_bf16_f32 v138, v140, v141\n" \
"v_cvt_pk_bf16_f32 v139, v142, v143\n" \
"buffer_store_dwordx4 v[136:139], v7, s[36:39], s63 offen offset:1024 sc1\n" \
"v_accvgpr_read_b32 v83, a27\n" \
"v_accvgpr_read_b32 v84, a28\n" \
"v_accvgpr_read_b32 v85, a29\n" \
"v_accvgpr_read_b32 v86, a30\n" \
"v_accvgpr_read_b32 v87, a31\n" \
"v_accvgpr_read_b32 v88, a56\n" \
"v_pk_mul_f32 v[80:81], v[80:81], v[188:189] op_sel_hi:[1,0]\n" \
"v_pk_mul_f32 v[82:83], v[82:83], v[188:189] op_sel_hi:[1,0]\n" \
"v_pk_mul_f32 v[84:85], v[84:85], v[188:189] op_sel_hi:[1,0]\n" \
"v_pk_mul_f32 v[86:87], v[86:87], v[188:189] op_sel_hi:[1,0]\n" \
"v_cvt_pk_bf16_f32 v80, v80, v81\n" \
"v_cvt_pk_bf16_f32 v81, v82, v83\n" \
"v_cvt_pk_bf16_f32 v82, v84, v85\n" \
"v_cvt_pk_bf16_f32 v83, v86, v87\n" \
"buffer_store_dwordx4 v[80:83], v7, s[36:39], s63 offen offset:2048 sc1\n" \
"v_accvgpr_read_b32 v89, a57\n" \
"v_accvgpr_read_b32 v90, a58\n" \
"v_accvgpr_read_b32 v91, a59\n" \
"v_accvgpr_read_b32 v92, a60\n" \
"v_accvgpr_read_b32 v93, a61\n" \
"v_accvgpr_read_b32 v94, a62\n" \
"v_accvgpr_read_b32 v95, a63\n" \
"v_pk_mul_f32 v[88:89], v[88:89], v[24:25] op_sel_hi:[1,0]\n" \
"v_pk_mul_f32 v[90:91], v[90:91], v[24:25] op_sel_hi:[1,0]\n" \
"v_pk_mul_f32 v[92:93], v[92:93], v[24:25] op_sel_hi:[1,0]\n" \
"v_pk_mul_f32 v[94:95], v[94:95], v[24:25] op_sel_hi:[1,0]\n" \
"v_cvt_pk_bf16_f32 v88, v88, v89\n" \
"v_cvt_pk_bf16_f32 v89, v90, v91\n" \
"v_cvt_pk_bf16_f32 v90, v92, v93\n" \
"v_cvt_pk_bf16_f32 v91, v94, v95\n" \
"buffer_store_dwordx4 v[88:91], v7, s[36:39], s63 offen offset:3072 sc1\n" \
"v_mfma_f32_32x32x16_bf16 v[192:207], v[96:99], v[96:99], 0\n" \
"v_mfma_f32_32x32x16_bf16 v[208:223], v[96:99], v[96:99], 0\n" \
"v_mfma_f32_32x32x16_bf16 v[224:239], v[96:99], v[96:99], 0\n" \
"v_mfma_f32_32x32x16_bf16 v[240:255], v[96:99], v[96:99], 0\n" \
"v_mfma_f32_32x32x16_bf16 a[0:15], v[96:99], v[96:99], 0\n" \
"v_mfma_f32_32x32x16_bf16 a[16:31], v[96:99], v[96:99], 0\n" \
"v_mfma_f32_32x32x16_bf16 a[32:47], v[96:99], v[96:99], 0\n" \
"v_mfma_f32_32x32x16_bf16 a[48:63], v[96:99], v[96:99], 0\n" \
"v_mfma_f32_32x32x16_bf16 v[160:175], v[96:99], v[96:99], 0\n" \
"v_mov_b32 v80, 0xff800000\n" \
"v_mov_b32 v81, 0xff800000\n" \
"v_mov_b32 v82, 0xff800000\n" \
"v_mov_b32 v83, 0xff800000\n" \
"v_mov_b32 v84, 0xff800000\n" \
"v_mov_b32 v85, 0xff800000\n" \
"v_mov_b32 v86, 0xff800000\n" \
"v_mov_b32 v87, 0xff800000\n" \
"v_mov_b32 v88, 0xff800000\n" \
"v_mov_b32 v89, 0xff800000\n" \
"v_mov_b32 v90, 0xff800000\n" \
"v_mov_b32 v91, 0xff800000\n" \
"v_mov_b32 v92, 0xff800000\n" \
"v_mov_b32 v93, 0xff800000\n" \
"v_mov_b32 v94, 0xff800000\n" \
"v_mov_b32 v95, 0xff800000\n" \
"s_branch LFIN_%=\n" \
".p2align 6\n" \
"LH23_%=:\ns_cbranch_execz LH27_%=\n" \
"s_nop 0\n" \
"LDISP_%=:\n" \
"s_cmp_eq_u32 s44, 16\n" \
"s_cbranch_scc1 LFIN_%=\n" \
"s_cmp_eq_u32 s44, 15\ns_cselect_b32 s16, s56, s16\ns_cselect_b32 s9, s57, s9\ns_cselect_b32 s17, s58, s17\ns_cselect_b32 s19, s59, s19\n" \
"s_cbranch_scc0 LD14_%=\n" \
"buffer_load_dword v1, v191, s[36:39], 0 offen sc1\n" \
"s_branch LEVEN_%=\n" \
"LD14_%=:\n" \
"buffer_load_dwordx2 v[18:19], v17, s[36:39], 0 offen sc1\n" \
"s_branch LEVEN_%=\n" \
".p2align 6\n" \
"LH24_%=:\ns_cbranch_execz LH28_%=\n" \
"s_nop 0\n" \
"LFIN_%=:\n" \
"s_waitcnt lgkmcnt(6)\n" \
"ds_read_b128 a[128:131], v5 offset:16384\n" \
"ds_read_b128 a[132:135], v5 offset:17408\n" \
"s_mov_b32 s81, 786432\n" \
"ds_read_b128 a[136:139], v5 offset:18432\n" \
"ds_read_b128 a[140:143], v5 offset:19456\n" \
"s_waitcnt vmcnt(8)\n" \
"s_barrier\n" \
"ds_read_b128 a[144:147], v5 offset:20480\n" \
"ds_read_b128 a[148:151], v5 offset:21504\n" \
"v_exp_f32 v80, v80\n" \
"ds_read_b128 a[152:155], v5 offset:22528\n" \
"ds_read_b128 a[156:159], v5 offset:23552\n" \
"v_exp_f32 v81, v81\n" \
"ds_read_b128 v[96:99], v29 offset:32768\n" \
"v_exp_f32 v82, v82\n" \
"v_cvt_pk_bf16_f32 v136, v80, v81\n" \
"v_mfma_f32_16x16x32_bf16 v[160:163], v[180:183], v[128:131], v[160:163]\n" \
"s_add_u32 m0, s79, 32768\n" \
"ds_read_b128 v[100:103], v29 offset:34816\n" \
"v_exp_f32 v83, v83\n" \
"v_mfma_f32_16x16x32_bf16 v[164:167], v[180:183], v[132:135], v[164:167]\n" \
"ds_read_b128 v[104:107], v29 offset:33792\n" \
"s_waitcnt lgkmcnt(9)\n" \
"v_cvt_pk_bf16_f32 v137, v82, v83\n" \
"v_mfma_f32_16x16x32_bf16 v[192:195], a[128:131], v[128:131], v[192:195]\n" \
"ds_read_b128 v[108:111], v29 offset:35840\n" \
"v_exp_f32 v84, v84\n" \
"v_mfma_f32_16x16x32_bf16 v[196:199], a[132:135], v[128:131], v[196:199]\n" \
"ds_read_b128 v[112:115], v29 offset:36864\n" \
"v_exp_f32 v85, v85\n" \
"v_mfma_f32_16x16x32_bf16 v[224:227], a[128:131], v[132:135], v[224:227]\n" \
"ds_read_b128 v[116:119], v29 offset:38912\n" \
"v_exp_f32 v86, v86\n" \
"v_mfma_f32_16x16x32_bf16 v[228:231], a[132:135], v[132:135], v[228:231]\n" \
"ds_read_b128 v[120:123], v29 offset:37888\n" \
"s_waitcnt lgkmcnt(7)\n" \
"v_cvt_pk_bf16_f32 v138, v84, v85\n" \
"v_mfma_f32_16x16x32_bf16 v[200:203], a[136:139], v[128:131], v[200:203]\n" \
"ds_read_b128 v[124:127], v29 offset:39936\n" \
"v_exp_f32 v87, v87\n" \
"v_mfma_f32_16x16x32_bf16 v[204:207], a[140:143], v[128:131], v[204:207]\n" \
"ds_write2_b32 v184, v160, v164 offset1:64\n" \
"v_exp_f32 v88, v88\n" \
"v_mfma_f32_16x16x32_bf16 v[232:235], a[136:139], v[132:135], v[232:235]\n" \
"buffer_load_dwordx4 v12, s[28:31], s81 offen offset:0 lds\n" \
"v_mfma_f32_16x16x32_bf16 v[236:239], a[140:143], v[132:135], v[236:239]\n" \
"s_add_u32 m0, s79, 33664\n" \
"v_exp_f32 v89, v89\n" \
"v_cvt_pk_bf16_f32 v139, v86, v87\n" \
"v_mfma_f32_16x16x32_bf16 v[208:211], a[144:147], v[128:131], v[208:211]\n" \
"v_exp_f32 v90, v90\n" \
"v_cvt_pk_bf16_f32 v140, v88, v89\n" \
"v_mfma_f32_16x16x32_bf16 v[212:215], a[148:151], v[128:131], v[212:215]\n" \
"v_exp_f32 v91, v91\n" \
"v_cvt_pk_bf16_f32 v192, v192, v193\n" \
"v_mfma_f32_16x16x32_bf16 v[240:243], a[144:147], v[132:135], v[240:243]\n" \
"v_exp_f32 v92, v92\n" \
"v_cvt_pk_bf16_f32 v141, v90, v91\n" \
"v_mfma_f32_16x16x32_bf16 v[244:247], a[148:151], v[132:135], v[244:247]\n" \
"v_exp_f32 v93, v93\n" \
"v_cvt_pk_bf16_f32 v193, v194, v195\n" \
"v_mfma_f32_16x16x32_bf16 v[216:219], a[152:155], v[128:131], v[216:219]\n" \
"v_exp_f32 v94, v94\n" \
"v_cvt_pk_bf16_f32 v142, v92, v93\n" \
"v_mfma_f32_16x16x32_bf16 v[220:223], a[156:159], v[128:131], v[220:223]\n" \
"v_exp_f32 v95, v95\n" \
"v_cvt_pk_bf16_f32 v194, v196, v197\n" \
"v_mfma_f32_16x16x32_bf16 v[248:251], a[152:155], v[132:135], v[248:251]\n" \
"v_cvt_pk_bf16_f32 v143, v94, v95\n" \
"v_cvt_pk_bf16_f32 v195, v198, v199\n" \
"v_cvt_pk_bf16_f32 v224, v224, v225\n" \
"v_mfma_f32_16x16x32_bf16 v[252:255], a[156:159], v[132:135], v[252:255]\n" \
"buffer_load_dwordx4 v12, s[28:31], s81 offen offset:128 lds\n" \
"v_mfma_f32_16x16x32_bf16 v[168:171], v[180:183], v[136:139], v[168:171]\n" \
"s_mov_b32 s81, 851968\n" \
"ds_write_b128 v186, v[192:195] offset:0\n" \
"v_cvt_pk_bf16_f32 v225, v226, v227\n" \
"v_cvt_pk_bf16_f32 v226, v228, v229\n" \
"v_mfma_f32_16x16x32_bf16 v[172:175], v[180:183], v[140:143], v[172:175]\n" \
"s_add_u32 m0, s79, 40960\n" \
"v_cvt_pk_bf16_f32 v227, v230, v231\n" \
"v_cvt_pk_bf16_f32 v200, v200, v201\n" \
"v_cvt_pk_bf16_f32 v201, v202, v203\n" \
"v_mfma_f32_16x16x32_bf16 a[0:3], a[128:131], v[136:139], a[0:3]\n" \
"ds_write_b128 v186, v[224:227] offset:4096\n" \
"v_cvt_pk_bf16_f32 v202, v204, v205\n" \
"v_cvt_pk_bf16_f32 v203, v206, v207\n" \
"v_mfma_f32_16x16x32_bf16 a[4:7], a[132:135], v[136:139], a[4:7]\n" \
"buffer_load_dwordx4 v12, s[28:31], s81 offen offset:0 lds\n" \
"v_mfma_f32_16x16x32_bf16 a[32:35], a[128:131], v[140:143], a[32:35]\n" \
"s_add_u32 m0, s79, 41856\n" \
"ds_write_b128 v186, v[200:203] offset:1024\n" \
"v_cvt_pk_bf16_f32 v232, v232, v233\n" \
"v_cvt_pk_bf16_f32 v233, v234, v235\n" \
"v_mfma_f32_16x16x32_bf16 a[36:39], a[132:135], v[140:143], a[36:39]\n" \
"v_cvt_pk_bf16_f32 v234, v236, v237\n" \
"v_cvt_pk_bf16_f32 v235, v238, v239\n" \
"v_cvt_pk_bf16_f32 v208, v208, v209\n" \
"v_mfma_f32_16x16x32_bf16 a[8:11], a[136:139], v[136:139], a[8:11]\n" \
"ds_write_b128 v186, v[232:235] offset:5120\n" \
"v_cvt_pk_bf16_f32 v209, v210, v211\n" \
"v_cvt_pk_bf16_f32 v210, v212, v213\n" \
"v_mfma_f32_16x16x32_bf16 a[12:15], a[140:143], v[136:139], a[12:15]\n" \
"buffer_load_dwordx4 v12, s[28:31], s81 offen offset:128 lds\n" \
"v_mfma_f32_16x16x32_bf16 a[40:43], a[136:139], v[140:143], a[40:43]\n" \
"s_add_u32 s80, s65, 16384\n" \
"v_cvt_pk_bf16_f32 v211, v214, v215\n" \
"v_cvt_pk_bf16_f32 v240, v240, v241\n" \
"v_cvt_pk_bf16_f32 v241, v242, v243\n" \
"v_mfma_f32_16x16x32_bf16 a[44:47], a[140:143], v[140:143], a[44:47]\n" \
"s_add_u32 m0, s22, 49152\n" \
"ds_write_b128 v186, v[208:211] offset:2048\n" \
"v_cvt_pk_bf16_f32 v242, v244, v245\n" \
"v_cvt_pk_bf16_f32 v243, v246, v247\n" \
"v_mfma_f32_16x16x32_bf16 a[16:19], a[144:147], v[136:139], a[16:19]\n" \
"ds_write_b128 v186, v[240:243] offset:6144\n" \
"v_cvt_pk_bf16_f32 v216, v216, v217\n" \
"v_cvt_pk_bf16_f32 v217, v218, v219\n" \
"v_mfma_f32_16x16x32_bf16 a[20:23], a[148:151], v[136:139], a[20:23]\n" \
"buffer_load_dwordx4 v4, s[36:39], s80 offen offset:0 sc1 lds\n" \
"v_mfma_f32_16x16x32_bf16 a[48:51], a[144:147], v[140:143], a[48:51]\n" \
"v_cvt_pk_bf16_f32 v218, v220, v221\n" \
"v_cvt_pk_bf16_f32 v219, v222, v223\n" \
"v_cvt_pk_bf16_f32 v248, v248, v249\n" \
"v_mfma_f32_16x16x32_bf16 a[52:55], a[148:151], v[140:143], a[52:55]\n" \
"ds_write_b128 v186, v[216:219] offset:3072\n" \
"v_cvt_pk_bf16_f32 v249, v250, v251\n" \
"v_cvt_pk_bf16_f32 v250, v252, v253\n" \
"v_mfma_f32_16x16x32_bf16 a[24:27], a[152:155], v[136:139], a[24:27]\n" \
"v_cvt_pk_bf16_f32 v251, v254, v255\n" \
"v_and_b32 v188, 16, v4\n" \
"v_mov_b32 v189, 0x3f80\n" \
"v_mfma_f32_16x16x32_bf16 a[28:31], a[156:159], v[136:139], a[28:31]\n" \
"buffer_load_dwordx4 v4, s[36:39], s80 offen offset:1024 sc1 lds\n" \
"v_mfma_f32_16x16x32_bf16 a[56:59], a[152:155], v[140:143], a[56:59]\n" \
"s_waitcnt lgkmcnt(14)\n" \
"ds_write_b128 v186, v[248:251] offset:7168\n" \
"v_lshlrev_b32 v188, v188, v189\n" \
"v_mov_b32 v22, 0\n" \
"v_mfma_f32_16x16x32_bf16 a[60:63], a[156:159], v[140:143], a[60:63]\n" \
"s_mov_b32 s94, 0x300003\n" \
"v_mov_b32 v23, 0\n" \
"v_mov_b32 v26, 0\n" \
"v_mov_b32 v27, 0\n" \
"v_mfma_f32_16x16x32_bf16 v[32:35], a[160:163], v[96:99], 0\n" \
"s_mov_b32 s95, 0x30000300\n" \
"v_cndmask_b32 v24, 0, v188, s[94:95]\n" \
"s_mov_b32 s94, 0xc0000c\n" \
"v_mfma_f32_16x16x32_bf16 v[32:35], a[168:171], v[100:103], v[32:35]\n" \
"s_waitcnt lgkmcnt(0)\n" \
"s_mov_b32 s95, 0xc0000c00\n" \
"v_cndmask_b32 v25, 0, v188, s[94:95]\n" \
"v_mfma_f32_16x16x32_bf16 v[36:39], a[164:167], v[96:99], 0\n" \
"s_waitcnt lgkmcnt(13)\n" \
"s_barrier\n" \
"ds_read2_b32 v[190:191], v185 offset1:64\n" \
"v_mfma_f32_16x16x32_bf16 v[36:39], a[172:175], v[100:103], v[36:39]\n" \
"buffer_load_dwordx4 v4, s[36:39], s80 offen offset:2048 sc1 lds\n" \
"v_mfma_f32_16x16x32_bf16 v[40:43], a[160:163], v[104:107], 0\n" \
"ds_read_b128 v[96:99], v29 offset:40960\n" \
"v_add_u32 v176, 0xfffe0000, v13\n" \
"v_mfma_f32_16x16x32_bf16 v[40:43], a[168:171], v[108:111], v[40:43]\n" \
"ds_read_b128 v[128:131], v187 offset:0\n" \
"v_add_u32 v177, 0xfffe0000, v14\n" \
"v_mfma_f32_16x16x32_bf16 v[44:47], a[164:167], v[104:107], 0\n" \
"s_waitcnt lgkmcnt(14)\n" \
"ds_read_b128 v[100:103], v29 offset:43008\n" \
"v_add_u32 v178, 0xfffe0000, v15\n" \
"v_mfma_f32_16x16x32_bf16 v[44:47], a[172:175], v[108:111], v[44:47]\n" \
"buffer_load_dwordx4 v4, s[36:39], s80 offen offset:3072 sc1 lds\n" \
"v_mfma_f32_16x16x32_bf16 v[48:51], a[160:163], v[112:115], 0\n" \
"s_mov_b32 s81, 917504\n" \
"ds_read_b128 v[104:107], v29 offset:41984\n" \
"s_add_u32 m0, s79, 81920\n" \
"v_mfma_f32_16x16x32_bf16 v[48:51], a[168:171], v[116:119], v[48:51]\n" \
"ds_read_b128 v[132:135], v187 offset:1024\n" \
"v_add_u32 v179, 0xfffe0000, v16\n" \
"v_mfma_f32_16x16x32_bf16 v[52:55], a[164:167], v[112:115], 0\n" \
"s_waitcnt lgkmcnt(15)\n" \
"ds_read_b128 v[108:111], v29 offset:44032\n" \
"s_waitcnt vmcnt(8)\n" \
"v_mfma_f32_16x16x32_bf16 v[52:55], a[172:175], v[116:119], v[52:55]\n" \
"buffer_load_dwordx4 v12, s[28:31], s81 offen offset:0 lds\n" \
"v_mfma_f32_16x16x32_bf16 v[56:59], a[160:163], v[120:123], 0\n" \
"s_add_u32 m0, s79, 82816\n" \
"ds_read_b128 v[112:115], v29 offset:45056\n" \
"v_add_u32 v180, 0xffff0000, v13\n" \
"v_mfma_f32_16x16x32_bf16 v[56:59], a[168:171], v[124:127], v[56:59]\n" \
"ds_read_b128 v[136:139], v187 offset:2048\n" \
"v_add_u32 v181, 0xffff0000, v14\n" \
"v_mfma_f32_16x16x32_bf16 v[60:63], a[164:167], v[120:123], 0\n" \
"ds_read_b128 v[116:119], v29 offset:47104\n" \
"v_add_u32 v182, 0xffff0000, v15\n" \
"v_mfma_f32_16x16x32_bf16 v[60:63], a[172:175], v[124:127], v[60:63]\n" \
"s_waitcnt vmcnt(17) lgkmcnt(6)\n" \
"s_barrier\n" \
"ds_read_b128 v[140:143], v187 offset:3072\n" \
"v_mfma_f32_16x16x32_bf16 v[64:67], a[160:163], v[96:99], 0\n" \
"ds_read_b128 v[120:123], v29 offset:46080\n" \
"v_add_u32 v183, 0xffff0000, v16\n" \
"v_mfma_f32_16x16x32_bf16 v[64:67], a[168:171], v[100:103], v[64:67]\n" \
"buffer_load_dwordx4 v12, s[28:31], s81 offen offset:128 lds\n" \
"v_mfma_f32_16x16x32_bf16 v[68:71], a[164:167], v[96:99], 0\n" \
"s_waitcnt lgkmcnt(5)\n" \
"ds_read_b128 v[124:127], v29 offset:48128\n" \
"s_mov_b32 s81, 983040\n" \
"v_mfma_f32_16x16x32_bf16 v[68:71], a[172:175], v[100:103], v[68:71]\n" \
"s_add_u32 m0, s79, 90112\n" \
"ds_read_b128 v[144:147], v187 offset:4096\n" \
"s_waitcnt lgkmcnt(13)\n" \
"v_mfma_f32_16x16x32_bf16 v[72:75], a[160:163], v[104:107], 0\n" \
"LH25_%=:\ns_cbranch_execz LH29_%=\n" \
"ds_read_b128 v[96:99], v29 offset:49152\n" \
"v_add_f32 v190, v168, v190\n" \
"v_mfma_f32_16x16x32_bf16 v[72:75], a[168:171], v[108:111], v[72:75]\n" \
"buffer_load_dwordx4 v12, s[28:31], s81 offen offset:0 lds\n" \
"v_mfma_f32_16x16x32_bf16 v[76:79], a[164:167], v[104:107], 0\n" \
"s_add_u32 m0, s79, 91008\n" \
"ds_read_b128 v[100:103], v29 offset:51200\n" \
"v_add_f32 v191, v172, v191\n" \
"v_accvgpr_read_b32 v192, a0\n" \
"v_accvgpr_read_b32 v193, a1\n" \
"v_accvgpr_read_b32 v194, a2\n" \
"v_accvgpr_read_b32 v195, a3\n" \
"v_accvgpr_read_b32 v196, a4\n" \
"v_accvgpr_read_b32 v197, a5\n" \
"v_accvgpr_read_b32 v198, a6\n" \
"v_accvgpr_read_b32 v199, a7\n" \
"v_mfma_f32_16x16x32_bf16 v[76:79], a[172:175], v[108:111], v[76:79]\n" \
"s_waitcnt lgkmcnt(13)\n" \
"ds_read_b128 v[148:151], v187 offset:5120\n" \
"v_subrev_u32 v188, 0x1f800000, v190\n" \
"v_mfma_f32_16x16x32_bf16 v[192:195], v[24:27], v[128:131], v[192:195]\n" \
"s_waitcnt lgkmcnt(7)\n" \
"ds_read_b128 v[152:155], v187 offset:6144\n" \
"v_subrev_u32 v189, 0x1f800000, v191\n" \
"v_mfma_f32_16x16x32_bf16 v[196:199], v[22:25], v[128:131], v[196:199]\n" \
"buffer_load_dwordx4 v12, s[28:31], s81 offen offset:128 lds\n" \
"v_mfma_f32_16x16x32_bf16 v[32:35], a[176:179], v[112:115], v[32:35]\n" \
"s_add_u32 s80, s66, 16384\n" \
"ds_read_b128 v[104:107], v29 offset:50176\n" \
"s_add_u32 m0, s22, 65536\n" \
"v_mfma_f32_16x16x32_bf16 v[32:35], a[184:187], v[116:119], v[32:35]\n" \
"ds_read_b128 v[156:159], v187 offset:7168\n" \
"v_max_u32 v188, v188, v189\n" \
"v_mfma_f32_16x16x32_bf16 v[36:39], a[180:183], v[112:115], v[36:39]\n" \
"s_waitcnt lgkmcnt(7)\n" \
"ds_read_b128 v[108:111], v29 offset:52224\n" \
"v_cmp_le_u32 vcc, 0x40000000, v188\n" \
"v_mfma_f32_16x16x32_bf16 v[36:39], a[188:191], v[116:119], v[36:39]\n" \
"buffer_load_dwordx4 v4, s[36:39], s80 offen offset:0 sc1 lds\n" \
"v_mfma_f32_16x16x32_bf16 v[40:43], a[176:179], v[120:123], v[40:43]\n" \
"s_or_b64 s[4:5], s[4:5], vcc\n" \
"ds_read_b128 v[112:115], v29 offset:53248\n" \
"s_nop 0\n" \
"v_mfma_f32_16x16x32_bf16 v[40:43], a[184:187], v[124:127], v[40:43]\n" \
"v_rcp_f32 v186, v190\n" \
"v_mfma_f32_16x16x32_bf16 v[44:47], a[180:183], v[120:123], v[44:47]\n" \
"ds_read_b128 v[116:119], v29 offset:55296\n" \
"v_accvgpr_read_b32 v200, a8\n" \
"v_mfma_f32_16x16x32_bf16 v[44:47], a[188:191], v[124:127], v[44:47]\n" \
"s_waitcnt vmcnt(9) lgkmcnt(3)\n" \
"s_barrier\n" \
"v_accvgpr_read_b32 v201, a9\n" \
"v_mfma_f32_16x16x32_bf16 v[48:51], a[176:179], v[96:99], v[48:51]\n" \
"ds_read_b128 v[120:123], v29 offset:54272\n" \
"v_accvgpr_read_b32 v202, a10\n" \
"v_mfma_f32_16x16x32_bf16 v[48:51], a[184:187], v[100:103], v[48:51]\n" \
"buffer_load_dwordx4 v4, s[36:39], s80 offen offset:1024 sc1 lds\n" \
"v_mfma_f32_16x16x32_bf16 v[52:55], a[180:183], v[96:99], v[52:55]\n" \
"ds_read_b128 v[124:127], v29 offset:56320\n" \
"v_accvgpr_read_b32 v203, a11\n" \
"v_accvgpr_read_b32 v204, a12\n" \
"v_accvgpr_read_b32 v205, a13\n" \
"v_accvgpr_read_b32 v206, a14\n" \
"v_accvgpr_read_b32 v207, a15\n" \
"v_mfma_f32_16x16x32_bf16 v[52:55], a[188:191], v[100:103], v[52:55]\n" \
"s_waitcnt lgkmcnt(15)\n" \
"ds_read_b64_tr_b16 a[64:65], v176 offset:32768\n" \
"v_accvgpr_read_b32 v208, a16\n" \
"v_mfma_f32_16x16x32_bf16 v[200:203], v[24:27], v[132:135], v[200:203]\n" \
"s_waitcnt lgkmcnt(5)\n" \
"ds_read_b64_tr_b16 a[66:67], v177 offset:32768\n" \
"v_accvgpr_read_b32 v209, a17\n" \
"v_mfma_f32_16x16x32_bf16 v[204:207], v[22:25], v[132:135], v[204:207]\n" \
"buffer_load_dwordx4 v4, s[36:39], s80 offen offset:2048 sc1 lds\n" \
"v_mfma_f32_16x16x32_bf16 v[56:59], a[176:179], v[104:107], v[56:59]\n" \
"ds_read_b128 v[96:99], v29 offset:57344\n" \
"v_accvgpr_read_b32 v210, a18\n" \
"v_mfma_f32_16x16x32_bf16 v[56:59], a[184:187], v[108:111], v[56:59]\n" \
"ds_read_b64_tr_b16 a[68:69], v178 offset:32768\n" \
"v_accvgpr_read_b32 v211, a19\n" \
"v_mfma_f32_16x16x32_bf16 v[60:63], a[180:183], v[104:107], v[60:63]\n" \
"s_waitcnt lgkmcnt(6)\n" \
"ds_read_b128 v[100:103], v29 offset:59392\n" \
"v_accvgpr_read_b32 v212, a20\n" \
"v_mfma_f32_16x16x32_bf16 v[60:63], a[188:191], v[108:111], v[60:63]\n" \
"buffer_load_dwordx4 v4, s[36:39], s80 offen offset:3072 sc1 lds\n" \
"v_mfma_f32_16x16x32_bf16 v[64:67], a[176:179], v[112:115], v[64:67]\n" \
"s_mov_b32 s81, 1048576\n" \
"ds_read_b128 v[104:107], v29 offset:58368\n" \
"s_add_u32 m0, s79, 98304\n" \
"v_mfma_f32_16x16x32_bf16 v[64:67], a[184:187], v[116:119], v[64:67]\n" \
"ds_read_b64_tr_b16 a[70:71], v179 offset:32768\n" \
"v_accvgpr_read_b32 v213, a21\n" \
"v_mfma_f32_16x16x32_bf16 v[68:71], a[180:183], v[112:115], v[68:71]\n" \
"s_waitcnt lgkmcnt(7)\n" \
"ds_read_b128 v[108:111], v29 offset:60416\n" \
"v_accvgpr_read_b32 v214, a22\n" \
"v_mfma_f32_16x16x32_bf16 v[68:71], a[188:191], v[116:119], v[68:71]\n" \
"buffer_load_dwordx4 v12, s[28:31], s81 offen offset:0 lds\n" \
"v_mfma_f32_16x16x32_bf16 v[72:75], a[176:179], v[120:123], v[72:75]\n" \
"s_add_u32 m0, s79, 99200\n" \
"ds_read_b128 v[112:115], v29 offset:61440\n" \
"v_accvgpr_read_b32 v215, a23\n" \
"v_mfma_f32_16x16x32_bf16 v[72:75], a[184:187], v[124:127], v[72:75]\n" \
"ds_read_b64_tr_b16 a[72:73], v176 offset:40960\n" \
"v_accvgpr_read_b32 v216, a24\n" \
"v_mfma_f32_16x16x32_bf16 v[76:79], a[180:183], v[120:123], v[76:79]\n" \
"ds_read_b128 v[116:119], v29 offset:63488\n" \
"v_accvgpr_read_b32 v217, a25\n" \
"v_mfma_f32_16x16x32_bf16 v[76:79], a[188:191], v[124:127], v[76:79]\n" \
"s_waitcnt lgkmcnt(15)\n" \
"ds_read_b64_tr_b16 a[74:75], v177 offset:40960\n" \
"v_accvgpr_read_b32 v218, a26\n" \
"v_mfma_f32_16x16x32_bf16 v[208:211], v[24:27], v[136:139], v[208:211]\n" \
"buffer_load_dwordx4 v12, s[28:31], s81 offen offset:128 lds\n" \
"v_mfma_f32_16x16x32_bf16 v[212:215], v[22:25], v[136:139], v[212:215]\n" \
"s_waitcnt vmcnt(22) lgkmcnt(7)\n" \
"s_barrier\n" \
"s_mov_b32 s81, 1114112\n" \
"v_mfma_f32_16x16x32_bf16 v[32:35], a[192:195], v[96:99], v[32:35]\n" \
"s_add_u32 m0, s79, 106496\n" \
"ds_read_b128 v[120:123], v29 offset:62464\n" \
"v_accvgpr_read_b32 v219, a27\n" \
"v_mfma_f32_16x16x32_bf16 v[32:35], a[200:203], v[100:103], v[32:35]\n" \
"ds_read_b64_tr_b16 a[76:77], v178 offset:40960\n" \
"v_accvgpr_read_b32 v220, a28\n" \
"v_mfma_f32_16x16x32_bf16 v[36:39], a[196:199], v[96:99], v[36:39]\n" \
"s_waitcnt lgkmcnt(6)\n" \
"ds_read_b128 v[124:127], v29 offset:64512\n" \
"v_accvgpr_read_b32 v221, a29\n" \
"v_mfma_f32_16x16x32_bf16 v[36:39], a[204:207], v[100:103], v[36:39]\n" \
"buffer_load_dwordx4 v12, s[28:31], s81 offen offset:0 lds\n" \
"v_mfma_f32_16x16x32_bf16 v[40:43], a[192:195], v[104:107], v[40:43]\n" \
"s_add_u32 m0, s79, 107392\n" \
"ds_read_b128 v[96:99], v28 offset:0\n" \
"v_accvgpr_read_b32 v222, a30\n" \
"v_mfma_f32_16x16x32_bf16 v[40:43], a[200:203], v[108:111], v[40:43]\n" \
"ds_read_b64_tr_b16 a[78:79], v179 offset:40960\n" \
"v_accvgpr_read_b32 v223, a31\n" \
"v_mfma_f32_16x16x32_bf16 v[44:47], a[196:199], v[104:107], v[44:47]\n" \
"s_waitcnt lgkmcnt(6)\n" \
"ds_read_b128 v[100:103], v28 offset:2048\n" \
"v_accvgpr_read_b32 v224, a32\n" \
"v_mfma_f32_16x16x32_bf16 v[44:47], a[204:207], v[108:111], v[44:47]\n" \
"buffer_load_dwordx4 v12, s[28:31], s81 offen offset:128 lds\n" \
"v_mfma_f32_16x16x32_bf16 v[48:51], a[192:195], v[112:115], v[48:51]\n" \
"s_add_u32 s80, s64, 32768\n" \
"ds_read_b128 v[104:107], v28 offset:1024\n" \
"s_add_u32 m0, s22, 131072\n" \
"v_mfma_f32_16x16x32_bf16 v[48:51], a[200:203], v[116:119], v[48:51]\n" \
"v_rcp_f32 v187, v191\n" \
"v_mfma_f32_16x16x32_bf16 v[52:55], a[196:199], v[112:115], v[52:55]\n" \
"ds_read_b128 v[108:111], v28 offset:3072\n" \
"v_bfe_u32 v188, v4, 7, 2\n" \
"v_mfma_f32_16x16x32_bf16 v[52:55], a[204:207], v[116:119], v[52:55]\n" \
"s_waitcnt lgkmcnt(15)\n" \
"s_cmp_lg_u64 s[54:55], 0\ns_cbranch_scc1 LFBC_%=\nLFBCR_%=:\n" \
"v_mfma_f32_16x16x32_bf16 v[216:219], v[24:27], v[140:143], v[216:219]\n" \
"s_waitcnt lgkmcnt(5)\n" \
"v_min_u32 v188, 0, v188\n" \
"v_lshl_add_u32 v17, v188, 8, s10\n" \
"v_mfma_f32_16x16x32_bf16 v[220:223], v[22:25], v[140:143], v[220:223]\n" \
"buffer_store_dwordx2 v[10:11], v9, s[36:39], s52 offen sc1\n" \
"v_mfma_f32_16x16x32_bf16 v[56:59], a[192:195], v[120:123], v[56:59]\n" \
"ds_read_b128 v[112:115], v28 offset:4096\n" \
"v_bfe_u32 v188, v4, 4, 3\n" \
"v_mfma_f32_16x16x32_bf16 v[56:59], a[200:203], v[124:127], v[56:59]\n" \
"buffer_load_dwordx4 v4, s[36:39], s80 offen offset:0 sc1 lds\n" \
"v_mfma_f32_16x16x32_bf16 v[60:63], a[196:199], v[120:123], v[60:63]\n" \
"s_andn2_b32 s94, s88, 0xc0\n" \
"ds_read_b128 v[116:119], v28 offset:6144\n" \
"v_lshl_add_u32 v17, v188, 3, v17\n" \
"v_mfma_f32_16x16x32_bf16 v[60:63], a[204:207], v[124:127], v[60:63]\n" \
"s_waitcnt lgkmcnt(4)\n" \
"s_barrier\n" \
"v_and_b32 v188, 0x30, v4\n" \
"v_mfma_f32_16x16x32_bf16 v[64:67], a[192:195], v[96:99], v[64:67]\n" \
"ds_read_b128 v[120:123], v28 offset:5120\n" \
"v_mul_f32 v192, v186, v192\n" \
"v_mfma_f32_16x16x32_bf16 v[64:67], a[200:203], v[100:103], v[64:67]\n" \
"buffer_load_dwordx4 v4, s[36:39], s80 offen offset:1024 sc1 lds\n" \
"v_mfma_f32_16x16x32_bf16 v[68:71], a[196:199], v[96:99], v[68:71]\n" \
"s_waitcnt lgkmcnt(3)\n" \
"ds_read_b128 v[124:127], v28 offset:7168\n" \
"v_mul_f32 v193, v186, v193\n" \
"v_mfma_f32_16x16x32_bf16 v[68:71], a[204:207], v[100:103], v[68:71]\n" \
"s_mov_b32 exec_lo, 0\nv_lshl_add_u32 v17, v188, 2, s94\ns_mov_b32 exec_lo, -1\n" \
"v_mfma_f32_16x16x32_bf16 v[72:75], a[192:195], v[104:107], v[72:75]\n" \
"s_sub_u32 s96, s72, 109051904\n" \
"ds_read_b128 v[96:99], v28 offset:8192\n" \
"s_lshr_b32 s96, s96, 11\n" \
"v_mfma_f32_16x16x32_bf16 v[72:75], a[200:203], v[108:111], v[72:75]\n" \
"buffer_load_dwordx4 v4, s[36:39], s80 offen offset:2048 sc1 lds\n" \
"v_mfma_f32_16x16x32_bf16 v[76:79], a[196:199], v[104:107], v[76:79]\n" \
"s_mul_i32 s97, s96, 10923\n" \
"ds_read_b128 v[100:103], v28 offset:10240\n" \
"s_lshr_b32 s97, s97, 16\n" \
"v_accvgpr_read_b32 v225, a33\n" \
"v_accvgpr_read_b32 v226, a34\n" \
"v_accvgpr_read_b32 v227, a35\n" \
"v_accvgpr_read_b32 v228, a36\n" \
"v_accvgpr_read_b32 v229, a37\n" \
"v_accvgpr_read_b32 v230, a38\n" \
"v_accvgpr_read_b32 v231, a39\n" \
"v_mfma_f32_16x16x32_bf16 v[76:79], a[204:207], v[108:111], v[76:79]\n" \
"s_waitcnt lgkmcnt(15)\n" \
"s_lshl_b32 s97, s97, 1\n" \
"s_sub_u32 s96, s96, s97\n" \
"v_mfma_f32_16x16x32_bf16 v[224:227], v[24:27], v[144:147], v[224:227]\n" \
"s_waitcnt lgkmcnt(4)\n" \
"s_lshl_b32 s96, s96, 3\n" \
"s_add_u32 s23, s96, 94437376\n" \
"v_mfma_f32_16x16x32_bf16 v[228:231], v[22:25], v[144:147], v[228:231]\n" \
"buffer_load_dwordx4 v4, s[36:39], s80 offen offset:3072 sc1 lds\n" \
"v_mfma_f32_16x16x32_bf16 v[32:35], a[208:211], v[112:115], v[32:35]\n" \
"s_mov_b32 s81, 1179648\n" \
"ds_read_b128 v[104:107], v28 offset:9216\n" \
"s_add_u32 m0, s79, 147456\n" \
"v_mfma_f32_16x16x32_bf16 v[32:35], a[216:219], v[116:119], v[32:35]\n" \
"s_sub_u32 s96, s73, 109051904\n" \
"s_lshr_b32 s96, s96, 11\n" \
"s_mul_i32 s97, s96, 10923\n" \
"v_mfma_f32_16x16x32_bf16 v[36:39], a[212:215], v[112:115], v[36:39]\n" \
"s_waitcnt lgkmcnt(3)\n" \
"ds_read_b128 v[108:111], v28 offset:11264\n" \
"s_lshr_b32 s97, s97, 16\n" \
"v_mfma_f32_16x16x32_bf16 v[36:39], a[220:223], v[116:119], v[36:39]\n" \
"buffer_load_dwordx4 v12, s[28:31], s81 offen offset:0 lds\n" \
"v_mfma_f32_16x16x32_bf16 v[40:43], a[208:211], v[120:123], v[40:43]\n" \
"s_add_u32 m0, s79, 148352\n" \
"ds_read_b128 v[112:115], v28 offset:12288\n" \
"s_lshl_b32 s97, s97, 1\n" \
"v_mfma_f32_16x16x32_bf16 v[40:43], a[216:219], v[124:127], v[40:43]\n" \
"s_sub_u32 s96, s96, s97\n" \
"s_lshl_b32 s96, s96, 3\n" \
"s_add_u32 s24, s96, 94437376\n" \
"v_mfma_f32_16x16x32_bf16 v[44:47], a[212:215], v[120:123], v[44:47]\n" \
"s_sub_u32 s96, s74, 109051904\n" \
"ds_read_b128 v[116:119], v28 offset:14336\n" \
"s_lshr_b32 s96, s96, 11\n" \
"v_mfma_f32_16x16x32_bf16 v[44:47], a[220:223], v[124:127], v[44:47]\n" \
"s_waitcnt vmcnt(14) lgkmcnt(4)\n" \
"s_barrier\n" \
"s_mul_i32 s97, s96, 10923\n" \
"v_mfma_f32_16x16x32_bf16 v[48:51], a[208:211], v[96:99], v[48:51]\n" \
"s_lshr_b32 s97, s97, 16\n" \
"ds_read_b128 v[120:123], v28 offset:13312\n" \
"s_lshl_b32 s97, s97, 1\n" \
"v_mfma_f32_16x16x32_bf16 v[48:51], a[216:219], v[100:103], v[48:51]\n" \
"buffer_load_dwordx4 v12, s[28:31], s81 offen offset:128 lds\n" \
"v_mfma_f32_16x16x32_bf16 v[52:55], a[212:215], v[96:99], v[52:55]\n" \
"s_mov_b32 s81, 1245184\n" \
"ds_read_b128 v[124:127], v28 offset:15360\n" \
"s_add_u32 m0, s79, 155648\n" \
"v_accvgpr_read_b32 v232, a40\n" \
"v_accvgpr_read_b32 v233, a41\n" \
"v_accvgpr_read_b32 v234, a42\n" \
"v_accvgpr_read_b32 v235, a43\n" \
"v_accvgpr_read_b32 v236, a44\n" \
"v_accvgpr_read_b32 v237, a45\n" \
"v_accvgpr_read_b32 v238, a46\n" \
"v_accvgpr_read_b32 v239, a47\n" \
"v_mfma_f32_16x16x32_bf16 v[52:55], a[220:223], v[100:103], v[52:55]\n" \
"s_waitcnt lgkmcnt(15)\n" \
"ds_read_b64_tr_b16 a[80:81], v180 offset:16384\n" \
"s_sub_u32 s96, s96, s97\n" \
"v_mfma_f32_16x16x32_bf16 v[232:235], v[24:27], v[148:151], v[232:235]\n" \
"s_waitcnt lgkmcnt(5)\n" \
"ds_read_b64_tr_b16 a[82:83], v181 offset:16384\n" \
"s_lshl_b32 s96, s96, 3\n" \
"v_mfma_f32_16x16x32_bf16 v[236:239], v[22:25], v[148:151], v[236:239]\n" \
"buffer_load_dwordx4 v12, s[28:31], s81 offen offset:0 lds\n" \
"v_mfma_f32_16x16x32_bf16 v[56:59], a[208:211], v[104:107], v[56:59]\n" \
"s_add_u32 m0, s79, 156544\n" \
"ds_read_b128 v[96:99], v28 offset:16384\n" \
"s_add_u32 s45, s96, 94437376\n" \
"v_mfma_f32_16x16x32_bf16 v[56:59], a[216:219], v[108:111], v[56:59]\n" \
"ds_read_b64_tr_b16 a[84:85], v182 offset:16384\n" \
"v_mul_f32 v194, v186, v194\n" \
"v_mfma_f32_16x16x32_bf16 v[60:63], a[212:215], v[104:107], v[60:63]\n" \
"s_waitcnt lgkmcnt(6)\n" \
"ds_read_b128 v[100:103], v28 offset:18432\n" \
"v_mul_f32 v195, v186, v195\n" \
"v_mfma_f32_16x16x32_bf16 v[60:63], a[220:223], v[108:111], v[60:63]\n" \
"buffer_load_dwordx2 v[18:19], v17, s[36:39], 0 offen sc1\n" \
"v_mfma_f32_16x16x32_bf16 v[64:67], a[208:211], v[112:115], v[64:67]\n" \
"ds_read_b128 v[104:107], v28 offset:17408\n" \
"v_mul_f32 v196, v186, v196\n" \
"v_mfma_f32_16x16x32_bf16 v[64:67], a[216:219], v[116:119], v[64:67]\n" \
"ds_read_b64_tr_b16 a[86:87], v183 offset:16384\n" \
"v_mul_f32 v197, v186, v197\n" \
"v_mfma_f32_16x16x32_bf16 v[68:71], a[212:215], v[112:115], v[68:71]\n" \
"s_waitcnt lgkmcnt(7)\n" \
"ds_read_b128 v[108:111], v28 offset:19456\n" \
"v_mul_f32 v198, v186, v198\n" \
"v_mfma_f32_16x16x32_bf16 v[68:71], a[220:223], v[116:119], v[68:71]\n" \
"buffer_load_dwordx4 v12, s[28:31], s81 offen offset:128 lds\n" \
"v_mfma_f32_16x16x32_bf16 v[72:75], a[208:211], v[120:123], v[72:75]\n" \
"s_add_u32 s80, s65, 32768\n" \
"ds_read_b128 v[112:115], v28 offset:20480\n" \
"s_add_u32 m0, s22, 32768\n" \
"v_mfma_f32_16x16x32_bf16 v[72:75], a[216:219], v[124:127], v[72:75]\n" \
"ds_read_b64_tr_b16 a[88:89], v180 offset:24576\n" \
"v_mul_f32 v199, v186, v199\n" \
"v_mfma_f32_16x16x32_bf16 v[76:79], a[212:215], v[120:123], v[76:79]\n" \
"ds_read_b128 v[116:119], v28 offset:22528\n" \
"v_cvt_pk_bf16_f32 v192, v192, v193\n" \
"v_accvgpr_read_b32 v240, a48\n" \
"v_accvgpr_read_b32 v241, a49\n" \
"v_accvgpr_read_b32 v242, a50\n" \
"v_accvgpr_read_b32 v243, a51\n" \
"v_accvgpr_read_b32 v244, a52\n" \
"v_accvgpr_read_b32 v245, a53\n" \
"v_accvgpr_read_b32 v246, a54\n" \
"v_accvgpr_read_b32 v247, a55\n" \
"v_mfma_f32_16x16x32_bf16 v[76:79], a[220:223], v[124:127], v[76:79]\n" \
"s_waitcnt lgkmcnt(15)\n" \
"ds_read_b64_tr_b16 a[90:91], v181 offset:24576\n" \
"v_cvt_pk_bf16_f32 v193, v194, v195\n" \
"v_mfma_f32_16x16x32_bf16 v[240:243], v[24:27], v[152:155], v[240:243]\n" \
"buffer_load_dwordx4 v4, s[36:39], s80 offen offset:0 sc1 lds\n" \
"v_mfma_f32_16x16x32_bf16 v[244:247], v[22:25], v[152:155], v[244:247]\n" \
"s_waitcnt lgkmcnt(7)\n" \
"s_barrier\n" \
"ds_read_b64_tr_b16 a[92:93], v182 offset:24576\n" \
"v_mfma_f32_16x16x32_bf16 v[32:35], a[224:227], v[96:99], v[32:35]\n" \
"ds_read_b128 v[120:123], v28 offset:21504\n" \
"v_cvt_pk_bf16_f32 v194, v196, v197\n" \
"v_mfma_f32_16x16x32_bf16 v[32:35], a[232:235], v[100:103], v[32:35]\n" \
"ds_read_b64_tr_b16 a[94:95], v183 offset:24576\n" \
"v_cvt_pk_bf16_f32 v195, v198, v199\n" \
"v_mfma_f32_16x16x32_bf16 v[36:39], a[228:231], v[96:99], v[36:39]\n" \
"s_waitcnt lgkmcnt(7)\n" \
"ds_read_b128 v[124:127], v28 offset:23552\n" \
"v_mul_f32 v200, v186, v200\n" \
"v_mfma_f32_16x16x32_bf16 v[36:39], a[236:239], v[100:103], v[36:39]\n" \
"buffer_store_dwordx4 v[192:195], v4, s[36:39], s51 offen offset:0 sc1\n" \
"v_mfma_f32_16x16x32_bf16 v[40:43], a[224:227], v[104:107], v[40:43]\n" \
"ds_read_b128 v[96:99], v28 offset:24576\n" \
"v_mul_f32 v201, v186, v201\n" \
"v_mfma_f32_16x16x32_bf16 v[40:43], a[232:235], v[108:111], v[40:43]\n" \
"v_mul_f32 v202, v186, v202\n" \
"v_mul_f32 v203, v186, v203\n" \
"v_mfma_f32_16x16x32_bf16 v[44:47], a[228:231], v[104:107], v[44:47]\n" \
"s_waitcnt lgkmcnt(6)\n" \
"ds_read_b128 v[100:103], v28 offset:26624\n" \
"v_mul_f32 v204, v186, v204\n" \
"v_mfma_f32_16x16x32_bf16 v[44:47], a[236:239], v[108:111], v[44:47]\n" \
"buffer_load_dwordx4 v4, s[36:39], s80 offen offset:1024 sc1 lds\n" \
"v_mfma_f32_16x16x32_bf16 v[48:51], a[224:227], v[112:115], v[48:51]\n" \
"ds_read_b128 v[104:107], v28 offset:25600\n" \
"v_mul_f32 v205, v186, v205\n" \
"v_mfma_f32_16x16x32_bf16 v[48:51], a[232:235], v[116:119], v[48:51]\n" \
"v_mul_f32 v206, v186, v206\n" \
"v_mul_f32 v207, v186, v207\n" \
"v_mfma_f32_16x16x32_bf16 v[52:55], a[228:231], v[112:115], v[52:55]\n" \
"ds_read_b128 v[108:111], v28 offset:27648\n" \
"v_cvt_pk_bf16_f32 v200, v200, v201\n" \
"v_accvgpr_read_b32 v248, a56\n" \
"v_accvgpr_read_b32 v249, a57\n" \
"v_accvgpr_read_b32 v250, a58\n" \
"v_accvgpr_read_b32 v251, a59\n" \
"v_accvgpr_read_b32 v252, a60\n" \
"v_accvgpr_read_b32 v253, a61\n" \
"v_accvgpr_read_b32 v254, a62\n" \
"v_accvgpr_read_b32 v255, a63\n" \
"v_mfma_f32_16x16x32_bf16 v[52:55], a[236:239], v[116:119], v[52:55]\n" \
"s_waitcnt lgkmcnt(15)\n" \
"v_cvt_pk_bf16_f32 v201, v202, v203\n" \
"v_mfma_f32_16x16x32_bf16 v[248:251], v[24:27], v[156:159], v[248:251]\n" \
"s_waitcnt lgkmcnt(4)\n" \
"v_cvt_pk_bf16_f32 v202, v204, v205\n" \
"v_cvt_pk_bf16_f32 v203, v206, v207\n" \
"v_mfma_f32_16x16x32_bf16 v[252:255], v[22:25], v[156:159], v[252:255]\n" \
"buffer_store_dwordx4 v[200:203], v4, s[36:39], s51 offen offset:2048 sc1\n" \
"v_mfma_f32_16x16x32_bf16 v[56:59], a[224:227], v[120:123], v[56:59]\n" \
"ds_read_b128 v[112:115], v28 offset:28672\n" \
"v_mul_f32 v208, v186, v208\n" \
"v_mfma_f32_16x16x32_bf16 v[56:59], a[232:235], v[124:127], v[56:59]\n" \
"v_mul_f32 v209, v186, v209\n" \
"v_mul_f32 v210, v186, v210\n" \
"v_mfma_f32_16x16x32_bf16 v[60:63], a[228:231], v[120:123], v[60:63]\n" \
"ds_read_b128 v[116:119], v28 offset:30720\n" \
"s_waitcnt vmcnt(5)\n" \
"v_mfma_f32_16x16x32_bf16 v[60:63], a[236:239], v[124:127], v[60:63]\n" \
"s_waitcnt vmcnt(14) lgkmcnt(4)\n" \
"s_barrier\n" \
"v_cmp_ne_u64 vcc, s[14:15], v[18:19]\n" \
"v_mfma_f32_16x16x32_bf16 v[64:67], a[224:227], v[96:99], v[64:67]\n" \
"ds_read_b128 v[120:123], v28 offset:29696\n" \
"s_nop 0\n" \
"v_mfma_f32_16x16x32_bf16 v[64:67], a[232:235], v[100:103], v[64:67]\n" \
"buffer_load_dwordx4 v4, s[36:39], s80 offen offset:2048 sc1 lds\n" \
"v_mfma_f32_16x16x32_bf16 v[68:71], a[228:231], v[96:99], v[68:71]\n" \
"s_waitcnt lgkmcnt(3)\n" \
"ds_read_b128 v[124:127], v28 offset:31744\n" \
"s_cbranch_vccnz LPS_%=\nLPSR_%=:\n" \
"v_mfma_f32_16x16x32_bf16 v[68:71], a[236:239], v[100:103], v[68:71]\n" \
"ds_read_b64_tr_b16 a[128:129], v180 offset:32768\n" \
"v_mul_f32 v211, v186, v211\n" \
"v_mfma_f32_16x16x32_bf16 v[72:75], a[224:227], v[104:107], v[72:75]\n" \
"ds_read_b128 v[96:99], v28 offset:49152\n" \
"v_mul_f32 v212, v186, v212\n" \
"v_mfma_f32_16x16x32_bf16 v[72:75], a[232:235], v[108:111], v[72:75]\n" \
"s_mov_b64 exec, 0xffff\nbuffer_load_dword v1, v2, s[36:39], s67 offen sc1\ns_mov_b64 exec, -1\n" \
"v_mfma_f32_16x16x32_bf16 v[76:79], a[228:231], v[104:107], v[76:79]\n" \
"s_waitcnt lgkmcnt(4)\n" \
"ds_read_b128 v[100:103], v28 offset:51200\n" \
"v_mul_f32 v213, v186, v213\n" \
"v_mfma_f32_16x16x32_bf16 v[76:79], a[236:239], v[108:111], v[76:79]\n" \
"ds_read_b64_tr_b16 a[130:131], v181 offset:32768\n" \
"v_mul_f32 v214, v186, v214\n" \
"v_mfma_f32_16x16x32_bf16 v[32:35], a[240:243], v[112:115], v[32:35]\n" \
"ds_read_b128 v[104:107], v28 offset:50176\n" \
"v_mul_f32 v215, v186, v215\n" \
"v_mfma_f32_16x16x32_bf16 v[32:35], a[248:251], v[116:119], v[32:35]\n" \
"buffer_load_dwordx4 v4, s[36:39], s80 offen offset:3072 sc1 lds\n" \
"v_mfma_f32_16x16x32_bf16 v[36:39], a[244:247], v[112:115], v[36:39]\n" \
"s_waitcnt lgkmcnt(5)\n" \
"ds_read_b128 v[108:111], v28 offset:52224\n" \
"s_add_u32 s80, s67, 0\n" \
"v_mfma_f32_16x16x32_bf16 v[36:39], a[252:255], v[116:119], v[36:39]\n" \
"s_add_u32 m0, s22, 114688\n" \
"ds_read_b64_tr_b16 a[132:133], v182 offset:32768\n" \
"v_cvt_pk_bf16_f32 v208, v208, v209\n" \
"v_mfma_f32_16x16x32_bf16 v[40:43], a[240:243], v[120:123], v[40:43]\n" \
"ds_read_b128 v[112:115], v28 offset:53248\n" \
"v_cvt_pk_bf16_f32 v209, v210, v211\n" \
"v_mfma_f32_16x16x32_bf16 v[40:43], a[248:251], v[124:127], v[40:43]\n" \
"buffer_load_dwordx4 v31, s[36:39], s80 offen offset:0 sc1 lds\n" \
"v_mfma_f32_16x16x32_bf16 v[44:47], a[244:247], v[120:123], v[44:47]\n" \
"ds_read_b128 v[116:119], v28 offset:55296\n" \
"v_cvt_pk_bf16_f32 v210, v212, v213\n" \
"v_mfma_f32_16x16x32_bf16 v[44:47], a[252:255], v[124:127], v[44:47]\n" \
"s_waitcnt lgkmcnt(6)\n" \
"s_barrier\n" \
"ds_read_b64_tr_b16 a[134:135], v183 offset:32768\n" \
"v_mfma_f32_16x16x32_bf16 v[48:51], a[240:243], v[96:99], v[48:51]\n" \
"ds_read_b128 v[120:123], v28 offset:54272\n" \
"v_cvt_pk_bf16_f32 v211, v214, v215\n" \
"v_mfma_f32_16x16x32_bf16 v[48:51], a[248:251], v[100:103], v[48:51]\n" \
"buffer_store_dwordx4 v[208:211], v7, s[36:39], s51 offen offset:0 sc1\n" \
"v_mfma_f32_16x16x32_bf16 v[52:55], a[244:247], v[96:99], v[52:55]\n" \
"s_waitcnt lgkmcnt(5)\n" \
"ds_read_b128 v[124:127], v28 offset:56320\n" \
"v_mul_f32 v216, v186, v216\n" \
"v_mfma_f32_16x16x32_bf16 v[52:55], a[252:255], v[100:103], v[52:55]\n" \
"ds_read_b64_tr_b16 a[136:137], v180 offset:40960\n" \
"v_mul_f32 v217, v186, v217\n" \
"v_mfma_f32_16x16x32_bf16 v[56:59], a[240:243], v[104:107], v[56:59]\n" \
"ds_read_b128 v[96:99], v28 offset:57344\n" \
"v_mul_f32 v218, v186, v218\n" \
"v_mfma_f32_16x16x32_bf16 v[56:59], a[248:251], v[108:111], v[56:59]\n" \
"ds_read_b64_tr_b16 a[138:139], v181 offset:40960\n" \
"v_mul_f32 v219, v186, v219\n" \
"v_mfma_f32_16x16x32_bf16 v[60:63], a[244:247], v[104:107], v[60:63]\n" \
"s_waitcnt lgkmcnt(6)\n" \
"ds_read_b128 v[100:103], v28 offset:59392\n" \
"v_mul_f32 v220, v186, v220\n" \
"v_mfma_f32_16x16x32_bf16 v[60:63], a[252:255], v[108:111], v[60:63]\n" \
"buffer_load_dwordx4 v31, s[36:39], s80 offen offset:1024 sc1 lds\n" \
"v_mfma_f32_16x16x32_bf16 v[64:67], a[240:243], v[112:115], v[64:67]\n" \
"ds_read_b128 v[104:107], v28 offset:58368\n" \
"v_mul_f32 v221, v186, v221\n" \
"v_mfma_f32_16x16x32_bf16 v[64:67], a[248:251], v[116:119], v[64:67]\n" \
"ds_read_b64_tr_b16 a[140:141], v182 offset:40960\n" \
"v_mul_f32 v222, v186, v222\n" \
"v_mfma_f32_16x16x32_bf16 v[68:71], a[244:247], v[112:115], v[68:71]\n" \
"s_waitcnt lgkmcnt(6)\n" \
"ds_read_b128 v[108:111], v28 offset:60416\n" \
"ds_read_b64_tr_b16 a[142:143], v183 offset:40960\n" \
"v_mfma_f32_16x16x32_bf16 v[68:71], a[252:255], v[116:119], v[68:71]\n" \
"buffer_load_dwordx4 v31, s[36:39], s80 offen offset:2048 sc1 lds\n" \
"v_mfma_f32_16x16x32_bf16 v[72:75], a[240:243], v[120:123], v[72:75]\n" \
"ds_read_b128 v[112:115], v28 offset:61440\n" \
"v_mul_f32 v223, v186, v223\n" \
"v_mfma_f32_16x16x32_bf16 v[72:75], a[248:251], v[124:127], v[72:75]\n" \
"v_cvt_pk_bf16_f32 v216, v216, v217\n" \
"v_cvt_pk_bf16_f32 v217, v218, v219\n" \
"v_mfma_f32_16x16x32_bf16 v[76:79], a[244:247], v[120:123], v[76:79]\n" \
"ds_read_b128 v[116:119], v28 offset:63488\n" \
"v_cvt_pk_bf16_f32 v218, v220, v221\n" \
"v_mfma_f32_16x16x32_bf16 v[76:79], a[252:255], v[124:127], v[76:79]\n" \
"s_waitcnt vmcnt(25) lgkmcnt(2)\n" \
"s_barrier\n" \
"v_cvt_pk_bf16_f32 v219, v222, v223\n" \
"v_mfma_f32_16x16x32_bf16 v[32:35], a[64:67], v[96:99], v[32:35]\n" \
"ds_read_b128 v[120:123], v28 offset:62464\n" \
"v_mul_f32 v224, v187, v224\n" \
"v_mfma_f32_16x16x32_bf16 v[32:35], a[72:75], v[100:103], v[32:35]\n" \
"buffer_store_dwordx4 v[216:219], v7, s[36:39], s51 offen offset:2048 sc1\n" \
"v_mfma_f32_16x16x32_bf16 v[36:39], a[68:71], v[96:99], v[36:39]\n" \
"s_waitcnt lgkmcnt(4)\n" \
"ds_read_b128 v[124:127], v28 offset:64512\n" \
"v_mul_f32 v225, v187, v225\n" \
"v_mfma_f32_16x16x32_bf16 v[36:39], a[76:79], v[100:103], v[36:39]\n" \
"v_mul_f32 v226, v187, v226\n" \
"v_mul_f32 v227, v187, v227\n" \
"v_mfma_f32_16x16x32_bf16 v[40:43], a[64:67], v[104:107], v[40:43]\n" \
"ds_read_b128 v[96:99], v29 offset:0\n" \
"v_mul_f32 v228, v187, v228\n" \
"v_mfma_f32_16x16x32_bf16 v[40:43], a[72:75], v[108:111], v[40:43]\n" \
"v_mul_f32 v229, v187, v229\n" \
"v_mul_f32 v230, v187, v230\n" \
"v_mfma_f32_16x16x32_bf16 v[44:47], a[68:71], v[104:107], v[44:47]\n" \
"s_waitcnt lgkmcnt(3)\n" \
"ds_read_b128 v[100:103], v29 offset:2048\n" \
"v_mul_f32 v231, v187, v231\n" \
"v_mfma_f32_16x16x32_bf16 v[44:47], a[76:79], v[108:111], v[44:47]\n" \
"buffer_load_dwordx4 v31, s[36:39], s80 offen offset:3072 sc1 lds\n" \
"v_mfma_f32_16x16x32_bf16 v[48:51], a[64:67], v[112:115], v[48:51]\n" \
"s_add_u32 s80, s67, 16384\n" \
"ds_read_b128 v[104:107], v29 offset:1024\n" \
"s_add_u32 m0, s22, 0\n" \
"v_mfma_f32_16x16x32_bf16 v[48:51], a[72:75], v[116:119], v[48:51]\n" \
"v_cvt_pk_bf16_f32 v224, v224, v225\n" \
"v_cvt_pk_bf16_f32 v225, v226, v227\n" \
"v_mfma_f32_16x16x32_bf16 v[52:55], a[68:71], v[112:115], v[52:55]\n" \
"s_waitcnt lgkmcnt(3)\n" \
"ds_read_b128 v[108:111], v29 offset:3072\n" \
"v_cvt_pk_bf16_f32 v226, v228, v229\n" \
"v_mfma_f32_16x16x32_bf16 v[52:55], a[76:79], v[116:119], v[52:55]\n" \
"buffer_load_dwordx4 v31, s[36:39], s80 offen offset:0 sc1 lds\n" \
"v_mfma_f32_16x16x32_bf16 v[56:59], a[64:67], v[120:123], v[56:59]\n" \
"ds_read_b128 v[112:115], v29 offset:4096\n" \
"v_cvt_pk_bf16_f32 v227, v230, v231\n" \
"v_mfma_f32_16x16x32_bf16 v[56:59], a[72:75], v[124:127], v[56:59]\n" \
"v_mul_f32 v232, v187, v232\n" \
"v_mul_f32 v233, v187, v233\n" \
"v_mfma_f32_16x16x32_bf16 v[60:63], a[68:71], v[120:123], v[60:63]\n" \
"ds_read_b128 v[116:119], v29 offset:6144\n" \
"v_mul_f32 v234, v187, v234\n" \
"v_mfma_f32_16x16x32_bf16 v[60:63], a[76:79], v[124:127], v[60:63]\n" \
"s_waitcnt vmcnt(14) lgkmcnt(4)\n" \
"s_barrier\n" \
"v_mul_f32 v235, v187, v235\n" \
"v_mfma_f32_16x16x32_bf16 v[64:67], a[64:67], v[96:99], v[64:67]\n" \
"ds_read_b128 v[120:123], v29 offset:5120\n" \
"v_mul_f32 v236, v187, v236\n" \
"v_mfma_f32_16x16x32_bf16 v[64:67], a[72:75], v[100:103], v[64:67]\n" \
"buffer_store_dwordx4 v[224:227], v4, s[36:39], s51 offen offset:1024 sc1\n" \
"v_mfma_f32_16x16x32_bf16 v[68:71], a[68:71], v[96:99], v[68:71]\n" \
"s_waitcnt lgkmcnt(3)\n" \
"ds_read_b128 v[124:127], v29 offset:7168\n" \
"v_mul_f32 v237, v187, v237\n" \
"v_mfma_f32_16x16x32_bf16 v[68:71], a[76:79], v[100:103], v[68:71]\n" \
"ds_read_b64_tr_b16 a[144:145], v13 offset:16384\n" \
"v_mul_f32 v238, v187, v238\n" \
"v_mfma_f32_16x16x32_bf16 v[72:75], a[64:67], v[104:107], v[72:75]\n" \
"ds_read_b128 v[96:99], v29 offset:8192\n" \
"v_mul_f32 v239, v187, v239\n" \
"v_mfma_f32_16x16x32_bf16 v[72:75], a[72:75], v[108:111], v[72:75]\n" \
"ds_read_b64_tr_b16 a[146:147], v14 offset:16384\n" \
"v_cvt_pk_bf16_f32 v232, v232, v233\n" \
"v_mfma_f32_16x16x32_bf16 v[76:79], a[68:71], v[104:107], v[76:79]\n" \
"s_waitcnt lgkmcnt(5)\n" \
"ds_read_b128 v[100:103], v29 offset:10240\n" \
"v_cvt_pk_bf16_f32 v233, v234, v235\n" \
"v_mfma_f32_16x16x32_bf16 v[76:79], a[76:79], v[108:111], v[76:79]\n" \
"buffer_load_dwordx4 v31, s[36:39], s80 offen offset:1024 sc1 lds\n" \
"v_mfma_f32_16x16x32_bf16 v[32:35], a[80:83], v[112:115], v[32:35]\n" \
"ds_read_b128 v[104:107], v29 offset:9216\n" \
"v_cvt_pk_bf16_f32 v234, v236, v237\n" \
"v_mfma_f32_16x16x32_bf16 v[32:35], a[88:91], v[116:119], v[32:35]\n" \
"ds_read_b64_tr_b16 a[148:149], v15 offset:16384\n" \
"v_cvt_pk_bf16_f32 v235, v238, v239\n" \
"v_mfma_f32_16x16x32_bf16 v[36:39], a[84:87], v[112:115], v[36:39]\n" \
"s_waitcnt lgkmcnt(6)\n" \
"ds_read_b128 v[108:111], v29 offset:11264\n" \
"v_mul_f32 v240, v187, v240\n" \
"v_mfma_f32_16x16x32_bf16 v[36:39], a[92:95], v[116:119], v[36:39]\n" \
"buffer_store_dwordx4 v[232:235], v4, s[36:39], s51 offen offset:3072 sc1\n" \
"v_mfma_f32_16x16x32_bf16 v[40:43], a[80:83], v[120:123], v[40:43]\n" \
"ds_read_b128 v[112:115], v29 offset:12288\n" \
"v_mul_f32 v241, v187, v241\n" \
"v_mfma_f32_16x16x32_bf16 v[40:43], a[88:91], v[124:127], v[40:43]\n" \
"LH26_%=:\ns_cbranch_execz LHR2_%=\n" \
"ds_read_b64_tr_b16 a[150:151], v16 offset:16384\n" \
"v_mul_f32 v242, v187, v242\n" \
"v_mfma_f32_16x16x32_bf16 v[44:47], a[84:87], v[120:123], v[44:47]\n" \
"ds_read_b128 v[116:119], v29 offset:14336\n" \
"v_mul_f32 v243, v187, v243\n" \
"v_mfma_f32_16x16x32_bf16 v[44:47], a[92:95], v[124:127], v[44:47]\n" \
"s_waitcnt vmcnt(22) lgkmcnt(6)\n" \
"s_barrier\n" \
"ds_read_b64_tr_b16 a[152:153], v13 offset:24576\n" \
"v_mfma_f32_16x16x32_bf16 v[48:51], a[80:83], v[96:99], v[48:51]\n" \
"ds_read_b128 v[120:123], v29 offset:13312\n" \
"v_mul_f32 v244, v187, v244\n" \
"v_mfma_f32_16x16x32_bf16 v[48:51], a[88:91], v[100:103], v[48:51]\n" \
"ds_read_b64_tr_b16 a[154:155], v14 offset:24576\n" \
"v_mul_f32 v245, v187, v245\n" \
"v_mfma_f32_16x16x32_bf16 v[52:55], a[84:87], v[96:99], v[52:55]\n" \
"s_waitcnt lgkmcnt(6)\n" \
"ds_read_b128 v[124:127], v29 offset:15360\n" \
"v_mul_f32 v246, v187, v246\n" \
"v_mfma_f32_16x16x32_bf16 v[52:55], a[92:95], v[100:103], v[52:55]\n" \
"buffer_load_dwordx4 v31, s[36:39], s80 offen offset:2048 sc1 lds\n" \
"v_mfma_f32_16x16x32_bf16 v[56:59], a[80:83], v[104:107], v[56:59]\n" \
"ds_read_b128 v[96:99], v30 offset:0\n" \
"v_mul_f32 v247, v187, v247\n" \
"v_mfma_f32_16x16x32_bf16 v[56:59], a[88:91], v[108:111], v[56:59]\n" \
"ds_read_b64_tr_b16 a[156:157], v15 offset:24576\n" \
"v_cvt_pk_bf16_f32 v240, v240, v241\n" \
"v_mfma_f32_16x16x32_bf16 v[60:63], a[84:87], v[104:107], v[60:63]\n" \
"s_waitcnt lgkmcnt(6)\n" \
"ds_read_b128 v[100:103], v30 offset:2048\n" \
"v_cvt_pk_bf16_f32 v241, v242, v243\n" \
"v_mfma_f32_16x16x32_bf16 v[60:63], a[92:95], v[108:111], v[60:63]\n" \
"buffer_load_dwordx4 v31, s[36:39], s80 offen offset:3072 sc1 lds\n" \
"v_mfma_f32_16x16x32_bf16 v[64:67], a[80:83], v[112:115], v[64:67]\n" \
"s_add_u32 s80, s67, 32768\n" \
"ds_read_b128 v[104:107], v30 offset:1024\n" \
"s_add_u32 m0, s22, 81920\n" \
"v_mfma_f32_16x16x32_bf16 v[64:67], a[88:91], v[116:119], v[64:67]\n" \
"ds_read_b64_tr_b16 a[158:159], v16 offset:24576\n" \
"v_cvt_pk_bf16_f32 v242, v244, v245\n" \
"v_mfma_f32_16x16x32_bf16 v[68:71], a[84:87], v[112:115], v[68:71]\n" \
"s_waitcnt lgkmcnt(5)\n" \
"ds_read_b128 v[108:111], v30 offset:3072\n" \
"v_cvt_pk_bf16_f32 v243, v246, v247\n" \
"v_mfma_f32_16x16x32_bf16 v[68:71], a[92:95], v[116:119], v[68:71]\n" \
"buffer_store_dwordx4 v[240:243], v7, s[36:39], s51 offen offset:1024 sc1\n" \
"v_mfma_f32_16x16x32_bf16 v[72:75], a[80:83], v[120:123], v[72:75]\n" \
"ds_read_b128 v[112:115], v30 offset:4096\n" \
"v_mul_f32 v248, v187, v248\n" \
"v_mfma_f32_16x16x32_bf16 v[72:75], a[88:91], v[124:127], v[72:75]\n" \
"v_mul_f32 v249, v187, v249\n" \
"v_mul_f32 v250, v187, v250\n" \
"v_mfma_f32_16x16x32_bf16 v[76:79], a[84:87], v[120:123], v[76:79]\n" \
"ds_read_b128 v[116:119], v30 offset:6144\n" \
"v_mul_f32 v251, v187, v251\n" \
"v_mfma_f32_16x16x32_bf16 v[76:79], a[92:95], v[124:127], v[76:79]\n" \
"s_waitcnt vmcnt(7) lgkmcnt(3)\n" \
"s_barrier\n" \
"v_mul_f32 v252, v187, v252\n" \
"v_mfma_f32_16x16x32_bf16 v[32:35], a[128:131], v[96:99], v[32:35]\n" \
"ds_read_b128 v[120:123], v30 offset:5120\n" \
"v_mul_f32 v253, v187, v253\n" \
"v_mfma_f32_16x16x32_bf16 v[32:35], a[136:139], v[100:103], v[32:35]\n" \
"v_mul_f32 v254, v187, v254\n" \
"v_mul_f32 v255, v187, v255\n" \
"v_mfma_f32_16x16x32_bf16 v[36:39], a[132:135], v[96:99], v[36:39]\n" \
"s_waitcnt lgkmcnt(3)\n" \
"ds_read_b128 v[124:127], v30 offset:7168\n" \
"v_cvt_pk_bf16_f32 v248, v248, v249\n" \
"v_mfma_f32_16x16x32_bf16 v[36:39], a[140:143], v[100:103], v[36:39]\n" \
"buffer_load_dwordx4 v31, s[36:39], s80 offen offset:0 sc1 lds\n" \
"v_mfma_f32_16x16x32_bf16 v[40:43], a[128:131], v[104:107], v[40:43]\n" \
"ds_read_b128 v[96:99], v30 offset:8192\n" \
"v_cvt_pk_bf16_f32 v249, v250, v251\n" \
"v_mfma_f32_16x16x32_bf16 v[40:43], a[136:139], v[108:111], v[40:43]\n" \
"v_cvt_pk_bf16_f32 v250, v252, v253\n" \
"v_cvt_pk_bf16_f32 v251, v254, v255\n" \
"v_mfma_f32_16x16x32_bf16 v[44:47], a[132:135], v[104:107], v[44:47]\n" \
"s_waitcnt lgkmcnt(3)\n" \
"ds_read_b128 v[100:103], v30 offset:10240\n" \
"v_bfe_u32 v188, v4, 7, 1\n" \
"v_mfma_f32_16x16x32_bf16 v[44:47], a[140:143], v[108:111], v[44:47]\n" \
"buffer_store_dwordx4 v[248:251], v7, s[36:39], s51 offen offset:3072 sc1\n" \
"v_mfma_f32_16x16x32_bf16 v[48:51], a[128:131], v[112:115], v[48:51]\n" \
"ds_read_b128 v[104:107], v30 offset:9216\n" \
"v_bfe_u32 v189, v4, 4, 3\n" \
"v_mfma_f32_16x16x32_bf16 v[48:51], a[136:139], v[116:119], v[48:51]\n" \
"v_lshl_add_u32 v188, v189, 1, v188\n" \
"v_lshrrev_b32 v189, 8, v4\n" \
"v_mfma_f32_16x16x32_bf16 v[52:55], a[132:135], v[112:115], v[52:55]\n" \
"s_waitcnt lgkmcnt(3)\n" \
"ds_read_b128 v[108:111], v30 offset:11264\n" \
"v_lshlrev_b32 v184, 11, v188\n" \
"v_mfma_f32_16x16x32_bf16 v[52:55], a[140:143], v[116:119], v[52:55]\n" \
"v_and_b32 v188, 1, v189\n" \
"v_lshl_add_u32 v184, v188, 5, v184\n" \
"v_mfma_f32_16x16x32_bf16 v[56:59], a[128:131], v[120:123], v[56:59]\n" \
"ds_read_b128 v[112:115], v29 offset:49152\n" \
"v_lshrrev_b32 v188, 1, v189\n" \
"v_mfma_f32_16x16x32_bf16 v[56:59], a[136:139], v[124:127], v[56:59]\n" \
"v_lshl_add_u32 v184, v188, 4, v184\n" \
"v_bfe_u32 v188, v4, 7, 1\n" \
"v_mfma_f32_16x16x32_bf16 v[60:63], a[132:135], v[120:123], v[60:63]\n" \
"ds_read_b128 v[116:119], v29 offset:51200\n" \
"v_bfe_u32 v189, v4, 4, 3\n" \
"v_mfma_f32_16x16x32_bf16 v[60:63], a[140:143], v[124:127], v[60:63]\n" \
"s_waitcnt lgkmcnt(4)\n" \
"s_barrier\n" \
"v_lshl_add_u32 v188, v189, 1, v188\n" \
"v_mfma_f32_16x16x32_bf16 v[64:67], a[128:131], v[96:99], v[64:67]\n" \
"ds_read_b128 v[120:123], v29 offset:50176\n" \
"v_lshlrev_b32 v185, 3, v188\n" \
"v_mfma_f32_16x16x32_bf16 v[64:67], a[136:139], v[100:103], v[64:67]\n" \
"buffer_load_dwordx4 v31, s[36:39], s80 offen offset:1024 sc1 lds\n" \
"v_mfma_f32_16x16x32_bf16 v[68:71], a[132:135], v[96:99], v[68:71]\n" \
"s_waitcnt lgkmcnt(3)\n" \
"ds_read_b128 v[124:127], v29 offset:52224\n" \
"v_accvgpr_read_b32 v144, a96\n" \
"v_mfma_f32_16x16x32_bf16 v[68:71], a[140:143], v[100:103], v[68:71]\n" \
"v_accvgpr_read_b32 v145, a97\n" \
"v_accvgpr_read_b32 v146, a98\n" \
"v_mfma_f32_16x16x32_bf16 v[72:75], a[128:131], v[104:107], v[72:75]\n" \
"ds_read_b128 v[96:99], v29 offset:53248\n" \
"v_accvgpr_read_b32 v147, a99\n" \
"v_mfma_f32_16x16x32_bf16 v[72:75], a[136:139], v[108:111], v[72:75]\n" \
"buffer_load_dwordx4 v31, s[36:39], s80 offen offset:2048 sc1 lds\n" \
"v_mfma_f32_16x16x32_bf16 v[76:79], a[132:135], v[104:107], v[76:79]\n" \
"s_waitcnt lgkmcnt(3)\n" \
"ds_read_b128 v[100:103], v29 offset:55296\n" \
"v_accvgpr_read_b32 v148, a100\n" \
"v_mfma_f32_16x16x32_bf16 v[76:79], a[140:143], v[108:111], v[76:79]\n" \
"v_accvgpr_read_b32 v149, a101\n" \
"v_accvgpr_read_b32 v150, a102\n" \
"v_mfma_f32_16x16x32_bf16 v[80:83], a[160:163], v[112:115], 0\n" \
"ds_read_b128 v[104:107], v29 offset:54272\n" \
"v_accvgpr_read_b32 v151, a103\n" \
"v_mfma_f32_16x16x32_bf16 v[80:83], a[168:171], v[116:119], v[80:83]\n" \
"buffer_load_dwordx4 v31, s[36:39], s80 offen offset:3072 sc1 lds\n" \
"v_mfma_f32_16x16x32_bf16 v[84:87], a[164:167], v[112:115], 0\n" \
"s_waitcnt lgkmcnt(3)\n" \
"ds_read_b128 v[108:111], v29 offset:56320\n" \
"s_mov_b32 s81, 1310720\n" \
"v_mfma_f32_16x16x32_bf16 v[84:87], a[172:175], v[116:119], v[84:87]\n" \
"s_add_u32 m0, s79, 16384\n" \
"v_accvgpr_read_b32 v152, a104\n" \
"v_accvgpr_read_b32 v153, a105\n" \
"v_mfma_f32_16x16x32_bf16 v[88:91], a[160:163], v[120:123], 0\n" \
"ds_read_b128 v[112:115], v29 offset:57344\n" \
"v_accvgpr_read_b32 v154, a106\n" \
"v_mfma_f32_16x16x32_bf16 v[88:91], a[168:171], v[124:127], v[88:91]\n" \
"buffer_load_dwordx4 v12, s[28:31], s81 offen offset:0 lds\n" \
"v_mfma_f32_16x16x32_bf16 v[92:95], a[164:167], v[120:123], 0\n" \
"s_add_u32 m0, s79, 17280\n" \
"ds_read_b128 v[116:119], v29 offset:59392\n" \
"v_accvgpr_read_b32 v155, a107\n" \
"v_mfma_f32_16x16x32_bf16 v[92:95], a[172:175], v[124:127], v[92:95]\n" \
"s_waitcnt vmcnt(7) lgkmcnt(4)\n" \
"s_barrier\n" \
"v_accvgpr_read_b32 v156, a108\n" \
"v_mfma_f32_16x16x32_bf16 v[80:83], a[176:179], v[96:99], v[80:83]\n" \
"ds_read_b128 v[120:123], v29 offset:58368\n" \
"v_accvgpr_read_b32 v157, a109\n" \
"v_mfma_f32_16x16x32_bf16 v[80:83], a[184:187], v[100:103], v[80:83]\n" \
"buffer_load_dwordx4 v12, s[28:31], s81 offen offset:128 lds\n" \
"v_mfma_f32_16x16x32_bf16 v[84:87], a[180:183], v[96:99], v[84:87]\n" \
"s_waitcnt lgkmcnt(3)\n" \
"ds_read_b128 v[124:127], v29 offset:60416\n" \
"s_mov_b32 s81, 1376256\n" \
"v_mfma_f32_16x16x32_bf16 v[84:87], a[188:191], v[100:103], v[84:87]\n" \
"s_add_u32 m0, s79, 24576\n" \
"v_accvgpr_read_b32 v158, a110\n" \
"v_accvgpr_read_b32 v159, a111\n" \
"v_mfma_f32_16x16x32_bf16 v[88:91], a[176:179], v[104:107], v[88:91]\n" \
"ds_read_b128 v[96:99], v29 offset:61440\n" \
"v_accvgpr_read_b32 v128, a112\n" \
"v_mfma_f32_16x16x32_bf16 v[88:91], a[184:187], v[108:111], v[88:91]\n" \
"buffer_load_dwordx4 v12, s[28:31], s81 offen offset:0 lds\n" \
"v_mfma_f32_16x16x32_bf16 v[92:95], a[180:183], v[104:107], v[92:95]\n" \
"s_waitcnt lgkmcnt(3)\n" \
"ds_read_b128 v[100:103], v29 offset:63488\n" \
"s_add_u32 m0, s79, 25472\n" \
"v_mfma_f32_16x16x32_bf16 v[92:95], a[188:191], v[108:111], v[92:95]\n" \
"v_accvgpr_read_b32 v129, a113\n" \
"v_accvgpr_read_b32 v130, a114\n" \
"v_mfma_f32_16x16x32_bf16 v[80:83], a[192:195], v[112:115], v[80:83]\n" \
"ds_read_b128 v[104:107], v29 offset:62464\n" \
"v_accvgpr_read_b32 v131, a115\n" \
"v_mfma_f32_16x16x32_bf16 v[80:83], a[200:203], v[116:119], v[80:83]\n" \
"buffer_load_dwordx4 v12, s[28:31], s81 offen offset:128 lds\n" \
"v_mfma_f32_16x16x32_bf16 v[84:87], a[196:199], v[112:115], v[84:87]\n" \
"s_waitcnt lgkmcnt(3)\n" \
"ds_read_b128 v[108:111], v29 offset:64512\n" \
"s_mov_b32 s81, 1441792\n" \
"v_mfma_f32_16x16x32_bf16 v[84:87], a[204:207], v[116:119], v[84:87]\n" \
"s_add_u32 m0, s79, 98304\n" \
"v_accvgpr_read_b32 v132, a116\n" \
"v_accvgpr_read_b32 v133, a117\n" \
"v_mfma_f32_16x16x32_bf16 v[88:91], a[192:195], v[120:123], v[88:91]\n" \
"ds_read_b128 v[112:115], v28 offset:0\n" \
"v_accvgpr_read_b32 v134, a118\n" \
"v_mfma_f32_16x16x32_bf16 v[88:91], a[200:203], v[124:127], v[88:91]\n" \
"buffer_load_dwordx4 v12, s[28:31], s81 offen offset:0 lds\n" \
"v_mfma_f32_16x16x32_bf16 v[92:95], a[196:199], v[120:123], v[92:95]\n" \
"s_add_u32 m0, s79, 99200\n" \
"ds_read_b128 v[116:119], v28 offset:2048\n" \
"v_accvgpr_read_b32 v135, a119\n" \
"v_mfma_f32_16x16x32_bf16 v[92:95], a[204:207], v[124:127], v[92:95]\n" \
"s_waitcnt lgkmcnt(4)\n" \
"s_barrier\n" \
"v_accvgpr_read_b32 v136, a120\n" \
"v_mfma_f32_16x16x32_bf16 v[80:83], a[208:211], v[96:99], v[80:83]\n" \
"ds_read_b128 v[120:123], v28 offset:1024\n" \
"v_accvgpr_read_b32 v137, a121\n" \
"v_mfma_f32_16x16x32_bf16 v[80:83], a[216:219], v[100:103], v[80:83]\n" \
"buffer_load_dwordx4 v12, s[28:31], s81 offen offset:128 lds\n" \
"v_mfma_f32_16x16x32_bf16 v[84:87], a[212:215], v[96:99], v[84:87]\n" \
"s_waitcnt lgkmcnt(3)\n" \
"ds_read_b128 v[124:127], v28 offset:3072\n" \
"s_mov_b32 s81, 1507328\n" \
"v_mfma_f32_16x16x32_bf16 v[84:87], a[220:223], v[100:103], v[84:87]\n" \
"s_add_u32 m0, s79, 106496\n" \
"v_accvgpr_read_b32 v138, a122\n" \
"v_accvgpr_read_b32 v139, a123\n" \
"v_mfma_f32_16x16x32_bf16 v[88:91], a[208:211], v[104:107], v[88:91]\n" \
"ds_read_b128 v[96:99], v28 offset:4096\n" \
"v_accvgpr_read_b32 v140, a124\n" \
"v_mfma_f32_16x16x32_bf16 v[88:91], a[216:219], v[108:111], v[88:91]\n" \
"buffer_load_dwordx4 v12, s[28:31], s81 offen offset:0 lds\n" \
"v_mfma_f32_16x16x32_bf16 v[92:95], a[212:215], v[104:107], v[92:95]\n" \
"s_waitcnt lgkmcnt(3)\n" \
"ds_read_b128 v[100:103], v28 offset:6144\n" \
"s_add_u32 m0, s79, 107392\n" \
"v_mfma_f32_16x16x32_bf16 v[92:95], a[220:223], v[108:111], v[92:95]\n" \
"v_accvgpr_read_b32 v141, a125\n" \
"v_accvgpr_read_b32 v142, a126\n" \
"v_mfma_f32_16x16x32_bf16 v[80:83], a[224:227], v[112:115], v[80:83]\n" \
"ds_read_b128 v[104:107], v28 offset:5120\n" \
"v_accvgpr_read_b32 v143, a127\n" \
"v_mfma_f32_16x16x32_bf16 v[80:83], a[232:235], v[116:119], v[80:83]\n" \
"buffer_load_dwordx4 v12, s[28:31], s81 offen offset:128 lds\n" \
"v_mfma_f32_16x16x32_bf16 v[84:87], a[228:231], v[112:115], v[84:87]\n" \
"s_waitcnt lgkmcnt(3)\n" \
"ds_read_b128 v[108:111], v28 offset:7168\n" \
"s_add_u32 s80, s66, 32768\n" \
"v_mfma_f32_16x16x32_bf16 v[84:87], a[236:239], v[116:119], v[84:87]\n" \
"s_add_u32 m0, s22, 49152\n" \
"v_mfma_f32_16x16x32_bf16 v[88:91], a[224:227], v[120:123], v[88:91]\n" \
"ds_read_b128 v[112:115], v28 offset:8192\n" \
"v_mfma_f32_16x16x32_bf16 v[88:91], a[232:235], v[124:127], v[88:91]\n" \
"buffer_load_dwordx4 v4, s[36:39], s80 offen offset:0 sc1 lds\n" \
"v_mfma_f32_16x16x32_bf16 v[92:95], a[228:231], v[120:123], v[92:95]\n" \
"ds_read_b128 v[116:119], v28 offset:10240\n" \
"v_mfma_f32_16x16x32_bf16 v[92:95], a[236:239], v[124:127], v[92:95]\n" \
"s_waitcnt vmcnt(9) lgkmcnt(4)\n" \
"s_barrier\n" \
"v_mfma_f32_16x16x32_bf16 v[80:83], a[240:243], v[96:99], v[80:83]\n" \
"ds_read_b128 v[120:123], v28 offset:9216\n" \
"v_mfma_f32_16x16x32_bf16 v[80:83], a[248:251], v[100:103], v[80:83]\n" \
"buffer_load_dwordx4 v4, s[36:39], s80 offen offset:1024 sc1 lds\n" \
"v_mfma_f32_16x16x32_bf16 v[84:87], a[244:247], v[96:99], v[84:87]\n" \
"s_waitcnt lgkmcnt(3)\n" \
"ds_read_b128 v[124:127], v28 offset:11264\n" \
"v_mfma_f32_16x16x32_bf16 v[84:87], a[252:255], v[100:103], v[84:87]\n" \
"v_mfma_f32_16x16x32_bf16 v[88:91], a[240:243], v[104:107], v[88:91]\n" \
"ds_read_b128 v[96:99], v28 offset:12288\n" \
"v_mfma_f32_16x16x32_bf16 v[88:91], a[248:251], v[108:111], v[88:91]\n" \
"buffer_load_dwordx4 v4, s[36:39], s80 offen offset:2048 sc1 lds\n" \
"v_mfma_f32_16x16x32_bf16 v[92:95], a[244:247], v[104:107], v[92:95]\n" \
"s_waitcnt lgkmcnt(3)\n" \
"ds_read_b128 v[100:103], v28 offset:14336\n" \
"v_mfma_f32_16x16x32_bf16 v[92:95], a[252:255], v[108:111], v[92:95]\n" \
"v_mfma_f32_16x16x32_bf16 v[80:83], a[64:67], v[112:115], v[80:83]\n" \
"ds_read_b128 v[104:107], v28 offset:13312\n" \
"v_mfma_f32_16x16x32_bf16 v[80:83], a[72:75], v[116:119], v[80:83]\n" \
"buffer_load_dwordx4 v4, s[36:39], s80 offen offset:3072 sc1 lds\n" \
"v_mfma_f32_16x16x32_bf16 v[84:87], a[68:71], v[112:115], v[84:87]\n" \
"s_waitcnt lgkmcnt(3)\n" \
"ds_read_b128 v[108:111], v28 offset:15360\n" \
"s_mov_b32 s81, 1572864\n" \
"v_mfma_f32_16x16x32_bf16 v[84:87], a[76:79], v[116:119], v[84:87]\n" \
"s_add_u32 m0, s79, 65536\n" \
"v_mfma_f32_16x16x32_bf16 v[88:91], a[64:67], v[120:123], v[88:91]\n" \
"ds_read_b128 v[112:115], v29 offset:16384\n" \
"v_mfma_f32_16x16x32_bf16 v[88:91], a[72:75], v[124:127], v[88:91]\n" \
"buffer_load_dwordx4 v12, s[28:31], s81 offen offset:0 lds\n" \
"v_mfma_f32_16x16x32_bf16 v[92:95], a[68:71], v[120:123], v[92:95]\n" \
"ds_read_b128 v[116:119], v29 offset:18432\n" \
"s_waitcnt vmcnt(16)\n" \
"v_mfma_f32_16x16x32_bf16 v[92:95], a[76:79], v[124:127], v[92:95]\n" \
"s_waitcnt vmcnt(31) lgkmcnt(4)\n" \
"s_barrier\n" \
"s_add_u32 m0, s79, 66432\n" \
"v_mfma_f32_16x16x32_bf16 v[80:83], a[80:83], v[96:99], v[80:83]\n" \
"ds_read_b128 v[120:123], v29 offset:17408\n" \
"v_mfma_f32_16x16x32_bf16 v[80:83], a[88:91], v[100:103], v[80:83]\n" \
"buffer_load_dwordx4 v12, s[28:31], s81 offen offset:128 lds\n" \
"v_mfma_f32_16x16x32_bf16 v[84:87], a[84:87], v[96:99], v[84:87]\n" \
"s_waitcnt lgkmcnt(3)\n" \
"ds_read_b128 v[124:127], v29 offset:19456\n" \
"s_mov_b32 s81, 1638400\n" \
"v_mfma_f32_16x16x32_bf16 v[84:87], a[92:95], v[100:103], v[84:87]\n" \
"s_cmp_lg_u64 s[4:5], 0\ns_cbranch_scc1 LFBB_%=\nLFBBR_%=:\n" \
"s_add_u32 m0, s79, 73728\n" \
"v_mfma_f32_16x16x32_bf16 v[88:91], a[80:83], v[104:107], v[88:91]\n" \
"ds_read_b128 v[96:99], v30 offset:12288\n" \
"v_mfma_f32_16x16x32_bf16 v[88:91], a[88:91], v[108:111], v[88:91]\n" \
"buffer_store_dwordx2 v[10:11], v20, s[36:39], s53 offen sc1\n" \
"v_mfma_f32_16x16x32_bf16 v[92:95], a[84:87], v[104:107], v[92:95]\n" \
"s_waitcnt lgkmcnt(3)\n" \
"ds_read_b128 v[100:103], v30 offset:14336\n" \
"v_mfma_f32_16x16x32_bf16 v[92:95], a[92:95], v[108:111], v[92:95]\n" \
"buffer_load_dwordx4 v12, s[28:31], s81 offen offset:0 lds\n" \
"v_mfma_f32_16x16x32_bf16 v[80:83], a[128:131], v[112:115], v[80:83]\n" \
"s_add_u32 m0, s79, 74624\n" \
"ds_read_b128 v[104:107], v30 offset:13312\n" \
"v_mfma_f32_16x16x32_bf16 v[80:83], a[136:139], v[116:119], v[80:83]\n" \
"v_mfma_f32_16x16x32_bf16 v[84:87], a[132:135], v[112:115], v[84:87]\n" \
"s_waitcnt lgkmcnt(3)\n" \
"ds_read_b128 v[108:111], v30 offset:15360\n" \
"v_mfma_f32_16x16x32_bf16 v[84:87], a[140:143], v[116:119], v[84:87]\n" \
"buffer_load_dwordx4 v12, s[28:31], s81 offen offset:128 lds\n" \
"v_mfma_f32_16x16x32_bf16 v[88:91], a[128:131], v[120:123], v[88:91]\n" \
"s_add_u32 s80, s64, 49152\n" \
"ds_read_b128 v[112:115], v28 offset:32768\n" \
"s_add_u32 m0, s22, 147456\n" \
"v_mfma_f32_16x16x32_bf16 v[88:91], a[136:139], v[124:127], v[88:91]\n" \
"v_mfma_f32_16x16x32_bf16 v[92:95], a[132:135], v[120:123], v[92:95]\n" \
"ds_read_b128 v[116:119], v28 offset:34816\n" \
"v_mfma_f32_16x16x32_bf16 v[92:95], a[140:143], v[124:127], v[92:95]\n" \
"s_waitcnt vmcnt(13) lgkmcnt(4)\n" \
"s_barrier\n" \
"v_mfma_f32_16x16x32_bf16 v[32:35], a[144:147], v[96:99], v[32:35]\n" \
"ds_read_b128 v[120:123], v28 offset:33792\n" \
"v_mfma_f32_16x16x32_bf16 v[32:35], a[152:155], v[100:103], v[32:35]\n" \
"buffer_load_dwordx4 v4, s[36:39], s80 offen offset:0 sc1 lds\n" \
"v_mfma_f32_16x16x32_bf16 v[36:39], a[148:151], v[96:99], v[36:39]\n" \
"s_waitcnt lgkmcnt(3)\n" \
"ds_read_b128 v[124:127], v28 offset:35840\n" \
"v_mfma_f32_16x16x32_bf16 v[36:39], a[156:159], v[100:103], v[36:39]\n" \
"ds_read_b64_tr_b16 a[0:1], v176 offset:16384\n" \
"v_mfma_f32_16x16x32_bf16 v[40:43], a[144:147], v[104:107], v[40:43]\n" \
"ds_read_b128 v[96:99], v28 offset:36864\n" \
"v_mfma_f32_16x16x32_bf16 v[40:43], a[152:155], v[108:111], v[40:43]\n" \
"buffer_load_dwordx4 v4, s[36:39], s80 offen offset:1024 sc1 lds\n" \
"v_mfma_f32_16x16x32_bf16 v[44:47], a[148:151], v[104:107], v[44:47]\n" \
"s_waitcnt lgkmcnt(4)\n" \
"ds_read_b128 v[100:103], v28 offset:38912\n" \
"v_mfma_f32_16x16x32_bf16 v[44:47], a[156:159], v[108:111], v[44:47]\n" \
"ds_read_b64_tr_b16 a[2:3], v177 offset:16384\n" \
"v_mfma_f32_16x16x32_bf16 v[48:51], a[144:147], v[112:115], v[48:51]\n" \
"ds_read_b128 v[104:107], v28 offset:37888\n" \
"v_mfma_f32_16x16x32_bf16 v[48:51], a[152:155], v[116:119], v[48:51]\n" \
"buffer_load_dwordx4 v4, s[36:39], s80 offen offset:2048 sc1 lds\n" \
"v_mfma_f32_16x16x32_bf16 v[52:55], a[148:151], v[112:115], v[52:55]\n" \
"s_waitcnt lgkmcnt(5)\n" \
"ds_read_b128 v[108:111], v28 offset:39936\n" \
"v_mfma_f32_16x16x32_bf16 v[52:55], a[156:159], v[116:119], v[52:55]\n" \
"ds_read_b64_tr_b16 a[4:5], v178 offset:16384\n" \
"v_mfma_f32_16x16x32_bf16 v[56:59], a[144:147], v[120:123], v[56:59]\n" \
"ds_read_b128 v[112:115], v29 offset:20480\n" \
"v_mfma_f32_16x16x32_bf16 v[56:59], a[152:155], v[124:127], v[56:59]\n" \
"buffer_load_dwordx4 v4, s[36:39], s80 offen offset:3072 sc1 lds\n" \
"v_mfma_f32_16x16x32_bf16 v[60:63], a[148:151], v[120:123], v[60:63]\n" \
"s_mov_b32 s81, 1703936\n" \
"ds_read_b128 v[116:119], v29 offset:22528\n" \
"s_add_u32 m0, s79, 114688\n" \
"v_mfma_f32_16x16x32_bf16 v[60:63], a[156:159], v[124:127], v[60:63]\n" \
"s_waitcnt vmcnt(13) lgkmcnt(6)\n" \
"s_barrier\n" \
"ds_read_b64_tr_b16 a[6:7], v179 offset:16384\n" \
"v_mfma_f32_16x16x32_bf16 v[64:67], a[144:147], v[96:99], v[64:67]\n" \
"ds_read_b128 v[120:123], v29 offset:21504\n" \
"v_mfma_f32_16x16x32_bf16 v[64:67], a[152:155], v[100:103], v[64:67]\n" \
"buffer_load_dwordx4 v12, s[28:31], s81 offen offset:0 lds\n" \
"v_mfma_f32_16x16x32_bf16 v[68:71], a[148:151], v[96:99], v[68:71]\n" \
"s_waitcnt lgkmcnt(5)\n" \
"ds_read_b128 v[124:127], v29 offset:23552\n" \
"s_add_u32 m0, s79, 115584\n" \
"v_mfma_f32_16x16x32_bf16 v[68:71], a[156:159], v[100:103], v[68:71]\n" \
"ds_read_b64_tr_b16 a[8:9], v176 offset:24576\n" \
"v_mfma_f32_16x16x32_bf16 v[72:75], a[144:147], v[104:107], v[72:75]\n" \
"ds_read_b128 v[96:99], v28 offset:40960\n" \
"v_mfma_f32_16x16x32_bf16 v[72:75], a[152:155], v[108:111], v[72:75]\n" \
"buffer_load_dwordx4 v12, s[28:31], s81 offen offset:128 lds\n" \
"v_mfma_f32_16x16x32_bf16 v[76:79], a[148:151], v[104:107], v[76:79]\n" \
"s_waitcnt lgkmcnt(5)\n" \
"ds_read_b128 v[100:103], v28 offset:43008\n" \
"s_mov_b32 s81, 1769472\n" \
"v_mfma_f32_16x16x32_bf16 v[76:79], a[156:159], v[108:111], v[76:79]\n" \
"s_add_u32 m0, s79, 122880\n" \
"ds_read_b64_tr_b16 a[10:11], v177 offset:24576\n" \
"v_mfma_f32_16x16x32_bf16 v[80:83], a[144:147], v[112:115], v[80:83]\n" \
"ds_read_b128 v[104:107], v28 offset:41984\n" \
"v_mfma_f32_16x16x32_bf16 v[80:83], a[152:155], v[116:119], v[80:83]\n" \
"buffer_load_dwordx4 v12, s[28:31], s81 offen offset:0 lds\n" \
"v_mfma_f32_16x16x32_bf16 v[84:87], a[148:151], v[112:115], v[84:87]\n" \
"s_waitcnt lgkmcnt(5)\n" \
"ds_read_b128 v[108:111], v28 offset:44032\n" \
"s_add_u32 m0, s79, 123776\n" \
"ds_read_b64_tr_b16 a[12:13], v178 offset:24576\n" \
"ds_read_b64_tr_b16 a[14:15], v179 offset:24576\n" \
"v_mfma_f32_16x16x32_bf16 v[84:87], a[156:159], v[116:119], v[84:87]\n" \
"ds_read_b64_tr_b16 a[16:17], v180 offset:32768\n" \
"v_mfma_f32_16x16x32_bf16 v[88:91], a[144:147], v[120:123], v[88:91]\n" \
"ds_read_b128 v[112:115], v28 offset:45056\n" \
"v_mfma_f32_16x16x32_bf16 v[88:91], a[152:155], v[124:127], v[88:91]\n" \
"buffer_load_dwordx4 v12, s[28:31], s81 offen offset:128 lds\n" \
"v_mfma_f32_16x16x32_bf16 v[92:95], a[148:151], v[120:123], v[92:95]\n" \
"s_add_u32 s80, s67, 49152\n" \
"ds_read_b128 v[116:119], v28 offset:47104\n" \
"s_add_u32 m0, s22, 0\n" \
"v_mfma_f32_16x16x32_bf16 v[92:95], a[156:159], v[124:127], v[92:95]\n" \
"s_waitcnt vmcnt(13) lgkmcnt(3)\n" \
"s_barrier\n" \
"ds_read_b64_tr_b16 a[18:19], v181 offset:32768\n" \
"v_mfma_f32_16x16x32_bf16 v[32:35], a[0:3], v[96:99], v[32:35]\n" \
"ds_read_b128 v[120:123], v28 offset:46080\n" \
"v_mfma_f32_16x16x32_bf16 v[32:35], a[8:11], v[100:103], v[32:35]\n" \
"buffer_load_dwordx4 v31, s[36:39], s80 offen offset:0 sc1 lds\n" \
"v_mfma_f32_16x16x32_bf16 v[36:39], a[4:7], v[96:99], v[36:39]\n" \
"s_waitcnt lgkmcnt(7)\n" \
"ds_read_b128 v[124:127], v28 offset:48128\n" \
"v_mfma_f32_16x16x32_bf16 v[36:39], a[12:15], v[100:103], v[36:39]\n" \
"ds_read_b64_tr_b16 a[20:21], v182 offset:32768\n" \
"v_mfma_f32_16x16x32_bf16 v[40:43], a[0:3], v[104:107], v[40:43]\n" \
"ds_read_b128 v[96:99], v28 offset:49152\n" \
"v_mfma_f32_16x16x32_bf16 v[40:43], a[8:11], v[108:111], v[40:43]\n" \
"buffer_load_dwordx4 v31, s[36:39], s80 offen offset:1024 sc1 lds\n" \
"v_mfma_f32_16x16x32_bf16 v[44:47], a[4:7], v[104:107], v[44:47]\n" \
"s_waitcnt lgkmcnt(5)\n" \
"ds_read_b128 v[100:103], v28 offset:51200\n" \
"v_mfma_f32_16x16x32_bf16 v[44:47], a[12:15], v[108:111], v[44:47]\n" \
"ds_read_b64_tr_b16 a[22:23], v183 offset:32768\n" \
"v_mfma_f32_16x16x32_bf16 v[48:51], a[0:3], v[112:115], v[48:51]\n" \
"ds_read_b128 v[104:107], v28 offset:50176\n" \
"v_mfma_f32_16x16x32_bf16 v[48:51], a[8:11], v[116:119], v[48:51]\n" \
"buffer_load_dwordx4 v31, s[36:39], s80 offen offset:2048 sc1 lds\n" \
"v_mfma_f32_16x16x32_bf16 v[52:55], a[4:7], v[112:115], v[52:55]\n" \
"s_waitcnt lgkmcnt(5)\n" \
"ds_read_b128 v[108:111], v28 offset:52224\n" \
"ds_read_b64_tr_b16 a[24:25], v180 offset:40960\n" \
"ds_read_b64_tr_b16 a[26:27], v181 offset:40960\n" \
"ds_read_b64_tr_b16 a[28:29], v182 offset:40960\n" \
"ds_read_b64_tr_b16 a[30:31], v183 offset:40960\n" \
"v_mfma_f32_16x16x32_bf16 v[52:55], a[12:15], v[116:119], v[52:55]\n" \
"v_mfma_f32_16x16x32_bf16 v[56:59], a[0:3], v[120:123], v[56:59]\n" \
"ds_read_b128 v[112:115], v29 offset:24576\n" \
"v_mfma_f32_16x16x32_bf16 v[56:59], a[8:11], v[124:127], v[56:59]\n" \
"buffer_load_dwordx4 v31, s[36:39], s80 offen offset:3072 sc1 lds\n" \
"v_mfma_f32_16x16x32_bf16 v[60:63], a[4:7], v[120:123], v[60:63]\n" \
"s_add_u32 s80, s65, 49152\n" \
"ds_read_b128 v[116:119], v29 offset:26624\n" \
"s_add_u32 m0, s22, 131072\n" \
"v_mfma_f32_16x16x32_bf16 v[60:63], a[12:15], v[124:127], v[60:63]\n" \
"s_waitcnt lgkmcnt(2)\n" \
"s_barrier\n" \
"v_mfma_f32_16x16x32_bf16 v[64:67], a[0:3], v[96:99], v[64:67]\n" \
"ds_read_b128 v[120:123], v29 offset:25600\n" \
"v_mfma_f32_16x16x32_bf16 v[64:67], a[8:11], v[100:103], v[64:67]\n" \
"buffer_load_dwordx4 v4, s[36:39], s80 offen offset:0 sc1 lds\n" \
"v_mfma_f32_16x16x32_bf16 v[68:71], a[4:7], v[96:99], v[68:71]\n" \
"s_waitcnt lgkmcnt(7)\n" \
"ds_read_b128 v[124:127], v29 offset:27648\n" \
"v_mfma_f32_16x16x32_bf16 v[68:71], a[12:15], v[100:103], v[68:71]\n" \
"v_mfma_f32_16x16x32_bf16 v[72:75], a[0:3], v[104:107], v[72:75]\n" \
"ds_read_b128 v[96:99], v28 offset:53248\n" \
"v_mfma_f32_16x16x32_bf16 v[72:75], a[8:11], v[108:111], v[72:75]\n" \
"buffer_load_dwordx4 v4, s[36:39], s80 offen offset:1024 sc1 lds\n" \
"v_mfma_f32_16x16x32_bf16 v[76:79], a[4:7], v[104:107], v[76:79]\n" \
"s_waitcnt lgkmcnt(3)\n" \
"ds_read_b128 v[100:103], v28 offset:55296\n" \
"v_mfma_f32_16x16x32_bf16 v[76:79], a[12:15], v[108:111], v[76:79]\n" \
"v_mfma_f32_16x16x32_bf16 v[80:83], a[0:3], v[112:115], v[80:83]\n" \
"ds_read_b128 v[104:107], v28 offset:54272\n" \
"v_mfma_f32_16x16x32_bf16 v[80:83], a[8:11], v[116:119], v[80:83]\n" \
"buffer_load_dwordx4 v4, s[36:39], s80 offen offset:2048 sc1 lds\n" \
"v_mfma_f32_16x16x32_bf16 v[84:87], a[4:7], v[112:115], v[84:87]\n" \
"s_waitcnt lgkmcnt(3)\n" \
"ds_read_b128 v[108:111], v28 offset:56320\n" \
"v_bfe_u32 v186, v4, 7, 2\n" \
"v_mfma_f32_16x16x32_bf16 v[84:87], a[12:15], v[116:119], v[84:87]\n" \
"v_min_u32 v186, 3, v186\n" \
"v_lshl_add_u32 v17, v186, 8, s11\n" \
"v_mfma_f32_16x16x32_bf16 v[88:91], a[0:3], v[120:123], v[88:91]\n" \
"ds_read_b128 v[112:115], v28 offset:57344\n" \
"v_bfe_u32 v186, v4, 4, 3\n" \
"v_mfma_f32_16x16x32_bf16 v[88:91], a[8:11], v[124:127], v[88:91]\n" \
"buffer_load_dwordx4 v4, s[36:39], s80 offen offset:3072 sc1 lds\n" \
"v_mfma_f32_16x16x32_bf16 v[92:95], a[4:7], v[120:123], v[92:95]\n" \
"s_mov_b32 s81, 1835008\n" \
"ds_read_b128 v[116:119], v28 offset:59392\n" \
"v_lshl_add_u32 v17, v186, 3, v17\n" \
"v_mfma_f32_16x16x32_bf16 v[92:95], a[12:15], v[124:127], v[92:95]\n" \
"s_waitcnt vmcnt(16) lgkmcnt(4)\n" \
"s_barrier\n" \
"s_add_u32 m0, s79, 16384\n" \
"v_mfma_f32_16x16x32_bf16 v[32:35], a[16:19], v[96:99], v[32:35]\n" \
"ds_read_b128 v[120:123], v28 offset:58368\n" \
"v_mfma_f32_16x16x32_bf16 v[32:35], a[24:27], v[100:103], v[32:35]\n" \
"buffer_load_dwordx4 v12, s[28:31], s81 offen offset:0 lds\n" \
"v_mfma_f32_16x16x32_bf16 v[36:39], a[20:23], v[96:99], v[36:39]\n" \
"s_waitcnt lgkmcnt(3)\n" \
"ds_read_b128 v[124:127], v28 offset:60416\n" \
"s_add_u32 m0, s79, 17280\n" \
"v_mfma_f32_16x16x32_bf16 v[36:39], a[28:31], v[100:103], v[36:39]\n" \
"ds_read_b64_tr_b16 a[32:33], v180 offset:0\n" \
"v_mfma_f32_16x16x32_bf16 v[40:43], a[16:19], v[104:107], v[40:43]\n" \
"ds_read_b128 v[96:99], v28 offset:61440\n" \
"v_mfma_f32_16x16x32_bf16 v[40:43], a[24:27], v[108:111], v[40:43]\n" \
"buffer_load_dwordx4 v12, s[28:31], s81 offen offset:128 lds\n" \
"v_mfma_f32_16x16x32_bf16 v[44:47], a[20:23], v[104:107], v[44:47]\n" \
"s_waitcnt lgkmcnt(4)\n" \
"ds_read_b128 v[100:103], v28 offset:63488\n" \
"s_mov_b32 s81, 1900544\n" \
"v_mfma_f32_16x16x32_bf16 v[44:47], a[28:31], v[108:111], v[44:47]\n" \
"s_add_u32 m0, s79, 24576\n" \
"ds_read_b64_tr_b16 a[34:35], v181 offset:0\n" \
"v_mfma_f32_16x16x32_bf16 v[48:51], a[16:19], v[112:115], v[48:51]\n" \
"ds_read_b128 v[104:107], v28 offset:62464\n" \
"v_mfma_f32_16x16x32_bf16 v[48:51], a[24:27], v[116:119], v[48:51]\n" \
"buffer_load_dwordx2 v[18:19], v17, s[36:39], 0 offen sc1\n" \
"v_mfma_f32_16x16x32_bf16 v[52:55], a[20:23], v[112:115], v[52:55]\n" \
"s_waitcnt lgkmcnt(5)\n" \
"ds_read_b128 v[108:111], v28 offset:64512\n" \
"v_mfma_f32_16x16x32_bf16 v[52:55], a[28:31], v[116:119], v[52:55]\n" \
"ds_read_b64_tr_b16 a[36:37], v182 offset:0\n" \
"v_mfma_f32_16x16x32_bf16 v[56:59], a[16:19], v[120:123], v[56:59]\n" \
"ds_read_b128 v[112:115], v29 offset:28672\n" \
"v_mfma_f32_16x16x32_bf16 v[56:59], a[24:27], v[124:127], v[56:59]\n" \
"buffer_load_dwordx4 v12, s[28:31], s81 offen offset:0 lds\n" \
"v_mfma_f32_16x16x32_bf16 v[60:63], a[20:23], v[120:123], v[60:63]\n" \
"s_add_u32 m0, s79, 25472\n" \
"ds_read_b128 v[116:119], v29 offset:30720\n" \
"v_mfma_f32_16x16x32_bf16 v[60:63], a[28:31], v[124:127], v[60:63]\n" \
"s_waitcnt vmcnt(16) lgkmcnt(6)\n" \
"s_barrier\n" \
"ds_read_b64_tr_b16 a[38:39], v183 offset:0\n" \
"v_mfma_f32_16x16x32_bf16 v[64:67], a[16:19], v[96:99], v[64:67]\n" \
"ds_read_b128 v[120:123], v29 offset:29696\n" \
"v_mfma_f32_16x16x32_bf16 v[64:67], a[24:27], v[100:103], v[64:67]\n" \
"buffer_load_dwordx4 v12, s[28:31], s81 offen offset:128 lds\n" \
"v_mfma_f32_16x16x32_bf16 v[68:71], a[20:23], v[96:99], v[68:71]\n" \
"s_waitcnt lgkmcnt(5)\n" \
"ds_read_b128 v[124:127], v29 offset:31744\n" \
"s_mov_b32 s81, 1966080\n" \
"v_mfma_f32_16x16x32_bf16 v[68:71], a[28:31], v[100:103], v[68:71]\n" \
"s_add_u32 m0, s79, 32768\n" \
"ds_read_b64_tr_b16 a[40:41], v180 offset:8192\n" \
"v_mfma_f32_16x16x32_bf16 v[72:75], a[16:19], v[104:107], v[72:75]\n" \
"ds_read_b128 v[96:99], v30 offset:16384\n" \
"v_mfma_f32_16x16x32_bf16 v[72:75], a[24:27], v[108:111], v[72:75]\n" \
"buffer_load_dwordx4 v12, s[28:31], s81 offen offset:0 lds\n" \
"v_mfma_f32_16x16x32_bf16 v[76:79], a[20:23], v[104:107], v[76:79]\n" \
"LH27_%=:\ns_cbranch_execz LHR2_%=\n" \
"s_waitcnt lgkmcnt(5)\n" \
"ds_read_b128 v[100:103], v30 offset:18432\n" \
"s_add_u32 m0, s79, 33664\n" \
"v_mfma_f32_16x16x32_bf16 v[76:79], a[28:31], v[108:111], v[76:79]\n" \
"ds_read_b64_tr_b16 a[42:43], v181 offset:8192\n" \
"v_mfma_f32_16x16x32_bf16 v[80:83], a[16:19], v[112:115], v[80:83]\n" \
"ds_read_b128 v[104:107], v30 offset:17408\n" \
"v_mfma_f32_16x16x32_bf16 v[80:83], a[24:27], v[116:119], v[80:83]\n" \
"buffer_load_dwordx4 v12, s[28:31], s81 offen offset:128 lds\n" \
"v_mfma_f32_16x16x32_bf16 v[84:87], a[20:23], v[112:115], v[84:87]\n" \
"s_waitcnt lgkmcnt(5)\n" \
"ds_read_b128 v[108:111], v30 offset:19456\n" \
"s_mov_b32 s81, 2031616\n" \
"ds_read_b64_tr_b16 a[44:45], v182 offset:8192\n" \
"ds_read_b64_tr_b16 a[46:47], v183 offset:8192\n" \
"v_mfma_f32_16x16x32_bf16 v[84:87], a[28:31], v[116:119], v[84:87]\n" \
"s_add_u32 m0, s79, 40960\n" \
"v_mfma_f32_16x16x32_bf16 v[88:91], a[16:19], v[120:123], v[88:91]\n" \
"ds_read_b128 v[112:115], v30 offset:20480\n" \
"v_mfma_f32_16x16x32_bf16 v[88:91], a[24:27], v[124:127], v[88:91]\n" \
"buffer_load_dwordx4 v12, s[28:31], s81 offen offset:0 lds\n" \
"v_mfma_f32_16x16x32_bf16 v[92:95], a[20:23], v[120:123], v[92:95]\n" \
"s_add_u32 m0, s79, 41856\n" \
"ds_read_b128 v[116:119], v30 offset:22528\n" \
"v_mfma_f32_16x16x32_bf16 v[92:95], a[28:31], v[124:127], v[92:95]\n" \
"s_waitcnt vmcnt(12) lgkmcnt(2)\n" \
"s_barrier\n" \
"v_mfma_f32_16x16x32_bf16 v[32:35], a[32:35], v[96:99], v[32:35]\n" \
"ds_read_b128 v[120:123], v30 offset:21504\n" \
"v_mfma_f32_16x16x32_bf16 v[32:35], a[40:43], v[100:103], v[32:35]\n" \
"buffer_load_dwordx4 v12, s[28:31], s81 offen offset:128 lds\n" \
"v_mfma_f32_16x16x32_bf16 v[36:39], a[36:39], v[96:99], v[36:39]\n" \
"s_waitcnt lgkmcnt(5)\n" \
"ds_read_b128 v[124:127], v30 offset:23552\n" \
"s_add_u32 s80, s66, 49152\n" \
"v_mfma_f32_16x16x32_bf16 v[36:39], a[44:47], v[100:103], v[36:39]\n" \
"s_add_u32 m0, s22, 98304\n" \
"ds_read_b64_tr_b16 a[48:49], v180 offset:49152\n" \
"v_mfma_f32_16x16x32_bf16 v[40:43], a[32:35], v[104:107], v[40:43]\n" \
"ds_read_b128 v[96:99], v30 offset:24576\n" \
"v_mfma_f32_16x16x32_bf16 v[40:43], a[40:43], v[108:111], v[40:43]\n" \
"buffer_load_dwordx4 v4, s[36:39], s80 offen offset:0 sc1 lds\n" \
"v_mfma_f32_16x16x32_bf16 v[44:47], a[36:39], v[104:107], v[44:47]\n" \
"s_waitcnt lgkmcnt(4)\n" \
"ds_read_b128 v[100:103], v30 offset:26624\n" \
"v_mfma_f32_16x16x32_bf16 v[44:47], a[44:47], v[108:111], v[44:47]\n" \
"ds_read_b64_tr_b16 a[50:51], v181 offset:49152\n" \
"v_mfma_f32_16x16x32_bf16 v[48:51], a[32:35], v[112:115], v[48:51]\n" \
"ds_read_b128 v[104:107], v30 offset:25600\n" \
"v_mfma_f32_16x16x32_bf16 v[48:51], a[40:43], v[116:119], v[48:51]\n" \
"buffer_load_dwordx4 v4, s[36:39], s80 offen offset:1024 sc1 lds\n" \
"v_mfma_f32_16x16x32_bf16 v[52:55], a[36:39], v[112:115], v[52:55]\n" \
"s_waitcnt lgkmcnt(5)\n" \
"ds_read_b128 v[108:111], v30 offset:27648\n" \
"v_mfma_f32_16x16x32_bf16 v[52:55], a[44:47], v[116:119], v[52:55]\n" \
"ds_read_b64_tr_b16 a[52:53], v182 offset:49152\n" \
"v_mfma_f32_16x16x32_bf16 v[56:59], a[32:35], v[120:123], v[56:59]\n" \
"ds_read_b128 v[112:115], v28 offset:0\n" \
"v_mfma_f32_16x16x32_bf16 v[56:59], a[40:43], v[124:127], v[56:59]\n" \
"buffer_load_dwordx4 v4, s[36:39], s80 offen offset:2048 sc1 lds\n" \
"v_mfma_f32_16x16x32_bf16 v[60:63], a[36:39], v[120:123], v[60:63]\n" \
"ds_read_b128 v[116:119], v28 offset:2048\n" \
"v_mfma_f32_16x16x32_bf16 v[60:63], a[44:47], v[124:127], v[60:63]\n" \
"s_waitcnt vmcnt(12) lgkmcnt(6)\n" \
"s_barrier\n" \
"ds_read_b64_tr_b16 a[54:55], v183 offset:49152\n" \
"v_mfma_f32_16x16x32_bf16 v[64:67], a[32:35], v[96:99], v[64:67]\n" \
"ds_read_b128 v[120:123], v28 offset:1024\n" \
"s_waitcnt vmcnt(9)\n" \
"v_mfma_f32_16x16x32_bf16 v[64:67], a[40:43], v[100:103], v[64:67]\n" \
"buffer_load_dwordx4 v4, s[36:39], s80 offen offset:3072 sc1 lds\n" \
"v_mfma_f32_16x16x32_bf16 v[68:71], a[36:39], v[96:99], v[68:71]\n" \
"s_waitcnt lgkmcnt(5)\n" \
"ds_read_b128 v[124:127], v28 offset:3072\n" \
"v_cmp_ne_u64 vcc, s[14:15], v[18:19]\n" \
"v_mfma_f32_16x16x32_bf16 v[68:71], a[44:47], v[100:103], v[68:71]\n" \
"s_nop 0\n" \
"s_cbranch_vccnz LPL_%=\nLPLR_%=:\n" \
"v_mfma_f32_16x16x32_bf16 v[72:75], a[32:35], v[104:107], v[72:75]\n" \
"s_add_u32 s80, s68, 0\n" \
"ds_read_b128 v[96:99], v30 offset:28672\n" \
"s_add_u32 m0, s22, 49152\n" \
"v_mfma_f32_16x16x32_bf16 v[72:75], a[40:43], v[108:111], v[72:75]\n" \
"ds_read_b64_tr_b16 a[56:57], v180 offset:57344\n" \
"v_mfma_f32_16x16x32_bf16 v[76:79], a[36:39], v[104:107], v[76:79]\n" \
"s_waitcnt lgkmcnt(5)\n" \
"ds_read_b128 v[100:103], v30 offset:30720\n" \
"v_mfma_f32_16x16x32_bf16 v[76:79], a[44:47], v[108:111], v[76:79]\n" \
"buffer_load_dwordx4 v31, s[36:39], s80 offen offset:0 sc1 lds\n" \
"v_mfma_f32_16x16x32_bf16 v[80:83], a[32:35], v[112:115], v[80:83]\n" \
"ds_read_b128 v[104:107], v30 offset:29696\n" \
"v_mfma_f32_16x16x32_bf16 v[80:83], a[40:43], v[116:119], v[80:83]\n" \
"ds_read_b64_tr_b16 a[58:59], v181 offset:57344\n" \
"v_mfma_f32_16x16x32_bf16 v[84:87], a[36:39], v[112:115], v[84:87]\n" \
"s_waitcnt lgkmcnt(5)\n" \
"ds_read_b128 v[108:111], v30 offset:31744\n" \
"ds_read_b64_tr_b16 a[60:61], v182 offset:57344\n" \
"ds_read_b64_tr_b16 a[62:63], v183 offset:57344\n" \
"v_mfma_f32_16x16x32_bf16 v[84:87], a[44:47], v[116:119], v[84:87]\n" \
"buffer_load_dwordx4 v31, s[36:39], s80 offen offset:1024 sc1 lds\n" \
"v_mfma_f32_16x16x32_bf16 v[88:91], a[32:35], v[120:123], v[88:91]\n" \
"ds_read_b128 v[112:115], v30 offset:0\n" \
"v_mfma_f32_16x16x32_bf16 v[88:91], a[40:43], v[124:127], v[88:91]\n" \
"v_mfma_f32_16x16x32_bf16 v[92:95], a[36:39], v[120:123], v[92:95]\n" \
"ds_read_b128 v[116:119], v30 offset:2048\n" \
"v_mfma_f32_16x16x32_bf16 v[92:95], a[44:47], v[124:127], v[92:95]\n" \
"s_waitcnt vmcnt(10) lgkmcnt(2)\n" \
"s_barrier\n" \
"v_mfma_f32_16x16x32_bf16 v[32:35], a[48:51], v[96:99], v[32:35]\n" \
"ds_read_b128 v[120:123], v30 offset:1024\n" \
"v_mfma_f32_16x16x32_bf16 v[32:35], a[56:59], v[100:103], v[32:35]\n" \
"buffer_load_dwordx4 v31, s[36:39], s80 offen offset:2048 sc1 lds\n" \
"v_mfma_f32_16x16x32_bf16 v[36:39], a[52:55], v[96:99], v[36:39]\n" \
"s_waitcnt lgkmcnt(5)\n" \
"ds_read_b128 v[124:127], v30 offset:3072\n" \
"v_mfma_f32_16x16x32_bf16 v[36:39], a[60:63], v[100:103], v[36:39]\n" \
"ds_read_b64_tr_b16 a[96:97], v176 offset:16384\n" \
"v_mfma_f32_16x16x32_bf16 v[40:43], a[48:51], v[104:107], v[40:43]\n" \
"ds_read_b128 v[96:99], v30 offset:4096\n" \
"v_mfma_f32_16x16x32_bf16 v[40:43], a[56:59], v[108:111], v[40:43]\n" \
"buffer_load_dwordx4 v31, s[36:39], s80 offen offset:3072 sc1 lds\n" \
"v_mfma_f32_16x16x32_bf16 v[44:47], a[52:55], v[104:107], v[44:47]\n" \
"s_waitcnt lgkmcnt(4)\n" \
"ds_read_b128 v[100:103], v30 offset:6144\n" \
"s_add_u32 s80, s68, 16384\n" \
"v_mfma_f32_16x16x32_bf16 v[44:47], a[60:63], v[108:111], v[44:47]\n" \
"s_add_u32 m0, s22, 65536\n" \
"ds_read_b64_tr_b16 a[98:99], v177 offset:16384\n" \
"v_mfma_f32_16x16x32_bf16 v[48:51], a[48:51], v[112:115], v[48:51]\n" \
"ds_read_b128 v[104:107], v30 offset:5120\n" \
"v_mfma_f32_16x16x32_bf16 v[48:51], a[56:59], v[116:119], v[48:51]\n" \
"buffer_load_dwordx4 v31, s[36:39], s80 offen offset:0 sc1 lds\n" \
"v_mfma_f32_16x16x32_bf16 v[52:55], a[52:55], v[112:115], v[52:55]\n" \
"s_waitcnt lgkmcnt(5)\n" \
"ds_read_b128 v[108:111], v30 offset:7168\n" \
"v_mfma_f32_16x16x32_bf16 v[52:55], a[60:63], v[116:119], v[52:55]\n" \
"ds_read_b64_tr_b16 a[100:101], v178 offset:16384\n" \
"v_mfma_f32_16x16x32_bf16 v[56:59], a[48:51], v[120:123], v[56:59]\n" \
"ds_read_b128 v[112:115], v28 offset:4096\n" \
"v_mfma_f32_16x16x32_bf16 v[56:59], a[56:59], v[124:127], v[56:59]\n" \
"buffer_load_dwordx4 v31, s[36:39], s80 offen offset:1024 sc1 lds\n" \
"v_mfma_f32_16x16x32_bf16 v[60:63], a[52:55], v[120:123], v[60:63]\n" \
"ds_read_b128 v[116:119], v28 offset:6144\n" \
"v_mfma_f32_16x16x32_bf16 v[60:63], a[60:63], v[124:127], v[60:63]\n" \
"s_waitcnt lgkmcnt(6)\n" \
"s_barrier\n" \
"ds_read_b64_tr_b16 a[102:103], v179 offset:16384\n" \
"v_mfma_f32_16x16x32_bf16 v[64:67], a[48:51], v[96:99], v[64:67]\n" \
"ds_read_b128 v[120:123], v28 offset:5120\n" \
"v_mfma_f32_16x16x32_bf16 v[64:67], a[56:59], v[100:103], v[64:67]\n" \
"buffer_load_dwordx4 v31, s[36:39], s80 offen offset:2048 sc1 lds\n" \
"v_mfma_f32_16x16x32_bf16 v[68:71], a[52:55], v[96:99], v[68:71]\n" \
"s_waitcnt lgkmcnt(5)\n" \
"ds_read_b128 v[124:127], v28 offset:7168\n" \
"v_mfma_f32_16x16x32_bf16 v[68:71], a[60:63], v[100:103], v[68:71]\n" \
"ds_read_b64_tr_b16 a[104:105], v176 offset:24576\n" \
"v_mfma_f32_16x16x32_bf16 v[72:75], a[48:51], v[104:107], v[72:75]\n" \
"ds_read_b128 v[96:99], v30 offset:8192\n" \
"v_mfma_f32_16x16x32_bf16 v[72:75], a[56:59], v[108:111], v[72:75]\n" \
"buffer_load_dwordx4 v31, s[36:39], s80 offen offset:3072 sc1 lds\n" \
"v_mfma_f32_16x16x32_bf16 v[76:79], a[52:55], v[104:107], v[76:79]\n" \
"s_waitcnt lgkmcnt(5)\n" \
"ds_read_b128 v[100:103], v30 offset:10240\n" \
"s_add_u32 s80, s68, 32768\n" \
"v_mfma_f32_16x16x32_bf16 v[76:79], a[60:63], v[108:111], v[76:79]\n" \
"s_add_u32 m0, s22, 81920\n" \
"ds_read_b64_tr_b16 a[106:107], v177 offset:24576\n" \
"v_mfma_f32_16x16x32_bf16 v[80:83], a[48:51], v[112:115], v[80:83]\n" \
"ds_read_b128 v[104:107], v30 offset:9216\n" \
"v_mfma_f32_16x16x32_bf16 v[80:83], a[56:59], v[116:119], v[80:83]\n" \
"buffer_load_dwordx4 v31, s[36:39], s80 offen offset:0 sc1 lds\n" \
"v_mfma_f32_16x16x32_bf16 v[84:87], a[52:55], v[112:115], v[84:87]\n" \
"s_waitcnt lgkmcnt(5)\n" \
"ds_read_b128 v[108:111], v30 offset:11264\n" \
"ds_read_b64_tr_b16 a[108:109], v178 offset:24576\n" \
"ds_read_b64_tr_b16 a[110:111], v179 offset:24576\n" \
"v_mfma_f32_16x16x32_bf16 v[84:87], a[60:63], v[116:119], v[84:87]\n" \
"v_mfma_f32_16x16x32_bf16 v[88:91], a[48:51], v[120:123], v[88:91]\n" \
"ds_read_b128 v[112:115], v30 offset:12288\n" \
"v_mfma_f32_16x16x32_bf16 v[88:91], a[56:59], v[124:127], v[88:91]\n" \
"buffer_load_dwordx4 v31, s[36:39], s80 offen offset:1024 sc1 lds\n" \
"v_mfma_f32_16x16x32_bf16 v[92:95], a[52:55], v[120:123], v[92:95]\n" \
"ds_read_b128 v[116:119], v30 offset:14336\n" \
"v_mfma_f32_16x16x32_bf16 v[92:95], a[60:63], v[124:127], v[92:95]\n" \
"s_waitcnt vmcnt(10) lgkmcnt(2)\n" \
"s_barrier\n" \
"v_mfma_f32_16x16x32_bf16 v[32:35], a[96:99], v[96:99], v[32:35]\n" \
"ds_read_b128 v[120:123], v30 offset:13312\n" \
"v_mfma_f32_16x16x32_bf16 v[32:35], a[104:107], v[100:103], v[32:35]\n" \
"buffer_load_dwordx4 v31, s[36:39], s80 offen offset:2048 sc1 lds\n" \
"v_mfma_f32_16x16x32_bf16 v[36:39], a[100:103], v[96:99], v[36:39]\n" \
"s_waitcnt lgkmcnt(5)\n" \
"ds_read_b128 v[124:127], v30 offset:15360\n" \
"v_mfma_f32_16x16x32_bf16 v[36:39], a[108:111], v[100:103], v[36:39]\n" \
"ds_read_b64_tr_b16 a[112:113], v176 offset:32768\n" \
"v_mfma_f32_16x16x32_bf16 v[40:43], a[96:99], v[104:107], v[40:43]\n" \
"ds_read_b128 v[96:99], v29 offset:32768\n" \
"v_mfma_f32_16x16x32_bf16 v[40:43], a[104:107], v[108:111], v[40:43]\n" \
"buffer_load_dwordx4 v31, s[36:39], s80 offen offset:3072 sc1 lds\n" \
"v_mfma_f32_16x16x32_bf16 v[44:47], a[100:103], v[104:107], v[44:47]\n" \
"s_waitcnt lgkmcnt(4)\n" \
"ds_read_b128 v[100:103], v29 offset:34816\n" \
"s_add_u32 s80, s68, 49152\n" \
"v_mfma_f32_16x16x32_bf16 v[44:47], a[108:111], v[108:111], v[44:47]\n" \
"s_add_u32 m0, s22, 114688\n" \
"ds_read_b64_tr_b16 a[114:115], v177 offset:32768\n" \
"v_mfma_f32_16x16x32_bf16 v[48:51], a[96:99], v[112:115], v[48:51]\n" \
"ds_read_b128 v[104:107], v29 offset:33792\n" \
"v_mfma_f32_16x16x32_bf16 v[48:51], a[104:107], v[116:119], v[48:51]\n" \
"buffer_load_dwordx4 v31, s[36:39], s80 offen offset:0 sc1 lds\n" \
"v_mfma_f32_16x16x32_bf16 v[52:55], a[100:103], v[112:115], v[52:55]\n" \
"s_waitcnt lgkmcnt(5)\n" \
"ds_read_b128 v[108:111], v29 offset:35840\n" \
"v_mfma_f32_16x16x32_bf16 v[52:55], a[108:111], v[116:119], v[52:55]\n" \
"ds_read_b64_tr_b16 a[116:117], v178 offset:32768\n" \
"v_mfma_f32_16x16x32_bf16 v[56:59], a[96:99], v[120:123], v[56:59]\n" \
"ds_read_b128 v[112:115], v28 offset:8192\n" \
"v_mfma_f32_16x16x32_bf16 v[56:59], a[104:107], v[124:127], v[56:59]\n" \
"buffer_load_dwordx4 v31, s[36:39], s80 offen offset:1024 sc1 lds\n" \
"v_mfma_f32_16x16x32_bf16 v[60:63], a[100:103], v[120:123], v[60:63]\n" \
"ds_read_b128 v[116:119], v28 offset:10240\n" \
"v_mfma_f32_16x16x32_bf16 v[60:63], a[108:111], v[124:127], v[60:63]\n" \
"s_waitcnt lgkmcnt(6)\n" \
"s_barrier\n" \
"ds_read_b64_tr_b16 a[118:119], v179 offset:32768\n" \
"v_mfma_f32_16x16x32_bf16 v[64:67], a[96:99], v[96:99], v[64:67]\n" \
"ds_read_b128 v[120:123], v28 offset:9216\n" \
"v_mfma_f32_16x16x32_bf16 v[64:67], a[104:107], v[100:103], v[64:67]\n" \
"buffer_load_dwordx4 v31, s[36:39], s80 offen offset:2048 sc1 lds\n" \
"v_mfma_f32_16x16x32_bf16 v[68:71], a[100:103], v[96:99], v[68:71]\n" \
"s_waitcnt lgkmcnt(5)\n" \
"ds_read_b128 v[124:127], v28 offset:11264\n" \
"v_mfma_f32_16x16x32_bf16 v[68:71], a[108:111], v[100:103], v[68:71]\n" \
"ds_read_b64_tr_b16 a[120:121], v176 offset:40960\n" \
"v_mfma_f32_16x16x32_bf16 v[72:75], a[96:99], v[104:107], v[72:75]\n" \
"ds_read_b128 v[96:99], v29 offset:36864\n" \
"v_mfma_f32_16x16x32_bf16 v[72:75], a[104:107], v[108:111], v[72:75]\n" \
"buffer_load_dwordx4 v31, s[36:39], s80 offen offset:3072 sc1 lds\n" \
"v_mfma_f32_16x16x32_bf16 v[76:79], a[100:103], v[104:107], v[76:79]\n" \
"s_waitcnt lgkmcnt(5)\n" \
"ds_read_b128 v[100:103], v29 offset:38912\n" \
"s_add_u32 s80, s69, 0\n" \
"v_mfma_f32_16x16x32_bf16 v[76:79], a[108:111], v[108:111], v[76:79]\n" \
"s_add_u32 m0, s22, 147456\n" \
"ds_read_b64_tr_b16 a[122:123], v177 offset:40960\n" \
"v_mfma_f32_16x16x32_bf16 v[80:83], a[96:99], v[112:115], v[80:83]\n" \
"ds_read_b128 v[104:107], v29 offset:37888\n" \
"v_mfma_f32_16x16x32_bf16 v[80:83], a[104:107], v[116:119], v[80:83]\n" \
"buffer_load_dwordx4 v31, s[36:39], s80 offen offset:0 sc1 lds\n" \
"v_mfma_f32_16x16x32_bf16 v[84:87], a[100:103], v[112:115], v[84:87]\n" \
"s_waitcnt lgkmcnt(5)\n" \
"ds_read_b128 v[108:111], v29 offset:39936\n" \
"ds_read_b64_tr_b16 a[124:125], v178 offset:40960\n" \
"ds_read_b64_tr_b16 a[126:127], v179 offset:40960\n" \
"v_mfma_f32_16x16x32_bf16 v[84:87], a[108:111], v[116:119], v[84:87]\n" \
"v_mfma_f32_16x16x32_bf16 v[88:91], a[96:99], v[120:123], v[88:91]\n" \
"ds_read_b128 v[112:115], v29 offset:40960\n" \
"v_mfma_f32_16x16x32_bf16 v[88:91], a[104:107], v[124:127], v[88:91]\n" \
"buffer_load_dwordx4 v31, s[36:39], s80 offen offset:1024 sc1 lds\n" \
"v_mfma_f32_16x16x32_bf16 v[92:95], a[100:103], v[120:123], v[92:95]\n" \
"ds_read_b128 v[116:119], v29 offset:43008\n" \
"v_mfma_f32_16x16x32_bf16 v[92:95], a[108:111], v[124:127], v[92:95]\n" \
"s_waitcnt lgkmcnt(2)\n" \
"s_barrier\n" \
"v_mfma_f32_16x16x32_bf16 v[32:35], a[112:115], v[96:99], v[32:35]\n" \
"ds_read_b128 v[120:123], v29 offset:41984\n" \
"v_mfma_f32_16x16x32_bf16 v[32:35], a[120:123], v[100:103], v[32:35]\n" \
"buffer_load_dwordx2 v[176:177], v185, s[36:39], s86 offen offset:0 sc1\n" \
"v_mfma_f32_16x16x32_bf16 v[36:39], a[116:119], v[96:99], v[36:39]\n" \
"s_waitcnt lgkmcnt(5)\n" \
"ds_read_b128 v[124:127], v29 offset:44032\n" \
"v_mfma_f32_16x16x32_bf16 v[36:39], a[124:127], v[100:103], v[36:39]\n" \
"v_mfma_f32_16x16x32_bf16 v[40:43], a[112:115], v[104:107], v[40:43]\n" \
"ds_read_b128 v[96:99], v29 offset:45056\n" \
"v_mfma_f32_16x16x32_bf16 v[40:43], a[120:123], v[108:111], v[40:43]\n" \
"buffer_load_dwordx2 v[178:179], v185, s[36:39], s87 offen offset:0 sc1\n" \
"v_mfma_f32_16x16x32_bf16 v[44:47], a[116:119], v[104:107], v[44:47]\n" \
"s_waitcnt lgkmcnt(3)\n" \
"ds_read_b128 v[100:103], v29 offset:47104\n" \
"v_mfma_f32_16x16x32_bf16 v[44:47], a[124:127], v[108:111], v[44:47]\n" \
"v_mfma_f32_16x16x32_bf16 v[48:51], a[112:115], v[112:115], v[48:51]\n" \
"ds_read_b128 v[104:107], v29 offset:46080\n" \
"v_mfma_f32_16x16x32_bf16 v[48:51], a[120:123], v[116:119], v[48:51]\n" \
"buffer_load_dwordx4 v31, s[36:39], s80 offen offset:2048 sc1 lds\n" \
"v_mfma_f32_16x16x32_bf16 v[52:55], a[116:119], v[112:115], v[52:55]\n" \
"s_waitcnt lgkmcnt(3)\n" \
"ds_read_b128 v[108:111], v29 offset:48128\n" \
"v_mfma_f32_16x16x32_bf16 v[52:55], a[124:127], v[116:119], v[52:55]\n" \
"v_mfma_f32_16x16x32_bf16 v[56:59], a[112:115], v[120:123], v[56:59]\n" \
"ds_read_b128 v[112:115], v28 offset:12288\n" \
"v_mfma_f32_16x16x32_bf16 v[56:59], a[120:123], v[124:127], v[56:59]\n" \
"buffer_load_dwordx4 v31, s[36:39], s80 offen offset:3072 sc1 lds\n" \
"v_mfma_f32_16x16x32_bf16 v[60:63], a[116:119], v[120:123], v[60:63]\n" \
"s_add_u32 s80, s69, 16384\n" \
"ds_read_b128 v[116:119], v28 offset:14336\n" \
"s_add_u32 m0, s22, 16384\n" \
"v_mfma_f32_16x16x32_bf16 v[60:63], a[124:127], v[124:127], v[60:63]\n" \
"s_waitcnt vmcnt(18) lgkmcnt(4)\n" \
"s_barrier\n" \
"v_mfma_f32_16x16x32_bf16 v[64:67], a[112:115], v[96:99], v[64:67]\n" \
"ds_read_b128 v[120:123], v28 offset:13312\n" \
"v_mfma_f32_16x16x32_bf16 v[64:67], a[120:123], v[100:103], v[64:67]\n" \
"buffer_load_dwordx4 v31, s[36:39], s80 offen offset:0 sc1 lds\n" \
"v_mfma_f32_16x16x32_bf16 v[68:71], a[116:119], v[96:99], v[68:71]\n" \
"s_waitcnt lgkmcnt(3)\n" \
"ds_read_b128 v[124:127], v28 offset:15360\n" \
"v_mfma_f32_16x16x32_bf16 v[68:71], a[124:127], v[100:103], v[68:71]\n" \
"v_mfma_f32_16x16x32_bf16 v[72:75], a[112:115], v[104:107], v[72:75]\n" \
"ds_read_b128 v[96:99], v28 offset:49152\n" \
"v_mfma_f32_16x16x32_bf16 v[72:75], a[120:123], v[108:111], v[72:75]\n" \
"buffer_load_dwordx4 v31, s[36:39], s80 offen offset:1024 sc1 lds\n" \
"v_mfma_f32_16x16x32_bf16 v[76:79], a[116:119], v[104:107], v[76:79]\n" \
"s_waitcnt lgkmcnt(3)\n" \
"ds_read_b128 v[100:103], v28 offset:51200\n" \
"v_mfma_f32_16x16x32_bf16 v[76:79], a[124:127], v[108:111], v[76:79]\n" \
"v_mfma_f32_16x16x32_bf16 v[80:83], a[112:115], v[112:115], v[80:83]\n" \
"ds_read_b128 v[104:107], v28 offset:50176\n" \
"v_mfma_f32_16x16x32_bf16 v[80:83], a[120:123], v[116:119], v[80:83]\n" \
"buffer_load_dwordx4 v31, s[36:39], s80 offen offset:2048 sc1 lds\n" \
"v_mfma_f32_16x16x32_bf16 v[84:87], a[116:119], v[112:115], v[84:87]\n" \
"s_waitcnt lgkmcnt(3)\n" \
"ds_read_b128 v[108:111], v28 offset:52224\n" \
"v_mfma_f32_16x16x32_bf16 v[84:87], a[124:127], v[116:119], v[84:87]\n" \
"v_mfma_f32_16x16x32_bf16 v[88:91], a[112:115], v[120:123], v[88:91]\n" \
"ds_read_b128 v[112:115], v28 offset:53248\n" \
"v_mfma_f32_16x16x32_bf16 v[88:91], a[120:123], v[124:127], v[88:91]\n" \
"buffer_load_dwordx4 v31, s[36:39], s80 offen offset:3072 sc1 lds\n" \
"v_mfma_f32_16x16x32_bf16 v[92:95], a[116:119], v[120:123], v[92:95]\n" \
"s_add_u32 s80, s69, 32768\n" \
"ds_read_b128 v[116:119], v28 offset:55296\n" \
"s_add_u32 m0, s22, 131072\n" \
"v_mfma_f32_16x16x32_bf16 v[92:95], a[124:127], v[124:127], v[92:95]\n" \
"s_waitcnt lgkmcnt(4)\n" \
"s_barrier\n" \
"v_mfma_f32_16x16x32_bf16 v[192:195], a[160:163], v[96:99], 0\n" \
"ds_read_b128 v[120:123], v28 offset:54272\n" \
"v_mfma_f32_16x16x32_bf16 v[192:195], a[168:171], v[100:103], v[192:195]\n" \
"buffer_load_dwordx4 v31, s[36:39], s80 offen offset:0 sc1 lds\n" \
"v_mfma_f32_16x16x32_bf16 v[196:199], a[164:167], v[96:99], 0\n" \
"s_waitcnt lgkmcnt(3)\n" \
"ds_read_b128 v[124:127], v28 offset:56320\n" \
"v_cvt_pk_bf16_f32 v32, v32, v33\n" \
"v_mfma_f32_16x16x32_bf16 v[196:199], a[172:175], v[100:103], v[196:199]\n" \
"v_cvt_pk_bf16_f32 v33, v34, v35\n" \
"v_cvt_pk_bf16_f32 v34, v36, v37\n" \
"v_mfma_f32_16x16x32_bf16 v[200:203], a[160:163], v[104:107], 0\n" \
"ds_read_b128 v[96:99], v28 offset:57344\n" \
"v_cvt_pk_bf16_f32 v35, v38, v39\n" \
"v_mfma_f32_16x16x32_bf16 v[200:203], a[168:171], v[108:111], v[200:203]\n" \
"buffer_store_dwordx4 v[32:35], v4, s[36:39], s72 offen offset:0 sc1\n" \
"v_mfma_f32_16x16x32_bf16 v[204:207], a[164:167], v[104:107], 0\n" \
"s_waitcnt lgkmcnt(3)\n" \
"ds_read_b128 v[100:103], v28 offset:59392\n" \
"v_cvt_pk_bf16_f32 v40, v40, v41\n" \
"v_mfma_f32_16x16x32_bf16 v[204:207], a[172:175], v[108:111], v[204:207]\n" \
"v_cvt_pk_bf16_f32 v41, v42, v43\n" \
"v_cvt_pk_bf16_f32 v42, v44, v45\n" \
"v_mfma_f32_16x16x32_bf16 v[192:195], a[176:179], v[112:115], v[192:195]\n" \
"ds_read_b128 v[104:107], v28 offset:58368\n" \
"v_cvt_pk_bf16_f32 v43, v46, v47\n" \
"v_mfma_f32_16x16x32_bf16 v[192:195], a[184:187], v[116:119], v[192:195]\n" \
"buffer_load_dwordx4 v31, s[36:39], s80 offen offset:1024 sc1 lds\n" \
"v_mfma_f32_16x16x32_bf16 v[196:199], a[180:183], v[112:115], v[196:199]\n" \
"s_waitcnt lgkmcnt(3)\n" \
"ds_read_b128 v[108:111], v28 offset:60416\n" \
"v_mfma_f32_16x16x32_bf16 v[196:199], a[188:191], v[116:119], v[196:199]\n" \
"v_mfma_f32_16x16x32_bf16 v[200:203], a[176:179], v[120:123], v[200:203]\n" \
"ds_read_b128 v[112:115], v28 offset:61440\n" \
"v_mfma_f32_16x16x32_bf16 v[200:203], a[184:187], v[124:127], v[200:203]\n" \
"buffer_store_dwordx4 v[40:43], v4, s[36:39], s72 offen offset:1024 sc1\n" \
"v_mfma_f32_16x16x32_bf16 v[204:207], a[180:183], v[120:123], v[204:207]\n" \
"ds_read_b128 v[116:119], v28 offset:63488\n" \
"v_mfma_f32_16x16x32_bf16 v[204:207], a[188:191], v[124:127], v[204:207]\n" \
"s_waitcnt vmcnt(22) lgkmcnt(4)\n" \
"s_barrier\n" \
"v_mfma_f32_16x16x32_bf16 v[192:195], a[192:195], v[96:99], v[192:195]\n" \
"ds_read_b128 v[120:123], v28 offset:62464\n" \
"v_mfma_f32_16x16x32_bf16 v[192:195], a[200:203], v[100:103], v[192:195]\n" \
"v_mfma_f32_16x16x32_bf16 v[196:199], a[196:199], v[96:99], v[196:199]\n" \
"s_waitcnt lgkmcnt(3)\n" \
"ds_read_b128 v[124:127], v28 offset:64512\n" \
"v_mfma_f32_16x16x32_bf16 v[196:199], a[204:207], v[100:103], v[196:199]\n" \
"v_mfma_f32_16x16x32_bf16 v[200:203], a[192:195], v[104:107], v[200:203]\n" \
"ds_read_b128 v[96:99], v29 offset:0\n" \
"v_mfma_f32_16x16x32_bf16 v[200:203], a[200:203], v[108:111], v[200:203]\n" \
"buffer_load_dwordx4 v31, s[36:39], s80 offen offset:2048 sc1 lds\n" \
"v_mfma_f32_16x16x32_bf16 v[204:207], a[196:199], v[104:107], v[204:207]\n" \
"s_waitcnt lgkmcnt(3)\n" \
"ds_read_b128 v[100:103], v29 offset:2048\n" \
"v_mfma_f32_16x16x32_bf16 v[204:207], a[204:207], v[108:111], v[204:207]\n" \
"s_waitcnt vmcnt(11)\n" \
"v_mfma_f32_16x16x32_bf16 v[192:195], a[208:211], v[112:115], v[192:195]\n" \
"ds_read_b128 v[104:107], v29 offset:1024\n" \
"v_mfma_f32_16x16x32_bf16 v[192:195], a[216:219], v[116:119], v[192:195]\n" \
"buffer_load_dwordx4 v31, s[36:39], s80 offen offset:3072 sc1 lds\n" \
"v_mfma_f32_16x16x32_bf16 v[196:199], a[212:215], v[112:115], v[196:199]\n" \
"s_waitcnt lgkmcnt(3)\n" \
"ds_read_b128 v[108:111], v29 offset:3072\n" \
"s_add_u32 s80, s69, 49152\n" \
"v_mfma_f32_16x16x32_bf16 v[196:199], a[220:223], v[116:119], v[196:199]\n" \
"s_add_u32 m0, s22, 32768\n" \
"v_mfma_f32_16x16x32_bf16 v[200:203], a[208:211], v[120:123], v[200:203]\n" \
"ds_read_b128 v[112:115], v29 offset:4096\n" \
"v_mfma_f32_16x16x32_bf16 v[200:203], a[216:219], v[124:127], v[200:203]\n" \
"buffer_load_dwordx4 v31, s[36:39], s80 offen offset:0 sc1 lds\n" \
"v_mfma_f32_16x16x32_bf16 v[204:207], a[212:215], v[120:123], v[204:207]\n" \
"s_waitcnt lgkmcnt(3)\n" \
"ds_read_b128 v[116:119], v29 offset:6144\n" \
"v_mfma_f32_16x16x32_bf16 v[204:207], a[220:223], v[124:127], v[204:207]\n" \
"v_mfma_f32_16x16x32_bf16 v[192:195], a[224:227], v[96:99], v[192:195]\n" \
"ds_read_b128 v[120:123], v29 offset:5120\n" \
"v_mfma_f32_16x16x32_bf16 v[192:195], a[232:235], v[100:103], v[192:195]\n" \
"buffer_load_dwordx4 v31, s[36:39], s80 offen offset:1024 sc1 lds\n" \
"v_mfma_f32_16x16x32_bf16 v[196:199], a[228:231], v[96:99], v[196:199]\n" \
"s_waitcnt lgkmcnt(3)\n" \
"ds_read_b128 v[124:127], v29 offset:7168\n" \
"v_mfma_f32_16x16x32_bf16 v[196:199], a[236:239], v[100:103], v[196:199]\n" \
"v_mfma_f32_16x16x32_bf16 v[200:203], a[224:227], v[104:107], v[200:203]\n" \
"ds_read_b128 v[96:99], v29 offset:8192\n" \
"v_mfma_f32_16x16x32_bf16 v[200:203], a[232:235], v[108:111], v[200:203]\n" \
"buffer_load_dwordx4 v31, s[36:39], s80 offen offset:2048 sc1 lds\n" \
"v_mfma_f32_16x16x32_bf16 v[204:207], a[228:231], v[104:107], v[204:207]\n" \
"s_waitcnt lgkmcnt(3)\n" \
"ds_read_b128 v[100:103], v29 offset:10240\n" \
"v_cvt_pk_bf16_f32 v48, v48, v49\n" \
"v_mfma_f32_16x16x32_bf16 v[204:207], a[236:239], v[108:111], v[204:207]\n" \
"v_cvt_pk_bf16_f32 v49, v50, v51\n" \
"v_cvt_pk_bf16_f32 v50, v52, v53\n" \
"v_mfma_f32_16x16x32_bf16 v[192:195], a[240:243], v[112:115], v[192:195]\n" \
"ds_read_b128 v[104:107], v29 offset:9216\n" \
"v_cvt_pk_bf16_f32 v51, v54, v55\n" \
"v_mfma_f32_16x16x32_bf16 v[192:195], a[248:251], v[116:119], v[192:195]\n" \
"buffer_store_dwordx4 v[48:51], v4, s[36:39], s73 offen offset:0 sc1\n" \
"v_mfma_f32_16x16x32_bf16 v[196:199], a[244:247], v[112:115], v[196:199]\n" \
"s_waitcnt lgkmcnt(3)\n" \
"ds_read_b128 v[108:111], v29 offset:11264\n" \
"v_cvt_pk_bf16_f32 v56, v56, v57\n" \
"v_mfma_f32_16x16x32_bf16 v[196:199], a[252:255], v[116:119], v[196:199]\n" \
"v_cvt_pk_bf16_f32 v57, v58, v59\n" \
"v_cvt_pk_bf16_f32 v58, v60, v61\n" \
"v_mfma_f32_16x16x32_bf16 v[200:203], a[240:243], v[120:123], v[200:203]\n" \
"ds_read_b128 v[112:115], v29 offset:12288\n" \
"v_cvt_pk_bf16_f32 v59, v62, v63\n" \
"v_mfma_f32_16x16x32_bf16 v[200:203], a[248:251], v[124:127], v[200:203]\n" \
"v_mfma_f32_16x16x32_bf16 v[204:207], a[244:247], v[120:123], v[204:207]\n" \
"ds_read_b128 v[116:119], v29 offset:14336\n" \
"v_mfma_f32_16x16x32_bf16 v[204:207], a[252:255], v[124:127], v[204:207]\n" \
"s_waitcnt vmcnt(24) lgkmcnt(4)\n" \
"s_barrier\n" \
"v_mfma_f32_16x16x32_bf16 v[192:195], a[64:67], v[96:99], v[192:195]\n" \
"ds_read_b128 v[120:123], v29 offset:13312\n" \
"v_mfma_f32_16x16x32_bf16 v[192:195], a[72:75], v[100:103], v[192:195]\n" \
"buffer_store_dwordx4 v[56:59], v4, s[36:39], s73 offen offset:1024 sc1\n" \
"v_mfma_f32_16x16x32_bf16 v[196:199], a[68:71], v[96:99], v[196:199]\n" \
"s_waitcnt lgkmcnt(3)\n" \
"ds_read_b128 v[124:127], v29 offset:15360\n" \
"v_permlane16_swap_b32 v144, v146\n" \
"v_mfma_f32_16x16x32_bf16 v[196:199], a[76:79], v[100:103], v[196:199]\n" \
"v_permlane16_swap_b32 v145, v147\n" \
"v_sub_f32 v5, 1.0, v176\n" \
"v_mfma_f32_16x16x32_bf16 v[200:203], a[64:67], v[104:107], v[200:203]\n" \
"ds_read_b128 v[96:99], v29 offset:16384\n" \
"v_cvt_pk_bf16_f32 v5, v5, v5\n" \
"v_mfma_f32_16x16x32_bf16 v[200:203], a[72:75], v[108:111], v[200:203]\n" \
"v_and_b32 v5, 0xffff0000, v5\n" \
"v_lshlrev_b32 v32, 16, v144\n" \
"v_mfma_f32_16x16x32_bf16 v[204:207], a[68:71], v[104:107], v[204:207]\n" \
"s_waitcnt lgkmcnt(3)\n" \
"ds_read_b128 v[100:103], v29 offset:18432\n" \
"v_and_b32 v33, 0xffff0000, v144\n" \
"v_mfma_f32_16x16x32_bf16 v[204:207], a[76:79], v[108:111], v[204:207]\n" \
"v_lshlrev_b32 v188, 16, v128\n" \
"v_and_b32 v189, 0xffff0000, v128\n" \
"v_mfma_f32_16x16x32_bf16 v[192:195], a[80:83], v[112:115], v[192:195]\n" \
"ds_read_b128 v[104:107], v29 offset:17408\n" \
"v_mul_f32 v32, v176, v32\n" \
"v_mfma_f32_16x16x32_bf16 v[192:195], a[88:91], v[116:119], v[192:195]\n" \
"buffer_load_dwordx4 v31, s[36:39], s80 offen offset:3072 sc1 lds\n" \
"v_mfma_f32_16x16x32_bf16 v[196:199], a[84:87], v[112:115], v[196:199]\n" \
"s_waitcnt lgkmcnt(3)\n" \
"ds_read_b128 v[108:111], v29 offset:19456\n" \
"s_add_u32 s80, s70, 0\n" \
"v_mfma_f32_16x16x32_bf16 v[196:199], a[92:95], v[116:119], v[196:199]\n" \
"s_add_u32 m0, s22, 0\n" \
"v_mul_f32 v33, v176, v33\n" \
"v_mul_f32 v32, v188, v32\n" \
"v_mfma_f32_16x16x32_bf16 v[200:203], a[80:83], v[120:123], v[200:203]\n" \
"ds_read_b128 v[112:115], v29 offset:20480\n" \
"v_mul_f32 v33, v189, v33\n" \
"v_mfma_f32_16x16x32_bf16 v[200:203], a[88:91], v[124:127], v[200:203]\n" \
"buffer_load_dwordx4 v31, s[36:39], s80 offen offset:0 sc1 lds\n" \
"v_mfma_f32_16x16x32_bf16 v[204:207], a[84:87], v[120:123], v[204:207]\n" \
"s_waitcnt lgkmcnt(3)\n" \
"ds_read_b128 v[116:119], v29 offset:22528\n" \
"v_lshlrev_b32 v34, 16, v145\n" \
"v_mfma_f32_16x16x32_bf16 v[204:207], a[92:95], v[124:127], v[204:207]\n" \
"v_and_b32 v35, 0xffff0000, v145\n" \
"v_lshlrev_b32 v188, 16, v129\n" \
"v_mfma_f32_16x16x32_bf16 v[192:195], a[128:131], v[96:99], v[192:195]\n" \
"ds_read_b128 v[120:123], v29 offset:21504\n" \
"v_and_b32 v189, 0xffff0000, v129\n" \
"v_mfma_f32_16x16x32_bf16 v[192:195], a[136:139], v[100:103], v[192:195]\n" \
"buffer_load_dwordx4 v31, s[36:39], s80 offen offset:1024 sc1 lds\n" \
"v_mfma_f32_16x16x32_bf16 v[196:199], a[132:135], v[96:99], v[196:199]\n" \
"s_waitcnt lgkmcnt(3)\n" \
"ds_read_b128 v[124:127], v29 offset:23552\n" \
"v_mul_f32 v34, v176, v34\n" \
"v_mfma_f32_16x16x32_bf16 v[196:199], a[140:143], v[100:103], v[196:199]\n" \
"v_mul_f32 v35, v176, v35\n" \
"v_mul_f32 v34, v188, v34\n" \
"v_mfma_f32_16x16x32_bf16 v[200:203], a[128:131], v[104:107], v[200:203]\n" \
"ds_read_b128 v[96:99], v29 offset:24576\n" \
"v_mul_f32 v35, v189, v35\n" \
"v_mfma_f32_16x16x32_bf16 v[200:203], a[136:139], v[108:111], v[200:203]\n" \
"buffer_load_dwordx4 v31, s[36:39], s80 offen offset:2048 sc1 lds\n" \
"v_mfma_f32_16x16x32_bf16 v[204:207], a[132:135], v[104:107], v[204:207]\n" \
"s_waitcnt lgkmcnt(3)\n" \
"ds_read_b128 v[100:103], v29 offset:26624\n" \
"v_lshlrev_b32 v36, 16, v146\n" \
"v_mfma_f32_16x16x32_bf16 v[204:207], a[140:143], v[108:111], v[204:207]\n" \
"v_and_b32 v37, 0xffff0000, v146\n" \
"v_lshlrev_b32 v188, 16, v132\n" \
"v_mfma_f32_16x16x32_bf16 v[192:195], a[144:147], v[112:115], v[192:195]\n" \
"ds_read_b128 v[104:107], v29 offset:25600\n" \
"v_and_b32 v189, 0xffff0000, v132\n" \
"v_mfma_f32_16x16x32_bf16 v[192:195], a[152:155], v[116:119], v[192:195]\n" \
"buffer_load_dwordx4 v31, s[36:39], s80 offen offset:3072 sc1 lds\n" \
"v_mfma_f32_16x16x32_bf16 v[196:199], a[148:151], v[112:115], v[196:199]\n" \
"s_waitcnt lgkmcnt(3)\n" \
"ds_read_b128 v[108:111], v29 offset:27648\n" \
"s_add_u32 s80, s70, 16384\n" \
"v_mfma_f32_16x16x32_bf16 v[196:199], a[156:159], v[116:119], v[196:199]\n" \
"s_add_u32 m0, s22, 98304\n" \
"s_cmp_eq_u32 s89, 3\ns_cbranch_scc1 LSW_%=\nLSWR_%=:\n" \
"v_mfma_f32_16x16x32_bf16 v[200:203], a[144:147], v[120:123], v[200:203]\n" \
"ds_read_b128 v[112:115], v29 offset:28672\n" \
"v_cvt_pk_bf16_f32 v80, v80, v81\n" \
"v_mfma_f32_16x16x32_bf16 v[200:203], a[152:155], v[124:127], v[200:203]\n" \
"buffer_load_dwordx4 v31, s[36:39], s80 offen offset:0 sc1 lds\n" \
"v_mfma_f32_16x16x32_bf16 v[204:207], a[148:151], v[120:123], v[204:207]\n" \
"ds_read_b128 v[116:119], v29 offset:30720\n" \
"v_cvt_pk_bf16_f32 v81, v82, v83\n" \
"v_mfma_f32_16x16x32_bf16 v[204:207], a[156:159], v[124:127], v[204:207]\n" \
"s_waitcnt vmcnt(27) lgkmcnt(4)\n" \
"s_barrier\n" \
"v_cvt_pk_bf16_f32 v82, v84, v85\n" \
"v_mfma_f32_16x16x32_bf16 v[192:195], a[0:3], v[96:99], v[192:195]\n" \
"ds_read_b128 v[120:123], v29 offset:29696\n" \
"v_cvt_pk_bf16_f32 v83, v86, v87\n" \
"v_mfma_f32_16x16x32_bf16 v[192:195], a[8:11], v[100:103], v[192:195]\n" \
"buffer_store_dwordx4 v[80:83], v4, s[36:39], s74 offen offset:0 sc1\n" \
"v_mfma_f32_16x16x32_bf16 v[196:199], a[4:7], v[96:99], v[196:199]\n" \
"s_waitcnt lgkmcnt(3)\n" \
"ds_read_b128 v[124:127], v29 offset:31744\n" \
"v_cvt_pk_bf16_f32 v88, v88, v89\n" \
"v_mfma_f32_16x16x32_bf16 v[196:199], a[12:15], v[100:103], v[196:199]\n" \
"s_waitcnt vmcnt(14)\n" \
"v_cvt_pk_bf16_f32 v89, v90, v91\n" \
"v_cvt_pk_bf16_f32 v90, v92, v93\n" \
"v_mfma_f32_16x16x32_bf16 v[200:203], a[0:3], v[104:107], v[200:203]\n" \
"LH28_%=:\ns_cbranch_execz LHR2_%=\n" \
"ds_read_b128 v[96:99], v29 offset:49152\n" \
"v_cvt_pk_bf16_f32 v91, v94, v95\n" \
"v_mfma_f32_16x16x32_bf16 v[200:203], a[8:11], v[108:111], v[200:203]\n" \
"buffer_store_dwordx2 v[10:11], v20, s[36:39], s23 offen sc1\n" \
"v_mfma_f32_16x16x32_bf16 v[204:207], a[4:7], v[104:107], v[204:207]\n" \
"s_waitcnt lgkmcnt(3)\n" \
"ds_read_b128 v[100:103], v29 offset:51200\n" \
"v_mul_f32 v36, v176, v36\n" \
"v_mfma_f32_16x16x32_bf16 v[204:207], a[12:15], v[108:111], v[204:207]\n" \
"v_mul_f32 v37, v176, v37\n" \
"v_mul_f32 v36, v188, v36\n" \
"v_mfma_f32_16x16x32_bf16 v[192:195], a[16:19], v[112:115], v[192:195]\n" \
"ds_read_b128 v[104:107], v29 offset:50176\n" \
"v_mul_f32 v37, v189, v37\n" \
"v_mfma_f32_16x16x32_bf16 v[192:195], a[24:27], v[116:119], v[192:195]\n" \
"v_lshlrev_b32 v38, 16, v147\n" \
"v_and_b32 v39, 0xffff0000, v147\n" \
"v_mfma_f32_16x16x32_bf16 v[196:199], a[20:23], v[112:115], v[196:199]\n" \
"s_waitcnt lgkmcnt(3)\n" \
"ds_read_b128 v[108:111], v29 offset:52224\n" \
"v_lshlrev_b32 v188, 16, v133\n" \
"v_mfma_f32_16x16x32_bf16 v[196:199], a[28:31], v[116:119], v[196:199]\n" \
"buffer_store_dwordx4 v[88:91], v4, s[36:39], s74 offen offset:1024 sc1\n" \
"v_mfma_f32_16x16x32_bf16 v[200:203], a[16:19], v[120:123], v[200:203]\n" \
"ds_read_b128 v[112:115], v29 offset:53248\n" \
"v_and_b32 v189, 0xffff0000, v133\n" \
"v_mfma_f32_16x16x32_bf16 v[200:203], a[24:27], v[124:127], v[200:203]\n" \
"v_mul_f32 v38, v176, v38\n" \
"v_mul_f32 v39, v176, v39\n" \
"v_mfma_f32_16x16x32_bf16 v[204:207], a[20:23], v[120:123], v[204:207]\n" \
"s_waitcnt lgkmcnt(3)\n" \
"ds_read_b128 v[116:119], v29 offset:55296\n" \
"v_mul_f32 v38, v188, v38\n" \
"v_mfma_f32_16x16x32_bf16 v[204:207], a[28:31], v[124:127], v[204:207]\n" \
"v_mul_f32 v39, v189, v39\n" \
"v_permlane16_swap_b32 v148, v150\n" \
"v_mfma_f32_16x16x32_bf16 v[192:195], a[32:35], v[96:99], v[192:195]\n" \
"ds_read_b128 v[120:123], v29 offset:54272\n" \
"v_permlane16_swap_b32 v149, v151\n" \
"v_mfma_f32_16x16x32_bf16 v[192:195], a[40:43], v[100:103], v[192:195]\n" \
"v_sub_f32 v21, 1.0, v177\n" \
"v_cvt_pk_bf16_f32 v21, v21, v21\n" \
"v_mfma_f32_16x16x32_bf16 v[196:199], a[36:39], v[96:99], v[196:199]\n" \
"s_waitcnt lgkmcnt(3)\n" \
"ds_read_b128 v[124:127], v29 offset:56320\n" \
"v_and_b32 v21, 0xffff0000, v21\n" \
"v_mfma_f32_16x16x32_bf16 v[196:199], a[44:47], v[100:103], v[196:199]\n" \
"buffer_load_dwordx4 v31, s[36:39], s80 offen offset:1024 sc1 lds\n" \
"v_mfma_f32_16x16x32_bf16 v[200:203], a[32:35], v[104:107], v[200:203]\n" \
"ds_read_b128 v[96:99], v29 offset:57344\n" \
"v_lshlrev_b32 v40, 16, v148\n" \
"v_mfma_f32_16x16x32_bf16 v[200:203], a[40:43], v[108:111], v[200:203]\n" \
"v_and_b32 v41, 0xffff0000, v148\n" \
"v_lshlrev_b32 v188, 16, v130\n" \
"v_mfma_f32_16x16x32_bf16 v[204:207], a[36:39], v[104:107], v[204:207]\n" \
"s_waitcnt lgkmcnt(3)\n" \
"ds_read_b128 v[100:103], v29 offset:59392\n" \
"v_and_b32 v189, 0xffff0000, v130\n" \
"v_mfma_f32_16x16x32_bf16 v[204:207], a[44:47], v[108:111], v[204:207]\n" \
"buffer_load_dwordx4 v31, s[36:39], s80 offen offset:2048 sc1 lds\n" \
"v_mfma_f32_16x16x32_bf16 v[192:195], a[48:51], v[112:115], v[192:195]\n" \
"ds_read_b128 v[104:107], v29 offset:58368\n" \
"v_mul_f32 v40, v177, v40\n" \
"v_mfma_f32_16x16x32_bf16 v[192:195], a[56:59], v[116:119], v[192:195]\n" \
"v_mul_f32 v41, v177, v41\n" \
"v_mul_f32 v40, v188, v40\n" \
"v_mfma_f32_16x16x32_bf16 v[196:199], a[52:55], v[112:115], v[196:199]\n" \
"s_waitcnt lgkmcnt(3)\n" \
"ds_read_b128 v[108:111], v29 offset:60416\n" \
"v_mul_f32 v41, v189, v41\n" \
"v_mfma_f32_16x16x32_bf16 v[196:199], a[60:63], v[116:119], v[196:199]\n" \
"buffer_load_dwordx4 v31, s[36:39], s80 offen offset:3072 sc1 lds\n" \
"v_mfma_f32_16x16x32_bf16 v[200:203], a[48:51], v[120:123], v[200:203]\n" \
"s_add_u32 s80, s70, 32768\n" \
"ds_read_b128 v[112:115], v29 offset:61440\n" \
"s_add_u32 m0, s22, 49152\n" \
"v_mfma_f32_16x16x32_bf16 v[200:203], a[56:59], v[124:127], v[200:203]\n" \
"v_lshlrev_b32 v42, 16, v149\n" \
"v_and_b32 v43, 0xffff0000, v149\n" \
"v_mfma_f32_16x16x32_bf16 v[204:207], a[52:55], v[120:123], v[204:207]\n" \
"ds_read_b128 v[116:119], v29 offset:63488\n" \
"v_lshlrev_b32 v188, 16, v131\n" \
"v_mfma_f32_16x16x32_bf16 v[204:207], a[60:63], v[124:127], v[204:207]\n" \
"s_waitcnt vmcnt(27) lgkmcnt(4)\n" \
"s_barrier\n" \
"v_and_b32 v189, 0xffff0000, v131\n" \
"v_mfma_f32_16x16x32_bf16 v[192:195], a[96:99], v[96:99], v[192:195]\n" \
"ds_read_b128 v[120:123], v29 offset:62464\n" \
"v_mul_f32 v42, v177, v42\n" \
"v_mfma_f32_16x16x32_bf16 v[192:195], a[104:107], v[100:103], v[192:195]\n" \
"buffer_load_dwordx4 v31, s[36:39], s80 offen offset:0 sc1 lds\n" \
"v_mfma_f32_16x16x32_bf16 v[196:199], a[100:103], v[96:99], v[196:199]\n" \
"s_waitcnt lgkmcnt(3)\n" \
"ds_read_b128 v[124:127], v29 offset:64512\n" \
"v_mul_f32 v43, v177, v43\n" \
"v_mfma_f32_16x16x32_bf16 v[196:199], a[108:111], v[100:103], v[196:199]\n" \
"v_mul_f32 v42, v188, v42\n" \
"v_mul_f32 v43, v189, v43\n" \
"v_mfma_f32_16x16x32_bf16 v[200:203], a[96:99], v[104:107], v[200:203]\n" \
"ds_read_b128 v[96:99], v30 offset:16384\n" \
"v_lshlrev_b32 v44, 16, v150\n" \
"v_mfma_f32_16x16x32_bf16 v[200:203], a[104:107], v[108:111], v[200:203]\n" \
"buffer_load_dwordx4 v31, s[36:39], s80 offen offset:1024 sc1 lds\n" \
"v_mfma_f32_16x16x32_bf16 v[204:207], a[100:103], v[104:107], v[204:207]\n" \
"s_waitcnt lgkmcnt(3)\n" \
"ds_read_b128 v[100:103], v30 offset:18432\n" \
"v_and_b32 v45, 0xffff0000, v150\n" \
"v_mfma_f32_16x16x32_bf16 v[204:207], a[108:111], v[108:111], v[204:207]\n" \
"s_waitcnt vmcnt(14)\n" \
"v_lshlrev_b32 v188, 16, v134\n" \
"v_and_b32 v189, 0xffff0000, v134\n" \
"v_mfma_f32_16x16x32_bf16 v[192:195], a[112:115], v[112:115], v[192:195]\n" \
"ds_read_b128 v[104:107], v30 offset:17408\n" \
"v_mul_f32 v44, v177, v44\n" \
"v_mfma_f32_16x16x32_bf16 v[192:195], a[120:123], v[116:119], v[192:195]\n" \
"buffer_store_dwordx2 v[10:11], v20, s[36:39], s24 offen sc1\n" \
"v_mfma_f32_16x16x32_bf16 v[196:199], a[116:119], v[112:115], v[196:199]\n" \
"s_waitcnt lgkmcnt(3)\n" \
"ds_read_b128 v[108:111], v30 offset:19456\n" \
"v_mul_f32 v45, v177, v45\n" \
"v_mfma_f32_16x16x32_bf16 v[196:199], a[124:127], v[116:119], v[196:199]\n" \
"buffer_load_dwordx4 v31, s[36:39], s80 offen offset:2048 sc1 lds\n" \
"v_mfma_f32_16x16x32_bf16 v[200:203], a[112:115], v[120:123], v[200:203]\n" \
"ds_read_b128 v[112:115], v30 offset:20480\n" \
"v_mul_f32 v44, v188, v44\n" \
"v_mfma_f32_16x16x32_bf16 v[200:203], a[120:123], v[124:127], v[200:203]\n" \
"v_mul_f32 v45, v189, v45\n" \
"v_lshlrev_b32 v46, 16, v151\n" \
"v_mfma_f32_16x16x32_bf16 v[204:207], a[116:119], v[120:123], v[204:207]\n" \
"s_waitcnt lgkmcnt(3)\n" \
"ds_read_b128 v[116:119], v30 offset:22528\n" \
"v_and_b32 v47, 0xffff0000, v151\n" \
"v_mfma_f32_16x16x32_bf16 v[204:207], a[124:127], v[124:127], v[204:207]\n" \
"buffer_load_dwordx4 v31, s[36:39], s80 offen offset:3072 sc1 lds\n" \
"v_mfma_f32_16x16x32_bf16 v[208:211], a[160:163], v[96:99], 0\n" \
"s_add_u32 s80, s70, 49152\n" \
"ds_read_b128 v[120:123], v30 offset:21504\n" \
"s_add_u32 m0, s22, 65536\n" \
"v_mfma_f32_16x16x32_bf16 v[208:211], a[168:171], v[100:103], v[208:211]\n" \
"v_lshlrev_b32 v188, 16, v135\n" \
"v_and_b32 v189, 0xffff0000, v135\n" \
"v_mfma_f32_16x16x32_bf16 v[212:215], a[164:167], v[96:99], 0\n" \
"s_waitcnt lgkmcnt(3)\n" \
"ds_read_b128 v[124:127], v30 offset:23552\n" \
"v_cvt_pk_bf16_f32 v192, v192, v193\n" \
"v_mfma_f32_16x16x32_bf16 v[212:215], a[172:175], v[100:103], v[212:215]\n" \
"v_cvt_pk_bf16_f32 v193, v194, v195\n" \
"v_cvt_pk_bf16_f32 v194, v196, v197\n" \
"v_mfma_f32_16x16x32_bf16 v[216:219], a[160:163], v[104:107], 0\n" \
"ds_read_b128 v[96:99], v30 offset:24576\n" \
"v_cvt_pk_bf16_f32 v195, v198, v199\n" \
"v_mfma_f32_16x16x32_bf16 v[216:219], a[168:171], v[108:111], v[216:219]\n" \
"buffer_store_dwordx4 v[192:195], v4, s[36:39], s75 offen offset:0 sc1\n" \
"v_mfma_f32_16x16x32_bf16 v[220:223], a[164:167], v[104:107], 0\n" \
"s_waitcnt lgkmcnt(3)\n" \
"ds_read_b128 v[100:103], v30 offset:26624\n" \
"v_cvt_pk_bf16_f32 v200, v200, v201\n" \
"v_mfma_f32_16x16x32_bf16 v[220:223], a[172:175], v[108:111], v[220:223]\n" \
"v_cvt_pk_bf16_f32 v201, v202, v203\n" \
"v_cvt_pk_bf16_f32 v202, v204, v205\n" \
"v_mfma_f32_16x16x32_bf16 v[208:211], a[176:179], v[112:115], v[208:211]\n" \
"ds_read_b128 v[104:107], v30 offset:25600\n" \
"v_cvt_pk_bf16_f32 v203, v206, v207\n" \
"v_mfma_f32_16x16x32_bf16 v[208:211], a[184:187], v[116:119], v[208:211]\n" \
"buffer_load_dwordx4 v31, s[36:39], s80 offen offset:0 sc1 lds\n" \
"v_mfma_f32_16x16x32_bf16 v[212:215], a[180:183], v[112:115], v[212:215]\n" \
"s_waitcnt lgkmcnt(3)\n" \
"ds_read_b128 v[108:111], v30 offset:27648\n" \
"v_mul_f32 v46, v177, v46\n" \
"v_mfma_f32_16x16x32_bf16 v[212:215], a[188:191], v[116:119], v[212:215]\n" \
"v_mul_f32 v47, v177, v47\n" \
"v_mul_f32 v46, v188, v46\n" \
"v_mfma_f32_16x16x32_bf16 v[216:219], a[176:179], v[120:123], v[216:219]\n" \
"ds_read_b128 v[112:115], v30 offset:28672\n" \
"v_mul_f32 v47, v189, v47\n" \
"v_mfma_f32_16x16x32_bf16 v[216:219], a[184:187], v[124:127], v[216:219]\n" \
"buffer_store_dwordx4 v[200:203], v4, s[36:39], s75 offen offset:1024 sc1\n" \
"v_mfma_f32_16x16x32_bf16 v[220:223], a[180:183], v[120:123], v[220:223]\n" \
"ds_read_b128 v[116:119], v30 offset:30720\n" \
"v_permlane16_swap_b32 v152, v154\n" \
"v_mfma_f32_16x16x32_bf16 v[220:223], a[188:191], v[124:127], v[220:223]\n" \
"s_waitcnt vmcnt(31) lgkmcnt(4)\n" \
"s_barrier\n" \
"v_permlane16_swap_b32 v153, v155\n" \
"v_mfma_f32_16x16x32_bf16 v[208:211], a[192:195], v[96:99], v[208:211]\n" \
"ds_read_b128 v[120:123], v30 offset:29696\n" \
"v_sub_f32 v6, 1.0, v178\n" \
"v_mfma_f32_16x16x32_bf16 v[208:211], a[200:203], v[100:103], v[208:211]\n" \
"v_cvt_pk_bf16_f32 v6, v6, v6\n" \
"v_and_b32 v6, 0xffff0000, v6\n" \
"v_mfma_f32_16x16x32_bf16 v[212:215], a[196:199], v[96:99], v[212:215]\n" \
"s_waitcnt lgkmcnt(3)\n" \
"ds_read_b128 v[124:127], v30 offset:31744\n" \
"v_lshlrev_b32 v48, 16, v152\n" \
"v_mfma_f32_16x16x32_bf16 v[212:215], a[204:207], v[100:103], v[212:215]\n" \
"v_and_b32 v49, 0xffff0000, v152\n" \
"v_lshlrev_b32 v188, 16, v136\n" \
"v_mfma_f32_16x16x32_bf16 v[216:219], a[192:195], v[104:107], v[216:219]\n" \
"ds_read_b128 v[96:99], v28 offset:16384\n" \
"v_and_b32 v189, 0xffff0000, v136\n" \
"v_mfma_f32_16x16x32_bf16 v[216:219], a[200:203], v[108:111], v[216:219]\n" \
"buffer_load_dwordx4 v31, s[36:39], s80 offen offset:1024 sc1 lds\n" \
"v_mfma_f32_16x16x32_bf16 v[220:223], a[196:199], v[104:107], v[220:223]\n" \
"s_waitcnt lgkmcnt(3)\n" \
"ds_read_b128 v[100:103], v28 offset:18432\n" \
"v_mul_f32 v48, v178, v48\n" \
"v_mfma_f32_16x16x32_bf16 v[220:223], a[204:207], v[108:111], v[220:223]\n" \
"v_mul_f32 v49, v178, v49\n" \
"v_mul_f32 v48, v188, v48\n" \
"v_mfma_f32_16x16x32_bf16 v[208:211], a[208:211], v[112:115], v[208:211]\n" \
"ds_read_b128 v[104:107], v28 offset:17408\n" \
"v_mul_f32 v49, v189, v49\n" \
"v_mfma_f32_16x16x32_bf16 v[208:211], a[216:219], v[116:119], v[208:211]\n" \
"buffer_load_dwordx4 v31, s[36:39], s80 offen offset:2048 sc1 lds\n" \
"v_mfma_f32_16x16x32_bf16 v[212:215], a[212:215], v[112:115], v[212:215]\n" \
"s_waitcnt lgkmcnt(3)\n" \
"ds_read_b128 v[108:111], v28 offset:19456\n" \
"v_lshlrev_b32 v50, 16, v153\n" \
"v_mfma_f32_16x16x32_bf16 v[212:215], a[220:223], v[116:119], v[212:215]\n" \
"v_and_b32 v51, 0xffff0000, v153\n" \
"v_lshlrev_b32 v188, 16, v137\n" \
"v_mfma_f32_16x16x32_bf16 v[216:219], a[208:211], v[120:123], v[216:219]\n" \
"ds_read_b128 v[112:115], v28 offset:20480\n" \
"v_and_b32 v189, 0xffff0000, v137\n" \
"v_mfma_f32_16x16x32_bf16 v[216:219], a[216:219], v[124:127], v[216:219]\n" \
"buffer_load_dwordx4 v31, s[36:39], s80 offen offset:3072 sc1 lds\n" \
"v_mfma_f32_16x16x32_bf16 v[220:223], a[212:215], v[120:123], v[220:223]\n" \
"s_waitcnt lgkmcnt(3)\n" \
"ds_read_b128 v[116:119], v28 offset:22528\n" \
"v_mul_f32 v50, v178, v50\n" \
"v_mfma_f32_16x16x32_bf16 v[220:223], a[220:223], v[124:127], v[220:223]\n" \
"s_add_u32 s80, s71, 0\n" \
"s_add_u32 m0, s22, 81920\n" \
"v_mul_f32 v51, v178, v51\n" \
"v_mfma_f32_16x16x32_bf16 v[208:211], a[224:227], v[96:99], v[208:211]\n" \
"ds_read_b128 v[120:123], v28 offset:21504\n" \
"v_mul_f32 v50, v188, v50\n" \
"v_mfma_f32_16x16x32_bf16 v[208:211], a[232:235], v[100:103], v[208:211]\n" \
"s_waitcnt vmcnt(14)\n" \
"v_mul_f32 v51, v189, v51\n" \
"v_lshlrev_b32 v52, 16, v154\n" \
"v_mfma_f32_16x16x32_bf16 v[212:215], a[228:231], v[96:99], v[212:215]\n" \
"s_waitcnt lgkmcnt(3)\n" \
"ds_read_b128 v[124:127], v28 offset:23552\n" \
"v_and_b32 v53, 0xffff0000, v154\n" \
"v_mfma_f32_16x16x32_bf16 v[212:215], a[236:239], v[100:103], v[212:215]\n" \
"buffer_store_dwordx2 v[10:11], v20, s[36:39], s45 offen sc1\n" \
"v_mfma_f32_16x16x32_bf16 v[216:219], a[224:227], v[104:107], v[216:219]\n" \
"ds_read_b128 v[96:99], v28 offset:24576\n" \
"v_lshlrev_b32 v188, 16, v140\n" \
"v_mfma_f32_16x16x32_bf16 v[216:219], a[232:235], v[108:111], v[216:219]\n" \
"v_and_b32 v189, 0xffff0000, v140\n" \
"v_mul_f32 v52, v178, v52\n" \
"v_mfma_f32_16x16x32_bf16 v[220:223], a[228:231], v[104:107], v[220:223]\n" \
"s_waitcnt lgkmcnt(3)\n" \
"ds_read_b128 v[100:103], v28 offset:26624\n" \
"v_mul_f32 v53, v178, v53\n" \
"v_mfma_f32_16x16x32_bf16 v[220:223], a[236:239], v[108:111], v[220:223]\n" \
"buffer_load_dwordx4 v31, s[36:39], s80 offen offset:0 sc1 lds\n" \
"v_mfma_f32_16x16x32_bf16 v[208:211], a[240:243], v[112:115], v[208:211]\n" \
"ds_read_b128 v[104:107], v28 offset:25600\n" \
"v_mul_f32 v52, v188, v52\n" \
"v_mfma_f32_16x16x32_bf16 v[208:211], a[248:251], v[116:119], v[208:211]\n" \
"v_mul_f32 v53, v189, v53\n" \
"v_lshlrev_b32 v54, 16, v155\n" \
"v_mfma_f32_16x16x32_bf16 v[212:215], a[244:247], v[112:115], v[212:215]\n" \
"s_waitcnt lgkmcnt(3)\n" \
"ds_read_b128 v[108:111], v28 offset:27648\n" \
"v_and_b32 v55, 0xffff0000, v155\n" \
"v_mfma_f32_16x16x32_bf16 v[212:215], a[252:255], v[116:119], v[212:215]\n" \
"buffer_load_dwordx4 v31, s[36:39], s80 offen offset:1024 sc1 lds\n" \
"v_mfma_f32_16x16x32_bf16 v[216:219], a[240:243], v[120:123], v[216:219]\n" \
"ds_read_b128 v[112:115], v28 offset:28672\n" \
"v_lshlrev_b32 v188, 16, v141\n" \
"v_mfma_f32_16x16x32_bf16 v[216:219], a[248:251], v[124:127], v[216:219]\n" \
"v_and_b32 v189, 0xffff0000, v141\n" \
"v_mul_f32 v54, v178, v54\n" \
"v_mfma_f32_16x16x32_bf16 v[220:223], a[244:247], v[120:123], v[220:223]\n" \
"ds_read_b128 v[116:119], v28 offset:30720\n" \
"v_mul_f32 v55, v178, v55\n" \
"v_mfma_f32_16x16x32_bf16 v[220:223], a[252:255], v[124:127], v[220:223]\n" \
"s_waitcnt vmcnt(31) lgkmcnt(4)\n" \
"s_barrier\n" \
"v_mul_f32 v54, v188, v54\n" \
"v_mfma_f32_16x16x32_bf16 v[208:211], a[64:67], v[96:99], v[208:211]\n" \
"ds_read_b128 v[120:123], v28 offset:29696\n" \
"v_mul_f32 v55, v189, v55\n" \
"v_mfma_f32_16x16x32_bf16 v[208:211], a[72:75], v[100:103], v[208:211]\n" \
"buffer_load_dwordx4 v31, s[36:39], s80 offen offset:2048 sc1 lds\n" \
"v_mfma_f32_16x16x32_bf16 v[212:215], a[68:71], v[96:99], v[212:215]\n" \
"s_waitcnt lgkmcnt(3)\n" \
"ds_read_b128 v[124:127], v28 offset:31744\n" \
"v_permlane16_swap_b32 v156, v158\n" \
"v_mfma_f32_16x16x32_bf16 v[212:215], a[76:79], v[100:103], v[212:215]\n" \
"v_permlane16_swap_b32 v157, v159\n" \
"v_sub_f32 v8, 1.0, v179\n" \
"v_mfma_f32_16x16x32_bf16 v[216:219], a[64:67], v[104:107], v[216:219]\n" \
"ds_read_b128 v[96:99], v30 offset:0\n" \
"v_cvt_pk_bf16_f32 v8, v8, v8\n" \
"v_mfma_f32_16x16x32_bf16 v[216:219], a[72:75], v[108:111], v[216:219]\n" \
"buffer_load_dwordx4 v31, s[36:39], s80 offen offset:3072 sc1 lds\n" \
"v_mfma_f32_16x16x32_bf16 v[220:223], a[68:71], v[104:107], v[220:223]\n" \
"s_waitcnt lgkmcnt(3)\n" \
"ds_read_b128 v[100:103], v30 offset:2048\n" \
"v_and_b32 v8, 0xffff0000, v8\n" \
"v_mfma_f32_16x16x32_bf16 v[220:223], a[76:79], v[108:111], v[220:223]\n" \
"v_lshlrev_b32 v56, 16, v156\n" \
"v_and_b32 v57, 0xffff0000, v156\n" \
"v_mfma_f32_16x16x32_bf16 v[208:211], a[80:83], v[112:115], v[208:211]\n" \
"ds_read_b128 v[104:107], v30 offset:1024\n" \
"v_lshlrev_b32 v188, 16, v138\n" \
"v_mfma_f32_16x16x32_bf16 v[208:211], a[88:91], v[116:119], v[208:211]\n" \
"v_and_b32 v189, 0xffff0000, v138\n" \
"v_mul_f32 v56, v179, v56\n" \
"v_mfma_f32_16x16x32_bf16 v[212:215], a[84:87], v[112:115], v[212:215]\n" \
"s_waitcnt lgkmcnt(3)\n" \
"ds_read_b128 v[108:111], v30 offset:3072\n" \
"v_mul_f32 v57, v179, v57\n" \
"v_mfma_f32_16x16x32_bf16 v[212:215], a[92:95], v[116:119], v[212:215]\n" \
"v_mul_f32 v56, v188, v56\n" \
"v_mul_f32 v57, v189, v57\n" \
"v_mfma_f32_16x16x32_bf16 v[216:219], a[80:83], v[120:123], v[216:219]\n" \
"ds_read_b128 v[112:115], v30 offset:4096\n" \
"v_lshlrev_b32 v58, 16, v157\n" \
"v_mfma_f32_16x16x32_bf16 v[216:219], a[88:91], v[124:127], v[216:219]\n" \
"v_and_b32 v59, 0xffff0000, v157\n" \
"v_lshlrev_b32 v188, 16, v139\n" \
"v_mfma_f32_16x16x32_bf16 v[220:223], a[84:87], v[120:123], v[220:223]\n" \
"s_waitcnt lgkmcnt(3)\n" \
"ds_read_b128 v[116:119], v30 offset:6144\n" \
"v_and_b32 v189, 0xffff0000, v139\n" \
"v_mfma_f32_16x16x32_bf16 v[220:223], a[92:95], v[124:127], v[220:223]\n" \
"v_mul_f32 v58, v179, v58\n" \
"v_mul_f32 v59, v179, v59\n" \
"v_mfma_f32_16x16x32_bf16 v[208:211], a[128:131], v[96:99], v[208:211]\n" \
"ds_read_b128 v[120:123], v30 offset:5120\n" \
"v_mul_f32 v58, v188, v58\n" \
"v_mfma_f32_16x16x32_bf16 v[208:211], a[136:139], v[100:103], v[208:211]\n" \
"v_mul_f32 v59, v189, v59\n" \
"v_lshlrev_b32 v60, 16, v158\n" \
"v_mfma_f32_16x16x32_bf16 v[212:215], a[132:135], v[96:99], v[212:215]\n" \
"s_waitcnt lgkmcnt(3)\n" \
"ds_read_b128 v[124:127], v30 offset:7168\n" \
"v_and_b32 v61, 0xffff0000, v158\n" \
"v_mfma_f32_16x16x32_bf16 v[212:215], a[140:143], v[100:103], v[212:215]\n" \
"v_lshlrev_b32 v188, 16, v142\n" \
"v_and_b32 v189, 0xffff0000, v142\n" \
"v_mfma_f32_16x16x32_bf16 v[216:219], a[128:131], v[104:107], v[216:219]\n" \
"ds_read_b128 v[96:99], v30 offset:8192\n" \
"v_mul_f32 v60, v179, v60\n" \
"v_mfma_f32_16x16x32_bf16 v[216:219], a[136:139], v[108:111], v[216:219]\n" \
"v_mul_f32 v61, v179, v61\n" \
"v_mul_f32 v60, v188, v60\n" \
"v_mfma_f32_16x16x32_bf16 v[220:223], a[132:135], v[104:107], v[220:223]\n" \
"s_waitcnt lgkmcnt(3)\n" \
"ds_read_b128 v[100:103], v30 offset:10240\n" \
"v_mul_f32 v61, v189, v61\n" \
"v_mfma_f32_16x16x32_bf16 v[220:223], a[140:143], v[108:111], v[220:223]\n" \
"v_lshlrev_b32 v62, 16, v159\n" \
"v_and_b32 v63, 0xffff0000, v159\n" \
"v_mfma_f32_16x16x32_bf16 v[208:211], a[144:147], v[112:115], v[208:211]\n" \
"ds_read_b128 v[104:107], v30 offset:9216\n" \
"v_lshlrev_b32 v188, 16, v143\n" \
"v_mfma_f32_16x16x32_bf16 v[208:211], a[152:155], v[116:119], v[208:211]\n" \
"v_and_b32 v189, 0xffff0000, v143\n" \
"v_mul_f32 v62, v179, v62\n" \
"v_mfma_f32_16x16x32_bf16 v[212:215], a[148:151], v[112:115], v[212:215]\n" \
"s_waitcnt lgkmcnt(3)\n" \
"ds_read_b128 v[108:111], v30 offset:11264\n" \
"v_mul_f32 v63, v179, v63\n" \
"v_mfma_f32_16x16x32_bf16 v[212:215], a[156:159], v[116:119], v[212:215]\n" \
"s_add_u32 s80, s71, 16384\n" \
"s_add_u32 m0, s22, 114688\n" \
"v_mul_f32 v62, v188, v62\n" \
"v_mfma_f32_16x16x32_bf16 v[216:219], a[144:147], v[120:123], v[216:219]\n" \
"ds_read_b128 v[112:115], v30 offset:12288\n" \
"v_mul_f32 v63, v189, v63\n" \
"v_mfma_f32_16x16x32_bf16 v[216:219], a[152:155], v[124:127], v[216:219]\n" \
"v_mfma_f32_16x16x32_bf16 v[220:223], a[148:151], v[120:123], v[220:223]\n" \
"ds_read_b128 v[116:119], v30 offset:14336\n" \
"v_mfma_f32_16x16x32_bf16 v[220:223], a[156:159], v[124:127], v[220:223]\n" \
"s_waitcnt vmcnt(27) lgkmcnt(4)\n" \
"s_barrier\n" \
"v_mfma_f32_16x16x32_bf16 v[208:211], a[0:3], v[96:99], v[208:211]\n" \
"ds_read_b128 v[120:123], v30 offset:13312\n" \
"v_mfma_f32_16x16x32_bf16 v[208:211], a[8:11], v[100:103], v[208:211]\n" \
"v_mfma_f32_16x16x32_bf16 v[212:215], a[4:7], v[96:99], v[212:215]\n" \
"s_waitcnt lgkmcnt(3)\n" \
"ds_read_b128 v[124:127], v30 offset:15360\n" \
"v_mfma_f32_16x16x32_bf16 v[212:215], a[12:15], v[100:103], v[212:215]\n" \
"buffer_load_dwordx4 v31, s[36:39], s80 offen offset:0 sc1 lds\n" \
"v_mfma_f32_16x16x32_bf16 v[216:219], a[0:3], v[104:107], v[216:219]\n" \
"ds_read_b128 v[96:99], v28 offset:32768\n" \
"v_mfma_f32_16x16x32_bf16 v[216:219], a[8:11], v[108:111], v[216:219]\n" \
"v_mfma_f32_16x16x32_bf16 v[220:223], a[4:7], v[104:107], v[220:223]\n" \
"s_waitcnt lgkmcnt(3)\n" \
"ds_read_b128 v[100:103], v28 offset:34816\n" \
"v_mfma_f32_16x16x32_bf16 v[220:223], a[12:15], v[108:111], v[220:223]\n" \
"buffer_load_dwordx4 v31, s[36:39], s80 offen offset:1024 sc1 lds\n" \
"v_mfma_f32_16x16x32_bf16 v[208:211], a[16:19], v[112:115], v[208:211]\n" \
"ds_read_b128 v[104:107], v28 offset:33792\n" \
"v_mfma_f32_16x16x32_bf16 v[208:211], a[24:27], v[116:119], v[208:211]\n" \
"v_mfma_f32_16x16x32_bf16 v[212:215], a[20:23], v[112:115], v[212:215]\n" \
"s_waitcnt lgkmcnt(3)\n" \
"ds_read_b128 v[108:111], v28 offset:35840\n" \
"v_mfma_f32_16x16x32_bf16 v[212:215], a[28:31], v[116:119], v[212:215]\n" \
"buffer_load_dwordx4 v31, s[36:39], s80 offen offset:2048 sc1 lds\n" \
"v_mfma_f32_16x16x32_bf16 v[216:219], a[16:19], v[120:123], v[216:219]\n" \
"ds_read_b128 v[112:115], v28 offset:36864\n" \
"v_mfma_f32_16x16x32_bf16 v[216:219], a[24:27], v[124:127], v[216:219]\n" \
"v_mfma_f32_16x16x32_bf16 v[220:223], a[20:23], v[120:123], v[220:223]\n" \
"s_waitcnt lgkmcnt(3)\n" \
"ds_read_b128 v[116:119], v28 offset:38912\n" \
"v_mfma_f32_16x16x32_bf16 v[220:223], a[28:31], v[124:127], v[220:223]\n" \
"buffer_load_dwordx4 v31, s[36:39], s80 offen offset:3072 sc1 lds\n" \
"v_mfma_f32_16x16x32_bf16 v[208:211], a[32:35], v[96:99], v[208:211]\n" \
"ds_read_b128 v[120:123], v28 offset:37888\n" \
"v_mfma_f32_16x16x32_bf16 v[208:211], a[40:43], v[100:103], v[208:211]\n" \
"v_mfma_f32_16x16x32_bf16 v[212:215], a[36:39], v[96:99], v[212:215]\n" \
"s_waitcnt lgkmcnt(3)\n" \
"ds_read_b128 v[124:127], v28 offset:39936\n" \
"v_mfma_f32_16x16x32_bf16 v[212:215], a[44:47], v[100:103], v[212:215]\n" \
"v_mfma_f32_16x16x32_bf16 v[216:219], a[32:35], v[104:107], v[216:219]\n" \
"ds_read_b128 v[96:99], v28 offset:40960\n" \
"v_mfma_f32_16x16x32_bf16 v[216:219], a[40:43], v[108:111], v[216:219]\n" \
"v_mfma_f32_16x16x32_bf16 v[220:223], a[36:39], v[104:107], v[220:223]\n" \
"s_waitcnt lgkmcnt(3)\n" \
"ds_read_b128 v[100:103], v28 offset:43008\n" \
"v_mfma_f32_16x16x32_bf16 v[220:223], a[44:47], v[108:111], v[220:223]\n" \
"v_mfma_f32_16x16x32_bf16 v[208:211], a[48:51], v[112:115], v[208:211]\n" \
"ds_read_b128 v[104:107], v28 offset:41984\n" \
"v_mfma_f32_16x16x32_bf16 v[208:211], a[56:59], v[116:119], v[208:211]\n" \
"v_mfma_f32_16x16x32_bf16 v[212:215], a[52:55], v[112:115], v[212:215]\n" \
"s_waitcnt lgkmcnt(3)\n" \
"ds_read_b128 v[108:111], v28 offset:44032\n" \
"v_mfma_f32_16x16x32_bf16 v[212:215], a[60:63], v[116:119], v[212:215]\n" \
"s_add_u32 s80, s71, 32768\n" \
"s_add_u32 m0, s22, 147456\n" \
"v_mfma_f32_16x16x32_bf16 v[216:219], a[48:51], v[120:123], v[216:219]\n" \
"s_sub_u32 s96, s88, 94404608\n" \
"ds_read_b128 v[112:115], v28 offset:45056\n" \
"s_lshl_b32 s96, s96, 2\n" \
"v_mfma_f32_16x16x32_bf16 v[216:219], a[56:59], v[124:127], v[216:219]\n" \
"s_add_u32 s96, s96, 94437376\n" \
"v_bfe_u32 v17, v4, 4, 2\n" \
"v_min_u32 v17, 2, v17\n" \
"v_mfma_f32_16x16x32_bf16 v[220:223], a[52:55], v[120:123], v[220:223]\n" \
"ds_read_b128 v[116:119], v28 offset:47104\n" \
"v_lshl_add_u32 v17, v17, 3, s96\n" \
"v_mfma_f32_16x16x32_bf16 v[220:223], a[60:63], v[124:127], v[220:223]\n" \
"s_waitcnt vmcnt(27) lgkmcnt(4)\n" \
"s_barrier\n" \
"v_mfma_f32_16x16x32_bf16 v[208:211], a[96:99], v[96:99], v[208:211]\n" \
"ds_read_b128 v[120:123], v28 offset:46080\n" \
"v_mfma_f32_16x16x32_bf16 v[208:211], a[104:107], v[100:103], v[208:211]\n" \
"v_mfma_f32_16x16x32_bf16 v[212:215], a[100:103], v[96:99], v[212:215]\n" \
"s_waitcnt lgkmcnt(3)\n" \
"ds_read_b128 v[124:127], v28 offset:48128\n" \
"v_mfma_f32_16x16x32_bf16 v[212:215], a[108:111], v[100:103], v[212:215]\n" \
"buffer_load_dwordx4 v31, s[36:39], s80 offen offset:0 sc1 lds\n" \
"v_mfma_f32_16x16x32_bf16 v[216:219], a[96:99], v[104:107], v[216:219]\n" \
"ds_read_b128 v[96:99], v28 offset:0\n" \
"v_mfma_f32_16x16x32_bf16 v[216:219], a[104:107], v[108:111], v[216:219]\n" \
"v_mfma_f32_16x16x32_bf16 v[220:223], a[100:103], v[104:107], v[220:223]\n" \
"s_waitcnt lgkmcnt(3)\n" \
"ds_read_b128 v[100:103], v28 offset:2048\n" \
"v_mfma_f32_16x16x32_bf16 v[220:223], a[108:111], v[108:111], v[220:223]\n" \
"buffer_load_dwordx4 v31, s[36:39], s80 offen offset:1024 sc1 lds\n" \
"v_mfma_f32_16x16x32_bf16 v[208:211], a[112:115], v[112:115], v[208:211]\n" \
"ds_read_b128 v[104:107], v28 offset:1024\n" \
"v_mfma_f32_16x16x32_bf16 v[208:211], a[120:123], v[116:119], v[208:211]\n" \
"v_mfma_f32_16x16x32_bf16 v[212:215], a[116:119], v[112:115], v[212:215]\n" \
"s_waitcnt lgkmcnt(3)\n" \
"ds_read_b128 v[108:111], v28 offset:3072\n" \
"v_mfma_f32_16x16x32_bf16 v[212:215], a[124:127], v[116:119], v[212:215]\n" \
"buffer_load_dwordx2 v[18:19], v17, s[36:39], 0 offen sc1\n" \
"v_mfma_f32_16x16x32_bf16 v[216:219], a[112:115], v[120:123], v[216:219]\n" \
"ds_read_b128 v[112:115], v28 offset:4096\n" \
"v_mfma_f32_16x16x32_bf16 v[216:219], a[120:123], v[124:127], v[216:219]\n" \
"v_mfma_f32_16x16x32_bf16 v[220:223], a[116:119], v[120:123], v[220:223]\n" \
"s_waitcnt lgkmcnt(3)\n" \
"ds_read_b128 v[116:119], v28 offset:6144\n" \
"v_mfma_f32_16x16x32_bf16 v[220:223], a[124:127], v[124:127], v[220:223]\n" \
"buffer_load_dwordx4 v31, s[36:39], s80 offen offset:2048 sc1 lds\n" \
"v_mfma_f32_16x16x32_bf16 v[192:195], a[160:163], v[96:99], 0\n" \
"ds_read_b128 v[120:123], v28 offset:5120\n" \
"v_mfma_f32_16x16x32_bf16 v[192:195], a[168:171], v[100:103], v[192:195]\n" \
"v_mfma_f32_16x16x32_bf16 v[196:199], a[164:167], v[96:99], 0\n" \
"s_waitcnt lgkmcnt(3)\n" \
"ds_read_b128 v[124:127], v28 offset:7168\n" \
"v_cvt_pk_bf16_f32 v208, v208, v209\n" \
"v_mfma_f32_16x16x32_bf16 v[196:199], a[172:175], v[100:103], v[196:199]\n" \
"buffer_load_dwordx4 v31, s[36:39], s80 offen offset:3072 sc1 lds\n" \
"v_mfma_f32_16x16x32_bf16 v[200:203], a[160:163], v[104:107], 0\n" \
"ds_read_b128 v[96:99], v28 offset:8192\n" \
"v_cvt_pk_bf16_f32 v209, v210, v211\n" \
"v_mfma_f32_16x16x32_bf16 v[200:203], a[168:171], v[108:111], v[200:203]\n" \
"v_cvt_pk_bf16_f32 v210, v212, v213\n" \
"v_cvt_pk_bf16_f32 v211, v214, v215\n" \
"v_mfma_f32_16x16x32_bf16 v[204:207], a[164:167], v[104:107], 0\n" \
"s_waitcnt lgkmcnt(3)\n" \
"ds_read_b128 v[100:103], v28 offset:10240\n" \
"v_cvt_pk_bf16_f32 v216, v216, v217\n" \
"v_mfma_f32_16x16x32_bf16 v[204:207], a[172:175], v[108:111], v[204:207]\n" \
"buffer_store_dwordx4 v[208:211], v4, s[36:39], s76 offen offset:0 sc1\n" \
"v_mfma_f32_16x16x32_bf16 v[192:195], a[176:179], v[112:115], v[192:195]\n" \
"ds_read_b128 v[104:107], v28 offset:9216\n" \
"v_cvt_pk_bf16_f32 v217, v218, v219\n" \
"v_mfma_f32_16x16x32_bf16 v[192:195], a[184:187], v[116:119], v[192:195]\n" \
"v_cvt_pk_bf16_f32 v218, v220, v221\n" \
"v_cvt_pk_bf16_f32 v219, v222, v223\n" \
"v_mfma_f32_16x16x32_bf16 v[196:199], a[180:183], v[112:115], v[196:199]\n" \
"s_waitcnt lgkmcnt(3)\n" \
"ds_read_b128 v[108:111], v28 offset:11264\n" \
"v_mfma_f32_16x16x32_bf16 v[196:199], a[188:191], v[116:119], v[196:199]\n" \
"s_add_u32 s80, s71, 49152\n" \
"s_add_u32 m0, s22, 16384\n" \
"v_mfma_f32_16x16x32_bf16 v[200:203], a[176:179], v[120:123], v[200:203]\n" \
"ds_read_b128 v[112:115], v28 offset:12288\n" \
"v_mfma_f32_16x16x32_bf16 v[200:203], a[184:187], v[124:127], v[200:203]\n" \
"v_mfma_f32_16x16x32_bf16 v[204:207], a[180:183], v[120:123], v[204:207]\n" \
"ds_read_b128 v[116:119], v28 offset:14336\n" \
"v_mfma_f32_16x16x32_bf16 v[204:207], a[188:191], v[124:127], v[204:207]\n" \
"s_waitcnt vmcnt(26) lgkmcnt(4)\n" \
"s_barrier\n" \
"v_mfma_f32_16x16x32_bf16 v[192:195], a[192:195], v[96:99], v[192:195]\n" \
"ds_read_b128 v[120:123], v28 offset:13312\n" \
"v_mfma_f32_16x16x32_bf16 v[192:195], a[200:203], v[100:103], v[192:195]\n" \
"buffer_store_dwordx4 v[216:219], v4, s[36:39], s76 offen offset:1024 sc1\n" \
"v_mfma_f32_16x16x32_bf16 v[196:199], a[196:199], v[96:99], v[196:199]\n" \
"s_waitcnt lgkmcnt(3)\n" \
"ds_read_b128 v[124:127], v28 offset:15360\n" \
"v_mfma_f32_16x16x32_bf16 v[196:199], a[204:207], v[100:103], v[196:199]\n" \
"v_mfma_f32_16x16x32_bf16 v[200:203], a[192:195], v[104:107], v[200:203]\n" \
"ds_read_b128 v[96:99], v29 offset:32768\n" \
"v_mfma_f32_16x16x32_bf16 v[200:203], a[200:203], v[108:111], v[200:203]\n" \
"buffer_load_dwordx4 v31, s[36:39], s80 offen offset:0 sc1 lds\n" \
"v_mfma_f32_16x16x32_bf16 v[204:207], a[196:199], v[104:107], v[204:207]\n" \
"s_waitcnt lgkmcnt(3)\n" \
"ds_read_b128 v[100:103], v29 offset:34816\n" \
"v_mfma_f32_16x16x32_bf16 v[204:207], a[204:207], v[108:111], v[204:207]\n" \
"v_mfma_f32_16x16x32_bf16 v[192:195], a[208:211], v[112:115], v[192:195]\n" \
"ds_read_b128 v[104:107], v29 offset:33792\n" \
"v_mfma_f32_16x16x32_bf16 v[192:195], a[216:219], v[116:119], v[192:195]\n" \
"buffer_load_dwordx4 v31, s[36:39], s80 offen offset:1024 sc1 lds\n" \
"v_mfma_f32_16x16x32_bf16 v[196:199], a[212:215], v[112:115], v[196:199]\n" \
"s_waitcnt lgkmcnt(3)\n" \
"ds_read_b128 v[108:111], v29 offset:35840\n" \
"v_mfma_f32_16x16x32_bf16 v[196:199], a[220:223], v[116:119], v[196:199]\n" \
"v_mfma_f32_16x16x32_bf16 v[200:203], a[208:211], v[120:123], v[200:203]\n" \
"ds_read_b128 v[112:115], v29 offset:36864\n" \
"v_mfma_f32_16x16x32_bf16 v[200:203], a[216:219], v[124:127], v[200:203]\n" \
"buffer_load_dwordx4 v31, s[36:39], s80 offen offset:2048 sc1 lds\n" \
"v_mfma_f32_16x16x32_bf16 v[204:207], a[212:215], v[120:123], v[204:207]\n" \
"s_waitcnt lgkmcnt(3)\n" \
"ds_read_b128 v[116:119], v29 offset:38912\n" \
"v_mfma_f32_16x16x32_bf16 v[204:207], a[220:223], v[124:127], v[204:207]\n" \
"v_mfma_f32_16x16x32_bf16 v[192:195], a[224:227], v[96:99], v[192:195]\n" \
"ds_read_b128 v[120:123], v29 offset:37888\n" \
"v_mfma_f32_16x16x32_bf16 v[192:195], a[232:235], v[100:103], v[192:195]\n" \
"buffer_load_dwordx4 v31, s[36:39], s80 offen offset:3072 sc1 lds\n" \
"v_mfma_f32_16x16x32_bf16 v[196:199], a[228:231], v[96:99], v[196:199]\n" \
"s_waitcnt lgkmcnt(3)\n" \
"ds_read_b128 v[124:127], v29 offset:39936\n" \
"v_mfma_f32_16x16x32_bf16 v[196:199], a[236:239], v[100:103], v[196:199]\n" \
"v_mfma_f32_16x16x32_bf16 v[200:203], a[224:227], v[104:107], v[200:203]\n" \
"ds_read_b128 v[96:99], v29 offset:40960\n" \
"v_mfma_f32_16x16x32_bf16 v[200:203], a[232:235], v[108:111], v[200:203]\n" \
"v_mfma_f32_16x16x32_bf16 v[204:207], a[228:231], v[104:107], v[204:207]\n" \
"s_waitcnt lgkmcnt(3)\n" \
"ds_read_b128 v[100:103], v29 offset:43008\n" \
"s_waitcnt vmcnt(8)\n" \
"v_mfma_f32_16x16x32_bf16 v[204:207], a[236:239], v[108:111], v[204:207]\n" \
"v_cmp_ne_u64 vcc, s[14:15], v[18:19]\n" \
"s_nop 0\n" \
"v_mfma_f32_16x16x32_bf16 v[192:195], a[240:243], v[112:115], v[192:195]\n" \
"ds_read_b128 v[104:107], v29 offset:41984\n" \
"s_cbranch_vccnz LPE_%=\nLPER_%=:\n" \
"v_mfma_f32_16x16x32_bf16 v[192:195], a[248:251], v[116:119], v[192:195]\n" \
"s_add_u32 s97, s78, 0\n" \
"v_mfma_f32_16x16x32_bf16 v[196:199], a[244:247], v[112:115], v[196:199]\n" \
"s_waitcnt lgkmcnt(3)\n" \
"ds_read_b128 v[108:111], v29 offset:44032\n" \
"v_mfma_f32_16x16x32_bf16 v[196:199], a[252:255], v[116:119], v[196:199]\n" \
"buffer_load_dwordx4 v[128:131], v4, s[36:39], s97 offen offset:0 sc1\n" \
"v_mfma_f32_16x16x32_bf16 v[200:203], a[240:243], v[120:123], v[200:203]\n" \
"ds_read_b128 v[112:115], v29 offset:45056\n" \
"v_mfma_f32_16x16x32_bf16 v[200:203], a[248:251], v[124:127], v[200:203]\n" \
"v_mfma_f32_16x16x32_bf16 v[204:207], a[244:247], v[120:123], v[204:207]\n" \
"ds_read_b128 v[116:119], v29 offset:47104\n" \
"v_mfma_f32_16x16x32_bf16 v[204:207], a[252:255], v[124:127], v[204:207]\n" \
"LH29_%=:\ns_cbranch_execz LHR2_%=\n" \
"s_waitcnt vmcnt(27) lgkmcnt(4)\n" \
"s_barrier\n" \
"v_mfma_f32_16x16x32_bf16 v[192:195], a[64:67], v[96:99], v[192:195]\n" \
"ds_read_b128 v[120:123], v29 offset:46080\n" \
"v_mfma_f32_16x16x32_bf16 v[192:195], a[72:75], v[100:103], v[192:195]\n" \
"buffer_load_dwordx4 v[132:135], v4, s[36:39], s97 offen offset:1024 sc1\n" \
"v_mfma_f32_16x16x32_bf16 v[196:199], a[68:71], v[96:99], v[196:199]\n" \
"s_waitcnt lgkmcnt(3)\n" \
"ds_read_b128 v[124:127], v29 offset:48128\n" \
"v_mfma_f32_16x16x32_bf16 v[196:199], a[76:79], v[100:103], v[196:199]\n" \
"v_mfma_f32_16x16x32_bf16 v[200:203], a[64:67], v[104:107], v[200:203]\n" \
"ds_read_b128 v[96:99], v28 offset:49152\n" \
"v_mfma_f32_16x16x32_bf16 v[200:203], a[72:75], v[108:111], v[200:203]\n" \
"buffer_load_dwordx4 v[136:139], v4, s[36:39], s97 offen offset:2048 sc1\n" \
"v_mfma_f32_16x16x32_bf16 v[204:207], a[68:71], v[104:107], v[204:207]\n" \
"s_waitcnt lgkmcnt(3)\n" \
"ds_read_b128 v[100:103], v28 offset:51200\n" \
"v_mfma_f32_16x16x32_bf16 v[204:207], a[76:79], v[108:111], v[204:207]\n" \
"v_mfma_f32_16x16x32_bf16 v[192:195], a[80:83], v[112:115], v[192:195]\n" \
"ds_read_b128 v[104:107], v28 offset:50176\n" \
"v_mfma_f32_16x16x32_bf16 v[192:195], a[88:91], v[116:119], v[192:195]\n" \
"buffer_load_dwordx4 v[140:143], v4, s[36:39], s97 offen offset:3072 sc1\n" \
"v_mfma_f32_16x16x32_bf16 v[196:199], a[84:87], v[112:115], v[196:199]\n" \
"s_waitcnt lgkmcnt(3)\n" \
"ds_read_b128 v[108:111], v28 offset:52224\n" \
"s_add_u32 s97, s78, 4096\n" \
"v_mfma_f32_16x16x32_bf16 v[196:199], a[92:95], v[116:119], v[196:199]\n" \
"v_mfma_f32_16x16x32_bf16 v[200:203], a[80:83], v[120:123], v[200:203]\n" \
"ds_read_b128 v[112:115], v28 offset:53248\n" \
"v_mfma_f32_16x16x32_bf16 v[200:203], a[88:91], v[124:127], v[200:203]\n" \
"buffer_load_dwordx4 v[160:163], v4, s[36:39], s97 offen offset:0 sc1\n" \
"v_mfma_f32_16x16x32_bf16 v[204:207], a[84:87], v[120:123], v[204:207]\n" \
"s_waitcnt lgkmcnt(3)\n" \
"ds_read_b128 v[116:119], v28 offset:55296\n" \
"v_mfma_f32_16x16x32_bf16 v[204:207], a[92:95], v[124:127], v[204:207]\n" \
"v_mfma_f32_16x16x32_bf16 v[192:195], a[128:131], v[96:99], v[192:195]\n" \
"ds_read_b128 v[120:123], v28 offset:54272\n" \
"v_mfma_f32_16x16x32_bf16 v[192:195], a[136:139], v[100:103], v[192:195]\n" \
"buffer_load_dwordx4 v[164:167], v4, s[36:39], s97 offen offset:1024 sc1\n" \
"v_mfma_f32_16x16x32_bf16 v[196:199], a[132:135], v[96:99], v[196:199]\n" \
"s_waitcnt lgkmcnt(3)\n" \
"ds_read_b128 v[124:127], v28 offset:56320\n" \
"v_mfma_f32_16x16x32_bf16 v[196:199], a[140:143], v[100:103], v[196:199]\n" \
"v_mfma_f32_16x16x32_bf16 v[200:203], a[128:131], v[104:107], v[200:203]\n" \
"ds_read_b128 v[96:99], v28 offset:57344\n" \
"v_mfma_f32_16x16x32_bf16 v[200:203], a[136:139], v[108:111], v[200:203]\n" \
"v_mfma_f32_16x16x32_bf16 v[204:207], a[132:135], v[104:107], v[204:207]\n" \
"s_waitcnt lgkmcnt(3)\n" \
"ds_read_b128 v[100:103], v28 offset:59392\n" \
"v_mfma_f32_16x16x32_bf16 v[204:207], a[140:143], v[108:111], v[204:207]\n" \
"v_mfma_f32_16x16x32_bf16 v[192:195], a[144:147], v[112:115], v[192:195]\n" \
"ds_read_b128 v[104:107], v28 offset:58368\n" \
"v_mfma_f32_16x16x32_bf16 v[192:195], a[152:155], v[116:119], v[192:195]\n" \
"v_mfma_f32_16x16x32_bf16 v[196:199], a[148:151], v[112:115], v[196:199]\n" \
"s_waitcnt lgkmcnt(3)\n" \
"ds_read_b128 v[108:111], v28 offset:60416\n" \
"v_mfma_f32_16x16x32_bf16 v[196:199], a[156:159], v[116:119], v[196:199]\n" \
"v_mfma_f32_16x16x32_bf16 v[200:203], a[144:147], v[120:123], v[200:203]\n" \
"ds_read_b128 v[112:115], v28 offset:61440\n" \
"v_mfma_f32_16x16x32_bf16 v[200:203], a[152:155], v[124:127], v[200:203]\n" \
"v_mfma_f32_16x16x32_bf16 v[204:207], a[148:151], v[120:123], v[204:207]\n" \
"ds_read_b128 v[116:119], v28 offset:63488\n" \
"v_mfma_f32_16x16x32_bf16 v[204:207], a[156:159], v[124:127], v[204:207]\n" \
"s_waitcnt vmcnt(26) lgkmcnt(4)\n" \
"s_barrier\n" \
"v_mfma_f32_16x16x32_bf16 v[192:195], a[0:3], v[96:99], v[192:195]\n" \
"ds_read_b128 v[120:123], v28 offset:62464\n" \
"v_mfma_f32_16x16x32_bf16 v[192:195], a[8:11], v[100:103], v[192:195]\n" \
"v_mfma_f32_16x16x32_bf16 v[196:199], a[4:7], v[96:99], v[196:199]\n" \
"s_waitcnt lgkmcnt(3)\n" \
"ds_read_b128 v[124:127], v28 offset:64512\n" \
"v_mfma_f32_16x16x32_bf16 v[196:199], a[12:15], v[100:103], v[196:199]\n" \
"v_mfma_f32_16x16x32_bf16 v[200:203], a[0:3], v[104:107], v[200:203]\n" \
"ds_read_b128 v[96:99], v29 offset:0\n" \
"v_mfma_f32_16x16x32_bf16 v[200:203], a[8:11], v[108:111], v[200:203]\n" \
"v_mfma_f32_16x16x32_bf16 v[204:207], a[4:7], v[104:107], v[204:207]\n" \
"s_waitcnt lgkmcnt(3)\n" \
"ds_read_b128 v[100:103], v29 offset:2048\n" \
"v_mfma_f32_16x16x32_bf16 v[204:207], a[12:15], v[108:111], v[204:207]\n" \
"v_mfma_f32_16x16x32_bf16 v[192:195], a[16:19], v[112:115], v[192:195]\n" \
"ds_read_b128 v[104:107], v29 offset:1024\n" \
"v_mfma_f32_16x16x32_bf16 v[192:195], a[24:27], v[116:119], v[192:195]\n" \
"v_mfma_f32_16x16x32_bf16 v[196:199], a[20:23], v[112:115], v[196:199]\n" \
"s_waitcnt lgkmcnt(3)\n" \
"ds_read_b128 v[108:111], v29 offset:3072\n" \
"v_mfma_f32_16x16x32_bf16 v[196:199], a[28:31], v[116:119], v[196:199]\n" \
"v_mfma_f32_16x16x32_bf16 v[200:203], a[16:19], v[120:123], v[200:203]\n" \
"ds_read_b128 v[112:115], v29 offset:4096\n" \
"v_mfma_f32_16x16x32_bf16 v[200:203], a[24:27], v[124:127], v[200:203]\n" \
"v_mfma_f32_16x16x32_bf16 v[204:207], a[20:23], v[120:123], v[204:207]\n" \
"s_waitcnt lgkmcnt(3)\n" \
"ds_read_b128 v[116:119], v29 offset:6144\n" \
"v_mfma_f32_16x16x32_bf16 v[204:207], a[28:31], v[124:127], v[204:207]\n" \
"v_mfma_f32_16x16x32_bf16 v[192:195], a[32:35], v[96:99], v[192:195]\n" \
"ds_read_b128 v[120:123], v29 offset:5120\n" \
"v_mfma_f32_16x16x32_bf16 v[192:195], a[40:43], v[100:103], v[192:195]\n" \
"v_mfma_f32_16x16x32_bf16 v[196:199], a[36:39], v[96:99], v[196:199]\n" \
"s_waitcnt lgkmcnt(3)\n" \
"ds_read_b128 v[124:127], v29 offset:7168\n" \
"v_mfma_f32_16x16x32_bf16 v[196:199], a[44:47], v[100:103], v[196:199]\n" \
"v_mfma_f32_16x16x32_bf16 v[200:203], a[32:35], v[104:107], v[200:203]\n" \
"ds_read_b128 v[96:99], v29 offset:8192\n" \
"v_mfma_f32_16x16x32_bf16 v[200:203], a[40:43], v[108:111], v[200:203]\n" \
"v_mfma_f32_16x16x32_bf16 v[204:207], a[36:39], v[104:107], v[204:207]\n" \
"s_waitcnt lgkmcnt(3)\n" \
"ds_read_b128 v[100:103], v29 offset:10240\n" \
"v_mfma_f32_16x16x32_bf16 v[204:207], a[44:47], v[108:111], v[204:207]\n" \
"v_mfma_f32_16x16x32_bf16 v[192:195], a[48:51], v[112:115], v[192:195]\n" \
"ds_read_b128 v[104:107], v29 offset:9216\n" \
"v_mfma_f32_16x16x32_bf16 v[192:195], a[56:59], v[116:119], v[192:195]\n" \
"v_mfma_f32_16x16x32_bf16 v[196:199], a[52:55], v[112:115], v[196:199]\n" \
"s_waitcnt lgkmcnt(3)\n" \
"ds_read_b128 v[108:111], v29 offset:11264\n" \
"v_mfma_f32_16x16x32_bf16 v[196:199], a[60:63], v[116:119], v[196:199]\n" \
"v_mfma_f32_16x16x32_bf16 v[200:203], a[48:51], v[120:123], v[200:203]\n" \
"ds_read_b128 v[112:115], v29 offset:12288\n" \
"v_mfma_f32_16x16x32_bf16 v[200:203], a[56:59], v[124:127], v[200:203]\n" \
"v_mfma_f32_16x16x32_bf16 v[204:207], a[52:55], v[120:123], v[204:207]\n" \
"ds_read_b128 v[116:119], v29 offset:14336\n" \
"v_mfma_f32_16x16x32_bf16 v[204:207], a[60:63], v[124:127], v[204:207]\n" \
"s_waitcnt vmcnt(21) lgkmcnt(4)\n" \
"s_barrier\n" \
"v_mfma_f32_16x16x32_bf16 v[192:195], a[96:99], v[96:99], v[192:195]\n" \
"ds_read_b128 v[120:123], v29 offset:13312\n" \
"v_mfma_f32_16x16x32_bf16 v[192:195], a[104:107], v[100:103], v[192:195]\n" \
"v_mfma_f32_16x16x32_bf16 v[196:199], a[100:103], v[96:99], v[196:199]\n" \
"s_waitcnt lgkmcnt(3)\n" \
"ds_read_b128 v[124:127], v29 offset:15360\n" \
"v_mfma_f32_16x16x32_bf16 v[196:199], a[108:111], v[100:103], v[196:199]\n" \
"v_mfma_f32_16x16x32_bf16 v[200:203], a[96:99], v[104:107], v[200:203]\n" \
"ds_read_b128 v[96:99], v29 offset:16384\n" \
"v_mfma_f32_16x16x32_bf16 v[200:203], a[104:107], v[108:111], v[200:203]\n" \
"v_mfma_f32_16x16x32_bf16 v[204:207], a[100:103], v[104:107], v[204:207]\n" \
"s_waitcnt lgkmcnt(3)\n" \
"ds_read_b128 v[100:103], v29 offset:18432\n" \
"v_mfma_f32_16x16x32_bf16 v[204:207], a[108:111], v[108:111], v[204:207]\n" \
"v_mfma_f32_16x16x32_bf16 v[192:195], a[112:115], v[112:115], v[192:195]\n" \
"ds_read_b128 v[104:107], v29 offset:17408\n" \
"v_mfma_f32_16x16x32_bf16 v[192:195], a[120:123], v[116:119], v[192:195]\n" \
"v_mfma_f32_16x16x32_bf16 v[196:199], a[116:119], v[112:115], v[196:199]\n" \
"s_waitcnt lgkmcnt(3)\n" \
"ds_read_b128 v[108:111], v29 offset:19456\n" \
"v_mfma_f32_16x16x32_bf16 v[196:199], a[124:127], v[116:119], v[196:199]\n" \
"v_mfma_f32_16x16x32_bf16 v[200:203], a[112:115], v[120:123], v[200:203]\n" \
"ds_read_b128 v[112:115], v29 offset:20480\n" \
"v_mfma_f32_16x16x32_bf16 v[200:203], a[120:123], v[124:127], v[200:203]\n" \
"v_mfma_f32_16x16x32_bf16 v[204:207], a[116:119], v[120:123], v[204:207]\n" \
"ds_read_b128 v[116:119], v29 offset:22528\n" \
"v_mfma_f32_16x16x32_bf16 v[204:207], a[124:127], v[124:127], v[204:207]\n" \
"s_waitcnt vmcnt(0)\n" \
"v_mfma_f32_16x16x32_bf16 v[64:67], v[24:27], v[128:131], v[64:67]\n" \
"v_mfma_f32_16x16x32_bf16 v[68:71], v[22:25], v[128:131], v[68:71]\n" \
"v_mfma_f32_16x16x32_bf16 v[72:75], v[24:27], v[132:135], v[72:75]\n" \
"v_cvt_pk_bf16_f32 v192, v192, v193\n" \
"v_cvt_pk_bf16_f32 v193, v194, v195\n" \
"v_mfma_f32_16x16x32_bf16 v[76:79], v[22:25], v[132:135], v[76:79]\n" \
"v_cvt_pk_bf16_f32 v194, v196, v197\n" \
"v_cvt_pk_bf16_f32 v195, v198, v199\n" \
"v_mfma_f32_16x16x32_bf16 v[64:67], v[24:27], v[136:139], v[64:67]\n" \
"buffer_store_dwordx4 v[192:195], v4, s[36:39], s77 offen offset:0 sc1\n" \
"v_mfma_f32_16x16x32_bf16 v[68:71], v[22:25], v[136:139], v[68:71]\n" \
"v_cvt_pk_bf16_f32 v200, v200, v201\n" \
"v_cvt_pk_bf16_f32 v201, v202, v203\n" \
"v_mfma_f32_16x16x32_bf16 v[72:75], v[24:27], v[140:143], v[72:75]\n" \
"v_cvt_pk_bf16_f32 v202, v204, v205\n" \
"v_cvt_pk_bf16_f32 v203, v206, v207\n" \
"v_mfma_f32_16x16x32_bf16 v[76:79], v[22:25], v[140:143], v[76:79]\n" \
"v_mfma_f32_16x16x32_bf16 v[64:67], v[24:27], v[160:163], v[64:67]\n" \
"v_mfma_f32_16x16x32_bf16 v[68:71], v[22:25], v[160:163], v[68:71]\n" \
"v_mfma_f32_16x16x32_bf16 v[72:75], v[24:27], v[164:167], v[72:75]\n" \
"s_waitcnt lgkmcnt(4)\n" \
"v_mfma_f32_16x16x32_bf16 v[76:79], v[22:25], v[164:167], v[76:79]\n" \
"v_mfma_f32_16x16x32_bf16 v[240:243], a[160:163], v[96:99], 0\n" \
"ds_read_b128 v[120:123], v29 offset:21504\n" \
"v_mfma_f32_16x16x32_bf16 v[240:243], a[168:171], v[100:103], v[240:243]\n" \
"buffer_store_dwordx4 v[200:203], v4, s[36:39], s77 offen offset:1024 sc1\n" \
"v_mfma_f32_16x16x32_bf16 v[244:247], a[164:167], v[96:99], 0\n" \
"s_waitcnt lgkmcnt(3)\n" \
"ds_read_b128 v[124:127], v29 offset:23552\n" \
"v_fmac_f32 v32, v5, v64\n" \
"v_mfma_f32_16x16x32_bf16 v[244:247], a[172:175], v[100:103], v[244:247]\n" \
"v_fmac_f32 v33, v5, v65\n" \
"v_cvt_pk_bf16_f32 v80, v32, v33\n" \
"v_mfma_f32_16x16x32_bf16 v[248:251], a[160:163], v[104:107], 0\n" \
"ds_read_b128 v[96:99], v29 offset:24576\n" \
"v_fmac_f32 v34, v5, v66\n" \
"v_mfma_f32_16x16x32_bf16 v[248:251], a[168:171], v[108:111], v[248:251]\n" \
"v_fmac_f32 v35, v5, v67\n" \
"v_cvt_pk_bf16_f32 v81, v34, v35\n" \
"v_mfma_f32_16x16x32_bf16 v[252:255], a[164:167], v[104:107], 0\n" \
"s_waitcnt lgkmcnt(3)\n" \
"ds_read_b128 v[100:103], v29 offset:26624\n" \
"v_fmac_f32 v36, v5, v68\n" \
"v_mfma_f32_16x16x32_bf16 v[252:255], a[172:175], v[108:111], v[252:255]\n" \
"v_fmac_f32 v37, v5, v69\n" \
"v_cvt_pk_bf16_f32 v82, v36, v37\n" \
"v_mfma_f32_16x16x32_bf16 v[240:243], a[176:179], v[112:115], v[240:243]\n" \
"ds_read_b128 v[104:107], v29 offset:25600\n" \
"v_fmac_f32 v38, v5, v70\n" \
"v_mfma_f32_16x16x32_bf16 v[240:243], a[184:187], v[116:119], v[240:243]\n" \
"s_add_u32 s92, s82, 0\n" \
"v_fmac_f32 v39, v5, v71\n" \
"v_cvt_pk_bf16_f32 v83, v38, v39\n" \
"v_mfma_f32_16x16x32_bf16 v[244:247], a[180:183], v[112:115], v[244:247]\n" \
"s_waitcnt lgkmcnt(3)\n" \
"ds_read_b128 v[108:111], v29 offset:27648\n" \
"s_nop 0\n" \
"v_mfma_f32_16x16x32_bf16 v[244:247], a[188:191], v[116:119], v[244:247]\n" \
"v_permlane16_swap_b32 v80, v82\n" \
"v_permlane16_swap_b32 v81, v83\n" \
"v_mfma_f32_16x16x32_bf16 v[248:251], a[176:179], v[120:123], v[248:251]\n" \
"ds_read_b128 v[112:115], v29 offset:28672\n" \
"v_fmac_f32 v40, v21, v72\n" \
"v_mfma_f32_16x16x32_bf16 v[248:251], a[184:187], v[124:127], v[248:251]\n" \
"v_fmac_f32 v41, v21, v73\n" \
"v_cvt_pk_bf16_f32 v84, v40, v41\n" \
"v_mfma_f32_16x16x32_bf16 v[252:255], a[180:183], v[120:123], v[252:255]\n" \
"ds_read_b128 v[116:119], v29 offset:30720\n" \
"v_fmac_f32 v42, v21, v74\n" \
"v_mfma_f32_16x16x32_bf16 v[252:255], a[188:191], v[124:127], v[252:255]\n" \
"s_waitcnt vmcnt(19) lgkmcnt(4)\n" \
"s_barrier\n" \
"v_fmac_f32 v43, v21, v75\n" \
"v_mfma_f32_16x16x32_bf16 v[240:243], a[192:195], v[96:99], v[240:243]\n" \
"ds_read_b128 v[120:123], v29 offset:29696\n" \
"v_cvt_pk_bf16_f32 v85, v42, v43\n" \
"v_mfma_f32_16x16x32_bf16 v[240:243], a[200:203], v[100:103], v[240:243]\n" \
"v_fmac_f32 v44, v21, v76\n" \
"v_fmac_f32 v45, v21, v77\n" \
"v_mfma_f32_16x16x32_bf16 v[244:247], a[196:199], v[96:99], v[244:247]\n" \
"s_waitcnt lgkmcnt(3)\n" \
"ds_read_b128 v[124:127], v29 offset:31744\n" \
"v_cvt_pk_bf16_f32 v86, v44, v45\n" \
"v_mfma_f32_16x16x32_bf16 v[244:247], a[204:207], v[100:103], v[244:247]\n" \
"v_fmac_f32 v46, v21, v78\n" \
"v_fmac_f32 v47, v21, v79\n" \
"v_mfma_f32_16x16x32_bf16 v[248:251], a[192:195], v[104:107], v[248:251]\n" \
"s_add_u32 s97, s78, 4096\n" \
"ds_read_b128 v[96:99], v29 offset:49152\n" \
"v_cvt_pk_bf16_f32 v87, v46, v47\n" \
"v_mfma_f32_16x16x32_bf16 v[248:251], a[200:203], v[108:111], v[248:251]\n" \
"buffer_load_dwordx4 v[168:171], v4, s[36:39], s97 offen offset:2048 sc1\n" \
"v_mfma_f32_16x16x32_bf16 v[252:255], a[196:199], v[104:107], v[252:255]\n" \
"s_waitcnt lgkmcnt(3)\n" \
"ds_read_b128 v[100:103], v29 offset:51200\n" \
"s_add_u32 s93, s82, 32768\n" \
"v_mfma_f32_16x16x32_bf16 v[252:255], a[204:207], v[108:111], v[252:255]\n" \
"s_nop 0\n" \
"v_permlane16_swap_b32 v84, v86\n" \
"v_mfma_f32_16x16x32_bf16 v[240:243], a[208:211], v[112:115], v[240:243]\n" \
"ds_read_b128 v[104:107], v29 offset:50176\n" \
"v_permlane16_swap_b32 v85, v87\n" \
"v_mfma_f32_16x16x32_bf16 v[240:243], a[216:219], v[116:119], v[240:243]\n" \
"buffer_load_dwordx4 v[172:175], v4, s[36:39], s97 offen offset:3072 sc1\n" \
"v_mfma_f32_16x16x32_bf16 v[244:247], a[212:215], v[112:115], v[244:247]\n" \
"s_waitcnt lgkmcnt(3)\n" \
"ds_read_b128 v[108:111], v29 offset:52224\n" \
"s_add_u32 s97, s78, 8192\n" \
"v_mfma_f32_16x16x32_bf16 v[244:247], a[220:223], v[116:119], v[244:247]\n" \
"v_mfma_f32_16x16x32_bf16 v[248:251], a[208:211], v[120:123], v[248:251]\n" \
"ds_read_b128 v[112:115], v29 offset:53248\n" \
"v_mfma_f32_16x16x32_bf16 v[248:251], a[216:219], v[124:127], v[248:251]\n" \
"buffer_load_dwordx4 v[224:227], v4, s[36:39], s97 offen offset:0 sc1\n" \
"v_mfma_f32_16x16x32_bf16 v[252:255], a[212:215], v[120:123], v[252:255]\n" \
"s_waitcnt lgkmcnt(3)\n" \
"ds_read_b128 v[116:119], v29 offset:55296\n" \
"v_mfma_f32_16x16x32_bf16 v[252:255], a[220:223], v[124:127], v[252:255]\n" \
"v_mfma_f32_16x16x32_bf16 v[240:243], a[224:227], v[96:99], v[240:243]\n" \
"ds_read_b128 v[120:123], v29 offset:54272\n" \
"v_mfma_f32_16x16x32_bf16 v[240:243], a[232:235], v[100:103], v[240:243]\n" \
"buffer_load_dwordx4 v[228:231], v4, s[36:39], s97 offen offset:1024 sc1\n" \
"v_mfma_f32_16x16x32_bf16 v[244:247], a[228:231], v[96:99], v[244:247]\n" \
"s_waitcnt lgkmcnt(3)\n" \
"ds_read_b128 v[124:127], v29 offset:56320\n" \
"v_mfma_f32_16x16x32_bf16 v[244:247], a[236:239], v[100:103], v[244:247]\n" \
"v_mfma_f32_16x16x32_bf16 v[248:251], a[224:227], v[104:107], v[248:251]\n" \
"ds_read_b128 v[96:99], v29 offset:57344\n" \
"v_mfma_f32_16x16x32_bf16 v[248:251], a[232:235], v[108:111], v[248:251]\n" \
"v_mfma_f32_16x16x32_bf16 v[252:255], a[228:231], v[104:107], v[252:255]\n" \
"s_waitcnt lgkmcnt(3)\n" \
"ds_read_b128 v[100:103], v29 offset:59392\n" \
"v_mfma_f32_16x16x32_bf16 v[252:255], a[236:239], v[108:111], v[252:255]\n" \
"v_mfma_f32_16x16x32_bf16 v[240:243], a[240:243], v[112:115], v[240:243]\n" \
"ds_read_b128 v[104:107], v29 offset:58368\n" \
"v_mfma_f32_16x16x32_bf16 v[240:243], a[248:251], v[116:119], v[240:243]\n" \
"v_mfma_f32_16x16x32_bf16 v[244:247], a[244:247], v[112:115], v[244:247]\n" \
"s_waitcnt lgkmcnt(3)\n" \
"ds_read_b128 v[108:111], v29 offset:60416\n" \
"v_mfma_f32_16x16x32_bf16 v[244:247], a[252:255], v[116:119], v[244:247]\n" \
"v_mfma_f32_16x16x32_bf16 v[248:251], a[240:243], v[120:123], v[248:251]\n" \
"ds_read_b128 v[112:115], v29 offset:61440\n" \
"v_mfma_f32_16x16x32_bf16 v[248:251], a[248:251], v[124:127], v[248:251]\n" \
"v_mfma_f32_16x16x32_bf16 v[252:255], a[244:247], v[120:123], v[252:255]\n" \
"ds_read_b128 v[116:119], v29 offset:63488\n" \
"v_mfma_f32_16x16x32_bf16 v[252:255], a[252:255], v[124:127], v[252:255]\n" \
"s_waitcnt vmcnt(18) lgkmcnt(4)\n" \
"s_barrier\n" \
"v_mfma_f32_16x16x32_bf16 v[240:243], a[64:67], v[96:99], v[240:243]\n" \
"ds_read_b128 v[120:123], v29 offset:62464\n" \
"v_mfma_f32_16x16x32_bf16 v[240:243], a[72:75], v[100:103], v[240:243]\n" \
"v_mfma_f32_16x16x32_bf16 v[244:247], a[68:71], v[96:99], v[244:247]\n" \
"s_waitcnt lgkmcnt(3)\n" \
"ds_read_b128 v[124:127], v29 offset:64512\n" \
"v_mfma_f32_16x16x32_bf16 v[244:247], a[76:79], v[100:103], v[244:247]\n" \
"v_mfma_f32_16x16x32_bf16 v[248:251], a[64:67], v[104:107], v[248:251]\n" \
"ds_read_b128 v[96:99], v30 offset:16384\n" \
"v_mfma_f32_16x16x32_bf16 v[248:251], a[72:75], v[108:111], v[248:251]\n" \
"v_mfma_f32_16x16x32_bf16 v[252:255], a[68:71], v[104:107], v[252:255]\n" \
"s_waitcnt lgkmcnt(3)\n" \
"ds_read_b128 v[100:103], v30 offset:18432\n" \
"v_mfma_f32_16x16x32_bf16 v[252:255], a[76:79], v[108:111], v[252:255]\n" \
"v_mfma_f32_16x16x32_bf16 v[240:243], a[80:83], v[112:115], v[240:243]\n" \
"ds_read_b128 v[104:107], v30 offset:17408\n" \
"v_mfma_f32_16x16x32_bf16 v[240:243], a[88:91], v[116:119], v[240:243]\n" \
"v_mfma_f32_16x16x32_bf16 v[244:247], a[84:87], v[112:115], v[244:247]\n" \
"s_waitcnt lgkmcnt(3)\n" \
"ds_read_b128 v[108:111], v30 offset:19456\n" \
"v_mfma_f32_16x16x32_bf16 v[244:247], a[92:95], v[116:119], v[244:247]\n" \
"v_mfma_f32_16x16x32_bf16 v[248:251], a[80:83], v[120:123], v[248:251]\n" \
"ds_read_b128 v[112:115], v30 offset:20480\n" \
"v_mfma_f32_16x16x32_bf16 v[248:251], a[88:91], v[124:127], v[248:251]\n" \
"v_mfma_f32_16x16x32_bf16 v[252:255], a[84:87], v[120:123], v[252:255]\n" \
"s_waitcnt lgkmcnt(3)\n" \
"ds_read_b128 v[116:119], v30 offset:22528\n" \
"v_mfma_f32_16x16x32_bf16 v[252:255], a[92:95], v[124:127], v[252:255]\n" \
"v_mfma_f32_16x16x32_bf16 v[240:243], a[128:131], v[96:99], v[240:243]\n" \
"ds_read_b128 v[120:123], v30 offset:21504\n" \
"v_mfma_f32_16x16x32_bf16 v[240:243], a[136:139], v[100:103], v[240:243]\n" \
"v_mfma_f32_16x16x32_bf16 v[244:247], a[132:135], v[96:99], v[244:247]\n" \
"s_waitcnt lgkmcnt(3)\n" \
"ds_read_b128 v[124:127], v30 offset:23552\n" \
"v_mfma_f32_16x16x32_bf16 v[244:247], a[140:143], v[100:103], v[244:247]\n" \
"v_mfma_f32_16x16x32_bf16 v[248:251], a[128:131], v[104:107], v[248:251]\n" \
"ds_read_b128 v[96:99], v30 offset:24576\n" \
"v_mfma_f32_16x16x32_bf16 v[248:251], a[136:139], v[108:111], v[248:251]\n" \
"v_mfma_f32_16x16x32_bf16 v[252:255], a[132:135], v[104:107], v[252:255]\n" \
"s_waitcnt lgkmcnt(3)\n" \
"ds_read_b128 v[100:103], v30 offset:26624\n" \
"v_mfma_f32_16x16x32_bf16 v[252:255], a[140:143], v[108:111], v[252:255]\n" \
"v_mfma_f32_16x16x32_bf16 v[240:243], a[144:147], v[112:115], v[240:243]\n" \
"ds_read_b128 v[104:107], v30 offset:25600\n" \
"v_mfma_f32_16x16x32_bf16 v[240:243], a[152:155], v[116:119], v[240:243]\n" \
"v_mfma_f32_16x16x32_bf16 v[244:247], a[148:151], v[112:115], v[244:247]\n" \
"s_waitcnt lgkmcnt(3)\n" \
"ds_read_b128 v[108:111], v30 offset:27648\n" \
"v_mfma_f32_16x16x32_bf16 v[244:247], a[156:159], v[116:119], v[244:247]\n" \
"v_mfma_f32_16x16x32_bf16 v[248:251], a[144:147], v[120:123], v[248:251]\n" \
"ds_read_b128 v[112:115], v30 offset:28672\n" \
"v_mfma_f32_16x16x32_bf16 v[248:251], a[152:155], v[124:127], v[248:251]\n" \
"v_mfma_f32_16x16x32_bf16 v[252:255], a[148:151], v[120:123], v[252:255]\n" \
"ds_read_b128 v[116:119], v30 offset:30720\n" \
"v_mfma_f32_16x16x32_bf16 v[252:255], a[156:159], v[124:127], v[252:255]\n" \
"s_waitcnt vmcnt(12) lgkmcnt(4)\n" \
"s_barrier\n" \
"v_mfma_f32_16x16x32_bf16 v[240:243], a[0:3], v[96:99], v[240:243]\n" \
"ds_read_b128 v[120:123], v30 offset:29696\n" \
"v_mfma_f32_16x16x32_bf16 v[240:243], a[8:11], v[100:103], v[240:243]\n" \
"v_mfma_f32_16x16x32_bf16 v[244:247], a[4:7], v[96:99], v[244:247]\n" \
"s_waitcnt lgkmcnt(3)\n" \
"ds_read_b128 v[124:127], v30 offset:31744\n" \
"v_mfma_f32_16x16x32_bf16 v[244:247], a[12:15], v[100:103], v[244:247]\n" \
"v_mfma_f32_16x16x32_bf16 v[248:251], a[0:3], v[104:107], v[248:251]\n" \
"ds_read_b128 v[96:99], v28 offset:16384\n" \
"v_mfma_f32_16x16x32_bf16 v[248:251], a[8:11], v[108:111], v[248:251]\n" \
"v_mfma_f32_16x16x32_bf16 v[252:255], a[4:7], v[104:107], v[252:255]\n" \
"s_waitcnt lgkmcnt(3)\n" \
"ds_read_b128 v[100:103], v28 offset:18432\n" \
"v_mfma_f32_16x16x32_bf16 v[252:255], a[12:15], v[108:111], v[252:255]\n" \
"v_mfma_f32_16x16x32_bf16 v[240:243], a[16:19], v[112:115], v[240:243]\n" \
"ds_read_b128 v[104:107], v28 offset:17408\n" \
"v_mfma_f32_16x16x32_bf16 v[240:243], a[24:27], v[116:119], v[240:243]\n" \
"v_mfma_f32_16x16x32_bf16 v[244:247], a[20:23], v[112:115], v[244:247]\n" \
"s_waitcnt lgkmcnt(3)\n" \
"ds_read_b128 v[108:111], v28 offset:19456\n" \
"s_add_u32 s97, s78, 8192\n" \
"v_mfma_f32_16x16x32_bf16 v[244:247], a[28:31], v[116:119], v[244:247]\n" \
"buffer_load_dwordx4 v[232:235], v4, s[36:39], s97 offen offset:2048 sc1\n" \
"v_mfma_f32_16x16x32_bf16 v[248:251], a[16:19], v[120:123], v[248:251]\n" \
"ds_read_b128 v[112:115], v28 offset:20480\n" \
"s_waitcnt vmcnt(1)\n" \
"v_mfma_f32_16x16x32_bf16 v[248:251], a[24:27], v[124:127], v[248:251]\n" \
"v_max_u32 v18, v168, v169\n" \
"v_max3_u32 v18, v18, v170, v171\n" \
"v_mfma_f32_16x16x32_bf16 v[252:255], a[20:23], v[120:123], v[252:255]\n" \
"s_waitcnt lgkmcnt(3)\n" \
"ds_read_b128 v[116:119], v28 offset:22528\n" \
"v_max3_u32 v18, v18, v172, v173\n" \
"v_mfma_f32_16x16x32_bf16 v[252:255], a[28:31], v[124:127], v[252:255]\n" \
"buffer_load_dwordx4 v[236:239], v4, s[36:39], s97 offen offset:3072 sc1\n" \
"v_mfma_f32_16x16x32_bf16 v[240:243], a[32:35], v[96:99], v[240:243]\n" \
"ds_read_b128 v[120:123], v28 offset:21504\n" \
"v_max3_u32 v18, v18, v174, v175\n" \
"v_mfma_f32_16x16x32_bf16 v[240:243], a[40:43], v[100:103], v[240:243]\n" \
"buffer_store_dwordx4 v[80:83], v184, s[32:35], s92 offen\n" \
"v_mfma_f32_16x16x32_bf16 v[244:247], a[36:39], v[96:99], v[244:247]\n" \
"s_waitcnt lgkmcnt(3)\n" \
"ds_read_b128 v[124:127], v28 offset:23552\n" \
"v_max3_u32 v18, v18, v224, v225\n" \
"v_mfma_f32_16x16x32_bf16 v[244:247], a[44:47], v[100:103], v[244:247]\n" \
"v_max3_u32 v18, v18, v226, v227\n" \
"v_max3_u32 v18, v18, v228, v229\n" \
"v_mfma_f32_16x16x32_bf16 v[248:251], a[32:35], v[104:107], v[248:251]\n" \
"ds_read_b128 v[96:99], v28 offset:24576\n" \
"v_max3_u32 v18, v18, v230, v231\n" \
"v_mfma_f32_16x16x32_bf16 v[248:251], a[40:43], v[108:111], v[248:251]\n" \
"v_cmp_eq_u32 vcc, -1, v18\n" \
"s_cbranch_vccnz LIM_%=\nLIMR_%=:\n" \
"v_mfma_f32_16x16x32_bf16 v[252:255], a[36:39], v[104:107], v[252:255]\n" \
"s_waitcnt lgkmcnt(3)\n" \
"ds_read_b128 v[100:103], v28 offset:26624\n" \
"v_mfma_f32_16x16x32_bf16 v[252:255], a[44:47], v[108:111], v[252:255]\n" \
"v_mfma_f32_16x16x32_bf16 v[240:243], a[48:51], v[112:115], v[240:243]\n" \
"ds_read_b128 v[104:107], v28 offset:25600\n" \
"v_mfma_f32_16x16x32_bf16 v[240:243], a[56:59], v[116:119], v[240:243]\n" \
"buffer_store_dwordx4 v[84:87], v184, s[32:35], s93 offen\n" \
"v_mfma_f32_16x16x32_bf16 v[244:247], a[52:55], v[112:115], v[244:247]\n" \
"s_waitcnt lgkmcnt(3)\n" \
"ds_read_b128 v[108:111], v28 offset:27648\n" \
"v_mfma_f32_16x16x32_bf16 v[244:247], a[60:63], v[116:119], v[244:247]\n" \
"v_mfma_f32_16x16x32_bf16 v[248:251], a[48:51], v[120:123], v[248:251]\n" \
"ds_read_b128 v[112:115], v28 offset:28672\n" \
"v_mfma_f32_16x16x32_bf16 v[248:251], a[56:59], v[124:127], v[248:251]\n" \
"v_mfma_f32_16x16x32_bf16 v[252:255], a[52:55], v[120:123], v[252:255]\n" \
"ds_read_b128 v[116:119], v28 offset:30720\n" \
"v_mfma_f32_16x16x32_bf16 v[252:255], a[60:63], v[124:127], v[252:255]\n" \
"s_waitcnt lgkmcnt(4)\n" \
"s_barrier\n" \
"v_mfma_f32_16x16x32_bf16 v[240:243], a[96:99], v[96:99], v[240:243]\n" \
"ds_read_b128 v[120:123], v28 offset:29696\n" \
"v_mfma_f32_16x16x32_bf16 v[240:243], a[104:107], v[100:103], v[240:243]\n" \
"v_mfma_f32_16x16x32_bf16 v[244:247], a[100:103], v[96:99], v[244:247]\n" \
"s_waitcnt lgkmcnt(3)\n" \
"ds_read_b128 v[124:127], v28 offset:31744\n" \
"v_mfma_f32_16x16x32_bf16 v[244:247], a[108:111], v[100:103], v[244:247]\n" \
"v_mfma_f32_16x16x32_bf16 v[248:251], a[96:99], v[104:107], v[248:251]\n" \
"v_mfma_f32_16x16x32_bf16 v[248:251], a[104:107], v[108:111], v[248:251]\n" \
"v_mfma_f32_16x16x32_bf16 v[252:255], a[100:103], v[104:107], v[252:255]\n" \
"s_waitcnt lgkmcnt(2)\n" \
"v_mfma_f32_16x16x32_bf16 v[252:255], a[108:111], v[108:111], v[252:255]\n" \
"v_mfma_f32_16x16x32_bf16 v[240:243], a[112:115], v[112:115], v[240:243]\n" \
"v_mfma_f32_16x16x32_bf16 v[240:243], a[120:123], v[116:119], v[240:243]\n" \
"v_mfma_f32_16x16x32_bf16 v[244:247], a[116:119], v[112:115], v[244:247]\n" \
"s_waitcnt lgkmcnt(0)\n" \
"v_mfma_f32_16x16x32_bf16 v[244:247], a[124:127], v[116:119], v[244:247]\n" \
"v_mfma_f32_16x16x32_bf16 v[248:251], a[112:115], v[120:123], v[248:251]\n" \
"v_mfma_f32_16x16x32_bf16 v[248:251], a[120:123], v[124:127], v[248:251]\n" \
"v_mfma_f32_16x16x32_bf16 v[252:255], a[116:119], v[120:123], v[252:255]\n" \
"v_mfma_f32_16x16x32_bf16 v[252:255], a[124:127], v[124:127], v[252:255]\n" \
"v_mfma_f32_16x16x32_bf16 v[240:243], v[24:27], v[168:171], v[240:243]\n" \
"v_mfma_f32_16x16x32_bf16 v[244:247], v[22:25], v[168:171], v[244:247]\n" \
"v_mfma_f32_16x16x32_bf16 v[248:251], v[24:27], v[172:175], v[248:251]\n" \
"v_mfma_f32_16x16x32_bf16 v[252:255], v[22:25], v[172:175], v[252:255]\n" \
"v_mfma_f32_16x16x32_bf16 v[240:243], v[24:27], v[224:227], v[240:243]\n" \
"v_mfma_f32_16x16x32_bf16 v[244:247], v[22:25], v[224:227], v[244:247]\n" \
"v_mfma_f32_16x16x32_bf16 v[248:251], v[24:27], v[228:231], v[248:251]\n" \
"v_mfma_f32_16x16x32_bf16 v[252:255], v[22:25], v[228:231], v[252:255]\n" \
"s_mov_b32 s92, s83\n" \
"s_add_u32 s93, s83, 32768\n" \
"s_waitcnt vmcnt(2)\n" \
"v_max_u32 v18, v232, v233\n" \
"v_max3_u32 v18, v18, v234, v235\n" \
"v_max3_u32 v18, v18, v236, v237\n" \
"v_max3_u32 v18, v18, v238, v239\n" \
"v_cmp_eq_u32 vcc, -1, v18\n" \
"s_cbranch_vccnz LIN_%=\nLINR_%=:\n" \
"v_mfma_f32_16x16x32_bf16 v[240:243], v[24:27], v[232:235], v[240:243]\n" \
"v_mfma_f32_16x16x32_bf16 v[244:247], v[22:25], v[232:235], v[244:247]\n" \
"v_mfma_f32_16x16x32_bf16 v[248:251], v[24:27], v[236:239], v[248:251]\n" \
"v_mfma_f32_16x16x32_bf16 v[252:255], v[22:25], v[236:239], v[252:255]\n" \
"s_nop 3\n" \
"v_pk_fma_f32 v[240:241], v[240:241], v[6:7], v[48:49] op_sel_hi:[1,0,1]\n" \
"v_pk_fma_f32 v[242:243], v[242:243], v[6:7], v[50:51] op_sel_hi:[1,0,1]\n" \
"v_pk_fma_f32 v[244:245], v[244:245], v[6:7], v[52:53] op_sel_hi:[1,0,1]\n" \
"v_pk_fma_f32 v[246:247], v[246:247], v[6:7], v[54:55] op_sel_hi:[1,0,1]\n" \
"v_cvt_pk_bf16_f32 v80, v240, v241\n" \
"v_cvt_pk_bf16_f32 v81, v242, v243\n" \
"v_cvt_pk_bf16_f32 v82, v244, v245\n" \
"v_cvt_pk_bf16_f32 v83, v246, v247\n" \
"s_nop 1\n" \
"v_permlane16_swap_b32 v80, v82\n" \
"v_permlane16_swap_b32 v81, v83\n" \
"buffer_store_dwordx4 v[80:83], v184, s[32:35], s92 offen\n" \
"v_pk_fma_f32 v[248:249], v[248:249], v[8:9], v[56:57] op_sel_hi:[1,0,1]\n" \
"v_pk_fma_f32 v[250:251], v[250:251], v[8:9], v[58:59] op_sel_hi:[1,0,1]\n" \
"v_pk_fma_f32 v[252:253], v[252:253], v[8:9], v[60:61] op_sel_hi:[1,0,1]\n" \
"v_pk_fma_f32 v[254:255], v[254:255], v[8:9], v[62:63] op_sel_hi:[1,0,1]\n" \
"v_cvt_pk_bf16_f32 v84, v248, v249\n" \
"v_cvt_pk_bf16_f32 v85, v250, v251\n" \
"v_cvt_pk_bf16_f32 v86, v252, v253\n" \
"v_cvt_pk_bf16_f32 v87, v254, v255\n" \
"s_nop 1\n" \
"v_permlane16_swap_b32 v84, v86\n" \
"v_permlane16_swap_b32 v85, v87\n" \
"buffer_store_dwordx4 v[84:87], v184, s[32:35], s93 offen\n" \
"s_branch LEND_%=\n" \
"LPUK_%=:\n" \
"s_mov_b32 s25, 0\n" \
"LPUKL_%=:\n" \
"s_sleep 2\n" \
"buffer_load_dwordx2 v[58:59], v57, s[36:39], 0 offen sc1\n" \
"s_add_u32 s25, s25, 1\n" \
"s_waitcnt vmcnt(0)\n" \
"v_cmp_ne_u64 vcc, s[14:15], v[58:59]\n" \
"s_cmp_gt_u32 s25, 262144\n" \
"s_cbranch_scc1 LPUKR_%=\n" \
"s_cbranch_vccnz LPUKL_%=\n" \
"s_branch LPUKR_%=\n" \
"LPUV_%=:\n" \
"s_mov_b32 s25, 0\n" \
"LPUVL_%=:\n" \
"s_sleep 2\n" \
"buffer_load_dwordx2 v[58:59], v57, s[36:39], 0 offen sc1\n" \
"s_add_u32 s25, s25, 1\n" \
"s_waitcnt vmcnt(0)\n" \
"v_cmp_ne_u64 vcc, s[14:15], v[58:59]\n" \
"s_cmp_gt_u32 s25, 262144\n" \
"s_cbranch_scc1 LPUVR_%=\n" \
"s_cbranch_vccnz LPUVL_%=\n" \
"s_branch LPUVR_%=\n" \
"LPK_%=:\n" \
"s_mov_b32 s25, 0\n" \
"LPKL_%=:\n" \
"s_sleep 2\n" \
"buffer_load_dwordx2 v[58:59], v57, s[36:39], 0 offen sc1\n" \
"s_add_u32 s25, s25, 1\n" \
"s_waitcnt vmcnt(0)\n" \
"v_cmp_ne_u64 vcc, s[14:15], v[58:59]\n" \
"s_cmp_gt_u32 s25, 262144\n" \
"s_cbranch_scc1 LPKR_%=\n" \
"s_cbranch_vccnz LPKL_%=\n" \
"s_branch LPKR_%=\n" \
"LSW_%=:\n" \
"v_swap_b32 v64, v80\n" \
"v_swap_b32 v65, v81\n" \
"v_swap_b32 v66, v82\n" \
"v_swap_b32 v67, v83\n" \
"v_swap_b32 v68, v84\n" \
"v_swap_b32 v69, v85\n" \
"v_swap_b32 v70, v86\n" \
"v_swap_b32 v71, v87\n" \
"v_swap_b32 v72, v88\n" \
"v_swap_b32 v73, v89\n" \
"v_swap_b32 v74, v90\n" \
"v_swap_b32 v75, v91\n" \
"v_swap_b32 v76, v92\n" \
"v_swap_b32 v77, v93\n" \
"v_swap_b32 v78, v94\n" \
"v_swap_b32 v79, v95\n" \
"s_branch LSWR_%=\n" \
"LIM_%=:\n" \
"s_mov_b32 s25, 0\n" \
"LIML_%=:\n" \
"s_sleep 1\n" \
"s_add_u32 s98, s78, 4096\n" \
"buffer_load_dwordx4 v[168:171], v4, s[36:39], s98 offen offset:2048 sc1\n" \
"buffer_load_dwordx4 v[172:175], v4, s[36:39], s98 offen offset:3072 sc1\n" \
"s_add_u32 s98, s78, 8192\n" \
"buffer_load_dwordx4 v[224:227], v4, s[36:39], s98 offen offset:0 sc1\n" \
"buffer_load_dwordx4 v[228:231], v4, s[36:39], s98 offen offset:1024 sc1\n" \
"s_add_u32 s25, s25, 1\n" \
"s_waitcnt vmcnt(0)\n" \
"v_max_u32 v18, v168, v169\n" \
"v_max3_u32 v18, v18, v170, v171\n" \
"v_max3_u32 v18, v18, v172, v173\n" \
"v_max3_u32 v18, v18, v174, v175\n" \
"v_max3_u32 v18, v18, v224, v225\n" \
"v_max3_u32 v18, v18, v226, v227\n" \
"v_max3_u32 v18, v18, v228, v229\n" \
"v_max3_u32 v18, v18, v230, v231\n" \
"v_cmp_eq_u32 vcc, -1, v18\n" \
"s_cmp_gt_u32 s25, 262144\n" \
"s_cbranch_scc1 LIMR_%=\n" \
"s_cbranch_vccnz LIML_%=\n" \
"s_branch LIMR_%=\n" \
"LIN_%=:\n" \
"s_mov_b32 s25, 0\n" \
"LINL_%=:\n" \
"s_sleep 1\n" \
"s_add_u32 s98, s78, 8192\n" \
"buffer_load_dwordx4 v[232:235], v4, s[36:39], s98 offen offset:2048 sc1\n" \
"buffer_load_dwordx4 v[236:239], v4, s[36:39], s98 offen offset:3072 sc1\n" \
"s_add_u32 s25, s25, 1\n" \
"s_waitcnt vmcnt(0)\n" \
"v_max_u32 v18, v232, v233\n" \
"v_max3_u32 v18, v18, v234, v235\n" \
"v_max3_u32 v18, v18, v236, v237\n" \
"v_max3_u32 v18, v18, v238, v239\n" \
"v_cmp_eq_u32 vcc, -1, v18\n" \
"s_cmp_gt_u32 s25, 262144\n" \
"s_cbranch_scc1 LINR_%=\n" \
"s_cbranch_vccnz LINL_%=\n" \
"s_branch LINR_%=\n" \
"LPZ_%=:\n" \
"s_mov_b32 s25, 0\n" \
"LPZL_%=:\n" \
"s_sleep 2\n" \
"buffer_load_dwordx2 v[18:19], v17, s[36:39], 0 offen sc1\n" \
"s_add_u32 s25, s25, 1\n" \
"s_waitcnt vmcnt(0)\n" \
"v_cmp_ne_u64 vcc, s[14:15], v[18:19]\n" \
"s_cmp_gt_u32 s25, 262144\n" \
"s_cbranch_scc1 LPZR_%=\n" \
"s_cbranch_vccnz LPZL_%=\n" \
"s_branch LPZR_%=\n" \
"LPS_%=:\n" \
"s_mov_b32 s25, 0\n" \
"LPSL_%=:\n" \
"s_sleep 2\n" \
"buffer_load_dwordx2 v[18:19], v17, s[36:39], 0 offen sc1\n" \
"s_add_u32 s25, s25, 1\n" \
"s_waitcnt vmcnt(0)\n" \
"v_cmp_ne_u64 vcc, s[14:15], v[18:19]\n" \
"s_cmp_gt_u32 s25, 262144\n" \
"s_cbranch_scc1 LPSR_%=\n" \
"s_cbranch_vccnz LPSL_%=\n" \
"s_branch LPSR_%=\n" \
"LPL_%=:\n" \
"s_mov_b32 s25, 0\n" \
"LPLL_%=:\n" \
"s_sleep 2\n" \
"buffer_load_dwordx2 v[18:19], v17, s[36:39], 0 offen sc1\n" \
"s_add_u32 s25, s25, 1\n" \
"s_waitcnt vmcnt(0)\n" \
"v_cmp_ne_u64 vcc, s[14:15], v[18:19]\n" \
"s_cmp_gt_u32 s25, 262144\n" \
"s_cbranch_scc1 LPLR_%=\n" \
"s_cbranch_vccnz LPLL_%=\n" \
"s_branch LPLR_%=\n" \
"LPE_%=:\n" \
"s_mov_b32 s25, 0\n" \
"LPEL_%=:\n" \
"s_sleep 2\n" \
"buffer_load_dwordx2 v[18:19], v17, s[36:39], 0 offen sc1\n" \
"s_add_u32 s25, s25, 1\n" \
"s_waitcnt vmcnt(0)\n" \
"v_cmp_ne_u64 vcc, s[14:15], v[18:19]\n" \
"s_cmp_gt_u32 s25, 262144\n" \
"s_cbranch_scc1 LPER_%=\n" \
"s_cbranch_vccnz LPEL_%=\n" \
"s_branch LPER_%=\n" \
"LPE0_%=:\n" \
"s_mov_b32 s25, 0\n" \
"LPE0L_%=:\n" \
"s_sleep 2\n" \
"buffer_load_dwordx2 v[18:19], v17, s[36:39], 0 offen sc1\n" \
"s_add_u32 s25, s25, 1\n" \
"s_waitcnt vmcnt(0)\n" \
"v_cmp_ne_u64 vcc, s[14:15], v[18:19]\n" \
"s_cmp_gt_u32 s25, 262144\n" \
"s_cbranch_scc1 LPE0R_%=\n" \
"s_cbranch_vccnz LPE0L_%=\n" \
"s_branch LPE0R_%=\n" \
"LFBA_%=:\n" \
"s_mov_b32 s99, 0\n" \
"s_branch LFB_%=\n" \
"LFBC_%=:\n" \
"s_mov_b32 s99, 2\n" \
"s_branch LFB_%=\n" \
"LFBB_%=:\n" \
"s_mov_b32 s99, 1\n" \
"LFB_%=:\n" \
"s_waitcnt vmcnt(0) lgkmcnt(0)\n" \
"s_nop 15\n" \
"s_lshl_b32 s25, %1, 2\n" \
"s_add_u32 s25, s25, %2\n" \
"s_lshl_b32 s25, s25, 13\n" \
"s_add_u32 s25, s25, 16777216\n" \
"buffer_store_dwordx4 v[64:67], v4, s[36:39], s25 offen offset:0\n" \
"buffer_store_dwordx4 v[68:71], v4, s[36:39], s25 offen offset:1024\n" \
"buffer_store_dwordx4 v[72:75], v4, s[36:39], s25 offen offset:2048\n" \
"buffer_store_dwordx4 v[76:79], v4, s[36:39], s25 offen offset:3072\n" \
"buffer_store_dwordx4 v[80:83], v7, s[36:39], s25 offen offset:0\n" \
"buffer_store_dwordx4 v[84:87], v7, s[36:39], s25 offen offset:1024\n" \
"buffer_store_dwordx4 v[88:91], v7, s[36:39], s25 offen offset:2048\n" \
"buffer_store_dwordx4 v[92:95], v7, s[36:39], s25 offen offset:3072\n" \
"s_add_u32 s25, s25, 8388608\n" \
"buffer_store_dwordx4 v[96:99], v4, s[36:39], s25 offen offset:0\n" \
"buffer_store_dwordx4 v[100:103], v4, s[36:39], s25 offen offset:1024\n" \
"buffer_store_dwordx4 v[104:107], v4, s[36:39], s25 offen offset:2048\n" \
"buffer_store_dwordx4 v[108:111], v4, s[36:39], s25 offen offset:3072\n" \
"buffer_store_dwordx4 v[112:115], v7, s[36:39], s25 offen offset:0\n" \
"buffer_store_dwordx4 v[116:119], v7, s[36:39], s25 offen offset:1024\n" \
"buffer_store_dwordx4 v[120:123], v7, s[36:39], s25 offen offset:2048\n" \
"buffer_store_dwordx4 v[124:127], v7, s[36:39], s25 offen offset:3072\n" \
"s_add_u32 s25, s25, 58720256\n" \
"buffer_store_dwordx4 v[128:131], v4, s[36:39], s25 offen offset:0\n" \
"buffer_store_dwordx4 v[132:135], v4, s[36:39], s25 offen offset:1024\n" \
"buffer_store_dwordx4 v[136:139], v4, s[36:39], s25 offen offset:2048\n" \
"buffer_store_dwordx4 v[140:143], v4, s[36:39], s25 offen offset:3072\n" \
"buffer_store_dwordx4 v[144:147], v7, s[36:39], s25 offen offset:0\n" \
"buffer_store_dwordx4 v[148:151], v7, s[36:39], s25 offen offset:1024\n" \
"buffer_store_dwordx4 v[152:155], v7, s[36:39], s25 offen offset:2048\n" \
"buffer_store_dwordx4 v[156:159], v7, s[36:39], s25 offen offset:3072\n" \
"s_add_u32 s25, s25, 37748736\n" \
"buffer_store_dwordx4 v[160:163], v4, s[36:39], s25 offen offset:0\n" \
"buffer_store_dwordx4 v[164:167], v4, s[36:39], s25 offen offset:1024\n" \
"buffer_store_dwordx4 v[168:171], v4, s[36:39], s25 offen offset:2048\n" \
"buffer_store_dwordx4 v[172:175], v4, s[36:39], s25 offen offset:3072\n" \
"s_waitcnt vmcnt(0)\n" \
"v_writelane_b32 v172, vcc_lo, 0\n" \
"v_writelane_b32 v172, vcc_hi, 1\n" \
"v_writelane_b32 v172, s64, 2\n" \
"v_writelane_b32 v172, s65, 3\n" \
"v_writelane_b32 v172, s66, 4\n" \
"v_writelane_b32 v172, s67, 5\n" \
"v_writelane_b32 v172, s68, 6\n" \
"v_writelane_b32 v172, s69, 7\n" \
"v_writelane_b32 v172, s70, 8\n" \
"v_writelane_b32 v172, s71, 9\n" \
"v_writelane_b32 v172, s72, 10\n" \
"v_writelane_b32 v172, s73, 11\n" \
"s_cmp_eq_u32 s99, 1\n" \
"s_cselect_b32 s64, s51, s50\n" \
"s_cselect_b32 s65, 2, 4\n" \
"s_cselect_b64 s[54:55], s[54:55], 0\n" \
"s_lshr_b32 s64, s64, 18\n" \
"s_lshl_b32 s64, s64, 5\n" \
"v_mov_b32 v160, 0x3f803f80\n" \
"v_mov_b32 v161, 0x3f803f80\n" \
"v_mov_b32 v162, 0x3f803f80\n" \
"v_mov_b32 v163, 0x3f803f80\n" \
"LFG_%=:\n" \
"s_mov_b32 s69, 0\n" \
"v_bfe_u32 v167, v4, 4, 4\n" \
"v_lshrrev_b32 v168, 8, v4\n" \
"v_add_u32 v165, s64, v167\n" \
"s_lshl_b32 s67, s6, 8\n" \
"v_lshl_add_u32 v128, v167, 1, s67\n" \
"v_lshl_add_u32 v128, v168, 16, v128\n" \
"v_add_u32 v129, 0x2000, v128\n" \
"v_add_u32 v130, 0x2000, v129\n" \
"v_add_u32 v131, 0x2000, v130\n" \
"v_add_u32 v132, 0x2000, v131\n" \
"v_add_u32 v133, 0x2000, v132\n" \
"v_add_u32 v134, 0x2000, v133\n" \
"v_add_u32 v135, 0x2000, v134\n" \
"LFQ_%=:\n" \
"s_load_dwordx2 s[70:71], %0, 0x0\n" \
"s_load_dwordx2 s[72:73], %0, 0x38\n" \
"v_lshlrev_b32 v166, 11, v165\n" \
"v_lshl_add_u32 v166, v168, 4, v166\n" \
"s_mov_b32 s66, 0\n" \
"v_mov_b32 v136, 0\n" \
"v_mov_b32 v137, 0\n" \
"v_mov_b32 v138, 0\n" \
"v_mov_b32 v139, 0\n" \
"v_mov_b32 v140, 0\n" \
"v_mov_b32 v141, 0\n" \
"v_mov_b32 v142, 0\n" \
"v_mov_b32 v143, 0\n" \
"s_waitcnt lgkmcnt(0)\n" \
"LFK_%=:\n" \
"global_load_dwordx4 v[112:115], v166, s[70:71]\n" \
"global_load_ushort v96, v128, s[72:73] offset:0\n" \
"global_load_ushort v97, v129, s[72:73] offset:0\n" \
"global_load_ushort v98, v130, s[72:73] offset:0\n" \
"global_load_ushort v99, v131, s[72:73] offset:0\n" \
"global_load_ushort v100, v132, s[72:73] offset:0\n" \
"global_load_ushort v101, v133, s[72:73] offset:0\n" \
"global_load_ushort v102, v134, s[72:73] offset:0\n" \
"global_load_ushort v103, v135, s[72:73] offset:0\n" \
"global_load_ushort v104, v128, s[72:73] offset:32\n" \
"global_load_ushort v105, v129, s[72:73] offset:32\n" \
"global_load_ushort v106, v130, s[72:73] offset:32\n" \
"global_load_ushort v107, v131, s[72:73] offset:32\n" \
"global_load_ushort v108, v132, s[72:73] offset:32\n" \
"global_load_ushort v109, v133, s[72:73] offset:32\n" \
"global_load_ushort v110, v134, s[72:73] offset:32\n" \
"global_load_ushort v111, v135, s[72:73] offset:32\n" \
"v_add_u32 v166, 64, v166\n" \
"s_add_u32 s72, s72, 262144\ns_addc_u32 s73, s73, 0\n" \
"s_add_u32 s66, s66, 1\n" \
"s_waitcnt vmcnt(0)\n" \
"v_lshl_or_b32 v116, v97, 16, v96\n" \
"v_lshl_or_b32 v117, v99, 16, v98\n" \
"v_lshl_or_b32 v118, v101, 16, v100\n" \
"v_lshl_or_b32 v119, v103, 16, v102\n" \
"v_lshl_or_b32 v120, v105, 16, v104\n" \
"v_lshl_or_b32 v121, v107, 16, v106\n" \
"v_lshl_or_b32 v122, v109, 16, v108\n" \
"v_lshl_or_b32 v123, v111, 16, v110\n" \
"s_nop 1\n" \
"v_mfma_f32_16x16x32_bf16 v[136:139], v[116:119], v[112:115], v[136:139]\n" \
"v_mfma_f32_16x16x32_bf16 v[140:143], v[120:123], v[112:115], v[140:143]\n" \
"s_cmp_lt_u32 s66, 32\n" \
"s_cbranch_scc1 LFK_%=\n" \
"s_nop 7\n" \
"v_mov_b32 v144, v148\n" \
"v_mov_b32 v145, v149\n" \
"v_mov_b32 v146, v150\n" \
"v_mov_b32 v147, v151\n" \
"v_mov_b32 v148, v152\n" \
"v_mov_b32 v149, v153\n" \
"v_mov_b32 v150, v154\n" \
"v_mov_b32 v151, v155\n" \
"v_mov_b32 v152, v156\n" \
"v_mov_b32 v153, v157\n" \
"v_mov_b32 v154, v158\n" \
"v_mov_b32 v155, v159\n" \
"v_cvt_pk_bf16_f32 v156, v136, v137\n" \
"v_cvt_pk_bf16_f32 v157, v138, v139\n" \
"v_cvt_pk_bf16_f32 v158, v140, v141\n" \
"v_cvt_pk_bf16_f32 v159, v142, v143\n" \
"v_add_u32 v128, 64, v128\n" \
"v_add_u32 v129, 64, v129\n" \
"v_add_u32 v130, 64, v130\n" \
"v_add_u32 v131, 64, v131\n" \
"v_add_u32 v132, 64, v132\n" \
"v_add_u32 v133, 64, v133\n" \
"v_add_u32 v134, 64, v134\n" \
"v_add_u32 v135, 64, v135\n" \
"s_add_u32 s69, s69, 1\n" \
"s_cmp_lt_u32 s69, 4\n" \
"s_cbranch_scc1 LFQ_%=\n" \
"v_mov_b32 v64, 0\n" \
"v_mov_b32 v65, 0\n" \
"v_mov_b32 v66, 0\n" \
"v_mov_b32 v67, 0\n" \
"v_mov_b32 v68, 0\n" \
"v_mov_b32 v69, 0\n" \
"v_mov_b32 v70, 0\n" \
"v_mov_b32 v71, 0\n" \
"v_mov_b32 v72, 0\n" \
"v_mov_b32 v73, 0\n" \
"v_mov_b32 v74, 0\n" \
"v_mov_b32 v75, 0\n" \
"v_mov_b32 v76, 0\n" \
"v_mov_b32 v77, 0\n" \
"v_mov_b32 v78, 0\n" \
"v_mov_b32 v79, 0\n" \
"v_mov_b32 v80, 0\n" \
"v_mov_b32 v81, 0\n" \
"v_mov_b32 v82, 0\n" \
"v_mov_b32 v83, 0\n" \
"v_mov_b32 v84, 0\n" \
"v_mov_b32 v85, 0\n" \
"v_mov_b32 v86, 0\n" \
"v_mov_b32 v87, 0\n" \
"v_mov_b32 v88, 0\n" \
"v_mov_b32 v89, 0\n" \
"v_mov_b32 v90, 0\n" \
"v_mov_b32 v91, 0\n" \
"v_mov_b32 v92, 0\n" \
"v_mov_b32 v93, 0\n" \
"v_mov_b32 v94, 0\n" \
"v_mov_b32 v95, 0\n" \
"v_mov_b32 v132, 0\n" \
"v_mov_b32 v133, 0\n" \
"v_mov_b32 v134, 0\n" \
"v_mov_b32 v135, 0\n" \
"v_mov_b32 v164, 0xff800000\n" \
"v_mov_b32 v169, 0xff800000\n" \
"s_mov_b32 s69, 0\n" \
"s_lshr_b32 s68, s64, 5\n" \
"LFP_%=:\n" \
"s_mov_b32 s66, 0\n" \
"s_lshl_b32 s67, s6, 20\n" \
"s_add_u32 s67, s67, 33554432\n" \
"v_lshlrev_b32 v166, 2, v168\n" \
"v_sub_u32 v166, v165, v166\n" \
"LFT_%=:\n" \
"buffer_load_dwordx4 v[96:99], v4, s[36:39], s67 offen offset:0 sc1\n" \
"buffer_load_dwordx4 v[100:103], v4, s[36:39], s67 offen offset:1024 sc1\n" \
"buffer_load_dwordx4 v[104:107], v4, s[36:39], s67 offen offset:2048 sc1\n" \
"buffer_load_dwordx4 v[108:111], v4, s[36:39], s67 offen offset:3072 sc1\n" \
"buffer_load_dwordx4 v[112:115], v7, s[36:39], s67 offen offset:0 sc1\n" \
"buffer_load_dwordx4 v[116:119], v7, s[36:39], s67 offen offset:1024 sc1\n" \
"buffer_load_dwordx4 v[120:123], v7, s[36:39], s67 offen offset:2048 sc1\n" \
"buffer_load_dwordx4 v[124:127], v7, s[36:39], s67 offen offset:3072 sc1\n" \
"s_add_u32 s67, s67, 8192\n" \
"s_waitcnt vmcnt(0)\n" \
"v_mfma_f32_16x16x32_bf16 v[136:139], v[96:99], v[144:147], 0\n" \
"v_mfma_f32_16x16x32_bf16 v[136:139], v[100:103], v[148:151], v[136:139]\n" \
"v_mfma_f32_16x16x32_bf16 v[136:139], v[104:107], v[152:155], v[136:139]\n" \
"v_mfma_f32_16x16x32_bf16 v[136:139], v[108:111], v[156:159], v[136:139]\n" \
"v_mfma_f32_16x16x32_bf16 v[140:143], v[112:115], v[144:147], 0\n" \
"v_mfma_f32_16x16x32_bf16 v[140:143], v[116:119], v[148:151], v[140:143]\n" \
"v_mfma_f32_16x16x32_bf16 v[140:143], v[120:123], v[152:155], v[140:143]\n" \
"v_mfma_f32_16x16x32_bf16 v[140:143], v[124:127], v[156:159], v[140:143]\n" \
"s_nop 7\n" \
"v_cmp_gt_i32 vcc, 0, v166\n" \
"s_nop 1\n" \
"v_cndmask_b32 v136, v136, v169, vcc\n" \
"v_cmp_gt_i32 vcc, 1, v166\n" \
"s_nop 1\n" \
"v_cndmask_b32 v137, v137, v169, vcc\n" \
"v_cmp_gt_i32 vcc, 2, v166\n" \
"s_nop 1\n" \
"v_cndmask_b32 v138, v138, v169, vcc\n" \
"v_cmp_gt_i32 vcc, 3, v166\n" \
"s_nop 1\n" \
"v_cndmask_b32 v139, v139, v169, vcc\n" \
"v_cmp_gt_i32 vcc, 16, v166\n" \
"s_nop 1\n" \
"v_cndmask_b32 v140, v140, v169, vcc\n" \
"v_cmp_gt_i32 vcc, 17, v166\n" \
"s_nop 1\n" \
"v_cndmask_b32 v141, v141, v169, vcc\n" \
"v_cmp_gt_i32 vcc, 18, v166\n" \
"s_nop 1\n" \
"v_cndmask_b32 v142, v142, v169, vcc\n" \
"v_cmp_gt_i32 vcc, 19, v166\n" \
"s_nop 1\n" \
"v_cndmask_b32 v143, v143, v169, vcc\n" \
"s_cmp_lg_u32 s69, 0\n" \
"s_cbranch_scc1 LF2_%=\n" \
"v_max3_f32 v164, v164, v136, v137\n" \
"v_max3_f32 v164, v164, v138, v139\n" \
"v_max3_f32 v164, v164, v140, v141\n" \
"v_max3_f32 v164, v164, v142, v143\n" \
"s_branch LFN_%=\n" \
"LF2_%=:\n" \
"buffer_load_dwordx4 v[96:99], v4, s[36:39], s67 offen offset:0 sc1\n" \
"buffer_load_dwordx4 v[100:103], v4, s[36:39], s67 offen offset:1024 sc1\n" \
"buffer_load_dwordx4 v[104:107], v4, s[36:39], s67 offen offset:2048 sc1\n" \
"buffer_load_dwordx4 v[108:111], v4, s[36:39], s67 offen offset:3072 sc1\n" \
"buffer_load_dwordx4 v[112:115], v7, s[36:39], s67 offen offset:0 sc1\n" \
"buffer_load_dwordx4 v[116:119], v7, s[36:39], s67 offen offset:1024 sc1\n" \
"buffer_load_dwordx4 v[120:123], v7, s[36:39], s67 offen offset:2048 sc1\n" \
"buffer_load_dwordx4 v[124:127], v7, s[36:39], s67 offen offset:3072 sc1\n" \
"v_sub_f32 v136, v136, v164\n" \
"v_sub_f32 v137, v137, v164\n" \
"v_sub_f32 v138, v138, v164\n" \
"v_sub_f32 v139, v139, v164\n" \
"v_sub_f32 v140, v140, v164\n" \
"v_sub_f32 v141, v141, v164\n" \
"v_sub_f32 v142, v142, v164\n" \
"v_sub_f32 v143, v143, v164\n" \
"v_exp_f32 v136, v136\n" \
"v_exp_f32 v137, v137\n" \
"v_exp_f32 v138, v138\n" \
"v_exp_f32 v139, v139\n" \
"v_exp_f32 v140, v140\n" \
"v_exp_f32 v141, v141\n" \
"v_exp_f32 v142, v142\n" \
"v_exp_f32 v143, v143\n" \
"s_nop 0\n" \
"v_cvt_pk_bf16_f32 v128, v136, v137\n" \
"v_cvt_pk_bf16_f32 v129, v138, v139\n" \
"v_cvt_pk_bf16_f32 v130, v140, v141\n" \
"v_cvt_pk_bf16_f32 v131, v142, v143\n" \
"s_waitcnt vmcnt(0)\n" \
"s_nop 1\n" \
"v_mfma_f32_16x16x32_bf16 v[132:135], v[160:163], v[128:131], v[132:135]\n" \
"v_mfma_f32_16x16x32_bf16 v[64:67], v[96:99], v[128:131], v[64:67]\n" \
"v_mfma_f32_16x16x32_bf16 v[68:71], v[100:103], v[128:131], v[68:71]\n" \
"v_mfma_f32_16x16x32_bf16 v[72:75], v[104:107], v[128:131], v[72:75]\n" \
"v_mfma_f32_16x16x32_bf16 v[76:79], v[108:111], v[128:131], v[76:79]\n" \
"v_mfma_f32_16x16x32_bf16 v[80:83], v[112:115], v[128:131], v[80:83]\n" \
"v_mfma_f32_16x16x32_bf16 v[84:87], v[116:119], v[128:131], v[84:87]\n" \
"v_mfma_f32_16x16x32_bf16 v[88:91], v[120:123], v[128:131], v[88:91]\n" \
"v_mfma_f32_16x16x32_bf16 v[92:95], v[124:127], v[128:131], v[92:95]\n" \
"LFN_%=:\n" \
"s_add_u32 s67, s67, 8192\n" \
"v_subrev_u32 v166, 32, v166\n" \
"s_add_u32 s66, s66, 1\n" \
"s_cmp_le_u32 s66, s68\n" \
"s_cbranch_scc1 LFT_%=\n" \
"v_mov_b32 v170, v164\n" \
"s_nop 1\n" \
"v_permlane32_swap_b32 v164, v170\n" \
"s_nop 0\n" \
"v_max_f32 v164, v164, v170\n" \
"v_mov_b32 v170, v164\n" \
"s_nop 1\n" \
"v_permlane16_swap_b32 v164, v170\n" \
"s_nop 0\n" \
"v_max_f32 v164, v164, v170\n" \
"s_add_u32 s69, s69, 1\n" \
"s_cmp_lt_u32 s69, 2\n" \
"s_cbranch_scc1 LFP_%=\n" \
"s_nop 7\n" \
"v_rcp_f32 v170, v132\n" \
"s_lshl_b32 s67, s68, 5\n" \
"s_add_u32 s67, s67, s6\n" \
"s_lshl_b32 s67, s67, 13\n" \
"s_and_b32 s66, s64, 16\n" \
"s_lshl_b32 s66, s66, 6\n" \
"s_add_u32 s67, s67, s66\n" \
"v_mul_f32 v64, v170, v64\n" \
"v_mul_f32 v65, v170, v65\n" \
"v_mul_f32 v66, v170, v66\n" \
"v_mul_f32 v67, v170, v67\n" \
"v_mul_f32 v68, v170, v68\n" \
"v_mul_f32 v69, v170, v69\n" \
"v_mul_f32 v70, v170, v70\n" \
"v_mul_f32 v71, v170, v71\n" \
"v_mul_f32 v72, v170, v72\n" \
"v_mul_f32 v73, v170, v73\n" \
"v_mul_f32 v74, v170, v74\n" \
"v_mul_f32 v75, v170, v75\n" \
"v_mul_f32 v76, v170, v76\n" \
"v_mul_f32 v77, v170, v77\n" \
"v_mul_f32 v78, v170, v78\n" \
"v_mul_f32 v79, v170, v79\n" \
"v_mul_f32 v80, v170, v80\n" \
"v_mul_f32 v81, v170, v81\n" \
"v_mul_f32 v82, v170, v82\n" \
"v_mul_f32 v83, v170, v83\n" \
"v_mul_f32 v84, v170, v84\n" \
"v_mul_f32 v85, v170, v85\n" \
"v_mul_f32 v86, v170, v86\n" \
"v_mul_f32 v87, v170, v87\n" \
"v_mul_f32 v88, v170, v88\n" \
"v_mul_f32 v89, v170, v89\n" \
"v_mul_f32 v90, v170, v90\n" \
"v_mul_f32 v91, v170, v91\n" \
"v_mul_f32 v92, v170, v92\n" \
"v_mul_f32 v93, v170, v93\n" \
"v_mul_f32 v94, v170, v94\n" \
"v_mul_f32 v95, v170, v95\n" \
"v_cvt_pk_bf16_f32 v64, v64, v65\n" \
"v_cvt_pk_bf16_f32 v65, v66, v67\n" \
"v_cvt_pk_bf16_f32 v66, v68, v69\n" \
"v_cvt_pk_bf16_f32 v67, v70, v71\n" \
"v_cvt_pk_bf16_f32 v68, v72, v73\n" \
"v_cvt_pk_bf16_f32 v69, v74, v75\n" \
"v_cvt_pk_bf16_f32 v70, v76, v77\n" \
"v_cvt_pk_bf16_f32 v71, v78, v79\n" \
"v_cvt_pk_bf16_f32 v72, v80, v81\n" \
"v_cvt_pk_bf16_f32 v73, v82, v83\n" \
"v_cvt_pk_bf16_f32 v74, v84, v85\n" \
"v_cvt_pk_bf16_f32 v75, v86, v87\n" \
"v_cvt_pk_bf16_f32 v76, v88, v89\n" \
"v_cvt_pk_bf16_f32 v77, v90, v91\n" \
"v_cvt_pk_bf16_f32 v78, v92, v93\n" \
"v_cvt_pk_bf16_f32 v79, v94, v95\n" \
"buffer_store_dwordx4 v[64:67], v4, s[36:39], s67 offen offset:0 sc1\n" \
"buffer_store_dwordx4 v[68:71], v4, s[36:39], s67 offen offset:2048 sc1\n" \
"buffer_store_dwordx4 v[72:75], v7, s[36:39], s67 offen offset:0 sc1\n" \
"buffer_store_dwordx4 v[76:79], v7, s[36:39], s67 offen offset:2048 sc1\n" \
"s_waitcnt vmcnt(0)\n" \
"s_add_u32 s64, s64, 16\n" \
"s_sub_u32 s65, s65, 1\n" \
"s_cmp_lg_u32 s65, 0\n" \
"s_cbranch_scc1 LFG_%=\n" \
"v_readlane_b32 vcc_lo, v172, 0\n" \
"v_readlane_b32 vcc_hi, v172, 1\n" \
"v_readlane_b32 s64, v172, 2\n" \
"v_readlane_b32 s65, v172, 3\n" \
"v_readlane_b32 s66, v172, 4\n" \
"v_readlane_b32 s67, v172, 5\n" \
"v_readlane_b32 s68, v172, 6\n" \
"v_readlane_b32 s69, v172, 7\n" \
"v_readlane_b32 s70, v172, 8\n" \
"v_readlane_b32 s71, v172, 9\n" \
"v_readlane_b32 s72, v172, 10\n" \
"v_readlane_b32 s73, v172, 11\n" \
"s_lshl_b32 s25, %1, 2\n" \
"s_add_u32 s25, s25, %2\n" \
"s_lshl_b32 s25, s25, 13\n" \
"s_add_u32 s25, s25, 16777216\n" \
"buffer_load_dwordx4 v[64:67], v4, s[36:39], s25 offen offset:0\n" \
"buffer_load_dwordx4 v[68:71], v4, s[36:39], s25 offen offset:1024\n" \
"buffer_load_dwordx4 v[72:75], v4, s[36:39], s25 offen offset:2048\n" \
"buffer_load_dwordx4 v[76:79], v4, s[36:39], s25 offen offset:3072\n" \
"buffer_load_dwordx4 v[80:83], v7, s[36:39], s25 offen offset:0\n" \
"buffer_load_dwordx4 v[84:87], v7, s[36:39], s25 offen offset:1024\n" \
"buffer_load_dwordx4 v[88:91], v7, s[36:39], s25 offen offset:2048\n" \
"buffer_load_dwordx4 v[92:95], v7, s[36:39], s25 offen offset:3072\n" \
"s_add_u32 s25, s25, 8388608\n" \
"buffer_load_dwordx4 v[96:99], v4, s[36:39], s25 offen offset:0\n" \
"buffer_load_dwordx4 v[100:103], v4, s[36:39], s25 offen offset:1024\n" \
"buffer_load_dwordx4 v[104:107], v4, s[36:39], s25 offen offset:2048\n" \
"buffer_load_dwordx4 v[108:111], v4, s[36:39], s25 offen offset:3072\n" \
"buffer_load_dwordx4 v[112:115], v7, s[36:39], s25 offen offset:0\n" \
"buffer_load_dwordx4 v[116:119], v7, s[36:39], s25 offen offset:1024\n" \
"buffer_load_dwordx4 v[120:123], v7, s[36:39], s25 offen offset:2048\n" \
"buffer_load_dwordx4 v[124:127], v7, s[36:39], s25 offen offset:3072\n" \
"s_add_u32 s25, s25, 58720256\n" \
"buffer_load_dwordx4 v[128:131], v4, s[36:39], s25 offen offset:0\n" \
"buffer_load_dwordx4 v[132:135], v4, s[36:39], s25 offen offset:1024\n" \
"buffer_load_dwordx4 v[136:139], v4, s[36:39], s25 offen offset:2048\n" \
"buffer_load_dwordx4 v[140:143], v4, s[36:39], s25 offen offset:3072\n" \
"buffer_load_dwordx4 v[144:147], v7, s[36:39], s25 offen offset:0\n" \
"buffer_load_dwordx4 v[148:151], v7, s[36:39], s25 offen offset:1024\n" \
"buffer_load_dwordx4 v[152:155], v7, s[36:39], s25 offen offset:2048\n" \
"buffer_load_dwordx4 v[156:159], v7, s[36:39], s25 offen offset:3072\n" \
"s_add_u32 s25, s25, 37748736\n" \
"buffer_load_dwordx4 v[160:163], v4, s[36:39], s25 offen offset:0\n" \
"buffer_load_dwordx4 v[164:167], v4, s[36:39], s25 offen offset:1024\n" \
"buffer_load_dwordx4 v[168:171], v4, s[36:39], s25 offen offset:2048\n" \
"buffer_load_dwordx4 v[172:175], v4, s[36:39], s25 offen offset:3072\n" \
"s_waitcnt vmcnt(0)\n" \
"s_cmp_eq_u32 s99, 0\n" \
"s_cbranch_scc1 LFBAR_%=\n" \
"s_cmp_eq_u32 s99, 1\n" \
"s_cbranch_scc1 LFBBR_%=\n" \
"s_branch LFBCR_%=\n" \
"LEND_%=:\n" \
"s_waitcnt vmcnt(0) lgkmcnt(0)\n" \
""
#define QKV_ASM_CLOBBERS "v1", "v2", "v3", "v4", "v5", "v6", "v7", "v8", "v9", "v10", "v11", "v12", "v13", "v14", "v15", "v16", "v17", "v18", "v19", "v20", "v21", "v22", "v23", "v24", "v25", "v26", "v27", "v28", "v29", "v30", "v31", "v32", "v33", "v34", "v35", "v36", "v37", "v38", "v39", "v40", "v41", "v42", "v43", "v44", "v45", "v46", "v47", "v48", "v49", "v50", "v51", "v52", "v53", "v54", "v55", "v56", "v57", "v58", "v59", "v60", "v61", "v62", "v63", "v64", "v65", "v66", "v67", "v68", "v69", "v70", "v71", "v72", "v73", "v74", "v75", "v76", "v77", "v78", "v79", "v80", "v81", "v82", "v83", "v84", "v85", "v86", "v87", "v88", "v89", "v90", "v91", "v92", "v93", "v94", "v95", "v96", "v97", "v98", "v99", "v100", "v101", "v102", "v103", "v104", "v105", "v106", "v107", "v108", "v109", "v110", "v111", "v112", "v113", "v114", "v115", "v116", "v117", "v118", "v119", "v120", "v121", "v122", "v123", "v124", "v125", "v126", "v127", "v128", "v129", "v130", "v131", "v132", "v133", "v134", "v135", "v136", "v137", "v138", "v139", "v140", "v141", "v142", "v143", "v144", "v145", "v146", "v147", "v148", "v149", "v150", "v151", "v152", "v153", "v154", "v155", "v156", "v157", "v158", "v159", "v160", "v161", "v162", "v163", "v164", "v165", "v166", "v167", "v168", "v169", "v170", "v171", "v172", "v173", "v174", "v175", "v176", "v177", "v178", "v179", "v180", "v181", "v182", "v183", "v184", "v185", "v186", "v187", "v188", "v189", "v190", "v191", "v192", "v193", "v194", "v195", "v196", "v197", "v198", "v199", "v200", "v201", "v202", "v203", "v204", "v205", "v206", "v207", "v208", "v209", "v210", "v211", "v212", "v213", "v214", "v215", "v216", "v217", "v218", "v219", "v220", "v221", "v222", "v223", "v224", "v225", "v226", "v227", "v228", "v229", "v230", "v231", "v232", "v233", "v234", "v235", "v236", "v237", "v238", "v239", "v240", "v241", "v242", "v243", "v244", "v245", "v246", "v247", "v248", "v249", "v250", "v251", "v252", "v253", "v254", "v255", "a0", "a1", "a2", "a3", "a4", "a5", "a6", "a7", "a8", "a9", "a10", "a11", "a12", "a13", "a14", "a15", "a16", "a17", "a18", "a19", "a20", "a21", "a22", "a23", "a24", "a25", "a26", "a27", "a28", "a29", "a30", "a31", "a32", "a33", "a34", "a35", "a36", "a37", "a38", "a39", "a40", "a41", "a42", "a43", "a44", "a45", "a46", "a47", "a48", "a49", "a50", "a51", "a52", "a53", "a54", "a55", "a56", "a57", "a58", "a59", "a60", "a61", "a62", "a63", "a64", "a65", "a66", "a67", "a68", "a69", "a70", "a71", "a72", "a73", "a74", "a75", "a76", "a77", "a78", "a79", "a80", "a81", "a82", "a83", "a84", "a85", "a86", "a87", "a88", "a89", "a90", "a91", "a92", "a93", "a94", "a95", "a96", "a97", "a98", "a99", "a100", "a101", "a102", "a103", "a104", "a105", "a106", "a107", "a108", "a109", "a110", "a111", "a112", "a113", "a114", "a115", "a116", "a117", "a118", "a119", "a120", "a121", "a122", "a123", "a124", "a125", "a126", "a127", "a128", "a129", "a130", "a131", "a132", "a133", "a134", "a135", "a136", "a137", "a138", "a139", "a140", "a141", "a142", "a143", "a144", "a145", "a146", "a147", "a148", "a149", "a150", "a151", "a152", "a153", "a154", "a155", "a156", "a157", "a158", "a159", "a160", "a161", "a162", "a163", "a164", "a165", "a166", "a167", "a168", "a169", "a170", "a171", "a172", "a173", "a174", "a175", "a176", "a177", "a178", "a179", "a180", "a181", "a182", "a183", "a184", "a185", "a186", "a187", "a188", "a189", "a190", "a191", "a192", "a193", "a194", "a195", "a196", "a197", "a198", "a199", "a200", "a201", "a202", "a203", "a204", "a205", "a206", "a207", "a208", "a209", "a210", "a211", "a212", "a213", "a214", "a215", "a216", "a217", "a218", "a219", "a220", "a221", "a222", "a223", "a224", "a225", "a226", "a227", "a228", "a229", "a230", "a231", "a232", "a233", "a234", "a235", "a236", "a237", "a238", "a239", "a240", "a241", "a242", "a243", "a244", "a245", "a246", "a247", "a248", "a249", "a250", "a251", "a252", "a253", "a254", "a255", "s4", "s5", "s6", "s7", "s8", "s9", "s10", "s11", "s12", "s13", "s14", "s15", "s16", "s17", "s18", "s19", "s20", "s21", "s22", "s23", "s24", "s25", "s26", "s27", "s28", "s29", "s30", "s31", "s32", "s33", "s34", "s35", "s36", "s37", "s38", "s39", "s40", "s41", "s42", "s43", "s44", "s45", "s46", "s47", "s48", "s49", "s50", "s51", "s52", "s53", "s54", "s55", "s56", "s57", "s58", "s59", "s60", "s61", "s62", "s63", "s64", "s65", "s66", "s67", "s68", "s69", "s70", "s71", "s72", "s73", "s74", "s75", "s76", "s77", "s78", "s79", "s80", "s81", "s82", "s83", "s84", "s85", "s86", "s87", "s88", "s89", "s90", "s91", "s92", "s93", "s94", "s95", "s96", "s97", "s98", "s99", "s100", "s101", "vcc", "scc", "memory"
constexpr int Q_LDS = 163840;
__global__ __launch_bounds__(256, 1) void fused_kernel(const void* x, const void* k, const void* v, const void* shk, const void* ssmk, const void* ssmD, const void* ckv,
                                                       const void* wq, const void* wuk, const void* wuv, const void* wo, const void* gw, void* out, void* ws) {
    const int w = __builtin_amdgcn_readfirstlane(threadIdx.x >> 6);
    const unsigned long long ka = (unsigned long long)__builtin_amdgcn_kernarg_segment_ptr();
    asm volatile(QKV_ASM_TEXT :: "s"(ka), "s"(blockIdx.x), "s"(w) : QKV_ASM_CLOBBERS);
}

extern "C" void kernel_launch(void* const* d_in, const int* in_sizes, int n_in, void* d_out, int out_size, void* d_ws, size_t ws_size,
                              hipStream_t stream) {
    (void)hipFuncSetAttribute((const void*)fused_kernel, hipFuncAttributeMaxDynamicSharedMemorySize, Q_LDS);
    fused_kernel<<<256, 256, Q_LDS, stream>>>(d_in[0], d_in[1], d_in[2], d_in[3], d_in[4], d_in[5], d_in[7], d_in[8], d_in[9], d_in[10], d_in[11], d_in[12], d_out, d_ws);
}
```

```cpp
#include <hip/hip_runtime.h>
#include <stdint.h>
#define QKV_ASM_TEXT \
"s_nop 0\n" \
"s_load_dwordx16 s[64:79], %0, 0x0\n" \
"s_load_dwordx8 s[80:87], %0, 0x40\n" \
"s_load_dwordx4 s[88:91], %0, 0x60\n" \
"v_mbcnt_lo_u32_b32 v23, -1, 0\n" \
"v_mbcnt_hi_u32_b32 v23, -1, v23\n" \
"s_bfe_u32 s58, %1, 0x30003\n" \
"s_and_b32 s59, %1, 7\n" \
"s_lshl_b32 s59, s59, 1\n" \
"s_bfe_u32 s60, %1, 0x10006\n" \
"s_add_u32 s59, s59, s60\n" \
"v_lshlrev_b32 v4, 4, v23\n" \
"s_getpc_b64 s[40:41]\nLCTBEG_%=:\n" \
"s_add_u32 s40, s40, LBEG_%=-LCTBEG_%=\ns_addc_u32 s41, s41, 0\n" \
"s_and_b32 s41, s41, 0xffff\n" \
"s_mov_b32 s42, LEND_%=-LBEG_%=\n" \
"s_mov_b32 s43, 0x00020000\n" \
"s_lshr_b32 s62, %1, 3\n" \
"s_lshl_b32 s62, s62, 2\n" \
"s_add_u32 s62, s62, %2\n" \
"s_and_b32 s62, s62, 31\n" \
"s_lshl_b32 s62, s62, 13\n" \
"v_lshlrev_b32 v64, 3, v4\n" \
"buffer_load_dword v64, v64, s[40:43], s62 offen\n" \
"s_lshl_b32 s60, %2, 2\n" \
"v_lshrrev_b32 v24, 4, v23\n" \
"v_add_u32 v25, s60, v24\n" \
"v_xor_b32 v25, v25, v23\n" \
"v_and_b32 v25, 7, v25\n" \
"s_lshl_b32 s61, %2, 3\n" \
"v_lshrrev_b32 v26, 3, v23\n" \
"v_add_u32 v26, s61, v26\n" \
"v_lshlrev_b32 v26, 10, v26\n" \
"v_lshl_add_u32 v13, v25, 4, v26\n" \
"v_add_u32 v63, v13, v26\n" \
"v_add_u32 v14, 0x8000, v13\n" \
"v_add_u32 v15, 0x8000, v14\n" \
"v_add_u32 v16, 0x8000, v15\n" \
"v_add_u32 v17, 0x8000, v16\n" \
"v_add_u32 v18, 0x8000, v17\n" \
"v_add_u32 v19, 0x8000, v18\n" \
"v_add_u32 v20, 0x8000, v19\n" \
"v_bfe_u32 v25, v23, 2, 3\n" \
"v_add_u32 v25, s61, v25\n" \
"v_lshlrev_b32 v25, 13, v25\n" \
"s_lshl_b32 s61, %2, 1\n" \
"v_and_b32 v26, 1, v24\n" \
"v_add_u32 v26, s61, v26\n" \
"v_xor_b32 v26, v26, v23\n" \
"v_and_b32 v26, 3, v26\n" \
"v_lshrrev_b32 v27, 5, v23\n" \
"v_lshl_add_u32 v26, v27, 2, v26\n" \
"v_lshl_add_u32 v21, v26, 4, v25\n" \
"s_lshl_b32 s50, %2, 10\n" \
"s_add_u32 s50, s50, 65536\n" \
"s_lshl_b32 s22, %2, 12\n" \
"s_lshl_b32 s51, %2, 11\n" \
"s_mov_b32 s31, 0x00020000\n" \
"s_mov_b32 s35, 0x00020000\n" \
"s_mov_b32 s30, 262144\n" \
"s_lshl_b32 s60, s58, 18\n" \
"s_lshl_b32 s61, s59, 9\n" \
"s_sub_u32 s34, 4194304, s61\n" \
"s_mov_b32 s46, s60\n" \
"s_mov_b32 s47, s61\n" \
"s_lshl_b32 s57, s59, 1\n" \
"s_and_b32 s60, %2, 1\n" \
"s_add_u32 s57, s57, s60\n" \
"s_lshl_b32 s57, s57, 20\n" \
"s_lshl_b32 s60, s58, 17\n" \
"s_add_u32 s57, s57, s60\n" \
"s_lshr_b32 s60, %2, 1\n" \
"s_lshl_b32 s60, s60, 16\n" \
"s_add_u32 s57, s57, s60\n" \
"s_add_u32 s57, s57, 33554432\n" \
"s_lshl_b32 s24, s59, 7\n" \
"s_lshl_b32 s60, s58, 3\n" \
"s_add_u32 s24, s24, s60\n" \
"s_lshl_b32 s60, %2, 27\n" \
"s_add_u32 s24, s24, s60\n" \
"s_and_b32 s23, %1, 7\n" \
"s_lshl_b32 s23, s23, 8\n" \
"s_add_u32 s23, s23, s60\n" \
"s_and_b32 s60, %1, 0xf8\n" \
"s_add_u32 s23, s23, s60\n" \
"s_add_u32 s23, s23, 94380032\n" \
"s_cmp_lt_u32 %1, 128\n" \
"s_cselect_b32 s60, 0, 8192\n" \
"s_cselect_b32 s61, 0, 64\n" \
"s_cselect_b32 s62, 14, 14\n" \
"s_cselect_b32 s63, 10, 11\n" \
"s_cselect_b32 s98, 12, 10\n" \
"s_cselect_b32 s20, 0x3e0293ee, 1.0\n" \
"s_add_u32 s57, s57, s60\n" \
"s_add_u32 s24, s24, s61\n" \
"s_add_u32 s24, s24, 94375936\n" \
"s_lshl_b32 s92, 1, s62\n" \
"s_lshl_b32 s93, 1, s63\n" \
"s_lshl_b32 s98, 1, s98\n" \
"s_and_b32 s6, %1, 7\n" \
"s_lshl_b32 s8, s6, 5\n" \
"s_lshl_b32 s6, s6, 2\n" \
"s_lshr_b32 s58, %1, 6\n" \
"s_add_u32 s6, s6, s58\n" \
"s_lshr_b32 s58, %1, 3\n" \
"s_add_u32 s8, s8, s58\n" \
"s_and_b32 s7, s58, 7\n" \
"s_lshl_b32 s58, s6, 8\n" \
"s_mov_b32 s94, s58\n" \
"s_sub_u32 s49, 8388608, s58\n" \
"s_waitcnt lgkmcnt(0)\n" \
"s_cmp_lt_u32 %1, 128\n" \
"s_cselect_b64 s[32:33], s[80:81], s[82:83]\n" \
"s_add_u32 s28, s76, s46\ns_addc_u32 s29, s77, 0\n" \
"s_and_b32 s29, s29, 0xffff\n" \
"s_add_u32 s32, s32, s47\ns_addc_u32 s33, s33, 0\n" \
"s_and_b32 s33, s33, 0xffff\n" \
"s_mov_b32 s99, 94371840\n" \
"s_load_dwordx2 s[14:15], s[90:91], s99\n" \
"s_mov_b32 s46, 0\n" \
"s_add_u32 m0, s50, 65536\n" \
"s_mov_b64 s[36:37], s[90:91]\n" \
"buffer_load_dwordx4 v13, s[28:31], s46 offen lds\n" \
"s_and_b32 s37, s37, 0xffff\n" \
"s_mov_b32 s38, 134217728\n" \
"s_mov_b32 s39, 0x00020000\n" \
"v_lshrrev_b32 v23, 4, v4\n" \
"s_add_u32 m0, s50, 69632\n" \
"v_and_b32 v24, 15, v23\n" \
"buffer_load_dwordx4 v14, s[28:31], s46 offen lds\n" \
"v_lshrrev_b32 v25, 4, v23\n" \
"s_lshr_b32 s58, %2, 1\n" \
"s_lshl_b32 s58, s58, 14\n" \
"s_and_b32 s54, %2, 1\n" \
"s_add_u32 m0, s50, 73728\n" \
"s_lshl_b32 s54, s54, 13\n" \
"buffer_load_dwordx4 v15, s[28:31], s46 offen lds\n" \
"s_add_u32 s58, s58, 65536\n" \
"v_lshrrev_b32 v26, 1, v24\n" \
"v_xor_b32 v26, v26, v25\n" \
"v_lshlrev_b32 v26, 4, v26\n" \
"s_add_u32 m0, s50, 77824\n" \
"v_lshl_add_u32 v26, v24, 7, v26\n" \
"buffer_load_dwordx4 v16, s[28:31], s46 offen lds\n" \
"v_add_u32 v5, s58, v26\n" \
"v_xor_b32 v6, 64, v5\n" \
"v_add_u32 v7, 0x10000, v5\n" \
"v_add_u32 v8, 0x10000, v6\n" \
"s_mov_b32 s47, 0\n" \
"s_add_u32 m0, s22, 0\n" \
"s_and_b32 s58, %2, 1\n" \
"buffer_load_dwordx4 v21, s[32:35], s47 offen offset:0 nt lds\n" \
"s_lshl_b32 s58, s58, 11\n" \
"v_lshrrev_b32 v26, 2, v24\n" \
"v_lshlrev_b32 v26, 6, v26\n" \
"v_and_b32 v27, 1, v23\n" \
"s_mov_b32 s46, 0\n" \
"s_add_u32 m0, s50, 81920\n" \
"v_lshl_add_u32 v26, v27, 3, v26\n" \
"buffer_load_dwordx4 v17, s[28:31], s46 offen lds\n" \
"v_bfe_u32 v27, v23, 1, 1\n" \
"v_lshlrev_b32 v28, 1, v25\n" \
"v_xor_b32 v27, v27, v28\n" \
"v_and_b32 v27, 3, v27\n" \
"s_add_u32 m0, s22, 1792\n" \
"v_lshl_add_u32 v26, v27, 4, v26\n" \
"buffer_load_dwordx4 v21, s[32:35], s47 offen offset:256 nt lds\n" \
"v_lshl_add_u32 v9, v25, 12, v26\n" \
"v_add_u32 v9, s58, v9\n" \
"v_lshl_add_u32 v62, v25, 11, s58\n" \
"v_xor_b32 v10, 0x110, v9\n" \
"s_add_u32 m0, s50, 86016\n" \
"v_xor_b32 v11, 32, v9\n" \
"buffer_load_dwordx4 v18, s[28:31], s46 offen lds\n" \
"v_xor_b32 v12, 0x130, v9\n" \
"s_and_b32 s62, %1, 7\n" \
"s_lshl_b32 s62, s62, 7\n" \
"s_lshl_b32 s63, %2, 6\n" \
"s_mov_b32 s47, 262144\n" \
"s_add_u32 m0, s22, 1024\n" \
"s_and_b32 s63, s63, 127\n" \
"buffer_load_dwordx4 v21, s[32:35], s47 offen offset:0 nt lds\n" \
"s_add_u32 s62, s62, s63\n" \
"v_bfe_u32 v24, v4, 4, 4\n" \
"v_lshl_add_u32 v24, v24, 2, s62\n" \
"s_lshr_b32 s62, %2, 1\n" \
"s_add_u32 m0, s50, 90112\n" \
"s_lshl_b32 s62, s62, 2\n" \
"buffer_load_dwordx4 v19, s[28:31], s46 offen lds\n" \
"s_and_b32 s63, %1, 0xf8\n" \
"s_add_u32 s63, s63, s62\n" \
"v_lshrrev_b32 v25, 8, v4\n" \
"v_lshrrev_b32 v23, 7, v4\n" \
"s_add_u32 m0, s22, 2816\n" \
"v_add_u32 v25, s63, v25\n" \
"buffer_load_dwordx4 v21, s[32:35], s47 offen offset:256 nt lds\n" \
"v_lshlrev_b32 v26, 14, v25\n" \
"v_lshl_add_u32 v59, v24, 1, v26\n" \
"v_add_u32 v54, 0xffffe800, v59\n" \
"v_add_u32 v55, 0xfffff800, v59\n" \
"s_add_u32 m0, s50, 94208\n" \
"v_lshlrev_b32 v60, 12, v24\n" \
"buffer_load_dwordx4 v20, s[28:31], s46 offen lds\n" \
"v_lshl_add_u32 v60, v25, 4, v60\n" \
"v_lshlrev_b32 v27, 3, v24\n" \
"s_lshl_b32 s62, %1, 2\n" \
"s_add_u32 s62, s62, %2\n" \
"s_mov_b32 s47, 0\n" \
"s_add_u32 m0, s22, 16256\n" \
"s_lshl_b32 s53, s62, 12\n" \
"buffer_load_dwordx4 v21, s[32:35], s47 offen offset:128 nt lds\n" \
"s_and_b32 s63, s62, 7\n" \
"s_lshl_b32 s63, s63, 4\n" \
"s_add_u32 s63, s63, 71303168\n" \
"v_and_b32 v61, 4, v23\n" \
"s_add_u32 m0, s22, 18048\n" \
"v_lshl_add_u32 v61, v61, 1, s63\n" \
"buffer_load_dwordx4 v21, s[32:35], s47 offen offset:384 nt lds\n" \
"s_bfe_u32 s63, s62, 0x10003\n" \
"s_lshl_b32 s63, s63, 2\n" \
"s_lshr_b32 s62, s62, 4\n" \
"s_lshl_b32 s62, s62, 7\n" \
"s_mov_b32 s47, 262144\n" \
"s_add_u32 m0, s22, 17280\n" \
"s_add_u32 s62, s62, s63\n" \
"buffer_load_dwordx4 v21, s[32:35], s47 offen offset:128 nt lds\n" \
"v_add_u32 v61, s62, v61\n" \
"v_lshlrev_b32 v2, 3, v4\n" \
"v_and_b32 v1, 0x380, v2\n" \
"v_lshrrev_b32 v23, 7, v4\n" \
"s_add_u32 m0, s22, 19072\n" \
"v_lshl_add_u32 v1, v23, 13, v1\n" \
"buffer_load_dwordx4 v21, s[32:35], s47 offen offset:384 nt lds\n" \
"s_lshr_b32 s58, %1, 3\n" \
"s_lshl_b32 s58, s58, 2\n" \
"s_add_u32 s58, s58, %2\n" \
"s_and_b32 s60, %1, 7\n" \
"s_mov_b32 s47, 524288\n" \
"s_add_u32 m0, s22, 32768\n" \
"s_lshl_b32 s60, s60, 10\n" \
"buffer_load_dwordx4 v21, s[32:35], s47 offen offset:0 nt lds\n" \
"s_lshl_b32 s59, s58, 14\n" \
"s_add_u32 s59, s59, s60\n" \
"s_mov_b64 s[4:5], s[90:91]\n" \
"s_lshl_b32 s95, s58, 11\n" \
"s_add_u32 m0, s22, 34560\n" \
"s_lshl_b32 s96, s60, 8\n" \
"buffer_load_dwordx4 v21, s[32:35], s47 offen offset:256 nt lds\n" \
"s_add_u32 s95, s95, s96\n" \
"s_add_u32 s96, s76, s95\ns_addc_u32 s97, s77, 0\n" \
"s_add_u32 s76, s78, s59\ns_addc_u32 s77, s79, 0\n" \
"s_and_b32 s59, s59, 1048575\n" \
"s_mov_b32 s47, 786432\n" \
"s_add_u32 m0, s22, 33792\n" \
"s_cmp_lt_u32 s58, 64\ns_cselect_b64 s[84:85], s[80:81], s[82:83]\n" \
"buffer_load_dwordx4 v21, s[32:35], s47 offen offset:0 nt lds\n" \
"s_add_u32 s84, s84, s59\ns_addc_u32 s85, s85, 0\n" \
"s_lshl_b32 s59, s8, 2\n" \
"s_add_u32 s59, s59, %2\n" \
"s_lshl_b32 s59, s59, 12\n" \
"s_add_u32 m0, s22, 35584\n" \
"s_add_u32 s88, s72, s59\ns_addc_u32 s89, s73, 0\n" \
"buffer_load_dwordx4 v21, s[32:35], s47 offen offset:256 nt lds\n" \
"s_mov_b64 s[4:5], s[90:91]\n" \
"s_mov_b64 s[10:11], s[72:73]\n" \
"s_mov_b64 s[12:13], s[74:75]\n" \
"s_add_u32 s44, s78, s94\ns_addc_u32 s45, s79, 0\n" \
"s_mov_b32 s46, 128\n" \
"s_add_u32 m0, s50, 0\n" \
"s_mov_b64 s[40:41], s[66:67]\n" \
"buffer_load_dwordx4 v13, s[28:31], s46 offen lds\n" \
"s_mov_b64 s[80:81], s[68:69]\n" \
"s_and_b32 s81, s81, 0xffff\n" \
"s_mov_b32 s82, 4194304\n" \
"s_mov_b32 s83, 0x00020000\n" \
"s_add_u32 m0, s50, 4096\n" \
"s_and_b32 s45, s45, 0xffff\n" \
"buffer_load_dwordx4 v14, s[28:31], s46 offen lds\n" \
"s_and_b32 s41, s41, 0xffff\n" \
"s_mov_b32 s42, 4194304\n" \
"s_mov_b32 s43, 0x00020000\n" \
"s_mov_b64 exec, 0xffff\nglobal_load_dword v22, v1, s[84:85]\ns_mov_b64 exec, -1\n" \
"s_mov_b64 exec, 0xffff\nglobal_load_dword v22, v2, s[96:97]\ns_mov_b64 exec, -1\n" \
"s_cmp_lt_u32 %1, 128\ns_cbranch_scc0 LHV_%=\ns_mov_b64 exec, 0\ns_cmp_eq_u32 %2, 0\ns_cbranch_scc1 LH0_%=\ns_cmp_eq_u32 %2, 1\ns_cbranch_scc1 LH1_%=\ns_cmp_eq_u32 %2, 2\ns_cbranch_scc1 LH2_%=\ns_cmp_eq_u32 %2, 3\ns_cbranch_scc1 LH3_%=\nLHV_%=:\ns_mov_b64 exec, 0\ns_cmp_eq_u32 %2, 0\ns_cbranch_scc1 LH4_%=\ns_cmp_eq_u32 %2, 1\ns_cbranch_scc1 LH5_%=\ns_cmp_eq_u32 %2, 2\ns_cbranch_scc1 LH6_%=\ns_cmp_eq_u32 %2, 3\ns_cbranch_scc1 LH7_%=\nLHR0_%=:\ns_mov_b64 exec, -1\n" \
"s_waitcnt vmcnt(8)\n" \
"s_barrier\n" \
"s_add_u32 m0, s50, 8192\n" \
"ds_read_b64_tr_b16 a[64:65], v9 offset:0\n" \
"ds_read_b64_tr_b16 a[66:67], v10 offset:0\n" \
"ds_read_b64_tr_b16 a[68:69], v9 offset:1024\n" \
"ds_read_b64_tr_b16 a[70:71], v10 offset:1024\n" \
"ds_read_b128 a[0:3], v7 offset:0\n" \
"ds_read_b128 a[4:7], v8 offset:0\n" \
"buffer_load_dwordx4 v15, s[28:31], s46 offen lds\n" \
"s_add_u32 m0, s50, 12288\n" \
"ds_read_b128 a[8:11], v7 offset:2048\n" \
"ds_read_b128 a[12:15], v8 offset:2048\n" \
"ds_read_b128 a[16:19], v7 offset:4096\n" \
"ds_read_b128 a[20:23], v8 offset:4096\n" \
"ds_read_b128 a[24:27], v7 offset:6144\n" \
"ds_read_b128 a[28:31], v8 offset:6144\n" \
"buffer_load_dwordx4 v16, s[28:31], s46 offen lds\n" \
"s_mov_b32 s46, 128\n" \
"s_add_u32 m0, s50, 16384\n" \
"ds_read_b128 a[32:35], v7 offset:8192\n" \
"ds_read_b128 a[36:39], v8 offset:8192\n" \
"ds_read_b128 a[40:43], v7 offset:10240\n" \
"ds_read_b128 a[44:47], v8 offset:10240\n" \
"ds_read_b128 a[48:51], v7 offset:12288\n" \
"ds_read_b128 a[52:55], v8 offset:12288\n" \
"buffer_load_dwordx4 v17, s[28:31], s46 offen lds\n" \
"s_add_u32 m0, s50, 20480\n" \
"ds_read_b128 a[56:59], v7 offset:14336\n" \
"ds_read_b128 a[60:63], v8 offset:14336\n" \
"ds_read_b64_tr_b16 a[72:73], v11 offset:0\n" \
"ds_read_b64_tr_b16 a[74:75], v12 offset:0\n" \
"ds_read_b64_tr_b16 a[76:77], v11 offset:1024\n" \
"ds_read_b64_tr_b16 a[78:79], v12 offset:1024\n" \
"buffer_load_dwordx4 v18, s[28:31], s46 offen lds\n" \
"s_add_u32 m0, s50, 24576\n" \
"ds_read_b64_tr_b16 a[80:81], v9 offset:512\n" \
"ds_read_b64_tr_b16 a[82:83], v10 offset:512\n" \
"ds_read_b64_tr_b16 a[84:85], v9 offset:1536\n" \
"ds_read_b64_tr_b16 a[86:87], v10 offset:1536\n" \
"ds_read_b64_tr_b16 a[88:89], v11 offset:512\n" \
"ds_read_b64_tr_b16 a[90:91], v12 offset:512\n" \
"buffer_load_dwordx4 v19, s[28:31], s46 offen lds\n" \
"s_add_u32 m0, s50, 28672\n" \
"ds_read_b64_tr_b16 a[92:93], v11 offset:1536\n" \
"ds_read_b64_tr_b16 a[94:95], v12 offset:1536\n" \
"buffer_load_dwordx4 v20, s[28:31], s46 offen lds\n" \
"s_waitcnt lgkmcnt(0)\n" \
"s_branch LBEG_%=\n" \
".p2align 12\n" \
"LBEG_%=:\n" \
"s_cmp_lt_u32 %1, 128\n" \
"s_cbranch_scc0 LAV_%=\n" \
"v_mfma_f32_16x16x32_bf16 v[64:67], a[64:67], a[0:3], 0\n" \
"s_mov_b32 s47, 524288\n" \
"s_add_u32 m0, s22, 49024\n" \
"v_mfma_f32_16x16x32_bf16 v[64:67], a[68:71], a[4:7], v[64:67]\n" \
"buffer_load_dwordx4 v21, s[32:35], s47 offen offset:128 nt lds\n" \
"v_mfma_f32_16x16x32_bf16 v[72:75], a[64:67], a[8:11], 0\n" \
"s_add_u32 s14, s14, 1\ns_addc_u32 s15, s15, 0\n" \
"v_mfma_f32_16x16x32_bf16 v[72:75], a[68:71], a[12:15], v[72:75]\n" \
"v_mfma_f32_16x16x32_bf16 v[80:83], a[64:67], a[16:19], 0\n" \
"s_add_u32 m0, s22, 50816\n" \
"ds_read_b64_tr_b16 a[96:97], v9 offset:16384\n" \
"v_mfma_f32_16x16x32_bf16 v[80:83], a[68:71], a[20:23], v[80:83]\n" \
"buffer_load_dwordx4 v21, s[32:35], s47 offen offset:384 nt lds\n" \
"v_mfma_f32_16x16x32_bf16 v[88:91], a[64:67], a[24:27], 0\n" \
"ds_read_b64_tr_b16 a[98:99], v10 offset:16384\n" \
"v_mfma_f32_16x16x32_bf16 v[88:91], a[68:71], a[28:31], v[88:91]\n" \
"v_mfma_f32_16x16x32_bf16 v[96:99], a[64:67], a[32:35], 0\n" \
"s_mov_b32 s47, 786432\n" \
"s_add_u32 m0, s22, 50048\n" \
"ds_read_b64_tr_b16 a[100:101], v9 offset:17408\n" \
"v_mfma_f32_16x16x32_bf16 v[96:99], a[68:71], a[36:39], v[96:99]\n" \
"buffer_load_dwordx4 v21, s[32:35], s47 offen offset:128 nt lds\n" \
"v_mfma_f32_16x16x32_bf16 v[104:107], a[64:67], a[40:43], 0\n" \
"ds_read_b64_tr_b16 a[102:103], v10 offset:17408\n" \
"v_mfma_f32_16x16x32_bf16 v[104:107], a[68:71], a[44:47], v[104:107]\n" \
"v_mfma_f32_16x16x32_bf16 v[112:115], a[64:67], a[48:51], 0\n" \
"s_add_u32 m0, s22, 51840\n" \
"ds_read_b64_tr_b16 a[104:105], v11 offset:16384\n" \
"v_mfma_f32_16x16x32_bf16 v[112:115], a[68:71], a[52:55], v[112:115]\n" \
"buffer_load_dwordx4 v21, s[32:35], s47 offen offset:384 nt lds\n" \
"v_mfma_f32_16x16x32_bf16 v[120:123], a[64:67], a[56:59], 0\n" \
"ds_read_b64_tr_b16 a[106:107], v12 offset:16384\n" \
"v_mfma_f32_16x16x32_bf16 v[120:123], a[68:71], a[60:63], v[120:123]\n" \
"v_mfma_f32_16x16x32_bf16 v[68:71], a[72:75], a[0:3], 0\n" \
"s_mov_b32 s46, 256\n" \
"s_add_u32 m0, s50, 32768\n" \
"ds_read_b64_tr_b16 a[108:109], v11 offset:17408\n" \
"v_mfma_f32_16x16x32_bf16 v[68:71], a[76:79], a[4:7], v[68:71]\n" \
"buffer_load_dwordx4 v13, s[28:31], s46 offen lds\n" \
"v_mfma_f32_16x16x32_bf16 v[76:79], a[72:75], a[8:11], 0\n" \
"ds_read_b64_tr_b16 a[110:111], v12 offset:17408\n" \
"v_mfma_f32_16x16x32_bf16 v[76:79], a[76:79], a[12:15], v[76:79]\n" \
"v_mfma_f32_16x16x32_bf16 v[84:87], a[72:75], a[16:19], 0\n" \
"s_add_u32 m0, s50, 36864\n" \
"ds_read_b64_tr_b16 a[112:113], v9 offset:16896\n" \
"v_mfma_f32_16x16x32_bf16 v[84:87], a[76:79], a[20:23], v[84:87]\n" \
"buffer_load_dwordx4 v14, s[28:31], s46 offen lds\n" \
"v_mfma_f32_16x16x32_bf16 v[92:95], a[72:75], a[24:27], 0\n" \
"ds_read_b64_tr_b16 a[114:115], v10 offset:16896\n" \
"v_mfma_f32_16x16x32_bf16 v[92:95], a[76:79], a[28:31], v[92:95]\n" \
"v_mfma_f32_16x16x32_bf16 v[100:103], a[72:75], a[32:35], 0\n" \
"s_add_u32 m0, s50, 40960\n" \
"ds_read_b64_tr_b16 a[116:117], v9 offset:17920\n" \
"v_mfma_f32_16x16x32_bf16 v[100:103], a[76:79], a[36:39], v[100:103]\n" \
"buffer_load_dwordx4 v15, s[28:31], s46 offen lds\n" \
"v_mfma_f32_16x16x32_bf16 v[108:111], a[72:75], a[40:43], 0\n" \
"ds_read_b64_tr_b16 a[118:119], v10 offset:17920\n" \
"v_mfma_f32_16x16x32_bf16 v[108:111], a[76:79], a[44:47], v[108:111]\n" \
"v_mfma_f32_16x16x32_bf16 v[116:119], a[72:75], a[48:51], 0\n" \
"s_add_u32 m0, s50, 45056\n" \
"ds_read_b64_tr_b16 a[120:121], v11 offset:16896\n" \
"v_mfma_f32_16x16x32_bf16 v[116:119], a[76:79], a[52:55], v[116:119]\n" \
"buffer_load_dwordx4 v16, s[28:31], s46 offen lds\n" \
"v_mfma_f32_16x16x32_bf16 v[124:127], a[72:75], a[56:59], 0\n" \
"ds_read_b64_tr_b16 a[122:123], v12 offset:16896\n" \
"v_mfma_f32_16x16x32_bf16 v[124:127], a[76:79], a[60:63], v[124:127]\n" \
"v_mfma_f32_16x16x32_bf16 v[128:131], a[80:83], a[0:3], 0\n" \
"s_mov_b32 s46, 256\n" \
"s_add_u32 m0, s50, 49152\n" \
"ds_read_b64_tr_b16 a[124:125], v11 offset:17920\n" \
"v_mfma_f32_16x16x32_bf16 v[128:131], a[84:87], a[4:7], v[128:131]\n" \
"buffer_load_dwordx4 v17, s[28:31], s46 offen lds\n" \
"v_mfma_f32_16x16x32_bf16 v[136:139], a[80:83], a[8:11], 0\n" \
"ds_read_b64_tr_b16 a[126:127], v12 offset:17920\n" \
"v_mfma_f32_16x16x32_bf16 v[136:139], a[84:87], a[12:15], v[136:139]\n" \
"v_mfma_f32_16x16x32_bf16 v[144:147], a[80:83], a[16:19], 0\n" \
"s_add_u32 m0, s50, 53248\n" \
"v_mfma_f32_16x16x32_bf16 v[144:147], a[84:87], a[20:23], v[144:147]\n" \
"buffer_load_dwordx4 v18, s[28:31], s46 offen lds\n" \
"v_mfma_f32_16x16x32_bf16 v[152:155], a[80:83], a[24:27], 0\n" \
"v_mfma_f32_16x16x32_bf16 v[152:155], a[84:87], a[28:31], v[152:155]\n" \
"v_mfma_f32_16x16x32_bf16 v[160:163], a[80:83], a[32:35], 0\n" \
"s_add_u32 m0, s50, 57344\n" \
"v_mfma_f32_16x16x32_bf16 v[160:163], a[84:87], a[36:39], v[160:163]\n" \
"buffer_load_dwordx4 v19, s[28:31], s46 offen lds\n" \
"v_mfma_f32_16x16x32_bf16 v[168:171], a[80:83], a[40:43], 0\n" \
"v_mfma_f32_16x16x32_bf16 v[168:171], a[84:87], a[44:47], v[168:171]\n" \
"s_waitcnt vmcnt(21) lgkmcnt(0)\n" \
"v_mfma_f32_16x16x32_bf16 v[176:179], a[80:83], a[48:51], 0\n" \
"s_add_u32 m0, s50, 61440\n" \
"s_barrier\n" \
"v_mfma_f32_16x16x32_bf16 v[176:179], a[84:87], a[52:55], v[176:179]\n" \
"buffer_load_dwordx4 v20, s[28:31], s46 offen lds\n" \
"v_mfma_f32_16x16x32_bf16 v[184:187], a[80:83], a[56:59], 0\n" \
"v_mfma_f32_16x16x32_bf16 v[184:187], a[84:87], a[60:63], v[184:187]\n" \
"v_mfma_f32_16x16x32_bf16 v[132:135], a[88:91], a[0:3], 0\n" \
"s_mov_b32 s47, 1048576\n" \
"s_add_u32 m0, s22, 0\n" \
"v_mfma_f32_16x16x32_bf16 v[132:135], a[92:95], a[4:7], v[132:135]\n" \
"buffer_load_dwordx4 v21, s[32:35], s47 offen offset:0 lds\n" \
"v_mfma_f32_16x16x32_bf16 v[140:143], a[88:91], a[8:11], 0\n" \
"v_mfma_f32_16x16x32_bf16 v[140:143], a[92:95], a[12:15], v[140:143]\n" \
"v_mfma_f32_16x16x32_bf16 v[148:151], a[88:91], a[16:19], 0\n" \
"s_add_u32 m0, s22, 1792\n" \
"v_mfma_f32_16x16x32_bf16 v[148:151], a[92:95], a[20:23], v[148:151]\n" \
"buffer_load_dwordx4 v21, s[32:35], s47 offen offset:256 lds\n" \
"v_mfma_f32_16x16x32_bf16 v[156:159], a[88:91], a[24:27], 0\n" \
"v_mfma_f32_16x16x32_bf16 v[156:159], a[92:95], a[28:31], v[156:159]\n" \
"v_mfma_f32_16x16x32_bf16 v[164:167], a[88:91], a[32:35], 0\n" \
"s_mov_b32 s47, 1310720\n" \
"s_add_u32 m0, s22, 1024\n" \
"v_mfma_f32_16x16x32_bf16 v[164:167], a[92:95], a[36:39], v[164:167]\n" \
"buffer_load_dwordx4 v21, s[32:35], s47 offen offset:0 lds\n" \
"v_mfma_f32_16x16x32_bf16 v[172:175], a[88:91], a[40:43], 0\n" \
"v_mfma_f32_16x16x32_bf16 v[172:175], a[92:95], a[44:47], v[172:175]\n" \
"v_mfma_f32_16x16x32_bf16 v[180:183], a[88:91], a[48:51], 0\n" \
"s_add_u32 m0, s22, 2816\n" \
"v_mfma_f32_16x16x32_bf16 v[180:183], a[92:95], a[52:55], v[180:183]\n" \
"buffer_load_dwordx4 v21, s[32:35], s47 offen offset:256 lds\n" \
"v_mfma_f32_16x16x32_bf16 v[188:191], a[88:91], a[56:59], 0\n" \
"v_mfma_f32_16x16x32_bf16 v[188:191], a[92:95], a[60:63], v[188:191]\n" \
"v_mfma_f32_16x16x32_bf16 v[192:195], a[96:99], a[0:3], 0\n" \
"s_mov_b32 s47, 1048576\n" \
"s_add_u32 m0, s22, 16256\n" \
"v_mfma_f32_16x16x32_bf16 v[192:195], a[100:103], a[4:7], v[192:195]\n" \
"buffer_load_dwordx4 v21, s[32:35], s47 offen offset:128 lds\n" \
"v_mfma_f32_16x16x32_bf16 v[200:203], a[96:99], a[8:11], 0\n" \
"v_mfma_f32_16x16x32_bf16 v[200:203], a[100:103], a[12:15], v[200:203]\n" \
"v_mfma_f32_16x16x32_bf16 v[208:211], a[96:99], a[16:19], 0\n" \
"s_mov_b32 s46, 384\n" \
"s_add_u32 m0, s50, 65536\n" \
"ds_read_b64_tr_b16 a[64:65], v9 offset:32768\n" \
"v_mfma_f32_16x16x32_bf16 v[208:211], a[100:103], a[20:23], v[208:211]\n" \
"buffer_load_dwordx4 v13, s[28:31], s46 offen lds\n" \
"v_mfma_f32_16x16x32_bf16 v[216:219], a[96:99], a[24:27], 0\n" \
"ds_read_b64_tr_b16 a[66:67], v10 offset:32768\n" \
"v_mfma_f32_16x16x32_bf16 v[216:219], a[100:103], a[28:31], v[216:219]\n" \
"v_mfma_f32_16x16x32_bf16 v[224:227], a[96:99], a[32:35], 0\n" \
"s_add_u32 m0, s22, 18048\n" \
"ds_read_b64_tr_b16 a[68:69], v9 offset:33792\n" \
"v_mfma_f32_16x16x32_bf16 v[224:227], a[100:103], a[36:39], v[224:227]\n" \
"buffer_load_dwordx4 v21, s[32:35], s47 offen offset:384 lds\n" \
"v_mfma_f32_16x16x32_bf16 v[232:235], a[96:99], a[40:43], 0\n" \
"ds_read_b64_tr_b16 a[70:71], v10 offset:33792\n" \
"v_mfma_f32_16x16x32_bf16 v[232:235], a[100:103], a[44:47], v[232:235]\n" \
"v_mfma_f32_16x16x32_bf16 v[240:243], a[96:99], a[48:51], 0\n" \
"s_add_u32 m0, s50, 69632\n" \
"ds_read_b64_tr_b16 a[72:73], v11 offset:32768\n" \
"v_mfma_f32_16x16x32_bf16 v[240:243], a[100:103], a[52:55], v[240:243]\n" \
"buffer_load_dwordx4 v14, s[28:31], s46 offen lds\n" \
"v_mfma_f32_16x16x32_bf16 v[248:251], a[96:99], a[56:59], 0\n" \
"ds_read_b64_tr_b16 a[74:75], v12 offset:32768\n" \
"v_mfma_f32_16x16x32_bf16 v[248:251], a[100:103], a[60:63], v[248:251]\n" \
"v_mfma_f32_16x16x32_bf16 v[196:199], a[104:107], a[0:3], 0\n" \
"s_mov_b32 s47, 1310720\n" \
"s_add_u32 m0, s22, 17280\n" \
"ds_read_b64_tr_b16 a[76:77], v11 offset:33792\n" \
"v_mfma_f32_16x16x32_bf16 v[196:199], a[108:111], a[4:7], v[196:199]\n" \
"buffer_load_dwordx4 v21, s[32:35], s47 offen offset:128 lds\n" \
"v_mfma_f32_16x16x32_bf16 v[204:207], a[104:107], a[8:11], 0\n" \
"ds_read_b64_tr_b16 a[78:79], v12 offset:33792\n" \
"v_mfma_f32_16x16x32_bf16 v[204:207], a[108:111], a[12:15], v[204:207]\n" \
"v_mfma_f32_16x16x32_bf16 v[212:215], a[104:107], a[16:19], 0\n" \
"s_add_u32 m0, s50, 73728\n" \
"ds_read_b64_tr_b16 a[80:81], v9 offset:33280\n" \
"v_mfma_f32_16x16x32_bf16 v[212:215], a[108:111], a[20:23], v[212:215]\n" \
"buffer_load_dwordx4 v15, s[28:31], s46 offen lds\n" \
"v_mfma_f32_16x16x32_bf16 v[220:223], a[104:107], a[24:27], 0\n" \
"ds_read_b64_tr_b16 a[82:83], v10 offset:33280\n" \
"v_mfma_f32_16x16x32_bf16 v[220:223], a[108:111], a[28:31], v[220:223]\n" \
"v_mfma_f32_16x16x32_bf16 v[228:231], a[104:107], a[32:35], 0\n" \
"s_add_u32 m0, s22, 19072\n" \
"ds_read_b64_tr_b16 a[84:85], v9 offset:34304\n" \
"v_mfma_f32_16x16x32_bf16 v[228:231], a[108:111], a[36:39], v[228:231]\n" \
"buffer_load_dwordx4 v21, s[32:35], s47 offen offset:384 lds\n" \
"v_mfma_f32_16x16x32_bf16 v[236:239], a[104:107], a[40:43], 0\n" \
"ds_read_b64_tr_b16 a[86:87], v10 offset:34304\n" \
"v_mfma_f32_16x16x32_bf16 v[236:239], a[108:111], a[44:47], v[236:239]\n" \
"v_mfma_f32_16x16x32_bf16 v[244:247], a[104:107], a[48:51], 0\n" \
"s_add_u32 m0, s50, 77824\n" \
"ds_read_b64_tr_b16 a[88:89], v11 offset:33280\n" \
"v_mfma_f32_16x16x32_bf16 v[244:247], a[108:111], a[52:55], v[244:247]\n" \
"buffer_load_dwordx4 v16, s[28:31], s46 offen lds\n" \
"v_mfma_f32_16x16x32_bf16 v[252:255], a[104:107], a[56:59], 0\n" \
"ds_read_b64_tr_b16 a[90:91], v12 offset:33280\n" \
"v_mfma_f32_16x16x32_bf16 v[252:255], a[108:111], a[60:63], v[252:255]\n" \
"v_mfma_f32_16x16x32_bf16 a[128:131], a[112:115], a[0:3], 0\n" \
"ds_read_b64_tr_b16 a[92:93], v11 offset:34304\n" \
"v_mfma_f32_16x16x32_bf16 a[128:131], a[116:119], a[4:7], a[128:131]\n" \
"global_load_dwordx4 a[230:233], v27, s[70:71] offset:0\n" \
"v_mfma_f32_16x16x32_bf16 a[136:139], a[112:115], a[8:11], 0\n" \
"ds_read_b64_tr_b16 a[94:95], v12 offset:34304\n" \
"v_mfma_f32_16x16x32_bf16 a[136:139], a[116:119], a[12:15], a[136:139]\n" \
"v_mfma_f32_16x16x32_bf16 a[144:147], a[112:115], a[16:19], 0\n" \
"v_mfma_f32_16x16x32_bf16 a[144:147], a[116:119], a[20:23], a[144:147]\n" \
"global_load_dwordx4 a[234:237], v27, s[70:71] offset:16\n" \
"v_mfma_f32_16x16x32_bf16 a[152:155], a[112:115], a[24:27], 0\n" \
"v_mfma_f32_16x16x32_bf16 a[152:155], a[116:119], a[28:31], a[152:155]\n" \
"v_mfma_f32_16x16x32_bf16 a[160:163], a[112:115], a[32:35], 0\n" \
"v_mfma_f32_16x16x32_bf16 a[160:163], a[116:119], a[36:39], a[160:163]\n" \
"buffer_load_dwordx2 a[192:193], v54, s[40:43], 0 offen offset:0 nt\n" \
"v_mfma_f32_16x16x32_bf16 a[168:171], a[112:115], a[40:43], 0\n" \
"v_mfma_f32_16x16x32_bf16 a[168:171], a[116:119], a[44:47], a[168:171]\n" \
"s_waitcnt vmcnt(23) lgkmcnt(0)\n" \
"v_mfma_f32_16x16x32_bf16 a[176:179], a[112:115], a[48:51], 0\n" \
"s_barrier\n" \
"v_mfma_f32_16x16x32_bf16 a[176:179], a[116:119], a[52:55], a[176:179]\n" \
"buffer_load_dwordx2 a[194:195], v54, s[40:43], 0 offen offset:2048 nt\n" \
"v_mfma_f32_16x16x32_bf16 a[184:187], a[112:115], a[56:59], 0\n" \
"v_mfma_f32_16x16x32_bf16 a[184:187], a[116:119], a[60:63], a[184:187]\n" \
"v_mfma_f32_16x16x32_bf16 a[132:135], a[120:123], a[0:3], 0\n" \
"v_mfma_f32_16x16x32_bf16 a[132:135], a[124:127], a[4:7], a[132:135]\n" \
"buffer_load_dwordx2 a[196:197], v55, s[40:43], 0 offen offset:0 nt\n" \
"v_mfma_f32_16x16x32_bf16 a[140:143], a[120:123], a[8:11], 0\n" \
"s_add_u32 s84, s84, 1048576\ns_addc_u32 s85, s85, 0\n" \
"ds_read_b128 a[0:3], v5 offset:0\n" \
"v_mfma_f32_16x16x32_bf16 a[140:143], a[124:127], a[12:15], a[140:143]\n" \
"s_mov_b32 s52, 0\n" \
"ds_read_b128 a[4:7], v6 offset:0\n" \
"v_mfma_f32_16x16x32_bf16 a[148:151], a[120:123], a[16:19], 0\n" \
"ds_read_b128 a[8:11], v5 offset:2048\n" \
"v_mfma_f32_16x16x32_bf16 a[148:151], a[124:127], a[20:23], a[148:151]\n" \
"ds_read_b128 a[12:15], v6 offset:2048\n" \
"v_mfma_f32_16x16x32_bf16 a[156:159], a[120:123], a[24:27], 0\n" \
"ds_read_b128 a[16:19], v5 offset:4096\n" \
"v_mfma_f32_16x16x32_bf16 a[156:159], a[124:127], a[28:31], a[156:159]\n" \
"ds_read_b128 a[20:23], v6 offset:4096\n" \
"v_mfma_f32_16x16x32_bf16 a[164:167], a[120:123], a[32:35], 0\n" \
"ds_read_b128 a[24:27], v5 offset:6144\n" \
"v_mfma_f32_16x16x32_bf16 a[164:167], a[124:127], a[36:39], a[164:167]\n" \
"ds_read_b128 a[28:31], v6 offset:6144\n" \
"v_mfma_f32_16x16x32_bf16 a[172:175], a[120:123], a[40:43], 0\n" \
"ds_read_b128 a[32:35], v5 offset:8192\n" \
"v_mfma_f32_16x16x32_bf16 a[172:175], a[124:127], a[44:47], a[172:175]\n" \
"ds_read_b128 a[36:39], v6 offset:8192\n" \
"v_mfma_f32_16x16x32_bf16 a[180:183], a[120:123], a[48:51], 0\n" \
"ds_read_b128 a[40:43], v5 offset:10240\n" \
"v_mfma_f32_16x16x32_bf16 a[180:183], a[124:127], a[52:55], a[180:183]\n" \
"ds_read_b128 a[44:47], v6 offset:10240\n" \
"v_mfma_f32_16x16x32_bf16 a[188:191], a[120:123], a[56:59], 0\n" \
"ds_read_b128 a[48:51], v5 offset:12288\n" \
"v_mfma_f32_16x16x32_bf16 a[188:191], a[124:127], a[60:63], a[188:191]\n" \
"s_waitcnt lgkmcnt(9)\n" \
"ds_read_b128 a[52:55], v6 offset:12288\n" \
"v_mfma_f32_16x16x32_bf16 v[64:67], a[64:67], a[0:3], v[64:67]\n" \
"s_mov_b32 s47, 1572864\n" \
"s_add_u32 m0, s22, 32768\n" \
"ds_read_b128 a[56:59], v5 offset:14336\n" \
"v_mfma_f32_16x16x32_bf16 v[64:67], a[68:71], a[4:7], v[64:67]\n" \
"buffer_load_dwordx4 v21, s[32:35], s47 offen offset:0 lds\n" \
"v_mfma_f32_16x16x32_bf16 v[72:75], a[64:67], a[8:11], v[72:75]\n" \
"s_waitcnt lgkmcnt(7)\n" \
"ds_read_b128 a[60:63], v6 offset:14336\n" \
"v_mfma_f32_16x16x32_bf16 v[72:75], a[68:71], a[12:15], v[72:75]\n" \
"v_mfma_f32_16x16x32_bf16 v[80:83], a[64:67], a[16:19], v[80:83]\n" \
"ds_read_b64_tr_b16 a[96:97], v9 offset:49152\n" \
"v_mfma_f32_16x16x32_bf16 v[80:83], a[68:71], a[20:23], v[80:83]\n" \
"s_mov_b64 exec, 0xffff\nglobal_load_dword v22, v1, s[84:85]\ns_mov_b64 exec, -1\n" \
"v_mfma_f32_16x16x32_bf16 v[88:91], a[64:67], a[24:27], v[88:91]\n" \
"s_waitcnt lgkmcnt(5)\n" \
"ds_read_b64_tr_b16 a[98:99], v10 offset:49152\n" \
"v_mfma_f32_16x16x32_bf16 v[88:91], a[68:71], a[28:31], v[88:91]\n" \
"v_mfma_f32_16x16x32_bf16 v[96:99], a[64:67], a[32:35], v[96:99]\n" \
"s_mov_b32 s46, 384\n" \
"s_add_u32 m0, s50, 81920\n" \
"ds_read_b64_tr_b16 a[100:101], v9 offset:50176\n" \
"v_mfma_f32_16x16x32_bf16 v[96:99], a[68:71], a[36:39], v[96:99]\n" \
"buffer_load_dwordx4 v17, s[28:31], s46 offen lds\n" \
"v_mfma_f32_16x16x32_bf16 v[104:107], a[64:67], a[40:43], v[104:107]\n" \
"s_waitcnt lgkmcnt(3)\n" \
"ds_read_b64_tr_b16 a[102:103], v10 offset:50176\n" \
"v_mfma_f32_16x16x32_bf16 v[104:107], a[68:71], a[44:47], v[104:107]\n" \
"v_mfma_f32_16x16x32_bf16 v[112:115], a[64:67], a[48:51], v[112:115]\n" \
"ds_read_b64_tr_b16 a[104:105], v11 offset:49152\n" \
"v_mfma_f32_16x16x32_bf16 v[112:115], a[68:71], a[52:55], v[112:115]\n" \
"buffer_load_dwordx2 a[198:199], v59, s[40:43], s52 offen offset:0 nt\n" \
"v_mfma_f32_16x16x32_bf16 v[120:123], a[64:67], a[56:59], v[120:123]\n" \
"ds_read_b64_tr_b16 a[106:107], v12 offset:49152\n" \
"v_mfma_f32_16x16x32_bf16 v[120:123], a[68:71], a[60:63], v[120:123]\n" \
"v_mfma_f32_16x16x32_bf16 v[68:71], a[72:75], a[0:3], v[68:71]\n" \
"s_add_u32 m0, s22, 34560\n" \
"ds_read_b64_tr_b16 a[108:109], v11 offset:50176\n" \
"v_mfma_f32_16x16x32_bf16 v[68:71], a[76:79], a[4:7], v[68:71]\n" \
"buffer_load_dwordx4 v21, s[32:35], s47 offen offset:256 lds\n" \
"v_mfma_f32_16x16x32_bf16 v[76:79], a[72:75], a[8:11], v[76:79]\n" \
"ds_read_b64_tr_b16 a[110:111], v12 offset:50176\n" \
"v_mfma_f32_16x16x32_bf16 v[76:79], a[76:79], a[12:15], v[76:79]\n" \
"v_mfma_f32_16x16x32_bf16 v[84:87], a[72:75], a[16:19], v[84:87]\n" \
"ds_read_b64_tr_b16 a[112:113], v9 offset:49664\n" \
"v_mfma_f32_16x16x32_bf16 v[84:87], a[76:79], a[20:23], v[84:87]\n" \
"buffer_load_dwordx2 a[214:215], v59, s[80:83], s52 offen offset:0 nt\n" \
"v_mfma_f32_16x16x32_bf16 v[92:95], a[72:75], a[24:27], v[92:95]\n" \
"ds_read_b64_tr_b16 a[114:115], v10 offset:49664\n" \
"v_mfma_f32_16x16x32_bf16 v[92:95], a[76:79], a[28:31], v[92:95]\n" \
"v_mfma_f32_16x16x32_bf16 v[100:103], a[72:75], a[32:35], v[100:103]\n" \
"s_add_u32 m0, s50, 86016\n" \
"ds_read_b64_tr_b16 a[116:117], v9 offset:50688\n" \
"v_mfma_f32_16x16x32_bf16 v[100:103], a[76:79], a[36:39], v[100:103]\n" \
"buffer_load_dwordx4 v18, s[28:31], s46 offen lds\n" \
"v_mfma_f32_16x16x32_bf16 v[108:111], a[72:75], a[40:43], v[108:111]\n" \
"ds_read_b64_tr_b16 a[118:119], v10 offset:50688\n" \
"v_mfma_f32_16x16x32_bf16 v[108:111], a[76:79], a[44:47], v[108:111]\n" \
"v_mfma_f32_16x16x32_bf16 v[116:119], a[72:75], a[48:51], v[116:119]\n" \
"ds_read_b64_tr_b16 a[120:121], v11 offset:49664\n" \
"v_mfma_f32_16x16x32_bf16 v[116:119], a[76:79], a[52:55], v[116:119]\n" \
"buffer_load_dwordx2 a[200:201], v59, s[40:43], s52 offen offset:2048 nt\n" \
"v_mfma_f32_16x16x32_bf16 v[124:127], a[72:75], a[56:59], v[124:127]\n" \
"ds_read_b64_tr_b16 a[122:123], v12 offset:49664\n" \
"v_mfma_f32_16x16x32_bf16 v[124:127], a[76:79], a[60:63], v[124:127]\n" \
"v_mfma_f32_16x16x32_bf16 v[128:131], a[80:83], a[0:3], v[128:131]\n" \
"s_mov_b32 s47, 1835008\n" \
"s_add_u32 m0, s22, 33792\n" \
"ds_read_b64_tr_b16 a[124:125], v11 offset:50688\n" \
"v_mfma_f32_16x16x32_bf16 v[128:131], a[84:87], a[4:7], v[128:131]\n" \
"buffer_load_dwordx4 v21, s[32:35], s47 offen offset:0 lds\n" \
"v_mfma_f32_16x16x32_bf16 v[136:139], a[80:83], a[8:11], v[136:139]\n" \
"ds_read_b64_tr_b16 a[126:127], v12 offset:50688\n" \
"v_mfma_f32_16x16x32_bf16 v[136:139], a[84:87], a[12:15], v[136:139]\n" \
"v_mfma_f32_16x16x32_bf16 v[144:147], a[80:83], a[16:19], v[144:147]\n" \
"v_mfma_f32_16x16x32_bf16 v[144:147], a[84:87], a[20:23], v[144:147]\n" \
"buffer_load_dwordx2 a[216:217], v59, s[80:83], s52 offen offset:2048 nt\n" \
"v_mfma_f32_16x16x32_bf16 v[152:155], a[80:83], a[24:27], v[152:155]\n" \
"s_mov_b32 s52, 4096\n" \
"v_mfma_f32_16x16x32_bf16 v[152:155], a[84:87], a[28:31], v[152:155]\n" \
"v_mfma_f32_16x16x32_bf16 v[160:163], a[80:83], a[32:35], v[160:163]\n" \
"s_add_u32 m0, s50, 90112\n" \
"v_mfma_f32_16x16x32_bf16 v[160:163], a[84:87], a[36:39], v[160:163]\n" \
"buffer_load_dwordx4 v19, s[28:31], s46 offen lds\n" \
"v_mfma_f32_16x16x32_bf16 v[168:171], a[80:83], a[40:43], v[168:171]\n" \
"v_mfma_f32_16x16x32_bf16 v[168:171], a[84:87], a[44:47], v[168:171]\n" \
"s_waitcnt vmcnt(24) lgkmcnt(0)\n" \
"v_mfma_f32_16x16x32_bf16 v[176:179], a[80:83], a[48:51], v[176:179]\n" \
"s_barrier\n" \
"v_mfma_f32_16x16x32_bf16 v[176:179], a[84:87], a[52:55], v[176:179]\n" \
"buffer_load_dwordx2 a[202:203], v59, s[40:43], s52 offen offset:0 nt\n" \
"v_mfma_f32_16x16x32_bf16 v[184:187], a[80:83], a[56:59], v[184:187]\n" \
"v_mfma_f32_16x16x32_bf16 v[184:187], a[84:87], a[60:63], v[184:187]\n" \
"v_mfma_f32_16x16x32_bf16 v[132:135], a[88:91], a[0:3], v[132:135]\n" \
"s_add_u32 m0, s22, 35584\n" \
"v_mfma_f32_16x16x32_bf16 v[132:135], a[92:95], a[4:7], v[132:135]\n" \
"buffer_load_dwordx4 v21, s[32:35], s47 offen offset:256 lds\n" \
"v_mfma_f32_16x16x32_bf16 v[140:143], a[88:91], a[8:11], v[140:143]\n" \
"v_mfma_f32_16x16x32_bf16 v[140:143], a[92:95], a[12:15], v[140:143]\n" \
"v_mfma_f32_16x16x32_bf16 v[148:151], a[88:91], a[16:19], v[148:151]\n" \
"v_mfma_f32_16x16x32_bf16 v[148:151], a[92:95], a[20:23], v[148:151]\n" \
"buffer_load_dwordx2 a[218:219], v59, s[80:83], s52 offen offset:0 nt\n" \
"v_mfma_f32_16x16x32_bf16 v[156:159], a[88:91], a[24:27], v[156:159]\n" \
"v_mfma_f32_16x16x32_bf16 v[156:159], a[92:95], a[28:31], v[156:159]\n" \
"v_mfma_f32_16x16x32_bf16 v[164:167], a[88:91], a[32:35], v[164:167]\n" \
"s_add_u32 m0, s50, 94208\n" \
"v_mfma_f32_16x16x32_bf16 v[164:167], a[92:95], a[36:39], v[164:167]\n" \
"buffer_load_dwordx4 v20, s[28:31], s46 offen lds\n" \
"v_mfma_f32_16x16x32_bf16 v[172:175], a[88:91], a[40:43], v[172:175]\n" \
"v_mfma_f32_16x16x32_bf16 v[172:175], a[92:95], a[44:47], v[172:175]\n" \
"v_mfma_f32_16x16x32_bf16 v[180:183], a[88:91], a[48:51], v[180:183]\n" \
"v_mfma_f32_16x16x32_bf16 v[180:183], a[92:95], a[52:55], v[180:183]\n" \
"buffer_load_dwordx2 a[204:205], v59, s[40:43], s52 offen offset:2048 nt\n" \
"v_mfma_f32_16x16x32_bf16 v[188:191], a[88:91], a[56:59], v[188:191]\n" \
"v_mfma_f32_16x16x32_bf16 v[188:191], a[92:95], a[60:63], v[188:191]\n" \
"v_mfma_f32_16x16x32_bf16 v[192:195], a[96:99], a[0:3], v[192:195]\n" \
"s_mov_b32 s47, 1572864\n" \
"s_add_u32 m0, s22, 49024\n" \
"v_mfma_f32_16x16x32_bf16 v[192:195], a[100:103], a[4:7], v[192:195]\n" \
"buffer_load_dwordx4 v21, s[32:35], s47 offen offset:128 lds\n" \
"v_mfma_f32_16x16x32_bf16 v[200:203], a[96:99], a[8:11], v[200:203]\n" \
"v_mfma_f32_16x16x32_bf16 v[200:203], a[100:103], a[12:15], v[200:203]\n" \
"v_mfma_f32_16x16x32_bf16 v[208:211], a[96:99], a[16:19], v[208:211]\n" \
"s_mov_b32 s46, 512\n" \
"s_add_u32 m0, s50, 0\n" \
"ds_read_b64_tr_b16 a[64:65], v9 offset:0\n" \
"v_mfma_f32_16x16x32_bf16 v[208:211], a[100:103], a[20:23], v[208:211]\n" \
"buffer_load_dwordx4 v13, s[28:31], s46 offen lds\n" \
"v_mfma_f32_16x16x32_bf16 v[216:219], a[96:99], a[24:27], v[216:219]\n" \
"ds_read_b64_tr_b16 a[66:67], v10 offset:0\n" \
"v_mfma_f32_16x16x32_bf16 v[216:219], a[100:103], a[28:31], v[216:219]\n" \
"s_waitcnt vmcnt(12)\n" \
"v_accvgpr_read_b32 v43, a230\n" \
"v_lshlrev_b32 v42, 16, v43\n" \
"v_mfma_f32_16x16x32_bf16 v[224:227], a[96:99], a[32:35], v[224:227]\n" \
"ds_read_b64_tr_b16 a[68:69], v9 offset:1024\n" \
"v_and_b32 v43, 0xffff0000, v43\n" \
"v_mfma_f32_16x16x32_bf16 v[224:227], a[100:103], a[36:39], v[224:227]\n" \
"buffer_load_dwordx2 a[220:221], v59, s[80:83], s52 offen offset:2048 nt\n" \
"v_mfma_f32_16x16x32_bf16 v[232:235], a[96:99], a[40:43], v[232:235]\n" \
"s_mov_b32 s52, 8192\n" \
"ds_read_b64_tr_b16 a[70:71], v10 offset:1024\n" \
"v_accvgpr_read_b32 v45, a231\n" \
"v_mfma_f32_16x16x32_bf16 v[232:235], a[100:103], a[44:47], v[232:235]\n" \
"v_lshlrev_b32 v44, 16, v45\n" \
"v_and_b32 v45, 0xffff0000, v45\n" \
"v_mfma_f32_16x16x32_bf16 v[240:243], a[96:99], a[48:51], v[240:243]\n" \
"s_add_u32 m0, s22, 50816\n" \
"ds_read_b64_tr_b16 a[72:73], v11 offset:0\n" \
"v_accvgpr_read_b32 v47, a232\n" \
"v_mfma_f32_16x16x32_bf16 v[240:243], a[100:103], a[52:55], v[240:243]\n" \
"buffer_load_dwordx4 v21, s[32:35], s47 offen offset:384 lds\n" \
"v_mfma_f32_16x16x32_bf16 v[248:251], a[96:99], a[56:59], v[248:251]\n" \
"ds_read_b64_tr_b16 a[74:75], v12 offset:0\n" \
"v_lshlrev_b32 v46, 16, v47\n" \
"v_mfma_f32_16x16x32_bf16 v[248:251], a[100:103], a[60:63], v[248:251]\n" \
"v_and_b32 v47, 0xffff0000, v47\n" \
"v_accvgpr_read_b32 v49, a233\n" \
"v_mfma_f32_16x16x32_bf16 v[196:199], a[104:107], a[0:3], v[196:199]\n" \
"s_add_u32 m0, s50, 4096\n" \
"ds_read_b64_tr_b16 a[76:77], v11 offset:1024\n" \
"v_lshlrev_b32 v48, 16, v49\n" \
"v_mfma_f32_16x16x32_bf16 v[196:199], a[108:111], a[4:7], v[196:199]\n" \
"buffer_load_dwordx4 v14, s[28:31], s46 offen lds\n" \
"v_mfma_f32_16x16x32_bf16 v[204:207], a[104:107], a[8:11], v[204:207]\n" \
"ds_read_b64_tr_b16 a[78:79], v12 offset:1024\n" \
"v_and_b32 v49, 0xffff0000, v49\n" \
"v_mfma_f32_16x16x32_bf16 v[204:207], a[108:111], a[12:15], v[204:207]\n" \
"v_accvgpr_read_b32 v36, a192\n" \
"v_lshlrev_b32 v30, 16, v36\n" \
"v_mfma_f32_16x16x32_bf16 v[212:215], a[104:107], a[16:19], v[212:215]\n" \
"ds_read_b64_tr_b16 a[80:81], v9 offset:512\n" \
"v_and_b32 v36, 0xffff0000, v36\n" \
"v_mfma_f32_16x16x32_bf16 v[212:215], a[108:111], a[20:23], v[212:215]\n" \
"buffer_load_dwordx2 a[206:207], v59, s[40:43], s52 offen offset:0 nt\n" \
"v_mfma_f32_16x16x32_bf16 v[220:223], a[104:107], a[24:27], v[220:223]\n" \
"ds_read_b64_tr_b16 a[82:83], v10 offset:512\n" \
"v_accvgpr_read_b32 v37, a194\n" \
"v_mfma_f32_16x16x32_bf16 v[220:223], a[108:111], a[28:31], v[220:223]\n" \
"v_lshlrev_b32 v31, 16, v37\n" \
"v_and_b32 v37, 0xffff0000, v37\n" \
"v_mfma_f32_16x16x32_bf16 v[228:231], a[104:107], a[32:35], v[228:231]\n" \
"s_mov_b32 s47, 1835008\n" \
"s_add_u32 m0, s22, 50048\n" \
"ds_read_b64_tr_b16 a[84:85], v9 offset:1536\n" \
"v_mfma_f32_16x16x32_bf16 v[228:231], a[108:111], a[36:39], v[228:231]\n" \
"buffer_load_dwordx4 v21, s[32:35], s47 offen offset:128 lds\n" \
"v_mfma_f32_16x16x32_bf16 v[236:239], a[104:107], a[40:43], v[236:239]\n" \
"ds_read_b64_tr_b16 a[86:87], v10 offset:1536\n" \
"v_accvgpr_read_b32 v38, a196\n" \
"v_mfma_f32_16x16x32_bf16 v[236:239], a[108:111], a[44:47], v[236:239]\n" \
"v_lshlrev_b32 v32, 16, v38\n" \
"v_and_b32 v38, 0xffff0000, v38\n" \
"v_mfma_f32_16x16x32_bf16 v[244:247], a[104:107], a[48:51], v[244:247]\n" \
"s_add_u32 m0, s50, 8192\n" \
"ds_read_b64_tr_b16 a[88:89], v11 offset:512\n" \
"v_accvgpr_read_b32 v39, a198\n" \
"v_mfma_f32_16x16x32_bf16 v[244:247], a[108:111], a[52:55], v[244:247]\n" \
"buffer_load_dwordx4 v15, s[28:31], s46 offen lds\n" \
"v_mfma_f32_16x16x32_bf16 v[252:255], a[104:107], a[56:59], v[252:255]\n" \
"ds_read_b64_tr_b16 a[90:91], v12 offset:512\n" \
"v_lshlrev_b32 v33, 16, v39\n" \
"v_mfma_f32_16x16x32_bf16 v[252:255], a[108:111], a[60:63], v[252:255]\n" \
"v_and_b32 v39, 0xffff0000, v39\n" \
"v_mul_f32 v25, v30, v45\n" \
"v_mfma_f32_16x16x32_bf16 a[128:131], a[112:115], a[0:3], a[128:131]\n" \
"ds_read_b64_tr_b16 a[92:93], v11 offset:1536\n" \
"v_mul_f32 v26, v36, v49\n" \
"v_mfma_f32_16x16x32_bf16 a[128:131], a[116:119], a[4:7], a[128:131]\n" \
"buffer_load_dwordx2 a[222:223], v59, s[80:83], s52 offen offset:0 nt\n" \
"v_mfma_f32_16x16x32_bf16 a[136:139], a[112:115], a[8:11], a[136:139]\n" \
"ds_read_b64_tr_b16 a[94:95], v12 offset:1536\n" \
"v_fmac_f32 v25, v31, v44\n" \
"v_mfma_f32_16x16x32_bf16 a[136:139], a[116:119], a[12:15], a[136:139]\n" \
"v_fmac_f32 v26, v37, v48\n" \
"v_fmac_f32 v25, v32, v43\n" \
"v_mfma_f32_16x16x32_bf16 a[144:147], a[112:115], a[16:19], a[144:147]\n" \
"s_add_u32 m0, s22, 51840\n" \
"v_fmac_f32 v26, v38, v47\n" \
"v_fmac_f32 v25, v33, v42\n" \
"v_mfma_f32_16x16x32_bf16 a[144:147], a[116:119], a[20:23], a[144:147]\n" \
"buffer_load_dwordx4 v21, s[32:35], s47 offen offset:384 lds\n" \
"v_mfma_f32_16x16x32_bf16 a[152:155], a[112:115], a[24:27], a[152:155]\n" \
"v_fmac_f32 v26, v39, v46\n" \
"v_accvgpr_read_b32 v28, a214\n" \
"v_mfma_f32_16x16x32_bf16 a[152:155], a[116:119], a[28:31], a[152:155]\n" \
"v_lshlrev_b32 v27, 16, v28\n" \
"v_and_b32 v28, 0xffff0000, v28\n" \
"v_mfma_f32_16x16x32_bf16 a[160:163], a[112:115], a[32:35], a[160:163]\n" \
"v_mul_f32 v29, v25, v27\n" \
"v_mfma_f32_16x16x32_bf16 a[160:163], a[116:119], a[36:39], a[160:163]\n" \
"buffer_load_dwordx2 a[208:209], v59, s[40:43], s52 offen offset:2048 nt\n" \
"v_mfma_f32_16x16x32_bf16 a[168:171], a[112:115], a[40:43], a[168:171]\n" \
"s_waitcnt vmcnt(17)\n" \
"v_mul_f32 v58, v26, v28\n" \
"v_accvgpr_read_b32 v40, a200\n" \
"v_mfma_f32_16x16x32_bf16 a[168:171], a[116:119], a[44:47], a[168:171]\n" \
"s_waitcnt vmcnt(33) lgkmcnt(0)\n" \
"v_lshlrev_b32 v34, 16, v40\n" \
"v_and_b32 v40, 0xffff0000, v40\n" \
"v_mfma_f32_16x16x32_bf16 a[176:179], a[112:115], a[48:51], a[176:179]\n" \
"s_add_u32 m0, s50, 12288\n" \
"s_barrier\n" \
"v_mul_f32 v25, v31, v45\n" \
"v_mfma_f32_16x16x32_bf16 a[176:179], a[116:119], a[52:55], a[176:179]\n" \
"buffer_load_dwordx4 v16, s[28:31], s46 offen lds\n" \
"v_mfma_f32_16x16x32_bf16 a[184:187], a[112:115], a[56:59], a[184:187]\n" \
"v_mul_f32 v26, v37, v49\n" \
"v_fmac_f32 v25, v32, v44\n" \
"v_mfma_f32_16x16x32_bf16 a[184:187], a[116:119], a[60:63], a[184:187]\n" \
"v_fmac_f32 v26, v38, v48\n" \
"v_fmac_f32 v25, v33, v43\n" \
"v_mfma_f32_16x16x32_bf16 a[132:135], a[120:123], a[0:3], a[132:135]\n" \
"v_fmac_f32 v26, v39, v47\n" \
"v_mfma_f32_16x16x32_bf16 a[132:135], a[124:127], a[4:7], a[132:135]\n" \
"buffer_load_dwordx2 a[224:225], v59, s[80:83], s52 offen offset:2048 nt\n" \
"v_mfma_f32_16x16x32_bf16 a[140:143], a[120:123], a[8:11], a[140:143]\n" \
"s_mov_b32 s52, 12288\n" \
"ds_read_b128 a[0:3], v5 offset:32768\n" \
"v_fmac_f32 v25, v34, v42\n" \
"v_mfma_f32_16x16x32_bf16 a[140:143], a[124:127], a[12:15], a[140:143]\n" \
"ds_read_b128 a[4:7], v6 offset:32768\n" \
"v_fmac_f32 v26, v40, v46\n" \
"v_mfma_f32_16x16x32_bf16 a[148:151], a[120:123], a[16:19], a[148:151]\n" \
"ds_read_b128 a[8:11], v5 offset:34816\n" \
"v_accvgpr_read_b32 v28, a216\n" \
"v_mfma_f32_16x16x32_bf16 a[148:151], a[124:127], a[20:23], a[148:151]\n" \
"ds_read_b128 a[12:15], v6 offset:34816\n" \
"v_lshlrev_b32 v27, 16, v28\n" \
"v_mfma_f32_16x16x32_bf16 a[156:159], a[120:123], a[24:27], a[156:159]\n" \
"ds_read_b128 a[16:19], v5 offset:36864\n" \
"v_and_b32 v28, 0xffff0000, v28\n" \
"v_mfma_f32_16x16x32_bf16 a[156:159], a[124:127], a[28:31], a[156:159]\n" \
"ds_read_b128 a[20:23], v6 offset:36864\n" \
"v_mul_f32 v25, v25, v27\n" \
"v_mfma_f32_16x16x32_bf16 a[164:167], a[120:123], a[32:35], a[164:167]\n" \
"ds_read_b128 a[24:27], v5 offset:38912\n" \
"v_mul_f32 v26, v26, v28\n" \
"v_mfma_f32_16x16x32_bf16 a[164:167], a[124:127], a[36:39], a[164:167]\n" \
"ds_read_b128 a[28:31], v6 offset:38912\n" \
"v_cvt_pk_bf16_f32 v53, v25, v29\n" \
"v_mfma_f32_16x16x32_bf16 a[172:175], a[120:123], a[40:43], a[172:175]\n" \
"ds_read_b128 a[32:35], v5 offset:40960\n" \
"v_cvt_pk_bf16_f32 v57, v26, v58\n" \
"v_mfma_f32_16x16x32_bf16 a[172:175], a[124:127], a[44:47], a[172:175]\n" \
"ds_read_b128 a[36:39], v6 offset:40960\n" \
"s_waitcnt vmcnt(15)\n" \
"v_mfma_f32_16x16x32_bf16 a[180:183], a[120:123], a[48:51], a[180:183]\n" \
"ds_read_b128 a[40:43], v5 offset:43008\n" \
"v_accvgpr_read_b32 v41, a202\n" \
"v_mfma_f32_16x16x32_bf16 a[180:183], a[124:127], a[52:55], a[180:183]\n" \
"LH0_%=:\ns_cbranch_execz LH8_%=\n" \
"ds_read_b128 a[44:47], v6 offset:43008\n" \
"v_lshlrev_b32 v35, 16, v41\n" \
"v_mfma_f32_16x16x32_bf16 a[188:191], a[120:123], a[56:59], a[188:191]\n" \
"ds_read_b128 a[48:51], v5 offset:45056\n" \
"v_and_b32 v41, 0xffff0000, v41\n" \
"v_mfma_f32_16x16x32_bf16 a[188:191], a[124:127], a[60:63], a[188:191]\n" \
"s_waitcnt lgkmcnt(9)\n" \
"ds_read_b128 a[52:55], v6 offset:45056\n" \
"v_mul_f32 v25, v32, v45\n" \
"v_mfma_f32_16x16x32_bf16 v[64:67], a[64:67], a[0:3], v[64:67]\n" \
"s_mov_b32 s47, 2097152\n" \
"s_add_u32 m0, s22, 0\n" \
"ds_read_b128 a[56:59], v5 offset:47104\n" \
"v_mfma_f32_16x16x32_bf16 v[64:67], a[68:71], a[4:7], v[64:67]\n" \
"buffer_load_dwordx4 v21, s[32:35], s47 offen offset:0 lds\n" \
"v_mfma_f32_16x16x32_bf16 v[72:75], a[64:67], a[8:11], v[72:75]\n" \
"s_waitcnt lgkmcnt(7)\n" \
"ds_read_b128 a[60:63], v6 offset:47104\n" \
"v_mul_f32 v26, v38, v49\n" \
"v_mfma_f32_16x16x32_bf16 v[72:75], a[68:71], a[12:15], v[72:75]\n" \
"v_fmac_f32 v25, v33, v44\n" \
"v_fmac_f32 v26, v39, v48\n" \
"v_mfma_f32_16x16x32_bf16 v[80:83], a[64:67], a[16:19], v[80:83]\n" \
"ds_read_b64_tr_b16 a[96:97], v9 offset:16384\n" \
"v_fmac_f32 v25, v34, v43\n" \
"v_mfma_f32_16x16x32_bf16 v[80:83], a[68:71], a[20:23], v[80:83]\n" \
"buffer_load_dwordx2 a[210:211], v59, s[40:43], s52 offen offset:0 nt\n" \
"v_mfma_f32_16x16x32_bf16 v[88:91], a[64:67], a[24:27], v[88:91]\n" \
"s_waitcnt lgkmcnt(5)\n" \
"ds_read_b64_tr_b16 a[98:99], v10 offset:16384\n" \
"v_fmac_f32 v26, v40, v47\n" \
"v_mfma_f32_16x16x32_bf16 v[88:91], a[68:71], a[28:31], v[88:91]\n" \
"v_fmac_f32 v25, v35, v42\n" \
"v_fmac_f32 v26, v41, v46\n" \
"v_mfma_f32_16x16x32_bf16 v[96:99], a[64:67], a[32:35], v[96:99]\n" \
"s_mov_b32 s46, 512\n" \
"s_add_u32 m0, s50, 16384\n" \
"ds_read_b64_tr_b16 a[100:101], v9 offset:17408\n" \
"v_mfma_f32_16x16x32_bf16 v[96:99], a[68:71], a[36:39], v[96:99]\n" \
"buffer_load_dwordx4 v17, s[28:31], s46 offen lds\n" \
"v_mfma_f32_16x16x32_bf16 v[104:107], a[64:67], a[40:43], v[104:107]\n" \
"s_waitcnt lgkmcnt(3)\n" \
"ds_read_b64_tr_b16 a[102:103], v10 offset:17408\n" \
"v_accvgpr_read_b32 v28, a218\n" \
"v_mfma_f32_16x16x32_bf16 v[104:107], a[68:71], a[44:47], v[104:107]\n" \
"v_lshlrev_b32 v27, 16, v28\n" \
"v_and_b32 v28, 0xffff0000, v28\n" \
"v_mfma_f32_16x16x32_bf16 v[112:115], a[64:67], a[48:51], v[112:115]\n" \
"ds_read_b64_tr_b16 a[104:105], v11 offset:16384\n" \
"v_mul_f32 v29, v25, v27\n" \
"v_mfma_f32_16x16x32_bf16 v[112:115], a[68:71], a[52:55], v[112:115]\n" \
"buffer_load_dwordx2 a[226:227], v59, s[80:83], s52 offen offset:0 nt\n" \
"v_mfma_f32_16x16x32_bf16 v[120:123], a[64:67], a[56:59], v[120:123]\n" \
"ds_read_b64_tr_b16 a[106:107], v12 offset:16384\n" \
"v_mul_f32 v58, v26, v28\n" \
"v_mfma_f32_16x16x32_bf16 v[120:123], a[68:71], a[60:63], v[120:123]\n" \
"s_waitcnt vmcnt(14)\n" \
"v_accvgpr_read_b32 v36, a204\n" \
"v_lshlrev_b32 v30, 16, v36\n" \
"v_mfma_f32_16x16x32_bf16 v[68:71], a[72:75], a[0:3], v[68:71]\n" \
"s_add_u32 m0, s22, 1792\n" \
"ds_read_b64_tr_b16 a[108:109], v11 offset:17408\n" \
"v_and_b32 v36, 0xffff0000, v36\n" \
"v_mfma_f32_16x16x32_bf16 v[68:71], a[76:79], a[4:7], v[68:71]\n" \
"buffer_load_dwordx4 v21, s[32:35], s47 offen offset:256 lds\n" \
"v_mfma_f32_16x16x32_bf16 v[76:79], a[72:75], a[8:11], v[76:79]\n" \
"ds_read_b64_tr_b16 a[110:111], v12 offset:17408\n" \
"v_mul_f32 v25, v33, v45\n" \
"v_mfma_f32_16x16x32_bf16 v[76:79], a[76:79], a[12:15], v[76:79]\n" \
"v_mul_f32 v26, v39, v49\n" \
"v_fmac_f32 v25, v34, v44\n" \
"v_mfma_f32_16x16x32_bf16 v[84:87], a[72:75], a[16:19], v[84:87]\n" \
"ds_read_b64_tr_b16 a[112:113], v9 offset:16896\n" \
"v_fmac_f32 v26, v40, v48\n" \
"v_mfma_f32_16x16x32_bf16 v[84:87], a[76:79], a[20:23], v[84:87]\n" \
"buffer_load_dwordx2 a[212:213], v59, s[40:43], s52 offen offset:2048 nt\n" \
"v_mfma_f32_16x16x32_bf16 v[92:95], a[72:75], a[24:27], v[92:95]\n" \
"ds_read_b64_tr_b16 a[114:115], v10 offset:16896\n" \
"v_fmac_f32 v25, v35, v43\n" \
"v_mfma_f32_16x16x32_bf16 v[92:95], a[76:79], a[28:31], v[92:95]\n" \
"v_fmac_f32 v26, v41, v47\n" \
"v_fmac_f32 v25, v30, v42\n" \
"v_mfma_f32_16x16x32_bf16 v[100:103], a[72:75], a[32:35], v[100:103]\n" \
"s_add_u32 m0, s50, 20480\n" \
"ds_read_b64_tr_b16 a[116:117], v9 offset:17920\n" \
"v_fmac_f32 v26, v36, v46\n" \
"v_mfma_f32_16x16x32_bf16 v[100:103], a[76:79], a[36:39], v[100:103]\n" \
"buffer_load_dwordx4 v18, s[28:31], s46 offen lds\n" \
"v_mfma_f32_16x16x32_bf16 v[108:111], a[72:75], a[40:43], v[108:111]\n" \
"ds_read_b64_tr_b16 a[118:119], v10 offset:17920\n" \
"v_accvgpr_read_b32 v28, a220\n" \
"v_mfma_f32_16x16x32_bf16 v[108:111], a[76:79], a[44:47], v[108:111]\n" \
"v_lshlrev_b32 v27, 16, v28\n" \
"v_and_b32 v28, 0xffff0000, v28\n" \
"v_mfma_f32_16x16x32_bf16 v[116:119], a[72:75], a[48:51], v[116:119]\n" \
"ds_read_b64_tr_b16 a[120:121], v11 offset:16896\n" \
"v_mul_f32 v25, v25, v27\n" \
"v_mfma_f32_16x16x32_bf16 v[116:119], a[76:79], a[52:55], v[116:119]\n" \
"buffer_load_dwordx2 a[228:229], v59, s[80:83], s52 offen offset:2048 nt\n" \
"v_mfma_f32_16x16x32_bf16 v[124:127], a[72:75], a[56:59], v[124:127]\n" \
"ds_read_b64_tr_b16 a[122:123], v12 offset:16896\n" \
"v_mul_f32 v26, v26, v28\n" \
"v_mfma_f32_16x16x32_bf16 v[124:127], a[76:79], a[60:63], v[124:127]\n" \
"s_waitcnt vmcnt(12)\n" \
"v_cvt_pk_bf16_f32 v52, v25, v29\n" \
"v_cvt_pk_bf16_f32 v56, v26, v58\n" \
"v_mfma_f32_16x16x32_bf16 v[128:131], a[80:83], a[0:3], v[128:131]\n" \
"s_mov_b32 s47, 2359296\n" \
"s_add_u32 m0, s22, 1024\n" \
"ds_read_b64_tr_b16 a[124:125], v11 offset:17920\n" \
"v_mfma_f32_16x16x32_bf16 v[128:131], a[84:87], a[4:7], v[128:131]\n" \
"buffer_load_dwordx4 v21, s[32:35], s47 offen offset:0 lds\n" \
"v_mfma_f32_16x16x32_bf16 v[136:139], a[80:83], a[8:11], v[136:139]\n" \
"ds_read_b64_tr_b16 a[126:127], v12 offset:17920\n" \
"v_accvgpr_read_b32 v37, a206\n" \
"v_mfma_f32_16x16x32_bf16 v[136:139], a[84:87], a[12:15], v[136:139]\n" \
"v_lshlrev_b32 v31, 16, v37\n" \
"v_and_b32 v37, 0xffff0000, v37\n" \
"v_mfma_f32_16x16x32_bf16 v[144:147], a[80:83], a[16:19], v[144:147]\n" \
"v_mul_f32 v25, v34, v45\n" \
"v_mul_f32 v26, v40, v49\n" \
"v_mfma_f32_16x16x32_bf16 v[144:147], a[84:87], a[20:23], v[144:147]\n" \
"v_fmac_f32 v25, v35, v44\n" \
"v_mfma_f32_16x16x32_bf16 v[152:155], a[80:83], a[24:27], v[152:155]\n" \
"v_fmac_f32 v26, v41, v48\n" \
"v_fmac_f32 v25, v30, v43\n" \
"v_mfma_f32_16x16x32_bf16 v[152:155], a[84:87], a[28:31], v[152:155]\n" \
"v_fmac_f32 v26, v36, v47\n" \
"v_fmac_f32 v25, v31, v42\n" \
"v_mfma_f32_16x16x32_bf16 v[160:163], a[80:83], a[32:35], v[160:163]\n" \
"s_add_u32 m0, s50, 24576\n" \
"v_fmac_f32 v26, v37, v46\n" \
"v_mfma_f32_16x16x32_bf16 v[160:163], a[84:87], a[36:39], v[160:163]\n" \
"buffer_load_dwordx4 v19, s[28:31], s46 offen lds\n" \
"v_mfma_f32_16x16x32_bf16 v[168:171], a[80:83], a[40:43], v[168:171]\n" \
"v_accvgpr_read_b32 v28, a222\n" \
"v_lshlrev_b32 v27, 16, v28\n" \
"v_mfma_f32_16x16x32_bf16 v[168:171], a[84:87], a[44:47], v[168:171]\n" \
"s_waitcnt vmcnt(26) lgkmcnt(0)\n" \
"v_and_b32 v28, 0xffff0000, v28\n" \
"v_mul_f32 v29, v25, v27\n" \
"v_mfma_f32_16x16x32_bf16 v[176:179], a[80:83], a[48:51], v[176:179]\n" \
"s_barrier\n" \
"v_mul_f32 v58, v26, v28\n" \
"s_waitcnt vmcnt(10)\n" \
"v_mfma_f32_16x16x32_bf16 v[176:179], a[84:87], a[52:55], v[176:179]\n" \
"v_accvgpr_read_b32 v38, a208\n" \
"v_lshlrev_b32 v32, 16, v38\n" \
"v_mfma_f32_16x16x32_bf16 v[184:187], a[80:83], a[56:59], v[184:187]\n" \
"v_and_b32 v38, 0xffff0000, v38\n" \
"v_mul_f32 v25, v35, v45\n" \
"v_mfma_f32_16x16x32_bf16 v[184:187], a[84:87], a[60:63], v[184:187]\n" \
"v_mul_f32 v26, v41, v49\n" \
"v_mfma_f32_16x16x32_bf16 v[132:135], a[88:91], a[0:3], v[132:135]\n" \
"s_add_u32 m0, s22, 2816\n" \
"v_fmac_f32 v25, v30, v44\n" \
"v_fmac_f32 v26, v36, v48\n" \
"v_mfma_f32_16x16x32_bf16 v[132:135], a[92:95], a[4:7], v[132:135]\n" \
"buffer_load_dwordx4 v21, s[32:35], s47 offen offset:256 lds\n" \
"v_mfma_f32_16x16x32_bf16 v[140:143], a[88:91], a[8:11], v[140:143]\n" \
"s_add_u32 s84, s84, 1048576\ns_addc_u32 s85, s85, 0\n" \
"v_fmac_f32 v25, v31, v43\n" \
"v_mfma_f32_16x16x32_bf16 v[140:143], a[92:95], a[12:15], v[140:143]\n" \
"v_fmac_f32 v26, v37, v47\n" \
"v_fmac_f32 v25, v32, v42\n" \
"v_mfma_f32_16x16x32_bf16 v[148:151], a[88:91], a[16:19], v[148:151]\n" \
"v_fmac_f32 v26, v38, v46\n" \
"v_accvgpr_read_b32 v28, a224\n" \
"v_mfma_f32_16x16x32_bf16 v[148:151], a[92:95], a[20:23], v[148:151]\n" \
"v_lshlrev_b32 v27, 16, v28\n" \
"v_mfma_f32_16x16x32_bf16 v[156:159], a[88:91], a[24:27], v[156:159]\n" \
"v_and_b32 v28, 0xffff0000, v28\n" \
"v_mul_f32 v25, v25, v27\n" \
"v_mfma_f32_16x16x32_bf16 v[156:159], a[92:95], a[28:31], v[156:159]\n" \
"v_mul_f32 v26, v26, v28\n" \
"v_cvt_pk_bf16_f32 v51, v25, v29\n" \
"v_mfma_f32_16x16x32_bf16 v[164:167], a[88:91], a[32:35], v[164:167]\n" \
"s_add_u32 m0, s50, 28672\n" \
"v_cvt_pk_bf16_f32 v55, v26, v58\n" \
"v_mfma_f32_16x16x32_bf16 v[164:167], a[92:95], a[36:39], v[164:167]\n" \
"buffer_load_dwordx4 v20, s[28:31], s46 offen lds\n" \
"v_mfma_f32_16x16x32_bf16 v[172:175], a[88:91], a[40:43], v[172:175]\n" \
"v_mfma_f32_16x16x32_bf16 v[172:175], a[92:95], a[44:47], v[172:175]\n" \
"v_mfma_f32_16x16x32_bf16 v[180:183], a[88:91], a[48:51], v[180:183]\n" \
"v_mfma_f32_16x16x32_bf16 v[180:183], a[92:95], a[52:55], v[180:183]\n" \
"s_mov_b64 exec, 0xffff\nglobal_load_dword v22, v1, s[84:85]\ns_mov_b64 exec, -1\n" \
"v_mfma_f32_16x16x32_bf16 v[188:191], a[88:91], a[56:59], v[188:191]\n" \
"v_mfma_f32_16x16x32_bf16 v[188:191], a[92:95], a[60:63], v[188:191]\n" \
"s_waitcnt vmcnt(9)\n" \
"v_accvgpr_read_b32 v39, a210\n" \
"v_lshlrev_b32 v33, 16, v39\n" \
"v_mfma_f32_16x16x32_bf16 v[192:195], a[96:99], a[0:3], v[192:195]\n" \
"s_mov_b32 s47, 2097152\n" \
"s_add_u32 m0, s22, 16256\n" \
"v_and_b32 v39, 0xffff0000, v39\n" \
"v_mfma_f32_16x16x32_bf16 v[192:195], a[100:103], a[4:7], v[192:195]\n" \
"buffer_load_dwordx4 v21, s[32:35], s47 offen offset:128 lds\n" \
"v_mfma_f32_16x16x32_bf16 v[200:203], a[96:99], a[8:11], v[200:203]\n" \
"v_mul_f32 v25, v30, v45\n" \
"v_mul_f32 v26, v36, v49\n" \
"v_mfma_f32_16x16x32_bf16 v[200:203], a[100:103], a[12:15], v[200:203]\n" \
"v_fmac_f32 v25, v31, v44\n" \
"v_fmac_f32 v26, v37, v48\n" \
"v_mfma_f32_16x16x32_bf16 v[208:211], a[96:99], a[16:19], v[208:211]\n" \
"s_mov_b32 s46, 640\n" \
"s_add_u32 m0, s50, 32768\n" \
"ds_read_b64_tr_b16 a[64:65], v9 offset:32768\n" \
"v_mfma_f32_16x16x32_bf16 v[208:211], a[100:103], a[20:23], v[208:211]\n" \
"buffer_load_dwordx4 v13, s[28:31], s46 offen lds\n" \
"v_mfma_f32_16x16x32_bf16 v[216:219], a[96:99], a[24:27], v[216:219]\n" \
"ds_read_b64_tr_b16 a[66:67], v10 offset:32768\n" \
"v_fmac_f32 v25, v32, v43\n" \
"v_mfma_f32_16x16x32_bf16 v[216:219], a[100:103], a[28:31], v[216:219]\n" \
"v_fmac_f32 v26, v38, v47\n" \
"v_fmac_f32 v25, v33, v42\n" \
"v_mfma_f32_16x16x32_bf16 v[224:227], a[96:99], a[32:35], v[224:227]\n" \
"ds_read_b64_tr_b16 a[68:69], v9 offset:33792\n" \
"v_fmac_f32 v26, v39, v46\n" \
"v_mfma_f32_16x16x32_bf16 v[224:227], a[100:103], a[36:39], v[224:227]\n" \
"v_accvgpr_read_b32 v28, a226\n" \
"v_lshlrev_b32 v27, 16, v28\n" \
"v_mfma_f32_16x16x32_bf16 v[232:235], a[96:99], a[40:43], v[232:235]\n" \
"ds_read_b64_tr_b16 a[70:71], v10 offset:33792\n" \
"v_and_b32 v28, 0xffff0000, v28\n" \
"v_mfma_f32_16x16x32_bf16 v[232:235], a[100:103], a[44:47], v[232:235]\n" \
"v_mul_f32 v29, v25, v27\n" \
"v_mul_f32 v58, v26, v28\n" \
"v_mfma_f32_16x16x32_bf16 v[240:243], a[96:99], a[48:51], v[240:243]\n" \
"s_add_u32 m0, s22, 18048\n" \
"ds_read_b64_tr_b16 a[72:73], v11 offset:32768\n" \
"v_mfma_f32_16x16x32_bf16 v[240:243], a[100:103], a[52:55], v[240:243]\n" \
"buffer_load_dwordx4 v21, s[32:35], s47 offen offset:384 lds\n" \
"v_mfma_f32_16x16x32_bf16 v[248:251], a[96:99], a[56:59], v[248:251]\n" \
"ds_read_b64_tr_b16 a[74:75], v12 offset:32768\n" \
"v_mfma_f32_16x16x32_bf16 v[248:251], a[100:103], a[60:63], v[248:251]\n" \
"s_waitcnt vmcnt(8)\n" \
"v_accvgpr_read_b32 v40, a212\n" \
"v_lshlrev_b32 v34, 16, v40\n" \
"v_mfma_f32_16x16x32_bf16 v[196:199], a[104:107], a[0:3], v[196:199]\n" \
"s_add_u32 m0, s50, 36864\n" \
"ds_read_b64_tr_b16 a[76:77], v11 offset:33792\n" \
"v_and_b32 v40, 0xffff0000, v40\n" \
"v_mfma_f32_16x16x32_bf16 v[196:199], a[108:111], a[4:7], v[196:199]\n" \
"buffer_load_dwordx4 v14, s[28:31], s46 offen lds\n" \
"v_mfma_f32_16x16x32_bf16 v[204:207], a[104:107], a[8:11], v[204:207]\n" \
"ds_read_b64_tr_b16 a[78:79], v12 offset:33792\n" \
"v_mul_f32 v25, v31, v45\n" \
"v_mfma_f32_16x16x32_bf16 v[204:207], a[108:111], a[12:15], v[204:207]\n" \
"v_mul_f32 v26, v37, v49\n" \
"v_fmac_f32 v25, v32, v44\n" \
"v_mfma_f32_16x16x32_bf16 v[212:215], a[104:107], a[16:19], v[212:215]\n" \
"ds_read_b64_tr_b16 a[80:81], v9 offset:33280\n" \
"v_fmac_f32 v26, v38, v48\n" \
"v_mfma_f32_16x16x32_bf16 v[212:215], a[108:111], a[20:23], v[212:215]\n" \
"v_fmac_f32 v25, v33, v43\n" \
"v_fmac_f32 v26, v39, v47\n" \
"v_mfma_f32_16x16x32_bf16 v[220:223], a[104:107], a[24:27], v[220:223]\n" \
"ds_read_b64_tr_b16 a[82:83], v10 offset:33280\n" \
"v_fmac_f32 v25, v34, v42\n" \
"v_mfma_f32_16x16x32_bf16 v[220:223], a[108:111], a[28:31], v[220:223]\n" \
"v_fmac_f32 v26, v40, v46\n" \
"v_accvgpr_read_b32 v28, a228\n" \
"v_mfma_f32_16x16x32_bf16 v[228:231], a[104:107], a[32:35], v[228:231]\n" \
"s_mov_b32 s47, 2359296\n" \
"s_add_u32 m0, s22, 17280\n" \
"ds_read_b64_tr_b16 a[84:85], v9 offset:34304\n" \
"v_mfma_f32_16x16x32_bf16 v[228:231], a[108:111], a[36:39], v[228:231]\n" \
"buffer_load_dwordx4 v21, s[32:35], s47 offen offset:128 lds\n" \
"v_mfma_f32_16x16x32_bf16 v[236:239], a[104:107], a[40:43], v[236:239]\n" \
"ds_read_b64_tr_b16 a[86:87], v10 offset:34304\n" \
"v_lshlrev_b32 v27, 16, v28\n" \
"v_mfma_f32_16x16x32_bf16 v[236:239], a[108:111], a[44:47], v[236:239]\n" \
"v_and_b32 v28, 0xffff0000, v28\n" \
"v_mul_f32 v25, v25, v27\n" \
"v_mfma_f32_16x16x32_bf16 v[244:247], a[104:107], a[48:51], v[244:247]\n" \
"s_add_u32 m0, s50, 40960\n" \
"ds_read_b64_tr_b16 a[88:89], v11 offset:33280\n" \
"v_mul_f32 v26, v26, v28\n" \
"v_mfma_f32_16x16x32_bf16 v[244:247], a[108:111], a[52:55], v[244:247]\n" \
"buffer_load_dwordx4 v15, s[28:31], s46 offen lds\n" \
"v_mfma_f32_16x16x32_bf16 v[252:255], a[104:107], a[56:59], v[252:255]\n" \
"ds_read_b64_tr_b16 a[90:91], v12 offset:33280\n" \
"v_cvt_pk_bf16_f32 v50, v25, v29\n" \
"v_mfma_f32_16x16x32_bf16 v[252:255], a[108:111], a[60:63], v[252:255]\n" \
"s_mov_b32 s94, 79691776\n" \
"v_cvt_pk_bf16_f32 v54, v26, v58\n" \
"v_accvgpr_read_b32 v43, a234\n" \
"v_mfma_f32_16x16x32_bf16 a[128:131], a[112:115], a[0:3], a[128:131]\n" \
"ds_read_b64_tr_b16 a[92:93], v11 offset:34304\n" \
"v_lshlrev_b32 v42, 16, v43\n" \
"v_mfma_f32_16x16x32_bf16 a[128:131], a[116:119], a[4:7], a[128:131]\n" \
"buffer_store_dwordx4 v[50:53], v60, s[36:39], s94 offen offset:0 sc1\n" \
"v_mfma_f32_16x16x32_bf16 a[136:139], a[112:115], a[8:11], a[136:139]\n" \
"s_mov_b32 s95, 79695872\n" \
"ds_read_b64_tr_b16 a[94:95], v12 offset:34304\n" \
"v_and_b32 v43, 0xffff0000, v43\n" \
"v_mfma_f32_16x16x32_bf16 a[136:139], a[116:119], a[12:15], a[136:139]\n" \
"v_accvgpr_read_b32 v45, a235\n" \
"v_lshlrev_b32 v44, 16, v45\n" \
"v_mfma_f32_16x16x32_bf16 a[144:147], a[112:115], a[16:19], a[144:147]\n" \
"s_add_u32 m0, s22, 19072\n" \
"v_and_b32 v45, 0xffff0000, v45\n" \
"v_accvgpr_read_b32 v47, a236\n" \
"v_mfma_f32_16x16x32_bf16 a[144:147], a[116:119], a[20:23], a[144:147]\n" \
"buffer_load_dwordx4 v21, s[32:35], s47 offen offset:384 lds\n" \
"v_mfma_f32_16x16x32_bf16 a[152:155], a[112:115], a[24:27], a[152:155]\n" \
"v_lshlrev_b32 v46, 16, v47\n" \
"v_and_b32 v47, 0xffff0000, v47\n" \
"v_mfma_f32_16x16x32_bf16 a[152:155], a[116:119], a[28:31], a[152:155]\n" \
"v_accvgpr_read_b32 v49, a237\n" \
"v_lshlrev_b32 v48, 16, v49\n" \
"v_mfma_f32_16x16x32_bf16 a[160:163], a[112:115], a[32:35], a[160:163]\n" \
"v_and_b32 v49, 0xffff0000, v49\n" \
"v_mfma_f32_16x16x32_bf16 a[160:163], a[116:119], a[36:39], a[160:163]\n" \
"v_accvgpr_read_b32 v36, a193\n" \
"v_lshlrev_b32 v30, 16, v36\n" \
"v_mfma_f32_16x16x32_bf16 a[168:171], a[112:115], a[40:43], a[168:171]\n" \
"v_and_b32 v36, 0xffff0000, v36\n" \
"v_accvgpr_read_b32 v37, a195\n" \
"v_mfma_f32_16x16x32_bf16 a[168:171], a[116:119], a[44:47], a[168:171]\n" \
"s_waitcnt vmcnt(24) lgkmcnt(0)\n" \
"v_lshlrev_b32 v31, 16, v37\n" \
"v_mfma_f32_16x16x32_bf16 a[176:179], a[112:115], a[48:51], a[176:179]\n" \
"s_add_u32 m0, s50, 45056\n" \
"s_barrier\n" \
"v_and_b32 v37, 0xffff0000, v37\n" \
"v_mfma_f32_16x16x32_bf16 a[176:179], a[116:119], a[52:55], a[176:179]\n" \
"buffer_load_dwordx4 v16, s[28:31], s46 offen lds\n" \
"v_mfma_f32_16x16x32_bf16 a[184:187], a[112:115], a[56:59], a[184:187]\n" \
"v_accvgpr_read_b32 v38, a197\n" \
"v_lshlrev_b32 v32, 16, v38\n" \
"v_mfma_f32_16x16x32_bf16 a[184:187], a[116:119], a[60:63], a[184:187]\n" \
"buffer_store_dwordx4 v[54:57], v60, s[36:39], s95 offen offset:0 sc1\n" \
"v_mfma_f32_16x16x32_bf16 a[132:135], a[120:123], a[0:3], a[132:135]\n" \
"v_and_b32 v38, 0xffff0000, v38\n" \
"v_accvgpr_read_b32 v39, a199\n" \
"v_mfma_f32_16x16x32_bf16 a[132:135], a[124:127], a[4:7], a[132:135]\n" \
"v_lshlrev_b32 v33, 16, v39\n" \
"v_and_b32 v39, 0xffff0000, v39\n" \
"v_mfma_f32_16x16x32_bf16 a[140:143], a[120:123], a[8:11], a[140:143]\n" \
"ds_read_b128 a[0:3], v7 offset:0\n" \
"v_mul_f32 v25, v30, v45\n" \
"v_mfma_f32_16x16x32_bf16 a[140:143], a[124:127], a[12:15], a[140:143]\n" \
"ds_read_b128 a[4:7], v8 offset:0\n" \
"v_mul_f32 v26, v36, v49\n" \
"v_mfma_f32_16x16x32_bf16 a[148:151], a[120:123], a[16:19], a[148:151]\n" \
"ds_read_b128 a[8:11], v7 offset:2048\n" \
"v_fmac_f32 v25, v31, v44\n" \
"v_mfma_f32_16x16x32_bf16 a[148:151], a[124:127], a[20:23], a[148:151]\n" \
"ds_read_b128 a[12:15], v8 offset:2048\n" \
"v_fmac_f32 v26, v37, v48\n" \
"v_mfma_f32_16x16x32_bf16 a[156:159], a[120:123], a[24:27], a[156:159]\n" \
"ds_read_b128 a[16:19], v7 offset:4096\n" \
"v_fmac_f32 v25, v32, v43\n" \
"v_mfma_f32_16x16x32_bf16 a[156:159], a[124:127], a[28:31], a[156:159]\n" \
"ds_read_b128 a[20:23], v8 offset:4096\n" \
"v_fmac_f32 v26, v38, v47\n" \
"v_mfma_f32_16x16x32_bf16 a[164:167], a[120:123], a[32:35], a[164:167]\n" \
"ds_read_b128 a[24:27], v7 offset:6144\n" \
"v_fmac_f32 v25, v33, v42\n" \
"v_mfma_f32_16x16x32_bf16 a[164:167], a[124:127], a[36:39], a[164:167]\n" \
"ds_read_b128 a[28:31], v8 offset:6144\n" \
"v_fmac_f32 v26, v39, v46\n" \
"v_mfma_f32_16x16x32_bf16 a[172:175], a[120:123], a[40:43], a[172:175]\n" \
"ds_read_b128 a[32:35], v7 offset:8192\n" \
"v_accvgpr_read_b32 v28, a215\n" \
"v_mfma_f32_16x16x32_bf16 a[172:175], a[124:127], a[44:47], a[172:175]\n" \
"ds_read_b128 a[36:39], v8 offset:8192\n" \
"v_lshlrev_b32 v27, 16, v28\n" \
"v_mfma_f32_16x16x32_bf16 a[180:183], a[120:123], a[48:51], a[180:183]\n" \
"ds_read_b128 a[40:43], v7 offset:10240\n" \
"v_and_b32 v28, 0xffff0000, v28\n" \
"v_mfma_f32_16x16x32_bf16 a[180:183], a[124:127], a[52:55], a[180:183]\n" \
"ds_read_b128 a[44:47], v8 offset:10240\n" \
"v_mul_f32 v29, v25, v27\n" \
"v_mfma_f32_16x16x32_bf16 a[188:191], a[120:123], a[56:59], a[188:191]\n" \
"ds_read_b128 a[48:51], v7 offset:12288\n" \
"v_mul_f32 v58, v26, v28\n" \
"v_mfma_f32_16x16x32_bf16 a[188:191], a[124:127], a[60:63], a[188:191]\n" \
"s_waitcnt lgkmcnt(9)\n" \
"ds_read_b128 a[52:55], v8 offset:12288\n" \
"v_accvgpr_read_b32 v40, a201\n" \
"v_mfma_f32_16x16x32_bf16 v[64:67], a[64:67], a[0:3], v[64:67]\n" \
"s_mov_b32 s47, 2621440\n" \
"s_add_u32 m0, s22, 32768\n" \
"ds_read_b128 a[56:59], v7 offset:14336\n" \
"v_mfma_f32_16x16x32_bf16 v[64:67], a[68:71], a[4:7], v[64:67]\n" \
"buffer_load_dwordx4 v21, s[32:35], s47 offen offset:0 lds\n" \
"v_mfma_f32_16x16x32_bf16 v[72:75], a[64:67], a[8:11], v[72:75]\n" \
"s_waitcnt lgkmcnt(7)\n" \
"ds_read_b128 a[60:63], v8 offset:14336\n" \
"v_lshlrev_b32 v34, 16, v40\n" \
"v_mfma_f32_16x16x32_bf16 v[72:75], a[68:71], a[12:15], v[72:75]\n" \
"v_and_b32 v40, 0xffff0000, v40\n" \
"v_mul_f32 v25, v31, v45\n" \
"v_mfma_f32_16x16x32_bf16 v[80:83], a[64:67], a[16:19], v[80:83]\n" \
"s_mov_b32 s46, 640\n" \
"s_add_u32 m0, s50, 49152\n" \
"ds_read_b64_tr_b16 a[96:97], v9 offset:49152\n" \
"v_mfma_f32_16x16x32_bf16 v[80:83], a[68:71], a[20:23], v[80:83]\n" \
"buffer_load_dwordx4 v17, s[28:31], s46 offen lds\n" \
"v_mfma_f32_16x16x32_bf16 v[88:91], a[64:67], a[24:27], v[88:91]\n" \
"s_waitcnt lgkmcnt(5)\n" \
"ds_read_b64_tr_b16 a[98:99], v10 offset:49152\n" \
"v_mul_f32 v26, v37, v49\n" \
"v_mfma_f32_16x16x32_bf16 v[88:91], a[68:71], a[28:31], v[88:91]\n" \
"v_fmac_f32 v25, v32, v44\n" \
"v_fmac_f32 v26, v38, v48\n" \
"v_mfma_f32_16x16x32_bf16 v[96:99], a[64:67], a[32:35], v[96:99]\n" \
"s_add_u32 m0, s22, 34560\n" \
"ds_read_b64_tr_b16 a[100:101], v9 offset:50176\n" \
"v_fmac_f32 v25, v33, v43\n" \
"v_mfma_f32_16x16x32_bf16 v[96:99], a[68:71], a[36:39], v[96:99]\n" \
"buffer_load_dwordx4 v21, s[32:35], s47 offen offset:256 lds\n" \
"v_mfma_f32_16x16x32_bf16 v[104:107], a[64:67], a[40:43], v[104:107]\n" \
"s_waitcnt lgkmcnt(3)\n" \
"ds_read_b64_tr_b16 a[102:103], v10 offset:50176\n" \
"v_fmac_f32 v26, v39, v47\n" \
"v_mfma_f32_16x16x32_bf16 v[104:107], a[68:71], a[44:47], v[104:107]\n" \
"v_fmac_f32 v25, v34, v42\n" \
"v_fmac_f32 v26, v40, v46\n" \
"v_mfma_f32_16x16x32_bf16 v[112:115], a[64:67], a[48:51], v[112:115]\n" \
"s_add_u32 m0, s50, 53248\n" \
"ds_read_b64_tr_b16 a[104:105], v11 offset:49152\n" \
"v_accvgpr_read_b32 v28, a217\n" \
"v_mfma_f32_16x16x32_bf16 v[112:115], a[68:71], a[52:55], v[112:115]\n" \
"buffer_load_dwordx4 v18, s[28:31], s46 offen lds\n" \
"v_mfma_f32_16x16x32_bf16 v[120:123], a[64:67], a[56:59], v[120:123]\n" \
"ds_read_b64_tr_b16 a[106:107], v12 offset:49152\n" \
"v_lshlrev_b32 v27, 16, v28\n" \
"v_mfma_f32_16x16x32_bf16 v[120:123], a[68:71], a[60:63], v[120:123]\n" \
"v_and_b32 v28, 0xffff0000, v28\n" \
"v_mul_f32 v25, v25, v27\n" \
"v_mfma_f32_16x16x32_bf16 v[68:71], a[72:75], a[0:3], v[68:71]\n" \
"s_mov_b32 s47, 2883584\n" \
"s_add_u32 m0, s22, 33792\n" \
"ds_read_b64_tr_b16 a[108:109], v11 offset:50176\n" \
"v_mfma_f32_16x16x32_bf16 v[68:71], a[76:79], a[4:7], v[68:71]\n" \
"buffer_load_dwordx4 v21, s[32:35], s47 offen offset:0 lds\n" \
"v_mfma_f32_16x16x32_bf16 v[76:79], a[72:75], a[8:11], v[76:79]\n" \
"ds_read_b64_tr_b16 a[110:111], v12 offset:50176\n" \
"v_mul_f32 v26, v26, v28\n" \
"v_mfma_f32_16x16x32_bf16 v[76:79], a[76:79], a[12:15], v[76:79]\n" \
"v_cvt_pk_bf16_f32 v53, v25, v29\n" \
"v_cvt_pk_bf16_f32 v57, v26, v58\n" \
"v_mfma_f32_16x16x32_bf16 v[84:87], a[72:75], a[16:19], v[84:87]\n" \
"s_add_u32 m0, s50, 57344\n" \
"ds_read_b64_tr_b16 a[112:113], v9 offset:49664\n" \
"v_accvgpr_read_b32 v41, a203\n" \
"v_mfma_f32_16x16x32_bf16 v[84:87], a[76:79], a[20:23], v[84:87]\n" \
"buffer_load_dwordx4 v19, s[28:31], s46 offen lds\n" \
"v_mfma_f32_16x16x32_bf16 v[92:95], a[72:75], a[24:27], v[92:95]\n" \
"ds_read_b64_tr_b16 a[114:115], v10 offset:49664\n" \
"v_lshlrev_b32 v35, 16, v41\n" \
"v_mfma_f32_16x16x32_bf16 v[92:95], a[76:79], a[28:31], v[92:95]\n" \
"v_and_b32 v41, 0xffff0000, v41\n" \
"v_mul_f32 v25, v32, v45\n" \
"v_mfma_f32_16x16x32_bf16 v[100:103], a[72:75], a[32:35], v[100:103]\n" \
"s_add_u32 m0, s22, 35584\n" \
"ds_read_b64_tr_b16 a[116:117], v9 offset:50688\n" \
"v_mul_f32 v26, v38, v49\n" \
"v_mfma_f32_16x16x32_bf16 v[100:103], a[76:79], a[36:39], v[100:103]\n" \
"buffer_load_dwordx4 v21, s[32:35], s47 offen offset:256 lds\n" \
"v_mfma_f32_16x16x32_bf16 v[108:111], a[72:75], a[40:43], v[108:111]\n" \
"s_add_u32 s84, s84, 1048576\ns_addc_u32 s85, s85, 0\n" \
"ds_read_b64_tr_b16 a[118:119], v10 offset:50688\n" \
"v_mfma_f32_16x16x32_bf16 v[108:111], a[76:79], a[44:47], v[108:111]\n" \
"v_fmac_f32 v25, v33, v44\n" \
"v_fmac_f32 v26, v39, v48\n" \
"v_mfma_f32_16x16x32_bf16 v[116:119], a[72:75], a[48:51], v[116:119]\n" \
"s_add_u32 m0, s50, 61440\n" \
"ds_read_b64_tr_b16 a[120:121], v11 offset:49664\n" \
"v_fmac_f32 v25, v34, v43\n" \
"v_mfma_f32_16x16x32_bf16 v[116:119], a[76:79], a[52:55], v[116:119]\n" \
"buffer_load_dwordx4 v20, s[28:31], s46 offen lds\n" \
"v_mfma_f32_16x16x32_bf16 v[124:127], a[72:75], a[56:59], v[124:127]\n" \
"ds_read_b64_tr_b16 a[122:123], v12 offset:49664\n" \
"v_fmac_f32 v26, v40, v47\n" \
"v_mfma_f32_16x16x32_bf16 v[124:127], a[76:79], a[60:63], v[124:127]\n" \
"v_fmac_f32 v25, v35, v42\n" \
"v_fmac_f32 v26, v41, v46\n" \
"v_mfma_f32_16x16x32_bf16 v[128:131], a[80:83], a[0:3], v[128:131]\n" \
"ds_read_b64_tr_b16 a[124:125], v11 offset:50688\n" \
"v_accvgpr_read_b32 v28, a219\n" \
"v_mfma_f32_16x16x32_bf16 v[128:131], a[84:87], a[4:7], v[128:131]\n" \
"s_mov_b64 exec, 0xffff\nglobal_load_dword v22, v1, s[84:85]\ns_mov_b64 exec, -1\n" \
"v_mfma_f32_16x16x32_bf16 v[136:139], a[80:83], a[8:11], v[136:139]\n" \
"ds_read_b64_tr_b16 a[126:127], v12 offset:50688\n" \
"v_lshlrev_b32 v27, 16, v28\n" \
"v_mfma_f32_16x16x32_bf16 v[136:139], a[84:87], a[12:15], v[136:139]\n" \
"v_and_b32 v28, 0xffff0000, v28\n" \
"v_mul_f32 v29, v25, v27\n" \
"v_mfma_f32_16x16x32_bf16 v[144:147], a[80:83], a[16:19], v[144:147]\n" \
"v_mul_f32 v58, v26, v28\n" \
"v_accvgpr_read_b32 v36, a205\n" \
"v_mfma_f32_16x16x32_bf16 v[144:147], a[84:87], a[20:23], v[144:147]\n" \
"s_mov_b64 exec, 0xffff\nglobal_load_dword v22, v1, s[76:77]\ns_mov_b64 exec, -1\n" \
"v_mfma_f32_16x16x32_bf16 v[152:155], a[80:83], a[24:27], v[152:155]\n" \
"v_lshlrev_b32 v30, 16, v36\n" \
"v_and_b32 v36, 0xffff0000, v36\n" \
"v_mfma_f32_16x16x32_bf16 v[152:155], a[84:87], a[28:31], v[152:155]\n" \
"v_mul_f32 v25, v33, v45\n" \
"v_mul_f32 v26, v39, v49\n" \
"v_mfma_f32_16x16x32_bf16 v[160:163], a[80:83], a[32:35], v[160:163]\n" \
"v_fmac_f32 v25, v34, v44\n" \
"v_mfma_f32_16x16x32_bf16 v[160:163], a[84:87], a[36:39], v[160:163]\n" \
"v_fmac_f32 v26, v40, v48\n" \
"v_fmac_f32 v25, v35, v43\n" \
"v_mfma_f32_16x16x32_bf16 v[168:171], a[80:83], a[40:43], v[168:171]\n" \
"v_fmac_f32 v26, v41, v47\n" \
"v_fmac_f32 v25, v30, v42\n" \
"v_mfma_f32_16x16x32_bf16 v[168:171], a[84:87], a[44:47], v[168:171]\n" \
"s_waitcnt vmcnt(22) lgkmcnt(0)\n" \
"v_fmac_f32 v26, v36, v46\n" \
"v_mfma_f32_16x16x32_bf16 v[176:179], a[80:83], a[48:51], v[176:179]\n" \
"s_barrier\n" \
"v_accvgpr_read_b32 v28, a221\n" \
"v_lshlrev_b32 v27, 16, v28\n" \
"v_mfma_f32_16x16x32_bf16 v[176:179], a[84:87], a[52:55], v[176:179]\n" \
"v_and_b32 v28, 0xffff0000, v28\n" \
"v_mul_f32 v25, v25, v27\n" \
"v_mfma_f32_16x16x32_bf16 v[184:187], a[80:83], a[56:59], v[184:187]\n" \
"v_mul_f32 v26, v26, v28\n" \
"v_mfma_f32_16x16x32_bf16 v[184:187], a[84:87], a[60:63], v[184:187]\n" \
"v_cvt_pk_bf16_f32 v52, v25, v29\n" \
"v_cvt_pk_bf16_f32 v56, v26, v58\n" \
"v_mfma_f32_16x16x32_bf16 v[132:135], a[88:91], a[0:3], v[132:135]\n" \
"v_accvgpr_read_b32 v37, a207\n" \
"v_lshlrev_b32 v31, 16, v37\n" \
"v_mfma_f32_16x16x32_bf16 v[132:135], a[92:95], a[4:7], v[132:135]\n" \
"v_and_b32 v37, 0xffff0000, v37\n" \
"v_mfma_f32_16x16x32_bf16 v[140:143], a[88:91], a[8:11], v[140:143]\n" \
"v_mul_f32 v25, v34, v45\n" \
"v_mul_f32 v26, v40, v49\n" \
"v_mfma_f32_16x16x32_bf16 v[140:143], a[92:95], a[12:15], v[140:143]\n" \
"v_fmac_f32 v25, v35, v44\n" \
"v_fmac_f32 v26, v41, v48\n" \
"v_mfma_f32_16x16x32_bf16 v[148:151], a[88:91], a[16:19], v[148:151]\n" \
"v_fmac_f32 v25, v30, v43\n" \
"v_mfma_f32_16x16x32_bf16 v[148:151], a[92:95], a[20:23], v[148:151]\n" \
"v_fmac_f32 v26, v36, v47\n" \
"v_fmac_f32 v25, v31, v42\n" \
"v_mfma_f32_16x16x32_bf16 v[156:159], a[88:91], a[24:27], v[156:159]\n" \
"v_fmac_f32 v26, v37, v46\n" \
"v_accvgpr_read_b32 v28, a223\n" \
"v_mfma_f32_16x16x32_bf16 v[156:159], a[92:95], a[28:31], v[156:159]\n" \
"v_lshlrev_b32 v27, 16, v28\n" \
"v_mfma_f32_16x16x32_bf16 v[164:167], a[88:91], a[32:35], v[164:167]\n" \
"v_and_b32 v28, 0xffff0000, v28\n" \
"v_mul_f32 v29, v25, v27\n" \
"v_mfma_f32_16x16x32_bf16 v[164:167], a[92:95], a[36:39], v[164:167]\n" \
"v_mul_f32 v58, v26, v28\n" \
"v_accvgpr_read_b32 v38, a209\n" \
"v_mfma_f32_16x16x32_bf16 v[172:175], a[88:91], a[40:43], v[172:175]\n" \
"v_lshlrev_b32 v32, 16, v38\n" \
"v_mfma_f32_16x16x32_bf16 v[172:175], a[92:95], a[44:47], v[172:175]\n" \
"v_and_b32 v38, 0xffff0000, v38\n" \
"v_mul_f32 v25, v35, v45\n" \
"v_mfma_f32_16x16x32_bf16 v[180:183], a[88:91], a[48:51], v[180:183]\n" \
"v_mul_f32 v26, v41, v49\n" \
"v_fmac_f32 v25, v30, v44\n" \
"v_mfma_f32_16x16x32_bf16 v[180:183], a[92:95], a[52:55], v[180:183]\n" \
"v_fmac_f32 v26, v36, v48\n" \
"v_mfma_f32_16x16x32_bf16 v[188:191], a[88:91], a[56:59], v[188:191]\n" \
"v_fmac_f32 v25, v31, v43\n" \
"v_fmac_f32 v26, v37, v47\n" \
"v_mfma_f32_16x16x32_bf16 v[188:191], a[92:95], a[60:63], v[188:191]\n" \
"v_fmac_f32 v25, v32, v42\n" \
"v_fmac_f32 v26, v38, v46\n" \
"v_mfma_f32_16x16x32_bf16 v[192:195], a[96:99], a[0:3], v[192:195]\n" \
"v_accvgpr_read_b32 v28, a225\n" \
"v_mfma_f32_16x16x32_bf16 v[192:195], a[100:103], a[4:7], v[192:195]\n" \
"v_lshlrev_b32 v27, 16, v28\n" \
"v_and_b32 v28, 0xffff0000, v28\n" \
"v_mfma_f32_16x16x32_bf16 v[200:203], a[96:99], a[8:11], v[200:203]\n" \
"v_mul_f32 v25, v25, v27\n" \
"v_mul_f32 v26, v26, v28\n" \
"v_mfma_f32_16x16x32_bf16 v[200:203], a[100:103], a[12:15], v[200:203]\n" \
"v_cvt_pk_bf16_f32 v51, v25, v29\n" \
"v_mfma_f32_16x16x32_bf16 v[208:211], a[96:99], a[16:19], v[208:211]\n" \
"ds_read_b64_tr_b16 a[64:65], v9 offset:0\n" \
"v_cvt_pk_bf16_f32 v55, v26, v58\n" \
"v_mfma_f32_16x16x32_bf16 v[208:211], a[100:103], a[20:23], v[208:211]\n" \
"v_accvgpr_read_b32 v39, a211\n" \
"v_lshlrev_b32 v33, 16, v39\n" \
"v_mfma_f32_16x16x32_bf16 v[216:219], a[96:99], a[24:27], v[216:219]\n" \
"ds_read_b64_tr_b16 a[66:67], v10 offset:0\n" \
"v_and_b32 v39, 0xffff0000, v39\n" \
"v_mfma_f32_16x16x32_bf16 v[216:219], a[100:103], a[28:31], v[216:219]\n" \
"v_mul_f32 v25, v30, v45\n" \
"v_mul_f32 v26, v36, v49\n" \
"v_mfma_f32_16x16x32_bf16 v[224:227], a[96:99], a[32:35], v[224:227]\n" \
"LH1_%=:\ns_cbranch_execz LH9_%=\n" \
"ds_read_b64_tr_b16 a[68:69], v9 offset:1024\n" \
"v_fmac_f32 v25, v31, v44\n" \
"v_mfma_f32_16x16x32_bf16 v[224:227], a[100:103], a[36:39], v[224:227]\n" \
"v_fmac_f32 v26, v37, v48\n" \
"v_fmac_f32 v25, v32, v43\n" \
"v_mfma_f32_16x16x32_bf16 v[232:235], a[96:99], a[40:43], v[232:235]\n" \
"ds_read_b64_tr_b16 a[70:71], v10 offset:1024\n" \
"v_fmac_f32 v26, v38, v47\n" \
"v_mfma_f32_16x16x32_bf16 v[232:235], a[100:103], a[44:47], v[232:235]\n" \
"v_fmac_f32 v25, v33, v42\n" \
"v_fmac_f32 v26, v39, v46\n" \
"v_mfma_f32_16x16x32_bf16 v[240:243], a[96:99], a[48:51], v[240:243]\n" \
"ds_read_b64_tr_b16 a[72:73], v11 offset:0\n" \
"v_accvgpr_read_b32 v28, a227\n" \
"v_mfma_f32_16x16x32_bf16 v[240:243], a[100:103], a[52:55], v[240:243]\n" \
"v_lshlrev_b32 v27, 16, v28\n" \
"v_and_b32 v28, 0xffff0000, v28\n" \
"v_mfma_f32_16x16x32_bf16 v[248:251], a[96:99], a[56:59], v[248:251]\n" \
"ds_read_b64_tr_b16 a[74:75], v12 offset:0\n" \
"v_mul_f32 v29, v25, v27\n" \
"v_mfma_f32_16x16x32_bf16 v[248:251], a[100:103], a[60:63], v[248:251]\n" \
"v_mul_f32 v58, v26, v28\n" \
"v_accvgpr_read_b32 v40, a213\n" \
"v_mfma_f32_16x16x32_bf16 v[196:199], a[104:107], a[0:3], v[196:199]\n" \
"ds_read_b64_tr_b16 a[76:77], v11 offset:1024\n" \
"v_lshlrev_b32 v34, 16, v40\n" \
"v_mfma_f32_16x16x32_bf16 v[196:199], a[108:111], a[4:7], v[196:199]\n" \
"v_and_b32 v40, 0xffff0000, v40\n" \
"v_mul_f32 v25, v31, v45\n" \
"v_mfma_f32_16x16x32_bf16 v[204:207], a[104:107], a[8:11], v[204:207]\n" \
"ds_read_b64_tr_b16 a[78:79], v12 offset:1024\n" \
"v_mul_f32 v26, v37, v49\n" \
"v_mfma_f32_16x16x32_bf16 v[204:207], a[108:111], a[12:15], v[204:207]\n" \
"v_fmac_f32 v25, v32, v44\n" \
"v_fmac_f32 v26, v38, v48\n" \
"v_mfma_f32_16x16x32_bf16 v[212:215], a[104:107], a[16:19], v[212:215]\n" \
"ds_read_b64_tr_b16 a[80:81], v9 offset:512\n" \
"v_fmac_f32 v25, v33, v43\n" \
"v_mfma_f32_16x16x32_bf16 v[212:215], a[108:111], a[20:23], v[212:215]\n" \
"v_fmac_f32 v26, v39, v47\n" \
"v_fmac_f32 v25, v34, v42\n" \
"v_mfma_f32_16x16x32_bf16 v[220:223], a[104:107], a[24:27], v[220:223]\n" \
"ds_read_b64_tr_b16 a[82:83], v10 offset:512\n" \
"v_fmac_f32 v26, v40, v46\n" \
"v_mfma_f32_16x16x32_bf16 v[220:223], a[108:111], a[28:31], v[220:223]\n" \
"v_accvgpr_read_b32 v28, a229\n" \
"v_lshlrev_b32 v27, 16, v28\n" \
"v_mfma_f32_16x16x32_bf16 v[228:231], a[104:107], a[32:35], v[228:231]\n" \
"ds_read_b64_tr_b16 a[84:85], v9 offset:1536\n" \
"v_and_b32 v28, 0xffff0000, v28\n" \
"v_mfma_f32_16x16x32_bf16 v[228:231], a[108:111], a[36:39], v[228:231]\n" \
"v_mul_f32 v25, v25, v27\n" \
"v_mul_f32 v26, v26, v28\n" \
"v_mfma_f32_16x16x32_bf16 v[236:239], a[104:107], a[40:43], v[236:239]\n" \
"ds_read_b64_tr_b16 a[86:87], v10 offset:1536\n" \
"v_cvt_pk_bf16_f32 v50, v25, v29\n" \
"v_mfma_f32_16x16x32_bf16 v[236:239], a[108:111], a[44:47], v[236:239]\n" \
"s_mov_b32 s94, 79699968\n" \
"v_cvt_pk_bf16_f32 v54, v26, v58\n" \
"v_mfma_f32_16x16x32_bf16 v[244:247], a[104:107], a[48:51], v[244:247]\n" \
"ds_read_b64_tr_b16 a[88:89], v11 offset:512\n" \
"v_mfma_f32_16x16x32_bf16 v[244:247], a[108:111], a[52:55], v[244:247]\n" \
"buffer_store_dwordx4 v[50:53], v60, s[36:39], s94 offen offset:0 sc1\n" \
"v_mfma_f32_16x16x32_bf16 v[252:255], a[104:107], a[56:59], v[252:255]\n" \
"s_mov_b32 s95, 79704064\n" \
"ds_read_b64_tr_b16 a[90:91], v12 offset:512\n" \
"v_mfma_f32_16x16x32_bf16 v[252:255], a[108:111], a[60:63], v[252:255]\n" \
"v_mfma_f32_16x16x32_bf16 a[128:131], a[112:115], a[0:3], a[128:131]\n" \
"ds_read_b64_tr_b16 a[92:93], v11 offset:1536\n" \
"v_mfma_f32_16x16x32_bf16 a[128:131], a[116:119], a[4:7], a[128:131]\n" \
"v_mfma_f32_16x16x32_bf16 a[136:139], a[112:115], a[8:11], a[136:139]\n" \
"ds_read_b64_tr_b16 a[94:95], v12 offset:1536\n" \
"v_mfma_f32_16x16x32_bf16 a[136:139], a[116:119], a[12:15], a[136:139]\n" \
"v_mfma_f32_16x16x32_bf16 a[144:147], a[112:115], a[16:19], a[144:147]\n" \
"v_mfma_f32_16x16x32_bf16 a[144:147], a[116:119], a[20:23], a[144:147]\n" \
"v_mfma_f32_16x16x32_bf16 a[152:155], a[112:115], a[24:27], a[152:155]\n" \
"v_mfma_f32_16x16x32_bf16 a[152:155], a[116:119], a[28:31], a[152:155]\n" \
"v_mfma_f32_16x16x32_bf16 a[160:163], a[112:115], a[32:35], a[160:163]\n" \
"v_mfma_f32_16x16x32_bf16 a[160:163], a[116:119], a[36:39], a[160:163]\n" \
"v_mfma_f32_16x16x32_bf16 a[168:171], a[112:115], a[40:43], a[168:171]\n" \
"v_mfma_f32_16x16x32_bf16 a[168:171], a[116:119], a[44:47], a[168:171]\n" \
"s_waitcnt vmcnt(13) lgkmcnt(0)\n" \
"v_mfma_f32_16x16x32_bf16 a[176:179], a[112:115], a[48:51], a[176:179]\n" \
"s_barrier\n" \
"v_mfma_f32_16x16x32_bf16 a[176:179], a[116:119], a[52:55], a[176:179]\n" \
"buffer_store_dwordx4 v[54:57], v60, s[36:39], s95 offen offset:0 sc1\n" \
"v_mfma_f32_16x16x32_bf16 a[184:187], a[112:115], a[56:59], a[184:187]\n" \
"s_lshl_b32 s62, s8, 2\n" \
"s_add_u32 s62, s62, %2\n" \
"s_lshl_b32 s63, s62, 12\n" \
"v_mfma_f32_16x16x32_bf16 a[184:187], a[116:119], a[60:63], a[184:187]\n" \
"s_add_u32 s68, s10, s63\ns_addc_u32 s69, s11, 0\n" \
"s_and_b32 s69, s69, 0xffff\n" \
"v_mfma_f32_16x16x32_bf16 a[132:135], a[120:123], a[0:3], a[132:135]\n" \
"s_mov_b32 s70, 4096\n" \
"s_mov_b32 s71, 0x00020000\n" \
"s_add_u32 s63, s63, 79691776\n" \
"v_mfma_f32_16x16x32_bf16 a[132:135], a[124:127], a[4:7], a[132:135]\n" \
"s_add_u32 s72, s4, s63\ns_addc_u32 s73, s5, 0\n" \
"s_and_b32 s73, s73, 0xffff\n" \
"v_mfma_f32_16x16x32_bf16 a[140:143], a[120:123], a[8:11], a[140:143]\n" \
"s_mov_b32 s74, 4096\n" \
"ds_read_b128 a[0:3], v5 offset:0\n" \
"s_mov_b32 s75, 0x00020000\n" \
"v_mfma_f32_16x16x32_bf16 a[140:143], a[124:127], a[12:15], a[140:143]\n" \
"ds_read_b128 a[4:7], v6 offset:0\n" \
"v_add_u32 v23, -32, v4\n" \
"v_mfma_f32_16x16x32_bf16 a[148:151], a[120:123], a[16:19], a[148:151]\n" \
"ds_read_b128 a[8:11], v5 offset:2048\n" \
"v_add_u32 v24, 992, v4\n" \
"v_mfma_f32_16x16x32_bf16 a[148:151], a[124:127], a[20:23], a[148:151]\n" \
"ds_read_b128 a[12:15], v6 offset:2048\n" \
"v_add_u32 v25, -16, v4\n" \
"v_mfma_f32_16x16x32_bf16 a[156:159], a[120:123], a[24:27], a[156:159]\n" \
"ds_read_b128 a[16:19], v5 offset:4096\n" \
"v_add_u32 v26, -512, v4\n" \
"v_mfma_f32_16x16x32_bf16 a[156:159], a[124:127], a[28:31], a[156:159]\n" \
"s_lshr_b32 s63, %2, 1\n" \
"ds_read_b128 a[20:23], v6 offset:4096\n" \
"v_add_u32 v27, 512, v4\n" \
"v_mfma_f32_16x16x32_bf16 a[164:167], a[120:123], a[32:35], a[164:167]\n" \
"s_lshl_b32 s63, s63, 16\n" \
"ds_read_b128 a[24:27], v5 offset:6144\n" \
"s_lshl_b32 s56, %2, 14\n" \
"v_mfma_f32_16x16x32_bf16 a[164:167], a[124:127], a[36:39], a[164:167]\n" \
"s_add_u32 s56, s56, s63\n" \
"ds_read_b128 a[28:31], v6 offset:6144\n" \
"v_add_u32 v55, s56, v4\n" \
"v_mfma_f32_16x16x32_bf16 a[172:175], a[120:123], a[40:43], a[172:175]\n" \
"s_lshl_b32 s62, s62, 1\n" \
"ds_read_b128 a[32:35], v5 offset:8192\n" \
"v_mov_b32 v28, 0\n" \
"v_mfma_f32_16x16x32_bf16 a[172:175], a[124:127], a[44:47], a[172:175]\n" \
"ds_read_b128 a[36:39], v6 offset:8192\n" \
"v_mov_b32 v29, s62\n" \
"v_mfma_f32_16x16x32_bf16 a[180:183], a[120:123], a[48:51], a[180:183]\n" \
"s_and_b32 s62, %1, 7\n" \
"ds_read_b128 a[40:43], v5 offset:10240\n" \
"v_lshlrev_b32 v56, 23, v4\n" \
"v_mfma_f32_16x16x32_bf16 a[180:183], a[124:127], a[52:55], a[180:183]\n" \
"s_lshl_b32 s62, s62, 8\n" \
"ds_read_b128 a[44:47], v6 offset:10240\n" \
"s_add_u32 s62, s62, 94380032\n" \
"v_mfma_f32_16x16x32_bf16 a[188:191], a[120:123], a[56:59], a[188:191]\n" \
"ds_read_b128 a[48:51], v5 offset:12288\n" \
"v_bfe_u32 v57, v4, 4, 5\n" \
"v_mfma_f32_16x16x32_bf16 a[188:191], a[124:127], a[60:63], a[188:191]\n" \
"s_waitcnt lgkmcnt(9)\n" \
"ds_read_b128 a[52:55], v6 offset:12288\n" \
"v_lshl_add_u32 v57, v57, 3, s62\n" \
"v_mfma_f32_16x16x32_bf16 v[64:67], a[64:67], a[0:3], v[64:67]\n" \
"s_mov_b32 s47, 2621440\n" \
"s_add_u32 m0, s22, 49024\n" \
"ds_read_b128 a[56:59], v5 offset:14336\n" \
"v_mfma_f32_16x16x32_bf16 v[64:67], a[68:71], a[4:7], v[64:67]\n" \
"buffer_load_dwordx4 v21, s[32:35], s47 offen offset:128 lds\n" \
"v_mfma_f32_16x16x32_bf16 v[72:75], a[64:67], a[8:11], v[72:75]\n" \
"s_waitcnt lgkmcnt(7)\n" \
"ds_read_b128 a[60:63], v6 offset:14336\n" \
"v_mfma_f32_16x16x32_bf16 v[72:75], a[68:71], a[12:15], v[72:75]\n" \
"v_mfma_f32_16x16x32_bf16 v[80:83], a[64:67], a[16:19], v[80:83]\n" \
"s_add_u32 m0, s22, 50816\n" \
"ds_read_b64_tr_b16 a[96:97], v9 offset:16384\n" \
"v_mfma_f32_16x16x32_bf16 v[80:83], a[68:71], a[20:23], v[80:83]\n" \
"buffer_load_dwordx4 v21, s[32:35], s47 offen offset:384 lds\n" \
"v_mfma_f32_16x16x32_bf16 v[88:91], a[64:67], a[24:27], v[88:91]\n" \
"s_waitcnt lgkmcnt(5)\n" \
"ds_read_b64_tr_b16 a[98:99], v10 offset:16384\n" \
"v_mfma_f32_16x16x32_bf16 v[88:91], a[68:71], a[28:31], v[88:91]\n" \
"v_mfma_f32_16x16x32_bf16 v[96:99], a[64:67], a[32:35], v[96:99]\n" \
"s_mov_b32 s47, 2883584\n" \
"s_add_u32 m0, s22, 50048\n" \
"ds_read_b64_tr_b16 a[100:101], v9 offset:17408\n" \
"v_mfma_f32_16x16x32_bf16 v[96:99], a[68:71], a[36:39], v[96:99]\n" \
"buffer_load_dwordx4 v21, s[32:35], s47 offen offset:128 lds\n" \
"v_mfma_f32_16x16x32_bf16 v[104:107], a[64:67], a[40:43], v[104:107]\n" \
"s_waitcnt lgkmcnt(3)\n" \
"ds_read_b64_tr_b16 a[102:103], v10 offset:17408\n" \
"v_mfma_f32_16x16x32_bf16 v[104:107], a[68:71], a[44:47], v[104:107]\n" \
"v_mfma_f32_16x16x32_bf16 v[112:115], a[64:67], a[48:51], v[112:115]\n" \
"s_add_u32 m0, s22, 51840\n" \
"ds_read_b64_tr_b16 a[104:105], v11 offset:16384\n" \
"v_mfma_f32_16x16x32_bf16 v[112:115], a[68:71], a[52:55], v[112:115]\n" \
"buffer_load_dwordx4 v21, s[32:35], s47 offen offset:384 lds\n" \
"v_mfma_f32_16x16x32_bf16 v[120:123], a[64:67], a[56:59], v[120:123]\n" \
"ds_read_b64_tr_b16 a[106:107], v12 offset:16384\n" \
"v_mfma_f32_16x16x32_bf16 v[120:123], a[68:71], a[60:63], v[120:123]\n" \
"v_mfma_f32_16x16x32_bf16 v[68:71], a[72:75], a[0:3], v[68:71]\n" \
"s_mov_b32 s46, 768\n" \
"s_add_u32 m0, s50, 65536\n" \
"ds_read_b64_tr_b16 a[108:109], v11 offset:17408\n" \
"v_mfma_f32_16x16x32_bf16 v[68:71], a[76:79], a[4:7], v[68:71]\n" \
"buffer_load_dwordx4 v13, s[28:31], s46 offen lds\n" \
"v_mfma_f32_16x16x32_bf16 v[76:79], a[72:75], a[8:11], v[76:79]\n" \
"ds_read_b64_tr_b16 a[110:111], v12 offset:17408\n" \
"v_mfma_f32_16x16x32_bf16 v[76:79], a[76:79], a[12:15], v[76:79]\n" \
"v_mfma_f32_16x16x32_bf16 v[84:87], a[72:75], a[16:19], v[84:87]\n" \
"s_add_u32 m0, s50, 69632\n" \
"ds_read_b64_tr_b16 a[112:113], v9 offset:16896\n" \
"v_mfma_f32_16x16x32_bf16 v[84:87], a[76:79], a[20:23], v[84:87]\n" \
"buffer_load_dwordx4 v14, s[28:31], s46 offen lds\n" \
"v_mfma_f32_16x16x32_bf16 v[92:95], a[72:75], a[24:27], v[92:95]\n" \
"ds_read_b64_tr_b16 a[114:115], v10 offset:16896\n" \
"v_mfma_f32_16x16x32_bf16 v[92:95], a[76:79], a[28:31], v[92:95]\n" \
"v_mfma_f32_16x16x32_bf16 v[100:103], a[72:75], a[32:35], v[100:103]\n" \
"s_add_u32 m0, s50, 73728\n" \
"ds_read_b64_tr_b16 a[116:117], v9 offset:17920\n" \
"v_mfma_f32_16x16x32_bf16 v[100:103], a[76:79], a[36:39], v[100:103]\n" \
"buffer_load_dwordx4 v15, s[28:31], s46 offen lds\n" \
"v_mfma_f32_16x16x32_bf16 v[108:111], a[72:75], a[40:43], v[108:111]\n" \
"ds_read_b64_tr_b16 a[118:119], v10 offset:17920\n" \
"v_mfma_f32_16x16x32_bf16 v[108:111], a[76:79], a[44:47], v[108:111]\n" \
"v_mfma_f32_16x16x32_bf16 v[116:119], a[72:75], a[48:51], v[116:119]\n" \
"s_add_u32 m0, s50, 77824\n" \
"ds_read_b64_tr_b16 a[120:121], v11 offset:16896\n" \
"v_mfma_f32_16x16x32_bf16 v[116:119], a[76:79], a[52:55], v[116:119]\n" \
"buffer_load_dwordx4 v16, s[28:31], s46 offen lds\n" \
"v_mfma_f32_16x16x32_bf16 v[124:127], a[72:75], a[56:59], v[124:127]\n" \
"ds_read_b64_tr_b16 a[122:123], v12 offset:16896\n" \
"v_mfma_f32_16x16x32_bf16 v[124:127], a[76:79], a[60:63], v[124:127]\n" \
"v_mfma_f32_16x16x32_bf16 v[128:131], a[80:83], a[0:3], v[128:131]\n" \
"s_mov_b32 s46, 768\n" \
"s_add_u32 m0, s50, 81920\n" \
"ds_read_b64_tr_b16 a[124:125], v11 offset:17920\n" \
"v_mfma_f32_16x16x32_bf16 v[128:131], a[84:87], a[4:7], v[128:131]\n" \
"buffer_load_dwordx4 v17, s[28:31], s46 offen lds\n" \
"v_mfma_f32_16x16x32_bf16 v[136:139], a[80:83], a[8:11], v[136:139]\n" \
"ds_read_b64_tr_b16 a[126:127], v12 offset:17920\n" \
"v_mfma_f32_16x16x32_bf16 v[136:139], a[84:87], a[12:15], v[136:139]\n" \
"v_mfma_f32_16x16x32_bf16 v[144:147], a[80:83], a[16:19], v[144:147]\n" \
"s_add_u32 m0, s50, 86016\n" \
"v_mfma_f32_16x16x32_bf16 v[144:147], a[84:87], a[20:23], v[144:147]\n" \
"buffer_load_dwordx4 v18, s[28:31], s46 offen lds\n" \
"v_mfma_f32_16x16x32_bf16 v[152:155], a[80:83], a[24:27], v[152:155]\n" \
"v_mfma_f32_16x16x32_bf16 v[152:155], a[84:87], a[28:31], v[152:155]\n" \
"v_mfma_f32_16x16x32_bf16 v[160:163], a[80:83], a[32:35], v[160:163]\n" \
"s_add_u32 m0, s50, 90112\n" \
"v_mfma_f32_16x16x32_bf16 v[160:163], a[84:87], a[36:39], v[160:163]\n" \
"buffer_load_dwordx4 v19, s[28:31], s46 offen lds\n" \
"v_mfma_f32_16x16x32_bf16 v[168:171], a[80:83], a[40:43], v[168:171]\n" \
"v_mfma_f32_16x16x32_bf16 v[168:171], a[84:87], a[44:47], v[168:171]\n" \
"s_waitcnt vmcnt(16) lgkmcnt(0)\n" \
"v_mfma_f32_16x16x32_bf16 v[176:179], a[80:83], a[48:51], v[176:179]\n" \
"s_add_u32 m0, s50, 94208\n" \
"s_barrier\n" \
"v_mfma_f32_16x16x32_bf16 v[176:179], a[84:87], a[52:55], v[176:179]\n" \
"buffer_load_dwordx4 v20, s[28:31], s46 offen lds\n" \
"v_mfma_f32_16x16x32_bf16 v[184:187], a[80:83], a[56:59], v[184:187]\n" \
"v_mfma_f32_16x16x32_bf16 v[184:187], a[84:87], a[60:63], v[184:187]\n" \
"v_mfma_f32_16x16x32_bf16 v[132:135], a[88:91], a[0:3], v[132:135]\n" \
"s_mov_b32 s47, 3145728\n" \
"s_add_u32 m0, s22, 0\n" \
"v_mfma_f32_16x16x32_bf16 v[132:135], a[92:95], a[4:7], v[132:135]\n" \
"buffer_load_dwordx4 v21, s[32:35], s47 offen offset:0 lds\n" \
"v_mfma_f32_16x16x32_bf16 v[140:143], a[88:91], a[8:11], v[140:143]\n" \
"v_mfma_f32_16x16x32_bf16 v[140:143], a[92:95], a[12:15], v[140:143]\n" \
"v_mfma_f32_16x16x32_bf16 v[148:151], a[88:91], a[16:19], v[148:151]\n" \
"s_add_u32 m0, s22, 1792\n" \
"v_mfma_f32_16x16x32_bf16 v[148:151], a[92:95], a[20:23], v[148:151]\n" \
"buffer_load_dwordx4 v21, s[32:35], s47 offen offset:256 lds\n" \
"v_mfma_f32_16x16x32_bf16 v[156:159], a[88:91], a[24:27], v[156:159]\n" \
"v_mfma_f32_16x16x32_bf16 v[156:159], a[92:95], a[28:31], v[156:159]\n" \
"v_mfma_f32_16x16x32_bf16 v[164:167], a[88:91], a[32:35], v[164:167]\n" \
"s_mov_b32 s47, 3407872\n" \
"s_add_u32 m0, s22, 1024\n" \
"v_mfma_f32_16x16x32_bf16 v[164:167], a[92:95], a[36:39], v[164:167]\n" \
"buffer_load_dwordx4 v21, s[32:35], s47 offen offset:0 lds\n" \
"v_mfma_f32_16x16x32_bf16 v[172:175], a[88:91], a[40:43], v[172:175]\n" \
"s_add_u32 s90, s64, s53\ns_addc_u32 s91, s65, 0\n" \
"v_mfma_f32_16x16x32_bf16 v[172:175], a[92:95], a[44:47], v[172:175]\n" \
"v_mfma_f32_16x16x32_bf16 v[180:183], a[88:91], a[48:51], v[180:183]\n" \
"s_add_u32 m0, s22, 2816\n" \
"v_mfma_f32_16x16x32_bf16 v[180:183], a[92:95], a[52:55], v[180:183]\n" \
"buffer_load_dwordx4 v21, s[32:35], s47 offen offset:256 lds\n" \
"v_mfma_f32_16x16x32_bf16 v[188:191], a[88:91], a[56:59], v[188:191]\n" \
"v_mfma_f32_16x16x32_bf16 v[188:191], a[92:95], a[60:63], v[188:191]\n" \
"v_mfma_f32_16x16x32_bf16 v[192:195], a[96:99], a[0:3], v[192:195]\n" \
"v_mfma_f32_16x16x32_bf16 v[192:195], a[100:103], a[4:7], v[192:195]\n" \
"global_load_dwordx4 a[240:243], v4, s[90:91] offset:0\n" \
"v_mfma_f32_16x16x32_bf16 v[200:203], a[96:99], a[8:11], v[200:203]\n" \
"v_mfma_f32_16x16x32_bf16 v[200:203], a[100:103], a[12:15], v[200:203]\n" \
"v_mfma_f32_16x16x32_bf16 v[208:211], a[96:99], a[16:19], v[208:211]\n" \
"ds_read_b64_tr_b16 a[64:65], v9 offset:32768\n" \
"v_mfma_f32_16x16x32_bf16 v[208:211], a[100:103], a[20:23], v[208:211]\n" \
"global_load_dwordx4 a[244:247], v4, s[90:91] offset:1024\n" \
"v_mfma_f32_16x16x32_bf16 v[216:219], a[96:99], a[24:27], v[216:219]\n" \
"ds_read_b64_tr_b16 a[66:67], v10 offset:32768\n" \
"v_mfma_f32_16x16x32_bf16 v[216:219], a[100:103], a[28:31], v[216:219]\n" \
"v_mfma_f32_16x16x32_bf16 v[224:227], a[96:99], a[32:35], v[224:227]\n" \
"ds_read_b64_tr_b16 a[68:69], v9 offset:33792\n" \
"v_mfma_f32_16x16x32_bf16 v[224:227], a[100:103], a[36:39], v[224:227]\n" \
"global_load_dwordx4 a[248:251], v4, s[90:91] offset:2048\n" \
"v_mfma_f32_16x16x32_bf16 v[232:235], a[96:99], a[40:43], v[232:235]\n" \
"ds_read_b64_tr_b16 a[70:71], v10 offset:33792\n" \
"v_mfma_f32_16x16x32_bf16 v[232:235], a[100:103], a[44:47], v[232:235]\n" \
"v_mfma_f32_16x16x32_bf16 v[240:243], a[96:99], a[48:51], v[240:243]\n" \
"ds_read_b64_tr_b16 a[72:73], v11 offset:32768\n" \
"v_mfma_f32_16x16x32_bf16 v[240:243], a[100:103], a[52:55], v[240:243]\n" \
"global_load_dwordx4 a[252:255], v4, s[90:91] offset:3072\n" \
"v_mfma_f32_16x16x32_bf16 v[248:251], a[96:99], a[56:59], v[248:251]\n" \
"ds_read_b64_tr_b16 a[74:75], v12 offset:32768\n" \
"v_mfma_f32_16x16x32_bf16 v[248:251], a[100:103], a[60:63], v[248:251]\n" \
"v_mfma_f32_16x16x32_bf16 v[196:199], a[104:107], a[0:3], v[196:199]\n" \
"ds_read_b64_tr_b16 a[76:77], v11 offset:33792\n" \
"v_mfma_f32_16x16x32_bf16 v[196:199], a[108:111], a[4:7], v[196:199]\n" \
"v_mfma_f32_16x16x32_bf16 v[204:207], a[104:107], a[8:11], v[204:207]\n" \
"s_add_u32 s76, s76, 2097152\ns_addc_u32 s77, s77, 0\n" \
"ds_read_b64_tr_b16 a[78:79], v12 offset:33792\n" \
"v_mfma_f32_16x16x32_bf16 v[204:207], a[108:111], a[12:15], v[204:207]\n" \
"v_mfma_f32_16x16x32_bf16 v[212:215], a[104:107], a[16:19], v[212:215]\n" \
"ds_read_b64_tr_b16 a[80:81], v9 offset:33280\n" \
"v_mfma_f32_16x16x32_bf16 v[212:215], a[108:111], a[20:23], v[212:215]\n" \
"v_mfma_f32_16x16x32_bf16 v[220:223], a[104:107], a[24:27], v[220:223]\n" \
"ds_read_b64_tr_b16 a[82:83], v10 offset:33280\n" \
"v_mfma_f32_16x16x32_bf16 v[220:223], a[108:111], a[28:31], v[220:223]\n" \
"v_mfma_f32_16x16x32_bf16 v[228:231], a[104:107], a[32:35], v[228:231]\n" \
"ds_read_b64_tr_b16 a[84:85], v9 offset:34304\n" \
"v_mfma_f32_16x16x32_bf16 v[228:231], a[108:111], a[36:39], v[228:231]\n" \
"v_mfma_f32_16x16x32_bf16 v[236:239], a[104:107], a[40:43], v[236:239]\n" \
"ds_read_b64_tr_b16 a[86:87], v10 offset:34304\n" \
"v_mfma_f32_16x16x32_bf16 v[236:239], a[108:111], a[44:47], v[236:239]\n" \
"s_mov_b64 exec, 0xffff\nglobal_load_dword v22, v1, s[76:77]\ns_mov_b64 exec, -1\n" \
"v_mfma_f32_16x16x32_bf16 v[244:247], a[104:107], a[48:51], v[244:247]\n" \
"ds_read_b64_tr_b16 a[88:89], v11 offset:33280\n" \
"v_mfma_f32_16x16x32_bf16 v[244:247], a[108:111], a[52:55], v[244:247]\n" \
"v_mfma_f32_16x16x32_bf16 v[252:255], a[104:107], a[56:59], v[252:255]\n" \
"ds_read_b64_tr_b16 a[90:91], v12 offset:33280\n" \
"v_mfma_f32_16x16x32_bf16 v[252:255], a[108:111], a[60:63], v[252:255]\n" \
"v_mfma_f32_16x16x32_bf16 a[128:131], a[112:115], a[0:3], a[128:131]\n" \
"ds_read_b64_tr_b16 a[92:93], v11 offset:34304\n" \
"v_mfma_f32_16x16x32_bf16 a[128:131], a[116:119], a[4:7], a[128:131]\n" \
"v_mfma_f32_16x16x32_bf16 a[136:139], a[112:115], a[8:11], a[136:139]\n" \
"ds_read_b64_tr_b16 a[94:95], v12 offset:34304\n" \
"v_mfma_f32_16x16x32_bf16 a[136:139], a[116:119], a[12:15], a[136:139]\n" \
"v_mfma_f32_16x16x32_bf16 a[144:147], a[112:115], a[16:19], a[144:147]\n" \
"v_mfma_f32_16x16x32_bf16 a[144:147], a[116:119], a[20:23], a[144:147]\n" \
"v_mfma_f32_16x16x32_bf16 a[152:155], a[112:115], a[24:27], a[152:155]\n" \
"v_mfma_f32_16x16x32_bf16 a[152:155], a[116:119], a[28:31], a[152:155]\n" \
"global_load_dwordx4 a[192:195], v4, s[86:87] offset:0\n" \
"v_mfma_f32_16x16x32_bf16 a[160:163], a[112:115], a[32:35], a[160:163]\n" \
"v_mov_b32 v58, s14\n" \
"v_mfma_f32_16x16x32_bf16 a[160:163], a[116:119], a[36:39], a[160:163]\n" \
"v_mov_b32 v59, s15\n" \
"v_mfma_f32_16x16x32_bf16 a[168:171], a[112:115], a[40:43], a[168:171]\n" \
"v_mfma_f32_16x16x32_bf16 a[168:171], a[116:119], a[44:47], a[168:171]\n" \
"s_waitcnt vmcnt(18) lgkmcnt(0)\n" \
"v_mfma_f32_16x16x32_bf16 a[176:179], a[112:115], a[48:51], a[176:179]\n" \
"s_barrier\n" \
"v_mfma_f32_16x16x32_bf16 a[176:179], a[116:119], a[52:55], a[176:179]\n" \
"v_mfma_f32_16x16x32_bf16 a[184:187], a[112:115], a[56:59], a[184:187]\n" \
"v_mfma_f32_16x16x32_bf16 a[184:187], a[116:119], a[60:63], a[184:187]\n" \
"buffer_store_dwordx2 v[58:59], v56, s[36:39], s23 offen sc1\n" \
"v_mfma_f32_16x16x32_bf16 a[132:135], a[120:123], a[0:3], a[132:135]\n" \
"v_mfma_f32_16x16x32_bf16 a[132:135], a[124:127], a[4:7], a[132:135]\n" \
"v_mfma_f32_16x16x32_bf16 a[140:143], a[120:123], a[8:11], a[140:143]\n" \
"ds_read_b128 a[0:3], v5 offset:32768\n" \
"v_mfma_f32_16x16x32_bf16 a[140:143], a[124:127], a[12:15], a[140:143]\n" \
"ds_read_b128 a[4:7], v6 offset:32768\n" \
"v_mfma_f32_16x16x32_bf16 a[148:151], a[120:123], a[16:19], a[148:151]\n" \
"ds_read_b128 a[8:11], v5 offset:34816\n" \
"v_mfma_f32_16x16x32_bf16 a[148:151], a[124:127], a[20:23], a[148:151]\n" \
"ds_read_b128 a[12:15], v6 offset:34816\n" \
"v_mfma_f32_16x16x32_bf16 a[156:159], a[120:123], a[24:27], a[156:159]\n" \
"ds_read_b128 a[16:19], v5 offset:36864\n" \
"v_mfma_f32_16x16x32_bf16 a[156:159], a[124:127], a[28:31], a[156:159]\n" \
"ds_read_b128 a[20:23], v6 offset:36864\n" \
"v_mfma_f32_16x16x32_bf16 a[164:167], a[120:123], a[32:35], a[164:167]\n" \
"ds_read_b128 a[24:27], v5 offset:38912\n" \
"v_mfma_f32_16x16x32_bf16 a[164:167], a[124:127], a[36:39], a[164:167]\n" \
"ds_read_b128 a[28:31], v6 offset:38912\n" \
"v_mfma_f32_16x16x32_bf16 a[172:175], a[120:123], a[40:43], a[172:175]\n" \
"ds_read_b128 a[32:35], v5 offset:40960\n" \
"v_mfma_f32_16x16x32_bf16 a[172:175], a[124:127], a[44:47], a[172:175]\n" \
"ds_read_b128 a[36:39], v6 offset:40960\n" \
"v_mfma_f32_16x16x32_bf16 a[180:183], a[120:123], a[48:51], a[180:183]\n" \
"ds_read_b128 a[40:43], v5 offset:43008\n" \
"v_mfma_f32_16x16x32_bf16 a[180:183], a[124:127], a[52:55], a[180:183]\n" \
"ds_read_b128 a[44:47], v6 offset:43008\n" \
"v_mfma_f32_16x16x32_bf16 a[188:191], a[120:123], a[56:59], a[188:191]\n" \
"ds_read_b128 a[48:51], v5 offset:45056\n" \
"v_mfma_f32_16x16x32_bf16 a[188:191], a[124:127], a[60:63], a[188:191]\n" \
"s_waitcnt lgkmcnt(9)\n" \
"ds_read_b128 a[52:55], v6 offset:45056\n" \
"v_mfma_f32_16x16x32_bf16 v[64:67], a[64:67], a[0:3], v[64:67]\n" \
"s_mov_b32 s47, 3145728\n" \
"s_add_u32 m0, s22, 16256\n" \
"ds_read_b128 a[56:59], v5 offset:47104\n" \
"v_mfma_f32_16x16x32_bf16 v[64:67], a[68:71], a[4:7], v[64:67]\n" \
"buffer_load_dwordx4 v21, s[32:35], s47 offen offset:128 lds\n" \
"v_mfma_f32_16x16x32_bf16 v[72:75], a[64:67], a[8:11], v[72:75]\n" \
"s_waitcnt lgkmcnt(7)\n" \
"ds_read_b128 a[60:63], v6 offset:47104\n" \
"v_mfma_f32_16x16x32_bf16 v[72:75], a[68:71], a[12:15], v[72:75]\n" \
"v_mfma_f32_16x16x32_bf16 v[80:83], a[64:67], a[16:19], v[80:83]\n" \
"s_add_u32 m0, s22, 18048\n" \
"ds_read_b64_tr_b16 a[96:97], v9 offset:49152\n" \
"v_mfma_f32_16x16x32_bf16 v[80:83], a[68:71], a[20:23], v[80:83]\n" \
"buffer_load_dwordx4 v21, s[32:35], s47 offen offset:384 lds\n" \
"v_mfma_f32_16x16x32_bf16 v[88:91], a[64:67], a[24:27], v[88:91]\n" \
"s_waitcnt lgkmcnt(5)\n" \
"ds_read_b64_tr_b16 a[98:99], v10 offset:49152\n" \
"v_mfma_f32_16x16x32_bf16 v[88:91], a[68:71], a[28:31], v[88:91]\n" \
"v_mfma_f32_16x16x32_bf16 v[96:99], a[64:67], a[32:35], v[96:99]\n" \
"s_mov_b32 s47, 3407872\n" \
"s_add_u32 m0, s22, 17280\n" \
"ds_read_b64_tr_b16 a[100:101], v9 offset:50176\n" \
"v_mfma_f32_16x16x32_bf16 v[96:99], a[68:71], a[36:39], v[96:99]\n" \
"buffer_load_dwordx4 v21, s[32:35], s47 offen offset:128 lds\n" \
"v_mfma_f32_16x16x32_bf16 v[104:107], a[64:67], a[40:43], v[104:107]\n" \
"s_waitcnt lgkmcnt(3)\n" \
"ds_read_b64_tr_b16 a[102:103], v10 offset:50176\n" \
"v_mfma_f32_16x16x32_bf16 v[104:107], a[68:71], a[44:47], v[104:107]\n" \
"v_mfma_f32_16x16x32_bf16 v[112:115], a[64:67], a[48:51], v[112:115]\n" \
"s_add_u32 m0, s22, 19072\n" \
"ds_read_b64_tr_b16 a[104:105], v11 offset:49152\n" \
"v_mfma_f32_16x16x32_bf16 v[112:115], a[68:71], a[52:55], v[112:115]\n" \
"buffer_load_dwordx4 v21, s[32:35], s47 offen offset:384 lds\n" \
"v_mfma_f32_16x16x32_bf16 v[120:123], a[64:67], a[56:59], v[120:123]\n" \
"ds_read_b64_tr_b16 a[106:107], v12 offset:49152\n" \
"v_mfma_f32_16x16x32_bf16 v[120:123], a[68:71], a[60:63], v[120:123]\n" \
"v_mfma_f32_16x16x32_bf16 v[68:71], a[72:75], a[0:3], v[68:71]\n" \
"s_mov_b32 s46, 896\n" \
"s_add_u32 m0, s50, 0\n" \
"ds_read_b64_tr_b16 a[108:109], v11 offset:50176\n" \
"v_mfma_f32_16x16x32_bf16 v[68:71], a[76:79], a[4:7], v[68:71]\n" \
"buffer_load_dwordx4 v13, s[28:31], s46 offen lds\n" \
"v_mfma_f32_16x16x32_bf16 v[76:79], a[72:75], a[8:11], v[76:79]\n" \
"ds_read_b64_tr_b16 a[110:111], v12 offset:50176\n" \
"v_mfma_f32_16x16x32_bf16 v[76:79], a[76:79], a[12:15], v[76:79]\n" \
"v_mfma_f32_16x16x32_bf16 v[84:87], a[72:75], a[16:19], v[84:87]\n" \
"s_add_u32 m0, s50, 4096\n" \
"ds_read_b64_tr_b16 a[112:113], v9 offset:49664\n" \
"v_mfma_f32_16x16x32_bf16 v[84:87], a[76:79], a[20:23], v[84:87]\n" \
"buffer_load_dwordx4 v14, s[28:31], s46 offen lds\n" \
"v_mfma_f32_16x16x32_bf16 v[92:95], a[72:75], a[24:27], v[92:95]\n" \
"ds_read_b64_tr_b16 a[114:115], v10 offset:49664\n" \
"v_mfma_f32_16x16x32_bf16 v[92:95], a[76:79], a[28:31], v[92:95]\n" \
"v_mfma_f32_16x16x32_bf16 v[100:103], a[72:75], a[32:35], v[100:103]\n" \
"s_add_u32 m0, s50, 8192\n" \
"ds_read_b64_tr_b16 a[116:117], v9 offset:50688\n" \
"v_mfma_f32_16x16x32_bf16 v[100:103], a[76:79], a[36:39], v[100:103]\n" \
"buffer_load_dwordx4 v15, s[28:31], s46 offen lds\n" \
"v_mfma_f32_16x16x32_bf16 v[108:111], a[72:75], a[40:43], v[108:111]\n" \
"ds_read_b64_tr_b16 a[118:119], v10 offset:50688\n" \
"v_mfma_f32_16x16x32_bf16 v[108:111], a[76:79], a[44:47], v[108:111]\n" \
"v_mfma_f32_16x16x32_bf16 v[116:119], a[72:75], a[48:51], v[116:119]\n" \
"s_add_u32 m0, s50, 12288\n" \
"ds_read_b64_tr_b16 a[120:121], v11 offset:49664\n" \
"v_mfma_f32_16x16x32_bf16 v[116:119], a[76:79], a[52:55], v[116:119]\n" \
"buffer_load_dwordx4 v16, s[28:31], s46 offen lds\n" \
"v_mfma_f32_16x16x32_bf16 v[124:127], a[72:75], a[56:59], v[124:127]\n" \
"ds_read_b64_tr_b16 a[122:123], v12 offset:49664\n" \
"v_mfma_f32_16x16x32_bf16 v[124:127], a[76:79], a[60:63], v[124:127]\n" \
"v_mfma_f32_16x16x32_bf16 v[128:131], a[80:83], a[0:3], v[128:131]\n" \
"s_mov_b32 s46, 896\n" \
"s_add_u32 m0, s50, 16384\n" \
"ds_read_b64_tr_b16 a[124:125], v11 offset:50688\n" \
"v_mfma_f32_16x16x32_bf16 v[128:131], a[84:87], a[4:7], v[128:131]\n" \
"buffer_load_dwordx4 v17, s[28:31], s46 offen lds\n" \
"v_mfma_f32_16x16x32_bf16 v[136:139], a[80:83], a[8:11], v[136:139]\n" \
"ds_read_b64_tr_b16 a[126:127], v12 offset:50688\n" \
"v_mfma_f32_16x16x32_bf16 v[136:139], a[84:87], a[12:15], v[136:139]\n" \
"v_mfma_f32_16x16x32_bf16 v[144:147], a[80:83], a[16:19], v[144:147]\n" \
"s_add_u32 m0, s50, 20480\n" \
"v_mfma_f32_16x16x32_bf16 v[144:147], a[84:87], a[20:23], v[144:147]\n" \
"buffer_load_dwordx4 v18, s[28:31], s46 offen lds\n" \
"v_mfma_f32_16x16x32_bf16 v[152:155], a[80:83], a[24:27], v[152:155]\n" \
"v_mfma_f32_16x16x32_bf16 v[152:155], a[84:87], a[28:31], v[152:155]\n" \
"v_mfma_f32_16x16x32_bf16 v[160:163], a[80:83], a[32:35], v[160:163]\n" \
"s_add_u32 m0, s50, 24576\n" \
"v_mfma_f32_16x16x32_bf16 v[160:163], a[84:87], a[36:39], v[160:163]\n" \
"buffer_load_dwordx4 v19, s[28:31], s46 offen lds\n" \
"v_mfma_f32_16x16x32_bf16 v[168:171], a[80:83], a[40:43], v[168:171]\n" \
"v_mfma_f32_16x16x32_bf16 v[168:171], a[84:87], a[44:47], v[168:171]\n" \
"s_waitcnt vmcnt(18) lgkmcnt(0)\n" \
"v_mfma_f32_16x16x32_bf16 v[176:179], a[80:83], a[48:51], v[176:179]\n" \
"s_add_u32 m0, s50, 28672\n" \
"s_barrier\n" \
"v_mfma_f32_16x16x32_bf16 v[176:179], a[84:87], a[52:55], v[176:179]\n" \
"buffer_load_dwordx4 v20, s[28:31], s46 offen lds\n" \
"v_mfma_f32_16x16x32_bf16 v[184:187], a[80:83], a[56:59], v[184:187]\n" \
"v_mfma_f32_16x16x32_bf16 v[184:187], a[84:87], a[60:63], v[184:187]\n" \
"v_mfma_f32_16x16x32_bf16 v[132:135], a[88:91], a[0:3], v[132:135]\n" \
"s_mov_b32 s47, 3670016\n" \
"s_add_u32 m0, s22, 32768\n" \
"v_mfma_f32_16x16x32_bf16 v[132:135], a[92:95], a[4:7], v[132:135]\n" \
"buffer_load_dwordx4 v21, s[32:35], s47 offen offset:0 lds\n" \
"v_mfma_f32_16x16x32_bf16 v[140:143], a[88:91], a[8:11], v[140:143]\n" \
"v_mfma_f32_16x16x32_bf16 v[140:143], a[92:95], a[12:15], v[140:143]\n" \
"v_mfma_f32_16x16x32_bf16 v[148:151], a[88:91], a[16:19], v[148:151]\n" \
"s_add_u32 m0, s22, 34560\n" \
"v_mfma_f32_16x16x32_bf16 v[148:151], a[92:95], a[20:23], v[148:151]\n" \
"buffer_load_dwordx4 v21, s[32:35], s47 offen offset:256 lds\n" \
"v_mfma_f32_16x16x32_bf16 v[156:159], a[88:91], a[24:27], v[156:159]\n" \
"v_mfma_f32_16x16x32_bf16 v[156:159], a[92:95], a[28:31], v[156:159]\n" \
"v_mfma_f32_16x16x32_bf16 v[164:167], a[88:91], a[32:35], v[164:167]\n" \
"s_mov_b32 s47, 3932160\n" \
"s_add_u32 m0, s22, 33792\n" \
"v_mfma_f32_16x16x32_bf16 v[164:167], a[92:95], a[36:39], v[164:167]\n" \
"buffer_load_dwordx4 v21, s[32:35], s47 offen offset:0 lds\n" \
"v_mfma_f32_16x16x32_bf16 v[172:175], a[88:91], a[40:43], v[172:175]\n" \
"v_mfma_f32_16x16x32_bf16 v[172:175], a[92:95], a[44:47], v[172:175]\n" \
"v_mfma_f32_16x16x32_bf16 v[180:183], a[88:91], a[48:51], v[180:183]\n" \
"s_add_u32 m0, s22, 35584\n" \
"v_mfma_f32_16x16x32_bf16 v[180:183], a[92:95], a[52:55], v[180:183]\n" \
"buffer_load_dwordx4 v21, s[32:35], s47 offen offset:256 lds\n" \
"v_mfma_f32_16x16x32_bf16 v[188:191], a[88:91], a[56:59], v[188:191]\n" \
"v_mfma_f32_16x16x32_bf16 v[188:191], a[92:95], a[60:63], v[188:191]\n" \
"v_mfma_f32_16x16x32_bf16 v[192:195], a[96:99], a[0:3], v[192:195]\n" \
"v_mfma_f32_16x16x32_bf16 v[192:195], a[100:103], a[4:7], v[192:195]\n" \
"buffer_load_dwordx4 v[30:33], v23, s[68:71], 0 offen offset:0 nt\n" \
"v_mfma_f32_16x16x32_bf16 v[200:203], a[96:99], a[8:11], v[200:203]\n" \
"s_mov_b64 s[28:29], s[64:65]\n" \
"v_mfma_f32_16x16x32_bf16 v[200:203], a[100:103], a[12:15], v[200:203]\n" \
"v_mfma_f32_16x16x32_bf16 v[208:211], a[96:99], a[16:19], v[208:211]\n" \
"ds_read_b64_tr_b16 a[64:65], v9 offset:0\n" \
"v_mfma_f32_16x16x32_bf16 v[208:211], a[100:103], a[20:23], v[208:211]\n" \
"buffer_load_dwordx4 v[34:37], v24, s[68:71], 0 offen offset:0 nt\n" \
"v_mfma_f32_16x16x32_bf16 v[216:219], a[96:99], a[24:27], v[216:219]\n" \
"s_and_b32 s29, s29, 0xffff\n" \
"ds_read_b64_tr_b16 a[66:67], v10 offset:0\n" \
"v_mfma_f32_16x16x32_bf16 v[216:219], a[100:103], a[28:31], v[216:219]\n" \
"v_mfma_f32_16x16x32_bf16 v[224:227], a[96:99], a[32:35], v[224:227]\n" \
"ds_read_b64_tr_b16 a[68:69], v9 offset:1024\n" \
"v_mfma_f32_16x16x32_bf16 v[224:227], a[100:103], a[36:39], v[224:227]\n" \
"buffer_load_dwordx4 v[38:41], v24, s[68:71], 0 offen offset:1024 nt\n" \
"v_mfma_f32_16x16x32_bf16 v[232:235], a[96:99], a[40:43], v[232:235]\n" \
"s_mov_b32 s30, 4194304\n" \
"ds_read_b64_tr_b16 a[70:71], v10 offset:1024\n" \
"v_mfma_f32_16x16x32_bf16 v[232:235], a[100:103], a[44:47], v[232:235]\n" \
"v_mfma_f32_16x16x32_bf16 v[240:243], a[96:99], a[48:51], v[240:243]\n" \
"ds_read_b64_tr_b16 a[72:73], v11 offset:0\n" \
"v_mfma_f32_16x16x32_bf16 v[240:243], a[100:103], a[52:55], v[240:243]\n" \
"buffer_load_dwordx4 v[42:45], v24, s[68:71], 0 offen offset:2048 nt\n" \
"v_mfma_f32_16x16x32_bf16 v[248:251], a[96:99], a[56:59], v[248:251]\n" \
"s_lshl_b32 s60, s7, 18\n" \
"ds_read_b64_tr_b16 a[74:75], v12 offset:0\n" \
"v_mfma_f32_16x16x32_bf16 v[248:251], a[100:103], a[60:63], v[248:251]\n" \
"LH2_%=:\ns_cbranch_execz LH10_%=\n" \
"v_mfma_f32_16x16x32_bf16 v[196:199], a[104:107], a[0:3], v[196:199]\n" \
"ds_read_b64_tr_b16 a[76:77], v11 offset:1024\n" \
"v_mfma_f32_16x16x32_bf16 v[196:199], a[108:111], a[4:7], v[196:199]\n" \
"buffer_load_dwordx4 v[46:49], v24, s[68:71], 0 offen offset:3072 nt\n" \
"v_mfma_f32_16x16x32_bf16 v[204:207], a[104:107], a[8:11], v[204:207]\n" \
"s_sub_u32 s61, 3932160, s60\n" \
"ds_read_b64_tr_b16 a[78:79], v12 offset:1024\n" \
"v_mfma_f32_16x16x32_bf16 v[204:207], a[108:111], a[12:15], v[204:207]\n" \
"v_mfma_f32_16x16x32_bf16 v[212:215], a[104:107], a[16:19], v[212:215]\n" \
"ds_read_b64_tr_b16 a[80:81], v9 offset:512\n" \
"v_mfma_f32_16x16x32_bf16 v[212:215], a[108:111], a[20:23], v[212:215]\n" \
"buffer_load_dword v50, v25, s[68:71], 0 offen offset:0\n" \
"v_mfma_f32_16x16x32_bf16 v[220:223], a[104:107], a[24:27], v[220:223]\n" \
"ds_read_b64_tr_b16 a[82:83], v10 offset:512\n" \
"v_add_u32 v13, s60, v63\n" \
"v_mfma_f32_16x16x32_bf16 v[220:223], a[108:111], a[28:31], v[220:223]\n" \
"v_mfma_f32_16x16x32_bf16 v[228:231], a[104:107], a[32:35], v[228:231]\n" \
"ds_read_b64_tr_b16 a[84:85], v9 offset:1536\n" \
"v_mfma_f32_16x16x32_bf16 v[228:231], a[108:111], a[36:39], v[228:231]\n" \
"buffer_load_dword v51, v24, s[68:71], 0 offen offset:16\n" \
"v_mfma_f32_16x16x32_bf16 v[236:239], a[104:107], a[40:43], v[236:239]\n" \
"ds_read_b64_tr_b16 a[86:87], v10 offset:1536\n" \
"v_add_u32 v17, s61, v63\n" \
"v_mfma_f32_16x16x32_bf16 v[236:239], a[108:111], a[44:47], v[236:239]\n" \
"v_mfma_f32_16x16x32_bf16 v[244:247], a[104:107], a[48:51], v[244:247]\n" \
"ds_read_b64_tr_b16 a[88:89], v11 offset:512\n" \
"v_mfma_f32_16x16x32_bf16 v[244:247], a[108:111], a[52:55], v[244:247]\n" \
"buffer_load_dword v52, v24, s[68:71], 0 offen offset:1040\n" \
"v_mfma_f32_16x16x32_bf16 v[252:255], a[104:107], a[56:59], v[252:255]\n" \
"ds_read_b64_tr_b16 a[90:91], v12 offset:512\n" \
"v_add_u32 v14, 0x10000, v13\n" \
"v_mfma_f32_16x16x32_bf16 v[252:255], a[108:111], a[60:63], v[252:255]\n" \
"v_mfma_f32_16x16x32_bf16 a[128:131], a[112:115], a[0:3], a[128:131]\n" \
"ds_read_b64_tr_b16 a[92:93], v11 offset:1536\n" \
"v_mfma_f32_16x16x32_bf16 a[128:131], a[116:119], a[4:7], a[128:131]\n" \
"buffer_load_dword v53, v24, s[68:71], 0 offen offset:2064\n" \
"v_mfma_f32_16x16x32_bf16 a[136:139], a[112:115], a[8:11], a[136:139]\n" \
"ds_read_b64_tr_b16 a[94:95], v12 offset:1536\n" \
"v_add_u32 v18, 0x10000, v17\n" \
"v_mfma_f32_16x16x32_bf16 a[136:139], a[116:119], a[12:15], a[136:139]\n" \
"v_mfma_f32_16x16x32_bf16 a[144:147], a[112:115], a[16:19], a[144:147]\n" \
"v_mfma_f32_16x16x32_bf16 a[144:147], a[116:119], a[20:23], a[144:147]\n" \
"buffer_load_dword v54, v24, s[68:71], 0 offen offset:3088\n" \
"v_mfma_f32_16x16x32_bf16 a[152:155], a[112:115], a[24:27], a[152:155]\n" \
"v_add_u32 v15, 0x10000, v14\n" \
"v_mfma_f32_16x16x32_bf16 a[152:155], a[116:119], a[28:31], a[152:155]\n" \
"v_mfma_f32_16x16x32_bf16 a[160:163], a[112:115], a[32:35], a[160:163]\n" \
"v_mfma_f32_16x16x32_bf16 a[160:163], a[116:119], a[36:39], a[160:163]\n" \
"global_load_short_d16_hi v28, v29, s[12:13]\n" \
"v_mfma_f32_16x16x32_bf16 a[168:171], a[112:115], a[40:43], a[168:171]\n" \
"v_add_u32 v19, 0x10000, v18\n" \
"v_mfma_f32_16x16x32_bf16 a[168:171], a[116:119], a[44:47], a[168:171]\n" \
"s_waitcnt vmcnt(23) lgkmcnt(0)\n" \
"v_mfma_f32_16x16x32_bf16 a[176:179], a[112:115], a[48:51], a[176:179]\n" \
"s_barrier\n" \
"v_mfma_f32_16x16x32_bf16 a[176:179], a[116:119], a[52:55], a[176:179]\n" \
"global_load_dwordx4 a[196:199], v4, s[86:87] offset:1024\n" \
"v_mfma_f32_16x16x32_bf16 a[184:187], a[112:115], a[56:59], a[184:187]\n" \
"v_add_u32 v16, 0x10000, v15\n" \
"v_mfma_f32_16x16x32_bf16 a[184:187], a[116:119], a[60:63], a[184:187]\n" \
"v_mfma_f32_16x16x32_bf16 a[132:135], a[120:123], a[0:3], a[132:135]\n" \
"v_mfma_f32_16x16x32_bf16 a[132:135], a[124:127], a[4:7], a[132:135]\n" \
"v_mfma_f32_16x16x32_bf16 a[140:143], a[120:123], a[8:11], a[140:143]\n" \
"ds_read_b128 a[0:3], v7 offset:0\n" \
"v_add_u32 v20, 0x10000, v19\n" \
"v_mfma_f32_16x16x32_bf16 a[140:143], a[124:127], a[12:15], a[140:143]\n" \
"ds_read_b128 a[4:7], v8 offset:0\n" \
"v_mfma_f32_16x16x32_bf16 a[148:151], a[120:123], a[16:19], a[148:151]\n" \
"ds_read_b128 a[8:11], v7 offset:2048\n" \
"v_mfma_f32_16x16x32_bf16 a[148:151], a[124:127], a[20:23], a[148:151]\n" \
"ds_read_b128 a[12:15], v8 offset:2048\n" \
"v_mfma_f32_16x16x32_bf16 a[156:159], a[120:123], a[24:27], a[156:159]\n" \
"ds_read_b128 a[16:19], v7 offset:4096\n" \
"v_mfma_f32_16x16x32_bf16 a[156:159], a[124:127], a[28:31], a[156:159]\n" \
"ds_read_b128 a[20:23], v8 offset:4096\n" \
"v_mfma_f32_16x16x32_bf16 a[164:167], a[120:123], a[32:35], a[164:167]\n" \
"ds_read_b128 a[24:27], v7 offset:6144\n" \
"v_mfma_f32_16x16x32_bf16 a[164:167], a[124:127], a[36:39], a[164:167]\n" \
"ds_read_b128 a[28:31], v8 offset:6144\n" \
"v_mfma_f32_16x16x32_bf16 a[172:175], a[120:123], a[40:43], a[172:175]\n" \
"ds_read_b128 a[32:35], v7 offset:8192\n" \
"v_mfma_f32_16x16x32_bf16 a[172:175], a[124:127], a[44:47], a[172:175]\n" \
"ds_read_b128 a[36:39], v8 offset:8192\n" \
"v_mfma_f32_16x16x32_bf16 a[180:183], a[120:123], a[48:51], a[180:183]\n" \
"ds_read_b128 a[40:43], v7 offset:10240\n" \
"v_mfma_f32_16x16x32_bf16 a[180:183], a[124:127], a[52:55], a[180:183]\n" \
"ds_read_b128 a[44:47], v8 offset:10240\n" \
"v_mfma_f32_16x16x32_bf16 a[188:191], a[120:123], a[56:59], a[188:191]\n" \
"ds_read_b128 a[48:51], v7 offset:12288\n" \
"v_mfma_f32_16x16x32_bf16 a[188:191], a[124:127], a[60:63], a[188:191]\n" \
"s_waitcnt lgkmcnt(9)\n" \
"ds_read_b128 a[52:55], v8 offset:12288\n" \
"v_mfma_f32_16x16x32_bf16 v[64:67], a[64:67], a[0:3], v[64:67]\n" \
"ds_read_b128 a[56:59], v7 offset:14336\n" \
"v_mfma_f32_16x16x32_bf16 v[64:67], a[68:71], a[4:7], v[64:67]\n" \
"buffer_load_dwordx2 v[58:59], v57, s[36:39], 0 offen sc1\n" \
"v_mfma_f32_16x16x32_bf16 v[72:75], a[64:67], a[8:11], v[72:75]\n" \
"s_waitcnt lgkmcnt(7)\n" \
"ds_read_b128 a[60:63], v8 offset:14336\n" \
"v_mfma_f32_16x16x32_bf16 v[72:75], a[68:71], a[12:15], v[72:75]\n" \
"v_mfma_f32_16x16x32_bf16 v[80:83], a[64:67], a[16:19], v[80:83]\n" \
"s_mov_b32 s47, 3670016\n" \
"s_add_u32 m0, s22, 49024\n" \
"ds_read_b64_tr_b16 a[96:97], v9 offset:16384\n" \
"v_mfma_f32_16x16x32_bf16 v[80:83], a[68:71], a[20:23], v[80:83]\n" \
"buffer_load_dwordx4 v21, s[32:35], s47 offen offset:128 lds\n" \
"v_mfma_f32_16x16x32_bf16 v[88:91], a[64:67], a[24:27], v[88:91]\n" \
"s_waitcnt lgkmcnt(5)\n" \
"ds_read_b64_tr_b16 a[98:99], v10 offset:16384\n" \
"v_mfma_f32_16x16x32_bf16 v[88:91], a[68:71], a[28:31], v[88:91]\n" \
"v_mfma_f32_16x16x32_bf16 v[96:99], a[64:67], a[32:35], v[96:99]\n" \
"s_add_u32 m0, s22, 50816\n" \
"ds_read_b64_tr_b16 a[100:101], v9 offset:17408\n" \
"v_mfma_f32_16x16x32_bf16 v[96:99], a[68:71], a[36:39], v[96:99]\n" \
"buffer_load_dwordx4 v21, s[32:35], s47 offen offset:384 lds\n" \
"v_mfma_f32_16x16x32_bf16 v[104:107], a[64:67], a[40:43], v[104:107]\n" \
"s_waitcnt lgkmcnt(3)\n" \
"ds_read_b64_tr_b16 a[102:103], v10 offset:17408\n" \
"v_mfma_f32_16x16x32_bf16 v[104:107], a[68:71], a[44:47], v[104:107]\n" \
"v_mfma_f32_16x16x32_bf16 v[112:115], a[64:67], a[48:51], v[112:115]\n" \
"s_mov_b32 s47, 3932160\n" \
"s_add_u32 m0, s22, 50048\n" \
"ds_read_b64_tr_b16 a[104:105], v11 offset:16384\n" \
"v_mfma_f32_16x16x32_bf16 v[112:115], a[68:71], a[52:55], v[112:115]\n" \
"buffer_load_dwordx4 v21, s[32:35], s47 offen offset:128 lds\n" \
"v_mfma_f32_16x16x32_bf16 v[120:123], a[64:67], a[56:59], v[120:123]\n" \
"ds_read_b64_tr_b16 a[106:107], v12 offset:16384\n" \
"v_mfma_f32_16x16x32_bf16 v[120:123], a[68:71], a[60:63], v[120:123]\n" \
"s_add_u32 s76, s76, 2097152\ns_addc_u32 s77, s77, 0\n" \
"v_mfma_f32_16x16x32_bf16 v[68:71], a[72:75], a[0:3], v[68:71]\n" \
"s_add_u32 m0, s22, 51840\n" \
"ds_read_b64_tr_b16 a[108:109], v11 offset:17408\n" \
"v_mfma_f32_16x16x32_bf16 v[68:71], a[76:79], a[4:7], v[68:71]\n" \
"buffer_load_dwordx4 v21, s[32:35], s47 offen offset:384 lds\n" \
"v_mfma_f32_16x16x32_bf16 v[76:79], a[72:75], a[8:11], v[76:79]\n" \
"ds_read_b64_tr_b16 a[110:111], v12 offset:17408\n" \
"v_mfma_f32_16x16x32_bf16 v[76:79], a[76:79], a[12:15], v[76:79]\n" \
"v_mfma_f32_16x16x32_bf16 v[84:87], a[72:75], a[16:19], v[84:87]\n" \
"ds_read_b64_tr_b16 a[112:113], v9 offset:16896\n" \
"v_mfma_f32_16x16x32_bf16 v[84:87], a[76:79], a[20:23], v[84:87]\n" \
"v_mfma_f32_16x16x32_bf16 v[92:95], a[72:75], a[24:27], v[92:95]\n" \
"ds_read_b64_tr_b16 a[114:115], v10 offset:16896\n" \
"v_mfma_f32_16x16x32_bf16 v[92:95], a[76:79], a[28:31], v[92:95]\n" \
"s_mov_b64 exec, 0xffff\nglobal_load_dword v22, v1, s[76:77]\ns_mov_b64 exec, -1\n" \
"v_mfma_f32_16x16x32_bf16 v[100:103], a[72:75], a[32:35], v[100:103]\n" \
"s_add_u32 s76, s76, 2097152\ns_addc_u32 s77, s77, 0\n" \
"ds_read_b64_tr_b16 a[116:117], v9 offset:17920\n" \
"v_mfma_f32_16x16x32_bf16 v[100:103], a[76:79], a[36:39], v[100:103]\n" \
"v_mfma_f32_16x16x32_bf16 v[108:111], a[72:75], a[40:43], v[108:111]\n" \
"ds_read_b64_tr_b16 a[118:119], v10 offset:17920\n" \
"v_mfma_f32_16x16x32_bf16 v[108:111], a[76:79], a[44:47], v[108:111]\n" \
"s_mov_b64 exec, 0xffff\nglobal_load_dword v22, v1, s[76:77]\ns_mov_b64 exec, -1\n" \
"v_mfma_f32_16x16x32_bf16 v[116:119], a[72:75], a[48:51], v[116:119]\n" \
"ds_read_b64_tr_b16 a[120:121], v11 offset:16896\n" \
"v_mfma_f32_16x16x32_bf16 v[116:119], a[76:79], a[52:55], v[116:119]\n" \
"v_mfma_f32_16x16x32_bf16 v[124:127], a[72:75], a[56:59], v[124:127]\n" \
"ds_read_b64_tr_b16 a[122:123], v12 offset:16896\n" \
"v_mfma_f32_16x16x32_bf16 v[124:127], a[76:79], a[60:63], v[124:127]\n" \
"v_mfma_f32_16x16x32_bf16 v[128:131], a[80:83], a[0:3], v[128:131]\n" \
"ds_read_b64_tr_b16 a[124:125], v11 offset:17920\n" \
"v_mfma_f32_16x16x32_bf16 v[128:131], a[84:87], a[4:7], v[128:131]\n" \
"v_mfma_f32_16x16x32_bf16 v[136:139], a[80:83], a[8:11], v[136:139]\n" \
"ds_read_b64_tr_b16 a[126:127], v12 offset:17920\n" \
"v_mfma_f32_16x16x32_bf16 v[136:139], a[84:87], a[12:15], v[136:139]\n" \
"v_mfma_f32_16x16x32_bf16 v[144:147], a[80:83], a[16:19], v[144:147]\n" \
"v_mfma_f32_16x16x32_bf16 v[144:147], a[84:87], a[20:23], v[144:147]\n" \
"v_mfma_f32_16x16x32_bf16 v[152:155], a[80:83], a[24:27], v[152:155]\n" \
"v_mfma_f32_16x16x32_bf16 v[152:155], a[84:87], a[28:31], v[152:155]\n" \
"v_mfma_f32_16x16x32_bf16 v[160:163], a[80:83], a[32:35], v[160:163]\n" \
"v_mfma_f32_16x16x32_bf16 v[160:163], a[84:87], a[36:39], v[160:163]\n" \
"v_mfma_f32_16x16x32_bf16 v[168:171], a[80:83], a[40:43], v[168:171]\n" \
"v_mfma_f32_16x16x32_bf16 v[168:171], a[84:87], a[44:47], v[168:171]\n" \
"s_waitcnt vmcnt(19) lgkmcnt(0)\n" \
"v_mfma_f32_16x16x32_bf16 v[176:179], a[80:83], a[48:51], v[176:179]\n" \
"s_barrier\n" \
"v_mfma_f32_16x16x32_bf16 v[176:179], a[84:87], a[52:55], v[176:179]\n" \
"v_mfma_f32_16x16x32_bf16 v[184:187], a[80:83], a[56:59], v[184:187]\n" \
"v_mfma_f32_16x16x32_bf16 v[184:187], a[84:87], a[60:63], v[184:187]\n" \
"v_mfma_f32_16x16x32_bf16 v[132:135], a[88:91], a[0:3], v[132:135]\n" \
"v_mfma_f32_16x16x32_bf16 v[132:135], a[92:95], a[4:7], v[132:135]\n" \
"v_mfma_f32_16x16x32_bf16 v[140:143], a[88:91], a[8:11], v[140:143]\n" \
"v_mfma_f32_16x16x32_bf16 v[140:143], a[92:95], a[12:15], v[140:143]\n" \
"v_mfma_f32_16x16x32_bf16 v[148:151], a[88:91], a[16:19], v[148:151]\n" \
"v_mfma_f32_16x16x32_bf16 v[148:151], a[92:95], a[20:23], v[148:151]\n" \
"v_mfma_f32_16x16x32_bf16 v[156:159], a[88:91], a[24:27], v[156:159]\n" \
"v_mfma_f32_16x16x32_bf16 v[156:159], a[92:95], a[28:31], v[156:159]\n" \
"v_mfma_f32_16x16x32_bf16 v[164:167], a[88:91], a[32:35], v[164:167]\n" \
"v_mfma_f32_16x16x32_bf16 v[164:167], a[92:95], a[36:39], v[164:167]\n" \
"v_mfma_f32_16x16x32_bf16 v[172:175], a[88:91], a[40:43], v[172:175]\n" \
"v_mfma_f32_16x16x32_bf16 v[172:175], a[92:95], a[44:47], v[172:175]\n" \
"s_waitcnt vmcnt(6)\n" \
"v_mfma_f32_16x16x32_bf16 v[180:183], a[88:91], a[48:51], v[180:183]\n" \
"v_cmp_ne_u64 vcc, s[14:15], v[58:59]\n" \
"v_mfma_f32_16x16x32_bf16 v[180:183], a[92:95], a[52:55], v[180:183]\n" \
"v_mfma_f32_16x16x32_bf16 v[188:191], a[88:91], a[56:59], v[188:191]\n" \
"s_cbranch_vccnz LPUK_%=\nLPUKR_%=:\n" \
"v_mfma_f32_16x16x32_bf16 v[188:191], a[92:95], a[60:63], v[188:191]\n" \
"v_mfma_f32_16x16x32_bf16 v[192:195], a[96:99], a[0:3], v[192:195]\n" \
"s_add_u32 m0, s56, 10304\n" \
"v_mfma_f32_16x16x32_bf16 v[192:195], a[100:103], a[4:7], v[192:195]\n" \
"buffer_load_dwordx4 v26, s[72:75], 0 offen offset:0 sc1 lds\n" \
"v_mfma_f32_16x16x32_bf16 v[200:203], a[96:99], a[8:11], v[200:203]\n" \
"s_mov_b64 s[32:33], s[44:45]\n" \
"v_mfma_f32_16x16x32_bf16 v[200:203], a[100:103], a[12:15], v[200:203]\n" \
"v_mfma_f32_16x16x32_bf16 v[208:211], a[96:99], a[16:19], v[208:211]\n" \
"s_add_u32 m0, s56, 11328\n" \
"ds_read_b64_tr_b16 a[64:65], v9 offset:32768\n" \
"s_waitcnt vmcnt(9)\n" \
"v_mfma_f32_16x16x32_bf16 v[208:211], a[100:103], a[20:23], v[208:211]\n" \
"buffer_load_dwordx4 v27, s[72:75], 0 offen offset:0 sc1 lds\n" \
"v_mfma_f32_16x16x32_bf16 v[216:219], a[96:99], a[24:27], v[216:219]\n" \
"s_mov_b32 s34, s49\n" \
"ds_read_b64_tr_b16 a[66:67], v10 offset:32768\n" \
"v_alignbyte_b32 v30, v31, v30, 2\n" \
"v_mfma_f32_16x16x32_bf16 v[216:219], a[100:103], a[28:31], v[216:219]\n" \
"v_alignbyte_b32 v31, v32, v31, 2\n" \
"v_alignbyte_b32 v32, v33, v32, 2\n" \
"v_mfma_f32_16x16x32_bf16 v[224:227], a[96:99], a[32:35], v[224:227]\n" \
"s_add_u32 m0, s56, 11328\n" \
"ds_read_b64_tr_b16 a[68:69], v9 offset:33792\n" \
"v_alignbyte_b32 v33, v50, v33, 2\n" \
"v_mfma_f32_16x16x32_bf16 v[224:227], a[100:103], a[36:39], v[224:227]\n" \
"buffer_load_dwordx4 v27, s[72:75], 0 offen offset:1024 sc1 lds\n" \
"v_mfma_f32_16x16x32_bf16 v[232:235], a[96:99], a[40:43], v[232:235]\n" \
"ds_read_b64_tr_b16 a[70:71], v10 offset:33792\n" \
"v_mfma_f32_16x16x32_bf16 v[232:235], a[100:103], a[44:47], v[232:235]\n" \
"ds_write_b128 v55, v[30:33] offset:5184\n" \
"v_alignbyte_b32 v34, v35, v34, 2\n" \
"v_mfma_f32_16x16x32_bf16 v[240:243], a[96:99], a[48:51], v[240:243]\n" \
"s_add_u32 m0, s56, 11328\n" \
"ds_read_b64_tr_b16 a[72:73], v11 offset:32768\n" \
"v_alignbyte_b32 v35, v36, v35, 2\n" \
"v_mfma_f32_16x16x32_bf16 v[240:243], a[100:103], a[52:55], v[240:243]\n" \
"buffer_load_dwordx4 v27, s[72:75], 0 offen offset:2048 sc1 lds\n" \
"v_mfma_f32_16x16x32_bf16 v[248:251], a[96:99], a[56:59], v[248:251]\n" \
"ds_read_b64_tr_b16 a[74:75], v12 offset:32768\n" \
"v_alignbyte_b32 v36, v37, v36, 2\n" \
"v_mfma_f32_16x16x32_bf16 v[248:251], a[100:103], a[60:63], v[248:251]\n" \
"v_alignbyte_b32 v37, v51, v37, 2\n" \
"ds_write_b128 v55, v[34:37] offset:6208\n" \
"v_mfma_f32_16x16x32_bf16 v[196:199], a[104:107], a[0:3], v[196:199]\n" \
"s_add_u32 m0, s56, 11328\n" \
"ds_read_b64_tr_b16 a[76:77], v11 offset:33792\n" \
"v_alignbyte_b32 v38, v39, v38, 2\n" \
"v_mfma_f32_16x16x32_bf16 v[196:199], a[108:111], a[4:7], v[196:199]\n" \
"buffer_load_dwordx4 v27, s[72:75], 0 offen offset:3072 sc1 lds\n" \
"v_mfma_f32_16x16x32_bf16 v[204:207], a[104:107], a[8:11], v[204:207]\n" \
"ds_read_b64_tr_b16 a[78:79], v12 offset:33792\n" \
"v_alignbyte_b32 v39, v40, v39, 2\n" \
"v_mfma_f32_16x16x32_bf16 v[204:207], a[108:111], a[12:15], v[204:207]\n" \
"v_alignbyte_b32 v40, v41, v40, 2\n" \
"v_alignbyte_b32 v41, v52, v41, 2\n" \
"v_mfma_f32_16x16x32_bf16 v[212:215], a[104:107], a[16:19], v[212:215]\n" \
"s_add_u32 m0, s56, 0\n" \
"ds_read_b64_tr_b16 a[80:81], v9 offset:33280\n" \
"v_mfma_f32_16x16x32_bf16 v[212:215], a[108:111], a[20:23], v[212:215]\n" \
"buffer_load_dwordx4 v23, s[68:71], 0 offen offset:0 lds\n" \
"v_mfma_f32_16x16x32_bf16 v[220:223], a[104:107], a[24:27], v[220:223]\n" \
"ds_read_b64_tr_b16 a[82:83], v10 offset:33280\n" \
"v_mfma_f32_16x16x32_bf16 v[220:223], a[108:111], a[28:31], v[220:223]\n" \
"ds_write_b128 v55, v[38:41] offset:7232\n" \
"v_alignbyte_b32 v42, v43, v42, 2\n" \
"v_mfma_f32_16x16x32_bf16 v[228:231], a[104:107], a[32:35], v[228:231]\n" \
"s_add_u32 m0, s56, 1024\n" \
"ds_read_b64_tr_b16 a[84:85], v9 offset:34304\n" \
"v_alignbyte_b32 v43, v44, v43, 2\n" \
"v_mfma_f32_16x16x32_bf16 v[228:231], a[108:111], a[36:39], v[228:231]\n" \
"buffer_load_dwordx4 v24, s[68:71], 0 offen offset:0 lds\n" \
"v_mfma_f32_16x16x32_bf16 v[236:239], a[104:107], a[40:43], v[236:239]\n" \
"ds_read_b64_tr_b16 a[86:87], v10 offset:34304\n" \
"v_alignbyte_b32 v44, v45, v44, 2\n" \
"v_mfma_f32_16x16x32_bf16 v[236:239], a[108:111], a[44:47], v[236:239]\n" \
"v_alignbyte_b32 v45, v53, v45, 2\n" \
"ds_write_b128 v55, v[42:45] offset:8256\n" \
"v_mfma_f32_16x16x32_bf16 v[244:247], a[104:107], a[48:51], v[244:247]\n" \
"s_add_u32 m0, s56, 1024\n" \
"ds_read_b64_tr_b16 a[88:89], v11 offset:33280\n" \
"v_alignbyte_b32 v46, v47, v46, 2\n" \
"v_mfma_f32_16x16x32_bf16 v[244:247], a[108:111], a[52:55], v[244:247]\n" \
"buffer_load_dwordx4 v24, s[68:71], 0 offen offset:1024 lds\n" \
"v_mfma_f32_16x16x32_bf16 v[252:255], a[104:107], a[56:59], v[252:255]\n" \
"ds_read_b64_tr_b16 a[90:91], v12 offset:33280\n" \
"v_alignbyte_b32 v47, v48, v47, 2\n" \
"v_mfma_f32_16x16x32_bf16 v[252:255], a[108:111], a[60:63], v[252:255]\n" \
"v_alignbyte_b32 v48, v49, v48, 2\n" \
"v_alignbyte_b32 v49, v54, v49, 2\n" \
"v_mfma_f32_16x16x32_bf16 a[128:131], a[112:115], a[0:3], a[128:131]\n" \
"s_add_u32 m0, s56, 1024\n" \
"ds_read_b64_tr_b16 a[92:93], v11 offset:34304\n" \
"v_mfma_f32_16x16x32_bf16 a[128:131], a[116:119], a[4:7], a[128:131]\n" \
"buffer_load_dwordx4 v24, s[68:71], 0 offen offset:2048 lds\n" \
"v_mfma_f32_16x16x32_bf16 a[136:139], a[112:115], a[8:11], a[136:139]\n" \
"ds_read_b64_tr_b16 a[94:95], v12 offset:34304\n" \
"v_mfma_f32_16x16x32_bf16 a[136:139], a[116:119], a[12:15], a[136:139]\n" \
"ds_write_b128 v55, v[46:49] offset:9280\n" \
"v_lshrrev_b32 v30, 4, v4\n" \
"v_mfma_f32_16x16x32_bf16 a[144:147], a[112:115], a[16:19], a[144:147]\n" \
"s_add_u32 m0, s56, 1024\n" \
"v_and_b32 v31, 15, v30\n" \
"v_lshrrev_b32 v32, 4, v30\n" \
"v_mfma_f32_16x16x32_bf16 a[144:147], a[116:119], a[20:23], a[144:147]\n" \
"buffer_load_dwordx4 v24, s[68:71], 0 offen offset:3072 lds\n" \
"v_mfma_f32_16x16x32_bf16 a[152:155], a[112:115], a[24:27], a[152:155]\n" \
"v_add_u32 v33, 1, v31\n" \
"v_and_b32 v34, 1, v33\n" \
"v_mfma_f32_16x16x32_bf16 a[152:155], a[116:119], a[28:31], a[152:155]\n" \
"v_and_b32 v33, 30, v33\n" \
"v_mul_u32_u24 v34, 5184, v34\n" \
"v_mfma_f32_16x16x32_bf16 a[160:163], a[112:115], a[32:35], a[160:163]\n" \
"s_mov_b32 s46, 0\n" \
"s_add_u32 m0, s50, 65536\n" \
"v_lshl_add_u32 v33, v33, 1, v34\n" \
"v_mfma_f32_16x16x32_bf16 a[160:163], a[116:119], a[36:39], a[160:163]\n" \
"buffer_load_dwordx4 v13, s[28:31], s46 offen lds\n" \
"v_mfma_f32_16x16x32_bf16 a[168:171], a[112:115], a[40:43], a[168:171]\n" \
"v_lshl_add_u32 v33, v32, 4, v33\n" \
"v_add_u32 v46, s56, v33\n" \
"v_mfma_f32_16x16x32_bf16 a[168:171], a[116:119], a[44:47], a[168:171]\n" \
"s_waitcnt vmcnt(13) lgkmcnt(0)\n" \
"v_add_u32 v47, 1024, v46\n" \
"v_add_u32 v48, 2048, v46\n" \
"v_mfma_f32_16x16x32_bf16 a[176:179], a[112:115], a[48:51], a[176:179]\n" \
"s_add_u32 m0, s50, 69632\n" \
"s_barrier\n" \
"v_add_u32 v49, 3072, v46\n" \
"v_mfma_f32_16x16x32_bf16 a[176:179], a[116:119], a[52:55], a[176:179]\n" \
"buffer_load_dwordx4 v14, s[28:31], s46 offen lds\n" \
"v_mfma_f32_16x16x32_bf16 a[184:187], a[112:115], a[56:59], a[184:187]\n" \
"v_lshlrev_b32 v33, 1, v31\n" \
"v_sub_u32 v33, v33, v32\n" \
"v_mfma_f32_16x16x32_bf16 a[184:187], a[116:119], a[60:63], a[184:187]\n" \
"v_lshlrev_b32 v33, 4, v33\n" \
"v_add_u32 v33, 10320, v33\n" \
"v_mfma_f32_16x16x32_bf16 a[132:135], a[120:123], a[0:3], a[132:135]\n" \
"s_add_u32 m0, s50, 73728\n" \
"v_add_u32 v50, s56, v33\n" \
"v_mfma_f32_16x16x32_bf16 a[132:135], a[124:127], a[4:7], a[132:135]\n" \
"buffer_load_dwordx4 v15, s[28:31], s46 offen lds\n" \
"v_mfma_f32_16x16x32_bf16 a[140:143], a[120:123], a[8:11], a[140:143]\n" \
"ds_read_b128 a[0:3], v5 offset:0\n" \
"v_lshlrev_b32 v33, 5, v31\n" \
"v_mfma_f32_16x16x32_bf16 a[140:143], a[124:127], a[12:15], a[140:143]\n" \
"ds_read_b128 a[4:7], v6 offset:0\n" \
"v_lshrrev_b32 v55, 2, v30\n" \
"v_mfma_f32_16x16x32_bf16 a[148:151], a[120:123], a[16:19], a[148:151]\n" \
"ds_read_b128 a[8:11], v5 offset:2048\n" \
"v_and_b32 v55, 8, v55\n" \
"v_mfma_f32_16x16x32_bf16 a[148:151], a[124:127], a[20:23], a[148:151]\n" \
"ds_read_b128 a[12:15], v6 offset:2048\n" \
"v_xor_b32 v55, v30, v55\n" \
"v_mfma_f32_16x16x32_bf16 a[156:159], a[120:123], a[24:27], a[156:159]\n" \
"ds_read_b128 a[16:19], v5 offset:4096\n" \
"v_lshl_add_u32 v55, v55, 3, s22\n" \
"v_mfma_f32_16x16x32_bf16 a[156:159], a[124:127], a[28:31], a[156:159]\n" \
"ds_read_b128 a[20:23], v6 offset:4096\n" \
"v_add_u32 v33, s56, v33\n" \
"v_mfma_f32_16x16x32_bf16 a[164:167], a[120:123], a[32:35], a[164:167]\n" \
"ds_read_b128 a[24:27], v5 offset:6144\n" \
"v_xor_b32 v32, 1, v32\n" \
"v_mfma_f32_16x16x32_bf16 a[164:167], a[124:127], a[36:39], a[164:167]\n" \
"ds_read_b128 a[28:31], v6 offset:6144\n" \
"v_lshl_add_u32 v51, v32, 3, v33\n" \
"v_mfma_f32_16x16x32_bf16 a[172:175], a[120:123], a[40:43], a[172:175]\n" \
"ds_read_b128 a[32:35], v5 offset:8192\n" \
"v_mfma_f32_16x16x32_bf16 a[172:175], a[124:127], a[44:47], a[172:175]\n" \
"ds_read_b128 a[36:39], v6 offset:8192\n" \
"v_mfma_f32_16x16x32_bf16 a[180:183], a[120:123], a[48:51], a[180:183]\n" \
"ds_read_b128 a[40:43], v5 offset:10240\n" \
"v_mfma_f32_16x16x32_bf16 a[180:183], a[124:127], a[52:55], a[180:183]\n" \
"ds_read_b128 a[44:47], v6 offset:10240\n" \
"v_mfma_f32_16x16x32_bf16 a[188:191], a[120:123], a[56:59], a[188:191]\n" \
"ds_read_b128 a[48:51], v5 offset:12288\n" \
"v_mfma_f32_16x16x32_bf16 a[188:191], a[124:127], a[60:63], a[188:191]\n" \
"s_waitcnt lgkmcnt(9)\n" \
"ds_read_b128 a[52:55], v6 offset:12288\n" \
"v_mfma_f32_16x16x32_bf16 v[64:67], a[64:67], a[0:3], v[64:67]\n" \
"s_add_u32 m0, s50, 77824\n" \
"ds_read_b128 a[56:59], v5 offset:14336\n" \
"v_mfma_f32_16x16x32_bf16 v[64:67], a[68:71], a[4:7], v[64:67]\n" \
"buffer_load_dwordx4 v16, s[28:31], s46 offen lds\n" \
"v_mfma_f32_16x16x32_bf16 v[72:75], a[64:67], a[8:11], v[72:75]\n" \
"s_waitcnt lgkmcnt(7)\n" \
"ds_read_b128 a[60:63], v6 offset:14336\n" \
"v_mfma_f32_16x16x32_bf16 v[72:75], a[68:71], a[12:15], v[72:75]\n" \
"v_mfma_f32_16x16x32_bf16 v[80:83], a[64:67], a[16:19], v[80:83]\n" \
"s_mov_b32 s46, 0\n" \
"s_add_u32 m0, s50, 81920\n" \
"ds_read_b64_tr_b16 a[96:97], v9 offset:49152\n" \
"v_mfma_f32_16x16x32_bf16 v[80:83], a[68:71], a[20:23], v[80:83]\n" \
"buffer_load_dwordx4 v17, s[28:31], s46 offen lds\n" \
"v_mfma_f32_16x16x32_bf16 v[88:91], a[64:67], a[24:27], v[88:91]\n" \
"s_waitcnt lgkmcnt(5)\n" \
"ds_read_b64_tr_b16 a[98:99], v10 offset:49152\n" \
"v_mfma_f32_16x16x32_bf16 v[88:91], a[68:71], a[28:31], v[88:91]\n" \
"v_mfma_f32_16x16x32_bf16 v[96:99], a[64:67], a[32:35], v[96:99]\n" \
"s_add_u32 m0, s50, 86016\n" \
"ds_read_b64_tr_b16 a[100:101], v9 offset:50176\n" \
"v_mfma_f32_16x16x32_bf16 v[96:99], a[68:71], a[36:39], v[96:99]\n" \
"buffer_load_dwordx4 v18, s[28:31], s46 offen lds\n" \
"v_mfma_f32_16x16x32_bf16 v[104:107], a[64:67], a[40:43], v[104:107]\n" \
"s_waitcnt lgkmcnt(3)\n" \
"ds_read_b64_tr_b16 a[102:103], v10 offset:50176\n" \
"v_mfma_f32_16x16x32_bf16 v[104:107], a[68:71], a[44:47], v[104:107]\n" \
"v_mfma_f32_16x16x32_bf16 v[112:115], a[64:67], a[48:51], v[112:115]\n" \
"s_add_u32 m0, s50, 90112\n" \
"ds_read_b64_tr_b16 a[104:105], v11 offset:49152\n" \
"v_mfma_f32_16x16x32_bf16 v[112:115], a[68:71], a[52:55], v[112:115]\n" \
"buffer_load_dwordx4 v19, s[28:31], s46 offen lds\n" \
"v_mfma_f32_16x16x32_bf16 v[120:123], a[64:67], a[56:59], v[120:123]\n" \
"ds_read_b64_tr_b16 a[106:107], v12 offset:49152\n" \
"v_mfma_f32_16x16x32_bf16 v[120:123], a[68:71], a[60:63], v[120:123]\n" \
"v_mfma_f32_16x16x32_bf16 v[68:71], a[72:75], a[0:3], v[68:71]\n" \
"s_add_u32 m0, s50, 94208\n" \
"ds_read_b64_tr_b16 a[108:109], v11 offset:50176\n" \
"v_mfma_f32_16x16x32_bf16 v[68:71], a[76:79], a[4:7], v[68:71]\n" \
"buffer_load_dwordx4 v20, s[28:31], s46 offen lds\n" \
"v_mfma_f32_16x16x32_bf16 v[76:79], a[72:75], a[8:11], v[76:79]\n" \
"ds_read_b64_tr_b16 a[110:111], v12 offset:50176\n" \
"v_mfma_f32_16x16x32_bf16 v[76:79], a[76:79], a[12:15], v[76:79]\n" \
"v_mfma_f32_16x16x32_bf16 v[84:87], a[72:75], a[16:19], v[84:87]\n" \
"s_mov_b32 s47, 0\n" \
"s_add_u32 m0, s51, 32768\n" \
"ds_read_b64_tr_b16 a[112:113], v9 offset:49664\n" \
"v_mfma_f32_16x16x32_bf16 v[84:87], a[76:79], a[20:23], v[84:87]\n" \
"buffer_load_dwordx4 v21, s[32:35], s47 offen offset:0 lds\n" \
"v_mfma_f32_16x16x32_bf16 v[92:95], a[72:75], a[24:27], v[92:95]\n" \
"ds_read_b64_tr_b16 a[114:115], v10 offset:49664\n" \
"v_mfma_f32_16x16x32_bf16 v[92:95], a[76:79], a[28:31], v[92:95]\n" \
"v_mfma_f32_16x16x32_bf16 v[100:103], a[72:75], a[32:35], v[100:103]\n" \
"s_add_u32 m0, s51, 33664\n" \
"ds_read_b64_tr_b16 a[116:117], v9 offset:50688\n" \
"v_mfma_f32_16x16x32_bf16 v[100:103], a[76:79], a[36:39], v[100:103]\n" \
"buffer_load_dwordx4 v21, s[32:35], s47 offen offset:128 lds\n" \
"v_mfma_f32_16x16x32_bf16 v[108:111], a[72:75], a[40:43], v[108:111]\n" \
"ds_read_b64_tr_b16 a[118:119], v10 offset:50688\n" \
"v_mfma_f32_16x16x32_bf16 v[108:111], a[76:79], a[44:47], v[108:111]\n" \
"v_mfma_f32_16x16x32_bf16 v[116:119], a[72:75], a[48:51], v[116:119]\n" \
"s_mov_b32 s47, 262144\n" \
"s_add_u32 m0, s51, 40960\n" \
"ds_read_b64_tr_b16 a[120:121], v11 offset:49664\n" \
"v_mfma_f32_16x16x32_bf16 v[116:119], a[76:79], a[52:55], v[116:119]\n" \
"buffer_load_dwordx4 v21, s[32:35], s47 offen offset:0 lds\n" \
"v_mfma_f32_16x16x32_bf16 v[124:127], a[72:75], a[56:59], v[124:127]\n" \
"ds_read_b64_tr_b16 a[122:123], v12 offset:49664\n" \
"v_mfma_f32_16x16x32_bf16 v[124:127], a[76:79], a[60:63], v[124:127]\n" \
"v_mfma_f32_16x16x32_bf16 v[128:131], a[80:83], a[0:3], v[128:131]\n" \
"s_add_u32 m0, s51, 41856\n" \
"ds_read_b64_tr_b16 a[124:125], v11 offset:50688\n" \
"v_mfma_f32_16x16x32_bf16 v[128:131], a[84:87], a[4:7], v[128:131]\n" \
"buffer_load_dwordx4 v21, s[32:35], s47 offen offset:128 lds\n" \
"v_mfma_f32_16x16x32_bf16 v[136:139], a[80:83], a[8:11], v[136:139]\n" \
"s_mov_b32 s96, s57\n" \
"ds_read_b64_tr_b16 a[126:127], v12 offset:50688\n" \
"v_mul_f32 v64, s20, v64\n" \
"v_mfma_f32_16x16x32_bf16 v[136:139], a[84:87], a[12:15], v[136:139]\n" \
"v_mul_f32 v65, s20, v65\n" \
"v_mul_f32 v66, s20, v66\n" \
"v_mfma_f32_16x16x32_bf16 v[144:147], a[80:83], a[16:19], v[144:147]\n" \
"v_mul_f32 v67, s20, v67\n" \
"v_cvt_pk_bf16_f32 v30, v64, v65\n" \
"v_mfma_f32_16x16x32_bf16 v[144:147], a[84:87], a[20:23], v[144:147]\n" \
"v_cvt_pk_bf16_f32 v31, v66, v67\n" \
"v_mfma_f32_16x16x32_bf16 v[152:155], a[80:83], a[24:27], v[152:155]\n" \
"v_mul_f32 v68, s20, v68\n" \
"v_mul_f32 v69, s20, v69\n" \
"v_mfma_f32_16x16x32_bf16 v[152:155], a[84:87], a[28:31], v[152:155]\n" \
"v_mul_f32 v70, s20, v70\n" \
"v_mul_f32 v71, s20, v71\n" \
"v_mfma_f32_16x16x32_bf16 v[160:163], a[80:83], a[32:35], v[160:163]\n" \
"v_cvt_pk_bf16_f32 v32, v68, v69\n" \
"v_mfma_f32_16x16x32_bf16 v[160:163], a[84:87], a[36:39], v[160:163]\n" \
"v_cvt_pk_bf16_f32 v33, v70, v71\n" \
"v_mul_f32 v72, s20, v72\n" \
"v_mfma_f32_16x16x32_bf16 v[168:171], a[80:83], a[40:43], v[168:171]\n" \
"buffer_store_dwordx4 v[30:33], v4, s[36:39], s96 offen offset:0 sc1\n" \
"v_mfma_f32_16x16x32_bf16 v[168:171], a[84:87], a[44:47], v[168:171]\n" \
"s_waitcnt lgkmcnt(0)\n" \
"s_add_u32 s97, s96, s98\n" \
"v_mul_f32 v73, s20, v73\n" \
"v_mfma_f32_16x16x32_bf16 v[176:179], a[80:83], a[48:51], v[176:179]\n" \
"s_barrier\n" \
"v_mul_f32 v74, s20, v74\n" \
"v_mul_f32 v75, s20, v75\n" \
"v_mfma_f32_16x16x32_bf16 v[176:179], a[84:87], a[52:55], v[176:179]\n" \
"v_cvt_pk_bf16_f32 v34, v72, v73\n" \
"v_cvt_pk_bf16_f32 v35, v74, v75\n" \
"v_mfma_f32_16x16x32_bf16 v[184:187], a[80:83], a[56:59], v[184:187]\n" \
"v_mul_f32 v76, s20, v76\n" \
"v_mfma_f32_16x16x32_bf16 v[184:187], a[84:87], a[60:63], v[184:187]\n" \
"v_add_u32 v5, s54, v5\n" \
"v_mul_f32 v77, s20, v77\n" \
"v_mfma_f32_16x16x32_bf16 v[132:135], a[88:91], a[0:3], v[132:135]\n" \
"v_mul_f32 v78, s20, v78\n" \
"v_mul_f32 v79, s20, v79\n" \
"v_mfma_f32_16x16x32_bf16 v[132:135], a[92:95], a[4:7], v[132:135]\n" \
"v_cvt_pk_bf16_f32 v36, v76, v77\n" \
"v_mfma_f32_16x16x32_bf16 v[140:143], a[88:91], a[8:11], v[140:143]\n" \
"v_add_u32 v6, s54, v6\n" \
"v_cvt_pk_bf16_f32 v37, v78, v79\n" \
"v_mfma_f32_16x16x32_bf16 v[140:143], a[92:95], a[12:15], v[140:143]\n" \
"v_add_u32 v7, s54, v7\n" \
"v_mul_f32 v80, s20, v80\n" \
"v_mfma_f32_16x16x32_bf16 v[148:151], a[88:91], a[16:19], v[148:151]\n" \
"v_mul_f32 v81, s20, v81\n" \
"v_mfma_f32_16x16x32_bf16 v[148:151], a[92:95], a[20:23], v[148:151]\n" \
"v_mul_f32 v82, s20, v82\n" \
"v_mul_f32 v83, s20, v83\n" \
"v_mfma_f32_16x16x32_bf16 v[156:159], a[88:91], a[24:27], v[156:159]\n" \
"v_add_u32 v8, s54, v8\n" \
"v_cvt_pk_bf16_f32 v38, v80, v81\n" \
"v_mfma_f32_16x16x32_bf16 v[156:159], a[92:95], a[28:31], v[156:159]\n" \
"v_sub_u32 v9, v9, v62\n" \
"v_mfma_f32_16x16x32_bf16 v[164:167], a[88:91], a[32:35], v[164:167]\n" \
"buffer_store_dwordx4 v[34:37], v4, s[36:39], s97 offen offset:0 sc1\n" \
"v_mfma_f32_16x16x32_bf16 v[164:167], a[92:95], a[36:39], v[164:167]\n" \
"s_add_u32 s96, s96, s92\n" \
"v_cvt_pk_bf16_f32 v39, v82, v83\n" \
"v_mul_f32 v84, s20, v84\n" \
"v_mfma_f32_16x16x32_bf16 v[172:175], a[88:91], a[40:43], v[172:175]\n" \
"v_sub_u32 v10, v10, v62\n" \
"v_mul_f32 v85, s20, v85\n" \
"v_mfma_f32_16x16x32_bf16 v[172:175], a[92:95], a[44:47], v[172:175]\n" \
"v_sub_u32 v11, v11, v62\n" \
"v_mfma_f32_16x16x32_bf16 v[180:183], a[88:91], a[48:51], v[180:183]\n" \
"v_mul_f32 v86, s20, v86\n" \
"v_mul_f32 v87, s20, v87\n" \
"v_mfma_f32_16x16x32_bf16 v[180:183], a[92:95], a[52:55], v[180:183]\n" \
"v_cvt_pk_bf16_f32 v40, v84, v85\n" \
"v_cvt_pk_bf16_f32 v41, v86, v87\n" \
"v_mfma_f32_16x16x32_bf16 v[188:191], a[88:91], a[56:59], v[188:191]\n" \
"v_sub_u32 v12, v12, v62\n" \
"v_mfma_f32_16x16x32_bf16 v[188:191], a[92:95], a[60:63], v[188:191]\n" \
"v_mul_f32 v88, s20, v88\n" \
"v_mul_f32 v89, s20, v89\n" \
"v_mfma_f32_16x16x32_bf16 v[192:195], a[96:99], a[0:3], v[192:195]\n" \
"s_mov_b32 s46, 128\n" \
"s_add_u32 m0, s50, 0\n" \
"s_waitcnt vmcnt(14) lgkmcnt(0)\n" \
"v_mfma_f32_16x16x32_bf16 v[192:195], a[100:103], a[4:7], v[192:195]\n" \
"buffer_load_dwordx4 v13, s[28:31], s46 offen lds\n" \
"v_mfma_f32_16x16x32_bf16 v[200:203], a[96:99], a[8:11], v[200:203]\n" \
"ds_read_b128 a[200:203], v50 offset:512\n" \
"v_mul_f32 v90, s20, v90\n" \
"v_mfma_f32_16x16x32_bf16 v[200:203], a[100:103], a[12:15], v[200:203]\n" \
"v_mul_f32 v91, s20, v91\n" \
"v_cvt_pk_bf16_f32 v42, v88, v89\n" \
"v_mfma_f32_16x16x32_bf16 v[208:211], a[96:99], a[16:19], v[208:211]\n" \
"ds_read2_b32 a[64:65], v46 offset0:0 offset1:1\n" \
"v_cvt_pk_bf16_f32 v43, v90, v91\n" \
"v_mfma_f32_16x16x32_bf16 v[208:211], a[100:103], a[20:23], v[208:211]\n" \
"LH3_%=:\ns_cbranch_execz LH11_%=\n" \
"v_mul_f32 v92, s20, v92\n" \
"v_mul_f32 v93, s20, v93\n" \
"v_mfma_f32_16x16x32_bf16 v[216:219], a[96:99], a[24:27], v[216:219]\n" \
"ds_read2_b32 a[66:67], v46 offset0:2 offset1:3\n" \
"v_mul_f32 v94, s20, v94\n" \
"v_mfma_f32_16x16x32_bf16 v[216:219], a[100:103], a[28:31], v[216:219]\n" \
"buffer_store_dwordx4 v[38:41], v4, s[36:39], s96 offen offset:0 sc1\n" \
"v_mfma_f32_16x16x32_bf16 v[224:227], a[96:99], a[32:35], v[224:227]\n" \
"s_add_u32 m0, s50, 4096\n" \
"ds_read_b128 a[204:207], v50 offset:1024\n" \
"s_add_u32 s97, s96, s98\n" \
"v_mfma_f32_16x16x32_bf16 v[224:227], a[100:103], a[36:39], v[224:227]\n" \
"buffer_load_dwordx4 v14, s[28:31], s46 offen lds\n" \
"v_mfma_f32_16x16x32_bf16 v[232:235], a[96:99], a[40:43], v[232:235]\n" \
"ds_read2_b32 a[68:69], v46 offset0:128 offset1:129\n" \
"v_mul_f32 v95, s20, v95\n" \
"v_mfma_f32_16x16x32_bf16 v[232:235], a[100:103], a[44:47], v[232:235]\n" \
"v_cvt_pk_bf16_f32 v44, v92, v93\n" \
"v_cvt_pk_bf16_f32 v45, v94, v95\n" \
"v_mfma_f32_16x16x32_bf16 v[240:243], a[96:99], a[48:51], v[240:243]\n" \
"ds_read2_b32 a[70:71], v46 offset0:130 offset1:131\n" \
"v_mul_f32 v96, s20, v96\n" \
"v_mfma_f32_16x16x32_bf16 v[240:243], a[100:103], a[52:55], v[240:243]\n" \
"v_mul_f32 v97, s20, v97\n" \
"v_mul_f32 v98, s20, v98\n" \
"v_mfma_f32_16x16x32_bf16 v[248:251], a[96:99], a[56:59], v[248:251]\n" \
"ds_read_b128 a[208:211], v50 offset:1536\n" \
"v_mul_f32 v99, s20, v99\n" \
"v_mfma_f32_16x16x32_bf16 v[248:251], a[100:103], a[60:63], v[248:251]\n" \
"v_cvt_pk_bf16_f32 v30, v96, v97\n" \
"v_cvt_pk_bf16_f32 v31, v98, v99\n" \
"v_mfma_f32_16x16x32_bf16 v[196:199], a[104:107], a[0:3], v[196:199]\n" \
"s_add_u32 m0, s50, 8192\n" \
"ds_read2_b32 a[72:73], v47 offset0:0 offset1:1\n" \
"v_mul_f32 v100, s20, v100\n" \
"v_mfma_f32_16x16x32_bf16 v[196:199], a[108:111], a[4:7], v[196:199]\n" \
"buffer_load_dwordx4 v15, s[28:31], s46 offen lds\n" \
"v_mfma_f32_16x16x32_bf16 v[204:207], a[104:107], a[8:11], v[204:207]\n" \
"ds_read2_b32 a[74:75], v47 offset0:2 offset1:3\n" \
"v_mul_f32 v101, s20, v101\n" \
"v_mfma_f32_16x16x32_bf16 v[204:207], a[108:111], a[12:15], v[204:207]\n" \
"v_mul_f32 v102, s20, v102\n" \
"v_mul_f32 v103, s20, v103\n" \
"v_mfma_f32_16x16x32_bf16 v[212:215], a[104:107], a[16:19], v[212:215]\n" \
"ds_read_b128 a[212:215], v50 offset:2048\n" \
"v_cvt_pk_bf16_f32 v32, v100, v101\n" \
"v_mfma_f32_16x16x32_bf16 v[212:215], a[108:111], a[20:23], v[212:215]\n" \
"buffer_store_dwordx4 v[42:45], v4, s[36:39], s97 offen offset:0 sc1\n" \
"v_mfma_f32_16x16x32_bf16 v[220:223], a[104:107], a[24:27], v[220:223]\n" \
"s_add_u32 s96, s96, s92\n" \
"ds_read2_b32 a[76:77], v47 offset0:128 offset1:129\n" \
"v_cvt_pk_bf16_f32 v33, v102, v103\n" \
"v_mfma_f32_16x16x32_bf16 v[220:223], a[108:111], a[28:31], v[220:223]\n" \
"v_mul_f32 v104, s20, v104\n" \
"v_mul_f32 v105, s20, v105\n" \
"v_mfma_f32_16x16x32_bf16 v[228:231], a[104:107], a[32:35], v[228:231]\n" \
"s_add_u32 m0, s50, 12288\n" \
"ds_read2_b32 a[78:79], v47 offset0:130 offset1:131\n" \
"v_mul_f32 v106, s20, v106\n" \
"v_mfma_f32_16x16x32_bf16 v[228:231], a[108:111], a[36:39], v[228:231]\n" \
"buffer_load_dwordx4 v16, s[28:31], s46 offen lds\n" \
"v_mfma_f32_16x16x32_bf16 v[236:239], a[104:107], a[40:43], v[236:239]\n" \
"ds_read_b128 a[216:219], v50 offset:2560\n" \
"v_mul_f32 v107, s20, v107\n" \
"v_mfma_f32_16x16x32_bf16 v[236:239], a[108:111], a[44:47], v[236:239]\n" \
"v_cvt_pk_bf16_f32 v34, v104, v105\n" \
"v_cvt_pk_bf16_f32 v35, v106, v107\n" \
"v_mfma_f32_16x16x32_bf16 v[244:247], a[104:107], a[48:51], v[244:247]\n" \
"ds_read2_b32 a[80:81], v48 offset0:0 offset1:1\n" \
"v_mul_f32 v108, s20, v108\n" \
"v_mfma_f32_16x16x32_bf16 v[244:247], a[108:111], a[52:55], v[244:247]\n" \
"v_mul_f32 v109, s20, v109\n" \
"v_mul_f32 v110, s20, v110\n" \
"v_mfma_f32_16x16x32_bf16 v[252:255], a[104:107], a[56:59], v[252:255]\n" \
"ds_read2_b32 a[82:83], v48 offset0:2 offset1:3\n" \
"v_mul_f32 v111, s20, v111\n" \
"v_mfma_f32_16x16x32_bf16 v[252:255], a[108:111], a[60:63], v[252:255]\n" \
"v_cvt_pk_bf16_f32 v36, v108, v109\n" \
"v_cvt_pk_bf16_f32 v37, v110, v111\n" \
"v_mfma_f32_16x16x32_bf16 a[128:131], a[112:115], a[0:3], a[128:131]\n" \
"s_mov_b32 s46, 128\n" \
"s_add_u32 m0, s50, 16384\n" \
"ds_read_b128 a[220:223], v50 offset:3072\n" \
"v_mfma_f32_16x16x32_bf16 a[128:131], a[116:119], a[4:7], a[128:131]\n" \
"buffer_load_dwordx4 v17, s[28:31], s46 offen lds\n" \
"v_mfma_f32_16x16x32_bf16 a[136:139], a[112:115], a[8:11], a[136:139]\n" \
"ds_read2_b32 a[84:85], v48 offset0:128 offset1:129\n" \
"v_mul_f32 v112, s20, v112\n" \
"v_mfma_f32_16x16x32_bf16 a[136:139], a[116:119], a[12:15], a[136:139]\n" \
"buffer_store_dwordx4 v[30:33], v4, s[36:39], s96 offen offset:0 sc1\n" \
"v_mfma_f32_16x16x32_bf16 a[144:147], a[112:115], a[16:19], a[144:147]\n" \
"s_add_u32 s97, s96, s98\n" \
"ds_read2_b32 a[86:87], v48 offset0:130 offset1:131\n" \
"v_mul_f32 v113, s20, v113\n" \
"v_mfma_f32_16x16x32_bf16 a[144:147], a[116:119], a[20:23], a[144:147]\n" \
"v_mul_f32 v114, s20, v114\n" \
"v_mul_f32 v115, s20, v115\n" \
"v_mfma_f32_16x16x32_bf16 a[152:155], a[112:115], a[24:27], a[152:155]\n" \
"ds_read_b128 a[224:227], v50 offset:3584\n" \
"v_cvt_pk_bf16_f32 v38, v112, v113\n" \
"v_mfma_f32_16x16x32_bf16 a[152:155], a[116:119], a[28:31], a[152:155]\n" \
"v_cvt_pk_bf16_f32 v39, v114, v115\n" \
"v_mul_f32 v116, s20, v116\n" \
"v_mfma_f32_16x16x32_bf16 a[160:163], a[112:115], a[32:35], a[160:163]\n" \
"s_add_u32 m0, s50, 20480\n" \
"ds_read2_b32 a[88:89], v49 offset0:0 offset1:1\n" \
"v_mul_f32 v117, s20, v117\n" \
"v_mfma_f32_16x16x32_bf16 a[160:163], a[116:119], a[36:39], a[160:163]\n" \
"buffer_load_dwordx4 v18, s[28:31], s46 offen lds\n" \
"v_mfma_f32_16x16x32_bf16 a[168:171], a[112:115], a[40:43], a[168:171]\n" \
"ds_read2_b32 a[90:91], v49 offset0:2 offset1:3\n" \
"v_mul_f32 v118, s20, v118\n" \
"v_mfma_f32_16x16x32_bf16 a[168:171], a[116:119], a[44:47], a[168:171]\n" \
"v_mul_f32 v119, s20, v119\n" \
"v_cvt_pk_bf16_f32 v40, v116, v117\n" \
"v_mfma_f32_16x16x32_bf16 a[176:179], a[112:115], a[48:51], a[176:179]\n" \
"ds_read_b128 a[228:231], v50 offset:4096\n" \
"v_cvt_pk_bf16_f32 v41, v118, v119\n" \
"v_mfma_f32_16x16x32_bf16 a[176:179], a[116:119], a[52:55], a[176:179]\n" \
"v_mul_f32 v120, s20, v120\n" \
"v_mul_f32 v121, s20, v121\n" \
"v_mfma_f32_16x16x32_bf16 a[184:187], a[112:115], a[56:59], a[184:187]\n" \
"ds_read2_b32 a[92:93], v49 offset0:128 offset1:129\n" \
"v_mul_f32 v122, s20, v122\n" \
"v_mfma_f32_16x16x32_bf16 a[184:187], a[116:119], a[60:63], a[184:187]\n" \
"v_mul_f32 v123, s20, v123\n" \
"v_cvt_pk_bf16_f32 v42, v120, v121\n" \
"v_mfma_f32_16x16x32_bf16 a[132:135], a[120:123], a[0:3], a[132:135]\n" \
"s_add_u32 m0, s50, 24576\n" \
"ds_read2_b32 a[94:95], v49 offset0:130 offset1:131\n" \
"v_cvt_pk_bf16_f32 v43, v122, v123\n" \
"v_mfma_f32_16x16x32_bf16 a[132:135], a[124:127], a[4:7], a[132:135]\n" \
"buffer_load_dwordx4 v19, s[28:31], s46 offen lds\n" \
"v_mfma_f32_16x16x32_bf16 a[140:143], a[120:123], a[8:11], a[140:143]\n" \
"v_mul_f32 v124, s20, v124\n" \
"v_mul_f32 v125, s20, v125\n" \
"v_mfma_f32_16x16x32_bf16 a[140:143], a[124:127], a[12:15], a[140:143]\n" \
"buffer_store_dwordx4 v[34:37], v4, s[36:39], s97 offen offset:0 sc1\n" \
"v_mfma_f32_16x16x32_bf16 a[148:151], a[120:123], a[16:19], a[148:151]\n" \
"s_add_u32 s96, s96, s92\n" \
"v_mul_f32 v126, s20, v126\n" \
"v_mul_f32 v127, s20, v127\n" \
"v_mfma_f32_16x16x32_bf16 a[148:151], a[124:127], a[20:23], a[148:151]\n" \
"v_cvt_pk_bf16_f32 v44, v124, v125\n" \
"v_cvt_pk_bf16_f32 v45, v126, v127\n" \
"v_mfma_f32_16x16x32_bf16 a[156:159], a[120:123], a[24:27], a[156:159]\n" \
"v_mul_f32 v128, s20, v128\n" \
"v_mfma_f32_16x16x32_bf16 a[156:159], a[124:127], a[28:31], a[156:159]\n" \
"v_mul_f32 v129, s20, v129\n" \
"v_mul_f32 v130, s20, v130\n" \
"v_mfma_f32_16x16x32_bf16 a[164:167], a[120:123], a[32:35], a[164:167]\n" \
"s_add_u32 m0, s50, 28672\n" \
"v_mul_f32 v131, s20, v131\n" \
"v_cvt_pk_bf16_f32 v30, v128, v129\n" \
"v_mfma_f32_16x16x32_bf16 a[164:167], a[124:127], a[36:39], a[164:167]\n" \
"buffer_load_dwordx4 v20, s[28:31], s46 offen lds\n" \
"v_mfma_f32_16x16x32_bf16 a[172:175], a[120:123], a[40:43], a[172:175]\n" \
"v_cvt_pk_bf16_f32 v31, v130, v131\n" \
"v_mul_f32 v132, s20, v132\n" \
"v_mfma_f32_16x16x32_bf16 a[172:175], a[124:127], a[44:47], a[172:175]\n" \
"v_mul_f32 v133, s20, v133\n" \
"v_mul_f32 v134, s20, v134\n" \
"v_mfma_f32_16x16x32_bf16 a[180:183], a[120:123], a[48:51], a[180:183]\n" \
"v_mul_f32 v135, s20, v135\n" \
"v_mfma_f32_16x16x32_bf16 a[180:183], a[124:127], a[52:55], a[180:183]\n" \
"v_cvt_pk_bf16_f32 v32, v132, v133\n" \
"v_cvt_pk_bf16_f32 v33, v134, v135\n" \
"v_mfma_f32_16x16x32_bf16 a[188:191], a[120:123], a[56:59], a[188:191]\n" \
"s_waitcnt lgkmcnt(0)\n" \
"v_mul_f32 v136, s20, v136\n" \
"v_mul_f32 v137, s20, v137\n" \
"v_mfma_f32_16x16x32_bf16 a[188:191], a[124:127], a[60:63], a[188:191]\n" \
"v_mul_f32 v138, s20, v138\n" \
"s_branch LC_%=\n" \
"LAV_%=:\n" \
"v_mfma_f32_16x16x32_bf16 v[64:67], a[0:3], a[64:67], 0\n" \
"s_mov_b32 s47, 524288\n" \
"s_add_u32 m0, s22, 49024\n" \
"v_mfma_f32_16x16x32_bf16 v[64:67], a[4:7], a[68:71], v[64:67]\n" \
"buffer_load_dwordx4 v21, s[32:35], s47 offen offset:128 nt lds\n" \
"v_mfma_f32_16x16x32_bf16 v[68:71], a[8:11], a[64:67], 0\n" \
"s_add_u32 s14, s14, 1\ns_addc_u32 s15, s15, 0\n" \
"v_mfma_f32_16x16x32_bf16 v[68:71], a[12:15], a[68:71], v[68:71]\n" \
"v_mfma_f32_16x16x32_bf16 v[80:83], a[16:19], a[64:67], 0\n" \
"s_add_u32 m0, s22, 50816\n" \
"ds_read_b64_tr_b16 a[96:97], v9 offset:16384\n" \
"v_mfma_f32_16x16x32_bf16 v[80:83], a[20:23], a[68:71], v[80:83]\n" \
"buffer_load_dwordx4 v21, s[32:35], s47 offen offset:384 nt lds\n" \
"v_mfma_f32_16x16x32_bf16 v[84:87], a[24:27], a[64:67], 0\n" \
"ds_read_b64_tr_b16 a[98:99], v10 offset:16384\n" \
"v_mfma_f32_16x16x32_bf16 v[84:87], a[28:31], a[68:71], v[84:87]\n" \
"v_mfma_f32_16x16x32_bf16 v[96:99], a[32:35], a[64:67], 0\n" \
"s_mov_b32 s47, 786432\n" \
"s_add_u32 m0, s22, 50048\n" \
"ds_read_b64_tr_b16 a[100:101], v9 offset:17408\n" \
"v_mfma_f32_16x16x32_bf16 v[96:99], a[36:39], a[68:71], v[96:99]\n" \
"buffer_load_dwordx4 v21, s[32:35], s47 offen offset:128 nt lds\n" \
"v_mfma_f32_16x16x32_bf16 v[100:103], a[40:43], a[64:67], 0\n" \
"ds_read_b64_tr_b16 a[102:103], v10 offset:17408\n" \
"v_mfma_f32_16x16x32_bf16 v[100:103], a[44:47], a[68:71], v[100:103]\n" \
"v_mfma_f32_16x16x32_bf16 v[112:115], a[48:51], a[64:67], 0\n" \
"s_add_u32 m0, s22, 51840\n" \
"ds_read_b64_tr_b16 a[104:105], v11 offset:16384\n" \
"v_mfma_f32_16x16x32_bf16 v[112:115], a[52:55], a[68:71], v[112:115]\n" \
"buffer_load_dwordx4 v21, s[32:35], s47 offen offset:384 nt lds\n" \
"v_mfma_f32_16x16x32_bf16 v[116:119], a[56:59], a[64:67], 0\n" \
"ds_read_b64_tr_b16 a[106:107], v12 offset:16384\n" \
"v_mfma_f32_16x16x32_bf16 v[116:119], a[60:63], a[68:71], v[116:119]\n" \
"v_mfma_f32_16x16x32_bf16 v[72:75], a[0:3], a[72:75], 0\n" \
"s_mov_b32 s46, 256\n" \
"s_add_u32 m0, s50, 32768\n" \
"ds_read_b64_tr_b16 a[108:109], v11 offset:17408\n" \
"v_mfma_f32_16x16x32_bf16 v[72:75], a[4:7], a[76:79], v[72:75]\n" \
"buffer_load_dwordx4 v13, s[28:31], s46 offen lds\n" \
"v_mfma_f32_16x16x32_bf16 v[76:79], a[8:11], a[72:75], 0\n" \
"ds_read_b64_tr_b16 a[110:111], v12 offset:17408\n" \
"v_mfma_f32_16x16x32_bf16 v[76:79], a[12:15], a[76:79], v[76:79]\n" \
"v_mfma_f32_16x16x32_bf16 v[88:91], a[16:19], a[72:75], 0\n" \
"s_add_u32 m0, s50, 36864\n" \
"ds_read_b64_tr_b16 a[112:113], v9 offset:16896\n" \
"v_mfma_f32_16x16x32_bf16 v[88:91], a[20:23], a[76:79], v[88:91]\n" \
"buffer_load_dwordx4 v14, s[28:31], s46 offen lds\n" \
"v_mfma_f32_16x16x32_bf16 v[92:95], a[24:27], a[72:75], 0\n" \
"ds_read_b64_tr_b16 a[114:115], v10 offset:16896\n" \
"v_mfma_f32_16x16x32_bf16 v[92:95], a[28:31], a[76:79], v[92:95]\n" \
"v_mfma_f32_16x16x32_bf16 v[104:107], a[32:35], a[72:75], 0\n" \
"s_add_u32 m0, s50, 40960\n" \
"ds_read_b64_tr_b16 a[116:117], v9 offset:17920\n" \
"v_mfma_f32_16x16x32_bf16 v[104:107], a[36:39], a[76:79], v[104:107]\n" \
"buffer_load_dwordx4 v15, s[28:31], s46 offen lds\n" \
"v_mfma_f32_16x16x32_bf16 v[108:111], a[40:43], a[72:75], 0\n" \
"ds_read_b64_tr_b16 a[118:119], v10 offset:17920\n" \
"v_mfma_f32_16x16x32_bf16 v[108:111], a[44:47], a[76:79], v[108:111]\n" \
"v_mfma_f32_16x16x32_bf16 v[120:123], a[48:51], a[72:75], 0\n" \
"s_add_u32 m0, s50, 45056\n" \
"ds_read_b64_tr_b16 a[120:121], v11 offset:16896\n" \
"v_mfma_f32_16x16x32_bf16 v[120:123], a[52:55], a[76:79], v[120:123]\n" \
"buffer_load_dwordx4 v16, s[28:31], s46 offen lds\n" \
"v_mfma_f32_16x16x32_bf16 v[124:127], a[56:59], a[72:75], 0\n" \
"ds_read_b64_tr_b16 a[122:123], v12 offset:16896\n" \
"v_mfma_f32_16x16x32_bf16 v[124:127], a[60:63], a[76:79], v[124:127]\n" \
"v_mfma_f32_16x16x32_bf16 v[128:131], a[0:3], a[80:83], 0\n" \
"s_mov_b32 s46, 256\n" \
"s_add_u32 m0, s50, 49152\n" \
"ds_read_b64_tr_b16 a[124:125], v11 offset:17920\n" \
"v_mfma_f32_16x16x32_bf16 v[128:131], a[4:7], a[84:87], v[128:131]\n" \
"buffer_load_dwordx4 v17, s[28:31], s46 offen lds\n" \
"v_mfma_f32_16x16x32_bf16 v[132:135], a[8:11], a[80:83], 0\n" \
"ds_read_b64_tr_b16 a[126:127], v12 offset:17920\n" \
"v_mfma_f32_16x16x32_bf16 v[132:135], a[12:15], a[84:87], v[132:135]\n" \
"v_mfma_f32_16x16x32_bf16 v[144:147], a[16:19], a[80:83], 0\n" \
"s_add_u32 m0, s50, 53248\n" \
"v_mfma_f32_16x16x32_bf16 v[144:147], a[20:23], a[84:87], v[144:147]\n" \
"buffer_load_dwordx4 v18, s[28:31], s46 offen lds\n" \
"v_mfma_f32_16x16x32_bf16 v[148:151], a[24:27], a[80:83], 0\n" \
"v_mfma_f32_16x16x32_bf16 v[148:151], a[28:31], a[84:87], v[148:151]\n" \
"v_mfma_f32_16x16x32_bf16 v[160:163], a[32:35], a[80:83], 0\n" \
"s_add_u32 m0, s50, 57344\n" \
"v_mfma_f32_16x16x32_bf16 v[160:163], a[36:39], a[84:87], v[160:163]\n" \
"buffer_load_dwordx4 v19, s[28:31], s46 offen lds\n" \
"v_mfma_f32_16x16x32_bf16 v[164:167], a[40:43], a[80:83], 0\n" \
"v_mfma_f32_16x16x32_bf16 v[164:167], a[44:47], a[84:87], v[164:167]\n" \
"s_waitcnt vmcnt(21) lgkmcnt(0)\n" \
"v_mfma_f32_16x16x32_bf16 v[176:179], a[48:51], a[80:83], 0\n" \
"s_add_u32 m0, s50, 61440\n" \
"s_barrier\n" \
"v_mfma_f32_16x16x32_bf16 v[176:179], a[52:55], a[84:87], v[176:179]\n" \
"buffer_load_dwordx4 v20, s[28:31], s46 offen lds\n" \
"v_mfma_f32_16x16x32_bf16 v[180:183], a[56:59], a[80:83], 0\n" \
"v_mfma_f32_16x16x32_bf16 v[180:183], a[60:63], a[84:87], v[180:183]\n" \
"v_mfma_f32_16x16x32_bf16 v[136:139], a[0:3], a[88:91], 0\n" \
"s_mov_b32 s47, 1048576\n" \
"s_add_u32 m0, s22, 0\n" \
"v_mfma_f32_16x16x32_bf16 v[136:139], a[4:7], a[92:95], v[136:139]\n" \
"buffer_load_dwordx4 v21, s[32:35], s47 offen offset:0 lds\n" \
"v_mfma_f32_16x16x32_bf16 v[140:143], a[8:11], a[88:91], 0\n" \
"v_mfma_f32_16x16x32_bf16 v[140:143], a[12:15], a[92:95], v[140:143]\n" \
"v_mfma_f32_16x16x32_bf16 v[152:155], a[16:19], a[88:91], 0\n" \
"s_add_u32 m0, s22, 1792\n" \
"v_mfma_f32_16x16x32_bf16 v[152:155], a[20:23], a[92:95], v[152:155]\n" \
"buffer_load_dwordx4 v21, s[32:35], s47 offen offset:256 lds\n" \
"v_mfma_f32_16x16x32_bf16 v[156:159], a[24:27], a[88:91], 0\n" \
"v_mfma_f32_16x16x32_bf16 v[156:159], a[28:31], a[92:95], v[156:159]\n" \
"v_mfma_f32_16x16x32_bf16 v[168:171], a[32:35], a[88:91], 0\n" \
"s_mov_b32 s47, 1310720\n" \
"s_add_u32 m0, s22, 1024\n" \
"v_mfma_f32_16x16x32_bf16 v[168:171], a[36:39], a[92:95], v[168:171]\n" \
"buffer_load_dwordx4 v21, s[32:35], s47 offen offset:0 lds\n" \
"v_mfma_f32_16x16x32_bf16 v[172:175], a[40:43], a[88:91], 0\n" \
"v_mfma_f32_16x16x32_bf16 v[172:175], a[44:47], a[92:95], v[172:175]\n" \
"v_mfma_f32_16x16x32_bf16 v[184:187], a[48:51], a[88:91], 0\n" \
"s_add_u32 m0, s22, 2816\n" \
"v_mfma_f32_16x16x32_bf16 v[184:187], a[52:55], a[92:95], v[184:187]\n" \
"buffer_load_dwordx4 v21, s[32:35], s47 offen offset:256 lds\n" \
"v_mfma_f32_16x16x32_bf16 v[188:191], a[56:59], a[88:91], 0\n" \
"v_mfma_f32_16x16x32_bf16 v[188:191], a[60:63], a[92:95], v[188:191]\n" \
"v_mfma_f32_16x16x32_bf16 v[192:195], a[0:3], a[96:99], 0\n" \
"s_mov_b32 s47, 1048576\n" \
"s_add_u32 m0, s22, 16256\n" \
"v_mfma_f32_16x16x32_bf16 v[192:195], a[4:7], a[100:103], v[192:195]\n" \
"buffer_load_dwordx4 v21, s[32:35], s47 offen offset:128 lds\n" \
"v_mfma_f32_16x16x32_bf16 v[196:199], a[8:11], a[96:99], 0\n" \
"v_mfma_f32_16x16x32_bf16 v[196:199], a[12:15], a[100:103], v[196:199]\n" \
"v_mfma_f32_16x16x32_bf16 v[208:211], a[16:19], a[96:99], 0\n" \
"s_mov_b32 s46, 384\n" \
"s_add_u32 m0, s50, 65536\n" \
"ds_read_b64_tr_b16 a[64:65], v9 offset:32768\n" \
"v_mfma_f32_16x16x32_bf16 v[208:211], a[20:23], a[100:103], v[208:211]\n" \
"buffer_load_dwordx4 v13, s[28:31], s46 offen lds\n" \
"v_mfma_f32_16x16x32_bf16 v[212:215], a[24:27], a[96:99], 0\n" \
"ds_read_b64_tr_b16 a[66:67], v10 offset:32768\n" \
"v_mfma_f32_16x16x32_bf16 v[212:215], a[28:31], a[100:103], v[212:215]\n" \
"v_mfma_f32_16x16x32_bf16 v[224:227], a[32:35], a[96:99], 0\n" \
"s_add_u32 m0, s22, 18048\n" \
"ds_read_b64_tr_b16 a[68:69], v9 offset:33792\n" \
"v_mfma_f32_16x16x32_bf16 v[224:227], a[36:39], a[100:103], v[224:227]\n" \
"buffer_load_dwordx4 v21, s[32:35], s47 offen offset:384 lds\n" \
"v_mfma_f32_16x16x32_bf16 v[228:231], a[40:43], a[96:99], 0\n" \
"ds_read_b64_tr_b16 a[70:71], v10 offset:33792\n" \
"v_mfma_f32_16x16x32_bf16 v[228:231], a[44:47], a[100:103], v[228:231]\n" \
"v_mfma_f32_16x16x32_bf16 v[240:243], a[48:51], a[96:99], 0\n" \
"s_add_u32 m0, s50, 69632\n" \
"ds_read_b64_tr_b16 a[72:73], v11 offset:32768\n" \
"v_mfma_f32_16x16x32_bf16 v[240:243], a[52:55], a[100:103], v[240:243]\n" \
"buffer_load_dwordx4 v14, s[28:31], s46 offen lds\n" \
"v_mfma_f32_16x16x32_bf16 v[244:247], a[56:59], a[96:99], 0\n" \
"ds_read_b64_tr_b16 a[74:75], v12 offset:32768\n" \
"v_mfma_f32_16x16x32_bf16 v[244:247], a[60:63], a[100:103], v[244:247]\n" \
"v_mfma_f32_16x16x32_bf16 v[200:203], a[0:3], a[104:107], 0\n" \
"s_mov_b32 s47, 1310720\n" \
"s_add_u32 m0, s22, 17280\n" \
"ds_read_b64_tr_b16 a[76:77], v11 offset:33792\n" \
"v_mfma_f32_16x16x32_bf16 v[200:203], a[4:7], a[108:111], v[200:203]\n" \
"buffer_load_dwordx4 v21, s[32:35], s47 offen offset:128 lds\n" \
"v_mfma_f32_16x16x32_bf16 v[204:207], a[8:11], a[104:107], 0\n" \
"ds_read_b64_tr_b16 a[78:79], v12 offset:33792\n" \
"v_mfma_f32_16x16x32_bf16 v[204:207], a[12:15], a[108:111], v[204:207]\n" \
"v_mfma_f32_16x16x32_bf16 v[216:219], a[16:19], a[104:107], 0\n" \
"s_add_u32 m0, s50, 73728\n" \
"ds_read_b64_tr_b16 a[80:81], v9 offset:33280\n" \
"v_mfma_f32_16x16x32_bf16 v[216:219], a[20:23], a[108:111], v[216:219]\n" \
"buffer_load_dwordx4 v15, s[28:31], s46 offen lds\n" \
"v_mfma_f32_16x16x32_bf16 v[220:223], a[24:27], a[104:107], 0\n" \
"ds_read_b64_tr_b16 a[82:83], v10 offset:33280\n" \
"v_mfma_f32_16x16x32_bf16 v[220:223], a[28:31], a[108:111], v[220:223]\n" \
"v_mfma_f32_16x16x32_bf16 v[232:235], a[32:35], a[104:107], 0\n" \
"s_add_u32 m0, s22, 19072\n" \
"ds_read_b64_tr_b16 a[84:85], v9 offset:34304\n" \
"v_mfma_f32_16x16x32_bf16 v[232:235], a[36:39], a[108:111], v[232:235]\n" \
"buffer_load_dwordx4 v21, s[32:35], s47 offen offset:384 lds\n" \
"v_mfma_f32_16x16x32_bf16 v[236:239], a[40:43], a[104:107], 0\n" \
"ds_read_b64_tr_b16 a[86:87], v10 offset:34304\n" \
"v_mfma_f32_16x16x32_bf16 v[236:239], a[44:47], a[108:111], v[236:239]\n" \
"v_mfma_f32_16x16x32_bf16 v[248:251], a[48:51], a[104:107], 0\n" \
"s_add_u32 m0, s50, 77824\n" \
"ds_read_b64_tr_b16 a[88:89], v11 offset:33280\n" \
"v_mfma_f32_16x16x32_bf16 v[248:251], a[52:55], a[108:111], v[248:251]\n" \
"buffer_load_dwordx4 v16, s[28:31], s46 offen lds\n" \
"v_mfma_f32_16x16x32_bf16 v[252:255], a[56:59], a[104:107], 0\n" \
"ds_read_b64_tr_b16 a[90:91], v12 offset:33280\n" \
"v_mfma_f32_16x16x32_bf16 v[252:255], a[60:63], a[108:111], v[252:255]\n" \
"v_mfma_f32_16x16x32_bf16 a[128:131], a[0:3], a[112:115], 0\n" \
"ds_read_b64_tr_b16 a[92:93], v11 offset:34304\n" \
"v_mfma_f32_16x16x32_bf16 a[128:131], a[4:7], a[116:119], a[128:131]\n" \
"global_load_dwordx4 a[230:233], v27, s[70:71] offset:0\n" \
"v_mfma_f32_16x16x32_bf16 a[132:135], a[8:11], a[112:115], 0\n" \
"ds_read_b64_tr_b16 a[94:95], v12 offset:34304\n" \
"v_mfma_f32_16x16x32_bf16 a[132:135], a[12:15], a[116:119], a[132:135]\n" \
"v_mfma_f32_16x16x32_bf16 a[144:147], a[16:19], a[112:115], 0\n" \
"v_mfma_f32_16x16x32_bf16 a[144:147], a[20:23], a[116:119], a[144:147]\n" \
"global_load_dwordx4 a[234:237], v27, s[70:71] offset:16\n" \
"v_mfma_f32_16x16x32_bf16 a[148:151], a[24:27], a[112:115], 0\n" \
"v_mfma_f32_16x16x32_bf16 a[148:151], a[28:31], a[116:119], a[148:151]\n" \
"v_mfma_f32_16x16x32_bf16 a[160:163], a[32:35], a[112:115], 0\n" \
"v_mfma_f32_16x16x32_bf16 a[160:163], a[36:39], a[116:119], a[160:163]\n" \
"buffer_load_dwordx2 a[192:193], v54, s[40:43], 0 offen offset:0 nt\n" \
"v_mfma_f32_16x16x32_bf16 a[164:167], a[40:43], a[112:115], 0\n" \
"v_mfma_f32_16x16x32_bf16 a[164:167], a[44:47], a[116:119], a[164:167]\n" \
"s_waitcnt vmcnt(23) lgkmcnt(0)\n" \
"v_mfma_f32_16x16x32_bf16 a[176:179], a[48:51], a[112:115], 0\n" \
"s_barrier\n" \
"v_mfma_f32_16x16x32_bf16 a[176:179], a[52:55], a[116:119], a[176:179]\n" \
"buffer_load_dwordx2 a[194:195], v54, s[40:43], 0 offen offset:2048 nt\n" \
"v_mfma_f32_16x16x32_bf16 a[180:183], a[56:59], a[112:115], 0\n" \
"v_mfma_f32_16x16x32_bf16 a[180:183], a[60:63], a[116:119], a[180:183]\n" \
"v_mfma_f32_16x16x32_bf16 a[136:139], a[0:3], a[120:123], 0\n" \
"v_mfma_f32_16x16x32_bf16 a[136:139], a[4:7], a[124:127], a[136:139]\n" \
"buffer_load_dwordx2 a[196:197], v55, s[40:43], 0 offen offset:0 nt\n" \
"v_mfma_f32_16x16x32_bf16 a[140:143], a[8:11], a[120:123], 0\n" \
"s_add_u32 s84, s84, 1048576\ns_addc_u32 s85, s85, 0\n" \
"ds_read_b128 a[0:3], v5 offset:0\n" \
"v_mfma_f32_16x16x32_bf16 a[140:143], a[12:15], a[124:127], a[140:143]\n" \
"s_mov_b32 s52, 0\n" \
"ds_read_b128 a[4:7], v6 offset:0\n" \
"v_mfma_f32_16x16x32_bf16 a[152:155], a[16:19], a[120:123], 0\n" \
"ds_read_b128 a[8:11], v5 offset:2048\n" \
"v_mfma_f32_16x16x32_bf16 a[152:155], a[20:23], a[124:127], a[152:155]\n" \
"ds_read_b128 a[12:15], v6 offset:2048\n" \
"v_mfma_f32_16x16x32_bf16 a[156:159], a[24:27], a[120:123], 0\n" \
"ds_read_b128 a[16:19], v5 offset:4096\n" \
"v_mfma_f32_16x16x32_bf16 a[156:159], a[28:31], a[124:127], a[156:159]\n" \
"ds_read_b128 a[20:23], v6 offset:4096\n" \
"v_mfma_f32_16x16x32_bf16 a[168:171], a[32:35], a[120:123], 0\n" \
"ds_read_b128 a[24:27], v5 offset:6144\n" \
"v_mfma_f32_16x16x32_bf16 a[168:171], a[36:39], a[124:127], a[168:171]\n" \
"ds_read_b128 a[28:31], v6 offset:6144\n" \
"v_mfma_f32_16x16x32_bf16 a[172:175], a[40:43], a[120:123], 0\n" \
"ds_read_b128 a[32:35], v5 offset:8192\n" \
"v_mfma_f32_16x16x32_bf16 a[172:175], a[44:47], a[124:127], a[172:175]\n" \
"ds_read_b128 a[36:39], v6 offset:8192\n" \
"v_mfma_f32_16x16x32_bf16 a[184:187], a[48:51], a[120:123], 0\n" \
"ds_read_b128 a[40:43], v5 offset:10240\n" \
"v_mfma_f32_16x16x32_bf16 a[184:187], a[52:55], a[124:127], a[184:187]\n" \
"ds_read_b128 a[44:47], v6 offset:10240\n" \
"v_mfma_f32_16x16x32_bf16 a[188:191], a[56:59], a[120:123], 0\n" \
"ds_read_b128 a[48:51], v5 offset:12288\n" \
"v_mfma_f32_16x16x32_bf16 a[188:191], a[60:63], a[124:127], a[188:191]\n" \
"s_waitcnt lgkmcnt(9)\n" \
"ds_read_b128 a[52:55], v6 offset:12288\n" \
"v_mfma_f32_16x16x32_bf16 v[64:67], a[0:3], a[64:67], v[64:67]\n" \
"s_mov_b32 s47, 1572864\n" \
"s_add_u32 m0, s22, 32768\n" \
"ds_read_b128 a[56:59], v5 offset:14336\n" \
"v_mfma_f32_16x16x32_bf16 v[64:67], a[4:7], a[68:71], v[64:67]\n" \
"buffer_load_dwordx4 v21, s[32:35], s47 offen offset:0 lds\n" \
"v_mfma_f32_16x16x32_bf16 v[68:71], a[8:11], a[64:67], v[68:71]\n" \
"s_waitcnt lgkmcnt(7)\n" \
"ds_read_b128 a[60:63], v6 offset:14336\n" \
"v_mfma_f32_16x16x32_bf16 v[68:71], a[12:15], a[68:71], v[68:71]\n" \
"v_mfma_f32_16x16x32_bf16 v[80:83], a[16:19], a[64:67], v[80:83]\n" \
"ds_read_b64_tr_b16 a[96:97], v9 offset:49152\n" \
"v_mfma_f32_16x16x32_bf16 v[80:83], a[20:23], a[68:71], v[80:83]\n" \
"s_mov_b64 exec, 0xffff\nglobal_load_dword v22, v1, s[84:85]\ns_mov_b64 exec, -1\n" \
"v_mfma_f32_16x16x32_bf16 v[84:87], a[24:27], a[64:67], v[84:87]\n" \
"s_waitcnt lgkmcnt(5)\n" \
"ds_read_b64_tr_b16 a[98:99], v10 offset:49152\n" \
"v_mfma_f32_16x16x32_bf16 v[84:87], a[28:31], a[68:71], v[84:87]\n" \
"v_mfma_f32_16x16x32_bf16 v[96:99], a[32:35], a[64:67], v[96:99]\n" \
"s_mov_b32 s46, 384\n" \
"s_add_u32 m0, s50, 81920\n" \
"ds_read_b64_tr_b16 a[100:101], v9 offset:50176\n" \
"v_mfma_f32_16x16x32_bf16 v[96:99], a[36:39], a[68:71], v[96:99]\n" \
"buffer_load_dwordx4 v17, s[28:31], s46 offen lds\n" \
"v_mfma_f32_16x16x32_bf16 v[100:103], a[40:43], a[64:67], v[100:103]\n" \
"s_waitcnt lgkmcnt(3)\n" \
"ds_read_b64_tr_b16 a[102:103], v10 offset:50176\n" \
"v_mfma_f32_16x16x32_bf16 v[100:103], a[44:47], a[68:71], v[100:103]\n" \
"v_mfma_f32_16x16x32_bf16 v[112:115], a[48:51], a[64:67], v[112:115]\n" \
"ds_read_b64_tr_b16 a[104:105], v11 offset:49152\n" \
"v_mfma_f32_16x16x32_bf16 v[112:115], a[52:55], a[68:71], v[112:115]\n" \
"buffer_load_dwordx2 a[198:199], v59, s[40:43], s52 offen offset:0 nt\n" \
"v_mfma_f32_16x16x32_bf16 v[116:119], a[56:59], a[64:67], v[116:119]\n" \
"ds_read_b64_tr_b16 a[106:107], v12 offset:49152\n" \
"v_mfma_f32_16x16x32_bf16 v[116:119], a[60:63], a[68:71], v[116:119]\n" \
"v_mfma_f32_16x16x32_bf16 v[72:75], a[0:3], a[72:75], v[72:75]\n" \
"s_add_u32 m0, s22, 34560\n" \
"ds_read_b64_tr_b16 a[108:109], v11 offset:50176\n" \
"v_mfma_f32_16x16x32_bf16 v[72:75], a[4:7], a[76:79], v[72:75]\n" \
"buffer_load_dwordx4 v21, s[32:35], s47 offen offset:256 lds\n" \
"v_mfma_f32_16x16x32_bf16 v[76:79], a[8:11], a[72:75], v[76:79]\n" \
"ds_read_b64_tr_b16 a[110:111], v12 offset:50176\n" \
"v_mfma_f32_16x16x32_bf16 v[76:79], a[12:15], a[76:79], v[76:79]\n" \
"v_mfma_f32_16x16x32_bf16 v[88:91], a[16:19], a[72:75], v[88:91]\n" \
"ds_read_b64_tr_b16 a[112:113], v9 offset:49664\n" \
"v_mfma_f32_16x16x32_bf16 v[88:91], a[20:23], a[76:79], v[88:91]\n" \
"buffer_load_dwordx2 a[214:215], v59, s[80:83], s52 offen offset:0 nt\n" \
"v_mfma_f32_16x16x32_bf16 v[92:95], a[24:27], a[72:75], v[92:95]\n" \
"ds_read_b64_tr_b16 a[114:115], v10 offset:49664\n" \
"v_mfma_f32_16x16x32_bf16 v[92:95], a[28:31], a[76:79], v[92:95]\n" \
"v_mfma_f32_16x16x32_bf16 v[104:107], a[32:35], a[72:75], v[104:107]\n" \
"s_add_u32 m0, s50, 86016\n" \
"ds_read_b64_tr_b16 a[116:117], v9 offset:50688\n" \
"v_mfma_f32_16x16x32_bf16 v[104:107], a[36:39], a[76:79], v[104:107]\n" \
"buffer_load_dwordx4 v18, s[28:31], s46 offen lds\n" \
"v_mfma_f32_16x16x32_bf16 v[108:111], a[40:43], a[72:75], v[108:111]\n" \
"ds_read_b64_tr_b16 a[118:119], v10 offset:50688\n" \
"v_mfma_f32_16x16x32_bf16 v[108:111], a[44:47], a[76:79], v[108:111]\n" \
"v_mfma_f32_16x16x32_bf16 v[120:123], a[48:51], a[72:75], v[120:123]\n" \
"ds_read_b64_tr_b16 a[120:121], v11 offset:49664\n" \
"v_mfma_f32_16x16x32_bf16 v[120:123], a[52:55], a[76:79], v[120:123]\n" \
"buffer_load_dwordx2 a[200:201], v59, s[40:43], s52 offen offset:2048 nt\n" \
"v_mfma_f32_16x16x32_bf16 v[124:127], a[56:59], a[72:75], v[124:127]\n" \
"ds_read_b64_tr_b16 a[122:123], v12 offset:49664\n" \
"v_mfma_f32_16x16x32_bf16 v[124:127], a[60:63], a[76:79], v[124:127]\n" \
"v_mfma_f32_16x16x32_bf16 v[128:131], a[0:3], a[80:83], v[128:131]\n" \
"s_mov_b32 s47, 1835008\n" \
"s_add_u32 m0, s22, 33792\n" \
"ds_read_b64_tr_b16 a[124:125], v11 offset:50688\n" \
"v_mfma_f32_16x16x32_bf16 v[128:131], a[4:7], a[84:87], v[128:131]\n" \
"buffer_load_dwordx4 v21, s[32:35], s47 offen offset:0 lds\n" \
"v_mfma_f32_16x16x32_bf16 v[132:135], a[8:11], a[80:83], v[132:135]\n" \
"ds_read_b64_tr_b16 a[126:127], v12 offset:50688\n" \
"v_mfma_f32_16x16x32_bf16 v[132:135], a[12:15], a[84:87], v[132:135]\n" \
"v_mfma_f32_16x16x32_bf16 v[144:147], a[16:19], a[80:83], v[144:147]\n" \
"v_mfma_f32_16x16x32_bf16 v[144:147], a[20:23], a[84:87], v[144:147]\n" \
"buffer_load_dwordx2 a[216:217], v59, s[80:83], s52 offen offset:2048 nt\n" \
"v_mfma_f32_16x16x32_bf16 v[148:151], a[24:27], a[80:83], v[148:151]\n" \
"s_mov_b32 s52, 4096\n" \
"v_mfma_f32_16x16x32_bf16 v[148:151], a[28:31], a[84:87], v[148:151]\n" \
"v_mfma_f32_16x16x32_bf16 v[160:163], a[32:35], a[80:83], v[160:163]\n" \
"s_add_u32 m0, s50, 90112\n" \
"v_mfma_f32_16x16x32_bf16 v[160:163], a[36:39], a[84:87], v[160:163]\n" \
"buffer_load_dwordx4 v19, s[28:31], s46 offen lds\n" \
"v_mfma_f32_16x16x32_bf16 v[164:167], a[40:43], a[80:83], v[164:167]\n" \
"v_mfma_f32_16x16x32_bf16 v[164:167], a[44:47], a[84:87], v[164:167]\n" \
"s_waitcnt vmcnt(24) lgkmcnt(0)\n" \
"v_mfma_f32_16x16x32_bf16 v[176:179], a[48:51], a[80:83], v[176:179]\n" \
"s_barrier\n" \
"v_mfma_f32_16x16x32_bf16 v[176:179], a[52:55], a[84:87], v[176:179]\n" \
"buffer_load_dwordx2 a[202:203], v59, s[40:43], s52 offen offset:0 nt\n" \
"v_mfma_f32_16x16x32_bf16 v[180:183], a[56:59], a[80:83], v[180:183]\n" \
"v_mfma_f32_16x16x32_bf16 v[180:183], a[60:63], a[84:87], v[180:183]\n" \
"v_mfma_f32_16x16x32_bf16 v[136:139], a[0:3], a[88:91], v[136:139]\n" \
"s_add_u32 m0, s22, 35584\n" \
"v_mfma_f32_16x16x32_bf16 v[136:139], a[4:7], a[92:95], v[136:139]\n" \
"buffer_load_dwordx4 v21, s[32:35], s47 offen offset:256 lds\n" \
"v_mfma_f32_16x16x32_bf16 v[140:143], a[8:11], a[88:91], v[140:143]\n" \
"v_mfma_f32_16x16x32_bf16 v[140:143], a[12:15], a[92:95], v[140:143]\n" \
"v_mfma_f32_16x16x32_bf16 v[152:155], a[16:19], a[88:91], v[152:155]\n" \
"v_mfma_f32_16x16x32_bf16 v[152:155], a[20:23], a[92:95], v[152:155]\n" \
"buffer_load_dwordx2 a[218:219], v59, s[80:83], s52 offen offset:0 nt\n" \
"v_mfma_f32_16x16x32_bf16 v[156:159], a[24:27], a[88:91], v[156:159]\n" \
"v_mfma_f32_16x16x32_bf16 v[156:159], a[28:31], a[92:95], v[156:159]\n" \
"v_mfma_f32_16x16x32_bf16 v[168:171], a[32:35], a[88:91], v[168:171]\n" \
"s_add_u32 m0, s50, 94208\n" \
"v_mfma_f32_16x16x32_bf16 v[168:171], a[36:39], a[92:95], v[168:171]\n" \
"buffer_load_dwordx4 v20, s[28:31], s46 offen lds\n" \
"v_mfma_f32_16x16x32_bf16 v[172:175], a[40:43], a[88:91], v[172:175]\n" \
"v_mfma_f32_16x16x32_bf16 v[172:175], a[44:47], a[92:95], v[172:175]\n" \
"v_mfma_f32_16x16x32_bf16 v[184:187], a[48:51], a[88:91], v[184:187]\n" \
"v_mfma_f32_16x16x32_bf16 v[184:187], a[52:55], a[92:95], v[184:187]\n" \
"buffer_load_dwordx2 a[204:205], v59, s[40:43], s52 offen offset:2048 nt\n" \
"v_mfma_f32_16x16x32_bf16 v[188:191], a[56:59], a[88:91], v[188:191]\n" \
"v_mfma_f32_16x16x32_bf16 v[188:191], a[60:63], a[92:95], v[188:191]\n" \
"v_mfma_f32_16x16x32_bf16 v[192:195], a[0:3], a[96:99], v[192:195]\n" \
"s_mov_b32 s47, 1572864\n" \
"s_add_u32 m0, s22, 49024\n" \
"v_mfma_f32_16x16x32_bf16 v[192:195], a[4:7], a[100:103], v[192:195]\n" \
"buffer_load_dwordx4 v21, s[32:35], s47 offen offset:128 lds\n" \
"v_mfma_f32_16x16x32_bf16 v[196:199], a[8:11], a[96:99], v[196:199]\n" \
"v_mfma_f32_16x16x32_bf16 v[196:199], a[12:15], a[100:103], v[196:199]\n" \
"v_mfma_f32_16x16x32_bf16 v[208:211], a[16:19], a[96:99], v[208:211]\n" \
"s_mov_b32 s46, 512\n" \
"s_add_u32 m0, s50, 0\n" \
"ds_read_b64_tr_b16 a[64:65], v9 offset:0\n" \
"v_mfma_f32_16x16x32_bf16 v[208:211], a[20:23], a[100:103], v[208:211]\n" \
"LH4_%=:\ns_cbranch_execz LH8_%=\n" \
"buffer_load_dwordx4 v13, s[28:31], s46 offen lds\n" \
"v_mfma_f32_16x16x32_bf16 v[212:215], a[24:27], a[96:99], v[212:215]\n" \
"ds_read_b64_tr_b16 a[66:67], v10 offset:0\n" \
"v_mfma_f32_16x16x32_bf16 v[212:215], a[28:31], a[100:103], v[212:215]\n" \
"s_waitcnt vmcnt(12)\n" \
"v_accvgpr_read_b32 v43, a230\n" \
"v_lshlrev_b32 v42, 16, v43\n" \
"v_mfma_f32_16x16x32_bf16 v[224:227], a[32:35], a[96:99], v[224:227]\n" \
"ds_read_b64_tr_b16 a[68:69], v9 offset:1024\n" \
"v_and_b32 v43, 0xffff0000, v43\n" \
"v_mfma_f32_16x16x32_bf16 v[224:227], a[36:39], a[100:103], v[224:227]\n" \
"buffer_load_dwordx2 a[220:221], v59, s[80:83], s52 offen offset:2048 nt\n" \
"v_mfma_f32_16x16x32_bf16 v[228:231], a[40:43], a[96:99], v[228:231]\n" \
"s_mov_b32 s52, 8192\n" \
"ds_read_b64_tr_b16 a[70:71], v10 offset:1024\n" \
"v_accvgpr_read_b32 v45, a231\n" \
"v_mfma_f32_16x16x32_bf16 v[228:231], a[44:47], a[100:103], v[228:231]\n" \
"v_lshlrev_b32 v44, 16, v45\n" \
"v_and_b32 v45, 0xffff0000, v45\n" \
"v_mfma_f32_16x16x32_bf16 v[240:243], a[48:51], a[96:99], v[240:243]\n" \
"s_add_u32 m0, s22, 50816\n" \
"ds_read_b64_tr_b16 a[72:73], v11 offset:0\n" \
"v_accvgpr_read_b32 v47, a232\n" \
"v_mfma_f32_16x16x32_bf16 v[240:243], a[52:55], a[100:103], v[240:243]\n" \
"buffer_load_dwordx4 v21, s[32:35], s47 offen offset:384 lds\n" \
"v_mfma_f32_16x16x32_bf16 v[244:247], a[56:59], a[96:99], v[244:247]\n" \
"ds_read_b64_tr_b16 a[74:75], v12 offset:0\n" \
"v_lshlrev_b32 v46, 16, v47\n" \
"v_mfma_f32_16x16x32_bf16 v[244:247], a[60:63], a[100:103], v[244:247]\n" \
"v_and_b32 v47, 0xffff0000, v47\n" \
"v_accvgpr_read_b32 v49, a233\n" \
"v_mfma_f32_16x16x32_bf16 v[200:203], a[0:3], a[104:107], v[200:203]\n" \
"s_add_u32 m0, s50, 4096\n" \
"ds_read_b64_tr_b16 a[76:77], v11 offset:1024\n" \
"v_lshlrev_b32 v48, 16, v49\n" \
"v_mfma_f32_16x16x32_bf16 v[200:203], a[4:7], a[108:111], v[200:203]\n" \
"buffer_load_dwordx4 v14, s[28:31], s46 offen lds\n" \
"v_mfma_f32_16x16x32_bf16 v[204:207], a[8:11], a[104:107], v[204:207]\n" \
"ds_read_b64_tr_b16 a[78:79], v12 offset:1024\n" \
"v_and_b32 v49, 0xffff0000, v49\n" \
"v_mfma_f32_16x16x32_bf16 v[204:207], a[12:15], a[108:111], v[204:207]\n" \
"v_accvgpr_read_b32 v36, a192\n" \
"v_lshlrev_b32 v30, 16, v36\n" \
"v_mfma_f32_16x16x32_bf16 v[216:219], a[16:19], a[104:107], v[216:219]\n" \
"ds_read_b64_tr_b16 a[80:81], v9 offset:512\n" \
"v_and_b32 v36, 0xffff0000, v36\n" \
"v_mfma_f32_16x16x32_bf16 v[216:219], a[20:23], a[108:111], v[216:219]\n" \
"buffer_load_dwordx2 a[206:207], v59, s[40:43], s52 offen offset:0 nt\n" \
"v_mfma_f32_16x16x32_bf16 v[220:223], a[24:27], a[104:107], v[220:223]\n" \
"ds_read_b64_tr_b16 a[82:83], v10 offset:512\n" \
"v_accvgpr_read_b32 v37, a194\n" \
"v_mfma_f32_16x16x32_bf16 v[220:223], a[28:31], a[108:111], v[220:223]\n" \
"v_lshlrev_b32 v31, 16, v37\n" \
"v_and_b32 v37, 0xffff0000, v37\n" \
"v_mfma_f32_16x16x32_bf16 v[232:235], a[32:35], a[104:107], v[232:235]\n" \
"s_mov_b32 s47, 1835008\n" \
"s_add_u32 m0, s22, 50048\n" \
"ds_read_b64_tr_b16 a[84:85], v9 offset:1536\n" \
"v_mfma_f32_16x16x32_bf16 v[232:235], a[36:39], a[108:111], v[232:235]\n" \
"buffer_load_dwordx4 v21, s[32:35], s47 offen offset:128 lds\n" \
"v_mfma_f32_16x16x32_bf16 v[236:239], a[40:43], a[104:107], v[236:239]\n" \
"ds_read_b64_tr_b16 a[86:87], v10 offset:1536\n" \
"v_accvgpr_read_b32 v38, a196\n" \
"v_mfma_f32_16x16x32_bf16 v[236:239], a[44:47], a[108:111], v[236:239]\n" \
"v_lshlrev_b32 v32, 16, v38\n" \
"v_and_b32 v38, 0xffff0000, v38\n" \
"v_mfma_f32_16x16x32_bf16 v[248:251], a[48:51], a[104:107], v[248:251]\n" \
"s_add_u32 m0, s50, 8192\n" \
"ds_read_b64_tr_b16 a[88:89], v11 offset:512\n" \
"v_accvgpr_read_b32 v39, a198\n" \
"v_mfma_f32_16x16x32_bf16 v[248:251], a[52:55], a[108:111], v[248:251]\n" \
"buffer_load_dwordx4 v15, s[28:31], s46 offen lds\n" \
"v_mfma_f32_16x16x32_bf16 v[252:255], a[56:59], a[104:107], v[252:255]\n" \
"ds_read_b64_tr_b16 a[90:91], v12 offset:512\n" \
"v_lshlrev_b32 v33, 16, v39\n" \
"v_mfma_f32_16x16x32_bf16 v[252:255], a[60:63], a[108:111], v[252:255]\n" \
"v_and_b32 v39, 0xffff0000, v39\n" \
"v_mul_f32 v25, v30, v45\n" \
"v_mfma_f32_16x16x32_bf16 a[128:131], a[0:3], a[112:115], a[128:131]\n" \
"ds_read_b64_tr_b16 a[92:93], v11 offset:1536\n" \
"v_mul_f32 v26, v36, v49\n" \
"v_mfma_f32_16x16x32_bf16 a[128:131], a[4:7], a[116:119], a[128:131]\n" \
"buffer_load_dwordx2 a[222:223], v59, s[80:83], s52 offen offset:0 nt\n" \
"v_mfma_f32_16x16x32_bf16 a[132:135], a[8:11], a[112:115], a[132:135]\n" \
"ds_read_b64_tr_b16 a[94:95], v12 offset:1536\n" \
"v_fmac_f32 v25, v31, v44\n" \
"v_mfma_f32_16x16x32_bf16 a[132:135], a[12:15], a[116:119], a[132:135]\n" \
"v_fmac_f32 v26, v37, v48\n" \
"v_fmac_f32 v25, v32, v43\n" \
"v_mfma_f32_16x16x32_bf16 a[144:147], a[16:19], a[112:115], a[144:147]\n" \
"s_add_u32 m0, s22, 51840\n" \
"v_fmac_f32 v26, v38, v47\n" \
"v_fmac_f32 v25, v33, v42\n" \
"v_mfma_f32_16x16x32_bf16 a[144:147], a[20:23], a[116:119], a[144:147]\n" \
"buffer_load_dwordx4 v21, s[32:35], s47 offen offset:384 lds\n" \
"v_mfma_f32_16x16x32_bf16 a[148:151], a[24:27], a[112:115], a[148:151]\n" \
"v_fmac_f32 v26, v39, v46\n" \
"v_accvgpr_read_b32 v28, a214\n" \
"v_mfma_f32_16x16x32_bf16 a[148:151], a[28:31], a[116:119], a[148:151]\n" \
"v_lshlrev_b32 v27, 16, v28\n" \
"v_and_b32 v28, 0xffff0000, v28\n" \
"v_mfma_f32_16x16x32_bf16 a[160:163], a[32:35], a[112:115], a[160:163]\n" \
"v_mul_f32 v29, v25, v27\n" \
"v_mfma_f32_16x16x32_bf16 a[160:163], a[36:39], a[116:119], a[160:163]\n" \
"buffer_load_dwordx2 a[208:209], v59, s[40:43], s52 offen offset:2048 nt\n" \
"v_mfma_f32_16x16x32_bf16 a[164:167], a[40:43], a[112:115], a[164:167]\n" \
"s_waitcnt vmcnt(17)\n" \
"v_mul_f32 v58, v26, v28\n" \
"v_accvgpr_read_b32 v40, a200\n" \
"v_mfma_f32_16x16x32_bf16 a[164:167], a[44:47], a[116:119], a[164:167]\n" \
"s_waitcnt vmcnt(33) lgkmcnt(0)\n" \
"v_lshlrev_b32 v34, 16, v40\n" \
"v_and_b32 v40, 0xffff0000, v40\n" \
"v_mfma_f32_16x16x32_bf16 a[176:179], a[48:51], a[112:115], a[176:179]\n" \
"s_add_u32 m0, s50, 12288\n" \
"s_barrier\n" \
"v_mul_f32 v25, v31, v45\n" \
"v_mfma_f32_16x16x32_bf16 a[176:179], a[52:55], a[116:119], a[176:179]\n" \
"buffer_load_dwordx4 v16, s[28:31], s46 offen lds\n" \
"v_mfma_f32_16x16x32_bf16 a[180:183], a[56:59], a[112:115], a[180:183]\n" \
"v_mul_f32 v26, v37, v49\n" \
"v_fmac_f32 v25, v32, v44\n" \
"v_mfma_f32_16x16x32_bf16 a[180:183], a[60:63], a[116:119], a[180:183]\n" \
"v_fmac_f32 v26, v38, v48\n" \
"v_fmac_f32 v25, v33, v43\n" \
"v_mfma_f32_16x16x32_bf16 a[136:139], a[0:3], a[120:123], a[136:139]\n" \
"v_fmac_f32 v26, v39, v47\n" \
"v_mfma_f32_16x16x32_bf16 a[136:139], a[4:7], a[124:127], a[136:139]\n" \
"buffer_load_dwordx2 a[224:225], v59, s[80:83], s52 offen offset:2048 nt\n" \
"v_mfma_f32_16x16x32_bf16 a[140:143], a[8:11], a[120:123], a[140:143]\n" \
"s_mov_b32 s52, 12288\n" \
"ds_read_b128 a[0:3], v5 offset:32768\n" \
"v_fmac_f32 v25, v34, v42\n" \
"v_mfma_f32_16x16x32_bf16 a[140:143], a[12:15], a[124:127], a[140:143]\n" \
"ds_read_b128 a[4:7], v6 offset:32768\n" \
"v_fmac_f32 v26, v40, v46\n" \
"v_mfma_f32_16x16x32_bf16 a[152:155], a[16:19], a[120:123], a[152:155]\n" \
"ds_read_b128 a[8:11], v5 offset:34816\n" \
"v_accvgpr_read_b32 v28, a216\n" \
"v_mfma_f32_16x16x32_bf16 a[152:155], a[20:23], a[124:127], a[152:155]\n" \
"ds_read_b128 a[12:15], v6 offset:34816\n" \
"v_lshlrev_b32 v27, 16, v28\n" \
"v_mfma_f32_16x16x32_bf16 a[156:159], a[24:27], a[120:123], a[156:159]\n" \
"ds_read_b128 a[16:19], v5 offset:36864\n" \
"v_and_b32 v28, 0xffff0000, v28\n" \
"v_mfma_f32_16x16x32_bf16 a[156:159], a[28:31], a[124:127], a[156:159]\n" \
"ds_read_b128 a[20:23], v6 offset:36864\n" \
"v_mul_f32 v25, v25, v27\n" \
"v_mfma_f32_16x16x32_bf16 a[168:171], a[32:35], a[120:123], a[168:171]\n" \
"ds_read_b128 a[24:27], v5 offset:38912\n" \
"v_mul_f32 v26, v26, v28\n" \
"v_mfma_f32_16x16x32_bf16 a[168:171], a[36:39], a[124:127], a[168:171]\n" \
"ds_read_b128 a[28:31], v6 offset:38912\n" \
"v_cvt_pk_bf16_f32 v53, v25, v29\n" \
"v_mfma_f32_16x16x32_bf16 a[172:175], a[40:43], a[120:123], a[172:175]\n" \
"ds_read_b128 a[32:35], v5 offset:40960\n" \
"v_cvt_pk_bf16_f32 v57, v26, v58\n" \
"v_mfma_f32_16x16x32_bf16 a[172:175], a[44:47], a[124:127], a[172:175]\n" \
"ds_read_b128 a[36:39], v6 offset:40960\n" \
"s_waitcnt vmcnt(15)\n" \
"v_mfma_f32_16x16x32_bf16 a[184:187], a[48:51], a[120:123], a[184:187]\n" \
"ds_read_b128 a[40:43], v5 offset:43008\n" \
"v_accvgpr_read_b32 v41, a202\n" \
"v_mfma_f32_16x16x32_bf16 a[184:187], a[52:55], a[124:127], a[184:187]\n" \
"ds_read_b128 a[44:47], v6 offset:43008\n" \
"v_lshlrev_b32 v35, 16, v41\n" \
"v_mfma_f32_16x16x32_bf16 a[188:191], a[56:59], a[120:123], a[188:191]\n" \
"ds_read_b128 a[48:51], v5 offset:45056\n" \
"v_and_b32 v41, 0xffff0000, v41\n" \
"v_mfma_f32_16x16x32_bf16 a[188:191], a[60:63], a[124:127], a[188:191]\n" \
"s_waitcnt lgkmcnt(9)\n" \
"ds_read_b128 a[52:55], v6 offset:45056\n" \
"v_mul_f32 v25, v32, v45\n" \
"v_mfma_f32_16x16x32_bf16 v[64:67], a[0:3], a[64:67], v[64:67]\n" \
"s_mov_b32 s47, 2097152\n" \
"s_add_u32 m0, s22, 0\n" \
"ds_read_b128 a[56:59], v5 offset:47104\n" \
"v_mfma_f32_16x16x32_bf16 v[64:67], a[4:7], a[68:71], v[64:67]\n" \
"buffer_load_dwordx4 v21, s[32:35], s47 offen offset:0 lds\n" \
"v_mfma_f32_16x16x32_bf16 v[68:71], a[8:11], a[64:67], v[68:71]\n" \
"s_waitcnt lgkmcnt(7)\n" \
"ds_read_b128 a[60:63], v6 offset:47104\n" \
"v_mul_f32 v26, v38, v49\n" \
"v_mfma_f32_16x16x32_bf16 v[68:71], a[12:15], a[68:71], v[68:71]\n" \
"v_fmac_f32 v25, v33, v44\n" \
"v_fmac_f32 v26, v39, v48\n" \
"v_mfma_f32_16x16x32_bf16 v[80:83], a[16:19], a[64:67], v[80:83]\n" \
"ds_read_b64_tr_b16 a[96:97], v9 offset:16384\n" \
"v_fmac_f32 v25, v34, v43\n" \
"v_mfma_f32_16x16x32_bf16 v[80:83], a[20:23], a[68:71], v[80:83]\n" \
"buffer_load_dwordx2 a[210:211], v59, s[40:43], s52 offen offset:0 nt\n" \
"v_mfma_f32_16x16x32_bf16 v[84:87], a[24:27], a[64:67], v[84:87]\n" \
"s_waitcnt lgkmcnt(5)\n" \
"ds_read_b64_tr_b16 a[98:99], v10 offset:16384\n" \
"v_fmac_f32 v26, v40, v47\n" \
"v_mfma_f32_16x16x32_bf16 v[84:87], a[28:31], a[68:71], v[84:87]\n" \
"v_fmac_f32 v25, v35, v42\n" \
"v_fmac_f32 v26, v41, v46\n" \
"v_mfma_f32_16x16x32_bf16 v[96:99], a[32:35], a[64:67], v[96:99]\n" \
"s_mov_b32 s46, 512\n" \
"s_add_u32 m0, s50, 16384\n" \
"ds_read_b64_tr_b16 a[100:101], v9 offset:17408\n" \
"v_mfma_f32_16x16x32_bf16 v[96:99], a[36:39], a[68:71], v[96:99]\n" \
"buffer_load_dwordx4 v17, s[28:31], s46 offen lds\n" \
"v_mfma_f32_16x16x32_bf16 v[100:103], a[40:43], a[64:67], v[100:103]\n" \
"s_waitcnt lgkmcnt(3)\n" \
"ds_read_b64_tr_b16 a[102:103], v10 offset:17408\n" \
"v_accvgpr_read_b32 v28, a218\n" \
"v_mfma_f32_16x16x32_bf16 v[100:103], a[44:47], a[68:71], v[100:103]\n" \
"v_lshlrev_b32 v27, 16, v28\n" \
"v_and_b32 v28, 0xffff0000, v28\n" \
"v_mfma_f32_16x16x32_bf16 v[112:115], a[48:51], a[64:67], v[112:115]\n" \
"ds_read_b64_tr_b16 a[104:105], v11 offset:16384\n" \
"v_mul_f32 v29, v25, v27\n" \
"v_mfma_f32_16x16x32_bf16 v[112:115], a[52:55], a[68:71], v[112:115]\n" \
"buffer_load_dwordx2 a[226:227], v59, s[80:83], s52 offen offset:0 nt\n" \
"v_mfma_f32_16x16x32_bf16 v[116:119], a[56:59], a[64:67], v[116:119]\n" \
"ds_read_b64_tr_b16 a[106:107], v12 offset:16384\n" \
"v_mul_f32 v58, v26, v28\n" \
"v_mfma_f32_16x16x32_bf16 v[116:119], a[60:63], a[68:71], v[116:119]\n" \
"s_waitcnt vmcnt(14)\n" \
"v_accvgpr_read_b32 v36, a204\n" \
"v_lshlrev_b32 v30, 16, v36\n" \
"v_mfma_f32_16x16x32_bf16 v[72:75], a[0:3], a[72:75], v[72:75]\n" \
"s_add_u32 m0, s22, 1792\n" \
"ds_read_b64_tr_b16 a[108:109], v11 offset:17408\n" \
"v_and_b32 v36, 0xffff0000, v36\n" \
"v_mfma_f32_16x16x32_bf16 v[72:75], a[4:7], a[76:79], v[72:75]\n" \
"buffer_load_dwordx4 v21, s[32:35], s47 offen offset:256 lds\n" \
"v_mfma_f32_16x16x32_bf16 v[76:79], a[8:11], a[72:75], v[76:79]\n" \
"ds_read_b64_tr_b16 a[110:111], v12 offset:17408\n" \
"v_mul_f32 v25, v33, v45\n" \
"v_mfma_f32_16x16x32_bf16 v[76:79], a[12:15], a[76:79], v[76:79]\n" \
"v_mul_f32 v26, v39, v49\n" \
"v_fmac_f32 v25, v34, v44\n" \
"v_mfma_f32_16x16x32_bf16 v[88:91], a[16:19], a[72:75], v[88:91]\n" \
"ds_read_b64_tr_b16 a[112:113], v9 offset:16896\n" \
"v_fmac_f32 v26, v40, v48\n" \
"v_mfma_f32_16x16x32_bf16 v[88:91], a[20:23], a[76:79], v[88:91]\n" \
"buffer_load_dwordx2 a[212:213], v59, s[40:43], s52 offen offset:2048 nt\n" \
"v_mfma_f32_16x16x32_bf16 v[92:95], a[24:27], a[72:75], v[92:95]\n" \
"ds_read_b64_tr_b16 a[114:115], v10 offset:16896\n" \
"v_fmac_f32 v25, v35, v43\n" \
"v_mfma_f32_16x16x32_bf16 v[92:95], a[28:31], a[76:79], v[92:95]\n" \
"v_fmac_f32 v26, v41, v47\n" \
"v_fmac_f32 v25, v30, v42\n" \
"v_mfma_f32_16x16x32_bf16 v[104:107], a[32:35], a[72:75], v[104:107]\n" \
"s_add_u32 m0, s50, 20480\n" \
"ds_read_b64_tr_b16 a[116:117], v9 offset:17920\n" \
"v_fmac_f32 v26, v36, v46\n" \
"v_mfma_f32_16x16x32_bf16 v[104:107], a[36:39], a[76:79], v[104:107]\n" \
"buffer_load_dwordx4 v18, s[28:31], s46 offen lds\n" \
"v_mfma_f32_16x16x32_bf16 v[108:111], a[40:43], a[72:75], v[108:111]\n" \
"ds_read_b64_tr_b16 a[118:119], v10 offset:17920\n" \
"v_accvgpr_read_b32 v28, a220\n" \
"v_mfma_f32_16x16x32_bf16 v[108:111], a[44:47], a[76:79], v[108:111]\n" \
"v_lshlrev_b32 v27, 16, v28\n" \
"v_and_b32 v28, 0xffff0000, v28\n" \
"v_mfma_f32_16x16x32_bf16 v[120:123], a[48:51], a[72:75], v[120:123]\n" \
"ds_read_b64_tr_b16 a[120:121], v11 offset:16896\n" \
"v_mul_f32 v25, v25, v27\n" \
"v_mfma_f32_16x16x32_bf16 v[120:123], a[52:55], a[76:79], v[120:123]\n" \
"buffer_load_dwordx2 a[228:229], v59, s[80:83], s52 offen offset:2048 nt\n" \
"v_mfma_f32_16x16x32_bf16 v[124:127], a[56:59], a[72:75], v[124:127]\n" \
"ds_read_b64_tr_b16 a[122:123], v12 offset:16896\n" \
"v_mul_f32 v26, v26, v28\n" \
"v_mfma_f32_16x16x32_bf16 v[124:127], a[60:63], a[76:79], v[124:127]\n" \
"s_waitcnt vmcnt(12)\n" \
"v_cvt_pk_bf16_f32 v52, v25, v29\n" \
"v_cvt_pk_bf16_f32 v56, v26, v58\n" \
"v_mfma_f32_16x16x32_bf16 v[128:131], a[0:3], a[80:83], v[128:131]\n" \
"s_mov_b32 s47, 2359296\n" \
"s_add_u32 m0, s22, 1024\n" \
"ds_read_b64_tr_b16 a[124:125], v11 offset:17920\n" \
"v_mfma_f32_16x16x32_bf16 v[128:131], a[4:7], a[84:87], v[128:131]\n" \
"buffer_load_dwordx4 v21, s[32:35], s47 offen offset:0 lds\n" \
"v_mfma_f32_16x16x32_bf16 v[132:135], a[8:11], a[80:83], v[132:135]\n" \
"ds_read_b64_tr_b16 a[126:127], v12 offset:17920\n" \
"v_accvgpr_read_b32 v37, a206\n" \
"v_mfma_f32_16x16x32_bf16 v[132:135], a[12:15], a[84:87], v[132:135]\n" \
"v_lshlrev_b32 v31, 16, v37\n" \
"v_and_b32 v37, 0xffff0000, v37\n" \
"v_mfma_f32_16x16x32_bf16 v[144:147], a[16:19], a[80:83], v[144:147]\n" \
"v_mul_f32 v25, v34, v45\n" \
"v_mul_f32 v26, v40, v49\n" \
"v_mfma_f32_16x16x32_bf16 v[144:147], a[20:23], a[84:87], v[144:147]\n" \
"v_fmac_f32 v25, v35, v44\n" \
"v_mfma_f32_16x16x32_bf16 v[148:151], a[24:27], a[80:83], v[148:151]\n" \
"v_fmac_f32 v26, v41, v48\n" \
"v_fmac_f32 v25, v30, v43\n" \
"v_mfma_f32_16x16x32_bf16 v[148:151], a[28:31], a[84:87], v[148:151]\n" \
"v_fmac_f32 v26, v36, v47\n" \
"v_fmac_f32 v25, v31, v42\n" \
"v_mfma_f32_16x16x32_bf16 v[160:163], a[32:35], a[80:83], v[160:163]\n" \
"s_add_u32 m0, s50, 24576\n" \
"v_fmac_f32 v26, v37, v46\n" \
"v_mfma_f32_16x16x32_bf16 v[160:163], a[36:39], a[84:87], v[160:163]\n" \
"buffer_load_dwordx4 v19, s[28:31], s46 offen lds\n" \
"v_mfma_f32_16x16x32_bf16 v[164:167], a[40:43], a[80:83], v[164:167]\n" \
"v_accvgpr_read_b32 v28, a222\n" \
"v_lshlrev_b32 v27, 16, v28\n" \
"v_mfma_f32_16x16x32_bf16 v[164:167], a[44:47], a[84:87], v[164:167]\n" \
"s_waitcnt vmcnt(26) lgkmcnt(0)\n" \
"v_and_b32 v28, 0xffff0000, v28\n" \
"v_mul_f32 v29, v25, v27\n" \
"v_mfma_f32_16x16x32_bf16 v[176:179], a[48:51], a[80:83], v[176:179]\n" \
"s_barrier\n" \
"v_mul_f32 v58, v26, v28\n" \
"s_waitcnt vmcnt(10)\n" \
"v_mfma_f32_16x16x32_bf16 v[176:179], a[52:55], a[84:87], v[176:179]\n" \
"v_accvgpr_read_b32 v38, a208\n" \
"v_lshlrev_b32 v32, 16, v38\n" \
"v_mfma_f32_16x16x32_bf16 v[180:183], a[56:59], a[80:83], v[180:183]\n" \
"v_and_b32 v38, 0xffff0000, v38\n" \
"v_mul_f32 v25, v35, v45\n" \
"v_mfma_f32_16x16x32_bf16 v[180:183], a[60:63], a[84:87], v[180:183]\n" \
"v_mul_f32 v26, v41, v49\n" \
"v_mfma_f32_16x16x32_bf16 v[136:139], a[0:3], a[88:91], v[136:139]\n" \
"s_add_u32 m0, s22, 2816\n" \
"v_fmac_f32 v25, v30, v44\n" \
"v_fmac_f32 v26, v36, v48\n" \
"v_mfma_f32_16x16x32_bf16 v[136:139], a[4:7], a[92:95], v[136:139]\n" \
"buffer_load_dwordx4 v21, s[32:35], s47 offen offset:256 lds\n" \
"v_mfma_f32_16x16x32_bf16 v[140:143], a[8:11], a[88:91], v[140:143]\n" \
"s_add_u32 s84, s84, 1048576\ns_addc_u32 s85, s85, 0\n" \
"v_fmac_f32 v25, v31, v43\n" \
"v_mfma_f32_16x16x32_bf16 v[140:143], a[12:15], a[92:95], v[140:143]\n" \
"v_fmac_f32 v26, v37, v47\n" \
"v_fmac_f32 v25, v32, v42\n" \
"v_mfma_f32_16x16x32_bf16 v[152:155], a[16:19], a[88:91], v[152:155]\n" \
"v_fmac_f32 v26, v38, v46\n" \
"v_accvgpr_read_b32 v28, a224\n" \
"v_mfma_f32_16x16x32_bf16 v[152:155], a[20:23], a[92:95], v[152:155]\n" \
"v_lshlrev_b32 v27, 16, v28\n" \
"v_mfma_f32_16x16x32_bf16 v[156:159], a[24:27], a[88:91], v[156:159]\n" \
"v_and_b32 v28, 0xffff0000, v28\n" \
"v_mul_f32 v25, v25, v27\n" \
"v_mfma_f32_16x16x32_bf16 v[156:159], a[28:31], a[92:95], v[156:159]\n" \
"v_mul_f32 v26, v26, v28\n" \
"v_cvt_pk_bf16_f32 v51, v25, v29\n" \
"v_mfma_f32_16x16x32_bf16 v[168:171], a[32:35], a[88:91], v[168:171]\n" \
"s_add_u32 m0, s50, 28672\n" \
"v_cvt_pk_bf16_f32 v55, v26, v58\n" \
"v_mfma_f32_16x16x32_bf16 v[168:171], a[36:39], a[92:95], v[168:171]\n" \
"buffer_load_dwordx4 v20, s[28:31], s46 offen lds\n" \
"v_mfma_f32_16x16x32_bf16 v[172:175], a[40:43], a[88:91], v[172:175]\n" \
"v_mfma_f32_16x16x32_bf16 v[172:175], a[44:47], a[92:95], v[172:175]\n" \
"v_mfma_f32_16x16x32_bf16 v[184:187], a[48:51], a[88:91], v[184:187]\n" \
"v_mfma_f32_16x16x32_bf16 v[184:187], a[52:55], a[92:95], v[184:187]\n" \
"s_mov_b64 exec, 0xffff\nglobal_load_dword v22, v1, s[84:85]\ns_mov_b64 exec, -1\n" \
"v_mfma_f32_16x16x32_bf16 v[188:191], a[56:59], a[88:91], v[188:191]\n" \
"v_mfma_f32_16x16x32_bf16 v[188:191], a[60:63], a[92:95], v[188:191]\n" \
"s_waitcnt vmcnt(9)\n" \
"v_accvgpr_read_b32 v39, a210\n" \
"v_lshlrev_b32 v33, 16, v39\n" \
"v_mfma_f32_16x16x32_bf16 v[192:195], a[0:3], a[96:99], v[192:195]\n" \
"s_mov_b32 s47, 2097152\n" \
"s_add_u32 m0, s22, 16256\n" \
"v_and_b32 v39, 0xffff0000, v39\n" \
"v_mfma_f32_16x16x32_bf16 v[192:195], a[4:7], a[100:103], v[192:195]\n" \
"buffer_load_dwordx4 v21, s[32:35], s47 offen offset:128 lds\n" \
"v_mfma_f32_16x16x32_bf16 v[196:199], a[8:11], a[96:99], v[196:199]\n" \
"v_mul_f32 v25, v30, v45\n" \
"v_mul_f32 v26, v36, v49\n" \
"v_mfma_f32_16x16x32_bf16 v[196:199], a[12:15], a[100:103], v[196:199]\n" \
"v_fmac_f32 v25, v31, v44\n" \
"v_fmac_f32 v26, v37, v48\n" \
"v_mfma_f32_16x16x32_bf16 v[208:211], a[16:19], a[96:99], v[208:211]\n" \
"s_mov_b32 s46, 640\n" \
"s_add_u32 m0, s50, 32768\n" \
"ds_read_b64_tr_b16 a[64:65], v9 offset:32768\n" \
"v_mfma_f32_16x16x32_bf16 v[208:211], a[20:23], a[100:103], v[208:211]\n" \
"buffer_load_dwordx4 v13, s[28:31], s46 offen lds\n" \
"v_mfma_f32_16x16x32_bf16 v[212:215], a[24:27], a[96:99], v[212:215]\n" \
"ds_read_b64_tr_b16 a[66:67], v10 offset:32768\n" \
"v_fmac_f32 v25, v32, v43\n" \
"v_mfma_f32_16x16x32_bf16 v[212:215], a[28:31], a[100:103], v[212:215]\n" \
"v_fmac_f32 v26, v38, v47\n" \
"v_fmac_f32 v25, v33, v42\n" \
"v_mfma_f32_16x16x32_bf16 v[224:227], a[32:35], a[96:99], v[224:227]\n" \
"ds_read_b64_tr_b16 a[68:69], v9 offset:33792\n" \
"v_fmac_f32 v26, v39, v46\n" \
"v_mfma_f32_16x16x32_bf16 v[224:227], a[36:39], a[100:103], v[224:227]\n" \
"v_accvgpr_read_b32 v28, a226\n" \
"v_lshlrev_b32 v27, 16, v28\n" \
"v_mfma_f32_16x16x32_bf16 v[228:231], a[40:43], a[96:99], v[228:231]\n" \
"ds_read_b64_tr_b16 a[70:71], v10 offset:33792\n" \
"v_and_b32 v28, 0xffff0000, v28\n" \
"v_mfma_f32_16x16x32_bf16 v[228:231], a[44:47], a[100:103], v[228:231]\n" \
"v_mul_f32 v29, v25, v27\n" \
"v_mul_f32 v58, v26, v28\n" \
"v_mfma_f32_16x16x32_bf16 v[240:243], a[48:51], a[96:99], v[240:243]\n" \
"s_add_u32 m0, s22, 18048\n" \
"ds_read_b64_tr_b16 a[72:73], v11 offset:32768\n" \
"v_mfma_f32_16x16x32_bf16 v[240:243], a[52:55], a[100:103], v[240:243]\n" \
"buffer_load_dwordx4 v21, s[32:35], s47 offen offset:384 lds\n" \
"v_mfma_f32_16x16x32_bf16 v[244:247], a[56:59], a[96:99], v[244:247]\n" \
"ds_read_b64_tr_b16 a[74:75], v12 offset:32768\n" \
"v_mfma_f32_16x16x32_bf16 v[244:247], a[60:63], a[100:103], v[244:247]\n" \
"s_waitcnt vmcnt(8)\n" \
"v_accvgpr_read_b32 v40, a212\n" \
"v_lshlrev_b32 v34, 16, v40\n" \
"v_mfma_f32_16x16x32_bf16 v[200:203], a[0:3], a[104:107], v[200:203]\n" \
"s_add_u32 m0, s50, 36864\n" \
"ds_read_b64_tr_b16 a[76:77], v11 offset:33792\n" \
"v_and_b32 v40, 0xffff0000, v40\n" \
"v_mfma_f32_16x16x32_bf16 v[200:203], a[4:7], a[108:111], v[200:203]\n" \
"buffer_load_dwordx4 v14, s[28:31], s46 offen lds\n" \
"v_mfma_f32_16x16x32_bf16 v[204:207], a[8:11], a[104:107], v[204:207]\n" \
"ds_read_b64_tr_b16 a[78:79], v12 offset:33792\n" \
"v_mul_f32 v25, v31, v45\n" \
"v_mfma_f32_16x16x32_bf16 v[204:207], a[12:15], a[108:111], v[204:207]\n" \
"v_mul_f32 v26, v37, v49\n" \
"v_fmac_f32 v25, v32, v44\n" \
"v_mfma_f32_16x16x32_bf16 v[216:219], a[16:19], a[104:107], v[216:219]\n" \
"ds_read_b64_tr_b16 a[80:81], v9 offset:33280\n" \
"v_fmac_f32 v26, v38, v48\n" \
"v_mfma_f32_16x16x32_bf16 v[216:219], a[20:23], a[108:111], v[216:219]\n" \
"v_fmac_f32 v25, v33, v43\n" \
"v_fmac_f32 v26, v39, v47\n" \
"v_mfma_f32_16x16x32_bf16 v[220:223], a[24:27], a[104:107], v[220:223]\n" \
"ds_read_b64_tr_b16 a[82:83], v10 offset:33280\n" \
"v_fmac_f32 v25, v34, v42\n" \
"v_mfma_f32_16x16x32_bf16 v[220:223], a[28:31], a[108:111], v[220:223]\n" \
"v_fmac_f32 v26, v40, v46\n" \
"v_accvgpr_read_b32 v28, a228\n" \
"v_mfma_f32_16x16x32_bf16 v[232:235], a[32:35], a[104:107], v[232:235]\n" \
"s_mov_b32 s47, 2359296\n" \
"s_add_u32 m0, s22, 17280\n" \
"ds_read_b64_tr_b16 a[84:85], v9 offset:34304\n" \
"v_mfma_f32_16x16x32_bf16 v[232:235], a[36:39], a[108:111], v[232:235]\n" \
"buffer_load_dwordx4 v21, s[32:35], s47 offen offset:128 lds\n" \
"v_mfma_f32_16x16x32_bf16 v[236:239], a[40:43], a[104:107], v[236:239]\n" \
"ds_read_b64_tr_b16 a[86:87], v10 offset:34304\n" \
"v_lshlrev_b32 v27, 16, v28\n" \
"v_mfma_f32_16x16x32_bf16 v[236:239], a[44:47], a[108:111], v[236:239]\n" \
"v_and_b32 v28, 0xffff0000, v28\n" \
"v_mul_f32 v25, v25, v27\n" \
"v_mfma_f32_16x16x32_bf16 v[248:251], a[48:51], a[104:107], v[248:251]\n" \
"s_add_u32 m0, s50, 40960\n" \
"ds_read_b64_tr_b16 a[88:89], v11 offset:33280\n" \
"v_mul_f32 v26, v26, v28\n" \
"v_mfma_f32_16x16x32_bf16 v[248:251], a[52:55], a[108:111], v[248:251]\n" \
"buffer_load_dwordx4 v15, s[28:31], s46 offen lds\n" \
"v_mfma_f32_16x16x32_bf16 v[252:255], a[56:59], a[104:107], v[252:255]\n" \
"ds_read_b64_tr_b16 a[90:91], v12 offset:33280\n" \
"v_cvt_pk_bf16_f32 v50, v25, v29\n" \
"v_mfma_f32_16x16x32_bf16 v[252:255], a[60:63], a[108:111], v[252:255]\n" \
"s_mov_b32 s94, 79691776\n" \
"v_cvt_pk_bf16_f32 v54, v26, v58\n" \
"v_accvgpr_read_b32 v43, a234\n" \
"v_mfma_f32_16x16x32_bf16 a[128:131], a[0:3], a[112:115], a[128:131]\n" \
"ds_read_b64_tr_b16 a[92:93], v11 offset:34304\n" \
"v_lshlrev_b32 v42, 16, v43\n" \
"v_mfma_f32_16x16x32_bf16 a[128:131], a[4:7], a[116:119], a[128:131]\n" \
"buffer_store_dwordx4 v[50:53], v60, s[36:39], s94 offen offset:0 sc1\n" \
"v_mfma_f32_16x16x32_bf16 a[132:135], a[8:11], a[112:115], a[132:135]\n" \
"s_mov_b32 s95, 79695872\n" \
"ds_read_b64_tr_b16 a[94:95], v12 offset:34304\n" \
"v_and_b32 v43, 0xffff0000, v43\n" \
"v_mfma_f32_16x16x32_bf16 a[132:135], a[12:15], a[116:119], a[132:135]\n" \
"v_accvgpr_read_b32 v45, a235\n" \
"v_lshlrev_b32 v44, 16, v45\n" \
"v_mfma_f32_16x16x32_bf16 a[144:147], a[16:19], a[112:115], a[144:147]\n" \
"s_add_u32 m0, s22, 19072\n" \
"v_and_b32 v45, 0xffff0000, v45\n" \
"v_accvgpr_read_b32 v47, a236\n" \
"v_mfma_f32_16x16x32_bf16 a[144:147], a[20:23], a[116:119], a[144:147]\n" \
"buffer_load_dwordx4 v21, s[32:35], s47 offen offset:384 lds\n" \
"v_mfma_f32_16x16x32_bf16 a[148:151], a[24:27], a[112:115], a[148:151]\n" \
"v_lshlrev_b32 v46, 16, v47\n" \
"v_and_b32 v47, 0xffff0000, v47\n" \
"v_mfma_f32_16x16x32_bf16 a[148:151], a[28:31], a[116:119], a[148:151]\n" \
"v_accvgpr_read_b32 v49, a237\n" \
"v_lshlrev_b32 v48, 16, v49\n" \
"v_mfma_f32_16x16x32_bf16 a[160:163], a[32:35], a[112:115], a[160:163]\n" \
"v_and_b32 v49, 0xffff0000, v49\n" \
"v_mfma_f32_16x16x32_bf16 a[160:163], a[36:39], a[116:119], a[160:163]\n" \
"v_accvgpr_read_b32 v36, a193\n" \
"v_lshlrev_b32 v30, 16, v36\n" \
"v_mfma_f32_16x16x32_bf16 a[164:167], a[40:43], a[112:115], a[164:167]\n" \
"v_and_b32 v36, 0xffff0000, v36\n" \
"v_accvgpr_read_b32 v37, a195\n" \
"v_mfma_f32_16x16x32_bf16 a[164:167], a[44:47], a[116:119], a[164:167]\n" \
"s_waitcnt vmcnt(24) lgkmcnt(0)\n" \
"v_lshlrev_b32 v31, 16, v37\n" \
"v_mfma_f32_16x16x32_bf16 a[176:179], a[48:51], a[112:115], a[176:179]\n" \
"s_add_u32 m0, s50, 45056\n" \
"s_barrier\n" \
"v_and_b32 v37, 0xffff0000, v37\n" \
"v_mfma_f32_16x16x32_bf16 a[176:179], a[52:55], a[116:119], a[176:179]\n" \
"buffer_load_dwordx4 v16, s[28:31], s46 offen lds\n" \
"v_mfma_f32_16x16x32_bf16 a[180:183], a[56:59], a[112:115], a[180:183]\n" \
"v_accvgpr_read_b32 v38, a197\n" \
"v_lshlrev_b32 v32, 16, v38\n" \
"v_mfma_f32_16x16x32_bf16 a[180:183], a[60:63], a[116:119], a[180:183]\n" \
"buffer_store_dwordx4 v[54:57], v60, s[36:39], s95 offen offset:0 sc1\n" \
"v_mfma_f32_16x16x32_bf16 a[136:139], a[0:3], a[120:123], a[136:139]\n" \
"v_and_b32 v38, 0xffff0000, v38\n" \
"v_accvgpr_read_b32 v39, a199\n" \
"v_mfma_f32_16x16x32_bf16 a[136:139], a[4:7], a[124:127], a[136:139]\n" \
"v_lshlrev_b32 v33, 16, v39\n" \
"v_and_b32 v39, 0xffff0000, v39\n" \
"v_mfma_f32_16x16x32_bf16 a[140:143], a[8:11], a[120:123], a[140:143]\n" \
"ds_read_b128 a[0:3], v7 offset:0\n" \
"v_mul_f32 v25, v30, v45\n" \
"v_mfma_f32_16x16x32_bf16 a[140:143], a[12:15], a[124:127], a[140:143]\n" \
"ds_read_b128 a[4:7], v8 offset:0\n" \
"v_mul_f32 v26, v36, v49\n" \
"v_mfma_f32_16x16x32_bf16 a[152:155], a[16:19], a[120:123], a[152:155]\n" \
"ds_read_b128 a[8:11], v7 offset:2048\n" \
"v_fmac_f32 v25, v31, v44\n" \
"v_mfma_f32_16x16x32_bf16 a[152:155], a[20:23], a[124:127], a[152:155]\n" \
"ds_read_b128 a[12:15], v8 offset:2048\n" \
"v_fmac_f32 v26, v37, v48\n" \
"v_mfma_f32_16x16x32_bf16 a[156:159], a[24:27], a[120:123], a[156:159]\n" \
"ds_read_b128 a[16:19], v7 offset:4096\n" \
"v_fmac_f32 v25, v32, v43\n" \
"v_mfma_f32_16x16x32_bf16 a[156:159], a[28:31], a[124:127], a[156:159]\n" \
"ds_read_b128 a[20:23], v8 offset:4096\n" \
"v_fmac_f32 v26, v38, v47\n" \
"v_mfma_f32_16x16x32_bf16 a[168:171], a[32:35], a[120:123], a[168:171]\n" \
"ds_read_b128 a[24:27], v7 offset:6144\n" \
"v_fmac_f32 v25, v33, v42\n" \
"v_mfma_f32_16x16x32_bf16 a[168:171], a[36:39], a[124:127], a[168:171]\n" \
"ds_read_b128 a[28:31], v8 offset:6144\n" \
"v_fmac_f32 v26, v39, v46\n" \
"v_mfma_f32_16x16x32_bf16 a[172:175], a[40:43], a[120:123], a[172:175]\n" \
"ds_read_b128 a[32:35], v7 offset:8192\n" \
"v_accvgpr_read_b32 v28, a215\n" \
"v_mfma_f32_16x16x32_bf16 a[172:175], a[44:47], a[124:127], a[172:175]\n" \
"ds_read_b128 a[36:39], v8 offset:8192\n" \
"v_lshlrev_b32 v27, 16, v28\n" \
"v_mfma_f32_16x16x32_bf16 a[184:187], a[48:51], a[120:123], a[184:187]\n" \
"ds_read_b128 a[40:43], v7 offset:10240\n" \
"v_and_b32 v28, 0xffff0000, v28\n" \
"v_mfma_f32_16x16x32_bf16 a[184:187], a[52:55], a[124:127], a[184:187]\n" \
"ds_read_b128 a[44:47], v8 offset:10240\n" \
"v_mul_f32 v29, v25, v27\n" \
"v_mfma_f32_16x16x32_bf16 a[188:191], a[56:59], a[120:123], a[188:191]\n" \
"ds_read_b128 a[48:51], v7 offset:12288\n" \
"v_mul_f32 v58, v26, v28\n" \
"v_mfma_f32_16x16x32_bf16 a[188:191], a[60:63], a[124:127], a[188:191]\n" \
"s_waitcnt lgkmcnt(9)\n" \
"ds_read_b128 a[52:55], v8 offset:12288\n" \
"v_accvgpr_read_b32 v40, a201\n" \
"v_mfma_f32_16x16x32_bf16 v[64:67], a[0:3], a[64:67], v[64:67]\n" \
"s_mov_b32 s47, 2621440\n" \
"s_add_u32 m0, s22, 32768\n" \
"ds_read_b128 a[56:59], v7 offset:14336\n" \
"v_mfma_f32_16x16x32_bf16 v[64:67], a[4:7], a[68:71], v[64:67]\n" \
"buffer_load_dwordx4 v21, s[32:35], s47 offen offset:0 lds\n" \
"v_mfma_f32_16x16x32_bf16 v[68:71], a[8:11], a[64:67], v[68:71]\n" \
"s_waitcnt lgkmcnt(7)\n" \
"ds_read_b128 a[60:63], v8 offset:14336\n" \
"v_lshlrev_b32 v34, 16, v40\n" \
"v_mfma_f32_16x16x32_bf16 v[68:71], a[12:15], a[68:71], v[68:71]\n" \
"v_and_b32 v40, 0xffff0000, v40\n" \
"v_mul_f32 v25, v31, v45\n" \
"v_mfma_f32_16x16x32_bf16 v[80:83], a[16:19], a[64:67], v[80:83]\n" \
"s_mov_b32 s46, 640\n" \
"s_add_u32 m0, s50, 49152\n" \
"ds_read_b64_tr_b16 a[96:97], v9 offset:49152\n" \
"v_mfma_f32_16x16x32_bf16 v[80:83], a[20:23], a[68:71], v[80:83]\n" \
"buffer_load_dwordx4 v17, s[28:31], s46 offen lds\n" \
"v_mfma_f32_16x16x32_bf16 v[84:87], a[24:27], a[64:67], v[84:87]\n" \
"s_waitcnt lgkmcnt(5)\n" \
"ds_read_b64_tr_b16 a[98:99], v10 offset:49152\n" \
"v_mul_f32 v26, v37, v49\n" \
"v_mfma_f32_16x16x32_bf16 v[84:87], a[28:31], a[68:71], v[84:87]\n" \
"v_fmac_f32 v25, v32, v44\n" \
"v_fmac_f32 v26, v38, v48\n" \
"v_mfma_f32_16x16x32_bf16 v[96:99], a[32:35], a[64:67], v[96:99]\n" \
"s_add_u32 m0, s22, 34560\n" \
"ds_read_b64_tr_b16 a[100:101], v9 offset:50176\n" \
"v_fmac_f32 v25, v33, v43\n" \
"v_mfma_f32_16x16x32_bf16 v[96:99], a[36:39], a[68:71], v[96:99]\n" \
"buffer_load_dwordx4 v21, s[32:35], s47 offen offset:256 lds\n" \
"v_mfma_f32_16x16x32_bf16 v[100:103], a[40:43], a[64:67], v[100:103]\n" \
"s_waitcnt lgkmcnt(3)\n" \
"ds_read_b64_tr_b16 a[102:103], v10 offset:50176\n" \
"v_fmac_f32 v26, v39, v47\n" \
"v_mfma_f32_16x16x32_bf16 v[100:103], a[44:47], a[68:71], v[100:103]\n" \
"LH5_%=:\ns_cbranch_execz LH9_%=\n" \
"v_fmac_f32 v25, v34, v42\n" \
"v_fmac_f32 v26, v40, v46\n" \
"v_mfma_f32_16x16x32_bf16 v[112:115], a[48:51], a[64:67], v[112:115]\n" \
"s_add_u32 m0, s50, 53248\n" \
"ds_read_b64_tr_b16 a[104:105], v11 offset:49152\n" \
"v_accvgpr_read_b32 v28, a217\n" \
"v_mfma_f32_16x16x32_bf16 v[112:115], a[52:55], a[68:71], v[112:115]\n" \
"buffer_load_dwordx4 v18, s[28:31], s46 offen lds\n" \
"v_mfma_f32_16x16x32_bf16 v[116:119], a[56:59], a[64:67], v[116:119]\n" \
"ds_read_b64_tr_b16 a[106:107], v12 offset:49152\n" \
"v_lshlrev_b32 v27, 16, v28\n" \
"v_mfma_f32_16x16x32_bf16 v[116:119], a[60:63], a[68:71], v[116:119]\n" \
"v_and_b32 v28, 0xffff0000, v28\n" \
"v_mul_f32 v25, v25, v27\n" \
"v_mfma_f32_16x16x32_bf16 v[72:75], a[0:3], a[72:75], v[72:75]\n" \
"s_mov_b32 s47, 2883584\n" \
"s_add_u32 m0, s22, 33792\n" \
"ds_read_b64_tr_b16 a[108:109], v11 offset:50176\n" \
"v_mfma_f32_16x16x32_bf16 v[72:75], a[4:7], a[76:79], v[72:75]\n" \
"buffer_load_dwordx4 v21, s[32:35], s47 offen offset:0 lds\n" \
"v_mfma_f32_16x16x32_bf16 v[76:79], a[8:11], a[72:75], v[76:79]\n" \
"ds_read_b64_tr_b16 a[110:111], v12 offset:50176\n" \
"v_mul_f32 v26, v26, v28\n" \
"v_mfma_f32_16x16x32_bf16 v[76:79], a[12:15], a[76:79], v[76:79]\n" \
"v_cvt_pk_bf16_f32 v53, v25, v29\n" \
"v_cvt_pk_bf16_f32 v57, v26, v58\n" \
"v_mfma_f32_16x16x32_bf16 v[88:91], a[16:19], a[72:75], v[88:91]\n" \
"s_add_u32 m0, s50, 57344\n" \
"ds_read_b64_tr_b16 a[112:113], v9 offset:49664\n" \
"v_accvgpr_read_b32 v41, a203\n" \
"v_mfma_f32_16x16x32_bf16 v[88:91], a[20:23], a[76:79], v[88:91]\n" \
"buffer_load_dwordx4 v19, s[28:31], s46 offen lds\n" \
"v_mfma_f32_16x16x32_bf16 v[92:95], a[24:27], a[72:75], v[92:95]\n" \
"ds_read_b64_tr_b16 a[114:115], v10 offset:49664\n" \
"v_lshlrev_b32 v35, 16, v41\n" \
"v_mfma_f32_16x16x32_bf16 v[92:95], a[28:31], a[76:79], v[92:95]\n" \
"v_and_b32 v41, 0xffff0000, v41\n" \
"v_mul_f32 v25, v32, v45\n" \
"v_mfma_f32_16x16x32_bf16 v[104:107], a[32:35], a[72:75], v[104:107]\n" \
"s_add_u32 m0, s22, 35584\n" \
"ds_read_b64_tr_b16 a[116:117], v9 offset:50688\n" \
"v_mul_f32 v26, v38, v49\n" \
"v_mfma_f32_16x16x32_bf16 v[104:107], a[36:39], a[76:79], v[104:107]\n" \
"buffer_load_dwordx4 v21, s[32:35], s47 offen offset:256 lds\n" \
"v_mfma_f32_16x16x32_bf16 v[108:111], a[40:43], a[72:75], v[108:111]\n" \
"s_add_u32 s84, s84, 1048576\ns_addc_u32 s85, s85, 0\n" \
"ds_read_b64_tr_b16 a[118:119], v10 offset:50688\n" \
"v_mfma_f32_16x16x32_bf16 v[108:111], a[44:47], a[76:79], v[108:111]\n" \
"v_fmac_f32 v25, v33, v44\n" \
"v_fmac_f32 v26, v39, v48\n" \
"v_mfma_f32_16x16x32_bf16 v[120:123], a[48:51], a[72:75], v[120:123]\n" \
"s_add_u32 m0, s50, 61440\n" \
"ds_read_b64_tr_b16 a[120:121], v11 offset:49664\n" \
"v_fmac_f32 v25, v34, v43\n" \
"v_mfma_f32_16x16x32_bf16 v[120:123], a[52:55], a[76:79], v[120:123]\n" \
"buffer_load_dwordx4 v20, s[28:31], s46 offen lds\n" \
"v_mfma_f32_16x16x32_bf16 v[124:127], a[56:59], a[72:75], v[124:127]\n" \
"ds_read_b64_tr_b16 a[122:123], v12 offset:49664\n" \
"v_fmac_f32 v26, v40, v47\n" \
"v_mfma_f32_16x16x32_bf16 v[124:127], a[60:63], a[76:79], v[124:127]\n" \
"v_fmac_f32 v25, v35, v42\n" \
"v_fmac_f32 v26, v41, v46\n" \
"v_mfma_f32_16x16x32_bf16 v[128:131], a[0:3], a[80:83], v[128:131]\n" \
"ds_read_b64_tr_b16 a[124:125], v11 offset:50688\n" \
"v_accvgpr_read_b32 v28, a219\n" \
"v_mfma_f32_16x16x32_bf16 v[128:131], a[4:7], a[84:87], v[128:131]\n" \
"s_mov_b64 exec, 0xffff\nglobal_load_dword v22, v1, s[84:85]\ns_mov_b64 exec, -1\n" \
"v_mfma_f32_16x16x32_bf16 v[132:135], a[8:11], a[80:83], v[132:135]\n" \
"ds_read_b64_tr_b16 a[126:127], v12 offset:50688\n" \
"v_lshlrev_b32 v27, 16, v28\n" \
"v_mfma_f32_16x16x32_bf16 v[132:135], a[12:15], a[84:87], v[132:135]\n" \
"v_and_b32 v28, 0xffff0000, v28\n" \
"v_mul_f32 v29, v25, v27\n" \
"v_mfma_f32_16x16x32_bf16 v[144:147], a[16:19], a[80:83], v[144:147]\n" \
"v_mul_f32 v58, v26, v28\n" \
"v_accvgpr_read_b32 v36, a205\n" \
"v_mfma_f32_16x16x32_bf16 v[144:147], a[20:23], a[84:87], v[144:147]\n" \
"s_mov_b64 exec, 0xffff\nglobal_load_dword v22, v1, s[76:77]\ns_mov_b64 exec, -1\n" \
"v_mfma_f32_16x16x32_bf16 v[148:151], a[24:27], a[80:83], v[148:151]\n" \
"v_lshlrev_b32 v30, 16, v36\n" \
"v_and_b32 v36, 0xffff0000, v36\n" \
"v_mfma_f32_16x16x32_bf16 v[148:151], a[28:31], a[84:87], v[148:151]\n" \
"v_mul_f32 v25, v33, v45\n" \
"v_mul_f32 v26, v39, v49\n" \
"v_mfma_f32_16x16x32_bf16 v[160:163], a[32:35], a[80:83], v[160:163]\n" \
"v_fmac_f32 v25, v34, v44\n" \
"v_mfma_f32_16x16x32_bf16 v[160:163], a[36:39], a[84:87], v[160:163]\n" \
"v_fmac_f32 v26, v40, v48\n" \
"v_fmac_f32 v25, v35, v43\n" \
"v_mfma_f32_16x16x32_bf16 v[164:167], a[40:43], a[80:83], v[164:167]\n" \
"v_fmac_f32 v26, v41, v47\n" \
"v_fmac_f32 v25, v30, v42\n" \
"v_mfma_f32_16x16x32_bf16 v[164:167], a[44:47], a[84:87], v[164:167]\n" \
"s_waitcnt vmcnt(22) lgkmcnt(0)\n" \
"v_fmac_f32 v26, v36, v46\n" \
"v_mfma_f32_16x16x32_bf16 v[176:179], a[48:51], a[80:83], v[176:179]\n" \
"s_barrier\n" \
"v_accvgpr_read_b32 v28, a221\n" \
"v_lshlrev_b32 v27, 16, v28\n" \
"v_mfma_f32_16x16x32_bf16 v[176:179], a[52:55], a[84:87], v[176:179]\n" \
"v_and_b32 v28, 0xffff0000, v28\n" \
"v_mul_f32 v25, v25, v27\n" \
"v_mfma_f32_16x16x32_bf16 v[180:183], a[56:59], a[80:83], v[180:183]\n" \
"v_mul_f32 v26, v26, v28\n" \
"v_mfma_f32_16x16x32_bf16 v[180:183], a[60:63], a[84:87], v[180:183]\n" \
"v_cvt_pk_bf16_f32 v52, v25, v29\n" \
"v_cvt_pk_bf16_f32 v56, v26, v58\n" \
"v_mfma_f32_16x16x32_bf16 v[136:139], a[0:3], a[88:91], v[136:139]\n" \
"v_accvgpr_read_b32 v37, a207\n" \
"v_lshlrev_b32 v31, 16, v37\n" \
"v_mfma_f32_16x16x32_bf16 v[136:139], a[4:7], a[92:95], v[136:139]\n" \
"v_and_b32 v37, 0xffff0000, v37\n" \
"v_mfma_f32_16x16x32_bf16 v[140:143], a[8:11], a[88:91], v[140:143]\n" \
"v_mul_f32 v25, v34, v45\n" \
"v_mul_f32 v26, v40, v49\n" \
"v_mfma_f32_16x16x32_bf16 v[140:143], a[12:15], a[92:95], v[140:143]\n" \
"v_fmac_f32 v25, v35, v44\n" \
"v_fmac_f32 v26, v41, v48\n" \
"v_mfma_f32_16x16x32_bf16 v[152:155], a[16:19], a[88:91], v[152:155]\n" \
"v_fmac_f32 v25, v30, v43\n" \
"v_mfma_f32_16x16x32_bf16 v[152:155], a[20:23], a[92:95], v[152:155]\n" \
"v_fmac_f32 v26, v36, v47\n" \
"v_fmac_f32 v25, v31, v42\n" \
"v_mfma_f32_16x16x32_bf16 v[156:159], a[24:27], a[88:91], v[156:159]\n" \
"v_fmac_f32 v26, v37, v46\n" \
"v_accvgpr_read_b32 v28, a223\n" \
"v_mfma_f32_16x16x32_bf16 v[156:159], a[28:31], a[92:95], v[156:159]\n" \
"v_lshlrev_b32 v27, 16, v28\n" \
"v_mfma_f32_16x16x32_bf16 v[168:171], a[32:35], a[88:91], v[168:171]\n" \
"v_and_b32 v28, 0xffff0000, v28\n" \
"v_mul_f32 v29, v25, v27\n" \
"v_mfma_f32_16x16x32_bf16 v[168:171], a[36:39], a[92:95], v[168:171]\n" \
"v_mul_f32 v58, v26, v28\n" \
"v_accvgpr_read_b32 v38, a209\n" \
"v_mfma_f32_16x16x32_bf16 v[172:175], a[40:43], a[88:91], v[172:175]\n" \
"v_lshlrev_b32 v32, 16, v38\n" \
"v_mfma_f32_16x16x32_bf16 v[172:175], a[44:47], a[92:95], v[172:175]\n" \
"v_and_b32 v38, 0xffff0000, v38\n" \
"v_mul_f32 v25, v35, v45\n" \
"v_mfma_f32_16x16x32_bf16 v[184:187], a[48:51], a[88:91], v[184:187]\n" \
"v_mul_f32 v26, v41, v49\n" \
"v_fmac_f32 v25, v30, v44\n" \
"v_mfma_f32_16x16x32_bf16 v[184:187], a[52:55], a[92:95], v[184:187]\n" \
"v_fmac_f32 v26, v36, v48\n" \
"v_mfma_f32_16x16x32_bf16 v[188:191], a[56:59], a[88:91], v[188:191]\n" \
"v_fmac_f32 v25, v31, v43\n" \
"v_fmac_f32 v26, v37, v47\n" \
"v_mfma_f32_16x16x32_bf16 v[188:191], a[60:63], a[92:95], v[188:191]\n" \
"v_fmac_f32 v25, v32, v42\n" \
"v_fmac_f32 v26, v38, v46\n" \
"v_mfma_f32_16x16x32_bf16 v[192:195], a[0:3], a[96:99], v[192:195]\n" \
"v_accvgpr_read_b32 v28, a225\n" \
"v_mfma_f32_16x16x32_bf16 v[192:195], a[4:7], a[100:103], v[192:195]\n" \
"v_lshlrev_b32 v27, 16, v28\n" \
"v_and_b32 v28, 0xffff0000, v28\n" \
"v_mfma_f32_16x16x32_bf16 v[196:199], a[8:11], a[96:99], v[196:199]\n" \
"v_mul_f32 v25, v25, v27\n" \
"v_mul_f32 v26, v26, v28\n" \
"v_mfma_f32_16x16x32_bf16 v[196:199], a[12:15], a[100:103], v[196:199]\n" \
"v_cvt_pk_bf16_f32 v51, v25, v29\n" \
"v_mfma_f32_16x16x32_bf16 v[208:211], a[16:19], a[96:99], v[208:211]\n" \
"ds_read_b64_tr_b16 a[64:65], v9 offset:0\n" \
"v_cvt_pk_bf16_f32 v55, v26, v58\n" \
"v_mfma_f32_16x16x32_bf16 v[208:211], a[20:23], a[100:103], v[208:211]\n" \
"v_accvgpr_read_b32 v39, a211\n" \
"v_lshlrev_b32 v33, 16, v39\n" \
"v_mfma_f32_16x16x32_bf16 v[212:215], a[24:27], a[96:99], v[212:215]\n" \
"ds_read_b64_tr_b16 a[66:67], v10 offset:0\n" \
"v_and_b32 v39, 0xffff0000, v39\n" \
"v_mfma_f32_16x16x32_bf16 v[212:215], a[28:31], a[100:103], v[212:215]\n" \
"v_mul_f32 v25, v30, v45\n" \
"v_mul_f32 v26, v36, v49\n" \
"v_mfma_f32_16x16x32_bf16 v[224:227], a[32:35], a[96:99], v[224:227]\n" \
"ds_read_b64_tr_b16 a[68:69], v9 offset:1024\n" \
"v_fmac_f32 v25, v31, v44\n" \
"v_mfma_f32_16x16x32_bf16 v[224:227], a[36:39], a[100:103], v[224:227]\n" \
"v_fmac_f32 v26, v37, v48\n" \
"v_fmac_f32 v25, v32, v43\n" \
"v_mfma_f32_16x16x32_bf16 v[228:231], a[40:43], a[96:99], v[228:231]\n" \
"ds_read_b64_tr_b16 a[70:71], v10 offset:1024\n" \
"v_fmac_f32 v26, v38, v47\n" \
"v_mfma_f32_16x16x32_bf16 v[228:231], a[44:47], a[100:103], v[228:231]\n" \
"v_fmac_f32 v25, v33, v42\n" \
"v_fmac_f32 v26, v39, v46\n" \
"v_mfma_f32_16x16x32_bf16 v[240:243], a[48:51], a[96:99], v[240:243]\n" \
"ds_read_b64_tr_b16 a[72:73], v11 offset:0\n" \
"v_accvgpr_read_b32 v28, a227\n" \
"v_mfma_f32_16x16x32_bf16 v[240:243], a[52:55], a[100:103], v[240:243]\n" \
"v_lshlrev_b32 v27, 16, v28\n" \
"v_and_b32 v28, 0xffff0000, v28\n" \
"v_mfma_f32_16x16x32_bf16 v[244:247], a[56:59], a[96:99], v[244:247]\n" \
"ds_read_b64_tr_b16 a[74:75], v12 offset:0\n" \
"v_mul_f32 v29, v25, v27\n" \
"v_mfma_f32_16x16x32_bf16 v[244:247], a[60:63], a[100:103], v[244:247]\n" \
"v_mul_f32 v58, v26, v28\n" \
"v_accvgpr_read_b32 v40, a213\n" \
"v_mfma_f32_16x16x32_bf16 v[200:203], a[0:3], a[104:107], v[200:203]\n" \
"ds_read_b64_tr_b16 a[76:77], v11 offset:1024\n" \
"v_lshlrev_b32 v34, 16, v40\n" \
"v_mfma_f32_16x16x32_bf16 v[200:203], a[4:7], a[108:111], v[200:203]\n" \
"v_and_b32 v40, 0xffff0000, v40\n" \
"v_mul_f32 v25, v31, v45\n" \
"v_mfma_f32_16x16x32_bf16 v[204:207], a[8:11], a[104:107], v[204:207]\n" \
"ds_read_b64_tr_b16 a[78:79], v12 offset:1024\n" \
"v_mul_f32 v26, v37, v49\n" \
"v_mfma_f32_16x16x32_bf16 v[204:207], a[12:15], a[108:111], v[204:207]\n" \
"v_fmac_f32 v25, v32, v44\n" \
"v_fmac_f32 v26, v38, v48\n" \
"v_mfma_f32_16x16x32_bf16 v[216:219], a[16:19], a[104:107], v[216:219]\n" \
"ds_read_b64_tr_b16 a[80:81], v9 offset:512\n" \
"v_fmac_f32 v25, v33, v43\n" \
"v_mfma_f32_16x16x32_bf16 v[216:219], a[20:23], a[108:111], v[216:219]\n" \
"v_fmac_f32 v26, v39, v47\n" \
"v_fmac_f32 v25, v34, v42\n" \
"v_mfma_f32_16x16x32_bf16 v[220:223], a[24:27], a[104:107], v[220:223]\n" \
"ds_read_b64_tr_b16 a[82:83], v10 offset:512\n" \
"v_fmac_f32 v26, v40, v46\n" \
"v_mfma_f32_16x16x32_bf16 v[220:223], a[28:31], a[108:111], v[220:223]\n" \
"v_accvgpr_read_b32 v28, a229\n" \
"v_lshlrev_b32 v27, 16, v28\n" \
"v_mfma_f32_16x16x32_bf16 v[232:235], a[32:35], a[104:107], v[232:235]\n" \
"ds_read_b64_tr_b16 a[84:85], v9 offset:1536\n" \
"v_and_b32 v28, 0xffff0000, v28\n" \
"v_mfma_f32_16x16x32_bf16 v[232:235], a[36:39], a[108:111], v[232:235]\n" \
"v_mul_f32 v25, v25, v27\n" \
"v_mul_f32 v26, v26, v28\n" \
"v_mfma_f32_16x16x32_bf16 v[236:239], a[40:43], a[104:107], v[236:239]\n" \
"ds_read_b64_tr_b16 a[86:87], v10 offset:1536\n" \
"v_cvt_pk_bf16_f32 v50, v25, v29\n" \
"v_mfma_f32_16x16x32_bf16 v[236:239], a[44:47], a[108:111], v[236:239]\n" \
"s_mov_b32 s94, 79699968\n" \
"v_cvt_pk_bf16_f32 v54, v26, v58\n" \
"v_mfma_f32_16x16x32_bf16 v[248:251], a[48:51], a[104:107], v[248:251]\n" \
"ds_read_b64_tr_b16 a[88:89], v11 offset:512\n" \
"v_mfma_f32_16x16x32_bf16 v[248:251], a[52:55], a[108:111], v[248:251]\n" \
"buffer_store_dwordx4 v[50:53], v60, s[36:39], s94 offen offset:0 sc1\n" \
"v_mfma_f32_16x16x32_bf16 v[252:255], a[56:59], a[104:107], v[252:255]\n" \
"s_mov_b32 s95, 79704064\n" \
"ds_read_b64_tr_b16 a[90:91], v12 offset:512\n" \
"v_mfma_f32_16x16x32_bf16 v[252:255], a[60:63], a[108:111], v[252:255]\n" \
"v_mfma_f32_16x16x32_bf16 a[128:131], a[0:3], a[112:115], a[128:131]\n" \
"ds_read_b64_tr_b16 a[92:93], v11 offset:1536\n" \
"v_mfma_f32_16x16x32_bf16 a[128:131], a[4:7], a[116:119], a[128:131]\n" \
"v_mfma_f32_16x16x32_bf16 a[132:135], a[8:11], a[112:115], a[132:135]\n" \
"ds_read_b64_tr_b16 a[94:95], v12 offset:1536\n" \
"v_mfma_f32_16x16x32_bf16 a[132:135], a[12:15], a[116:119], a[132:135]\n" \
"v_mfma_f32_16x16x32_bf16 a[144:147], a[16:19], a[112:115], a[144:147]\n" \
"v_mfma_f32_16x16x32_bf16 a[144:147], a[20:23], a[116:119], a[144:147]\n" \
"v_mfma_f32_16x16x32_bf16 a[148:151], a[24:27], a[112:115], a[148:151]\n" \
"v_mfma_f32_16x16x32_bf16 a[148:151], a[28:31], a[116:119], a[148:151]\n" \
"v_mfma_f32_16x16x32_bf16 a[160:163], a[32:35], a[112:115], a[160:163]\n" \
"v_mfma_f32_16x16x32_bf16 a[160:163], a[36:39], a[116:119], a[160:163]\n" \
"v_mfma_f32_16x16x32_bf16 a[164:167], a[40:43], a[112:115], a[164:167]\n" \
"v_mfma_f32_16x16x32_bf16 a[164:167], a[44:47], a[116:119], a[164:167]\n" \
"s_waitcnt vmcnt(13) lgkmcnt(0)\n" \
"v_mfma_f32_16x16x32_bf16 a[176:179], a[48:51], a[112:115], a[176:179]\n" \
"s_barrier\n" \
"v_mfma_f32_16x16x32_bf16 a[176:179], a[52:55], a[116:119], a[176:179]\n" \
"buffer_store_dwordx4 v[54:57], v60, s[36:39], s95 offen offset:0 sc1\n" \
"v_mfma_f32_16x16x32_bf16 a[180:183], a[56:59], a[112:115], a[180:183]\n" \
"s_lshl_b32 s62, s8, 2\n" \
"s_add_u32 s62, s62, %2\n" \
"s_lshl_b32 s63, s62, 12\n" \
"v_mfma_f32_16x16x32_bf16 a[180:183], a[60:63], a[116:119], a[180:183]\n" \
"s_add_u32 s68, s10, s63\ns_addc_u32 s69, s11, 0\n" \
"s_and_b32 s69, s69, 0xffff\n" \
"v_mfma_f32_16x16x32_bf16 a[136:139], a[0:3], a[120:123], a[136:139]\n" \
"s_mov_b32 s70, 4096\n" \
"s_mov_b32 s71, 0x00020000\n" \
"s_add_u32 s63, s63, 79691776\n" \
"v_mfma_f32_16x16x32_bf16 a[136:139], a[4:7], a[124:127], a[136:139]\n" \
"s_add_u32 s72, s4, s63\ns_addc_u32 s73, s5, 0\n" \
"s_and_b32 s73, s73, 0xffff\n" \
"v_mfma_f32_16x16x32_bf16 a[140:143], a[8:11], a[120:123], a[140:143]\n" \
"s_mov_b32 s74, 4096\n" \
"ds_read_b128 a[0:3], v5 offset:0\n" \
"s_mov_b32 s75, 0x00020000\n" \
"v_mfma_f32_16x16x32_bf16 a[140:143], a[12:15], a[124:127], a[140:143]\n" \
"ds_read_b128 a[4:7], v6 offset:0\n" \
"v_add_u32 v23, -32, v4\n" \
"v_mfma_f32_16x16x32_bf16 a[152:155], a[16:19], a[120:123], a[152:155]\n" \
"ds_read_b128 a[8:11], v5 offset:2048\n" \
"v_add_u32 v24, 992, v4\n" \
"v_mfma_f32_16x16x32_bf16 a[152:155], a[20:23], a[124:127], a[152:155]\n" \
"ds_read_b128 a[12:15], v6 offset:2048\n" \
"v_add_u32 v25, -16, v4\n" \
"v_mfma_f32_16x16x32_bf16 a[156:159], a[24:27], a[120:123], a[156:159]\n" \
"ds_read_b128 a[16:19], v5 offset:4096\n" \
"v_add_u32 v26, -512, v4\n" \
"v_mfma_f32_16x16x32_bf16 a[156:159], a[28:31], a[124:127], a[156:159]\n" \
"s_lshr_b32 s63, %2, 1\n" \
"ds_read_b128 a[20:23], v6 offset:4096\n" \
"v_add_u32 v27, 512, v4\n" \
"v_mfma_f32_16x16x32_bf16 a[168:171], a[32:35], a[120:123], a[168:171]\n" \
"s_lshl_b32 s63, s63, 16\n" \
"ds_read_b128 a[24:27], v5 offset:6144\n" \
"s_lshl_b32 s56, %2, 14\n" \
"v_mfma_f32_16x16x32_bf16 a[168:171], a[36:39], a[124:127], a[168:171]\n" \
"s_add_u32 s56, s56, s63\n" \
"ds_read_b128 a[28:31], v6 offset:6144\n" \
"v_add_u32 v55, s56, v4\n" \
"v_mfma_f32_16x16x32_bf16 a[172:175], a[40:43], a[120:123], a[172:175]\n" \
"s_lshl_b32 s62, s62, 1\n" \
"ds_read_b128 a[32:35], v5 offset:8192\n" \
"v_mov_b32 v28, 0\n" \
"v_mfma_f32_16x16x32_bf16 a[172:175], a[44:47], a[124:127], a[172:175]\n" \
"ds_read_b128 a[36:39], v6 offset:8192\n" \
"v_mov_b32 v29, s62\n" \
"v_mfma_f32_16x16x32_bf16 a[184:187], a[48:51], a[120:123], a[184:187]\n" \
"s_and_b32 s62, %1, 7\n" \
"ds_read_b128 a[40:43], v5 offset:10240\n" \
"v_lshlrev_b32 v56, 23, v4\n" \
"v_mfma_f32_16x16x32_bf16 a[184:187], a[52:55], a[124:127], a[184:187]\n" \
"s_lshl_b32 s62, s62, 8\n" \
"ds_read_b128 a[44:47], v6 offset:10240\n" \
"s_add_u32 s62, s62, 94380032\n" \
"v_mfma_f32_16x16x32_bf16 a[188:191], a[56:59], a[120:123], a[188:191]\n" \
"ds_read_b128 a[48:51], v5 offset:12288\n" \
"v_bfe_u32 v57, v4, 4, 5\n" \
"v_mfma_f32_16x16x32_bf16 a[188:191], a[60:63], a[124:127], a[188:191]\n" \
"s_waitcnt lgkmcnt(9)\n" \
"ds_read_b128 a[52:55], v6 offset:12288\n" \
"v_lshl_add_u32 v57, v57, 3, s62\n" \
"v_mfma_f32_16x16x32_bf16 v[64:67], a[0:3], a[64:67], v[64:67]\n" \
"s_mov_b32 s47, 2621440\n" \
"s_add_u32 m0, s22, 49024\n" \
"ds_read_b128 a[56:59], v5 offset:14336\n" \
"v_mfma_f32_16x16x32_bf16 v[64:67], a[4:7], a[68:71], v[64:67]\n" \
"buffer_load_dwordx4 v21, s[32:35], s47 offen offset:128 lds\n" \
"v_mfma_f32_16x16x32_bf16 v[68:71], a[8:11], a[64:67], v[68:71]\n" \
"s_waitcnt lgkmcnt(7)\n" \
"ds_read_b128 a[60:63], v6 offset:14336\n" \
"v_mfma_f32_16x16x32_bf16 v[68:71], a[12:15], a[68:71], v[68:71]\n" \
"v_mfma_f32_16x16x32_bf16 v[80:83], a[16:19], a[64:67], v[80:83]\n" \
"s_add_u32 m0, s22, 50816\n" \
"ds_read_b64_tr_b16 a[96:97], v9 offset:16384\n" \
"v_mfma_f32_16x16x32_bf16 v[80:83], a[20:23], a[68:71], v[80:83]\n" \
"buffer_load_dwordx4 v21, s[32:35], s47 offen offset:384 lds\n" \
"v_mfma_f32_16x16x32_bf16 v[84:87], a[24:27], a[64:67], v[84:87]\n" \
"s_waitcnt lgkmcnt(5)\n" \
"ds_read_b64_tr_b16 a[98:99], v10 offset:16384\n" \
"v_mfma_f32_16x16x32_bf16 v[84:87], a[28:31], a[68:71], v[84:87]\n" \
"v_mfma_f32_16x16x32_bf16 v[96:99], a[32:35], a[64:67], v[96:99]\n" \
"s_mov_b32 s47, 2883584\n" \
"s_add_u32 m0, s22, 50048\n" \
"ds_read_b64_tr_b16 a[100:101], v9 offset:17408\n" \
"v_mfma_f32_16x16x32_bf16 v[96:99], a[36:39], a[68:71], v[96:99]\n" \
"buffer_load_dwordx4 v21, s[32:35], s47 offen offset:128 lds\n" \
"v_mfma_f32_16x16x32_bf16 v[100:103], a[40:43], a[64:67], v[100:103]\n" \
"s_waitcnt lgkmcnt(3)\n" \
"ds_read_b64_tr_b16 a[102:103], v10 offset:17408\n" \
"v_mfma_f32_16x16x32_bf16 v[100:103], a[44:47], a[68:71], v[100:103]\n" \
"v_mfma_f32_16x16x32_bf16 v[112:115], a[48:51], a[64:67], v[112:115]\n" \
"s_add_u32 m0, s22, 51840\n" \
"ds_read_b64_tr_b16 a[104:105], v11 offset:16384\n" \
"v_mfma_f32_16x16x32_bf16 v[112:115], a[52:55], a[68:71], v[112:115]\n" \
"buffer_load_dwordx4 v21, s[32:35], s47 offen offset:384 lds\n" \
"v_mfma_f32_16x16x32_bf16 v[116:119], a[56:59], a[64:67], v[116:119]\n" \
"ds_read_b64_tr_b16 a[106:107], v12 offset:16384\n" \
"v_mfma_f32_16x16x32_bf16 v[116:119], a[60:63], a[68:71], v[116:119]\n" \
"v_mfma_f32_16x16x32_bf16 v[72:75], a[0:3], a[72:75], v[72:75]\n" \
"s_mov_b32 s46, 768\n" \
"s_add_u32 m0, s50, 65536\n" \
"ds_read_b64_tr_b16 a[108:109], v11 offset:17408\n" \
"v_mfma_f32_16x16x32_bf16 v[72:75], a[4:7], a[76:79], v[72:75]\n" \
"buffer_load_dwordx4 v13, s[28:31], s46 offen lds\n" \
"v_mfma_f32_16x16x32_bf16 v[76:79], a[8:11], a[72:75], v[76:79]\n" \
"ds_read_b64_tr_b16 a[110:111], v12 offset:17408\n" \
"v_mfma_f32_16x16x32_bf16 v[76:79], a[12:15], a[76:79], v[76:79]\n" \
"v_mfma_f32_16x16x32_bf16 v[88:91], a[16:19], a[72:75], v[88:91]\n" \
"s_add_u32 m0, s50, 69632\n" \
"ds_read_b64_tr_b16 a[112:113], v9 offset:16896\n" \
"v_mfma_f32_16x16x32_bf16 v[88:91], a[20:23], a[76:79], v[88:91]\n" \
"buffer_load_dwordx4 v14, s[28:31], s46 offen lds\n" \
"v_mfma_f32_16x16x32_bf16 v[92:95], a[24:27], a[72:75], v[92:95]\n" \
"ds_read_b64_tr_b16 a[114:115], v10 offset:16896\n" \
"v_mfma_f32_16x16x32_bf16 v[92:95], a[28:31], a[76:79], v[92:95]\n" \
"v_mfma_f32_16x16x32_bf16 v[104:107], a[32:35], a[72:75], v[104:107]\n" \
"s_add_u32 m0, s50, 73728\n" \
"ds_read_b64_tr_b16 a[116:117], v9 offset:17920\n" \
"v_mfma_f32_16x16x32_bf16 v[104:107], a[36:39], a[76:79], v[104:107]\n" \
"buffer_load_dwordx4 v15, s[28:31], s46 offen lds\n" \
"v_mfma_f32_16x16x32_bf16 v[108:111], a[40:43], a[72:75], v[108:111]\n" \
"ds_read_b64_tr_b16 a[118:119], v10 offset:17920\n" \
"v_mfma_f32_16x16x32_bf16 v[108:111], a[44:47], a[76:79], v[108:111]\n" \
"v_mfma_f32_16x16x32_bf16 v[120:123], a[48:51], a[72:75], v[120:123]\n" \
"s_add_u32 m0, s50, 77824\n" \
"ds_read_b64_tr_b16 a[120:121], v11 offset:16896\n" \
"v_mfma_f32_16x16x32_bf16 v[120:123], a[52:55], a[76:79], v[120:123]\n" \
"buffer_load_dwordx4 v16, s[28:31], s46 offen lds\n" \
"v_mfma_f32_16x16x32_bf16 v[124:127], a[56:59], a[72:75], v[124:127]\n" \
"ds_read_b64_tr_b16 a[122:123], v12 offset:16896\n" \
"v_mfma_f32_16x16x32_bf16 v[124:127], a[60:63], a[76:79], v[124:127]\n" \
"v_mfma_f32_16x16x32_bf16 v[128:131], a[0:3], a[80:83], v[128:131]\n" \
"s_mov_b32 s46, 768\n" \
"s_add_u32 m0, s50, 81920\n" \
"ds_read_b64_tr_b16 a[124:125], v11 offset:17920\n" \
"v_mfma_f32_16x16x32_bf16 v[128:131], a[4:7], a[84:87], v[128:131]\n" \
"buffer_load_dwordx4 v17, s[28:31], s46 offen lds\n" \
"v_mfma_f32_16x16x32_bf16 v[132:135], a[8:11], a[80:83], v[132:135]\n" \
"ds_read_b64_tr_b16 a[126:127], v12 offset:17920\n" \
"v_mfma_f32_16x16x32_bf16 v[132:135], a[12:15], a[84:87], v[132:135]\n" \
"v_mfma_f32_16x16x32_bf16 v[144:147], a[16:19], a[80:83], v[144:147]\n" \
"s_add_u32 m0, s50, 86016\n" \
"v_mfma_f32_16x16x32_bf16 v[144:147], a[20:23], a[84:87], v[144:147]\n" \
"buffer_load_dwordx4 v18, s[28:31], s46 offen lds\n" \
"v_mfma_f32_16x16x32_bf16 v[148:151], a[24:27], a[80:83], v[148:151]\n" \
"v_mfma_f32_16x16x32_bf16 v[148:151], a[28:31], a[84:87], v[148:151]\n" \
"v_mfma_f32_16x16x32_bf16 v[160:163], a[32:35], a[80:83], v[160:163]\n" \
"s_add_u32 m0, s50, 90112\n" \
"v_mfma_f32_16x16x32_bf16 v[160:163], a[36:39], a[84:87], v[160:163]\n" \
"buffer_load_dwordx4 v19, s[28:31], s46 offen lds\n" \
"v_mfma_f32_16x16x32_bf16 v[164:167], a[40:43], a[80:83], v[164:167]\n" \
"v_mfma_f32_16x16x32_bf16 v[164:167], a[44:47], a[84:87], v[164:167]\n" \
"s_waitcnt vmcnt(16) lgkmcnt(0)\n" \
"v_mfma_f32_16x16x32_bf16 v[176:179], a[48:51], a[80:83], v[176:179]\n" \
"s_add_u32 m0, s50, 94208\n" \
"s_barrier\n" \
"v_mfma_f32_16x16x32_bf16 v[176:179], a[52:55], a[84:87], v[176:179]\n" \
"buffer_load_dwordx4 v20, s[28:31], s46 offen lds\n" \
"v_mfma_f32_16x16x32_bf16 v[180:183], a[56:59], a[80:83], v[180:183]\n" \
"v_mfma_f32_16x16x32_bf16 v[180:183], a[60:63], a[84:87], v[180:183]\n" \
"v_mfma_f32_16x16x32_bf16 v[136:139], a[0:3], a[88:91], v[136:139]\n" \
"s_mov_b32 s47, 3145728\n" \
"s_add_u32 m0, s22, 0\n" \
"v_mfma_f32_16x16x32_bf16 v[136:139], a[4:7], a[92:95], v[136:139]\n" \
"buffer_load_dwordx4 v21, s[32:35], s47 offen offset:0 lds\n" \
"v_mfma_f32_16x16x32_bf16 v[140:143], a[8:11], a[88:91], v[140:143]\n" \
"v_mfma_f32_16x16x32_bf16 v[140:143], a[12:15], a[92:95], v[140:143]\n" \
"v_mfma_f32_16x16x32_bf16 v[152:155], a[16:19], a[88:91], v[152:155]\n" \
"s_add_u32 m0, s22, 1792\n" \
"v_mfma_f32_16x16x32_bf16 v[152:155], a[20:23], a[92:95], v[152:155]\n" \
"buffer_load_dwordx4 v21, s[32:35], s47 offen offset:256 lds\n" \
"v_mfma_f32_16x16x32_bf16 v[156:159], a[24:27], a[88:91], v[156:159]\n" \
"v_mfma_f32_16x16x32_bf16 v[156:159], a[28:31], a[92:95], v[156:159]\n" \
"v_mfma_f32_16x16x32_bf16 v[168:171], a[32:35], a[88:91], v[168:171]\n" \
"s_mov_b32 s47, 3407872\n" \
"s_add_u32 m0, s22, 1024\n" \
"v_mfma_f32_16x16x32_bf16 v[168:171], a[36:39], a[92:95], v[168:171]\n" \
"buffer_load_dwordx4 v21, s[32:35], s47 offen offset:0 lds\n" \
"v_mfma_f32_16x16x32_bf16 v[172:175], a[40:43], a[88:91], v[172:175]\n" \
"s_add_u32 s90, s64, s53\ns_addc_u32 s91, s65, 0\n" \
"v_mfma_f32_16x16x32_bf16 v[172:175], a[44:47], a[92:95], v[172:175]\n" \
"v_mfma_f32_16x16x32_bf16 v[184:187], a[48:51], a[88:91], v[184:187]\n" \
"s_add_u32 m0, s22, 2816\n" \
"v_mfma_f32_16x16x32_bf16 v[184:187], a[52:55], a[92:95], v[184:187]\n" \
"buffer_load_dwordx4 v21, s[32:35], s47 offen offset:256 lds\n" \
"v_mfma_f32_16x16x32_bf16 v[188:191], a[56:59], a[88:91], v[188:191]\n" \
"v_mfma_f32_16x16x32_bf16 v[188:191], a[60:63], a[92:95], v[188:191]\n" \
"v_mfma_f32_16x16x32_bf16 v[192:195], a[0:3], a[96:99], v[192:195]\n" \
"v_mfma_f32_16x16x32_bf16 v[192:195], a[4:7], a[100:103], v[192:195]\n" \
"global_load_dwordx4 a[240:243], v4, s[90:91] offset:0\n" \
"v_mfma_f32_16x16x32_bf16 v[196:199], a[8:11], a[96:99], v[196:199]\n" \
"v_mfma_f32_16x16x32_bf16 v[196:199], a[12:15], a[100:103], v[196:199]\n" \
"v_mfma_f32_16x16x32_bf16 v[208:211], a[16:19], a[96:99], v[208:211]\n" \
"ds_read_b64_tr_b16 a[64:65], v9 offset:32768\n" \
"v_mfma_f32_16x16x32_bf16 v[208:211], a[20:23], a[100:103], v[208:211]\n" \
"global_load_dwordx4 a[244:247], v4, s[90:91] offset:1024\n" \
"v_mfma_f32_16x16x32_bf16 v[212:215], a[24:27], a[96:99], v[212:215]\n" \
"ds_read_b64_tr_b16 a[66:67], v10 offset:32768\n" \
"v_mfma_f32_16x16x32_bf16 v[212:215], a[28:31], a[100:103], v[212:215]\n" \
"v_mfma_f32_16x16x32_bf16 v[224:227], a[32:35], a[96:99], v[224:227]\n" \
"ds_read_b64_tr_b16 a[68:69], v9 offset:33792\n" \
"v_mfma_f32_16x16x32_bf16 v[224:227], a[36:39], a[100:103], v[224:227]\n" \
"global_load_dwordx4 a[248:251], v4, s[90:91] offset:2048\n" \
"v_mfma_f32_16x16x32_bf16 v[228:231], a[40:43], a[96:99], v[228:231]\n" \
"ds_read_b64_tr_b16 a[70:71], v10 offset:33792\n" \
"v_mfma_f32_16x16x32_bf16 v[228:231], a[44:47], a[100:103], v[228:231]\n" \
"v_mfma_f32_16x16x32_bf16 v[240:243], a[48:51], a[96:99], v[240:243]\n" \
"ds_read_b64_tr_b16 a[72:73], v11 offset:32768\n" \
"v_mfma_f32_16x16x32_bf16 v[240:243], a[52:55], a[100:103], v[240:243]\n" \
"global_load_dwordx4 a[252:255], v4, s[90:91] offset:3072\n" \
"v_mfma_f32_16x16x32_bf16 v[244:247], a[56:59], a[96:99], v[244:247]\n" \
"ds_read_b64_tr_b16 a[74:75], v12 offset:32768\n" \
"v_mfma_f32_16x16x32_bf16 v[244:247], a[60:63], a[100:103], v[244:247]\n" \
"v_mfma_f32_16x16x32_bf16 v[200:203], a[0:3], a[104:107], v[200:203]\n" \
"ds_read_b64_tr_b16 a[76:77], v11 offset:33792\n" \
"v_mfma_f32_16x16x32_bf16 v[200:203], a[4:7], a[108:111], v[200:203]\n" \
"v_mfma_f32_16x16x32_bf16 v[204:207], a[8:11], a[104:107], v[204:207]\n" \
"s_add_u32 s76, s76, 2097152\ns_addc_u32 s77, s77, 0\n" \
"ds_read_b64_tr_b16 a[78:79], v12 offset:33792\n" \
"v_mfma_f32_16x16x32_bf16 v[204:207], a[12:15], a[108:111], v[204:207]\n" \
"v_mfma_f32_16x16x32_bf16 v[216:219], a[16:19], a[104:107], v[216:219]\n" \
"ds_read_b64_tr_b16 a[80:81], v9 offset:33280\n" \
"v_mfma_f32_16x16x32_bf16 v[216:219], a[20:23], a[108:111], v[216:219]\n" \
"v_mfma_f32_16x16x32_bf16 v[220:223], a[24:27], a[104:107], v[220:223]\n" \
"ds_read_b64_tr_b16 a[82:83], v10 offset:33280\n" \
"v_mfma_f32_16x16x32_bf16 v[220:223], a[28:31], a[108:111], v[220:223]\n" \
"v_mfma_f32_16x16x32_bf16 v[232:235], a[32:35], a[104:107], v[232:235]\n" \
"ds_read_b64_tr_b16 a[84:85], v9 offset:34304\n" \
"v_mfma_f32_16x16x32_bf16 v[232:235], a[36:39], a[108:111], v[232:235]\n" \
"v_mfma_f32_16x16x32_bf16 v[236:239], a[40:43], a[104:107], v[236:239]\n" \
"ds_read_b64_tr_b16 a[86:87], v10 offset:34304\n" \
"v_mfma_f32_16x16x32_bf16 v[236:239], a[44:47], a[108:111], v[236:239]\n" \
"s_mov_b64 exec, 0xffff\nglobal_load_dword v22, v1, s[76:77]\ns_mov_b64 exec, -1\n" \
"v_mfma_f32_16x16x32_bf16 v[248:251], a[48:51], a[104:107], v[248:251]\n" \
"ds_read_b64_tr_b16 a[88:89], v11 offset:33280\n" \
"v_mfma_f32_16x16x32_bf16 v[248:251], a[52:55], a[108:111], v[248:251]\n" \
"v_mfma_f32_16x16x32_bf16 v[252:255], a[56:59], a[104:107], v[252:255]\n" \
"ds_read_b64_tr_b16 a[90:91], v12 offset:33280\n" \
"v_mfma_f32_16x16x32_bf16 v[252:255], a[60:63], a[108:111], v[252:255]\n" \
"v_mfma_f32_16x16x32_bf16 a[128:131], a[0:3], a[112:115], a[128:131]\n" \
"ds_read_b64_tr_b16 a[92:93], v11 offset:34304\n" \
"v_mfma_f32_16x16x32_bf16 a[128:131], a[4:7], a[116:119], a[128:131]\n" \
"v_mfma_f32_16x16x32_bf16 a[132:135], a[8:11], a[112:115], a[132:135]\n" \
"ds_read_b64_tr_b16 a[94:95], v12 offset:34304\n" \
"v_mfma_f32_16x16x32_bf16 a[132:135], a[12:15], a[116:119], a[132:135]\n" \
"v_mfma_f32_16x16x32_bf16 a[144:147], a[16:19], a[112:115], a[144:147]\n" \
"v_mfma_f32_16x16x32_bf16 a[144:147], a[20:23], a[116:119], a[144:147]\n" \
"v_mfma_f32_16x16x32_bf16 a[148:151], a[24:27], a[112:115], a[148:151]\n" \
"v_mfma_f32_16x16x32_bf16 a[148:151], a[28:31], a[116:119], a[148:151]\n" \
"global_load_dwordx4 a[192:195], v4, s[86:87] offset:0\n" \
"v_mfma_f32_16x16x32_bf16 a[160:163], a[32:35], a[112:115], a[160:163]\n" \
"v_mov_b32 v58, s14\n" \
"v_mfma_f32_16x16x32_bf16 a[160:163], a[36:39], a[116:119], a[160:163]\n" \
"v_mov_b32 v59, s15\n" \
"v_mfma_f32_16x16x32_bf16 a[164:167], a[40:43], a[112:115], a[164:167]\n" \
"v_mfma_f32_16x16x32_bf16 a[164:167], a[44:47], a[116:119], a[164:167]\n" \
"s_waitcnt vmcnt(18) lgkmcnt(0)\n" \
"v_mfma_f32_16x16x32_bf16 a[176:179], a[48:51], a[112:115], a[176:179]\n" \
"s_barrier\n" \
"v_mfma_f32_16x16x32_bf16 a[176:179], a[52:55], a[116:119], a[176:179]\n" \
"v_mfma_f32_16x16x32_bf16 a[180:183], a[56:59], a[112:115], a[180:183]\n" \
"v_mfma_f32_16x16x32_bf16 a[180:183], a[60:63], a[116:119], a[180:183]\n" \
"buffer_store_dwordx2 v[58:59], v56, s[36:39], s23 offen sc1\n" \
"v_mfma_f32_16x16x32_bf16 a[136:139], a[0:3], a[120:123], a[136:139]\n" \
"v_mfma_f32_16x16x32_bf16 a[136:139], a[4:7], a[124:127], a[136:139]\n" \
"v_mfma_f32_16x16x32_bf16 a[140:143], a[8:11], a[120:123], a[140:143]\n" \
"ds_read_b128 a[0:3], v5 offset:32768\n" \
"v_mfma_f32_16x16x32_bf16 a[140:143], a[12:15], a[124:127], a[140:143]\n" \
"ds_read_b128 a[4:7], v6 offset:32768\n" \
"v_mfma_f32_16x16x32_bf16 a[152:155], a[16:19], a[120:123], a[152:155]\n" \
"ds_read_b128 a[8:11], v5 offset:34816\n" \
"v_mfma_f32_16x16x32_bf16 a[152:155], a[20:23], a[124:127], a[152:155]\n" \
"ds_read_b128 a[12:15], v6 offset:34816\n" \
"v_mfma_f32_16x16x32_bf16 a[156:159], a[24:27], a[120:123], a[156:159]\n" \
"ds_read_b128 a[16:19], v5 offset:36864\n" \
"v_mfma_f32_16x16x32_bf16 a[156:159], a[28:31], a[124:127], a[156:159]\n" \
"ds_read_b128 a[20:23], v6 offset:36864\n" \
"v_mfma_f32_16x16x32_bf16 a[168:171], a[32:35], a[120:123], a[168:171]\n" \
"ds_read_b128 a[24:27], v5 offset:38912\n" \
"v_mfma_f32_16x16x32_bf16 a[168:171], a[36:39], a[124:127], a[168:171]\n" \
"ds_read_b128 a[28:31], v6 offset:38912\n" \
"v_mfma_f32_16x16x32_bf16 a[172:175], a[40:43], a[120:123], a[172:175]\n" \
"ds_read_b128 a[32:35], v5 offset:40960\n" \
"v_mfma_f32_16x16x32_bf16 a[172:175], a[44:47], a[124:127], a[172:175]\n" \
"ds_read_b128 a[36:39], v6 offset:40960\n" \
"v_mfma_f32_16x16x32_bf16 a[184:187], a[48:51], a[120:123], a[184:187]\n" \
"ds_read_b128 a[40:43], v5 offset:43008\n" \
"v_mfma_f32_16x16x32_bf16 a[184:187], a[52:55], a[124:127], a[184:187]\n" \
"ds_read_b128 a[44:47], v6 offset:43008\n" \
"v_mfma_f32_16x16x32_bf16 a[188:191], a[56:59], a[120:123], a[188:191]\n" \
"ds_read_b128 a[48:51], v5 offset:45056\n" \
"v_mfma_f32_16x16x32_bf16 a[188:191], a[60:63], a[124:127], a[188:191]\n" \
"s_waitcnt lgkmcnt(9)\n" \
"ds_read_b128 a[52:55], v6 offset:45056\n" \
"v_mfma_f32_16x16x32_bf16 v[64:67], a[0:3], a[64:67], v[64:67]\n" \
"s_mov_b32 s47, 3145728\n" \
"s_add_u32 m0, s22, 16256\n" \
"ds_read_b128 a[56:59], v5 offset:47104\n" \
"v_mfma_f32_16x16x32_bf16 v[64:67], a[4:7], a[68:71], v[64:67]\n" \
"LH6_%=:\ns_cbranch_execz LH10_%=\n" \
"buffer_load_dwordx4 v21, s[32:35], s47 offen offset:128 lds\n" \
"v_mfma_f32_16x16x32_bf16 v[68:71], a[8:11], a[64:67], v[68:71]\n" \
"s_waitcnt lgkmcnt(7)\n" \
"ds_read_b128 a[60:63], v6 offset:47104\n" \
"v_mfma_f32_16x16x32_bf16 v[68:71], a[12:15], a[68:71], v[68:71]\n" \
"v_mfma_f32_16x16x32_bf16 v[80:83], a[16:19], a[64:67], v[80:83]\n" \
"s_add_u32 m0, s22, 18048\n" \
"ds_read_b64_tr_b16 a[96:97], v9 offset:49152\n" \
"v_mfma_f32_16x16x32_bf16 v[80:83], a[20:23], a[68:71], v[80:83]\n" \
"buffer_load_dwordx4 v21, s[32:35], s47 offen offset:384 lds\n" \
"v_mfma_f32_16x16x32_bf16 v[84:87], a[24:27], a[64:67], v[84:87]\n" \
"s_waitcnt lgkmcnt(5)\n" \
"ds_read_b64_tr_b16 a[98:99], v10 offset:49152\n" \
"v_mfma_f32_16x16x32_bf16 v[84:87], a[28:31], a[68:71], v[84:87]\n" \
"v_mfma_f32_16x16x32_bf16 v[96:99], a[32:35], a[64:67], v[96:99]\n" \
"s_mov_b32 s47, 3407872\n" \
"s_add_u32 m0, s22, 17280\n" \
"ds_read_b64_tr_b16 a[100:101], v9 offset:50176\n" \
"v_mfma_f32_16x16x32_bf16 v[96:99], a[36:39], a[68:71], v[96:99]\n" \
"buffer_load_dwordx4 v21, s[32:35], s47 offen offset:128 lds\n" \
"v_mfma_f32_16x16x32_bf16 v[100:103], a[40:43], a[64:67], v[100:103]\n" \
"s_waitcnt lgkmcnt(3)\n" \
"ds_read_b64_tr_b16 a[102:103], v10 offset:50176\n" \
"v_mfma_f32_16x16x32_bf16 v[100:103], a[44:47], a[68:71], v[100:103]\n" \
"v_mfma_f32_16x16x32_bf16 v[112:115], a[48:51], a[64:67], v[112:115]\n" \
"s_add_u32 m0, s22, 19072\n" \
"ds_read_b64_tr_b16 a[104:105], v11 offset:49152\n" \
"v_mfma_f32_16x16x32_bf16 v[112:115], a[52:55], a[68:71], v[112:115]\n" \
"buffer_load_dwordx4 v21, s[32:35], s47 offen offset:384 lds\n" \
"v_mfma_f32_16x16x32_bf16 v[116:119], a[56:59], a[64:67], v[116:119]\n" \
"ds_read_b64_tr_b16 a[106:107], v12 offset:49152\n" \
"v_mfma_f32_16x16x32_bf16 v[116:119], a[60:63], a[68:71], v[116:119]\n" \
"v_mfma_f32_16x16x32_bf16 v[72:75], a[0:3], a[72:75], v[72:75]\n" \
"s_mov_b32 s46, 896\n" \
"s_add_u32 m0, s50, 0\n" \
"ds_read_b64_tr_b16 a[108:109], v11 offset:50176\n" \
"v_mfma_f32_16x16x32_bf16 v[72:75], a[4:7], a[76:79], v[72:75]\n" \
"buffer_load_dwordx4 v13, s[28:31], s46 offen lds\n" \
"v_mfma_f32_16x16x32_bf16 v[76:79], a[8:11], a[72:75], v[76:79]\n" \
"ds_read_b64_tr_b16 a[110:111], v12 offset:50176\n" \
"v_mfma_f32_16x16x32_bf16 v[76:79], a[12:15], a[76:79], v[76:79]\n" \
"v_mfma_f32_16x16x32_bf16 v[88:91], a[16:19], a[72:75], v[88:91]\n" \
"s_add_u32 m0, s50, 4096\n" \
"ds_read_b64_tr_b16 a[112:113], v9 offset:49664\n" \
"v_mfma_f32_16x16x32_bf16 v[88:91], a[20:23], a[76:79], v[88:91]\n" \
"buffer_load_dwordx4 v14, s[28:31], s46 offen lds\n" \
"v_mfma_f32_16x16x32_bf16 v[92:95], a[24:27], a[72:75], v[92:95]\n" \
"ds_read_b64_tr_b16 a[114:115], v10 offset:49664\n" \
"v_mfma_f32_16x16x32_bf16 v[92:95], a[28:31], a[76:79], v[92:95]\n" \
"v_mfma_f32_16x16x32_bf16 v[104:107], a[32:35], a[72:75], v[104:107]\n" \
"s_add_u32 m0, s50, 8192\n" \
"ds_read_b64_tr_b16 a[116:117], v9 offset:50688\n" \
"v_mfma_f32_16x16x32_bf16 v[104:107], a[36:39], a[76:79], v[104:107]\n" \
"buffer_load_dwordx4 v15, s[28:31], s46 offen lds\n" \
"v_mfma_f32_16x16x32_bf16 v[108:111], a[40:43], a[72:75], v[108:111]\n" \
"ds_read_b64_tr_b16 a[118:119], v10 offset:50688\n" \
"v_mfma_f32_16x16x32_bf16 v[108:111], a[44:47], a[76:79], v[108:111]\n" \
"v_mfma_f32_16x16x32_bf16 v[120:123], a[48:51], a[72:75], v[120:123]\n" \
"s_add_u32 m0, s50, 12288\n" \
"ds_read_b64_tr_b16 a[120:121], v11 offset:49664\n" \
"v_mfma_f32_16x16x32_bf16 v[120:123], a[52:55], a[76:79], v[120:123]\n" \
"buffer_load_dwordx4 v16, s[28:31], s46 offen lds\n" \
"v_mfma_f32_16x16x32_bf16 v[124:127], a[56:59], a[72:75], v[124:127]\n" \
"ds_read_b64_tr_b16 a[122:123], v12 offset:49664\n" \
"v_mfma_f32_16x16x32_bf16 v[124:127], a[60:63], a[76:79], v[124:127]\n" \
"v_mfma_f32_16x16x32_bf16 v[128:131], a[0:3], a[80:83], v[128:131]\n" \
"s_mov_b32 s46, 896\n" \
"s_add_u32 m0, s50, 16384\n" \
"ds_read_b64_tr_b16 a[124:125], v11 offset:50688\n" \
"v_mfma_f32_16x16x32_bf16 v[128:131], a[4:7], a[84:87], v[128:131]\n" \
"buffer_load_dwordx4 v17, s[28:31], s46 offen lds\n" \
"v_mfma_f32_16x16x32_bf16 v[132:135], a[8:11], a[80:83], v[132:135]\n" \
"ds_read_b64_tr_b16 a[126:127], v12 offset:50688\n" \
"v_mfma_f32_16x16x32_bf16 v[132:135], a[12:15], a[84:87], v[132:135]\n" \
"v_mfma_f32_16x16x32_bf16 v[144:147], a[16:19], a[80:83], v[144:147]\n" \
"s_add_u32 m0, s50, 20480\n" \
"v_mfma_f32_16x16x32_bf16 v[144:147], a[20:23], a[84:87], v[144:147]\n" \
"buffer_load_dwordx4 v18, s[28:31], s46 offen lds\n" \
"v_mfma_f32_16x16x32_bf16 v[148:151], a[24:27], a[80:83], v[148:151]\n" \
"v_mfma_f32_16x16x32_bf16 v[148:151], a[28:31], a[84:87], v[148:151]\n" \
"v_mfma_f32_16x16x32_bf16 v[160:163], a[32:35], a[80:83], v[160:163]\n" \
"s_add_u32 m0, s50, 24576\n" \
"v_mfma_f32_16x16x32_bf16 v[160:163], a[36:39], a[84:87], v[160:163]\n" \
"buffer_load_dwordx4 v19, s[28:31], s46 offen lds\n" \
"v_mfma_f32_16x16x32_bf16 v[164:167], a[40:43], a[80:83], v[164:167]\n" \
"v_mfma_f32_16x16x32_bf16 v[164:167], a[44:47], a[84:87], v[164:167]\n" \
"s_waitcnt vmcnt(18) lgkmcnt(0)\n" \
"v_mfma_f32_16x16x32_bf16 v[176:179], a[48:51], a[80:83], v[176:179]\n" \
"s_add_u32 m0, s50, 28672\n" \
"s_barrier\n" \
"v_mfma_f32_16x16x32_bf16 v[176:179], a[52:55], a[84:87], v[176:179]\n" \
"buffer_load_dwordx4 v20, s[28:31], s46 offen lds\n" \
"v_mfma_f32_16x16x32_bf16 v[180:183], a[56:59], a[80:83], v[180:183]\n" \
"v_mfma_f32_16x16x32_bf16 v[180:183], a[60:63], a[84:87], v[180:183]\n" \
"v_mfma_f32_16x16x32_bf16 v[136:139], a[0:3], a[88:91], v[136:139]\n" \
"s_mov_b32 s47, 3670016\n" \
"s_add_u32 m0, s22, 32768\n" \
"v_mfma_f32_16x16x32_bf16 v[136:139], a[4:7], a[92:95], v[136:139]\n" \
"buffer_load_dwordx4 v21, s[32:35], s47 offen offset:0 lds\n" \
"v_mfma_f32_16x16x32_bf16 v[140:143], a[8:11], a[88:91], v[140:143]\n" \
"v_mfma_f32_16x16x32_bf16 v[140:143], a[12:15], a[92:95], v[140:143]\n" \
"v_mfma_f32_16x16x32_bf16 v[152:155], a[16:19], a[88:91], v[152:155]\n" \
"s_add_u32 m0, s22, 34560\n" \
"v_mfma_f32_16x16x32_bf16 v[152:155], a[20:23], a[92:95], v[152:155]\n" \
"buffer_load_dwordx4 v21, s[32:35], s47 offen offset:256 lds\n" \
"v_mfma_f32_16x16x32_bf16 v[156:159], a[24:27], a[88:91], v[156:159]\n" \
"v_mfma_f32_16x16x32_bf16 v[156:159], a[28:31], a[92:95], v[156:159]\n" \
"v_mfma_f32_16x16x32_bf16 v[168:171], a[32:35], a[88:91], v[168:171]\n" \
"s_mov_b32 s47, 3932160\n" \
"s_add_u32 m0, s22, 33792\n" \
"v_mfma_f32_16x16x32_bf16 v[168:171], a[36:39], a[92:95], v[168:171]\n" \
"buffer_load_dwordx4 v21, s[32:35], s47 offen offset:0 lds\n" \
"v_mfma_f32_16x16x32_bf16 v[172:175], a[40:43], a[88:91], v[172:175]\n" \
"v_mfma_f32_16x16x32_bf16 v[172:175], a[44:47], a[92:95], v[172:175]\n" \
"v_mfma_f32_16x16x32_bf16 v[184:187], a[48:51], a[88:91], v[184:187]\n" \
"s_add_u32 m0, s22, 35584\n" \
"v_mfma_f32_16x16x32_bf16 v[184:187], a[52:55], a[92:95], v[184:187]\n" \
"buffer_load_dwordx4 v21, s[32:35], s47 offen offset:256 lds\n" \
"v_mfma_f32_16x16x32_bf16 v[188:191], a[56:59], a[88:91], v[188:191]\n" \
"v_mfma_f32_16x16x32_bf16 v[188:191], a[60:63], a[92:95], v[188:191]\n" \
"v_mfma_f32_16x16x32_bf16 v[192:195], a[0:3], a[96:99], v[192:195]\n" \
"v_mfma_f32_16x16x32_bf16 v[192:195], a[4:7], a[100:103], v[192:195]\n" \
"buffer_load_dwordx4 v[30:33], v23, s[68:71], 0 offen offset:0 nt\n" \
"v_mfma_f32_16x16x32_bf16 v[196:199], a[8:11], a[96:99], v[196:199]\n" \
"s_mov_b64 s[28:29], s[64:65]\n" \
"v_mfma_f32_16x16x32_bf16 v[196:199], a[12:15], a[100:103], v[196:199]\n" \
"v_mfma_f32_16x16x32_bf16 v[208:211], a[16:19], a[96:99], v[208:211]\n" \
"ds_read_b64_tr_b16 a[64:65], v9 offset:0\n" \
"v_mfma_f32_16x16x32_bf16 v[208:211], a[20:23], a[100:103], v[208:211]\n" \
"buffer_load_dwordx4 v[34:37], v24, s[68:71], 0 offen offset:0 nt\n" \
"v_mfma_f32_16x16x32_bf16 v[212:215], a[24:27], a[96:99], v[212:215]\n" \
"s_and_b32 s29, s29, 0xffff\n" \
"ds_read_b64_tr_b16 a[66:67], v10 offset:0\n" \
"v_mfma_f32_16x16x32_bf16 v[212:215], a[28:31], a[100:103], v[212:215]\n" \
"v_mfma_f32_16x16x32_bf16 v[224:227], a[32:35], a[96:99], v[224:227]\n" \
"ds_read_b64_tr_b16 a[68:69], v9 offset:1024\n" \
"v_mfma_f32_16x16x32_bf16 v[224:227], a[36:39], a[100:103], v[224:227]\n" \
"buffer_load_dwordx4 v[38:41], v24, s[68:71], 0 offen offset:1024 nt\n" \
"v_mfma_f32_16x16x32_bf16 v[228:231], a[40:43], a[96:99], v[228:231]\n" \
"s_mov_b32 s30, 4194304\n" \
"ds_read_b64_tr_b16 a[70:71], v10 offset:1024\n" \
"v_mfma_f32_16x16x32_bf16 v[228:231], a[44:47], a[100:103], v[228:231]\n" \
"v_mfma_f32_16x16x32_bf16 v[240:243], a[48:51], a[96:99], v[240:243]\n" \
"ds_read_b64_tr_b16 a[72:73], v11 offset:0\n" \
"v_mfma_f32_16x16x32_bf16 v[240:243], a[52:55], a[100:103], v[240:243]\n" \
"buffer_load_dwordx4 v[42:45], v24, s[68:71], 0 offen offset:2048 nt\n" \
"v_mfma_f32_16x16x32_bf16 v[244:247], a[56:59], a[96:99], v[244:247]\n" \
"s_lshl_b32 s60, s7, 18\n" \
"ds_read_b64_tr_b16 a[74:75], v12 offset:0\n" \
"v_mfma_f32_16x16x32_bf16 v[244:247], a[60:63], a[100:103], v[244:247]\n" \
"v_mfma_f32_16x16x32_bf16 v[200:203], a[0:3], a[104:107], v[200:203]\n" \
"ds_read_b64_tr_b16 a[76:77], v11 offset:1024\n" \
"v_mfma_f32_16x16x32_bf16 v[200:203], a[4:7], a[108:111], v[200:203]\n" \
"buffer_load_dwordx4 v[46:49], v24, s[68:71], 0 offen offset:3072 nt\n" \
"v_mfma_f32_16x16x32_bf16 v[204:207], a[8:11], a[104:107], v[204:207]\n" \
"s_sub_u32 s61, 3932160, s60\n" \
"ds_read_b64_tr_b16 a[78:79], v12 offset:1024\n" \
"v_mfma_f32_16x16x32_bf16 v[204:207], a[12:15], a[108:111], v[204:207]\n" \
"v_mfma_f32_16x16x32_bf16 v[216:219], a[16:19], a[104:107], v[216:219]\n" \
"ds_read_b64_tr_b16 a[80:81], v9 offset:512\n" \
"v_mfma_f32_16x16x32_bf16 v[216:219], a[20:23], a[108:111], v[216:219]\n" \
"buffer_load_dword v50, v25, s[68:71], 0 offen offset:0\n" \
"v_mfma_f32_16x16x32_bf16 v[220:223], a[24:27], a[104:107], v[220:223]\n" \
"ds_read_b64_tr_b16 a[82:83], v10 offset:512\n" \
"v_add_u32 v13, s60, v63\n" \
"v_mfma_f32_16x16x32_bf16 v[220:223], a[28:31], a[108:111], v[220:223]\n" \
"v_mfma_f32_16x16x32_bf16 v[232:235], a[32:35], a[104:107], v[232:235]\n" \
"ds_read_b64_tr_b16 a[84:85], v9 offset:1536\n" \
"v_mfma_f32_16x16x32_bf16 v[232:235], a[36:39], a[108:111], v[232:235]\n" \
"buffer_load_dword v51, v24, s[68:71], 0 offen offset:16\n" \
"v_mfma_f32_16x16x32_bf16 v[236:239], a[40:43], a[104:107], v[236:239]\n" \
"ds_read_b64_tr_b16 a[86:87], v10 offset:1536\n" \
"v_add_u32 v17, s61, v63\n" \
"v_mfma_f32_16x16x32_bf16 v[236:239], a[44:47], a[108:111], v[236:239]\n" \
"v_mfma_f32_16x16x32_bf16 v[248:251], a[48:51], a[104:107], v[248:251]\n" \
"ds_read_b64_tr_b16 a[88:89], v11 offset:512\n" \
"v_mfma_f32_16x16x32_bf16 v[248:251], a[52:55], a[108:111], v[248:251]\n" \
"buffer_load_dword v52, v24, s[68:71], 0 offen offset:1040\n" \
"v_mfma_f32_16x16x32_bf16 v[252:255], a[56:59], a[104:107], v[252:255]\n" \
"ds_read_b64_tr_b16 a[90:91], v12 offset:512\n" \
"v_add_u32 v14, 0x10000, v13\n" \
"v_mfma_f32_16x16x32_bf16 v[252:255], a[60:63], a[108:111], v[252:255]\n" \
"v_mfma_f32_16x16x32_bf16 a[128:131], a[0:3], a[112:115], a[128:131]\n" \
"ds_read_b64_tr_b16 a[92:93], v11 offset:1536\n" \
"v_mfma_f32_16x16x32_bf16 a[128:131], a[4:7], a[116:119], a[128:131]\n" \
"buffer_load_dword v53, v24, s[68:71], 0 offen offset:2064\n" \
"v_mfma_f32_16x16x32_bf16 a[132:135], a[8:11], a[112:115], a[132:135]\n" \
"ds_read_b64_tr_b16 a[94:95], v12 offset:1536\n" \
"v_add_u32 v18, 0x10000, v17\n" \
"v_mfma_f32_16x16x32_bf16 a[132:135], a[12:15], a[116:119], a[132:135]\n" \
"v_mfma_f32_16x16x32_bf16 a[144:147], a[16:19], a[112:115], a[144:147]\n" \
"v_mfma_f32_16x16x32_bf16 a[144:147], a[20:23], a[116:119], a[144:147]\n" \
"buffer_load_dword v54, v24, s[68:71], 0 offen offset:3088\n" \
"v_mfma_f32_16x16x32_bf16 a[148:151], a[24:27], a[112:115], a[148:151]\n" \
"v_add_u32 v15, 0x10000, v14\n" \
"v_mfma_f32_16x16x32_bf16 a[148:151], a[28:31], a[116:119], a[148:151]\n" \
"v_mfma_f32_16x16x32_bf16 a[160:163], a[32:35], a[112:115], a[160:163]\n" \
"v_mfma_f32_16x16x32_bf16 a[160:163], a[36:39], a[116:119], a[160:163]\n" \
"global_load_short_d16_hi v28, v29, s[12:13]\n" \
"v_mfma_f32_16x16x32_bf16 a[164:167], a[40:43], a[112:115], a[164:167]\n" \
"v_add_u32 v19, 0x10000, v18\n" \
"v_mfma_f32_16x16x32_bf16 a[164:167], a[44:47], a[116:119], a[164:167]\n" \
"s_waitcnt vmcnt(23) lgkmcnt(0)\n" \
"v_mfma_f32_16x16x32_bf16 a[176:179], a[48:51], a[112:115], a[176:179]\n" \
"s_barrier\n" \
"v_mfma_f32_16x16x32_bf16 a[176:179], a[52:55], a[116:119], a[176:179]\n" \
"global_load_dwordx4 a[196:199], v4, s[86:87] offset:1024\n" \
"v_mfma_f32_16x16x32_bf16 a[180:183], a[56:59], a[112:115], a[180:183]\n" \
"v_add_u32 v16, 0x10000, v15\n" \
"v_mfma_f32_16x16x32_bf16 a[180:183], a[60:63], a[116:119], a[180:183]\n" \
"v_mfma_f32_16x16x32_bf16 a[136:139], a[0:3], a[120:123], a[136:139]\n" \
"v_mfma_f32_16x16x32_bf16 a[136:139], a[4:7], a[124:127], a[136:139]\n" \
"v_mfma_f32_16x16x32_bf16 a[140:143], a[8:11], a[120:123], a[140:143]\n" \
"ds_read_b128 a[0:3], v7 offset:0\n" \
"v_add_u32 v20, 0x10000, v19\n" \
"v_mfma_f32_16x16x32_bf16 a[140:143], a[12:15], a[124:127], a[140:143]\n" \
"ds_read_b128 a[4:7], v8 offset:0\n" \
"v_mfma_f32_16x16x32_bf16 a[152:155], a[16:19], a[120:123], a[152:155]\n" \
"ds_read_b128 a[8:11], v7 offset:2048\n" \
"v_mfma_f32_16x16x32_bf16 a[152:155], a[20:23], a[124:127], a[152:155]\n" \
"ds_read_b128 a[12:15], v8 offset:2048\n" \
"v_mfma_f32_16x16x32_bf16 a[156:159], a[24:27], a[120:123], a[156:159]\n" \
"ds_read_b128 a[16:19], v7 offset:4096\n" \
"v_mfma_f32_16x16x32_bf16 a[156:159], a[28:31], a[124:127], a[156:159]\n" \
"ds_read_b128 a[20:23], v8 offset:4096\n" \
"v_mfma_f32_16x16x32_bf16 a[168:171], a[32:35], a[120:123], a[168:171]\n" \
"ds_read_b128 a[24:27], v7 offset:6144\n" \
"v_mfma_f32_16x16x32_bf16 a[168:171], a[36:39], a[124:127], a[168:171]\n" \
"ds_read_b128 a[28:31], v8 offset:6144\n" \
"v_mfma_f32_16x16x32_bf16 a[172:175], a[40:43], a[120:123], a[172:175]\n" \
"ds_read_b128 a[32:35], v7 offset:8192\n" \
"v_mfma_f32_16x16x32_bf16 a[172:175], a[44:47], a[124:127], a[172:175]\n" \
"ds_read_b128 a[36:39], v8 offset:8192\n" \
"v_mfma_f32_16x16x32_bf16 a[184:187], a[48:51], a[120:123], a[184:187]\n" \
"ds_read_b128 a[40:43], v7 offset:10240\n" \
"v_mfma_f32_16x16x32_bf16 a[184:187], a[52:55], a[124:127], a[184:187]\n" \
"ds_read_b128 a[44:47], v8 offset:10240\n" \
"v_mfma_f32_16x16x32_bf16 a[188:191], a[56:59], a[120:123], a[188:191]\n" \
"ds_read_b128 a[48:51], v7 offset:12288\n" \
"v_mfma_f32_16x16x32_bf16 a[188:191], a[60:63], a[124:127], a[188:191]\n" \
"s_waitcnt lgkmcnt(9)\n" \
"ds_read_b128 a[52:55], v8 offset:12288\n" \
"v_mfma_f32_16x16x32_bf16 v[64:67], a[0:3], a[64:67], v[64:67]\n" \
"ds_read_b128 a[56:59], v7 offset:14336\n" \
"v_mfma_f32_16x16x32_bf16 v[64:67], a[4:7], a[68:71], v[64:67]\n" \
"buffer_load_dwordx2 v[58:59], v57, s[36:39], 0 offen sc1\n" \
"v_mfma_f32_16x16x32_bf16 v[68:71], a[8:11], a[64:67], v[68:71]\n" \
"s_waitcnt lgkmcnt(7)\n" \
"ds_read_b128 a[60:63], v8 offset:14336\n" \
"v_mfma_f32_16x16x32_bf16 v[68:71], a[12:15], a[68:71], v[68:71]\n" \
"v_mfma_f32_16x16x32_bf16 v[80:83], a[16:19], a[64:67], v[80:83]\n" \
"s_mov_b32 s47, 3670016\n" \
"s_add_u32 m0, s22, 49024\n" \
"ds_read_b64_tr_b16 a[96:97], v9 offset:16384\n" \
"v_mfma_f32_16x16x32_bf16 v[80:83], a[20:23], a[68:71], v[80:83]\n" \
"buffer_load_dwordx4 v21, s[32:35], s47 offen offset:128 lds\n" \
"v_mfma_f32_16x16x32_bf16 v[84:87], a[24:27], a[64:67], v[84:87]\n" \
"s_waitcnt lgkmcnt(5)\n" \
"ds_read_b64_tr_b16 a[98:99], v10 offset:16384\n" \
"v_mfma_f32_16x16x32_bf16 v[84:87], a[28:31], a[68:71], v[84:87]\n" \
"v_mfma_f32_16x16x32_bf16 v[96:99], a[32:35], a[64:67], v[96:99]\n" \
"s_add_u32 m0, s22, 50816\n" \
"ds_read_b64_tr_b16 a[100:101], v9 offset:17408\n" \
"v_mfma_f32_16x16x32_bf16 v[96:99], a[36:39], a[68:71], v[96:99]\n" \
"buffer_load_dwordx4 v21, s[32:35], s47 offen offset:384 lds\n" \
"v_mfma_f32_16x16x32_bf16 v[100:103], a[40:43], a[64:67], v[100:103]\n" \
"s_waitcnt lgkmcnt(3)\n" \
"ds_read_b64_tr_b16 a[102:103], v10 offset:17408\n" \
"v_mfma_f32_16x16x32_bf16 v[100:103], a[44:47], a[68:71], v[100:103]\n" \
"v_mfma_f32_16x16x32_bf16 v[112:115], a[48:51], a[64:67], v[112:115]\n" \
"s_mov_b32 s47, 3932160\n" \
"s_add_u32 m0, s22, 50048\n" \
"ds_read_b64_tr_b16 a[104:105], v11 offset:16384\n" \
"v_mfma_f32_16x16x32_bf16 v[112:115], a[52:55], a[68:71], v[112:115]\n" \
"buffer_load_dwordx4 v21, s[32:35], s47 offen offset:128 lds\n" \
"v_mfma_f32_16x16x32_bf16 v[116:119], a[56:59], a[64:67], v[116:119]\n" \
"ds_read_b64_tr_b16 a[106:107], v12 offset:16384\n" \
"v_mfma_f32_16x16x32_bf16 v[116:119], a[60:63], a[68:71], v[116:119]\n" \
"s_add_u32 s76, s76, 2097152\ns_addc_u32 s77, s77, 0\n" \
"v_mfma_f32_16x16x32_bf16 v[72:75], a[0:3], a[72:75], v[72:75]\n" \
"s_add_u32 m0, s22, 51840\n" \
"ds_read_b64_tr_b16 a[108:109], v11 offset:17408\n" \
"v_mfma_f32_16x16x32_bf16 v[72:75], a[4:7], a[76:79], v[72:75]\n" \
"buffer_load_dwordx4 v21, s[32:35], s47 offen offset:384 lds\n" \
"v_mfma_f32_16x16x32_bf16 v[76:79], a[8:11], a[72:75], v[76:79]\n" \
"ds_read_b64_tr_b16 a[110:111], v12 offset:17408\n" \
"v_mfma_f32_16x16x32_bf16 v[76:79], a[12:15], a[76:79], v[76:79]\n" \
"v_mfma_f32_16x16x32_bf16 v[88:91], a[16:19], a[72:75], v[88:91]\n" \
"ds_read_b64_tr_b16 a[112:113], v9 offset:16896\n" \
"v_mfma_f32_16x16x32_bf16 v[88:91], a[20:23], a[76:79], v[88:91]\n" \
"v_mfma_f32_16x16x32_bf16 v[92:95], a[24:27], a[72:75], v[92:95]\n" \
"ds_read_b64_tr_b16 a[114:115], v10 offset:16896\n" \
"v_mfma_f32_16x16x32_bf16 v[92:95], a[28:31], a[76:79], v[92:95]\n" \
"s_mov_b64 exec, 0xffff\nglobal_load_dword v22, v1, s[76:77]\ns_mov_b64 exec, -1\n" \
"v_mfma_f32_16x16x32_bf16 v[104:107], a[32:35], a[72:75], v[104:107]\n" \
"s_add_u32 s76, s76, 2097152\ns_addc_u32 s77, s77, 0\n" \
"ds_read_b64_tr_b16 a[116:117], v9 offset:17920\n" \
"v_mfma_f32_16x16x32_bf16 v[104:107], a[36:39], a[76:79], v[104:107]\n" \
"v_mfma_f32_16x16x32_bf16 v[108:111], a[40:43], a[72:75], v[108:111]\n" \
"ds_read_b64_tr_b16 a[118:119], v10 offset:17920\n" \
"v_mfma_f32_16x16x32_bf16 v[108:111], a[44:47], a[76:79], v[108:111]\n" \
"s_mov_b64 exec, 0xffff\nglobal_load_dword v22, v1, s[76:77]\ns_mov_b64 exec, -1\n" \
"v_mfma_f32_16x16x32_bf16 v[120:123], a[48:51], a[72:75], v[120:123]\n" \
"ds_read_b64_tr_b16 a[120:121], v11 offset:16896\n" \
"v_mfma_f32_16x16x32_bf16 v[120:123], a[52:55], a[76:79], v[120:123]\n" \
"v_mfma_f32_16x16x32_bf16 v[124:127], a[56:59], a[72:75], v[124:127]\n" \
"ds_read_b64_tr_b16 a[122:123], v12 offset:16896\n" \
"v_mfma_f32_16x16x32_bf16 v[124:127], a[60:63], a[76:79], v[124:127]\n" \
"v_mfma_f32_16x16x32_bf16 v[128:131], a[0:3], a[80:83], v[128:131]\n" \
"ds_read_b64_tr_b16 a[124:125], v11 offset:17920\n" \
"v_mfma_f32_16x16x32_bf16 v[128:131], a[4:7], a[84:87], v[128:131]\n" \
"v_mfma_f32_16x16x32_bf16 v[132:135], a[8:11], a[80:83], v[132:135]\n" \
"ds_read_b64_tr_b16 a[126:127], v12 offset:17920\n" \
"v_mfma_f32_16x16x32_bf16 v[132:135], a[12:15], a[84:87], v[132:135]\n" \
"v_mfma_f32_16x16x32_bf16 v[144:147], a[16:19], a[80:83], v[144:147]\n" \
"v_mfma_f32_16x16x32_bf16 v[144:147], a[20:23], a[84:87], v[144:147]\n" \
"v_mfma_f32_16x16x32_bf16 v[148:151], a[24:27], a[80:83], v[148:151]\n" \
"v_mfma_f32_16x16x32_bf16 v[148:151], a[28:31], a[84:87], v[148:151]\n" \
"v_mfma_f32_16x16x32_bf16 v[160:163], a[32:35], a[80:83], v[160:163]\n" \
"v_mfma_f32_16x16x32_bf16 v[160:163], a[36:39], a[84:87], v[160:163]\n" \
"v_mfma_f32_16x16x32_bf16 v[164:167], a[40:43], a[80:83], v[164:167]\n" \
"v_mfma_f32_16x16x32_bf16 v[164:167], a[44:47], a[84:87], v[164:167]\n" \
"s_waitcnt vmcnt(19) lgkmcnt(0)\n" \
"v_mfma_f32_16x16x32_bf16 v[176:179], a[48:51], a[80:83], v[176:179]\n" \
"s_barrier\n" \
"v_mfma_f32_16x16x32_bf16 v[176:179], a[52:55], a[84:87], v[176:179]\n" \
"v_mfma_f32_16x16x32_bf16 v[180:183], a[56:59], a[80:83], v[180:183]\n" \
"v_mfma_f32_16x16x32_bf16 v[180:183], a[60:63], a[84:87], v[180:183]\n" \
"v_mfma_f32_16x16x32_bf16 v[136:139], a[0:3], a[88:91], v[136:139]\n" \
"v_mfma_f32_16x16x32_bf16 v[136:139], a[4:7], a[92:95], v[136:139]\n" \
"v_mfma_f32_16x16x32_bf16 v[140:143], a[8:11], a[88:91], v[140:143]\n" \
"v_mfma_f32_16x16x32_bf16 v[140:143], a[12:15], a[92:95], v[140:143]\n" \
"v_mfma_f32_16x16x32_bf16 v[152:155], a[16:19], a[88:91], v[152:155]\n" \
"v_mfma_f32_16x16x32_bf16 v[152:155], a[20:23], a[92:95], v[152:155]\n" \
"v_mfma_f32_16x16x32_bf16 v[156:159], a[24:27], a[88:91], v[156:159]\n" \
"v_mfma_f32_16x16x32_bf16 v[156:159], a[28:31], a[92:95], v[156:159]\n" \
"v_mfma_f32_16x16x32_bf16 v[168:171], a[32:35], a[88:91], v[168:171]\n" \
"v_mfma_f32_16x16x32_bf16 v[168:171], a[36:39], a[92:95], v[168:171]\n" \
"v_mfma_f32_16x16x32_bf16 v[172:175], a[40:43], a[88:91], v[172:175]\n" \
"v_mfma_f32_16x16x32_bf16 v[172:175], a[44:47], a[92:95], v[172:175]\n" \
"s_waitcnt vmcnt(6)\n" \
"v_mfma_f32_16x16x32_bf16 v[184:187], a[48:51], a[88:91], v[184:187]\n" \
"v_cmp_ne_u64 vcc, s[14:15], v[58:59]\n" \
"v_mfma_f32_16x16x32_bf16 v[184:187], a[52:55], a[92:95], v[184:187]\n" \
"v_mfma_f32_16x16x32_bf16 v[188:191], a[56:59], a[88:91], v[188:191]\n" \
"s_cbranch_vccnz LPUV_%=\nLPUVR_%=:\n" \
"v_mfma_f32_16x16x32_bf16 v[188:191], a[60:63], a[92:95], v[188:191]\n" \
"v_mfma_f32_16x16x32_bf16 v[192:195], a[0:3], a[96:99], v[192:195]\n" \
"s_add_u32 m0, s56, 10304\n" \
"v_mfma_f32_16x16x32_bf16 v[192:195], a[4:7], a[100:103], v[192:195]\n" \
"buffer_load_dwordx4 v26, s[72:75], 0 offen offset:0 sc1 lds\n" \
"v_mfma_f32_16x16x32_bf16 v[196:199], a[8:11], a[96:99], v[196:199]\n" \
"s_mov_b64 s[32:33], s[44:45]\n" \
"v_mfma_f32_16x16x32_bf16 v[196:199], a[12:15], a[100:103], v[196:199]\n" \
"v_mfma_f32_16x16x32_bf16 v[208:211], a[16:19], a[96:99], v[208:211]\n" \
"s_add_u32 m0, s56, 11328\n" \
"ds_read_b64_tr_b16 a[64:65], v9 offset:32768\n" \
"s_waitcnt vmcnt(9)\n" \
"v_mfma_f32_16x16x32_bf16 v[208:211], a[20:23], a[100:103], v[208:211]\n" \
"buffer_load_dwordx4 v27, s[72:75], 0 offen offset:0 sc1 lds\n" \
"v_mfma_f32_16x16x32_bf16 v[212:215], a[24:27], a[96:99], v[212:215]\n" \
"s_mov_b32 s34, s49\n" \
"ds_read_b64_tr_b16 a[66:67], v10 offset:32768\n" \
"v_alignbyte_b32 v30, v31, v30, 2\n" \
"v_mfma_f32_16x16x32_bf16 v[212:215], a[28:31], a[100:103], v[212:215]\n" \
"v_alignbyte_b32 v31, v32, v31, 2\n" \
"v_alignbyte_b32 v32, v33, v32, 2\n" \
"v_mfma_f32_16x16x32_bf16 v[224:227], a[32:35], a[96:99], v[224:227]\n" \
"s_add_u32 m0, s56, 11328\n" \
"ds_read_b64_tr_b16 a[68:69], v9 offset:33792\n" \
"v_alignbyte_b32 v33, v50, v33, 2\n" \
"v_mfma_f32_16x16x32_bf16 v[224:227], a[36:39], a[100:103], v[224:227]\n" \
"buffer_load_dwordx4 v27, s[72:75], 0 offen offset:1024 sc1 lds\n" \
"v_mfma_f32_16x16x32_bf16 v[228:231], a[40:43], a[96:99], v[228:231]\n" \
"ds_read_b64_tr_b16 a[70:71], v10 offset:33792\n" \
"v_mfma_f32_16x16x32_bf16 v[228:231], a[44:47], a[100:103], v[228:231]\n" \
"ds_write_b128 v55, v[30:33] offset:5184\n" \
"v_alignbyte_b32 v34, v35, v34, 2\n" \
"v_mfma_f32_16x16x32_bf16 v[240:243], a[48:51], a[96:99], v[240:243]\n" \
"s_add_u32 m0, s56, 11328\n" \
"ds_read_b64_tr_b16 a[72:73], v11 offset:32768\n" \
"v_alignbyte_b32 v35, v36, v35, 2\n" \
"v_mfma_f32_16x16x32_bf16 v[240:243], a[52:55], a[100:103], v[240:243]\n" \
"buffer_load_dwordx4 v27, s[72:75], 0 offen offset:2048 sc1 lds\n" \
"v_mfma_f32_16x16x32_bf16 v[244:247], a[56:59], a[96:99], v[244:247]\n" \
"ds_read_b64_tr_b16 a[74:75], v12 offset:32768\n" \
"v_alignbyte_b32 v36, v37, v36, 2\n" \
"v_mfma_f32_16x16x32_bf16 v[244:247], a[60:63], a[100:103], v[244:247]\n" \
"v_alignbyte_b32 v37, v51, v37, 2\n" \
"ds_write_b128 v55, v[34:37] offset:6208\n" \
"v_mfma_f32_16x16x32_bf16 v[200:203], a[0:3], a[104:107], v[200:203]\n" \
"s_add_u32 m0, s56, 11328\n" \
"ds_read_b64_tr_b16 a[76:77], v11 offset:33792\n" \
"v_alignbyte_b32 v38, v39, v38, 2\n" \
"v_mfma_f32_16x16x32_bf16 v[200:203], a[4:7], a[108:111], v[200:203]\n" \
"buffer_load_dwordx4 v27, s[72:75], 0 offen offset:3072 sc1 lds\n" \
"v_mfma_f32_16x16x32_bf16 v[204:207], a[8:11], a[104:107], v[204:207]\n" \
"ds_read_b64_tr_b16 a[78:79], v12 offset:33792\n" \
"v_alignbyte_b32 v39, v40, v39, 2\n" \
"v_mfma_f32_16x16x32_bf16 v[204:207], a[12:15], a[108:111], v[204:207]\n" \
"v_alignbyte_b32 v40, v41, v40, 2\n" \
"v_alignbyte_b32 v41, v52, v41, 2\n" \
"v_mfma_f32_16x16x32_bf16 v[216:219], a[16:19], a[104:107], v[216:219]\n" \
"s_add_u32 m0, s56, 0\n" \
"ds_read_b64_tr_b16 a[80:81], v9 offset:33280\n" \
"v_mfma_f32_16x16x32_bf16 v[216:219], a[20:23], a[108:111], v[216:219]\n" \
"buffer_load_dwordx4 v23, s[68:71], 0 offen offset:0 lds\n" \
"v_mfma_f32_16x16x32_bf16 v[220:223], a[24:27], a[104:107], v[220:223]\n" \
"ds_read_b64_tr_b16 a[82:83], v10 offset:33280\n" \
"v_mfma_f32_16x16x32_bf16 v[220:223], a[28:31], a[108:111], v[220:223]\n" \
"ds_write_b128 v55, v[38:41] offset:7232\n" \
"v_alignbyte_b32 v42, v43, v42, 2\n" \
"v_mfma_f32_16x16x32_bf16 v[232:235], a[32:35], a[104:107], v[232:235]\n" \
"s_add_u32 m0, s56, 1024\n" \
"ds_read_b64_tr_b16 a[84:85], v9 offset:34304\n" \
"v_alignbyte_b32 v43, v44, v43, 2\n" \
"v_mfma_f32_16x16x32_bf16 v[232:235], a[36:39], a[108:111], v[232:235]\n" \
"buffer_load_dwordx4 v24, s[68:71], 0 offen offset:0 lds\n" \
"v_mfma_f32_16x16x32_bf16 v[236:239], a[40:43], a[104:107], v[236:239]\n" \
"ds_read_b64_tr_b16 a[86:87], v10 offset:34304\n" \
"v_alignbyte_b32 v44, v45, v44, 2\n" \
"v_mfma_f32_16x16x32_bf16 v[236:239], a[44:47], a[108:111], v[236:239]\n" \
"v_alignbyte_b32 v45, v53, v45, 2\n" \
"ds_write_b128 v55, v[42:45] offset:8256\n" \
"v_mfma_f32_16x16x32_bf16 v[248:251], a[48:51], a[104:107], v[248:251]\n" \
"s_add_u32 m0, s56, 1024\n" \
"ds_read_b64_tr_b16 a[88:89], v11 offset:33280\n" \
"v_alignbyte_b32 v46, v47, v46, 2\n" \
"v_mfma_f32_16x16x32_bf16 v[248:251], a[52:55], a[108:111], v[248:251]\n" \
"buffer_load_dwordx4 v24, s[68:71], 0 offen offset:1024 lds\n" \
"v_mfma_f32_16x16x32_bf16 v[252:255], a[56:59], a[104:107], v[252:255]\n" \
"ds_read_b64_tr_b16 a[90:91], v12 offset:33280\n" \
"v_alignbyte_b32 v47, v48, v47, 2\n" \
"v_mfma_f32_16x16x32_bf16 v[252:255], a[60:63], a[108:111], v[252:255]\n" \
"v_alignbyte_b32 v48, v49, v48, 2\n" \
"v_alignbyte_b32 v49, v54, v49, 2\n" \
"v_mfma_f32_16x16x32_bf16 a[128:131], a[0:3], a[112:115], a[128:131]\n" \
"s_add_u32 m0, s56, 1024\n" \
"ds_read_b64_tr_b16 a[92:93], v11 offset:34304\n" \
"v_mfma_f32_16x16x32_bf16 a[128:131], a[4:7], a[116:119], a[128:131]\n" \
"buffer_load_dwordx4 v24, s[68:71], 0 offen offset:2048 lds\n" \
"v_mfma_f32_16x16x32_bf16 a[132:135], a[8:11], a[112:115], a[132:135]\n" \
"ds_read_b64_tr_b16 a[94:95], v12 offset:34304\n" \
"v_mfma_f32_16x16x32_bf16 a[132:135], a[12:15], a[116:119], a[132:135]\n" \
"ds_write_b128 v55, v[46:49] offset:9280\n" \
"v_lshrrev_b32 v30, 4, v4\n" \
"v_mfma_f32_16x16x32_bf16 a[144:147], a[16:19], a[112:115], a[144:147]\n" \
"s_add_u32 m0, s56, 1024\n" \
"v_and_b32 v31, 15, v30\n" \
"v_lshrrev_b32 v32, 4, v30\n" \
"v_mfma_f32_16x16x32_bf16 a[144:147], a[20:23], a[116:119], a[144:147]\n" \
"buffer_load_dwordx4 v24, s[68:71], 0 offen offset:3072 lds\n" \
"v_mfma_f32_16x16x32_bf16 a[148:151], a[24:27], a[112:115], a[148:151]\n" \
"v_add_u32 v33, 1, v31\n" \
"v_and_b32 v34, 1, v33\n" \
"v_mfma_f32_16x16x32_bf16 a[148:151], a[28:31], a[116:119], a[148:151]\n" \
"v_and_b32 v33, 30, v33\n" \
"v_mul_u32_u24 v34, 5184, v34\n" \
"v_mfma_f32_16x16x32_bf16 a[160:163], a[32:35], a[112:115], a[160:163]\n" \
"s_mov_b32 s46, 0\n" \
"s_add_u32 m0, s50, 65536\n" \
"v_lshl_add_u32 v33, v33, 1, v34\n" \
"v_mfma_f32_16x16x32_bf16 a[160:163], a[36:39], a[116:119], a[160:163]\n" \
"buffer_load_dwordx4 v13, s[28:31], s46 offen lds\n" \
"v_mfma_f32_16x16x32_bf16 a[164:167], a[40:43], a[112:115], a[164:167]\n" \
"v_lshl_add_u32 v33, v32, 4, v33\n" \
"v_add_u32 v46, s56, v33\n" \
"v_mfma_f32_16x16x32_bf16 a[164:167], a[44:47], a[116:119], a[164:167]\n" \
"s_waitcnt vmcnt(13) lgkmcnt(0)\n" \
"v_add_u32 v47, 1024, v46\n" \
"v_add_u32 v48, 2048, v46\n" \
"v_mfma_f32_16x16x32_bf16 a[176:179], a[48:51], a[112:115], a[176:179]\n" \
"s_add_u32 m0, s50, 69632\n" \
"s_barrier\n" \
"v_add_u32 v49, 3072, v46\n" \
"v_mfma_f32_16x16x32_bf16 a[176:179], a[52:55], a[116:119], a[176:179]\n" \
"buffer_load_dwordx4 v14, s[28:31], s46 offen lds\n" \
"v_mfma_f32_16x16x32_bf16 a[180:183], a[56:59], a[112:115], a[180:183]\n" \
"v_lshlrev_b32 v33, 1, v31\n" \
"v_sub_u32 v33, v33, v32\n" \
"v_mfma_f32_16x16x32_bf16 a[180:183], a[60:63], a[116:119], a[180:183]\n" \
"v_lshlrev_b32 v33, 4, v33\n" \
"v_add_u32 v33, 10320, v33\n" \
"v_mfma_f32_16x16x32_bf16 a[136:139], a[0:3], a[120:123], a[136:139]\n" \
"s_add_u32 m0, s50, 73728\n" \
"v_add_u32 v50, s56, v33\n" \
"v_mfma_f32_16x16x32_bf16 a[136:139], a[4:7], a[124:127], a[136:139]\n" \
"buffer_load_dwordx4 v15, s[28:31], s46 offen lds\n" \
"v_mfma_f32_16x16x32_bf16 a[140:143], a[8:11], a[120:123], a[140:143]\n" \
"ds_read_b128 a[0:3], v5 offset:0\n" \
"v_lshlrev_b32 v33, 5, v31\n" \
"v_mfma_f32_16x16x32_bf16 a[140:143], a[12:15], a[124:127], a[140:143]\n" \
"ds_read_b128 a[4:7], v6 offset:0\n" \
"v_lshrrev_b32 v55, 2, v30\n" \
"v_mfma_f32_16x16x32_bf16 a[152:155], a[16:19], a[120:123], a[152:155]\n" \
"ds_read_b128 a[8:11], v5 offset:2048\n" \
"v_and_b32 v55, 8, v55\n" \
"v_mfma_f32_16x16x32_bf16 a[152:155], a[20:23], a[124:127], a[152:155]\n" \
"ds_read_b128 a[12:15], v6 offset:2048\n" \
"v_xor_b32 v55, v30, v55\n" \
"v_mfma_f32_16x16x32_bf16 a[156:159], a[24:27], a[120:123], a[156:159]\n" \
"ds_read_b128 a[16:19], v5 offset:4096\n" \
"v_lshl_add_u32 v55, v55, 3, s22\n" \
"v_mfma_f32_16x16x32_bf16 a[156:159], a[28:31], a[124:127], a[156:159]\n" \
"ds_read_b128 a[20:23], v6 offset:4096\n" \
"v_add_u32 v33, s56, v33\n" \
"v_mfma_f32_16x16x32_bf16 a[168:171], a[32:35], a[120:123], a[168:171]\n" \
"ds_read_b128 a[24:27], v5 offset:6144\n" \
"v_xor_b32 v32, 1, v32\n" \
"v_mfma_f32_16x16x32_bf16 a[168:171], a[36:39], a[124:127], a[168:171]\n" \
"ds_read_b128 a[28:31], v6 offset:6144\n" \
"v_lshl_add_u32 v51, v32, 3, v33\n" \
"v_mfma_f32_16x16x32_bf16 a[172:175], a[40:43], a[120:123], a[172:175]\n" \
"ds_read_b128 a[32:35], v5 offset:8192\n" \
"v_mfma_f32_16x16x32_bf16 a[172:175], a[44:47], a[124:127], a[172:175]\n" \
"ds_read_b128 a[36:39], v6 offset:8192\n" \
"v_mfma_f32_16x16x32_bf16 a[184:187], a[48:51], a[120:123], a[184:187]\n" \
"ds_read_b128 a[40:43], v5 offset:10240\n" \
"v_mfma_f32_16x16x32_bf16 a[184:187], a[52:55], a[124:127], a[184:187]\n" \
"ds_read_b128 a[44:47], v6 offset:10240\n" \
"v_mfma_f32_16x16x32_bf16 a[188:191], a[56:59], a[120:123], a[188:191]\n" \
"ds_read_b128 a[48:51], v5 offset:12288\n" \
"v_mfma_f32_16x16x32_bf16 a[188:191], a[60:63], a[124:127], a[188:191]\n" \
"s_waitcnt lgkmcnt(9)\n" \
"ds_read_b128 a[52:55], v6 offset:12288\n" \
"v_mfma_f32_16x16x32_bf16 v[64:67], a[0:3], a[64:67], v[64:67]\n" \
"s_add_u32 m0, s50, 77824\n" \
"ds_read_b128 a[56:59], v5 offset:14336\n" \
"v_mfma_f32_16x16x32_bf16 v[64:67], a[4:7], a[68:71], v[64:67]\n" \
"buffer_load_dwordx4 v16, s[28:31], s46 offen lds\n" \
"v_mfma_f32_16x16x32_bf16 v[68:71], a[8:11], a[64:67], v[68:71]\n" \
"LH7_%=:\ns_cbranch_execz LH11_%=\n" \
"s_waitcnt lgkmcnt(7)\n" \
"ds_read_b128 a[60:63], v6 offset:14336\n" \
"v_mfma_f32_16x16x32_bf16 v[68:71], a[12:15], a[68:71], v[68:71]\n" \
"v_mfma_f32_16x16x32_bf16 v[80:83], a[16:19], a[64:67], v[80:83]\n" \
"s_mov_b32 s46, 0\n" \
"s_add_u32 m0, s50, 81920\n" \
"ds_read_b64_tr_b16 a[96:97], v9 offset:49152\n" \
"v_mfma_f32_16x16x32_bf16 v[80:83], a[20:23], a[68:71], v[80:83]\n" \
"buffer_load_dwordx4 v17, s[28:31], s46 offen lds\n" \
"v_mfma_f32_16x16x32_bf16 v[84:87], a[24:27], a[64:67], v[84:87]\n" \
"s_waitcnt lgkmcnt(5)\n" \
"ds_read_b64_tr_b16 a[98:99], v10 offset:49152\n" \
"v_mfma_f32_16x16x32_bf16 v[84:87], a[28:31], a[68:71], v[84:87]\n" \
"v_mfma_f32_16x16x32_bf16 v[96:99], a[32:35], a[64:67], v[96:99]\n" \
"s_add_u32 m0, s50, 86016\n" \
"ds_read_b64_tr_b16 a[100:101], v9 offset:50176\n" \
"v_mfma_f32_16x16x32_bf16 v[96:99], a[36:39], a[68:71], v[96:99]\n" \
"buffer_load_dwordx4 v18, s[28:31], s46 offen lds\n" \
"v_mfma_f32_16x16x32_bf16 v[100:103], a[40:43], a[64:67], v[100:103]\n" \
"s_waitcnt lgkmcnt(3)\n" \
"ds_read_b64_tr_b16 a[102:103], v10 offset:50176\n" \
"v_mfma_f32_16x16x32_bf16 v[100:103], a[44:47], a[68:71], v[100:103]\n" \
"v_mfma_f32_16x16x32_bf16 v[112:115], a[48:51], a[64:67], v[112:115]\n" \
"s_add_u32 m0, s50, 90112\n" \
"ds_read_b64_tr_b16 a[104:105], v11 offset:49152\n" \
"v_mfma_f32_16x16x32_bf16 v[112:115], a[52:55], a[68:71], v[112:115]\n" \
"buffer_load_dwordx4 v19, s[28:31], s46 offen lds\n" \
"v_mfma_f32_16x16x32_bf16 v[116:119], a[56:59], a[64:67], v[116:119]\n" \
"ds_read_b64_tr_b16 a[106:107], v12 offset:49152\n" \
"v_mfma_f32_16x16x32_bf16 v[116:119], a[60:63], a[68:71], v[116:119]\n" \
"v_mfma_f32_16x16x32_bf16 v[72:75], a[0:3], a[72:75], v[72:75]\n" \
"s_add_u32 m0, s50, 94208\n" \
"ds_read_b64_tr_b16 a[108:109], v11 offset:50176\n" \
"v_mfma_f32_16x16x32_bf16 v[72:75], a[4:7], a[76:79], v[72:75]\n" \
"buffer_load_dwordx4 v20, s[28:31], s46 offen lds\n" \
"v_mfma_f32_16x16x32_bf16 v[76:79], a[8:11], a[72:75], v[76:79]\n" \
"ds_read_b64_tr_b16 a[110:111], v12 offset:50176\n" \
"v_mfma_f32_16x16x32_bf16 v[76:79], a[12:15], a[76:79], v[76:79]\n" \
"v_mfma_f32_16x16x32_bf16 v[88:91], a[16:19], a[72:75], v[88:91]\n" \
"s_mov_b32 s47, 0\n" \
"s_add_u32 m0, s51, 32768\n" \
"ds_read_b64_tr_b16 a[112:113], v9 offset:49664\n" \
"v_mfma_f32_16x16x32_bf16 v[88:91], a[20:23], a[76:79], v[88:91]\n" \
"buffer_load_dwordx4 v21, s[32:35], s47 offen offset:0 lds\n" \
"v_mfma_f32_16x16x32_bf16 v[92:95], a[24:27], a[72:75], v[92:95]\n" \
"ds_read_b64_tr_b16 a[114:115], v10 offset:49664\n" \
"v_mfma_f32_16x16x32_bf16 v[92:95], a[28:31], a[76:79], v[92:95]\n" \
"v_mfma_f32_16x16x32_bf16 v[104:107], a[32:35], a[72:75], v[104:107]\n" \
"s_add_u32 m0, s51, 33664\n" \
"ds_read_b64_tr_b16 a[116:117], v9 offset:50688\n" \
"v_mfma_f32_16x16x32_bf16 v[104:107], a[36:39], a[76:79], v[104:107]\n" \
"buffer_load_dwordx4 v21, s[32:35], s47 offen offset:128 lds\n" \
"v_mfma_f32_16x16x32_bf16 v[108:111], a[40:43], a[72:75], v[108:111]\n" \
"ds_read_b64_tr_b16 a[118:119], v10 offset:50688\n" \
"v_mfma_f32_16x16x32_bf16 v[108:111], a[44:47], a[76:79], v[108:111]\n" \
"v_mfma_f32_16x16x32_bf16 v[120:123], a[48:51], a[72:75], v[120:123]\n" \
"s_mov_b32 s47, 262144\n" \
"s_add_u32 m0, s51, 40960\n" \
"ds_read_b64_tr_b16 a[120:121], v11 offset:49664\n" \
"v_mfma_f32_16x16x32_bf16 v[120:123], a[52:55], a[76:79], v[120:123]\n" \
"buffer_load_dwordx4 v21, s[32:35], s47 offen offset:0 lds\n" \
"v_mfma_f32_16x16x32_bf16 v[124:127], a[56:59], a[72:75], v[124:127]\n" \
"ds_read_b64_tr_b16 a[122:123], v12 offset:49664\n" \
"v_mfma_f32_16x16x32_bf16 v[124:127], a[60:63], a[76:79], v[124:127]\n" \
"v_mfma_f32_16x16x32_bf16 v[128:131], a[0:3], a[80:83], v[128:131]\n" \
"s_add_u32 m0, s51, 41856\n" \
"ds_read_b64_tr_b16 a[124:125], v11 offset:50688\n" \
"v_mfma_f32_16x16x32_bf16 v[128:131], a[4:7], a[84:87], v[128:131]\n" \
"buffer_load_dwordx4 v21, s[32:35], s47 offen offset:128 lds\n" \
"v_mfma_f32_16x16x32_bf16 v[132:135], a[8:11], a[80:83], v[132:135]\n" \
"s_mov_b32 s96, s57\n" \
"ds_read_b64_tr_b16 a[126:127], v12 offset:50688\n" \
"v_mul_f32 v64, s20, v64\n" \
"v_mfma_f32_16x16x32_bf16 v[132:135], a[12:15], a[84:87], v[132:135]\n" \
"v_mul_f32 v65, s20, v65\n" \
"v_mul_f32 v66, s20, v66\n" \
"v_mfma_f32_16x16x32_bf16 v[144:147], a[16:19], a[80:83], v[144:147]\n" \
"v_mul_f32 v67, s20, v67\n" \
"v_cvt_pk_bf16_f32 v30, v64, v65\n" \
"v_mfma_f32_16x16x32_bf16 v[144:147], a[20:23], a[84:87], v[144:147]\n" \
"v_cvt_pk_bf16_f32 v31, v66, v67\n" \
"v_mfma_f32_16x16x32_bf16 v[148:151], a[24:27], a[80:83], v[148:151]\n" \
"v_mul_f32 v68, s20, v68\n" \
"v_mul_f32 v69, s20, v69\n" \
"v_mfma_f32_16x16x32_bf16 v[148:151], a[28:31], a[84:87], v[148:151]\n" \
"v_mul_f32 v70, s20, v70\n" \
"v_mul_f32 v71, s20, v71\n" \
"v_mfma_f32_16x16x32_bf16 v[160:163], a[32:35], a[80:83], v[160:163]\n" \
"v_cvt_pk_bf16_f32 v32, v68, v69\n" \
"v_mfma_f32_16x16x32_bf16 v[160:163], a[36:39], a[84:87], v[160:163]\n" \
"v_cvt_pk_bf16_f32 v33, v70, v71\n" \
"v_mul_f32 v72, s20, v72\n" \
"v_mfma_f32_16x16x32_bf16 v[164:167], a[40:43], a[80:83], v[164:167]\n" \
"buffer_store_dwordx4 v[30:33], v4, s[36:39], s96 offen offset:0 sc1\n" \
"v_mfma_f32_16x16x32_bf16 v[164:167], a[44:47], a[84:87], v[164:167]\n" \
"s_waitcnt lgkmcnt(0)\n" \
"s_add_u32 s97, s96, s98\n" \
"v_mul_f32 v73, s20, v73\n" \
"v_mfma_f32_16x16x32_bf16 v[176:179], a[48:51], a[80:83], v[176:179]\n" \
"s_barrier\n" \
"v_mul_f32 v74, s20, v74\n" \
"v_mul_f32 v75, s20, v75\n" \
"v_mfma_f32_16x16x32_bf16 v[176:179], a[52:55], a[84:87], v[176:179]\n" \
"v_cvt_pk_bf16_f32 v34, v72, v73\n" \
"v_cvt_pk_bf16_f32 v35, v74, v75\n" \
"v_mfma_f32_16x16x32_bf16 v[180:183], a[56:59], a[80:83], v[180:183]\n" \
"v_mul_f32 v76, s20, v76\n" \
"v_mfma_f32_16x16x32_bf16 v[180:183], a[60:63], a[84:87], v[180:183]\n" \
"v_add_u32 v5, s54, v5\n" \
"v_mul_f32 v77, s20, v77\n" \
"v_mfma_f32_16x16x32_bf16 v[136:139], a[0:3], a[88:91], v[136:139]\n" \
"v_mul_f32 v78, s20, v78\n" \
"v_mul_f32 v79, s20, v79\n" \
"v_mfma_f32_16x16x32_bf16 v[136:139], a[4:7], a[92:95], v[136:139]\n" \
"v_cvt_pk_bf16_f32 v36, v76, v77\n" \
"v_mfma_f32_16x16x32_bf16 v[140:143], a[8:11], a[88:91], v[140:143]\n" \
"v_add_u32 v6, s54, v6\n" \
"v_cvt_pk_bf16_f32 v37, v78, v79\n" \
"v_mfma_f32_16x16x32_bf16 v[140:143], a[12:15], a[92:95], v[140:143]\n" \
"v_add_u32 v7, s54, v7\n" \
"v_mul_f32 v80, s20, v80\n" \
"v_mfma_f32_16x16x32_bf16 v[152:155], a[16:19], a[88:91], v[152:155]\n" \
"v_mul_f32 v81, s20, v81\n" \
"v_mfma_f32_16x16x32_bf16 v[152:155], a[20:23], a[92:95], v[152:155]\n" \
"v_mul_f32 v82, s20, v82\n" \
"v_mul_f32 v83, s20, v83\n" \
"v_mfma_f32_16x16x32_bf16 v[156:159], a[24:27], a[88:91], v[156:159]\n" \
"v_add_u32 v8, s54, v8\n" \
"v_cvt_pk_bf16_f32 v38, v80, v81\n" \
"v_mfma_f32_16x16x32_bf16 v[156:159], a[28:31], a[92:95], v[156:159]\n" \
"v_sub_u32 v9, v9, v62\n" \
"v_mfma_f32_16x16x32_bf16 v[168:171], a[32:35], a[88:91], v[168:171]\n" \
"buffer_store_dwordx4 v[34:37], v4, s[36:39], s97 offen offset:0 sc1\n" \
"v_mfma_f32_16x16x32_bf16 v[168:171], a[36:39], a[92:95], v[168:171]\n" \
"s_add_u32 s96, s96, s92\n" \
"v_cvt_pk_bf16_f32 v39, v82, v83\n" \
"v_mul_f32 v84, s20, v84\n" \
"v_mfma_f32_16x16x32_bf16 v[172:175], a[40:43], a[88:91], v[172:175]\n" \
"v_sub_u32 v10, v10, v62\n" \
"v_mul_f32 v85, s20, v85\n" \
"v_mfma_f32_16x16x32_bf16 v[172:175], a[44:47], a[92:95], v[172:175]\n" \
"v_sub_u32 v11, v11, v62\n" \
"v_mfma_f32_16x16x32_bf16 v[184:187], a[48:51], a[88:91], v[184:187]\n" \
"v_mul_f32 v86, s20, v86\n" \
"v_mul_f32 v87, s20, v87\n" \
"v_mfma_f32_16x16x32_bf16 v[184:187], a[52:55], a[92:95], v[184:187]\n" \
"v_cvt_pk_bf16_f32 v40, v84, v85\n" \
"v_cvt_pk_bf16_f32 v41, v86, v87\n" \
"v_mfma_f32_16x16x32_bf16 v[188:191], a[56:59], a[88:91], v[188:191]\n" \
"v_sub_u32 v12, v12, v62\n" \
"v_mfma_f32_16x16x32_bf16 v[188:191], a[60:63], a[92:95], v[188:191]\n" \
"v_mul_f32 v88, s20, v88\n" \
"v_mul_f32 v89, s20, v89\n" \
"v_mfma_f32_16x16x32_bf16 v[192:195], a[0:3], a[96:99], v[192:195]\n" \
"s_mov_b32 s46, 128\n" \
"s_add_u32 m0, s50, 0\n" \
"s_waitcnt vmcnt(14) lgkmcnt(0)\n" \
"v_mfma_f32_16x16x32_bf16 v[192:195], a[4:7], a[100:103], v[192:195]\n" \
"buffer_load_dwordx4 v13, s[28:31], s46 offen lds\n" \
"v_mfma_f32_16x16x32_bf16 v[196:199], a[8:11], a[96:99], v[196:199]\n" \
"ds_read_b128 a[200:203], v50 offset:512\n" \
"v_mul_f32 v90, s20, v90\n" \
"v_mfma_f32_16x16x32_bf16 v[196:199], a[12:15], a[100:103], v[196:199]\n" \
"v_mul_f32 v91, s20, v91\n" \
"v_cvt_pk_bf16_f32 v42, v88, v89\n" \
"v_mfma_f32_16x16x32_bf16 v[208:211], a[16:19], a[96:99], v[208:211]\n" \
"ds_read2_b32 a[64:65], v46 offset0:0 offset1:1\n" \
"v_cvt_pk_bf16_f32 v43, v90, v91\n" \
"v_mfma_f32_16x16x32_bf16 v[208:211], a[20:23], a[100:103], v[208:211]\n" \
"v_mul_f32 v92, s20, v92\n" \
"v_mul_f32 v93, s20, v93\n" \
"v_mfma_f32_16x16x32_bf16 v[212:215], a[24:27], a[96:99], v[212:215]\n" \
"ds_read2_b32 a[66:67], v46 offset0:2 offset1:3\n" \
"v_mul_f32 v94, s20, v94\n" \
"v_mfma_f32_16x16x32_bf16 v[212:215], a[28:31], a[100:103], v[212:215]\n" \
"buffer_store_dwordx4 v[38:41], v4, s[36:39], s96 offen offset:0 sc1\n" \
"v_mfma_f32_16x16x32_bf16 v[224:227], a[32:35], a[96:99], v[224:227]\n" \
"s_add_u32 m0, s50, 4096\n" \
"ds_read_b128 a[204:207], v50 offset:1024\n" \
"s_add_u32 s97, s96, s98\n" \
"v_mfma_f32_16x16x32_bf16 v[224:227], a[36:39], a[100:103], v[224:227]\n" \
"buffer_load_dwordx4 v14, s[28:31], s46 offen lds\n" \
"v_mfma_f32_16x16x32_bf16 v[228:231], a[40:43], a[96:99], v[228:231]\n" \
"ds_read2_b32 a[68:69], v46 offset0:128 offset1:129\n" \
"v_mul_f32 v95, s20, v95\n" \
"v_mfma_f32_16x16x32_bf16 v[228:231], a[44:47], a[100:103], v[228:231]\n" \
"v_cvt_pk_bf16_f32 v44, v92, v93\n" \
"v_cvt_pk_bf16_f32 v45, v94, v95\n" \
"v_mfma_f32_16x16x32_bf16 v[240:243], a[48:51], a[96:99], v[240:243]\n" \
"ds_read2_b32 a[70:71], v46 offset0:130 offset1:131\n" \
"v_mul_f32 v96, s20, v96\n" \
"v_mfma_f32_16x16x32_bf16 v[240:243], a[52:55], a[100:103], v[240:243]\n" \
"v_mul_f32 v97, s20, v97\n" \
"v_mul_f32 v98, s20, v98\n" \
"v_mfma_f32_16x16x32_bf16 v[244:247], a[56:59], a[96:99], v[244:247]\n" \
"ds_read_b128 a[208:211], v50 offset:1536\n" \
"v_mul_f32 v99, s20, v99\n" \
"v_mfma_f32_16x16x32_bf16 v[244:247], a[60:63], a[100:103], v[244:247]\n" \
"v_cvt_pk_bf16_f32 v30, v96, v97\n" \
"v_cvt_pk_bf16_f32 v31, v98, v99\n" \
"v_mfma_f32_16x16x32_bf16 v[200:203], a[0:3], a[104:107], v[200:203]\n" \
"s_add_u32 m0, s50, 8192\n" \
"ds_read2_b32 a[72:73], v47 offset0:0 offset1:1\n" \
"v_mul_f32 v100, s20, v100\n" \
"v_mfma_f32_16x16x32_bf16 v[200:203], a[4:7], a[108:111], v[200:203]\n" \
"buffer_load_dwordx4 v15, s[28:31], s46 offen lds\n" \
"v_mfma_f32_16x16x32_bf16 v[204:207], a[8:11], a[104:107], v[204:207]\n" \
"ds_read2_b32 a[74:75], v47 offset0:2 offset1:3\n" \
"v_mul_f32 v101, s20, v101\n" \
"v_mfma_f32_16x16x32_bf16 v[204:207], a[12:15], a[108:111], v[204:207]\n" \
"v_mul_f32 v102, s20, v102\n" \
"v_mul_f32 v103, s20, v103\n" \
"v_mfma_f32_16x16x32_bf16 v[216:219], a[16:19], a[104:107], v[216:219]\n" \
"ds_read_b128 a[212:215], v50 offset:2048\n" \
"v_cvt_pk_bf16_f32 v32, v100, v101\n" \
"v_mfma_f32_16x16x32_bf16 v[216:219], a[20:23], a[108:111], v[216:219]\n" \
"buffer_store_dwordx4 v[42:45], v4, s[36:39], s97 offen offset:0 sc1\n" \
"v_mfma_f32_16x16x32_bf16 v[220:223], a[24:27], a[104:107], v[220:223]\n" \
"s_add_u32 s96, s96, s92\n" \
"ds_read2_b32 a[76:77], v47 offset0:128 offset1:129\n" \
"v_cvt_pk_bf16_f32 v33, v102, v103\n" \
"v_mfma_f32_16x16x32_bf16 v[220:223], a[28:31], a[108:111], v[220:223]\n" \
"v_mul_f32 v104, s20, v104\n" \
"v_mul_f32 v105, s20, v105\n" \
"v_mfma_f32_16x16x32_bf16 v[232:235], a[32:35], a[104:107], v[232:235]\n" \
"s_add_u32 m0, s50, 12288\n" \
"ds_read2_b32 a[78:79], v47 offset0:130 offset1:131\n" \
"v_mul_f32 v106, s20, v106\n" \
"v_mfma_f32_16x16x32_bf16 v[232:235], a[36:39], a[108:111], v[232:235]\n" \
"buffer_load_dwordx4 v16, s[28:31], s46 offen lds\n" \
"v_mfma_f32_16x16x32_bf16 v[236:239], a[40:43], a[104:107], v[236:239]\n" \
"ds_read_b128 a[216:219], v50 offset:2560\n" \
"v_mul_f32 v107, s20, v107\n" \
"v_mfma_f32_16x16x32_bf16 v[236:239], a[44:47], a[108:111], v[236:239]\n" \
"v_cvt_pk_bf16_f32 v34, v104, v105\n" \
"v_cvt_pk_bf16_f32 v35, v106, v107\n" \
"v_mfma_f32_16x16x32_bf16 v[248:251], a[48:51], a[104:107], v[248:251]\n" \
"ds_read2_b32 a[80:81], v48 offset0:0 offset1:1\n" \
"v_mul_f32 v108, s20, v108\n" \
"v_mfma_f32_16x16x32_bf16 v[248:251], a[52:55], a[108:111], v[248:251]\n" \
"v_mul_f32 v109, s20, v109\n" \
"v_mul_f32 v110, s20, v110\n" \
"v_mfma_f32_16x16x32_bf16 v[252:255], a[56:59], a[104:107], v[252:255]\n" \
"ds_read2_b32 a[82:83], v48 offset0:2 offset1:3\n" \
"v_mul_f32 v111, s20, v111\n" \
"v_mfma_f32_16x16x32_bf16 v[252:255], a[60:63], a[108:111], v[252:255]\n" \
"v_cvt_pk_bf16_f32 v36, v108, v109\n" \
"v_cvt_pk_bf16_f32 v37, v110, v111\n" \
"v_mfma_f32_16x16x32_bf16 a[128:131], a[0:3], a[112:115], a[128:131]\n" \
"s_mov_b32 s46, 128\n" \
"s_add_u32 m0, s50, 16384\n" \
"ds_read_b128 a[220:223], v50 offset:3072\n" \
"v_mfma_f32_16x16x32_bf16 a[128:131], a[4:7], a[116:119], a[128:131]\n" \
"buffer_load_dwordx4 v17, s[28:31], s46 offen lds\n" \
"v_mfma_f32_16x16x32_bf16 a[132:135], a[8:11], a[112:115], a[132:135]\n" \
"ds_read2_b32 a[84:85], v48 offset0:128 offset1:129\n" \
"v_mul_f32 v112, s20, v112\n" \
"v_mfma_f32_16x16x32_bf16 a[132:135], a[12:15], a[116:119], a[132:135]\n" \
"buffer_store_dwordx4 v[30:33], v4, s[36:39], s96 offen offset:0 sc1\n" \
"v_mfma_f32_16x16x32_bf16 a[144:147], a[16:19], a[112:115], a[144:147]\n" \
"s_add_u32 s97, s96, s98\n" \
"ds_read2_b32 a[86:87], v48 offset0:130 offset1:131\n" \
"v_mul_f32 v113, s20, v113\n" \
"v_mfma_f32_16x16x32_bf16 a[144:147], a[20:23], a[116:119], a[144:147]\n" \
"v_mul_f32 v114, s20, v114\n" \
"v_mul_f32 v115, s20, v115\n" \
"v_mfma_f32_16x16x32_bf16 a[148:151], a[24:27], a[112:115], a[148:151]\n" \
"ds_read_b128 a[224:227], v50 offset:3584\n" \
"v_cvt_pk_bf16_f32 v38, v112, v113\n" \
"v_mfma_f32_16x16x32_bf16 a[148:151], a[28:31], a[116:119], a[148:151]\n" \
"v_cvt_pk_bf16_f32 v39, v114, v115\n" \
"v_mul_f32 v116, s20, v116\n" \
"v_mfma_f32_16x16x32_bf16 a[160:163], a[32:35], a[112:115], a[160:163]\n" \
"s_add_u32 m0, s50, 20480\n" \
"ds_read2_b32 a[88:89], v49 offset0:0 offset1:1\n" \
"v_mul_f32 v117, s20, v117\n" \
"v_mfma_f32_16x16x32_bf16 a[160:163], a[36:39], a[116:119], a[160:163]\n" \
"buffer_load_dwordx4 v18, s[28:31], s46 offen lds\n" \
"v_mfma_f32_16x16x32_bf16 a[164:167], a[40:43], a[112:115], a[164:167]\n" \
"ds_read2_b32 a[90:91], v49 offset0:2 offset1:3\n" \
"v_mul_f32 v118, s20, v118\n" \
"v_mfma_f32_16x16x32_bf16 a[164:167], a[44:47], a[116:119], a[164:167]\n" \
"v_mul_f32 v119, s20, v119\n" \
"v_cvt_pk_bf16_f32 v40, v116, v117\n" \
"v_mfma_f32_16x16x32_bf16 a[176:179], a[48:51], a[112:115], a[176:179]\n" \
"ds_read_b128 a[228:231], v50 offset:4096\n" \
"v_cvt_pk_bf16_f32 v41, v118, v119\n" \
"v_mfma_f32_16x16x32_bf16 a[176:179], a[52:55], a[116:119], a[176:179]\n" \
"v_mul_f32 v120, s20, v120\n" \
"v_mul_f32 v121, s20, v121\n" \
"v_mfma_f32_16x16x32_bf16 a[180:183], a[56:59], a[112:115], a[180:183]\n" \
"ds_read2_b32 a[92:93], v49 offset0:128 offset1:129\n" \
"v_mul_f32 v122, s20, v122\n" \
"v_mfma_f32_16x16x32_bf16 a[180:183], a[60:63], a[116:119], a[180:183]\n" \
"v_mul_f32 v123, s20, v123\n" \
"v_cvt_pk_bf16_f32 v42, v120, v121\n" \
"v_mfma_f32_16x16x32_bf16 a[136:139], a[0:3], a[120:123], a[136:139]\n" \
"s_add_u32 m0, s50, 24576\n" \
"ds_read2_b32 a[94:95], v49 offset0:130 offset1:131\n" \
"v_cvt_pk_bf16_f32 v43, v122, v123\n" \
"v_mfma_f32_16x16x32_bf16 a[136:139], a[4:7], a[124:127], a[136:139]\n" \
"buffer_load_dwordx4 v19, s[28:31], s46 offen lds\n" \
"v_mfma_f32_16x16x32_bf16 a[140:143], a[8:11], a[120:123], a[140:143]\n" \
"v_mul_f32 v124, s20, v124\n" \
"v_mul_f32 v125, s20, v125\n" \
"v_mfma_f32_16x16x32_bf16 a[140:143], a[12:15], a[124:127], a[140:143]\n" \
"buffer_store_dwordx4 v[34:37], v4, s[36:39], s97 offen offset:0 sc1\n" \
"v_mfma_f32_16x16x32_bf16 a[152:155], a[16:19], a[120:123], a[152:155]\n" \
"s_add_u32 s96, s96, s92\n" \
"v_mul_f32 v126, s20, v126\n" \
"v_mul_f32 v127, s20, v127\n" \
"v_mfma_f32_16x16x32_bf16 a[152:155], a[20:23], a[124:127], a[152:155]\n" \
"v_cvt_pk_bf16_f32 v44, v124, v125\n" \
"v_cvt_pk_bf16_f32 v45, v126, v127\n" \
"v_mfma_f32_16x16x32_bf16 a[156:159], a[24:27], a[120:123], a[156:159]\n" \
"v_mul_f32 v128, s20, v128\n" \
"v_mfma_f32_16x16x32_bf16 a[156:159], a[28:31], a[124:127], a[156:159]\n" \
"v_mul_f32 v129, s20, v129\n" \
"v_mul_f32 v130, s20, v130\n" \
"v_mfma_f32_16x16x32_bf16 a[168:171], a[32:35], a[120:123], a[168:171]\n" \
"s_add_u32 m0, s50, 28672\n" \
"v_mul_f32 v131, s20, v131\n" \
"v_cvt_pk_bf16_f32 v30, v128, v129\n" \
"v_mfma_f32_16x16x32_bf16 a[168:171], a[36:39], a[124:127], a[168:171]\n" \
"buffer_load_dwordx4 v20, s[28:31], s46 offen lds\n" \
"v_mfma_f32_16x16x32_bf16 a[172:175], a[40:43], a[120:123], a[172:175]\n" \
"v_cvt_pk_bf16_f32 v31, v130, v131\n" \
"v_mul_f32 v132, s20, v132\n" \
"v_mfma_f32_16x16x32_bf16 a[172:175], a[44:47], a[124:127], a[172:175]\n" \
"v_mul_f32 v133, s20, v133\n" \
"v_mul_f32 v134, s20, v134\n" \
"v_mfma_f32_16x16x32_bf16 a[184:187], a[48:51], a[120:123], a[184:187]\n" \
"v_mul_f32 v135, s20, v135\n" \
"v_mfma_f32_16x16x32_bf16 a[184:187], a[52:55], a[124:127], a[184:187]\n" \
"v_cvt_pk_bf16_f32 v32, v132, v133\n" \
"v_cvt_pk_bf16_f32 v33, v134, v135\n" \
"v_mfma_f32_16x16x32_bf16 a[188:191], a[56:59], a[120:123], a[188:191]\n" \
"s_waitcnt lgkmcnt(0)\n" \
"v_mul_f32 v136, s20, v136\n" \
"v_mul_f32 v137, s20, v137\n" \
"v_mfma_f32_16x16x32_bf16 a[188:191], a[60:63], a[124:127], a[188:191]\n" \
"v_mul_f32 v138, s20, v138\n" \
"LC_%=:\n" \
"v_mfma_f32_16x16x32_bf16 v[64:67], a[64:67], a[200:203], 0\n" \
"ds_read_b128 a[32:35], v50 offset:448\n" \
"v_mul_f32 v139, s20, v139\n" \
"v_mfma_f32_16x16x32_bf16 v[68:71], a[64:67], a[204:207], 0\n" \
"buffer_store_dwordx4 v[38:41], v4, s[36:39], s96 offen offset:0 sc1\n" \
"v_mfma_f32_16x16x32_bf16 v[68:71], a[68:71], a[200:203], v[68:71]\n" \
"s_add_u32 s97, s96, s98\n" \
"ds_read2_b32 a[0:1], v46 offset0:16 offset1:17\n" \
"v_cvt_pk_bf16_f32 v34, v136, v137\n" \
"v_mfma_f32_16x16x32_bf16 v[72:75], a[64:67], a[208:211], 0\n" \
"ds_read2_b32 a[2:3], v46 offset0:18 offset1:19\n" \
"v_cvt_pk_bf16_f32 v35, v138, v139\n" \
"v_mfma_f32_16x16x32_bf16 v[72:75], a[68:71], a[204:207], v[72:75]\n" \
"s_mov_b32 s47, 524288\n" \
"s_add_u32 m0, s51, 49152\n" \
"ds_read_b128 a[36:39], v50 offset:960\n" \
"v_mfma_f32_16x16x32_bf16 v[72:75], a[72:75], a[200:203], v[72:75]\n" \
"buffer_load_dwordx4 v21, s[32:35], s47 offen offset:0 lds\n" \
"v_mfma_f32_16x16x32_bf16 v[76:79], a[64:67], a[212:215], 0\n" \
"ds_read2_b32 a[4:5], v46 offset0:144 offset1:145\n" \
"v_mul_f32 v140, s20, v140\n" \
"v_mfma_f32_16x16x32_bf16 v[76:79], a[68:71], a[208:211], v[76:79]\n" \
"ds_read2_b32 a[6:7], v46 offset0:146 offset1:147\n" \
"v_mul_f32 v141, s20, v141\n" \
"v_mfma_f32_16x16x32_bf16 v[76:79], a[72:75], a[204:207], v[76:79]\n" \
"ds_read_b128 a[40:43], v50 offset:1472\n" \
"v_mul_f32 v142, s20, v142\n" \
"v_mfma_f32_16x16x32_bf16 v[76:79], a[76:79], a[200:203], v[76:79]\n" \
"v_mul_f32 v143, s20, v143\n" \
"v_cvt_pk_bf16_f32 v36, v140, v141\n" \
"v_mfma_f32_16x16x32_bf16 v[80:83], a[64:67], a[216:219], 0\n" \
"ds_read2_b32 a[8:9], v47 offset0:16 offset1:17\n" \
"v_cvt_pk_bf16_f32 v37, v142, v143\n" \
"v_mfma_f32_16x16x32_bf16 v[80:83], a[68:71], a[212:215], v[80:83]\n" \
"ds_read2_b32 a[10:11], v47 offset0:18 offset1:19\n" \
"v_mul_f32 v144, s20, v144\n" \
"v_mfma_f32_16x16x32_bf16 v[80:83], a[72:75], a[208:211], v[80:83]\n" \
"s_add_u32 m0, s51, 50048\n" \
"ds_read_b128 a[44:47], v50 offset:1984\n" \
"v_mul_f32 v145, s20, v145\n" \
"v_mfma_f32_16x16x32_bf16 v[80:83], a[76:79], a[204:207], v[80:83]\n" \
"buffer_load_dwordx4 v21, s[32:35], s47 offen offset:128 lds\n" \
"v_mfma_f32_16x16x32_bf16 v[80:83], a[80:83], a[200:203], v[80:83]\n" \
"ds_read2_b32 a[12:13], v47 offset0:144 offset1:145\n" \
"v_mul_f32 v146, s20, v146\n" \
"v_mfma_f32_16x16x32_bf16 v[84:87], a[64:67], a[220:223], 0\n" \
"ds_read2_b32 a[14:15], v47 offset0:146 offset1:147\n" \
"v_mul_f32 v147, s20, v147\n" \
"v_mfma_f32_16x16x32_bf16 v[84:87], a[68:71], a[216:219], v[84:87]\n" \
"ds_read_b128 a[48:51], v50 offset:2496\n" \
"v_cvt_pk_bf16_f32 v38, v144, v145\n" \
"v_mfma_f32_16x16x32_bf16 v[84:87], a[72:75], a[212:215], v[84:87]\n" \
"buffer_store_dwordx4 v[42:45], v4, s[36:39], s97 offen offset:0 sc1\n" \
"v_mfma_f32_16x16x32_bf16 v[84:87], a[76:79], a[208:211], v[84:87]\n" \
"s_add_u32 s57, s57, s93\n" \
"ds_read2_b32 a[16:17], v48 offset0:16 offset1:17\n" \
"s_mov_b32 s96, s57\n" \
"v_mfma_f32_16x16x32_bf16 v[84:87], a[80:83], a[204:207], v[84:87]\n" \
"ds_read2_b32 a[18:19], v48 offset0:18 offset1:19\n" \
"v_cvt_pk_bf16_f32 v39, v146, v147\n" \
"v_mfma_f32_16x16x32_bf16 v[84:87], a[84:87], a[200:203], v[84:87]\n" \
"s_mov_b32 s47, 786432\n" \
"s_add_u32 m0, s51, 57344\n" \
"ds_read_b128 a[52:55], v50 offset:3008\n" \
"v_mfma_f32_16x16x32_bf16 v[88:91], a[64:67], a[224:227], 0\n" \
"buffer_load_dwordx4 v21, s[32:35], s47 offen offset:0 lds\n" \
"v_mfma_f32_16x16x32_bf16 v[88:91], a[68:71], a[220:223], v[88:91]\n" \
"ds_read2_b32 a[20:21], v48 offset0:144 offset1:145\n" \
"v_mul_f32 v148, s20, v148\n" \
"v_mfma_f32_16x16x32_bf16 v[88:91], a[72:75], a[216:219], v[88:91]\n" \
"ds_read2_b32 a[22:23], v48 offset0:146 offset1:147\n" \
"v_mul_f32 v149, s20, v149\n" \
"v_mfma_f32_16x16x32_bf16 v[88:91], a[76:79], a[212:215], v[88:91]\n" \
"ds_read_b128 a[56:59], v50 offset:3520\n" \
"v_mul_f32 v150, s20, v150\n" \
"v_mfma_f32_16x16x32_bf16 v[88:91], a[80:83], a[208:211], v[88:91]\n" \
"v_mul_f32 v151, s20, v151\n" \
"v_cvt_pk_bf16_f32 v40, v148, v149\n" \
"v_mfma_f32_16x16x32_bf16 v[88:91], a[84:87], a[204:207], v[88:91]\n" \
"ds_read2_b32 a[24:25], v49 offset0:16 offset1:17\n" \
"v_cvt_pk_bf16_f32 v41, v150, v151\n" \
"v_mfma_f32_16x16x32_bf16 v[88:91], a[88:91], a[200:203], v[88:91]\n" \
"ds_read2_b32 a[26:27], v49 offset0:18 offset1:19\n" \
"v_mul_f32 v152, s20, v152\n" \
"v_mfma_f32_16x16x32_bf16 v[92:95], a[64:67], a[228:231], 0\n" \
"s_add_u32 m0, s51, 58240\n" \
"ds_read_b128 a[60:63], v50 offset:4032\n" \
"v_mul_f32 v153, s20, v153\n" \
"v_mfma_f32_16x16x32_bf16 v[92:95], a[68:71], a[224:227], v[92:95]\n" \
"buffer_load_dwordx4 v21, s[32:35], s47 offen offset:128 lds\n" \
"v_mfma_f32_16x16x32_bf16 v[92:95], a[72:75], a[220:223], v[92:95]\n" \
"ds_read2_b32 a[28:29], v49 offset0:144 offset1:145\n" \
"v_mul_f32 v154, s20, v154\n" \
"v_mfma_f32_16x16x32_bf16 v[92:95], a[76:79], a[216:219], v[92:95]\n" \
"ds_read2_b32 a[30:31], v49 offset0:146 offset1:147\n" \
"v_mul_f32 v155, s20, v155\n" \
"v_mfma_f32_16x16x32_bf16 v[92:95], a[80:83], a[212:215], v[92:95]\n" \
"buffer_store_dwordx4 v[30:33], v4, s[36:39], s96 offen offset:0 sc1\n" \
"v_mfma_f32_16x16x32_bf16 v[92:95], a[84:87], a[208:211], v[92:95]\n" \
"s_add_u32 s97, s96, s98\n" \
"v_cvt_pk_bf16_f32 v42, v152, v153\n" \
"v_cvt_pk_bf16_f32 v43, v154, v155\n" \
"v_mfma_f32_16x16x32_bf16 v[92:95], a[88:91], a[204:207], v[92:95]\n" \
"v_mul_f32 v156, s20, v156\n" \
"v_mul_f32 v157, s20, v157\n" \
"v_mfma_f32_16x16x32_bf16 v[92:95], a[92:95], a[200:203], v[92:95]\n" \
"s_waitcnt lgkmcnt(0)\n" \
"v_mul_f32 v158, s20, v158\n" \
"v_mfma_f32_16x16x32_bf16 v[64:67], a[0:3], a[32:35], v[64:67]\n" \
"ds_read_b128 a[200:203], v50 offset:384\n" \
"v_mul_f32 v159, s20, v159\n" \
"v_mfma_f32_16x16x32_bf16 v[68:71], a[0:3], a[36:39], v[68:71]\n" \
"v_cvt_pk_bf16_f32 v44, v156, v157\n" \
"v_cvt_pk_bf16_f32 v45, v158, v159\n" \
"v_mfma_f32_16x16x32_bf16 v[68:71], a[4:7], a[32:35], v[68:71]\n" \
"ds_read2_b32 a[64:65], v46 offset0:32 offset1:33\n" \
"v_mul_f32 v160, s20, v160\n" \
"v_mfma_f32_16x16x32_bf16 v[72:75], a[0:3], a[40:43], v[72:75]\n" \
"ds_read2_b32 a[66:67], v46 offset0:34 offset1:35\n" \
"v_mul_f32 v161, s20, v161\n" \
"v_mfma_f32_16x16x32_bf16 v[72:75], a[4:7], a[36:39], v[72:75]\n" \
"ds_read_b128 a[204:207], v50 offset:896\n" \
"v_mul_f32 v162, s20, v162\n" \
"v_mfma_f32_16x16x32_bf16 v[72:75], a[8:11], a[32:35], v[72:75]\n" \
"v_mul_f32 v163, s20, v163\n" \
"v_cvt_pk_bf16_f32 v30, v160, v161\n" \
"v_mfma_f32_16x16x32_bf16 v[76:79], a[0:3], a[44:47], v[76:79]\n" \
"ds_read2_b32 a[68:69], v46 offset0:160 offset1:161\n" \
"v_cvt_pk_bf16_f32 v31, v162, v163\n" \
"v_mfma_f32_16x16x32_bf16 v[76:79], a[4:7], a[40:43], v[76:79]\n" \
"ds_read2_b32 a[70:71], v46 offset0:162 offset1:163\n" \
"v_mul_f32 v164, s20, v164\n" \
"v_mfma_f32_16x16x32_bf16 v[76:79], a[8:11], a[36:39], v[76:79]\n" \
"ds_read_b128 a[208:211], v50 offset:1408\n" \
"v_mul_f32 v165, s20, v165\n" \
"v_mfma_f32_16x16x32_bf16 v[76:79], a[12:15], a[32:35], v[76:79]\n" \
"buffer_store_dwordx4 v[34:37], v4, s[36:39], s97 offen offset:0 sc1\n" \
"v_mfma_f32_16x16x32_bf16 v[80:83], a[0:3], a[48:51], v[80:83]\n" \
"s_add_u32 s96, s96, s92\n" \
"ds_read2_b32 a[72:73], v47 offset0:32 offset1:33\n" \
"v_mul_f32 v166, s20, v166\n" \
"v_mfma_f32_16x16x32_bf16 v[80:83], a[4:7], a[44:47], v[80:83]\n" \
"ds_read2_b32 a[74:75], v47 offset0:34 offset1:35\n" \
"v_mul_f32 v167, s20, v167\n" \
"v_mfma_f32_16x16x32_bf16 v[80:83], a[8:11], a[40:43], v[80:83]\n" \
"ds_read_b128 a[212:215], v50 offset:1920\n" \
"v_cvt_pk_bf16_f32 v32, v164, v165\n" \
"v_mfma_f32_16x16x32_bf16 v[80:83], a[12:15], a[36:39], v[80:83]\n" \
"v_cvt_pk_bf16_f32 v33, v166, v167\n" \
"v_mul_f32 v168, s20, v168\n" \
"v_mfma_f32_16x16x32_bf16 v[80:83], a[16:19], a[32:35], v[80:83]\n" \
"ds_read2_b32 a[76:77], v47 offset0:160 offset1:161\n" \
"v_mul_f32 v169, s20, v169\n" \
"v_mfma_f32_16x16x32_bf16 v[84:87], a[0:3], a[52:55], v[84:87]\n" \
"ds_read2_b32 a[78:79], v47 offset0:162 offset1:163\n" \
"v_mul_f32 v170, s20, v170\n" \
"v_mfma_f32_16x16x32_bf16 v[84:87], a[4:7], a[48:51], v[84:87]\n" \
"ds_read_b128 a[216:219], v50 offset:2432\n" \
"v_mul_f32 v171, s20, v171\n" \
"v_mfma_f32_16x16x32_bf16 v[84:87], a[8:11], a[44:47], v[84:87]\n" \
"v_cvt_pk_bf16_f32 v34, v168, v169\n" \
"v_cvt_pk_bf16_f32 v35, v170, v171\n" \
"v_mfma_f32_16x16x32_bf16 v[84:87], a[12:15], a[40:43], v[84:87]\n" \
"ds_read2_b32 a[80:81], v48 offset0:32 offset1:33\n" \
"v_mul_f32 v172, s20, v172\n" \
"v_mfma_f32_16x16x32_bf16 v[84:87], a[16:19], a[36:39], v[84:87]\n" \
"ds_read2_b32 a[82:83], v48 offset0:34 offset1:35\n" \
"v_mul_f32 v173, s20, v173\n" \
"v_mfma_f32_16x16x32_bf16 v[84:87], a[20:23], a[32:35], v[84:87]\n" \
"ds_read_b128 a[220:223], v50 offset:2944\n" \
"v_mul_f32 v174, s20, v174\n" \
"v_mfma_f32_16x16x32_bf16 v[88:91], a[0:3], a[56:59], v[88:91]\n" \
"buffer_store_dwordx4 v[38:41], v4, s[36:39], s96 offen offset:0 sc1\n" \
"v_mfma_f32_16x16x32_bf16 v[88:91], a[4:7], a[52:55], v[88:91]\n" \
"s_add_u32 s97, s96, s98\n" \
"ds_read2_b32 a[84:85], v48 offset0:160 offset1:161\n" \
"v_mul_f32 v175, s20, v175\n" \
"v_mfma_f32_16x16x32_bf16 v[88:91], a[8:11], a[48:51], v[88:91]\n" \
"ds_read2_b32 a[86:87], v48 offset0:162 offset1:163\n" \
"v_cvt_pk_bf16_f32 v36, v172, v173\n" \
"v_mfma_f32_16x16x32_bf16 v[88:91], a[12:15], a[44:47], v[88:91]\n" \
"ds_read_b128 a[224:227], v50 offset:3456\n" \
"v_cvt_pk_bf16_f32 v37, v174, v175\n" \
"v_mfma_f32_16x16x32_bf16 v[88:91], a[16:19], a[40:43], v[88:91]\n" \
"v_mul_f32 v176, s20, v176\n" \
"v_mul_f32 v177, s20, v177\n" \
"v_mfma_f32_16x16x32_bf16 v[88:91], a[20:23], a[36:39], v[88:91]\n" \
"ds_read2_b32 a[88:89], v49 offset0:32 offset1:33\n" \
"v_mul_f32 v178, s20, v178\n" \
"v_mfma_f32_16x16x32_bf16 v[88:91], a[24:27], a[32:35], v[88:91]\n" \
"ds_read2_b32 a[90:91], v49 offset0:34 offset1:35\n" \
"v_mul_f32 v179, s20, v179\n" \
"v_mfma_f32_16x16x32_bf16 v[92:95], a[0:3], a[60:63], v[92:95]\n" \
"ds_read_b128 a[228:231], v50 offset:3968\n" \
"v_cvt_pk_bf16_f32 v38, v176, v177\n" \
"v_mfma_f32_16x16x32_bf16 v[92:95], a[4:7], a[56:59], v[92:95]\n" \
"v_cvt_pk_bf16_f32 v39, v178, v179\n" \
"v_mul_f32 v180, s20, v180\n" \
"v_mfma_f32_16x16x32_bf16 v[92:95], a[8:11], a[52:55], v[92:95]\n" \
"ds_read2_b32 a[92:93], v49 offset0:160 offset1:161\n" \
"v_mul_f32 v181, s20, v181\n" \
"v_mfma_f32_16x16x32_bf16 v[92:95], a[12:15], a[48:51], v[92:95]\n" \
"ds_read2_b32 a[94:95], v49 offset0:162 offset1:163\n" \
"v_mul_f32 v182, s20, v182\n" \
"v_mfma_f32_16x16x32_bf16 v[92:95], a[16:19], a[44:47], v[92:95]\n" \
"buffer_store_dwordx4 v[42:45], v4, s[36:39], s97 offen offset:0 sc1\n" \
"v_mfma_f32_16x16x32_bf16 v[92:95], a[20:23], a[40:43], v[92:95]\n" \
"s_add_u32 s96, s96, s92\n" \
"v_mul_f32 v183, s20, v183\n" \
"v_cvt_pk_bf16_f32 v40, v180, v181\n" \
"v_mfma_f32_16x16x32_bf16 v[92:95], a[24:27], a[36:39], v[92:95]\n" \
"v_cvt_pk_bf16_f32 v41, v182, v183\n" \
"v_mul_f32 v184, s20, v184\n" \
"v_mfma_f32_16x16x32_bf16 v[92:95], a[28:31], a[32:35], v[92:95]\n" \
"s_waitcnt lgkmcnt(0)\n" \
"v_mul_f32 v185, s20, v185\n" \
"v_mfma_f32_16x16x32_bf16 v[64:67], a[64:67], a[200:203], v[64:67]\n" \
"ds_read_b128 a[32:35], v50 offset:320\n" \
"v_mul_f32 v186, s20, v186\n" \
"v_mfma_f32_16x16x32_bf16 v[68:71], a[64:67], a[204:207], v[68:71]\n" \
"v_mul_f32 v187, s20, v187\n" \
"v_cvt_pk_bf16_f32 v42, v184, v185\n" \
"v_mfma_f32_16x16x32_bf16 v[68:71], a[68:71], a[200:203], v[68:71]\n" \
"ds_read2_b32 a[0:1], v46 offset0:48 offset1:49\n" \
"v_cvt_pk_bf16_f32 v43, v186, v187\n" \
"v_mfma_f32_16x16x32_bf16 v[72:75], a[64:67], a[208:211], v[72:75]\n" \
"ds_read2_b32 a[2:3], v46 offset0:50 offset1:51\n" \
"v_mul_f32 v188, s20, v188\n" \
"v_mfma_f32_16x16x32_bf16 v[72:75], a[68:71], a[204:207], v[72:75]\n" \
"ds_read_b128 a[36:39], v50 offset:832\n" \
"v_mul_f32 v189, s20, v189\n" \
"v_mfma_f32_16x16x32_bf16 v[72:75], a[72:75], a[200:203], v[72:75]\n" \
"v_mul_f32 v190, s20, v190\n" \
"v_mul_f32 v191, s20, v191\n" \
"v_mfma_f32_16x16x32_bf16 v[76:79], a[64:67], a[212:215], v[76:79]\n" \
"ds_read2_b32 a[4:5], v46 offset0:176 offset1:177\n" \
"v_cvt_pk_bf16_f32 v44, v188, v189\n" \
"v_mfma_f32_16x16x32_bf16 v[76:79], a[68:71], a[208:211], v[76:79]\n" \
"ds_read2_b32 a[6:7], v46 offset0:178 offset1:179\n" \
"v_cvt_pk_bf16_f32 v45, v190, v191\n" \
"v_mfma_f32_16x16x32_bf16 v[76:79], a[72:75], a[204:207], v[76:79]\n" \
"ds_read_b128 a[40:43], v50 offset:1344\n" \
"v_mul_f32 v192, s20, v192\n" \
"v_mfma_f32_16x16x32_bf16 v[76:79], a[76:79], a[200:203], v[76:79]\n" \
"buffer_store_dwordx4 v[30:33], v4, s[36:39], s96 offen offset:0 sc1\n" \
"v_mfma_f32_16x16x32_bf16 v[80:83], a[64:67], a[216:219], v[80:83]\n" \
"s_add_u32 s97, s96, s98\n" \
"ds_read2_b32 a[8:9], v47 offset0:48 offset1:49\n" \
"v_mul_f32 v193, s20, v193\n" \
"v_mfma_f32_16x16x32_bf16 v[80:83], a[68:71], a[212:215], v[80:83]\n" \
"ds_read2_b32 a[10:11], v47 offset0:50 offset1:51\n" \
"v_mul_f32 v194, s20, v194\n" \
"v_mfma_f32_16x16x32_bf16 v[80:83], a[72:75], a[208:211], v[80:83]\n" \
"LH8_%=:\ns_cbranch_execz LH12_%=\n" \
"ds_read_b128 a[44:47], v50 offset:1856\n" \
"v_mul_f32 v195, s20, v195\n" \
"v_mfma_f32_16x16x32_bf16 v[80:83], a[76:79], a[204:207], v[80:83]\n" \
"v_cvt_pk_bf16_f32 v30, v192, v193\n" \
"v_cvt_pk_bf16_f32 v31, v194, v195\n" \
"v_mfma_f32_16x16x32_bf16 v[80:83], a[80:83], a[200:203], v[80:83]\n" \
"ds_read2_b32 a[12:13], v47 offset0:176 offset1:177\n" \
"v_mul_f32 v196, s20, v196\n" \
"v_mfma_f32_16x16x32_bf16 v[84:87], a[64:67], a[220:223], v[84:87]\n" \
"ds_read2_b32 a[14:15], v47 offset0:178 offset1:179\n" \
"v_mul_f32 v197, s20, v197\n" \
"v_mfma_f32_16x16x32_bf16 v[84:87], a[68:71], a[216:219], v[84:87]\n" \
"ds_read_b128 a[48:51], v50 offset:2368\n" \
"v_mul_f32 v198, s20, v198\n" \
"v_mfma_f32_16x16x32_bf16 v[84:87], a[72:75], a[212:215], v[84:87]\n" \
"v_mul_f32 v199, s20, v199\n" \
"v_cvt_pk_bf16_f32 v32, v196, v197\n" \
"v_mfma_f32_16x16x32_bf16 v[84:87], a[76:79], a[208:211], v[84:87]\n" \
"ds_read2_b32 a[16:17], v48 offset0:48 offset1:49\n" \
"v_cvt_pk_bf16_f32 v33, v198, v199\n" \
"v_mfma_f32_16x16x32_bf16 v[84:87], a[80:83], a[204:207], v[84:87]\n" \
"ds_read2_b32 a[18:19], v48 offset0:50 offset1:51\n" \
"v_mul_f32 v200, s20, v200\n" \
"v_mfma_f32_16x16x32_bf16 v[84:87], a[84:87], a[200:203], v[84:87]\n" \
"ds_read_b128 a[52:55], v50 offset:2880\n" \
"v_mul_f32 v201, s20, v201\n" \
"v_mfma_f32_16x16x32_bf16 v[88:91], a[64:67], a[224:227], v[88:91]\n" \
"buffer_store_dwordx4 v[34:37], v4, s[36:39], s97 offen offset:0 sc1\n" \
"v_mfma_f32_16x16x32_bf16 v[88:91], a[68:71], a[220:223], v[88:91]\n" \
"s_add_u32 s96, s96, s92\n" \
"ds_read2_b32 a[20:21], v48 offset0:176 offset1:177\n" \
"v_mul_f32 v202, s20, v202\n" \
"v_mfma_f32_16x16x32_bf16 v[88:91], a[72:75], a[216:219], v[88:91]\n" \
"ds_read2_b32 a[22:23], v48 offset0:178 offset1:179\n" \
"v_mul_f32 v203, s20, v203\n" \
"v_mfma_f32_16x16x32_bf16 v[88:91], a[76:79], a[212:215], v[88:91]\n" \
"ds_read_b128 a[56:59], v50 offset:3392\n" \
"v_cvt_pk_bf16_f32 v34, v200, v201\n" \
"v_mfma_f32_16x16x32_bf16 v[88:91], a[80:83], a[208:211], v[88:91]\n" \
"v_cvt_pk_bf16_f32 v35, v202, v203\n" \
"v_mul_f32 v204, s20, v204\n" \
"v_mfma_f32_16x16x32_bf16 v[88:91], a[84:87], a[204:207], v[88:91]\n" \
"ds_read2_b32 a[24:25], v49 offset0:48 offset1:49\n" \
"v_mul_f32 v205, s20, v205\n" \
"v_mfma_f32_16x16x32_bf16 v[88:91], a[88:91], a[200:203], v[88:91]\n" \
"ds_read2_b32 a[26:27], v49 offset0:50 offset1:51\n" \
"v_mul_f32 v206, s20, v206\n" \
"v_mfma_f32_16x16x32_bf16 v[92:95], a[64:67], a[228:231], v[92:95]\n" \
"ds_read_b128 a[60:63], v50 offset:3904\n" \
"v_mul_f32 v207, s20, v207\n" \
"v_mfma_f32_16x16x32_bf16 v[92:95], a[68:71], a[224:227], v[92:95]\n" \
"v_cvt_pk_bf16_f32 v36, v204, v205\n" \
"v_cvt_pk_bf16_f32 v37, v206, v207\n" \
"v_mfma_f32_16x16x32_bf16 v[92:95], a[72:75], a[220:223], v[92:95]\n" \
"ds_read2_b32 a[28:29], v49 offset0:176 offset1:177\n" \
"v_mul_f32 v208, s20, v208\n" \
"v_mfma_f32_16x16x32_bf16 v[92:95], a[76:79], a[216:219], v[92:95]\n" \
"ds_read2_b32 a[30:31], v49 offset0:178 offset1:179\n" \
"v_mul_f32 v209, s20, v209\n" \
"v_mfma_f32_16x16x32_bf16 v[92:95], a[80:83], a[212:215], v[92:95]\n" \
"buffer_store_dwordx4 v[38:41], v4, s[36:39], s96 offen offset:0 sc1\n" \
"v_mfma_f32_16x16x32_bf16 v[92:95], a[84:87], a[208:211], v[92:95]\n" \
"s_add_u32 s97, s96, s98\n" \
"v_mul_f32 v210, s20, v210\n" \
"v_mul_f32 v211, s20, v211\n" \
"v_mfma_f32_16x16x32_bf16 v[92:95], a[88:91], a[204:207], v[92:95]\n" \
"v_cvt_pk_bf16_f32 v38, v208, v209\n" \
"v_cvt_pk_bf16_f32 v39, v210, v211\n" \
"v_mfma_f32_16x16x32_bf16 v[92:95], a[92:95], a[200:203], v[92:95]\n" \
"s_waitcnt lgkmcnt(0)\n" \
"v_mul_f32 v212, s20, v212\n" \
"v_mfma_f32_16x16x32_bf16 v[64:67], a[0:3], a[32:35], v[64:67]\n" \
"ds_read_b128 a[200:203], v50 offset:256\n" \
"v_mul_f32 v213, s20, v213\n" \
"v_mfma_f32_16x16x32_bf16 v[68:71], a[0:3], a[36:39], v[68:71]\n" \
"v_mul_f32 v214, s20, v214\n" \
"v_mul_f32 v215, s20, v215\n" \
"v_mfma_f32_16x16x32_bf16 v[68:71], a[4:7], a[32:35], v[68:71]\n" \
"ds_read2_b32 a[64:65], v46 offset0:64 offset1:65\n" \
"v_cvt_pk_bf16_f32 v40, v212, v213\n" \
"v_mfma_f32_16x16x32_bf16 v[72:75], a[0:3], a[40:43], v[72:75]\n" \
"ds_read2_b32 a[66:67], v46 offset0:66 offset1:67\n" \
"v_cvt_pk_bf16_f32 v41, v214, v215\n" \
"v_mfma_f32_16x16x32_bf16 v[72:75], a[4:7], a[36:39], v[72:75]\n" \
"ds_read_b128 a[204:207], v50 offset:768\n" \
"v_mul_f32 v216, s20, v216\n" \
"v_mfma_f32_16x16x32_bf16 v[72:75], a[8:11], a[32:35], v[72:75]\n" \
"v_mul_f32 v217, s20, v217\n" \
"v_mul_f32 v218, s20, v218\n" \
"v_mfma_f32_16x16x32_bf16 v[76:79], a[0:3], a[44:47], v[76:79]\n" \
"ds_read2_b32 a[68:69], v46 offset0:192 offset1:193\n" \
"v_mul_f32 v219, s20, v219\n" \
"v_mfma_f32_16x16x32_bf16 v[76:79], a[4:7], a[40:43], v[76:79]\n" \
"ds_read2_b32 a[70:71], v46 offset0:194 offset1:195\n" \
"v_mfma_f32_16x16x32_bf16 v[76:79], a[8:11], a[36:39], v[76:79]\n" \
"ds_read_b128 a[208:211], v50 offset:1280\n" \
"v_mfma_f32_16x16x32_bf16 v[76:79], a[12:15], a[32:35], v[76:79]\n" \
"buffer_store_dwordx4 v[42:45], v4, s[36:39], s97 offen offset:0 sc1\n" \
"v_mfma_f32_16x16x32_bf16 v[80:83], a[0:3], a[48:51], v[80:83]\n" \
"s_add_u32 s57, s57, s93\n" \
"ds_read2_b32 a[72:73], v47 offset0:64 offset1:65\n" \
"s_mov_b32 s96, s57\n" \
"v_mfma_f32_16x16x32_bf16 v[80:83], a[4:7], a[44:47], v[80:83]\n" \
"ds_read2_b32 a[74:75], v47 offset0:66 offset1:67\n" \
"v_cvt_pk_bf16_f32 v42, v216, v217\n" \
"v_mfma_f32_16x16x32_bf16 v[80:83], a[8:11], a[40:43], v[80:83]\n" \
"ds_read_b128 a[212:215], v50 offset:1792\n" \
"v_cvt_pk_bf16_f32 v43, v218, v219\n" \
"v_mfma_f32_16x16x32_bf16 v[80:83], a[12:15], a[36:39], v[80:83]\n" \
"v_mul_f32 v220, s20, v220\n" \
"v_mul_f32 v221, s20, v221\n" \
"v_mfma_f32_16x16x32_bf16 v[80:83], a[16:19], a[32:35], v[80:83]\n" \
"ds_read2_b32 a[76:77], v47 offset0:192 offset1:193\n" \
"v_mul_f32 v222, s20, v222\n" \
"v_mfma_f32_16x16x32_bf16 v[84:87], a[0:3], a[52:55], v[84:87]\n" \
"ds_read2_b32 a[78:79], v47 offset0:194 offset1:195\n" \
"v_mul_f32 v223, s20, v223\n" \
"v_mfma_f32_16x16x32_bf16 v[84:87], a[4:7], a[48:51], v[84:87]\n" \
"ds_read_b128 a[216:219], v50 offset:2304\n" \
"v_cvt_pk_bf16_f32 v44, v220, v221\n" \
"v_mfma_f32_16x16x32_bf16 v[84:87], a[8:11], a[44:47], v[84:87]\n" \
"v_cvt_pk_bf16_f32 v45, v222, v223\n" \
"v_mul_f32 v224, s20, v224\n" \
"v_mfma_f32_16x16x32_bf16 v[84:87], a[12:15], a[40:43], v[84:87]\n" \
"ds_read2_b32 a[80:81], v48 offset0:64 offset1:65\n" \
"v_mul_f32 v225, s20, v225\n" \
"v_mfma_f32_16x16x32_bf16 v[84:87], a[16:19], a[36:39], v[84:87]\n" \
"ds_read2_b32 a[82:83], v48 offset0:66 offset1:67\n" \
"v_mul_f32 v226, s20, v226\n" \
"v_mfma_f32_16x16x32_bf16 v[84:87], a[20:23], a[32:35], v[84:87]\n" \
"ds_read_b128 a[220:223], v50 offset:2816\n" \
"v_mul_f32 v227, s20, v227\n" \
"v_mfma_f32_16x16x32_bf16 v[88:91], a[0:3], a[56:59], v[88:91]\n" \
"buffer_store_dwordx4 v[30:33], v4, s[36:39], s96 offen offset:0 sc1\n" \
"v_mfma_f32_16x16x32_bf16 v[88:91], a[4:7], a[52:55], v[88:91]\n" \
"s_add_u32 s97, s96, s98\n" \
"ds_read2_b32 a[84:85], v48 offset0:192 offset1:193\n" \
"v_cvt_pk_bf16_f32 v30, v224, v225\n" \
"v_mfma_f32_16x16x32_bf16 v[88:91], a[8:11], a[48:51], v[88:91]\n" \
"ds_read2_b32 a[86:87], v48 offset0:194 offset1:195\n" \
"v_cvt_pk_bf16_f32 v31, v226, v227\n" \
"v_mfma_f32_16x16x32_bf16 v[88:91], a[12:15], a[44:47], v[88:91]\n" \
"ds_read_b128 a[224:227], v50 offset:3328\n" \
"v_mul_f32 v228, s20, v228\n" \
"v_mfma_f32_16x16x32_bf16 v[88:91], a[16:19], a[40:43], v[88:91]\n" \
"v_mul_f32 v229, s20, v229\n" \
"v_mul_f32 v230, s20, v230\n" \
"v_mfma_f32_16x16x32_bf16 v[88:91], a[20:23], a[36:39], v[88:91]\n" \
"ds_read2_b32 a[88:89], v49 offset0:64 offset1:65\n" \
"v_mul_f32 v231, s20, v231\n" \
"v_mfma_f32_16x16x32_bf16 v[88:91], a[24:27], a[32:35], v[88:91]\n" \
"ds_read2_b32 a[90:91], v49 offset0:66 offset1:67\n" \
"v_cvt_pk_bf16_f32 v32, v228, v229\n" \
"v_mfma_f32_16x16x32_bf16 v[92:95], a[0:3], a[60:63], v[92:95]\n" \
"ds_read_b128 a[228:231], v50 offset:3840\n" \
"v_cvt_pk_bf16_f32 v33, v230, v231\n" \
"v_mfma_f32_16x16x32_bf16 v[92:95], a[4:7], a[56:59], v[92:95]\n" \
"v_mul_f32 v232, s20, v232\n" \
"v_mul_f32 v233, s20, v233\n" \
"v_mfma_f32_16x16x32_bf16 v[92:95], a[8:11], a[52:55], v[92:95]\n" \
"ds_read2_b32 a[92:93], v49 offset0:192 offset1:193\n" \
"v_mul_f32 v234, s20, v234\n" \
"v_mfma_f32_16x16x32_bf16 v[92:95], a[12:15], a[48:51], v[92:95]\n" \
"ds_read2_b32 a[94:95], v49 offset0:194 offset1:195\n" \
"v_mul_f32 v235, s20, v235\n" \
"v_mfma_f32_16x16x32_bf16 v[92:95], a[16:19], a[44:47], v[92:95]\n" \
"buffer_store_dwordx4 v[34:37], v4, s[36:39], s97 offen offset:0 sc1\n" \
"v_mfma_f32_16x16x32_bf16 v[92:95], a[20:23], a[40:43], v[92:95]\n" \
"s_add_u32 s96, s96, s92\n" \
"v_cvt_pk_bf16_f32 v34, v232, v233\n" \
"v_cvt_pk_bf16_f32 v35, v234, v235\n" \
"v_mfma_f32_16x16x32_bf16 v[92:95], a[24:27], a[36:39], v[92:95]\n" \
"v_mul_f32 v236, s20, v236\n" \
"v_mul_f32 v237, s20, v237\n" \
"v_mfma_f32_16x16x32_bf16 v[92:95], a[28:31], a[32:35], v[92:95]\n" \
"s_waitcnt lgkmcnt(0)\n" \
"v_mul_f32 v238, s20, v238\n" \
"v_mfma_f32_16x16x32_bf16 v[64:67], a[64:67], a[200:203], v[64:67]\n" \
"ds_read_b128 a[32:35], v50 offset:192\n" \
"v_mul_f32 v239, s20, v239\n" \
"v_mfma_f32_16x16x32_bf16 v[68:71], a[64:67], a[204:207], v[68:71]\n" \
"v_cvt_pk_bf16_f32 v36, v236, v237\n" \
"v_cvt_pk_bf16_f32 v37, v238, v239\n" \
"v_mfma_f32_16x16x32_bf16 v[68:71], a[68:71], a[200:203], v[68:71]\n" \
"ds_read2_b32 a[0:1], v46 offset0:80 offset1:81\n" \
"v_mul_f32 v240, s20, v240\n" \
"v_mfma_f32_16x16x32_bf16 v[72:75], a[64:67], a[208:211], v[72:75]\n" \
"ds_read2_b32 a[2:3], v46 offset0:82 offset1:83\n" \
"v_mul_f32 v241, s20, v241\n" \
"v_mfma_f32_16x16x32_bf16 v[72:75], a[68:71], a[204:207], v[72:75]\n" \
"ds_read_b128 a[36:39], v50 offset:704\n" \
"v_mul_f32 v242, s20, v242\n" \
"v_mfma_f32_16x16x32_bf16 v[72:75], a[72:75], a[200:203], v[72:75]\n" \
"v_mul_f32 v243, s20, v243\n" \
"v_mfma_f32_16x16x32_bf16 v[76:79], a[64:67], a[212:215], v[76:79]\n" \
"ds_read2_b32 a[4:5], v46 offset0:208 offset1:209\n" \
"v_mfma_f32_16x16x32_bf16 v[76:79], a[68:71], a[208:211], v[76:79]\n" \
"ds_read2_b32 a[6:7], v46 offset0:210 offset1:211\n" \
"v_mfma_f32_16x16x32_bf16 v[76:79], a[72:75], a[204:207], v[76:79]\n" \
"ds_read_b128 a[40:43], v50 offset:1216\n" \
"v_mfma_f32_16x16x32_bf16 v[76:79], a[76:79], a[200:203], v[76:79]\n" \
"buffer_store_dwordx4 v[38:41], v4, s[36:39], s96 offen offset:0 sc1\n" \
"v_mfma_f32_16x16x32_bf16 v[80:83], a[64:67], a[216:219], v[80:83]\n" \
"s_add_u32 s97, s96, s98\n" \
"ds_read2_b32 a[8:9], v47 offset0:80 offset1:81\n" \
"v_cvt_pk_bf16_f32 v38, v240, v241\n" \
"v_mfma_f32_16x16x32_bf16 v[80:83], a[68:71], a[212:215], v[80:83]\n" \
"ds_read2_b32 a[10:11], v47 offset0:82 offset1:83\n" \
"v_cvt_pk_bf16_f32 v39, v242, v243\n" \
"v_mfma_f32_16x16x32_bf16 v[80:83], a[72:75], a[208:211], v[80:83]\n" \
"ds_read_b128 a[44:47], v50 offset:1728\n" \
"v_mul_f32 v244, s20, v244\n" \
"v_mfma_f32_16x16x32_bf16 v[80:83], a[76:79], a[204:207], v[80:83]\n" \
"v_mul_f32 v245, s20, v245\n" \
"v_mul_f32 v246, s20, v246\n" \
"v_mfma_f32_16x16x32_bf16 v[80:83], a[80:83], a[200:203], v[80:83]\n" \
"ds_read2_b32 a[12:13], v47 offset0:208 offset1:209\n" \
"v_mul_f32 v247, s20, v247\n" \
"v_mfma_f32_16x16x32_bf16 v[84:87], a[64:67], a[220:223], v[84:87]\n" \
"ds_read2_b32 a[14:15], v47 offset0:210 offset1:211\n" \
"v_cvt_pk_bf16_f32 v40, v244, v245\n" \
"v_mfma_f32_16x16x32_bf16 v[84:87], a[68:71], a[216:219], v[84:87]\n" \
"ds_read_b128 a[48:51], v50 offset:2240\n" \
"v_cvt_pk_bf16_f32 v41, v246, v247\n" \
"v_mfma_f32_16x16x32_bf16 v[84:87], a[72:75], a[212:215], v[84:87]\n" \
"v_mul_f32 v248, s20, v248\n" \
"v_mul_f32 v249, s20, v249\n" \
"v_mfma_f32_16x16x32_bf16 v[84:87], a[76:79], a[208:211], v[84:87]\n" \
"ds_read2_b32 a[16:17], v48 offset0:80 offset1:81\n" \
"v_mul_f32 v250, s20, v250\n" \
"v_mfma_f32_16x16x32_bf16 v[84:87], a[80:83], a[204:207], v[84:87]\n" \
"ds_read2_b32 a[18:19], v48 offset0:82 offset1:83\n" \
"v_mul_f32 v251, s20, v251\n" \
"v_mfma_f32_16x16x32_bf16 v[84:87], a[84:87], a[200:203], v[84:87]\n" \
"ds_read_b128 a[52:55], v50 offset:2752\n" \
"v_mfma_f32_16x16x32_bf16 v[88:91], a[64:67], a[224:227], v[88:91]\n" \
"buffer_store_dwordx4 v[42:45], v4, s[36:39], s97 offen offset:0 sc1\n" \
"v_mfma_f32_16x16x32_bf16 v[88:91], a[68:71], a[220:223], v[88:91]\n" \
"s_add_u32 s96, s96, s92\n" \
"ds_read2_b32 a[20:21], v48 offset0:208 offset1:209\n" \
"v_cvt_pk_bf16_f32 v42, v248, v249\n" \
"v_mfma_f32_16x16x32_bf16 v[88:91], a[72:75], a[216:219], v[88:91]\n" \
"ds_read2_b32 a[22:23], v48 offset0:210 offset1:211\n" \
"v_cvt_pk_bf16_f32 v43, v250, v251\n" \
"v_mfma_f32_16x16x32_bf16 v[88:91], a[76:79], a[212:215], v[88:91]\n" \
"ds_read_b128 a[56:59], v50 offset:3264\n" \
"v_mul_f32 v252, s20, v252\n" \
"v_mfma_f32_16x16x32_bf16 v[88:91], a[80:83], a[208:211], v[88:91]\n" \
"v_mul_f32 v253, s20, v253\n" \
"v_mul_f32 v254, s20, v254\n" \
"v_mfma_f32_16x16x32_bf16 v[88:91], a[84:87], a[204:207], v[88:91]\n" \
"ds_read2_b32 a[24:25], v49 offset0:80 offset1:81\n" \
"v_mul_f32 v255, s20, v255\n" \
"v_mfma_f32_16x16x32_bf16 v[88:91], a[88:91], a[200:203], v[88:91]\n" \
"ds_read2_b32 a[26:27], v49 offset0:82 offset1:83\n" \
"v_cvt_pk_bf16_f32 v44, v252, v253\n" \
"v_mfma_f32_16x16x32_bf16 v[92:95], a[64:67], a[228:231], v[92:95]\n" \
"ds_read_b128 a[60:63], v50 offset:3776\n" \
"v_cvt_pk_bf16_f32 v45, v254, v255\n" \
"v_mfma_f32_16x16x32_bf16 v[92:95], a[68:71], a[224:227], v[92:95]\n" \
"v_accvgpr_read_b32 v248, a128\n" \
"v_accvgpr_read_b32 v249, a129\n" \
"v_mfma_f32_16x16x32_bf16 v[92:95], a[72:75], a[220:223], v[92:95]\n" \
"ds_read2_b32 a[28:29], v49 offset0:208 offset1:209\n" \
"v_accvgpr_read_b32 v250, a130\n" \
"v_mfma_f32_16x16x32_bf16 v[92:95], a[76:79], a[216:219], v[92:95]\n" \
"ds_read2_b32 a[30:31], v49 offset0:210 offset1:211\n" \
"v_accvgpr_read_b32 v251, a131\n" \
"v_mfma_f32_16x16x32_bf16 v[92:95], a[80:83], a[212:215], v[92:95]\n" \
"buffer_store_dwordx4 v[30:33], v4, s[36:39], s96 offen offset:0 sc1\n" \
"v_mfma_f32_16x16x32_bf16 v[92:95], a[84:87], a[208:211], v[92:95]\n" \
"s_add_u32 s97, s96, s98\n" \
"v_mul_f32 v248, s20, v248\n" \
"v_mul_f32 v249, s20, v249\n" \
"v_mfma_f32_16x16x32_bf16 v[92:95], a[88:91], a[204:207], v[92:95]\n" \
"v_mul_f32 v250, s20, v250\n" \
"v_mul_f32 v251, s20, v251\n" \
"v_mfma_f32_16x16x32_bf16 v[92:95], a[92:95], a[200:203], v[92:95]\n" \
"s_waitcnt lgkmcnt(0)\n" \
"v_cvt_pk_bf16_f32 v30, v248, v249\n" \
"v_mfma_f32_16x16x32_bf16 v[64:67], a[0:3], a[32:35], v[64:67]\n" \
"ds_read_b128 a[200:203], v50 offset:128\n" \
"v_cvt_pk_bf16_f32 v31, v250, v251\n" \
"v_mfma_f32_16x16x32_bf16 v[68:71], a[0:3], a[36:39], v[68:71]\n" \
"v_accvgpr_read_b32 v252, a132\n" \
"v_accvgpr_read_b32 v253, a133\n" \
"v_mfma_f32_16x16x32_bf16 v[68:71], a[4:7], a[32:35], v[68:71]\n" \
"ds_read2_b32 a[64:65], v46 offset0:96 offset1:97\n" \
"v_accvgpr_read_b32 v254, a134\n" \
"v_mfma_f32_16x16x32_bf16 v[72:75], a[0:3], a[40:43], v[72:75]\n" \
"ds_read2_b32 a[66:67], v46 offset0:98 offset1:99\n" \
"v_accvgpr_read_b32 v255, a135\n" \
"v_mfma_f32_16x16x32_bf16 v[72:75], a[4:7], a[36:39], v[72:75]\n" \
"ds_read_b128 a[204:207], v50 offset:640\n" \
"v_mul_f32 v252, s20, v252\n" \
"v_mfma_f32_16x16x32_bf16 v[72:75], a[8:11], a[32:35], v[72:75]\n" \
"v_mul_f32 v253, s20, v253\n" \
"v_mul_f32 v254, s20, v254\n" \
"v_mfma_f32_16x16x32_bf16 v[76:79], a[0:3], a[44:47], v[76:79]\n" \
"ds_read2_b32 a[68:69], v46 offset0:224 offset1:225\n" \
"v_mul_f32 v255, s20, v255\n" \
"v_mfma_f32_16x16x32_bf16 v[76:79], a[4:7], a[40:43], v[76:79]\n" \
"ds_read2_b32 a[70:71], v46 offset0:226 offset1:227\n" \
"v_cvt_pk_bf16_f32 v32, v252, v253\n" \
"v_mfma_f32_16x16x32_bf16 v[76:79], a[8:11], a[36:39], v[76:79]\n" \
"ds_read_b128 a[208:211], v50 offset:1152\n" \
"v_cvt_pk_bf16_f32 v33, v254, v255\n" \
"v_mfma_f32_16x16x32_bf16 v[76:79], a[12:15], a[32:35], v[76:79]\n" \
"buffer_store_dwordx4 v[34:37], v4, s[36:39], s97 offen offset:0 sc1\n" \
"v_mfma_f32_16x16x32_bf16 v[80:83], a[0:3], a[48:51], v[80:83]\n" \
"s_add_u32 s96, s96, s92\n" \
"ds_read2_b32 a[72:73], v47 offset0:96 offset1:97\n" \
"v_accvgpr_read_b32 v248, a136\n" \
"v_mfma_f32_16x16x32_bf16 v[80:83], a[4:7], a[44:47], v[80:83]\n" \
"ds_read2_b32 a[74:75], v47 offset0:98 offset1:99\n" \
"v_accvgpr_read_b32 v249, a137\n" \
"v_mfma_f32_16x16x32_bf16 v[80:83], a[8:11], a[40:43], v[80:83]\n" \
"ds_read_b128 a[212:215], v50 offset:1664\n" \
"v_accvgpr_read_b32 v250, a138\n" \
"v_mfma_f32_16x16x32_bf16 v[80:83], a[12:15], a[36:39], v[80:83]\n" \
"v_accvgpr_read_b32 v251, a139\n" \
"v_mul_f32 v248, s20, v248\n" \
"v_mfma_f32_16x16x32_bf16 v[80:83], a[16:19], a[32:35], v[80:83]\n" \
"ds_read2_b32 a[76:77], v47 offset0:224 offset1:225\n" \
"v_mul_f32 v249, s20, v249\n" \
"v_mfma_f32_16x16x32_bf16 v[84:87], a[0:3], a[52:55], v[84:87]\n" \
"ds_read2_b32 a[78:79], v47 offset0:226 offset1:227\n" \
"v_mul_f32 v250, s20, v250\n" \
"v_mfma_f32_16x16x32_bf16 v[84:87], a[4:7], a[48:51], v[84:87]\n" \
"ds_read_b128 a[216:219], v50 offset:2176\n" \
"v_mul_f32 v251, s20, v251\n" \
"v_mfma_f32_16x16x32_bf16 v[84:87], a[8:11], a[44:47], v[84:87]\n" \
"v_cvt_pk_bf16_f32 v34, v248, v249\n" \
"v_cvt_pk_bf16_f32 v35, v250, v251\n" \
"v_mfma_f32_16x16x32_bf16 v[84:87], a[12:15], a[40:43], v[84:87]\n" \
"ds_read2_b32 a[80:81], v48 offset0:96 offset1:97\n" \
"v_accvgpr_read_b32 v252, a140\n" \
"v_mfma_f32_16x16x32_bf16 v[84:87], a[16:19], a[36:39], v[84:87]\n" \
"ds_read2_b32 a[82:83], v48 offset0:98 offset1:99\n" \
"v_accvgpr_read_b32 v253, a141\n" \
"v_mfma_f32_16x16x32_bf16 v[84:87], a[20:23], a[32:35], v[84:87]\n" \
"ds_read_b128 a[220:223], v50 offset:2688\n" \
"v_accvgpr_read_b32 v254, a142\n" \
"v_mfma_f32_16x16x32_bf16 v[88:91], a[0:3], a[56:59], v[88:91]\n" \
"buffer_store_dwordx4 v[38:41], v4, s[36:39], s96 offen offset:0 sc1\n" \
"v_mfma_f32_16x16x32_bf16 v[88:91], a[4:7], a[52:55], v[88:91]\n" \
"s_add_u32 s97, s96, s98\n" \
"ds_read2_b32 a[84:85], v48 offset0:224 offset1:225\n" \
"v_accvgpr_read_b32 v255, a143\n" \
"v_mfma_f32_16x16x32_bf16 v[88:91], a[8:11], a[48:51], v[88:91]\n" \
"ds_read2_b32 a[86:87], v48 offset0:226 offset1:227\n" \
"v_mul_f32 v252, s20, v252\n" \
"v_mfma_f32_16x16x32_bf16 v[88:91], a[12:15], a[44:47], v[88:91]\n" \
"ds_read_b128 a[224:227], v50 offset:3200\n" \
"v_mul_f32 v253, s20, v253\n" \
"v_mfma_f32_16x16x32_bf16 v[88:91], a[16:19], a[40:43], v[88:91]\n" \
"v_mul_f32 v254, s20, v254\n" \
"v_mul_f32 v255, s20, v255\n" \
"v_mfma_f32_16x16x32_bf16 v[88:91], a[20:23], a[36:39], v[88:91]\n" \
"ds_read2_b32 a[88:89], v49 offset0:96 offset1:97\n" \
"v_cvt_pk_bf16_f32 v36, v252, v253\n" \
"v_mfma_f32_16x16x32_bf16 v[88:91], a[24:27], a[32:35], v[88:91]\n" \
"ds_read2_b32 a[90:91], v49 offset0:98 offset1:99\n" \
"v_cvt_pk_bf16_f32 v37, v254, v255\n" \
"v_mfma_f32_16x16x32_bf16 v[92:95], a[0:3], a[60:63], v[92:95]\n" \
"ds_read_b128 a[228:231], v50 offset:3712\n" \
"v_accvgpr_read_b32 v248, a144\n" \
"v_mfma_f32_16x16x32_bf16 v[92:95], a[4:7], a[56:59], v[92:95]\n" \
"v_accvgpr_read_b32 v249, a145\n" \
"v_accvgpr_read_b32 v250, a146\n" \
"v_mfma_f32_16x16x32_bf16 v[92:95], a[8:11], a[52:55], v[92:95]\n" \
"ds_read2_b32 a[92:93], v49 offset0:224 offset1:225\n" \
"v_accvgpr_read_b32 v251, a147\n" \
"v_mfma_f32_16x16x32_bf16 v[92:95], a[12:15], a[48:51], v[92:95]\n" \
"ds_read2_b32 a[94:95], v49 offset0:226 offset1:227\n" \
"v_mul_f32 v248, s20, v248\n" \
"v_mfma_f32_16x16x32_bf16 v[92:95], a[16:19], a[44:47], v[92:95]\n" \
"buffer_store_dwordx4 v[42:45], v4, s[36:39], s97 offen offset:0 sc1\n" \
"v_mfma_f32_16x16x32_bf16 v[92:95], a[20:23], a[40:43], v[92:95]\n" \
"s_add_u32 s57, s57, s93\n" \
"s_mov_b32 s96, s57\n" \
"v_mul_f32 v249, s20, v249\n" \
"v_mfma_f32_16x16x32_bf16 v[92:95], a[24:27], a[36:39], v[92:95]\n" \
"v_mul_f32 v250, s20, v250\n" \
"v_mul_f32 v251, s20, v251\n" \
"v_mfma_f32_16x16x32_bf16 v[92:95], a[28:31], a[32:35], v[92:95]\n" \
"s_waitcnt lgkmcnt(0)\n" \
"v_cvt_pk_bf16_f32 v38, v248, v249\n" \
"v_cvt_pk_bf16_f32 v39, v250, v251\n" \
"v_mfma_f32_16x16x32_bf16 v[64:67], a[64:67], a[200:203], v[64:67]\n" \
"ds_read_b128 a[32:35], v50 offset:64\n" \
"v_accvgpr_read_b32 v252, a148\n" \
"v_mfma_f32_16x16x32_bf16 v[68:71], a[64:67], a[204:207], v[68:71]\n" \
"v_accvgpr_read_b32 v253, a149\n" \
"v_accvgpr_read_b32 v254, a150\n" \
"v_mfma_f32_16x16x32_bf16 v[68:71], a[68:71], a[200:203], v[68:71]\n" \
"ds_read2_b32 a[0:1], v46 offset0:112 offset1:113\n" \
"v_accvgpr_read_b32 v255, a151\n" \
"v_mfma_f32_16x16x32_bf16 v[72:75], a[64:67], a[208:211], v[72:75]\n" \
"ds_read2_b32 a[2:3], v46 offset0:114 offset1:115\n" \
"v_mul_f32 v252, s20, v252\n" \
"v_mfma_f32_16x16x32_bf16 v[72:75], a[68:71], a[204:207], v[72:75]\n" \
"ds_read_b128 a[36:39], v50 offset:576\n" \
"v_mul_f32 v253, s20, v253\n" \
"v_mfma_f32_16x16x32_bf16 v[72:75], a[72:75], a[200:203], v[72:75]\n" \
"v_mul_f32 v254, s20, v254\n" \
"v_mul_f32 v255, s20, v255\n" \
"v_mfma_f32_16x16x32_bf16 v[76:79], a[64:67], a[212:215], v[76:79]\n" \
"ds_read2_b32 a[4:5], v46 offset0:240 offset1:241\n" \
"v_cvt_pk_bf16_f32 v40, v252, v253\n" \
"v_mfma_f32_16x16x32_bf16 v[76:79], a[68:71], a[208:211], v[76:79]\n" \
"ds_read2_b32 a[6:7], v46 offset0:242 offset1:243\n" \
"v_cvt_pk_bf16_f32 v41, v254, v255\n" \
"v_mfma_f32_16x16x32_bf16 v[76:79], a[72:75], a[204:207], v[76:79]\n" \
"ds_read_b128 a[40:43], v50 offset:1088\n" \
"v_accvgpr_read_b32 v248, a152\n" \
"v_mfma_f32_16x16x32_bf16 v[76:79], a[76:79], a[200:203], v[76:79]\n" \
"buffer_store_dwordx4 v[30:33], v4, s[36:39], s96 offen offset:0 sc1\n" \
"v_mfma_f32_16x16x32_bf16 v[80:83], a[64:67], a[216:219], v[80:83]\n" \
"s_add_u32 s97, s96, s98\n" \
"ds_read2_b32 a[8:9], v47 offset0:112 offset1:113\n" \
"v_accvgpr_read_b32 v249, a153\n" \
"v_mfma_f32_16x16x32_bf16 v[80:83], a[68:71], a[212:215], v[80:83]\n" \
"ds_read2_b32 a[10:11], v47 offset0:114 offset1:115\n" \
"v_accvgpr_read_b32 v250, a154\n" \
"v_mfma_f32_16x16x32_bf16 v[80:83], a[72:75], a[208:211], v[80:83]\n" \
"ds_read_b128 a[44:47], v50 offset:1600\n" \
"v_accvgpr_read_b32 v251, a155\n" \
"v_mfma_f32_16x16x32_bf16 v[80:83], a[76:79], a[204:207], v[80:83]\n" \
"v_mul_f32 v248, s20, v248\n" \
"v_mul_f32 v249, s20, v249\n" \
"v_mfma_f32_16x16x32_bf16 v[80:83], a[80:83], a[200:203], v[80:83]\n" \
"ds_read2_b32 a[12:13], v47 offset0:240 offset1:241\n" \
"v_mul_f32 v250, s20, v250\n" \
"v_mfma_f32_16x16x32_bf16 v[84:87], a[64:67], a[220:223], v[84:87]\n" \
"ds_read2_b32 a[14:15], v47 offset0:242 offset1:243\n" \
"v_mul_f32 v251, s20, v251\n" \
"v_mfma_f32_16x16x32_bf16 v[84:87], a[68:71], a[216:219], v[84:87]\n" \
"ds_read_b128 a[48:51], v50 offset:2112\n" \
"v_cvt_pk_bf16_f32 v42, v248, v249\n" \
"v_mfma_f32_16x16x32_bf16 v[84:87], a[72:75], a[212:215], v[84:87]\n" \
"v_cvt_pk_bf16_f32 v43, v250, v251\n" \
"v_accvgpr_read_b32 v252, a156\n" \
"v_mfma_f32_16x16x32_bf16 v[84:87], a[76:79], a[208:211], v[84:87]\n" \
"ds_read2_b32 a[16:17], v48 offset0:112 offset1:113\n" \
"v_accvgpr_read_b32 v253, a157\n" \
"v_mfma_f32_16x16x32_bf16 v[84:87], a[80:83], a[204:207], v[84:87]\n" \
"ds_read2_b32 a[18:19], v48 offset0:114 offset1:115\n" \
"v_accvgpr_read_b32 v254, a158\n" \
"v_mfma_f32_16x16x32_bf16 v[84:87], a[84:87], a[200:203], v[84:87]\n" \
"ds_read_b128 a[52:55], v50 offset:2624\n" \
"v_accvgpr_read_b32 v255, a159\n" \
"v_mfma_f32_16x16x32_bf16 v[88:91], a[64:67], a[224:227], v[88:91]\n" \
"buffer_store_dwordx4 v[34:37], v4, s[36:39], s97 offen offset:0 sc1\n" \
"v_mfma_f32_16x16x32_bf16 v[88:91], a[68:71], a[220:223], v[88:91]\n" \
"s_add_u32 s96, s96, s92\n" \
"ds_read2_b32 a[20:21], v48 offset0:240 offset1:241\n" \
"v_mul_f32 v252, s20, v252\n" \
"v_mfma_f32_16x16x32_bf16 v[88:91], a[72:75], a[216:219], v[88:91]\n" \
"ds_read2_b32 a[22:23], v48 offset0:242 offset1:243\n" \
"v_mul_f32 v253, s20, v253\n" \
"v_mfma_f32_16x16x32_bf16 v[88:91], a[76:79], a[212:215], v[88:91]\n" \
"ds_read_b128 a[56:59], v50 offset:3136\n" \
"v_mul_f32 v254, s20, v254\n" \
"v_mfma_f32_16x16x32_bf16 v[88:91], a[80:83], a[208:211], v[88:91]\n" \
"v_mul_f32 v255, s20, v255\n" \
"v_cvt_pk_bf16_f32 v44, v252, v253\n" \
"v_mfma_f32_16x16x32_bf16 v[88:91], a[84:87], a[204:207], v[88:91]\n" \
"ds_read2_b32 a[24:25], v49 offset0:112 offset1:113\n" \
"v_cvt_pk_bf16_f32 v45, v254, v255\n" \
"v_mfma_f32_16x16x32_bf16 v[88:91], a[88:91], a[200:203], v[88:91]\n" \
"ds_read2_b32 a[26:27], v49 offset0:114 offset1:115\n" \
"v_accvgpr_read_b32 v248, a160\n" \
"v_mfma_f32_16x16x32_bf16 v[92:95], a[64:67], a[228:231], v[92:95]\n" \
"ds_read_b128 a[60:63], v50 offset:3648\n" \
"v_accvgpr_read_b32 v249, a161\n" \
"v_mfma_f32_16x16x32_bf16 v[92:95], a[68:71], a[224:227], v[92:95]\n" \
"v_accvgpr_read_b32 v250, a162\n" \
"v_accvgpr_read_b32 v251, a163\n" \
"v_mfma_f32_16x16x32_bf16 v[92:95], a[72:75], a[220:223], v[92:95]\n" \
"ds_read2_b32 a[28:29], v49 offset0:240 offset1:241\n" \
"v_mul_f32 v248, s20, v248\n" \
"v_mfma_f32_16x16x32_bf16 v[92:95], a[76:79], a[216:219], v[92:95]\n" \
"ds_read2_b32 a[30:31], v49 offset0:242 offset1:243\n" \
"v_mul_f32 v249, s20, v249\n" \
"v_mfma_f32_16x16x32_bf16 v[92:95], a[80:83], a[212:215], v[92:95]\n" \
"buffer_store_dwordx4 v[38:41], v4, s[36:39], s96 offen offset:0 sc1\n" \
"v_mfma_f32_16x16x32_bf16 v[92:95], a[84:87], a[208:211], v[92:95]\n" \
"s_add_u32 s97, s96, s98\n" \
"v_mul_f32 v250, s20, v250\n" \
"v_mul_f32 v251, s20, v251\n" \
"v_mfma_f32_16x16x32_bf16 v[92:95], a[88:91], a[204:207], v[92:95]\n" \
"v_cvt_pk_bf16_f32 v30, v248, v249\n" \
"v_cvt_pk_bf16_f32 v31, v250, v251\n" \
"v_mfma_f32_16x16x32_bf16 v[92:95], a[92:95], a[200:203], v[92:95]\n" \
"s_waitcnt lgkmcnt(0)\n" \
"v_accvgpr_read_b32 v252, a164\n" \
"v_mfma_f32_16x16x32_bf16 v[64:67], a[0:3], a[32:35], v[64:67]\n" \
"s_lshl_b32 s59, %2, 10\n" \
"ds_read_b64 v[192:193], v51 offset:10816\n" \
"v_and_b32 v243, 0x70, v4\n" \
"v_mfma_f32_16x16x32_bf16 v[68:71], a[0:3], a[36:39], v[68:71]\n" \
"ds_read_b64 v[194:195], v51 offset:11328\n" \
"v_lshrrev_b32 v240, 7, v4\n" \
"v_mfma_f32_16x16x32_bf16 v[68:71], a[4:7], a[32:35], v[68:71]\n" \
"ds_read_b64 v[196:197], v51 offset:11840\n" \
"v_lshl_add_u32 v240, v240, 9, v243\n" \
"v_mfma_f32_16x16x32_bf16 v[72:75], a[0:3], a[40:43], v[72:75]\n" \
"ds_read_b64 v[198:199], v51 offset:12352\n" \
"v_add_u32 v240, s22, v240\n" \
"v_mfma_f32_16x16x32_bf16 v[72:75], a[4:7], a[36:39], v[72:75]\n" \
"ds_read_b64 v[200:201], v51 offset:12864\n" \
"v_and_b32 v243, 0x30, v4\n" \
"v_mfma_f32_16x16x32_bf16 v[72:75], a[8:11], a[32:35], v[72:75]\n" \
"ds_read_b64 v[202:203], v51 offset:13376\n" \
"v_lshl_add_u32 v241, v243, 3, s59\n" \
"v_mfma_f32_16x16x32_bf16 v[76:79], a[0:3], a[44:47], v[76:79]\n" \
"ds_read_b64 v[204:205], v51 offset:13888\n" \
"v_and_b32 v243, 0x200, v4\n" \
"v_mfma_f32_16x16x32_bf16 v[76:79], a[4:7], a[40:43], v[76:79]\n" \
"ds_read_b64 v[206:207], v51 offset:14400\n" \
"v_add_u32 v241, v243, v241\n" \
"v_mfma_f32_16x16x32_bf16 v[76:79], a[8:11], a[36:39], v[76:79]\n" \
"buffer_store_dwordx4 v[42:45], v4, s[36:39], s97 offen offset:0 sc1\n" \
"v_mfma_f32_16x16x32_bf16 v[76:79], a[12:15], a[32:35], v[76:79]\n" \
"s_add_u32 s96, s96, s92\n" \
"v_bfe_u32 v243, v4, 6, 3\n" \
"v_lshl_add_u32 v241, v243, 4, v241\n" \
"v_mfma_f32_16x16x32_bf16 v[80:83], a[0:3], a[48:51], v[80:83]\n" \
"v_and_b32 v243, 0x20, v4\n" \
"v_lshlrev_b32 v243, 1, v243\n" \
"v_mfma_f32_16x16x32_bf16 v[80:83], a[4:7], a[44:47], v[80:83]\n" \
"s_waitcnt vmcnt(40) lgkmcnt(0)\n" \
"v_xor_b32 v241, v243, v241\n" \
"s_lshl_b32 s59, s8, 14\n" \
"v_mfma_f32_16x16x32_bf16 v[80:83], a[8:11], a[40:43], v[80:83]\n" \
"s_barrier\n" \
"ds_read_b64_tr_b16 a[64:65], v9 offset:33280\n" \
"v_and_b32 v208, 0xffff0000, v193\n" \
"v_mfma_f32_16x16x32_bf16 v[80:83], a[12:15], a[36:39], v[80:83]\n" \
"s_add_u32 s59, s59, 67108864\n" \
"ds_read_b64_tr_b16 a[66:67], v10 offset:33280\n" \
"v_lshlrev_b32 v209, 16, v193\n" \
"v_mfma_f32_16x16x32_bf16 v[80:83], a[16:19], a[32:35], v[80:83]\n" \
"s_mov_b32 s94, 0x05040100\n" \
"ds_read_b64_tr_b16 a[68:69], v9 offset:41472\n" \
"v_and_b32 v210, 0xffff0000, v192\n" \
"v_mfma_f32_16x16x32_bf16 v[84:87], a[0:3], a[52:55], v[84:87]\n" \
"s_mov_b32 s95, 0x07060302\n" \
"ds_read_b64_tr_b16 a[70:71], v10 offset:41472\n" \
"v_lshlrev_b32 v211, 16, v192\n" \
"v_mfma_f32_16x16x32_bf16 v[84:87], a[4:7], a[48:51], v[84:87]\n" \
"s_mov_b32 s46, 256\n" \
"s_add_u32 m0, s50, 32768\n" \
"ds_read_b128 a[200:203], v7 offset:0\n" \
"v_mfma_f32_16x16x32_bf16 v[84:87], a[8:11], a[44:47], v[84:87]\n" \
"buffer_load_dwordx4 v13, s[28:31], s46 offen lds\n" \
"v_mfma_f32_16x16x32_bf16 v[84:87], a[12:15], a[40:43], v[84:87]\n" \
"ds_read_b128 a[204:207], v8 offset:0\n" \
"v_fma_f32 v208, v208, v28, v64\n" \
"v_mfma_f32_16x16x32_bf16 v[84:87], a[16:19], a[36:39], v[84:87]\n" \
"ds_read_b128 a[208:211], v7 offset:2048\n" \
"v_fma_f32 v209, v209, v28, v65\n" \
"v_mfma_f32_16x16x32_bf16 v[84:87], a[20:23], a[32:35], v[84:87]\n" \
"ds_read_b128 a[212:215], v8 offset:2048\n" \
"v_fma_f32 v210, v210, v28, v66\n" \
"v_mfma_f32_16x16x32_bf16 v[88:91], a[0:3], a[56:59], v[88:91]\n" \
"ds_read_b128 a[216:219], v7 offset:4096\n" \
"v_fma_f32 v211, v211, v28, v67\n" \
"v_mfma_f32_16x16x32_bf16 v[88:91], a[4:7], a[52:55], v[88:91]\n" \
"s_add_u32 m0, s50, 36864\n" \
"ds_read_b128 a[220:223], v8 offset:4096\n" \
"v_and_b32 v212, 0xffff0000, v195\n" \
"v_mfma_f32_16x16x32_bf16 v[88:91], a[8:11], a[48:51], v[88:91]\n" \
"buffer_load_dwordx4 v14, s[28:31], s46 offen lds\n" \
"v_mfma_f32_16x16x32_bf16 v[88:91], a[12:15], a[44:47], v[88:91]\n" \
"LH9_%=:\ns_cbranch_execz LH13_%=\n" \
"ds_read_b128 a[224:227], v7 offset:6144\n" \
"v_lshlrev_b32 v213, 16, v195\n" \
"v_mfma_f32_16x16x32_bf16 v[88:91], a[16:19], a[40:43], v[88:91]\n" \
"ds_read_b128 a[228:231], v8 offset:6144\n" \
"v_and_b32 v214, 0xffff0000, v194\n" \
"v_mfma_f32_16x16x32_bf16 v[88:91], a[20:23], a[36:39], v[88:91]\n" \
"ds_read_b64_tr_b16 a[72:73], v11 offset:33280\n" \
"v_lshlrev_b32 v215, 16, v194\n" \
"v_mfma_f32_16x16x32_bf16 v[88:91], a[24:27], a[32:35], v[88:91]\n" \
"ds_read_b64_tr_b16 a[74:75], v12 offset:33280\n" \
"v_fma_f32 v212, v212, v28, v68\n" \
"v_mfma_f32_16x16x32_bf16 v[92:95], a[0:3], a[60:63], v[92:95]\n" \
"s_add_u32 m0, s50, 40960\n" \
"ds_read_b64_tr_b16 a[76:77], v11 offset:41472\n" \
"v_fma_f32 v213, v213, v28, v69\n" \
"v_mfma_f32_16x16x32_bf16 v[92:95], a[4:7], a[56:59], v[92:95]\n" \
"buffer_load_dwordx4 v15, s[28:31], s46 offen lds\n" \
"v_mfma_f32_16x16x32_bf16 v[92:95], a[8:11], a[52:55], v[92:95]\n" \
"ds_read_b64_tr_b16 a[78:79], v12 offset:41472\n" \
"v_fma_f32 v214, v214, v28, v70\n" \
"v_mfma_f32_16x16x32_bf16 v[92:95], a[12:15], a[48:51], v[92:95]\n" \
"ds_read_b64_tr_b16 a[80:81], v9 offset:33792\n" \
"v_fma_f32 v215, v215, v28, v71\n" \
"v_mfma_f32_16x16x32_bf16 v[92:95], a[16:19], a[44:47], v[92:95]\n" \
"ds_read_b64_tr_b16 a[82:83], v10 offset:33792\n" \
"v_cvt_pk_bf16_f32 v192, v208, v209\n" \
"v_mfma_f32_16x16x32_bf16 v[92:95], a[20:23], a[40:43], v[92:95]\n" \
"ds_read_b64_tr_b16 a[84:85], v9 offset:41984\n" \
"v_cvt_pk_bf16_f32 v193, v210, v211\n" \
"v_mfma_f32_16x16x32_bf16 v[92:95], a[24:27], a[36:39], v[92:95]\n" \
"ds_read_b64_tr_b16 a[86:87], v10 offset:41984\n" \
"v_cvt_pk_bf16_f32 v194, v212, v213\n" \
"v_mfma_f32_16x16x32_bf16 v[92:95], a[28:31], a[32:35], v[92:95]\n" \
"s_waitcnt lgkmcnt(8)\n" \
"v_cvt_pk_bf16_f32 v195, v214, v215\n" \
"v_and_b32 v216, 0xffff0000, v197\n" \
"v_mfma_f32_16x16x32_bf16 v[96:99], a[64:67], a[200:203], 0\n" \
"s_add_u32 m0, s50, 45056\n" \
"ds_read_b64_tr_b16 a[88:89], v11 offset:33792\n" \
"v_lshlrev_b32 v217, 16, v197\n" \
"v_mfma_f32_16x16x32_bf16 v[96:99], a[68:71], a[204:207], v[96:99]\n" \
"buffer_load_dwordx4 v16, s[28:31], s46 offen lds\n" \
"v_mfma_f32_16x16x32_bf16 v[104:107], a[64:67], a[208:211], 0\n" \
"ds_read_b64_tr_b16 a[90:91], v12 offset:33792\n" \
"v_and_b32 v218, 0xffff0000, v196\n" \
"v_mfma_f32_16x16x32_bf16 v[104:107], a[68:71], a[212:215], v[104:107]\n" \
"ds_read_b64_tr_b16 a[92:93], v11 offset:41984\n" \
"v_lshlrev_b32 v219, 16, v196\n" \
"v_mfma_f32_16x16x32_bf16 v[112:115], a[64:67], a[216:219], 0\n" \
"s_mov_b32 s46, 256\n" \
"s_add_u32 m0, s50, 49152\n" \
"ds_read_b64_tr_b16 a[94:95], v12 offset:41984\n" \
"v_mfma_f32_16x16x32_bf16 v[112:115], a[68:71], a[220:223], v[112:115]\n" \
"buffer_load_dwordx4 v17, s[28:31], s46 offen lds\n" \
"v_mfma_f32_16x16x32_bf16 v[120:123], a[64:67], a[224:227], 0\n" \
"s_waitcnt lgkmcnt(8)\n" \
"ds_read_b64_tr_b16 a[96:97], v9 offset:34304\n" \
"v_fma_f32 v216, v216, v28, v72\n" \
"v_mfma_f32_16x16x32_bf16 v[120:123], a[68:71], a[228:231], v[120:123]\n" \
"ds_read_b64_tr_b16 a[98:99], v10 offset:34304\n" \
"v_fma_f32 v217, v217, v28, v73\n" \
"v_mfma_f32_16x16x32_bf16 v[100:103], a[72:75], a[200:203], 0\n" \
"s_add_u32 m0, s50, 53248\n" \
"ds_read_b64_tr_b16 a[100:101], v9 offset:42496\n" \
"v_fma_f32 v218, v218, v28, v74\n" \
"v_mfma_f32_16x16x32_bf16 v[100:103], a[76:79], a[204:207], v[100:103]\n" \
"buffer_load_dwordx4 v18, s[28:31], s46 offen lds\n" \
"v_mfma_f32_16x16x32_bf16 v[108:111], a[72:75], a[208:211], 0\n" \
"ds_read_b64_tr_b16 a[102:103], v10 offset:42496\n" \
"v_fma_f32 v219, v219, v28, v75\n" \
"v_mfma_f32_16x16x32_bf16 v[108:111], a[76:79], a[212:215], v[108:111]\n" \
"ds_read_b64_tr_b16 a[104:105], v11 offset:34304\n" \
"v_and_b32 v220, 0xffff0000, v199\n" \
"v_mfma_f32_16x16x32_bf16 v[116:119], a[72:75], a[216:219], 0\n" \
"s_add_u32 m0, s50, 57344\n" \
"ds_read_b64_tr_b16 a[106:107], v12 offset:34304\n" \
"v_lshlrev_b32 v221, 16, v199\n" \
"v_mfma_f32_16x16x32_bf16 v[116:119], a[76:79], a[220:223], v[116:119]\n" \
"buffer_load_dwordx4 v19, s[28:31], s46 offen lds\n" \
"v_mfma_f32_16x16x32_bf16 v[124:127], a[72:75], a[224:227], 0\n" \
"s_waitcnt lgkmcnt(10)\n" \
"ds_read_b64_tr_b16 a[108:109], v11 offset:42496\n" \
"v_and_b32 v222, 0xffff0000, v198\n" \
"v_mfma_f32_16x16x32_bf16 v[124:127], a[76:79], a[228:231], v[124:127]\n" \
"ds_read_b64_tr_b16 a[110:111], v12 offset:42496\n" \
"v_lshlrev_b32 v223, 16, v198\n" \
"v_mfma_f32_16x16x32_bf16 v[128:131], a[80:83], a[200:203], 0\n" \
"s_add_u32 m0, s50, 61440\n" \
"ds_read_b64_tr_b16 a[64:65], v9 offset:32768\n" \
"v_fma_f32 v220, v220, v28, v76\n" \
"v_mfma_f32_16x16x32_bf16 v[128:131], a[84:87], a[204:207], v[128:131]\n" \
"buffer_load_dwordx4 v20, s[28:31], s46 offen lds\n" \
"v_mfma_f32_16x16x32_bf16 v[136:139], a[80:83], a[208:211], 0\n" \
"ds_read_b64_tr_b16 a[66:67], v10 offset:32768\n" \
"v_fma_f32 v221, v221, v28, v77\n" \
"v_mfma_f32_16x16x32_bf16 v[136:139], a[84:87], a[212:215], v[136:139]\n" \
"ds_read_b64_tr_b16 a[68:69], v9 offset:40960\n" \
"v_fma_f32 v222, v222, v28, v78\n" \
"v_mfma_f32_16x16x32_bf16 v[144:147], a[80:83], a[216:219], 0\n" \
"s_mov_b32 s47, 1048576\n" \
"s_add_u32 m0, s51, 0\n" \
"ds_read_b64_tr_b16 a[70:71], v10 offset:40960\n" \
"v_mfma_f32_16x16x32_bf16 v[144:147], a[84:87], a[220:223], v[144:147]\n" \
"buffer_load_dwordx4 v21, s[32:35], s47 offen offset:0 lds\n" \
"v_mfma_f32_16x16x32_bf16 v[152:155], a[80:83], a[224:227], 0\n" \
"s_waitcnt lgkmcnt(12)\n" \
"ds_read_b64_tr_b16 a[72:73], v11 offset:32768\n" \
"v_fma_f32 v223, v223, v28, v79\n" \
"v_mfma_f32_16x16x32_bf16 v[152:155], a[84:87], a[228:231], v[152:155]\n" \
"ds_read_b64_tr_b16 a[74:75], v12 offset:32768\n" \
"v_and_b32 v224, 0xffff0000, v201\n" \
"v_mfma_f32_16x16x32_bf16 v[132:135], a[88:91], a[200:203], 0\n" \
"s_add_u32 m0, s51, 896\n" \
"ds_read_b64_tr_b16 a[76:77], v11 offset:40960\n" \
"v_lshlrev_b32 v225, 16, v201\n" \
"v_mfma_f32_16x16x32_bf16 v[132:135], a[92:95], a[204:207], v[132:135]\n" \
"buffer_load_dwordx4 v21, s[32:35], s47 offen offset:128 lds\n" \
"v_mfma_f32_16x16x32_bf16 v[140:143], a[88:91], a[208:211], 0\n" \
"ds_read_b64_tr_b16 a[78:79], v12 offset:40960\n" \
"v_and_b32 v226, 0xffff0000, v200\n" \
"v_mfma_f32_16x16x32_bf16 v[140:143], a[92:95], a[212:215], v[140:143]\n" \
"ds_write2st64_b64 v55, v[192:193], v[194:195] offset0:32 offset1:33\n" \
"v_lshlrev_b32 v227, 16, v200\n" \
"v_mfma_f32_16x16x32_bf16 v[148:151], a[88:91], a[216:219], 0\n" \
"s_mov_b32 s47, 1310720\n" \
"s_add_u32 m0, s51, 8192\n" \
"v_fma_f32 v224, v224, v28, v80\n" \
"v_mfma_f32_16x16x32_bf16 v[148:151], a[92:95], a[220:223], v[148:151]\n" \
"buffer_load_dwordx4 v21, s[32:35], s47 offen offset:0 lds\n" \
"v_mfma_f32_16x16x32_bf16 v[156:159], a[88:91], a[224:227], 0\n" \
"s_waitcnt vmcnt(31) lgkmcnt(0)\n" \
"v_fma_f32 v225, v225, v28, v81\n" \
"v_fma_f32 v226, v226, v28, v82\n" \
"v_mfma_f32_16x16x32_bf16 v[156:159], a[92:95], a[228:231], v[156:159]\n" \
"s_barrier\n" \
"v_fma_f32 v227, v227, v28, v83\n" \
"v_and_b32 v228, 0xffff0000, v203\n" \
"v_mfma_f32_16x16x32_bf16 v[160:163], a[96:99], a[200:203], 0\n" \
"s_add_u32 m0, s51, 9088\n" \
"ds_read_b64_tr_b16 a[80:81], v9 offset:49664\n" \
"v_lshlrev_b32 v229, 16, v203\n" \
"v_mfma_f32_16x16x32_bf16 v[160:163], a[100:103], a[204:207], v[160:163]\n" \
"buffer_load_dwordx4 v21, s[32:35], s47 offen offset:128 lds\n" \
"v_mfma_f32_16x16x32_bf16 v[168:171], a[96:99], a[208:211], 0\n" \
"ds_read_b64_tr_b16 a[82:83], v10 offset:49664\n" \
"v_and_b32 v230, 0xffff0000, v202\n" \
"v_mfma_f32_16x16x32_bf16 v[168:171], a[100:103], a[212:215], v[168:171]\n" \
"ds_read_b64_tr_b16 a[84:85], v9 offset:57856\n" \
"v_lshlrev_b32 v231, 16, v202\n" \
"v_mfma_f32_16x16x32_bf16 v[176:179], a[96:99], a[216:219], 0\n" \
"ds_read_b64_tr_b16 a[86:87], v10 offset:57856\n" \
"v_fma_f32 v228, v228, v28, v84\n" \
"v_mfma_f32_16x16x32_bf16 v[176:179], a[100:103], a[220:223], v[176:179]\n" \
"s_mov_b32 s46, 384\n" \
"s_add_u32 m0, s50, 65536\n" \
"ds_read_b128 a[0:3], v5 offset:0\n" \
"v_mfma_f32_16x16x32_bf16 v[184:187], a[96:99], a[224:227], 0\n" \
"buffer_load_dwordx4 v13, s[28:31], s46 offen lds\n" \
"v_mfma_f32_16x16x32_bf16 v[184:187], a[100:103], a[228:231], v[184:187]\n" \
"ds_read_b128 a[4:7], v6 offset:0\n" \
"v_fma_f32 v229, v229, v28, v85\n" \
"v_mfma_f32_16x16x32_bf16 v[164:167], a[104:107], a[200:203], 0\n" \
"ds_read_b128 a[8:11], v5 offset:2048\n" \
"v_fma_f32 v230, v230, v28, v86\n" \
"v_mfma_f32_16x16x32_bf16 v[164:167], a[108:111], a[204:207], v[164:167]\n" \
"ds_read_b128 a[12:15], v6 offset:2048\n" \
"v_fma_f32 v231, v231, v28, v87\n" \
"v_mfma_f32_16x16x32_bf16 v[172:175], a[104:107], a[208:211], 0\n" \
"s_add_u32 m0, s50, 69632\n" \
"ds_read_b128 a[16:19], v5 offset:4096\n" \
"v_and_b32 v232, 0xffff0000, v205\n" \
"v_mfma_f32_16x16x32_bf16 v[172:175], a[108:111], a[212:215], v[172:175]\n" \
"buffer_load_dwordx4 v14, s[28:31], s46 offen lds\n" \
"v_lshlrev_b32 v233, 16, v205\n" \
"v_and_b32 v234, 0xffff0000, v204\n" \
"v_lshlrev_b32 v235, 16, v204\n" \
"v_fma_f32 v232, v232, v28, v88\n" \
"v_fma_f32 v233, v233, v28, v89\n" \
"v_fma_f32 v234, v234, v28, v90\n" \
"v_fma_f32 v235, v235, v28, v91\n" \
"v_and_b32 v236, 0xffff0000, v207\n" \
"v_lshlrev_b32 v237, 16, v207\n" \
"v_and_b32 v238, 0xffff0000, v206\n" \
"v_lshlrev_b32 v239, 16, v206\n" \
"v_fma_f32 v236, v236, v28, v92\n" \
"v_fma_f32 v237, v237, v28, v93\n" \
"v_fma_f32 v238, v238, v28, v94\n" \
"v_fma_f32 v239, v239, v28, v95\n" \
"v_mfma_f32_16x16x32_bf16 v[180:183], a[104:107], a[216:219], 0\n" \
"ds_read_b128 a[20:23], v6 offset:4096\n" \
"v_cvt_pk_bf16_f32 v196, v216, v217\n" \
"v_mfma_f32_16x16x32_bf16 v[180:183], a[108:111], a[220:223], v[180:183]\n" \
"ds_read_b128 a[24:27], v5 offset:6144\n" \
"v_cvt_pk_bf16_f32 v197, v218, v219\n" \
"v_cvt_pk_bf16_f32 v198, v220, v221\n" \
"v_cvt_pk_bf16_f32 v199, v222, v223\n" \
"v_mfma_f32_16x16x32_bf16 v[188:191], a[104:107], a[224:227], 0\n" \
"ds_write2st64_b64 v55, v[196:197], v[198:199] offset0:34 offset1:35\n" \
"v_cvt_pk_bf16_f32 v200, v224, v225\n" \
"v_mfma_f32_16x16x32_bf16 v[188:191], a[108:111], a[228:231], v[188:191]\n" \
"s_add_u32 m0, s50, 73728\n" \
"ds_read_b128 a[28:31], v6 offset:6144\n" \
"v_cvt_pk_bf16_f32 v201, v226, v227\n" \
"v_mfma_f32_16x16x32_bf16 v[64:67], a[64:67], a[200:203], 0\n" \
"buffer_load_dwordx4 v15, s[28:31], s46 offen lds\n" \
"v_mfma_f32_16x16x32_bf16 v[64:67], a[68:71], a[204:207], v[64:67]\n" \
"ds_read_b64_tr_b16 a[88:89], v11 offset:49664\n" \
"v_cvt_pk_bf16_f32 v202, v228, v229\n" \
"v_mfma_f32_16x16x32_bf16 v[72:75], a[64:67], a[208:211], 0\n" \
"ds_read_b64_tr_b16 a[90:91], v12 offset:49664\n" \
"v_cvt_pk_bf16_f32 v203, v230, v231\n" \
"v_mfma_f32_16x16x32_bf16 v[72:75], a[68:71], a[212:215], v[72:75]\n" \
"ds_read_b64_tr_b16 a[92:93], v11 offset:57856\n" \
"v_cvt_pk_bf16_f32 v204, v232, v233\n" \
"v_mfma_f32_16x16x32_bf16 v[80:83], a[64:67], a[216:219], 0\n" \
"s_add_u32 m0, s50, 77824\n" \
"ds_write2st64_b64 v55, v[200:201], v[202:203] offset0:36 offset1:37\n" \
"v_cvt_pk_bf16_f32 v205, v234, v235\n" \
"v_mfma_f32_16x16x32_bf16 v[80:83], a[68:71], a[220:223], v[80:83]\n" \
"buffer_load_dwordx4 v16, s[28:31], s46 offen lds\n" \
"v_mfma_f32_16x16x32_bf16 v[88:91], a[64:67], a[224:227], 0\n" \
"ds_read_b64_tr_b16 a[94:95], v12 offset:57856\n" \
"v_cvt_pk_bf16_f32 v206, v236, v237\n" \
"v_mfma_f32_16x16x32_bf16 v[88:91], a[68:71], a[228:231], v[88:91]\n" \
"ds_read_b64_tr_b16 a[96:97], v9 offset:50176\n" \
"v_cvt_pk_bf16_f32 v207, v238, v239\n" \
"v_mfma_f32_16x16x32_bf16 v[68:71], a[72:75], a[200:203], 0\n" \
"ds_read_b64_tr_b16 a[98:99], v10 offset:50176\n" \
"v_accvgpr_read_b32 v253, a165\n" \
"v_mfma_f32_16x16x32_bf16 v[68:71], a[76:79], a[204:207], v[68:71]\n" \
"ds_read_b64_tr_b16 a[100:101], v9 offset:58368\n" \
"v_accvgpr_read_b32 v254, a166\n" \
"v_mfma_f32_16x16x32_bf16 v[76:79], a[72:75], a[208:211], 0\n" \
"ds_write2st64_b64 v55, v[204:205], v[206:207] offset0:38 offset1:39\n" \
"v_accvgpr_read_b32 v255, a167\n" \
"v_mfma_f32_16x16x32_bf16 v[76:79], a[76:79], a[212:215], v[76:79]\n" \
"ds_read_b64_tr_b16 a[102:103], v10 offset:58368\n" \
"v_mul_f32 v252, s20, v252\n" \
"v_mfma_f32_16x16x32_bf16 v[84:87], a[72:75], a[216:219], 0\n" \
"ds_read_b64_tr_b16 a[104:105], v11 offset:50176\n" \
"v_mul_f32 v253, s20, v253\n" \
"v_mfma_f32_16x16x32_bf16 v[84:87], a[76:79], a[220:223], v[84:87]\n" \
"ds_read_b64_tr_b16 a[106:107], v12 offset:50176\n" \
"v_mul_f32 v254, s20, v254\n" \
"v_mfma_f32_16x16x32_bf16 v[92:95], a[72:75], a[224:227], 0\n" \
"ds_read_b64_tr_b16 a[108:109], v11 offset:58368\n" \
"v_mul_f32 v255, s20, v255\n" \
"v_mfma_f32_16x16x32_bf16 v[92:95], a[76:79], a[228:231], v[92:95]\n" \
"s_waitcnt lgkmcnt(13)\n" \
"ds_read_b64_tr_b16 a[110:111], v12 offset:58368\n" \
"s_waitcnt lgkmcnt(5)\n" \
"s_barrier\n" \
"v_mfma_f32_16x16x32_bf16 v[96:99], a[80:83], a[0:3], v[96:99]\n" \
"s_mov_b32 s46, 384\n" \
"s_add_u32 m0, s50, 81920\n" \
"ds_read_b64_tr_b16 a[64:65], v9 offset:50688\n" \
"v_mfma_f32_16x16x32_bf16 v[96:99], a[84:87], a[4:7], v[96:99]\n" \
"buffer_load_dwordx4 v17, s[28:31], s46 offen lds\n" \
"v_mfma_f32_16x16x32_bf16 v[104:107], a[80:83], a[8:11], v[104:107]\n" \
"ds_read_b64_tr_b16 a[66:67], v10 offset:50688\n" \
"v_cvt_pk_bf16_f32 v32, v252, v253\n" \
"v_mfma_f32_16x16x32_bf16 v[104:107], a[84:87], a[12:15], v[104:107]\n" \
"ds_read_b64_tr_b16 a[68:69], v9 offset:58880\n" \
"v_cvt_pk_bf16_f32 v33, v254, v255\n" \
"v_mfma_f32_16x16x32_bf16 v[112:115], a[80:83], a[16:19], v[112:115]\n" \
"s_add_u32 m0, s50, 86016\n" \
"ds_read_b64_tr_b16 a[70:71], v10 offset:58880\n" \
"v_accvgpr_read_b32 v248, a168\n" \
"v_mfma_f32_16x16x32_bf16 v[112:115], a[84:87], a[20:23], v[112:115]\n" \
"buffer_load_dwordx4 v18, s[28:31], s46 offen lds\n" \
"v_mfma_f32_16x16x32_bf16 v[120:123], a[80:83], a[24:27], v[120:123]\n" \
"s_waitcnt lgkmcnt(13)\n" \
"ds_read_b64_tr_b16 a[72:73], v11 offset:50688\n" \
"v_accvgpr_read_b32 v249, a169\n" \
"v_mfma_f32_16x16x32_bf16 v[120:123], a[84:87], a[28:31], v[120:123]\n" \
"ds_read_b64_tr_b16 a[74:75], v12 offset:50688\n" \
"v_accvgpr_read_b32 v250, a170\n" \
"v_mfma_f32_16x16x32_bf16 v[100:103], a[88:91], a[0:3], v[100:103]\n" \
"s_add_u32 m0, s50, 90112\n" \
"ds_read_b64_tr_b16 a[76:77], v11 offset:58880\n" \
"v_accvgpr_read_b32 v251, a171\n" \
"v_mfma_f32_16x16x32_bf16 v[100:103], a[92:95], a[4:7], v[100:103]\n" \
"buffer_load_dwordx4 v19, s[28:31], s46 offen lds\n" \
"v_mfma_f32_16x16x32_bf16 v[108:111], a[88:91], a[8:11], v[108:111]\n" \
"ds_read_b64_tr_b16 a[78:79], v12 offset:58880\n" \
"v_mul_f32 v248, s20, v248\n" \
"v_mfma_f32_16x16x32_bf16 v[108:111], a[92:95], a[12:15], v[108:111]\n" \
"ds_read_b64_tr_b16 a[80:81], v9 offset:49152\n" \
"v_mul_f32 v249, s20, v249\n" \
"v_mfma_f32_16x16x32_bf16 v[116:119], a[88:91], a[16:19], v[116:119]\n" \
"s_add_u32 m0, s50, 94208\n" \
"ds_read_b64_tr_b16 a[82:83], v10 offset:49152\n" \
"v_mul_f32 v250, s20, v250\n" \
"v_mfma_f32_16x16x32_bf16 v[116:119], a[92:95], a[20:23], v[116:119]\n" \
"buffer_load_dwordx4 v20, s[28:31], s46 offen lds\n" \
"v_mfma_f32_16x16x32_bf16 v[124:127], a[88:91], a[24:27], v[124:127]\n" \
"s_waitcnt lgkmcnt(14)\n" \
"ds_read_b64_tr_b16 a[84:85], v9 offset:57344\n" \
"v_mul_f32 v251, s20, v251\n" \
"v_mfma_f32_16x16x32_bf16 v[124:127], a[92:95], a[28:31], v[124:127]\n" \
"ds_read_b64_tr_b16 a[86:87], v10 offset:57344\n" \
"v_cvt_pk_bf16_f32 v34, v248, v249\n" \
"v_mfma_f32_16x16x32_bf16 v[128:131], a[96:99], a[0:3], v[128:131]\n" \
"s_mov_b32 s47, 2097152\n" \
"s_add_u32 m0, s51, 32768\n" \
"ds_read_b64_tr_b16 a[88:89], v11 offset:49152\n" \
"v_mfma_f32_16x16x32_bf16 v[128:131], a[100:103], a[4:7], v[128:131]\n" \
"buffer_load_dwordx4 v21, s[32:35], s47 offen offset:0 lds\n" \
"v_mfma_f32_16x16x32_bf16 v[136:139], a[96:99], a[8:11], v[136:139]\n" \
"ds_read_b64_tr_b16 a[90:91], v12 offset:49152\n" \
"v_cvt_pk_bf16_f32 v35, v250, v251\n" \
"v_mfma_f32_16x16x32_bf16 v[136:139], a[100:103], a[12:15], v[136:139]\n" \
"ds_read_b64_tr_b16 a[92:93], v11 offset:57344\n" \
"v_accvgpr_read_b32 v252, a172\n" \
"v_mfma_f32_16x16x32_bf16 v[144:147], a[96:99], a[16:19], v[144:147]\n" \
"s_add_u32 m0, s51, 33664\n" \
"ds_read_b64_tr_b16 a[94:95], v12 offset:57344\n" \
"v_accvgpr_read_b32 v253, a173\n" \
"v_mfma_f32_16x16x32_bf16 v[144:147], a[100:103], a[20:23], v[144:147]\n" \
"buffer_load_dwordx4 v21, s[32:35], s47 offen offset:128 lds\n" \
"v_mfma_f32_16x16x32_bf16 v[152:155], a[96:99], a[24:27], v[152:155]\n" \
"s_waitcnt lgkmcnt(15)\n" \
"ds_read_b128 v[208:211], v241 offset:16384\n" \
"v_accvgpr_read_b32 v254, a174\n" \
"v_mfma_f32_16x16x32_bf16 v[152:155], a[100:103], a[28:31], v[152:155]\n" \
"buffer_store_dwordx4 v[30:33], v4, s[36:39], s96 offen offset:0 sc1\n" \
"v_mfma_f32_16x16x32_bf16 v[132:135], a[104:107], a[0:3], v[132:135]\n" \
"s_mov_b32 s47, 2359296\n" \
"s_add_u32 m0, s51, 40960\n" \
"s_add_u32 s97, s96, s98\n" \
"v_mfma_f32_16x16x32_bf16 v[132:135], a[108:111], a[4:7], v[132:135]\n" \
"buffer_load_dwordx4 v21, s[32:35], s47 offen offset:0 lds\n" \
"v_mfma_f32_16x16x32_bf16 v[140:143], a[104:107], a[8:11], v[140:143]\n" \
"ds_read_b128 v[212:215], v241 offset:20480\n" \
"v_accvgpr_read_b32 v255, a175\n" \
"v_mfma_f32_16x16x32_bf16 v[140:143], a[108:111], a[12:15], v[140:143]\n" \
"ds_read_b128 v[216:219], v241 offset:24576\n" \
"ds_read_b128 v[220:223], v241 offset:28672\n" \
"v_mfma_f32_16x16x32_bf16 v[148:151], a[104:107], a[16:19], v[148:151]\n" \
"s_add_u32 m0, s51, 41856\n" \
"v_mul_f32 v252, s20, v252\n" \
"v_mul_f32 v253, s20, v253\n" \
"v_mfma_f32_16x16x32_bf16 v[148:151], a[108:111], a[20:23], v[148:151]\n" \
"buffer_load_dwordx4 v21, s[32:35], s47 offen offset:128 lds\n" \
"v_mfma_f32_16x16x32_bf16 v[156:159], a[104:107], a[24:27], v[156:159]\n" \
"s_waitcnt vmcnt(13) lgkmcnt(0)\n" \
"v_mul_f32 v254, s20, v254\n" \
"v_mul_f32 v255, s20, v255\n" \
"v_mfma_f32_16x16x32_bf16 v[156:159], a[108:111], a[28:31], v[156:159]\n" \
"s_barrier\n" \
"v_cvt_pk_bf16_f32 v36, v252, v253\n" \
"v_cvt_pk_bf16_f32 v37, v254, v255\n" \
"v_mfma_f32_16x16x32_bf16 v[160:163], a[64:67], a[0:3], v[160:163]\n" \
"s_mov_b32 s47, 1572864\n" \
"s_add_u32 m0, s51, 16384\n" \
"ds_read_b64_tr_b16 a[96:97], v9 offset:512\n" \
"v_mfma_f32_16x16x32_bf16 v[160:163], a[68:71], a[4:7], v[160:163]\n" \
"buffer_load_dwordx4 v21, s[32:35], s47 offen offset:0 lds\n" \
"v_mfma_f32_16x16x32_bf16 v[168:171], a[64:67], a[8:11], v[168:171]\n" \
"ds_read_b64_tr_b16 a[98:99], v10 offset:512\n" \
"v_perm_b32 v224, v212, v208, s94\n" \
"v_mfma_f32_16x16x32_bf16 v[168:171], a[68:71], a[12:15], v[168:171]\n" \
"ds_read_b64_tr_b16 a[100:101], v9 offset:8704\n" \
"v_perm_b32 v225, v220, v216, s94\n" \
"v_mfma_f32_16x16x32_bf16 v[176:179], a[64:67], a[16:19], v[176:179]\n" \
"ds_read_b64_tr_b16 a[102:103], v10 offset:8704\n" \
"v_perm_b32 v226, v214, v210, s94\n" \
"v_mfma_f32_16x16x32_bf16 v[176:179], a[68:71], a[20:23], v[176:179]\n" \
"s_add_u32 m0, s51, 17280\n" \
"ds_read_b128 a[200:203], v5 offset:32768\n" \
"v_perm_b32 v227, v222, v218, s94\n" \
"v_mfma_f32_16x16x32_bf16 v[184:187], a[64:67], a[24:27], v[184:187]\n" \
"buffer_load_dwordx4 v21, s[32:35], s47 offen offset:128 lds\n" \
"v_mfma_f32_16x16x32_bf16 v[184:187], a[68:71], a[28:31], v[184:187]\n" \
"ds_read_b128 a[204:207], v6 offset:32768\n" \
"v_perm_b32 v228, v212, v208, s95\n" \
"v_mfma_f32_16x16x32_bf16 v[164:167], a[72:75], a[0:3], v[164:167]\n" \
"ds_read_b128 a[208:211], v5 offset:34816\n" \
"v_perm_b32 v229, v220, v216, s95\n" \
"v_mfma_f32_16x16x32_bf16 v[164:167], a[76:79], a[4:7], v[164:167]\n" \
"ds_read_b128 a[212:215], v6 offset:34816\n" \
"v_perm_b32 v230, v214, v210, s95\n" \
"v_mfma_f32_16x16x32_bf16 v[172:175], a[72:75], a[8:11], v[172:175]\n" \
"s_mov_b32 s47, 1835008\n" \
"s_add_u32 m0, s51, 24576\n" \
"ds_read_b128 a[216:219], v5 offset:36864\n" \
"v_mfma_f32_16x16x32_bf16 v[172:175], a[76:79], a[12:15], v[172:175]\n" \
"buffer_load_dwordx4 v21, s[32:35], s47 offen offset:0 lds\n" \
"v_mfma_f32_16x16x32_bf16 v[180:183], a[72:75], a[16:19], v[180:183]\n" \
"ds_read_b128 a[220:223], v6 offset:36864\n" \
"v_perm_b32 v231, v222, v218, s95\n" \
"v_mfma_f32_16x16x32_bf16 v[180:183], a[76:79], a[20:23], v[180:183]\n" \
"ds_read_b128 a[224:227], v5 offset:38912\n" \
"v_perm_b32 v232, v213, v209, s94\n" \
"v_mfma_f32_16x16x32_bf16 v[188:191], a[72:75], a[24:27], v[188:191]\n" \
"ds_read_b128 a[228:231], v6 offset:38912\n" \
"v_perm_b32 v233, v221, v217, s94\n" \
"v_mfma_f32_16x16x32_bf16 v[188:191], a[76:79], a[28:31], v[188:191]\n" \
"s_add_u32 m0, s51, 25472\n" \
"ds_read_b64_tr_b16 a[104:105], v11 offset:512\n" \
"v_perm_b32 v234, v215, v211, s94\n" \
"v_mfma_f32_16x16x32_bf16 v[64:67], a[80:83], a[0:3], v[64:67]\n" \
"buffer_load_dwordx4 v21, s[32:35], s47 offen offset:128 lds\n" \
"v_mfma_f32_16x16x32_bf16 v[64:67], a[84:87], a[4:7], v[64:67]\n" \
"ds_read_b64_tr_b16 a[106:107], v12 offset:512\n" \
"v_perm_b32 v235, v223, v219, s94\n" \
"v_mfma_f32_16x16x32_bf16 v[72:75], a[80:83], a[8:11], v[72:75]\n" \
"ds_read_b64_tr_b16 a[108:109], v11 offset:8704\n" \
"v_perm_b32 v236, v213, v209, s95\n" \
"v_mfma_f32_16x16x32_bf16 v[72:75], a[84:87], a[12:15], v[72:75]\n" \
"ds_read_b64_tr_b16 a[110:111], v12 offset:8704\n" \
"v_perm_b32 v237, v221, v217, s95\n" \
"v_mfma_f32_16x16x32_bf16 v[80:83], a[80:83], a[16:19], v[80:83]\n" \
"s_mov_b32 s46, 512\n" \
"s_add_u32 m0, s50, 0\n" \
"ds_read_b64_tr_b16 a[64:65], v9 offset:1024\n" \
"v_mfma_f32_16x16x32_bf16 v[80:83], a[84:87], a[20:23], v[80:83]\n" \
"buffer_load_dwordx4 v13, s[28:31], s46 offen lds\n" \
"v_mfma_f32_16x16x32_bf16 v[88:91], a[80:83], a[24:27], v[88:91]\n" \
"ds_read_b64_tr_b16 a[66:67], v10 offset:1024\n" \
"v_perm_b32 v238, v215, v211, s95\n" \
"v_mfma_f32_16x16x32_bf16 v[88:91], a[84:87], a[28:31], v[88:91]\n" \
"ds_read_b64_tr_b16 a[68:69], v9 offset:9216\n" \
"v_perm_b32 v239, v223, v219, s95\n" \
"v_mfma_f32_16x16x32_bf16 v[68:71], a[88:91], a[0:3], v[68:71]\n" \
"ds_read_b64_tr_b16 a[70:71], v10 offset:9216\n" \
"v_accvgpr_read_b32 v248, a176\n" \
"v_mfma_f32_16x16x32_bf16 v[68:71], a[92:95], a[4:7], v[68:71]\n" \
"s_add_u32 m0, s50, 4096\n" \
"ds_read_b64_tr_b16 a[72:73], v11 offset:1024\n" \
"v_accvgpr_read_b32 v249, a177\n" \
"v_mfma_f32_16x16x32_bf16 v[76:79], a[88:91], a[8:11], v[76:79]\n" \
"buffer_load_dwordx4 v14, s[28:31], s46 offen lds\n" \
"v_mfma_f32_16x16x32_bf16 v[76:79], a[92:95], a[12:15], v[76:79]\n" \
"ds_read_b64_tr_b16 a[74:75], v12 offset:1024\n" \
"v_accvgpr_read_b32 v250, a178\n" \
"v_mfma_f32_16x16x32_bf16 v[84:87], a[88:91], a[16:19], v[84:87]\n" \
"ds_read_b64_tr_b16 a[76:77], v11 offset:9216\n" \
"v_accvgpr_read_b32 v251, a179\n" \
"v_mfma_f32_16x16x32_bf16 v[84:87], a[92:95], a[20:23], v[84:87]\n" \
"ds_read_b64_tr_b16 a[78:79], v12 offset:9216\n" \
"v_mul_f32 v248, s20, v248\n" \
"v_mfma_f32_16x16x32_bf16 v[92:95], a[88:91], a[24:27], v[92:95]\n" \
"buffer_store_dwordx4 v[224:227], v240, s[36:39], s59 offen offset:0 sc1\n" \
"v_mfma_f32_16x16x32_bf16 v[92:95], a[92:95], a[28:31], v[92:95]\n" \
"s_waitcnt lgkmcnt(12)\n" \
"v_mul_f32 v249, s20, v249\n" \
"v_mul_f32 v250, s20, v250\n" \
"v_mfma_f32_16x16x32_bf16 v[96:99], a[96:99], a[200:203], v[96:99]\n" \
"s_add_u32 m0, s50, 8192\n" \
"ds_read_b64_tr_b16 a[80:81], v9 offset:1536\n" \
"v_mul_f32 v251, s20, v251\n" \
"v_mfma_f32_16x16x32_bf16 v[96:99], a[100:103], a[204:207], v[96:99]\n" \
"buffer_load_dwordx4 v15, s[28:31], s46 offen lds\n" \
"v_mfma_f32_16x16x32_bf16 v[104:107], a[96:99], a[208:211], v[104:107]\n" \
"ds_read_b64_tr_b16 a[82:83], v10 offset:1536\n" \
"v_cvt_pk_bf16_f32 v38, v248, v249\n" \
"v_mfma_f32_16x16x32_bf16 v[104:107], a[100:103], a[212:215], v[104:107]\n" \
"ds_read_b64_tr_b16 a[84:85], v9 offset:9728\n" \
"v_cvt_pk_bf16_f32 v39, v250, v251\n" \
"v_mfma_f32_16x16x32_bf16 v[112:115], a[96:99], a[216:219], v[112:115]\n" \
"s_add_u32 m0, s50, 12288\n" \
"ds_read_b64_tr_b16 a[86:87], v10 offset:9728\n" \
"v_accvgpr_read_b32 v252, a180\n" \
"v_mfma_f32_16x16x32_bf16 v[112:115], a[100:103], a[220:223], v[112:115]\n" \
"buffer_load_dwordx4 v16, s[28:31], s46 offen lds\n" \
"v_mfma_f32_16x16x32_bf16 v[120:123], a[96:99], a[224:227], v[120:123]\n" \
"s_waitcnt lgkmcnt(12)\n" \
"ds_read_b64_tr_b16 a[88:89], v11 offset:1536\n" \
"v_accvgpr_read_b32 v253, a181\n" \
"v_mfma_f32_16x16x32_bf16 v[120:123], a[100:103], a[228:231], v[120:123]\n" \
"ds_read_b64_tr_b16 a[90:91], v12 offset:1536\n" \
"v_accvgpr_read_b32 v254, a182\n" \
"v_mfma_f32_16x16x32_bf16 v[100:103], a[104:107], a[200:203], v[100:103]\n" \
"s_mov_b32 s46, 512\n" \
"s_add_u32 m0, s50, 16384\n" \
"ds_read_b64_tr_b16 a[92:93], v11 offset:9728\n" \
"v_mfma_f32_16x16x32_bf16 v[100:103], a[108:111], a[204:207], v[100:103]\n" \
"buffer_load_dwordx4 v17, s[28:31], s46 offen lds\n" \
"v_mfma_f32_16x16x32_bf16 v[108:111], a[104:107], a[208:211], v[108:111]\n" \
"ds_read_b64_tr_b16 a[94:95], v12 offset:9728\n" \
"v_accvgpr_read_b32 v255, a183\n" \
"v_mfma_f32_16x16x32_bf16 v[108:111], a[108:111], a[212:215], v[108:111]\n" \
"ds_read_b64_tr_b16 a[96:97], v9 offset:0\n" \
"v_mul_f32 v252, s20, v252\n" \
"v_mfma_f32_16x16x32_bf16 v[116:119], a[104:107], a[216:219], v[116:119]\n" \
"s_add_u32 m0, s50, 20480\n" \
"ds_read_b64_tr_b16 a[98:99], v10 offset:0\n" \
"v_mul_f32 v253, s20, v253\n" \
"v_mfma_f32_16x16x32_bf16 v[116:119], a[108:111], a[220:223], v[116:119]\n" \
"buffer_load_dwordx4 v18, s[28:31], s46 offen lds\n" \
"v_mfma_f32_16x16x32_bf16 v[124:127], a[104:107], a[224:227], v[124:127]\n" \
"s_waitcnt lgkmcnt(14)\n" \
"ds_read_b64_tr_b16 a[100:101], v9 offset:8192\n" \
"v_mul_f32 v254, s20, v254\n" \
"v_mfma_f32_16x16x32_bf16 v[124:127], a[108:111], a[228:231], v[124:127]\n" \
"ds_read_b64_tr_b16 a[102:103], v10 offset:8192\n" \
"v_mul_f32 v255, s20, v255\n" \
"v_mfma_f32_16x16x32_bf16 v[128:131], a[64:67], a[200:203], v[128:131]\n" \
"s_add_u32 m0, s50, 24576\n" \
"ds_read_b64_tr_b16 a[104:105], v11 offset:0\n" \
"v_cvt_pk_bf16_f32 v40, v252, v253\n" \
"v_mfma_f32_16x16x32_bf16 v[128:131], a[68:71], a[204:207], v[128:131]\n" \
"buffer_load_dwordx4 v19, s[28:31], s46 offen lds\n" \
"v_mfma_f32_16x16x32_bf16 v[136:139], a[64:67], a[208:211], v[136:139]\n" \
"ds_read_b64_tr_b16 a[106:107], v12 offset:0\n" \
"v_cvt_pk_bf16_f32 v41, v254, v255\n" \
"v_mfma_f32_16x16x32_bf16 v[136:139], a[68:71], a[212:215], v[136:139]\n" \
"ds_read_b64_tr_b16 a[108:109], v11 offset:8192\n" \
"v_accvgpr_read_b32 v248, a184\n" \
"v_mfma_f32_16x16x32_bf16 v[144:147], a[64:67], a[216:219], v[144:147]\n" \
"s_add_u32 m0, s50, 28672\n" \
"ds_read_b64_tr_b16 a[110:111], v12 offset:8192\n" \
"v_accvgpr_read_b32 v249, a185\n" \
"v_mfma_f32_16x16x32_bf16 v[144:147], a[68:71], a[220:223], v[144:147]\n" \
"buffer_load_dwordx4 v20, s[28:31], s46 offen lds\n" \
"v_mfma_f32_16x16x32_bf16 v[152:155], a[64:67], a[224:227], v[152:155]\n" \
"s_waitcnt lgkmcnt(15)\n" \
"v_accvgpr_read_b32 v250, a186\n" \
"v_accvgpr_read_b32 v251, a187\n" \
"v_mfma_f32_16x16x32_bf16 v[152:155], a[68:71], a[228:231], v[152:155]\n" \
"v_mul_f32 v248, s20, v248\n" \
"v_mul_f32 v249, s20, v249\n" \
"v_mfma_f32_16x16x32_bf16 v[132:135], a[72:75], a[200:203], v[132:135]\n" \
"s_mov_b32 s47, 2621440\n" \
"s_add_u32 m0, s51, 49152\n" \
"v_mul_f32 v250, s20, v250\n" \
"v_mfma_f32_16x16x32_bf16 v[132:135], a[76:79], a[204:207], v[132:135]\n" \
"buffer_load_dwordx4 v21, s[32:35], s47 offen offset:0 lds\n" \
"v_mfma_f32_16x16x32_bf16 v[140:143], a[72:75], a[208:211], v[140:143]\n" \
"v_mul_f32 v251, s20, v251\n" \
"v_cvt_pk_bf16_f32 v42, v248, v249\n" \
"v_mfma_f32_16x16x32_bf16 v[140:143], a[76:79], a[212:215], v[140:143]\n" \
"v_cvt_pk_bf16_f32 v43, v250, v251\n" \
"v_accvgpr_read_b32 v252, a188\n" \
"v_mfma_f32_16x16x32_bf16 v[148:151], a[72:75], a[216:219], v[148:151]\n" \
"s_add_u32 m0, s51, 50048\n" \
"v_accvgpr_read_b32 v253, a189\n" \
"v_mfma_f32_16x16x32_bf16 v[148:151], a[76:79], a[220:223], v[148:151]\n" \
"buffer_load_dwordx4 v21, s[32:35], s47 offen offset:128 lds\n" \
"v_mfma_f32_16x16x32_bf16 v[156:159], a[72:75], a[224:227], v[156:159]\n" \
"LH10_%=:\ns_cbranch_execz LH14_%=\n" \
"s_waitcnt vmcnt(11) lgkmcnt(0)\n" \
"v_accvgpr_read_b32 v254, a190\n" \
"v_accvgpr_read_b32 v255, a191\n" \
"v_mfma_f32_16x16x32_bf16 v[156:159], a[76:79], a[228:231], v[156:159]\n" \
"s_barrier\n" \
"v_mul_f32 v252, s20, v252\n" \
"v_mul_f32 v253, s20, v253\n" \
"v_mfma_f32_16x16x32_bf16 v[160:163], a[80:83], a[200:203], v[160:163]\n" \
"s_mov_b32 s47, 2883584\n" \
"s_add_u32 m0, s51, 57344\n" \
"ds_read_b64_tr_b16 a[64:65], v9 offset:16896\n" \
"v_mfma_f32_16x16x32_bf16 v[160:163], a[84:87], a[204:207], v[160:163]\n" \
"buffer_load_dwordx4 v21, s[32:35], s47 offen offset:0 lds\n" \
"v_mfma_f32_16x16x32_bf16 v[168:171], a[80:83], a[208:211], v[168:171]\n" \
"ds_read_b64_tr_b16 a[66:67], v10 offset:16896\n" \
"v_mul_f32 v254, s20, v254\n" \
"v_mfma_f32_16x16x32_bf16 v[168:171], a[84:87], a[212:215], v[168:171]\n" \
"ds_read_b64_tr_b16 a[68:69], v9 offset:25088\n" \
"v_mul_f32 v255, s20, v255\n" \
"v_mfma_f32_16x16x32_bf16 v[176:179], a[80:83], a[216:219], v[176:179]\n" \
"ds_read_b64_tr_b16 a[70:71], v10 offset:25088\n" \
"v_cvt_pk_bf16_f32 v44, v252, v253\n" \
"v_mfma_f32_16x16x32_bf16 v[176:179], a[84:87], a[220:223], v[176:179]\n" \
"s_add_u32 m0, s51, 58240\n" \
"ds_read_b128 a[0:3], v7 offset:0\n" \
"v_cvt_pk_bf16_f32 v45, v254, v255\n" \
"v_mfma_f32_16x16x32_bf16 v[184:187], a[80:83], a[224:227], v[184:187]\n" \
"buffer_load_dwordx4 v21, s[32:35], s47 offen offset:128 lds\n" \
"v_mfma_f32_16x16x32_bf16 v[184:187], a[84:87], a[228:231], v[184:187]\n" \
"ds_read_b128 a[4:7], v8 offset:0\n" \
"v_mfma_f32_16x16x32_bf16 v[164:167], a[88:91], a[200:203], v[164:167]\n" \
"ds_read_b128 a[8:11], v7 offset:2048\n" \
"v_mfma_f32_16x16x32_bf16 v[164:167], a[92:95], a[204:207], v[164:167]\n" \
"ds_read_b128 a[12:15], v8 offset:2048\n" \
"v_mfma_f32_16x16x32_bf16 v[172:175], a[88:91], a[208:211], v[172:175]\n" \
"s_mov_b32 s46, 640\n" \
"s_add_u32 m0, s50, 32768\n" \
"ds_read_b128 a[16:19], v7 offset:4096\n" \
"v_mfma_f32_16x16x32_bf16 v[172:175], a[92:95], a[212:215], v[172:175]\n" \
"buffer_load_dwordx4 v13, s[28:31], s46 offen lds\n" \
"v_mfma_f32_16x16x32_bf16 v[180:183], a[88:91], a[216:219], v[180:183]\n" \
"ds_read_b128 a[20:23], v8 offset:4096\n" \
"v_mfma_f32_16x16x32_bf16 v[180:183], a[92:95], a[220:223], v[180:183]\n" \
"ds_read_b128 a[24:27], v7 offset:6144\n" \
"v_mfma_f32_16x16x32_bf16 v[188:191], a[88:91], a[224:227], v[188:191]\n" \
"ds_read_b128 a[28:31], v8 offset:6144\n" \
"v_mfma_f32_16x16x32_bf16 v[188:191], a[92:95], a[228:231], v[188:191]\n" \
"s_add_u32 m0, s50, 36864\n" \
"ds_read_b64_tr_b16 a[72:73], v11 offset:16896\n" \
"v_mfma_f32_16x16x32_bf16 v[64:67], a[96:99], a[200:203], v[64:67]\n" \
"buffer_load_dwordx4 v14, s[28:31], s46 offen lds\n" \
"v_mfma_f32_16x16x32_bf16 v[64:67], a[100:103], a[204:207], v[64:67]\n" \
"ds_read_b64_tr_b16 a[74:75], v12 offset:16896\n" \
"v_mfma_f32_16x16x32_bf16 v[72:75], a[96:99], a[208:211], v[72:75]\n" \
"ds_read_b64_tr_b16 a[76:77], v11 offset:25088\n" \
"v_mfma_f32_16x16x32_bf16 v[72:75], a[100:103], a[212:215], v[72:75]\n" \
"ds_read_b64_tr_b16 a[78:79], v12 offset:25088\n" \
"v_mfma_f32_16x16x32_bf16 v[80:83], a[96:99], a[216:219], v[80:83]\n" \
"s_add_u32 m0, s50, 40960\n" \
"ds_read_b64_tr_b16 a[80:81], v9 offset:17408\n" \
"v_mfma_f32_16x16x32_bf16 v[80:83], a[100:103], a[220:223], v[80:83]\n" \
"buffer_load_dwordx4 v15, s[28:31], s46 offen lds\n" \
"v_mfma_f32_16x16x32_bf16 v[88:91], a[96:99], a[224:227], v[88:91]\n" \
"ds_read_b64_tr_b16 a[82:83], v10 offset:17408\n" \
"v_mfma_f32_16x16x32_bf16 v[88:91], a[100:103], a[228:231], v[88:91]\n" \
"ds_read_b64_tr_b16 a[84:85], v9 offset:25600\n" \
"v_mfma_f32_16x16x32_bf16 v[68:71], a[104:107], a[200:203], v[68:71]\n" \
"ds_read_b64_tr_b16 a[86:87], v10 offset:25600\n" \
"v_mfma_f32_16x16x32_bf16 v[68:71], a[108:111], a[204:207], v[68:71]\n" \
"s_add_u32 m0, s50, 45056\n" \
"ds_read_b64_tr_b16 a[88:89], v11 offset:17408\n" \
"v_mfma_f32_16x16x32_bf16 v[76:79], a[104:107], a[208:211], v[76:79]\n" \
"buffer_load_dwordx4 v16, s[28:31], s46 offen lds\n" \
"v_mfma_f32_16x16x32_bf16 v[76:79], a[108:111], a[212:215], v[76:79]\n" \
"ds_read_b64_tr_b16 a[90:91], v12 offset:17408\n" \
"v_mfma_f32_16x16x32_bf16 v[84:87], a[104:107], a[216:219], v[84:87]\n" \
"ds_read_b64_tr_b16 a[92:93], v11 offset:25600\n" \
"v_mfma_f32_16x16x32_bf16 v[84:87], a[108:111], a[220:223], v[84:87]\n" \
"ds_read_b64_tr_b16 a[94:95], v12 offset:25600\n" \
"v_mfma_f32_16x16x32_bf16 v[92:95], a[104:107], a[224:227], v[92:95]\n" \
"buffer_store_dwordx4 v[228:231], v240, s[36:39], s59 offen offset:128 sc1\n" \
"v_mfma_f32_16x16x32_bf16 v[92:95], a[108:111], a[228:231], v[92:95]\n" \
"s_waitcnt lgkmcnt(12)\n" \
"v_mfma_f32_16x16x32_bf16 v[96:99], a[64:67], a[0:3], v[96:99]\n" \
"s_mov_b32 s46, 640\n" \
"s_add_u32 m0, s50, 49152\n" \
"ds_read_b64_tr_b16 a[96:97], v9 offset:17920\n" \
"v_mfma_f32_16x16x32_bf16 v[96:99], a[68:71], a[4:7], v[96:99]\n" \
"buffer_load_dwordx4 v17, s[28:31], s46 offen lds\n" \
"v_mfma_f32_16x16x32_bf16 v[104:107], a[64:67], a[8:11], v[104:107]\n" \
"ds_read_b64_tr_b16 a[98:99], v10 offset:17920\n" \
"s_waitcnt vmcnt(63)\n" \
"v_mfma_f32_16x16x32_bf16 v[104:107], a[68:71], a[12:15], v[104:107]\n" \
"ds_read_b64_tr_b16 a[100:101], v9 offset:26112\n" \
"s_load_dwordx2 s[100:101], %0, 0x50\n" \
"v_mfma_f32_16x16x32_bf16 v[112:115], a[64:67], a[16:19], v[112:115]\n" \
"s_add_u32 m0, s50, 53248\n" \
"ds_read_b64_tr_b16 a[102:103], v10 offset:26112\n" \
"s_lshr_b32 s64, %1, 3\n" \
"v_mfma_f32_16x16x32_bf16 v[112:115], a[68:71], a[20:23], v[112:115]\n" \
"buffer_load_dwordx4 v18, s[28:31], s46 offen lds\n" \
"v_mfma_f32_16x16x32_bf16 v[120:123], a[64:67], a[24:27], v[120:123]\n" \
"s_waitcnt lgkmcnt(12)\n" \
"ds_read_b64_tr_b16 a[104:105], v11 offset:17920\n" \
"s_lshl_b32 s64, s64, 2\n" \
"v_mfma_f32_16x16x32_bf16 v[120:123], a[68:71], a[28:31], v[120:123]\n" \
"s_add_u32 s64, s64, %2\n" \
"v_mfma_f32_16x16x32_bf16 v[24:27], a[240:243], a[192:195], 0\n" \
"v_mfma_f32_16x16x32_bf16 v[100:103], a[72:75], a[0:3], v[100:103]\n" \
"s_add_u32 m0, s50, 57344\n" \
"ds_read_b64_tr_b16 a[106:107], v12 offset:17920\n" \
"s_lshl_b32 s64, s64, 11\n" \
"v_mfma_f32_16x16x32_bf16 v[100:103], a[76:79], a[4:7], v[100:103]\n" \
"buffer_load_dwordx4 v19, s[28:31], s46 offen lds\n" \
"v_mfma_f32_16x16x32_bf16 v[108:111], a[72:75], a[8:11], v[108:111]\n" \
"s_bfe_u32 s65, %1, 0x20001\n" \
"ds_read_b64_tr_b16 a[108:109], v11 offset:26112\n" \
"s_lshl_b32 s65, s65, 21\n" \
"v_mfma_f32_16x16x32_bf16 v[108:111], a[76:79], a[12:15], v[108:111]\n" \
"s_add_u32 s64, s64, s65\n" \
"ds_read_b64_tr_b16 a[110:111], v12 offset:26112\n" \
"v_mfma_f32_16x16x32_bf16 v[116:119], a[72:75], a[16:19], v[116:119]\n" \
"s_add_u32 m0, s50, 61440\n" \
"ds_read_b64_tr_b16 a[64:65], v9 offset:16384\n" \
"v_mfma_f32_16x16x32_bf16 v[116:119], a[76:79], a[20:23], v[116:119]\n" \
"buffer_load_dwordx4 v20, s[28:31], s46 offen lds\n" \
"v_mfma_f32_16x16x32_bf16 v[124:127], a[72:75], a[24:27], v[124:127]\n" \
"s_waitcnt lgkmcnt(13)\n" \
"ds_read_b64_tr_b16 a[66:67], v10 offset:16384\n" \
"v_mfma_f32_16x16x32_bf16 v[124:127], a[76:79], a[28:31], v[124:127]\n" \
"ds_read_b64_tr_b16 a[68:69], v9 offset:24576\n" \
"v_mfma_f32_16x16x32_bf16 v[128:131], a[80:83], a[0:3], v[128:131]\n" \
"s_mov_b32 s47, 3145728\n" \
"s_add_u32 m0, s51, 0\n" \
"ds_read_b64_tr_b16 a[70:71], v10 offset:24576\n" \
"v_mfma_f32_16x16x32_bf16 v[128:131], a[84:87], a[4:7], v[128:131]\n" \
"buffer_load_dwordx4 v21, s[32:35], s47 offen offset:0 lds\n" \
"v_mfma_f32_16x16x32_bf16 v[136:139], a[80:83], a[8:11], v[136:139]\n" \
"ds_read_b64_tr_b16 a[72:73], v11 offset:16384\n" \
"v_mfma_f32_16x16x32_bf16 v[136:139], a[84:87], a[12:15], v[136:139]\n" \
"s_mov_b32 s66, 0x100001\n" \
"v_mfma_f32_16x16x32_bf16 v[24:27], a[244:247], a[196:199], v[24:27]\n" \
"v_mfma_f32_16x16x32_bf16 v[144:147], a[80:83], a[16:19], v[144:147]\n" \
"s_add_u32 m0, s51, 896\n" \
"ds_read_b64_tr_b16 a[74:75], v12 offset:16384\n" \
"s_mov_b32 s67, 0x10000100\n" \
"v_mfma_f32_16x16x32_bf16 v[144:147], a[84:87], a[20:23], v[144:147]\n" \
"buffer_load_dwordx4 v21, s[32:35], s47 offen offset:128 lds\n" \
"v_mfma_f32_16x16x32_bf16 v[152:155], a[80:83], a[24:27], v[152:155]\n" \
"s_waitcnt lgkmcnt(14)\n" \
"ds_read_b64_tr_b16 a[76:77], v11 offset:24576\n" \
"v_cndmask_b32 v23, 0, v24, s[66:67]\n" \
"v_mfma_f32_16x16x32_bf16 v[152:155], a[84:87], a[28:31], v[152:155]\n" \
"s_mov_b32 s66, 0x200002\n" \
"ds_read_b64_tr_b16 a[78:79], v12 offset:24576\n" \
"s_mov_b32 s67, 0x20000200\n" \
"v_mfma_f32_16x16x32_bf16 v[132:135], a[88:91], a[0:3], v[132:135]\n" \
"s_mov_b32 s47, 3407872\n" \
"s_add_u32 m0, s51, 8192\n" \
"v_cndmask_b32 v23, v23, v25, s[66:67]\n" \
"v_mfma_f32_16x16x32_bf16 v[132:135], a[92:95], a[4:7], v[132:135]\n" \
"buffer_load_dwordx4 v21, s[32:35], s47 offen offset:0 lds\n" \
"v_mfma_f32_16x16x32_bf16 v[140:143], a[88:91], a[8:11], v[140:143]\n" \
"s_mov_b32 s66, 0x400004\n" \
"s_mov_b32 s67, 0x40000400\n" \
"v_cndmask_b32 v23, v23, v26, s[66:67]\n" \
"v_mfma_f32_16x16x32_bf16 v[140:143], a[92:95], a[12:15], v[140:143]\n" \
"s_mov_b32 s66, 0x800008\n" \
"s_mov_b32 s67, 0x80000800\n" \
"v_cndmask_b32 v23, v23, v27, s[66:67]\n" \
"v_mfma_f32_16x16x32_bf16 v[148:151], a[88:91], a[16:19], v[148:151]\n" \
"s_add_u32 m0, s51, 9088\n" \
"v_mfma_f32_16x16x32_bf16 v[24:27], a[248:251], a[192:195], 0\n" \
"v_mfma_f32_16x16x32_bf16 v[148:151], a[92:95], a[20:23], v[148:151]\n" \
"buffer_load_dwordx4 v21, s[32:35], s47 offen offset:128 lds\n" \
"v_mfma_f32_16x16x32_bf16 v[156:159], a[88:91], a[24:27], v[156:159]\n" \
"s_waitcnt vmcnt(17) lgkmcnt(0)\n" \
"v_mfma_f32_16x16x32_bf16 v[24:27], a[252:255], a[196:199], v[24:27]\n" \
"v_mfma_f32_16x16x32_bf16 v[156:159], a[92:95], a[28:31], v[156:159]\n" \
"s_barrier\n" \
"s_mov_b32 s66, 0x100001\n" \
"s_mov_b32 s67, 0x10000100\n" \
"v_mfma_f32_16x16x32_bf16 v[160:163], a[96:99], a[0:3], v[160:163]\n" \
"ds_read_b64_tr_b16 a[80:81], v9 offset:33280\n" \
"v_mfma_f32_16x16x32_bf16 v[160:163], a[100:103], a[4:7], v[160:163]\n" \
"ds_read_b64_tr_b16 a[82:83], v10 offset:33280\n" \
"v_cndmask_b32 v54, 0, v24, s[66:67]\n" \
"v_mfma_f32_16x16x32_bf16 v[168:171], a[96:99], a[8:11], v[168:171]\n" \
"s_mov_b32 s46, 768\n" \
"s_add_u32 m0, s50, 65536\n" \
"ds_read_b64_tr_b16 a[84:85], v9 offset:41472\n" \
"v_mfma_f32_16x16x32_bf16 v[168:171], a[100:103], a[12:15], v[168:171]\n" \
"buffer_load_dwordx4 v13, s[28:31], s46 offen lds\n" \
"v_mfma_f32_16x16x32_bf16 v[176:179], a[96:99], a[16:19], v[176:179]\n" \
"s_mov_b32 s66, 0x200002\n" \
"ds_read_b64_tr_b16 a[86:87], v10 offset:41472\n" \
"s_mov_b32 s67, 0x20000200\n" \
"v_mfma_f32_16x16x32_bf16 v[176:179], a[100:103], a[20:23], v[176:179]\n" \
"ds_read_b128 a[200:203], v5 offset:0\n" \
"v_cndmask_b32 v54, v54, v25, s[66:67]\n" \
"v_mfma_f32_16x16x32_bf16 v[184:187], a[96:99], a[24:27], v[184:187]\n" \
"s_mov_b32 s66, 0x400004\n" \
"ds_read_b128 a[204:207], v6 offset:0\n" \
"s_mov_b32 s67, 0x40000400\n" \
"v_mfma_f32_16x16x32_bf16 v[184:187], a[100:103], a[28:31], v[184:187]\n" \
"ds_read_b128 a[208:211], v5 offset:2048\n" \
"v_cndmask_b32 v54, v54, v26, s[66:67]\n" \
"v_mfma_f32_16x16x32_bf16 v[164:167], a[104:107], a[0:3], v[164:167]\n" \
"s_mov_b32 s66, 0x800008\n" \
"ds_read_b128 a[212:215], v6 offset:2048\n" \
"s_mov_b32 s67, 0x80000800\n" \
"v_mfma_f32_16x16x32_bf16 v[164:167], a[108:111], a[4:7], v[164:167]\n" \
"ds_read_b128 a[216:219], v5 offset:4096\n" \
"v_cndmask_b32 v54, v54, v27, s[66:67]\n" \
"v_mfma_f32_16x16x32_bf16 v[172:175], a[104:107], a[8:11], v[172:175]\n" \
"s_add_u32 m0, s50, 69632\n" \
"ds_read_b128 a[220:223], v6 offset:4096\n" \
"v_mfma_f32_16x16x32_bf16 v[172:175], a[108:111], a[12:15], v[172:175]\n" \
"buffer_load_dwordx4 v14, s[28:31], s46 offen lds\n" \
"v_mfma_f32_16x16x32_bf16 v[180:183], a[104:107], a[16:19], v[180:183]\n" \
"ds_read_b128 a[224:227], v5 offset:6144\n" \
"v_permlane32_swap_b32 v23, v54\n" \
"v_mfma_f32_16x16x32_bf16 v[180:183], a[108:111], a[20:23], v[180:183]\n" \
"ds_read_b128 a[228:231], v6 offset:6144\n" \
"v_add_f32 v23, v23, v54\n" \
"v_mfma_f32_16x16x32_bf16 v[188:191], a[104:107], a[24:27], v[188:191]\n" \
"ds_read_b64_tr_b16 a[88:89], v11 offset:33280\n" \
"v_mov_b32 v54, v23\n" \
"v_mfma_f32_16x16x32_bf16 v[188:191], a[108:111], a[28:31], v[188:191]\n" \
"ds_read_b64_tr_b16 a[90:91], v12 offset:33280\n" \
"v_mfma_f32_16x16x32_bf16 v[64:67], a[64:67], a[0:3], v[64:67]\n" \
"ds_read_b64_tr_b16 a[92:93], v11 offset:41472\n" \
"v_permlane16_swap_b32 v23, v54\n" \
"v_mfma_f32_16x16x32_bf16 v[64:67], a[68:71], a[4:7], v[64:67]\n" \
"ds_read_b64_tr_b16 a[94:95], v12 offset:41472\n" \
"v_add_f32 v23, v23, v54\n" \
"v_mfma_f32_16x16x32_bf16 v[72:75], a[64:67], a[8:11], v[72:75]\n" \
"s_add_u32 m0, s50, 73728\n" \
"ds_read_b64_tr_b16 a[96:97], v9 offset:33792\n" \
"v_mfma_f32_16x16x32_bf16 v[72:75], a[68:71], a[12:15], v[72:75]\n" \
"buffer_load_dwordx4 v15, s[28:31], s46 offen lds\n" \
"v_mfma_f32_16x16x32_bf16 v[80:83], a[64:67], a[16:19], v[80:83]\n" \
"ds_read_b64_tr_b16 a[98:99], v10 offset:33792\n" \
"v_add_f32 v23, v23, v23 row_ror:8 row_mask:0xf bank_mask:0xf\n" \
"v_mfma_f32_16x16x32_bf16 v[80:83], a[68:71], a[20:23], v[80:83]\n" \
"ds_read_b64_tr_b16 a[100:101], v9 offset:41984\n" \
"v_mfma_f32_16x16x32_bf16 v[88:91], a[64:67], a[24:27], v[88:91]\n" \
"ds_read_b64_tr_b16 a[102:103], v10 offset:41984\n" \
"v_add_f32 v23, v23, v23 row_ror:4 row_mask:0xf bank_mask:0xf\n" \
"v_mfma_f32_16x16x32_bf16 v[88:91], a[68:71], a[28:31], v[88:91]\n" \
"ds_read_b64_tr_b16 a[104:105], v11 offset:33792\n" \
"v_mfma_f32_16x16x32_bf16 v[68:71], a[72:75], a[0:3], v[68:71]\n" \
"ds_read_b64_tr_b16 a[106:107], v12 offset:33792\n" \
"v_add_f32 v23, v23, v23 row_ror:2 row_mask:0xf bank_mask:0xf\n" \
"v_mfma_f32_16x16x32_bf16 v[68:71], a[76:79], a[4:7], v[68:71]\n" \
"ds_read_b64_tr_b16 a[108:109], v11 offset:41984\n" \
"v_mfma_f32_16x16x32_bf16 v[76:79], a[72:75], a[8:11], v[76:79]\n" \
"s_add_u32 m0, s50, 77824\n" \
"ds_read_b64_tr_b16 a[110:111], v12 offset:41984\n" \
"v_add_f32 v23, v23, v23 row_ror:1 row_mask:0xf bank_mask:0xf\n" \
"v_mfma_f32_16x16x32_bf16 v[76:79], a[76:79], a[12:15], v[76:79]\n" \
"buffer_load_dwordx4 v16, s[28:31], s46 offen lds\n" \
"v_mfma_f32_16x16x32_bf16 v[84:87], a[72:75], a[16:19], v[84:87]\n" \
"v_cvt_pk_bf16_f32 v23, v23, v23\n" \
"v_and_b32 v23, 0xffff0000, v23\n" \
"v_mfma_f32_16x16x32_bf16 v[84:87], a[76:79], a[20:23], v[84:87]\n" \
"buffer_store_dwordx4 v[232:235], v240, s[36:39], s59 offen offset:256 sc1\n" \
"v_mfma_f32_16x16x32_bf16 v[92:95], a[72:75], a[24:27], v[92:95]\n" \
"v_mul_f32 v23, 0xbfb8aa3b, v23\n" \
"v_mfma_f32_16x16x32_bf16 v[92:95], a[76:79], a[28:31], v[92:95]\n" \
"s_waitcnt lgkmcnt(12)\n" \
"v_exp_f32 v23, v23\n" \
"v_mfma_f32_16x16x32_bf16 v[96:99], a[80:83], a[200:203], v[96:99]\n" \
"s_mov_b32 s46, 768\n" \
"s_add_u32 m0, s50, 81920\n" \
"ds_read_b64_tr_b16 a[64:65], v9 offset:34304\n" \
"v_mfma_f32_16x16x32_bf16 v[96:99], a[84:87], a[204:207], v[96:99]\n" \
"buffer_load_dwordx4 v17, s[28:31], s46 offen lds\n" \
"v_mfma_f32_16x16x32_bf16 v[104:107], a[80:83], a[208:211], v[104:107]\n" \
"ds_read_b64_tr_b16 a[66:67], v10 offset:34304\n" \
"v_add_f32 v23, 1.0, v23\n" \
"v_mfma_f32_16x16x32_bf16 v[104:107], a[84:87], a[212:215], v[104:107]\n" \
"s_add_u32 s100, s100, s64\ns_addc_u32 s101, s101, 0\n" \
"ds_read_b64_tr_b16 a[68:69], v9 offset:42496\n" \
"v_mfma_f32_16x16x32_bf16 v[112:115], a[80:83], a[216:219], v[112:115]\n" \
"s_add_u32 m0, s50, 86016\n" \
"ds_read_b64_tr_b16 a[70:71], v10 offset:42496\n" \
"v_mfma_f32_16x16x32_bf16 v[112:115], a[84:87], a[220:223], v[112:115]\n" \
"buffer_load_dwordx4 v18, s[28:31], s46 offen lds\n" \
"v_mfma_f32_16x16x32_bf16 v[120:123], a[80:83], a[224:227], v[120:123]\n" \
"s_waitcnt lgkmcnt(12)\n" \
"ds_read_b64_tr_b16 a[72:73], v11 offset:34304\n" \
"v_mfma_f32_16x16x32_bf16 v[120:123], a[84:87], a[228:231], v[120:123]\n" \
"v_rcp_f32 v23, v23\n" \
"v_mfma_f32_16x16x32_bf16 v[100:103], a[88:91], a[200:203], v[100:103]\n" \
"s_add_u32 m0, s50, 90112\n" \
"ds_read_b64_tr_b16 a[74:75], v12 offset:34304\n" \
"v_mfma_f32_16x16x32_bf16 v[100:103], a[92:95], a[204:207], v[100:103]\n" \
"buffer_load_dwordx4 v19, s[28:31], s46 offen lds\n" \
"v_mfma_f32_16x16x32_bf16 v[108:111], a[88:91], a[208:211], v[108:111]\n" \
"ds_read_b64_tr_b16 a[76:77], v11 offset:42496\n" \
"v_cvt_pk_bf16_f32 v23, v23, v23\n" \
"v_mfma_f32_16x16x32_bf16 v[108:111], a[92:95], a[212:215], v[108:111]\n" \
"ds_read_b64_tr_b16 a[78:79], v12 offset:42496\n" \
"v_and_b32 v23, 0xffff0000, v23\n" \
"v_mfma_f32_16x16x32_bf16 v[116:119], a[88:91], a[216:219], v[116:119]\n" \
"s_add_u32 m0, s50, 94208\n" \
"ds_read_b64_tr_b16 a[80:81], v9 offset:32768\n" \
"v_mfma_f32_16x16x32_bf16 v[116:119], a[92:95], a[220:223], v[116:119]\n" \
"buffer_load_dwordx4 v20, s[28:31], s46 offen lds\n" \
"v_mfma_f32_16x16x32_bf16 v[124:127], a[88:91], a[224:227], v[124:127]\n" \
"s_waitcnt lgkmcnt(13)\n" \
"ds_read_b64_tr_b16 a[82:83], v10 offset:32768\n" \
"v_mfma_f32_16x16x32_bf16 v[124:127], a[92:95], a[228:231], v[124:127]\n" \
"ds_read_b64_tr_b16 a[84:85], v9 offset:40960\n" \
"v_mfma_f32_16x16x32_bf16 v[128:131], a[96:99], a[200:203], v[128:131]\n" \
"s_mov_b32 s47, 3670016\n" \
"s_add_u32 m0, s51, 16384\n" \
"ds_read_b64_tr_b16 a[86:87], v10 offset:40960\n" \
"v_mfma_f32_16x16x32_bf16 v[128:131], a[100:103], a[204:207], v[128:131]\n" \
"buffer_load_dwordx4 v21, s[32:35], s47 offen offset:0 lds\n" \
"v_mfma_f32_16x16x32_bf16 v[136:139], a[96:99], a[208:211], v[136:139]\n" \
"ds_read_b64_tr_b16 a[88:89], v11 offset:32768\n" \
"v_mfma_f32_16x16x32_bf16 v[136:139], a[100:103], a[212:215], v[136:139]\n" \
"buffer_store_dword v23, v61, s[36:39], 0 offen sc1\n" \
"v_mfma_f32_16x16x32_bf16 v[144:147], a[96:99], a[216:219], v[144:147]\n" \
"s_add_u32 m0, s51, 17280\n" \
"ds_read_b64_tr_b16 a[90:91], v12 offset:32768\n" \
"v_mfma_f32_16x16x32_bf16 v[144:147], a[100:103], a[220:223], v[144:147]\n" \
"buffer_load_dwordx4 v21, s[32:35], s47 offen offset:128 lds\n" \
"v_mfma_f32_16x16x32_bf16 v[152:155], a[96:99], a[224:227], v[152:155]\n" \
"s_waitcnt lgkmcnt(14)\n" \
"ds_read_b64_tr_b16 a[92:93], v11 offset:40960\n" \
"v_mfma_f32_16x16x32_bf16 v[152:155], a[100:103], a[228:231], v[152:155]\n" \
"ds_read_b64_tr_b16 a[94:95], v12 offset:40960\n" \
"v_mfma_f32_16x16x32_bf16 v[132:135], a[104:107], a[200:203], v[132:135]\n" \
"s_mov_b32 s47, 3932160\n" \
"s_add_u32 m0, s51, 24576\n" \
"v_mfma_f32_16x16x32_bf16 v[132:135], a[108:111], a[204:207], v[132:135]\n" \
"buffer_load_dwordx4 v21, s[32:35], s47 offen offset:0 lds\n" \
"v_mfma_f32_16x16x32_bf16 v[140:143], a[104:107], a[208:211], v[140:143]\n" \
"v_mfma_f32_16x16x32_bf16 v[140:143], a[108:111], a[212:215], v[140:143]\n" \
"buffer_store_dwordx4 v[236:239], v240, s[36:39], s59 offen offset:384 sc1\n" \
"v_mfma_f32_16x16x32_bf16 v[148:151], a[104:107], a[216:219], v[148:151]\n" \
"s_add_u32 m0, s51, 25472\n" \
"s_mov_b64 s[40:41], s[4:5]\n" \
"s_and_b32 s41, s41, 0xffff\n" \
"v_mfma_f32_16x16x32_bf16 v[148:151], a[108:111], a[220:223], v[148:151]\n" \
"buffer_load_dwordx4 v21, s[32:35], s47 offen offset:128 lds\n" \
"v_mfma_f32_16x16x32_bf16 v[156:159], a[104:107], a[224:227], v[156:159]\n" \
"s_waitcnt vmcnt(19) lgkmcnt(0)\n" \
"s_lshl_b32 s62, s6, 20\n" \
"s_add_u32 s16, s62, 33554432\n" \
"v_mfma_f32_16x16x32_bf16 v[156:159], a[108:111], a[228:231], v[156:159]\n" \
"s_barrier\n" \
"s_lshl_b32 s63, s7, 16\n" \
"s_sub_u32 s63, 1048576, s63\n" \
"v_mfma_f32_16x16x32_bf16 v[160:163], a[64:67], a[200:203], v[160:163]\n" \
"s_add_u32 s42, s16, s63\n" \
"ds_read_b64_tr_b16 a[96:97], v9 offset:49664\n" \
"v_mfma_f32_16x16x32_bf16 v[160:163], a[68:71], a[204:207], v[160:163]\n" \
"ds_read_b64_tr_b16 a[98:99], v10 offset:49664\n" \
"v_mfma_f32_16x16x32_bf16 v[168:171], a[64:67], a[208:211], v[168:171]\n" \
"s_mov_b32 s46, 896\n" \
"s_add_u32 m0, s50, 0\n" \
"ds_read_b64_tr_b16 a[100:101], v9 offset:57856\n" \
"v_mfma_f32_16x16x32_bf16 v[168:171], a[68:71], a[212:215], v[168:171]\n" \
"buffer_load_dwordx4 v13, s[28:31], s46 offen lds\n" \
"v_mfma_f32_16x16x32_bf16 v[176:179], a[64:67], a[216:219], v[176:179]\n" \
"ds_read_b64_tr_b16 a[102:103], v10 offset:57856\n" \
"v_mfma_f32_16x16x32_bf16 v[176:179], a[68:71], a[220:223], v[176:179]\n" \
"ds_read_b128 a[0:3], v5 offset:32768\n" \
"v_mfma_f32_16x16x32_bf16 v[184:187], a[64:67], a[224:227], v[184:187]\n" \
"ds_read_b128 a[4:7], v6 offset:32768\n" \
"v_mfma_f32_16x16x32_bf16 v[184:187], a[68:71], a[228:231], v[184:187]\n" \
"ds_read_b128 a[8:11], v5 offset:34816\n" \
"v_mfma_f32_16x16x32_bf16 v[164:167], a[72:75], a[200:203], v[164:167]\n" \
"ds_read_b128 a[12:15], v6 offset:34816\n" \
"v_mfma_f32_16x16x32_bf16 v[164:167], a[76:79], a[204:207], v[164:167]\n" \
"ds_read_b128 a[16:19], v5 offset:36864\n" \
"v_mfma_f32_16x16x32_bf16 v[172:175], a[72:75], a[208:211], v[172:175]\n" \
"s_add_u32 m0, s50, 4096\n" \
"ds_read_b128 a[20:23], v6 offset:36864\n" \
"v_mfma_f32_16x16x32_bf16 v[172:175], a[76:79], a[212:215], v[172:175]\n" \
"buffer_load_dwordx4 v14, s[28:31], s46 offen lds\n" \
"v_mfma_f32_16x16x32_bf16 v[180:183], a[72:75], a[216:219], v[180:183]\n" \
"ds_read_b128 a[24:27], v5 offset:38912\n" \
"v_mfma_f32_16x16x32_bf16 v[180:183], a[76:79], a[220:223], v[180:183]\n" \
"ds_read_b128 a[28:31], v6 offset:38912\n" \
"v_mfma_f32_16x16x32_bf16 v[188:191], a[72:75], a[224:227], v[188:191]\n" \
"ds_read_b64_tr_b16 a[104:105], v11 offset:49664\n" \
"v_mfma_f32_16x16x32_bf16 v[188:191], a[76:79], a[228:231], v[188:191]\n" \
"ds_read_b64_tr_b16 a[106:107], v12 offset:49664\n" \
"v_mfma_f32_16x16x32_bf16 v[64:67], a[80:83], a[200:203], v[64:67]\n" \
"ds_read_b64_tr_b16 a[108:109], v11 offset:57856\n" \
"v_mfma_f32_16x16x32_bf16 v[64:67], a[84:87], a[204:207], v[64:67]\n" \
"ds_read_b64_tr_b16 a[110:111], v12 offset:57856\n" \
"v_mfma_f32_16x16x32_bf16 v[72:75], a[80:83], a[208:211], v[72:75]\n" \
"s_add_u32 m0, s50, 8192\n" \
"ds_read_b64_tr_b16 a[64:65], v9 offset:50176\n" \
"v_mfma_f32_16x16x32_bf16 v[72:75], a[84:87], a[212:215], v[72:75]\n" \
"buffer_load_dwordx4 v15, s[28:31], s46 offen lds\n" \
"v_mfma_f32_16x16x32_bf16 v[80:83], a[80:83], a[216:219], v[80:83]\n" \
"ds_read_b64_tr_b16 a[66:67], v10 offset:50176\n" \
"v_mfma_f32_16x16x32_bf16 v[80:83], a[84:87], a[220:223], v[80:83]\n" \
"ds_read_b64_tr_b16 a[68:69], v9 offset:58368\n" \
"v_mfma_f32_16x16x32_bf16 v[88:91], a[80:83], a[224:227], v[88:91]\n" \
"ds_read_b64_tr_b16 a[70:71], v10 offset:58368\n" \
"v_mfma_f32_16x16x32_bf16 v[88:91], a[84:87], a[228:231], v[88:91]\n" \
"ds_read_b64_tr_b16 a[72:73], v11 offset:50176\n" \
"v_mfma_f32_16x16x32_bf16 v[68:71], a[88:91], a[200:203], v[68:71]\n" \
"ds_read_b64_tr_b16 a[74:75], v12 offset:50176\n" \
"v_mfma_f32_16x16x32_bf16 v[68:71], a[92:95], a[204:207], v[68:71]\n" \
"ds_read_b64_tr_b16 a[76:77], v11 offset:58368\n" \
"v_mfma_f32_16x16x32_bf16 v[76:79], a[88:91], a[208:211], v[76:79]\n" \
"s_add_u32 m0, s50, 12288\n" \
"ds_read_b64_tr_b16 a[78:79], v12 offset:58368\n" \
"v_mfma_f32_16x16x32_bf16 v[76:79], a[92:95], a[212:215], v[76:79]\n" \
"buffer_load_dwordx4 v16, s[28:31], s46 offen lds\n" \
"v_mfma_f32_16x16x32_bf16 v[84:87], a[88:91], a[216:219], v[84:87]\n" \
"s_cmp_lt_u32 %2, 2\ns_cselect_b32 s62, 1064960, 0\ns_cselect_b32 s9, -1, 1\n" \
"v_mfma_f32_16x16x32_bf16 v[84:87], a[92:95], a[220:223], v[84:87]\n" \
"buffer_store_dwordx4 v[34:37], v4, s[36:39], s97 offen offset:0 sc1\n" \
"v_mfma_f32_16x16x32_bf16 v[92:95], a[88:91], a[224:227], v[92:95]\n" \
"s_add_u32 s96, s96, s92\n" \
"s_add_u32 s16, s16, s62\n" \
"s_add_u32 s17, s16, 8192\n" \
"v_mfma_f32_16x16x32_bf16 v[92:95], a[92:95], a[228:231], v[92:95]\n" \
"s_waitcnt lgkmcnt(12)\n" \
"s_lshl_b32 s9, s9, 14\n" \
"s_mov_b32 s19, s9\n" \
"v_mfma_f32_16x16x32_bf16 v[96:99], a[96:99], a[0:3], v[96:99]\n" \
"s_mov_b32 s46, 896\n" \
"s_add_u32 m0, s50, 16384\n" \
"ds_read_b64_tr_b16 a[80:81], v9 offset:50688\n" \
"v_mfma_f32_16x16x32_bf16 v[96:99], a[100:103], a[4:7], v[96:99]\n" \
"buffer_load_dwordx4 v17, s[28:31], s46 offen lds\n" \
"v_mfma_f32_16x16x32_bf16 v[104:107], a[96:99], a[8:11], v[104:107]\n" \
"s_and_b32 s63, %2, 1\n" \
"ds_read_b64_tr_b16 a[82:83], v10 offset:50688\n" \
"s_lshl_b32 s63, s63, 12\n" \
"v_mfma_f32_16x16x32_bf16 v[104:107], a[100:103], a[12:15], v[104:107]\n" \
"s_lshr_b32 s62, s6, 1\n" \
"ds_read_b64_tr_b16 a[84:85], v9 offset:58880\n" \
"v_add_u32 v60, s63, v4\n" \
"v_mfma_f32_16x16x32_bf16 v[112:115], a[96:99], a[16:19], v[112:115]\n" \
"s_add_u32 m0, s50, 20480\n" \
"ds_read_b64_tr_b16 a[86:87], v10 offset:58880\n" \
"s_lshl_b32 s62, s62, 7\n" \
"v_mfma_f32_16x16x32_bf16 v[112:115], a[100:103], a[20:23], v[112:115]\n" \
"buffer_load_dwordx4 v18, s[28:31], s46 offen lds\n" \
"v_mfma_f32_16x16x32_bf16 v[120:123], a[96:99], a[24:27], v[120:123]\n" \
"s_waitcnt lgkmcnt(12)\n" \
"ds_read_b64_tr_b16 a[88:89], v11 offset:50688\n" \
"s_add_u32 s62, s62, 94375936\n" \
"v_mfma_f32_16x16x32_bf16 v[120:123], a[100:103], a[28:31], v[120:123]\n" \
"v_bfe_u32 v57, v4, 4, 4\n" \
"v_lshl_add_u32 v57, v57, 3, s62\n" \
"v_mfma_f32_16x16x32_bf16 v[100:103], a[104:107], a[0:3], v[100:103]\n" \
"s_add_u32 m0, s50, 24576\n" \
"ds_read_b64_tr_b16 a[90:91], v12 offset:50688\n" \
"v_mfma_f32_16x16x32_bf16 v[100:103], a[108:111], a[4:7], v[100:103]\n" \
"buffer_load_dwordx4 v19, s[28:31], s46 offen lds\n" \
"v_mfma_f32_16x16x32_bf16 v[108:111], a[104:107], a[8:11], v[108:111]\n" \
"ds_read_b64_tr_b16 a[92:93], v11 offset:58880\n" \
"v_mfma_f32_16x16x32_bf16 v[108:111], a[108:111], a[12:15], v[108:111]\n" \
"ds_read_b64_tr_b16 a[94:95], v12 offset:58880\n" \
"v_mfma_f32_16x16x32_bf16 v[116:119], a[104:107], a[16:19], v[116:119]\n" \
"s_add_u32 m0, s50, 28672\n" \
"ds_read_b64_tr_b16 a[96:97], v9 offset:49152\n" \
"v_mfma_f32_16x16x32_bf16 v[116:119], a[108:111], a[20:23], v[116:119]\n" \
"buffer_load_dwordx4 v20, s[28:31], s46 offen lds\n" \
"v_mfma_f32_16x16x32_bf16 v[124:127], a[104:107], a[24:27], v[124:127]\n" \
"s_waitcnt lgkmcnt(13)\n" \
"ds_read_b64_tr_b16 a[98:99], v10 offset:49152\n" \
"v_mfma_f32_16x16x32_bf16 v[124:127], a[108:111], a[28:31], v[124:127]\n" \
"ds_read_b64_tr_b16 a[100:101], v9 offset:57344\n" \
"v_mfma_f32_16x16x32_bf16 v[128:131], a[64:67], a[0:3], v[128:131]\n" \
"s_mov_b32 s47, 4194304\n" \
"s_add_u32 m0, s51, 32768\n" \
"ds_read_b64_tr_b16 a[102:103], v10 offset:57344\n" \
"v_mfma_f32_16x16x32_bf16 v[128:131], a[68:71], a[4:7], v[128:131]\n" \
"buffer_load_dwordx4 v21, s[32:35], s47 offen offset:0 lds\n" \
"v_mfma_f32_16x16x32_bf16 v[136:139], a[64:67], a[8:11], v[136:139]\n" \
"ds_read_b64_tr_b16 a[104:105], v11 offset:49152\n" \
"v_mfma_f32_16x16x32_bf16 v[136:139], a[68:71], a[12:15], v[136:139]\n" \
"v_mfma_f32_16x16x32_bf16 v[144:147], a[64:67], a[16:19], v[144:147]\n" \
"s_add_u32 m0, s51, 33664\n" \
"ds_read_b64_tr_b16 a[106:107], v12 offset:49152\n" \
"v_mfma_f32_16x16x32_bf16 v[144:147], a[68:71], a[20:23], v[144:147]\n" \
"buffer_load_dwordx4 v21, s[32:35], s47 offen offset:128 lds\n" \
"v_mfma_f32_16x16x32_bf16 v[152:155], a[64:67], a[24:27], v[152:155]\n" \
"s_waitcnt lgkmcnt(14)\n" \
"ds_read_b64_tr_b16 a[108:109], v11 offset:57344\n" \
"v_mfma_f32_16x16x32_bf16 v[152:155], a[68:71], a[28:31], v[152:155]\n" \
"ds_read_b64_tr_b16 a[110:111], v12 offset:57344\n" \
"v_mfma_f32_16x16x32_bf16 v[132:135], a[72:75], a[0:3], v[132:135]\n" \
"s_mov_b32 s47, 4456448\n" \
"s_add_u32 m0, s51, 40960\n" \
"v_mfma_f32_16x16x32_bf16 v[132:135], a[76:79], a[4:7], v[132:135]\n" \
"buffer_load_dwordx4 v21, s[32:35], s47 offen offset:0 lds\n" \
"v_mfma_f32_16x16x32_bf16 v[140:143], a[72:75], a[8:11], v[140:143]\n" \
"v_mfma_f32_16x16x32_bf16 v[140:143], a[76:79], a[12:15], v[140:143]\n" \
"buffer_store_dwordx4 v[38:41], v4, s[36:39], s96 offen offset:0 sc1\n" \
"v_mfma_f32_16x16x32_bf16 v[148:151], a[72:75], a[16:19], v[148:151]\n" \
"s_add_u32 m0, s51, 41856\n" \
"s_add_u32 s97, s96, s98\n" \
"v_mfma_f32_16x16x32_bf16 v[148:151], a[76:79], a[20:23], v[148:151]\n" \
"buffer_load_dwordx4 v21, s[32:35], s47 offen offset:128 lds\n" \
"v_mfma_f32_16x16x32_bf16 v[156:159], a[72:75], a[24:27], v[156:159]\n" \
"s_waitcnt vmcnt(20) lgkmcnt(0)\n" \
"v_mfma_f32_16x16x32_bf16 v[156:159], a[76:79], a[28:31], v[156:159]\n" \
"s_barrier\n" \
"v_mfma_f32_16x16x32_bf16 v[160:163], a[80:83], a[0:3], v[160:163]\n" \
"ds_read_b64_tr_b16 a[64:65], v9 offset:512\n" \
"v_mfma_f32_16x16x32_bf16 v[160:163], a[84:87], a[4:7], v[160:163]\n" \
"ds_read_b64_tr_b16 a[66:67], v10 offset:512\n" \
"v_mfma_f32_16x16x32_bf16 v[168:171], a[80:83], a[8:11], v[168:171]\n" \
"s_mov_b32 s46, 1024\n" \
"s_add_u32 m0, s50, 32768\n" \
"ds_read_b64_tr_b16 a[68:69], v9 offset:8704\n" \
"v_mfma_f32_16x16x32_bf16 v[168:171], a[84:87], a[12:15], v[168:171]\n" \
"buffer_load_dwordx4 v13, s[28:31], s46 offen lds\n" \
"v_mfma_f32_16x16x32_bf16 v[176:179], a[80:83], a[16:19], v[176:179]\n" \
"ds_read_b64_tr_b16 a[70:71], v10 offset:8704\n" \
"v_mfma_f32_16x16x32_bf16 v[176:179], a[84:87], a[20:23], v[176:179]\n" \
"ds_read_b128 a[200:203], v7 offset:0\n" \
"v_mfma_f32_16x16x32_bf16 v[184:187], a[80:83], a[24:27], v[184:187]\n" \
"ds_read_b128 a[204:207], v8 offset:0\n" \
"v_mfma_f32_16x16x32_bf16 v[184:187], a[84:87], a[28:31], v[184:187]\n" \
"ds_read_b128 a[208:211], v7 offset:2048\n" \
"v_mfma_f32_16x16x32_bf16 v[164:167], a[88:91], a[0:3], v[164:167]\n" \
"ds_read_b128 a[212:215], v8 offset:2048\n" \
"v_mfma_f32_16x16x32_bf16 v[164:167], a[92:95], a[4:7], v[164:167]\n" \
"ds_read_b128 a[216:219], v7 offset:4096\n" \
"v_mfma_f32_16x16x32_bf16 v[172:175], a[88:91], a[8:11], v[172:175]\n" \
"s_add_u32 m0, s50, 36864\n" \
"ds_read_b128 a[220:223], v8 offset:4096\n" \
"v_mfma_f32_16x16x32_bf16 v[172:175], a[92:95], a[12:15], v[172:175]\n" \
"LH11_%=:\ns_cbranch_execz LH15_%=\n" \
"buffer_load_dwordx4 v14, s[28:31], s46 offen lds\n" \
"v_mfma_f32_16x16x32_bf16 v[180:183], a[88:91], a[16:19], v[180:183]\n" \
"ds_read_b128 a[224:227], v7 offset:6144\n" \
"v_mfma_f32_16x16x32_bf16 v[180:183], a[92:95], a[20:23], v[180:183]\n" \
"ds_read_b128 a[228:231], v8 offset:6144\n" \
"v_mfma_f32_16x16x32_bf16 v[188:191], a[88:91], a[24:27], v[188:191]\n" \
"ds_read_b64_tr_b16 a[72:73], v11 offset:512\n" \
"v_mfma_f32_16x16x32_bf16 v[188:191], a[92:95], a[28:31], v[188:191]\n" \
"ds_read_b64_tr_b16 a[74:75], v12 offset:512\n" \
"v_mfma_f32_16x16x32_bf16 v[64:67], a[96:99], a[0:3], v[64:67]\n" \
"ds_read_b64_tr_b16 a[76:77], v11 offset:8704\n" \
"v_mfma_f32_16x16x32_bf16 v[64:67], a[100:103], a[4:7], v[64:67]\n" \
"ds_read_b64_tr_b16 a[78:79], v12 offset:8704\n" \
"v_mfma_f32_16x16x32_bf16 v[72:75], a[96:99], a[8:11], v[72:75]\n" \
"s_add_u32 m0, s50, 40960\n" \
"ds_read_b64_tr_b16 a[80:81], v9 offset:1024\n" \
"v_mfma_f32_16x16x32_bf16 v[72:75], a[100:103], a[12:15], v[72:75]\n" \
"buffer_load_dwordx4 v15, s[28:31], s46 offen lds\n" \
"v_mfma_f32_16x16x32_bf16 v[80:83], a[96:99], a[16:19], v[80:83]\n" \
"ds_read_b64_tr_b16 a[82:83], v10 offset:1024\n" \
"v_mfma_f32_16x16x32_bf16 v[80:83], a[100:103], a[20:23], v[80:83]\n" \
"ds_read_b64_tr_b16 a[84:85], v9 offset:9216\n" \
"v_mfma_f32_16x16x32_bf16 v[88:91], a[96:99], a[24:27], v[88:91]\n" \
"ds_read_b64_tr_b16 a[86:87], v10 offset:9216\n" \
"v_mfma_f32_16x16x32_bf16 v[88:91], a[100:103], a[28:31], v[88:91]\n" \
"ds_read_b64_tr_b16 a[88:89], v11 offset:1024\n" \
"v_mfma_f32_16x16x32_bf16 v[68:71], a[104:107], a[0:3], v[68:71]\n" \
"ds_read_b64_tr_b16 a[90:91], v12 offset:1024\n" \
"v_mfma_f32_16x16x32_bf16 v[68:71], a[108:111], a[4:7], v[68:71]\n" \
"ds_read_b64_tr_b16 a[92:93], v11 offset:9216\n" \
"v_mfma_f32_16x16x32_bf16 v[76:79], a[104:107], a[8:11], v[76:79]\n" \
"s_add_u32 m0, s50, 45056\n" \
"ds_read_b64_tr_b16 a[94:95], v12 offset:9216\n" \
"v_mfma_f32_16x16x32_bf16 v[76:79], a[108:111], a[12:15], v[76:79]\n" \
"buffer_load_dwordx4 v16, s[28:31], s46 offen lds\n" \
"v_mfma_f32_16x16x32_bf16 v[84:87], a[104:107], a[16:19], v[84:87]\n" \
"v_mfma_f32_16x16x32_bf16 v[84:87], a[108:111], a[20:23], v[84:87]\n" \
"buffer_store_dwordx4 v[42:45], v4, s[36:39], s97 offen offset:0 sc1\n" \
"v_mfma_f32_16x16x32_bf16 v[92:95], a[104:107], a[24:27], v[92:95]\n" \
"v_mfma_f32_16x16x32_bf16 v[92:95], a[108:111], a[28:31], v[92:95]\n" \
"s_waitcnt lgkmcnt(12)\n" \
"v_mfma_f32_16x16x32_bf16 v[96:99], a[64:67], a[200:203], v[96:99]\n" \
"s_mov_b32 s46, 1024\n" \
"s_add_u32 m0, s50, 49152\n" \
"ds_read_b64_tr_b16 a[96:97], v9 offset:1536\n" \
"v_mfma_f32_16x16x32_bf16 v[96:99], a[68:71], a[204:207], v[96:99]\n" \
"buffer_load_dwordx4 v17, s[28:31], s46 offen lds\n" \
"v_mfma_f32_16x16x32_bf16 v[104:107], a[64:67], a[208:211], v[104:107]\n" \
"ds_read_b64_tr_b16 a[98:99], v10 offset:1536\n" \
"v_mfma_f32_16x16x32_bf16 v[104:107], a[68:71], a[212:215], v[104:107]\n" \
"ds_read_b64_tr_b16 a[100:101], v9 offset:9728\n" \
"v_mfma_f32_16x16x32_bf16 v[112:115], a[64:67], a[216:219], v[112:115]\n" \
"s_add_u32 m0, s50, 53248\n" \
"ds_read_b64_tr_b16 a[102:103], v10 offset:9728\n" \
"v_mfma_f32_16x16x32_bf16 v[112:115], a[68:71], a[220:223], v[112:115]\n" \
"buffer_load_dwordx4 v18, s[28:31], s46 offen lds\n" \
"v_mfma_f32_16x16x32_bf16 v[120:123], a[64:67], a[224:227], v[120:123]\n" \
"s_waitcnt lgkmcnt(12)\n" \
"ds_read_b64_tr_b16 a[104:105], v11 offset:1536\n" \
"v_mfma_f32_16x16x32_bf16 v[120:123], a[68:71], a[228:231], v[120:123]\n" \
"v_mfma_f32_16x16x32_bf16 v[100:103], a[72:75], a[200:203], v[100:103]\n" \
"s_add_u32 m0, s50, 57344\n" \
"ds_read_b64_tr_b16 a[106:107], v12 offset:1536\n" \
"v_mfma_f32_16x16x32_bf16 v[100:103], a[76:79], a[204:207], v[100:103]\n" \
"buffer_load_dwordx4 v19, s[28:31], s46 offen lds\n" \
"v_mfma_f32_16x16x32_bf16 v[108:111], a[72:75], a[208:211], v[108:111]\n" \
"ds_read_b64_tr_b16 a[108:109], v11 offset:9728\n" \
"v_mfma_f32_16x16x32_bf16 v[108:111], a[76:79], a[212:215], v[108:111]\n" \
"ds_read_b64_tr_b16 a[110:111], v12 offset:9728\n" \
"v_mfma_f32_16x16x32_bf16 v[116:119], a[72:75], a[216:219], v[116:119]\n" \
"s_add_u32 m0, s50, 61440\n" \
"ds_read_b64_tr_b16 a[64:65], v9 offset:0\n" \
"v_mfma_f32_16x16x32_bf16 v[116:119], a[76:79], a[220:223], v[116:119]\n" \
"buffer_load_dwordx4 v20, s[28:31], s46 offen lds\n" \
"v_mfma_f32_16x16x32_bf16 v[124:127], a[72:75], a[224:227], v[124:127]\n" \
"s_waitcnt lgkmcnt(13)\n" \
"ds_read_b64_tr_b16 a[66:67], v10 offset:0\n" \
"v_mfma_f32_16x16x32_bf16 v[124:127], a[76:79], a[228:231], v[124:127]\n" \
"ds_read_b64_tr_b16 a[68:69], v9 offset:8192\n" \
"v_mfma_f32_16x16x32_bf16 v[128:131], a[80:83], a[200:203], v[128:131]\n" \
"s_mov_b32 s47, 4718592\n" \
"s_add_u32 m0, s51, 49152\n" \
"ds_read_b64_tr_b16 a[70:71], v10 offset:8192\n" \
"v_mfma_f32_16x16x32_bf16 v[128:131], a[84:87], a[204:207], v[128:131]\n" \
"buffer_load_dwordx4 v21, s[32:35], s47 offen offset:0 lds\n" \
"v_mfma_f32_16x16x32_bf16 v[136:139], a[80:83], a[208:211], v[136:139]\n" \
"ds_read_b64_tr_b16 a[72:73], v11 offset:0\n" \
"v_mfma_f32_16x16x32_bf16 v[136:139], a[84:87], a[212:215], v[136:139]\n" \
"v_mfma_f32_16x16x32_bf16 v[144:147], a[80:83], a[216:219], v[144:147]\n" \
"s_add_u32 m0, s51, 50048\n" \
"ds_read_b64_tr_b16 a[74:75], v12 offset:0\n" \
"v_mfma_f32_16x16x32_bf16 v[144:147], a[84:87], a[220:223], v[144:147]\n" \
"buffer_load_dwordx4 v21, s[32:35], s47 offen offset:128 lds\n" \
"v_mfma_f32_16x16x32_bf16 v[152:155], a[80:83], a[224:227], v[152:155]\n" \
"s_waitcnt lgkmcnt(14)\n" \
"ds_read_b64_tr_b16 a[76:77], v11 offset:8192\n" \
"v_mfma_f32_16x16x32_bf16 v[152:155], a[84:87], a[228:231], v[152:155]\n" \
"ds_read_b64_tr_b16 a[78:79], v12 offset:8192\n" \
"v_mfma_f32_16x16x32_bf16 v[132:135], a[88:91], a[200:203], v[132:135]\n" \
"s_mov_b32 s47, 4980736\n" \
"s_add_u32 m0, s51, 57344\n" \
"v_mfma_f32_16x16x32_bf16 v[132:135], a[92:95], a[204:207], v[132:135]\n" \
"buffer_load_dwordx4 v21, s[32:35], s47 offen offset:0 lds\n" \
"v_mfma_f32_16x16x32_bf16 v[140:143], a[88:91], a[208:211], v[140:143]\n" \
"v_mfma_f32_16x16x32_bf16 v[140:143], a[92:95], a[212:215], v[140:143]\n" \
"v_mfma_f32_16x16x32_bf16 v[148:151], a[88:91], a[216:219], v[148:151]\n" \
"s_add_u32 m0, s51, 58240\n" \
"v_mfma_f32_16x16x32_bf16 v[148:151], a[92:95], a[220:223], v[148:151]\n" \
"buffer_load_dwordx4 v21, s[32:35], s47 offen offset:128 lds\n" \
"v_mfma_f32_16x16x32_bf16 v[156:159], a[88:91], a[224:227], v[156:159]\n" \
"s_waitcnt vmcnt(18) lgkmcnt(0)\n" \
"v_mfma_f32_16x16x32_bf16 v[156:159], a[92:95], a[228:231], v[156:159]\n" \
"s_barrier\n" \
"v_mfma_f32_16x16x32_bf16 v[160:163], a[96:99], a[200:203], v[160:163]\n" \
"ds_read_b64_tr_b16 a[80:81], v9 offset:16896\n" \
"v_mfma_f32_16x16x32_bf16 v[160:163], a[100:103], a[204:207], v[160:163]\n" \
"ds_read_b64_tr_b16 a[82:83], v10 offset:16896\n" \
"v_mfma_f32_16x16x32_bf16 v[168:171], a[96:99], a[208:211], v[168:171]\n" \
"s_mov_b32 s46, 1152\n" \
"s_add_u32 m0, s50, 65536\n" \
"ds_read_b64_tr_b16 a[84:85], v9 offset:25088\n" \
"v_mfma_f32_16x16x32_bf16 v[168:171], a[100:103], a[212:215], v[168:171]\n" \
"buffer_load_dwordx4 v13, s[28:31], s46 offen lds\n" \
"v_mfma_f32_16x16x32_bf16 v[176:179], a[96:99], a[216:219], v[176:179]\n" \
"ds_read_b64_tr_b16 a[86:87], v10 offset:25088\n" \
"v_mfma_f32_16x16x32_bf16 v[176:179], a[100:103], a[220:223], v[176:179]\n" \
"ds_read_b128 a[0:3], v5 offset:0\n" \
"v_mfma_f32_16x16x32_bf16 v[184:187], a[96:99], a[224:227], v[184:187]\n" \
"ds_read_b128 a[4:7], v6 offset:0\n" \
"v_mfma_f32_16x16x32_bf16 v[184:187], a[100:103], a[228:231], v[184:187]\n" \
"ds_read_b128 a[8:11], v5 offset:2048\n" \
"v_mfma_f32_16x16x32_bf16 v[164:167], a[104:107], a[200:203], v[164:167]\n" \
"ds_read_b128 a[12:15], v6 offset:2048\n" \
"v_mfma_f32_16x16x32_bf16 v[164:167], a[108:111], a[204:207], v[164:167]\n" \
"ds_read_b128 a[16:19], v5 offset:4096\n" \
"v_mfma_f32_16x16x32_bf16 v[172:175], a[104:107], a[208:211], v[172:175]\n" \
"s_add_u32 m0, s50, 69632\n" \
"ds_read_b128 a[20:23], v6 offset:4096\n" \
"v_mfma_f32_16x16x32_bf16 v[172:175], a[108:111], a[212:215], v[172:175]\n" \
"buffer_load_dwordx4 v14, s[28:31], s46 offen lds\n" \
"v_mfma_f32_16x16x32_bf16 v[180:183], a[104:107], a[216:219], v[180:183]\n" \
"ds_read_b128 a[24:27], v5 offset:6144\n" \
"v_mfma_f32_16x16x32_bf16 v[180:183], a[108:111], a[220:223], v[180:183]\n" \
"ds_read_b128 a[28:31], v6 offset:6144\n" \
"v_mfma_f32_16x16x32_bf16 v[188:191], a[104:107], a[224:227], v[188:191]\n" \
"ds_read_b64_tr_b16 a[88:89], v11 offset:16896\n" \
"v_mfma_f32_16x16x32_bf16 v[188:191], a[108:111], a[228:231], v[188:191]\n" \
"ds_read_b64_tr_b16 a[90:91], v12 offset:16896\n" \
"v_mfma_f32_16x16x32_bf16 v[64:67], a[64:67], a[200:203], v[64:67]\n" \
"ds_read_b64_tr_b16 a[92:93], v11 offset:25088\n" \
"v_mfma_f32_16x16x32_bf16 v[64:67], a[68:71], a[204:207], v[64:67]\n" \
"ds_read_b64_tr_b16 a[94:95], v12 offset:25088\n" \
"v_mfma_f32_16x16x32_bf16 v[72:75], a[64:67], a[208:211], v[72:75]\n" \
"s_add_u32 m0, s50, 73728\n" \
"ds_read_b64_tr_b16 a[96:97], v9 offset:17408\n" \
"v_mfma_f32_16x16x32_bf16 v[72:75], a[68:71], a[212:215], v[72:75]\n" \
"buffer_load_dwordx4 v15, s[28:31], s46 offen lds\n" \
"v_mfma_f32_16x16x32_bf16 v[80:83], a[64:67], a[216:219], v[80:83]\n" \
"ds_read_b64_tr_b16 a[98:99], v10 offset:17408\n" \
"v_mfma_f32_16x16x32_bf16 v[80:83], a[68:71], a[220:223], v[80:83]\n" \
"ds_read_b64_tr_b16 a[100:101], v9 offset:25600\n" \
"v_mfma_f32_16x16x32_bf16 v[88:91], a[64:67], a[224:227], v[88:91]\n" \
"ds_read_b64_tr_b16 a[102:103], v10 offset:25600\n" \
"v_mfma_f32_16x16x32_bf16 v[88:91], a[68:71], a[228:231], v[88:91]\n" \
"ds_read_b64_tr_b16 a[104:105], v11 offset:17408\n" \
"v_mfma_f32_16x16x32_bf16 v[68:71], a[72:75], a[200:203], v[68:71]\n" \
"ds_read_b64_tr_b16 a[106:107], v12 offset:17408\n" \
"v_mfma_f32_16x16x32_bf16 v[68:71], a[76:79], a[204:207], v[68:71]\n" \
"ds_read_b64_tr_b16 a[108:109], v11 offset:25600\n" \
"v_mfma_f32_16x16x32_bf16 v[76:79], a[72:75], a[208:211], v[76:79]\n" \
"s_add_u32 m0, s50, 77824\n" \
"ds_read_b64_tr_b16 a[110:111], v12 offset:25600\n" \
"v_mfma_f32_16x16x32_bf16 v[76:79], a[76:79], a[212:215], v[76:79]\n" \
"buffer_load_dwordx4 v16, s[28:31], s46 offen lds\n" \
"v_mfma_f32_16x16x32_bf16 v[84:87], a[72:75], a[216:219], v[84:87]\n" \
"v_mfma_f32_16x16x32_bf16 v[84:87], a[76:79], a[220:223], v[84:87]\n" \
"v_mfma_f32_16x16x32_bf16 v[92:95], a[72:75], a[224:227], v[92:95]\n" \
"v_mfma_f32_16x16x32_bf16 v[92:95], a[76:79], a[228:231], v[92:95]\n" \
"s_waitcnt lgkmcnt(12)\n" \
"v_mfma_f32_16x16x32_bf16 v[96:99], a[80:83], a[0:3], v[96:99]\n" \
"s_mov_b32 s46, 1152\n" \
"s_add_u32 m0, s50, 81920\n" \
"ds_read_b64_tr_b16 a[64:65], v9 offset:17920\n" \
"v_mfma_f32_16x16x32_bf16 v[96:99], a[84:87], a[4:7], v[96:99]\n" \
"buffer_load_dwordx4 v17, s[28:31], s46 offen lds\n" \
"v_mfma_f32_16x16x32_bf16 v[104:107], a[80:83], a[8:11], v[104:107]\n" \
"ds_read_b64_tr_b16 a[66:67], v10 offset:17920\n" \
"v_mfma_f32_16x16x32_bf16 v[104:107], a[84:87], a[12:15], v[104:107]\n" \
"ds_read_b64_tr_b16 a[68:69], v9 offset:26112\n" \
"v_mfma_f32_16x16x32_bf16 v[112:115], a[80:83], a[16:19], v[112:115]\n" \
"s_add_u32 m0, s50, 86016\n" \
"ds_read_b64_tr_b16 a[70:71], v10 offset:26112\n" \
"v_mfma_f32_16x16x32_bf16 v[112:115], a[84:87], a[20:23], v[112:115]\n" \
"buffer_load_dwordx4 v18, s[28:31], s46 offen lds\n" \
"v_mfma_f32_16x16x32_bf16 v[120:123], a[80:83], a[24:27], v[120:123]\n" \
"s_waitcnt lgkmcnt(12)\n" \
"ds_read_b64_tr_b16 a[72:73], v11 offset:17920\n" \
"v_mfma_f32_16x16x32_bf16 v[120:123], a[84:87], a[28:31], v[120:123]\n" \
"v_mfma_f32_16x16x32_bf16 v[100:103], a[88:91], a[0:3], v[100:103]\n" \
"s_add_u32 m0, s50, 90112\n" \
"ds_read_b64_tr_b16 a[74:75], v12 offset:17920\n" \
"v_mfma_f32_16x16x32_bf16 v[100:103], a[92:95], a[4:7], v[100:103]\n" \
"buffer_load_dwordx4 v19, s[28:31], s46 offen lds\n" \
"v_mfma_f32_16x16x32_bf16 v[108:111], a[88:91], a[8:11], v[108:111]\n" \
"ds_read_b64_tr_b16 a[76:77], v11 offset:26112\n" \
"v_mfma_f32_16x16x32_bf16 v[108:111], a[92:95], a[12:15], v[108:111]\n" \
"ds_read_b64_tr_b16 a[78:79], v12 offset:26112\n" \
"v_mfma_f32_16x16x32_bf16 v[116:119], a[88:91], a[16:19], v[116:119]\n" \
"s_add_u32 m0, s50, 94208\n" \
"ds_read_b64_tr_b16 a[80:81], v9 offset:16384\n" \
"v_mfma_f32_16x16x32_bf16 v[116:119], a[92:95], a[20:23], v[116:119]\n" \
"buffer_load_dwordx4 v20, s[28:31], s46 offen lds\n" \
"v_mfma_f32_16x16x32_bf16 v[124:127], a[88:91], a[24:27], v[124:127]\n" \
"s_waitcnt lgkmcnt(13)\n" \
"ds_read_b64_tr_b16 a[82:83], v10 offset:16384\n" \
"v_mfma_f32_16x16x32_bf16 v[124:127], a[92:95], a[28:31], v[124:127]\n" \
"ds_read_b64_tr_b16 a[84:85], v9 offset:24576\n" \
"v_mfma_f32_16x16x32_bf16 v[128:131], a[96:99], a[0:3], v[128:131]\n" \
"s_mov_b32 s47, 5242880\n" \
"s_add_u32 m0, s51, 0\n" \
"ds_read_b64_tr_b16 a[86:87], v10 offset:24576\n" \
"v_mfma_f32_16x16x32_bf16 v[128:131], a[100:103], a[4:7], v[128:131]\n" \
"buffer_load_dwordx4 v21, s[32:35], s47 offen offset:0 lds\n" \
"v_mfma_f32_16x16x32_bf16 v[136:139], a[96:99], a[8:11], v[136:139]\n" \
"ds_read_b64_tr_b16 a[88:89], v11 offset:16384\n" \
"v_mfma_f32_16x16x32_bf16 v[136:139], a[100:103], a[12:15], v[136:139]\n" \
"v_mfma_f32_16x16x32_bf16 v[144:147], a[96:99], a[16:19], v[144:147]\n" \
"s_add_u32 m0, s51, 896\n" \
"ds_read_b64_tr_b16 a[90:91], v12 offset:16384\n" \
"v_mfma_f32_16x16x32_bf16 v[144:147], a[100:103], a[20:23], v[144:147]\n" \
"buffer_load_dwordx4 v21, s[32:35], s47 offen offset:128 lds\n" \
"v_mfma_f32_16x16x32_bf16 v[152:155], a[96:99], a[24:27], v[152:155]\n" \
"s_waitcnt lgkmcnt(14)\n" \
"ds_read_b64_tr_b16 a[92:93], v11 offset:24576\n" \
"v_mfma_f32_16x16x32_bf16 v[152:155], a[100:103], a[28:31], v[152:155]\n" \
"ds_read_b64_tr_b16 a[94:95], v12 offset:24576\n" \
"v_mfma_f32_16x16x32_bf16 v[132:135], a[104:107], a[0:3], v[132:135]\n" \
"s_mov_b32 s47, 5505024\n" \
"s_add_u32 m0, s51, 8192\n" \
"v_mfma_f32_16x16x32_bf16 v[132:135], a[108:111], a[4:7], v[132:135]\n" \
"buffer_load_dwordx4 v21, s[32:35], s47 offen offset:0 lds\n" \
"v_mfma_f32_16x16x32_bf16 v[140:143], a[104:107], a[8:11], v[140:143]\n" \
"v_mfma_f32_16x16x32_bf16 v[140:143], a[108:111], a[12:15], v[140:143]\n" \
"v_mfma_f32_16x16x32_bf16 v[148:151], a[104:107], a[16:19], v[148:151]\n" \
"s_add_u32 m0, s51, 9088\n" \
"v_mfma_f32_16x16x32_bf16 v[148:151], a[108:111], a[20:23], v[148:151]\n" \
"buffer_load_dwordx4 v21, s[32:35], s47 offen offset:128 lds\n" \
"v_mfma_f32_16x16x32_bf16 v[156:159], a[104:107], a[24:27], v[156:159]\n" \
"s_waitcnt vmcnt(16) lgkmcnt(0)\n" \
"v_mfma_f32_16x16x32_bf16 v[156:159], a[108:111], a[28:31], v[156:159]\n" \
"s_barrier\n" \
"v_mfma_f32_16x16x32_bf16 v[160:163], a[64:67], a[0:3], v[160:163]\n" \
"ds_read_b64_tr_b16 a[96:97], v9 offset:33280\n" \
"v_mfma_f32_16x16x32_bf16 v[160:163], a[68:71], a[4:7], v[160:163]\n" \
"ds_read_b64_tr_b16 a[98:99], v10 offset:33280\n" \
"v_mfma_f32_16x16x32_bf16 v[168:171], a[64:67], a[8:11], v[168:171]\n" \
"s_mov_b32 s46, 1280\n" \
"s_add_u32 m0, s50, 0\n" \
"ds_read_b64_tr_b16 a[100:101], v9 offset:41472\n" \
"v_mfma_f32_16x16x32_bf16 v[168:171], a[68:71], a[12:15], v[168:171]\n" \
"buffer_load_dwordx4 v13, s[28:31], s46 offen lds\n" \
"v_mfma_f32_16x16x32_bf16 v[176:179], a[64:67], a[16:19], v[176:179]\n" \
"ds_read_b64_tr_b16 a[102:103], v10 offset:41472\n" \
"v_mfma_f32_16x16x32_bf16 v[176:179], a[68:71], a[20:23], v[176:179]\n" \
"ds_read_b128 a[200:203], v5 offset:32768\n" \
"v_mfma_f32_16x16x32_bf16 v[184:187], a[64:67], a[24:27], v[184:187]\n" \
"ds_read_b128 a[204:207], v6 offset:32768\n" \
"v_mfma_f32_16x16x32_bf16 v[184:187], a[68:71], a[28:31], v[184:187]\n" \
"ds_read_b128 a[208:211], v5 offset:34816\n" \
"v_mfma_f32_16x16x32_bf16 v[164:167], a[72:75], a[0:3], v[164:167]\n" \
"ds_read_b128 a[212:215], v6 offset:34816\n" \
"v_mfma_f32_16x16x32_bf16 v[164:167], a[76:79], a[4:7], v[164:167]\n" \
"ds_read_b128 a[216:219], v5 offset:36864\n" \
"v_mfma_f32_16x16x32_bf16 v[172:175], a[72:75], a[8:11], v[172:175]\n" \
"s_add_u32 m0, s50, 4096\n" \
"ds_read_b128 a[220:223], v6 offset:36864\n" \
"v_mfma_f32_16x16x32_bf16 v[172:175], a[76:79], a[12:15], v[172:175]\n" \
"buffer_load_dwordx4 v14, s[28:31], s46 offen lds\n" \
"v_mfma_f32_16x16x32_bf16 v[180:183], a[72:75], a[16:19], v[180:183]\n" \
"ds_read_b128 a[224:227], v5 offset:38912\n" \
"v_mfma_f32_16x16x32_bf16 v[180:183], a[76:79], a[20:23], v[180:183]\n" \
"ds_read_b128 a[228:231], v6 offset:38912\n" \
"v_mfma_f32_16x16x32_bf16 v[188:191], a[72:75], a[24:27], v[188:191]\n" \
"ds_read_b64_tr_b16 a[104:105], v11 offset:33280\n" \
"v_mfma_f32_16x16x32_bf16 v[188:191], a[76:79], a[28:31], v[188:191]\n" \
"ds_read_b64_tr_b16 a[106:107], v12 offset:33280\n" \
"v_mfma_f32_16x16x32_bf16 v[64:67], a[80:83], a[0:3], v[64:67]\n" \
"ds_read_b64_tr_b16 a[108:109], v11 offset:41472\n" \
"v_mfma_f32_16x16x32_bf16 v[64:67], a[84:87], a[4:7], v[64:67]\n" \
"ds_read_b64_tr_b16 a[110:111], v12 offset:41472\n" \
"v_mfma_f32_16x16x32_bf16 v[72:75], a[80:83], a[8:11], v[72:75]\n" \
"s_add_u32 m0, s50, 8192\n" \
"ds_read_b64_tr_b16 a[64:65], v9 offset:33792\n" \
"v_mfma_f32_16x16x32_bf16 v[72:75], a[84:87], a[12:15], v[72:75]\n" \
"buffer_load_dwordx4 v15, s[28:31], s46 offen lds\n" \
"v_mfma_f32_16x16x32_bf16 v[80:83], a[80:83], a[16:19], v[80:83]\n" \
"ds_read_b64_tr_b16 a[66:67], v10 offset:33792\n" \
"v_mfma_f32_16x16x32_bf16 v[80:83], a[84:87], a[20:23], v[80:83]\n" \
"ds_read_b64_tr_b16 a[68:69], v9 offset:41984\n" \
"v_mfma_f32_16x16x32_bf16 v[88:91], a[80:83], a[24:27], v[88:91]\n" \
"ds_read_b64_tr_b16 a[70:71], v10 offset:41984\n" \
"v_mfma_f32_16x16x32_bf16 v[88:91], a[84:87], a[28:31], v[88:91]\n" \
"ds_read_b64_tr_b16 a[72:73], v11 offset:33792\n" \
"v_mfma_f32_16x16x32_bf16 v[68:71], a[88:91], a[0:3], v[68:71]\n" \
"ds_read_b64_tr_b16 a[74:75], v12 offset:33792\n" \
"v_mfma_f32_16x16x32_bf16 v[68:71], a[92:95], a[4:7], v[68:71]\n" \
"ds_read_b64_tr_b16 a[76:77], v11 offset:41984\n" \
"v_mfma_f32_16x16x32_bf16 v[76:79], a[88:91], a[8:11], v[76:79]\n" \
"s_add_u32 m0, s50, 12288\n" \
"ds_read_b64_tr_b16 a[78:79], v12 offset:41984\n" \
"v_mfma_f32_16x16x32_bf16 v[76:79], a[92:95], a[12:15], v[76:79]\n" \
"buffer_load_dwordx4 v16, s[28:31], s46 offen lds\n" \
"v_mfma_f32_16x16x32_bf16 v[84:87], a[88:91], a[16:19], v[84:87]\n" \
"v_mfma_f32_16x16x32_bf16 v[84:87], a[92:95], a[20:23], v[84:87]\n" \
"v_mfma_f32_16x16x32_bf16 v[92:95], a[88:91], a[24:27], v[92:95]\n" \
"v_mfma_f32_16x16x32_bf16 v[92:95], a[92:95], a[28:31], v[92:95]\n" \
"s_waitcnt lgkmcnt(12)\n" \
"v_mfma_f32_16x16x32_bf16 v[96:99], a[96:99], a[200:203], v[96:99]\n" \
"s_mov_b32 s46, 1280\n" \
"s_add_u32 m0, s50, 16384\n" \
"ds_read_b64_tr_b16 a[80:81], v9 offset:34304\n" \
"v_mfma_f32_16x16x32_bf16 v[96:99], a[100:103], a[204:207], v[96:99]\n" \
"buffer_load_dwordx4 v17, s[28:31], s46 offen lds\n" \
"v_mfma_f32_16x16x32_bf16 v[104:107], a[96:99], a[208:211], v[104:107]\n" \
"ds_read_b64_tr_b16 a[82:83], v10 offset:34304\n" \
"v_mfma_f32_16x16x32_bf16 v[104:107], a[100:103], a[212:215], v[104:107]\n" \
"ds_read_b64_tr_b16 a[84:85], v9 offset:42496\n" \
"v_mfma_f32_16x16x32_bf16 v[112:115], a[96:99], a[216:219], v[112:115]\n" \
"s_add_u32 m0, s50, 20480\n" \
"ds_read_b64_tr_b16 a[86:87], v10 offset:42496\n" \
"v_mfma_f32_16x16x32_bf16 v[112:115], a[100:103], a[220:223], v[112:115]\n" \
"buffer_load_dwordx4 v18, s[28:31], s46 offen lds\n" \
"v_mfma_f32_16x16x32_bf16 v[120:123], a[96:99], a[224:227], v[120:123]\n" \
"s_waitcnt lgkmcnt(12)\n" \
"ds_read_b64_tr_b16 a[88:89], v11 offset:34304\n" \
"v_mfma_f32_16x16x32_bf16 v[120:123], a[100:103], a[228:231], v[120:123]\n" \
"v_mfma_f32_16x16x32_bf16 v[100:103], a[104:107], a[200:203], v[100:103]\n" \
"s_add_u32 m0, s50, 24576\n" \
"ds_read_b64_tr_b16 a[90:91], v12 offset:34304\n" \
"v_mfma_f32_16x16x32_bf16 v[100:103], a[108:111], a[204:207], v[100:103]\n" \
"buffer_load_dwordx4 v19, s[28:31], s46 offen lds\n" \
"v_mfma_f32_16x16x32_bf16 v[108:111], a[104:107], a[208:211], v[108:111]\n" \
"ds_read_b64_tr_b16 a[92:93], v11 offset:42496\n" \
"v_mfma_f32_16x16x32_bf16 v[108:111], a[108:111], a[212:215], v[108:111]\n" \
"ds_read_b64_tr_b16 a[94:95], v12 offset:42496\n" \
"v_mfma_f32_16x16x32_bf16 v[116:119], a[104:107], a[216:219], v[116:119]\n" \
"s_add_u32 m0, s50, 28672\n" \
"ds_read_b64_tr_b16 a[96:97], v9 offset:32768\n" \
"v_mfma_f32_16x16x32_bf16 v[116:119], a[108:111], a[220:223], v[116:119]\n" \
"buffer_load_dwordx4 v20, s[28:31], s46 offen lds\n" \
"v_mfma_f32_16x16x32_bf16 v[124:127], a[104:107], a[224:227], v[124:127]\n" \
"s_waitcnt lgkmcnt(13)\n" \
"ds_read_b64_tr_b16 a[98:99], v10 offset:32768\n" \
"v_mfma_f32_16x16x32_bf16 v[124:127], a[108:111], a[228:231], v[124:127]\n" \
"ds_read_b64_tr_b16 a[100:101], v9 offset:40960\n" \
"v_mfma_f32_16x16x32_bf16 v[128:131], a[64:67], a[200:203], v[128:131]\n" \
"s_mov_b32 s47, 5767168\n" \
"s_add_u32 m0, s51, 16384\n" \
"ds_read_b64_tr_b16 a[102:103], v10 offset:40960\n" \
"v_mfma_f32_16x16x32_bf16 v[128:131], a[68:71], a[204:207], v[128:131]\n" \
"buffer_load_dwordx4 v21, s[32:35], s47 offen offset:0 lds\n" \
"v_mfma_f32_16x16x32_bf16 v[136:139], a[64:67], a[208:211], v[136:139]\n" \
"ds_read_b64_tr_b16 a[104:105], v11 offset:32768\n" \
"v_mfma_f32_16x16x32_bf16 v[136:139], a[68:71], a[212:215], v[136:139]\n" \
"v_mfma_f32_16x16x32_bf16 v[144:147], a[64:67], a[216:219], v[144:147]\n" \
"s_add_u32 m0, s51, 17280\n" \
"ds_read_b64_tr_b16 a[106:107], v12 offset:32768\n" \
"v_mfma_f32_16x16x32_bf16 v[144:147], a[68:71], a[220:223], v[144:147]\n" \
"buffer_load_dwordx4 v21, s[32:35], s47 offen offset:128 lds\n" \
"v_mfma_f32_16x16x32_bf16 v[152:155], a[64:67], a[224:227], v[152:155]\n" \
"s_waitcnt lgkmcnt(14)\n" \
"ds_read_b64_tr_b16 a[108:109], v11 offset:40960\n" \
"v_mfma_f32_16x16x32_bf16 v[152:155], a[68:71], a[228:231], v[152:155]\n" \
"ds_read_b64_tr_b16 a[110:111], v12 offset:40960\n" \
"v_mfma_f32_16x16x32_bf16 v[132:135], a[72:75], a[200:203], v[132:135]\n" \
"s_mov_b32 s47, 6029312\n" \
"s_add_u32 m0, s51, 24576\n" \
"v_mfma_f32_16x16x32_bf16 v[132:135], a[76:79], a[204:207], v[132:135]\n" \
"buffer_load_dwordx4 v21, s[32:35], s47 offen offset:0 lds\n" \
"v_mfma_f32_16x16x32_bf16 v[140:143], a[72:75], a[208:211], v[140:143]\n" \
"v_mfma_f32_16x16x32_bf16 v[140:143], a[76:79], a[212:215], v[140:143]\n" \
"v_mfma_f32_16x16x32_bf16 v[148:151], a[72:75], a[216:219], v[148:151]\n" \
"s_add_u32 m0, s51, 25472\n" \
"v_mfma_f32_16x16x32_bf16 v[148:151], a[76:79], a[220:223], v[148:151]\n" \
"buffer_load_dwordx4 v21, s[32:35], s47 offen offset:128 lds\n" \
"v_mfma_f32_16x16x32_bf16 v[156:159], a[72:75], a[224:227], v[156:159]\n" \
"s_waitcnt vmcnt(16) lgkmcnt(0)\n" \
"v_mfma_f32_16x16x32_bf16 v[156:159], a[76:79], a[228:231], v[156:159]\n" \
"s_barrier\n" \
"v_mfma_f32_16x16x32_bf16 v[160:163], a[80:83], a[200:203], v[160:163]\n" \
"ds_read_b64_tr_b16 a[64:65], v9 offset:49664\n" \
"v_mfma_f32_16x16x32_bf16 v[160:163], a[84:87], a[204:207], v[160:163]\n" \
"ds_read_b64_tr_b16 a[66:67], v10 offset:49664\n" \
"v_mfma_f32_16x16x32_bf16 v[168:171], a[80:83], a[208:211], v[168:171]\n" \
"s_mov_b32 s46, 1408\n" \
"s_add_u32 m0, s50, 32768\n" \
"ds_read_b64_tr_b16 a[68:69], v9 offset:57856\n" \
"v_mfma_f32_16x16x32_bf16 v[168:171], a[84:87], a[212:215], v[168:171]\n" \
"buffer_load_dwordx4 v13, s[28:31], s46 offen lds\n" \
"v_mfma_f32_16x16x32_bf16 v[176:179], a[80:83], a[216:219], v[176:179]\n" \
"ds_read_b64_tr_b16 a[70:71], v10 offset:57856\n" \
"v_mfma_f32_16x16x32_bf16 v[176:179], a[84:87], a[220:223], v[176:179]\n" \
"ds_read_b128 a[0:3], v7 offset:0\n" \
"v_mfma_f32_16x16x32_bf16 v[184:187], a[80:83], a[224:227], v[184:187]\n" \
"ds_read_b128 a[4:7], v8 offset:0\n" \
"v_mfma_f32_16x16x32_bf16 v[184:187], a[84:87], a[228:231], v[184:187]\n" \
"ds_read_b128 a[8:11], v7 offset:2048\n" \
"v_mfma_f32_16x16x32_bf16 v[164:167], a[88:91], a[200:203], v[164:167]\n" \
"ds_read_b128 a[12:15], v8 offset:2048\n" \
"v_mfma_f32_16x16x32_bf16 v[164:167], a[92:95], a[204:207], v[164:167]\n" \
"ds_read_b128 a[16:19], v7 offset:4096\n" \
"v_mfma_f32_16x16x32_bf16 v[172:175], a[88:91], a[208:211], v[172:175]\n" \
"s_add_u32 m0, s50, 36864\n" \
"ds_read_b128 a[20:23], v8 offset:4096\n" \
"v_mfma_f32_16x16x32_bf16 v[172:175], a[92:95], a[212:215], v[172:175]\n" \
"buffer_load_dwordx4 v14, s[28:31], s46 offen lds\n" \
"v_mfma_f32_16x16x32_bf16 v[180:183], a[88:91], a[216:219], v[180:183]\n" \
"ds_read_b128 a[24:27], v7 offset:6144\n" \
"v_mfma_f32_16x16x32_bf16 v[180:183], a[92:95], a[220:223], v[180:183]\n" \
"ds_read_b128 a[28:31], v8 offset:6144\n" \
"v_mfma_f32_16x16x32_bf16 v[188:191], a[88:91], a[224:227], v[188:191]\n" \
"ds_read_b64_tr_b16 a[72:73], v11 offset:49664\n" \
"v_mfma_f32_16x16x32_bf16 v[188:191], a[92:95], a[228:231], v[188:191]\n" \
"ds_read_b64_tr_b16 a[74:75], v12 offset:49664\n" \
"v_mfma_f32_16x16x32_bf16 v[64:67], a[96:99], a[200:203], v[64:67]\n" \
"ds_read_b64_tr_b16 a[76:77], v11 offset:57856\n" \
"v_mfma_f32_16x16x32_bf16 v[64:67], a[100:103], a[204:207], v[64:67]\n" \
"ds_read_b64_tr_b16 a[78:79], v12 offset:57856\n" \
"v_mfma_f32_16x16x32_bf16 v[72:75], a[96:99], a[208:211], v[72:75]\n" \
"s_add_u32 m0, s50, 40960\n" \
"ds_read_b64_tr_b16 a[80:81], v9 offset:50176\n" \
"v_mfma_f32_16x16x32_bf16 v[72:75], a[100:103], a[212:215], v[72:75]\n" \
"buffer_load_dwordx4 v15, s[28:31], s46 offen lds\n" \
"v_mfma_f32_16x16x32_bf16 v[80:83], a[96:99], a[216:219], v[80:83]\n" \
"ds_read_b64_tr_b16 a[82:83], v10 offset:50176\n" \
"v_mfma_f32_16x16x32_bf16 v[80:83], a[100:103], a[220:223], v[80:83]\n" \
"ds_read_b64_tr_b16 a[84:85], v9 offset:58368\n" \
"v_mfma_f32_16x16x32_bf16 v[88:91], a[96:99], a[224:227], v[88:91]\n" \
"ds_read_b64_tr_b16 a[86:87], v10 offset:58368\n" \
"v_mfma_f32_16x16x32_bf16 v[88:91], a[100:103], a[228:231], v[88:91]\n" \
"ds_read_b64_tr_b16 a[88:89], v11 offset:50176\n" \
"v_mfma_f32_16x16x32_bf16 v[68:71], a[104:107], a[200:203], v[68:71]\n" \
"ds_read_b64_tr_b16 a[90:91], v12 offset:50176\n" \
"v_mfma_f32_16x16x32_bf16 v[68:71], a[108:111], a[204:207], v[68:71]\n" \
"ds_read_b64_tr_b16 a[92:93], v11 offset:58368\n" \
"v_mfma_f32_16x16x32_bf16 v[76:79], a[104:107], a[208:211], v[76:79]\n" \
"s_add_u32 m0, s50, 45056\n" \
"ds_read_b64_tr_b16 a[94:95], v12 offset:58368\n" \
"v_mfma_f32_16x16x32_bf16 v[76:79], a[108:111], a[212:215], v[76:79]\n" \
"buffer_load_dwordx4 v16, s[28:31], s46 offen lds\n" \
"v_mfma_f32_16x16x32_bf16 v[84:87], a[104:107], a[216:219], v[84:87]\n" \
"v_mfma_f32_16x16x32_bf16 v[84:87], a[108:111], a[220:223], v[84:87]\n" \
"v_mfma_f32_16x16x32_bf16 v[92:95], a[104:107], a[224:227], v[92:95]\n" \
"v_mfma_f32_16x16x32_bf16 v[92:95], a[108:111], a[228:231], v[92:95]\n" \
"s_waitcnt lgkmcnt(12)\n" \
"v_mfma_f32_16x16x32_bf16 v[96:99], a[64:67], a[0:3], v[96:99]\n" \
"s_mov_b32 s46, 1408\n" \
"s_add_u32 m0, s50, 49152\n" \
"ds_read_b64_tr_b16 a[96:97], v9 offset:50688\n" \
"v_mfma_f32_16x16x32_bf16 v[96:99], a[68:71], a[4:7], v[96:99]\n" \
"buffer_load_dwordx4 v17, s[28:31], s46 offen lds\n" \
"v_mfma_f32_16x16x32_bf16 v[104:107], a[64:67], a[8:11], v[104:107]\n" \
"s_bfe_u32 s89, %1, 0x20001\n" \
"ds_read_b64_tr_b16 a[98:99], v10 offset:50688\n" \
"s_and_b32 s90, %1, 1\n" \
"v_mfma_f32_16x16x32_bf16 v[104:107], a[68:71], a[12:15], v[104:107]\n" \
"s_lshl_b32 s90, s90, 2\n" \
"ds_read_b64_tr_b16 a[100:101], v9 offset:58880\n" \
"s_bfe_u32 s92, %1, 0x20003\n" \
"v_mfma_f32_16x16x32_bf16 v[112:115], a[64:67], a[16:19], v[112:115]\n" \
"s_add_u32 m0, s50, 53248\n" \
"ds_read_b64_tr_b16 a[102:103], v10 offset:58880\n" \
"s_add_u32 s90, s90, s92\n" \
"v_mfma_f32_16x16x32_bf16 v[112:115], a[68:71], a[20:23], v[112:115]\n" \
"buffer_load_dwordx4 v18, s[28:31], s46 offen lds\n" \
"v_mfma_f32_16x16x32_bf16 v[120:123], a[64:67], a[24:27], v[120:123]\n" \
"s_waitcnt lgkmcnt(12)\n" \
"ds_read_b64_tr_b16 a[104:105], v11 offset:50688\n" \
"s_lshl_b32 s92, %1, 2\n" \
"v_mfma_f32_16x16x32_bf16 v[120:123], a[68:71], a[28:31], v[120:123]\n" \
"s_add_u32 s92, s92, %2\n" \
"s_mul_i32 s93, s92, 12288\n" \
"s_add_u32 s78, s93, 109051904\n" \
"v_mfma_f32_16x16x32_bf16 v[100:103], a[72:75], a[0:3], v[100:103]\n" \
"s_add_u32 m0, s50, 57344\n" \
"ds_read_b64_tr_b16 a[106:107], v12 offset:50688\n" \
"s_lshl_b32 s88, s92, 3\n" \
"v_mfma_f32_16x16x32_bf16 v[100:103], a[76:79], a[4:7], v[100:103]\n" \
"buffer_load_dwordx4 v19, s[28:31], s46 offen lds\n" \
"v_mfma_f32_16x16x32_bf16 v[108:111], a[72:75], a[8:11], v[108:111]\n" \
"s_add_u32 s88, s88, 94404608\n" \
"ds_read_b64_tr_b16 a[108:109], v11 offset:58880\n" \
"v_mfma_f32_16x16x32_bf16 v[108:111], a[76:79], a[12:15], v[108:111]\n" \
"s_cmp_eq_u32 s92, 0\ns_cselect_b32 s12, 94371840, -1\n" \
"ds_read_b64_tr_b16 a[110:111], v12 offset:58880\n" \
"v_mfma_f32_16x16x32_bf16 v[116:119], a[72:75], a[16:19], v[116:119]\n" \
"s_add_u32 m0, s50, 61440\n" \
"ds_read_b64_tr_b16 a[64:65], v9 offset:49152\n" \
"s_mul_i32 s80, s90, 12\n" \
"v_mfma_f32_16x16x32_bf16 v[116:119], a[76:79], a[20:23], v[116:119]\n" \
"buffer_load_dwordx4 v20, s[28:31], s46 offen lds\n" \
"v_mfma_f32_16x16x32_bf16 v[124:127], a[72:75], a[24:27], v[124:127]\n" \
"s_waitcnt lgkmcnt(13)\n" \
"ds_read_b64_tr_b16 a[66:67], v10 offset:49152\n" \
"s_add_u32 s80, s80, s89\n" \
"v_mfma_f32_16x16x32_bf16 v[124:127], a[76:79], a[28:31], v[124:127]\n" \
"s_lshl_b32 s80, s80, 16\n" \
"ds_read_b64_tr_b16 a[68:69], v9 offset:57344\n" \
"s_add_u32 s92, %2, 0\n" \
"v_mfma_f32_16x16x32_bf16 v[128:131], a[80:83], a[0:3], v[128:131]\n" \
"s_mov_b32 s47, 6291456\n" \
"s_add_u32 m0, s51, 32768\n" \
"ds_read_b64_tr_b16 a[70:71], v10 offset:57344\n" \
"v_mfma_f32_16x16x32_bf16 v[128:131], a[84:87], a[4:7], v[128:131]\n" \
"buffer_load_dwordx4 v21, s[32:35], s47 offen offset:0 lds\n" \
"v_mfma_f32_16x16x32_bf16 v[136:139], a[80:83], a[8:11], v[136:139]\n" \
"s_mul_i32 s93, s92, 43\n" \
"ds_read_b64_tr_b16 a[72:73], v11 offset:49152\n" \
"s_lshr_b32 s93, s93, 7\n" \
"v_mfma_f32_16x16x32_bf16 v[136:139], a[84:87], a[12:15], v[136:139]\n" \
"s_lshl_b32 s93, s93, 12\n" \
"s_add_u32 s64, s80, s93\n" \
"s_add_u32 s92, s92, s89\n" \
"v_mfma_f32_16x16x32_bf16 v[144:147], a[80:83], a[16:19], v[144:147]\n" \
"s_add_u32 m0, s51, 33664\n" \
"ds_read_b64_tr_b16 a[74:75], v12 offset:49152\n" \
"s_add_u32 s92, s92, 1\n" \
"v_mfma_f32_16x16x32_bf16 v[144:147], a[84:87], a[20:23], v[144:147]\n" \
"buffer_load_dwordx4 v21, s[32:35], s47 offen offset:128 lds\n" \
"v_mfma_f32_16x16x32_bf16 v[152:155], a[80:83], a[24:27], v[152:155]\n" \
"LH12_%=:\ns_cbranch_execz LHR0_%=\n" \
"s_waitcnt lgkmcnt(14)\n" \
"ds_read_b64_tr_b16 a[76:77], v11 offset:57344\n" \
"s_mul_i32 s93, s92, 43\n" \
"v_mfma_f32_16x16x32_bf16 v[152:155], a[84:87], a[28:31], v[152:155]\n" \
"s_lshr_b32 s93, s93, 7\n" \
"ds_read_b64_tr_b16 a[78:79], v12 offset:57344\n" \
"s_mul_i32 s93, s93, 3\n" \
"v_mfma_f32_16x16x32_bf16 v[132:135], a[88:91], a[0:3], v[132:135]\n" \
"s_mov_b32 s47, 6553600\n" \
"s_add_u32 m0, s51, 40960\n" \
"s_sub_u32 s93, s92, s93\n" \
"v_mfma_f32_16x16x32_bf16 v[132:135], a[92:95], a[4:7], v[132:135]\n" \
"buffer_load_dwordx4 v21, s[32:35], s47 offen offset:0 lds\n" \
"v_mfma_f32_16x16x32_bf16 v[140:143], a[88:91], a[8:11], v[140:143]\n" \
"s_lshl_b32 s93, s93, 18\n" \
"s_add_u32 s64, s64, s93\n" \
"s_add_u32 s92, %2, 4\n" \
"v_mfma_f32_16x16x32_bf16 v[140:143], a[92:95], a[12:15], v[140:143]\n" \
"s_mul_i32 s93, s92, 43\n" \
"s_lshr_b32 s93, s93, 7\n" \
"s_lshl_b32 s93, s93, 12\n" \
"v_mfma_f32_16x16x32_bf16 v[148:151], a[88:91], a[16:19], v[148:151]\n" \
"s_add_u32 m0, s51, 41856\n" \
"s_add_u32 s65, s80, s93\n" \
"s_add_u32 s92, s92, s89\n" \
"v_mfma_f32_16x16x32_bf16 v[148:151], a[92:95], a[20:23], v[148:151]\n" \
"buffer_load_dwordx4 v21, s[32:35], s47 offen offset:128 lds\n" \
"v_mfma_f32_16x16x32_bf16 v[156:159], a[88:91], a[24:27], v[156:159]\n" \
"s_waitcnt vmcnt(16) lgkmcnt(0)\n" \
"s_add_u32 s92, s92, 1\n" \
"s_mul_i32 s93, s92, 43\n" \
"v_mfma_f32_16x16x32_bf16 v[156:159], a[92:95], a[28:31], v[156:159]\n" \
"s_barrier\n" \
"s_lshr_b32 s93, s93, 7\n" \
"s_mul_i32 s93, s93, 3\n" \
"v_mfma_f32_16x16x32_bf16 v[160:163], a[96:99], a[0:3], v[160:163]\n" \
"s_sub_u32 s93, s92, s93\n" \
"ds_read_b64_tr_b16 a[80:81], v9 offset:512\n" \
"s_lshl_b32 s93, s93, 18\n" \
"v_mfma_f32_16x16x32_bf16 v[160:163], a[100:103], a[4:7], v[160:163]\n" \
"s_add_u32 s65, s65, s93\n" \
"ds_read_b64_tr_b16 a[82:83], v10 offset:512\n" \
"s_add_u32 s92, %2, 8\n" \
"v_mfma_f32_16x16x32_bf16 v[168:171], a[96:99], a[8:11], v[168:171]\n" \
"s_mov_b32 s46, 1536\n" \
"s_add_u32 m0, s50, 65536\n" \
"ds_read_b64_tr_b16 a[84:85], v9 offset:8704\n" \
"v_mov_b32 v58, s14\n" \
"v_mfma_f32_16x16x32_bf16 v[168:171], a[100:103], a[12:15], v[168:171]\n" \
"buffer_load_dwordx4 v13, s[28:31], s46 offen lds\n" \
"v_mov_b32 v59, s15\n" \
"v_mfma_f32_16x16x32_bf16 v[176:179], a[96:99], a[16:19], v[176:179]\n" \
"s_mul_i32 s93, s92, 43\n" \
"ds_read_b64_tr_b16 a[86:87], v10 offset:8704\n" \
"s_lshr_b32 s93, s93, 7\n" \
"v_mfma_f32_16x16x32_bf16 v[176:179], a[100:103], a[20:23], v[176:179]\n" \
"s_lshl_b32 s93, s93, 12\n" \
"ds_read_b128 a[200:203], v5 offset:0\n" \
"s_add_u32 s66, s80, s93\n" \
"v_mfma_f32_16x16x32_bf16 v[184:187], a[96:99], a[24:27], v[184:187]\n" \
"s_add_u32 s92, s92, s89\n" \
"ds_read_b128 a[204:207], v6 offset:0\n" \
"s_add_u32 s92, s92, 1\n" \
"v_mfma_f32_16x16x32_bf16 v[184:187], a[100:103], a[28:31], v[184:187]\n" \
"s_mul_i32 s93, s92, 43\n" \
"ds_read_b128 a[208:211], v5 offset:2048\n" \
"s_lshr_b32 s93, s93, 7\n" \
"v_mfma_f32_16x16x32_bf16 v[164:167], a[104:107], a[0:3], v[164:167]\n" \
"s_mul_i32 s93, s93, 3\n" \
"ds_read_b128 a[212:215], v6 offset:2048\n" \
"s_sub_u32 s93, s92, s93\n" \
"v_mfma_f32_16x16x32_bf16 v[164:167], a[108:111], a[4:7], v[164:167]\n" \
"s_lshl_b32 s93, s93, 18\n" \
"ds_read_b128 a[216:219], v5 offset:4096\n" \
"s_add_u32 s66, s66, s93\n" \
"v_mfma_f32_16x16x32_bf16 v[172:175], a[104:107], a[8:11], v[172:175]\n" \
"s_add_u32 m0, s50, 69632\n" \
"ds_read_b128 a[220:223], v6 offset:4096\n" \
"s_and_b32 s92, %2, 1\n" \
"v_mfma_f32_16x16x32_bf16 v[172:175], a[108:111], a[12:15], v[172:175]\n" \
"buffer_load_dwordx4 v14, s[28:31], s46 offen lds\n" \
"v_mfma_f32_16x16x32_bf16 v[180:183], a[104:107], a[16:19], v[180:183]\n" \
"s_lshl_b32 s92, s92, 12\n" \
"ds_read_b128 a[224:227], v5 offset:6144\n" \
"s_sub_u32 s57, s66, s64\n" \
"v_mfma_f32_16x16x32_bf16 v[180:183], a[108:111], a[20:23], v[180:183]\n" \
"s_sub_u32 s56, s64, s57\n" \
"ds_read_b128 a[228:231], v6 offset:6144\n" \
"s_sub_u32 s56, s56, s92\n" \
"v_mfma_f32_16x16x32_bf16 v[188:191], a[104:107], a[24:27], v[188:191]\n" \
"s_sub_u32 s59, s64, s65\n" \
"ds_read_b64_tr_b16 a[88:89], v11 offset:512\n" \
"s_add_u32 s59, s59, 16384\n" \
"v_mfma_f32_16x16x32_bf16 v[188:191], a[108:111], a[28:31], v[188:191]\n" \
"s_sub_u32 s58, s65, s59\n" \
"ds_read_b64_tr_b16 a[90:91], v12 offset:512\n" \
"s_sub_u32 s58, s58, s92\n" \
"v_mfma_f32_16x16x32_bf16 v[64:67], a[64:67], a[0:3], v[64:67]\n" \
"s_lshl_b32 s92, s90, 18\n" \
"ds_read_b64_tr_b16 a[92:93], v11 offset:8704\n" \
"s_lshl_b32 s93, s89, 16\n" \
"v_mfma_f32_16x16x32_bf16 v[64:67], a[68:71], a[4:7], v[64:67]\n" \
"s_add_u32 s67, s92, s93\n" \
"ds_read_b64_tr_b16 a[94:95], v12 offset:8704\n" \
"s_add_u32 s67, s67, 6291456\n" \
"v_mfma_f32_16x16x32_bf16 v[72:75], a[64:67], a[8:11], v[72:75]\n" \
"s_add_u32 m0, s50, 73728\n" \
"ds_read_b64_tr_b16 a[96:97], v9 offset:1024\n" \
"s_lshl_b32 s92, s90, 20\n" \
"v_mfma_f32_16x16x32_bf16 v[72:75], a[68:71], a[12:15], v[72:75]\n" \
"buffer_load_dwordx4 v15, s[28:31], s46 offen lds\n" \
"v_mfma_f32_16x16x32_bf16 v[80:83], a[64:67], a[16:19], v[80:83]\n" \
"s_add_u32 s94, s92, s93\n" \
"ds_read_b64_tr_b16 a[98:99], v10 offset:1024\n" \
"s_add_u32 s94, s94, 8388608\n" \
"v_mfma_f32_16x16x32_bf16 v[80:83], a[68:71], a[20:23], v[80:83]\n" \
"s_add_u32 s95, s89, 1\n" \
"ds_read_b64_tr_b16 a[100:101], v9 offset:9216\n" \
"s_and_b32 s95, s95, 3\n" \
"v_mfma_f32_16x16x32_bf16 v[88:91], a[64:67], a[24:27], v[88:91]\n" \
"s_lshl_b32 s92, s95, 18\n" \
"ds_read_b64_tr_b16 a[102:103], v10 offset:9216\n" \
"s_add_u32 s68, s94, s92\n" \
"v_mfma_f32_16x16x32_bf16 v[88:91], a[68:71], a[28:31], v[88:91]\n" \
"ds_read_b64_tr_b16 a[104:105], v11 offset:1024\n" \
"buffer_store_dwordx2 v[58:59], v56, s[36:39], s24 offen sc1\n" \
"v_mfma_f32_16x16x32_bf16 v[68:71], a[72:75], a[0:3], v[68:71]\n" \
"s_sub_u32 s92, s95, s89\n" \
"ds_read_b64_tr_b16 a[106:107], v12 offset:1024\n" \
"s_mul_i32 s93, s92, 98304\n" \
"v_mfma_f32_16x16x32_bf16 v[68:71], a[76:79], a[4:7], v[68:71]\n" \
"s_add_u32 s75, s78, s93\n" \
"ds_read_b64_tr_b16 a[108:109], v11 offset:9216\n" \
"s_add_u32 s75, s75, 6144\n" \
"v_mfma_f32_16x16x32_bf16 v[76:79], a[72:75], a[8:11], v[76:79]\n" \
"s_add_u32 m0, s50, 77824\n" \
"ds_read_b64_tr_b16 a[110:111], v12 offset:9216\n" \
"s_add_u32 s95, s89, 2\n" \
"v_mfma_f32_16x16x32_bf16 v[76:79], a[76:79], a[12:15], v[76:79]\n" \
"buffer_load_dwordx4 v16, s[28:31], s46 offen lds\n" \
"v_mfma_f32_16x16x32_bf16 v[84:87], a[72:75], a[16:19], v[84:87]\n" \
"s_and_b32 s95, s95, 3\n" \
"s_lshl_b32 s92, s95, 18\n" \
"s_add_u32 s69, s94, s92\n" \
"v_mfma_f32_16x16x32_bf16 v[84:87], a[76:79], a[20:23], v[84:87]\n" \
"s_sub_u32 s92, s95, s89\n" \
"s_mul_i32 s93, s92, 98304\n" \
"s_add_u32 s76, s78, s93\n" \
"v_mfma_f32_16x16x32_bf16 v[92:95], a[72:75], a[24:27], v[92:95]\n" \
"s_add_u32 s76, s76, 8192\n" \
"s_add_u32 s95, s89, 3\n" \
"s_and_b32 s95, s95, 3\n" \
"v_mfma_f32_16x16x32_bf16 v[92:95], a[76:79], a[28:31], v[92:95]\n" \
"s_waitcnt lgkmcnt(12)\n" \
"s_lshl_b32 s92, s95, 18\n" \
"s_add_u32 s70, s94, s92\n" \
"v_mfma_f32_16x16x32_bf16 v[96:99], a[80:83], a[200:203], v[96:99]\n" \
"s_mov_b32 s46, 1536\n" \
"s_add_u32 m0, s50, 81920\n" \
"ds_read_b64_tr_b16 a[64:65], v9 offset:1536\n" \
"v_mfma_f32_16x16x32_bf16 v[96:99], a[84:87], a[204:207], v[96:99]\n" \
"buffer_load_dwordx4 v17, s[28:31], s46 offen lds\n" \
"v_mfma_f32_16x16x32_bf16 v[104:107], a[80:83], a[208:211], v[104:107]\n" \
"s_sub_u32 s92, s95, s89\n" \
"ds_read_b64_tr_b16 a[66:67], v10 offset:1536\n" \
"s_mul_i32 s93, s92, 98304\n" \
"v_mfma_f32_16x16x32_bf16 v[104:107], a[84:87], a[212:215], v[104:107]\n" \
"s_add_u32 s77, s78, s93\n" \
"ds_read_b64_tr_b16 a[68:69], v9 offset:9728\n" \
"s_add_u32 s77, s77, 10240\n" \
"v_mfma_f32_16x16x32_bf16 v[112:115], a[80:83], a[216:219], v[112:115]\n" \
"s_add_u32 m0, s50, 86016\n" \
"ds_read_b64_tr_b16 a[70:71], v10 offset:9728\n" \
"s_add_u32 s95, s89, 4\n" \
"v_mfma_f32_16x16x32_bf16 v[112:115], a[84:87], a[220:223], v[112:115]\n" \
"buffer_load_dwordx4 v18, s[28:31], s46 offen lds\n" \
"v_mfma_f32_16x16x32_bf16 v[120:123], a[80:83], a[224:227], v[120:123]\n" \
"s_waitcnt lgkmcnt(12)\n" \
"ds_read_b64_tr_b16 a[72:73], v11 offset:1536\n" \
"s_and_b32 s95, s95, 3\n" \
"v_mfma_f32_16x16x32_bf16 v[120:123], a[84:87], a[228:231], v[120:123]\n" \
"s_lshl_b32 s92, s95, 18\n" \
"s_add_u32 s71, s94, s92\n" \
"s_add_u32 s92, s89, 1\n" \
"v_mfma_f32_16x16x32_bf16 v[100:103], a[88:91], a[200:203], v[100:103]\n" \
"s_add_u32 m0, s50, 90112\n" \
"ds_read_b64_tr_b16 a[74:75], v12 offset:1536\n" \
"s_mul_i32 s95, s92, 43\n" \
"v_mfma_f32_16x16x32_bf16 v[100:103], a[92:95], a[204:207], v[100:103]\n" \
"buffer_load_dwordx4 v19, s[28:31], s46 offen lds\n" \
"v_mfma_f32_16x16x32_bf16 v[108:111], a[88:91], a[208:211], v[108:111]\n" \
"s_lshr_b32 s95, s95, 7\n" \
"ds_read_b64_tr_b16 a[76:77], v11 offset:9728\n" \
"s_mul_i32 s95, s95, 3\n" \
"v_mfma_f32_16x16x32_bf16 v[108:111], a[92:95], a[212:215], v[108:111]\n" \
"s_sub_u32 s95, s92, s95\n" \
"ds_read_b64_tr_b16 a[78:79], v12 offset:9728\n" \
"s_sub_u32 s92, s95, s89\n" \
"v_mfma_f32_16x16x32_bf16 v[116:119], a[88:91], a[216:219], v[116:119]\n" \
"s_add_u32 m0, s50, 94208\n" \
"ds_read_b64_tr_b16 a[80:81], v9 offset:0\n" \
"s_mul_i32 s93, s92, 98304\n" \
"v_mfma_f32_16x16x32_bf16 v[116:119], a[92:95], a[220:223], v[116:119]\n" \
"buffer_load_dwordx4 v20, s[28:31], s46 offen lds\n" \
"v_mfma_f32_16x16x32_bf16 v[124:127], a[88:91], a[224:227], v[124:127]\n" \
"s_waitcnt lgkmcnt(13)\n" \
"ds_read_b64_tr_b16 a[82:83], v10 offset:0\n" \
"s_add_u32 s72, s78, s93\n" \
"v_mfma_f32_16x16x32_bf16 v[124:127], a[92:95], a[228:231], v[124:127]\n" \
"s_sub_u32 s92, s92, 1\n" \
"ds_read_b64_tr_b16 a[84:85], v9 offset:8192\n" \
"s_and_b32 s92, s92, 3\n" \
"v_mfma_f32_16x16x32_bf16 v[128:131], a[96:99], a[200:203], v[128:131]\n" \
"s_mov_b32 s47, 6815744\n" \
"s_add_u32 m0, s51, 49152\n" \
"ds_read_b64_tr_b16 a[86:87], v10 offset:8192\n" \
"v_mfma_f32_16x16x32_bf16 v[128:131], a[100:103], a[204:207], v[128:131]\n" \
"buffer_load_dwordx4 v21, s[32:35], s47 offen offset:0 lds\n" \
"v_mfma_f32_16x16x32_bf16 v[136:139], a[96:99], a[208:211], v[136:139]\n" \
"s_lshl_b32 s92, s92, 11\n" \
"ds_read_b64_tr_b16 a[88:89], v11 offset:0\n" \
"s_add_u32 s72, s72, s92\n" \
"v_mfma_f32_16x16x32_bf16 v[136:139], a[100:103], a[212:215], v[136:139]\n" \
"s_add_u32 s92, s89, 2\n" \
"s_mul_i32 s95, s92, 43\n" \
"s_lshr_b32 s95, s95, 7\n" \
"v_mfma_f32_16x16x32_bf16 v[144:147], a[96:99], a[216:219], v[144:147]\n" \
"s_add_u32 m0, s51, 50048\n" \
"ds_read_b64_tr_b16 a[90:91], v12 offset:0\n" \
"s_mul_i32 s95, s95, 3\n" \
"v_mfma_f32_16x16x32_bf16 v[144:147], a[100:103], a[220:223], v[144:147]\n" \
"buffer_load_dwordx4 v21, s[32:35], s47 offen offset:128 lds\n" \
"v_mfma_f32_16x16x32_bf16 v[152:155], a[96:99], a[224:227], v[152:155]\n" \
"s_waitcnt lgkmcnt(14)\n" \
"ds_read_b64_tr_b16 a[92:93], v11 offset:8192\n" \
"s_sub_u32 s95, s92, s95\n" \
"v_mfma_f32_16x16x32_bf16 v[152:155], a[100:103], a[228:231], v[152:155]\n" \
"s_sub_u32 s92, s95, s89\n" \
"ds_read_b64_tr_b16 a[94:95], v12 offset:8192\n" \
"s_mul_i32 s93, s92, 98304\n" \
"v_mfma_f32_16x16x32_bf16 v[132:135], a[104:107], a[200:203], v[132:135]\n" \
"s_mov_b32 s47, 7077888\n" \
"s_add_u32 m0, s51, 57344\n" \
"s_add_u32 s73, s78, s93\n" \
"v_mfma_f32_16x16x32_bf16 v[132:135], a[108:111], a[204:207], v[132:135]\n" \
"buffer_load_dwordx4 v21, s[32:35], s47 offen offset:0 lds\n" \
"v_mfma_f32_16x16x32_bf16 v[140:143], a[104:107], a[208:211], v[140:143]\n" \
"s_sub_u32 s92, s92, 1\n" \
"s_and_b32 s92, s92, 3\n" \
"s_lshl_b32 s92, s92, 11\n" \
"v_mfma_f32_16x16x32_bf16 v[140:143], a[108:111], a[212:215], v[140:143]\n" \
"s_add_u32 s73, s73, s92\n" \
"s_cmp_lt_u32 s89, 3\ns_cselect_b32 s95, 3, 0\n" \
"v_mfma_f32_16x16x32_bf16 v[148:151], a[104:107], a[216:219], v[148:151]\n" \
"s_add_u32 m0, s51, 58240\n" \
"s_sub_u32 s92, s95, s89\n" \
"s_mul_i32 s93, s92, 98304\n" \
"v_mfma_f32_16x16x32_bf16 v[148:151], a[108:111], a[220:223], v[148:151]\n" \
"buffer_load_dwordx4 v21, s[32:35], s47 offen offset:128 lds\n" \
"v_mfma_f32_16x16x32_bf16 v[156:159], a[104:107], a[224:227], v[156:159]\n" \
"s_waitcnt vmcnt(17) lgkmcnt(0)\n" \
"s_add_u32 s74, s78, s93\n" \
"s_sub_u32 s92, s92, 1\n" \
"v_mfma_f32_16x16x32_bf16 v[156:159], a[108:111], a[228:231], v[156:159]\n" \
"s_barrier\n" \
"s_and_b32 s92, s92, 3\n" \
"s_lshl_b32 s92, s92, 11\n" \
"v_mfma_f32_16x16x32_bf16 v[160:163], a[64:67], a[200:203], v[160:163]\n" \
"s_add_u32 s74, s74, s92\n" \
"ds_read_b64_tr_b16 a[96:97], v9 offset:16896\n" \
"s_mul_i32 s96, s90, 3\n" \
"v_mfma_f32_16x16x32_bf16 v[160:163], a[68:71], a[204:207], v[160:163]\n" \
"s_add_u32 s96, s96, s89\n" \
"ds_read_b64_tr_b16 a[98:99], v10 offset:16896\n" \
"s_add_u32 s92, s90, 24\n" \
"v_mfma_f32_16x16x32_bf16 v[168:171], a[64:67], a[208:211], v[168:171]\n" \
"s_mov_b32 s46, 1664\n" \
"s_add_u32 m0, s50, 0\n" \
"ds_read_b64_tr_b16 a[100:101], v9 offset:25088\n" \
"v_mfma_f32_16x16x32_bf16 v[168:171], a[68:71], a[212:215], v[168:171]\n" \
"buffer_load_dwordx4 v13, s[28:31], s46 offen lds\n" \
"v_mfma_f32_16x16x32_bf16 v[176:179], a[64:67], a[216:219], v[176:179]\n" \
"s_cmp_lt_u32 s89, 3\ns_cselect_b32 s96, s96, s92\n" \
"ds_read_b64_tr_b16 a[102:103], v10 offset:25088\n" \
"v_mfma_f32_16x16x32_bf16 v[176:179], a[68:71], a[220:223], v[176:179]\n" \
"s_lshl_b32 s96, s96, 5\n" \
"ds_read_b128 a[0:3], v5 offset:32768\n" \
"s_lshl_b32 s97, s90, 2\n" \
"v_mfma_f32_16x16x32_bf16 v[184:187], a[64:67], a[224:227], v[184:187]\n" \
"s_add_u32 s97, s97, s89\n" \
"ds_read_b128 a[4:7], v6 offset:32768\n" \
"s_add_u32 s97, s97, 32\n" \
"v_mfma_f32_16x16x32_bf16 v[184:187], a[68:71], a[228:231], v[184:187]\n" \
"s_lshl_b32 s97, s97, 5\n" \
"ds_read_b128 a[8:11], v5 offset:34816\n" \
"s_lshr_b32 s92, %1, 5\n" \
"v_mfma_f32_16x16x32_bf16 v[164:167], a[72:75], a[200:203], v[164:167]\n" \
"s_lshl_b32 s92, s92, 2\n" \
"ds_read_b128 a[12:15], v6 offset:34816\n" \
"s_add_u32 s92, s92, %2\n" \
"v_mfma_f32_16x16x32_bf16 v[164:167], a[76:79], a[204:207], v[164:167]\n" \
"s_lshl_b32 s93, s92, 6\n" \
"ds_read_b128 a[16:19], v5 offset:36864\n" \
"s_lshl_b32 s92, s92, 17\n" \
"v_mfma_f32_16x16x32_bf16 v[172:175], a[72:75], a[208:211], v[172:175]\n" \
"s_add_u32 m0, s50, 4096\n" \
"ds_read_b128 a[20:23], v6 offset:36864\n" \
"s_add_u32 s92, s92, 67108864\n" \
"v_mfma_f32_16x16x32_bf16 v[172:175], a[76:79], a[212:215], v[172:175]\n" \
"buffer_load_dwordx4 v14, s[28:31], s46 offen lds\n" \
"v_mfma_f32_16x16x32_bf16 v[180:183], a[72:75], a[216:219], v[180:183]\n" \
"s_lshl_b32 s82, s96, 11\n" \
"ds_read_b128 a[24:27], v5 offset:38912\n" \
"s_add_u32 s82, s82, s93\n" \
"v_mfma_f32_16x16x32_bf16 v[180:183], a[76:79], a[220:223], v[180:183]\n" \
"s_lshl_b32 s86, s96, 2\n" \
"ds_read_b128 a[28:31], v6 offset:38912\n" \
"s_add_u32 s86, s86, 71303168\n" \
"v_mfma_f32_16x16x32_bf16 v[188:191], a[72:75], a[224:227], v[188:191]\n" \
"s_lshl_b32 s84, s96, 3\n" \
"ds_read_b64_tr_b16 a[104:105], v11 offset:16896\n" \
"s_add_u32 s84, s84, s92\n" \
"v_mfma_f32_16x16x32_bf16 v[188:191], a[76:79], a[228:231], v[188:191]\n" \
"s_and_b32 s96, s96, 32\n" \
"ds_read_b64_tr_b16 a[106:107], v12 offset:16896\n" \
"s_mul_i32 s96, s96, 6\n" \
"v_mfma_f32_16x16x32_bf16 v[64:67], a[80:83], a[200:203], v[64:67]\n" \
"s_sub_u32 s84, s84, s96\n" \
"ds_read_b64_tr_b16 a[108:109], v11 offset:25088\n" \
"s_lshl_b32 s83, s97, 11\n" \
"v_mfma_f32_16x16x32_bf16 v[64:67], a[84:87], a[204:207], v[64:67]\n" \
"s_add_u32 s83, s83, s93\n" \
"ds_read_b64_tr_b16 a[110:111], v12 offset:25088\n" \
"s_lshl_b32 s87, s97, 2\n" \
"v_mfma_f32_16x16x32_bf16 v[72:75], a[80:83], a[208:211], v[72:75]\n" \
"s_add_u32 m0, s50, 8192\n" \
"ds_read_b64_tr_b16 a[64:65], v9 offset:17408\n" \
"s_add_u32 s87, s87, 71303168\n" \
"v_mfma_f32_16x16x32_bf16 v[72:75], a[84:87], a[212:215], v[72:75]\n" \
"buffer_load_dwordx4 v15, s[28:31], s46 offen lds\n" \
"v_mfma_f32_16x16x32_bf16 v[80:83], a[80:83], a[216:219], v[80:83]\n" \
"s_lshl_b32 s85, s97, 3\n" \
"ds_read_b64_tr_b16 a[66:67], v10 offset:17408\n" \
"s_add_u32 s85, s85, s92\n" \
"v_mfma_f32_16x16x32_bf16 v[80:83], a[84:87], a[220:223], v[80:83]\n" \
"s_and_b32 s97, s97, 32\n" \
"ds_read_b64_tr_b16 a[68:69], v9 offset:25600\n" \
"s_mul_i32 s97, s97, 6\n" \
"v_mfma_f32_16x16x32_bf16 v[88:91], a[80:83], a[224:227], v[88:91]\n" \
"s_sub_u32 s85, s85, s97\n" \
"ds_read_b64_tr_b16 a[70:71], v10 offset:25600\n" \
"s_lshl_b32 s93, s89, 6\n" \
"v_mfma_f32_16x16x32_bf16 v[88:91], a[84:87], a[228:231], v[88:91]\n" \
"s_add_u32 s93, s93, 94388224\n" \
"ds_read_b64_tr_b16 a[72:73], v11 offset:17408\n" \
"s_lshl_b32 s92, s90, 8\n" \
"v_mfma_f32_16x16x32_bf16 v[68:71], a[88:91], a[200:203], v[68:71]\n" \
"s_add_u32 s10, s93, s92\n" \
"ds_read_b64_tr_b16 a[74:75], v12 offset:17408\n" \
"s_add_u32 s10, s10, 6144\n" \
"v_mfma_f32_16x16x32_bf16 v[68:71], a[92:95], a[204:207], v[68:71]\n" \
"s_lshl_b32 s92, s90, 10\n" \
"ds_read_b64_tr_b16 a[76:77], v11 offset:25600\n" \
"s_add_u32 s11, s93, s92\n" \
"v_mfma_f32_16x16x32_bf16 v[76:79], a[88:91], a[208:211], v[76:79]\n" \
"s_add_u32 m0, s50, 12288\n" \
"ds_read_b64_tr_b16 a[78:79], v12 offset:25600\n" \
"s_add_u32 s11, s11, 8192\n" \
"v_mfma_f32_16x16x32_bf16 v[76:79], a[92:95], a[212:215], v[76:79]\n" \
"buffer_load_dwordx4 v16, s[28:31], s46 offen lds\n" \
"v_mfma_f32_16x16x32_bf16 v[84:87], a[88:91], a[216:219], v[84:87]\n" \
"s_mul_i32 s92, s90, 768\n" \
"s_add_u32 s98, s93, s92\n" \
"v_mfma_f32_16x16x32_bf16 v[84:87], a[92:95], a[220:223], v[84:87]\n" \
"v_mfma_f32_16x16x32_bf16 v[92:95], a[88:91], a[224:227], v[92:95]\n" \
"v_mfma_f32_16x16x32_bf16 v[92:95], a[92:95], a[228:231], v[92:95]\n" \
"s_waitcnt lgkmcnt(12)\n" \
"v_mfma_f32_16x16x32_bf16 v[96:99], a[96:99], a[0:3], v[96:99]\n" \
"s_mov_b32 s46, 1664\n" \
"s_add_u32 m0, s50, 16384\n" \
"ds_read_b64_tr_b16 a[80:81], v9 offset:17920\n" \
"v_mfma_f32_16x16x32_bf16 v[96:99], a[100:103], a[4:7], v[96:99]\n" \
"buffer_load_dwordx4 v17, s[28:31], s46 offen lds\n" \
"v_mfma_f32_16x16x32_bf16 v[104:107], a[96:99], a[8:11], v[104:107]\n" \
"ds_read_b64_tr_b16 a[82:83], v10 offset:17920\n" \
"v_mfma_f32_16x16x32_bf16 v[104:107], a[100:103], a[12:15], v[104:107]\n" \
"ds_read_b64_tr_b16 a[84:85], v9 offset:26112\n" \
"v_mfma_f32_16x16x32_bf16 v[112:115], a[96:99], a[16:19], v[112:115]\n" \
"s_add_u32 m0, s50, 20480\n" \
"ds_read_b64_tr_b16 a[86:87], v10 offset:26112\n" \
"v_mfma_f32_16x16x32_bf16 v[112:115], a[100:103], a[20:23], v[112:115]\n" \
"buffer_load_dwordx4 v18, s[28:31], s46 offen lds\n" \
"v_mfma_f32_16x16x32_bf16 v[120:123], a[96:99], a[24:27], v[120:123]\n" \
"s_waitcnt lgkmcnt(12)\n" \
"ds_read_b64_tr_b16 a[88:89], v11 offset:17920\n" \
"v_mfma_f32_16x16x32_bf16 v[120:123], a[100:103], a[28:31], v[120:123]\n" \
"v_mfma_f32_16x16x32_bf16 v[100:103], a[104:107], a[0:3], v[100:103]\n" \
"s_add_u32 m0, s50, 24576\n" \
"ds_read_b64_tr_b16 a[90:91], v12 offset:17920\n" \
"v_mfma_f32_16x16x32_bf16 v[100:103], a[108:111], a[4:7], v[100:103]\n" \
"buffer_load_dwordx4 v19, s[28:31], s46 offen lds\n" \
"v_mfma_f32_16x16x32_bf16 v[108:111], a[104:107], a[8:11], v[108:111]\n" \
"ds_read_b64_tr_b16 a[92:93], v11 offset:26112\n" \
"v_mfma_f32_16x16x32_bf16 v[108:111], a[108:111], a[12:15], v[108:111]\n" \
"ds_read_b64_tr_b16 a[94:95], v12 offset:26112\n" \
"v_mfma_f32_16x16x32_bf16 v[116:119], a[104:107], a[16:19], v[116:119]\n" \
"s_add_u32 m0, s50, 28672\n" \
"ds_read_b64_tr_b16 a[96:97], v9 offset:16384\n" \
"v_mfma_f32_16x16x32_bf16 v[116:119], a[108:111], a[20:23], v[116:119]\n" \
"buffer_load_dwordx4 v20, s[28:31], s46 offen lds\n" \
"v_mfma_f32_16x16x32_bf16 v[124:127], a[104:107], a[24:27], v[124:127]\n" \
"s_waitcnt lgkmcnt(13)\n" \
"ds_read_b64_tr_b16 a[98:99], v10 offset:16384\n" \
"v_mfma_f32_16x16x32_bf16 v[124:127], a[108:111], a[28:31], v[124:127]\n" \
"ds_read_b64_tr_b16 a[100:101], v9 offset:24576\n" \
"v_mfma_f32_16x16x32_bf16 v[128:131], a[64:67], a[0:3], v[128:131]\n" \
"s_mov_b32 s47, 7340032\n" \
"s_add_u32 m0, s51, 0\n" \
"ds_read_b64_tr_b16 a[102:103], v10 offset:24576\n" \
"v_mfma_f32_16x16x32_bf16 v[128:131], a[68:71], a[4:7], v[128:131]\n" \
"buffer_load_dwordx4 v21, s[32:35], s47 offen offset:0 lds\n" \
"v_mfma_f32_16x16x32_bf16 v[136:139], a[64:67], a[8:11], v[136:139]\n" \
"ds_read_b64_tr_b16 a[104:105], v11 offset:16384\n" \
"v_mfma_f32_16x16x32_bf16 v[136:139], a[68:71], a[12:15], v[136:139]\n" \
"v_mfma_f32_16x16x32_bf16 v[144:147], a[64:67], a[16:19], v[144:147]\n" \
"s_add_u32 m0, s51, 896\n" \
"ds_read_b64_tr_b16 a[106:107], v12 offset:16384\n" \
"v_mfma_f32_16x16x32_bf16 v[144:147], a[68:71], a[20:23], v[144:147]\n" \
"buffer_load_dwordx4 v21, s[32:35], s47 offen offset:128 lds\n" \
"v_mfma_f32_16x16x32_bf16 v[152:155], a[64:67], a[24:27], v[152:155]\n" \
"s_waitcnt lgkmcnt(14)\n" \
"ds_read_b64_tr_b16 a[108:109], v11 offset:24576\n" \
"v_mfma_f32_16x16x32_bf16 v[152:155], a[68:71], a[28:31], v[152:155]\n" \
"ds_read_b64_tr_b16 a[110:111], v12 offset:24576\n" \
"v_mfma_f32_16x16x32_bf16 v[132:135], a[72:75], a[0:3], v[132:135]\n" \
"s_mov_b32 s47, 7602176\n" \
"s_add_u32 m0, s51, 8192\n" \
"v_mfma_f32_16x16x32_bf16 v[132:135], a[76:79], a[4:7], v[132:135]\n" \
"buffer_load_dwordx4 v21, s[32:35], s47 offen offset:0 lds\n" \
"v_mfma_f32_16x16x32_bf16 v[140:143], a[72:75], a[8:11], v[140:143]\n" \
"v_mfma_f32_16x16x32_bf16 v[140:143], a[76:79], a[12:15], v[140:143]\n" \
"v_mfma_f32_16x16x32_bf16 v[148:151], a[72:75], a[16:19], v[148:151]\n" \
"s_add_u32 m0, s51, 9088\n" \
"v_mfma_f32_16x16x32_bf16 v[148:151], a[76:79], a[20:23], v[148:151]\n" \
"buffer_load_dwordx4 v21, s[32:35], s47 offen offset:128 lds\n" \
"v_mfma_f32_16x16x32_bf16 v[156:159], a[72:75], a[24:27], v[156:159]\n" \
"s_waitcnt vmcnt(16) lgkmcnt(0)\n" \
"v_mfma_f32_16x16x32_bf16 v[156:159], a[76:79], a[28:31], v[156:159]\n" \
"s_barrier\n" \
"v_mfma_f32_16x16x32_bf16 v[160:163], a[80:83], a[0:3], v[160:163]\n" \
"ds_read_b64_tr_b16 a[64:65], v9 offset:33280\n" \
"v_mfma_f32_16x16x32_bf16 v[160:163], a[84:87], a[4:7], v[160:163]\n" \
"ds_read_b64_tr_b16 a[66:67], v10 offset:33280\n" \
"v_mfma_f32_16x16x32_bf16 v[168:171], a[80:83], a[8:11], v[168:171]\n" \
"s_mov_b32 s46, 1792\n" \
"s_add_u32 m0, s50, 32768\n" \
"ds_read_b64_tr_b16 a[68:69], v9 offset:41472\n" \
"v_mfma_f32_16x16x32_bf16 v[168:171], a[84:87], a[12:15], v[168:171]\n" \
"buffer_load_dwordx4 v13, s[28:31], s46 offen lds\n" \
"v_mfma_f32_16x16x32_bf16 v[176:179], a[80:83], a[16:19], v[176:179]\n" \
"ds_read_b64_tr_b16 a[70:71], v10 offset:41472\n" \
"v_mfma_f32_16x16x32_bf16 v[176:179], a[84:87], a[20:23], v[176:179]\n" \
"ds_read_b128 a[200:203], v7 offset:0\n" \
"v_mfma_f32_16x16x32_bf16 v[184:187], a[80:83], a[24:27], v[184:187]\n" \
"ds_read_b128 a[204:207], v8 offset:0\n" \
"v_mfma_f32_16x16x32_bf16 v[184:187], a[84:87], a[28:31], v[184:187]\n" \
"ds_read_b128 a[208:211], v7 offset:2048\n" \
"v_mfma_f32_16x16x32_bf16 v[164:167], a[88:91], a[0:3], v[164:167]\n" \
"ds_read_b128 a[212:215], v8 offset:2048\n" \
"v_mfma_f32_16x16x32_bf16 v[164:167], a[92:95], a[4:7], v[164:167]\n" \
"ds_read_b128 a[216:219], v7 offset:4096\n" \
"v_mfma_f32_16x16x32_bf16 v[172:175], a[88:91], a[8:11], v[172:175]\n" \
"s_add_u32 m0, s50, 36864\n" \
"ds_read_b128 a[220:223], v8 offset:4096\n" \
"v_mfma_f32_16x16x32_bf16 v[172:175], a[92:95], a[12:15], v[172:175]\n" \
"buffer_load_dwordx4 v14, s[28:31], s46 offen lds\n" \
"v_mfma_f32_16x16x32_bf16 v[180:183], a[88:91], a[16:19], v[180:183]\n" \
"ds_read_b128 a[224:227], v7 offset:6144\n" \
"v_mfma_f32_16x16x32_bf16 v[180:183], a[92:95], a[20:23], v[180:183]\n" \
"ds_read_b128 a[228:231], v8 offset:6144\n" \
"v_mfma_f32_16x16x32_bf16 v[188:191], a[88:91], a[24:27], v[188:191]\n" \
"ds_read_b64_tr_b16 a[72:73], v11 offset:33280\n" \
"v_mfma_f32_16x16x32_bf16 v[188:191], a[92:95], a[28:31], v[188:191]\n" \
"ds_read_b64_tr_b16 a[74:75], v12 offset:33280\n" \
"v_mfma_f32_16x16x32_bf16 v[64:67], a[96:99], a[0:3], v[64:67]\n" \
"ds_read_b64_tr_b16 a[76:77], v11 offset:41472\n" \
"v_mfma_f32_16x16x32_bf16 v[64:67], a[100:103], a[4:7], v[64:67]\n" \
"ds_read_b64_tr_b16 a[78:79], v12 offset:41472\n" \
"v_mfma_f32_16x16x32_bf16 v[72:75], a[96:99], a[8:11], v[72:75]\n" \
"s_add_u32 m0, s50, 40960\n" \
"ds_read_b64_tr_b16 a[80:81], v9 offset:33792\n" \
"v_mfma_f32_16x16x32_bf16 v[72:75], a[100:103], a[12:15], v[72:75]\n" \
"buffer_load_dwordx4 v15, s[28:31], s46 offen lds\n" \
"v_mfma_f32_16x16x32_bf16 v[80:83], a[96:99], a[16:19], v[80:83]\n" \
"ds_read_b64_tr_b16 a[82:83], v10 offset:33792\n" \
"v_mfma_f32_16x16x32_bf16 v[80:83], a[100:103], a[20:23], v[80:83]\n" \
"ds_read_b64_tr_b16 a[84:85], v9 offset:41984\n" \
"v_mfma_f32_16x16x32_bf16 v[88:91], a[96:99], a[24:27], v[88:91]\n" \
"ds_read_b64_tr_b16 a[86:87], v10 offset:41984\n" \
"v_mfma_f32_16x16x32_bf16 v[88:91], a[100:103], a[28:31], v[88:91]\n" \
"ds_read_b64_tr_b16 a[88:89], v11 offset:33792\n" \
"v_mfma_f32_16x16x32_bf16 v[68:71], a[104:107], a[0:3], v[68:71]\n" \
"ds_read_b64_tr_b16 a[90:91], v12 offset:33792\n" \
"v_mfma_f32_16x16x32_bf16 v[68:71], a[108:111], a[4:7], v[68:71]\n" \
"ds_read_b64_tr_b16 a[92:93], v11 offset:41984\n" \
"v_mfma_f32_16x16x32_bf16 v[76:79], a[104:107], a[8:11], v[76:79]\n" \
"s_add_u32 m0, s50, 45056\n" \
"ds_read_b64_tr_b16 a[94:95], v12 offset:41984\n" \
"v_mfma_f32_16x16x32_bf16 v[76:79], a[108:111], a[12:15], v[76:79]\n" \
"buffer_load_dwordx4 v16, s[28:31], s46 offen lds\n" \
"v_mfma_f32_16x16x32_bf16 v[84:87], a[104:107], a[16:19], v[84:87]\n" \
"v_mfma_f32_16x16x32_bf16 v[84:87], a[108:111], a[20:23], v[84:87]\n" \
"v_mfma_f32_16x16x32_bf16 v[92:95], a[104:107], a[24:27], v[92:95]\n" \
"v_mfma_f32_16x16x32_bf16 v[92:95], a[108:111], a[28:31], v[92:95]\n" \
"s_waitcnt lgkmcnt(12)\n" \
"v_mfma_f32_16x16x32_bf16 v[96:99], a[64:67], a[200:203], v[96:99]\n" \
"s_mov_b32 s46, 1792\n" \
"s_add_u32 m0, s50, 49152\n" \
"ds_read_b64_tr_b16 a[96:97], v9 offset:34304\n" \
"v_mfma_f32_16x16x32_bf16 v[96:99], a[68:71], a[204:207], v[96:99]\n" \
"buffer_load_dwordx4 v17, s[28:31], s46 offen lds\n" \
"v_mfma_f32_16x16x32_bf16 v[104:107], a[64:67], a[208:211], v[104:107]\n" \
"ds_read_b64_tr_b16 a[98:99], v10 offset:34304\n" \
"v_mfma_f32_16x16x32_bf16 v[104:107], a[68:71], a[212:215], v[104:107]\n" \
"ds_read_b64_tr_b16 a[100:101], v9 offset:42496\n" \
"v_mfma_f32_16x16x32_bf16 v[112:115], a[64:67], a[216:219], v[112:115]\n" \
"s_add_u32 m0, s50, 53248\n" \
"ds_read_b64_tr_b16 a[102:103], v10 offset:42496\n" \
"v_mfma_f32_16x16x32_bf16 v[112:115], a[68:71], a[220:223], v[112:115]\n" \
"buffer_load_dwordx4 v18, s[28:31], s46 offen lds\n" \
"v_mfma_f32_16x16x32_bf16 v[120:123], a[64:67], a[224:227], v[120:123]\n" \
"s_waitcnt lgkmcnt(12)\n" \
"ds_read_b64_tr_b16 a[104:105], v11 offset:34304\n" \
"v_mfma_f32_16x16x32_bf16 v[120:123], a[68:71], a[228:231], v[120:123]\n" \
"v_mfma_f32_16x16x32_bf16 v[100:103], a[72:75], a[200:203], v[100:103]\n" \
"s_add_u32 m0, s50, 57344\n" \
"ds_read_b64_tr_b16 a[106:107], v12 offset:34304\n" \
"v_mfma_f32_16x16x32_bf16 v[100:103], a[76:79], a[204:207], v[100:103]\n" \
"buffer_load_dwordx4 v19, s[28:31], s46 offen lds\n" \
"v_mfma_f32_16x16x32_bf16 v[108:111], a[72:75], a[208:211], v[108:111]\n" \
"ds_read_b64_tr_b16 a[108:109], v11 offset:42496\n" \
"v_mfma_f32_16x16x32_bf16 v[108:111], a[76:79], a[212:215], v[108:111]\n" \
"ds_read_b64_tr_b16 a[110:111], v12 offset:42496\n" \
"v_mfma_f32_16x16x32_bf16 v[116:119], a[72:75], a[216:219], v[116:119]\n" \
"s_add_u32 m0, s50, 61440\n" \
"ds_read_b64_tr_b16 a[64:65], v9 offset:32768\n" \
"v_mfma_f32_16x16x32_bf16 v[116:119], a[76:79], a[220:223], v[116:119]\n" \
"buffer_load_dwordx4 v20, s[28:31], s46 offen lds\n" \
"v_mfma_f32_16x16x32_bf16 v[124:127], a[72:75], a[224:227], v[124:127]\n" \
"s_waitcnt lgkmcnt(13)\n" \
"ds_read_b64_tr_b16 a[66:67], v10 offset:32768\n" \
"v_mfma_f32_16x16x32_bf16 v[124:127], a[76:79], a[228:231], v[124:127]\n" \
"ds_read_b64_tr_b16 a[68:69], v9 offset:40960\n" \
"v_mfma_f32_16x16x32_bf16 v[128:131], a[80:83], a[200:203], v[128:131]\n" \
"s_mov_b32 s47, 7864320\n" \
"s_add_u32 m0, s51, 16384\n" \
"ds_read_b64_tr_b16 a[70:71], v10 offset:40960\n" \
"v_mfma_f32_16x16x32_bf16 v[128:131], a[84:87], a[204:207], v[128:131]\n" \
"buffer_load_dwordx4 v21, s[32:35], s47 offen offset:0 lds\n" \
"v_mfma_f32_16x16x32_bf16 v[136:139], a[80:83], a[208:211], v[136:139]\n" \
"ds_read_b64_tr_b16 a[72:73], v11 offset:32768\n" \
"v_mfma_f32_16x16x32_bf16 v[136:139], a[84:87], a[212:215], v[136:139]\n" \
"v_mfma_f32_16x16x32_bf16 v[144:147], a[80:83], a[216:219], v[144:147]\n" \
"s_add_u32 m0, s51, 17280\n" \
"ds_read_b64_tr_b16 a[74:75], v12 offset:32768\n" \
"v_mfma_f32_16x16x32_bf16 v[144:147], a[84:87], a[220:223], v[144:147]\n" \
"buffer_load_dwordx4 v21, s[32:35], s47 offen offset:128 lds\n" \
"v_mfma_f32_16x16x32_bf16 v[152:155], a[80:83], a[224:227], v[152:155]\n" \
"s_waitcnt lgkmcnt(14)\n" \
"ds_read_b64_tr_b16 a[76:77], v11 offset:40960\n" \
"v_mfma_f32_16x16x32_bf16 v[152:155], a[84:87], a[228:231], v[152:155]\n" \
"ds_read_b64_tr_b16 a[78:79], v12 offset:40960\n" \
"v_mfma_f32_16x16x32_bf16 v[132:135], a[88:91], a[200:203], v[132:135]\n" \
"s_mov_b32 s47, 8126464\n" \
"s_add_u32 m0, s51, 24576\n" \
"v_mfma_f32_16x16x32_bf16 v[132:135], a[92:95], a[204:207], v[132:135]\n" \
"buffer_load_dwordx4 v21, s[32:35], s47 offen offset:0 lds\n" \
"v_mfma_f32_16x16x32_bf16 v[140:143], a[88:91], a[208:211], v[140:143]\n" \
"v_mfma_f32_16x16x32_bf16 v[140:143], a[92:95], a[212:215], v[140:143]\n" \
"v_mfma_f32_16x16x32_bf16 v[148:151], a[88:91], a[216:219], v[148:151]\n" \
"s_add_u32 m0, s51, 25472\n" \
"v_mfma_f32_16x16x32_bf16 v[148:151], a[92:95], a[220:223], v[148:151]\n" \
"buffer_load_dwordx4 v21, s[32:35], s47 offen offset:128 lds\n" \
"v_mfma_f32_16x16x32_bf16 v[156:159], a[88:91], a[224:227], v[156:159]\n" \
"s_waitcnt vmcnt(16) lgkmcnt(0)\n" \
"v_mfma_f32_16x16x32_bf16 v[156:159], a[92:95], a[228:231], v[156:159]\n" \
"s_barrier\n" \
"v_mfma_f32_16x16x32_bf16 v[160:163], a[96:99], a[200:203], v[160:163]\n" \
"LH13_%=:\ns_cbranch_execz LHR0_%=\n" \
"ds_read_b64_tr_b16 a[80:81], v9 offset:49664\n" \
"v_mfma_f32_16x16x32_bf16 v[160:163], a[100:103], a[204:207], v[160:163]\n" \
"ds_read_b64_tr_b16 a[82:83], v10 offset:49664\n" \
"v_mfma_f32_16x16x32_bf16 v[168:171], a[96:99], a[208:211], v[168:171]\n" \
"s_mov_b32 s46, 1920\n" \
"s_add_u32 m0, s50, 65536\n" \
"ds_read_b64_tr_b16 a[84:85], v9 offset:57856\n" \
"v_mfma_f32_16x16x32_bf16 v[168:171], a[100:103], a[212:215], v[168:171]\n" \
"buffer_load_dwordx4 v13, s[28:31], s46 offen lds\n" \
"v_mfma_f32_16x16x32_bf16 v[176:179], a[96:99], a[216:219], v[176:179]\n" \
"ds_read_b64_tr_b16 a[86:87], v10 offset:57856\n" \
"v_mfma_f32_16x16x32_bf16 v[176:179], a[100:103], a[220:223], v[176:179]\n" \
"ds_read_b128 a[0:3], v5 offset:0\n" \
"v_mfma_f32_16x16x32_bf16 v[184:187], a[96:99], a[224:227], v[184:187]\n" \
"ds_read_b128 a[4:7], v6 offset:0\n" \
"v_mfma_f32_16x16x32_bf16 v[184:187], a[100:103], a[228:231], v[184:187]\n" \
"ds_read_b128 a[8:11], v5 offset:2048\n" \
"v_mfma_f32_16x16x32_bf16 v[164:167], a[104:107], a[200:203], v[164:167]\n" \
"ds_read_b128 a[12:15], v6 offset:2048\n" \
"v_mfma_f32_16x16x32_bf16 v[164:167], a[108:111], a[204:207], v[164:167]\n" \
"ds_read_b128 a[16:19], v5 offset:4096\n" \
"v_mfma_f32_16x16x32_bf16 v[172:175], a[104:107], a[208:211], v[172:175]\n" \
"s_add_u32 m0, s50, 69632\n" \
"ds_read_b128 a[20:23], v6 offset:4096\n" \
"v_mfma_f32_16x16x32_bf16 v[172:175], a[108:111], a[212:215], v[172:175]\n" \
"buffer_load_dwordx4 v14, s[28:31], s46 offen lds\n" \
"v_mfma_f32_16x16x32_bf16 v[180:183], a[104:107], a[216:219], v[180:183]\n" \
"ds_read_b128 a[24:27], v5 offset:6144\n" \
"v_mfma_f32_16x16x32_bf16 v[180:183], a[108:111], a[220:223], v[180:183]\n" \
"ds_read_b128 a[28:31], v6 offset:6144\n" \
"v_mfma_f32_16x16x32_bf16 v[188:191], a[104:107], a[224:227], v[188:191]\n" \
"ds_read_b64_tr_b16 a[88:89], v11 offset:49664\n" \
"v_mfma_f32_16x16x32_bf16 v[188:191], a[108:111], a[228:231], v[188:191]\n" \
"ds_read_b64_tr_b16 a[90:91], v12 offset:49664\n" \
"v_mfma_f32_16x16x32_bf16 v[64:67], a[64:67], a[200:203], v[64:67]\n" \
"ds_read_b64_tr_b16 a[92:93], v11 offset:57856\n" \
"v_mfma_f32_16x16x32_bf16 v[64:67], a[68:71], a[204:207], v[64:67]\n" \
"ds_read_b64_tr_b16 a[94:95], v12 offset:57856\n" \
"v_mfma_f32_16x16x32_bf16 v[72:75], a[64:67], a[208:211], v[72:75]\n" \
"s_add_u32 m0, s50, 73728\n" \
"ds_read_b64_tr_b16 a[96:97], v9 offset:50176\n" \
"v_mfma_f32_16x16x32_bf16 v[72:75], a[68:71], a[212:215], v[72:75]\n" \
"buffer_load_dwordx4 v15, s[28:31], s46 offen lds\n" \
"v_mfma_f32_16x16x32_bf16 v[80:83], a[64:67], a[216:219], v[80:83]\n" \
"ds_read_b64_tr_b16 a[98:99], v10 offset:50176\n" \
"v_mfma_f32_16x16x32_bf16 v[80:83], a[68:71], a[220:223], v[80:83]\n" \
"ds_read_b64_tr_b16 a[100:101], v9 offset:58368\n" \
"v_mfma_f32_16x16x32_bf16 v[88:91], a[64:67], a[224:227], v[88:91]\n" \
"ds_read_b64_tr_b16 a[102:103], v10 offset:58368\n" \
"v_mfma_f32_16x16x32_bf16 v[88:91], a[68:71], a[228:231], v[88:91]\n" \
"ds_read_b64_tr_b16 a[104:105], v11 offset:50176\n" \
"v_mfma_f32_16x16x32_bf16 v[68:71], a[72:75], a[200:203], v[68:71]\n" \
"ds_read_b64_tr_b16 a[106:107], v12 offset:50176\n" \
"v_mfma_f32_16x16x32_bf16 v[68:71], a[76:79], a[204:207], v[68:71]\n" \
"ds_read_b64_tr_b16 a[108:109], v11 offset:58368\n" \
"v_mfma_f32_16x16x32_bf16 v[76:79], a[72:75], a[208:211], v[76:79]\n" \
"s_add_u32 m0, s50, 77824\n" \
"ds_read_b64_tr_b16 a[110:111], v12 offset:58368\n" \
"v_mfma_f32_16x16x32_bf16 v[76:79], a[76:79], a[212:215], v[76:79]\n" \
"buffer_load_dwordx4 v16, s[28:31], s46 offen lds\n" \
"v_mfma_f32_16x16x32_bf16 v[84:87], a[72:75], a[216:219], v[84:87]\n" \
"v_mfma_f32_16x16x32_bf16 v[84:87], a[76:79], a[220:223], v[84:87]\n" \
"v_mfma_f32_16x16x32_bf16 v[92:95], a[72:75], a[224:227], v[92:95]\n" \
"v_mfma_f32_16x16x32_bf16 v[92:95], a[76:79], a[228:231], v[92:95]\n" \
"s_waitcnt lgkmcnt(12)\n" \
"v_mfma_f32_16x16x32_bf16 v[96:99], a[80:83], a[0:3], v[96:99]\n" \
"ds_read_b64_tr_b16 a[64:65], v9 offset:50688\n" \
"v_mfma_f32_16x16x32_bf16 v[96:99], a[84:87], a[4:7], v[96:99]\n" \
"buffer_load_dwordx2 v[58:59], v57, s[36:39], 0 offen sc1\n" \
"v_mfma_f32_16x16x32_bf16 v[104:107], a[80:83], a[8:11], v[104:107]\n" \
"ds_read_b64_tr_b16 a[66:67], v10 offset:50688\n" \
"v_mfma_f32_16x16x32_bf16 v[104:107], a[84:87], a[12:15], v[104:107]\n" \
"ds_read_b64_tr_b16 a[68:69], v9 offset:58880\n" \
"v_mfma_f32_16x16x32_bf16 v[112:115], a[80:83], a[16:19], v[112:115]\n" \
"s_mov_b32 s46, 1920\n" \
"s_add_u32 m0, s50, 81920\n" \
"ds_read_b64_tr_b16 a[70:71], v10 offset:58880\n" \
"v_mfma_f32_16x16x32_bf16 v[112:115], a[84:87], a[20:23], v[112:115]\n" \
"buffer_load_dwordx4 v17, s[28:31], s46 offen lds\n" \
"v_mfma_f32_16x16x32_bf16 v[120:123], a[80:83], a[24:27], v[120:123]\n" \
"s_waitcnt lgkmcnt(12)\n" \
"ds_read_b64_tr_b16 a[72:73], v11 offset:50688\n" \
"v_mfma_f32_16x16x32_bf16 v[120:123], a[84:87], a[28:31], v[120:123]\n" \
"ds_read_b64_tr_b16 a[74:75], v12 offset:50688\n" \
"v_mfma_f32_16x16x32_bf16 v[100:103], a[88:91], a[0:3], v[100:103]\n" \
"s_add_u32 m0, s50, 86016\n" \
"ds_read_b64_tr_b16 a[76:77], v11 offset:58880\n" \
"v_mfma_f32_16x16x32_bf16 v[100:103], a[92:95], a[4:7], v[100:103]\n" \
"buffer_load_dwordx4 v18, s[28:31], s46 offen lds\n" \
"v_mfma_f32_16x16x32_bf16 v[108:111], a[88:91], a[8:11], v[108:111]\n" \
"ds_read_b64_tr_b16 a[78:79], v12 offset:58880\n" \
"v_mfma_f32_16x16x32_bf16 v[108:111], a[92:95], a[12:15], v[108:111]\n" \
"ds_read_b64_tr_b16 a[80:81], v9 offset:49152\n" \
"v_mfma_f32_16x16x32_bf16 v[116:119], a[88:91], a[16:19], v[116:119]\n" \
"s_add_u32 m0, s50, 90112\n" \
"ds_read_b64_tr_b16 a[82:83], v10 offset:49152\n" \
"v_mfma_f32_16x16x32_bf16 v[116:119], a[92:95], a[20:23], v[116:119]\n" \
"buffer_load_dwordx4 v19, s[28:31], s46 offen lds\n" \
"v_mfma_f32_16x16x32_bf16 v[124:127], a[88:91], a[24:27], v[124:127]\n" \
"s_waitcnt lgkmcnt(14)\n" \
"ds_read_b64_tr_b16 a[84:85], v9 offset:57344\n" \
"v_mfma_f32_16x16x32_bf16 v[124:127], a[92:95], a[28:31], v[124:127]\n" \
"ds_read_b64_tr_b16 a[86:87], v10 offset:57344\n" \
"v_mfma_f32_16x16x32_bf16 v[128:131], a[96:99], a[0:3], v[128:131]\n" \
"s_add_u32 m0, s50, 94208\n" \
"ds_read_b64_tr_b16 a[88:89], v11 offset:49152\n" \
"v_mfma_f32_16x16x32_bf16 v[128:131], a[100:103], a[4:7], v[128:131]\n" \
"buffer_load_dwordx4 v20, s[28:31], s46 offen lds\n" \
"v_mfma_f32_16x16x32_bf16 v[136:139], a[96:99], a[8:11], v[136:139]\n" \
"ds_read_b64_tr_b16 a[90:91], v12 offset:49152\n" \
"v_mfma_f32_16x16x32_bf16 v[136:139], a[100:103], a[12:15], v[136:139]\n" \
"ds_read_b64_tr_b16 a[92:93], v11 offset:57344\n" \
"v_mfma_f32_16x16x32_bf16 v[144:147], a[96:99], a[16:19], v[144:147]\n" \
"s_mov_b32 s44, 0\n" \
"ds_read_b64_tr_b16 a[94:95], v12 offset:57344\n" \
"s_mov_b64 s[54:55], 0\n" \
"v_mfma_f32_16x16x32_bf16 v[144:147], a[100:103], a[20:23], v[144:147]\n" \
"s_mov_b64 exec, 0xffff\nglobal_load_dword v1, v2, s[100:101]\ns_mov_b64 exec, -1\n" \
"v_mfma_f32_16x16x32_bf16 v[152:155], a[96:99], a[24:27], v[152:155]\n" \
"s_waitcnt lgkmcnt(15)\n" \
"s_add_u32 s100, s100, 262144\ns_addc_u32 s101, s101, 0\n" \
"v_mfma_f32_16x16x32_bf16 v[152:155], a[100:103], a[28:31], v[152:155]\n" \
"s_mov_b64 exec, 0xffff\nglobal_load_dword v1, v2, s[100:101]\ns_mov_b64 exec, -1\n" \
"v_mfma_f32_16x16x32_bf16 v[132:135], a[104:107], a[0:3], v[132:135]\n" \
"s_add_u32 s100, s100, 262144\ns_addc_u32 s101, s101, 0\n" \
"s_mov_b32 s49, s22\n" \
"v_mfma_f32_16x16x32_bf16 v[132:135], a[108:111], a[4:7], v[132:135]\n" \
"s_mov_b64 exec, 0xffff\nglobal_load_dword v1, v2, s[100:101]\ns_mov_b64 exec, -1\n" \
"v_mfma_f32_16x16x32_bf16 v[140:143], a[104:107], a[8:11], v[140:143]\n" \
"s_load_dwordx2 s[28:29], %0, 0x50\n" \
"s_load_dwordx2 s[32:33], %0, 0x60\n" \
"s_lshl_b32 s79, %2, 11\n" \
"v_mfma_f32_16x16x32_bf16 v[140:143], a[108:111], a[12:15], v[140:143]\n" \
"s_lshl_b32 s20, s89, 21\n" \
"s_lshr_b32 s21, %1, 5\n" \
"s_lshl_b32 s21, s21, 8\n" \
"v_mfma_f32_16x16x32_bf16 v[148:151], a[104:107], a[16:19], v[148:151]\n" \
"s_waitcnt vmcnt(7)\n" \
"s_add_u32 s18, s20, s21\n" \
"s_sub_u32 s30, 8388608, s18\n" \
"v_mfma_f32_16x16x32_bf16 v[148:151], a[108:111], a[20:23], v[148:151]\n" \
"s_lshl_b32 s20, %2, 2\n" \
"v_cmp_ne_u64 vcc, s[14:15], v[58:59]\n" \
"s_lshl_b32 s21, %2, 1\n" \
"v_mfma_f32_16x16x32_bf16 v[156:159], a[104:107], a[24:27], v[156:159]\n" \
"s_waitcnt lgkmcnt(0)\n" \
"v_bfe_u32 v29, v4, 6, 2\n" \
"v_add_u32 v29, s20, v29\n" \
"v_mfma_f32_16x16x32_bf16 v[156:159], a[108:111], a[28:31], v[156:159]\n" \
"s_cbranch_vccnz LPK_%=\nLPKR_%=:\n" \
"s_barrier\n" \
"v_bfe_u32 v31, v4, 8, 1\n" \
"v_mfma_f32_16x16x32_bf16 v[160:163], a[64:67], a[0:3], v[160:163]\n" \
"s_add_u32 s48, s22, 0x10000\n" \
"ds_read_b64_tr_b16 a[96:97], v9 offset:512\n" \
"v_lshl_add_u32 v29, v31, 4, v29\n" \
"v_mfma_f32_16x16x32_bf16 v[160:163], a[68:71], a[4:7], v[160:163]\n" \
"s_lshl_b32 s60, s7, 1\n" \
"ds_read_b64_tr_b16 a[98:99], v10 offset:512\n" \
"v_lshlrev_b32 v29, 11, v29\n" \
"v_mfma_f32_16x16x32_bf16 v[168:171], a[64:67], a[8:11], v[168:171]\n" \
"s_add_u32 s60, s60, %2\n" \
"ds_read_b64_tr_b16 a[100:101], v9 offset:8704\n" \
"v_add_u32 v31, s21, v31\n" \
"v_mfma_f32_16x16x32_bf16 v[168:171], a[68:71], a[12:15], v[168:171]\n" \
"s_sub_u32 s46, s60, 1\n" \
"ds_read_b64_tr_b16 a[102:103], v10 offset:8704\n" \
"v_lshrrev_b32 v28, 4, v4\n" \
"v_mfma_f32_16x16x32_bf16 v[176:179], a[64:67], a[16:19], v[176:179]\n" \
"s_xor_b32 s62, %2, 3\n" \
"ds_read_b128 a[200:203], v5 offset:32768\n" \
"v_xor_b32 v31, v31, v28\n" \
"v_mfma_f32_16x16x32_bf16 v[176:179], a[68:71], a[20:23], v[176:179]\n" \
"ds_read_b128 a[204:207], v6 offset:32768\n" \
"v_and_b32 v31, 3, v31\n" \
"v_mfma_f32_16x16x32_bf16 v[184:187], a[64:67], a[24:27], v[184:187]\n" \
"ds_read_b128 a[208:211], v5 offset:34816\n" \
"v_lshrrev_b32 v28, 9, v4\n" \
"v_mfma_f32_16x16x32_bf16 v[184:187], a[68:71], a[28:31], v[184:187]\n" \
"ds_read_b128 a[212:215], v6 offset:34816\n" \
"v_lshl_add_u32 v31, v28, 2, v31\n" \
"v_mfma_f32_16x16x32_bf16 v[164:167], a[72:75], a[0:3], v[164:167]\n" \
"s_lshl_b32 s20, %2, 9\n" \
"ds_read_b128 a[216:219], v5 offset:36864\n" \
"v_lshl_add_u32 v30, v31, 4, v29\n" \
"v_mfma_f32_16x16x32_bf16 v[164:167], a[76:79], a[4:7], v[164:167]\n" \
"s_add_u32 s20, s20, 0x20000\n" \
"ds_read_b128 a[220:223], v6 offset:36864\n" \
"v_add_u32 v13, s20, v9\n" \
"v_mfma_f32_16x16x32_bf16 v[172:175], a[72:75], a[8:11], v[172:175]\n" \
"ds_read_b128 a[224:227], v5 offset:38912\n" \
"v_add_u32 v14, s20, v10\n" \
"v_mfma_f32_16x16x32_bf16 v[172:175], a[76:79], a[12:15], v[172:175]\n" \
"ds_read_b128 a[228:231], v6 offset:38912\n" \
"v_add_u32 v15, s20, v11\n" \
"v_mfma_f32_16x16x32_bf16 v[180:183], a[72:75], a[16:19], v[180:183]\n" \
"ds_read_b64_tr_b16 a[104:105], v11 offset:512\n" \
"v_add_u32 v16, s20, v12\n" \
"v_mfma_f32_16x16x32_bf16 v[180:183], a[76:79], a[20:23], v[180:183]\n" \
"ds_read_b64_tr_b16 a[106:107], v12 offset:512\n" \
"v_lshlrev_b32 v20, 23, v4\n" \
"v_mfma_f32_16x16x32_bf16 v[188:191], a[72:75], a[24:27], v[188:191]\n" \
"ds_read_b64_tr_b16 a[108:109], v11 offset:8704\n" \
"v_mfma_f32_16x16x32_bf16 v[188:191], a[76:79], a[28:31], v[188:191]\n" \
"ds_read_b64_tr_b16 a[110:111], v12 offset:8704\n" \
"v_mfma_f32_16x16x32_bf16 v[64:67], a[80:83], a[0:3], v[64:67]\n" \
"ds_read_b64_tr_b16 a[64:65], v9 offset:1024\n" \
"v_mfma_f32_16x16x32_bf16 v[64:67], a[84:87], a[4:7], v[64:67]\n" \
"ds_read_b64_tr_b16 a[66:67], v10 offset:1024\n" \
"v_mfma_f32_16x16x32_bf16 v[72:75], a[80:83], a[8:11], v[72:75]\n" \
"ds_read_b64_tr_b16 a[68:69], v9 offset:9216\n" \
"v_mfma_f32_16x16x32_bf16 v[72:75], a[84:87], a[12:15], v[72:75]\n" \
"ds_read_b64_tr_b16 a[70:71], v10 offset:9216\n" \
"v_mfma_f32_16x16x32_bf16 v[80:83], a[80:83], a[16:19], v[80:83]\n" \
"ds_read_b64_tr_b16 a[72:73], v11 offset:1024\n" \
"v_mfma_f32_16x16x32_bf16 v[80:83], a[84:87], a[20:23], v[80:83]\n" \
"ds_read_b64_tr_b16 a[74:75], v12 offset:1024\n" \
"v_mfma_f32_16x16x32_bf16 v[88:91], a[80:83], a[24:27], v[88:91]\n" \
"ds_read_b64_tr_b16 a[76:77], v11 offset:9216\n" \
"v_mfma_f32_16x16x32_bf16 v[88:91], a[84:87], a[28:31], v[88:91]\n" \
"ds_read_b64_tr_b16 a[78:79], v12 offset:9216\n" \
"v_mfma_f32_16x16x32_bf16 v[68:71], a[88:91], a[0:3], v[68:71]\n" \
"s_cmp_lt_u32 %2, 2\ns_cselect_b32 s46, s46, 14\ns_cselect_b32 s62, s62, %2\ns_cselect_b32 s61, 56, 57\n" \
"v_mfma_f32_16x16x32_bf16 v[68:71], a[92:95], a[4:7], v[68:71]\n" \
"s_cmp_lt_u32 s7, 3\ns_cselect_b32 s45, 8, 0\n" \
"v_mfma_f32_16x16x32_bf16 v[76:79], a[88:91], a[8:11], v[76:79]\n" \
"s_cmp_lt_i32 s46, s45\ns_cselect_b32 s23, 99, s46\ns_cselect_b32 s24, s45, 99\n" \
"v_mfma_f32_16x16x32_bf16 v[76:79], a[92:95], a[12:15], v[76:79]\n" \
"s_min_i32 s46, s46, s45\n" \
"s_lshl_b32 s60, s60, 6\n" \
"s_add_u32 s60, s60, s6\n" \
"v_mfma_f32_16x16x32_bf16 v[84:87], a[88:91], a[16:19], v[84:87]\n" \
"s_lshl_b32 s50, s60, 13\n" \
"s_lshl_b32 s52, s60, 3\n" \
"s_add_u32 s52, s52, 94388224\n" \
"v_mfma_f32_16x16x32_bf16 v[84:87], a[92:95], a[20:23], v[84:87]\n" \
"s_lshr_b32 s60, %2, 1\n" \
"s_lshl_b32 s60, s60, 28\n" \
"s_add_u32 s52, s52, s60\n" \
"v_mfma_f32_16x16x32_bf16 v[92:95], a[88:91], a[24:27], v[92:95]\n" \
"s_lshl_b32 s62, s62, 1\n" \
"s_add_u32 s61, s61, s62\n" \
"s_lshl_b32 s60, s7, 2\n" \
"v_mfma_f32_16x16x32_bf16 v[92:95], a[92:95], a[28:31], v[92:95]\n" \
"s_waitcnt lgkmcnt(12)\n" \
"s_sub_u32 s61, s61, s60\n" \
"s_lshl_b32 s61, s61, 5\n" \
"v_mfma_f32_16x16x32_bf16 v[96:99], a[96:99], a[200:203], v[96:99]\n" \
"s_add_u32 m0, s22, 32768\n" \
"ds_read_b64_tr_b16 a[80:81], v9 offset:1536\n" \
"s_add_u32 s61, s61, s6\n" \
"v_mfma_f32_16x16x32_bf16 v[96:99], a[100:103], a[204:207], v[96:99]\n" \
"buffer_load_dwordx4 v60, s[40:43], s16 offen offset:0 sc1 lds\n" \
"v_mfma_f32_16x16x32_bf16 v[104:107], a[96:99], a[208:211], v[104:107]\n" \
"s_lshl_b32 s51, s61, 13\n" \
"ds_read_b64_tr_b16 a[82:83], v10 offset:1536\n" \
"s_lshl_b32 s53, s61, 3\n" \
"v_mfma_f32_16x16x32_bf16 v[104:107], a[100:103], a[212:215], v[104:107]\n" \
"s_add_u32 s53, s53, 94388224\n" \
"ds_read_b64_tr_b16 a[84:85], v9 offset:9728\n" \
"s_and_b32 s62, s62, 2\n" \
"v_mfma_f32_16x16x32_bf16 v[112:115], a[96:99], a[216:219], v[112:115]\n" \
"s_lshl_b32 s62, s62, 12\n" \
"ds_read_b64_tr_b16 a[86:87], v10 offset:9728\n" \
"s_add_u32 s62, s62, 131072\n" \
"v_mfma_f32_16x16x32_bf16 v[112:115], a[100:103], a[220:223], v[112:115]\n" \
"buffer_load_dwordx4 v60, s[40:43], s16 offen offset:1024 sc1 lds\n" \
"v_mfma_f32_16x16x32_bf16 v[120:123], a[96:99], a[224:227], v[120:123]\n" \
"s_waitcnt lgkmcnt(12)\n" \
"ds_read_b64_tr_b16 a[88:89], v11 offset:1536\n" \
"v_add_u32 v21, s62, v4\n" \
"v_mfma_f32_16x16x32_bf16 v[120:123], a[100:103], a[228:231], v[120:123]\n" \
"s_or_b32 s60, s7, %2\n" \
"ds_read_b64_tr_b16 a[90:91], v12 offset:1536\n" \
"v_mfma_f32_16x16x32_bf16 v[100:103], a[104:107], a[200:203], v[100:103]\n" \
"s_cmp_eq_u32 s60, 0\ns_cselect_b32 s60, 0xff800000, 0\n" \
"ds_read_b64_tr_b16 a[92:93], v11 offset:9728\n" \
"v_mfma_f32_16x16x32_bf16 v[100:103], a[108:111], a[204:207], v[100:103]\n" \
"buffer_load_dwordx4 v60, s[40:43], s16 offen offset:2048 sc1 lds\n" \
"v_mfma_f32_16x16x32_bf16 v[108:111], a[104:107], a[208:211], v[108:111]\n" \
"ds_read_b64_tr_b16 a[94:95], v12 offset:9728\n" \
"v_lshrrev_b32 v22, 6, v4\n" \
"v_mfma_f32_16x16x32_bf16 v[108:111], a[108:111], a[212:215], v[108:111]\n" \
"ds_read_b64_tr_b16 a[96:97], v9 offset:0\n" \
"v_and_b32 v22, 12, v22\n" \
"v_mfma_f32_16x16x32_bf16 v[116:119], a[104:107], a[216:219], v[116:119]\n" \
"ds_read_b64_tr_b16 a[98:99], v10 offset:0\n" \
"v_bfe_u32 v23, v4, 4, 4\n" \
"v_mfma_f32_16x16x32_bf16 v[116:119], a[108:111], a[220:223], v[116:119]\n" \
"buffer_load_dwordx4 v60, s[40:43], s16 offen offset:3072 sc1 lds\n" \
"v_mfma_f32_16x16x32_bf16 v[124:127], a[104:107], a[224:227], v[124:127]\n" \
"s_waitcnt lgkmcnt(14)\n" \
"ds_read_b64_tr_b16 a[100:101], v9 offset:8192\n" \
"v_sub_u32 v22, v23, v22\n" \
"v_mfma_f32_16x16x32_bf16 v[124:127], a[108:111], a[228:231], v[124:127]\n" \
"ds_read_b64_tr_b16 a[102:103], v10 offset:8192\n" \
"v_subrev_u32 v240, 0, v22\n" \
"v_mfma_f32_16x16x32_bf16 v[128:131], a[64:67], a[200:203], v[128:131]\n" \
"s_add_u32 s16, s16, s9\n" \
"s_add_u32 m0, s22, 65536\n" \
"ds_read_b64_tr_b16 a[104:105], v11 offset:0\n" \
"v_mfma_f32_16x16x32_bf16 v[128:131], a[68:71], a[204:207], v[128:131]\n" \
"buffer_load_dwordx4 v60, s[40:43], s16 offen offset:0 sc1 lds\n" \
"v_mfma_f32_16x16x32_bf16 v[136:139], a[64:67], a[208:211], v[136:139]\n" \
"ds_read_b64_tr_b16 a[106:107], v12 offset:0\n" \
"v_ashrrev_i32 v240, 31, v240\n" \
"v_mfma_f32_16x16x32_bf16 v[136:139], a[68:71], a[212:215], v[136:139]\n" \
"ds_read_b64_tr_b16 a[108:109], v11 offset:8192\n" \
"v_and_b32 v240, 0xff800000, v240\n" \
"v_mfma_f32_16x16x32_bf16 v[144:147], a[64:67], a[216:219], v[144:147]\n" \
"ds_read_b64_tr_b16 a[110:111], v12 offset:8192\n" \
"v_and_b32 v24, s60, v240\n" \
"v_mfma_f32_16x16x32_bf16 v[144:147], a[68:71], a[220:223], v[144:147]\n" \
"buffer_load_dwordx4 v60, s[40:43], s16 offen offset:1024 sc1 lds\n" \
"v_mfma_f32_16x16x32_bf16 v[152:155], a[64:67], a[224:227], v[152:155]\n" \
"s_waitcnt lgkmcnt(15)\n" \
"v_mov_b32 v244, s60\n" \
"v_subrev_u32 v241, 1, v22\n" \
"v_mfma_f32_16x16x32_bf16 v[152:155], a[68:71], a[228:231], v[152:155]\n" \
"v_ashrrev_i32 v241, 31, v241\n" \
"v_and_b32 v241, 0xff800000, v241\n" \
"v_mfma_f32_16x16x32_bf16 v[132:135], a[72:75], a[200:203], v[132:135]\n" \
"v_and_b32 v25, s60, v241\n" \
"v_mfma_f32_16x16x32_bf16 v[132:135], a[76:79], a[204:207], v[132:135]\n" \
"buffer_load_dwordx4 v60, s[40:43], s16 offen offset:2048 sc1 lds\n" \
"v_mfma_f32_16x16x32_bf16 v[140:143], a[72:75], a[208:211], v[140:143]\n" \
"v_mov_b32 v245, s60\n" \
"v_subrev_u32 v242, 2, v22\n" \
"v_mfma_f32_16x16x32_bf16 v[140:143], a[76:79], a[212:215], v[140:143]\n" \
"v_ashrrev_i32 v242, 31, v242\n" \
"v_and_b32 v242, 0xff800000, v242\n" \
"v_mfma_f32_16x16x32_bf16 v[148:151], a[72:75], a[216:219], v[148:151]\n" \
"v_and_b32 v26, s60, v242\n" \
"v_mfma_f32_16x16x32_bf16 v[148:151], a[76:79], a[220:223], v[148:151]\n" \
"buffer_load_dwordx4 v60, s[40:43], s16 offen offset:3072 sc1 lds\n" \
"v_mfma_f32_16x16x32_bf16 v[156:159], a[72:75], a[224:227], v[156:159]\n" \
"s_waitcnt vmcnt(11) lgkmcnt(0)\n" \
"v_mov_b32 v246, s60\n" \
"v_subrev_u32 v243, 3, v22\n" \
"v_mfma_f32_16x16x32_bf16 v[156:159], a[76:79], a[228:231], v[156:159]\n" \
"s_barrier\n" \
"v_ashrrev_i32 v243, 31, v243\n" \
"v_and_b32 v243, 0xff800000, v243\n" \
"v_mfma_f32_16x16x32_bf16 v[160:163], a[80:83], a[200:203], v[160:163]\n" \
"s_add_u32 m0, s22, 81920\n" \
"ds_read_b64_tr_b16 a[64:65], v9 offset:16384\n" \
"v_and_b32 v27, s60, v243\n" \
"v_mfma_f32_16x16x32_bf16 v[160:163], a[84:87], a[204:207], v[160:163]\n" \
"buffer_load_dwordx4 v60, s[40:43], s17 offen offset:0 sc1 lds\n" \
"v_mfma_f32_16x16x32_bf16 v[168:171], a[80:83], a[208:211], v[168:171]\n" \
"s_add_u32 s28, s28, s18\ns_addc_u32 s29, s29, 0\n" \
"ds_read_b64_tr_b16 a[66:67], v10 offset:16384\n" \
"v_mfma_f32_16x16x32_bf16 v[168:171], a[84:87], a[212:215], v[168:171]\n" \
"s_and_b32 s29, s29, 0xffff\n" \
"ds_read_b64_tr_b16 a[68:69], v9 offset:24576\n" \
"s_and_b32 s33, s33, 0xffff\n" \
"v_mfma_f32_16x16x32_bf16 v[176:179], a[80:83], a[216:219], v[176:179]\n" \
"s_mov_b32 s34, 4194304\n" \
"ds_read_b64_tr_b16 a[70:71], v10 offset:24576\n" \
"s_mov_b32 s35, 0x00020000\n" \
"v_mfma_f32_16x16x32_bf16 v[176:179], a[84:87], a[220:223], v[176:179]\n" \
"buffer_load_dwordx4 v60, s[40:43], s17 offen offset:1024 sc1 lds\n" \
"v_mfma_f32_16x16x32_bf16 v[184:187], a[80:83], a[224:227], v[184:187]\n" \
"ds_read_b128 a[0:3], v7 offset:0\n" \
"v_mov_b32 v247, s60\n" \
"v_mfma_f32_16x16x32_bf16 v[184:187], a[84:87], a[228:231], v[184:187]\n" \
"ds_read_b128 a[4:7], v8 offset:0\n" \
"v_bfe_i32 v249, v4, 4, 1\n" \
"v_mfma_f32_16x16x32_bf16 v[164:167], a[88:91], a[200:203], v[164:167]\n" \
"ds_read_b128 a[8:11], v7 offset:2048\n" \
"v_lshrrev_b32 v248, 4, v4\n" \
"v_mfma_f32_16x16x32_bf16 v[164:167], a[92:95], a[204:207], v[164:167]\n" \
"buffer_load_dwordx4 v60, s[40:43], s17 offen offset:2048 sc1 lds\n" \
"v_mfma_f32_16x16x32_bf16 v[172:175], a[88:91], a[208:211], v[172:175]\n" \
"ds_read_b128 a[12:15], v8 offset:2048\n" \
"v_and_b32 v249, 0xaaaaaaaa, v249\n" \
"v_mfma_f32_16x16x32_bf16 v[172:175], a[92:95], a[212:215], v[172:175]\n" \
"ds_read_b128 a[16:19], v7 offset:4096\n" \
"v_xnor_b32 v248, v248, v4\n" \
"v_mfma_f32_16x16x32_bf16 v[180:183], a[88:91], a[216:219], v[180:183]\n" \
"ds_read_b128 a[20:23], v8 offset:4096\n" \
"v_or_b32 v249, 0x44444444, v249\n" \
"v_mfma_f32_16x16x32_bf16 v[180:183], a[92:95], a[220:223], v[180:183]\n" \
"buffer_load_dwordx4 v60, s[40:43], s17 offen offset:3072 sc1 lds\n" \
"v_mfma_f32_16x16x32_bf16 v[188:191], a[88:91], a[224:227], v[188:191]\n" \
"ds_read_b128 a[24:27], v7 offset:6144\n" \
"v_bfe_i32 v248, v248, 5, 1\n" \
"v_mfma_f32_16x16x32_bf16 v[188:191], a[92:95], a[228:231], v[188:191]\n" \
"ds_read_b128 a[28:31], v8 offset:6144\n" \
"v_and_b32 v248, 0x3f803f80, v248\n" \
"v_mfma_f32_16x16x32_bf16 v[64:67], a[96:99], a[200:203], v[64:67]\n" \
"s_add_u32 s16, s16, s9\n" \
"s_add_u32 m0, s22, 98304\n" \
"ds_read_b64_tr_b16 a[72:73], v11 offset:16384\n" \
"v_mfma_f32_16x16x32_bf16 v[64:67], a[100:103], a[204:207], v[64:67]\n" \
"buffer_load_dwordx4 v60, s[40:43], s16 offen offset:0 sc1 lds\n" \
"v_mfma_f32_16x16x32_bf16 v[72:75], a[96:99], a[208:211], v[72:75]\n" \
"ds_read_b64_tr_b16 a[74:75], v12 offset:16384\n" \
"s_load_dwordx2 s[60:61], %0, 0x0\n" \
"v_mfma_f32_16x16x32_bf16 v[72:75], a[100:103], a[212:215], v[72:75]\n" \
"ds_read_b64_tr_b16 a[76:77], v11 offset:24576\n" \
"v_mfma_f32_16x16x32_bf16 v[80:83], a[96:99], a[216:219], v[80:83]\n" \
"ds_read_b64_tr_b16 a[78:79], v12 offset:24576\n" \
"v_mfma_f32_16x16x32_bf16 v[80:83], a[100:103], a[220:223], v[80:83]\n" \
"buffer_load_dwordx4 v60, s[40:43], s16 offen offset:1024 sc1 lds\n" \
"v_mfma_f32_16x16x32_bf16 v[88:91], a[96:99], a[224:227], v[88:91]\n" \
"ds_read_b64_tr_b16 a[80:81], v9 offset:16896\n" \
"v_mfma_f32_16x16x32_bf16 v[88:91], a[100:103], a[228:231], v[88:91]\n" \
"ds_read_b64_tr_b16 a[82:83], v10 offset:16896\n" \
"v_mfma_f32_16x16x32_bf16 v[68:71], a[104:107], a[200:203], v[68:71]\n" \
"ds_read_b64_tr_b16 a[84:85], v9 offset:25088\n" \
"v_mfma_f32_16x16x32_bf16 v[68:71], a[108:111], a[204:207], v[68:71]\n" \
"buffer_load_dwordx4 v60, s[40:43], s16 offen offset:2048 sc1 lds\n" \
"v_mfma_f32_16x16x32_bf16 v[76:79], a[104:107], a[208:211], v[76:79]\n" \
"ds_read_b64_tr_b16 a[86:87], v10 offset:25088\n" \
"v_mfma_f32_16x16x32_bf16 v[76:79], a[108:111], a[212:215], v[76:79]\n" \
"ds_read_b64_tr_b16 a[88:89], v11 offset:16896\n" \
"v_mfma_f32_16x16x32_bf16 v[84:87], a[104:107], a[216:219], v[84:87]\n" \
"ds_read_b64_tr_b16 a[90:91], v12 offset:16896\n" \
"v_mfma_f32_16x16x32_bf16 v[84:87], a[108:111], a[220:223], v[84:87]\n" \
"buffer_load_dwordx4 v60, s[40:43], s16 offen offset:3072 sc1 lds\n" \
"v_mfma_f32_16x16x32_bf16 v[92:95], a[104:107], a[224:227], v[92:95]\n" \
"ds_read_b64_tr_b16 a[92:93], v11 offset:25088\n" \
"v_mfma_f32_16x16x32_bf16 v[92:95], a[108:111], a[228:231], v[92:95]\n" \
"s_waitcnt lgkmcnt(11)\n" \
"ds_read_b64_tr_b16 a[94:95], v12 offset:25088\n" \
"s_mov_b64 exec, 0\ns_cmp_eq_u32 %2, 0\ns_cbranch_scc1 LH16_%=\ns_cmp_eq_u32 %2, 1\ns_cbranch_scc1 LH17_%=\ns_cmp_eq_u32 %2, 2\ns_cbranch_scc1 LH18_%=\ns_cmp_eq_u32 %2, 3\ns_cbranch_scc1 LH19_%=\nLHR1_%=:\ns_mov_b64 exec, -1\n" \
"v_mfma_f32_16x16x32_bf16 v[64:67], a[64:67], a[0:3], v[64:67]\n" \
"ds_read_b64_tr_b16 a[96:97], v9 offset:17408\n" \
"v_mfma_f32_16x16x32_bf16 v[64:67], a[68:71], a[4:7], v[64:67]\n" \
"ds_read_b64_tr_b16 a[98:99], v10 offset:17408\n" \
"v_mfma_f32_16x16x32_bf16 v[72:75], a[64:67], a[8:11], v[72:75]\n" \
"ds_read_b64_tr_b16 a[100:101], v9 offset:25600\n" \
"v_mfma_f32_16x16x32_bf16 v[72:75], a[68:71], a[12:15], v[72:75]\n" \
"ds_read_b64_tr_b16 a[102:103], v10 offset:25600\n" \
"v_mfma_f32_16x16x32_bf16 v[80:83], a[64:67], a[16:19], v[80:83]\n" \
"s_add_u32 s17, s17, s19\n" \
"s_add_u32 m0, s22, 114688\n" \
"ds_read_b64_tr_b16 a[104:105], v11 offset:17408\n" \
"v_mfma_f32_16x16x32_bf16 v[80:83], a[68:71], a[20:23], v[80:83]\n" \
"buffer_load_dwordx4 v60, s[40:43], s17 offen offset:0 sc1 lds\n" \
"v_mfma_f32_16x16x32_bf16 v[88:91], a[64:67], a[24:27], v[88:91]\n" \
"s_waitcnt lgkmcnt(13)\n" \
"ds_read_b64_tr_b16 a[106:107], v12 offset:17408\n" \
"v_mfma_f32_16x16x32_bf16 v[88:91], a[68:71], a[28:31], v[88:91]\n" \
"ds_read_b64_tr_b16 a[108:109], v11 offset:25600\n" \
"v_mfma_f32_16x16x32_bf16 v[68:71], a[72:75], a[0:3], v[68:71]\n" \
"ds_read_b64_tr_b16 a[110:111], v12 offset:25600\n" \
"v_mfma_f32_16x16x32_bf16 v[68:71], a[76:79], a[4:7], v[68:71]\n" \
"ds_read_b64_tr_b16 a[64:65], v9 offset:17920\n" \
"v_mfma_f32_16x16x32_bf16 v[76:79], a[72:75], a[8:11], v[76:79]\n" \
"ds_read_b64_tr_b16 a[66:67], v10 offset:17920\n" \
"v_mfma_f32_16x16x32_bf16 v[76:79], a[76:79], a[12:15], v[76:79]\n" \
"buffer_load_dwordx4 v60, s[40:43], s17 offen offset:1024 sc1 lds\n" \
"v_mfma_f32_16x16x32_bf16 v[84:87], a[72:75], a[16:19], v[84:87]\n" \
"ds_read_b64_tr_b16 a[68:69], v9 offset:26112\n" \
"v_cvt_pk_bf16_f32 v32, v64, v65\n" \
"v_mfma_f32_16x16x32_bf16 v[84:87], a[76:79], a[20:23], v[84:87]\n" \
"ds_read_b64_tr_b16 a[70:71], v10 offset:26112\n" \
"v_cvt_pk_bf16_f32 v33, v66, v67\n" \
"v_mfma_f32_16x16x32_bf16 v[92:95], a[72:75], a[24:27], v[92:95]\n" \
"s_waitcnt lgkmcnt(15)\n" \
"v_cvt_pk_bf16_f32 v34, v68, v69\n" \
"v_cvt_pk_bf16_f32 v35, v70, v71\n" \
"v_mfma_f32_16x16x32_bf16 v[92:95], a[76:79], a[28:31], v[92:95]\n" \
"v_cvt_pk_bf16_f32 v48, v72, v73\n" \
"v_cvt_pk_bf16_f32 v49, v74, v75\n" \
"v_mfma_f32_16x16x32_bf16 v[96:99], a[80:83], a[0:3], v[96:99]\n" \
"s_lshl_b32 s25, s45, 2\n" \
"ds_read_b64_tr_b16 a[72:73], v11 offset:17920\n" \
"s_and_b32 s25, s25, 32\n" \
"v_mfma_f32_16x16x32_bf16 v[96:99], a[84:87], a[4:7], v[96:99]\n" \
"buffer_load_dwordx4 v60, s[40:43], s17 offen offset:2048 sc1 lds\n" \
"v_mfma_f32_16x16x32_bf16 v[104:107], a[80:83], a[8:11], v[104:107]\n" \
"s_add_u32 s25, s25, %2\n" \
"ds_read_b64_tr_b16 a[74:75], v12 offset:17920\n" \
"v_cvt_pk_bf16_f32 v50, v76, v77\n" \
"v_mfma_f32_16x16x32_bf16 v[104:107], a[84:87], a[12:15], v[104:107]\n" \
"s_cmp_ge_u32 s25, 34\ns_cselect_b64 vcc, -1, 0\n" \
"ds_read_b64_tr_b16 a[76:77], v11 offset:26112\n" \
"v_mfma_f32_16x16x32_bf16 v[112:115], a[80:83], a[16:19], v[112:115]\n" \
"ds_read_b64_tr_b16 a[78:79], v12 offset:26112\n" \
"v_cvt_pk_bf16_f32 v51, v78, v79\n" \
"v_mfma_f32_16x16x32_bf16 v[112:115], a[84:87], a[20:23], v[112:115]\n" \
"buffer_load_dwordx4 v60, s[40:43], s17 offen offset:3072 sc1 lds\n" \
"v_mfma_f32_16x16x32_bf16 v[120:123], a[80:83], a[24:27], v[120:123]\n" \
"s_waitcnt lgkmcnt(15)\n" \
"v_cvt_pk_bf16_f32 v192, v80, v81\n" \
"v_cvt_pk_bf16_f32 v193, v82, v83\n" \
"v_mfma_f32_16x16x32_bf16 v[120:123], a[84:87], a[28:31], v[120:123]\n" \
"v_cvt_pk_bf16_f32 v194, v84, v85\n" \
"v_cvt_pk_bf16_f32 v195, v86, v87\n" \
"v_mfma_f32_16x16x32_bf16 v[100:103], a[88:91], a[0:3], v[100:103]\n" \
"v_cvt_pk_bf16_f32 v208, v88, v89\n" \
"v_mfma_f32_16x16x32_bf16 v[100:103], a[92:95], a[4:7], v[100:103]\n" \
"s_waitcnt vmcnt(16) lgkmcnt(0)\n" \
"v_cvt_pk_bf16_f32 v209, v90, v91\n" \
"v_cvt_pk_bf16_f32 v210, v92, v93\n" \
"v_mfma_f32_16x16x32_bf16 v[108:111], a[88:91], a[8:11], v[108:111]\n" \
"s_barrier\n" \
"v_cvt_pk_bf16_f32 v211, v94, v95\n" \
"v_mfma_f32_16x16x32_bf16 v[108:111], a[92:95], a[12:15], v[108:111]\n" \
"v_add_u32 v5, 0x2000, v4\n" \
"v_mov_b32 v6, v60\n" \
"v_mfma_f32_16x16x32_bf16 v[116:119], a[88:91], a[16:19], v[116:119]\n" \
"s_add_u32 s16, s16, s9\n" \
"v_add_u32 v7, 0x1000, v4\n" \
"s_add_u32 m0, s49, 0\n" \
"v_mfma_f32_16x16x32_bf16 v[116:119], a[92:95], a[20:23], v[116:119]\n" \
"buffer_load_dwordx4 v6, s[40:43], s16 offen offset:0 sc1 lds\n" \
"v_mfma_f32_16x16x32_bf16 v[124:127], a[88:91], a[24:27], v[124:127]\n" \
"ds_read_b128 a[128:131], v5 offset:32768\n" \
"v_cvt_pk_bf16_f32 v36, v96, v97\n" \
"v_mfma_f32_16x16x32_bf16 v[124:127], a[92:95], a[28:31], v[124:127]\n" \
"s_cbranch_vccz LQW1_%=\nds_write_b128 v21, v[32:35] offset:16384\nLQW1_%=:\n" \
"v_mfma_f32_16x16x32_bf16 v[128:131], a[96:99], a[0:3], v[128:131]\n" \
"s_cbranch_vccz LQW2_%=\nds_write_b128 v21, v[48:51] offset:20480\nLQW2_%=:\n" \
"v_mfma_f32_16x16x32_bf16 v[128:131], a[100:103], a[4:7], v[128:131]\n" \
"buffer_load_dwordx4 v6, s[40:43], s16 offen offset:1024 sc1 lds\n" \
"v_mfma_f32_16x16x32_bf16 v[136:139], a[96:99], a[8:11], v[136:139]\n" \
"ds_read_b128 a[132:135], v5 offset:33792\n" \
"v_cvt_pk_bf16_f32 v37, v98, v99\n" \
"v_mfma_f32_16x16x32_bf16 v[136:139], a[100:103], a[12:15], v[136:139]\n" \
"s_cbranch_vccz LQW3_%=\nds_write_b128 v21, v[192:195] offset:0\nLQW3_%=:\n" \
"v_mfma_f32_16x16x32_bf16 v[144:147], a[96:99], a[16:19], v[144:147]\n" \
"v_cvt_pk_bf16_f32 v38, v100, v101\n" \
"v_cvt_pk_bf16_f32 v39, v102, v103\n" \
"v_mfma_f32_16x16x32_bf16 v[144:147], a[100:103], a[20:23], v[144:147]\n" \
"buffer_load_dwordx4 v6, s[40:43], s16 offen offset:2048 sc1 lds\n" \
"v_mfma_f32_16x16x32_bf16 v[152:155], a[96:99], a[24:27], v[152:155]\n" \
"ds_read_b128 a[144:147], v5 offset:36864\n" \
"v_cvt_pk_bf16_f32 v52, v104, v105\n" \
"v_mfma_f32_16x16x32_bf16 v[152:155], a[100:103], a[28:31], v[152:155]\n" \
"s_cbranch_vccz LQW4_%=\nds_write_b128 v21, v[208:211] offset:4096\nLQW4_%=:\n" \
"v_mfma_f32_16x16x32_bf16 v[132:135], a[104:107], a[0:3], v[132:135]\n" \
"s_cbranch_vccz LQW5_%=\nds_write_b128 v21, v[36:39] offset:17408\nLQW5_%=:\n" \
"v_mfma_f32_16x16x32_bf16 v[132:135], a[108:111], a[4:7], v[132:135]\n" \
"buffer_load_dwordx4 v6, s[40:43], s16 offen offset:3072 sc1 lds\n" \
"v_mfma_f32_16x16x32_bf16 v[140:143], a[104:107], a[8:11], v[140:143]\n" \
"s_add_u32 s17, s17, s19\n" \
"ds_read_b128 a[148:151], v5 offset:37888\n" \
"s_add_u32 m0, s49, 16384\n" \
"v_mfma_f32_16x16x32_bf16 v[140:143], a[108:111], a[12:15], v[140:143]\n" \
"v_cvt_pk_bf16_f32 v53, v106, v107\n" \
"v_cvt_pk_bf16_f32 v54, v108, v109\n" \
"v_mfma_f32_16x16x32_bf16 v[148:151], a[104:107], a[16:19], v[148:151]\n" \
"LH14_%=:\ns_cbranch_execz LHR0_%=\n" \
"v_cvt_pk_bf16_f32 v55, v110, v111\n" \
"v_cvt_pk_bf16_f32 v196, v112, v113\n" \
"v_mfma_f32_16x16x32_bf16 v[148:151], a[108:111], a[20:23], v[148:151]\n" \
"buffer_load_dwordx4 v6, s[40:43], s17 offen offset:0 sc1 lds\n" \
"v_mfma_f32_16x16x32_bf16 v[156:159], a[104:107], a[24:27], v[156:159]\n" \
"ds_read_b128 a[136:139], v5 offset:34816\n" \
"v_cvt_pk_bf16_f32 v197, v114, v115\n" \
"v_mfma_f32_16x16x32_bf16 v[156:159], a[108:111], a[28:31], v[156:159]\n" \
"s_cbranch_vccz LQW6_%=\nds_write_b128 v21, v[52:55] offset:21504\nLQW6_%=:\n" \
"v_mfma_f32_16x16x32_bf16 v[160:163], a[64:67], a[0:3], v[160:163]\n" \
"v_cvt_pk_bf16_f32 v198, v116, v117\n" \
"v_cvt_pk_bf16_f32 v199, v118, v119\n" \
"v_mfma_f32_16x16x32_bf16 v[160:163], a[68:71], a[4:7], v[160:163]\n" \
"buffer_load_dwordx4 v6, s[40:43], s17 offen offset:1024 sc1 lds\n" \
"v_mfma_f32_16x16x32_bf16 v[168:171], a[64:67], a[8:11], v[168:171]\n" \
"ds_read_b128 a[140:143], v5 offset:35840\n" \
"v_cvt_pk_bf16_f32 v212, v120, v121\n" \
"v_mfma_f32_16x16x32_bf16 v[168:171], a[68:71], a[12:15], v[168:171]\n" \
"s_cbranch_vccz LQW7_%=\nds_write_b128 v21, v[196:199] offset:1024\nLQW7_%=:\n" \
"v_mfma_f32_16x16x32_bf16 v[176:179], a[64:67], a[16:19], v[176:179]\n" \
"v_cvt_pk_bf16_f32 v213, v122, v123\n" \
"v_cvt_pk_bf16_f32 v214, v124, v125\n" \
"v_mfma_f32_16x16x32_bf16 v[176:179], a[68:71], a[20:23], v[176:179]\n" \
"buffer_load_dwordx4 v6, s[40:43], s17 offen offset:2048 sc1 lds\n" \
"v_mfma_f32_16x16x32_bf16 v[184:187], a[64:67], a[24:27], v[184:187]\n" \
"ds_read_b128 a[152:155], v5 offset:38912\n" \
"v_cvt_pk_bf16_f32 v215, v126, v127\n" \
"v_mfma_f32_16x16x32_bf16 v[184:187], a[68:71], a[28:31], v[184:187]\n" \
"s_cbranch_vccz LQW8_%=\nds_write_b128 v21, v[212:215] offset:5120\nLQW8_%=:\n" \
"v_mfma_f32_16x16x32_bf16 v[164:167], a[72:75], a[0:3], v[164:167]\n" \
"v_cvt_pk_bf16_f32 v40, v128, v129\n" \
"v_cvt_pk_bf16_f32 v41, v130, v131\n" \
"v_mfma_f32_16x16x32_bf16 v[164:167], a[76:79], a[4:7], v[164:167]\n" \
"buffer_load_dwordx4 v6, s[40:43], s17 offen offset:3072 sc1 lds\n" \
"v_mfma_f32_16x16x32_bf16 v[172:175], a[72:75], a[8:11], v[172:175]\n" \
"ds_read_b128 a[156:159], v5 offset:39936\n" \
"v_cvt_pk_bf16_f32 v42, v132, v133\n" \
"v_mfma_f32_16x16x32_bf16 v[172:175], a[76:79], a[12:15], v[172:175]\n" \
"v_cvt_pk_bf16_f32 v43, v134, v135\n" \
"v_cvt_pk_bf16_f32 v56, v136, v137\n" \
"v_mfma_f32_16x16x32_bf16 v[180:183], a[72:75], a[16:19], v[180:183]\n" \
"s_cbranch_vccz LQW9_%=\nds_write_b128 v21, v[40:43] offset:18432\nLQW9_%=:\n" \
"v_mfma_f32_16x16x32_bf16 v[180:183], a[76:79], a[20:23], v[180:183]\n" \
"v_cvt_pk_bf16_f32 v57, v138, v139\n" \
"v_cvt_pk_bf16_f32 v58, v140, v141\n" \
"v_mfma_f32_16x16x32_bf16 v[188:191], a[72:75], a[24:27], v[188:191]\n" \
"v_xor_b32 v5, 0x10000, v5\n" \
"v_cvt_pk_bf16_f32 v59, v142, v143\n" \
"v_mfma_f32_16x16x32_bf16 v[188:191], a[76:79], a[28:31], v[188:191]\n" \
"s_waitcnt lgkmcnt(0)\n" \
"s_cbranch_vccz LQW10_%=\nds_write_b128 v21, v[56:59] offset:22528\nLQW10_%=:\n" \
"v_mfma_f32_16x16x32_bf16 v[64:67], a[128:131], v[32:35], v[24:27]\n" \
"v_cvt_pk_bf16_f32 v200, v144, v145\n" \
"v_cvt_pk_bf16_f32 v201, v146, v147\n" \
"v_mfma_f32_16x16x32_bf16 v[64:67], a[132:135], v[36:39], v[64:67]\n" \
"s_waitcnt vmcnt(16)\n" \
"v_cvt_pk_bf16_f32 v202, v148, v149\n" \
"v_cvt_pk_bf16_f32 v203, v150, v151\n" \
"v_mfma_f32_16x16x32_bf16 v[72:75], a[128:131], v[48:51], 0\n" \
"s_barrier\n" \
"s_cbranch_vccz LQW11_%=\nds_write_b128 v21, v[200:203] offset:2048\nLQW11_%=:\n" \
"v_mfma_f32_16x16x32_bf16 v[72:75], a[132:135], v[52:55], v[72:75]\n" \
"v_cvt_pk_bf16_f32 v216, v152, v153\n" \
"v_cvt_pk_bf16_f32 v217, v154, v155\n" \
"v_mfma_f32_16x16x32_bf16 v[80:83], a[128:131], v[192:195], 0\n" \
"v_cvt_pk_bf16_f32 v218, v156, v157\n" \
"v_cvt_pk_bf16_f32 v219, v158, v159\n" \
"v_mfma_f32_16x16x32_bf16 v[80:83], a[132:135], v[196:199], v[80:83]\n" \
"s_cbranch_vccz LQW12_%=\nds_write_b128 v21, v[216:219] offset:6144\nLQW12_%=:\n" \
"v_mfma_f32_16x16x32_bf16 v[88:91], a[128:131], v[208:211], 0\n" \
"v_cvt_pk_bf16_f32 v44, v160, v161\n" \
"v_cvt_pk_bf16_f32 v45, v162, v163\n" \
"v_mfma_f32_16x16x32_bf16 v[88:91], a[132:135], v[212:215], v[88:91]\n" \
"ds_read_b128 a[128:131], v5 offset:0\n" \
"v_cvt_pk_bf16_f32 v46, v164, v165\n" \
"v_mfma_f32_16x16x32_bf16 v[68:71], a[144:147], v[32:35], v[244:247]\n" \
"ds_read_b128 a[132:135], v5 offset:1024\n" \
"v_cvt_pk_bf16_f32 v47, v166, v167\n" \
"v_mfma_f32_16x16x32_bf16 v[68:71], a[148:151], v[36:39], v[68:71]\n" \
"s_cbranch_vccz LQW13_%=\nds_write_b128 v21, v[44:47] offset:19456\nLQW13_%=:\n" \
"v_mfma_f32_16x16x32_bf16 v[76:79], a[144:147], v[48:51], v[24:27]\n" \
"v_cvt_pk_bf16_f32 v60, v168, v169\n" \
"v_cvt_pk_bf16_f32 v61, v170, v171\n" \
"v_mfma_f32_16x16x32_bf16 v[76:79], a[148:151], v[52:55], v[76:79]\n" \
"v_cvt_pk_bf16_f32 v62, v172, v173\n" \
"v_cvt_pk_bf16_f32 v63, v174, v175\n" \
"v_mfma_f32_16x16x32_bf16 v[84:87], a[144:147], v[192:195], 0\n" \
"s_cbranch_vccz LQW14_%=\nds_write_b128 v21, v[60:63] offset:23552\nLQW14_%=:\n" \
"v_mfma_f32_16x16x32_bf16 v[84:87], a[148:151], v[196:199], v[84:87]\n" \
"v_cvt_pk_bf16_f32 v204, v176, v177\n" \
"v_cvt_pk_bf16_f32 v205, v178, v179\n" \
"v_mfma_f32_16x16x32_bf16 v[92:95], a[144:147], v[208:211], 0\n" \
"v_cvt_pk_bf16_f32 v206, v180, v181\n" \
"v_cvt_pk_bf16_f32 v207, v182, v183\n" \
"v_mfma_f32_16x16x32_bf16 v[92:95], a[148:151], v[212:215], v[92:95]\n" \
"ds_read_b128 a[144:147], v5 offset:4096\n" \
"v_cvt_pk_bf16_f32 v220, v184, v185\n" \
"v_mfma_f32_16x16x32_bf16 v[64:67], a[136:139], v[40:43], v[64:67]\n" \
"ds_read_b128 a[148:151], v5 offset:5120\n" \
"v_cvt_pk_bf16_f32 v221, v186, v187\n" \
"v_mfma_f32_16x16x32_bf16 v[64:67], a[140:143], v[44:47], v[64:67]\n" \
"s_cbranch_vccz LQW15_%=\nds_write_b128 v21, v[204:207] offset:3072\nLQW15_%=:\n" \
"v_mfma_f32_16x16x32_bf16 v[72:75], a[136:139], v[56:59], v[72:75]\n" \
"v_cvt_pk_bf16_f32 v222, v188, v189\n" \
"v_cvt_pk_bf16_f32 v223, v190, v191\n" \
"v_mfma_f32_16x16x32_bf16 v[72:75], a[140:143], v[60:63], v[72:75]\n" \
"s_cbranch_vccz LQW16_%=\nds_write_b128 v21, v[220:223] offset:7168\nLQW16_%=:\n" \
"v_mfma_f32_16x16x32_bf16 v[80:83], a[136:139], v[200:203], v[80:83]\n" \
"v_exp_f32 v64, v64\n" \
"v_mfma_f32_16x16x32_bf16 v[80:83], a[140:143], v[204:207], v[80:83]\n" \
"v_exp_f32 v65, v65\n" \
"v_mfma_f32_16x16x32_bf16 v[88:91], a[136:139], v[216:219], v[88:91]\n" \
"v_exp_f32 v66, v66\n" \
"v_mfma_f32_16x16x32_bf16 v[88:91], a[140:143], v[220:223], v[88:91]\n" \
"ds_read_b128 a[136:139], v5 offset:2048\n" \
"v_accvgpr_write_b32 a64, v192\n" \
"v_mfma_f32_16x16x32_bf16 v[68:71], a[152:155], v[40:43], v[68:71]\n" \
"ds_read_b128 a[140:143], v5 offset:3072\n" \
"v_accvgpr_write_b32 a65, v193\n" \
"v_mfma_f32_16x16x32_bf16 v[68:71], a[156:159], v[44:47], v[68:71]\n" \
"v_exp_f32 v67, v67\n" \
"v_mfma_f32_16x16x32_bf16 v[76:79], a[152:155], v[56:59], v[76:79]\n" \
"v_exp_f32 v72, v72\n" \
"v_mfma_f32_16x16x32_bf16 v[76:79], a[156:159], v[60:63], v[76:79]\n" \
"v_exp_f32 v73, v73\n" \
"v_mfma_f32_16x16x32_bf16 v[84:87], a[152:155], v[200:203], v[84:87]\n" \
"v_exp_f32 v74, v74\n" \
"v_mfma_f32_16x16x32_bf16 v[84:87], a[156:159], v[204:207], v[84:87]\n" \
"v_exp_f32 v75, v75\n" \
"v_mfma_f32_16x16x32_bf16 v[92:95], a[152:155], v[216:219], v[92:95]\n" \
"v_exp_f32 v80, v80\n" \
"v_mfma_f32_16x16x32_bf16 v[92:95], a[156:159], v[220:223], v[92:95]\n" \
"s_waitcnt lgkmcnt(0)\n" \
"v_exp_f32 v81, v81\n" \
"v_mfma_f32_16x16x32_bf16 v[96:99], a[128:131], v[32:35], v[244:247]\n" \
"v_exp_f32 v82, v82\n" \
"v_mfma_f32_16x16x32_bf16 v[96:99], a[132:135], v[36:39], v[96:99]\n" \
"s_add_u32 s16, s16, s9\n" \
"v_exp_f32 v83, v83\n" \
"v_mfma_f32_16x16x32_bf16 v[104:107], a[128:131], v[48:51], v[244:247]\n" \
"ds_read_b128 a[152:155], v5 offset:6144\n" \
"v_cvt_pk_bf16_f32 v128, v64, v65\n" \
"v_mfma_f32_16x16x32_bf16 v[104:107], a[132:135], v[52:55], v[104:107]\n" \
"s_add_u32 m0, s49, 32768\n" \
"v_exp_f32 v88, v88\n" \
"v_mfma_f32_16x16x32_bf16 v[112:115], a[128:131], v[192:195], v[24:27]\n" \
"buffer_load_dwordx4 v6, s[40:43], s16 offen offset:0 sc1 lds\n" \
"v_mfma_f32_16x16x32_bf16 v[112:115], a[132:135], v[196:199], v[112:115]\n" \
"v_exp_f32 v89, v89\n" \
"v_mfma_f32_16x16x32_bf16 v[120:123], a[128:131], v[208:211], 0\n" \
"ds_read_b128 a[156:159], v5 offset:7168\n" \
"v_cvt_pk_bf16_f32 v129, v66, v67\n" \
"v_mfma_f32_16x16x32_bf16 v[120:123], a[132:135], v[212:215], v[120:123]\n" \
"s_waitcnt lgkmcnt(4)\n" \
"v_exp_f32 v90, v90\n" \
"v_mfma_f32_16x16x32_bf16 v[96:99], a[136:139], v[40:43], v[96:99]\n" \
"v_exp_f32 v91, v91\n" \
"v_mfma_f32_16x16x32_bf16 v[96:99], a[140:143], v[44:47], v[96:99]\n" \
"v_exp_f32 v68, v68\n" \
"v_mfma_f32_16x16x32_bf16 v[104:107], a[136:139], v[56:59], v[104:107]\n" \
"ds_read_b128 a[128:131], v5 offset:16384\n" \
"v_cvt_pk_bf16_f32 v132, v72, v73\n" \
"v_mfma_f32_16x16x32_bf16 v[104:107], a[140:143], v[60:63], v[104:107]\n" \
"v_exp_f32 v69, v69\n" \
"v_mfma_f32_16x16x32_bf16 v[112:115], a[136:139], v[200:203], v[112:115]\n" \
"buffer_load_dwordx4 v6, s[40:43], s16 offen offset:1024 sc1 lds\n" \
"v_mfma_f32_16x16x32_bf16 v[112:115], a[140:143], v[204:207], v[112:115]\n" \
"v_exp_f32 v70, v70\n" \
"v_mfma_f32_16x16x32_bf16 v[120:123], a[136:139], v[216:219], v[120:123]\n" \
"ds_read_b128 a[132:135], v5 offset:17408\n" \
"v_cvt_pk_bf16_f32 v133, v74, v75\n" \
"v_mfma_f32_16x16x32_bf16 v[120:123], a[140:143], v[220:223], v[120:123]\n" \
"s_waitcnt lgkmcnt(4)\n" \
"v_exp_f32 v71, v71\n" \
"v_mfma_f32_16x16x32_bf16 v[100:103], a[144:147], v[32:35], v[244:247]\n" \
"v_exp_f32 v76, v76\n" \
"v_accvgpr_write_b32 a66, v194\n" \
"v_accvgpr_write_b32 a67, v195\n" \
"v_mfma_f32_16x16x32_bf16 v[100:103], a[148:151], v[36:39], v[100:103]\n" \
"v_exp_f32 v77, v77\n" \
"v_accvgpr_write_b32 a80, v208\n" \
"v_accvgpr_write_b32 a81, v209\n" \
"v_mfma_f32_16x16x32_bf16 v[108:111], a[144:147], v[48:51], v[244:247]\n" \
"ds_read_b128 a[136:139], v5 offset:18432\n" \
"v_cvt_pk_bf16_f32 v136, v80, v81\n" \
"v_accvgpr_write_b32 a82, v210\n" \
"v_accvgpr_write_b32 a83, v211\n" \
"v_mfma_f32_16x16x32_bf16 v[108:111], a[148:151], v[52:55], v[108:111]\n" \
"v_exp_f32 v78, v78\n" \
"v_accvgpr_write_b32 a68, v196\n" \
"v_accvgpr_write_b32 a69, v197\n" \
"v_mfma_f32_16x16x32_bf16 v[116:119], a[144:147], v[192:195], v[244:247]\n" \
"buffer_load_dwordx4 v6, s[40:43], s16 offen offset:2048 sc1 lds\n" \
"v_accvgpr_write_b32 a70, v198\n" \
"v_accvgpr_write_b32 a71, v199\n" \
"v_mfma_f32_16x16x32_bf16 v[116:119], a[148:151], v[196:199], v[116:119]\n" \
"v_exp_f32 v79, v79\n" \
"v_accvgpr_write_b32 a84, v212\n" \
"v_accvgpr_write_b32 a85, v213\n" \
"v_mfma_f32_16x16x32_bf16 v[124:127], a[144:147], v[208:211], v[24:27]\n" \
"ds_read_b128 a[140:143], v5 offset:19456\n" \
"v_cvt_pk_bf16_f32 v137, v82, v83\n" \
"v_accvgpr_write_b32 a86, v214\n" \
"v_accvgpr_write_b32 a87, v215\n" \
"v_mfma_f32_16x16x32_bf16 v[124:127], a[148:151], v[212:215], v[124:127]\n" \
"s_waitcnt lgkmcnt(4)\n" \
"v_exp_f32 v84, v84\n" \
"v_accvgpr_write_b32 a72, v200\n" \
"v_accvgpr_write_b32 a73, v201\n" \
"v_mfma_f32_16x16x32_bf16 v[100:103], a[152:155], v[40:43], v[100:103]\n" \
"v_exp_f32 v85, v85\n" \
"v_accvgpr_write_b32 a74, v202\n" \
"v_accvgpr_write_b32 a75, v203\n" \
"v_mfma_f32_16x16x32_bf16 v[100:103], a[156:159], v[44:47], v[100:103]\n" \
"v_exp_f32 v86, v86\n" \
"v_accvgpr_write_b32 a88, v216\n" \
"v_accvgpr_write_b32 a89, v217\n" \
"v_mfma_f32_16x16x32_bf16 v[108:111], a[152:155], v[56:59], v[108:111]\n" \
"ds_read_b128 a[144:147], v5 offset:20480\n" \
"v_cvt_pk_bf16_f32 v140, v88, v89\n" \
"v_accvgpr_write_b32 a90, v218\n" \
"v_accvgpr_write_b32 a91, v219\n" \
"v_mfma_f32_16x16x32_bf16 v[108:111], a[156:159], v[60:63], v[108:111]\n" \
"v_exp_f32 v87, v87\n" \
"v_accvgpr_write_b32 a76, v204\n" \
"v_accvgpr_write_b32 a77, v205\n" \
"v_mov_b32 v168, v249\n" \
"v_mov_b32 v180, v248\n" \
"v_mov_b32 v181, v248\n" \
"v_mov_b32 v182, v248\n" \
"v_mov_b32 v183, v248\n" \
"v_mov_b32 v160, 0\n" \
"v_mov_b32 v161, 0\n" \
"v_mov_b32 v162, 0\n" \
"v_mov_b32 v163, 0\n" \
"v_mfma_f32_16x16x32_bf16 v[116:119], a[152:155], v[200:203], v[116:119]\n" \
"buffer_load_dwordx4 v6, s[40:43], s16 offen offset:3072 sc1 lds\n" \
"v_accvgpr_write_b32 a78, v206\n" \
"v_accvgpr_write_b32 a79, v207\n" \
"v_mfma_f32_16x16x32_bf16 v[116:119], a[156:159], v[204:207], v[116:119]\n" \
"v_exp_f32 v92, v92\n" \
"v_accvgpr_write_b32 a92, v220\n" \
"v_accvgpr_write_b32 a93, v221\n" \
"v_mov_b32 v176, v240\n" \
"v_mov_b32 v177, v241\n" \
"v_mov_b32 v178, v242\n" \
"v_mov_b32 v179, v243\n" \
"v_mfma_f32_16x16x32_bf16 v[124:127], a[152:155], v[216:219], v[124:127]\n" \
"ds_read_b128 a[148:151], v5 offset:21504\n" \
"v_exp_f32 v93, v93\n" \
"v_cvt_pk_bf16_f32 v141, v90, v91\n" \
"v_cvt_pk_bf16_f32 v130, v68, v69\n" \
"v_cvt_pk_bf16_f32 v131, v70, v71\n" \
"v_cvt_pk_bf16_f32 v134, v76, v77\n" \
"v_cvt_pk_bf16_f32 v135, v78, v79\n" \
"v_accvgpr_write_b32 a94, v222\n" \
"v_accvgpr_write_b32 a95, v223\n" \
"v_mfma_f32_16x16x32_bf16 v[124:127], a[156:159], v[220:223], v[124:127]\n" \
"s_waitcnt lgkmcnt(4)\n" \
"v_exp_f32 v94, v94\n" \
"v_smfmac_f32_16x16x64_bf16 v[160:163], v[180:183], v[128:135], v168\n" \
"v_exp_f32 v95, v95\n" \
"v_mfma_f32_16x16x32_bf16 v[192:195], a[128:131], v[128:131], 0\n" \
"v_exp_f32 v96, v96\n" \
"v_mfma_f32_16x16x32_bf16 v[196:199], a[132:135], v[128:131], 0\n" \
"v_cvt_pk_bf16_f32 v138, v84, v85\n" \
"v_cvt_pk_bf16_f32 v139, v86, v87\n" \
"v_mfma_f32_16x16x32_bf16 v[224:227], a[128:131], v[132:135], 0\n" \
"ds_read_b128 a[152:155], v5 offset:22528\n" \
"v_cvt_pk_bf16_f32 v142, v92, v93\n" \
"v_mfma_f32_16x16x32_bf16 v[228:231], a[132:135], v[132:135], 0\n" \
"v_exp_f32 v97, v97\n" \
"v_mfma_f32_16x16x32_bf16 a[0:3], a[128:131], v[136:139], 0\n" \
"v_exp_f32 v98, v98\n" \
"v_cvt_pk_bf16_f32 v143, v94, v95\n" \
"v_mfma_f32_16x16x32_bf16 a[4:7], a[132:135], v[136:139], 0\n" \
"v_cvt_pk_bf16_f32 v144, v96, v97\n" \
"v_mov_b32 v164, 0\n" \
"v_mfma_f32_16x16x32_bf16 a[32:35], a[128:131], v[140:143], 0\n" \
"ds_read_b128 a[156:159], v5 offset:23552\n" \
"s_waitcnt lgkmcnt(12)\n" \
"v_mfma_f32_16x16x32_bf16 a[36:39], a[132:135], v[140:143], 0\n" \
"s_waitcnt vmcnt(12) lgkmcnt(4)\n" \
"v_exp_f32 v99, v99\n" \
"v_mfma_f32_16x16x32_bf16 v[200:203], a[136:139], v[128:131], 0\n" \
"s_barrier\n" \
"v_exp_f32 v100, v100\n" \
"v_mfma_f32_16x16x32_bf16 v[204:207], a[140:143], v[128:131], 0\n" \
"v_cvt_pk_bf16_f32 v145, v98, v99\n" \
"v_mov_b32 v165, 0\n" \
"v_mfma_f32_16x16x32_bf16 v[232:235], a[136:139], v[132:135], 0\n" \
"s_add_u32 s17, s17, s19\n" \
"ds_read_b128 a[128:131], v5 offset:32768\n" \
"v_mov_b32 v166, 0\n" \
"v_mfma_f32_16x16x32_bf16 v[236:239], a[140:143], v[132:135], 0\n" \
"v_exp_f32 v101, v101\n" \
"v_mfma_f32_16x16x32_bf16 a[8:11], a[136:139], v[136:139], 0\n" \
"s_add_u32 m0, s49, 49152\n" \
"v_exp_f32 v102, v102\n" \
"v_mfma_f32_16x16x32_bf16 a[12:15], a[140:143], v[136:139], 0\n" \
"buffer_load_dwordx4 v6, s[40:43], s17 offen offset:0 sc1 lds\n" \
"v_mfma_f32_16x16x32_bf16 a[40:43], a[136:139], v[140:143], 0\n" \
"ds_read_b128 a[132:135], v5 offset:33792\n" \
"v_cvt_pk_bf16_f32 v146, v100, v101\n" \
"v_mfma_f32_16x16x32_bf16 a[44:47], a[140:143], v[140:143], 0\n" \
"s_waitcnt lgkmcnt(4)\n" \
"v_exp_f32 v103, v103\n" \
"v_mfma_f32_16x16x32_bf16 v[208:211], a[144:147], v[128:131], 0\n" \
"v_exp_f32 v104, v104\n" \
"v_mfma_f32_16x16x32_bf16 v[212:215], a[148:151], v[128:131], 0\n" \
"v_cvt_pk_bf16_f32 v147, v102, v103\n" \
"v_mov_b32 v167, 0\n" \
"v_mfma_f32_16x16x32_bf16 v[240:243], a[144:147], v[132:135], 0\n" \
"ds_read_b128 a[136:139], v5 offset:34816\n" \
"v_lshrrev_b32 v9, 5, v4\n" \
"v_mfma_f32_16x16x32_bf16 v[244:247], a[148:151], v[132:135], 0\n" \
"v_exp_f32 v105, v105\n" \
"v_mfma_f32_16x16x32_bf16 a[16:19], a[144:147], v[136:139], 0\n" \
"v_exp_f32 v106, v106\n" \
"v_mfma_f32_16x16x32_bf16 a[20:23], a[148:151], v[136:139], 0\n" \
"buffer_load_dwordx4 v6, s[40:43], s17 offen offset:1024 sc1 lds\n" \
"v_mfma_f32_16x16x32_bf16 a[48:51], a[144:147], v[140:143], 0\n" \
"ds_read_b128 a[140:143], v5 offset:35840\n" \
"v_cvt_pk_bf16_f32 v148, v104, v105\n" \
"v_mfma_f32_16x16x32_bf16 a[52:55], a[148:151], v[140:143], 0\n" \
"s_waitcnt lgkmcnt(4)\n" \
"v_exp_f32 v107, v107\n" \
"v_mfma_f32_16x16x32_bf16 v[216:219], a[152:155], v[128:131], 0\n" \
"v_exp_f32 v108, v108\n" \
"v_mfma_f32_16x16x32_bf16 v[220:223], a[156:159], v[128:131], 0\n" \
"buffer_load_dwordx4 v6, s[40:43], s17 offen offset:2048 sc1 lds\n" \
"v_mfma_f32_16x16x32_bf16 v[248:251], a[152:155], v[132:135], 0\n" \
"ds_read_b128 a[144:147], v5 offset:36864\n" \
"v_cvt_pk_bf16_f32 v149, v106, v107\n" \
"v_mfma_f32_16x16x32_bf16 v[252:255], a[156:159], v[132:135], 0\n" \
"v_exp_f32 v109, v109\n" \
"v_mfma_f32_16x16x32_bf16 a[24:27], a[152:155], v[136:139], 0\n" \
"v_exp_f32 v110, v110\n" \
"v_mfma_f32_16x16x32_bf16 a[28:31], a[156:159], v[136:139], 0\n" \
"buffer_load_dwordx4 v6, s[40:43], s17 offen offset:3072 sc1 lds\n" \
"v_mfma_f32_16x16x32_bf16 a[56:59], a[152:155], v[140:143], 0\n" \
"ds_read_b128 a[148:151], v5 offset:37888\n" \
"v_cvt_pk_bf16_f32 v150, v108, v109\n" \
"v_mfma_f32_16x16x32_bf16 a[60:63], a[156:159], v[140:143], 0\n" \
"s_waitcnt lgkmcnt(4)\n" \
"v_exp_f32 v111, v111\n" \
"v_smfmac_f32_16x16x64_bf16 v[164:167], v[180:183], v[136:143], v168\n" \
"v_cvt_pk_bf16_f32 v151, v110, v111\n" \
"v_lshlrev_b32 v9, 27, v9\n" \
"v_lshl_add_u32 v9, v4, 4, v9\n" \
"v_mov_b32 v10, s14\n" \
"v_mov_b32 v11, s15\n" \
"s_cmp_lt_i32 s46, 0\n" \
"s_cbranch_scc1 LAD2_%=\n" \
"LCHK_%=:\n" \
"s_cmp_eq_u32 s44, s46\n" \
"s_cbranch_scc1 LMID_%=\n" \
"s_branch LODD_%=\n" \
".p2align 12\n" \
"LH15_%=:\ns_cbranch_execz LHR0_%=\n" \
"s_nop 0\n" \
"LODD_%=:\n" \
"v_mfma_f32_16x16x32_bf16 v[64:67], a[128:131], v[32:35], 0\n" \
"v_exp_f32 v112, v112\n" \
"v_mfma_f32_16x16x32_bf16 v[64:67], a[132:135], v[36:39], v[64:67]\n" \
"s_add_u32 s16, s16, s9\n" \
"v_mfma_f32_16x16x32_bf16 v[72:75], a[128:131], v[48:51], 0\n" \
"ds_read_b128 a[152:155], v5 offset:38912\n" \
"v_mfma_f32_16x16x32_bf16 v[72:75], a[132:135], v[52:55], v[72:75]\n" \
"s_add_u32 m0, s48, 0\n" \
"v_exp_f32 v113, v113\n" \
"v_mfma_f32_16x16x32_bf16 v[80:83], a[128:131], a[64:67], 0\n" \
"buffer_load_dwordx4 v6, s[40:43], s16 offen offset:0 sc1 lds\n" \
"v_mfma_f32_16x16x32_bf16 v[80:83], a[132:135], a[68:71], v[80:83]\n" \
"v_exp_f32 v114, v114\n" \
"v_mfma_f32_16x16x32_bf16 v[88:91], a[128:131], a[80:83], 0\n" \
"ds_read_b128 a[156:159], v5 offset:39936\n" \
"v_cvt_pk_bf16_f32 v152, v112, v113\n" \
"v_mfma_f32_16x16x32_bf16 v[88:91], a[132:135], a[84:87], v[88:91]\n" \
"s_waitcnt lgkmcnt(4)\n" \
"v_exp_f32 v115, v115\n" \
"v_mfma_f32_16x16x32_bf16 v[64:67], a[136:139], v[40:43], v[64:67]\n" \
"v_exp_f32 v116, v116\n" \
"v_mfma_f32_16x16x32_bf16 v[64:67], a[140:143], v[44:47], v[64:67]\n" \
"s_xor_b32 s49, s49, 0x10000\n" \
"v_cvt_pk_bf16_f32 v153, v114, v115\n" \
"v_mfma_f32_16x16x32_bf16 v[72:75], a[136:139], v[56:59], v[72:75]\n" \
"ds_read_b128 a[128:131], v5 offset:49152\n" \
"v_mfma_f32_16x16x32_bf16 v[72:75], a[140:143], v[60:63], v[72:75]\n" \
"v_exp_f32 v117, v117\n" \
"v_mfma_f32_16x16x32_bf16 v[80:83], a[136:139], a[72:75], v[80:83]\n" \
"buffer_load_dwordx4 v6, s[40:43], s16 offen offset:1024 sc1 lds\n" \
"v_mfma_f32_16x16x32_bf16 v[80:83], a[140:143], a[76:79], v[80:83]\n" \
"v_exp_f32 v118, v118\n" \
"v_mfma_f32_16x16x32_bf16 v[88:91], a[136:139], a[88:91], v[88:91]\n" \
"ds_read_b128 a[132:135], v5 offset:50176\n" \
"v_cvt_pk_bf16_f32 v154, v116, v117\n" \
"v_mfma_f32_16x16x32_bf16 v[88:91], a[140:143], a[92:95], v[88:91]\n" \
"s_waitcnt lgkmcnt(4)\n" \
"v_exp_f32 v119, v119\n" \
"v_mfma_f32_16x16x32_bf16 v[68:71], a[144:147], v[32:35], 0\n" \
"v_exp_f32 v120, v120\n" \
"v_mfma_f32_16x16x32_bf16 v[68:71], a[148:151], v[36:39], v[68:71]\n" \
"v_cvt_pk_bf16_f32 v155, v118, v119\n" \
"v_mfma_f32_16x16x32_bf16 v[76:79], a[144:147], v[48:51], 0\n" \
"ds_read_b128 a[136:139], v5 offset:51200\n" \
"v_mfma_f32_16x16x32_bf16 v[76:79], a[148:151], v[52:55], v[76:79]\n" \
"v_exp_f32 v121, v121\n" \
"v_mfma_f32_16x16x32_bf16 v[84:87], a[144:147], a[64:67], 0\n" \
"buffer_load_dwordx4 v6, s[40:43], s16 offen offset:2048 sc1 lds\n" \
"v_mfma_f32_16x16x32_bf16 v[84:87], a[148:151], a[68:71], v[84:87]\n" \
"v_exp_f32 v122, v122\n" \
"v_mfma_f32_16x16x32_bf16 v[92:95], a[144:147], a[80:83], 0\n" \
"ds_read_b128 a[140:143], v5 offset:52224\n" \
"v_cvt_pk_bf16_f32 v156, v120, v121\n" \
"v_mfma_f32_16x16x32_bf16 v[92:95], a[148:151], a[84:87], v[92:95]\n" \
"s_waitcnt lgkmcnt(4)\n" \
"v_exp_f32 v123, v123\n" \
"v_mfma_f32_16x16x32_bf16 v[68:71], a[152:155], v[40:43], v[68:71]\n" \
"v_exp_f32 v124, v124\n" \
"v_mfma_f32_16x16x32_bf16 v[68:71], a[156:159], v[44:47], v[68:71]\n" \
"v_cvt_pk_bf16_f32 v157, v122, v123\n" \
"v_mfma_f32_16x16x32_bf16 v[76:79], a[152:155], v[56:59], v[76:79]\n" \
"ds_read_b128 a[144:147], v5 offset:53248\n" \
"v_mfma_f32_16x16x32_bf16 v[76:79], a[156:159], v[60:63], v[76:79]\n" \
"v_exp_f32 v125, v125\n" \
"v_mfma_f32_16x16x32_bf16 v[84:87], a[152:155], a[72:75], v[84:87]\n" \
"buffer_load_dwordx4 v6, s[40:43], s16 offen offset:3072 sc1 lds\n" \
"v_mfma_f32_16x16x32_bf16 v[84:87], a[156:159], a[76:79], v[84:87]\n" \
"v_exp_f32 v126, v126\n" \
"v_mfma_f32_16x16x32_bf16 v[92:95], a[152:155], a[88:91], v[92:95]\n" \
"ds_read_b128 a[148:151], v5 offset:54272\n" \
"v_cvt_pk_bf16_f32 v158, v124, v125\n" \
"v_mfma_f32_16x16x32_bf16 v[92:95], a[156:159], a[92:95], v[92:95]\n" \
"s_waitcnt lgkmcnt(4)\n" \
"v_exp_f32 v127, v127\n" \
"v_smfmac_f32_16x16x64_bf16 v[160:163], v[180:183], v[144:151], v168\n" \
"v_cvt_pk_bf16_f32 v159, v126, v127\n" \
"v_mfma_f32_16x16x32_bf16 v[192:195], a[128:131], v[144:147], v[192:195]\n" \
"v_exp_f32 v64, v64\n" \
"v_mfma_f32_16x16x32_bf16 v[196:199], a[132:135], v[144:147], v[196:199]\n" \
"v_mfma_f32_16x16x32_bf16 v[224:227], a[128:131], v[148:151], v[224:227]\n" \
"ds_read_b128 a[152:155], v5 offset:55296\n" \
"v_mfma_f32_16x16x32_bf16 v[228:231], a[132:135], v[148:151], v[228:231]\n" \
"v_exp_f32 v65, v65\n" \
"v_mfma_f32_16x16x32_bf16 a[0:3], a[128:131], v[152:155], a[0:3]\n" \
"v_exp_f32 v66, v66\n" \
"v_mfma_f32_16x16x32_bf16 a[4:7], a[132:135], v[152:155], a[4:7]\n" \
"v_cvt_pk_bf16_f32 v128, v64, v65\n" \
"v_mfma_f32_16x16x32_bf16 a[32:35], a[128:131], v[156:159], a[32:35]\n" \
"ds_read_b128 a[156:159], v5 offset:56320\n" \
"v_mfma_f32_16x16x32_bf16 a[36:39], a[132:135], v[156:159], a[36:39]\n" \
"s_waitcnt vmcnt(12) lgkmcnt(4)\n" \
"v_exp_f32 v67, v67\n" \
"v_mfma_f32_16x16x32_bf16 v[200:203], a[136:139], v[144:147], v[200:203]\n" \
"s_barrier\n" \
"v_xor_b32 v5, 0x10000, v5\n" \
"v_cvt_pk_bf16_f32 v129, v66, v67\n" \
"v_mfma_f32_16x16x32_bf16 v[204:207], a[140:143], v[144:147], v[204:207]\n" \
"v_exp_f32 v68, v68\n" \
"v_mfma_f32_16x16x32_bf16 v[232:235], a[136:139], v[148:151], v[232:235]\n" \
"s_add_u32 s17, s17, s19\n" \
"ds_read_b128 a[128:131], v5 offset:0\n" \
"v_mfma_f32_16x16x32_bf16 v[236:239], a[140:143], v[148:151], v[236:239]\n" \
"v_exp_f32 v69, v69\n" \
"v_mfma_f32_16x16x32_bf16 a[8:11], a[136:139], v[152:155], a[8:11]\n" \
"s_add_u32 m0, s48, 16384\n" \
"v_exp_f32 v70, v70\n" \
"v_mfma_f32_16x16x32_bf16 a[12:15], a[140:143], v[152:155], a[12:15]\n" \
"buffer_load_dwordx4 v6, s[40:43], s17 offen offset:0 sc1 lds\n" \
"v_mfma_f32_16x16x32_bf16 a[40:43], a[136:139], v[156:159], a[40:43]\n" \
"ds_read_b128 a[132:135], v5 offset:1024\n" \
"v_cvt_pk_bf16_f32 v130, v68, v69\n" \
"v_mfma_f32_16x16x32_bf16 a[44:47], a[140:143], v[156:159], a[44:47]\n" \
"s_waitcnt lgkmcnt(4)\n" \
"v_exp_f32 v71, v71\n" \
"v_mfma_f32_16x16x32_bf16 v[208:211], a[144:147], v[144:147], v[208:211]\n" \
"v_exp_f32 v72, v72\n" \
"v_mfma_f32_16x16x32_bf16 v[212:215], a[148:151], v[144:147], v[212:215]\n" \
"v_cvt_pk_bf16_f32 v131, v70, v71\n" \
"v_mfma_f32_16x16x32_bf16 v[240:243], a[144:147], v[148:151], v[240:243]\n" \
"ds_read_b128 a[136:139], v5 offset:2048\n" \
"v_mfma_f32_16x16x32_bf16 v[244:247], a[148:151], v[148:151], v[244:247]\n" \
"v_exp_f32 v73, v73\n" \
"v_mfma_f32_16x16x32_bf16 a[16:19], a[144:147], v[152:155], a[16:19]\n" \
"v_exp_f32 v74, v74\n" \
"v_mfma_f32_16x16x32_bf16 a[20:23], a[148:151], v[152:155], a[20:23]\n" \
"buffer_load_dwordx4 v6, s[40:43], s17 offen offset:1024 sc1 lds\n" \
"v_mfma_f32_16x16x32_bf16 a[48:51], a[144:147], v[156:159], a[48:51]\n" \
"ds_read_b128 a[140:143], v5 offset:3072\n" \
"v_cvt_pk_bf16_f32 v132, v72, v73\n" \
"v_mfma_f32_16x16x32_bf16 a[52:55], a[148:151], v[156:159], a[52:55]\n" \
"s_waitcnt lgkmcnt(4)\n" \
"v_exp_f32 v75, v75\n" \
"v_mfma_f32_16x16x32_bf16 v[216:219], a[152:155], v[144:147], v[216:219]\n" \
"v_exp_f32 v76, v76\n" \
"v_mfma_f32_16x16x32_bf16 v[220:223], a[156:159], v[144:147], v[220:223]\n" \
"s_add_u32 s44, s44, 1\n" \
"buffer_load_dwordx4 v6, s[40:43], s17 offen offset:2048 sc1 lds\n" \
"v_mfma_f32_16x16x32_bf16 v[248:251], a[152:155], v[148:151], v[248:251]\n" \
"ds_read_b128 a[144:147], v5 offset:4096\n" \
"v_cvt_pk_bf16_f32 v133, v74, v75\n" \
"v_mfma_f32_16x16x32_bf16 v[252:255], a[156:159], v[148:151], v[252:255]\n" \
"v_exp_f32 v77, v77\n" \
"v_mfma_f32_16x16x32_bf16 a[24:27], a[152:155], v[152:155], a[24:27]\n" \
"v_exp_f32 v78, v78\n" \
"v_mfma_f32_16x16x32_bf16 a[28:31], a[156:159], v[152:155], a[28:31]\n" \
"s_xor_b32 s48, s48, 0x10000\n" \
"buffer_load_dwordx4 v6, s[40:43], s17 offen offset:3072 sc1 lds\n" \
"v_mfma_f32_16x16x32_bf16 a[56:59], a[152:155], v[156:159], a[56:59]\n" \
"ds_read_b128 a[148:151], v5 offset:5120\n" \
"v_cvt_pk_bf16_f32 v134, v76, v77\n" \
"v_mfma_f32_16x16x32_bf16 a[60:63], a[156:159], v[156:159], a[60:63]\n" \
"s_waitcnt lgkmcnt(4)\n" \
"v_exp_f32 v79, v79\n" \
"v_smfmac_f32_16x16x64_bf16 v[164:167], v[180:183], v[152:159], v168\n" \
"v_cvt_pk_bf16_f32 v135, v78, v79\n" \
"LBOT_%=:\n" \
"s_cmp_ge_u32 s44, 14\n" \
"s_cbranch_scc1 LDISP_%=\n" \
"LEVEN_%=:\n" \
"v_mfma_f32_16x16x32_bf16 v[96:99], a[128:131], v[32:35], 0\n" \
"v_exp_f32 v80, v80\n" \
"v_mfma_f32_16x16x32_bf16 v[96:99], a[132:135], v[36:39], v[96:99]\n" \
"s_add_u32 s16, s16, s9\n" \
"v_mfma_f32_16x16x32_bf16 v[104:107], a[128:131], v[48:51], 0\n" \
"ds_read_b128 a[152:155], v5 offset:6144\n" \
"v_mfma_f32_16x16x32_bf16 v[104:107], a[132:135], v[52:55], v[104:107]\n" \
"s_add_u32 m0, s49, 32768\n" \
"v_exp_f32 v81, v81\n" \
"v_mfma_f32_16x16x32_bf16 v[112:115], a[128:131], a[64:67], 0\n" \
"buffer_load_dwordx4 v6, s[40:43], s16 offen offset:0 sc1 lds\n" \
"v_mfma_f32_16x16x32_bf16 v[112:115], a[132:135], a[68:71], v[112:115]\n" \
"v_exp_f32 v82, v82\n" \
"v_mfma_f32_16x16x32_bf16 v[120:123], a[128:131], a[80:83], 0\n" \
"ds_read_b128 a[156:159], v5 offset:7168\n" \
"v_cvt_pk_bf16_f32 v136, v80, v81\n" \
"v_mfma_f32_16x16x32_bf16 v[120:123], a[132:135], a[84:87], v[120:123]\n" \
"s_waitcnt lgkmcnt(4)\n" \
"v_exp_f32 v83, v83\n" \
"v_mfma_f32_16x16x32_bf16 v[96:99], a[136:139], v[40:43], v[96:99]\n" \
"v_exp_f32 v84, v84\n" \
"v_mfma_f32_16x16x32_bf16 v[96:99], a[140:143], v[44:47], v[96:99]\n" \
"v_cvt_pk_bf16_f32 v137, v82, v83\n" \
"v_mfma_f32_16x16x32_bf16 v[104:107], a[136:139], v[56:59], v[104:107]\n" \
"ds_read_b128 a[128:131], v5 offset:16384\n" \
"v_mfma_f32_16x16x32_bf16 v[104:107], a[140:143], v[60:63], v[104:107]\n" \
"v_exp_f32 v85, v85\n" \
"v_mfma_f32_16x16x32_bf16 v[112:115], a[136:139], a[72:75], v[112:115]\n" \
"buffer_load_dwordx4 v6, s[40:43], s16 offen offset:1024 sc1 lds\n" \
"v_mfma_f32_16x16x32_bf16 v[112:115], a[140:143], a[76:79], v[112:115]\n" \
"v_exp_f32 v86, v86\n" \
"v_mfma_f32_16x16x32_bf16 v[120:123], a[136:139], a[88:91], v[120:123]\n" \
"ds_read_b128 a[132:135], v5 offset:17408\n" \
"v_cvt_pk_bf16_f32 v138, v84, v85\n" \
"v_mfma_f32_16x16x32_bf16 v[120:123], a[140:143], a[92:95], v[120:123]\n" \
"s_waitcnt lgkmcnt(4)\n" \
"v_exp_f32 v87, v87\n" \
"v_mfma_f32_16x16x32_bf16 v[100:103], a[144:147], v[32:35], 0\n" \
"v_exp_f32 v88, v88\n" \
"v_mfma_f32_16x16x32_bf16 v[100:103], a[148:151], v[36:39], v[100:103]\n" \
"v_cvt_pk_bf16_f32 v139, v86, v87\n" \
"v_mfma_f32_16x16x32_bf16 v[108:111], a[144:147], v[48:51], 0\n" \
"ds_read_b128 a[136:139], v5 offset:18432\n" \
"v_mfma_f32_16x16x32_bf16 v[108:111], a[148:151], v[52:55], v[108:111]\n" \
"v_exp_f32 v89, v89\n" \
"v_mfma_f32_16x16x32_bf16 v[116:119], a[144:147], a[64:67], 0\n" \
"buffer_load_dwordx4 v6, s[40:43], s16 offen offset:2048 sc1 lds\n" \
"v_mfma_f32_16x16x32_bf16 v[116:119], a[148:151], a[68:71], v[116:119]\n" \
"v_exp_f32 v90, v90\n" \
"v_mfma_f32_16x16x32_bf16 v[124:127], a[144:147], a[80:83], 0\n" \
"ds_read_b128 a[140:143], v5 offset:19456\n" \
"v_cvt_pk_bf16_f32 v140, v88, v89\n" \
"v_mfma_f32_16x16x32_bf16 v[124:127], a[148:151], a[84:87], v[124:127]\n" \
"s_waitcnt lgkmcnt(4)\n" \
"v_exp_f32 v91, v91\n" \
"v_mfma_f32_16x16x32_bf16 v[100:103], a[152:155], v[40:43], v[100:103]\n" \
"v_exp_f32 v92, v92\n" \
"v_mfma_f32_16x16x32_bf16 v[100:103], a[156:159], v[44:47], v[100:103]\n" \
"v_cvt_pk_bf16_f32 v141, v90, v91\n" \
"v_mfma_f32_16x16x32_bf16 v[108:111], a[152:155], v[56:59], v[108:111]\n" \
"ds_read_b128 a[144:147], v5 offset:20480\n" \
"v_mfma_f32_16x16x32_bf16 v[108:111], a[156:159], v[60:63], v[108:111]\n" \
"v_exp_f32 v93, v93\n" \
"v_mfma_f32_16x16x32_bf16 v[116:119], a[152:155], a[72:75], v[116:119]\n" \
"buffer_load_dwordx4 v6, s[40:43], s16 offen offset:3072 sc1 lds\n" \
"v_mfma_f32_16x16x32_bf16 v[116:119], a[156:159], a[76:79], v[116:119]\n" \
"v_exp_f32 v94, v94\n" \
"v_mfma_f32_16x16x32_bf16 v[124:127], a[152:155], a[88:91], v[124:127]\n" \
"ds_read_b128 a[148:151], v5 offset:21504\n" \
"v_cvt_pk_bf16_f32 v142, v92, v93\n" \
"v_mfma_f32_16x16x32_bf16 v[124:127], a[156:159], a[92:95], v[124:127]\n" \
"s_waitcnt lgkmcnt(4)\n" \
"v_exp_f32 v95, v95\n" \
"v_smfmac_f32_16x16x64_bf16 v[160:163], v[180:183], v[128:135], v168\n" \
"v_cvt_pk_bf16_f32 v143, v94, v95\n" \
"v_mfma_f32_16x16x32_bf16 v[192:195], a[128:131], v[128:131], v[192:195]\n" \
"v_exp_f32 v96, v96\n" \
"v_mfma_f32_16x16x32_bf16 v[196:199], a[132:135], v[128:131], v[196:199]\n" \
"v_mfma_f32_16x16x32_bf16 v[224:227], a[128:131], v[132:135], v[224:227]\n" \
"ds_read_b128 a[152:155], v5 offset:22528\n" \
"v_mfma_f32_16x16x32_bf16 v[228:231], a[132:135], v[132:135], v[228:231]\n" \
"v_exp_f32 v97, v97\n" \
"v_mfma_f32_16x16x32_bf16 a[0:3], a[128:131], v[136:139], a[0:3]\n" \
"v_exp_f32 v98, v98\n" \
"v_mfma_f32_16x16x32_bf16 a[4:7], a[132:135], v[136:139], a[4:7]\n" \
"v_cvt_pk_bf16_f32 v144, v96, v97\n" \
"v_mfma_f32_16x16x32_bf16 a[32:35], a[128:131], v[140:143], a[32:35]\n" \
"ds_read_b128 a[156:159], v5 offset:23552\n" \
"v_mfma_f32_16x16x32_bf16 a[36:39], a[132:135], v[140:143], a[36:39]\n" \
"s_waitcnt vmcnt(12) lgkmcnt(4)\n" \
"v_exp_f32 v99, v99\n" \
"v_mfma_f32_16x16x32_bf16 v[200:203], a[136:139], v[128:131], v[200:203]\n" \
"s_barrier\n" \
"v_exp_f32 v100, v100\n" \
"v_mfma_f32_16x16x32_bf16 v[204:207], a[140:143], v[128:131], v[204:207]\n" \
"v_cvt_pk_bf16_f32 v145, v98, v99\n" \
"v_mfma_f32_16x16x32_bf16 v[232:235], a[136:139], v[132:135], v[232:235]\n" \
"s_add_u32 s17, s17, s19\n" \
"ds_read_b128 a[128:131], v5 offset:32768\n" \
"v_mfma_f32_16x16x32_bf16 v[236:239], a[140:143], v[132:135], v[236:239]\n" \
"v_exp_f32 v101, v101\n" \
"v_mfma_f32_16x16x32_bf16 a[8:11], a[136:139], v[136:139], a[8:11]\n" \
"s_add_u32 m0, s49, 49152\n" \
"v_exp_f32 v102, v102\n" \
"v_mfma_f32_16x16x32_bf16 a[12:15], a[140:143], v[136:139], a[12:15]\n" \
"buffer_load_dwordx4 v6, s[40:43], s17 offen offset:0 sc1 lds\n" \
"v_mfma_f32_16x16x32_bf16 a[40:43], a[136:139], v[140:143], a[40:43]\n" \
"ds_read_b128 a[132:135], v5 offset:33792\n" \
"v_cvt_pk_bf16_f32 v146, v100, v101\n" \
"v_mfma_f32_16x16x32_bf16 a[44:47], a[140:143], v[140:143], a[44:47]\n" \
"s_waitcnt lgkmcnt(4)\n" \
"v_exp_f32 v103, v103\n" \
"v_mfma_f32_16x16x32_bf16 v[208:211], a[144:147], v[128:131], v[208:211]\n" \
"v_exp_f32 v104, v104\n" \
"v_mfma_f32_16x16x32_bf16 v[212:215], a[148:151], v[128:131], v[212:215]\n" \
"v_cvt_pk_bf16_f32 v147, v102, v103\n" \
"v_mfma_f32_16x16x32_bf16 v[240:243], a[144:147], v[132:135], v[240:243]\n" \
"ds_read_b128 a[136:139], v5 offset:34816\n" \
"v_mfma_f32_16x16x32_bf16 v[244:247], a[148:151], v[132:135], v[244:247]\n" \
"v_exp_f32 v105, v105\n" \
"v_mfma_f32_16x16x32_bf16 a[16:19], a[144:147], v[136:139], a[16:19]\n" \
"v_exp_f32 v106, v106\n" \
"v_mfma_f32_16x16x32_bf16 a[20:23], a[148:151], v[136:139], a[20:23]\n" \
"buffer_load_dwordx4 v6, s[40:43], s17 offen offset:1024 sc1 lds\n" \
"v_mfma_f32_16x16x32_bf16 a[48:51], a[144:147], v[140:143], a[48:51]\n" \
"ds_read_b128 a[140:143], v5 offset:35840\n" \
"v_cvt_pk_bf16_f32 v148, v104, v105\n" \
"v_mfma_f32_16x16x32_bf16 a[52:55], a[148:151], v[140:143], a[52:55]\n" \
"s_waitcnt lgkmcnt(4)\n" \
"v_exp_f32 v107, v107\n" \
"v_mfma_f32_16x16x32_bf16 v[216:219], a[152:155], v[128:131], v[216:219]\n" \
"v_exp_f32 v108, v108\n" \
"v_mfma_f32_16x16x32_bf16 v[220:223], a[156:159], v[128:131], v[220:223]\n" \
"buffer_load_dwordx4 v6, s[40:43], s17 offen offset:2048 sc1 lds\n" \
"v_mfma_f32_16x16x32_bf16 v[248:251], a[152:155], v[132:135], v[248:251]\n" \
"ds_read_b128 a[144:147], v5 offset:36864\n" \
"v_cvt_pk_bf16_f32 v149, v106, v107\n" \
"v_mfma_f32_16x16x32_bf16 v[252:255], a[156:159], v[132:135], v[252:255]\n" \
"v_exp_f32 v109, v109\n" \
"v_mfma_f32_16x16x32_bf16 a[24:27], a[152:155], v[136:139], a[24:27]\n" \
"v_exp_f32 v110, v110\n" \
"v_mfma_f32_16x16x32_bf16 a[28:31], a[156:159], v[136:139], a[28:31]\n" \
"buffer_load_dwordx4 v6, s[40:43], s17 offen offset:3072 sc1 lds\n" \
"v_mfma_f32_16x16x32_bf16 a[56:59], a[152:155], v[140:143], a[56:59]\n" \
"ds_read_b128 a[148:151], v5 offset:37888\n" \
"v_cvt_pk_bf16_f32 v150, v108, v109\n" \
"v_mfma_f32_16x16x32_bf16 a[60:63], a[156:159], v[140:143], a[60:63]\n" \
"s_waitcnt lgkmcnt(4)\n" \
"v_exp_f32 v111, v111\n" \
"v_smfmac_f32_16x16x64_bf16 v[164:167], v[180:183], v[136:143], v168\n" \
"v_cvt_pk_bf16_f32 v151, v110, v111\n" \
"s_cmp_lg_u32 s44, s46\n" \
"s_cbranch_scc1 LODD_%=\n" \
"LMID_%=:\n" \
"s_cmp_eq_u32 s44, s45\n" \
"s_cbranch_scc1 LWS_%=\n" \
"s_cmp_lt_u32 %2, 2\n" \
"s_cbranch_scc1 LS1_%=\n" \
"s_cmp_eq_u32 s44, 15\n" \
"s_cbranch_scc1 LS1_%=\n" \
"s_lshl_b32 s46, s7, 2\n" \
"s_add_u32 s46, s46, %2\n" \
"s_waitcnt vmcnt(8)\n" \
"v_cmp_ne_u64 vcc, s[14:15], v[18:19]\n" \
"s_cmp_eq_u32 s46, 30\ns_cselect_b32 s46, 15, 99\n" \
"s_cbranch_vccnz LPE0_%=\nLPE0R_%=:\n" \
"s_branch LODD_%=\n" \
".p2align 6\n" \
"LH16_%=:\ns_cbranch_execz LH20_%=\n" \
"s_nop 0\n" \
"LWS_%=:\n" \
"v_mfma_f32_16x16x32_bf16 v[64:67], a[128:131], v[32:35], 0\n" \
"s_mov_b32 s81, 0\n" \
"v_exp_f32 v112, v112\n" \
"v_mfma_f32_16x16x32_bf16 v[64:67], a[132:135], v[36:39], v[64:67]\n" \
"s_add_u32 s16, s16, s9\n" \
"v_mov_b32 v12, v30\n" \
"v_mov_b32 v28, -1\n" \
"v_mfma_f32_16x16x32_bf16 v[72:75], a[128:131], v[48:51], 0\n" \
"ds_read_b128 a[152:155], v5 offset:38912\n" \
"v_mov_b32 v29, -1\n" \
"v_mfma_f32_16x16x32_bf16 v[72:75], a[132:135], v[52:55], v[72:75]\n" \
"s_add_u32 m0, s48, 0\n" \
"v_exp_f32 v113, v113\n" \
"v_mfma_f32_16x16x32_bf16 v[80:83], a[128:131], a[64:67], 0\n" \
"buffer_load_dwordx4 v6, s[40:43], s16 offen offset:0 sc1 lds\n" \
"v_mfma_f32_16x16x32_bf16 v[80:83], a[132:135], a[68:71], v[80:83]\n" \
"v_exp_f32 v114, v114\n" \
"v_mfma_f32_16x16x32_bf16 v[88:91], a[128:131], a[80:83], 0\n" \
"ds_read_b128 a[156:159], v5 offset:39936\n" \
"v_cvt_pk_bf16_f32 v152, v112, v113\n" \
"v_mfma_f32_16x16x32_bf16 v[88:91], a[132:135], a[84:87], v[88:91]\n" \
"s_waitcnt lgkmcnt(4)\n" \
"v_exp_f32 v115, v115\n" \
"s_add_u32 m0, s79, 131072\n" \
"v_mfma_f32_16x16x32_bf16 v[64:67], a[136:139], v[40:43], v[64:67]\n" \
"buffer_load_dwordx4 v12, s[28:31], s81 offen offset:0 lds\n" \
"v_mfma_f32_16x16x32_bf16 v[64:67], a[140:143], v[44:47], v[64:67]\n" \
"s_xor_b32 s49, s49, 0x10000\n" \
"v_exp_f32 v116, v116\n" \
"v_mfma_f32_16x16x32_bf16 v[72:75], a[136:139], v[56:59], v[72:75]\n" \
"ds_read_b128 a[128:131], v5 offset:49152\n" \
"v_cvt_pk_bf16_f32 v153, v114, v115\n" \
"v_mfma_f32_16x16x32_bf16 v[72:75], a[140:143], v[60:63], v[72:75]\n" \
"s_add_u32 m0, s48, 0\n" \
"v_exp_f32 v117, v117\n" \
"v_mfma_f32_16x16x32_bf16 v[80:83], a[136:139], a[72:75], v[80:83]\n" \
"buffer_load_dwordx4 v6, s[40:43], s16 offen offset:1024 sc1 lds\n" \
"v_mfma_f32_16x16x32_bf16 v[80:83], a[140:143], a[76:79], v[80:83]\n" \
"v_exp_f32 v118, v118\n" \
"v_mfma_f32_16x16x32_bf16 v[88:91], a[136:139], a[88:91], v[88:91]\n" \
"ds_read_b128 a[132:135], v5 offset:50176\n" \
"v_cvt_pk_bf16_f32 v154, v116, v117\n" \
"v_mfma_f32_16x16x32_bf16 v[88:91], a[140:143], a[92:95], v[88:91]\n" \
"s_waitcnt lgkmcnt(4)\n" \
"v_exp_f32 v119, v119\n" \
"s_add_u32 m0, s79, 131968\n" \
"v_mfma_f32_16x16x32_bf16 v[68:71], a[144:147], v[32:35], 0\n" \
"buffer_load_dwordx4 v12, s[28:31], s81 offen offset:128 lds\n" \
"v_mfma_f32_16x16x32_bf16 v[68:71], a[148:151], v[36:39], v[68:71]\n" \
"s_mov_b32 s81, 65536\n" \
"v_exp_f32 v120, v120\n" \
"v_mfma_f32_16x16x32_bf16 v[76:79], a[144:147], v[48:51], 0\n" \
"ds_read_b128 a[136:139], v5 offset:51200\n" \
"v_cvt_pk_bf16_f32 v155, v118, v119\n" \
"v_mfma_f32_16x16x32_bf16 v[76:79], a[148:151], v[52:55], v[76:79]\n" \
"s_add_u32 m0, s48, 0\n" \
"v_exp_f32 v121, v121\n" \
"v_mfma_f32_16x16x32_bf16 v[84:87], a[144:147], a[64:67], 0\n" \
"buffer_load_dwordx4 v6, s[40:43], s16 offen offset:2048 sc1 lds\n" \
"v_mfma_f32_16x16x32_bf16 v[84:87], a[148:151], a[68:71], v[84:87]\n" \
"v_exp_f32 v122, v122\n" \
"v_mfma_f32_16x16x32_bf16 v[92:95], a[144:147], a[80:83], 0\n" \
"ds_read_b128 a[140:143], v5 offset:52224\n" \
"v_cvt_pk_bf16_f32 v156, v120, v121\n" \
"v_mfma_f32_16x16x32_bf16 v[92:95], a[148:151], a[84:87], v[92:95]\n" \
"s_waitcnt lgkmcnt(4)\n" \
"v_exp_f32 v123, v123\n" \
"s_add_u32 m0, s79, 139264\n" \
"v_mfma_f32_16x16x32_bf16 v[68:71], a[152:155], v[40:43], v[68:71]\n" \
"buffer_load_dwordx4 v12, s[28:31], s81 offen offset:0 lds\n" \
"v_mfma_f32_16x16x32_bf16 v[68:71], a[156:159], v[44:47], v[68:71]\n" \
"v_exp_f32 v124, v124\n" \
"v_mfma_f32_16x16x32_bf16 v[76:79], a[152:155], v[56:59], v[76:79]\n" \
"ds_read_b128 a[144:147], v5 offset:53248\n" \
"v_cvt_pk_bf16_f32 v157, v122, v123\n" \
"v_mfma_f32_16x16x32_bf16 v[76:79], a[156:159], v[60:63], v[76:79]\n" \
"s_add_u32 m0, s48, 0\n" \
"v_exp_f32 v125, v125\n" \
"v_mfma_f32_16x16x32_bf16 v[84:87], a[152:155], a[72:75], v[84:87]\n" \
"buffer_load_dwordx4 v6, s[40:43], s16 offen offset:3072 sc1 lds\n" \
"v_mfma_f32_16x16x32_bf16 v[84:87], a[156:159], a[76:79], v[84:87]\n" \
"v_exp_f32 v126, v126\n" \
"v_mfma_f32_16x16x32_bf16 v[92:95], a[152:155], a[88:91], v[92:95]\n" \
"ds_read_b128 a[148:151], v5 offset:54272\n" \
"v_cvt_pk_bf16_f32 v158, v124, v125\n" \
"v_mfma_f32_16x16x32_bf16 v[92:95], a[156:159], a[92:95], v[92:95]\n" \
"s_waitcnt lgkmcnt(4)\n" \
"v_exp_f32 v127, v127\n" \
"s_add_u32 m0, s79, 140160\n" \
"v_smfmac_f32_16x16x64_bf16 v[160:163], v[180:183], v[144:151], v168\n" \
"buffer_load_dwordx4 v12, s[28:31], s81 offen offset:128 lds\n" \
"v_mfma_f32_16x16x32_bf16 v[192:195], a[128:131], v[144:147], v[192:195]\n" \
"s_mov_b32 s81, 131072\n" \
"v_exp_f32 v64, v64\n" \
"v_mfma_f32_16x16x32_bf16 v[196:199], a[132:135], v[144:147], v[196:199]\n" \
"v_cvt_pk_bf16_f32 v159, v126, v127\n" \
"v_mov_b32 v30, -1\n" \
"v_mfma_f32_16x16x32_bf16 v[224:227], a[128:131], v[148:151], v[224:227]\n" \
"s_add_u32 s80, s78, 4096\n" \
"ds_read_b128 a[152:155], v5 offset:55296\n" \
"v_mov_b32 v31, -1\n" \
"v_mfma_f32_16x16x32_bf16 v[228:231], a[132:135], v[148:151], v[228:231]\n" \
"buffer_store_dwordx4 v[28:31], v4, s[36:39], s80 offen offset:2048 sc1\n" \
"v_mfma_f32_16x16x32_bf16 a[0:3], a[128:131], v[152:155], a[0:3]\n" \
"v_bfe_u32 v189, v4, 7, 1\n" \
"v_bfe_u32 v190, v4, 4, 3\n" \
"v_mfma_f32_16x16x32_bf16 a[4:7], a[132:135], v[152:155], a[4:7]\n" \
"v_exp_f32 v65, v65\n" \
"v_mfma_f32_16x16x32_bf16 a[32:35], a[128:131], v[156:159], a[32:35]\n" \
"ds_read_b128 a[156:159], v5 offset:56320\n" \
"v_lshl_add_u32 v189, v190, 1, v189\n" \
"v_mfma_f32_16x16x32_bf16 a[36:39], a[132:135], v[156:159], a[36:39]\n" \
"s_waitcnt vmcnt(17) lgkmcnt(4)\n" \
"v_exp_f32 v66, v66\n" \
"v_mfma_f32_16x16x32_bf16 v[200:203], a[136:139], v[144:147], v[200:203]\n" \
"s_barrier\n" \
"v_xor_b32 v5, 0x10000, v5\n" \
"v_lshrrev_b32 v190, 8, v4\n" \
"v_mfma_f32_16x16x32_bf16 v[204:207], a[140:143], v[144:147], v[204:207]\n" \
"v_exp_f32 v67, v67\n" \
"v_mfma_f32_16x16x32_bf16 v[232:235], a[136:139], v[148:151], v[232:235]\n" \
"s_add_u32 s17, s17, s19\n" \
"ds_read_b128 a[128:131], v5 offset:0\n" \
"v_lshlrev_b32 v188, 11, v189\n" \
"v_mfma_f32_16x16x32_bf16 v[236:239], a[140:143], v[148:151], v[236:239]\n" \
"v_exp_f32 v68, v68\n" \
"v_mfma_f32_16x16x32_bf16 a[8:11], a[136:139], v[152:155], a[8:11]\n" \
"s_add_u32 m0, s48, 16384\n" \
"v_exp_f32 v69, v69\n" \
"v_mfma_f32_16x16x32_bf16 a[12:15], a[140:143], v[152:155], a[12:15]\n" \
"buffer_load_dwordx4 v6, s[40:43], s17 offen offset:0 sc1 lds\n" \
"v_mfma_f32_16x16x32_bf16 a[40:43], a[136:139], v[156:159], a[40:43]\n" \
"ds_read_b128 a[132:135], v5 offset:1024\n" \
"v_and_b32 v189, 1, v190\n" \
"v_mfma_f32_16x16x32_bf16 a[44:47], a[140:143], v[156:159], a[44:47]\n" \
"s_waitcnt lgkmcnt(4)\n" \
"v_exp_f32 v70, v70\n" \
"v_mfma_f32_16x16x32_bf16 v[208:211], a[144:147], v[144:147], v[208:211]\n" \
"v_exp_f32 v71, v71\n" \
"v_mfma_f32_16x16x32_bf16 v[212:215], a[148:151], v[144:147], v[212:215]\n" \
"v_exp_f32 v72, v72\n" \
"v_mfma_f32_16x16x32_bf16 v[240:243], a[144:147], v[148:151], v[240:243]\n" \
"ds_read_b128 a[136:139], v5 offset:2048\n" \
"v_lshl_add_u32 v188, v189, 5, v188\n" \
"v_mfma_f32_16x16x32_bf16 v[244:247], a[148:151], v[148:151], v[244:247]\n" \
"v_exp_f32 v73, v73\n" \
"v_mfma_f32_16x16x32_bf16 a[16:19], a[144:147], v[152:155], a[16:19]\n" \
"s_add_u32 m0, s48, 16384\n" \
"v_exp_f32 v74, v74\n" \
"v_mfma_f32_16x16x32_bf16 a[20:23], a[148:151], v[152:155], a[20:23]\n" \
"buffer_load_dwordx4 v6, s[40:43], s17 offen offset:1024 sc1 lds\n" \
"v_mfma_f32_16x16x32_bf16 a[48:51], a[144:147], v[156:159], a[48:51]\n" \
"ds_read_b128 a[140:143], v5 offset:3072\n" \
"v_lshrrev_b32 v189, 1, v190\n" \
"v_mfma_f32_16x16x32_bf16 a[52:55], a[148:151], v[156:159], a[52:55]\n" \
"s_waitcnt lgkmcnt(4)\n" \
"v_exp_f32 v75, v75\n" \
"v_mfma_f32_16x16x32_bf16 v[216:219], a[152:155], v[144:147], v[216:219]\n" \
"s_add_u32 m0, s48, 16384\n" \
"v_exp_f32 v76, v76\n" \
"v_mfma_f32_16x16x32_bf16 v[220:223], a[156:159], v[144:147], v[220:223]\n" \
"s_add_u32 s44, s44, 1\n" \
"buffer_load_dwordx4 v6, s[40:43], s17 offen offset:2048 sc1 lds\n" \
"v_mfma_f32_16x16x32_bf16 v[248:251], a[152:155], v[148:151], v[248:251]\n" \
"ds_read_b128 a[144:147], v5 offset:4096\n" \
"v_lshl_add_u32 v188, v189, 4, v188\n" \
"v_mfma_f32_16x16x32_bf16 v[252:255], a[156:159], v[148:151], v[252:255]\n" \
"v_exp_f32 v77, v77\n" \
"v_mfma_f32_16x16x32_bf16 a[24:27], a[152:155], v[152:155], a[24:27]\n" \
"s_add_u32 m0, s48, 16384\n" \
"v_exp_f32 v78, v78\n" \
"v_mfma_f32_16x16x32_bf16 a[28:31], a[156:159], v[152:155], a[28:31]\n" \
"s_xor_b32 s48, s48, 0x10000\n" \
"buffer_load_dwordx4 v6, s[40:43], s17 offen offset:3072 sc1 lds\n" \
"v_mfma_f32_16x16x32_bf16 a[56:59], a[152:155], v[156:159], a[56:59]\n" \
"ds_read_b128 a[148:151], v5 offset:5120\n" \
"v_add_u32 v190, s83, v188\n" \
"v_mfma_f32_16x16x32_bf16 a[60:63], a[156:159], v[156:159], a[60:63]\n" \
"s_waitcnt lgkmcnt(4)\n" \
"v_exp_f32 v79, v79\n" \
"v_smfmac_f32_16x16x64_bf16 v[164:167], v[180:183], v[152:159], v168\n" \
"v_add_u32 v188, s82, v188\n" \
"v_cvt_pk_bf16_f32 v128, v64, v65\n" \
"v_cvt_pk_bf16_f32 v129, v66, v67\n" \
"v_cvt_pk_bf16_f32 v130, v68, v69\n" \
"v_cvt_pk_bf16_f32 v131, v70, v71\n" \
"v_cvt_pk_bf16_f32 v132, v72, v73\n" \
"v_cvt_pk_bf16_f32 v133, v74, v75\n" \
"v_cvt_pk_bf16_f32 v134, v76, v77\n" \
"v_cvt_pk_bf16_f32 v135, v78, v79\n" \
"v_mfma_f32_16x16x32_bf16 v[96:99], a[128:131], v[32:35], 0\n" \
"v_add_u32 v189, 0x8000, v188\n" \
"v_add_u32 v191, 0x8000, v190\n" \
"v_mfma_f32_16x16x32_bf16 v[96:99], a[132:135], v[36:39], v[96:99]\n" \
"s_add_u32 s16, s16, s9\n" \
"v_exp_f32 v80, v80\n" \
"v_mfma_f32_16x16x32_bf16 v[104:107], a[128:131], v[48:51], 0\n" \
"ds_read_b128 a[152:155], v5 offset:6144\n" \
"v_mfma_f32_16x16x32_bf16 v[104:107], a[132:135], v[52:55], v[104:107]\n" \
"s_add_u32 m0, s49, 32768\n" \
"v_exp_f32 v81, v81\n" \
"v_mfma_f32_16x16x32_bf16 v[112:115], a[128:131], a[64:67], 0\n" \
"buffer_load_dwordx4 v6, s[40:43], s16 offen offset:0 sc1 lds\n" \
"v_mfma_f32_16x16x32_bf16 v[112:115], a[132:135], a[68:71], v[112:115]\n" \
"v_exp_f32 v82, v82\n" \
"v_mfma_f32_16x16x32_bf16 v[120:123], a[128:131], a[80:83], 0\n" \
"ds_read_b128 a[156:159], v5 offset:7168\n" \
"v_cvt_pk_bf16_f32 v136, v80, v81\n" \
"v_mfma_f32_16x16x32_bf16 v[120:123], a[132:135], a[84:87], v[120:123]\n" \
"s_waitcnt lgkmcnt(4)\n" \
"v_exp_f32 v83, v83\n" \
"s_add_u32 m0, s79, 147456\n" \
"v_mfma_f32_16x16x32_bf16 v[96:99], a[136:139], v[40:43], v[96:99]\n" \
"buffer_load_dwordx4 v12, s[28:31], s81 offen offset:0 lds\n" \
"v_mfma_f32_16x16x32_bf16 v[96:99], a[140:143], v[44:47], v[96:99]\n" \
"v_exp_f32 v84, v84\n" \
"v_mfma_f32_16x16x32_bf16 v[104:107], a[136:139], v[56:59], v[104:107]\n" \
"ds_read_b128 a[128:131], v5 offset:16384\n" \
"v_cvt_pk_bf16_f32 v137, v82, v83\n" \
"v_mfma_f32_16x16x32_bf16 v[104:107], a[140:143], v[60:63], v[104:107]\n" \
"s_add_u32 m0, s49, 32768\n" \
"v_exp_f32 v85, v85\n" \
"v_mfma_f32_16x16x32_bf16 v[112:115], a[136:139], a[72:75], v[112:115]\n" \
"buffer_load_dwordx4 v6, s[40:43], s16 offen offset:1024 sc1 lds\n" \
"v_mfma_f32_16x16x32_bf16 v[112:115], a[140:143], a[76:79], v[112:115]\n" \
"v_exp_f32 v86, v86\n" \
"v_mfma_f32_16x16x32_bf16 v[120:123], a[136:139], a[88:91], v[120:123]\n" \
"ds_read_b128 a[132:135], v5 offset:17408\n" \
"v_cvt_pk_bf16_f32 v138, v84, v85\n" \
"v_mfma_f32_16x16x32_bf16 v[120:123], a[140:143], a[92:95], v[120:123]\n" \
"s_waitcnt lgkmcnt(4)\n" \
"v_exp_f32 v87, v87\n" \
"s_add_u32 m0, s79, 148352\n" \
"v_mfma_f32_16x16x32_bf16 v[100:103], a[144:147], v[32:35], 0\n" \
"buffer_load_dwordx4 v12, s[28:31], s81 offen offset:128 lds\n" \
"v_mfma_f32_16x16x32_bf16 v[100:103], a[148:151], v[36:39], v[100:103]\n" \
"s_mov_b32 s81, 196608\n" \
"v_exp_f32 v88, v88\n" \
"v_mfma_f32_16x16x32_bf16 v[108:111], a[144:147], v[48:51], 0\n" \
"ds_read_b128 a[136:139], v5 offset:18432\n" \
"v_cvt_pk_bf16_f32 v139, v86, v87\n" \
"v_mfma_f32_16x16x32_bf16 v[108:111], a[148:151], v[52:55], v[108:111]\n" \
"s_add_u32 m0, s49, 32768\n" \
"v_exp_f32 v89, v89\n" \
"v_mfma_f32_16x16x32_bf16 v[116:119], a[144:147], a[64:67], 0\n" \
"buffer_load_dwordx4 v6, s[40:43], s16 offen offset:2048 sc1 lds\n" \
"v_mfma_f32_16x16x32_bf16 v[116:119], a[148:151], a[68:71], v[116:119]\n" \
"v_exp_f32 v90, v90\n" \
"v_mfma_f32_16x16x32_bf16 v[124:127], a[144:147], a[80:83], 0\n" \
"ds_read_b128 a[140:143], v5 offset:19456\n" \
"v_cvt_pk_bf16_f32 v140, v88, v89\n" \
"v_mfma_f32_16x16x32_bf16 v[124:127], a[148:151], a[84:87], v[124:127]\n" \
"s_waitcnt lgkmcnt(4)\n" \
"v_exp_f32 v91, v91\n" \
"s_add_u32 m0, s79, 155648\n" \
"v_mfma_f32_16x16x32_bf16 v[100:103], a[152:155], v[40:43], v[100:103]\n" \
"buffer_load_dwordx4 v12, s[28:31], s81 offen offset:0 lds\n" \
"v_mfma_f32_16x16x32_bf16 v[100:103], a[156:159], v[44:47], v[100:103]\n" \
"v_exp_f32 v92, v92\n" \
"v_mfma_f32_16x16x32_bf16 v[108:111], a[152:155], v[56:59], v[108:111]\n" \
"ds_read_b128 a[144:147], v5 offset:20480\n" \
"v_cvt_pk_bf16_f32 v141, v90, v91\n" \
"v_mfma_f32_16x16x32_bf16 v[108:111], a[156:159], v[60:63], v[108:111]\n" \
"s_add_u32 m0, s49, 32768\n" \
"v_exp_f32 v93, v93\n" \
"v_mfma_f32_16x16x32_bf16 v[116:119], a[152:155], a[72:75], v[116:119]\n" \
"buffer_load_dwordx4 v6, s[40:43], s16 offen offset:3072 sc1 lds\n" \
"v_mfma_f32_16x16x32_bf16 v[116:119], a[156:159], a[76:79], v[116:119]\n" \
"v_exp_f32 v94, v94\n" \
"v_mfma_f32_16x16x32_bf16 v[124:127], a[152:155], a[88:91], v[124:127]\n" \
"ds_read_b128 a[148:151], v5 offset:21504\n" \
"v_cvt_pk_bf16_f32 v142, v92, v93\n" \
"v_mfma_f32_16x16x32_bf16 v[124:127], a[156:159], a[92:95], v[124:127]\n" \
"LH17_%=:\ns_cbranch_execz LH21_%=\n" \
"s_waitcnt lgkmcnt(4)\n" \
"v_exp_f32 v95, v95\n" \
"s_add_u32 m0, s79, 156544\n" \
"v_smfmac_f32_16x16x64_bf16 v[160:163], v[180:183], v[128:135], v168\n" \
"buffer_load_dwordx4 v12, s[28:31], s81 offen offset:128 lds\n" \
"v_mfma_f32_16x16x32_bf16 v[192:195], a[128:131], v[128:131], v[192:195]\n" \
"s_mov_b32 s81, 262144\n" \
"v_exp_f32 v96, v96\n" \
"v_mfma_f32_16x16x32_bf16 v[196:199], a[132:135], v[128:131], v[196:199]\n" \
"buffer_store_dwordx4 v[28:31], v4, s[36:39], s80 offen offset:3072 sc1\n" \
"v_mfma_f32_16x16x32_bf16 v[224:227], a[128:131], v[132:135], v[224:227]\n" \
"s_add_u32 s80, s78, 8192\n" \
"ds_read_b128 a[152:155], v5 offset:22528\n" \
"v_cvt_pk_bf16_f32 v143, v94, v95\n" \
"v_mfma_f32_16x16x32_bf16 v[228:231], a[132:135], v[132:135], v[228:231]\n" \
"global_load_dwordx4 a[96:99], v188, s[60:61]\n" \
"v_mfma_f32_16x16x32_bf16 a[0:3], a[128:131], v[136:139], a[0:3]\n" \
"v_exp_f32 v97, v97\n" \
"v_mfma_f32_16x16x32_bf16 a[4:7], a[132:135], v[136:139], a[4:7]\n" \
"buffer_store_dwordx4 v[28:31], v4, s[36:39], s80 offen offset:0 sc1\n" \
"v_mfma_f32_16x16x32_bf16 a[32:35], a[128:131], v[140:143], a[32:35]\n" \
"ds_read_b128 a[156:159], v5 offset:23552\n" \
"v_cvt_pk_bf16_f32 v144, v96, v97\n" \
"v_mfma_f32_16x16x32_bf16 a[36:39], a[132:135], v[140:143], a[36:39]\n" \
"s_waitcnt vmcnt(16) lgkmcnt(4)\n" \
"v_exp_f32 v98, v98\n" \
"v_mfma_f32_16x16x32_bf16 v[200:203], a[136:139], v[128:131], v[200:203]\n" \
"s_barrier\n" \
"v_exp_f32 v99, v99\n" \
"v_mfma_f32_16x16x32_bf16 v[204:207], a[140:143], v[128:131], v[204:207]\n" \
"global_load_dwordx4 a[100:103], v189, s[60:61]\n" \
"v_mfma_f32_16x16x32_bf16 v[232:235], a[136:139], v[132:135], v[232:235]\n" \
"s_add_u32 s17, s17, s19\n" \
"ds_read_b128 a[128:131], v5 offset:32768\n" \
"v_cvt_pk_bf16_f32 v145, v98, v99\n" \
"v_mfma_f32_16x16x32_bf16 v[236:239], a[140:143], v[132:135], v[236:239]\n" \
"v_exp_f32 v100, v100\n" \
"v_mfma_f32_16x16x32_bf16 a[8:11], a[136:139], v[136:139], a[8:11]\n" \
"s_add_u32 m0, s49, 49152\n" \
"v_exp_f32 v101, v101\n" \
"v_mfma_f32_16x16x32_bf16 a[12:15], a[140:143], v[136:139], a[12:15]\n" \
"buffer_load_dwordx4 v6, s[40:43], s17 offen offset:0 sc1 lds\n" \
"v_mfma_f32_16x16x32_bf16 a[40:43], a[136:139], v[140:143], a[40:43]\n" \
"ds_read_b128 a[132:135], v5 offset:33792\n" \
"v_cvt_pk_bf16_f32 v146, v100, v101\n" \
"v_mfma_f32_16x16x32_bf16 a[44:47], a[140:143], v[140:143], a[44:47]\n" \
"s_waitcnt lgkmcnt(4)\n" \
"v_exp_f32 v102, v102\n" \
"v_mfma_f32_16x16x32_bf16 v[208:211], a[144:147], v[128:131], v[208:211]\n" \
"v_exp_f32 v103, v103\n" \
"v_mfma_f32_16x16x32_bf16 v[212:215], a[148:151], v[128:131], v[212:215]\n" \
"v_exp_f32 v104, v104\n" \
"v_mfma_f32_16x16x32_bf16 v[240:243], a[144:147], v[132:135], v[240:243]\n" \
"ds_read_b128 a[136:139], v5 offset:34816\n" \
"v_cvt_pk_bf16_f32 v147, v102, v103\n" \
"v_mfma_f32_16x16x32_bf16 v[244:247], a[148:151], v[132:135], v[244:247]\n" \
"v_exp_f32 v105, v105\n" \
"v_mfma_f32_16x16x32_bf16 a[16:19], a[144:147], v[136:139], a[16:19]\n" \
"s_add_u32 m0, s49, 49152\n" \
"v_exp_f32 v106, v106\n" \
"v_mfma_f32_16x16x32_bf16 a[20:23], a[148:151], v[136:139], a[20:23]\n" \
"buffer_load_dwordx4 v6, s[40:43], s17 offen offset:1024 sc1 lds\n" \
"v_mfma_f32_16x16x32_bf16 a[48:51], a[144:147], v[140:143], a[48:51]\n" \
"ds_read_b128 a[140:143], v5 offset:35840\n" \
"v_cvt_pk_bf16_f32 v148, v104, v105\n" \
"v_mfma_f32_16x16x32_bf16 a[52:55], a[148:151], v[140:143], a[52:55]\n" \
"s_waitcnt lgkmcnt(4)\n" \
"v_exp_f32 v107, v107\n" \
"v_mfma_f32_16x16x32_bf16 v[216:219], a[152:155], v[128:131], v[216:219]\n" \
"s_add_u32 m0, s49, 49152\n" \
"v_exp_f32 v108, v108\n" \
"v_mfma_f32_16x16x32_bf16 v[220:223], a[156:159], v[128:131], v[220:223]\n" \
"buffer_load_dwordx4 v6, s[40:43], s17 offen offset:2048 sc1 lds\n" \
"v_mfma_f32_16x16x32_bf16 v[248:251], a[152:155], v[132:135], v[248:251]\n" \
"ds_read_b128 a[144:147], v5 offset:36864\n" \
"v_cvt_pk_bf16_f32 v149, v106, v107\n" \
"v_mfma_f32_16x16x32_bf16 v[252:255], a[156:159], v[132:135], v[252:255]\n" \
"v_exp_f32 v109, v109\n" \
"v_mfma_f32_16x16x32_bf16 a[24:27], a[152:155], v[136:139], a[24:27]\n" \
"s_add_u32 m0, s49, 49152\n" \
"v_exp_f32 v110, v110\n" \
"v_mfma_f32_16x16x32_bf16 a[28:31], a[156:159], v[136:139], a[28:31]\n" \
"buffer_load_dwordx4 v6, s[40:43], s17 offen offset:3072 sc1 lds\n" \
"v_mfma_f32_16x16x32_bf16 a[56:59], a[152:155], v[140:143], a[56:59]\n" \
"ds_read_b128 a[148:151], v5 offset:37888\n" \
"v_cvt_pk_bf16_f32 v150, v108, v109\n" \
"v_mfma_f32_16x16x32_bf16 a[60:63], a[156:159], v[140:143], a[60:63]\n" \
"s_waitcnt lgkmcnt(4)\n" \
"v_exp_f32 v111, v111\n" \
"v_smfmac_f32_16x16x64_bf16 v[164:167], v[180:183], v[136:143], v168\n" \
"ds_read_b64_tr_b16 a[160:161], v13 offset:0\n" \
"v_cvt_pk_bf16_f32 v151, v110, v111\n" \
"v_mfma_f32_16x16x32_bf16 v[64:67], a[128:131], v[32:35], 0\n" \
"global_load_dwordx4 a[104:107], v190, s[60:61]\n" \
"v_mfma_f32_16x16x32_bf16 v[64:67], a[132:135], v[36:39], v[64:67]\n" \
"s_add_u32 s16, s16, s9\n" \
"ds_read_b64_tr_b16 a[162:163], v14 offset:0\n" \
"v_mfma_f32_16x16x32_bf16 v[72:75], a[128:131], v[48:51], 0\n" \
"s_add_u32 s100, s100, 262144\ns_addc_u32 s101, s101, 0\n" \
"ds_read_b128 a[152:155], v5 offset:38912\n" \
"v_mfma_f32_16x16x32_bf16 v[72:75], a[132:135], v[52:55], v[72:75]\n" \
"s_add_u32 m0, s48, 0\n" \
"ds_read_b64_tr_b16 a[164:165], v15 offset:0\n" \
"v_mfma_f32_16x16x32_bf16 v[80:83], a[128:131], a[64:67], 0\n" \
"buffer_load_dwordx4 v6, s[40:43], s16 offen offset:0 sc1 lds\n" \
"v_mfma_f32_16x16x32_bf16 v[80:83], a[132:135], a[68:71], v[80:83]\n" \
"ds_read_b64_tr_b16 a[166:167], v16 offset:0\n" \
"v_mfma_f32_16x16x32_bf16 v[88:91], a[128:131], a[80:83], 0\n" \
"ds_read_b128 a[156:159], v5 offset:39936\n" \
"v_mfma_f32_16x16x32_bf16 v[88:91], a[132:135], a[84:87], v[88:91]\n" \
"s_waitcnt lgkmcnt(4)\n" \
"ds_read_b64_tr_b16 a[168:169], v13 offset:8192\n" \
"v_mfma_f32_16x16x32_bf16 v[64:67], a[136:139], v[40:43], v[64:67]\n" \
"global_load_dwordx4 a[108:111], v191, s[60:61]\n" \
"v_mfma_f32_16x16x32_bf16 v[64:67], a[140:143], v[44:47], v[64:67]\n" \
"s_xor_b32 s49, s49, 0x10000\n" \
"s_lshl_b32 s92, %2, 9\n" \
"s_add_u32 s92, s92, 94375936\n" \
"v_mfma_f32_16x16x32_bf16 v[72:75], a[136:139], v[56:59], v[72:75]\n" \
"ds_read_b128 a[128:131], v5 offset:49152\n" \
"v_lshrrev_b32 v17, 1, v4\n" \
"v_mfma_f32_16x16x32_bf16 v[72:75], a[140:143], v[60:63], v[72:75]\n" \
"s_add_u32 m0, s48, 0\n" \
"v_exp_f32 v112, v112\n" \
"v_mfma_f32_16x16x32_bf16 v[80:83], a[136:139], a[72:75], v[80:83]\n" \
"buffer_load_dwordx4 v6, s[40:43], s16 offen offset:1024 sc1 lds\n" \
"v_mfma_f32_16x16x32_bf16 v[80:83], a[140:143], a[76:79], v[80:83]\n" \
"v_exp_f32 v113, v113\n" \
"v_mfma_f32_16x16x32_bf16 v[88:91], a[136:139], a[88:91], v[88:91]\n" \
"s_mov_b64 s[4:5], 0\n" \
"ds_read_b128 a[132:135], v5 offset:50176\n" \
"v_add_u32 v17, s92, v17\n" \
"v_mfma_f32_16x16x32_bf16 v[88:91], a[140:143], a[92:95], v[88:91]\n" \
"s_waitcnt lgkmcnt(4)\n" \
"v_exp_f32 v114, v114\n" \
"v_mfma_f32_16x16x32_bf16 v[68:71], a[144:147], v[32:35], 0\n" \
"s_lshr_b32 s92, %1, 5\n" \
"v_exp_f32 v115, v115\n" \
"v_mfma_f32_16x16x32_bf16 v[68:71], a[148:151], v[36:39], v[68:71]\n" \
"s_lshl_b32 s92, s92, 2\n" \
"v_exp_f32 v116, v116\n" \
"v_mfma_f32_16x16x32_bf16 v[76:79], a[144:147], v[48:51], 0\n" \
"s_add_u32 s92, s92, %2\n" \
"ds_read_b128 a[136:139], v5 offset:51200\n" \
"s_lshl_b32 s92, s92, 11\n" \
"v_mfma_f32_16x16x32_bf16 v[76:79], a[148:151], v[52:55], v[76:79]\n" \
"s_add_u32 m0, s48, 0\n" \
"v_exp_f32 v117, v117\n" \
"v_mfma_f32_16x16x32_bf16 v[84:87], a[144:147], a[64:67], 0\n" \
"buffer_load_dwordx4 v6, s[40:43], s16 offen offset:2048 sc1 lds\n" \
"v_mfma_f32_16x16x32_bf16 v[84:87], a[148:151], a[68:71], v[84:87]\n" \
"v_exp_f32 v118, v118\n" \
"v_mfma_f32_16x16x32_bf16 v[92:95], a[144:147], a[80:83], 0\n" \
"ds_read_b128 a[140:143], v5 offset:52224\n" \
"v_and_b32 v188, 0xf0, v4\n" \
"v_mfma_f32_16x16x32_bf16 v[92:95], a[148:151], a[84:87], v[92:95]\n" \
"s_waitcnt lgkmcnt(4)\n" \
"v_exp_f32 v119, v119\n" \
"v_mfma_f32_16x16x32_bf16 v[68:71], a[152:155], v[40:43], v[68:71]\n" \
"v_exp_f32 v120, v120\n" \
"v_mfma_f32_16x16x32_bf16 v[68:71], a[156:159], v[44:47], v[68:71]\n" \
"v_exp_f32 v121, v121\n" \
"v_mfma_f32_16x16x32_bf16 v[76:79], a[152:155], v[56:59], v[76:79]\n" \
"ds_read_b128 a[144:147], v5 offset:53248\n" \
"v_lshl_add_u32 v188, v188, 3, s92\n" \
"v_mfma_f32_16x16x32_bf16 v[76:79], a[156:159], v[60:63], v[76:79]\n" \
"s_add_u32 m0, s48, 0\n" \
"v_exp_f32 v122, v122\n" \
"v_mfma_f32_16x16x32_bf16 v[84:87], a[152:155], a[72:75], v[84:87]\n" \
"buffer_load_dwordx4 v6, s[40:43], s16 offen offset:3072 sc1 lds\n" \
"v_mfma_f32_16x16x32_bf16 v[84:87], a[156:159], a[76:79], v[84:87]\n" \
"v_exp_f32 v123, v123\n" \
"v_mfma_f32_16x16x32_bf16 v[92:95], a[152:155], a[88:91], v[92:95]\n" \
"ds_read_b128 a[148:151], v5 offset:54272\n" \
"v_lshrrev_b32 v189, 8, v4\n" \
"v_mfma_f32_16x16x32_bf16 v[92:95], a[156:159], a[92:95], v[92:95]\n" \
"s_waitcnt lgkmcnt(4)\n" \
"v_exp_f32 v124, v124\n" \
"v_smfmac_f32_16x16x64_bf16 v[160:163], v[180:183], v[144:151], v168\n" \
"s_mov_b64 exec, 0xffff\nglobal_load_dword v1, v2, s[100:101]\ns_mov_b64 exec, -1\n" \
"v_mfma_f32_16x16x32_bf16 v[192:195], a[128:131], v[144:147], v[192:195]\n" \
"v_exp_f32 v125, v125\n" \
"v_mfma_f32_16x16x32_bf16 v[196:199], a[132:135], v[144:147], v[196:199]\n" \
"ds_read_b64_tr_b16 a[170:171], v14 offset:8192\n" \
"ds_read_b64_tr_b16 a[172:173], v15 offset:8192\n" \
"ds_read_b64_tr_b16 a[174:175], v16 offset:8192\n" \
"v_mfma_f32_16x16x32_bf16 v[224:227], a[128:131], v[148:151], v[224:227]\n" \
"ds_read_b128 a[152:155], v5 offset:55296\n" \
"v_lshl_add_u32 v190, v189, 18, v188\n" \
"v_exp_f32 v126, v126\n" \
"v_cvt_pk_bf16_f32 v152, v112, v113\n" \
"v_cvt_pk_bf16_f32 v153, v114, v115\n" \
"v_cvt_pk_bf16_f32 v154, v116, v117\n" \
"v_cvt_pk_bf16_f32 v155, v118, v119\n" \
"v_mfma_f32_16x16x32_bf16 v[228:231], a[132:135], v[148:151], v[228:231]\n" \
"v_exp_f32 v127, v127\n" \
"v_mfma_f32_16x16x32_bf16 a[0:3], a[128:131], v[152:155], a[0:3]\n" \
"v_exp_f32 v64, v64\n" \
"v_cvt_pk_bf16_f32 v156, v120, v121\n" \
"v_cvt_pk_bf16_f32 v157, v122, v123\n" \
"v_cvt_pk_bf16_f32 v158, v124, v125\n" \
"v_cvt_pk_bf16_f32 v159, v126, v127\n" \
"v_mfma_f32_16x16x32_bf16 a[4:7], a[132:135], v[152:155], a[4:7]\n" \
"s_waitcnt lgkmcnt(1)\n" \
"v_exp_f32 v65, v65\n" \
"v_mfma_f32_16x16x32_bf16 a[32:35], a[128:131], v[156:159], a[32:35]\n" \
"s_mul_i32 s92, s90, 12\n" \
"ds_read_b128 a[156:159], v5 offset:56320\n" \
"v_min_u32 v189, 2, v189\n" \
"v_mfma_f32_16x16x32_bf16 a[36:39], a[132:135], v[156:159], a[36:39]\n" \
"s_waitcnt vmcnt(15) lgkmcnt(4)\n" \
"v_exp_f32 v66, v66\n" \
"v_mfma_f32_16x16x32_bf16 v[200:203], a[136:139], v[144:147], v[200:203]\n" \
"s_barrier\n" \
"v_xor_b32 v5, 0x10000, v5\n" \
"s_add_u32 s92, s92, s89\n" \
"v_mfma_f32_16x16x32_bf16 v[204:207], a[140:143], v[144:147], v[204:207]\n" \
"s_lshl_b32 s92, s92, 16\n" \
"v_exp_f32 v67, v67\n" \
"v_mfma_f32_16x16x32_bf16 v[232:235], a[136:139], v[148:151], v[232:235]\n" \
"s_add_u32 s17, s17, s19\n" \
"ds_read_b128 a[128:131], v5 offset:0\n" \
"v_lshl_add_u32 v191, v189, 18, v188\n" \
"v_mfma_f32_16x16x32_bf16 v[236:239], a[140:143], v[148:151], v[236:239]\n" \
"v_exp_f32 v68, v68\n" \
"v_mfma_f32_16x16x32_bf16 a[8:11], a[136:139], v[152:155], a[8:11]\n" \
"s_add_u32 m0, s48, 16384\n" \
"v_exp_f32 v69, v69\n" \
"v_mfma_f32_16x16x32_bf16 a[12:15], a[140:143], v[152:155], a[12:15]\n" \
"buffer_load_dwordx4 v6, s[40:43], s17 offen offset:0 sc1 lds\n" \
"v_mfma_f32_16x16x32_bf16 a[40:43], a[136:139], v[156:159], a[40:43]\n" \
"ds_read_b128 a[132:135], v5 offset:1024\n" \
"v_add_u32 v191, s92, v191\n" \
"v_mfma_f32_16x16x32_bf16 a[44:47], a[140:143], v[156:159], a[44:47]\n" \
"s_waitcnt lgkmcnt(4)\n" \
"v_exp_f32 v70, v70\n" \
"v_mfma_f32_16x16x32_bf16 v[208:211], a[144:147], v[144:147], v[208:211]\n" \
"s_lshl_b32 s92, %2, 13\n" \
"v_exp_f32 v71, v71\n" \
"v_mfma_f32_16x16x32_bf16 v[212:215], a[148:151], v[144:147], v[212:215]\n" \
"s_add_u32 s92, s92, 131072\n" \
"v_exp_f32 v72, v72\n" \
"v_mfma_f32_16x16x32_bf16 v[240:243], a[144:147], v[148:151], v[240:243]\n" \
"ds_read_b128 a[136:139], v5 offset:2048\n" \
"v_add_u32 v186, s92, v4\n" \
"v_mfma_f32_16x16x32_bf16 v[244:247], a[148:151], v[148:151], v[244:247]\n" \
"v_exp_f32 v73, v73\n" \
"v_mfma_f32_16x16x32_bf16 a[16:19], a[144:147], v[152:155], a[16:19]\n" \
"s_add_u32 m0, s48, 16384\n" \
"v_exp_f32 v74, v74\n" \
"v_mfma_f32_16x16x32_bf16 a[20:23], a[148:151], v[152:155], a[20:23]\n" \
"buffer_load_dwordx4 v6, s[40:43], s17 offen offset:1024 sc1 lds\n" \
"v_mfma_f32_16x16x32_bf16 a[48:51], a[144:147], v[156:159], a[48:51]\n" \
"s_lshl_b32 s92, %2, 9\n" \
"ds_read_b128 a[140:143], v5 offset:3072\n" \
"v_xor_b32 v187, 0x6000, v186\n" \
"v_mfma_f32_16x16x32_bf16 a[52:55], a[148:151], v[156:159], a[52:55]\n" \
"s_waitcnt lgkmcnt(4)\n" \
"v_exp_f32 v75, v75\n" \
"v_mfma_f32_16x16x32_bf16 v[216:219], a[152:155], v[144:147], v[216:219]\n" \
"s_add_u32 m0, s48, 16384\n" \
"v_exp_f32 v76, v76\n" \
"v_mfma_f32_16x16x32_bf16 v[220:223], a[156:159], v[144:147], v[220:223]\n" \
"s_add_u32 s44, s44, 1\n" \
"buffer_load_dwordx4 v6, s[40:43], s17 offen offset:2048 sc1 lds\n" \
"v_mfma_f32_16x16x32_bf16 v[248:251], a[152:155], v[148:151], v[248:251]\n" \
"s_add_u32 s92, s92, 65536\n" \
"ds_read_b128 a[144:147], v5 offset:4096\n" \
"v_lshrrev_b32 v184, 2, v4\n" \
"v_mfma_f32_16x16x32_bf16 v[252:255], a[156:159], v[148:151], v[252:255]\n" \
"v_exp_f32 v77, v77\n" \
"v_mfma_f32_16x16x32_bf16 a[24:27], a[152:155], v[152:155], a[24:27]\n" \
"s_add_u32 m0, s48, 16384\n" \
"v_exp_f32 v78, v78\n" \
"v_mfma_f32_16x16x32_bf16 a[28:31], a[156:159], v[152:155], a[28:31]\n" \
"s_xor_b32 s48, s48, 0x10000\n" \
"buffer_load_dwordx4 v6, s[40:43], s17 offen offset:3072 sc1 lds\n" \
"v_mfma_f32_16x16x32_bf16 a[56:59], a[152:155], v[156:159], a[56:59]\n" \
"ds_read_b128 a[148:151], v5 offset:5120\n" \
"v_add_u32 v184, s92, v184\n" \
"v_mfma_f32_16x16x32_bf16 a[60:63], a[156:159], v[156:159], a[60:63]\n" \
"s_waitcnt lgkmcnt(4)\n" \
"v_exp_f32 v79, v79\n" \
"v_smfmac_f32_16x16x64_bf16 v[164:167], v[180:183], v[152:159], v168\n" \
"ds_read_b64_tr_b16 a[176:177], v13 offset:16384\n" \
"v_cvt_pk_bf16_f32 v128, v64, v65\n" \
"v_cvt_pk_bf16_f32 v129, v66, v67\n" \
"v_cvt_pk_bf16_f32 v130, v68, v69\n" \
"v_cvt_pk_bf16_f32 v131, v70, v71\n" \
"v_cvt_pk_bf16_f32 v132, v72, v73\n" \
"v_cvt_pk_bf16_f32 v133, v74, v75\n" \
"v_cvt_pk_bf16_f32 v134, v76, v77\n" \
"v_cvt_pk_bf16_f32 v135, v78, v79\n" \
"v_mfma_f32_16x16x32_bf16 v[96:99], a[128:131], v[32:35], 0\n" \
"ds_read_b64_tr_b16 a[178:179], v14 offset:16384\n" \
"v_xor_b32 v185, 0x600, v184\n" \
"v_mfma_f32_16x16x32_bf16 v[96:99], a[132:135], v[36:39], v[96:99]\n" \
"s_add_u32 s16, s16, s9\n" \
"ds_read_b64_tr_b16 a[180:181], v15 offset:16384\n" \
"v_bfe_u32 v188, v4, 5, 2\n" \
"v_mfma_f32_16x16x32_bf16 v[104:107], a[128:131], v[48:51], 0\n" \
"ds_read_b128 a[152:155], v5 offset:6144\n" \
"v_and_b32 v189, 0x10, v4\n" \
"v_mfma_f32_16x16x32_bf16 v[104:107], a[132:135], v[52:55], v[104:107]\n" \
"s_add_u32 m0, s49, 32768\n" \
"ds_read_b64_tr_b16 a[182:183], v16 offset:16384\n" \
"v_add_u32 v188, v189, v188\n" \
"v_mfma_f32_16x16x32_bf16 v[112:115], a[128:131], a[64:67], 0\n" \
"buffer_load_dwordx4 v6, s[40:43], s16 offen offset:0 sc1 lds\n" \
"v_mfma_f32_16x16x32_bf16 v[112:115], a[132:135], a[68:71], v[112:115]\n" \
"ds_read_b64_tr_b16 a[184:185], v13 offset:24576\n" \
"v_lshrrev_b32 v189, 8, v4\n" \
"v_mfma_f32_16x16x32_bf16 v[120:123], a[128:131], a[80:83], 0\n" \
"ds_read_b128 a[156:159], v5 offset:7168\n" \
"v_lshl_add_u32 v188, v189, 10, v188\n" \
"v_mfma_f32_16x16x32_bf16 v[120:123], a[132:135], a[84:87], v[120:123]\n" \
"s_waitcnt lgkmcnt(4)\n" \
"v_and_b32 v189, 0x80, v4\n" \
"v_lshl_add_u32 v188, v188, 4, v189\n" \
"s_add_u32 m0, s79, 131072\n" \
"v_mfma_f32_16x16x32_bf16 v[96:99], a[136:139], v[40:43], v[96:99]\n" \
"buffer_load_dwordx4 v12, s[28:31], s81 offen offset:0 lds\n" \
"v_mfma_f32_16x16x32_bf16 v[96:99], a[140:143], v[44:47], v[96:99]\n" \
"v_add_u32 v189, 0x10000, v188\n" \
"v_mfma_f32_16x16x32_bf16 v[104:107], a[136:139], v[56:59], v[104:107]\n" \
"ds_read_b128 a[128:131], v5 offset:16384\n" \
"v_mfma_f32_16x16x32_bf16 v[104:107], a[140:143], v[60:63], v[104:107]\n" \
"s_add_u32 m0, s49, 32768\n" \
"v_exp_f32 v80, v80\n" \
"v_mfma_f32_16x16x32_bf16 v[112:115], a[136:139], a[72:75], v[112:115]\n" \
"buffer_load_dwordx4 v6, s[40:43], s16 offen offset:1024 sc1 lds\n" \
"v_mfma_f32_16x16x32_bf16 v[112:115], a[140:143], a[76:79], v[112:115]\n" \
"v_exp_f32 v81, v81\n" \
"v_mfma_f32_16x16x32_bf16 v[120:123], a[136:139], a[88:91], v[120:123]\n" \
"ds_read_b128 a[132:135], v5 offset:17408\n" \
"v_cvt_pk_bf16_f32 v136, v80, v81\n" \
"v_mfma_f32_16x16x32_bf16 v[120:123], a[140:143], a[92:95], v[120:123]\n" \
"s_waitcnt lgkmcnt(4)\n" \
"v_exp_f32 v82, v82\n" \
"v_mfma_f32_16x16x32_bf16 v[100:103], a[144:147], v[32:35], 0\n" \
"v_exp_f32 v83, v83\n" \
"v_mfma_f32_16x16x32_bf16 v[100:103], a[148:151], v[36:39], v[100:103]\n" \
"v_exp_f32 v84, v84\n" \
"v_mfma_f32_16x16x32_bf16 v[108:111], a[144:147], v[48:51], 0\n" \
"ds_read_b128 a[136:139], v5 offset:18432\n" \
"v_cvt_pk_bf16_f32 v137, v82, v83\n" \
"v_mfma_f32_16x16x32_bf16 v[108:111], a[148:151], v[52:55], v[108:111]\n" \
"s_add_u32 m0, s49, 32768\n" \
"v_exp_f32 v85, v85\n" \
"v_mfma_f32_16x16x32_bf16 v[116:119], a[144:147], a[64:67], 0\n" \
"buffer_load_dwordx4 v6, s[40:43], s16 offen offset:2048 sc1 lds\n" \
"v_mfma_f32_16x16x32_bf16 v[116:119], a[148:151], a[68:71], v[116:119]\n" \
"v_exp_f32 v86, v86\n" \
"v_mfma_f32_16x16x32_bf16 v[124:127], a[144:147], a[80:83], 0\n" \
"ds_read_b128 a[140:143], v5 offset:19456\n" \
"v_cvt_pk_bf16_f32 v138, v84, v85\n" \
"v_mfma_f32_16x16x32_bf16 v[124:127], a[148:151], a[84:87], v[124:127]\n" \
"s_waitcnt lgkmcnt(4)\n" \
"v_exp_f32 v87, v87\n" \
"v_mfma_f32_16x16x32_bf16 v[100:103], a[152:155], v[40:43], v[100:103]\n" \
"v_exp_f32 v88, v88\n" \
"v_mfma_f32_16x16x32_bf16 v[100:103], a[156:159], v[44:47], v[100:103]\n" \
"v_exp_f32 v89, v89\n" \
"v_mfma_f32_16x16x32_bf16 v[108:111], a[152:155], v[56:59], v[108:111]\n" \
"ds_read_b128 a[144:147], v5 offset:20480\n" \
"v_cvt_pk_bf16_f32 v139, v86, v87\n" \
"v_mfma_f32_16x16x32_bf16 v[108:111], a[156:159], v[60:63], v[108:111]\n" \
"s_add_u32 m0, s49, 32768\n" \
"v_exp_f32 v90, v90\n" \
"v_mfma_f32_16x16x32_bf16 v[116:119], a[152:155], a[72:75], v[116:119]\n" \
"buffer_load_dwordx4 v6, s[40:43], s16 offen offset:3072 sc1 lds\n" \
"v_mfma_f32_16x16x32_bf16 v[116:119], a[156:159], a[76:79], v[116:119]\n" \
"v_exp_f32 v91, v91\n" \
"v_mfma_f32_16x16x32_bf16 v[124:127], a[152:155], a[88:91], v[124:127]\n" \
"ds_read_b128 a[148:151], v5 offset:21504\n" \
"v_cvt_pk_bf16_f32 v140, v88, v89\n" \
"v_mfma_f32_16x16x32_bf16 v[124:127], a[156:159], a[92:95], v[124:127]\n" \
"s_waitcnt lgkmcnt(4)\n" \
"v_exp_f32 v92, v92\n" \
"s_add_u32 m0, s79, 131968\n" \
"v_smfmac_f32_16x16x64_bf16 v[160:163], v[180:183], v[128:135], v168\n" \
"buffer_load_dwordx4 v12, s[28:31], s81 offen offset:128 lds\n" \
"v_mfma_f32_16x16x32_bf16 v[192:195], a[128:131], v[128:131], v[192:195]\n" \
"s_mov_b32 s81, 327680\n" \
"v_exp_f32 v93, v93\n" \
"v_mfma_f32_16x16x32_bf16 v[196:199], a[132:135], v[128:131], v[196:199]\n" \
"ds_read_b64_tr_b16 a[186:187], v14 offset:24576\n" \
"ds_read_b64_tr_b16 a[188:189], v15 offset:24576\n" \
"ds_read_b64_tr_b16 a[190:191], v16 offset:24576\n" \
"v_mfma_f32_16x16x32_bf16 v[224:227], a[128:131], v[132:135], v[224:227]\n" \
"ds_read_b128 a[152:155], v5 offset:22528\n" \
"v_exp_f32 v94, v94\n" \
"v_mfma_f32_16x16x32_bf16 v[228:231], a[132:135], v[132:135], v[228:231]\n" \
"s_add_u32 m0, s79, 139264\n" \
"v_exp_f32 v95, v95\n" \
"v_mfma_f32_16x16x32_bf16 a[0:3], a[128:131], v[136:139], a[0:3]\n" \
"buffer_load_dwordx4 v12, s[28:31], s81 offen offset:0 lds\n" \
"v_cvt_pk_bf16_f32 v141, v90, v91\n" \
"v_cvt_pk_bf16_f32 v142, v92, v93\n" \
"v_cvt_pk_bf16_f32 v143, v94, v95\n" \
"v_mfma_f32_16x16x32_bf16 a[4:7], a[132:135], v[136:139], a[4:7]\n" \
"s_waitcnt lgkmcnt(1)\n" \
"v_exp_f32 v96, v96\n" \
"v_mfma_f32_16x16x32_bf16 a[32:35], a[128:131], v[140:143], a[32:35]\n" \
"ds_read_b128 a[156:159], v5 offset:23552\n" \
"v_mfma_f32_16x16x32_bf16 a[36:39], a[132:135], v[140:143], a[36:39]\n" \
"s_waitcnt vmcnt(18) lgkmcnt(4)\n" \
"v_exp_f32 v97, v97\n" \
"v_mfma_f32_16x16x32_bf16 v[200:203], a[136:139], v[128:131], v[200:203]\n" \
"s_barrier\n" \
"v_exp_f32 v98, v98\n" \
"v_mfma_f32_16x16x32_bf16 v[204:207], a[140:143], v[128:131], v[204:207]\n" \
"v_exp_f32 v99, v99\n" \
"v_mfma_f32_16x16x32_bf16 v[232:235], a[136:139], v[132:135], v[232:235]\n" \
"s_add_u32 s17, s17, s19\n" \
"ds_read_b128 a[128:131], v5 offset:32768\n" \
"v_cvt_pk_bf16_f32 v144, v96, v97\n" \
"v_mfma_f32_16x16x32_bf16 v[236:239], a[140:143], v[132:135], v[236:239]\n" \
"v_exp_f32 v100, v100\n" \
"v_mfma_f32_16x16x32_bf16 a[8:11], a[136:139], v[136:139], a[8:11]\n" \
"s_add_u32 m0, s49, 49152\n" \
"v_exp_f32 v101, v101\n" \
"v_mfma_f32_16x16x32_bf16 a[12:15], a[140:143], v[136:139], a[12:15]\n" \
"buffer_load_dwordx4 v6, s[40:43], s17 offen offset:0 sc1 lds\n" \
"v_mfma_f32_16x16x32_bf16 a[40:43], a[136:139], v[140:143], a[40:43]\n" \
"ds_read_b128 a[132:135], v5 offset:33792\n" \
"v_cvt_pk_bf16_f32 v145, v98, v99\n" \
"v_mfma_f32_16x16x32_bf16 a[44:47], a[140:143], v[140:143], a[44:47]\n" \
"s_waitcnt lgkmcnt(4)\n" \
"v_exp_f32 v102, v102\n" \
"v_mfma_f32_16x16x32_bf16 v[208:211], a[144:147], v[128:131], v[208:211]\n" \
"v_exp_f32 v103, v103\n" \
"v_mfma_f32_16x16x32_bf16 v[212:215], a[148:151], v[128:131], v[212:215]\n" \
"v_exp_f32 v104, v104\n" \
"v_mfma_f32_16x16x32_bf16 v[240:243], a[144:147], v[132:135], v[240:243]\n" \
"ds_read_b128 a[136:139], v5 offset:34816\n" \
"v_cvt_pk_bf16_f32 v146, v100, v101\n" \
"v_mfma_f32_16x16x32_bf16 v[244:247], a[148:151], v[132:135], v[244:247]\n" \
"v_exp_f32 v105, v105\n" \
"v_mfma_f32_16x16x32_bf16 a[16:19], a[144:147], v[136:139], a[16:19]\n" \
"s_add_u32 m0, s49, 49152\n" \
"v_exp_f32 v106, v106\n" \
"v_mfma_f32_16x16x32_bf16 a[20:23], a[148:151], v[136:139], a[20:23]\n" \
"buffer_load_dwordx4 v6, s[40:43], s17 offen offset:1024 sc1 lds\n" \
"v_mfma_f32_16x16x32_bf16 a[48:51], a[144:147], v[140:143], a[48:51]\n" \
"ds_read_b128 a[140:143], v5 offset:35840\n" \
"v_cvt_pk_bf16_f32 v147, v102, v103\n" \
"v_mfma_f32_16x16x32_bf16 a[52:55], a[148:151], v[140:143], a[52:55]\n" \
"s_waitcnt lgkmcnt(4)\n" \
"v_exp_f32 v107, v107\n" \
"v_mfma_f32_16x16x32_bf16 v[216:219], a[152:155], v[128:131], v[216:219]\n" \
"s_add_u32 m0, s49, 49152\n" \
"v_exp_f32 v108, v108\n" \
"v_mfma_f32_16x16x32_bf16 v[220:223], a[156:159], v[128:131], v[220:223]\n" \
"buffer_load_dwordx4 v6, s[40:43], s17 offen offset:2048 sc1 lds\n" \
"v_mfma_f32_16x16x32_bf16 v[248:251], a[152:155], v[132:135], v[248:251]\n" \
"ds_read_b128 a[144:147], v5 offset:36864\n" \
"v_cvt_pk_bf16_f32 v148, v104, v105\n" \
"v_mfma_f32_16x16x32_bf16 v[252:255], a[156:159], v[132:135], v[252:255]\n" \
"v_exp_f32 v109, v109\n" \
"v_mfma_f32_16x16x32_bf16 a[24:27], a[152:155], v[136:139], a[24:27]\n" \
"s_add_u32 m0, s49, 49152\n" \
"v_exp_f32 v110, v110\n" \
"v_mfma_f32_16x16x32_bf16 a[28:31], a[156:159], v[136:139], a[28:31]\n" \
"buffer_load_dwordx4 v6, s[40:43], s17 offen offset:3072 sc1 lds\n" \
"v_mfma_f32_16x16x32_bf16 a[56:59], a[152:155], v[140:143], a[56:59]\n" \
"ds_read_b128 a[148:151], v5 offset:37888\n" \
"v_cvt_pk_bf16_f32 v149, v106, v107\n" \
"v_mfma_f32_16x16x32_bf16 a[60:63], a[156:159], v[140:143], a[60:63]\n" \
"s_waitcnt lgkmcnt(4)\n" \
"v_exp_f32 v111, v111\n" \
"v_smfmac_f32_16x16x64_bf16 v[164:167], v[180:183], v[136:143], v168\n" \
"v_cvt_pk_bf16_f32 v150, v108, v109\n" \
"v_cvt_pk_bf16_f32 v151, v110, v111\n" \
"v_mfma_f32_16x16x32_bf16 v[64:67], a[128:131], v[32:35], 0\n" \
"buffer_load_dwordx2 v[18:19], v17, s[36:39], 0 offen sc1\n" \
"v_mfma_f32_16x16x32_bf16 v[64:67], a[132:135], v[36:39], v[64:67]\n" \
"s_add_u32 s16, s16, s9\n" \
"v_exp_f32 v112, v112\n" \
"v_mfma_f32_16x16x32_bf16 v[72:75], a[128:131], v[48:51], 0\n" \
"ds_read_b128 a[152:155], v5 offset:38912\n" \
"v_mfma_f32_16x16x32_bf16 v[72:75], a[132:135], v[52:55], v[72:75]\n" \
"s_add_u32 m0, s48, 0\n" \
"v_exp_f32 v113, v113\n" \
"v_mfma_f32_16x16x32_bf16 v[80:83], a[128:131], a[64:67], 0\n" \
"buffer_load_dwordx4 v6, s[40:43], s16 offen offset:0 sc1 lds\n" \
"v_mfma_f32_16x16x32_bf16 v[80:83], a[132:135], a[68:71], v[80:83]\n" \
"v_exp_f32 v114, v114\n" \
"v_mfma_f32_16x16x32_bf16 v[88:91], a[128:131], a[80:83], 0\n" \
"ds_read_b128 a[156:159], v5 offset:39936\n" \
"v_cvt_pk_bf16_f32 v152, v112, v113\n" \
"v_mfma_f32_16x16x32_bf16 v[88:91], a[132:135], a[84:87], v[88:91]\n" \
"s_waitcnt lgkmcnt(4)\n" \
"v_exp_f32 v115, v115\n" \
"s_add_u32 m0, s79, 140160\n" \
"v_mfma_f32_16x16x32_bf16 v[64:67], a[136:139], v[40:43], v[64:67]\n" \
"buffer_load_dwordx4 v12, s[28:31], s81 offen offset:128 lds\n" \
"v_mfma_f32_16x16x32_bf16 v[64:67], a[140:143], v[44:47], v[64:67]\n" \
"s_xor_b32 s49, s49, 0x10000\n" \
"s_mov_b32 s81, 393216\n" \
"v_cvt_pk_bf16_f32 v153, v114, v115\n" \
"v_mfma_f32_16x16x32_bf16 v[72:75], a[136:139], v[56:59], v[72:75]\n" \
"ds_read_b128 a[128:131], v5 offset:49152\n" \
"v_mfma_f32_16x16x32_bf16 v[72:75], a[140:143], v[60:63], v[72:75]\n" \
"s_add_u32 m0, s48, 0\n" \
"v_exp_f32 v116, v116\n" \
"v_mfma_f32_16x16x32_bf16 v[80:83], a[136:139], a[72:75], v[80:83]\n" \
"buffer_load_dwordx4 v6, s[40:43], s16 offen offset:1024 sc1 lds\n" \
"v_mfma_f32_16x16x32_bf16 v[80:83], a[140:143], a[76:79], v[80:83]\n" \
"v_exp_f32 v117, v117\n" \
"v_mfma_f32_16x16x32_bf16 v[88:91], a[136:139], a[88:91], v[88:91]\n" \
"ds_read_b128 a[132:135], v5 offset:50176\n" \
"v_cvt_pk_bf16_f32 v154, v116, v117\n" \
"v_mfma_f32_16x16x32_bf16 v[88:91], a[140:143], a[92:95], v[88:91]\n" \
"s_waitcnt lgkmcnt(4)\n" \
"v_exp_f32 v118, v118\n" \
"s_add_u32 m0, s79, 147456\n" \
"v_mfma_f32_16x16x32_bf16 v[68:71], a[144:147], v[32:35], 0\n" \
"buffer_load_dwordx4 v12, s[28:31], s81 offen offset:0 lds\n" \
"v_mfma_f32_16x16x32_bf16 v[68:71], a[148:151], v[36:39], v[68:71]\n" \
"v_exp_f32 v119, v119\n" \
"v_mfma_f32_16x16x32_bf16 v[76:79], a[144:147], v[48:51], 0\n" \
"ds_read_b128 a[136:139], v5 offset:51200\n" \
"v_cvt_pk_bf16_f32 v155, v118, v119\n" \
"v_mfma_f32_16x16x32_bf16 v[76:79], a[148:151], v[52:55], v[76:79]\n" \
"s_add_u32 m0, s48, 0\n" \
"v_exp_f32 v120, v120\n" \
"v_mfma_f32_16x16x32_bf16 v[84:87], a[144:147], a[64:67], 0\n" \
"buffer_load_dwordx4 v6, s[40:43], s16 offen offset:2048 sc1 lds\n" \
"v_mfma_f32_16x16x32_bf16 v[84:87], a[148:151], a[68:71], v[84:87]\n" \
"v_exp_f32 v121, v121\n" \
"v_mfma_f32_16x16x32_bf16 v[92:95], a[144:147], a[80:83], 0\n" \
"ds_read_b128 a[140:143], v5 offset:52224\n" \
"v_cvt_pk_bf16_f32 v156, v120, v121\n" \
"v_mfma_f32_16x16x32_bf16 v[92:95], a[148:151], a[84:87], v[92:95]\n" \
"s_waitcnt lgkmcnt(4)\n" \
"v_exp_f32 v122, v122\n" \
"s_add_u32 m0, s79, 148352\n" \
"v_mfma_f32_16x16x32_bf16 v[68:71], a[152:155], v[40:43], v[68:71]\n" \
"buffer_load_dwordx4 v12, s[28:31], s81 offen offset:128 lds\n" \
"v_mfma_f32_16x16x32_bf16 v[68:71], a[156:159], v[44:47], v[68:71]\n" \
"s_mov_b32 s81, 458752\n" \
"v_exp_f32 v123, v123\n" \
"v_mfma_f32_16x16x32_bf16 v[76:79], a[152:155], v[56:59], v[76:79]\n" \
"ds_read_b128 a[144:147], v5 offset:53248\n" \
"v_cvt_pk_bf16_f32 v157, v122, v123\n" \
"v_mfma_f32_16x16x32_bf16 v[76:79], a[156:159], v[60:63], v[76:79]\n" \
"s_add_u32 m0, s48, 0\n" \
"v_exp_f32 v124, v124\n" \
"v_mfma_f32_16x16x32_bf16 v[84:87], a[152:155], a[72:75], v[84:87]\n" \
"buffer_load_dwordx4 v6, s[40:43], s16 offen offset:3072 sc1 lds\n" \
"v_mfma_f32_16x16x32_bf16 v[84:87], a[156:159], a[76:79], v[84:87]\n" \
"v_exp_f32 v125, v125\n" \
"v_mfma_f32_16x16x32_bf16 v[92:95], a[152:155], a[88:91], v[92:95]\n" \
"ds_read_b128 a[148:151], v5 offset:54272\n" \
"v_cvt_pk_bf16_f32 v158, v124, v125\n" \
"v_mfma_f32_16x16x32_bf16 v[92:95], a[156:159], a[92:95], v[92:95]\n" \
"s_waitcnt lgkmcnt(4)\n" \
"v_exp_f32 v126, v126\n" \
"s_add_u32 m0, s79, 155648\n" \
"v_smfmac_f32_16x16x64_bf16 v[160:163], v[180:183], v[144:151], v168\n" \
"buffer_load_dwordx4 v12, s[28:31], s81 offen offset:0 lds\n" \
"v_mfma_f32_16x16x32_bf16 v[192:195], a[128:131], v[144:147], v[192:195]\n" \
"v_exp_f32 v127, v127\n" \
"v_mfma_f32_16x16x32_bf16 v[196:199], a[132:135], v[144:147], v[196:199]\n" \
"buffer_store_dwordx4 v[28:31], v4, s[36:39], s80 offen offset:1024 sc1\n" \
"v_mfma_f32_16x16x32_bf16 v[224:227], a[128:131], v[148:151], v[224:227]\n" \
"s_add_u32 m0, s79, 156544\n" \
"ds_read_b128 a[152:155], v5 offset:55296\n" \
"v_cvt_pk_bf16_f32 v159, v126, v127\n" \
"v_mfma_f32_16x16x32_bf16 v[228:231], a[132:135], v[148:151], v[228:231]\n" \
"buffer_load_dwordx4 v12, s[28:31], s81 offen offset:128 lds\n" \
"v_mfma_f32_16x16x32_bf16 a[0:3], a[128:131], v[152:155], a[0:3]\n" \
"s_mov_b32 s81, 524288\n" \
"v_exp_f32 v64, v64\n" \
"v_mfma_f32_16x16x32_bf16 a[4:7], a[132:135], v[152:155], a[4:7]\n" \
"v_exp_f32 v65, v65\n" \
"v_mfma_f32_16x16x32_bf16 a[32:35], a[128:131], v[156:159], a[32:35]\n" \
"ds_read_b128 a[156:159], v5 offset:56320\n" \
"v_cvt_pk_bf16_f32 v128, v64, v65\n" \
"v_mfma_f32_16x16x32_bf16 a[36:39], a[132:135], v[156:159], a[36:39]\n" \
"s_waitcnt vmcnt(8) lgkmcnt(4)\n" \
"v_exp_f32 v66, v66\n" \
"v_mfma_f32_16x16x32_bf16 v[200:203], a[136:139], v[144:147], v[200:203]\n" \
"s_barrier\n" \
"v_xor_b32 v5, 0x10000, v5\n" \
"v_mfma_f32_16x16x32_bf16 v[204:207], a[140:143], v[144:147], v[204:207]\n" \
"v_exp_f32 v67, v67\n" \
"v_mfma_f32_16x16x32_bf16 v[232:235], a[136:139], v[148:151], v[232:235]\n" \
"s_add_u32 s17, s17, s19\n" \
"ds_read_b128 a[128:131], v5 offset:0\n" \
"v_cvt_pk_bf16_f32 v129, v66, v67\n" \
"v_mfma_f32_16x16x32_bf16 v[236:239], a[140:143], v[148:151], v[236:239]\n" \
"LH18_%=:\ns_cbranch_execz LH22_%=\n" \
"v_exp_f32 v68, v68\n" \
"v_mfma_f32_16x16x32_bf16 a[8:11], a[136:139], v[152:155], a[8:11]\n" \
"s_add_u32 m0, s48, 16384\n" \
"v_exp_f32 v69, v69\n" \
"v_mfma_f32_16x16x32_bf16 a[12:15], a[140:143], v[152:155], a[12:15]\n" \
"buffer_load_dwordx4 v6, s[40:43], s17 offen offset:0 sc1 lds\n" \
"v_mfma_f32_16x16x32_bf16 a[40:43], a[136:139], v[156:159], a[40:43]\n" \
"ds_read_b128 a[132:135], v5 offset:1024\n" \
"v_cvt_pk_bf16_f32 v130, v68, v69\n" \
"v_mfma_f32_16x16x32_bf16 a[44:47], a[140:143], v[156:159], a[44:47]\n" \
"s_waitcnt lgkmcnt(4)\n" \
"v_exp_f32 v70, v70\n" \
"v_mfma_f32_16x16x32_bf16 v[208:211], a[144:147], v[144:147], v[208:211]\n" \
"v_exp_f32 v71, v71\n" \
"v_mfma_f32_16x16x32_bf16 v[212:215], a[148:151], v[144:147], v[212:215]\n" \
"v_exp_f32 v72, v72\n" \
"v_mfma_f32_16x16x32_bf16 v[240:243], a[144:147], v[148:151], v[240:243]\n" \
"ds_read_b128 a[136:139], v5 offset:2048\n" \
"v_cvt_pk_bf16_f32 v131, v70, v71\n" \
"v_mfma_f32_16x16x32_bf16 v[244:247], a[148:151], v[148:151], v[244:247]\n" \
"v_exp_f32 v73, v73\n" \
"v_mfma_f32_16x16x32_bf16 a[16:19], a[144:147], v[152:155], a[16:19]\n" \
"s_add_u32 m0, s48, 16384\n" \
"v_exp_f32 v74, v74\n" \
"v_mfma_f32_16x16x32_bf16 a[20:23], a[148:151], v[152:155], a[20:23]\n" \
"buffer_load_dwordx4 v6, s[40:43], s17 offen offset:1024 sc1 lds\n" \
"v_mfma_f32_16x16x32_bf16 a[48:51], a[144:147], v[156:159], a[48:51]\n" \
"ds_read_b128 a[140:143], v5 offset:3072\n" \
"v_cvt_pk_bf16_f32 v132, v72, v73\n" \
"v_mfma_f32_16x16x32_bf16 a[52:55], a[148:151], v[156:159], a[52:55]\n" \
"s_waitcnt lgkmcnt(4)\n" \
"v_exp_f32 v75, v75\n" \
"v_mfma_f32_16x16x32_bf16 v[216:219], a[152:155], v[144:147], v[216:219]\n" \
"s_add_u32 m0, s48, 16384\n" \
"v_exp_f32 v76, v76\n" \
"v_mfma_f32_16x16x32_bf16 v[220:223], a[156:159], v[144:147], v[220:223]\n" \
"s_add_u32 s44, s44, 1\n" \
"buffer_load_dwordx4 v6, s[40:43], s17 offen offset:2048 sc1 lds\n" \
"v_mfma_f32_16x16x32_bf16 v[248:251], a[152:155], v[148:151], v[248:251]\n" \
"ds_read_b128 a[144:147], v5 offset:4096\n" \
"v_cvt_pk_bf16_f32 v133, v74, v75\n" \
"v_mfma_f32_16x16x32_bf16 v[252:255], a[156:159], v[148:151], v[252:255]\n" \
"v_exp_f32 v77, v77\n" \
"v_mfma_f32_16x16x32_bf16 a[24:27], a[152:155], v[152:155], a[24:27]\n" \
"s_add_u32 m0, s48, 16384\n" \
"v_exp_f32 v78, v78\n" \
"v_mfma_f32_16x16x32_bf16 a[28:31], a[156:159], v[152:155], a[28:31]\n" \
"s_xor_b32 s48, s48, 0x10000\n" \
"buffer_load_dwordx4 v6, s[40:43], s17 offen offset:3072 sc1 lds\n" \
"v_mfma_f32_16x16x32_bf16 a[56:59], a[152:155], v[156:159], a[56:59]\n" \
"ds_read_b128 a[148:151], v5 offset:5120\n" \
"v_cvt_pk_bf16_f32 v134, v76, v77\n" \
"v_mfma_f32_16x16x32_bf16 a[60:63], a[156:159], v[156:159], a[60:63]\n" \
"s_waitcnt lgkmcnt(4)\n" \
"v_exp_f32 v79, v79\n" \
"v_smfmac_f32_16x16x64_bf16 v[164:167], v[180:183], v[152:159], v168\n" \
"buffer_store_dwordx4 v[28:31], v4, s[36:39], s80 offen offset:2048 sc1\n" \
"v_cvt_pk_bf16_f32 v135, v78, v79\n" \
"v_mfma_f32_16x16x32_bf16 v[96:99], a[128:131], v[32:35], 0\n" \
"s_add_u32 s100, s100, 262144\ns_addc_u32 s101, s101, 0\n" \
"ds_read_b64_tr_b16 a[192:193], v13 offset:0\n" \
"v_mfma_f32_16x16x32_bf16 v[96:99], a[132:135], v[36:39], v[96:99]\n" \
"s_add_u32 s16, s16, s9\n" \
"ds_read_b64_tr_b16 a[194:195], v14 offset:0\n" \
"v_mfma_f32_16x16x32_bf16 v[104:107], a[128:131], v[48:51], 0\n" \
"ds_read_b128 a[152:155], v5 offset:6144\n" \
"v_mfma_f32_16x16x32_bf16 v[104:107], a[132:135], v[52:55], v[104:107]\n" \
"s_add_u32 m0, s49, 32768\n" \
"ds_read_b64_tr_b16 a[196:197], v15 offset:0\n" \
"v_mfma_f32_16x16x32_bf16 v[112:115], a[128:131], a[64:67], 0\n" \
"buffer_load_dwordx4 v6, s[40:43], s16 offen offset:0 sc1 lds\n" \
"v_mfma_f32_16x16x32_bf16 v[112:115], a[132:135], a[68:71], v[112:115]\n" \
"ds_read_b64_tr_b16 a[198:199], v16 offset:0\n" \
"v_mfma_f32_16x16x32_bf16 v[120:123], a[128:131], a[80:83], 0\n" \
"ds_read_b128 a[156:159], v5 offset:7168\n" \
"v_mfma_f32_16x16x32_bf16 v[120:123], a[132:135], a[84:87], v[120:123]\n" \
"s_waitcnt lgkmcnt(4)\n" \
"ds_read_b64_tr_b16 a[200:201], v13 offset:8192\n" \
"v_mfma_f32_16x16x32_bf16 v[96:99], a[136:139], v[40:43], v[96:99]\n" \
"s_mov_b64 exec, 0xffff\nglobal_load_dword v1, v2, s[100:101]\ns_mov_b64 exec, -1\n" \
"v_mfma_f32_16x16x32_bf16 v[96:99], a[140:143], v[44:47], v[96:99]\n" \
"s_add_u32 s100, s100, 262144\ns_addc_u32 s101, s101, 0\n" \
"v_mfma_f32_16x16x32_bf16 v[104:107], a[136:139], v[56:59], v[104:107]\n" \
"ds_read_b128 a[128:131], v5 offset:16384\n" \
"v_mfma_f32_16x16x32_bf16 v[104:107], a[140:143], v[60:63], v[104:107]\n" \
"s_add_u32 m0, s49, 32768\n" \
"v_exp_f32 v80, v80\n" \
"v_mfma_f32_16x16x32_bf16 v[112:115], a[136:139], a[72:75], v[112:115]\n" \
"buffer_load_dwordx4 v6, s[40:43], s16 offen offset:1024 sc1 lds\n" \
"v_mfma_f32_16x16x32_bf16 v[112:115], a[140:143], a[76:79], v[112:115]\n" \
"v_exp_f32 v81, v81\n" \
"v_mfma_f32_16x16x32_bf16 v[120:123], a[136:139], a[88:91], v[120:123]\n" \
"ds_read_b128 a[132:135], v5 offset:17408\n" \
"v_cvt_pk_bf16_f32 v136, v80, v81\n" \
"v_mfma_f32_16x16x32_bf16 v[120:123], a[140:143], a[92:95], v[120:123]\n" \
"s_waitcnt lgkmcnt(4)\n" \
"v_exp_f32 v82, v82\n" \
"v_mfma_f32_16x16x32_bf16 v[100:103], a[144:147], v[32:35], 0\n" \
"v_exp_f32 v83, v83\n" \
"v_mfma_f32_16x16x32_bf16 v[100:103], a[148:151], v[36:39], v[100:103]\n" \
"v_exp_f32 v84, v84\n" \
"v_mfma_f32_16x16x32_bf16 v[108:111], a[144:147], v[48:51], 0\n" \
"ds_read_b128 a[136:139], v5 offset:18432\n" \
"v_cvt_pk_bf16_f32 v137, v82, v83\n" \
"v_mfma_f32_16x16x32_bf16 v[108:111], a[148:151], v[52:55], v[108:111]\n" \
"s_add_u32 m0, s49, 32768\n" \
"v_exp_f32 v85, v85\n" \
"v_mfma_f32_16x16x32_bf16 v[116:119], a[144:147], a[64:67], 0\n" \
"buffer_load_dwordx4 v6, s[40:43], s16 offen offset:2048 sc1 lds\n" \
"v_mfma_f32_16x16x32_bf16 v[116:119], a[148:151], a[68:71], v[116:119]\n" \
"v_exp_f32 v86, v86\n" \
"v_mfma_f32_16x16x32_bf16 v[124:127], a[144:147], a[80:83], 0\n" \
"ds_read_b128 a[140:143], v5 offset:19456\n" \
"v_cvt_pk_bf16_f32 v138, v84, v85\n" \
"v_mfma_f32_16x16x32_bf16 v[124:127], a[148:151], a[84:87], v[124:127]\n" \
"s_waitcnt lgkmcnt(4)\n" \
"v_exp_f32 v87, v87\n" \
"v_mfma_f32_16x16x32_bf16 v[100:103], a[152:155], v[40:43], v[100:103]\n" \
"v_exp_f32 v88, v88\n" \
"v_mfma_f32_16x16x32_bf16 v[100:103], a[156:159], v[44:47], v[100:103]\n" \
"v_exp_f32 v89, v89\n" \
"v_mfma_f32_16x16x32_bf16 v[108:111], a[152:155], v[56:59], v[108:111]\n" \
"ds_read_b128 a[144:147], v5 offset:20480\n" \
"v_cvt_pk_bf16_f32 v139, v86, v87\n" \
"v_mfma_f32_16x16x32_bf16 v[108:111], a[156:159], v[60:63], v[108:111]\n" \
"s_add_u32 m0, s49, 32768\n" \
"v_exp_f32 v90, v90\n" \
"v_mfma_f32_16x16x32_bf16 v[116:119], a[152:155], a[72:75], v[116:119]\n" \
"buffer_load_dwordx4 v6, s[40:43], s16 offen offset:3072 sc1 lds\n" \
"v_mfma_f32_16x16x32_bf16 v[116:119], a[156:159], a[76:79], v[116:119]\n" \
"s_waitcnt vmcnt(20)\n" \
"v_exp_f32 v91, v91\n" \
"v_mfma_f32_16x16x32_bf16 v[124:127], a[152:155], a[88:91], v[124:127]\n" \
"ds_read_b128 a[148:151], v5 offset:21504\n" \
"v_cmp_ne_u64 vcc, s[14:15], v[18:19]\n" \
"v_mfma_f32_16x16x32_bf16 v[124:127], a[156:159], a[92:95], v[124:127]\n" \
"s_waitcnt lgkmcnt(4)\n" \
"v_exp_f32 v92, v92\n" \
"v_smfmac_f32_16x16x64_bf16 v[160:163], v[180:183], v[128:135], v168\n" \
"s_mov_b64 exec, 0xffff\nglobal_load_dword v1, v2, s[100:101]\ns_mov_b64 exec, -1\n" \
"v_mfma_f32_16x16x32_bf16 v[192:195], a[128:131], v[128:131], v[192:195]\n" \
"v_exp_f32 v93, v93\n" \
"v_mfma_f32_16x16x32_bf16 v[196:199], a[132:135], v[128:131], v[196:199]\n" \
"ds_read_b64_tr_b16 a[202:203], v14 offset:8192\n" \
"ds_read_b64_tr_b16 a[204:205], v15 offset:8192\n" \
"ds_read_b64_tr_b16 a[206:207], v16 offset:8192\n" \
"v_mfma_f32_16x16x32_bf16 v[224:227], a[128:131], v[132:135], v[224:227]\n" \
"ds_read_b128 a[152:155], v5 offset:22528\n" \
"s_nop 0\n" \
"v_exp_f32 v94, v94\n" \
"v_mfma_f32_16x16x32_bf16 v[228:231], a[132:135], v[132:135], v[228:231]\n" \
"s_cbranch_vccnz LPZ_%=\nLPZR_%=:\n" \
"v_exp_f32 v95, v95\n" \
"v_mfma_f32_16x16x32_bf16 a[0:3], a[128:131], v[136:139], a[0:3]\n" \
"v_exp_f32 v96, v96\n" \
"v_cvt_pk_bf16_f32 v140, v88, v89\n" \
"v_cvt_pk_bf16_f32 v141, v90, v91\n" \
"v_cvt_pk_bf16_f32 v142, v92, v93\n" \
"v_cvt_pk_bf16_f32 v143, v94, v95\n" \
"v_mfma_f32_16x16x32_bf16 a[4:7], a[132:135], v[136:139], a[4:7]\n" \
"s_waitcnt lgkmcnt(1)\n" \
"v_exp_f32 v97, v97\n" \
"v_mfma_f32_16x16x32_bf16 a[32:35], a[128:131], v[140:143], a[32:35]\n" \
"ds_read_b128 a[156:159], v5 offset:23552\n" \
"v_cvt_pk_bf16_f32 v144, v96, v97\n" \
"v_mfma_f32_16x16x32_bf16 a[36:39], a[132:135], v[140:143], a[36:39]\n" \
"s_waitcnt vmcnt(11) lgkmcnt(4)\n" \
"v_exp_f32 v98, v98\n" \
"v_mfma_f32_16x16x32_bf16 v[200:203], a[136:139], v[128:131], v[200:203]\n" \
"s_barrier\n" \
"v_exp_f32 v99, v99\n" \
"s_add_u32 m0, s79, 131072\n" \
"v_mfma_f32_16x16x32_bf16 v[204:207], a[140:143], v[128:131], v[204:207]\n" \
"buffer_load_dwordx4 v12, s[28:31], s81 offen offset:0 lds\n" \
"v_mfma_f32_16x16x32_bf16 v[232:235], a[136:139], v[132:135], v[232:235]\n" \
"s_add_u32 s17, s17, s19\n" \
"ds_read_b128 a[128:131], v5 offset:32768\n" \
"v_cvt_pk_bf16_f32 v145, v98, v99\n" \
"v_mfma_f32_16x16x32_bf16 v[236:239], a[140:143], v[132:135], v[236:239]\n" \
"v_exp_f32 v100, v100\n" \
"v_mfma_f32_16x16x32_bf16 a[8:11], a[136:139], v[136:139], a[8:11]\n" \
"s_add_u32 m0, s49, 49152\n" \
"v_exp_f32 v101, v101\n" \
"v_mfma_f32_16x16x32_bf16 a[12:15], a[140:143], v[136:139], a[12:15]\n" \
"buffer_load_dwordx4 v6, s[40:43], s17 offen offset:0 sc1 lds\n" \
"v_mfma_f32_16x16x32_bf16 a[40:43], a[136:139], v[140:143], a[40:43]\n" \
"ds_read_b128 a[132:135], v5 offset:33792\n" \
"v_cvt_pk_bf16_f32 v146, v100, v101\n" \
"v_mfma_f32_16x16x32_bf16 a[44:47], a[140:143], v[140:143], a[44:47]\n" \
"s_waitcnt lgkmcnt(4)\n" \
"v_exp_f32 v102, v102\n" \
"v_mfma_f32_16x16x32_bf16 v[208:211], a[144:147], v[128:131], v[208:211]\n" \
"v_exp_f32 v103, v103\n" \
"v_mfma_f32_16x16x32_bf16 v[212:215], a[148:151], v[128:131], v[212:215]\n" \
"v_exp_f32 v104, v104\n" \
"v_mfma_f32_16x16x32_bf16 v[240:243], a[144:147], v[132:135], v[240:243]\n" \
"ds_read_b128 a[136:139], v5 offset:34816\n" \
"v_cvt_pk_bf16_f32 v147, v102, v103\n" \
"v_mfma_f32_16x16x32_bf16 v[244:247], a[148:151], v[132:135], v[244:247]\n" \
"v_exp_f32 v105, v105\n" \
"v_mfma_f32_16x16x32_bf16 a[16:19], a[144:147], v[136:139], a[16:19]\n" \
"s_add_u32 m0, s49, 49152\n" \
"v_exp_f32 v106, v106\n" \
"v_mfma_f32_16x16x32_bf16 a[20:23], a[148:151], v[136:139], a[20:23]\n" \
"buffer_load_dwordx4 v6, s[40:43], s17 offen offset:1024 sc1 lds\n" \
"v_mfma_f32_16x16x32_bf16 a[48:51], a[144:147], v[140:143], a[48:51]\n" \
"ds_read_b128 a[140:143], v5 offset:35840\n" \
"v_cvt_pk_bf16_f32 v148, v104, v105\n" \
"v_mfma_f32_16x16x32_bf16 a[52:55], a[148:151], v[140:143], a[52:55]\n" \
"s_waitcnt lgkmcnt(4)\n" \
"v_exp_f32 v107, v107\n" \
"v_mfma_f32_16x16x32_bf16 v[216:219], a[152:155], v[128:131], v[216:219]\n" \
"s_add_u32 m0, s49, 49152\n" \
"v_exp_f32 v108, v108\n" \
"v_mfma_f32_16x16x32_bf16 v[220:223], a[156:159], v[128:131], v[220:223]\n" \
"buffer_load_dwordx4 v6, s[40:43], s17 offen offset:2048 sc1 lds\n" \
"v_mfma_f32_16x16x32_bf16 v[248:251], a[152:155], v[132:135], v[248:251]\n" \
"ds_read_b128 a[144:147], v5 offset:36864\n" \
"v_cvt_pk_bf16_f32 v149, v106, v107\n" \
"v_mfma_f32_16x16x32_bf16 v[252:255], a[156:159], v[132:135], v[252:255]\n" \
"v_exp_f32 v109, v109\n" \
"v_mfma_f32_16x16x32_bf16 a[24:27], a[152:155], v[136:139], a[24:27]\n" \
"s_add_u32 m0, s49, 49152\n" \
"v_exp_f32 v110, v110\n" \
"v_mfma_f32_16x16x32_bf16 a[28:31], a[156:159], v[136:139], a[28:31]\n" \
"buffer_load_dwordx4 v6, s[40:43], s17 offen offset:3072 sc1 lds\n" \
"v_mfma_f32_16x16x32_bf16 a[56:59], a[152:155], v[140:143], a[56:59]\n" \
"ds_read_b128 a[148:151], v5 offset:37888\n" \
"v_cvt_pk_bf16_f32 v150, v108, v109\n" \
"v_mfma_f32_16x16x32_bf16 a[60:63], a[156:159], v[140:143], a[60:63]\n" \
"s_waitcnt lgkmcnt(4)\n" \
"v_exp_f32 v111, v111\n" \
"v_smfmac_f32_16x16x64_bf16 v[164:167], v[180:183], v[136:143], v168\n" \
"ds_read_b64_tr_b16 a[208:209], v13 offset:16384\n" \
"v_cvt_pk_bf16_f32 v151, v110, v111\n" \
"v_mfma_f32_16x16x32_bf16 v[64:67], a[128:131], v[32:35], 0\n" \
"ds_read_b64_tr_b16 a[210:211], v14 offset:16384\n" \
"v_mfma_f32_16x16x32_bf16 v[64:67], a[132:135], v[36:39], v[64:67]\n" \
"s_add_u32 s16, s16, s9\n" \
"ds_read_b64_tr_b16 a[212:213], v15 offset:16384\n" \
"v_mfma_f32_16x16x32_bf16 v[72:75], a[128:131], v[48:51], 0\n" \
"ds_read_b128 a[152:155], v5 offset:38912\n" \
"v_mfma_f32_16x16x32_bf16 v[72:75], a[132:135], v[52:55], v[72:75]\n" \
"s_add_u32 m0, s48, 0\n" \
"ds_read_b64_tr_b16 a[214:215], v16 offset:16384\n" \
"v_mfma_f32_16x16x32_bf16 v[80:83], a[128:131], a[64:67], 0\n" \
"buffer_load_dwordx4 v6, s[40:43], s16 offen offset:0 sc1 lds\n" \
"v_mfma_f32_16x16x32_bf16 v[80:83], a[132:135], a[68:71], v[80:83]\n" \
"ds_read_b64_tr_b16 a[216:217], v13 offset:24576\n" \
"v_mfma_f32_16x16x32_bf16 v[88:91], a[128:131], a[80:83], 0\n" \
"ds_read_b128 a[156:159], v5 offset:39936\n" \
"v_mfma_f32_16x16x32_bf16 v[88:91], a[132:135], a[84:87], v[88:91]\n" \
"s_waitcnt lgkmcnt(4)\n" \
"v_exp_f32 v112, v112\n" \
"s_add_u32 m0, s79, 131968\n" \
"v_mfma_f32_16x16x32_bf16 v[64:67], a[136:139], v[40:43], v[64:67]\n" \
"buffer_load_dwordx4 v12, s[28:31], s81 offen offset:128 lds\n" \
"v_mfma_f32_16x16x32_bf16 v[64:67], a[140:143], v[44:47], v[64:67]\n" \
"s_xor_b32 s49, s49, 0x10000\n" \
"s_mov_b32 s81, 589824\n" \
"v_mfma_f32_16x16x32_bf16 v[72:75], a[136:139], v[56:59], v[72:75]\n" \
"ds_read_b128 a[128:131], v5 offset:49152\n" \
"v_mfma_f32_16x16x32_bf16 v[72:75], a[140:143], v[60:63], v[72:75]\n" \
"s_add_u32 m0, s48, 0\n" \
"ds_read_b64_tr_b16 a[218:219], v14 offset:24576\n" \
"v_mfma_f32_16x16x32_bf16 v[80:83], a[136:139], a[72:75], v[80:83]\n" \
"buffer_load_dwordx4 v6, s[40:43], s16 offen offset:1024 sc1 lds\n" \
"v_mfma_f32_16x16x32_bf16 v[80:83], a[140:143], a[76:79], v[80:83]\n" \
"v_exp_f32 v113, v113\n" \
"v_mfma_f32_16x16x32_bf16 v[88:91], a[136:139], a[88:91], v[88:91]\n" \
"ds_read_b128 a[132:135], v5 offset:50176\n" \
"v_cvt_pk_bf16_f32 v152, v112, v113\n" \
"v_mfma_f32_16x16x32_bf16 v[88:91], a[140:143], a[92:95], v[88:91]\n" \
"s_waitcnt lgkmcnt(4)\n" \
"v_exp_f32 v114, v114\n" \
"v_mfma_f32_16x16x32_bf16 v[68:71], a[144:147], v[32:35], 0\n" \
"v_exp_f32 v115, v115\n" \
"v_mfma_f32_16x16x32_bf16 v[68:71], a[148:151], v[36:39], v[68:71]\n" \
"v_exp_f32 v116, v116\n" \
"v_mfma_f32_16x16x32_bf16 v[76:79], a[144:147], v[48:51], 0\n" \
"ds_read_b128 a[136:139], v5 offset:51200\n" \
"v_cvt_pk_bf16_f32 v153, v114, v115\n" \
"v_mfma_f32_16x16x32_bf16 v[76:79], a[148:151], v[52:55], v[76:79]\n" \
"s_add_u32 m0, s48, 0\n" \
"v_exp_f32 v117, v117\n" \
"v_mfma_f32_16x16x32_bf16 v[84:87], a[144:147], a[64:67], 0\n" \
"buffer_load_dwordx4 v6, s[40:43], s16 offen offset:2048 sc1 lds\n" \
"v_mfma_f32_16x16x32_bf16 v[84:87], a[148:151], a[68:71], v[84:87]\n" \
"v_exp_f32 v118, v118\n" \
"v_mfma_f32_16x16x32_bf16 v[92:95], a[144:147], a[80:83], 0\n" \
"ds_read_b128 a[140:143], v5 offset:52224\n" \
"v_cvt_pk_bf16_f32 v154, v116, v117\n" \
"v_mfma_f32_16x16x32_bf16 v[92:95], a[148:151], a[84:87], v[92:95]\n" \
"s_waitcnt lgkmcnt(4)\n" \
"v_exp_f32 v119, v119\n" \
"v_mfma_f32_16x16x32_bf16 v[68:71], a[152:155], v[40:43], v[68:71]\n" \
"v_exp_f32 v120, v120\n" \
"v_mfma_f32_16x16x32_bf16 v[68:71], a[156:159], v[44:47], v[68:71]\n" \
"v_exp_f32 v121, v121\n" \
"v_mfma_f32_16x16x32_bf16 v[76:79], a[152:155], v[56:59], v[76:79]\n" \
"ds_read_b128 a[144:147], v5 offset:53248\n" \
"v_cvt_pk_bf16_f32 v155, v118, v119\n" \
"v_mfma_f32_16x16x32_bf16 v[76:79], a[156:159], v[60:63], v[76:79]\n" \
"s_add_u32 m0, s48, 0\n" \
"v_exp_f32 v122, v122\n" \
"v_mfma_f32_16x16x32_bf16 v[84:87], a[152:155], a[72:75], v[84:87]\n" \
"buffer_load_dwordx4 v6, s[40:43], s16 offen offset:3072 sc1 lds\n" \
"v_mfma_f32_16x16x32_bf16 v[84:87], a[156:159], a[76:79], v[84:87]\n" \
"v_exp_f32 v123, v123\n" \
"v_mfma_f32_16x16x32_bf16 v[92:95], a[152:155], a[88:91], v[92:95]\n" \
"ds_read_b128 a[148:151], v5 offset:54272\n" \
"v_cvt_pk_bf16_f32 v156, v120, v121\n" \
"v_mfma_f32_16x16x32_bf16 v[92:95], a[156:159], a[92:95], v[92:95]\n" \
"s_waitcnt lgkmcnt(4)\n" \
"v_exp_f32 v124, v124\n" \
"s_add_u32 m0, s79, 139264\n" \
"v_smfmac_f32_16x16x64_bf16 v[160:163], v[180:183], v[144:151], v168\n" \
"buffer_load_dwordx4 v12, s[28:31], s81 offen offset:0 lds\n" \
"v_mfma_f32_16x16x32_bf16 v[192:195], a[128:131], v[144:147], v[192:195]\n" \
"v_exp_f32 v125, v125\n" \
"v_mfma_f32_16x16x32_bf16 v[196:199], a[132:135], v[144:147], v[196:199]\n" \
"ds_read_b64_tr_b16 a[220:221], v15 offset:24576\n" \
"ds_read_b64_tr_b16 a[222:223], v16 offset:24576\n" \
"v_mfma_f32_16x16x32_bf16 v[224:227], a[128:131], v[148:151], v[224:227]\n" \
"ds_read_b128 a[152:155], v5 offset:55296\n" \
"v_exp_f32 v126, v126\n" \
"v_mfma_f32_16x16x32_bf16 v[228:231], a[132:135], v[148:151], v[228:231]\n" \
"v_exp_f32 v127, v127\n" \
"v_mfma_f32_16x16x32_bf16 a[0:3], a[128:131], v[152:155], a[0:3]\n" \
"v_exp_f32 v64, v64\n" \
"v_cvt_pk_bf16_f32 v157, v122, v123\n" \
"v_cvt_pk_bf16_f32 v158, v124, v125\n" \
"v_cvt_pk_bf16_f32 v159, v126, v127\n" \
"v_mfma_f32_16x16x32_bf16 a[4:7], a[132:135], v[152:155], a[4:7]\n" \
"s_waitcnt lgkmcnt(1)\n" \
"v_exp_f32 v65, v65\n" \
"v_mfma_f32_16x16x32_bf16 a[32:35], a[128:131], v[156:159], a[32:35]\n" \
"ds_read_b128 a[156:159], v5 offset:56320\n" \
"v_cvt_pk_bf16_f32 v128, v64, v65\n" \
"v_mfma_f32_16x16x32_bf16 a[36:39], a[132:135], v[156:159], a[36:39]\n" \
"s_waitcnt vmcnt(18) lgkmcnt(4)\n" \
"v_exp_f32 v66, v66\n" \
"v_mfma_f32_16x16x32_bf16 v[200:203], a[136:139], v[144:147], v[200:203]\n" \
"s_barrier\n" \
"v_xor_b32 v5, 0x10000, v5\n" \
"v_mfma_f32_16x16x32_bf16 v[204:207], a[140:143], v[144:147], v[204:207]\n" \
"v_exp_f32 v67, v67\n" \
"v_mfma_f32_16x16x32_bf16 v[232:235], a[136:139], v[148:151], v[232:235]\n" \
"s_add_u32 s17, s17, s19\n" \
"ds_read_b128 a[128:131], v5 offset:0\n" \
"v_cvt_pk_bf16_f32 v129, v66, v67\n" \
"v_mfma_f32_16x16x32_bf16 v[236:239], a[140:143], v[148:151], v[236:239]\n" \
"v_exp_f32 v68, v68\n" \
"v_mfma_f32_16x16x32_bf16 a[8:11], a[136:139], v[152:155], a[8:11]\n" \
"s_add_u32 m0, s48, 16384\n" \
"v_exp_f32 v69, v69\n" \
"v_mfma_f32_16x16x32_bf16 a[12:15], a[140:143], v[152:155], a[12:15]\n" \
"buffer_load_dwordx4 v6, s[40:43], s17 offen offset:0 sc1 lds\n" \
"v_mfma_f32_16x16x32_bf16 a[40:43], a[136:139], v[156:159], a[40:43]\n" \
"ds_read_b128 a[132:135], v5 offset:1024\n" \
"v_cvt_pk_bf16_f32 v130, v68, v69\n" \
"v_mfma_f32_16x16x32_bf16 a[44:47], a[140:143], v[156:159], a[44:47]\n" \
"s_waitcnt lgkmcnt(4)\n" \
"v_exp_f32 v70, v70\n" \
"v_mfma_f32_16x16x32_bf16 v[208:211], a[144:147], v[144:147], v[208:211]\n" \
"v_exp_f32 v71, v71\n" \
"v_mfma_f32_16x16x32_bf16 v[212:215], a[148:151], v[144:147], v[212:215]\n" \
"v_exp_f32 v72, v72\n" \
"v_mfma_f32_16x16x32_bf16 v[240:243], a[144:147], v[148:151], v[240:243]\n" \
"ds_read_b128 a[136:139], v5 offset:2048\n" \
"v_cvt_pk_bf16_f32 v131, v70, v71\n" \
"v_mfma_f32_16x16x32_bf16 v[244:247], a[148:151], v[148:151], v[244:247]\n" \
"v_exp_f32 v73, v73\n" \
"v_mfma_f32_16x16x32_bf16 a[16:19], a[144:147], v[152:155], a[16:19]\n" \
"s_add_u32 m0, s48, 16384\n" \
"v_exp_f32 v74, v74\n" \
"v_mfma_f32_16x16x32_bf16 a[20:23], a[148:151], v[152:155], a[20:23]\n" \
"buffer_load_dwordx4 v6, s[40:43], s17 offen offset:1024 sc1 lds\n" \
"v_mfma_f32_16x16x32_bf16 a[48:51], a[144:147], v[156:159], a[48:51]\n" \
"ds_read_b128 a[140:143], v5 offset:3072\n" \
"v_cvt_pk_bf16_f32 v132, v72, v73\n" \
"v_mfma_f32_16x16x32_bf16 a[52:55], a[148:151], v[156:159], a[52:55]\n" \
"s_waitcnt lgkmcnt(4)\n" \
"v_exp_f32 v75, v75\n" \
"v_mfma_f32_16x16x32_bf16 v[216:219], a[152:155], v[144:147], v[216:219]\n" \
"s_add_u32 m0, s48, 16384\n" \
"v_exp_f32 v76, v76\n" \
"v_mfma_f32_16x16x32_bf16 v[220:223], a[156:159], v[144:147], v[220:223]\n" \
"s_add_u32 s44, s44, 1\n" \
"buffer_load_dwordx4 v6, s[40:43], s17 offen offset:2048 sc1 lds\n" \
"v_mfma_f32_16x16x32_bf16 v[248:251], a[152:155], v[148:151], v[248:251]\n" \
"ds_read_b128 a[144:147], v5 offset:4096\n" \
"v_cvt_pk_bf16_f32 v133, v74, v75\n" \
"v_mfma_f32_16x16x32_bf16 v[252:255], a[156:159], v[148:151], v[252:255]\n" \
"v_exp_f32 v77, v77\n" \
"v_mfma_f32_16x16x32_bf16 a[24:27], a[152:155], v[152:155], a[24:27]\n" \
"s_add_u32 m0, s48, 16384\n" \
"v_exp_f32 v78, v78\n" \
"v_mfma_f32_16x16x32_bf16 a[28:31], a[156:159], v[152:155], a[28:31]\n" \
"s_xor_b32 s48, s48, 0x10000\n" \
"buffer_load_dwordx4 v6, s[40:43], s17 offen offset:3072 sc1 lds\n" \
"v_mfma_f32_16x16x32_bf16 a[56:59], a[152:155], v[156:159], a[56:59]\n" \
"ds_read_b128 a[148:151], v5 offset:5120\n" \
"v_cvt_pk_bf16_f32 v134, v76, v77\n" \
"v_mfma_f32_16x16x32_bf16 a[60:63], a[156:159], v[156:159], a[60:63]\n" \
"s_waitcnt lgkmcnt(4)\n" \
"v_exp_f32 v79, v79\n" \
"v_smfmac_f32_16x16x64_bf16 v[164:167], v[180:183], v[152:159], v168\n" \
"v_cvt_pk_bf16_f32 v135, v78, v79\n" \
"v_mfma_f32_16x16x32_bf16 v[96:99], a[128:131], v[32:35], 0\n" \
"buffer_load_dwordx4 a[112:115], v188, s[36:39], s84 offen sc1\n" \
"v_mfma_f32_16x16x32_bf16 v[96:99], a[132:135], v[36:39], v[96:99]\n" \
"s_add_u32 s16, s16, s9\n" \
"v_exp_f32 v80, v80\n" \
"v_mfma_f32_16x16x32_bf16 v[104:107], a[128:131], v[48:51], 0\n" \
"ds_read_b128 a[152:155], v5 offset:6144\n" \
"v_mfma_f32_16x16x32_bf16 v[104:107], a[132:135], v[52:55], v[104:107]\n" \
"s_add_u32 m0, s49, 32768\n" \
"v_exp_f32 v81, v81\n" \
"v_mfma_f32_16x16x32_bf16 v[112:115], a[128:131], a[64:67], 0\n" \
"buffer_load_dwordx4 v6, s[40:43], s16 offen offset:0 sc1 lds\n" \
"v_mfma_f32_16x16x32_bf16 v[112:115], a[132:135], a[68:71], v[112:115]\n" \
"v_exp_f32 v82, v82\n" \
"v_mfma_f32_16x16x32_bf16 v[120:123], a[128:131], a[80:83], 0\n" \
"ds_read_b128 a[156:159], v5 offset:7168\n" \
"v_cvt_pk_bf16_f32 v136, v80, v81\n" \
"v_mfma_f32_16x16x32_bf16 v[120:123], a[132:135], a[84:87], v[120:123]\n" \
"s_waitcnt lgkmcnt(4)\n" \
"v_exp_f32 v83, v83\n" \
"s_add_u32 m0, s79, 140160\n" \
"v_mfma_f32_16x16x32_bf16 v[96:99], a[136:139], v[40:43], v[96:99]\n" \
"buffer_load_dwordx4 v12, s[28:31], s81 offen offset:128 lds\n" \
"v_mfma_f32_16x16x32_bf16 v[96:99], a[140:143], v[44:47], v[96:99]\n" \
"s_mov_b32 s81, 655360\n" \
"v_exp_f32 v84, v84\n" \
"v_mfma_f32_16x16x32_bf16 v[104:107], a[136:139], v[56:59], v[104:107]\n" \
"ds_read_b128 a[128:131], v5 offset:16384\n" \
"v_cvt_pk_bf16_f32 v137, v82, v83\n" \
"v_mfma_f32_16x16x32_bf16 v[104:107], a[140:143], v[60:63], v[104:107]\n" \
"s_add_u32 m0, s49, 32768\n" \
"v_exp_f32 v85, v85\n" \
"v_mfma_f32_16x16x32_bf16 v[112:115], a[136:139], a[72:75], v[112:115]\n" \
"buffer_load_dwordx4 v6, s[40:43], s16 offen offset:1024 sc1 lds\n" \
"v_mfma_f32_16x16x32_bf16 v[112:115], a[140:143], a[76:79], v[112:115]\n" \
"v_exp_f32 v86, v86\n" \
"v_mfma_f32_16x16x32_bf16 v[120:123], a[136:139], a[88:91], v[120:123]\n" \
"ds_read_b128 a[132:135], v5 offset:17408\n" \
"v_cvt_pk_bf16_f32 v138, v84, v85\n" \
"v_mfma_f32_16x16x32_bf16 v[120:123], a[140:143], a[92:95], v[120:123]\n" \
"s_waitcnt lgkmcnt(4)\n" \
"v_exp_f32 v87, v87\n" \
"s_add_u32 m0, s79, 147456\n" \
"v_mfma_f32_16x16x32_bf16 v[100:103], a[144:147], v[32:35], 0\n" \
"buffer_load_dwordx4 v12, s[28:31], s81 offen offset:0 lds\n" \
"v_mfma_f32_16x16x32_bf16 v[100:103], a[148:151], v[36:39], v[100:103]\n" \
"v_exp_f32 v88, v88\n" \
"v_mfma_f32_16x16x32_bf16 v[108:111], a[144:147], v[48:51], 0\n" \
"ds_read_b128 a[136:139], v5 offset:18432\n" \
"v_cvt_pk_bf16_f32 v139, v86, v87\n" \
"v_mfma_f32_16x16x32_bf16 v[108:111], a[148:151], v[52:55], v[108:111]\n" \
"s_add_u32 m0, s49, 32768\n" \
"v_exp_f32 v89, v89\n" \
"v_mfma_f32_16x16x32_bf16 v[116:119], a[144:147], a[64:67], 0\n" \
"buffer_load_dwordx4 v6, s[40:43], s16 offen offset:2048 sc1 lds\n" \
"v_mfma_f32_16x16x32_bf16 v[116:119], a[148:151], a[68:71], v[116:119]\n" \
"v_exp_f32 v90, v90\n" \
"v_mfma_f32_16x16x32_bf16 v[124:127], a[144:147], a[80:83], 0\n" \
"ds_read_b128 a[140:143], v5 offset:19456\n" \
"v_cvt_pk_bf16_f32 v140, v88, v89\n" \
"v_mfma_f32_16x16x32_bf16 v[124:127], a[148:151], a[84:87], v[124:127]\n" \
"s_waitcnt lgkmcnt(4)\n" \
"v_exp_f32 v91, v91\n" \
"s_add_u32 m0, s79, 148352\n" \
"v_mfma_f32_16x16x32_bf16 v[100:103], a[152:155], v[40:43], v[100:103]\n" \
"buffer_load_dwordx4 v12, s[28:31], s81 offen offset:128 lds\n" \
"v_mfma_f32_16x16x32_bf16 v[100:103], a[156:159], v[44:47], v[100:103]\n" \
"s_mov_b32 s81, 720896\n" \
"v_exp_f32 v92, v92\n" \
"v_mfma_f32_16x16x32_bf16 v[108:111], a[152:155], v[56:59], v[108:111]\n" \
"ds_read_b128 a[144:147], v5 offset:20480\n" \
"v_cvt_pk_bf16_f32 v141, v90, v91\n" \
"v_mfma_f32_16x16x32_bf16 v[108:111], a[156:159], v[60:63], v[108:111]\n" \
"s_add_u32 m0, s49, 32768\n" \
"v_exp_f32 v93, v93\n" \
"v_mfma_f32_16x16x32_bf16 v[116:119], a[152:155], a[72:75], v[116:119]\n" \
"buffer_load_dwordx4 v6, s[40:43], s16 offen offset:3072 sc1 lds\n" \
"v_mfma_f32_16x16x32_bf16 v[116:119], a[156:159], a[76:79], v[116:119]\n" \
"v_exp_f32 v94, v94\n" \
"v_mfma_f32_16x16x32_bf16 v[124:127], a[152:155], a[88:91], v[124:127]\n" \
"ds_read_b128 a[148:151], v5 offset:21504\n" \
"v_cvt_pk_bf16_f32 v142, v92, v93\n" \
"v_mfma_f32_16x16x32_bf16 v[124:127], a[156:159], a[92:95], v[124:127]\n" \
"s_waitcnt lgkmcnt(4)\n" \
"v_exp_f32 v95, v95\n" \
"s_add_u32 m0, s79, 155648\n" \
"v_smfmac_f32_16x16x64_bf16 v[160:163], v[180:183], v[128:135], v168\n" \
"buffer_load_dwordx4 v12, s[28:31], s81 offen offset:0 lds\n" \
"v_mfma_f32_16x16x32_bf16 v[192:195], a[128:131], v[128:131], v[192:195]\n" \
"v_exp_f32 v96, v96\n" \
"v_mfma_f32_16x16x32_bf16 v[196:199], a[132:135], v[128:131], v[196:199]\n" \
"buffer_store_dwordx4 v[28:31], v4, s[36:39], s80 offen offset:3072 sc1\n" \
"v_mfma_f32_16x16x32_bf16 v[224:227], a[128:131], v[132:135], v[224:227]\n" \
"s_add_u32 m0, s79, 156544\n" \
"ds_read_b128 a[152:155], v5 offset:22528\n" \
"v_cvt_pk_bf16_f32 v143, v94, v95\n" \
"v_mfma_f32_16x16x32_bf16 v[228:231], a[132:135], v[132:135], v[228:231]\n" \
"buffer_load_dwordx4 v12, s[28:31], s81 offen offset:128 lds\n" \
"v_mfma_f32_16x16x32_bf16 a[0:3], a[128:131], v[136:139], a[0:3]\n" \
"v_bfe_u32 v28, v4, 7, 1\n" \
"v_bfe_u32 v29, v4, 4, 3\n" \
"v_mfma_f32_16x16x32_bf16 a[4:7], a[132:135], v[136:139], a[4:7]\n" \
"v_lshl_add_u32 v28, v29, 1, v28\n" \
"v_lshrrev_b32 v29, 8, v4\n" \
"v_mfma_f32_16x16x32_bf16 a[32:35], a[128:131], v[140:143], a[32:35]\n" \
"ds_read_b128 a[156:159], v5 offset:23552\n" \
"v_lshl_add_u32 v28, v29, 4, v28\n" \
"v_mfma_f32_16x16x32_bf16 a[36:39], a[132:135], v[140:143], a[36:39]\n" \
"s_waitcnt vmcnt(8) lgkmcnt(4)\n" \
"v_exp_f32 v97, v97\n" \
"v_mfma_f32_16x16x32_bf16 v[200:203], a[136:139], v[128:131], v[200:203]\n" \
"s_barrier\n" \
"v_exp_f32 v98, v98\n" \
"v_mfma_f32_16x16x32_bf16 v[204:207], a[140:143], v[128:131], v[204:207]\n" \
"v_exp_f32 v99, v99\n" \
"v_mfma_f32_16x16x32_bf16 v[232:235], a[136:139], v[132:135], v[232:235]\n" \
"s_add_u32 s17, s17, s19\n" \
"ds_read_b128 a[128:131], v5 offset:32768\n" \
"v_lshlrev_b32 v28, 4, v28\n" \
"v_mfma_f32_16x16x32_bf16 v[236:239], a[140:143], v[132:135], v[236:239]\n" \
"v_exp_f32 v100, v100\n" \
"v_mfma_f32_16x16x32_bf16 a[8:11], a[136:139], v[136:139], a[8:11]\n" \
"s_add_u32 m0, s49, 49152\n" \
"v_exp_f32 v101, v101\n" \
"v_mfma_f32_16x16x32_bf16 a[12:15], a[140:143], v[136:139], a[12:15]\n" \
"buffer_load_dwordx4 v6, s[40:43], s17 offen offset:0 sc1 lds\n" \
"v_mfma_f32_16x16x32_bf16 a[40:43], a[136:139], v[140:143], a[40:43]\n" \
"ds_read_b128 a[132:135], v5 offset:33792\n" \
"v_add_u32 v29, 0x10000, v28\n" \
"v_mfma_f32_16x16x32_bf16 a[44:47], a[140:143], v[140:143], a[44:47]\n" \
"s_waitcnt lgkmcnt(4)\n" \
"v_exp_f32 v102, v102\n" \
"v_mfma_f32_16x16x32_bf16 v[208:211], a[144:147], v[128:131], v[208:211]\n" \
"v_exp_f32 v103, v103\n" \
"v_mfma_f32_16x16x32_bf16 v[212:215], a[148:151], v[128:131], v[212:215]\n" \
"v_exp_f32 v104, v104\n" \
"v_mfma_f32_16x16x32_bf16 v[240:243], a[144:147], v[132:135], v[240:243]\n" \
"ds_read_b128 a[136:139], v5 offset:34816\n" \
"v_add_u32 v30, 0x20000, v28\n" \
"v_mfma_f32_16x16x32_bf16 v[244:247], a[148:151], v[132:135], v[244:247]\n" \
"v_exp_f32 v105, v105\n" \
"v_mfma_f32_16x16x32_bf16 a[16:19], a[144:147], v[136:139], a[16:19]\n" \
"s_add_u32 m0, s49, 49152\n" \
"v_exp_f32 v106, v106\n" \
"v_mfma_f32_16x16x32_bf16 a[20:23], a[148:151], v[136:139], a[20:23]\n" \
"buffer_load_dwordx4 v6, s[40:43], s17 offen offset:1024 sc1 lds\n" \
"v_mfma_f32_16x16x32_bf16 a[48:51], a[144:147], v[140:143], a[48:51]\n" \
"ds_read_b128 a[140:143], v5 offset:35840\n" \
"v_add_u32 v31, s22, v4\n" \
"v_mfma_f32_16x16x32_bf16 a[52:55], a[148:151], v[140:143], a[52:55]\n" \
"s_waitcnt lgkmcnt(4)\n" \
"v_exp_f32 v107, v107\n" \
"v_mfma_f32_16x16x32_bf16 v[216:219], a[152:155], v[128:131], v[216:219]\n" \
"s_add_u32 m0, s49, 49152\n" \
"v_exp_f32 v108, v108\n" \
"v_mfma_f32_16x16x32_bf16 v[220:223], a[156:159], v[128:131], v[220:223]\n" \
"buffer_load_dwordx4 v6, s[40:43], s17 offen offset:2048 sc1 lds\n" \
"v_mfma_f32_16x16x32_bf16 v[248:251], a[152:155], v[132:135], v[248:251]\n" \
"ds_read_b128 a[144:147], v5 offset:36864\n" \
"v_cvt_pk_bf16_f32 v144, v96, v97\n" \
"v_mfma_f32_16x16x32_bf16 v[252:255], a[156:159], v[132:135], v[252:255]\n" \
"v_exp_f32 v109, v109\n" \
"v_mfma_f32_16x16x32_bf16 a[24:27], a[152:155], v[136:139], a[24:27]\n" \
"s_add_u32 m0, s49, 49152\n" \
"v_exp_f32 v110, v110\n" \
"v_mfma_f32_16x16x32_bf16 a[28:31], a[156:159], v[136:139], a[28:31]\n" \
"buffer_load_dwordx4 v6, s[40:43], s17 offen offset:3072 sc1 lds\n" \
"v_mfma_f32_16x16x32_bf16 a[56:59], a[152:155], v[140:143], a[56:59]\n" \
"ds_read_b128 a[148:151], v5 offset:37888\n" \
"v_cvt_pk_bf16_f32 v145, v98, v99\n" \
"v_mfma_f32_16x16x32_bf16 a[60:63], a[156:159], v[140:143], a[60:63]\n" \
"s_waitcnt lgkmcnt(4)\n" \
"v_exp_f32 v111, v111\n" \
"v_smfmac_f32_16x16x64_bf16 v[164:167], v[180:183], v[136:143], v168\n" \
"ds_read_b64_tr_b16 a[224:225], v13 offset:0\n" \
"v_cvt_pk_bf16_f32 v146, v100, v101\n" \
"v_cvt_pk_bf16_f32 v147, v102, v103\n" \
"v_cvt_pk_bf16_f32 v148, v104, v105\n" \
"v_cvt_pk_bf16_f32 v149, v106, v107\n" \
"v_cvt_pk_bf16_f32 v150, v108, v109\n" \
"v_cvt_pk_bf16_f32 v151, v110, v111\n" \
"v_mfma_f32_16x16x32_bf16 v[64:67], a[128:131], v[32:35], 0\n" \
"LH19_%=:\ns_cbranch_execz LHR1_%=\n" \
"buffer_load_dwordx4 a[116:119], v189, s[36:39], s84 offen sc1\n" \
"v_mfma_f32_16x16x32_bf16 v[64:67], a[132:135], v[36:39], v[64:67]\n" \
"s_add_u32 s16, s16, s9\n" \
"ds_read_b64_tr_b16 a[226:227], v14 offset:0\n" \
"v_mfma_f32_16x16x32_bf16 v[72:75], a[128:131], v[48:51], 0\n" \
"s_add_u32 s100, s100, 262144\ns_addc_u32 s101, s101, 0\n" \
"ds_read_b128 a[152:155], v5 offset:38912\n" \
"v_mfma_f32_16x16x32_bf16 v[72:75], a[132:135], v[52:55], v[72:75]\n" \
"s_add_u32 m0, s48, 0\n" \
"ds_read_b64_tr_b16 a[228:229], v15 offset:0\n" \
"v_mfma_f32_16x16x32_bf16 v[80:83], a[128:131], a[64:67], 0\n" \
"buffer_load_dwordx4 v6, s[40:43], s16 offen offset:0 sc1 lds\n" \
"v_mfma_f32_16x16x32_bf16 v[80:83], a[132:135], a[68:71], v[80:83]\n" \
"ds_read_b64_tr_b16 a[230:231], v16 offset:0\n" \
"v_mfma_f32_16x16x32_bf16 v[88:91], a[128:131], a[80:83], 0\n" \
"ds_read_b128 a[156:159], v5 offset:39936\n" \
"v_mfma_f32_16x16x32_bf16 v[88:91], a[132:135], a[84:87], v[88:91]\n" \
"s_waitcnt lgkmcnt(4)\n" \
"ds_read_b64_tr_b16 a[232:233], v13 offset:8192\n" \
"v_mfma_f32_16x16x32_bf16 v[64:67], a[136:139], v[40:43], v[64:67]\n" \
"buffer_load_dwordx4 a[120:123], v188, s[36:39], s85 offen sc1\n" \
"v_mfma_f32_16x16x32_bf16 v[64:67], a[140:143], v[44:47], v[64:67]\n" \
"s_xor_b32 s49, s49, 0x10000\n" \
"v_exp_f32 v112, v112\n" \
"v_mfma_f32_16x16x32_bf16 v[72:75], a[136:139], v[56:59], v[72:75]\n" \
"ds_read_b128 a[128:131], v5 offset:49152\n" \
"v_mfma_f32_16x16x32_bf16 v[72:75], a[140:143], v[60:63], v[72:75]\n" \
"s_add_u32 m0, s48, 0\n" \
"ds_read_b64_tr_b16 a[234:235], v14 offset:8192\n" \
"v_mfma_f32_16x16x32_bf16 v[80:83], a[136:139], a[72:75], v[80:83]\n" \
"buffer_load_dwordx4 v6, s[40:43], s16 offen offset:1024 sc1 lds\n" \
"v_mfma_f32_16x16x32_bf16 v[80:83], a[140:143], a[76:79], v[80:83]\n" \
"v_exp_f32 v113, v113\n" \
"v_mfma_f32_16x16x32_bf16 v[88:91], a[136:139], a[88:91], v[88:91]\n" \
"ds_read_b128 a[132:135], v5 offset:50176\n" \
"v_cvt_pk_bf16_f32 v152, v112, v113\n" \
"v_mfma_f32_16x16x32_bf16 v[88:91], a[140:143], a[92:95], v[88:91]\n" \
"s_waitcnt lgkmcnt(4)\n" \
"v_exp_f32 v114, v114\n" \
"v_mfma_f32_16x16x32_bf16 v[68:71], a[144:147], v[32:35], 0\n" \
"v_exp_f32 v115, v115\n" \
"v_mfma_f32_16x16x32_bf16 v[68:71], a[148:151], v[36:39], v[68:71]\n" \
"v_exp_f32 v116, v116\n" \
"v_mfma_f32_16x16x32_bf16 v[76:79], a[144:147], v[48:51], 0\n" \
"ds_read_b128 a[136:139], v5 offset:51200\n" \
"v_cvt_pk_bf16_f32 v153, v114, v115\n" \
"v_mfma_f32_16x16x32_bf16 v[76:79], a[148:151], v[52:55], v[76:79]\n" \
"s_add_u32 m0, s48, 0\n" \
"v_exp_f32 v117, v117\n" \
"v_mfma_f32_16x16x32_bf16 v[84:87], a[144:147], a[64:67], 0\n" \
"buffer_load_dwordx4 v6, s[40:43], s16 offen offset:2048 sc1 lds\n" \
"v_mfma_f32_16x16x32_bf16 v[84:87], a[148:151], a[68:71], v[84:87]\n" \
"v_exp_f32 v118, v118\n" \
"v_mfma_f32_16x16x32_bf16 v[92:95], a[144:147], a[80:83], 0\n" \
"ds_read_b128 a[140:143], v5 offset:52224\n" \
"v_cvt_pk_bf16_f32 v154, v116, v117\n" \
"v_mfma_f32_16x16x32_bf16 v[92:95], a[148:151], a[84:87], v[92:95]\n" \
"s_waitcnt lgkmcnt(4)\n" \
"v_exp_f32 v119, v119\n" \
"v_mfma_f32_16x16x32_bf16 v[68:71], a[152:155], v[40:43], v[68:71]\n" \
"v_exp_f32 v120, v120\n" \
"v_mfma_f32_16x16x32_bf16 v[68:71], a[156:159], v[44:47], v[68:71]\n" \
"v_exp_f32 v121, v121\n" \
"v_mfma_f32_16x16x32_bf16 v[76:79], a[152:155], v[56:59], v[76:79]\n" \
"ds_read_b128 a[144:147], v5 offset:53248\n" \
"v_cvt_pk_bf16_f32 v155, v118, v119\n" \
"v_mfma_f32_16x16x32_bf16 v[76:79], a[156:159], v[60:63], v[76:79]\n" \
"s_add_u32 m0, s48, 0\n" \
"v_exp_f32 v122, v122\n" \
"v_mfma_f32_16x16x32_bf16 v[84:87], a[152:155], a[72:75], v[84:87]\n" \
"buffer_load_dwordx4 v6, s[40:43], s16 offen offset:3072 sc1 lds\n" \
"v_mfma_f32_16x16x32_bf16 v[84:87], a[156:159], a[76:79], v[84:87]\n" \
"s_lshl_b32 s25, s45, 2\n" \
"v_exp_f32 v123, v123\n" \
"v_mfma_f32_16x16x32_bf16 v[92:95], a[152:155], a[88:91], v[92:95]\n" \
"s_andn2_b32 s25, 32, s25\n" \
"ds_read_b128 a[148:151], v5 offset:54272\n" \
"s_add_u32 s25, s25, %2\n" \
"v_mfma_f32_16x16x32_bf16 v[92:95], a[156:159], a[92:95], v[92:95]\n" \
"s_waitcnt lgkmcnt(4)\n" \
"v_exp_f32 v124, v124\n" \
"v_smfmac_f32_16x16x64_bf16 v[160:163], v[180:183], v[144:151], v168\n" \
"buffer_load_dwordx4 a[124:127], v189, s[36:39], s85 offen sc1\n" \
"v_mfma_f32_16x16x32_bf16 v[192:195], a[128:131], v[144:147], v[192:195]\n" \
"v_exp_f32 v125, v125\n" \
"v_mfma_f32_16x16x32_bf16 v[196:199], a[132:135], v[144:147], v[196:199]\n" \
"ds_read_b64_tr_b16 a[236:237], v15 offset:8192\n" \
"ds_read_b64_tr_b16 a[238:239], v16 offset:8192\n" \
"v_mfma_f32_16x16x32_bf16 v[224:227], a[128:131], v[148:151], v[224:227]\n" \
"s_cmp_ge_u32 s25, 34\ns_cselect_b64 vcc, -1, 0\n" \
"ds_read_b128 a[152:155], v5 offset:55296\n" \
"v_exp_f32 v126, v126\n" \
"v_mfma_f32_16x16x32_bf16 v[228:231], a[132:135], v[148:151], v[228:231]\n" \
"v_exp_f32 v127, v127\n" \
"v_mfma_f32_16x16x32_bf16 a[0:3], a[128:131], v[152:155], a[0:3]\n" \
"v_exp_f32 v64, v64\n" \
"v_cvt_pk_bf16_f32 v156, v120, v121\n" \
"v_cvt_pk_bf16_f32 v157, v122, v123\n" \
"v_cvt_pk_bf16_f32 v158, v124, v125\n" \
"v_cvt_pk_bf16_f32 v159, v126, v127\n" \
"v_mfma_f32_16x16x32_bf16 a[4:7], a[132:135], v[152:155], a[4:7]\n" \
"s_waitcnt lgkmcnt(1)\n" \
"v_exp_f32 v65, v65\n" \
"v_mfma_f32_16x16x32_bf16 a[32:35], a[128:131], v[156:159], a[32:35]\n" \
"ds_read_b128 a[156:159], v5 offset:56320\n" \
"v_cvt_pk_bf16_f32 v128, v64, v65\n" \
"v_mfma_f32_16x16x32_bf16 a[36:39], a[132:135], v[156:159], a[36:39]\n" \
"s_waitcnt vmcnt(11) lgkmcnt(4)\n" \
"v_exp_f32 v66, v66\n" \
"v_mfma_f32_16x16x32_bf16 v[200:203], a[136:139], v[144:147], v[200:203]\n" \
"s_barrier\n" \
"v_xor_b32 v5, 0x10000, v5\n" \
"v_mfma_f32_16x16x32_bf16 v[204:207], a[140:143], v[144:147], v[204:207]\n" \
"v_exp_f32 v67, v67\n" \
"v_mfma_f32_16x16x32_bf16 v[232:235], a[136:139], v[148:151], v[232:235]\n" \
"s_add_u32 s17, s17, s19\n" \
"ds_read_b128 a[128:131], v5 offset:0\n" \
"s_waitcnt vmcnt(12)\n" \
"v_mfma_f32_16x16x32_bf16 v[236:239], a[140:143], v[148:151], v[236:239]\n" \
"v_exp_f32 v68, v68\n" \
"v_mfma_f32_16x16x32_bf16 a[8:11], a[136:139], v[152:155], a[8:11]\n" \
"s_add_u32 m0, s48, 16384\n" \
"v_exp_f32 v69, v69\n" \
"v_mfma_f32_16x16x32_bf16 a[12:15], a[140:143], v[152:155], a[12:15]\n" \
"buffer_load_dwordx4 v6, s[40:43], s17 offen offset:0 sc1 lds\n" \
"v_mfma_f32_16x16x32_bf16 a[40:43], a[136:139], v[156:159], a[40:43]\n" \
"ds_read_b128 a[132:135], v5 offset:1024\n" \
"v_cvt_pk_bf16_f32 v129, v66, v67\n" \
"v_mfma_f32_16x16x32_bf16 a[44:47], a[140:143], v[156:159], a[44:47]\n" \
"s_waitcnt lgkmcnt(4)\n" \
"v_exp_f32 v70, v70\n" \
"v_mfma_f32_16x16x32_bf16 v[208:211], a[144:147], v[144:147], v[208:211]\n" \
"v_exp_f32 v71, v71\n" \
"v_mfma_f32_16x16x32_bf16 v[212:215], a[148:151], v[144:147], v[212:215]\n" \
"v_exp_f32 v72, v72\n" \
"v_mfma_f32_16x16x32_bf16 v[240:243], a[144:147], v[148:151], v[240:243]\n" \
"ds_read_b128 a[136:139], v5 offset:2048\n" \
"v_cvt_pk_bf16_f32 v130, v68, v69\n" \
"v_mfma_f32_16x16x32_bf16 v[244:247], a[148:151], v[148:151], v[244:247]\n" \
"v_exp_f32 v73, v73\n" \
"v_mfma_f32_16x16x32_bf16 a[16:19], a[144:147], v[152:155], a[16:19]\n" \
"s_add_u32 m0, s48, 16384\n" \
"v_exp_f32 v74, v74\n" \
"v_mfma_f32_16x16x32_bf16 a[20:23], a[148:151], v[152:155], a[20:23]\n" \
"buffer_load_dwordx4 v6, s[40:43], s17 offen offset:1024 sc1 lds\n" \
"v_mfma_f32_16x16x32_bf16 a[48:51], a[144:147], v[156:159], a[48:51]\n" \
"ds_read_b128 a[140:143], v5 offset:3072\n" \
"v_cvt_pk_bf16_f32 v131, v70, v71\n" \
"v_mfma_f32_16x16x32_bf16 a[52:55], a[148:151], v[156:159], a[52:55]\n" \
"s_waitcnt lgkmcnt(4)\n" \
"v_exp_f32 v75, v75\n" \
"v_mfma_f32_16x16x32_bf16 v[216:219], a[152:155], v[144:147], v[216:219]\n" \
"s_add_u32 m0, s48, 16384\n" \
"v_exp_f32 v76, v76\n" \
"v_mfma_f32_16x16x32_bf16 v[220:223], a[156:159], v[144:147], v[220:223]\n" \
"s_add_u32 s44, s44, 1\n" \
"buffer_load_dwordx4 v6, s[40:43], s17 offen offset:2048 sc1 lds\n" \
"v_mfma_f32_16x16x32_bf16 v[248:251], a[152:155], v[148:151], v[248:251]\n" \
"ds_read_b128 a[144:147], v5 offset:4096\n" \
"v_cvt_pk_bf16_f32 v132, v72, v73\n" \
"v_mfma_f32_16x16x32_bf16 v[252:255], a[156:159], v[148:151], v[252:255]\n" \
"v_exp_f32 v77, v77\n" \
"v_mfma_f32_16x16x32_bf16 a[24:27], a[152:155], v[152:155], a[24:27]\n" \
"s_add_u32 m0, s48, 16384\n" \
"v_exp_f32 v78, v78\n" \
"v_mfma_f32_16x16x32_bf16 a[28:31], a[156:159], v[152:155], a[28:31]\n" \
"s_xor_b32 s48, s48, 0x10000\n" \
"buffer_load_dwordx4 v6, s[40:43], s17 offen offset:3072 sc1 lds\n" \
"v_mfma_f32_16x16x32_bf16 a[56:59], a[152:155], v[156:159], a[56:59]\n" \
"ds_read_b128 a[148:151], v5 offset:5120\n" \
"v_cvt_pk_bf16_f32 v133, v74, v75\n" \
"v_mfma_f32_16x16x32_bf16 a[60:63], a[156:159], v[156:159], a[60:63]\n" \
"s_waitcnt lgkmcnt(4)\n" \
"v_exp_f32 v79, v79\n" \
"v_smfmac_f32_16x16x64_bf16 v[164:167], v[180:183], v[152:159], v168\n" \
"buffer_store_dwordx2 v[10:11], v20, s[36:39], s12 offen sc1\n" \
"v_cvt_pk_bf16_f32 v134, v76, v77\n" \
"v_cvt_pk_bf16_f32 v135, v78, v79\n" \
"v_mfma_f32_16x16x32_bf16 v[96:99], a[128:131], v[32:35], 0\n" \
"ds_read_b64_tr_b16 a[240:241], v13 offset:16384\n" \
"v_bfe_u32 v188, v4, 7, 2\n" \
"v_mfma_f32_16x16x32_bf16 v[96:99], a[132:135], v[36:39], v[96:99]\n" \
"s_add_u32 s16, s16, s9\n" \
"ds_read_b64_tr_b16 a[242:243], v14 offset:16384\n" \
"v_min_u32 v188, 2, v188\n" \
"v_mfma_f32_16x16x32_bf16 v[104:107], a[128:131], v[48:51], 0\n" \
"ds_read_b128 a[152:155], v5 offset:6144\n" \
"v_lshl_add_u32 v17, v188, 8, s98\n" \
"v_mfma_f32_16x16x32_bf16 v[104:107], a[132:135], v[52:55], v[104:107]\n" \
"s_add_u32 m0, s49, 32768\n" \
"ds_read_b64_tr_b16 a[244:245], v15 offset:16384\n" \
"v_bfe_u32 v188, v4, 4, 3\n" \
"v_mfma_f32_16x16x32_bf16 v[112:115], a[128:131], a[64:67], 0\n" \
"buffer_load_dwordx4 v6, s[40:43], s16 offen offset:0 sc1 lds\n" \
"v_mfma_f32_16x16x32_bf16 v[112:115], a[132:135], a[68:71], v[112:115]\n" \
"ds_read_b64_tr_b16 a[246:247], v16 offset:16384\n" \
"v_lshl_add_u32 v17, v188, 3, v17\n" \
"v_mfma_f32_16x16x32_bf16 v[120:123], a[128:131], a[80:83], 0\n" \
"ds_read_b128 a[156:159], v5 offset:7168\n" \
"v_mfma_f32_16x16x32_bf16 v[120:123], a[132:135], a[84:87], v[120:123]\n" \
"s_waitcnt lgkmcnt(4)\n" \
"ds_read_b64_tr_b16 a[248:249], v13 offset:24576\n" \
"v_mfma_f32_16x16x32_bf16 v[96:99], a[136:139], v[40:43], v[96:99]\n" \
"buffer_store_dwordx2 v[10:11], v20, s[36:39], s88 offen sc1\n" \
"v_mfma_f32_16x16x32_bf16 v[96:99], a[140:143], v[44:47], v[96:99]\n" \
"s_mov_b32 s46, s23\n" \
"v_exp_f32 v80, v80\n" \
"v_mfma_f32_16x16x32_bf16 v[104:107], a[136:139], v[56:59], v[104:107]\n" \
"ds_read_b128 a[128:131], v5 offset:16384\n" \
"v_mfma_f32_16x16x32_bf16 v[104:107], a[140:143], v[60:63], v[104:107]\n" \
"s_add_u32 m0, s49, 32768\n" \
"ds_read_b64_tr_b16 a[250:251], v14 offset:24576\n" \
"v_mfma_f32_16x16x32_bf16 v[112:115], a[136:139], a[72:75], v[112:115]\n" \
"buffer_load_dwordx4 v6, s[40:43], s16 offen offset:1024 sc1 lds\n" \
"v_mfma_f32_16x16x32_bf16 v[112:115], a[140:143], a[76:79], v[112:115]\n" \
"v_exp_f32 v81, v81\n" \
"v_mfma_f32_16x16x32_bf16 v[120:123], a[136:139], a[88:91], v[120:123]\n" \
"ds_read_b128 a[132:135], v5 offset:17408\n" \
"v_cvt_pk_bf16_f32 v136, v80, v81\n" \
"v_mfma_f32_16x16x32_bf16 v[120:123], a[140:143], a[92:95], v[120:123]\n" \
"s_waitcnt lgkmcnt(4)\n" \
"v_exp_f32 v82, v82\n" \
"v_mfma_f32_16x16x32_bf16 v[100:103], a[144:147], v[32:35], 0\n" \
"v_exp_f32 v83, v83\n" \
"v_mfma_f32_16x16x32_bf16 v[100:103], a[148:151], v[36:39], v[100:103]\n" \
"v_exp_f32 v84, v84\n" \
"v_mfma_f32_16x16x32_bf16 v[108:111], a[144:147], v[48:51], 0\n" \
"ds_read_b128 a[136:139], v5 offset:18432\n" \
"v_cvt_pk_bf16_f32 v137, v82, v83\n" \
"v_mfma_f32_16x16x32_bf16 v[108:111], a[148:151], v[52:55], v[108:111]\n" \
"s_add_u32 m0, s49, 32768\n" \
"v_exp_f32 v85, v85\n" \
"v_mfma_f32_16x16x32_bf16 v[116:119], a[144:147], a[64:67], 0\n" \
"buffer_load_dwordx4 v6, s[40:43], s16 offen offset:2048 sc1 lds\n" \
"v_mfma_f32_16x16x32_bf16 v[116:119], a[148:151], a[68:71], v[116:119]\n" \
"v_exp_f32 v86, v86\n" \
"v_mfma_f32_16x16x32_bf16 v[124:127], a[144:147], a[80:83], 0\n" \
"ds_read_b128 a[140:143], v5 offset:19456\n" \
"v_cvt_pk_bf16_f32 v138, v84, v85\n" \
"v_mfma_f32_16x16x32_bf16 v[124:127], a[148:151], a[84:87], v[124:127]\n" \
"s_waitcnt lgkmcnt(4)\n" \
"v_exp_f32 v87, v87\n" \
"v_mfma_f32_16x16x32_bf16 v[100:103], a[152:155], v[40:43], v[100:103]\n" \
"v_exp_f32 v88, v88\n" \
"v_mfma_f32_16x16x32_bf16 v[100:103], a[156:159], v[44:47], v[100:103]\n" \
"v_exp_f32 v89, v89\n" \
"v_mfma_f32_16x16x32_bf16 v[108:111], a[152:155], v[56:59], v[108:111]\n" \
"ds_read_b128 a[144:147], v5 offset:20480\n" \
"v_cvt_pk_bf16_f32 v139, v86, v87\n" \
"v_mfma_f32_16x16x32_bf16 v[108:111], a[156:159], v[60:63], v[108:111]\n" \
"s_add_u32 m0, s49, 32768\n" \
"v_exp_f32 v90, v90\n" \
"v_mfma_f32_16x16x32_bf16 v[116:119], a[152:155], a[72:75], v[116:119]\n" \
"buffer_load_dwordx4 v6, s[40:43], s16 offen offset:3072 sc1 lds\n" \
"v_mfma_f32_16x16x32_bf16 v[116:119], a[156:159], a[76:79], v[116:119]\n" \
"v_exp_f32 v91, v91\n" \
"v_mfma_f32_16x16x32_bf16 v[124:127], a[152:155], a[88:91], v[124:127]\n" \
"ds_read_b128 a[148:151], v5 offset:21504\n" \
"v_cvt_pk_bf16_f32 v140, v88, v89\n" \
"v_mfma_f32_16x16x32_bf16 v[124:127], a[156:159], a[92:95], v[124:127]\n" \
"s_waitcnt lgkmcnt(4)\n" \
"v_exp_f32 v92, v92\n" \
"v_smfmac_f32_16x16x64_bf16 v[160:163], v[180:183], v[128:135], v168\n" \
"s_mov_b64 exec, 0xffff\nglobal_load_dword v1, v2, s[100:101]\ns_mov_b64 exec, -1\n" \
"v_mfma_f32_16x16x32_bf16 v[192:195], a[128:131], v[128:131], v[192:195]\n" \
"v_exp_f32 v93, v93\n" \
"v_mfma_f32_16x16x32_bf16 v[196:199], a[132:135], v[128:131], v[196:199]\n" \
"ds_read_b64_tr_b16 a[252:253], v15 offset:24576\n" \
"ds_read_b64_tr_b16 a[254:255], v16 offset:24576\n" \
"v_mfma_f32_16x16x32_bf16 v[224:227], a[128:131], v[132:135], v[224:227]\n" \
"s_add_u32 s100, s100, 262144\ns_addc_u32 s101, s101, 0\n" \
"ds_read_b128 a[152:155], v5 offset:22528\n" \
"v_exp_f32 v94, v94\n" \
"v_mfma_f32_16x16x32_bf16 v[228:231], a[132:135], v[132:135], v[228:231]\n" \
"v_exp_f32 v95, v95\n" \
"v_mfma_f32_16x16x32_bf16 a[0:3], a[128:131], v[136:139], a[0:3]\n" \
"s_mov_b64 exec, 0xffff\nglobal_load_dword v1, v2, s[100:101]\ns_mov_b64 exec, -1\n" \
"v_cvt_pk_bf16_f32 v141, v90, v91\n" \
"v_cvt_pk_bf16_f32 v142, v92, v93\n" \
"v_cvt_pk_bf16_f32 v143, v94, v95\n" \
"v_mfma_f32_16x16x32_bf16 a[4:7], a[132:135], v[136:139], a[4:7]\n" \
"s_waitcnt lgkmcnt(1)\n" \
"v_exp_f32 v96, v96\n" \
"v_mfma_f32_16x16x32_bf16 a[32:35], a[128:131], v[140:143], a[32:35]\n" \
"ds_read_b128 a[156:159], v5 offset:23552\n" \
"v_mov_b32 v2, v190\n" \
"v_mfma_f32_16x16x32_bf16 a[36:39], a[132:135], v[140:143], a[36:39]\n" \
"s_waitcnt vmcnt(19) lgkmcnt(4)\n" \
"v_exp_f32 v97, v97\n" \
"v_mfma_f32_16x16x32_bf16 v[200:203], a[136:139], v[128:131], v[200:203]\n" \
"s_barrier\n" \
"v_exp_f32 v98, v98\n" \
"v_mfma_f32_16x16x32_bf16 v[204:207], a[140:143], v[128:131], v[204:207]\n" \
"v_exp_f32 v99, v99\n" \
"v_mfma_f32_16x16x32_bf16 v[232:235], a[136:139], v[132:135], v[232:235]\n" \
"s_add_u32 s17, s17, s19\n" \
"ds_read_b128 a[128:131], v5 offset:32768\n" \
"v_cvt_pk_bf16_f32 v144, v96, v97\n" \
"v_mfma_f32_16x16x32_bf16 v[236:239], a[140:143], v[132:135], v[236:239]\n" \
"v_exp_f32 v100, v100\n" \
"v_mfma_f32_16x16x32_bf16 a[8:11], a[136:139], v[136:139], a[8:11]\n" \
"s_add_u32 m0, s49, 49152\n" \
"v_exp_f32 v101, v101\n" \
"v_mfma_f32_16x16x32_bf16 a[12:15], a[140:143], v[136:139], a[12:15]\n" \
"buffer_load_dwordx4 v6, s[40:43], s17 offen offset:0 sc1 lds\n" \
"v_mfma_f32_16x16x32_bf16 a[40:43], a[136:139], v[140:143], a[40:43]\n" \
"ds_read_b128 a[132:135], v5 offset:33792\n" \
"v_cvt_pk_bf16_f32 v145, v98, v99\n" \
"v_mfma_f32_16x16x32_bf16 a[44:47], a[140:143], v[140:143], a[44:47]\n" \
"s_waitcnt lgkmcnt(4)\n" \
"v_exp_f32 v102, v102\n" \
"v_mfma_f32_16x16x32_bf16 v[208:211], a[144:147], v[128:131], v[208:211]\n" \
"v_exp_f32 v103, v103\n" \
"v_mfma_f32_16x16x32_bf16 v[212:215], a[148:151], v[128:131], v[212:215]\n" \
"v_exp_f32 v104, v104\n" \
"v_mfma_f32_16x16x32_bf16 v[240:243], a[144:147], v[132:135], v[240:243]\n" \
"ds_read_b128 a[136:139], v5 offset:34816\n" \
"v_cvt_pk_bf16_f32 v146, v100, v101\n" \
"v_mfma_f32_16x16x32_bf16 v[244:247], a[148:151], v[132:135], v[244:247]\n" \
"v_exp_f32 v105, v105\n" \
"v_mfma_f32_16x16x32_bf16 a[16:19], a[144:147], v[136:139], a[16:19]\n" \
"s_add_u32 m0, s49, 49152\n" \
"v_exp_f32 v106, v106\n" \
"v_mfma_f32_16x16x32_bf16 a[20:23], a[148:151], v[136:139], a[20:23]\n" \
"buffer_load_dwordx4 v6, s[40:43], s17 offen offset:1024 sc1 lds\n" \
"v_mfma_f32_16x16x32_bf16 a[48:51], a[144:147], v[140:143], a[48:51]\n" \
"ds_read_b128 a[140:143], v5 offset:35840\n" \
"v_cvt_pk_bf16_f32 v147, v102, v103\n" \
"v_mfma_f32_16x16x32_bf16 a[52:55], a[148:151], v[140:143], a[52:55]\n" \
"s_waitcnt lgkmcnt(4)\n" \
"v_exp_f32 v107, v107\n" \
"v_mfma_f32_16x16x32_bf16 v[216:219], a[152:155], v[128:131], v[216:219]\n" \
"s_add_u32 m0, s49, 49152\n" \
"v_exp_f32 v108, v108\n" \
"v_mfma_f32_16x16x32_bf16 v[220:223], a[156:159], v[128:131], v[220:223]\n" \
"buffer_load_dwordx4 v6, s[40:43], s17 offen offset:2048 sc1 lds\n" \
"v_mfma_f32_16x16x32_bf16 v[248:251], a[152:155], v[132:135], v[248:251]\n" \
"ds_read_b128 a[144:147], v5 offset:36864\n" \
"v_cvt_pk_bf16_f32 v148, v104, v105\n" \
"v_mfma_f32_16x16x32_bf16 v[252:255], a[156:159], v[132:135], v[252:255]\n" \
"v_exp_f32 v109, v109\n" \
"v_mfma_f32_16x16x32_bf16 a[24:27], a[152:155], v[136:139], a[24:27]\n" \
"s_add_u32 m0, s49, 49152\n" \
"v_exp_f32 v110, v110\n" \
"v_mfma_f32_16x16x32_bf16 a[28:31], a[156:159], v[136:139], a[28:31]\n" \
"buffer_load_dwordx4 v6, s[40:43], s17 offen offset:3072 sc1 lds\n" \
"v_mfma_f32_16x16x32_bf16 a[56:59], a[152:155], v[140:143], a[56:59]\n" \
"ds_read_b128 a[148:151], v5 offset:37888\n" \
"v_cvt_pk_bf16_f32 v149, v106, v107\n" \
"v_mfma_f32_16x16x32_bf16 a[60:63], a[156:159], v[140:143], a[60:63]\n" \
"s_waitcnt lgkmcnt(4)\n" \
"v_exp_f32 v111, v111\n" \
"v_smfmac_f32_16x16x64_bf16 v[164:167], v[180:183], v[136:143], v168\n" \
"v_cvt_pk_bf16_f32 v150, v108, v109\n" \
"v_cvt_pk_bf16_f32 v151, v110, v111\n" \
"s_cbranch_vccz LQWB_%=\nds_write_b128 v21, v[32:35] offset:16384\n" \
"ds_write_b128 v21, v[36:39] offset:17408\n" \
"ds_write_b128 v21, v[40:43] offset:18432\n" \
"ds_write_b128 v21, v[44:47] offset:19456\n" \
"ds_write_b128 v21, v[48:51] offset:20480\n" \
"ds_write_b128 v21, v[52:55] offset:21504\n" \
"ds_write_b128 v21, v[56:59] offset:22528\n" \
"ds_write_b128 v21, v[60:63] offset:23552\n" \
"ds_write_b128 v21, a[64:67] offset:0\n" \
"ds_write_b128 v21, a[68:71] offset:1024\n" \
"ds_write_b128 v21, a[72:75] offset:2048\n" \
"ds_write_b128 v21, a[76:79] offset:3072\n" \
"ds_write_b128 v21, a[80:83] offset:4096\n" \
"ds_write_b128 v21, a[84:87] offset:5120\n" \
"ds_write_b128 v21, a[88:91] offset:6144\n" \
"ds_write_b128 v21, a[92:95] offset:7168\nLQWB_%=:\n" \
"s_mov_b64 exec, 0\ns_cmp_eq_u32 %2, 0\ns_cbranch_scc1 LH23_%=\ns_cmp_eq_u32 %2, 1\ns_cbranch_scc1 LH24_%=\ns_cmp_eq_u32 %2, 2\ns_cbranch_scc1 LH25_%=\ns_cmp_eq_u32 %2, 3\ns_cbranch_scc1 LH26_%=\nLHR2_%=:\ns_mov_b64 exec, -1\n" \
"s_branch LCHK_%=\n" \
"LAD2_%=:\n" \
"v_xor_b32 v8, 0x2000, v5\n" \
"ds_read_b128 v[32:35], v21 offset:0\n" \
"ds_read_b128 v[36:39], v21 offset:1024\n" \
"ds_read_b128 v[40:43], v21 offset:2048\n" \
"ds_read_b128 v[44:47], v21 offset:3072\n" \
"ds_read_b128 v[48:51], v21 offset:4096\n" \
"ds_read_b128 v[52:55], v21 offset:5120\n" \
"ds_read_b128 v[56:59], v21 offset:6144\n" \
"ds_read_b128 v[60:63], v21 offset:7168\n" \
"ds_read_b128 a[64:67], v21 offset:16384\n" \
"ds_read_b128 a[68:71], v21 offset:17408\n" \
"ds_read_b128 a[128:131], v5 offset:49152\n" \
"ds_read_b128 a[132:135], v5 offset:50176\n" \
"ds_read_b128 a[72:75], v21 offset:18432\n" \
"ds_read_b128 a[76:79], v21 offset:19456\n" \
"ds_read_b128 a[80:83], v21 offset:20480\n" \
"ds_read_b128 a[84:87], v21 offset:21504\n" \
"ds_read_b128 a[88:91], v21 offset:22528\n" \
"ds_read_b128 a[92:95], v21 offset:23552\n" \
"ds_read_b128 a[136:139], v5 offset:51200\n" \
"ds_read_b128 a[140:143], v5 offset:52224\n" \
"ds_read_b128 a[144:147], v5 offset:53248\n" \
"ds_read_b128 a[148:151], v5 offset:54272\n" \
"ds_read_b128 a[152:155], v5 offset:55296\n" \
"v_exp_f32 v112, v112\n" \
"s_nop 0\n" \
"v_exp_f32 v113, v113\n" \
"s_nop 0\n" \
"v_exp_f32 v114, v114\n" \
"s_nop 0\n" \
"v_cvt_pk_bf16_f32 v152, v112, v113\n" \
"v_exp_f32 v115, v115\n" \
"s_nop 0\n" \
"v_exp_f32 v120, v120\n" \
"s_nop 0\n" \
"v_exp_f32 v121, v121\n" \
"s_nop 0\n" \
"v_exp_f32 v122, v122\n" \
"s_nop 0\n" \
"v_exp_f32 v123, v123\n" \
"s_nop 0\n" \
"v_exp_f32 v124, v124\n" \
"s_nop 0\n" \
"v_exp_f32 v125, v125\n" \
"s_nop 0\n" \
"v_exp_f32 v126, v126\n" \
"s_nop 0\n" \
"v_exp_f32 v127, v127\n" \
"s_nop 0\n" \
"v_cvt_pk_bf16_f32 v153, v114, v115\n" \
"v_mov_b32 v154, 0\n" \
"v_mov_b32 v155, 0\n" \
"v_cvt_pk_bf16_f32 v156, v120, v121\n" \
"v_cvt_pk_bf16_f32 v157, v122, v123\n" \
"v_cvt_pk_bf16_f32 v158, v124, v125\n" \
"v_cvt_pk_bf16_f32 v159, v126, v127\n" \
"v_add_f32 v160, v160, v161\n" \
"v_add_f32 v162, v162, v163\n" \
"v_subrev_u32 v23, 0x1f800000, v160\n" \
"v_subrev_u32 v27, 0x1f800000, v162\n" \
"v_max_u32 v23, v23, v27\n" \
"v_cmp_le_u32 vcc, 0x40000000, v23\n" \
"s_nop 0\n" \
"s_or_b64 s[54:55], s[54:55], vcc\n" \
"v_rcp_f32 v22, v160\n" \
"s_nop 0\n" \
"v_rcp_f32 v26, v162\n" \
"s_nop 0\n" \
"s_waitcnt lgkmcnt(0)\n" \
"s_branch LS3_%=\n" \
".p2align 6\n" \
"LH20_%=:\ns_cbranch_execz LHR1_%=\n" \
"s_nop 0\n" \
"LS1_%=:\n" \
"v_mfma_f32_16x16x32_bf16 v[64:67], a[128:131], v[32:35], v[176:179]\n" \
"v_exp_f32 v112, v112\n" \
"v_mfma_f32_16x16x32_bf16 v[64:67], a[132:135], v[36:39], v[64:67]\n" \
"s_add_u32 s16, s16, s9\n" \
"v_mfma_f32_16x16x32_bf16 v[72:75], a[128:131], v[48:51], 0\n" \
"ds_read_b128 a[152:155], v5 offset:38912\n" \
"v_mfma_f32_16x16x32_bf16 v[72:75], a[132:135], v[52:55], v[72:75]\n" \
"s_add_u32 m0, s48, 0\n" \
"v_exp_f32 v113, v113\n" \
"v_mfma_f32_16x16x32_bf16 v[80:83], a[128:131], a[64:67], 0\n" \
"buffer_load_dwordx4 v6, s[40:43], s16 offen offset:0 sc1 lds\n" \
"v_mfma_f32_16x16x32_bf16 v[80:83], a[132:135], a[68:71], v[80:83]\n" \
"v_exp_f32 v114, v114\n" \
"v_mfma_f32_16x16x32_bf16 v[88:91], a[128:131], a[80:83], 0\n" \
"ds_read_b128 a[156:159], v5 offset:39936\n" \
"v_cvt_pk_bf16_f32 v152, v112, v113\n" \
"v_mfma_f32_16x16x32_bf16 v[88:91], a[132:135], a[84:87], v[88:91]\n" \
"s_waitcnt lgkmcnt(4)\n" \
"v_exp_f32 v115, v115\n" \
"v_mfma_f32_16x16x32_bf16 v[64:67], a[136:139], v[40:43], v[64:67]\n" \
"v_exp_f32 v116, v116\n" \
"v_mfma_f32_16x16x32_bf16 v[64:67], a[140:143], v[44:47], v[64:67]\n" \
"s_xor_b32 s49, s49, 0x10000\n" \
"v_cvt_pk_bf16_f32 v153, v114, v115\n" \
"v_mfma_f32_16x16x32_bf16 v[72:75], a[136:139], v[56:59], v[72:75]\n" \
"ds_read_b128 a[128:131], v5 offset:49152\n" \
"v_mfma_f32_16x16x32_bf16 v[72:75], a[140:143], v[60:63], v[72:75]\n" \
"v_exp_f32 v117, v117\n" \
"v_mfma_f32_16x16x32_bf16 v[80:83], a[136:139], a[72:75], v[80:83]\n" \
"buffer_load_dwordx4 v6, s[40:43], s16 offen offset:1024 sc1 lds\n" \
"v_mfma_f32_16x16x32_bf16 v[80:83], a[140:143], a[76:79], v[80:83]\n" \
"v_exp_f32 v118, v118\n" \
"v_mfma_f32_16x16x32_bf16 v[88:91], a[136:139], a[88:91], v[88:91]\n" \
"ds_read_b128 a[132:135], v5 offset:50176\n" \
"v_cvt_pk_bf16_f32 v154, v116, v117\n" \
"v_mfma_f32_16x16x32_bf16 v[88:91], a[140:143], a[92:95], v[88:91]\n" \
"s_waitcnt lgkmcnt(4)\n" \
"v_exp_f32 v119, v119\n" \
"v_mov_b32 v68, 0xff800000\n" \
"v_mov_b32 v69, 0xff800000\n" \
"v_exp_f32 v120, v120\n" \
"v_cvt_pk_bf16_f32 v155, v118, v119\n" \
"v_mfma_f32_16x16x32_bf16 v[76:79], a[144:147], v[48:51], v[176:179]\n" \
"ds_read_b128 a[136:139], v5 offset:51200\n" \
"v_mfma_f32_16x16x32_bf16 v[76:79], a[148:151], v[52:55], v[76:79]\n" \
"v_exp_f32 v121, v121\n" \
"v_mfma_f32_16x16x32_bf16 v[84:87], a[144:147], a[64:67], 0\n" \
"buffer_load_dwordx4 v6, s[40:43], s16 offen offset:2048 sc1 lds\n" \
"v_mfma_f32_16x16x32_bf16 v[84:87], a[148:151], a[68:71], v[84:87]\n" \
"v_exp_f32 v122, v122\n" \
"v_mfma_f32_16x16x32_bf16 v[92:95], a[144:147], a[80:83], 0\n" \
"ds_read_b128 a[140:143], v5 offset:52224\n" \
"v_cvt_pk_bf16_f32 v156, v120, v121\n" \
"v_mfma_f32_16x16x32_bf16 v[92:95], a[148:151], a[84:87], v[92:95]\n" \
"s_waitcnt lgkmcnt(4)\n" \
"v_exp_f32 v123, v123\n" \
"v_mov_b32 v70, 0xff800000\n" \
"v_mov_b32 v71, 0xff800000\n" \
"v_exp_f32 v124, v124\n" \
"v_cvt_pk_bf16_f32 v157, v122, v123\n" \
"v_mfma_f32_16x16x32_bf16 v[76:79], a[152:155], v[56:59], v[76:79]\n" \
"ds_read_b128 a[144:147], v5 offset:53248\n" \
"v_mfma_f32_16x16x32_bf16 v[76:79], a[156:159], v[60:63], v[76:79]\n" \
"v_exp_f32 v125, v125\n" \
"v_mfma_f32_16x16x32_bf16 v[84:87], a[152:155], a[72:75], v[84:87]\n" \
"buffer_load_dwordx4 v6, s[40:43], s16 offen offset:3072 sc1 lds\n" \
"v_mfma_f32_16x16x32_bf16 v[84:87], a[156:159], a[76:79], v[84:87]\n" \
"v_exp_f32 v126, v126\n" \
"v_mfma_f32_16x16x32_bf16 v[92:95], a[152:155], a[88:91], v[92:95]\n" \
"ds_read_b128 a[148:151], v5 offset:54272\n" \
"v_cvt_pk_bf16_f32 v158, v124, v125\n" \
"v_mfma_f32_16x16x32_bf16 v[92:95], a[156:159], a[92:95], v[92:95]\n" \
"s_waitcnt lgkmcnt(4)\n" \
"v_exp_f32 v127, v127\n" \
"v_smfmac_f32_16x16x64_bf16 v[160:163], v[180:183], v[144:151], v168\n" \
"v_cvt_pk_bf16_f32 v159, v126, v127\n" \
"v_mfma_f32_16x16x32_bf16 v[192:195], a[128:131], v[144:147], v[192:195]\n" \
"v_exp_f32 v64, v64\n" \
"v_mfma_f32_16x16x32_bf16 v[196:199], a[132:135], v[144:147], v[196:199]\n" \
"v_mfma_f32_16x16x32_bf16 v[224:227], a[128:131], v[148:151], v[224:227]\n" \
"ds_read_b128 a[152:155], v5 offset:55296\n" \
"v_mfma_f32_16x16x32_bf16 v[228:231], a[132:135], v[148:151], v[228:231]\n" \
"v_exp_f32 v65, v65\n" \
"v_mfma_f32_16x16x32_bf16 a[0:3], a[128:131], v[152:155], a[0:3]\n" \
"v_exp_f32 v66, v66\n" \
"v_mfma_f32_16x16x32_bf16 a[4:7], a[132:135], v[152:155], a[4:7]\n" \
"v_cvt_pk_bf16_f32 v128, v64, v65\n" \
"v_mfma_f32_16x16x32_bf16 a[32:35], a[128:131], v[156:159], a[32:35]\n" \
"ds_read_b128 a[156:159], v5 offset:56320\n" \
"v_mfma_f32_16x16x32_bf16 a[36:39], a[132:135], v[156:159], a[36:39]\n" \
"s_waitcnt vmcnt(12) lgkmcnt(4)\n" \
"v_exp_f32 v67, v67\n" \
"v_mfma_f32_16x16x32_bf16 v[200:203], a[136:139], v[144:147], v[200:203]\n" \
"s_barrier\n" \
"v_xor_b32 v5, 0x10000, v5\n" \
"v_cvt_pk_bf16_f32 v129, v66, v67\n" \
"v_mfma_f32_16x16x32_bf16 v[204:207], a[140:143], v[144:147], v[204:207]\n" \
"v_exp_f32 v68, v68\n" \
"v_mfma_f32_16x16x32_bf16 v[232:235], a[136:139], v[148:151], v[232:235]\n" \
"s_add_u32 s17, s17, s19\n" \
"ds_read_b128 a[128:131], v5 offset:0\n" \
"v_mfma_f32_16x16x32_bf16 v[236:239], a[140:143], v[148:151], v[236:239]\n" \
"v_exp_f32 v69, v69\n" \
"v_mfma_f32_16x16x32_bf16 a[8:11], a[136:139], v[152:155], a[8:11]\n" \
"s_add_u32 m0, s48, 16384\n" \
"v_exp_f32 v70, v70\n" \
"v_mfma_f32_16x16x32_bf16 a[12:15], a[140:143], v[152:155], a[12:15]\n" \
"buffer_load_dwordx4 v6, s[40:43], s17 offen offset:0 sc1 lds\n" \
"v_mfma_f32_16x16x32_bf16 a[40:43], a[136:139], v[156:159], a[40:43]\n" \
"ds_read_b128 a[132:135], v5 offset:1024\n" \
"v_cvt_pk_bf16_f32 v130, v68, v69\n" \
"v_mfma_f32_16x16x32_bf16 a[44:47], a[140:143], v[156:159], a[44:47]\n" \
"s_waitcnt lgkmcnt(4)\n" \
"v_exp_f32 v71, v71\n" \
"v_mfma_f32_16x16x32_bf16 v[208:211], a[144:147], v[144:147], v[208:211]\n" \
"v_exp_f32 v72, v72\n" \
"v_mfma_f32_16x16x32_bf16 v[212:215], a[148:151], v[144:147], v[212:215]\n" \
"v_cvt_pk_bf16_f32 v131, v70, v71\n" \
"v_mfma_f32_16x16x32_bf16 v[240:243], a[144:147], v[148:151], v[240:243]\n" \
"ds_read_b128 a[136:139], v5 offset:2048\n" \
"v_mfma_f32_16x16x32_bf16 v[244:247], a[148:151], v[148:151], v[244:247]\n" \
"v_exp_f32 v73, v73\n" \
"v_mfma_f32_16x16x32_bf16 a[16:19], a[144:147], v[152:155], a[16:19]\n" \
"v_exp_f32 v74, v74\n" \
"v_mfma_f32_16x16x32_bf16 a[20:23], a[148:151], v[152:155], a[20:23]\n" \
"buffer_load_dwordx4 v6, s[40:43], s17 offen offset:1024 sc1 lds\n" \
"v_mfma_f32_16x16x32_bf16 a[48:51], a[144:147], v[156:159], a[48:51]\n" \
"ds_read_b128 a[140:143], v5 offset:3072\n" \
"v_cvt_pk_bf16_f32 v132, v72, v73\n" \
"v_mfma_f32_16x16x32_bf16 a[52:55], a[148:151], v[156:159], a[52:55]\n" \
"s_waitcnt lgkmcnt(4)\n" \
"v_exp_f32 v75, v75\n" \
"v_mfma_f32_16x16x32_bf16 v[216:219], a[152:155], v[144:147], v[216:219]\n" \
"v_exp_f32 v76, v76\n" \
"v_mfma_f32_16x16x32_bf16 v[220:223], a[156:159], v[144:147], v[220:223]\n" \
"s_add_u32 s44, s44, 1\n" \
"buffer_load_dwordx4 v6, s[40:43], s17 offen offset:2048 sc1 lds\n" \
"v_mfma_f32_16x16x32_bf16 v[248:251], a[152:155], v[148:151], v[248:251]\n" \
"ds_read_b128 a[144:147], v5 offset:4096\n" \
"v_cvt_pk_bf16_f32 v133, v74, v75\n" \
"v_mfma_f32_16x16x32_bf16 v[252:255], a[156:159], v[148:151], v[252:255]\n" \
"LH21_%=:\ns_cbranch_execz LHR1_%=\n" \
"v_exp_f32 v77, v77\n" \
"v_mfma_f32_16x16x32_bf16 a[24:27], a[152:155], v[152:155], a[24:27]\n" \
"v_exp_f32 v78, v78\n" \
"v_mfma_f32_16x16x32_bf16 a[28:31], a[156:159], v[152:155], a[28:31]\n" \
"s_xor_b32 s48, s48, 0x10000\n" \
"buffer_load_dwordx4 v6, s[40:43], s17 offen offset:3072 sc1 lds\n" \
"v_mfma_f32_16x16x32_bf16 a[56:59], a[152:155], v[156:159], a[56:59]\n" \
"ds_read_b128 a[148:151], v5 offset:5120\n" \
"v_cvt_pk_bf16_f32 v134, v76, v77\n" \
"v_mfma_f32_16x16x32_bf16 a[60:63], a[156:159], v[156:159], a[60:63]\n" \
"s_waitcnt lgkmcnt(4)\n" \
"v_exp_f32 v79, v79\n" \
"v_smfmac_f32_16x16x64_bf16 v[164:167], v[180:183], v[152:159], v168\n" \
"v_cvt_pk_bf16_f32 v135, v78, v79\n" \
"s_cmp_ge_u32 %2, 2\n" \
"s_cbranch_scc1 LFIN_%=\n" \
"s_cmp_eq_u32 s44, 15\ns_cselect_b32 s16, s56, s16\ns_cselect_b32 s9, s57, s9\ns_cselect_b32 s17, s58, s17\ns_cselect_b32 s19, s59, s19\n" \
"v_mfma_f32_16x16x32_bf16 v[112:115], a[128:131], a[64:67], v[176:179]\n" \
"v_xor_b32 v8, 0x2000, v5\n" \
"ds_read_b128 v[32:35], v21 offset:0\n" \
"v_mfma_f32_16x16x32_bf16 v[112:115], a[132:135], a[68:71], v[112:115]\n" \
"s_add_u32 s16, s16, s9\n" \
"ds_read_b128 a[152:155], v5 offset:6144\n" \
"s_add_u32 m0, s49, 32768\n" \
"v_mfma_f32_16x16x32_bf16 v[120:123], a[128:131], a[80:83], 0\n" \
"buffer_load_dwordx4 v6, s[40:43], s16 offen offset:0 sc1 lds\n" \
"v_exp_f32 v80, v80\n" \
"v_mfma_f32_16x16x32_bf16 v[120:123], a[132:135], a[84:87], v[120:123]\n" \
"s_waitcnt lgkmcnt(4)\n" \
"ds_read_b128 a[156:159], v5 offset:7168\n" \
"v_exp_f32 v81, v81\n" \
"v_mfma_f32_16x16x32_bf16 v[112:115], a[136:139], a[72:75], v[112:115]\n" \
"ds_read_b128 a[128:131], v5 offset:16384\n" \
"v_exp_f32 v82, v82\n" \
"v_mfma_f32_16x16x32_bf16 v[112:115], a[140:143], a[76:79], v[112:115]\n" \
"ds_read_b128 a[132:135], v5 offset:17408\n" \
"v_exp_f32 v83, v83\n" \
"v_mfma_f32_16x16x32_bf16 v[120:123], a[136:139], a[88:91], v[120:123]\n" \
"v_exp_f32 v84, v84\n" \
"v_mfma_f32_16x16x32_bf16 v[120:123], a[140:143], a[92:95], v[120:123]\n" \
"s_waitcnt lgkmcnt(5)\n" \
"ds_read_b128 a[136:139], v5 offset:18432\n" \
"v_exp_f32 v85, v85\n" \
"v_mfma_f32_16x16x32_bf16 v[124:127], a[144:147], a[80:83], v[176:179]\n" \
"buffer_load_dwordx4 v6, s[40:43], s16 offen offset:1024 sc1 lds\n" \
"v_exp_f32 v86, v86\n" \
"v_mfma_f32_16x16x32_bf16 v[124:127], a[148:151], a[84:87], v[124:127]\n" \
"s_waitcnt lgkmcnt(3)\n" \
"ds_read_b128 a[140:143], v5 offset:19456\n" \
"v_exp_f32 v87, v87\n" \
"v_mfma_f32_16x16x32_bf16 v[124:127], a[152:155], a[88:91], v[124:127]\n" \
"ds_read_b128 a[144:147], v5 offset:20480\n" \
"v_exp_f32 v88, v88\n" \
"v_mfma_f32_16x16x32_bf16 v[124:127], a[156:159], a[92:95], v[124:127]\n" \
"ds_read_b128 a[148:151], v5 offset:21504\n" \
"v_exp_f32 v89, v89\n" \
"ds_read_b128 a[152:155], v5 offset:22528\n" \
"v_exp_f32 v90, v90\n" \
"v_cvt_pk_bf16_f32 v136, v80, v81\n" \
"buffer_load_dwordx4 v6, s[40:43], s16 offen offset:2048 sc1 lds\n" \
"v_exp_f32 v91, v91\n" \
"ds_read_b128 a[156:159], v5 offset:23552\n" \
"v_exp_f32 v92, v92\n" \
"v_cvt_pk_bf16_f32 v137, v82, v83\n" \
"v_exp_f32 v93, v93\n" \
"v_exp_f32 v94, v94\n" \
"v_exp_f32 v95, v95\n" \
"v_cvt_pk_bf16_f32 v138, v84, v85\n" \
"v_cvt_pk_bf16_f32 v139, v86, v87\n" \
"v_smfmac_f32_16x16x64_bf16 v[160:163], v[180:183], v[128:135], v168\n" \
"v_exp_f32 v112, v112\n" \
"v_exp_f32 v113, v113\n" \
"v_cvt_pk_bf16_f32 v140, v88, v89\n" \
"v_cvt_pk_bf16_f32 v141, v90, v91\n" \
"v_cvt_pk_bf16_f32 v142, v92, v93\n" \
"v_cvt_pk_bf16_f32 v143, v94, v95\n" \
"buffer_load_dwordx4 v6, s[40:43], s16 offen offset:3072 sc1 lds\n" \
"v_exp_f32 v114, v114\n" \
"v_smfmac_f32_16x16x64_bf16 v[164:167], v[180:183], v[136:143], v168\n" \
"s_waitcnt lgkmcnt(6)\n" \
"v_cvt_pk_bf16_f32 v152, v112, v113\n" \
"v_mfma_f32_16x16x32_bf16 v[192:195], a[128:131], v[128:131], v[192:195]\n" \
"v_exp_f32 v115, v115\n" \
"v_mfma_f32_16x16x32_bf16 v[196:199], a[132:135], v[128:131], v[196:199]\n" \
"v_exp_f32 v120, v120\n" \
"v_mfma_f32_16x16x32_bf16 v[224:227], a[128:131], v[132:135], v[224:227]\n" \
"v_exp_f32 v121, v121\n" \
"v_mfma_f32_16x16x32_bf16 v[228:231], a[132:135], v[132:135], v[228:231]\n" \
"v_exp_f32 v122, v122\n" \
"v_mfma_f32_16x16x32_bf16 a[0:3], a[128:131], v[136:139], a[0:3]\n" \
"v_exp_f32 v123, v123\n" \
"v_mfma_f32_16x16x32_bf16 a[4:7], a[132:135], v[136:139], a[4:7]\n" \
"v_exp_f32 v124, v124\n" \
"v_mfma_f32_16x16x32_bf16 a[32:35], a[128:131], v[140:143], a[32:35]\n" \
"v_exp_f32 v125, v125\n" \
"v_mfma_f32_16x16x32_bf16 a[36:39], a[132:135], v[140:143], a[36:39]\n" \
"s_waitcnt lgkmcnt(4)\n" \
"v_exp_f32 v126, v126\n" \
"v_mfma_f32_16x16x32_bf16 v[200:203], a[136:139], v[128:131], v[200:203]\n" \
"v_exp_f32 v127, v127\n" \
"v_mfma_f32_16x16x32_bf16 v[204:207], a[140:143], v[128:131], v[204:207]\n" \
"v_cvt_pk_bf16_f32 v153, v114, v115\n" \
"v_mov_b32 v154, 0\n" \
"v_mfma_f32_16x16x32_bf16 v[232:235], a[136:139], v[132:135], v[232:235]\n" \
"v_mov_b32 v155, 0\n" \
"v_cvt_pk_bf16_f32 v156, v120, v121\n" \
"v_mfma_f32_16x16x32_bf16 v[236:239], a[140:143], v[132:135], v[236:239]\n" \
"v_cvt_pk_bf16_f32 v157, v122, v123\n" \
"ds_read_b128 v[36:39], v21 offset:1024\n" \
"v_mfma_f32_16x16x32_bf16 a[8:11], a[136:139], v[136:139], a[8:11]\n" \
"v_cvt_pk_bf16_f32 v158, v124, v125\n" \
"v_cvt_pk_bf16_f32 v159, v126, v127\n" \
"v_mfma_f32_16x16x32_bf16 a[12:15], a[140:143], v[136:139], a[12:15]\n" \
"ds_read_b128 v[40:43], v21 offset:2048\n" \
"v_add_f32 v160, v160, v161\n" \
"v_mfma_f32_16x16x32_bf16 a[40:43], a[136:139], v[140:143], a[40:43]\n" \
"ds_read_b128 v[44:47], v21 offset:3072\n" \
"v_add_f32 v162, v162, v163\n" \
"v_mfma_f32_16x16x32_bf16 a[44:47], a[140:143], v[140:143], a[44:47]\n" \
"s_waitcnt lgkmcnt(5)\n" \
"ds_read_b128 v[48:51], v21 offset:4096\n" \
"v_subrev_u32 v23, 0x1f800000, v160\n" \
"v_mfma_f32_16x16x32_bf16 v[208:211], a[144:147], v[128:131], v[208:211]\n" \
"ds_read_b128 v[52:55], v21 offset:5120\n" \
"v_subrev_u32 v27, 0x1f800000, v162\n" \
"v_mfma_f32_16x16x32_bf16 v[212:215], a[148:151], v[128:131], v[212:215]\n" \
"ds_read_b128 v[56:59], v21 offset:6144\n" \
"v_max_u32 v23, v23, v27\n" \
"v_mfma_f32_16x16x32_bf16 v[240:243], a[144:147], v[132:135], v[240:243]\n" \
"s_add_u32 s17, s17, s19\n" \
"s_add_u32 m0, s49, 49152\n" \
"ds_read_b128 v[60:63], v21 offset:7168\n" \
"v_mfma_f32_16x16x32_bf16 v[244:247], a[148:151], v[132:135], v[244:247]\n" \
"s_waitcnt vmcnt(12) lgkmcnt(15)\n" \
"ds_read_b128 a[64:67], v21 offset:16384\n" \
"v_cmp_le_u32 vcc, 0x40000000, v23\n" \
"v_mfma_f32_16x16x32_bf16 a[16:19], a[144:147], v[136:139], a[16:19]\n" \
"s_barrier\n" \
"ds_read_b128 a[68:71], v21 offset:17408\n" \
"s_nop 0\n" \
"v_mfma_f32_16x16x32_bf16 a[20:23], a[148:151], v[136:139], a[20:23]\n" \
"buffer_load_dwordx4 v6, s[40:43], s17 offen offset:0 sc1 lds\n" \
"v_mfma_f32_16x16x32_bf16 a[48:51], a[144:147], v[140:143], a[48:51]\n" \
"s_or_b64 s[54:55], s[54:55], vcc\n" \
"ds_read_b128 a[128:131], v5 offset:49152\n" \
"v_mfma_f32_16x16x32_bf16 a[52:55], a[148:151], v[140:143], a[52:55]\n" \
"s_waitcnt lgkmcnt(10)\n" \
"ds_read_b128 a[132:135], v5 offset:50176\n" \
"ds_read_b128 a[72:75], v21 offset:18432\n" \
"ds_read_b128 a[76:79], v21 offset:19456\n" \
"ds_read_b128 a[80:83], v21 offset:20480\n" \
"ds_read_b128 a[84:87], v21 offset:21504\n" \
"ds_read_b128 a[88:91], v21 offset:22528\n" \
"ds_read_b128 a[92:95], v21 offset:23552\n" \
"v_mfma_f32_16x16x32_bf16 v[216:219], a[152:155], v[128:131], v[216:219]\n" \
"buffer_load_dwordx4 v6, s[40:43], s17 offen offset:1024 sc1 lds\n" \
"v_mfma_f32_16x16x32_bf16 v[220:223], a[156:159], v[128:131], v[220:223]\n" \
"ds_read_b128 a[136:139], v5 offset:51200\n" \
"v_mfma_f32_16x16x32_bf16 v[248:251], a[152:155], v[132:135], v[248:251]\n" \
"buffer_load_dwordx4 v6, s[40:43], s17 offen offset:2048 sc1 lds\n" \
"v_mfma_f32_16x16x32_bf16 v[252:255], a[156:159], v[132:135], v[252:255]\n" \
"ds_read_b128 a[140:143], v5 offset:52224\n" \
"v_mfma_f32_16x16x32_bf16 a[24:27], a[152:155], v[136:139], a[24:27]\n" \
"buffer_load_dwordx4 v6, s[40:43], s17 offen offset:3072 sc1 lds\n" \
"v_mfma_f32_16x16x32_bf16 a[28:31], a[156:159], v[136:139], a[28:31]\n" \
"ds_read_b128 a[144:147], v5 offset:53248\n" \
"v_mfma_f32_16x16x32_bf16 a[56:59], a[152:155], v[140:143], a[56:59]\n" \
"ds_read_b128 a[148:151], v5 offset:54272\n" \
"v_mfma_f32_16x16x32_bf16 a[60:63], a[156:159], v[140:143], a[60:63]\n" \
"ds_read_b128 a[152:155], v5 offset:55296\n" \
"v_rcp_f32 v22, v160\n" \
"v_rcp_f32 v26, v162\n" \
"LS3_%=:\n" \
"v_smfmac_f32_16x16x64_bf16 v[164:167], v[180:183], v[152:159], v168\n" \
"s_add_u32 s16, s16, s9\n" \
"ds_read_b128 a[156:159], v5 offset:56320\n" \
"s_waitcnt lgkmcnt(12)\n" \
"s_add_u32 m0, s48, 0\n" \
"v_mul_f32 v192, v22, v192\nv_mul_f32 v193, v22, v193\n" \
"v_mfma_f32_16x16x32_bf16 a[0:3], a[128:131], v[152:155], a[0:3]\n" \
"buffer_load_dwordx4 v6, s[40:43], s16 offen offset:0 sc1 lds\n" \
"v_mfma_f32_16x16x32_bf16 a[4:7], a[132:135], v[152:155], a[4:7]\n" \
"v_mul_f32 v194, v22, v194\nv_mul_f32 v195, v22, v195\n" \
"v_mfma_f32_16x16x32_bf16 a[32:35], a[128:131], v[156:159], a[32:35]\n" \
"v_mul_f32 v196, v22, v196\nv_mul_f32 v197, v22, v197\n" \
"v_mfma_f32_16x16x32_bf16 a[36:39], a[132:135], v[156:159], a[36:39]\n" \
"s_waitcnt lgkmcnt(4)\n" \
"ds_read_b128 a[128:131], v8 offset:32768\n" \
"v_mfma_f32_16x16x32_bf16 a[8:11], a[136:139], v[152:155], a[8:11]\n" \
"buffer_load_dwordx4 v6, s[40:43], s16 offen offset:1024 sc1 lds\n" \
"v_mfma_f32_16x16x32_bf16 a[12:15], a[140:143], v[152:155], a[12:15]\n" \
"ds_read_b128 a[132:135], v8 offset:33792\n" \
"v_mfma_f32_16x16x32_bf16 a[40:43], a[136:139], v[156:159], a[40:43]\n" \
"v_mul_f32 v198, v22, v198\nv_mul_f32 v199, v22, v199\n" \
"v_mfma_f32_16x16x32_bf16 a[44:47], a[140:143], v[156:159], a[44:47]\n" \
"s_waitcnt lgkmcnt(4)\n" \
"ds_read_b128 a[136:139], v8 offset:34816\n" \
"v_cvt_pk_bf16_f32 v192, v192, v193\n" \
"v_mfma_f32_16x16x32_bf16 a[16:19], a[144:147], v[152:155], a[16:19]\n" \
"ds_read_b128 a[140:143], v8 offset:35840\n" \
"v_cvt_pk_bf16_f32 v193, v194, v195\n" \
"v_mfma_f32_16x16x32_bf16 a[20:23], a[148:151], v[152:155], a[20:23]\n" \
"v_cvt_pk_bf16_f32 v194, v196, v197\n" \
"v_cvt_pk_bf16_f32 v195, v198, v199\n" \
"v_mfma_f32_16x16x32_bf16 a[48:51], a[144:147], v[156:159], a[48:51]\n" \
"buffer_load_dwordx4 v6, s[40:43], s16 offen offset:2048 sc1 lds\n" \
"v_mfma_f32_16x16x32_bf16 a[52:55], a[148:151], v[156:159], a[52:55]\n" \
"s_waitcnt lgkmcnt(4)\n" \
"ds_read_b128 a[144:147], v8 offset:36864\n" \
"v_accvgpr_read_b32 v80, a0\n" \
"v_mfma_f32_16x16x32_bf16 a[24:27], a[152:155], v[152:155], a[24:27]\n" \
"buffer_store_dwordx4 v[192:195], v4, s[36:39], s50 offen offset:0 sc1\n" \
"v_mfma_f32_16x16x32_bf16 a[28:31], a[156:159], v[152:155], a[28:31]\n" \
"ds_read_b128 a[148:151], v8 offset:37888\n" \
"v_accvgpr_read_b32 v81, a1\n" \
"v_mfma_f32_16x16x32_bf16 a[56:59], a[152:155], v[156:159], a[56:59]\n" \
"v_mul_f32 v224, v26, v224\nv_mul_f32 v225, v26, v225\n" \
"v_mfma_f32_16x16x32_bf16 a[60:63], a[156:159], v[156:159], a[60:63]\n" \
"ds_read_b128 a[152:155], v8 offset:38912\n" \
"v_accvgpr_read_b32 v82, a2\n" \
"buffer_load_dwordx4 v6, s[40:43], s16 offen offset:3072 sc1 lds\n" \
"v_mul_f32 v226, v26, v226\nv_mul_f32 v227, v26, v227\n" \
"ds_read_b128 a[156:159], v8 offset:39936\n" \
"v_pk_mul_f32 v[228:229], v[228:229], v[26:27] op_sel_hi:[1,0]\n" \
"v_pk_mul_f32 v[230:231], v[230:231], v[26:27] op_sel_hi:[1,0]\n" \
"v_cvt_pk_bf16_f32 v224, v224, v225\n" \
"v_cvt_pk_bf16_f32 v225, v226, v227\n" \
"v_cvt_pk_bf16_f32 v226, v228, v229\n" \
"v_cvt_pk_bf16_f32 v227, v230, v231\n" \
"v_pk_mul_f32 v[200:201], v[200:201], v[22:23] op_sel_hi:[1,0]\n" \
"buffer_store_dwordx4 v[224:227], v4, s[36:39], s50 offen offset:1024 sc1\n" \
"v_pk_mul_f32 v[202:203], v[202:203], v[22:23] op_sel_hi:[1,0]\n" \
"v_pk_mul_f32 v[204:205], v[204:205], v[22:23] op_sel_hi:[1,0]\n" \
"v_pk_mul_f32 v[206:207], v[206:207], v[22:23] op_sel_hi:[1,0]\n" \
"v_cvt_pk_bf16_f32 v200, v200, v201\n" \
"v_cvt_pk_bf16_f32 v201, v202, v203\n" \
"v_cvt_pk_bf16_f32 v202, v204, v205\n" \
"v_cvt_pk_bf16_f32 v203, v206, v207\n" \
"v_pk_mul_f32 v[232:233], v[232:233], v[26:27] op_sel_hi:[1,0]\n" \
"v_pk_mul_f32 v[234:235], v[234:235], v[26:27] op_sel_hi:[1,0]\n" \
"v_pk_mul_f32 v[236:237], v[236:237], v[26:27] op_sel_hi:[1,0]\n" \
"v_pk_mul_f32 v[238:239], v[238:239], v[26:27] op_sel_hi:[1,0]\n" \
"v_cvt_pk_bf16_f32 v232, v232, v233\n" \
"v_cvt_pk_bf16_f32 v233, v234, v235\n" \
"v_cvt_pk_bf16_f32 v234, v236, v237\n" \
"buffer_store_dwordx4 v[200:203], v4, s[36:39], s50 offen offset:2048 sc1\n" \
"v_cvt_pk_bf16_f32 v235, v238, v239\n" \
"v_pk_mul_f32 v[208:209], v[208:209], v[22:23] op_sel_hi:[1,0]\n" \
"v_pk_mul_f32 v[210:211], v[210:211], v[22:23] op_sel_hi:[1,0]\n" \
"v_pk_mul_f32 v[212:213], v[212:213], v[22:23] op_sel_hi:[1,0]\n" \
"v_pk_mul_f32 v[214:215], v[214:215], v[22:23] op_sel_hi:[1,0]\n" \
"v_cvt_pk_bf16_f32 v208, v208, v209\n" \
"v_cvt_pk_bf16_f32 v209, v210, v211\n" \
"v_cvt_pk_bf16_f32 v210, v212, v213\n" \
"v_cvt_pk_bf16_f32 v211, v214, v215\n" \
"v_pk_mul_f32 v[240:241], v[240:241], v[26:27] op_sel_hi:[1,0]\n" \
"v_pk_mul_f32 v[242:243], v[242:243], v[26:27] op_sel_hi:[1,0]\n" \
"v_pk_mul_f32 v[244:245], v[244:245], v[26:27] op_sel_hi:[1,0]\n" \
"v_pk_mul_f32 v[246:247], v[246:247], v[26:27] op_sel_hi:[1,0]\n" \
"v_cvt_pk_bf16_f32 v240, v240, v241\n" \
"buffer_store_dwordx4 v[232:235], v4, s[36:39], s50 offen offset:3072 sc1\n" \
"v_cvt_pk_bf16_f32 v241, v242, v243\n" \
"v_cvt_pk_bf16_f32 v242, v244, v245\n" \
"v_cvt_pk_bf16_f32 v243, v246, v247\n" \
"v_pk_mul_f32 v[216:217], v[216:217], v[22:23] op_sel_hi:[1,0]\n" \
"v_pk_mul_f32 v[218:219], v[218:219], v[22:23] op_sel_hi:[1,0]\n" \
"v_pk_mul_f32 v[220:221], v[220:221], v[22:23] op_sel_hi:[1,0]\n" \
"v_pk_mul_f32 v[222:223], v[222:223], v[22:23] op_sel_hi:[1,0]\n" \
"v_cvt_pk_bf16_f32 v216, v216, v217\n" \
"v_cvt_pk_bf16_f32 v217, v218, v219\n" \
"v_cvt_pk_bf16_f32 v218, v220, v221\n" \
"s_waitcnt lgkmcnt(6)\n" \
"v_cvt_pk_bf16_f32 v219, v222, v223\n" \
"v_pk_mul_f32 v[248:249], v[248:249], v[26:27] op_sel_hi:[1,0]\n" \
"v_pk_mul_f32 v[250:251], v[250:251], v[26:27] op_sel_hi:[1,0]\n" \
"v_mfma_f32_16x16x32_bf16 v[64:67], a[128:131], v[32:35], v[176:179]\n" \
"buffer_store_dwordx4 v[208:211], v7, s[36:39], s50 offen offset:0 sc1\n" \
"v_mfma_f32_16x16x32_bf16 v[64:67], a[132:135], v[36:39], v[64:67]\n" \
"v_mul_f32 v252, v26, v252\nv_mul_f32 v253, v26, v253\n" \
"v_mfma_f32_16x16x32_bf16 v[72:75], a[128:131], v[48:51], 0\n" \
"v_mul_f32 v254, v26, v254\nv_mul_f32 v255, v26, v255\n" \
"v_mfma_f32_16x16x32_bf16 v[72:75], a[132:135], v[52:55], v[72:75]\n" \
"s_waitcnt lgkmcnt(4)\n" \
"v_cvt_pk_bf16_f32 v248, v248, v249\n" \
"v_cvt_pk_bf16_f32 v249, v250, v251\n" \
"v_mfma_f32_16x16x32_bf16 v[64:67], a[136:139], v[40:43], v[64:67]\n" \
"buffer_store_dwordx4 v[240:243], v7, s[36:39], s50 offen offset:1024 sc1\n" \
"v_mfma_f32_16x16x32_bf16 v[64:67], a[140:143], v[44:47], v[64:67]\n" \
"s_add_u32 s63, s50, 262144\n" \
"v_cvt_pk_bf16_f32 v250, v252, v253\n" \
"v_cvt_pk_bf16_f32 v251, v254, v255\n" \
"v_mfma_f32_16x16x32_bf16 v[72:75], a[136:139], v[56:59], v[72:75]\n" \
"v_add_f32 v164, v164, v165\n" \
"v_add_f32 v166, v166, v167\n" \
"v_mfma_f32_16x16x32_bf16 v[72:75], a[140:143], v[60:63], v[72:75]\n" \
"s_waitcnt lgkmcnt(2)\n" \
"v_subrev_u32 v23, 0x1f800000, v164\n" \
"v_mfma_f32_16x16x32_bf16 v[76:79], a[144:147], v[48:51], v[176:179]\n" \
"buffer_store_dwordx4 v[216:219], v7, s[36:39], s50 offen offset:2048 sc1\n" \
"v_mfma_f32_16x16x32_bf16 v[76:79], a[148:151], v[52:55], v[76:79]\n" \
"s_waitcnt lgkmcnt(0)\n" \
"v_subrev_u32 v27, 0x1f800000, v166\n" \
"v_max_u32 v23, v23, v27\n" \
"v_mfma_f32_16x16x32_bf16 v[76:79], a[152:155], v[56:59], v[76:79]\n" \
"v_cmp_le_u32 vcc, 0x40000000, v23\n" \
"s_nop 0\n" \
"v_mfma_f32_16x16x32_bf16 v[76:79], a[156:159], v[60:63], v[76:79]\n" \
"s_or_b64 s[54:55], s[54:55], vcc\n" \
"v_rcp_f32 v188, v164\n" \
"buffer_store_dwordx4 v[248:251], v7, s[36:39], s50 offen offset:3072 sc1\n" \
"v_rcp_f32 v24, v166\n" \
"v_accvgpr_read_b32 v83, a3\n" \
"v_accvgpr_read_b32 v84, a4\n" \
"v_accvgpr_read_b32 v85, a5\n" \
"v_accvgpr_read_b32 v86, a6\n" \
"v_exp_f32 v64, v64\n" \
"v_exp_f32 v65, v65\n" \
"v_exp_f32 v66, v66\n" \
"v_exp_f32 v67, v67\n" \
"v_exp_f32 v72, v72\n" \
"v_exp_f32 v73, v73\n" \
"v_exp_f32 v74, v74\n" \
"v_exp_f32 v75, v75\n" \
"v_exp_f32 v76, v76\n" \
"v_exp_f32 v77, v77\n" \
"v_exp_f32 v78, v78\n" \
"v_exp_f32 v79, v79\n" \
"s_add_u32 s17, s17, s19\n" \
"s_add_u32 m0, s48, 16384\n" \
"v_cvt_pk_bf16_f32 v128, v64, v65\n" \
"v_cvt_pk_bf16_f32 v129, v66, v67\n" \
"s_waitcnt vmcnt(20) lgkmcnt(0)\n" \
"v_mov_b32 v130, 0\n" \
"v_mov_b32 v131, 0\n" \
"v_cvt_pk_bf16_f32 v132, v72, v73\n" \
"s_barrier\n" \
"v_xor_b32 v5, 0x12000, v5\n" \
"v_cvt_pk_bf16_f32 v133, v74, v75\n" \
"v_cvt_pk_bf16_f32 v134, v76, v77\n" \
"buffer_load_dwordx4 v6, s[40:43], s17 offen offset:0 sc1 lds\n" \
"v_cvt_pk_bf16_f32 v135, v78, v79\n" \
"v_accvgpr_read_b32 v87, a7\n" \
"ds_read_b128 a[128:131], v5 offset:0\n" \
"v_pk_mul_f32 v[80:81], v[80:81], v[188:189] op_sel_hi:[1,0]\n" \
"v_pk_mul_f32 v[82:83], v[82:83], v[188:189] op_sel_hi:[1,0]\n" \
"v_pk_mul_f32 v[84:85], v[84:85], v[188:189] op_sel_hi:[1,0]\n" \
"buffer_load_dwordx4 v6, s[40:43], s17 offen offset:1024 sc1 lds\n" \
"v_pk_mul_f32 v[86:87], v[86:87], v[188:189] op_sel_hi:[1,0]\n" \
"v_cvt_pk_bf16_f32 v80, v80, v81\n" \
"ds_read_b128 a[132:135], v5 offset:1024\n" \
"v_cvt_pk_bf16_f32 v81, v82, v83\n" \
"v_cvt_pk_bf16_f32 v82, v84, v85\n" \
"v_cvt_pk_bf16_f32 v83, v86, v87\n" \
"buffer_load_dwordx4 v6, s[40:43], s17 offen offset:2048 sc1 lds\n" \
"v_accvgpr_read_b32 v88, a32\n" \
"v_accvgpr_read_b32 v89, a33\n" \
"ds_read_b128 a[136:139], v5 offset:2048\n" \
"v_accvgpr_read_b32 v90, a34\n" \
"v_accvgpr_read_b32 v91, a35\n" \
"v_accvgpr_read_b32 v92, a36\n" \
"s_xor_b32 s48, s48, 0x10000\n" \
"buffer_load_dwordx4 v6, s[40:43], s17 offen offset:3072 sc1 lds\n" \
"s_xor_b32 s49, s49, 0x10000\n" \
"s_add_u32 s44, s44, 1\n" \
"ds_read_b128 a[140:143], v5 offset:3072\n" \
"v_accvgpr_read_b32 v93, a37\n" \
"v_accvgpr_read_b32 v94, a38\n" \
"buffer_store_dwordx4 v[80:83], v4, s[36:39], s63 offen offset:0 sc1\n" \
"v_accvgpr_read_b32 v95, a39\n" \
"v_accvgpr_read_b32 v136, a8\n" \
"ds_read_b128 a[144:147], v5 offset:4096\n" \
"v_pk_mul_f32 v[88:89], v[88:89], v[24:25] op_sel_hi:[1,0]\n" \
"v_pk_mul_f32 v[90:91], v[90:91], v[24:25] op_sel_hi:[1,0]\n" \
"v_pk_mul_f32 v[92:93], v[92:93], v[24:25] op_sel_hi:[1,0]\n" \
"ds_read_b128 a[148:151], v5 offset:5120\n" \
"v_pk_mul_f32 v[94:95], v[94:95], v[24:25] op_sel_hi:[1,0]\n" \
"v_cvt_pk_bf16_f32 v88, v88, v89\n" \
"v_cvt_pk_bf16_f32 v89, v90, v91\n" \
"ds_read_b128 a[152:155], v5 offset:6144\n" \
"v_cvt_pk_bf16_f32 v90, v92, v93\n" \
"v_cvt_pk_bf16_f32 v91, v94, v95\n" \
"v_accvgpr_read_b32 v137, a9\n" \
"buffer_store_dwordx4 v[88:91], v4, s[36:39], s63 offen offset:1024 sc1\n" \
"v_accvgpr_read_b32 v138, a10\n" \
"v_accvgpr_read_b32 v139, a11\n" \
"s_cmp_eq_u32 s44, 16\n" \
"s_cbranch_scc1 LAD32_%=\n" \
"s_cmp_eq_u32 s44, 15\ns_cselect_b32 s16, s56, s16\ns_cselect_b32 s9, s57, s9\ns_cselect_b32 s17, s58, s17\ns_cselect_b32 s19, s59, s19\n" \
"s_add_u32 s16, s16, s9\n" \
"ds_read_b128 a[156:159], v5 offset:7168\n" \
"s_add_u32 m0, s49, 32768\n" \
"v_accvgpr_read_b32 v140, a12\n" \
"s_waitcnt lgkmcnt(6)\n" \
"buffer_load_dwordx4 v6, s[40:43], s16 offen offset:0 sc1 lds\n" \
"v_accvgpr_read_b32 v141, a13\n" \
"v_mfma_f32_16x16x32_bf16 v[96:99], a[128:131], v[32:35], 0\n" \
"v_accvgpr_read_b32 v142, a14\n" \
"v_accvgpr_read_b32 v143, a15\n" \
"v_mfma_f32_16x16x32_bf16 v[96:99], a[132:135], v[36:39], v[96:99]\n" \
"v_mul_f32 v136, v188, v136\nv_mul_f32 v137, v188, v137\n" \
"v_mfma_f32_16x16x32_bf16 v[104:107], a[128:131], v[48:51], 0\n" \
"v_mul_f32 v138, v188, v138\nv_mul_f32 v139, v188, v139\n" \
"v_mfma_f32_16x16x32_bf16 v[104:107], a[132:135], v[52:55], v[104:107]\n" \
"v_mul_f32 v140, v188, v140\nv_mul_f32 v141, v188, v141\n" \
"v_mfma_f32_16x16x32_bf16 v[112:115], a[128:131], a[64:67], v[176:179]\n" \
"buffer_load_dwordx4 v6, s[40:43], s16 offen offset:1024 sc1 lds\n" \
"v_mfma_f32_16x16x32_bf16 v[112:115], a[132:135], a[68:71], v[112:115]\n" \
"v_mul_f32 v142, v188, v142\nv_mul_f32 v143, v188, v143\n" \
"v_mfma_f32_16x16x32_bf16 v[120:123], a[128:131], a[80:83], 0\n" \
"v_cvt_pk_bf16_f32 v136, v136, v137\n" \
"v_cvt_pk_bf16_f32 v137, v138, v139\n" \
"v_mfma_f32_16x16x32_bf16 v[120:123], a[132:135], a[84:87], v[120:123]\n" \
"s_waitcnt lgkmcnt(4)\n" \
"ds_read_b128 a[128:131], v5 offset:16384\n" \
"v_cvt_pk_bf16_f32 v138, v140, v141\n" \
"v_mfma_f32_16x16x32_bf16 v[96:99], a[136:139], v[40:43], v[96:99]\n" \
"ds_read_b128 a[132:135], v5 offset:17408\n" \
"v_cvt_pk_bf16_f32 v139, v142, v143\n" \
"v_mfma_f32_16x16x32_bf16 v[96:99], a[140:143], v[44:47], v[96:99]\n" \
"buffer_store_dwordx4 v[136:139], v4, s[36:39], s63 offen offset:2048 sc1\n" \
"v_mfma_f32_16x16x32_bf16 v[104:107], a[136:139], v[56:59], v[104:107]\n" \
"v_accvgpr_read_b32 v80, a40\n" \
"v_accvgpr_read_b32 v81, a41\n" \
"v_mfma_f32_16x16x32_bf16 v[104:107], a[140:143], v[60:63], v[104:107]\n" \
"buffer_load_dwordx4 v6, s[40:43], s16 offen offset:2048 sc1 lds\n" \
"v_mfma_f32_16x16x32_bf16 v[112:115], a[136:139], a[72:75], v[112:115]\n" \
"v_accvgpr_read_b32 v82, a42\n" \
"v_accvgpr_read_b32 v83, a43\n" \
"v_mfma_f32_16x16x32_bf16 v[112:115], a[140:143], a[76:79], v[112:115]\n" \
"v_accvgpr_read_b32 v84, a44\n" \
"v_accvgpr_read_b32 v85, a45\n" \
"v_mfma_f32_16x16x32_bf16 v[120:123], a[136:139], a[88:91], v[120:123]\n" \
"v_accvgpr_read_b32 v86, a46\n" \
"v_mfma_f32_16x16x32_bf16 v[120:123], a[140:143], a[92:95], v[120:123]\n" \
"s_waitcnt lgkmcnt(4)\n" \
"ds_read_b128 a[136:139], v5 offset:18432\n" \
"v_accvgpr_read_b32 v87, a47\n" \
"v_mfma_f32_16x16x32_bf16 v[100:103], a[144:147], v[32:35], 0\n" \
"buffer_load_dwordx4 v6, s[40:43], s16 offen offset:3072 sc1 lds\n" \
"v_mfma_f32_16x16x32_bf16 v[100:103], a[148:151], v[36:39], v[100:103]\n" \
"ds_read_b128 a[140:143], v5 offset:19456\n" \
"v_accvgpr_read_b32 v88, a16\n" \
"v_mfma_f32_16x16x32_bf16 v[108:111], a[144:147], v[48:51], 0\n" \
"v_mul_f32 v80, v24, v80\nv_mul_f32 v81, v24, v81\n" \
"v_mfma_f32_16x16x32_bf16 v[108:111], a[148:151], v[52:55], v[108:111]\n" \
"v_mul_f32 v82, v24, v82\nv_mul_f32 v83, v24, v83\n" \
"v_mfma_f32_16x16x32_bf16 v[124:127], a[144:147], a[80:83], v[176:179]\n" \
"v_mul_f32 v84, v24, v84\nv_mul_f32 v85, v24, v85\n" \
"v_mfma_f32_16x16x32_bf16 v[124:127], a[148:151], a[84:87], v[124:127]\n" \
"s_waitcnt lgkmcnt(4)\n" \
"ds_read_b128 a[144:147], v5 offset:20480\n" \
"v_accvgpr_read_b32 v89, a17\n" \
"v_mfma_f32_16x16x32_bf16 v[100:103], a[152:155], v[40:43], v[100:103]\n" \
"ds_read_b128 a[148:151], v5 offset:21504\n" \
"v_accvgpr_read_b32 v90, a18\n" \
"v_mfma_f32_16x16x32_bf16 v[100:103], a[156:159], v[44:47], v[100:103]\n" \
"v_mul_f32 v86, v24, v86\nv_mul_f32 v87, v24, v87\n" \
"v_mfma_f32_16x16x32_bf16 v[108:111], a[152:155], v[56:59], v[108:111]\n" \
"v_cvt_pk_bf16_f32 v80, v80, v81\n" \
"v_cvt_pk_bf16_f32 v81, v82, v83\n" \
"v_mfma_f32_16x16x32_bf16 v[108:111], a[156:159], v[60:63], v[108:111]\n" \
"v_cvt_pk_bf16_f32 v82, v84, v85\n" \
"v_cvt_pk_bf16_f32 v83, v86, v87\n" \
"v_mov_b32 v160, 0\n" \
"v_mov_b32 v161, 0\n" \
"v_mov_b32 v162, 0\n" \
"v_mov_b32 v163, 0\n" \
"v_mfma_f32_16x16x32_bf16 v[124:127], a[152:155], a[88:91], v[124:127]\n" \
"buffer_store_dwordx4 v[80:83], v4, s[36:39], s63 offen offset:3072 sc1\n" \
"v_mfma_f32_16x16x32_bf16 v[124:127], a[156:159], a[92:95], v[124:127]\n" \
"ds_read_b128 a[152:155], v5 offset:22528\n" \
"v_accvgpr_read_b32 v91, a19\n" \
"v_smfmac_f32_16x16x64_bf16 v[160:163], v[180:183], v[128:135], v168\n" \
"s_waitcnt lgkmcnt(5)\n" \
"ds_read_b128 a[156:159], v5 offset:23552\n" \
"v_mfma_f32_16x16x32_bf16 v[192:195], a[128:131], v[128:131], 0\n" \
"v_accvgpr_read_b32 v92, a20\n" \
"v_accvgpr_read_b32 v93, a21\n" \
"v_mfma_f32_16x16x32_bf16 v[196:199], a[132:135], v[128:131], 0\n" \
"v_exp_f32 v96, v96\n" \
"v_mfma_f32_16x16x32_bf16 v[224:227], a[128:131], v[132:135], 0\n" \
"v_exp_f32 v97, v97\n" \
"v_mfma_f32_16x16x32_bf16 v[228:231], a[132:135], v[132:135], 0\n" \
"s_waitcnt lgkmcnt(4)\n" \
"v_exp_f32 v98, v98\n" \
"v_mfma_f32_16x16x32_bf16 v[200:203], a[136:139], v[128:131], 0\n" \
"v_exp_f32 v99, v99\n" \
"v_mfma_f32_16x16x32_bf16 v[204:207], a[140:143], v[128:131], 0\n" \
"v_exp_f32 v100, v100\n" \
"v_mfma_f32_16x16x32_bf16 v[232:235], a[136:139], v[132:135], 0\n" \
"v_exp_f32 v101, v101\n" \
"v_mfma_f32_16x16x32_bf16 v[236:239], a[140:143], v[132:135], 0\n" \
"s_waitcnt lgkmcnt(2)\n" \
"v_exp_f32 v102, v102\n" \
"v_mfma_f32_16x16x32_bf16 v[208:211], a[144:147], v[128:131], 0\n" \
"v_exp_f32 v103, v103\n" \
"v_mfma_f32_16x16x32_bf16 v[212:215], a[148:151], v[128:131], 0\n" \
"v_exp_f32 v104, v104\n" \
"v_mfma_f32_16x16x32_bf16 v[240:243], a[144:147], v[132:135], 0\n" \
"v_exp_f32 v105, v105\n" \
"v_mfma_f32_16x16x32_bf16 v[244:247], a[148:151], v[132:135], 0\n" \
"s_waitcnt lgkmcnt(0)\n" \
"v_exp_f32 v106, v106\n" \
"v_mfma_f32_16x16x32_bf16 v[216:219], a[152:155], v[128:131], 0\n" \
"v_exp_f32 v107, v107\n" \
"v_mfma_f32_16x16x32_bf16 v[220:223], a[156:159], v[128:131], 0\n" \
"v_exp_f32 v108, v108\n" \
"v_mfma_f32_16x16x32_bf16 v[248:251], a[152:155], v[132:135], 0\n" \
"v_exp_f32 v109, v109\n" \
"v_mfma_f32_16x16x32_bf16 v[252:255], a[156:159], v[132:135], 0\n" \
"v_exp_f32 v110, v110\n" \
"v_exp_f32 v111, v111\n" \
"v_cvt_pk_bf16_f32 v144, v96, v97\n" \
"v_cvt_pk_bf16_f32 v145, v98, v99\n" \
"v_cvt_pk_bf16_f32 v146, v100, v101\n" \
"v_cvt_pk_bf16_f32 v147, v102, v103\n" \
"v_cvt_pk_bf16_f32 v148, v104, v105\n" \
"v_cvt_pk_bf16_f32 v149, v106, v107\n" \
"v_cvt_pk_bf16_f32 v150, v108, v109\n" \
"v_cvt_pk_bf16_f32 v151, v110, v111\n" \
"v_accvgpr_read_b32 v94, a22\n" \
"v_accvgpr_read_b32 v95, a23\n" \
"v_pk_mul_f32 v[88:89], v[88:89], v[188:189] op_sel_hi:[1,0]\n" \
"v_pk_mul_f32 v[90:91], v[90:91], v[188:189] op_sel_hi:[1,0]\n" \
"v_pk_mul_f32 v[92:93], v[92:93], v[188:189] op_sel_hi:[1,0]\n" \
"v_pk_mul_f32 v[94:95], v[94:95], v[188:189] op_sel_hi:[1,0]\n" \
"v_cvt_pk_bf16_f32 v88, v88, v89\n" \
"v_cvt_pk_bf16_f32 v89, v90, v91\n" \
"v_cvt_pk_bf16_f32 v90, v92, v93\n" \
"v_cvt_pk_bf16_f32 v91, v94, v95\n" \
"buffer_store_dwordx4 v[88:91], v7, s[36:39], s63 offen offset:0 sc1\n" \
"v_accvgpr_read_b32 v136, a48\n" \
"v_accvgpr_read_b32 v137, a49\n" \
"s_waitcnt vmcnt(25) lgkmcnt(8)\n" \
"s_add_u32 s17, s17, s19\n" \
"s_add_u32 m0, s49, 49152\n" \
"v_accvgpr_read_b32 v138, a50\n" \
"s_barrier\n" \
"v_accvgpr_read_b32 v139, a51\n" \
"v_accvgpr_read_b32 v140, a52\n" \
"v_accvgpr_read_b32 v141, a53\n" \
"buffer_load_dwordx4 v6, s[40:43], s17 offen offset:0 sc1 lds\n" \
"v_accvgpr_read_b32 v142, a54\n" \
"v_accvgpr_read_b32 v143, a55\n" \
"ds_read_b128 a[128:131], v5 offset:32768\n" \
"v_pk_mul_f32 v[136:137], v[136:137], v[24:25] op_sel_hi:[1,0]\n" \
"v_pk_mul_f32 v[138:139], v[138:139], v[24:25] op_sel_hi:[1,0]\n" \
"v_pk_mul_f32 v[140:141], v[140:141], v[24:25] op_sel_hi:[1,0]\n" \
"buffer_load_dwordx4 v6, s[40:43], s17 offen offset:1024 sc1 lds\n" \
"v_pk_mul_f32 v[142:143], v[142:143], v[24:25] op_sel_hi:[1,0]\n" \
"v_cvt_pk_bf16_f32 v136, v136, v137\n" \
"ds_read_b128 a[132:135], v5 offset:33792\n" \
"v_cvt_pk_bf16_f32 v137, v138, v139\n" \
"v_cvt_pk_bf16_f32 v138, v140, v141\n" \
"v_cvt_pk_bf16_f32 v139, v142, v143\n" \
"buffer_load_dwordx4 v6, s[40:43], s17 offen offset:2048 sc1 lds\n" \
"v_accvgpr_read_b32 v80, a24\n" \
"v_accvgpr_read_b32 v81, a25\n" \
"ds_read_b128 a[136:139], v5 offset:34816\n" \
"v_accvgpr_read_b32 v82, a26\n" \
"v_accvgpr_read_b32 v83, a27\n" \
"v_accvgpr_read_b32 v84, a28\n" \
"buffer_load_dwordx4 v6, s[40:43], s17 offen offset:3072 sc1 lds\n" \
"v_accvgpr_read_b32 v85, a29\n" \
"v_accvgpr_read_b32 v86, a30\n" \
"ds_read_b128 a[140:143], v5 offset:35840\n" \
"v_accvgpr_read_b32 v87, a31\n" \
"v_accvgpr_read_b32 v88, a56\n" \
"v_accvgpr_read_b32 v89, a57\n" \
"buffer_store_dwordx4 v[136:139], v7, s[36:39], s63 offen offset:1024 sc1\n" \
"v_pk_mul_f32 v[80:81], v[80:81], v[188:189] op_sel_hi:[1,0]\n" \
"v_pk_mul_f32 v[82:83], v[82:83], v[188:189] op_sel_hi:[1,0]\n" \
"ds_read_b128 a[144:147], v5 offset:36864\n" \
"v_pk_mul_f32 v[84:85], v[84:85], v[188:189] op_sel_hi:[1,0]\n" \
"v_pk_mul_f32 v[86:87], v[86:87], v[188:189] op_sel_hi:[1,0]\n" \
"v_cvt_pk_bf16_f32 v80, v80, v81\n" \
"ds_read_b128 a[148:151], v5 offset:37888\n" \
"v_cvt_pk_bf16_f32 v81, v82, v83\n" \
"v_cvt_pk_bf16_f32 v82, v84, v85\n" \
"v_cvt_pk_bf16_f32 v83, v86, v87\n" \
"v_accvgpr_read_b32 v90, a58\n" \
"v_accvgpr_read_b32 v91, a59\n" \
"v_accvgpr_read_b32 v92, a60\n" \
"v_accvgpr_read_b32 v93, a61\n" \
"buffer_store_dwordx4 v[80:83], v7, s[36:39], s63 offen offset:2048 sc1\n" \
"v_accvgpr_read_b32 v94, a62\n" \
"v_accvgpr_read_b32 v95, a63\n" \
"v_pk_mul_f32 v[88:89], v[88:89], v[24:25] op_sel_hi:[1,0]\n" \
"v_pk_mul_f32 v[90:91], v[90:91], v[24:25] op_sel_hi:[1,0]\n" \
"v_pk_mul_f32 v[92:93], v[92:93], v[24:25] op_sel_hi:[1,0]\n" \
"v_pk_mul_f32 v[94:95], v[94:95], v[24:25] op_sel_hi:[1,0]\n" \
"v_cvt_pk_bf16_f32 v88, v88, v89\n" \
"v_cvt_pk_bf16_f32 v89, v90, v91\n" \
"v_cvt_pk_bf16_f32 v90, v92, v93\n" \
"v_cvt_pk_bf16_f32 v91, v94, v95\n" \
"v_mov_b32 v116, 0xff800000\n" \
"v_mov_b32 v117, 0xff800000\n" \
"v_mov_b32 v118, 0xff800000\n" \
"v_mov_b32 v119, 0xff800000\n" \
"buffer_store_dwordx4 v[88:91], v7, s[36:39], s63 offen offset:3072 sc1\n" \
"s_waitcnt lgkmcnt(4)\n" \
"v_mfma_f32_16x16x32_bf16 v[64:67], a[128:131], v[32:35], 0\n" \
"LH22_%=:\ns_cbranch_execz LHR1_%=\n" \
"v_exp_f32 v112, v112\n" \
"v_mfma_f32_16x16x32_bf16 v[64:67], a[132:135], v[36:39], v[64:67]\n" \
"s_add_u32 s16, s16, s9\n" \
"v_mfma_f32_16x16x32_bf16 v[72:75], a[128:131], v[48:51], 0\n" \
"ds_read_b128 a[152:155], v5 offset:38912\n" \
"v_mfma_f32_16x16x32_bf16 v[72:75], a[132:135], v[52:55], v[72:75]\n" \
"s_add_u32 m0, s48, 0\n" \
"v_exp_f32 v113, v113\n" \
"v_mfma_f32_16x16x32_bf16 v[80:83], a[128:131], a[64:67], 0\n" \
"buffer_load_dwordx4 v6, s[40:43], s16 offen offset:0 sc1 lds\n" \
"v_mfma_f32_16x16x32_bf16 v[80:83], a[132:135], a[68:71], v[80:83]\n" \
"v_exp_f32 v114, v114\n" \
"v_mfma_f32_16x16x32_bf16 v[88:91], a[128:131], a[80:83], 0\n" \
"ds_read_b128 a[156:159], v5 offset:39936\n" \
"v_cvt_pk_bf16_f32 v152, v112, v113\n" \
"v_mfma_f32_16x16x32_bf16 v[88:91], a[132:135], a[84:87], v[88:91]\n" \
"s_waitcnt lgkmcnt(4)\n" \
"v_exp_f32 v115, v115\n" \
"v_mfma_f32_16x16x32_bf16 v[64:67], a[136:139], v[40:43], v[64:67]\n" \
"v_exp_f32 v116, v116\n" \
"v_mfma_f32_16x16x32_bf16 v[64:67], a[140:143], v[44:47], v[64:67]\n" \
"s_xor_b32 s49, s49, 0x10000\n" \
"v_cvt_pk_bf16_f32 v153, v114, v115\n" \
"v_mov_b32 v164, 0\n" \
"v_mfma_f32_16x16x32_bf16 v[72:75], a[136:139], v[56:59], v[72:75]\n" \
"ds_read_b128 a[128:131], v5 offset:49152\n" \
"v_mov_b32 v165, 0\n" \
"v_mfma_f32_16x16x32_bf16 v[72:75], a[140:143], v[60:63], v[72:75]\n" \
"v_exp_f32 v117, v117\n" \
"v_mfma_f32_16x16x32_bf16 v[80:83], a[136:139], a[72:75], v[80:83]\n" \
"buffer_load_dwordx4 v6, s[40:43], s16 offen offset:1024 sc1 lds\n" \
"v_mfma_f32_16x16x32_bf16 v[80:83], a[140:143], a[76:79], v[80:83]\n" \
"v_exp_f32 v118, v118\n" \
"v_mfma_f32_16x16x32_bf16 v[88:91], a[136:139], a[88:91], v[88:91]\n" \
"ds_read_b128 a[132:135], v5 offset:50176\n" \
"v_cvt_pk_bf16_f32 v154, v116, v117\n" \
"v_mfma_f32_16x16x32_bf16 v[88:91], a[140:143], a[92:95], v[88:91]\n" \
"s_waitcnt lgkmcnt(4)\n" \
"v_exp_f32 v119, v119\n" \
"v_mfma_f32_16x16x32_bf16 v[68:71], a[144:147], v[32:35], 0\n" \
"v_exp_f32 v120, v120\n" \
"v_mfma_f32_16x16x32_bf16 v[68:71], a[148:151], v[36:39], v[68:71]\n" \
"v_cvt_pk_bf16_f32 v155, v118, v119\n" \
"v_mov_b32 v166, 0\n" \
"v_mfma_f32_16x16x32_bf16 v[76:79], a[144:147], v[48:51], 0\n" \
"ds_read_b128 a[136:139], v5 offset:51200\n" \
"v_mov_b32 v167, 0\n" \
"v_mfma_f32_16x16x32_bf16 v[76:79], a[148:151], v[52:55], v[76:79]\n" \
"v_exp_f32 v121, v121\n" \
"v_mfma_f32_16x16x32_bf16 v[84:87], a[144:147], a[64:67], 0\n" \
"buffer_load_dwordx4 v6, s[40:43], s16 offen offset:2048 sc1 lds\n" \
"v_mfma_f32_16x16x32_bf16 v[84:87], a[148:151], a[68:71], v[84:87]\n" \
"v_exp_f32 v122, v122\n" \
"v_mfma_f32_16x16x32_bf16 v[92:95], a[144:147], a[80:83], 0\n" \
"ds_read_b128 a[140:143], v5 offset:52224\n" \
"v_cvt_pk_bf16_f32 v156, v120, v121\n" \
"v_mfma_f32_16x16x32_bf16 v[92:95], a[148:151], a[84:87], v[92:95]\n" \
"s_waitcnt lgkmcnt(4)\n" \
"v_exp_f32 v123, v123\n" \
"v_mfma_f32_16x16x32_bf16 v[68:71], a[152:155], v[40:43], v[68:71]\n" \
"v_exp_f32 v124, v124\n" \
"v_mfma_f32_16x16x32_bf16 v[68:71], a[156:159], v[44:47], v[68:71]\n" \
"v_cvt_pk_bf16_f32 v157, v122, v123\n" \
"v_mfma_f32_16x16x32_bf16 v[76:79], a[152:155], v[56:59], v[76:79]\n" \
"ds_read_b128 a[144:147], v5 offset:53248\n" \
"v_mfma_f32_16x16x32_bf16 v[76:79], a[156:159], v[60:63], v[76:79]\n" \
"v_exp_f32 v125, v125\n" \
"v_mfma_f32_16x16x32_bf16 v[84:87], a[152:155], a[72:75], v[84:87]\n" \
"buffer_load_dwordx4 v6, s[40:43], s16 offen offset:3072 sc1 lds\n" \
"v_mfma_f32_16x16x32_bf16 v[84:87], a[156:159], a[76:79], v[84:87]\n" \
"v_exp_f32 v126, v126\n" \
"v_mfma_f32_16x16x32_bf16 v[92:95], a[152:155], a[88:91], v[92:95]\n" \
"ds_read_b128 a[148:151], v5 offset:54272\n" \
"v_cvt_pk_bf16_f32 v158, v124, v125\n" \
"v_mfma_f32_16x16x32_bf16 v[92:95], a[156:159], a[92:95], v[92:95]\n" \
"s_waitcnt lgkmcnt(4)\n" \
"v_exp_f32 v127, v127\n" \
"v_smfmac_f32_16x16x64_bf16 v[160:163], v[180:183], v[144:151], v168\n" \
"v_cvt_pk_bf16_f32 v159, v126, v127\n" \
"v_mfma_f32_16x16x32_bf16 v[192:195], a[128:131], v[144:147], v[192:195]\n" \
"v_exp_f32 v64, v64\n" \
"v_mfma_f32_16x16x32_bf16 v[196:199], a[132:135], v[144:147], v[196:199]\n" \
"v_mfma_f32_16x16x32_bf16 v[224:227], a[128:131], v[148:151], v[224:227]\n" \
"ds_read_b128 a[152:155], v5 offset:55296\n" \
"v_mfma_f32_16x16x32_bf16 v[228:231], a[132:135], v[148:151], v[228:231]\n" \
"v_exp_f32 v65, v65\n" \
"v_mfma_f32_16x16x32_bf16 a[0:3], a[128:131], v[152:155], 0\n" \
"v_exp_f32 v66, v66\n" \
"v_mfma_f32_16x16x32_bf16 a[4:7], a[132:135], v[152:155], 0\n" \
"v_cvt_pk_bf16_f32 v128, v64, v65\n" \
"v_mfma_f32_16x16x32_bf16 a[32:35], a[128:131], v[156:159], 0\n" \
"ds_read_b128 a[156:159], v5 offset:56320\n" \
"v_mfma_f32_16x16x32_bf16 a[36:39], a[132:135], v[156:159], 0\n" \
"s_waitcnt vmcnt(20) lgkmcnt(4)\n" \
"v_exp_f32 v67, v67\n" \
"v_mfma_f32_16x16x32_bf16 v[200:203], a[136:139], v[144:147], v[200:203]\n" \
"s_barrier\n" \
"v_xor_b32 v5, 0x10000, v5\n" \
"v_cvt_pk_bf16_f32 v129, v66, v67\n" \
"v_mfma_f32_16x16x32_bf16 v[204:207], a[140:143], v[144:147], v[204:207]\n" \
"v_exp_f32 v68, v68\n" \
"v_mfma_f32_16x16x32_bf16 v[232:235], a[136:139], v[148:151], v[232:235]\n" \
"s_add_u32 s17, s17, s19\n" \
"ds_read_b128 a[128:131], v5 offset:0\n" \
"v_mfma_f32_16x16x32_bf16 v[236:239], a[140:143], v[148:151], v[236:239]\n" \
"v_exp_f32 v69, v69\n" \
"v_mfma_f32_16x16x32_bf16 a[8:11], a[136:139], v[152:155], 0\n" \
"s_add_u32 m0, s48, 16384\n" \
"v_exp_f32 v70, v70\n" \
"v_mfma_f32_16x16x32_bf16 a[12:15], a[140:143], v[152:155], 0\n" \
"buffer_load_dwordx4 v6, s[40:43], s17 offen offset:0 sc1 lds\n" \
"v_mfma_f32_16x16x32_bf16 a[40:43], a[136:139], v[156:159], 0\n" \
"ds_read_b128 a[132:135], v5 offset:1024\n" \
"v_cvt_pk_bf16_f32 v130, v68, v69\n" \
"v_mfma_f32_16x16x32_bf16 a[44:47], a[140:143], v[156:159], 0\n" \
"s_waitcnt lgkmcnt(4)\n" \
"s_waitcnt vmcnt(5)\n" \
"v_mfma_f32_16x16x32_bf16 v[208:211], a[144:147], v[144:147], v[208:211]\n" \
"v_exp_f32 v71, v71\n" \
"v_mfma_f32_16x16x32_bf16 v[212:215], a[148:151], v[144:147], v[212:215]\n" \
"v_exp_f32 v72, v72\n" \
"v_mfma_f32_16x16x32_bf16 v[240:243], a[144:147], v[148:151], v[240:243]\n" \
"ds_read_b128 a[136:139], v5 offset:2048\n" \
"v_cvt_pk_bf16_f32 v131, v70, v71\n" \
"v_mfma_f32_16x16x32_bf16 v[244:247], a[148:151], v[148:151], v[244:247]\n" \
"v_exp_f32 v73, v73\n" \
"v_mfma_f32_16x16x32_bf16 a[16:19], a[144:147], v[152:155], 0\n" \
"v_exp_f32 v74, v74\n" \
"v_mfma_f32_16x16x32_bf16 a[20:23], a[148:151], v[152:155], 0\n" \
"buffer_load_dwordx4 v6, s[40:43], s17 offen offset:1024 sc1 lds\n" \
"v_mfma_f32_16x16x32_bf16 a[48:51], a[144:147], v[156:159], 0\n" \
"ds_read_b128 a[140:143], v5 offset:3072\n" \
"v_cvt_pk_bf16_f32 v132, v72, v73\n" \
"v_mfma_f32_16x16x32_bf16 a[52:55], a[148:151], v[156:159], 0\n" \
"s_waitcnt lgkmcnt(4)\n" \
"v_exp_f32 v75, v75\n" \
"v_mfma_f32_16x16x32_bf16 v[216:219], a[152:155], v[144:147], v[216:219]\n" \
"v_exp_f32 v76, v76\n" \
"v_mfma_f32_16x16x32_bf16 v[220:223], a[156:159], v[144:147], v[220:223]\n" \
"s_add_u32 s44, s44, 1\n" \
"buffer_load_dwordx4 v6, s[40:43], s17 offen offset:2048 sc1 lds\n" \
"v_mfma_f32_16x16x32_bf16 v[248:251], a[152:155], v[148:151], v[248:251]\n" \
"ds_read_b128 a[144:147], v5 offset:4096\n" \
"v_cvt_pk_bf16_f32 v133, v74, v75\n" \
"v_mfma_f32_16x16x32_bf16 v[252:255], a[156:159], v[148:151], v[252:255]\n" \
"v_exp_f32 v77, v77\n" \
"v_mfma_f32_16x16x32_bf16 a[24:27], a[152:155], v[152:155], 0\n" \
"v_exp_f32 v78, v78\n" \
"v_mfma_f32_16x16x32_bf16 a[28:31], a[156:159], v[152:155], 0\n" \
"s_xor_b32 s48, s48, 0x10000\n" \
"buffer_load_dwordx4 v6, s[40:43], s17 offen offset:3072 sc1 lds\n" \
"v_mfma_f32_16x16x32_bf16 a[56:59], a[152:155], v[156:159], 0\n" \
"ds_read_b128 a[148:151], v5 offset:5120\n" \
"v_cvt_pk_bf16_f32 v134, v76, v77\n" \
"v_mfma_f32_16x16x32_bf16 a[60:63], a[156:159], v[156:159], 0\n" \
"s_waitcnt lgkmcnt(4)\n" \
"v_exp_f32 v79, v79\n" \
"v_smfmac_f32_16x16x64_bf16 v[164:167], v[180:183], v[152:159], v168\n" \
"s_cmp_lg_u64 s[54:55], 0\ns_cbranch_scc1 LFBA_%=\nLFBAR_%=:\n" \
"v_cvt_pk_bf16_f32 v135, v78, v79\n" \
"buffer_store_dwordx2 v[10:11], v9, s[36:39], s52 offen sc1\n" \
"s_mov_b32 s46, s24\n" \
"s_branch LBOT_%=\n" \
"LAD32_%=:\n" \
"v_mov_b32 v96, 0\n" \
"v_mov_b32 v97, 0\n" \
"v_mov_b32 v98, 0\n" \
"v_mov_b32 v99, 0\n" \
"v_accvgpr_read_b32 v140, a12\n" \
"v_accvgpr_read_b32 v141, a13\n" \
"v_accvgpr_read_b32 v142, a14\n" \
"v_accvgpr_read_b32 v143, a15\n" \
"v_pk_mul_f32 v[136:137], v[136:137], v[188:189] op_sel_hi:[1,0]\n" \
"v_pk_mul_f32 v[138:139], v[138:139], v[188:189] op_sel_hi:[1,0]\n" \
"v_pk_mul_f32 v[140:141], v[140:141], v[188:189] op_sel_hi:[1,0]\n" \
"v_pk_mul_f32 v[142:143], v[142:143], v[188:189] op_sel_hi:[1,0]\n" \
"v_cvt_pk_bf16_f32 v136, v136, v137\n" \
"v_cvt_pk_bf16_f32 v137, v138, v139\n" \
"v_cvt_pk_bf16_f32 v138, v140, v141\n" \
"v_cvt_pk_bf16_f32 v139, v142, v143\n" \
"buffer_store_dwordx4 v[136:139], v4, s[36:39], s63 offen offset:2048 sc1\n" \
"v_accvgpr_read_b32 v80, a40\n" \
"v_accvgpr_read_b32 v81, a41\n" \
"v_accvgpr_read_b32 v82, a42\n" \
"v_accvgpr_read_b32 v83, a43\n" \
"v_accvgpr_read_b32 v84, a44\n" \
"v_accvgpr_read_b32 v85, a45\n" \
"v_accvgpr_read_b32 v86, a46\n" \
"v_accvgpr_read_b32 v87, a47\n" \
"v_accvgpr_read_b32 v88, a16\n" \
"v_accvgpr_read_b32 v89, a17\n" \
"v_pk_mul_f32 v[80:81], v[80:81], v[24:25] op_sel_hi:[1,0]\n" \
"v_pk_mul_f32 v[82:83], v[82:83], v[24:25] op_sel_hi:[1,0]\n" \
"v_pk_mul_f32 v[84:85], v[84:85], v[24:25] op_sel_hi:[1,0]\n" \
"v_pk_mul_f32 v[86:87], v[86:87], v[24:25] op_sel_hi:[1,0]\n" \
"v_cvt_pk_bf16_f32 v80, v80, v81\n" \
"v_cvt_pk_bf16_f32 v81, v82, v83\n" \
"v_cvt_pk_bf16_f32 v82, v84, v85\n" \
"v_cvt_pk_bf16_f32 v83, v86, v87\n" \
"buffer_store_dwordx4 v[80:83], v4, s[36:39], s63 offen offset:3072 sc1\n" \
"v_accvgpr_read_b32 v90, a18\n" \
"v_accvgpr_read_b32 v91, a19\n" \
"v_accvgpr_read_b32 v92, a20\n" \
"v_accvgpr_read_b32 v93, a21\n" \
"v_accvgpr_read_b32 v94, a22\n" \
"v_accvgpr_read_b32 v95, a23\n" \
"v_pk_mul_f32 v[88:89], v[88:89], v[188:189] op_sel_hi:[1,0]\n" \
"v_pk_mul_f32 v[90:91], v[90:91], v[188:189] op_sel_hi:[1,0]\n" \
"v_pk_mul_f32 v[92:93], v[92:93], v[188:189] op_sel_hi:[1,0]\n" \
"v_pk_mul_f32 v[94:95], v[94:95], v[188:189] op_sel_hi:[1,0]\n" \
"v_cvt_pk_bf16_f32 v88, v88, v89\n" \
"v_cvt_pk_bf16_f32 v89, v90, v91\n" \
"v_cvt_pk_bf16_f32 v90, v92, v93\n" \
"v_cvt_pk_bf16_f32 v91, v94, v95\n" \
"buffer_store_dwordx4 v[88:91], v7, s[36:39], s63 offen offset:0 sc1\n" \
"v_accvgpr_read_b32 v136, a48\n" \
"v_accvgpr_read_b32 v137, a49\n" \
"v_accvgpr_read_b32 v138, a50\n" \
"v_accvgpr_read_b32 v139, a51\n" \
"v_accvgpr_read_b32 v140, a52\n" \
"v_accvgpr_read_b32 v141, a53\n" \
"v_accvgpr_read_b32 v142, a54\n" \
"v_accvgpr_read_b32 v143, a55\n" \
"v_accvgpr_read_b32 v80, a24\n" \
"v_accvgpr_read_b32 v81, a25\n" \
"v_pk_mul_f32 v[136:137], v[136:137], v[24:25] op_sel_hi:[1,0]\n" \
"v_pk_mul_f32 v[138:139], v[138:139], v[24:25] op_sel_hi:[1,0]\n" \
"v_pk_mul_f32 v[140:141], v[140:141], v[24:25] op_sel_hi:[1,0]\n" \
"v_pk_mul_f32 v[142:143], v[142:143], v[24:25] op_sel_hi:[1,0]\n" \
"v_cvt_pk_bf16_f32 v136, v136, v137\n" \
"v_cvt_pk_bf16_f32 v137, v138, v139\n" \
"v_cvt_pk_bf16_f32 v138, v140, v141\n" \
"v_cvt_pk_bf16_f32 v139, v142, v143\n" \
"buffer_store_dwordx4 v[136:139], v7, s[36:39], s63 offen offset:1024 sc1\n" \
"v_accvgpr_read_b32 v82, a26\n" \
"v_accvgpr_read_b32 v83, a27\n" \
"v_accvgpr_read_b32 v84, a28\n" \
"v_accvgpr_read_b32 v85, a29\n" \
"v_accvgpr_read_b32 v86, a30\n" \
"v_accvgpr_read_b32 v87, a31\n" \
"v_pk_mul_f32 v[80:81], v[80:81], v[188:189] op_sel_hi:[1,0]\n" \
"v_pk_mul_f32 v[82:83], v[82:83], v[188:189] op_sel_hi:[1,0]\n" \
"v_pk_mul_f32 v[84:85], v[84:85], v[188:189] op_sel_hi:[1,0]\n" \
"v_pk_mul_f32 v[86:87], v[86:87], v[188:189] op_sel_hi:[1,0]\n" \
"v_cvt_pk_bf16_f32 v80, v80, v81\n" \
"v_cvt_pk_bf16_f32 v81, v82, v83\n" \
"v_cvt_pk_bf16_f32 v82, v84, v85\n" \
"v_cvt_pk_bf16_f32 v83, v86, v87\n" \
"buffer_store_dwordx4 v[80:83], v7, s[36:39], s63 offen offset:2048 sc1\n" \
"v_accvgpr_read_b32 v88, a56\n" \
"v_accvgpr_read_b32 v89, a57\n" \
"v_accvgpr_read_b32 v90, a58\n" \
"v_accvgpr_read_b32 v91, a59\n" \
"v_accvgpr_read_b32 v92, a60\n" \
"v_accvgpr_read_b32 v93, a61\n" \
"v_accvgpr_read_b32 v94, a62\n" \
"v_accvgpr_read_b32 v95, a63\n" \
"v_pk_mul_f32 v[88:89], v[88:89], v[24:25] op_sel_hi:[1,0]\n" \
"v_pk_mul_f32 v[90:91], v[90:91], v[24:25] op_sel_hi:[1,0]\n" \
"v_pk_mul_f32 v[92:93], v[92:93], v[24:25] op_sel_hi:[1,0]\n" \
"v_pk_mul_f32 v[94:95], v[94:95], v[24:25] op_sel_hi:[1,0]\n" \
"v_cvt_pk_bf16_f32 v88, v88, v89\n" \
"v_cvt_pk_bf16_f32 v89, v90, v91\n" \
"v_cvt_pk_bf16_f32 v90, v92, v93\n" \
"v_cvt_pk_bf16_f32 v91, v94, v95\n" \
"buffer_store_dwordx4 v[88:91], v7, s[36:39], s63 offen offset:3072 sc1\n" \
"v_mfma_f32_32x32x16_bf16 v[192:207], v[96:99], v[96:99], 0\n" \
"v_mfma_f32_32x32x16_bf16 v[208:223], v[96:99], v[96:99], 0\n" \
"v_mfma_f32_32x32x16_bf16 v[224:239], v[96:99], v[96:99], 0\n" \
"v_mfma_f32_32x32x16_bf16 v[240:255], v[96:99], v[96:99], 0\n" \
"v_mfma_f32_32x32x16_bf16 a[0:15], v[96:99], v[96:99], 0\n" \
"v_mfma_f32_32x32x16_bf16 a[16:31], v[96:99], v[96:99], 0\n" \
"v_mfma_f32_32x32x16_bf16 a[32:47], v[96:99], v[96:99], 0\n" \
"v_mfma_f32_32x32x16_bf16 a[48:63], v[96:99], v[96:99], 0\n" \
"v_mov_b32 v160, 0\n" \
"v_mov_b32 v161, 0\n" \
"v_mov_b32 v162, 0\n" \
"v_mov_b32 v163, 0\n" \
"v_mov_b32 v164, 0\n" \
"v_mov_b32 v165, 0\n" \
"v_mov_b32 v166, 0\n" \
"v_mov_b32 v167, 0\n" \
"v_mov_b32 v80, 0xff800000\n" \
"v_mov_b32 v81, 0xff800000\n" \
"v_mov_b32 v82, 0xff800000\n" \
"v_mov_b32 v83, 0xff800000\n" \
"v_mov_b32 v84, 0xff800000\n" \
"v_mov_b32 v85, 0xff800000\n" \
"v_mov_b32 v86, 0xff800000\n" \
"v_mov_b32 v87, 0xff800000\n" \
"v_mov_b32 v88, 0xff800000\n" \
"v_mov_b32 v89, 0xff800000\n" \
"v_mov_b32 v90, 0xff800000\n" \
"v_mov_b32 v91, 0xff800000\n" \
"v_mov_b32 v92, 0xff800000\n" \
"v_mov_b32 v93, 0xff800000\n" \
"v_mov_b32 v94, 0xff800000\n" \
"v_mov_b32 v95, 0xff800000\n" \
"s_branch LFIN_%=\n" \
".p2align 6\n" \
"LH23_%=:\ns_cbranch_execz LH27_%=\n" \
"s_nop 0\n" \
"LDISP_%=:\n" \
"s_cmp_eq_u32 s44, 16\n" \
"s_cbranch_scc1 LFIN_%=\n" \
"s_cmp_eq_u32 s44, 15\ns_cselect_b32 s16, s56, s16\ns_cselect_b32 s9, s57, s9\ns_cselect_b32 s17, s58, s17\ns_cselect_b32 s19, s59, s19\n" \
"s_cbranch_scc0 LD14_%=\n" \
"buffer_load_dword v1, v191, s[36:39], 0 offen sc1\n" \
"s_branch LEVEN_%=\n" \
"LD14_%=:\n" \
"buffer_load_dwordx2 v[18:19], v17, s[36:39], 0 offen sc1\n" \
"s_branch LEVEN_%=\n" \
".p2align 6\n" \
"LH24_%=:\ns_cbranch_execz LH28_%=\n" \
"s_nop 0\n" \
"LFIN_%=:\n" \
"s_waitcnt lgkmcnt(6)\n" \
"ds_read_b128 a[128:131], v5 offset:16384\n" \
"ds_read_b128 a[132:135], v5 offset:17408\n" \
"s_mov_b32 s81, 786432\n" \
"ds_read_b128 a[136:139], v5 offset:18432\n" \
"ds_read_b128 a[140:143], v5 offset:19456\n" \
"s_waitcnt vmcnt(8)\n" \
"s_barrier\n" \
"ds_read_b128 a[144:147], v5 offset:20480\n" \
"ds_read_b128 a[148:151], v5 offset:21504\n" \
"v_exp_f32 v80, v80\n" \
"ds_read_b128 a[152:155], v5 offset:22528\n" \
"ds_read_b128 a[156:159], v5 offset:23552\n" \
"v_exp_f32 v81, v81\n" \
"ds_read_b128 v[96:99], v29 offset:32768\n" \
"v_exp_f32 v82, v82\n" \
"v_cvt_pk_bf16_f32 v136, v80, v81\n" \
"s_add_u32 m0, s79, 32768\n" \
"ds_read_b128 v[100:103], v29 offset:34816\n" \
"v_exp_f32 v83, v83\n" \
"v_smfmac_f32_16x16x64_bf16 v[160:163], v[180:183], v[128:135], v168\n" \
"ds_read_b128 v[104:107], v29 offset:33792\n" \
"s_waitcnt lgkmcnt(9)\n" \
"v_mfma_f32_16x16x32_bf16 v[192:195], a[128:131], v[128:131], v[192:195]\n" \
"ds_read_b128 v[108:111], v29 offset:35840\n" \
"v_exp_f32 v84, v84\n" \
"v_mfma_f32_16x16x32_bf16 v[196:199], a[132:135], v[128:131], v[196:199]\n" \
"ds_read_b128 v[112:115], v29 offset:36864\n" \
"v_exp_f32 v85, v85\n" \
"v_mfma_f32_16x16x32_bf16 v[224:227], a[128:131], v[132:135], v[224:227]\n" \
"ds_read_b128 v[116:119], v29 offset:38912\n" \
"v_exp_f32 v86, v86\n" \
"v_mfma_f32_16x16x32_bf16 v[228:231], a[132:135], v[132:135], v[228:231]\n" \
"ds_read_b128 v[120:123], v29 offset:37888\n" \
"s_waitcnt lgkmcnt(7)\n" \
"v_cvt_pk_bf16_f32 v137, v82, v83\n" \
"v_mfma_f32_16x16x32_bf16 v[200:203], a[136:139], v[128:131], v[200:203]\n" \
"ds_read_b128 v[124:127], v29 offset:39936\n" \
"v_add_f32 v160, v160, v161\n" \
"v_add_f32 v162, v162, v163\n" \
"v_mfma_f32_16x16x32_bf16 v[204:207], a[140:143], v[128:131], v[204:207]\n" \
"ds_write2_b32 v184, v160, v162 offset1:64\n" \
"v_exp_f32 v87, v87\n" \
"v_mfma_f32_16x16x32_bf16 v[232:235], a[136:139], v[132:135], v[232:235]\n" \
"v_exp_f32 v88, v88\n" \
"v_cvt_pk_bf16_f32 v138, v84, v85\n" \
"v_mfma_f32_16x16x32_bf16 v[236:239], a[140:143], v[132:135], v[236:239]\n" \
"v_exp_f32 v89, v89\n" \
"v_cvt_pk_bf16_f32 v139, v86, v87\n" \
"v_mfma_f32_16x16x32_bf16 v[208:211], a[144:147], v[128:131], v[208:211]\n" \
"v_exp_f32 v90, v90\n" \
"v_cvt_pk_bf16_f32 v140, v88, v89\n" \
"v_mfma_f32_16x16x32_bf16 v[212:215], a[148:151], v[128:131], v[212:215]\n" \
"v_exp_f32 v91, v91\n" \
"v_cvt_pk_bf16_f32 v192, v192, v193\n" \
"v_mfma_f32_16x16x32_bf16 v[240:243], a[144:147], v[132:135], v[240:243]\n" \
"v_exp_f32 v92, v92\n" \
"v_cvt_pk_bf16_f32 v141, v90, v91\n" \
"v_mfma_f32_16x16x32_bf16 v[244:247], a[148:151], v[132:135], v[244:247]\n" \
"v_exp_f32 v93, v93\n" \
"v_cvt_pk_bf16_f32 v193, v194, v195\n" \
"v_mfma_f32_16x16x32_bf16 v[216:219], a[152:155], v[128:131], v[216:219]\n" \
"v_exp_f32 v94, v94\n" \
"v_cvt_pk_bf16_f32 v142, v92, v93\n" \
"v_mfma_f32_16x16x32_bf16 v[220:223], a[156:159], v[128:131], v[220:223]\n" \
"v_exp_f32 v95, v95\n" \
"v_cvt_pk_bf16_f32 v194, v196, v197\n" \
"v_mfma_f32_16x16x32_bf16 v[248:251], a[152:155], v[132:135], v[248:251]\n" \
"v_cvt_pk_bf16_f32 v143, v94, v95\n" \
"v_cvt_pk_bf16_f32 v195, v198, v199\n" \
"v_cvt_pk_bf16_f32 v224, v224, v225\n" \
"v_mfma_f32_16x16x32_bf16 v[252:255], a[156:159], v[132:135], v[252:255]\n" \
"buffer_load_dwordx4 v12, s[28:31], s81 offen offset:0 lds\n" \
"ds_write_b128 v186, v[192:195] offset:0\n" \
"v_cvt_pk_bf16_f32 v225, v226, v227\n" \
"v_cvt_pk_bf16_f32 v226, v228, v229\n" \
"v_cvt_pk_bf16_f32 v227, v230, v231\n" \
"v_smfmac_f32_16x16x64_bf16 v[164:167], v[180:183], v[136:143], v168\n" \
"s_add_u32 m0, s79, 33664\n" \
"ds_write_b128 v186, v[224:227] offset:4096\n" \
"v_cvt_pk_bf16_f32 v200, v200, v201\n" \
"v_mfma_f32_16x16x32_bf16 a[0:3], a[128:131], v[136:139], a[0:3]\n" \
"v_cvt_pk_bf16_f32 v201, v202, v203\n" \
"v_cvt_pk_bf16_f32 v202, v204, v205\n" \
"v_cvt_pk_bf16_f32 v203, v206, v207\n" \
"v_mfma_f32_16x16x32_bf16 a[4:7], a[132:135], v[136:139], a[4:7]\n" \
"buffer_load_dwordx4 v12, s[28:31], s81 offen offset:128 lds\n" \
"v_mfma_f32_16x16x32_bf16 a[32:35], a[128:131], v[140:143], a[32:35]\n" \
"s_mov_b32 s81, 851968\n" \
"ds_write_b128 v186, v[200:203] offset:1024\n" \
"v_cvt_pk_bf16_f32 v232, v232, v233\n" \
"v_cvt_pk_bf16_f32 v233, v234, v235\n" \
"v_mfma_f32_16x16x32_bf16 a[36:39], a[132:135], v[140:143], a[36:39]\n" \
"s_add_u32 m0, s79, 40960\n" \
"v_cvt_pk_bf16_f32 v234, v236, v237\n" \
"v_cvt_pk_bf16_f32 v235, v238, v239\n" \
"v_cvt_pk_bf16_f32 v208, v208, v209\n" \
"v_mfma_f32_16x16x32_bf16 a[8:11], a[136:139], v[136:139], a[8:11]\n" \
"ds_write_b128 v186, v[232:235] offset:5120\n" \
"v_cvt_pk_bf16_f32 v209, v210, v211\n" \
"v_cvt_pk_bf16_f32 v210, v212, v213\n" \
"v_mfma_f32_16x16x32_bf16 a[12:15], a[140:143], v[136:139], a[12:15]\n" \
"buffer_load_dwordx4 v12, s[28:31], s81 offen offset:0 lds\n" \
"v_mfma_f32_16x16x32_bf16 a[40:43], a[136:139], v[140:143], a[40:43]\n" \
"s_add_u32 m0, s79, 41856\n" \
"v_cvt_pk_bf16_f32 v211, v214, v215\n" \
"v_cvt_pk_bf16_f32 v240, v240, v241\n" \
"v_cvt_pk_bf16_f32 v241, v242, v243\n" \
"v_mfma_f32_16x16x32_bf16 a[44:47], a[140:143], v[140:143], a[44:47]\n" \
"ds_write_b128 v186, v[208:211] offset:2048\n" \
"v_cvt_pk_bf16_f32 v242, v244, v245\n" \
"v_cvt_pk_bf16_f32 v243, v246, v247\n" \
"v_mfma_f32_16x16x32_bf16 a[16:19], a[144:147], v[136:139], a[16:19]\n" \
"ds_write_b128 v186, v[240:243] offset:6144\n" \
"v_cvt_pk_bf16_f32 v216, v216, v217\n" \
"v_cvt_pk_bf16_f32 v217, v218, v219\n" \
"v_mfma_f32_16x16x32_bf16 a[20:23], a[148:151], v[136:139], a[20:23]\n" \
"buffer_load_dwordx4 v12, s[28:31], s81 offen offset:128 lds\n" \
"v_mfma_f32_16x16x32_bf16 a[48:51], a[144:147], v[140:143], a[48:51]\n" \
"s_add_u32 s80, s65, 16384\n" \
"v_cvt_pk_bf16_f32 v218, v220, v221\n" \
"v_cvt_pk_bf16_f32 v219, v222, v223\n" \
"v_cvt_pk_bf16_f32 v248, v248, v249\n" \
"v_mfma_f32_16x16x32_bf16 a[52:55], a[148:151], v[140:143], a[52:55]\n" \
"s_add_u32 m0, s22, 49152\n" \
"ds_write_b128 v186, v[216:219] offset:3072\n" \
"v_cvt_pk_bf16_f32 v249, v250, v251\n" \
"v_cvt_pk_bf16_f32 v250, v252, v253\n" \
"v_mfma_f32_16x16x32_bf16 a[24:27], a[152:155], v[136:139], a[24:27]\n" \
"v_cvt_pk_bf16_f32 v251, v254, v255\n" \
"v_and_b32 v188, 16, v4\n" \
"v_mov_b32 v189, 0x3f80\n" \
"v_mfma_f32_16x16x32_bf16 a[28:31], a[156:159], v[136:139], a[28:31]\n" \
"buffer_load_dwordx4 v4, s[36:39], s80 offen offset:0 sc1 lds\n" \
"v_mfma_f32_16x16x32_bf16 a[56:59], a[152:155], v[140:143], a[56:59]\n" \
"s_waitcnt lgkmcnt(14)\n" \
"ds_write_b128 v186, v[248:251] offset:7168\n" \
"v_lshlrev_b32 v188, v188, v189\n" \
"v_mov_b32 v22, 0\n" \
"v_mfma_f32_16x16x32_bf16 a[60:63], a[156:159], v[140:143], a[60:63]\n" \
"s_mov_b32 s94, 0x300003\n" \
"v_mov_b32 v23, 0\n" \
"v_mov_b32 v26, 0\n" \
"v_mov_b32 v27, 0\n" \
"v_mfma_f32_16x16x32_bf16 v[32:35], a[160:163], v[96:99], 0\n" \
"s_mov_b32 s95, 0x30000300\n" \
"v_cndmask_b32 v24, 0, v188, s[94:95]\n" \
"s_mov_b32 s94, 0xc0000c\n" \
"v_mfma_f32_16x16x32_bf16 v[32:35], a[168:171], v[100:103], v[32:35]\n" \
"s_waitcnt lgkmcnt(0)\n" \
"s_mov_b32 s95, 0xc0000c00\n" \
"v_cndmask_b32 v25, 0, v188, s[94:95]\n" \
"v_mfma_f32_16x16x32_bf16 v[36:39], a[164:167], v[96:99], 0\n" \
"s_waitcnt lgkmcnt(13)\n" \
"s_barrier\n" \
"ds_read2_b32 v[190:191], v185 offset1:64\n" \
"v_mfma_f32_16x16x32_bf16 v[36:39], a[172:175], v[100:103], v[36:39]\n" \
"buffer_load_dwordx4 v4, s[36:39], s80 offen offset:1024 sc1 lds\n" \
"v_mfma_f32_16x16x32_bf16 v[40:43], a[160:163], v[104:107], 0\n" \
"ds_read_b128 v[96:99], v29 offset:40960\n" \
"v_add_u32 v176, 0xfffe0000, v13\n" \
"v_mfma_f32_16x16x32_bf16 v[40:43], a[168:171], v[108:111], v[40:43]\n" \
"ds_read_b128 v[128:131], v187 offset:0\n" \
"v_add_u32 v177, 0xfffe0000, v14\n" \
"v_mfma_f32_16x16x32_bf16 v[44:47], a[164:167], v[104:107], 0\n" \
"s_waitcnt lgkmcnt(14)\n" \
"ds_read_b128 v[100:103], v29 offset:43008\n" \
"v_add_u32 v178, 0xfffe0000, v15\n" \
"v_mfma_f32_16x16x32_bf16 v[44:47], a[172:175], v[108:111], v[44:47]\n" \
"buffer_load_dwordx4 v4, s[36:39], s80 offen offset:2048 sc1 lds\n" \
"v_mfma_f32_16x16x32_bf16 v[48:51], a[160:163], v[112:115], 0\n" \
"ds_read_b128 v[104:107], v29 offset:41984\n" \
"v_add_u32 v179, 0xfffe0000, v16\n" \
"v_mfma_f32_16x16x32_bf16 v[48:51], a[168:171], v[116:119], v[48:51]\n" \
"ds_read_b128 v[132:135], v187 offset:1024\n" \
"v_add_u32 v180, 0xffff0000, v13\n" \
"v_mfma_f32_16x16x32_bf16 v[52:55], a[164:167], v[112:115], 0\n" \
"s_waitcnt lgkmcnt(15)\n" \
"ds_read_b128 v[108:111], v29 offset:44032\n" \
"s_waitcnt vmcnt(7)\n" \
"v_mfma_f32_16x16x32_bf16 v[52:55], a[172:175], v[116:119], v[52:55]\n" \
"buffer_load_dwordx4 v4, s[36:39], s80 offen offset:3072 sc1 lds\n" \
"v_mfma_f32_16x16x32_bf16 v[56:59], a[160:163], v[120:123], 0\n" \
"s_mov_b32 s81, 917504\n" \
"ds_read_b128 v[112:115], v29 offset:45056\n" \
"s_add_u32 m0, s79, 81920\n" \
"v_mfma_f32_16x16x32_bf16 v[56:59], a[168:171], v[124:127], v[56:59]\n" \
"s_cmp_lg_u64 s[54:55], 0\ns_cbranch_scc1 LFBC_%=\nLFBCR_%=:\n" \
"ds_read_b128 v[136:139], v187 offset:2048\n" \
"v_mfma_f32_16x16x32_bf16 v[60:63], a[164:167], v[120:123], 0\n" \
"ds_read_b128 v[116:119], v29 offset:47104\n" \
"v_add_u32 v181, 0xffff0000, v14\n" \
"v_mfma_f32_16x16x32_bf16 v[60:63], a[172:175], v[124:127], v[60:63]\n" \
"s_waitcnt vmcnt(16) lgkmcnt(6)\n" \
"s_barrier\n" \
"ds_read_b128 v[140:143], v187 offset:3072\n" \
"v_mfma_f32_16x16x32_bf16 v[64:67], a[160:163], v[96:99], 0\n" \
"ds_read_b128 v[120:123], v29 offset:46080\n" \
"v_add_u32 v182, 0xffff0000, v15\n" \
"v_mfma_f32_16x16x32_bf16 v[64:67], a[168:171], v[100:103], v[64:67]\n" \
"buffer_store_dwordx2 v[10:11], v9, s[36:39], s52 offen sc1\n" \
"v_mfma_f32_16x16x32_bf16 v[68:71], a[164:167], v[96:99], 0\n" \
"s_waitcnt lgkmcnt(5)\n" \
"ds_read_b128 v[124:127], v29 offset:48128\n" \
"v_add_u32 v183, 0xffff0000, v16\n" \
"v_mfma_f32_16x16x32_bf16 v[68:71], a[172:175], v[100:103], v[68:71]\n" \
"buffer_load_dwordx4 v12, s[28:31], s81 offen offset:0 lds\n" \
"v_mfma_f32_16x16x32_bf16 v[72:75], a[160:163], v[104:107], 0\n" \
"s_add_u32 m0, s79, 82816\n" \
"ds_read_b128 v[96:99], v29 offset:49152\n" \
"v_bfe_u32 v18, v4, 7, 2\n" \
"v_mfma_f32_16x16x32_bf16 v[72:75], a[168:171], v[108:111], v[72:75]\n" \
"ds_read_b128 v[144:147], v187 offset:4096\n" \
"v_min_u32 v18, 0, v18\n" \
"v_mfma_f32_16x16x32_bf16 v[76:79], a[164:167], v[104:107], 0\n" \
"ds_read_b128 v[100:103], v29 offset:51200\n" \
"v_lshl_add_u32 v17, v18, 8, s10\n" \
"v_accvgpr_read_b32 v192, a0\n" \
"v_accvgpr_read_b32 v193, a1\n" \
"v_accvgpr_read_b32 v194, a2\n" \
"v_accvgpr_read_b32 v195, a3\n" \
"v_accvgpr_read_b32 v196, a4\n" \
"v_accvgpr_read_b32 v197, a5\n" \
"v_accvgpr_read_b32 v198, a6\n" \
"v_accvgpr_read_b32 v199, a7\n" \
"v_mfma_f32_16x16x32_bf16 v[76:79], a[172:175], v[108:111], v[76:79]\n" \
"LH25_%=:\ns_cbranch_execz LH29_%=\n" \
"s_waitcnt lgkmcnt(13)\n" \
"ds_read_b128 v[148:151], v187 offset:5120\n" \
"v_bfe_u32 v18, v4, 4, 3\n" \
"v_mfma_f32_16x16x32_bf16 v[192:195], v[24:27], v[128:131], v[192:195]\n" \
"s_waitcnt lgkmcnt(7)\n" \
"ds_read_b128 v[152:155], v187 offset:6144\n" \
"v_lshl_add_u32 v17, v18, 3, v17\n" \
"v_mfma_f32_16x16x32_bf16 v[196:199], v[22:25], v[128:131], v[196:199]\n" \
"buffer_load_dwordx4 v12, s[28:31], s81 offen offset:128 lds\n" \
"v_mfma_f32_16x16x32_bf16 v[32:35], a[176:179], v[112:115], v[32:35]\n" \
"s_mov_b32 s81, 983040\n" \
"ds_read_b128 v[104:107], v29 offset:50176\n" \
"s_add_u32 m0, s79, 90112\n" \
"v_mfma_f32_16x16x32_bf16 v[32:35], a[184:187], v[116:119], v[32:35]\n" \
"s_andn2_b32 s94, s88, 0xc0\n" \
"ds_read_b128 v[156:159], v187 offset:7168\n" \
"v_and_b32 v18, 0x30, v4\n" \
"v_mfma_f32_16x16x32_bf16 v[36:39], a[180:183], v[112:115], v[36:39]\n" \
"s_waitcnt lgkmcnt(7)\n" \
"ds_read_b128 v[108:111], v29 offset:52224\n" \
"s_waitcnt lgkmcnt(15)\n" \
"v_mfma_f32_16x16x32_bf16 v[36:39], a[188:191], v[116:119], v[36:39]\n" \
"buffer_load_dwordx4 v12, s[28:31], s81 offen offset:0 lds\n" \
"v_mfma_f32_16x16x32_bf16 v[40:43], a[176:179], v[120:123], v[40:43]\n" \
"s_add_u32 m0, s79, 91008\n" \
"ds_read_b128 v[112:115], v29 offset:53248\n" \
"v_add_f32 v164, v164, v165\n" \
"v_mfma_f32_16x16x32_bf16 v[40:43], a[184:187], v[124:127], v[40:43]\n" \
"s_mov_b32 exec_lo, 0\nv_lshl_add_u32 v17, v18, 2, s94\ns_mov_b32 exec_lo, -1\n" \
"v_mfma_f32_16x16x32_bf16 v[44:47], a[180:183], v[120:123], v[44:47]\n" \
"s_sub_u32 s96, s72, 109051904\n" \
"ds_read_b128 v[116:119], v29 offset:55296\n" \
"s_lshr_b32 s96, s96, 11\n" \
"v_mfma_f32_16x16x32_bf16 v[44:47], a[188:191], v[124:127], v[44:47]\n" \
"s_waitcnt lgkmcnt(3)\n" \
"s_barrier\n" \
"s_mul_i32 s97, s96, 10923\n" \
"v_mfma_f32_16x16x32_bf16 v[48:51], a[176:179], v[96:99], v[48:51]\n" \
"s_lshr_b32 s97, s97, 16\n" \
"ds_read_b128 v[120:123], v29 offset:54272\n" \
"s_lshl_b32 s97, s97, 1\n" \
"v_mfma_f32_16x16x32_bf16 v[48:51], a[184:187], v[100:103], v[48:51]\n" \
"buffer_load_dwordx4 v12, s[28:31], s81 offen offset:128 lds\n" \
"v_mfma_f32_16x16x32_bf16 v[52:55], a[180:183], v[96:99], v[52:55]\n" \
"s_add_u32 s80, s66, 16384\n" \
"ds_read_b128 v[124:127], v29 offset:56320\n" \
"s_add_u32 m0, s22, 65536\n" \
"v_accvgpr_read_b32 v200, a8\n" \
"v_accvgpr_read_b32 v201, a9\n" \
"v_accvgpr_read_b32 v202, a10\n" \
"v_accvgpr_read_b32 v203, a11\n" \
"v_accvgpr_read_b32 v204, a12\n" \
"v_accvgpr_read_b32 v205, a13\n" \
"v_accvgpr_read_b32 v206, a14\n" \
"v_accvgpr_read_b32 v207, a15\n" \
"v_mfma_f32_16x16x32_bf16 v[52:55], a[188:191], v[100:103], v[52:55]\n" \
"s_waitcnt lgkmcnt(15)\n" \
"s_sub_u32 s96, s96, s97\n" \
"s_lshl_b32 s96, s96, 3\n" \
"v_mfma_f32_16x16x32_bf16 v[200:203], v[24:27], v[132:135], v[200:203]\n" \
"s_waitcnt lgkmcnt(4)\n" \
"s_add_u32 s23, s96, 94437376\n" \
"s_sub_u32 s96, s73, 109051904\n" \
"v_mfma_f32_16x16x32_bf16 v[204:207], v[22:25], v[132:135], v[204:207]\n" \
"buffer_load_dwordx4 v4, s[36:39], s80 offen offset:0 sc1 lds\n" \
"v_mfma_f32_16x16x32_bf16 v[56:59], a[176:179], v[104:107], v[56:59]\n" \
"s_lshr_b32 s96, s96, 11\n" \
"ds_read_b128 v[96:99], v29 offset:57344\n" \
"s_mul_i32 s97, s96, 10923\n" \
"v_mfma_f32_16x16x32_bf16 v[56:59], a[184:187], v[108:111], v[56:59]\n" \
"s_lshr_b32 s97, s97, 16\n" \
"s_lshl_b32 s97, s97, 1\n" \
"s_sub_u32 s96, s96, s97\n" \
"v_mfma_f32_16x16x32_bf16 v[60:63], a[180:183], v[104:107], v[60:63]\n" \
"s_waitcnt lgkmcnt(3)\n" \
"ds_read_b128 v[100:103], v29 offset:59392\n" \
"s_lshl_b32 s96, s96, 3\n" \
"v_mfma_f32_16x16x32_bf16 v[60:63], a[188:191], v[108:111], v[60:63]\n" \
"buffer_load_dwordx4 v4, s[36:39], s80 offen offset:1024 sc1 lds\n" \
"v_mfma_f32_16x16x32_bf16 v[64:67], a[176:179], v[112:115], v[64:67]\n" \
"s_add_u32 s24, s96, 94437376\n" \
"ds_read_b128 v[104:107], v29 offset:58368\n" \
"s_sub_u32 s96, s74, 109051904\n" \
"v_mfma_f32_16x16x32_bf16 v[64:67], a[184:187], v[116:119], v[64:67]\n" \
"s_lshr_b32 s96, s96, 11\n" \
"s_mul_i32 s97, s96, 10923\n" \
"s_lshr_b32 s97, s97, 16\n" \
"v_mfma_f32_16x16x32_bf16 v[68:71], a[180:183], v[112:115], v[68:71]\n" \
"s_waitcnt lgkmcnt(3)\n" \
"ds_read_b128 v[108:111], v29 offset:60416\n" \
"s_lshl_b32 s97, s97, 1\n" \
"v_mfma_f32_16x16x32_bf16 v[68:71], a[188:191], v[116:119], v[68:71]\n" \
"buffer_load_dwordx4 v4, s[36:39], s80 offen offset:2048 sc1 lds\n" \
"v_mfma_f32_16x16x32_bf16 v[72:75], a[176:179], v[120:123], v[72:75]\n" \
"s_sub_u32 s96, s96, s97\n" \
"ds_read_b128 v[112:115], v29 offset:61440\n" \
"s_lshl_b32 s96, s96, 3\n" \
"v_mfma_f32_16x16x32_bf16 v[72:75], a[184:187], v[124:127], v[72:75]\n" \
"s_add_u32 s45, s96, 94437376\n" \
"v_add_f32 v166, v166, v167\n" \
"v_add_f32 v190, v164, v190\n" \
"v_mfma_f32_16x16x32_bf16 v[76:79], a[180:183], v[120:123], v[76:79]\n" \
"ds_read_b128 v[116:119], v29 offset:63488\n" \
"v_add_f32 v191, v166, v191\n" \
"v_accvgpr_read_b32 v208, a16\n" \
"v_accvgpr_read_b32 v209, a17\n" \
"v_accvgpr_read_b32 v210, a18\n" \
"v_accvgpr_read_b32 v211, a19\n" \
"v_accvgpr_read_b32 v212, a20\n" \
"v_accvgpr_read_b32 v213, a21\n" \
"v_accvgpr_read_b32 v214, a22\n" \
"v_accvgpr_read_b32 v215, a23\n" \
"v_mfma_f32_16x16x32_bf16 v[76:79], a[188:191], v[124:127], v[76:79]\n" \
"s_waitcnt lgkmcnt(15)\n" \
"v_subrev_u32 v188, 0x1f800000, v190\n" \
"v_mfma_f32_16x16x32_bf16 v[208:211], v[24:27], v[136:139], v[208:211]\n" \
"buffer_load_dwordx4 v4, s[36:39], s80 offen offset:3072 sc1 lds\n" \
"v_mfma_f32_16x16x32_bf16 v[212:215], v[22:25], v[136:139], v[212:215]\n" \
"s_waitcnt vmcnt(21) lgkmcnt(4)\n" \
"s_barrier\n" \
"s_mov_b32 s81, 1048576\n" \
"v_mfma_f32_16x16x32_bf16 v[32:35], a[192:195], v[96:99], v[32:35]\n" \
"s_add_u32 m0, s79, 98304\n" \
"ds_read_b128 v[120:123], v29 offset:62464\n" \
"v_subrev_u32 v189, 0x1f800000, v191\n" \
"v_mfma_f32_16x16x32_bf16 v[32:35], a[200:203], v[100:103], v[32:35]\n" \
"v_max_u32 v188, v188, v189\n" \
"v_cmp_le_u32 vcc, 0x40000000, v188\n" \
"v_mfma_f32_16x16x32_bf16 v[36:39], a[196:199], v[96:99], v[36:39]\n" \
"s_waitcnt lgkmcnt(3)\n" \
"ds_read_b128 v[124:127], v29 offset:64512\n" \
"s_nop 0\n" \
"v_mfma_f32_16x16x32_bf16 v[36:39], a[204:207], v[100:103], v[36:39]\n" \
"buffer_load_dwordx4 v12, s[28:31], s81 offen offset:0 lds\n" \
"v_mfma_f32_16x16x32_bf16 v[40:43], a[192:195], v[104:107], v[40:43]\n" \
"s_add_u32 m0, s79, 99200\n" \
"ds_read_b128 v[96:99], v28 offset:0\n" \
"s_or_b64 s[4:5], s[4:5], vcc\n" \
"v_mfma_f32_16x16x32_bf16 v[40:43], a[200:203], v[108:111], v[40:43]\n" \
"v_rcp_f32 v186, v190\n" \
"v_mfma_f32_16x16x32_bf16 v[44:47], a[196:199], v[104:107], v[44:47]\n" \
"s_waitcnt lgkmcnt(3)\n" \
"ds_read_b128 v[100:103], v28 offset:2048\n" \
"v_accvgpr_read_b32 v216, a24\n" \
"v_mfma_f32_16x16x32_bf16 v[44:47], a[204:207], v[108:111], v[44:47]\n" \
"buffer_load_dwordx4 v12, s[28:31], s81 offen offset:128 lds\n" \
"v_mfma_f32_16x16x32_bf16 v[48:51], a[192:195], v[112:115], v[48:51]\n" \
"s_mov_b32 s81, 1114112\n" \
"ds_read_b128 v[104:107], v28 offset:1024\n" \
"s_add_u32 m0, s79, 106496\n" \
"v_mfma_f32_16x16x32_bf16 v[48:51], a[200:203], v[116:119], v[48:51]\n" \
"v_rcp_f32 v187, v191\n" \
"v_mfma_f32_16x16x32_bf16 v[52:55], a[196:199], v[112:115], v[52:55]\n" \
"ds_read_b128 v[108:111], v28 offset:3072\n" \
"v_mul_f32 v192, v186, v192\n" \
"v_accvgpr_read_b32 v217, a25\n" \
"v_accvgpr_read_b32 v218, a26\n" \
"v_accvgpr_read_b32 v219, a27\n" \
"v_accvgpr_read_b32 v220, a28\n" \
"v_accvgpr_read_b32 v221, a29\n" \
"v_accvgpr_read_b32 v222, a30\n" \
"v_accvgpr_read_b32 v223, a31\n" \
"v_mfma_f32_16x16x32_bf16 v[52:55], a[204:207], v[116:119], v[52:55]\n" \
"s_waitcnt lgkmcnt(15)\n" \
"v_mul_f32 v193, v186, v193\n" \
"v_mul_f32 v194, v186, v194\n" \
"v_mfma_f32_16x16x32_bf16 v[216:219], v[24:27], v[140:143], v[216:219]\n" \
"s_waitcnt lgkmcnt(4)\n" \
"v_mul_f32 v195, v186, v195\n" \
"v_mfma_f32_16x16x32_bf16 v[220:223], v[22:25], v[140:143], v[220:223]\n" \
"buffer_load_dwordx4 v12, s[28:31], s81 offen offset:0 lds\n" \
"v_mfma_f32_16x16x32_bf16 v[56:59], a[192:195], v[120:123], v[56:59]\n" \
"s_add_u32 m0, s79, 107392\n" \
"ds_read_b128 v[112:115], v28 offset:4096\n" \
"v_mul_f32 v196, v186, v196\n" \
"v_mfma_f32_16x16x32_bf16 v[56:59], a[200:203], v[124:127], v[56:59]\n" \
"v_mul_f32 v197, v186, v197\n" \
"v_mul_f32 v198, v186, v198\n" \
"v_mfma_f32_16x16x32_bf16 v[60:63], a[196:199], v[120:123], v[60:63]\n" \
"ds_read_b128 v[116:119], v28 offset:6144\n" \
"v_mul_f32 v199, v186, v199\n" \
"v_mfma_f32_16x16x32_bf16 v[60:63], a[204:207], v[124:127], v[60:63]\n" \
"s_waitcnt vmcnt(16) lgkmcnt(4)\n" \
"s_barrier\n" \
"v_cvt_pk_bf16_f32 v192, v192, v193\n" \
"v_mfma_f32_16x16x32_bf16 v[64:67], a[192:195], v[96:99], v[64:67]\n" \
"ds_read_b128 v[120:123], v28 offset:5120\n" \
"v_cvt_pk_bf16_f32 v193, v194, v195\n" \
"v_mfma_f32_16x16x32_bf16 v[64:67], a[200:203], v[100:103], v[64:67]\n" \
"buffer_load_dwordx4 v12, s[28:31], s81 offen offset:128 lds\n" \
"v_mfma_f32_16x16x32_bf16 v[68:71], a[196:199], v[96:99], v[68:71]\n" \
"s_waitcnt lgkmcnt(3)\n" \
"ds_read_b128 v[124:127], v28 offset:7168\n" \
"s_mov_b32 s81, 1179648\n" \
"v_mfma_f32_16x16x32_bf16 v[68:71], a[204:207], v[100:103], v[68:71]\n" \
"s_add_u32 m0, s79, 131072\n" \
"ds_read_b64_tr_b16 a[64:65], v176 offset:32768\n" \
"v_cvt_pk_bf16_f32 v194, v196, v197\n" \
"v_mfma_f32_16x16x32_bf16 v[72:75], a[192:195], v[104:107], v[72:75]\n" \
"ds_read_b128 v[96:99], v28 offset:8192\n" \
"v_cvt_pk_bf16_f32 v195, v198, v199\n" \
"v_mfma_f32_16x16x32_bf16 v[72:75], a[200:203], v[108:111], v[72:75]\n" \
"buffer_store_dwordx4 v[192:195], v4, s[36:39], s51 offen offset:0 sc1\n" \
"v_mfma_f32_16x16x32_bf16 v[76:79], a[196:199], v[104:107], v[76:79]\n" \
"ds_read_b128 v[100:103], v28 offset:10240\n" \
"v_mul_f32 v200, v186, v200\n" \
"v_accvgpr_read_b32 v224, a32\n" \
"v_accvgpr_read_b32 v225, a33\n" \
"v_accvgpr_read_b32 v226, a34\n" \
"v_accvgpr_read_b32 v227, a35\n" \
"v_accvgpr_read_b32 v228, a36\n" \
"v_accvgpr_read_b32 v229, a37\n" \
"v_accvgpr_read_b32 v230, a38\n" \
"v_accvgpr_read_b32 v231, a39\n" \
"v_mfma_f32_16x16x32_bf16 v[76:79], a[204:207], v[108:111], v[76:79]\n" \
"s_waitcnt lgkmcnt(15)\n" \
"ds_read_b64_tr_b16 a[66:67], v177 offset:32768\n" \
"v_mul_f32 v201, v186, v201\n" \
"v_mfma_f32_16x16x32_bf16 v[224:227], v[24:27], v[144:147], v[224:227]\n" \
"s_waitcnt lgkmcnt(6)\n" \
"ds_read_b64_tr_b16 a[68:69], v178 offset:32768\n" \
"v_mul_f32 v202, v186, v202\n" \
"v_mfma_f32_16x16x32_bf16 v[228:231], v[22:25], v[144:147], v[228:231]\n" \
"buffer_load_dwordx2 v[18:19], v17, s[36:39], 0 offen sc1\n" \
"v_mfma_f32_16x16x32_bf16 v[32:35], a[208:211], v[112:115], v[32:35]\n" \
"ds_read_b128 v[104:107], v28 offset:9216\n" \
"v_mul_f32 v203, v186, v203\n" \
"v_mfma_f32_16x16x32_bf16 v[32:35], a[216:219], v[116:119], v[32:35]\n" \
"ds_read_b64_tr_b16 a[70:71], v179 offset:32768\n" \
"v_mul_f32 v204, v186, v204\n" \
"v_mfma_f32_16x16x32_bf16 v[36:39], a[212:215], v[112:115], v[36:39]\n" \
"s_waitcnt lgkmcnt(7)\n" \
"ds_read_b128 v[108:111], v28 offset:11264\n" \
"v_mul_f32 v205, v186, v205\n" \
"v_mfma_f32_16x16x32_bf16 v[36:39], a[220:223], v[116:119], v[36:39]\n" \
"buffer_load_dwordx4 v12, s[28:31], s81 offen offset:0 lds\n" \
"v_mfma_f32_16x16x32_bf16 v[40:43], a[208:211], v[120:123], v[40:43]\n" \
"s_add_u32 m0, s79, 131968\n" \
"ds_read_b128 v[112:115], v28 offset:12288\n" \
"v_mul_f32 v206, v186, v206\n" \
"v_mfma_f32_16x16x32_bf16 v[40:43], a[216:219], v[124:127], v[40:43]\n" \
"ds_read_b64_tr_b16 a[72:73], v176 offset:40960\n" \
"v_mul_f32 v207, v186, v207\n" \
"v_mfma_f32_16x16x32_bf16 v[44:47], a[212:215], v[120:123], v[44:47]\n" \
"ds_read_b128 v[116:119], v28 offset:14336\n" \
"v_cvt_pk_bf16_f32 v200, v200, v201\n" \
"v_mfma_f32_16x16x32_bf16 v[44:47], a[220:223], v[124:127], v[44:47]\n" \
"s_waitcnt vmcnt(24) lgkmcnt(8)\n" \
"s_barrier\n" \
"ds_read_b64_tr_b16 a[74:75], v177 offset:40960\n" \
"v_mfma_f32_16x16x32_bf16 v[48:51], a[208:211], v[96:99], v[48:51]\n" \
"ds_read_b128 v[120:123], v28 offset:13312\n" \
"v_cvt_pk_bf16_f32 v201, v202, v203\n" \
"v_mfma_f32_16x16x32_bf16 v[48:51], a[216:219], v[100:103], v[48:51]\n" \
"buffer_load_dwordx4 v12, s[28:31], s81 offen offset:128 lds\n" \
"v_mfma_f32_16x16x32_bf16 v[52:55], a[212:215], v[96:99], v[52:55]\n" \
"s_mov_b32 s81, 1245184\n" \
"ds_read_b128 v[124:127], v28 offset:15360\n" \
"s_add_u32 m0, s79, 139264\n" \
"v_accvgpr_read_b32 v232, a40\n" \
"v_accvgpr_read_b32 v233, a41\n" \
"v_accvgpr_read_b32 v234, a42\n" \
"v_accvgpr_read_b32 v235, a43\n" \
"v_accvgpr_read_b32 v236, a44\n" \
"v_accvgpr_read_b32 v237, a45\n" \
"v_accvgpr_read_b32 v238, a46\n" \
"v_accvgpr_read_b32 v239, a47\n" \
"v_mfma_f32_16x16x32_bf16 v[52:55], a[220:223], v[100:103], v[52:55]\n" \
"s_waitcnt lgkmcnt(15)\n" \
"ds_read_b64_tr_b16 a[76:77], v178 offset:40960\n" \
"v_cvt_pk_bf16_f32 v202, v204, v205\n" \
"v_mfma_f32_16x16x32_bf16 v[232:235], v[24:27], v[148:151], v[232:235]\n" \
"s_waitcnt lgkmcnt(7)\n" \
"ds_read_b64_tr_b16 a[78:79], v179 offset:40960\n" \
"v_cvt_pk_bf16_f32 v203, v206, v207\n" \
"v_mfma_f32_16x16x32_bf16 v[236:239], v[22:25], v[148:151], v[236:239]\n" \
"buffer_store_dwordx4 v[200:203], v4, s[36:39], s51 offen offset:2048 sc1\n" \
"v_mfma_f32_16x16x32_bf16 v[56:59], a[208:211], v[104:107], v[56:59]\n" \
"ds_read_b128 v[96:99], v28 offset:16384\n" \
"v_mul_f32 v208, v186, v208\n" \
"v_mfma_f32_16x16x32_bf16 v[56:59], a[216:219], v[108:111], v[56:59]\n" \
"v_mul_f32 v209, v186, v209\n" \
"v_mul_f32 v210, v186, v210\n" \
"v_mfma_f32_16x16x32_bf16 v[60:63], a[212:215], v[104:107], v[60:63]\n" \
"s_waitcnt lgkmcnt(6)\n" \
"ds_read_b128 v[100:103], v28 offset:18432\n" \
"v_mul_f32 v211, v186, v211\n" \
"v_mfma_f32_16x16x32_bf16 v[60:63], a[220:223], v[108:111], v[60:63]\n" \
"buffer_load_dwordx4 v12, s[28:31], s81 offen offset:0 lds\n" \
"v_mfma_f32_16x16x32_bf16 v[64:67], a[208:211], v[112:115], v[64:67]\n" \
"s_add_u32 m0, s79, 140160\n" \
"ds_read_b128 v[104:107], v28 offset:17408\n" \
"v_mul_f32 v212, v186, v212\n" \
"v_mfma_f32_16x16x32_bf16 v[64:67], a[216:219], v[116:119], v[64:67]\n" \
"v_mul_f32 v213, v186, v213\n" \
"v_mul_f32 v214, v186, v214\n" \
"v_mfma_f32_16x16x32_bf16 v[68:71], a[212:215], v[112:115], v[68:71]\n" \
"s_waitcnt lgkmcnt(5)\n" \
"ds_read_b128 v[108:111], v28 offset:19456\n" \
"v_mul_f32 v215, v186, v215\n" \
"v_mfma_f32_16x16x32_bf16 v[68:71], a[220:223], v[116:119], v[68:71]\n" \
"buffer_load_dwordx4 v12, s[28:31], s81 offen offset:128 lds\n" \
"v_mfma_f32_16x16x32_bf16 v[72:75], a[208:211], v[120:123], v[72:75]\n" \
"s_add_u32 s80, s64, 32768\n" \
"ds_read_b128 v[112:115], v28 offset:20480\n" \
"s_add_u32 m0, s22, 147456\n" \
"v_mfma_f32_16x16x32_bf16 v[72:75], a[216:219], v[124:127], v[72:75]\n" \
"v_cvt_pk_bf16_f32 v208, v208, v209\n" \
"v_cvt_pk_bf16_f32 v209, v210, v211\n" \
"v_mfma_f32_16x16x32_bf16 v[76:79], a[212:215], v[120:123], v[76:79]\n" \
"ds_read_b128 v[116:119], v28 offset:22528\n" \
"v_cvt_pk_bf16_f32 v210, v212, v213\n" \
"v_accvgpr_read_b32 v240, a48\n" \
"v_accvgpr_read_b32 v241, a49\n" \
"v_accvgpr_read_b32 v242, a50\n" \
"v_accvgpr_read_b32 v243, a51\n" \
"v_accvgpr_read_b32 v244, a52\n" \
"v_accvgpr_read_b32 v245, a53\n" \
"v_accvgpr_read_b32 v246, a54\n" \
"v_accvgpr_read_b32 v247, a55\n" \
"v_mfma_f32_16x16x32_bf16 v[76:79], a[220:223], v[124:127], v[76:79]\n" \
"s_waitcnt lgkmcnt(15)\n" \
"v_cvt_pk_bf16_f32 v211, v214, v215\n" \
"v_mfma_f32_16x16x32_bf16 v[240:243], v[24:27], v[152:155], v[240:243]\n" \
"buffer_store_dwordx4 v[208:211], v7, s[36:39], s51 offen offset:0 sc1\n" \
"v_mfma_f32_16x16x32_bf16 v[244:247], v[22:25], v[152:155], v[244:247]\n" \
"s_waitcnt vmcnt(16) lgkmcnt(4)\n" \
"s_barrier\n" \
"v_mul_f32 v216, v186, v216\n" \
"v_mfma_f32_16x16x32_bf16 v[32:35], a[224:227], v[96:99], v[32:35]\n" \
"ds_read_b128 v[120:123], v28 offset:21504\n" \
"v_mul_f32 v217, v186, v217\n" \
"v_mfma_f32_16x16x32_bf16 v[32:35], a[232:235], v[100:103], v[32:35]\n" \
"ds_read_b64_tr_b16 a[80:81], v180 offset:16384\n" \
"v_mul_f32 v218, v186, v218\n" \
"v_mfma_f32_16x16x32_bf16 v[36:39], a[228:231], v[96:99], v[36:39]\n" \
"s_waitcnt lgkmcnt(4)\n" \
"ds_read_b128 v[124:127], v28 offset:23552\n" \
"v_mul_f32 v219, v186, v219\n" \
"v_mfma_f32_16x16x32_bf16 v[36:39], a[236:239], v[100:103], v[36:39]\n" \
"ds_read_b64_tr_b16 a[82:83], v181 offset:16384\n" \
"v_mul_f32 v220, v186, v220\n" \
"v_mfma_f32_16x16x32_bf16 v[40:43], a[224:227], v[104:107], v[40:43]\n" \
"ds_read_b128 v[96:99], v28 offset:24576\n" \
"v_mul_f32 v221, v186, v221\n" \
"v_mfma_f32_16x16x32_bf16 v[40:43], a[232:235], v[108:111], v[40:43]\n" \
"buffer_load_dwordx4 v4, s[36:39], s80 offen offset:0 sc1 lds\n" \
"v_mfma_f32_16x16x32_bf16 v[44:47], a[228:231], v[104:107], v[44:47]\n" \
"s_waitcnt lgkmcnt(5)\n" \
"ds_read_b128 v[100:103], v28 offset:26624\n" \
"v_mul_f32 v222, v186, v222\n" \
"v_mfma_f32_16x16x32_bf16 v[44:47], a[236:239], v[108:111], v[44:47]\n" \
"ds_read_b64_tr_b16 a[84:85], v182 offset:16384\n" \
"v_mul_f32 v223, v186, v223\n" \
"v_mfma_f32_16x16x32_bf16 v[48:51], a[224:227], v[112:115], v[48:51]\n" \
"ds_read_b128 v[104:107], v28 offset:25600\n" \
"v_cvt_pk_bf16_f32 v216, v216, v217\n" \
"v_mfma_f32_16x16x32_bf16 v[48:51], a[232:235], v[116:119], v[48:51]\n" \
"buffer_load_dwordx4 v4, s[36:39], s80 offen offset:1024 sc1 lds\n" \
"v_mfma_f32_16x16x32_bf16 v[52:55], a[228:231], v[112:115], v[52:55]\n" \
"ds_read_b128 v[108:111], v28 offset:27648\n" \
"v_cvt_pk_bf16_f32 v217, v218, v219\n" \
"v_accvgpr_read_b32 v248, a56\n" \
"v_accvgpr_read_b32 v249, a57\n" \
"v_accvgpr_read_b32 v250, a58\n" \
"v_accvgpr_read_b32 v251, a59\n" \
"v_accvgpr_read_b32 v252, a60\n" \
"v_accvgpr_read_b32 v253, a61\n" \
"v_accvgpr_read_b32 v254, a62\n" \
"v_accvgpr_read_b32 v255, a63\n" \
"v_mfma_f32_16x16x32_bf16 v[52:55], a[236:239], v[116:119], v[52:55]\n" \
"s_waitcnt lgkmcnt(15)\n" \
"ds_read_b64_tr_b16 a[86:87], v183 offset:16384\n" \
"v_cvt_pk_bf16_f32 v218, v220, v221\n" \
"v_mfma_f32_16x16x32_bf16 v[248:251], v[24:27], v[156:159], v[248:251]\n" \
"s_waitcnt lgkmcnt(7)\n" \
"s_waitcnt vmcnt(8)\n" \
"v_cmp_ne_u64 vcc, s[14:15], v[18:19]\n" \
"v_mfma_f32_16x16x32_bf16 v[252:255], v[22:25], v[156:159], v[252:255]\n" \
"buffer_load_dwordx4 v4, s[36:39], s80 offen offset:2048 sc1 lds\n" \
"v_mfma_f32_16x16x32_bf16 v[56:59], a[224:227], v[120:123], v[56:59]\n" \
"ds_read_b128 v[112:115], v28 offset:28672\n" \
"s_nop 0\n" \
"v_mfma_f32_16x16x32_bf16 v[56:59], a[232:235], v[124:127], v[56:59]\n" \
"s_cbranch_vccnz LPS_%=\nLPSR_%=:\n" \
"ds_read_b64_tr_b16 a[88:89], v180 offset:24576\n" \
"v_cvt_pk_bf16_f32 v219, v222, v223\n" \
"v_mfma_f32_16x16x32_bf16 v[60:63], a[228:231], v[120:123], v[60:63]\n" \
"ds_read_b128 v[116:119], v28 offset:30720\n" \
"v_mul_f32 v224, v187, v224\n" \
"v_mfma_f32_16x16x32_bf16 v[60:63], a[236:239], v[124:127], v[60:63]\n" \
"s_waitcnt vmcnt(24) lgkmcnt(7)\n" \
"s_barrier\n" \
"ds_read_b64_tr_b16 a[90:91], v181 offset:24576\n" \
"v_mfma_f32_16x16x32_bf16 v[64:67], a[224:227], v[96:99], v[64:67]\n" \
"ds_read_b128 v[120:123], v28 offset:29696\n" \
"v_mul_f32 v225, v187, v225\n" \
"v_mfma_f32_16x16x32_bf16 v[64:67], a[232:235], v[100:103], v[64:67]\n" \
"s_mov_b64 exec, 0xffff\nbuffer_load_dword v1, v2, s[36:39], s67 offen sc1\ns_mov_b64 exec, -1\n" \
"v_mfma_f32_16x16x32_bf16 v[68:71], a[228:231], v[96:99], v[68:71]\n" \
"s_waitcnt lgkmcnt(6)\n" \
"ds_read_b128 v[124:127], v28 offset:31744\n" \
"v_mul_f32 v226, v187, v226\n" \
"v_mfma_f32_16x16x32_bf16 v[68:71], a[236:239], v[100:103], v[68:71]\n" \
"buffer_store_dwordx4 v[216:219], v7, s[36:39], s51 offen offset:2048 sc1\n" \
"v_mfma_f32_16x16x32_bf16 v[72:75], a[224:227], v[104:107], v[72:75]\n" \
"ds_read_b128 v[96:99], v28 offset:49152\n" \
"v_mul_f32 v227, v187, v227\n" \
"v_mfma_f32_16x16x32_bf16 v[72:75], a[232:235], v[108:111], v[72:75]\n" \
"ds_read_b64_tr_b16 a[92:93], v182 offset:24576\n" \
"v_mul_f32 v228, v187, v228\n" \
"v_mfma_f32_16x16x32_bf16 v[76:79], a[228:231], v[104:107], v[76:79]\n" \
"s_waitcnt lgkmcnt(5)\n" \
"ds_read_b128 v[100:103], v28 offset:51200\n" \
"v_mul_f32 v229, v187, v229\n" \
"v_mfma_f32_16x16x32_bf16 v[76:79], a[236:239], v[108:111], v[76:79]\n" \
"ds_read_b64_tr_b16 a[94:95], v183 offset:24576\n" \
"v_mul_f32 v230, v187, v230\n" \
"v_mfma_f32_16x16x32_bf16 v[32:35], a[240:243], v[112:115], v[32:35]\n" \
"ds_read_b128 v[104:107], v28 offset:50176\n" \
"v_mul_f32 v231, v187, v231\n" \
"v_mfma_f32_16x16x32_bf16 v[32:35], a[248:251], v[116:119], v[32:35]\n" \
"buffer_load_dwordx4 v4, s[36:39], s80 offen offset:3072 sc1 lds\n" \
"v_mfma_f32_16x16x32_bf16 v[36:39], a[244:247], v[112:115], v[36:39]\n" \
"s_waitcnt lgkmcnt(5)\n" \
"ds_read_b128 v[108:111], v28 offset:52224\n" \
"s_add_u32 s80, s65, 32768\n" \
"v_mfma_f32_16x16x32_bf16 v[36:39], a[252:255], v[116:119], v[36:39]\n" \
"s_add_u32 m0, s22, 114688\n" \
"v_cvt_pk_bf16_f32 v224, v224, v225\n" \
"v_cvt_pk_bf16_f32 v225, v226, v227\n" \
"v_mfma_f32_16x16x32_bf16 v[40:43], a[240:243], v[120:123], v[40:43]\n" \
"ds_read_b128 v[112:115], v28 offset:53248\n" \
"v_cvt_pk_bf16_f32 v226, v228, v229\n" \
"v_mfma_f32_16x16x32_bf16 v[40:43], a[248:251], v[124:127], v[40:43]\n" \
"buffer_load_dwordx4 v4, s[36:39], s80 offen offset:0 sc1 lds\n" \
"v_mfma_f32_16x16x32_bf16 v[44:47], a[244:247], v[120:123], v[44:47]\n" \
"ds_read_b128 v[116:119], v28 offset:55296\n" \
"v_cvt_pk_bf16_f32 v227, v230, v231\n" \
"v_mfma_f32_16x16x32_bf16 v[44:47], a[252:255], v[124:127], v[44:47]\n" \
"s_waitcnt lgkmcnt(4)\n" \
"s_barrier\n" \
"v_mul_f32 v232, v187, v232\n" \
"v_mfma_f32_16x16x32_bf16 v[48:51], a[240:243], v[96:99], v[48:51]\n" \
"ds_read_b128 v[120:123], v28 offset:54272\n" \
"v_mul_f32 v233, v187, v233\n" \
"v_mfma_f32_16x16x32_bf16 v[48:51], a[248:251], v[100:103], v[48:51]\n" \
"buffer_store_dwordx4 v[224:227], v4, s[36:39], s51 offen offset:1024 sc1\n" \
"v_mfma_f32_16x16x32_bf16 v[52:55], a[244:247], v[96:99], v[52:55]\n" \
"s_waitcnt lgkmcnt(3)\n" \
"ds_read_b128 v[124:127], v28 offset:56320\n" \
"v_mul_f32 v234, v187, v234\n" \
"v_mfma_f32_16x16x32_bf16 v[52:55], a[252:255], v[100:103], v[52:55]\n" \
"v_mul_f32 v235, v187, v235\n" \
"v_mul_f32 v236, v187, v236\n" \
"v_mfma_f32_16x16x32_bf16 v[56:59], a[240:243], v[104:107], v[56:59]\n" \
"ds_read_b128 v[96:99], v28 offset:57344\n" \
"v_mul_f32 v237, v187, v237\n" \
"v_mfma_f32_16x16x32_bf16 v[56:59], a[248:251], v[108:111], v[56:59]\n" \
"v_mul_f32 v238, v187, v238\n" \
"v_mul_f32 v239, v187, v239\n" \
"v_mfma_f32_16x16x32_bf16 v[60:63], a[244:247], v[104:107], v[60:63]\n" \
"s_waitcnt lgkmcnt(3)\n" \
"ds_read_b128 v[100:103], v28 offset:59392\n" \
"v_cvt_pk_bf16_f32 v232, v232, v233\n" \
"v_mfma_f32_16x16x32_bf16 v[60:63], a[252:255], v[108:111], v[60:63]\n" \
"v_cvt_pk_bf16_f32 v233, v234, v235\n" \
"v_cvt_pk_bf16_f32 v234, v236, v237\n" \
"v_mfma_f32_16x16x32_bf16 v[64:67], a[240:243], v[112:115], v[64:67]\n" \
"ds_read_b128 v[104:107], v28 offset:58368\n" \
"v_cvt_pk_bf16_f32 v235, v238, v239\n" \
"v_mfma_f32_16x16x32_bf16 v[64:67], a[248:251], v[116:119], v[64:67]\n" \
"buffer_store_dwordx4 v[232:235], v4, s[36:39], s51 offen offset:3072 sc1\n" \
"v_mfma_f32_16x16x32_bf16 v[68:71], a[244:247], v[112:115], v[68:71]\n" \
"s_waitcnt lgkmcnt(3)\n" \
"ds_read_b128 v[108:111], v28 offset:60416\n" \
"v_mul_f32 v240, v187, v240\n" \
"v_mfma_f32_16x16x32_bf16 v[68:71], a[252:255], v[116:119], v[68:71]\n" \
"v_mul_f32 v241, v187, v241\n" \
"v_mul_f32 v242, v187, v242\n" \
"v_mfma_f32_16x16x32_bf16 v[72:75], a[240:243], v[120:123], v[72:75]\n" \
"ds_read_b128 v[112:115], v28 offset:61440\n" \
"v_mul_f32 v243, v187, v243\n" \
"v_mfma_f32_16x16x32_bf16 v[72:75], a[248:251], v[124:127], v[72:75]\n" \
"v_mul_f32 v244, v187, v244\n" \
"v_mul_f32 v245, v187, v245\n" \
"v_mfma_f32_16x16x32_bf16 v[76:79], a[244:247], v[120:123], v[76:79]\n" \
"ds_read_b128 v[116:119], v28 offset:63488\n" \
"v_mul_f32 v246, v187, v246\n" \
"v_mfma_f32_16x16x32_bf16 v[76:79], a[252:255], v[124:127], v[76:79]\n" \
"s_waitcnt vmcnt(17) lgkmcnt(4)\n" \
"s_barrier\n" \
"v_mul_f32 v247, v187, v247\n" \
"v_mfma_f32_16x16x32_bf16 v[32:35], a[64:67], v[96:99], v[32:35]\n" \
"ds_read_b128 v[120:123], v28 offset:62464\n" \
"v_cvt_pk_bf16_f32 v240, v240, v241\n" \
"v_mfma_f32_16x16x32_bf16 v[32:35], a[72:75], v[100:103], v[32:35]\n" \
"buffer_load_dwordx4 v4, s[36:39], s80 offen offset:1024 sc1 lds\n" \
"v_mfma_f32_16x16x32_bf16 v[36:39], a[68:71], v[96:99], v[36:39]\n" \
"s_waitcnt lgkmcnt(3)\n" \
"ds_read_b128 v[124:127], v28 offset:64512\n" \
"v_cvt_pk_bf16_f32 v241, v242, v243\n" \
"v_mfma_f32_16x16x32_bf16 v[36:39], a[76:79], v[100:103], v[36:39]\n" \
"ds_read_b64_tr_b16 a[128:129], v180 offset:32768\n" \
"v_cvt_pk_bf16_f32 v242, v244, v245\n" \
"v_mfma_f32_16x16x32_bf16 v[40:43], a[64:67], v[104:107], v[40:43]\n" \
"ds_read_b128 v[96:99], v29 offset:0\n" \
"v_cvt_pk_bf16_f32 v243, v246, v247\n" \
"v_mfma_f32_16x16x32_bf16 v[40:43], a[72:75], v[108:111], v[40:43]\n" \
"buffer_store_dwordx4 v[240:243], v7, s[36:39], s51 offen offset:1024 sc1\n" \
"v_mfma_f32_16x16x32_bf16 v[44:47], a[68:71], v[104:107], v[44:47]\n" \
"s_waitcnt lgkmcnt(4)\n" \
"ds_read_b128 v[100:103], v29 offset:2048\n" \
"v_mul_f32 v248, v187, v248\n" \
"v_mfma_f32_16x16x32_bf16 v[44:47], a[76:79], v[108:111], v[44:47]\n" \
"ds_read_b64_tr_b16 a[130:131], v181 offset:32768\n" \
"v_mul_f32 v249, v187, v249\n" \
"v_mfma_f32_16x16x32_bf16 v[48:51], a[64:67], v[112:115], v[48:51]\n" \
"ds_read_b128 v[104:107], v29 offset:1024\n" \
"v_mul_f32 v250, v187, v250\n" \
"v_mfma_f32_16x16x32_bf16 v[48:51], a[72:75], v[116:119], v[48:51]\n" \
"ds_read_b64_tr_b16 a[132:133], v182 offset:32768\n" \
"v_mul_f32 v251, v187, v251\n" \
"v_mfma_f32_16x16x32_bf16 v[52:55], a[68:71], v[112:115], v[52:55]\n" \
"s_waitcnt lgkmcnt(6)\n" \
"ds_read_b128 v[108:111], v29 offset:3072\n" \
"v_mul_f32 v252, v187, v252\n" \
"v_mfma_f32_16x16x32_bf16 v[52:55], a[76:79], v[116:119], v[52:55]\n" \
"ds_read_b64_tr_b16 a[134:135], v183 offset:32768\n" \
"v_mul_f32 v253, v187, v253\n" \
"v_mfma_f32_16x16x32_bf16 v[56:59], a[64:67], v[120:123], v[56:59]\n" \
"ds_read_b128 v[112:115], v29 offset:4096\n" \
"v_mul_f32 v254, v187, v254\n" \
"v_mfma_f32_16x16x32_bf16 v[56:59], a[72:75], v[124:127], v[56:59]\n" \
"buffer_load_dwordx4 v4, s[36:39], s80 offen offset:2048 sc1 lds\n" \
"v_mfma_f32_16x16x32_bf16 v[60:63], a[68:71], v[120:123], v[60:63]\n" \
"ds_read_b128 v[116:119], v29 offset:6144\n" \
"v_mul_f32 v255, v187, v255\n" \
"v_mfma_f32_16x16x32_bf16 v[60:63], a[76:79], v[124:127], v[60:63]\n" \
"s_waitcnt vmcnt(13) lgkmcnt(7)\n" \
"s_barrier\n" \
"ds_read_b64_tr_b16 a[136:137], v180 offset:40960\n" \
"v_mfma_f32_16x16x32_bf16 v[64:67], a[64:67], v[96:99], v[64:67]\n" \
"ds_read_b128 v[120:123], v29 offset:5120\n" \
"v_cvt_pk_bf16_f32 v248, v248, v249\n" \
"v_mfma_f32_16x16x32_bf16 v[64:67], a[72:75], v[100:103], v[64:67]\n" \
"buffer_load_dwordx4 v4, s[36:39], s80 offen offset:3072 sc1 lds\n" \
"v_mfma_f32_16x16x32_bf16 v[68:71], a[68:71], v[96:99], v[68:71]\n" \
"s_waitcnt lgkmcnt(5)\n" \
"ds_read_b128 v[124:127], v29 offset:7168\n" \
"s_add_u32 s80, s67, 0\n" \
"v_mfma_f32_16x16x32_bf16 v[68:71], a[76:79], v[100:103], v[68:71]\n" \
"s_add_u32 m0, s22, 0\n" \
"ds_read_b64_tr_b16 a[138:139], v181 offset:40960\n" \
"v_cvt_pk_bf16_f32 v249, v250, v251\n" \
"v_mfma_f32_16x16x32_bf16 v[72:75], a[64:67], v[104:107], v[72:75]\n" \
"ds_read_b128 v[96:99], v29 offset:8192\n" \
"v_cvt_pk_bf16_f32 v250, v252, v253\n" \
"v_mfma_f32_16x16x32_bf16 v[72:75], a[72:75], v[108:111], v[72:75]\n" \
"buffer_load_dwordx4 v31, s[36:39], s80 offen offset:0 sc1 lds\n" \
"v_mfma_f32_16x16x32_bf16 v[76:79], a[68:71], v[104:107], v[76:79]\n" \
"s_waitcnt lgkmcnt(5)\n" \
"ds_read_b128 v[100:103], v29 offset:10240\n" \
"v_cvt_pk_bf16_f32 v251, v254, v255\n" \
"v_mfma_f32_16x16x32_bf16 v[76:79], a[76:79], v[108:111], v[76:79]\n" \
"ds_read_b64_tr_b16 a[140:141], v182 offset:40960\n" \
"v_bfe_u32 v188, v4, 7, 1\n" \
"v_mfma_f32_16x16x32_bf16 v[32:35], a[80:83], v[112:115], v[32:35]\n" \
"ds_read_b128 v[104:107], v29 offset:9216\n" \
"v_bfe_u32 v189, v4, 4, 3\n" \
"v_mfma_f32_16x16x32_bf16 v[32:35], a[88:91], v[116:119], v[32:35]\n" \
"buffer_store_dwordx4 v[248:251], v7, s[36:39], s51 offen offset:3072 sc1\n" \
"v_mfma_f32_16x16x32_bf16 v[36:39], a[84:87], v[112:115], v[36:39]\n" \
"s_waitcnt lgkmcnt(5)\n" \
"ds_read_b128 v[108:111], v29 offset:11264\n" \
"ds_read_b64_tr_b16 a[142:143], v183 offset:40960\n" \
"v_mfma_f32_16x16x32_bf16 v[36:39], a[92:95], v[116:119], v[36:39]\n" \
"ds_read_b64_tr_b16 a[144:145], v13 offset:0\n" \
"v_lshl_add_u32 v188, v189, 1, v188\n" \
"v_mfma_f32_16x16x32_bf16 v[40:43], a[80:83], v[120:123], v[40:43]\n" \
"LH26_%=:\ns_cbranch_execz LHR2_%=\n" \
"ds_read_b128 v[112:115], v29 offset:12288\n" \
"v_lshrrev_b32 v189, 8, v4\n" \
"v_mfma_f32_16x16x32_bf16 v[40:43], a[88:91], v[124:127], v[40:43]\n" \
"ds_read_b64_tr_b16 a[146:147], v14 offset:0\n" \
"v_lshlrev_b32 v184, 11, v188\n" \
"v_mfma_f32_16x16x32_bf16 v[44:47], a[84:87], v[120:123], v[44:47]\n" \
"ds_read_b128 v[116:119], v29 offset:14336\n" \
"v_and_b32 v188, 1, v189\n" \
"v_mfma_f32_16x16x32_bf16 v[44:47], a[92:95], v[124:127], v[44:47]\n" \
"s_waitcnt vmcnt(9) lgkmcnt(4)\n" \
"s_barrier\n" \
"ds_read_b64_tr_b16 a[148:149], v15 offset:0\n" \
"v_mfma_f32_16x16x32_bf16 v[48:51], a[80:83], v[96:99], v[48:51]\n" \
"ds_read_b128 v[120:123], v29 offset:13312\n" \
"v_lshl_add_u32 v184, v188, 5, v184\n" \
"v_mfma_f32_16x16x32_bf16 v[48:51], a[88:91], v[100:103], v[48:51]\n" \
"buffer_load_dwordx4 v31, s[36:39], s80 offen offset:1024 sc1 lds\n" \
"v_mfma_f32_16x16x32_bf16 v[52:55], a[84:87], v[96:99], v[52:55]\n" \
"s_waitcnt lgkmcnt(7)\n" \
"ds_read_b128 v[124:127], v29 offset:15360\n" \
"v_lshrrev_b32 v188, 1, v189\n" \
"v_mfma_f32_16x16x32_bf16 v[52:55], a[92:95], v[100:103], v[52:55]\n" \
"ds_read_b64_tr_b16 a[150:151], v16 offset:0\n" \
"v_lshl_add_u32 v184, v188, 4, v184\n" \
"v_mfma_f32_16x16x32_bf16 v[56:59], a[80:83], v[104:107], v[56:59]\n" \
"ds_read_b128 v[96:99], v30 offset:16384\n" \
"v_bfe_u32 v188, v4, 7, 1\n" \
"v_mfma_f32_16x16x32_bf16 v[56:59], a[88:91], v[108:111], v[56:59]\n" \
"buffer_load_dwordx4 v31, s[36:39], s80 offen offset:2048 sc1 lds\n" \
"v_mfma_f32_16x16x32_bf16 v[60:63], a[84:87], v[104:107], v[60:63]\n" \
"s_waitcnt lgkmcnt(5)\n" \
"ds_read_b128 v[100:103], v30 offset:18432\n" \
"v_bfe_u32 v189, v4, 4, 3\n" \
"v_mfma_f32_16x16x32_bf16 v[60:63], a[92:95], v[108:111], v[60:63]\n" \
"ds_read_b64_tr_b16 a[152:153], v13 offset:8192\n" \
"v_lshl_add_u32 v188, v189, 1, v188\n" \
"v_mfma_f32_16x16x32_bf16 v[64:67], a[80:83], v[112:115], v[64:67]\n" \
"ds_read_b128 v[104:107], v30 offset:17408\n" \
"v_lshlrev_b32 v185, 3, v188\n" \
"v_mfma_f32_16x16x32_bf16 v[64:67], a[88:91], v[116:119], v[64:67]\n" \
"buffer_load_dwordx4 v31, s[36:39], s80 offen offset:3072 sc1 lds\n" \
"v_mfma_f32_16x16x32_bf16 v[68:71], a[84:87], v[112:115], v[68:71]\n" \
"s_waitcnt lgkmcnt(5)\n" \
"ds_read_b128 v[108:111], v30 offset:19456\n" \
"s_add_u32 s80, s67, 16384\n" \
"ds_read_b64_tr_b16 a[154:155], v14 offset:8192\n" \
"ds_read_b64_tr_b16 a[156:157], v15 offset:8192\n" \
"ds_read_b64_tr_b16 a[158:159], v16 offset:8192\n" \
"v_mfma_f32_16x16x32_bf16 v[68:71], a[92:95], v[116:119], v[68:71]\n" \
"s_add_u32 m0, s22, 32768\n" \
"v_accvgpr_read_b32 v144, a96\n" \
"v_accvgpr_read_b32 v145, a97\n" \
"v_mfma_f32_16x16x32_bf16 v[72:75], a[80:83], v[120:123], v[72:75]\n" \
"ds_read_b128 v[112:115], v30 offset:20480\n" \
"v_accvgpr_read_b32 v146, a98\n" \
"v_mfma_f32_16x16x32_bf16 v[72:75], a[88:91], v[124:127], v[72:75]\n" \
"buffer_load_dwordx4 v31, s[36:39], s80 offen offset:0 sc1 lds\n" \
"v_mfma_f32_16x16x32_bf16 v[76:79], a[84:87], v[120:123], v[76:79]\n" \
"ds_read_b128 v[116:119], v30 offset:22528\n" \
"v_accvgpr_read_b32 v147, a99\n" \
"v_mfma_f32_16x16x32_bf16 v[76:79], a[92:95], v[124:127], v[76:79]\n" \
"s_waitcnt lgkmcnt(2)\n" \
"s_barrier\n" \
"v_accvgpr_read_b32 v148, a100\n" \
"v_mfma_f32_16x16x32_bf16 v[32:35], a[128:131], v[96:99], v[32:35]\n" \
"ds_read_b128 v[120:123], v30 offset:21504\n" \
"v_accvgpr_read_b32 v149, a101\n" \
"v_mfma_f32_16x16x32_bf16 v[32:35], a[136:139], v[100:103], v[32:35]\n" \
"buffer_load_dwordx4 v31, s[36:39], s80 offen offset:1024 sc1 lds\n" \
"v_mfma_f32_16x16x32_bf16 v[36:39], a[132:135], v[96:99], v[36:39]\n" \
"s_waitcnt lgkmcnt(6)\n" \
"ds_read_b128 v[124:127], v30 offset:23552\n" \
"v_accvgpr_read_b32 v150, a102\n" \
"v_mfma_f32_16x16x32_bf16 v[36:39], a[140:143], v[100:103], v[36:39]\n" \
"v_accvgpr_read_b32 v151, a103\n" \
"v_accvgpr_read_b32 v152, a104\n" \
"v_mfma_f32_16x16x32_bf16 v[40:43], a[128:131], v[104:107], v[40:43]\n" \
"ds_read_b128 v[96:99], v30 offset:24576\n" \
"v_accvgpr_read_b32 v153, a105\n" \
"v_mfma_f32_16x16x32_bf16 v[40:43], a[136:139], v[108:111], v[40:43]\n" \
"buffer_load_dwordx4 v31, s[36:39], s80 offen offset:2048 sc1 lds\n" \
"v_mfma_f32_16x16x32_bf16 v[44:47], a[132:135], v[104:107], v[44:47]\n" \
"s_waitcnt lgkmcnt(3)\n" \
"ds_read_b128 v[100:103], v30 offset:26624\n" \
"v_accvgpr_read_b32 v154, a106\n" \
"v_mfma_f32_16x16x32_bf16 v[44:47], a[140:143], v[108:111], v[44:47]\n" \
"v_accvgpr_read_b32 v155, a107\n" \
"v_accvgpr_read_b32 v156, a108\n" \
"v_mfma_f32_16x16x32_bf16 v[48:51], a[128:131], v[112:115], v[48:51]\n" \
"ds_read_b128 v[104:107], v30 offset:25600\n" \
"v_accvgpr_read_b32 v157, a109\n" \
"v_mfma_f32_16x16x32_bf16 v[48:51], a[136:139], v[116:119], v[48:51]\n" \
"buffer_load_dwordx4 v31, s[36:39], s80 offen offset:3072 sc1 lds\n" \
"v_mfma_f32_16x16x32_bf16 v[52:55], a[132:135], v[112:115], v[52:55]\n" \
"s_waitcnt lgkmcnt(3)\n" \
"ds_read_b128 v[108:111], v30 offset:27648\n" \
"s_mov_b32 s81, 1310720\n" \
"v_mfma_f32_16x16x32_bf16 v[52:55], a[140:143], v[116:119], v[52:55]\n" \
"s_add_u32 m0, s79, 16384\n" \
"v_accvgpr_read_b32 v158, a110\n" \
"v_accvgpr_read_b32 v159, a111\n" \
"v_mfma_f32_16x16x32_bf16 v[56:59], a[128:131], v[120:123], v[56:59]\n" \
"ds_read_b128 v[112:115], v30 offset:28672\n" \
"v_accvgpr_read_b32 v128, a112\n" \
"v_mfma_f32_16x16x32_bf16 v[56:59], a[136:139], v[124:127], v[56:59]\n" \
"buffer_load_dwordx4 v12, s[28:31], s81 offen offset:0 lds\n" \
"v_mfma_f32_16x16x32_bf16 v[60:63], a[132:135], v[120:123], v[60:63]\n" \
"s_add_u32 m0, s79, 17280\n" \
"ds_read_b128 v[116:119], v30 offset:30720\n" \
"v_accvgpr_read_b32 v129, a113\n" \
"v_mfma_f32_16x16x32_bf16 v[60:63], a[140:143], v[124:127], v[60:63]\n" \
"s_waitcnt vmcnt(10) lgkmcnt(4)\n" \
"s_barrier\n" \
"v_accvgpr_read_b32 v130, a114\n" \
"v_mfma_f32_16x16x32_bf16 v[64:67], a[128:131], v[96:99], v[64:67]\n" \
"ds_read_b128 v[120:123], v30 offset:29696\n" \
"v_accvgpr_read_b32 v131, a115\n" \
"v_mfma_f32_16x16x32_bf16 v[64:67], a[136:139], v[100:103], v[64:67]\n" \
"buffer_load_dwordx4 v12, s[28:31], s81 offen offset:128 lds\n" \
"v_mfma_f32_16x16x32_bf16 v[68:71], a[132:135], v[96:99], v[68:71]\n" \
"s_waitcnt lgkmcnt(3)\n" \
"ds_read_b128 v[124:127], v30 offset:31744\n" \
"s_mov_b32 s81, 1376256\n" \
"v_mfma_f32_16x16x32_bf16 v[68:71], a[140:143], v[100:103], v[68:71]\n" \
"s_add_u32 m0, s79, 24576\n" \
"v_accvgpr_read_b32 v132, a116\n" \
"v_accvgpr_read_b32 v133, a117\n" \
"v_mfma_f32_16x16x32_bf16 v[72:75], a[128:131], v[104:107], v[72:75]\n" \
"ds_read_b128 v[96:99], v29 offset:49152\n" \
"v_accvgpr_read_b32 v134, a118\n" \
"v_mfma_f32_16x16x32_bf16 v[72:75], a[136:139], v[108:111], v[72:75]\n" \
"buffer_load_dwordx4 v12, s[28:31], s81 offen offset:0 lds\n" \
"v_mfma_f32_16x16x32_bf16 v[76:79], a[132:135], v[104:107], v[76:79]\n" \
"s_waitcnt lgkmcnt(3)\n" \
"ds_read_b128 v[100:103], v29 offset:51200\n" \
"s_add_u32 m0, s79, 25472\n" \
"v_mfma_f32_16x16x32_bf16 v[76:79], a[140:143], v[108:111], v[76:79]\n" \
"v_accvgpr_read_b32 v135, a119\n" \
"v_accvgpr_read_b32 v136, a120\n" \
"v_mfma_f32_16x16x32_bf16 v[32:35], a[144:147], v[112:115], v[32:35]\n" \
"ds_read_b128 v[104:107], v29 offset:50176\n" \
"v_accvgpr_read_b32 v137, a121\n" \
"v_mfma_f32_16x16x32_bf16 v[32:35], a[152:155], v[116:119], v[32:35]\n" \
"buffer_load_dwordx4 v12, s[28:31], s81 offen offset:128 lds\n" \
"v_mfma_f32_16x16x32_bf16 v[36:39], a[148:151], v[112:115], v[36:39]\n" \
"s_waitcnt lgkmcnt(3)\n" \
"ds_read_b128 v[108:111], v29 offset:52224\n" \
"s_add_u32 s80, s67, 32768\n" \
"v_mfma_f32_16x16x32_bf16 v[36:39], a[156:159], v[116:119], v[36:39]\n" \
"s_add_u32 m0, s22, 81920\n" \
"v_accvgpr_read_b32 v138, a122\n" \
"v_accvgpr_read_b32 v139, a123\n" \
"v_mfma_f32_16x16x32_bf16 v[40:43], a[144:147], v[120:123], v[40:43]\n" \
"ds_read_b128 v[112:115], v29 offset:53248\n" \
"v_accvgpr_read_b32 v140, a124\n" \
"v_mfma_f32_16x16x32_bf16 v[40:43], a[152:155], v[124:127], v[40:43]\n" \
"buffer_load_dwordx4 v31, s[36:39], s80 offen offset:0 sc1 lds\n" \
"v_mfma_f32_16x16x32_bf16 v[44:47], a[148:151], v[120:123], v[44:47]\n" \
"ds_read_b128 v[116:119], v29 offset:55296\n" \
"v_accvgpr_read_b32 v141, a125\n" \
"v_mfma_f32_16x16x32_bf16 v[44:47], a[156:159], v[124:127], v[44:47]\n" \
"s_waitcnt vmcnt(9) lgkmcnt(4)\n" \
"s_barrier\n" \
"v_accvgpr_read_b32 v142, a126\n" \
"v_mfma_f32_16x16x32_bf16 v[48:51], a[144:147], v[96:99], v[48:51]\n" \
"ds_read_b128 v[120:123], v29 offset:54272\n" \
"v_accvgpr_read_b32 v143, a127\n" \
"v_mfma_f32_16x16x32_bf16 v[48:51], a[152:155], v[100:103], v[48:51]\n" \
"buffer_load_dwordx4 v31, s[36:39], s80 offen offset:1024 sc1 lds\n" \
"v_mfma_f32_16x16x32_bf16 v[52:55], a[148:151], v[96:99], v[52:55]\n" \
"s_waitcnt lgkmcnt(3)\n" \
"ds_read_b128 v[124:127], v29 offset:56320\n" \
"v_mfma_f32_16x16x32_bf16 v[52:55], a[156:159], v[100:103], v[52:55]\n" \
"v_mfma_f32_16x16x32_bf16 v[56:59], a[144:147], v[104:107], v[56:59]\n" \
"ds_read_b128 v[96:99], v28 offset:0\n" \
"v_mfma_f32_16x16x32_bf16 v[56:59], a[152:155], v[108:111], v[56:59]\n" \
"buffer_load_dwordx4 v31, s[36:39], s80 offen offset:2048 sc1 lds\n" \
"v_mfma_f32_16x16x32_bf16 v[60:63], a[148:151], v[104:107], v[60:63]\n" \
"s_waitcnt lgkmcnt(3)\n" \
"ds_read_b128 v[100:103], v28 offset:2048\n" \
"v_mfma_f32_16x16x32_bf16 v[60:63], a[156:159], v[108:111], v[60:63]\n" \
"v_mfma_f32_16x16x32_bf16 v[64:67], a[144:147], v[112:115], v[64:67]\n" \
"ds_read_b128 v[104:107], v28 offset:1024\n" \
"v_mfma_f32_16x16x32_bf16 v[64:67], a[152:155], v[116:119], v[64:67]\n" \
"buffer_load_dwordx4 v31, s[36:39], s80 offen offset:3072 sc1 lds\n" \
"v_mfma_f32_16x16x32_bf16 v[68:71], a[148:151], v[112:115], v[68:71]\n" \
"s_waitcnt lgkmcnt(3)\n" \
"ds_read_b128 v[108:111], v28 offset:3072\n" \
"s_mov_b32 s81, 1441792\n" \
"v_mfma_f32_16x16x32_bf16 v[68:71], a[156:159], v[116:119], v[68:71]\n" \
"s_add_u32 m0, s79, 49152\n" \
"v_mfma_f32_16x16x32_bf16 v[72:75], a[144:147], v[120:123], v[72:75]\n" \
"ds_read_b128 v[112:115], v28 offset:4096\n" \
"v_mfma_f32_16x16x32_bf16 v[72:75], a[152:155], v[124:127], v[72:75]\n" \
"buffer_load_dwordx4 v12, s[28:31], s81 offen offset:0 lds\n" \
"v_mfma_f32_16x16x32_bf16 v[76:79], a[148:151], v[120:123], v[76:79]\n" \
"s_add_u32 m0, s79, 50048\n" \
"ds_read_b128 v[116:119], v28 offset:6144\n" \
"v_mfma_f32_16x16x32_bf16 v[76:79], a[156:159], v[124:127], v[76:79]\n" \
"s_waitcnt lgkmcnt(4)\n" \
"s_barrier\n" \
"s_waitcnt vmcnt(16)\n" \
"v_mfma_f32_16x16x32_bf16 v[80:83], a[160:163], v[96:99], 0\n" \
"ds_read_b128 v[120:123], v28 offset:5120\n" \
"v_mfma_f32_16x16x32_bf16 v[80:83], a[168:171], v[100:103], v[80:83]\n" \
"buffer_load_dwordx4 v12, s[28:31], s81 offen offset:128 lds\n" \
"v_mfma_f32_16x16x32_bf16 v[84:87], a[164:167], v[96:99], 0\n" \
"s_waitcnt lgkmcnt(3)\n" \
"ds_read_b128 v[124:127], v28 offset:7168\n" \
"s_mov_b32 s81, 1507328\n" \
"v_mfma_f32_16x16x32_bf16 v[84:87], a[172:175], v[100:103], v[84:87]\n" \
"s_cmp_lg_u64 s[4:5], 0\ns_cbranch_scc1 LFBB_%=\nLFBBR_%=:\n" \
"s_add_u32 m0, s79, 57344\n" \
"v_mfma_f32_16x16x32_bf16 v[88:91], a[160:163], v[104:107], 0\n" \
"ds_read_b128 v[96:99], v28 offset:8192\n" \
"v_mfma_f32_16x16x32_bf16 v[88:91], a[168:171], v[108:111], v[88:91]\n" \
"buffer_store_dwordx2 v[10:11], v20, s[36:39], s53 offen sc1\n" \
"v_mfma_f32_16x16x32_bf16 v[92:95], a[164:167], v[104:107], 0\n" \
"s_waitcnt lgkmcnt(3)\n" \
"ds_read_b128 v[100:103], v28 offset:10240\n" \
"v_mfma_f32_16x16x32_bf16 v[92:95], a[172:175], v[108:111], v[92:95]\n" \
"buffer_load_dwordx4 v12, s[28:31], s81 offen offset:0 lds\n" \
"v_mfma_f32_16x16x32_bf16 v[80:83], a[176:179], v[112:115], v[80:83]\n" \
"s_add_u32 m0, s79, 58240\n" \
"ds_read_b128 v[104:107], v28 offset:9216\n" \
"v_mfma_f32_16x16x32_bf16 v[80:83], a[184:187], v[116:119], v[80:83]\n" \
"v_mfma_f32_16x16x32_bf16 v[84:87], a[180:183], v[112:115], v[84:87]\n" \
"s_waitcnt lgkmcnt(3)\n" \
"ds_read_b128 v[108:111], v28 offset:11264\n" \
"v_mfma_f32_16x16x32_bf16 v[84:87], a[188:191], v[116:119], v[84:87]\n" \
"buffer_load_dwordx4 v12, s[28:31], s81 offen offset:128 lds\n" \
"v_mfma_f32_16x16x32_bf16 v[88:91], a[176:179], v[120:123], v[88:91]\n" \
"s_add_u32 s80, s66, 32768\n" \
"ds_read_b128 v[112:115], v28 offset:12288\n" \
"s_add_u32 m0, s22, 98304\n" \
"v_mfma_f32_16x16x32_bf16 v[88:91], a[184:187], v[124:127], v[88:91]\n" \
"v_mfma_f32_16x16x32_bf16 v[92:95], a[180:183], v[120:123], v[92:95]\n" \
"ds_read_b128 v[116:119], v28 offset:14336\n" \
"v_mfma_f32_16x16x32_bf16 v[92:95], a[188:191], v[124:127], v[92:95]\n" \
"s_waitcnt vmcnt(13) lgkmcnt(4)\n" \
"s_barrier\n" \
"v_mfma_f32_16x16x32_bf16 v[80:83], a[192:195], v[96:99], v[80:83]\n" \
"ds_read_b128 v[120:123], v28 offset:13312\n" \
"v_mfma_f32_16x16x32_bf16 v[80:83], a[200:203], v[100:103], v[80:83]\n" \
"buffer_load_dwordx4 v4, s[36:39], s80 offen offset:0 sc1 lds\n" \
"v_mfma_f32_16x16x32_bf16 v[84:87], a[196:199], v[96:99], v[84:87]\n" \
"s_waitcnt lgkmcnt(3)\n" \
"ds_read_b128 v[124:127], v28 offset:15360\n" \
"v_mfma_f32_16x16x32_bf16 v[84:87], a[204:207], v[100:103], v[84:87]\n" \
"v_mfma_f32_16x16x32_bf16 v[88:91], a[192:195], v[104:107], v[88:91]\n" \
"ds_read_b128 v[96:99], v28 offset:32768\n" \
"v_mfma_f32_16x16x32_bf16 v[88:91], a[200:203], v[108:111], v[88:91]\n" \
"buffer_load_dwordx4 v4, s[36:39], s80 offen offset:1024 sc1 lds\n" \
"v_mfma_f32_16x16x32_bf16 v[92:95], a[196:199], v[104:107], v[92:95]\n" \
"s_waitcnt lgkmcnt(3)\n" \
"ds_read_b128 v[100:103], v28 offset:34816\n" \
"v_mfma_f32_16x16x32_bf16 v[92:95], a[204:207], v[108:111], v[92:95]\n" \
"v_mfma_f32_16x16x32_bf16 v[80:83], a[208:211], v[112:115], v[80:83]\n" \
"ds_read_b128 v[104:107], v28 offset:33792\n" \
"v_mfma_f32_16x16x32_bf16 v[80:83], a[216:219], v[116:119], v[80:83]\n" \
"buffer_load_dwordx4 v4, s[36:39], s80 offen offset:2048 sc1 lds\n" \
"v_mfma_f32_16x16x32_bf16 v[84:87], a[212:215], v[112:115], v[84:87]\n" \
"s_waitcnt lgkmcnt(3)\n" \
"ds_read_b128 v[108:111], v28 offset:35840\n" \
"v_mfma_f32_16x16x32_bf16 v[84:87], a[220:223], v[116:119], v[84:87]\n" \
"v_mfma_f32_16x16x32_bf16 v[88:91], a[208:211], v[120:123], v[88:91]\n" \
"ds_read_b128 v[112:115], v28 offset:36864\n" \
"v_mfma_f32_16x16x32_bf16 v[88:91], a[216:219], v[124:127], v[88:91]\n" \
"buffer_load_dwordx4 v4, s[36:39], s80 offen offset:3072 sc1 lds\n" \
"v_mfma_f32_16x16x32_bf16 v[92:95], a[212:215], v[120:123], v[92:95]\n" \
"s_mov_b32 s81, 1572864\n" \
"ds_read_b128 v[116:119], v28 offset:38912\n" \
"s_add_u32 m0, s79, 65536\n" \
"v_mfma_f32_16x16x32_bf16 v[92:95], a[220:223], v[124:127], v[92:95]\n" \
"s_waitcnt lgkmcnt(4)\n" \
"s_barrier\n" \
"v_mfma_f32_16x16x32_bf16 v[80:83], a[224:227], v[96:99], v[80:83]\n" \
"ds_read_b128 v[120:123], v28 offset:37888\n" \
"v_mfma_f32_16x16x32_bf16 v[80:83], a[232:235], v[100:103], v[80:83]\n" \
"buffer_load_dwordx4 v12, s[28:31], s81 offen offset:0 lds\n" \
"v_mfma_f32_16x16x32_bf16 v[84:87], a[228:231], v[96:99], v[84:87]\n" \
"s_waitcnt lgkmcnt(3)\n" \
"ds_read_b128 v[124:127], v28 offset:39936\n" \
"s_add_u32 m0, s79, 66432\n" \
"v_mfma_f32_16x16x32_bf16 v[84:87], a[236:239], v[100:103], v[84:87]\n" \
"v_mfma_f32_16x16x32_bf16 v[88:91], a[224:227], v[104:107], v[88:91]\n" \
"ds_read_b128 v[96:99], v28 offset:40960\n" \
"v_mfma_f32_16x16x32_bf16 v[88:91], a[232:235], v[108:111], v[88:91]\n" \
"buffer_load_dwordx4 v12, s[28:31], s81 offen offset:128 lds\n" \
"v_mfma_f32_16x16x32_bf16 v[92:95], a[228:231], v[104:107], v[92:95]\n" \
"s_waitcnt lgkmcnt(3)\n" \
"ds_read_b128 v[100:103], v28 offset:43008\n" \
"s_mov_b32 s81, 1638400\n" \
"v_mfma_f32_16x16x32_bf16 v[92:95], a[236:239], v[108:111], v[92:95]\n" \
"s_add_u32 m0, s79, 73728\n" \
"v_mfma_f32_16x16x32_bf16 v[80:83], a[240:243], v[112:115], v[80:83]\n" \
"ds_read_b128 v[104:107], v28 offset:41984\n" \
"v_mfma_f32_16x16x32_bf16 v[80:83], a[248:251], v[116:119], v[80:83]\n" \
"buffer_load_dwordx4 v12, s[28:31], s81 offen offset:0 lds\n" \
"v_mfma_f32_16x16x32_bf16 v[84:87], a[244:247], v[112:115], v[84:87]\n" \
"s_waitcnt lgkmcnt(3)\n" \
"ds_read_b128 v[108:111], v28 offset:44032\n" \
"s_add_u32 m0, s79, 74624\n" \
"v_mfma_f32_16x16x32_bf16 v[84:87], a[252:255], v[116:119], v[84:87]\n" \
"v_mfma_f32_16x16x32_bf16 v[88:91], a[240:243], v[120:123], v[88:91]\n" \
"ds_read_b128 v[112:115], v28 offset:45056\n" \
"v_mfma_f32_16x16x32_bf16 v[88:91], a[248:251], v[124:127], v[88:91]\n" \
"buffer_load_dwordx4 v12, s[28:31], s81 offen offset:128 lds\n" \
"v_mfma_f32_16x16x32_bf16 v[92:95], a[244:247], v[120:123], v[92:95]\n" \
"s_add_u32 s80, s64, 49152\n" \
"ds_read_b128 v[116:119], v28 offset:47104\n" \
"s_add_u32 m0, s22, 131072\n" \
"v_mfma_f32_16x16x32_bf16 v[92:95], a[252:255], v[124:127], v[92:95]\n" \
"s_waitcnt vmcnt(13) lgkmcnt(4)\n" \
"s_barrier\n" \
"v_mfma_f32_16x16x32_bf16 v[80:83], a[64:67], v[96:99], v[80:83]\n" \
"ds_read_b128 v[120:123], v28 offset:46080\n" \
"v_mfma_f32_16x16x32_bf16 v[80:83], a[72:75], v[100:103], v[80:83]\n" \
"buffer_load_dwordx4 v4, s[36:39], s80 offen offset:0 sc1 lds\n" \
"v_mfma_f32_16x16x32_bf16 v[84:87], a[68:71], v[96:99], v[84:87]\n" \
"s_waitcnt lgkmcnt(3)\n" \
"ds_read_b128 v[124:127], v28 offset:48128\n" \
"v_mfma_f32_16x16x32_bf16 v[84:87], a[76:79], v[100:103], v[84:87]\n" \
"ds_read_b64_tr_b16 a[0:1], v176 offset:16384\n" \
"v_mfma_f32_16x16x32_bf16 v[88:91], a[64:67], v[104:107], v[88:91]\n" \
"ds_read_b128 v[96:99], v29 offset:16384\n" \
"v_mfma_f32_16x16x32_bf16 v[88:91], a[72:75], v[108:111], v[88:91]\n" \
"buffer_load_dwordx4 v4, s[36:39], s80 offen offset:1024 sc1 lds\n" \
"v_mfma_f32_16x16x32_bf16 v[92:95], a[68:71], v[104:107], v[92:95]\n" \
"s_waitcnt lgkmcnt(4)\n" \
"ds_read_b128 v[100:103], v29 offset:18432\n" \
"v_mfma_f32_16x16x32_bf16 v[92:95], a[76:79], v[108:111], v[92:95]\n" \
"ds_read_b64_tr_b16 a[2:3], v177 offset:16384\n" \
"v_mfma_f32_16x16x32_bf16 v[80:83], a[80:83], v[112:115], v[80:83]\n" \
"ds_read_b128 v[104:107], v29 offset:17408\n" \
"v_mfma_f32_16x16x32_bf16 v[80:83], a[88:91], v[116:119], v[80:83]\n" \
"buffer_load_dwordx4 v4, s[36:39], s80 offen offset:2048 sc1 lds\n" \
"v_mfma_f32_16x16x32_bf16 v[84:87], a[84:87], v[112:115], v[84:87]\n" \
"s_waitcnt lgkmcnt(5)\n" \
"ds_read_b128 v[108:111], v29 offset:19456\n" \
"v_mfma_f32_16x16x32_bf16 v[84:87], a[92:95], v[116:119], v[84:87]\n" \
"ds_read_b64_tr_b16 a[4:5], v178 offset:16384\n" \
"v_mfma_f32_16x16x32_bf16 v[88:91], a[80:83], v[120:123], v[88:91]\n" \
"ds_read_b128 v[112:115], v29 offset:20480\n" \
"v_mfma_f32_16x16x32_bf16 v[88:91], a[88:91], v[124:127], v[88:91]\n" \
"buffer_load_dwordx4 v4, s[36:39], s80 offen offset:3072 sc1 lds\n" \
"v_mfma_f32_16x16x32_bf16 v[92:95], a[84:87], v[120:123], v[92:95]\n" \
"s_mov_b32 s81, 1703936\n" \
"ds_read_b128 v[116:119], v29 offset:22528\n" \
"s_add_u32 m0, s79, 147456\n" \
"v_mfma_f32_16x16x32_bf16 v[92:95], a[92:95], v[124:127], v[92:95]\n" \
"s_waitcnt lgkmcnt(6)\n" \
"s_barrier\n" \
"ds_read_b64_tr_b16 a[6:7], v179 offset:16384\n" \
"v_mfma_f32_16x16x32_bf16 v[80:83], a[128:131], v[96:99], v[80:83]\n" \
"ds_read_b128 v[120:123], v29 offset:21504\n" \
"v_mfma_f32_16x16x32_bf16 v[80:83], a[136:139], v[100:103], v[80:83]\n" \
"buffer_load_dwordx4 v12, s[28:31], s81 offen offset:0 lds\n" \
"v_mfma_f32_16x16x32_bf16 v[84:87], a[132:135], v[96:99], v[84:87]\n" \
"s_waitcnt lgkmcnt(5)\n" \
"ds_read_b128 v[124:127], v29 offset:23552\n" \
"s_add_u32 m0, s79, 148352\n" \
"v_mfma_f32_16x16x32_bf16 v[84:87], a[140:143], v[100:103], v[84:87]\n" \
"ds_read_b64_tr_b16 a[8:9], v176 offset:24576\n" \
"v_mfma_f32_16x16x32_bf16 v[88:91], a[128:131], v[104:107], v[88:91]\n" \
"ds_read_b128 v[96:99], v29 offset:57344\n" \
"v_mfma_f32_16x16x32_bf16 v[88:91], a[136:139], v[108:111], v[88:91]\n" \
"buffer_load_dwordx4 v12, s[28:31], s81 offen offset:128 lds\n" \
"v_mfma_f32_16x16x32_bf16 v[92:95], a[132:135], v[104:107], v[92:95]\n" \
"s_waitcnt lgkmcnt(5)\n" \
"ds_read_b128 v[100:103], v29 offset:59392\n" \
"s_mov_b32 s81, 1769472\n" \
"v_mfma_f32_16x16x32_bf16 v[92:95], a[140:143], v[108:111], v[92:95]\n" \
"s_add_u32 m0, s79, 155648\n" \
"ds_read_b64_tr_b16 a[10:11], v177 offset:24576\n" \
"v_mfma_f32_16x16x32_bf16 v[80:83], a[144:147], v[112:115], v[80:83]\n" \
"ds_read_b128 v[104:107], v29 offset:58368\n" \
"v_mfma_f32_16x16x32_bf16 v[80:83], a[152:155], v[116:119], v[80:83]\n" \
"buffer_load_dwordx4 v12, s[28:31], s81 offen offset:0 lds\n" \
"v_mfma_f32_16x16x32_bf16 v[84:87], a[148:151], v[112:115], v[84:87]\n" \
"s_waitcnt lgkmcnt(5)\n" \
"ds_read_b128 v[108:111], v29 offset:60416\n" \
"s_add_u32 m0, s79, 156544\n" \
"ds_read_b64_tr_b16 a[12:13], v178 offset:24576\n" \
"ds_read_b64_tr_b16 a[14:15], v179 offset:24576\n" \
"v_mfma_f32_16x16x32_bf16 v[84:87], a[156:159], v[116:119], v[84:87]\n" \
"v_mfma_f32_16x16x32_bf16 v[88:91], a[144:147], v[120:123], v[88:91]\n" \
"ds_read_b128 v[112:115], v29 offset:61440\n" \
"v_mfma_f32_16x16x32_bf16 v[88:91], a[152:155], v[124:127], v[88:91]\n" \
"buffer_load_dwordx4 v12, s[28:31], s81 offen offset:128 lds\n" \
"v_mfma_f32_16x16x32_bf16 v[92:95], a[148:151], v[120:123], v[92:95]\n" \
"s_add_u32 s80, s67, 49152\n" \
"ds_read_b128 v[116:119], v29 offset:63488\n" \
"s_add_u32 m0, s22, 0\n" \
"v_mfma_f32_16x16x32_bf16 v[92:95], a[156:159], v[124:127], v[92:95]\n" \
"s_waitcnt vmcnt(12) lgkmcnt(2)\n" \
"s_barrier\n" \
"v_mfma_f32_16x16x32_bf16 v[32:35], a[0:3], v[96:99], v[32:35]\n" \
"ds_read_b128 v[120:123], v29 offset:62464\n" \
"v_mfma_f32_16x16x32_bf16 v[32:35], a[8:11], v[100:103], v[32:35]\n" \
"buffer_load_dwordx4 v31, s[36:39], s80 offen offset:0 sc1 lds\n" \
"v_mfma_f32_16x16x32_bf16 v[36:39], a[4:7], v[96:99], v[36:39]\n" \
"s_waitcnt lgkmcnt(5)\n" \
"ds_read_b128 v[124:127], v29 offset:64512\n" \
"v_mfma_f32_16x16x32_bf16 v[36:39], a[12:15], v[100:103], v[36:39]\n" \
"ds_read_b64_tr_b16 a[16:17], v176 offset:49152\n" \
"v_mfma_f32_16x16x32_bf16 v[40:43], a[0:3], v[104:107], v[40:43]\n" \
"ds_read_b128 v[96:99], v29 offset:32768\n" \
"v_mfma_f32_16x16x32_bf16 v[40:43], a[8:11], v[108:111], v[40:43]\n" \
"buffer_load_dwordx4 v31, s[36:39], s80 offen offset:1024 sc1 lds\n" \
"v_mfma_f32_16x16x32_bf16 v[44:47], a[4:7], v[104:107], v[44:47]\n" \
"s_waitcnt lgkmcnt(4)\n" \
"ds_read_b128 v[100:103], v29 offset:34816\n" \
"v_mfma_f32_16x16x32_bf16 v[44:47], a[12:15], v[108:111], v[44:47]\n" \
"ds_read_b64_tr_b16 a[18:19], v177 offset:49152\n" \
"v_mfma_f32_16x16x32_bf16 v[48:51], a[0:3], v[112:115], v[48:51]\n" \
"ds_read_b128 v[104:107], v29 offset:33792\n" \
"v_mfma_f32_16x16x32_bf16 v[48:51], a[8:11], v[116:119], v[48:51]\n" \
"buffer_load_dwordx4 v31, s[36:39], s80 offen offset:2048 sc1 lds\n" \
"v_mfma_f32_16x16x32_bf16 v[52:55], a[4:7], v[112:115], v[52:55]\n" \
"s_waitcnt lgkmcnt(5)\n" \
"ds_read_b128 v[108:111], v29 offset:35840\n" \
"v_mfma_f32_16x16x32_bf16 v[52:55], a[12:15], v[116:119], v[52:55]\n" \
"ds_read_b64_tr_b16 a[20:21], v178 offset:49152\n" \
"v_mfma_f32_16x16x32_bf16 v[56:59], a[0:3], v[120:123], v[56:59]\n" \
"ds_read_b128 v[112:115], v29 offset:24576\n" \
"v_mfma_f32_16x16x32_bf16 v[56:59], a[8:11], v[124:127], v[56:59]\n" \
"buffer_load_dwordx4 v31, s[36:39], s80 offen offset:3072 sc1 lds\n" \
"v_mfma_f32_16x16x32_bf16 v[60:63], a[4:7], v[120:123], v[60:63]\n" \
"s_add_u32 s80, s65, 49152\n" \
"ds_read_b128 v[116:119], v29 offset:26624\n" \
"s_add_u32 m0, s22, 32768\n" \
"v_mfma_f32_16x16x32_bf16 v[60:63], a[12:15], v[124:127], v[60:63]\n" \
"s_waitcnt lgkmcnt(6)\n" \
"s_barrier\n" \
"ds_read_b64_tr_b16 a[22:23], v179 offset:49152\n" \
"v_mfma_f32_16x16x32_bf16 v[64:67], a[0:3], v[96:99], v[64:67]\n" \
"ds_read_b128 v[120:123], v29 offset:25600\n" \
"v_mfma_f32_16x16x32_bf16 v[64:67], a[8:11], v[100:103], v[64:67]\n" \
"buffer_load_dwordx4 v4, s[36:39], s80 offen offset:0 sc1 lds\n" \
"v_mfma_f32_16x16x32_bf16 v[68:71], a[4:7], v[96:99], v[68:71]\n" \
"s_waitcnt lgkmcnt(5)\n" \
"ds_read_b128 v[124:127], v29 offset:27648\n" \
"v_mfma_f32_16x16x32_bf16 v[68:71], a[12:15], v[100:103], v[68:71]\n" \
"ds_read_b64_tr_b16 a[24:25], v176 offset:57344\n" \
"v_mfma_f32_16x16x32_bf16 v[72:75], a[0:3], v[104:107], v[72:75]\n" \
"ds_read_b128 v[96:99], v29 offset:36864\n" \
"v_mfma_f32_16x16x32_bf16 v[72:75], a[8:11], v[108:111], v[72:75]\n" \
"buffer_load_dwordx4 v4, s[36:39], s80 offen offset:1024 sc1 lds\n" \
"v_mfma_f32_16x16x32_bf16 v[76:79], a[4:7], v[104:107], v[76:79]\n" \
"s_waitcnt lgkmcnt(5)\n" \
"ds_read_b128 v[100:103], v29 offset:38912\n" \
"v_mfma_f32_16x16x32_bf16 v[76:79], a[12:15], v[108:111], v[76:79]\n" \
"ds_read_b64_tr_b16 a[26:27], v177 offset:57344\n" \
"v_mfma_f32_16x16x32_bf16 v[80:83], a[0:3], v[112:115], v[80:83]\n" \
"ds_read_b128 v[104:107], v29 offset:37888\n" \
"v_mfma_f32_16x16x32_bf16 v[80:83], a[8:11], v[116:119], v[80:83]\n" \
"buffer_load_dwordx4 v4, s[36:39], s80 offen offset:2048 sc1 lds\n" \
"v_mfma_f32_16x16x32_bf16 v[84:87], a[4:7], v[112:115], v[84:87]\n" \
"s_waitcnt lgkmcnt(5)\n" \
"ds_read_b128 v[108:111], v29 offset:39936\n" \
"v_bfe_u32 v186, v4, 7, 2\n" \
"ds_read_b64_tr_b16 a[28:29], v178 offset:57344\n" \
"ds_read_b64_tr_b16 a[30:31], v179 offset:57344\n" \
"v_mfma_f32_16x16x32_bf16 v[84:87], a[12:15], v[116:119], v[84:87]\n" \
"v_min_u32 v186, 3, v186\n" \
"v_lshl_add_u32 v17, v186, 8, s11\n" \
"v_mfma_f32_16x16x32_bf16 v[88:91], a[0:3], v[120:123], v[88:91]\n" \
"ds_read_b128 v[112:115], v29 offset:40960\n" \
"v_bfe_u32 v186, v4, 4, 3\n" \
"v_mfma_f32_16x16x32_bf16 v[88:91], a[8:11], v[124:127], v[88:91]\n" \
"buffer_load_dwordx4 v4, s[36:39], s80 offen offset:3072 sc1 lds\n" \
"v_mfma_f32_16x16x32_bf16 v[92:95], a[4:7], v[120:123], v[92:95]\n" \
"s_mov_b32 s81, 1835008\n" \
"ds_read_b128 v[116:119], v29 offset:43008\n" \
"v_lshl_add_u32 v17, v186, 3, v17\n" \
"v_mfma_f32_16x16x32_bf16 v[92:95], a[12:15], v[124:127], v[92:95]\n" \
"s_waitcnt vmcnt(16) lgkmcnt(2)\n" \
"s_barrier\n" \
"s_add_u32 m0, s79, 16384\n" \
"v_mfma_f32_16x16x32_bf16 v[32:35], a[16:19], v[96:99], v[32:35]\n" \
"ds_read_b128 v[120:123], v29 offset:41984\n" \
"v_mfma_f32_16x16x32_bf16 v[32:35], a[24:27], v[100:103], v[32:35]\n" \
"buffer_load_dwordx4 v12, s[28:31], s81 offen offset:0 lds\n" \
"v_mfma_f32_16x16x32_bf16 v[36:39], a[20:23], v[96:99], v[36:39]\n" \
"s_waitcnt lgkmcnt(5)\n" \
"ds_read_b128 v[124:127], v29 offset:44032\n" \
"s_add_u32 m0, s79, 17280\n" \
"v_mfma_f32_16x16x32_bf16 v[36:39], a[28:31], v[100:103], v[36:39]\n" \
"ds_read_b64_tr_b16 a[32:33], v180 offset:0\n" \
"v_mfma_f32_16x16x32_bf16 v[40:43], a[16:19], v[104:107], v[40:43]\n" \
"ds_read_b128 v[96:99], v29 offset:45056\n" \
"v_mfma_f32_16x16x32_bf16 v[40:43], a[24:27], v[108:111], v[40:43]\n" \
"buffer_load_dwordx4 v12, s[28:31], s81 offen offset:128 lds\n" \
"v_mfma_f32_16x16x32_bf16 v[44:47], a[20:23], v[104:107], v[44:47]\n" \
"s_waitcnt lgkmcnt(4)\n" \
"ds_read_b128 v[100:103], v29 offset:47104\n" \
"s_mov_b32 s81, 1900544\n" \
"v_mfma_f32_16x16x32_bf16 v[44:47], a[28:31], v[108:111], v[44:47]\n" \
"s_add_u32 m0, s79, 24576\n" \
"ds_read_b64_tr_b16 a[34:35], v181 offset:0\n" \
"v_mfma_f32_16x16x32_bf16 v[48:51], a[16:19], v[112:115], v[48:51]\n" \
"ds_read_b128 v[104:107], v29 offset:46080\n" \
"v_mfma_f32_16x16x32_bf16 v[48:51], a[24:27], v[116:119], v[48:51]\n" \
"buffer_load_dwordx2 v[18:19], v17, s[36:39], 0 offen sc1\n" \
"v_mfma_f32_16x16x32_bf16 v[52:55], a[20:23], v[112:115], v[52:55]\n" \
"s_waitcnt lgkmcnt(5)\n" \
"ds_read_b128 v[108:111], v29 offset:48128\n" \
"v_mfma_f32_16x16x32_bf16 v[52:55], a[28:31], v[116:119], v[52:55]\n" \
"ds_read_b64_tr_b16 a[36:37], v182 offset:0\n" \
"v_mfma_f32_16x16x32_bf16 v[56:59], a[16:19], v[120:123], v[56:59]\n" \
"ds_read_b128 v[112:115], v29 offset:28672\n" \
"v_mfma_f32_16x16x32_bf16 v[56:59], a[24:27], v[124:127], v[56:59]\n" \
"buffer_load_dwordx4 v12, s[28:31], s81 offen offset:0 lds\n" \
"v_mfma_f32_16x16x32_bf16 v[60:63], a[20:23], v[120:123], v[60:63]\n" \
"s_add_u32 m0, s79, 25472\n" \
"ds_read_b128 v[116:119], v29 offset:30720\n" \
"v_mfma_f32_16x16x32_bf16 v[60:63], a[28:31], v[124:127], v[60:63]\n" \
"s_waitcnt vmcnt(16) lgkmcnt(6)\n" \
"s_barrier\n" \
"ds_read_b64_tr_b16 a[38:39], v183 offset:0\n" \
"v_mfma_f32_16x16x32_bf16 v[64:67], a[16:19], v[96:99], v[64:67]\n" \
"ds_read_b128 v[120:123], v29 offset:29696\n" \
"v_mfma_f32_16x16x32_bf16 v[64:67], a[24:27], v[100:103], v[64:67]\n" \
"buffer_load_dwordx4 v12, s[28:31], s81 offen offset:128 lds\n" \
"v_mfma_f32_16x16x32_bf16 v[68:71], a[20:23], v[96:99], v[68:71]\n" \
"s_waitcnt lgkmcnt(5)\n" \
"ds_read_b128 v[124:127], v29 offset:31744\n" \
"s_mov_b32 s81, 1966080\n" \
"v_mfma_f32_16x16x32_bf16 v[68:71], a[28:31], v[100:103], v[68:71]\n" \
"s_add_u32 m0, s79, 114688\n" \
"ds_read_b64_tr_b16 a[40:41], v180 offset:8192\n" \
"v_mfma_f32_16x16x32_bf16 v[72:75], a[16:19], v[104:107], v[72:75]\n" \
"ds_read_b128 v[96:99], v30 offset:0\n" \
"v_mfma_f32_16x16x32_bf16 v[72:75], a[24:27], v[108:111], v[72:75]\n" \
"buffer_load_dwordx4 v12, s[28:31], s81 offen offset:0 lds\n" \
"v_mfma_f32_16x16x32_bf16 v[76:79], a[20:23], v[104:107], v[76:79]\n" \
"s_waitcnt lgkmcnt(5)\n" \
"ds_read_b128 v[100:103], v30 offset:2048\n" \
"s_add_u32 m0, s79, 115584\n" \
"v_mfma_f32_16x16x32_bf16 v[76:79], a[28:31], v[108:111], v[76:79]\n" \
"ds_read_b64_tr_b16 a[42:43], v181 offset:8192\n" \
"v_mfma_f32_16x16x32_bf16 v[80:83], a[16:19], v[112:115], v[80:83]\n" \
"ds_read_b128 v[104:107], v30 offset:1024\n" \
"v_mfma_f32_16x16x32_bf16 v[80:83], a[24:27], v[116:119], v[80:83]\n" \
"buffer_load_dwordx4 v12, s[28:31], s81 offen offset:128 lds\n" \
"v_mfma_f32_16x16x32_bf16 v[84:87], a[20:23], v[112:115], v[84:87]\n" \
"LH27_%=:\ns_cbranch_execz LHR2_%=\n" \
"s_waitcnt lgkmcnt(5)\n" \
"ds_read_b128 v[108:111], v30 offset:3072\n" \
"s_mov_b32 s81, 2031616\n" \
"ds_read_b64_tr_b16 a[44:45], v182 offset:8192\n" \
"ds_read_b64_tr_b16 a[46:47], v183 offset:8192\n" \
"v_mfma_f32_16x16x32_bf16 v[84:87], a[28:31], v[116:119], v[84:87]\n" \
"s_add_u32 m0, s79, 122880\n" \
"v_mfma_f32_16x16x32_bf16 v[88:91], a[16:19], v[120:123], v[88:91]\n" \
"ds_read_b128 v[112:115], v30 offset:4096\n" \
"v_mfma_f32_16x16x32_bf16 v[88:91], a[24:27], v[124:127], v[88:91]\n" \
"buffer_load_dwordx4 v12, s[28:31], s81 offen offset:0 lds\n" \
"v_mfma_f32_16x16x32_bf16 v[92:95], a[20:23], v[120:123], v[92:95]\n" \
"s_add_u32 m0, s79, 123776\n" \
"ds_read_b128 v[116:119], v30 offset:6144\n" \
"v_mfma_f32_16x16x32_bf16 v[92:95], a[28:31], v[124:127], v[92:95]\n" \
"s_waitcnt vmcnt(12) lgkmcnt(2)\n" \
"s_barrier\n" \
"v_mfma_f32_16x16x32_bf16 v[32:35], a[32:35], v[96:99], v[32:35]\n" \
"ds_read_b128 v[120:123], v30 offset:5120\n" \
"v_mfma_f32_16x16x32_bf16 v[32:35], a[40:43], v[100:103], v[32:35]\n" \
"buffer_load_dwordx4 v12, s[28:31], s81 offen offset:128 lds\n" \
"v_mfma_f32_16x16x32_bf16 v[36:39], a[36:39], v[96:99], v[36:39]\n" \
"s_waitcnt lgkmcnt(5)\n" \
"ds_read_b128 v[124:127], v30 offset:7168\n" \
"s_add_u32 s80, s66, 49152\n" \
"v_mfma_f32_16x16x32_bf16 v[36:39], a[44:47], v[100:103], v[36:39]\n" \
"s_add_u32 m0, s22, 49152\n" \
"ds_read_b64_tr_b16 a[48:49], v13 offset:16384\n" \
"v_mfma_f32_16x16x32_bf16 v[40:43], a[32:35], v[104:107], v[40:43]\n" \
"ds_read_b128 v[96:99], v30 offset:8192\n" \
"v_mfma_f32_16x16x32_bf16 v[40:43], a[40:43], v[108:111], v[40:43]\n" \
"buffer_load_dwordx4 v4, s[36:39], s80 offen offset:0 sc1 lds\n" \
"v_mfma_f32_16x16x32_bf16 v[44:47], a[36:39], v[104:107], v[44:47]\n" \
"s_waitcnt lgkmcnt(4)\n" \
"ds_read_b128 v[100:103], v30 offset:10240\n" \
"v_mfma_f32_16x16x32_bf16 v[44:47], a[44:47], v[108:111], v[44:47]\n" \
"ds_read_b64_tr_b16 a[50:51], v14 offset:16384\n" \
"v_mfma_f32_16x16x32_bf16 v[48:51], a[32:35], v[112:115], v[48:51]\n" \
"ds_read_b128 v[104:107], v30 offset:9216\n" \
"v_mfma_f32_16x16x32_bf16 v[48:51], a[40:43], v[116:119], v[48:51]\n" \
"buffer_load_dwordx4 v4, s[36:39], s80 offen offset:1024 sc1 lds\n" \
"v_mfma_f32_16x16x32_bf16 v[52:55], a[36:39], v[112:115], v[52:55]\n" \
"s_waitcnt lgkmcnt(5)\n" \
"ds_read_b128 v[108:111], v30 offset:11264\n" \
"v_mfma_f32_16x16x32_bf16 v[52:55], a[44:47], v[116:119], v[52:55]\n" \
"ds_read_b64_tr_b16 a[52:53], v15 offset:16384\n" \
"v_mfma_f32_16x16x32_bf16 v[56:59], a[32:35], v[120:123], v[56:59]\n" \
"ds_read_b128 v[112:115], v28 offset:0\n" \
"v_mfma_f32_16x16x32_bf16 v[56:59], a[40:43], v[124:127], v[56:59]\n" \
"buffer_load_dwordx4 v4, s[36:39], s80 offen offset:2048 sc1 lds\n" \
"v_mfma_f32_16x16x32_bf16 v[60:63], a[36:39], v[120:123], v[60:63]\n" \
"ds_read_b128 v[116:119], v28 offset:2048\n" \
"v_mfma_f32_16x16x32_bf16 v[60:63], a[44:47], v[124:127], v[60:63]\n" \
"s_waitcnt vmcnt(12) lgkmcnt(6)\n" \
"s_barrier\n" \
"ds_read_b64_tr_b16 a[54:55], v16 offset:16384\n" \
"v_mfma_f32_16x16x32_bf16 v[64:67], a[32:35], v[96:99], v[64:67]\n" \
"ds_read_b128 v[120:123], v28 offset:1024\n" \
"s_waitcnt vmcnt(9)\n" \
"v_mfma_f32_16x16x32_bf16 v[64:67], a[40:43], v[100:103], v[64:67]\n" \
"buffer_load_dwordx4 v4, s[36:39], s80 offen offset:3072 sc1 lds\n" \
"v_mfma_f32_16x16x32_bf16 v[68:71], a[36:39], v[96:99], v[68:71]\n" \
"s_waitcnt lgkmcnt(5)\n" \
"ds_read_b128 v[124:127], v28 offset:3072\n" \
"v_cmp_ne_u64 vcc, s[14:15], v[18:19]\n" \
"v_mfma_f32_16x16x32_bf16 v[68:71], a[44:47], v[100:103], v[68:71]\n" \
"s_nop 0\n" \
"s_cbranch_vccnz LPL_%=\nLPLR_%=:\n" \
"v_mfma_f32_16x16x32_bf16 v[72:75], a[32:35], v[104:107], v[72:75]\n" \
"s_add_u32 s80, s68, 0\n" \
"ds_read_b128 v[96:99], v30 offset:12288\n" \
"s_add_u32 m0, s22, 65536\n" \
"v_mfma_f32_16x16x32_bf16 v[72:75], a[40:43], v[108:111], v[72:75]\n" \
"ds_read_b64_tr_b16 a[56:57], v13 offset:24576\n" \
"v_mfma_f32_16x16x32_bf16 v[76:79], a[36:39], v[104:107], v[76:79]\n" \
"s_waitcnt lgkmcnt(5)\n" \
"ds_read_b128 v[100:103], v30 offset:14336\n" \
"v_mfma_f32_16x16x32_bf16 v[76:79], a[44:47], v[108:111], v[76:79]\n" \
"buffer_load_dwordx4 v31, s[36:39], s80 offen offset:0 sc1 lds\n" \
"v_mfma_f32_16x16x32_bf16 v[80:83], a[32:35], v[112:115], v[80:83]\n" \
"ds_read_b128 v[104:107], v30 offset:13312\n" \
"v_mfma_f32_16x16x32_bf16 v[80:83], a[40:43], v[116:119], v[80:83]\n" \
"ds_read_b64_tr_b16 a[58:59], v14 offset:24576\n" \
"v_mfma_f32_16x16x32_bf16 v[84:87], a[36:39], v[112:115], v[84:87]\n" \
"s_waitcnt lgkmcnt(5)\n" \
"ds_read_b128 v[108:111], v30 offset:15360\n" \
"ds_read_b64_tr_b16 a[60:61], v15 offset:24576\n" \
"ds_read_b64_tr_b16 a[62:63], v16 offset:24576\n" \
"v_mfma_f32_16x16x32_bf16 v[84:87], a[44:47], v[116:119], v[84:87]\n" \
"buffer_load_dwordx4 v31, s[36:39], s80 offen offset:1024 sc1 lds\n" \
"v_mfma_f32_16x16x32_bf16 v[88:91], a[32:35], v[120:123], v[88:91]\n" \
"ds_read_b128 v[112:115], v28 offset:32768\n" \
"v_mfma_f32_16x16x32_bf16 v[88:91], a[40:43], v[124:127], v[88:91]\n" \
"v_mfma_f32_16x16x32_bf16 v[92:95], a[36:39], v[120:123], v[92:95]\n" \
"ds_read_b128 v[116:119], v28 offset:34816\n" \
"v_mfma_f32_16x16x32_bf16 v[92:95], a[44:47], v[124:127], v[92:95]\n" \
"s_waitcnt vmcnt(10) lgkmcnt(2)\n" \
"s_barrier\n" \
"v_mfma_f32_16x16x32_bf16 v[32:35], a[48:51], v[96:99], v[32:35]\n" \
"ds_read_b128 v[120:123], v28 offset:33792\n" \
"v_mfma_f32_16x16x32_bf16 v[32:35], a[56:59], v[100:103], v[32:35]\n" \
"buffer_load_dwordx4 v31, s[36:39], s80 offen offset:2048 sc1 lds\n" \
"v_mfma_f32_16x16x32_bf16 v[36:39], a[52:55], v[96:99], v[36:39]\n" \
"s_waitcnt lgkmcnt(5)\n" \
"ds_read_b128 v[124:127], v28 offset:35840\n" \
"v_mfma_f32_16x16x32_bf16 v[36:39], a[60:63], v[100:103], v[36:39]\n" \
"ds_read_b64_tr_b16 a[96:97], v176 offset:16384\n" \
"v_mfma_f32_16x16x32_bf16 v[40:43], a[48:51], v[104:107], v[40:43]\n" \
"ds_read_b128 v[96:99], v28 offset:36864\n" \
"v_mfma_f32_16x16x32_bf16 v[40:43], a[56:59], v[108:111], v[40:43]\n" \
"buffer_load_dwordx4 v31, s[36:39], s80 offen offset:3072 sc1 lds\n" \
"v_mfma_f32_16x16x32_bf16 v[44:47], a[52:55], v[104:107], v[44:47]\n" \
"s_waitcnt lgkmcnt(4)\n" \
"ds_read_b128 v[100:103], v28 offset:38912\n" \
"s_add_u32 s80, s68, 16384\n" \
"v_mfma_f32_16x16x32_bf16 v[44:47], a[60:63], v[108:111], v[44:47]\n" \
"s_add_u32 m0, s22, 81920\n" \
"ds_read_b64_tr_b16 a[98:99], v177 offset:16384\n" \
"v_mfma_f32_16x16x32_bf16 v[48:51], a[48:51], v[112:115], v[48:51]\n" \
"ds_read_b128 v[104:107], v28 offset:37888\n" \
"v_mfma_f32_16x16x32_bf16 v[48:51], a[56:59], v[116:119], v[48:51]\n" \
"buffer_load_dwordx4 v31, s[36:39], s80 offen offset:0 sc1 lds\n" \
"v_mfma_f32_16x16x32_bf16 v[52:55], a[52:55], v[112:115], v[52:55]\n" \
"s_waitcnt lgkmcnt(5)\n" \
"ds_read_b128 v[108:111], v28 offset:39936\n" \
"v_mfma_f32_16x16x32_bf16 v[52:55], a[60:63], v[116:119], v[52:55]\n" \
"ds_read_b64_tr_b16 a[100:101], v178 offset:16384\n" \
"v_mfma_f32_16x16x32_bf16 v[56:59], a[48:51], v[120:123], v[56:59]\n" \
"ds_read_b128 v[112:115], v28 offset:4096\n" \
"v_mfma_f32_16x16x32_bf16 v[56:59], a[56:59], v[124:127], v[56:59]\n" \
"buffer_load_dwordx4 v31, s[36:39], s80 offen offset:1024 sc1 lds\n" \
"v_mfma_f32_16x16x32_bf16 v[60:63], a[52:55], v[120:123], v[60:63]\n" \
"ds_read_b128 v[116:119], v28 offset:6144\n" \
"v_mfma_f32_16x16x32_bf16 v[60:63], a[60:63], v[124:127], v[60:63]\n" \
"s_waitcnt lgkmcnt(6)\n" \
"s_barrier\n" \
"ds_read_b64_tr_b16 a[102:103], v179 offset:16384\n" \
"v_mfma_f32_16x16x32_bf16 v[64:67], a[48:51], v[96:99], v[64:67]\n" \
"ds_read_b128 v[120:123], v28 offset:5120\n" \
"v_mfma_f32_16x16x32_bf16 v[64:67], a[56:59], v[100:103], v[64:67]\n" \
"buffer_load_dwordx4 v31, s[36:39], s80 offen offset:2048 sc1 lds\n" \
"v_mfma_f32_16x16x32_bf16 v[68:71], a[52:55], v[96:99], v[68:71]\n" \
"s_waitcnt lgkmcnt(5)\n" \
"ds_read_b128 v[124:127], v28 offset:7168\n" \
"v_mfma_f32_16x16x32_bf16 v[68:71], a[60:63], v[100:103], v[68:71]\n" \
"ds_read_b64_tr_b16 a[104:105], v176 offset:24576\n" \
"v_mfma_f32_16x16x32_bf16 v[72:75], a[48:51], v[104:107], v[72:75]\n" \
"ds_read_b128 v[96:99], v28 offset:40960\n" \
"v_mfma_f32_16x16x32_bf16 v[72:75], a[56:59], v[108:111], v[72:75]\n" \
"buffer_load_dwordx4 v31, s[36:39], s80 offen offset:3072 sc1 lds\n" \
"v_mfma_f32_16x16x32_bf16 v[76:79], a[52:55], v[104:107], v[76:79]\n" \
"s_waitcnt lgkmcnt(5)\n" \
"ds_read_b128 v[100:103], v28 offset:43008\n" \
"s_add_u32 s80, s68, 32768\n" \
"v_mfma_f32_16x16x32_bf16 v[76:79], a[60:63], v[108:111], v[76:79]\n" \
"s_add_u32 m0, s22, 98304\n" \
"ds_read_b64_tr_b16 a[106:107], v177 offset:24576\n" \
"v_mfma_f32_16x16x32_bf16 v[80:83], a[48:51], v[112:115], v[80:83]\n" \
"ds_read_b128 v[104:107], v28 offset:41984\n" \
"v_mfma_f32_16x16x32_bf16 v[80:83], a[56:59], v[116:119], v[80:83]\n" \
"buffer_load_dwordx4 v31, s[36:39], s80 offen offset:0 sc1 lds\n" \
"v_mfma_f32_16x16x32_bf16 v[84:87], a[52:55], v[112:115], v[84:87]\n" \
"s_waitcnt lgkmcnt(5)\n" \
"ds_read_b128 v[108:111], v28 offset:44032\n" \
"ds_read_b64_tr_b16 a[108:109], v178 offset:24576\n" \
"ds_read_b64_tr_b16 a[110:111], v179 offset:24576\n" \
"v_mfma_f32_16x16x32_bf16 v[84:87], a[60:63], v[116:119], v[84:87]\n" \
"v_mfma_f32_16x16x32_bf16 v[88:91], a[48:51], v[120:123], v[88:91]\n" \
"ds_read_b128 v[112:115], v28 offset:45056\n" \
"v_mfma_f32_16x16x32_bf16 v[88:91], a[56:59], v[124:127], v[88:91]\n" \
"buffer_load_dwordx4 v31, s[36:39], s80 offen offset:1024 sc1 lds\n" \
"v_mfma_f32_16x16x32_bf16 v[92:95], a[52:55], v[120:123], v[92:95]\n" \
"ds_read_b128 v[116:119], v28 offset:47104\n" \
"v_mfma_f32_16x16x32_bf16 v[92:95], a[60:63], v[124:127], v[92:95]\n" \
"s_waitcnt vmcnt(10) lgkmcnt(2)\n" \
"s_barrier\n" \
"v_mfma_f32_16x16x32_bf16 v[32:35], a[96:99], v[96:99], v[32:35]\n" \
"ds_read_b128 v[120:123], v28 offset:46080\n" \
"v_mfma_f32_16x16x32_bf16 v[32:35], a[104:107], v[100:103], v[32:35]\n" \
"buffer_load_dwordx4 v31, s[36:39], s80 offen offset:2048 sc1 lds\n" \
"v_mfma_f32_16x16x32_bf16 v[36:39], a[100:103], v[96:99], v[36:39]\n" \
"s_waitcnt lgkmcnt(5)\n" \
"ds_read_b128 v[124:127], v28 offset:48128\n" \
"v_mfma_f32_16x16x32_bf16 v[36:39], a[108:111], v[100:103], v[36:39]\n" \
"ds_read_b64_tr_b16 a[112:113], v180 offset:49152\n" \
"v_mfma_f32_16x16x32_bf16 v[40:43], a[96:99], v[104:107], v[40:43]\n" \
"ds_read_b128 v[96:99], v28 offset:49152\n" \
"v_mfma_f32_16x16x32_bf16 v[40:43], a[104:107], v[108:111], v[40:43]\n" \
"buffer_load_dwordx4 v31, s[36:39], s80 offen offset:3072 sc1 lds\n" \
"v_mfma_f32_16x16x32_bf16 v[44:47], a[100:103], v[104:107], v[44:47]\n" \
"s_waitcnt lgkmcnt(4)\n" \
"ds_read_b128 v[100:103], v28 offset:51200\n" \
"s_add_u32 s80, s68, 49152\n" \
"v_mfma_f32_16x16x32_bf16 v[44:47], a[108:111], v[108:111], v[44:47]\n" \
"s_add_u32 m0, s22, 147456\n" \
"ds_read_b64_tr_b16 a[114:115], v181 offset:49152\n" \
"v_mfma_f32_16x16x32_bf16 v[48:51], a[96:99], v[112:115], v[48:51]\n" \
"ds_read_b128 v[104:107], v28 offset:50176\n" \
"v_mfma_f32_16x16x32_bf16 v[48:51], a[104:107], v[116:119], v[48:51]\n" \
"buffer_load_dwordx4 v31, s[36:39], s80 offen offset:0 sc1 lds\n" \
"v_mfma_f32_16x16x32_bf16 v[52:55], a[100:103], v[112:115], v[52:55]\n" \
"s_waitcnt lgkmcnt(5)\n" \
"ds_read_b128 v[108:111], v28 offset:52224\n" \
"v_mfma_f32_16x16x32_bf16 v[52:55], a[108:111], v[116:119], v[52:55]\n" \
"ds_read_b64_tr_b16 a[116:117], v182 offset:49152\n" \
"v_mfma_f32_16x16x32_bf16 v[56:59], a[96:99], v[120:123], v[56:59]\n" \
"ds_read_b128 v[112:115], v28 offset:8192\n" \
"v_mfma_f32_16x16x32_bf16 v[56:59], a[104:107], v[124:127], v[56:59]\n" \
"buffer_load_dwordx4 v31, s[36:39], s80 offen offset:1024 sc1 lds\n" \
"v_mfma_f32_16x16x32_bf16 v[60:63], a[100:103], v[120:123], v[60:63]\n" \
"ds_read_b128 v[116:119], v28 offset:10240\n" \
"v_mfma_f32_16x16x32_bf16 v[60:63], a[108:111], v[124:127], v[60:63]\n" \
"s_waitcnt lgkmcnt(6)\n" \
"s_barrier\n" \
"ds_read_b64_tr_b16 a[118:119], v183 offset:49152\n" \
"v_mfma_f32_16x16x32_bf16 v[64:67], a[96:99], v[96:99], v[64:67]\n" \
"ds_read_b128 v[120:123], v28 offset:9216\n" \
"v_mfma_f32_16x16x32_bf16 v[64:67], a[104:107], v[100:103], v[64:67]\n" \
"buffer_load_dwordx4 v31, s[36:39], s80 offen offset:2048 sc1 lds\n" \
"v_mfma_f32_16x16x32_bf16 v[68:71], a[100:103], v[96:99], v[68:71]\n" \
"s_waitcnt lgkmcnt(5)\n" \
"ds_read_b128 v[124:127], v28 offset:11264\n" \
"v_mfma_f32_16x16x32_bf16 v[68:71], a[108:111], v[100:103], v[68:71]\n" \
"ds_read_b64_tr_b16 a[120:121], v180 offset:57344\n" \
"v_mfma_f32_16x16x32_bf16 v[72:75], a[96:99], v[104:107], v[72:75]\n" \
"ds_read_b128 v[96:99], v28 offset:53248\n" \
"v_mfma_f32_16x16x32_bf16 v[72:75], a[104:107], v[108:111], v[72:75]\n" \
"buffer_load_dwordx4 v31, s[36:39], s80 offen offset:3072 sc1 lds\n" \
"v_mfma_f32_16x16x32_bf16 v[76:79], a[100:103], v[104:107], v[76:79]\n" \
"s_waitcnt lgkmcnt(5)\n" \
"ds_read_b128 v[100:103], v28 offset:55296\n" \
"s_add_u32 s80, s69, 0\n" \
"v_mfma_f32_16x16x32_bf16 v[76:79], a[108:111], v[108:111], v[76:79]\n" \
"s_add_u32 m0, s22, 131072\n" \
"ds_read_b64_tr_b16 a[122:123], v181 offset:57344\n" \
"v_mfma_f32_16x16x32_bf16 v[80:83], a[96:99], v[112:115], v[80:83]\n" \
"ds_read_b128 v[104:107], v28 offset:54272\n" \
"v_mfma_f32_16x16x32_bf16 v[80:83], a[104:107], v[116:119], v[80:83]\n" \
"buffer_load_dwordx4 v31, s[36:39], s80 offen offset:0 sc1 lds\n" \
"v_mfma_f32_16x16x32_bf16 v[84:87], a[100:103], v[112:115], v[84:87]\n" \
"s_waitcnt lgkmcnt(5)\n" \
"ds_read_b128 v[108:111], v28 offset:56320\n" \
"ds_read_b64_tr_b16 a[124:125], v182 offset:57344\n" \
"ds_read_b64_tr_b16 a[126:127], v183 offset:57344\n" \
"v_mfma_f32_16x16x32_bf16 v[84:87], a[108:111], v[116:119], v[84:87]\n" \
"v_mfma_f32_16x16x32_bf16 v[88:91], a[96:99], v[120:123], v[88:91]\n" \
"ds_read_b128 v[112:115], v28 offset:57344\n" \
"v_mfma_f32_16x16x32_bf16 v[88:91], a[104:107], v[124:127], v[88:91]\n" \
"buffer_load_dwordx4 v31, s[36:39], s80 offen offset:1024 sc1 lds\n" \
"v_mfma_f32_16x16x32_bf16 v[92:95], a[100:103], v[120:123], v[92:95]\n" \
"ds_read_b128 v[116:119], v28 offset:59392\n" \
"v_mfma_f32_16x16x32_bf16 v[92:95], a[108:111], v[124:127], v[92:95]\n" \
"s_waitcnt lgkmcnt(2)\n" \
"s_barrier\n" \
"v_mfma_f32_16x16x32_bf16 v[32:35], a[112:115], v[96:99], v[32:35]\n" \
"ds_read_b128 v[120:123], v28 offset:58368\n" \
"v_mfma_f32_16x16x32_bf16 v[32:35], a[120:123], v[100:103], v[32:35]\n" \
"buffer_load_dwordx2 v[176:177], v185, s[36:39], s86 offen offset:0 sc1\n" \
"v_mfma_f32_16x16x32_bf16 v[36:39], a[116:119], v[96:99], v[36:39]\n" \
"s_waitcnt lgkmcnt(5)\n" \
"ds_read_b128 v[124:127], v28 offset:60416\n" \
"v_mfma_f32_16x16x32_bf16 v[36:39], a[124:127], v[100:103], v[36:39]\n" \
"v_mfma_f32_16x16x32_bf16 v[40:43], a[112:115], v[104:107], v[40:43]\n" \
"ds_read_b128 v[96:99], v28 offset:61440\n" \
"v_mfma_f32_16x16x32_bf16 v[40:43], a[120:123], v[108:111], v[40:43]\n" \
"buffer_load_dwordx2 v[178:179], v185, s[36:39], s87 offen offset:0 sc1\n" \
"v_mfma_f32_16x16x32_bf16 v[44:47], a[116:119], v[104:107], v[44:47]\n" \
"s_waitcnt lgkmcnt(3)\n" \
"ds_read_b128 v[100:103], v28 offset:63488\n" \
"v_mfma_f32_16x16x32_bf16 v[44:47], a[124:127], v[108:111], v[44:47]\n" \
"v_mfma_f32_16x16x32_bf16 v[48:51], a[112:115], v[112:115], v[48:51]\n" \
"ds_read_b128 v[104:107], v28 offset:62464\n" \
"v_mfma_f32_16x16x32_bf16 v[48:51], a[120:123], v[116:119], v[48:51]\n" \
"buffer_load_dwordx4 v31, s[36:39], s80 offen offset:2048 sc1 lds\n" \
"v_mfma_f32_16x16x32_bf16 v[52:55], a[116:119], v[112:115], v[52:55]\n" \
"s_waitcnt lgkmcnt(3)\n" \
"ds_read_b128 v[108:111], v28 offset:64512\n" \
"v_mfma_f32_16x16x32_bf16 v[52:55], a[124:127], v[116:119], v[52:55]\n" \
"v_mfma_f32_16x16x32_bf16 v[56:59], a[112:115], v[120:123], v[56:59]\n" \
"ds_read_b128 v[112:115], v28 offset:12288\n" \
"v_mfma_f32_16x16x32_bf16 v[56:59], a[120:123], v[124:127], v[56:59]\n" \
"buffer_load_dwordx4 v31, s[36:39], s80 offen offset:3072 sc1 lds\n" \
"v_mfma_f32_16x16x32_bf16 v[60:63], a[116:119], v[120:123], v[60:63]\n" \
"s_add_u32 s80, s69, 16384\n" \
"ds_read_b128 v[116:119], v28 offset:14336\n" \
"s_add_u32 m0, s22, 16384\n" \
"v_mfma_f32_16x16x32_bf16 v[60:63], a[124:127], v[124:127], v[60:63]\n" \
"s_waitcnt vmcnt(18) lgkmcnt(4)\n" \
"s_barrier\n" \
"v_mfma_f32_16x16x32_bf16 v[64:67], a[112:115], v[96:99], v[64:67]\n" \
"ds_read_b128 v[120:123], v28 offset:13312\n" \
"v_mfma_f32_16x16x32_bf16 v[64:67], a[120:123], v[100:103], v[64:67]\n" \
"buffer_load_dwordx4 v31, s[36:39], s80 offen offset:0 sc1 lds\n" \
"v_mfma_f32_16x16x32_bf16 v[68:71], a[116:119], v[96:99], v[68:71]\n" \
"s_waitcnt lgkmcnt(3)\n" \
"ds_read_b128 v[124:127], v28 offset:15360\n" \
"v_mfma_f32_16x16x32_bf16 v[68:71], a[124:127], v[100:103], v[68:71]\n" \
"v_mfma_f32_16x16x32_bf16 v[72:75], a[112:115], v[104:107], v[72:75]\n" \
"ds_read_b128 v[96:99], v29 offset:0\n" \
"v_mfma_f32_16x16x32_bf16 v[72:75], a[120:123], v[108:111], v[72:75]\n" \
"buffer_load_dwordx4 v31, s[36:39], s80 offen offset:1024 sc1 lds\n" \
"v_mfma_f32_16x16x32_bf16 v[76:79], a[116:119], v[104:107], v[76:79]\n" \
"s_waitcnt lgkmcnt(3)\n" \
"ds_read_b128 v[100:103], v29 offset:2048\n" \
"v_mfma_f32_16x16x32_bf16 v[76:79], a[124:127], v[108:111], v[76:79]\n" \
"v_mfma_f32_16x16x32_bf16 v[80:83], a[112:115], v[112:115], v[80:83]\n" \
"ds_read_b128 v[104:107], v29 offset:1024\n" \
"v_mfma_f32_16x16x32_bf16 v[80:83], a[120:123], v[116:119], v[80:83]\n" \
"buffer_load_dwordx4 v31, s[36:39], s80 offen offset:2048 sc1 lds\n" \
"v_mfma_f32_16x16x32_bf16 v[84:87], a[116:119], v[112:115], v[84:87]\n" \
"s_waitcnt lgkmcnt(3)\n" \
"ds_read_b128 v[108:111], v29 offset:3072\n" \
"v_mfma_f32_16x16x32_bf16 v[84:87], a[124:127], v[116:119], v[84:87]\n" \
"v_mfma_f32_16x16x32_bf16 v[88:91], a[112:115], v[120:123], v[88:91]\n" \
"ds_read_b128 v[112:115], v29 offset:4096\n" \
"v_mfma_f32_16x16x32_bf16 v[88:91], a[120:123], v[124:127], v[88:91]\n" \
"buffer_load_dwordx4 v31, s[36:39], s80 offen offset:3072 sc1 lds\n" \
"v_mfma_f32_16x16x32_bf16 v[92:95], a[116:119], v[120:123], v[92:95]\n" \
"s_add_u32 s80, s69, 32768\n" \
"ds_read_b128 v[116:119], v29 offset:6144\n" \
"s_add_u32 m0, s22, 32768\n" \
"v_mfma_f32_16x16x32_bf16 v[92:95], a[124:127], v[124:127], v[92:95]\n" \
"s_waitcnt lgkmcnt(4)\n" \
"s_barrier\n" \
"v_mfma_f32_16x16x32_bf16 v[192:195], a[160:163], v[96:99], 0\n" \
"ds_read_b128 v[120:123], v29 offset:5120\n" \
"v_mfma_f32_16x16x32_bf16 v[192:195], a[168:171], v[100:103], v[192:195]\n" \
"buffer_load_dwordx4 v31, s[36:39], s80 offen offset:0 sc1 lds\n" \
"v_mfma_f32_16x16x32_bf16 v[196:199], a[164:167], v[96:99], 0\n" \
"s_waitcnt lgkmcnt(3)\n" \
"ds_read_b128 v[124:127], v29 offset:7168\n" \
"v_cvt_pk_bf16_f32 v32, v32, v33\n" \
"v_mfma_f32_16x16x32_bf16 v[196:199], a[172:175], v[100:103], v[196:199]\n" \
"v_cvt_pk_bf16_f32 v33, v34, v35\n" \
"v_cvt_pk_bf16_f32 v34, v36, v37\n" \
"v_mfma_f32_16x16x32_bf16 v[200:203], a[160:163], v[104:107], 0\n" \
"ds_read_b128 v[96:99], v29 offset:8192\n" \
"v_cvt_pk_bf16_f32 v35, v38, v39\n" \
"v_mfma_f32_16x16x32_bf16 v[200:203], a[168:171], v[108:111], v[200:203]\n" \
"buffer_store_dwordx4 v[32:35], v4, s[36:39], s72 offen offset:0 sc1\n" \
"v_mfma_f32_16x16x32_bf16 v[204:207], a[164:167], v[104:107], 0\n" \
"s_waitcnt lgkmcnt(3)\n" \
"ds_read_b128 v[100:103], v29 offset:10240\n" \
"v_cvt_pk_bf16_f32 v40, v40, v41\n" \
"v_mfma_f32_16x16x32_bf16 v[204:207], a[172:175], v[108:111], v[204:207]\n" \
"v_cvt_pk_bf16_f32 v41, v42, v43\n" \
"v_cvt_pk_bf16_f32 v42, v44, v45\n" \
"v_mfma_f32_16x16x32_bf16 v[192:195], a[176:179], v[112:115], v[192:195]\n" \
"ds_read_b128 v[104:107], v29 offset:9216\n" \
"v_cvt_pk_bf16_f32 v43, v46, v47\n" \
"v_mfma_f32_16x16x32_bf16 v[192:195], a[184:187], v[116:119], v[192:195]\n" \
"v_mfma_f32_16x16x32_bf16 v[196:199], a[180:183], v[112:115], v[196:199]\n" \
"s_waitcnt lgkmcnt(3)\n" \
"ds_read_b128 v[108:111], v29 offset:11264\n" \
"v_mfma_f32_16x16x32_bf16 v[196:199], a[188:191], v[116:119], v[196:199]\n" \
"v_mfma_f32_16x16x32_bf16 v[200:203], a[176:179], v[120:123], v[200:203]\n" \
"ds_read_b128 v[112:115], v29 offset:12288\n" \
"v_mfma_f32_16x16x32_bf16 v[200:203], a[184:187], v[124:127], v[200:203]\n" \
"buffer_store_dwordx4 v[40:43], v4, s[36:39], s72 offen offset:1024 sc1\n" \
"v_mfma_f32_16x16x32_bf16 v[204:207], a[180:183], v[120:123], v[204:207]\n" \
"ds_read_b128 v[116:119], v29 offset:14336\n" \
"v_mfma_f32_16x16x32_bf16 v[204:207], a[188:191], v[124:127], v[204:207]\n" \
"s_waitcnt vmcnt(21) lgkmcnt(4)\n" \
"s_barrier\n" \
"v_mfma_f32_16x16x32_bf16 v[192:195], a[192:195], v[96:99], v[192:195]\n" \
"ds_read_b128 v[120:123], v29 offset:13312\n" \
"v_mfma_f32_16x16x32_bf16 v[192:195], a[200:203], v[100:103], v[192:195]\n" \
"v_mfma_f32_16x16x32_bf16 v[196:199], a[196:199], v[96:99], v[196:199]\n" \
"s_waitcnt lgkmcnt(3)\n" \
"ds_read_b128 v[124:127], v29 offset:15360\n" \
"v_mfma_f32_16x16x32_bf16 v[196:199], a[204:207], v[100:103], v[196:199]\n" \
"v_mfma_f32_16x16x32_bf16 v[200:203], a[192:195], v[104:107], v[200:203]\n" \
"ds_read_b128 v[96:99], v29 offset:16384\n" \
"v_mfma_f32_16x16x32_bf16 v[200:203], a[200:203], v[108:111], v[200:203]\n" \
"v_mfma_f32_16x16x32_bf16 v[204:207], a[196:199], v[104:107], v[204:207]\n" \
"s_waitcnt lgkmcnt(3)\n" \
"ds_read_b128 v[100:103], v29 offset:18432\n" \
"v_mfma_f32_16x16x32_bf16 v[204:207], a[204:207], v[108:111], v[204:207]\n" \
"s_waitcnt vmcnt(9)\n" \
"v_mfma_f32_16x16x32_bf16 v[192:195], a[208:211], v[112:115], v[192:195]\n" \
"ds_read_b128 v[104:107], v29 offset:17408\n" \
"v_mfma_f32_16x16x32_bf16 v[192:195], a[216:219], v[116:119], v[192:195]\n" \
"buffer_load_dwordx4 v31, s[36:39], s80 offen offset:1024 sc1 lds\n" \
"v_mfma_f32_16x16x32_bf16 v[196:199], a[212:215], v[112:115], v[196:199]\n" \
"s_waitcnt lgkmcnt(3)\n" \
"ds_read_b128 v[108:111], v29 offset:19456\n" \
"v_mfma_f32_16x16x32_bf16 v[196:199], a[220:223], v[116:119], v[196:199]\n" \
"v_mfma_f32_16x16x32_bf16 v[200:203], a[208:211], v[120:123], v[200:203]\n" \
"ds_read_b128 v[112:115], v29 offset:20480\n" \
"v_mfma_f32_16x16x32_bf16 v[200:203], a[216:219], v[124:127], v[200:203]\n" \
"buffer_load_dwordx4 v31, s[36:39], s80 offen offset:2048 sc1 lds\n" \
"v_mfma_f32_16x16x32_bf16 v[204:207], a[212:215], v[120:123], v[204:207]\n" \
"s_waitcnt lgkmcnt(3)\n" \
"ds_read_b128 v[116:119], v29 offset:22528\n" \
"v_mfma_f32_16x16x32_bf16 v[204:207], a[220:223], v[124:127], v[204:207]\n" \
"v_mfma_f32_16x16x32_bf16 v[192:195], a[224:227], v[96:99], v[192:195]\n" \
"ds_read_b128 v[120:123], v29 offset:21504\n" \
"v_mfma_f32_16x16x32_bf16 v[192:195], a[232:235], v[100:103], v[192:195]\n" \
"buffer_load_dwordx4 v31, s[36:39], s80 offen offset:3072 sc1 lds\n" \
"v_mfma_f32_16x16x32_bf16 v[196:199], a[228:231], v[96:99], v[196:199]\n" \
"s_waitcnt lgkmcnt(3)\n" \
"ds_read_b128 v[124:127], v29 offset:23552\n" \
"s_add_u32 s80, s69, 49152\n" \
"v_mfma_f32_16x16x32_bf16 v[196:199], a[236:239], v[100:103], v[196:199]\n" \
"s_add_u32 m0, s22, 114688\n" \
"v_mfma_f32_16x16x32_bf16 v[200:203], a[224:227], v[104:107], v[200:203]\n" \
"ds_read_b128 v[96:99], v29 offset:24576\n" \
"v_mfma_f32_16x16x32_bf16 v[200:203], a[232:235], v[108:111], v[200:203]\n" \
"buffer_load_dwordx4 v31, s[36:39], s80 offen offset:0 sc1 lds\n" \
"v_mfma_f32_16x16x32_bf16 v[204:207], a[228:231], v[104:107], v[204:207]\n" \
"s_waitcnt lgkmcnt(3)\n" \
"ds_read_b128 v[100:103], v29 offset:26624\n" \
"v_cvt_pk_bf16_f32 v48, v48, v49\n" \
"v_mfma_f32_16x16x32_bf16 v[204:207], a[236:239], v[108:111], v[204:207]\n" \
"v_cvt_pk_bf16_f32 v49, v50, v51\n" \
"v_cvt_pk_bf16_f32 v50, v52, v53\n" \
"v_mfma_f32_16x16x32_bf16 v[192:195], a[240:243], v[112:115], v[192:195]\n" \
"ds_read_b128 v[104:107], v29 offset:25600\n" \
"v_cvt_pk_bf16_f32 v51, v54, v55\n" \
"v_mfma_f32_16x16x32_bf16 v[192:195], a[248:251], v[116:119], v[192:195]\n" \
"buffer_store_dwordx4 v[48:51], v4, s[36:39], s73 offen offset:0 sc1\n" \
"v_mfma_f32_16x16x32_bf16 v[196:199], a[244:247], v[112:115], v[196:199]\n" \
"s_waitcnt lgkmcnt(3)\n" \
"ds_read_b128 v[108:111], v29 offset:27648\n" \
"v_cvt_pk_bf16_f32 v56, v56, v57\n" \
"v_mfma_f32_16x16x32_bf16 v[196:199], a[252:255], v[116:119], v[196:199]\n" \
"v_cvt_pk_bf16_f32 v57, v58, v59\n" \
"v_cvt_pk_bf16_f32 v58, v60, v61\n" \
"v_mfma_f32_16x16x32_bf16 v[200:203], a[240:243], v[120:123], v[200:203]\n" \
"ds_read_b128 v[112:115], v29 offset:28672\n" \
"v_cvt_pk_bf16_f32 v59, v62, v63\n" \
"v_mfma_f32_16x16x32_bf16 v[200:203], a[248:251], v[124:127], v[200:203]\n" \
"v_mfma_f32_16x16x32_bf16 v[204:207], a[244:247], v[120:123], v[204:207]\n" \
"ds_read_b128 v[116:119], v29 offset:30720\n" \
"v_mfma_f32_16x16x32_bf16 v[204:207], a[252:255], v[124:127], v[204:207]\n" \
"s_waitcnt vmcnt(22) lgkmcnt(4)\n" \
"s_barrier\n" \
"v_mfma_f32_16x16x32_bf16 v[192:195], a[64:67], v[96:99], v[192:195]\n" \
"ds_read_b128 v[120:123], v29 offset:29696\n" \
"v_mfma_f32_16x16x32_bf16 v[192:195], a[72:75], v[100:103], v[192:195]\n" \
"v_mfma_f32_16x16x32_bf16 v[196:199], a[68:71], v[96:99], v[196:199]\n" \
"s_waitcnt lgkmcnt(3)\n" \
"ds_read_b128 v[124:127], v29 offset:31744\n" \
"v_mfma_f32_16x16x32_bf16 v[196:199], a[76:79], v[100:103], v[196:199]\n" \
"v_mfma_f32_16x16x32_bf16 v[200:203], a[64:67], v[104:107], v[200:203]\n" \
"ds_read_b128 v[96:99], v29 offset:32768\n" \
"v_mfma_f32_16x16x32_bf16 v[200:203], a[72:75], v[108:111], v[200:203]\n" \
"buffer_store_dwordx4 v[56:59], v4, s[36:39], s73 offen offset:1024 sc1\n" \
"v_mfma_f32_16x16x32_bf16 v[204:207], a[68:71], v[104:107], v[204:207]\n" \
"s_waitcnt lgkmcnt(3)\n" \
"ds_read_b128 v[100:103], v29 offset:34816\n" \
"v_permlane16_swap_b32 v144, v146\n" \
"v_mfma_f32_16x16x32_bf16 v[204:207], a[76:79], v[108:111], v[204:207]\n" \
"v_permlane16_swap_b32 v145, v147\n" \
"v_sub_f32 v5, 1.0, v176\n" \
"v_mfma_f32_16x16x32_bf16 v[192:195], a[80:83], v[112:115], v[192:195]\n" \
"ds_read_b128 v[104:107], v29 offset:33792\n" \
"v_cvt_pk_bf16_f32 v5, v5, v5\n" \
"v_mfma_f32_16x16x32_bf16 v[192:195], a[88:91], v[116:119], v[192:195]\n" \
"v_and_b32 v5, 0xffff0000, v5\n" \
"v_lshlrev_b32 v32, 16, v144\n" \
"v_mfma_f32_16x16x32_bf16 v[196:199], a[84:87], v[112:115], v[196:199]\n" \
"s_waitcnt lgkmcnt(3)\n" \
"ds_read_b128 v[108:111], v29 offset:35840\n" \
"v_and_b32 v33, 0xffff0000, v144\n" \
"v_mfma_f32_16x16x32_bf16 v[196:199], a[92:95], v[116:119], v[196:199]\n" \
"v_lshlrev_b32 v188, 16, v128\n" \
"v_and_b32 v189, 0xffff0000, v128\n" \
"v_mfma_f32_16x16x32_bf16 v[200:203], a[80:83], v[120:123], v[200:203]\n" \
"ds_read_b128 v[112:115], v29 offset:36864\n" \
"v_mul_f32 v32, v176, v32\n" \
"v_mfma_f32_16x16x32_bf16 v[200:203], a[88:91], v[124:127], v[200:203]\n" \
"v_mul_f32 v33, v176, v33\n" \
"v_mul_f32 v32, v188, v32\n" \
"v_mfma_f32_16x16x32_bf16 v[204:207], a[84:87], v[120:123], v[204:207]\n" \
"s_waitcnt lgkmcnt(3)\n" \
"ds_read_b128 v[116:119], v29 offset:38912\n" \
"v_mul_f32 v33, v189, v33\n" \
"v_mfma_f32_16x16x32_bf16 v[204:207], a[92:95], v[124:127], v[204:207]\n" \
"v_lshlrev_b32 v34, 16, v145\n" \
"v_and_b32 v35, 0xffff0000, v145\n" \
"v_mfma_f32_16x16x32_bf16 v[192:195], a[128:131], v[96:99], v[192:195]\n" \
"ds_read_b128 v[120:123], v29 offset:37888\n" \
"v_lshlrev_b32 v188, 16, v129\n" \
"v_mfma_f32_16x16x32_bf16 v[192:195], a[136:139], v[100:103], v[192:195]\n" \
"buffer_load_dwordx4 v31, s[36:39], s80 offen offset:1024 sc1 lds\n" \
"v_mfma_f32_16x16x32_bf16 v[196:199], a[132:135], v[96:99], v[196:199]\n" \
"s_waitcnt lgkmcnt(3)\n" \
"ds_read_b128 v[124:127], v29 offset:39936\n" \
"v_and_b32 v189, 0xffff0000, v129\n" \
"v_mfma_f32_16x16x32_bf16 v[196:199], a[140:143], v[100:103], v[196:199]\n" \
"v_mul_f32 v34, v176, v34\n" \
"v_mul_f32 v35, v176, v35\n" \
"v_mfma_f32_16x16x32_bf16 v[200:203], a[128:131], v[104:107], v[200:203]\n" \
"ds_read_b128 v[96:99], v29 offset:40960\n" \
"v_mul_f32 v34, v188, v34\n" \
"v_mfma_f32_16x16x32_bf16 v[200:203], a[136:139], v[108:111], v[200:203]\n" \
"buffer_load_dwordx4 v31, s[36:39], s80 offen offset:2048 sc1 lds\n" \
"v_mfma_f32_16x16x32_bf16 v[204:207], a[132:135], v[104:107], v[204:207]\n" \
"s_waitcnt lgkmcnt(3)\n" \
"ds_read_b128 v[100:103], v29 offset:43008\n" \
"v_mul_f32 v35, v189, v35\n" \
"v_mfma_f32_16x16x32_bf16 v[204:207], a[140:143], v[108:111], v[204:207]\n" \
"v_lshlrev_b32 v36, 16, v146\n" \
"v_and_b32 v37, 0xffff0000, v146\n" \
"v_mfma_f32_16x16x32_bf16 v[192:195], a[144:147], v[112:115], v[192:195]\n" \
"ds_read_b128 v[104:107], v29 offset:41984\n" \
"v_lshlrev_b32 v188, 16, v132\n" \
"v_mfma_f32_16x16x32_bf16 v[192:195], a[152:155], v[116:119], v[192:195]\n" \
"buffer_load_dwordx4 v31, s[36:39], s80 offen offset:3072 sc1 lds\n" \
"v_mfma_f32_16x16x32_bf16 v[196:199], a[148:151], v[112:115], v[196:199]\n" \
"s_waitcnt lgkmcnt(3)\n" \
"ds_read_b128 v[108:111], v29 offset:44032\n" \
"s_add_u32 s80, s70, 0\n" \
"v_mfma_f32_16x16x32_bf16 v[196:199], a[156:159], v[116:119], v[196:199]\n" \
"s_add_u32 m0, s22, 0\n" \
"s_cmp_eq_u32 s89, 3\ns_cbranch_scc1 LSW_%=\nLSWR_%=:\n" \
"v_mfma_f32_16x16x32_bf16 v[200:203], a[144:147], v[120:123], v[200:203]\n" \
"ds_read_b128 v[112:115], v29 offset:45056\n" \
"v_cvt_pk_bf16_f32 v80, v80, v81\n" \
"v_mfma_f32_16x16x32_bf16 v[200:203], a[152:155], v[124:127], v[200:203]\n" \
"buffer_load_dwordx4 v31, s[36:39], s80 offen offset:0 sc1 lds\n" \
"v_mfma_f32_16x16x32_bf16 v[204:207], a[148:151], v[120:123], v[204:207]\n" \
"ds_read_b128 v[116:119], v29 offset:47104\n" \
"v_cvt_pk_bf16_f32 v81, v82, v83\n" \
"v_mfma_f32_16x16x32_bf16 v[204:207], a[156:159], v[124:127], v[204:207]\n" \
"s_waitcnt vmcnt(23) lgkmcnt(4)\n" \
"s_barrier\n" \
"v_cvt_pk_bf16_f32 v82, v84, v85\n" \
"v_mfma_f32_16x16x32_bf16 v[192:195], a[0:3], v[96:99], v[192:195]\n" \
"ds_read_b128 v[120:123], v29 offset:46080\n" \
"v_cvt_pk_bf16_f32 v83, v86, v87\n" \
"v_mfma_f32_16x16x32_bf16 v[192:195], a[8:11], v[100:103], v[192:195]\n" \
"buffer_store_dwordx4 v[80:83], v4, s[36:39], s74 offen offset:0 sc1\n" \
"v_mfma_f32_16x16x32_bf16 v[196:199], a[4:7], v[96:99], v[196:199]\n" \
"s_waitcnt lgkmcnt(3)\n" \
"ds_read_b128 v[124:127], v29 offset:48128\n" \
"v_cvt_pk_bf16_f32 v88, v88, v89\n" \
"v_mfma_f32_16x16x32_bf16 v[196:199], a[12:15], v[100:103], v[196:199]\n" \
"s_waitcnt vmcnt(11)\n" \
"v_cvt_pk_bf16_f32 v89, v90, v91\n" \
"v_cvt_pk_bf16_f32 v90, v92, v93\n" \
"v_mfma_f32_16x16x32_bf16 v[200:203], a[0:3], v[104:107], v[200:203]\n" \
"ds_read_b128 v[96:99], v30 offset:16384\n" \
"v_cvt_pk_bf16_f32 v91, v94, v95\n" \
"v_mfma_f32_16x16x32_bf16 v[200:203], a[8:11], v[108:111], v[200:203]\n" \
"buffer_store_dwordx2 v[10:11], v20, s[36:39], s23 offen sc1\n" \
"v_mfma_f32_16x16x32_bf16 v[204:207], a[4:7], v[104:107], v[204:207]\n" \
"s_waitcnt lgkmcnt(3)\n" \
"ds_read_b128 v[100:103], v30 offset:18432\n" \
"v_and_b32 v189, 0xffff0000, v132\n" \
"v_mfma_f32_16x16x32_bf16 v[204:207], a[12:15], v[108:111], v[204:207]\n" \
"v_mul_f32 v36, v176, v36\n" \
"v_mul_f32 v37, v176, v37\n" \
"v_mfma_f32_16x16x32_bf16 v[192:195], a[16:19], v[112:115], v[192:195]\n" \
"ds_read_b128 v[104:107], v30 offset:17408\n" \
"v_mul_f32 v36, v188, v36\n" \
"v_mfma_f32_16x16x32_bf16 v[192:195], a[24:27], v[116:119], v[192:195]\n" \
"v_mul_f32 v37, v189, v37\n" \
"v_lshlrev_b32 v38, 16, v147\n" \
"v_mfma_f32_16x16x32_bf16 v[196:199], a[20:23], v[112:115], v[196:199]\n" \
"LH28_%=:\ns_cbranch_execz LHR2_%=\n" \
"s_waitcnt lgkmcnt(3)\n" \
"ds_read_b128 v[108:111], v30 offset:19456\n" \
"v_and_b32 v39, 0xffff0000, v147\n" \
"v_mfma_f32_16x16x32_bf16 v[196:199], a[28:31], v[116:119], v[196:199]\n" \
"v_lshlrev_b32 v188, 16, v133\n" \
"v_and_b32 v189, 0xffff0000, v133\n" \
"v_mfma_f32_16x16x32_bf16 v[200:203], a[16:19], v[120:123], v[200:203]\n" \
"ds_read_b128 v[112:115], v30 offset:20480\n" \
"v_mul_f32 v38, v176, v38\n" \
"v_mfma_f32_16x16x32_bf16 v[200:203], a[24:27], v[124:127], v[200:203]\n" \
"v_mul_f32 v39, v176, v39\n" \
"v_mul_f32 v38, v188, v38\n" \
"v_mfma_f32_16x16x32_bf16 v[204:207], a[20:23], v[120:123], v[204:207]\n" \
"s_waitcnt lgkmcnt(3)\n" \
"ds_read_b128 v[116:119], v30 offset:22528\n" \
"v_mul_f32 v39, v189, v39\n" \
"v_mfma_f32_16x16x32_bf16 v[204:207], a[28:31], v[124:127], v[204:207]\n" \
"buffer_store_dwordx4 v[88:91], v4, s[36:39], s74 offen offset:1024 sc1\n" \
"v_mfma_f32_16x16x32_bf16 v[192:195], a[32:35], v[96:99], v[192:195]\n" \
"ds_read_b128 v[120:123], v30 offset:21504\n" \
"v_permlane16_swap_b32 v148, v150\n" \
"v_mfma_f32_16x16x32_bf16 v[192:195], a[40:43], v[100:103], v[192:195]\n" \
"v_permlane16_swap_b32 v149, v151\n" \
"v_sub_f32 v21, 1.0, v177\n" \
"v_mfma_f32_16x16x32_bf16 v[196:199], a[36:39], v[96:99], v[196:199]\n" \
"s_waitcnt lgkmcnt(3)\n" \
"ds_read_b128 v[124:127], v30 offset:23552\n" \
"v_cvt_pk_bf16_f32 v21, v21, v21\n" \
"v_mfma_f32_16x16x32_bf16 v[196:199], a[44:47], v[100:103], v[196:199]\n" \
"v_and_b32 v21, 0xffff0000, v21\n" \
"v_lshlrev_b32 v40, 16, v148\n" \
"v_mfma_f32_16x16x32_bf16 v[200:203], a[32:35], v[104:107], v[200:203]\n" \
"ds_read_b128 v[96:99], v30 offset:24576\n" \
"v_and_b32 v41, 0xffff0000, v148\n" \
"v_mfma_f32_16x16x32_bf16 v[200:203], a[40:43], v[108:111], v[200:203]\n" \
"v_lshlrev_b32 v188, 16, v130\n" \
"v_and_b32 v189, 0xffff0000, v130\n" \
"v_mfma_f32_16x16x32_bf16 v[204:207], a[36:39], v[104:107], v[204:207]\n" \
"s_waitcnt lgkmcnt(3)\n" \
"ds_read_b128 v[100:103], v30 offset:26624\n" \
"v_mul_f32 v40, v177, v40\n" \
"v_mfma_f32_16x16x32_bf16 v[204:207], a[44:47], v[108:111], v[204:207]\n" \
"v_mul_f32 v41, v177, v41\n" \
"v_mul_f32 v40, v188, v40\n" \
"v_mfma_f32_16x16x32_bf16 v[192:195], a[48:51], v[112:115], v[192:195]\n" \
"ds_read_b128 v[104:107], v30 offset:25600\n" \
"v_mul_f32 v41, v189, v41\n" \
"v_mfma_f32_16x16x32_bf16 v[192:195], a[56:59], v[116:119], v[192:195]\n" \
"v_lshlrev_b32 v42, 16, v149\n" \
"v_and_b32 v43, 0xffff0000, v149\n" \
"v_mfma_f32_16x16x32_bf16 v[196:199], a[52:55], v[112:115], v[196:199]\n" \
"s_waitcnt lgkmcnt(3)\n" \
"ds_read_b128 v[108:111], v30 offset:27648\n" \
"v_lshlrev_b32 v188, 16, v131\n" \
"v_mfma_f32_16x16x32_bf16 v[196:199], a[60:63], v[116:119], v[196:199]\n" \
"buffer_load_dwordx4 v31, s[36:39], s80 offen offset:1024 sc1 lds\n" \
"v_mfma_f32_16x16x32_bf16 v[200:203], a[48:51], v[120:123], v[200:203]\n" \
"ds_read_b128 v[112:115], v30 offset:28672\n" \
"v_and_b32 v189, 0xffff0000, v131\n" \
"v_mfma_f32_16x16x32_bf16 v[200:203], a[56:59], v[124:127], v[200:203]\n" \
"v_mul_f32 v42, v177, v42\n" \
"v_mul_f32 v43, v177, v43\n" \
"v_mfma_f32_16x16x32_bf16 v[204:207], a[52:55], v[120:123], v[204:207]\n" \
"ds_read_b128 v[116:119], v30 offset:30720\n" \
"v_mul_f32 v42, v188, v42\n" \
"v_mfma_f32_16x16x32_bf16 v[204:207], a[60:63], v[124:127], v[204:207]\n" \
"s_waitcnt vmcnt(21) lgkmcnt(4)\n" \
"s_barrier\n" \
"v_mul_f32 v43, v189, v43\n" \
"v_mfma_f32_16x16x32_bf16 v[192:195], a[96:99], v[96:99], v[192:195]\n" \
"ds_read_b128 v[120:123], v30 offset:29696\n" \
"v_lshlrev_b32 v44, 16, v150\n" \
"v_mfma_f32_16x16x32_bf16 v[192:195], a[104:107], v[100:103], v[192:195]\n" \
"buffer_load_dwordx4 v31, s[36:39], s80 offen offset:2048 sc1 lds\n" \
"v_mfma_f32_16x16x32_bf16 v[196:199], a[100:103], v[96:99], v[196:199]\n" \
"s_waitcnt lgkmcnt(3)\n" \
"ds_read_b128 v[124:127], v30 offset:31744\n" \
"v_and_b32 v45, 0xffff0000, v150\n" \
"v_mfma_f32_16x16x32_bf16 v[196:199], a[108:111], v[100:103], v[196:199]\n" \
"v_lshlrev_b32 v188, 16, v134\n" \
"v_and_b32 v189, 0xffff0000, v134\n" \
"v_mfma_f32_16x16x32_bf16 v[200:203], a[96:99], v[104:107], v[200:203]\n" \
"ds_read_b128 v[96:99], v30 offset:0\n" \
"v_mul_f32 v44, v177, v44\n" \
"v_mfma_f32_16x16x32_bf16 v[200:203], a[104:107], v[108:111], v[200:203]\n" \
"buffer_load_dwordx4 v31, s[36:39], s80 offen offset:3072 sc1 lds\n" \
"v_mfma_f32_16x16x32_bf16 v[204:207], a[100:103], v[104:107], v[204:207]\n" \
"s_waitcnt lgkmcnt(3)\n" \
"ds_read_b128 v[100:103], v30 offset:2048\n" \
"s_add_u32 s80, s70, 16384\n" \
"v_mfma_f32_16x16x32_bf16 v[204:207], a[108:111], v[108:111], v[204:207]\n" \
"s_add_u32 m0, s22, 49152\n" \
"v_mul_f32 v45, v177, v45\n" \
"v_mul_f32 v44, v188, v44\n" \
"v_mfma_f32_16x16x32_bf16 v[192:195], a[112:115], v[112:115], v[192:195]\n" \
"ds_read_b128 v[104:107], v30 offset:1024\n" \
"v_mul_f32 v45, v189, v45\n" \
"v_mfma_f32_16x16x32_bf16 v[192:195], a[120:123], v[116:119], v[192:195]\n" \
"buffer_load_dwordx4 v31, s[36:39], s80 offen offset:0 sc1 lds\n" \
"v_mfma_f32_16x16x32_bf16 v[196:199], a[116:119], v[112:115], v[196:199]\n" \
"s_waitcnt lgkmcnt(3)\n" \
"ds_read_b128 v[108:111], v30 offset:3072\n" \
"v_lshlrev_b32 v46, 16, v151\n" \
"v_mfma_f32_16x16x32_bf16 v[196:199], a[124:127], v[116:119], v[196:199]\n" \
"s_waitcnt vmcnt(11)\n" \
"v_and_b32 v47, 0xffff0000, v151\n" \
"v_lshlrev_b32 v188, 16, v135\n" \
"v_mfma_f32_16x16x32_bf16 v[200:203], a[112:115], v[120:123], v[200:203]\n" \
"ds_read_b128 v[112:115], v30 offset:4096\n" \
"v_and_b32 v189, 0xffff0000, v135\n" \
"v_mfma_f32_16x16x32_bf16 v[200:203], a[120:123], v[124:127], v[200:203]\n" \
"buffer_store_dwordx2 v[10:11], v20, s[36:39], s24 offen sc1\n" \
"v_mfma_f32_16x16x32_bf16 v[204:207], a[116:119], v[120:123], v[204:207]\n" \
"s_waitcnt lgkmcnt(3)\n" \
"ds_read_b128 v[116:119], v30 offset:6144\n" \
"v_mul_f32 v46, v177, v46\n" \
"v_mfma_f32_16x16x32_bf16 v[204:207], a[124:127], v[124:127], v[204:207]\n" \
"buffer_load_dwordx4 v31, s[36:39], s80 offen offset:1024 sc1 lds\n" \
"v_mfma_f32_16x16x32_bf16 v[208:211], a[160:163], v[96:99], 0\n" \
"ds_read_b128 v[120:123], v30 offset:5120\n" \
"v_mul_f32 v47, v177, v47\n" \
"v_mfma_f32_16x16x32_bf16 v[208:211], a[168:171], v[100:103], v[208:211]\n" \
"v_mul_f32 v46, v188, v46\n" \
"v_mul_f32 v47, v189, v47\n" \
"v_mfma_f32_16x16x32_bf16 v[212:215], a[164:167], v[96:99], 0\n" \
"s_waitcnt lgkmcnt(3)\n" \
"ds_read_b128 v[124:127], v30 offset:7168\n" \
"v_cvt_pk_bf16_f32 v192, v192, v193\n" \
"v_mfma_f32_16x16x32_bf16 v[212:215], a[172:175], v[100:103], v[212:215]\n" \
"buffer_load_dwordx4 v31, s[36:39], s80 offen offset:2048 sc1 lds\n" \
"v_mfma_f32_16x16x32_bf16 v[216:219], a[160:163], v[104:107], 0\n" \
"ds_read_b128 v[96:99], v30 offset:8192\n" \
"v_cvt_pk_bf16_f32 v193, v194, v195\n" \
"v_mfma_f32_16x16x32_bf16 v[216:219], a[168:171], v[108:111], v[216:219]\n" \
"v_cvt_pk_bf16_f32 v194, v196, v197\n" \
"v_cvt_pk_bf16_f32 v195, v198, v199\n" \
"v_mfma_f32_16x16x32_bf16 v[220:223], a[164:167], v[104:107], 0\n" \
"s_waitcnt lgkmcnt(3)\n" \
"ds_read_b128 v[100:103], v30 offset:10240\n" \
"v_cvt_pk_bf16_f32 v200, v200, v201\n" \
"v_mfma_f32_16x16x32_bf16 v[220:223], a[172:175], v[108:111], v[220:223]\n" \
"buffer_store_dwordx4 v[192:195], v4, s[36:39], s75 offen offset:0 sc1\n" \
"v_mfma_f32_16x16x32_bf16 v[208:211], a[176:179], v[112:115], v[208:211]\n" \
"ds_read_b128 v[104:107], v30 offset:9216\n" \
"v_cvt_pk_bf16_f32 v201, v202, v203\n" \
"v_mfma_f32_16x16x32_bf16 v[208:211], a[184:187], v[116:119], v[208:211]\n" \
"v_cvt_pk_bf16_f32 v202, v204, v205\n" \
"v_cvt_pk_bf16_f32 v203, v206, v207\n" \
"v_mfma_f32_16x16x32_bf16 v[212:215], a[180:183], v[112:115], v[212:215]\n" \
"s_waitcnt lgkmcnt(3)\n" \
"ds_read_b128 v[108:111], v30 offset:11264\n" \
"v_permlane16_swap_b32 v152, v154\n" \
"v_mfma_f32_16x16x32_bf16 v[212:215], a[188:191], v[116:119], v[212:215]\n" \
"v_permlane16_swap_b32 v153, v155\n" \
"v_sub_f32 v6, 1.0, v178\n" \
"v_mfma_f32_16x16x32_bf16 v[216:219], a[176:179], v[120:123], v[216:219]\n" \
"ds_read_b128 v[112:115], v30 offset:12288\n" \
"v_cvt_pk_bf16_f32 v6, v6, v6\n" \
"v_mfma_f32_16x16x32_bf16 v[216:219], a[184:187], v[124:127], v[216:219]\n" \
"v_and_b32 v6, 0xffff0000, v6\n" \
"v_lshlrev_b32 v48, 16, v152\n" \
"v_mfma_f32_16x16x32_bf16 v[220:223], a[180:183], v[120:123], v[220:223]\n" \
"ds_read_b128 v[116:119], v30 offset:14336\n" \
"v_and_b32 v49, 0xffff0000, v152\n" \
"v_mfma_f32_16x16x32_bf16 v[220:223], a[188:191], v[124:127], v[220:223]\n" \
"s_waitcnt vmcnt(24) lgkmcnt(4)\n" \
"s_barrier\n" \
"v_lshlrev_b32 v188, 16, v136\n" \
"v_mfma_f32_16x16x32_bf16 v[208:211], a[192:195], v[96:99], v[208:211]\n" \
"ds_read_b128 v[120:123], v30 offset:13312\n" \
"v_and_b32 v189, 0xffff0000, v136\n" \
"v_mfma_f32_16x16x32_bf16 v[208:211], a[200:203], v[100:103], v[208:211]\n" \
"buffer_store_dwordx4 v[200:203], v4, s[36:39], s75 offen offset:1024 sc1\n" \
"v_mfma_f32_16x16x32_bf16 v[212:215], a[196:199], v[96:99], v[212:215]\n" \
"s_waitcnt lgkmcnt(3)\n" \
"ds_read_b128 v[124:127], v30 offset:15360\n" \
"v_mul_f32 v48, v178, v48\n" \
"v_mfma_f32_16x16x32_bf16 v[212:215], a[204:207], v[100:103], v[212:215]\n" \
"v_mul_f32 v49, v178, v49\n" \
"v_mul_f32 v48, v188, v48\n" \
"v_mfma_f32_16x16x32_bf16 v[216:219], a[192:195], v[104:107], v[216:219]\n" \
"ds_read_b128 v[96:99], v28 offset:16384\n" \
"v_mul_f32 v49, v189, v49\n" \
"v_mfma_f32_16x16x32_bf16 v[216:219], a[200:203], v[108:111], v[216:219]\n" \
"v_lshlrev_b32 v50, 16, v153\n" \
"v_and_b32 v51, 0xffff0000, v153\n" \
"v_mfma_f32_16x16x32_bf16 v[220:223], a[196:199], v[104:107], v[220:223]\n" \
"s_waitcnt lgkmcnt(3)\n" \
"ds_read_b128 v[100:103], v28 offset:18432\n" \
"v_lshlrev_b32 v188, 16, v137\n" \
"v_mfma_f32_16x16x32_bf16 v[220:223], a[204:207], v[108:111], v[220:223]\n" \
"v_and_b32 v189, 0xffff0000, v137\n" \
"v_mul_f32 v50, v178, v50\n" \
"v_mfma_f32_16x16x32_bf16 v[208:211], a[208:211], v[112:115], v[208:211]\n" \
"ds_read_b128 v[104:107], v28 offset:17408\n" \
"v_mul_f32 v51, v178, v51\n" \
"v_mfma_f32_16x16x32_bf16 v[208:211], a[216:219], v[116:119], v[208:211]\n" \
"v_mul_f32 v50, v188, v50\n" \
"v_mul_f32 v51, v189, v51\n" \
"v_mfma_f32_16x16x32_bf16 v[212:215], a[212:215], v[112:115], v[212:215]\n" \
"s_waitcnt lgkmcnt(3)\n" \
"ds_read_b128 v[108:111], v28 offset:19456\n" \
"v_lshlrev_b32 v52, 16, v154\n" \
"v_mfma_f32_16x16x32_bf16 v[212:215], a[220:223], v[116:119], v[212:215]\n" \
"buffer_load_dwordx4 v31, s[36:39], s80 offen offset:3072 sc1 lds\n" \
"v_mfma_f32_16x16x32_bf16 v[216:219], a[208:211], v[120:123], v[216:219]\n" \
"s_add_u32 s80, s70, 32768\n" \
"ds_read_b128 v[112:115], v28 offset:20480\n" \
"s_add_u32 m0, s22, 65536\n" \
"v_mfma_f32_16x16x32_bf16 v[216:219], a[216:219], v[124:127], v[216:219]\n" \
"v_and_b32 v53, 0xffff0000, v154\n" \
"v_lshlrev_b32 v188, 16, v140\n" \
"v_mfma_f32_16x16x32_bf16 v[220:223], a[212:215], v[120:123], v[220:223]\n" \
"s_waitcnt lgkmcnt(3)\n" \
"ds_read_b128 v[116:119], v28 offset:22528\n" \
"v_and_b32 v189, 0xffff0000, v140\n" \
"v_mfma_f32_16x16x32_bf16 v[220:223], a[220:223], v[124:127], v[220:223]\n" \
"buffer_load_dwordx4 v31, s[36:39], s80 offen offset:0 sc1 lds\n" \
"v_mfma_f32_16x16x32_bf16 v[208:211], a[224:227], v[96:99], v[208:211]\n" \
"ds_read_b128 v[120:123], v28 offset:21504\n" \
"v_mul_f32 v52, v178, v52\n" \
"v_mfma_f32_16x16x32_bf16 v[208:211], a[232:235], v[100:103], v[208:211]\n" \
"v_mul_f32 v53, v178, v53\n" \
"v_mul_f32 v52, v188, v52\n" \
"v_mfma_f32_16x16x32_bf16 v[212:215], a[228:231], v[96:99], v[212:215]\n" \
"s_waitcnt lgkmcnt(3)\n" \
"ds_read_b128 v[124:127], v28 offset:23552\n" \
"v_mul_f32 v53, v189, v53\n" \
"v_mfma_f32_16x16x32_bf16 v[212:215], a[236:239], v[100:103], v[212:215]\n" \
"buffer_load_dwordx4 v31, s[36:39], s80 offen offset:1024 sc1 lds\n" \
"v_mfma_f32_16x16x32_bf16 v[216:219], a[224:227], v[104:107], v[216:219]\n" \
"ds_read_b128 v[96:99], v28 offset:24576\n" \
"v_lshlrev_b32 v54, 16, v155\n" \
"v_mfma_f32_16x16x32_bf16 v[216:219], a[232:235], v[108:111], v[216:219]\n" \
"s_waitcnt vmcnt(12)\n" \
"v_and_b32 v55, 0xffff0000, v155\n" \
"v_lshlrev_b32 v188, 16, v141\n" \
"v_mfma_f32_16x16x32_bf16 v[220:223], a[228:231], v[104:107], v[220:223]\n" \
"s_waitcnt lgkmcnt(3)\n" \
"ds_read_b128 v[100:103], v28 offset:26624\n" \
"v_and_b32 v189, 0xffff0000, v141\n" \
"v_mfma_f32_16x16x32_bf16 v[220:223], a[236:239], v[108:111], v[220:223]\n" \
"buffer_store_dwordx2 v[10:11], v20, s[36:39], s45 offen sc1\n" \
"v_mfma_f32_16x16x32_bf16 v[208:211], a[240:243], v[112:115], v[208:211]\n" \
"ds_read_b128 v[104:107], v28 offset:25600\n" \
"v_mul_f32 v54, v178, v54\n" \
"v_mfma_f32_16x16x32_bf16 v[208:211], a[248:251], v[116:119], v[208:211]\n" \
"buffer_load_dwordx4 v31, s[36:39], s80 offen offset:2048 sc1 lds\n" \
"v_mfma_f32_16x16x32_bf16 v[212:215], a[244:247], v[112:115], v[212:215]\n" \
"s_waitcnt lgkmcnt(3)\n" \
"ds_read_b128 v[108:111], v28 offset:27648\n" \
"v_mul_f32 v55, v178, v55\n" \
"v_mfma_f32_16x16x32_bf16 v[212:215], a[252:255], v[116:119], v[212:215]\n" \
"v_mul_f32 v54, v188, v54\n" \
"v_mul_f32 v55, v189, v55\n" \
"v_mfma_f32_16x16x32_bf16 v[216:219], a[240:243], v[120:123], v[216:219]\n" \
"ds_read_b128 v[112:115], v28 offset:28672\n" \
"v_permlane16_swap_b32 v156, v158\n" \
"v_mfma_f32_16x16x32_bf16 v[216:219], a[248:251], v[124:127], v[216:219]\n" \
"buffer_load_dwordx4 v31, s[36:39], s80 offen offset:3072 sc1 lds\n" \
"v_mfma_f32_16x16x32_bf16 v[220:223], a[244:247], v[120:123], v[220:223]\n" \
"s_add_u32 s80, s70, 49152\n" \
"ds_read_b128 v[116:119], v28 offset:30720\n" \
"s_add_u32 m0, s22, 81920\n" \
"v_mfma_f32_16x16x32_bf16 v[220:223], a[252:255], v[124:127], v[220:223]\n" \
"s_waitcnt vmcnt(25) lgkmcnt(4)\n" \
"s_barrier\n" \
"v_permlane16_swap_b32 v157, v159\n" \
"v_mfma_f32_16x16x32_bf16 v[208:211], a[64:67], v[96:99], v[208:211]\n" \
"ds_read_b128 v[120:123], v28 offset:29696\n" \
"v_sub_f32 v8, 1.0, v179\n" \
"v_mfma_f32_16x16x32_bf16 v[208:211], a[72:75], v[100:103], v[208:211]\n" \
"buffer_load_dwordx4 v31, s[36:39], s80 offen offset:0 sc1 lds\n" \
"v_mfma_f32_16x16x32_bf16 v[212:215], a[68:71], v[96:99], v[212:215]\n" \
"s_waitcnt lgkmcnt(3)\n" \
"ds_read_b128 v[124:127], v28 offset:31744\n" \
"v_cvt_pk_bf16_f32 v8, v8, v8\n" \
"v_mfma_f32_16x16x32_bf16 v[212:215], a[76:79], v[100:103], v[212:215]\n" \
"v_and_b32 v8, 0xffff0000, v8\n" \
"v_lshlrev_b32 v56, 16, v156\n" \
"v_mfma_f32_16x16x32_bf16 v[216:219], a[64:67], v[104:107], v[216:219]\n" \
"ds_read_b128 v[96:99], v28 offset:32768\n" \
"v_and_b32 v57, 0xffff0000, v156\n" \
"v_mfma_f32_16x16x32_bf16 v[216:219], a[72:75], v[108:111], v[216:219]\n" \
"buffer_load_dwordx4 v31, s[36:39], s80 offen offset:1024 sc1 lds\n" \
"v_mfma_f32_16x16x32_bf16 v[220:223], a[68:71], v[104:107], v[220:223]\n" \
"s_waitcnt lgkmcnt(3)\n" \
"ds_read_b128 v[100:103], v28 offset:34816\n" \
"v_lshlrev_b32 v188, 16, v138\n" \
"v_mfma_f32_16x16x32_bf16 v[220:223], a[76:79], v[108:111], v[220:223]\n" \
"v_and_b32 v189, 0xffff0000, v138\n" \
"v_mul_f32 v56, v179, v56\n" \
"v_mfma_f32_16x16x32_bf16 v[208:211], a[80:83], v[112:115], v[208:211]\n" \
"ds_read_b128 v[104:107], v28 offset:33792\n" \
"v_mul_f32 v57, v179, v57\n" \
"v_mfma_f32_16x16x32_bf16 v[208:211], a[88:91], v[116:119], v[208:211]\n" \
"buffer_load_dwordx4 v31, s[36:39], s80 offen offset:2048 sc1 lds\n" \
"v_mfma_f32_16x16x32_bf16 v[212:215], a[84:87], v[112:115], v[212:215]\n" \
"s_waitcnt lgkmcnt(3)\n" \
"ds_read_b128 v[108:111], v28 offset:35840\n" \
"v_mul_f32 v56, v188, v56\n" \
"v_mfma_f32_16x16x32_bf16 v[212:215], a[92:95], v[116:119], v[212:215]\n" \
"v_mul_f32 v57, v189, v57\n" \
"v_lshlrev_b32 v58, 16, v157\n" \
"v_mfma_f32_16x16x32_bf16 v[216:219], a[80:83], v[120:123], v[216:219]\n" \
"ds_read_b128 v[112:115], v28 offset:36864\n" \
"v_and_b32 v59, 0xffff0000, v157\n" \
"v_mfma_f32_16x16x32_bf16 v[216:219], a[88:91], v[124:127], v[216:219]\n" \
"buffer_load_dwordx4 v31, s[36:39], s80 offen offset:3072 sc1 lds\n" \
"v_mfma_f32_16x16x32_bf16 v[220:223], a[84:87], v[120:123], v[220:223]\n" \
"s_waitcnt lgkmcnt(3)\n" \
"ds_read_b128 v[116:119], v28 offset:38912\n" \
"s_add_u32 s80, s71, 0\n" \
"v_mfma_f32_16x16x32_bf16 v[220:223], a[92:95], v[124:127], v[220:223]\n" \
"s_add_u32 m0, s22, 98304\n" \
"v_lshlrev_b32 v188, 16, v139\n" \
"v_and_b32 v189, 0xffff0000, v139\n" \
"v_mfma_f32_16x16x32_bf16 v[208:211], a[128:131], v[96:99], v[208:211]\n" \
"ds_read_b128 v[120:123], v28 offset:37888\n" \
"v_mul_f32 v58, v179, v58\n" \
"v_mfma_f32_16x16x32_bf16 v[208:211], a[136:139], v[100:103], v[208:211]\n" \
"buffer_load_dwordx4 v31, s[36:39], s80 offen offset:0 sc1 lds\n" \
"v_mfma_f32_16x16x32_bf16 v[212:215], a[132:135], v[96:99], v[212:215]\n" \
"s_waitcnt lgkmcnt(3)\n" \
"ds_read_b128 v[124:127], v28 offset:39936\n" \
"v_mul_f32 v59, v179, v59\n" \
"v_mfma_f32_16x16x32_bf16 v[212:215], a[140:143], v[100:103], v[212:215]\n" \
"v_mul_f32 v58, v188, v58\n" \
"v_mul_f32 v59, v189, v59\n" \
"v_mfma_f32_16x16x32_bf16 v[216:219], a[128:131], v[104:107], v[216:219]\n" \
"ds_read_b128 v[96:99], v28 offset:40960\n" \
"v_lshlrev_b32 v60, 16, v158\n" \
"v_mfma_f32_16x16x32_bf16 v[216:219], a[136:139], v[108:111], v[216:219]\n" \
"buffer_load_dwordx4 v31, s[36:39], s80 offen offset:1024 sc1 lds\n" \
"v_mfma_f32_16x16x32_bf16 v[220:223], a[132:135], v[104:107], v[220:223]\n" \
"s_waitcnt lgkmcnt(3)\n" \
"ds_read_b128 v[100:103], v28 offset:43008\n" \
"v_and_b32 v61, 0xffff0000, v158\n" \
"v_mfma_f32_16x16x32_bf16 v[220:223], a[140:143], v[108:111], v[220:223]\n" \
"v_lshlrev_b32 v188, 16, v142\n" \
"v_and_b32 v189, 0xffff0000, v142\n" \
"v_mfma_f32_16x16x32_bf16 v[208:211], a[144:147], v[112:115], v[208:211]\n" \
"ds_read_b128 v[104:107], v28 offset:41984\n" \
"v_mul_f32 v60, v179, v60\n" \
"v_mfma_f32_16x16x32_bf16 v[208:211], a[152:155], v[116:119], v[208:211]\n" \
"buffer_load_dwordx4 v31, s[36:39], s80 offen offset:2048 sc1 lds\n" \
"v_mfma_f32_16x16x32_bf16 v[212:215], a[148:151], v[112:115], v[212:215]\n" \
"s_waitcnt lgkmcnt(3)\n" \
"ds_read_b128 v[108:111], v28 offset:44032\n" \
"v_mul_f32 v61, v179, v61\n" \
"v_mfma_f32_16x16x32_bf16 v[212:215], a[156:159], v[116:119], v[212:215]\n" \
"v_mul_f32 v60, v188, v60\n" \
"v_mul_f32 v61, v189, v61\n" \
"v_mfma_f32_16x16x32_bf16 v[216:219], a[144:147], v[120:123], v[216:219]\n" \
"ds_read_b128 v[112:115], v28 offset:45056\n" \
"v_lshlrev_b32 v62, 16, v159\n" \
"v_mfma_f32_16x16x32_bf16 v[216:219], a[152:155], v[124:127], v[216:219]\n" \
"buffer_load_dwordx4 v31, s[36:39], s80 offen offset:3072 sc1 lds\n" \
"v_mfma_f32_16x16x32_bf16 v[220:223], a[148:151], v[120:123], v[220:223]\n" \
"s_add_u32 s80, s71, 16384\n" \
"ds_read_b128 v[116:119], v28 offset:47104\n" \
"s_add_u32 m0, s22, 147456\n" \
"v_mfma_f32_16x16x32_bf16 v[220:223], a[156:159], v[124:127], v[220:223]\n" \
"s_waitcnt vmcnt(27) lgkmcnt(4)\n" \
"s_barrier\n" \
"v_and_b32 v63, 0xffff0000, v159\n" \
"v_mfma_f32_16x16x32_bf16 v[208:211], a[0:3], v[96:99], v[208:211]\n" \
"ds_read_b128 v[120:123], v28 offset:46080\n" \
"v_lshlrev_b32 v188, 16, v143\n" \
"v_mfma_f32_16x16x32_bf16 v[208:211], a[8:11], v[100:103], v[208:211]\n" \
"v_and_b32 v189, 0xffff0000, v143\n" \
"v_mul_f32 v62, v179, v62\n" \
"v_mfma_f32_16x16x32_bf16 v[212:215], a[4:7], v[96:99], v[212:215]\n" \
"s_waitcnt lgkmcnt(3)\n" \
"ds_read_b128 v[124:127], v28 offset:48128\n" \
"v_mul_f32 v63, v179, v63\n" \
"v_mfma_f32_16x16x32_bf16 v[212:215], a[12:15], v[100:103], v[212:215]\n" \
"buffer_load_dwordx4 v31, s[36:39], s80 offen offset:0 sc1 lds\n" \
"v_mfma_f32_16x16x32_bf16 v[216:219], a[0:3], v[104:107], v[216:219]\n" \
"ds_read_b128 v[96:99], v29 offset:49152\n" \
"v_mul_f32 v62, v188, v62\n" \
"v_mfma_f32_16x16x32_bf16 v[216:219], a[8:11], v[108:111], v[216:219]\n" \
"v_mul_f32 v63, v189, v63\n" \
"v_mfma_f32_16x16x32_bf16 v[220:223], a[4:7], v[104:107], v[220:223]\n" \
"s_waitcnt lgkmcnt(3)\n" \
"ds_read_b128 v[100:103], v29 offset:51200\n" \
"v_mfma_f32_16x16x32_bf16 v[220:223], a[12:15], v[108:111], v[220:223]\n" \
"buffer_load_dwordx4 v31, s[36:39], s80 offen offset:1024 sc1 lds\n" \
"v_mfma_f32_16x16x32_bf16 v[208:211], a[16:19], v[112:115], v[208:211]\n" \
"ds_read_b128 v[104:107], v29 offset:50176\n" \
"v_mfma_f32_16x16x32_bf16 v[208:211], a[24:27], v[116:119], v[208:211]\n" \
"v_mfma_f32_16x16x32_bf16 v[212:215], a[20:23], v[112:115], v[212:215]\n" \
"s_waitcnt lgkmcnt(3)\n" \
"ds_read_b128 v[108:111], v29 offset:52224\n" \
"v_mfma_f32_16x16x32_bf16 v[212:215], a[28:31], v[116:119], v[212:215]\n" \
"buffer_load_dwordx4 v31, s[36:39], s80 offen offset:2048 sc1 lds\n" \
"v_mfma_f32_16x16x32_bf16 v[216:219], a[16:19], v[120:123], v[216:219]\n" \
"ds_read_b128 v[112:115], v29 offset:53248\n" \
"v_mfma_f32_16x16x32_bf16 v[216:219], a[24:27], v[124:127], v[216:219]\n" \
"v_mfma_f32_16x16x32_bf16 v[220:223], a[20:23], v[120:123], v[220:223]\n" \
"s_waitcnt lgkmcnt(3)\n" \
"ds_read_b128 v[116:119], v29 offset:55296\n" \
"v_mfma_f32_16x16x32_bf16 v[220:223], a[28:31], v[124:127], v[220:223]\n" \
"buffer_load_dwordx4 v31, s[36:39], s80 offen offset:3072 sc1 lds\n" \
"v_mfma_f32_16x16x32_bf16 v[208:211], a[32:35], v[96:99], v[208:211]\n" \
"ds_read_b128 v[120:123], v29 offset:54272\n" \
"v_mfma_f32_16x16x32_bf16 v[208:211], a[40:43], v[100:103], v[208:211]\n" \
"v_mfma_f32_16x16x32_bf16 v[212:215], a[36:39], v[96:99], v[212:215]\n" \
"s_waitcnt lgkmcnt(3)\n" \
"ds_read_b128 v[124:127], v29 offset:56320\n" \
"v_mfma_f32_16x16x32_bf16 v[212:215], a[44:47], v[100:103], v[212:215]\n" \
"v_mfma_f32_16x16x32_bf16 v[216:219], a[32:35], v[104:107], v[216:219]\n" \
"ds_read_b128 v[96:99], v29 offset:57344\n" \
"v_mfma_f32_16x16x32_bf16 v[216:219], a[40:43], v[108:111], v[216:219]\n" \
"v_mfma_f32_16x16x32_bf16 v[220:223], a[36:39], v[104:107], v[220:223]\n" \
"s_waitcnt lgkmcnt(3)\n" \
"ds_read_b128 v[100:103], v29 offset:59392\n" \
"v_mfma_f32_16x16x32_bf16 v[220:223], a[44:47], v[108:111], v[220:223]\n" \
"v_mfma_f32_16x16x32_bf16 v[208:211], a[48:51], v[112:115], v[208:211]\n" \
"ds_read_b128 v[104:107], v29 offset:58368\n" \
"v_mfma_f32_16x16x32_bf16 v[208:211], a[56:59], v[116:119], v[208:211]\n" \
"v_mfma_f32_16x16x32_bf16 v[212:215], a[52:55], v[112:115], v[212:215]\n" \
"s_waitcnt lgkmcnt(3)\n" \
"ds_read_b128 v[108:111], v29 offset:60416\n" \
"v_mfma_f32_16x16x32_bf16 v[212:215], a[60:63], v[116:119], v[212:215]\n" \
"s_add_u32 s80, s71, 32768\n" \
"s_add_u32 m0, s22, 131072\n" \
"v_mfma_f32_16x16x32_bf16 v[216:219], a[48:51], v[120:123], v[216:219]\n" \
"s_sub_u32 s96, s88, 94404608\n" \
"ds_read_b128 v[112:115], v29 offset:61440\n" \
"s_lshl_b32 s96, s96, 2\n" \
"v_mfma_f32_16x16x32_bf16 v[216:219], a[56:59], v[124:127], v[216:219]\n" \
"s_add_u32 s96, s96, 94437376\n" \
"v_bfe_u32 v17, v4, 4, 2\n" \
"v_min_u32 v17, 2, v17\n" \
"v_mfma_f32_16x16x32_bf16 v[220:223], a[52:55], v[120:123], v[220:223]\n" \
"ds_read_b128 v[116:119], v29 offset:63488\n" \
"v_lshl_add_u32 v17, v17, 3, s96\n" \
"v_mfma_f32_16x16x32_bf16 v[220:223], a[60:63], v[124:127], v[220:223]\n" \
"s_waitcnt vmcnt(24) lgkmcnt(4)\n" \
"s_barrier\n" \
"v_mfma_f32_16x16x32_bf16 v[208:211], a[96:99], v[96:99], v[208:211]\n" \
"ds_read_b128 v[120:123], v29 offset:62464\n" \
"v_mfma_f32_16x16x32_bf16 v[208:211], a[104:107], v[100:103], v[208:211]\n" \
"v_mfma_f32_16x16x32_bf16 v[212:215], a[100:103], v[96:99], v[212:215]\n" \
"s_waitcnt lgkmcnt(3)\n" \
"ds_read_b128 v[124:127], v29 offset:64512\n" \
"v_mfma_f32_16x16x32_bf16 v[212:215], a[108:111], v[100:103], v[212:215]\n" \
"buffer_load_dwordx4 v31, s[36:39], s80 offen offset:0 sc1 lds\n" \
"v_mfma_f32_16x16x32_bf16 v[216:219], a[96:99], v[104:107], v[216:219]\n" \
"ds_read_b128 v[96:99], v28 offset:0\n" \
"v_mfma_f32_16x16x32_bf16 v[216:219], a[104:107], v[108:111], v[216:219]\n" \
"v_mfma_f32_16x16x32_bf16 v[220:223], a[100:103], v[104:107], v[220:223]\n" \
"s_waitcnt lgkmcnt(3)\n" \
"ds_read_b128 v[100:103], v28 offset:2048\n" \
"v_mfma_f32_16x16x32_bf16 v[220:223], a[108:111], v[108:111], v[220:223]\n" \
"buffer_load_dwordx4 v31, s[36:39], s80 offen offset:1024 sc1 lds\n" \
"v_mfma_f32_16x16x32_bf16 v[208:211], a[112:115], v[112:115], v[208:211]\n" \
"ds_read_b128 v[104:107], v28 offset:1024\n" \
"v_mfma_f32_16x16x32_bf16 v[208:211], a[120:123], v[116:119], v[208:211]\n" \
"v_mfma_f32_16x16x32_bf16 v[212:215], a[116:119], v[112:115], v[212:215]\n" \
"s_waitcnt lgkmcnt(3)\n" \
"ds_read_b128 v[108:111], v28 offset:3072\n" \
"v_mfma_f32_16x16x32_bf16 v[212:215], a[124:127], v[116:119], v[212:215]\n" \
"buffer_load_dwordx2 v[18:19], v17, s[36:39], 0 offen sc1\n" \
"v_mfma_f32_16x16x32_bf16 v[216:219], a[112:115], v[120:123], v[216:219]\n" \
"ds_read_b128 v[112:115], v28 offset:4096\n" \
"v_mfma_f32_16x16x32_bf16 v[216:219], a[120:123], v[124:127], v[216:219]\n" \
"v_mfma_f32_16x16x32_bf16 v[220:223], a[116:119], v[120:123], v[220:223]\n" \
"s_waitcnt lgkmcnt(3)\n" \
"ds_read_b128 v[116:119], v28 offset:6144\n" \
"v_mfma_f32_16x16x32_bf16 v[220:223], a[124:127], v[124:127], v[220:223]\n" \
"buffer_load_dwordx4 v31, s[36:39], s80 offen offset:2048 sc1 lds\n" \
"v_mfma_f32_16x16x32_bf16 v[192:195], a[160:163], v[96:99], 0\n" \
"ds_read_b128 v[120:123], v28 offset:5120\n" \
"v_mfma_f32_16x16x32_bf16 v[192:195], a[168:171], v[100:103], v[192:195]\n" \
"v_mfma_f32_16x16x32_bf16 v[196:199], a[164:167], v[96:99], 0\n" \
"s_waitcnt lgkmcnt(3)\n" \
"ds_read_b128 v[124:127], v28 offset:7168\n" \
"v_cvt_pk_bf16_f32 v208, v208, v209\n" \
"v_mfma_f32_16x16x32_bf16 v[196:199], a[172:175], v[100:103], v[196:199]\n" \
"buffer_load_dwordx4 v31, s[36:39], s80 offen offset:3072 sc1 lds\n" \
"v_mfma_f32_16x16x32_bf16 v[200:203], a[160:163], v[104:107], 0\n" \
"ds_read_b128 v[96:99], v28 offset:8192\n" \
"v_cvt_pk_bf16_f32 v209, v210, v211\n" \
"v_mfma_f32_16x16x32_bf16 v[200:203], a[168:171], v[108:111], v[200:203]\n" \
"v_cvt_pk_bf16_f32 v210, v212, v213\n" \
"v_cvt_pk_bf16_f32 v211, v214, v215\n" \
"v_mfma_f32_16x16x32_bf16 v[204:207], a[164:167], v[104:107], 0\n" \
"s_waitcnt lgkmcnt(3)\n" \
"ds_read_b128 v[100:103], v28 offset:10240\n" \
"v_cvt_pk_bf16_f32 v216, v216, v217\n" \
"v_mfma_f32_16x16x32_bf16 v[204:207], a[172:175], v[108:111], v[204:207]\n" \
"buffer_store_dwordx4 v[208:211], v4, s[36:39], s76 offen offset:0 sc1\n" \
"v_mfma_f32_16x16x32_bf16 v[192:195], a[176:179], v[112:115], v[192:195]\n" \
"ds_read_b128 v[104:107], v28 offset:9216\n" \
"v_cvt_pk_bf16_f32 v217, v218, v219\n" \
"v_mfma_f32_16x16x32_bf16 v[192:195], a[184:187], v[116:119], v[192:195]\n" \
"v_cvt_pk_bf16_f32 v218, v220, v221\n" \
"v_cvt_pk_bf16_f32 v219, v222, v223\n" \
"v_mfma_f32_16x16x32_bf16 v[196:199], a[180:183], v[112:115], v[196:199]\n" \
"s_waitcnt lgkmcnt(3)\n" \
"ds_read_b128 v[108:111], v28 offset:11264\n" \
"v_mfma_f32_16x16x32_bf16 v[196:199], a[188:191], v[116:119], v[196:199]\n" \
"s_add_u32 s80, s71, 49152\n" \
"s_add_u32 m0, s22, 16384\n" \
"v_mfma_f32_16x16x32_bf16 v[200:203], a[176:179], v[120:123], v[200:203]\n" \
"ds_read_b128 v[112:115], v28 offset:12288\n" \
"v_mfma_f32_16x16x32_bf16 v[200:203], a[184:187], v[124:127], v[200:203]\n" \
"v_mfma_f32_16x16x32_bf16 v[204:207], a[180:183], v[120:123], v[204:207]\n" \
"ds_read_b128 v[116:119], v28 offset:14336\n" \
"v_mfma_f32_16x16x32_bf16 v[204:207], a[188:191], v[124:127], v[204:207]\n" \
"s_waitcnt vmcnt(23) lgkmcnt(4)\n" \
"s_barrier\n" \
"v_mfma_f32_16x16x32_bf16 v[192:195], a[192:195], v[96:99], v[192:195]\n" \
"ds_read_b128 v[120:123], v28 offset:13312\n" \
"v_mfma_f32_16x16x32_bf16 v[192:195], a[200:203], v[100:103], v[192:195]\n" \
"buffer_store_dwordx4 v[216:219], v4, s[36:39], s76 offen offset:1024 sc1\n" \
"v_mfma_f32_16x16x32_bf16 v[196:199], a[196:199], v[96:99], v[196:199]\n" \
"s_waitcnt lgkmcnt(3)\n" \
"ds_read_b128 v[124:127], v28 offset:15360\n" \
"v_mfma_f32_16x16x32_bf16 v[196:199], a[204:207], v[100:103], v[196:199]\n" \
"v_mfma_f32_16x16x32_bf16 v[200:203], a[192:195], v[104:107], v[200:203]\n" \
"ds_read_b128 v[96:99], v28 offset:49152\n" \
"v_mfma_f32_16x16x32_bf16 v[200:203], a[200:203], v[108:111], v[200:203]\n" \
"v_mfma_f32_16x16x32_bf16 v[204:207], a[196:199], v[104:107], v[204:207]\n" \
"s_waitcnt lgkmcnt(3)\n" \
"ds_read_b128 v[100:103], v28 offset:51200\n" \
"v_mfma_f32_16x16x32_bf16 v[204:207], a[204:207], v[108:111], v[204:207]\n" \
"v_mfma_f32_16x16x32_bf16 v[192:195], a[208:211], v[112:115], v[192:195]\n" \
"ds_read_b128 v[104:107], v28 offset:50176\n" \
"v_mfma_f32_16x16x32_bf16 v[192:195], a[216:219], v[116:119], v[192:195]\n" \
"v_mfma_f32_16x16x32_bf16 v[196:199], a[212:215], v[112:115], v[196:199]\n" \
"s_waitcnt lgkmcnt(3)\n" \
"ds_read_b128 v[108:111], v28 offset:52224\n" \
"v_mfma_f32_16x16x32_bf16 v[196:199], a[220:223], v[116:119], v[196:199]\n" \
"buffer_load_dwordx4 v31, s[36:39], s80 offen offset:0 sc1 lds\n" \
"v_mfma_f32_16x16x32_bf16 v[200:203], a[208:211], v[120:123], v[200:203]\n" \
"ds_read_b128 v[112:115], v28 offset:53248\n" \
"v_mfma_f32_16x16x32_bf16 v[200:203], a[216:219], v[124:127], v[200:203]\n" \
"v_mfma_f32_16x16x32_bf16 v[204:207], a[212:215], v[120:123], v[204:207]\n" \
"s_waitcnt lgkmcnt(3)\n" \
"ds_read_b128 v[116:119], v28 offset:55296\n" \
"v_mfma_f32_16x16x32_bf16 v[204:207], a[220:223], v[124:127], v[204:207]\n" \
"buffer_load_dwordx4 v31, s[36:39], s80 offen offset:1024 sc1 lds\n" \
"v_mfma_f32_16x16x32_bf16 v[192:195], a[224:227], v[96:99], v[192:195]\n" \
"ds_read_b128 v[120:123], v28 offset:54272\n" \
"v_mfma_f32_16x16x32_bf16 v[192:195], a[232:235], v[100:103], v[192:195]\n" \
"v_mfma_f32_16x16x32_bf16 v[196:199], a[228:231], v[96:99], v[196:199]\n" \
"s_waitcnt lgkmcnt(3)\n" \
"ds_read_b128 v[124:127], v28 offset:56320\n" \
"v_mfma_f32_16x16x32_bf16 v[196:199], a[236:239], v[100:103], v[196:199]\n" \
"buffer_load_dwordx4 v31, s[36:39], s80 offen offset:2048 sc1 lds\n" \
"v_mfma_f32_16x16x32_bf16 v[200:203], a[224:227], v[104:107], v[200:203]\n" \
"ds_read_b128 v[96:99], v28 offset:57344\n" \
"v_mfma_f32_16x16x32_bf16 v[200:203], a[232:235], v[108:111], v[200:203]\n" \
"v_mfma_f32_16x16x32_bf16 v[204:207], a[228:231], v[104:107], v[204:207]\n" \
"s_waitcnt lgkmcnt(3)\n" \
"ds_read_b128 v[100:103], v28 offset:59392\n" \
"s_waitcnt vmcnt(7)\n" \
"v_mfma_f32_16x16x32_bf16 v[204:207], a[236:239], v[108:111], v[204:207]\n" \
"buffer_load_dwordx4 v31, s[36:39], s80 offen offset:3072 sc1 lds\n" \
"v_mfma_f32_16x16x32_bf16 v[192:195], a[240:243], v[112:115], v[192:195]\n" \
"ds_read_b128 v[104:107], v28 offset:58368\n" \
"v_cmp_ne_u64 vcc, s[14:15], v[18:19]\n" \
"v_mfma_f32_16x16x32_bf16 v[192:195], a[248:251], v[116:119], v[192:195]\n" \
"s_nop 0\n" \
"s_cbranch_vccnz LPE_%=\nLPER_%=:\n" \
"v_mfma_f32_16x16x32_bf16 v[196:199], a[244:247], v[112:115], v[196:199]\n" \
"s_waitcnt lgkmcnt(3)\n" \
"ds_read_b128 v[108:111], v28 offset:60416\n" \
"s_add_u32 s97, s78, 0\n" \
"v_mfma_f32_16x16x32_bf16 v[196:199], a[252:255], v[116:119], v[196:199]\n" \
"buffer_load_dwordx4 v[128:131], v4, s[36:39], s97 offen offset:0 sc1\n" \
"v_mfma_f32_16x16x32_bf16 v[200:203], a[240:243], v[120:123], v[200:203]\n" \
"ds_read_b128 v[112:115], v28 offset:61440\n" \
"v_mfma_f32_16x16x32_bf16 v[200:203], a[248:251], v[124:127], v[200:203]\n" \
"v_mfma_f32_16x16x32_bf16 v[204:207], a[244:247], v[120:123], v[204:207]\n" \
"ds_read_b128 v[116:119], v28 offset:63488\n" \
"v_mfma_f32_16x16x32_bf16 v[204:207], a[252:255], v[124:127], v[204:207]\n" \
"s_waitcnt vmcnt(24) lgkmcnt(4)\n" \
"s_barrier\n" \
"v_mfma_f32_16x16x32_bf16 v[192:195], a[64:67], v[96:99], v[192:195]\n" \
"ds_read_b128 v[120:123], v28 offset:62464\n" \
"v_mfma_f32_16x16x32_bf16 v[192:195], a[72:75], v[100:103], v[192:195]\n" \
"buffer_load_dwordx4 v[132:135], v4, s[36:39], s97 offen offset:1024 sc1\n" \
"v_mfma_f32_16x16x32_bf16 v[196:199], a[68:71], v[96:99], v[196:199]\n" \
"s_waitcnt lgkmcnt(3)\n" \
"ds_read_b128 v[124:127], v28 offset:64512\n" \
"v_mfma_f32_16x16x32_bf16 v[196:199], a[76:79], v[100:103], v[196:199]\n" \
"LH29_%=:\ns_cbranch_execz LHR2_%=\n" \
"v_mfma_f32_16x16x32_bf16 v[200:203], a[64:67], v[104:107], v[200:203]\n" \
"ds_read_b128 v[96:99], v29 offset:0\n" \
"v_mfma_f32_16x16x32_bf16 v[200:203], a[72:75], v[108:111], v[200:203]\n" \
"buffer_load_dwordx4 v[136:139], v4, s[36:39], s97 offen offset:2048 sc1\n" \
"v_mfma_f32_16x16x32_bf16 v[204:207], a[68:71], v[104:107], v[204:207]\n" \
"s_waitcnt lgkmcnt(3)\n" \
"ds_read_b128 v[100:103], v29 offset:2048\n" \
"v_mfma_f32_16x16x32_bf16 v[204:207], a[76:79], v[108:111], v[204:207]\n" \
"v_mfma_f32_16x16x32_bf16 v[192:195], a[80:83], v[112:115], v[192:195]\n" \
"ds_read_b128 v[104:107], v29 offset:1024\n" \
"v_mfma_f32_16x16x32_bf16 v[192:195], a[88:91], v[116:119], v[192:195]\n" \
"buffer_load_dwordx4 v[140:143], v4, s[36:39], s97 offen offset:3072 sc1\n" \
"v_mfma_f32_16x16x32_bf16 v[196:199], a[84:87], v[112:115], v[196:199]\n" \
"s_waitcnt lgkmcnt(3)\n" \
"ds_read_b128 v[108:111], v29 offset:3072\n" \
"s_add_u32 s97, s78, 4096\n" \
"v_mfma_f32_16x16x32_bf16 v[196:199], a[92:95], v[116:119], v[196:199]\n" \
"v_mfma_f32_16x16x32_bf16 v[200:203], a[80:83], v[120:123], v[200:203]\n" \
"ds_read_b128 v[112:115], v29 offset:4096\n" \
"v_mfma_f32_16x16x32_bf16 v[200:203], a[88:91], v[124:127], v[200:203]\n" \
"buffer_load_dwordx4 v[160:163], v4, s[36:39], s97 offen offset:0 sc1\n" \
"v_mfma_f32_16x16x32_bf16 v[204:207], a[84:87], v[120:123], v[204:207]\n" \
"s_waitcnt lgkmcnt(3)\n" \
"ds_read_b128 v[116:119], v29 offset:6144\n" \
"v_mfma_f32_16x16x32_bf16 v[204:207], a[92:95], v[124:127], v[204:207]\n" \
"v_mfma_f32_16x16x32_bf16 v[192:195], a[128:131], v[96:99], v[192:195]\n" \
"ds_read_b128 v[120:123], v29 offset:5120\n" \
"v_mfma_f32_16x16x32_bf16 v[192:195], a[136:139], v[100:103], v[192:195]\n" \
"buffer_load_dwordx4 v[164:167], v4, s[36:39], s97 offen offset:1024 sc1\n" \
"v_mfma_f32_16x16x32_bf16 v[196:199], a[132:135], v[96:99], v[196:199]\n" \
"s_waitcnt lgkmcnt(3)\n" \
"ds_read_b128 v[124:127], v29 offset:7168\n" \
"v_mfma_f32_16x16x32_bf16 v[196:199], a[140:143], v[100:103], v[196:199]\n" \
"v_mfma_f32_16x16x32_bf16 v[200:203], a[128:131], v[104:107], v[200:203]\n" \
"ds_read_b128 v[96:99], v29 offset:8192\n" \
"v_mfma_f32_16x16x32_bf16 v[200:203], a[136:139], v[108:111], v[200:203]\n" \
"v_mfma_f32_16x16x32_bf16 v[204:207], a[132:135], v[104:107], v[204:207]\n" \
"s_waitcnt lgkmcnt(3)\n" \
"ds_read_b128 v[100:103], v29 offset:10240\n" \
"v_mfma_f32_16x16x32_bf16 v[204:207], a[140:143], v[108:111], v[204:207]\n" \
"v_mfma_f32_16x16x32_bf16 v[192:195], a[144:147], v[112:115], v[192:195]\n" \
"ds_read_b128 v[104:107], v29 offset:9216\n" \
"v_mfma_f32_16x16x32_bf16 v[192:195], a[152:155], v[116:119], v[192:195]\n" \
"v_mfma_f32_16x16x32_bf16 v[196:199], a[148:151], v[112:115], v[196:199]\n" \
"s_waitcnt lgkmcnt(3)\n" \
"ds_read_b128 v[108:111], v29 offset:11264\n" \
"v_mfma_f32_16x16x32_bf16 v[196:199], a[156:159], v[116:119], v[196:199]\n" \
"v_mfma_f32_16x16x32_bf16 v[200:203], a[144:147], v[120:123], v[200:203]\n" \
"ds_read_b128 v[112:115], v29 offset:12288\n" \
"v_mfma_f32_16x16x32_bf16 v[200:203], a[152:155], v[124:127], v[200:203]\n" \
"v_mfma_f32_16x16x32_bf16 v[204:207], a[148:151], v[120:123], v[204:207]\n" \
"ds_read_b128 v[116:119], v29 offset:14336\n" \
"v_mfma_f32_16x16x32_bf16 v[204:207], a[156:159], v[124:127], v[204:207]\n" \
"s_waitcnt vmcnt(25) lgkmcnt(4)\n" \
"s_barrier\n" \
"v_mfma_f32_16x16x32_bf16 v[192:195], a[0:3], v[96:99], v[192:195]\n" \
"ds_read_b128 v[120:123], v29 offset:13312\n" \
"v_mfma_f32_16x16x32_bf16 v[192:195], a[8:11], v[100:103], v[192:195]\n" \
"v_mfma_f32_16x16x32_bf16 v[196:199], a[4:7], v[96:99], v[196:199]\n" \
"s_waitcnt lgkmcnt(3)\n" \
"ds_read_b128 v[124:127], v29 offset:15360\n" \
"v_mfma_f32_16x16x32_bf16 v[196:199], a[12:15], v[100:103], v[196:199]\n" \
"v_mfma_f32_16x16x32_bf16 v[200:203], a[0:3], v[104:107], v[200:203]\n" \
"ds_read_b128 v[96:99], v29 offset:16384\n" \
"v_mfma_f32_16x16x32_bf16 v[200:203], a[8:11], v[108:111], v[200:203]\n" \
"v_mfma_f32_16x16x32_bf16 v[204:207], a[4:7], v[104:107], v[204:207]\n" \
"s_waitcnt lgkmcnt(3)\n" \
"ds_read_b128 v[100:103], v29 offset:18432\n" \
"v_mfma_f32_16x16x32_bf16 v[204:207], a[12:15], v[108:111], v[204:207]\n" \
"v_mfma_f32_16x16x32_bf16 v[192:195], a[16:19], v[112:115], v[192:195]\n" \
"ds_read_b128 v[104:107], v29 offset:17408\n" \
"v_mfma_f32_16x16x32_bf16 v[192:195], a[24:27], v[116:119], v[192:195]\n" \
"v_mfma_f32_16x16x32_bf16 v[196:199], a[20:23], v[112:115], v[196:199]\n" \
"s_waitcnt lgkmcnt(3)\n" \
"ds_read_b128 v[108:111], v29 offset:19456\n" \
"v_mfma_f32_16x16x32_bf16 v[196:199], a[28:31], v[116:119], v[196:199]\n" \
"v_mfma_f32_16x16x32_bf16 v[200:203], a[16:19], v[120:123], v[200:203]\n" \
"ds_read_b128 v[112:115], v29 offset:20480\n" \
"v_mfma_f32_16x16x32_bf16 v[200:203], a[24:27], v[124:127], v[200:203]\n" \
"v_mfma_f32_16x16x32_bf16 v[204:207], a[20:23], v[120:123], v[204:207]\n" \
"s_waitcnt lgkmcnt(3)\n" \
"ds_read_b128 v[116:119], v29 offset:22528\n" \
"v_mfma_f32_16x16x32_bf16 v[204:207], a[28:31], v[124:127], v[204:207]\n" \
"v_mfma_f32_16x16x32_bf16 v[192:195], a[32:35], v[96:99], v[192:195]\n" \
"ds_read_b128 v[120:123], v29 offset:21504\n" \
"v_mfma_f32_16x16x32_bf16 v[192:195], a[40:43], v[100:103], v[192:195]\n" \
"v_mfma_f32_16x16x32_bf16 v[196:199], a[36:39], v[96:99], v[196:199]\n" \
"s_waitcnt lgkmcnt(3)\n" \
"ds_read_b128 v[124:127], v29 offset:23552\n" \
"v_mfma_f32_16x16x32_bf16 v[196:199], a[44:47], v[100:103], v[196:199]\n" \
"v_mfma_f32_16x16x32_bf16 v[200:203], a[32:35], v[104:107], v[200:203]\n" \
"ds_read_b128 v[96:99], v29 offset:24576\n" \
"v_mfma_f32_16x16x32_bf16 v[200:203], a[40:43], v[108:111], v[200:203]\n" \
"v_mfma_f32_16x16x32_bf16 v[204:207], a[36:39], v[104:107], v[204:207]\n" \
"s_waitcnt lgkmcnt(3)\n" \
"ds_read_b128 v[100:103], v29 offset:26624\n" \
"v_mfma_f32_16x16x32_bf16 v[204:207], a[44:47], v[108:111], v[204:207]\n" \
"v_mfma_f32_16x16x32_bf16 v[192:195], a[48:51], v[112:115], v[192:195]\n" \
"ds_read_b128 v[104:107], v29 offset:25600\n" \
"v_mfma_f32_16x16x32_bf16 v[192:195], a[56:59], v[116:119], v[192:195]\n" \
"v_mfma_f32_16x16x32_bf16 v[196:199], a[52:55], v[112:115], v[196:199]\n" \
"s_waitcnt lgkmcnt(3)\n" \
"ds_read_b128 v[108:111], v29 offset:27648\n" \
"v_mfma_f32_16x16x32_bf16 v[196:199], a[60:63], v[116:119], v[196:199]\n" \
"v_mfma_f32_16x16x32_bf16 v[200:203], a[48:51], v[120:123], v[200:203]\n" \
"ds_read_b128 v[112:115], v29 offset:28672\n" \
"v_mfma_f32_16x16x32_bf16 v[200:203], a[56:59], v[124:127], v[200:203]\n" \
"v_mfma_f32_16x16x32_bf16 v[204:207], a[52:55], v[120:123], v[204:207]\n" \
"ds_read_b128 v[116:119], v29 offset:30720\n" \
"v_mfma_f32_16x16x32_bf16 v[204:207], a[60:63], v[124:127], v[204:207]\n" \
"s_waitcnt vmcnt(21) lgkmcnt(4)\n" \
"s_barrier\n" \
"v_mfma_f32_16x16x32_bf16 v[192:195], a[96:99], v[96:99], v[192:195]\n" \
"ds_read_b128 v[120:123], v29 offset:29696\n" \
"v_mfma_f32_16x16x32_bf16 v[192:195], a[104:107], v[100:103], v[192:195]\n" \
"v_mfma_f32_16x16x32_bf16 v[196:199], a[100:103], v[96:99], v[196:199]\n" \
"s_waitcnt lgkmcnt(3)\n" \
"ds_read_b128 v[124:127], v29 offset:31744\n" \
"v_mfma_f32_16x16x32_bf16 v[196:199], a[108:111], v[100:103], v[196:199]\n" \
"v_mfma_f32_16x16x32_bf16 v[200:203], a[96:99], v[104:107], v[200:203]\n" \
"ds_read_b128 v[96:99], v29 offset:32768\n" \
"v_mfma_f32_16x16x32_bf16 v[200:203], a[104:107], v[108:111], v[200:203]\n" \
"v_mfma_f32_16x16x32_bf16 v[204:207], a[100:103], v[104:107], v[204:207]\n" \
"s_waitcnt lgkmcnt(3)\n" \
"ds_read_b128 v[100:103], v29 offset:34816\n" \
"v_mfma_f32_16x16x32_bf16 v[204:207], a[108:111], v[108:111], v[204:207]\n" \
"v_mfma_f32_16x16x32_bf16 v[192:195], a[112:115], v[112:115], v[192:195]\n" \
"ds_read_b128 v[104:107], v29 offset:33792\n" \
"v_mfma_f32_16x16x32_bf16 v[192:195], a[120:123], v[116:119], v[192:195]\n" \
"v_mfma_f32_16x16x32_bf16 v[196:199], a[116:119], v[112:115], v[196:199]\n" \
"s_waitcnt lgkmcnt(3)\n" \
"ds_read_b128 v[108:111], v29 offset:35840\n" \
"v_mfma_f32_16x16x32_bf16 v[196:199], a[124:127], v[116:119], v[196:199]\n" \
"v_mfma_f32_16x16x32_bf16 v[200:203], a[112:115], v[120:123], v[200:203]\n" \
"ds_read_b128 v[112:115], v29 offset:36864\n" \
"v_mfma_f32_16x16x32_bf16 v[200:203], a[120:123], v[124:127], v[200:203]\n" \
"v_mfma_f32_16x16x32_bf16 v[204:207], a[116:119], v[120:123], v[204:207]\n" \
"ds_read_b128 v[116:119], v29 offset:38912\n" \
"v_mfma_f32_16x16x32_bf16 v[204:207], a[124:127], v[124:127], v[204:207]\n" \
"s_waitcnt vmcnt(0)\n" \
"v_mfma_f32_16x16x32_bf16 v[64:67], v[24:27], v[128:131], v[64:67]\n" \
"v_mfma_f32_16x16x32_bf16 v[68:71], v[22:25], v[128:131], v[68:71]\n" \
"v_mfma_f32_16x16x32_bf16 v[72:75], v[24:27], v[132:135], v[72:75]\n" \
"v_cvt_pk_bf16_f32 v192, v192, v193\n" \
"v_cvt_pk_bf16_f32 v193, v194, v195\n" \
"v_mfma_f32_16x16x32_bf16 v[76:79], v[22:25], v[132:135], v[76:79]\n" \
"v_cvt_pk_bf16_f32 v194, v196, v197\n" \
"v_cvt_pk_bf16_f32 v195, v198, v199\n" \
"v_mfma_f32_16x16x32_bf16 v[64:67], v[24:27], v[136:139], v[64:67]\n" \
"buffer_store_dwordx4 v[192:195], v4, s[36:39], s77 offen offset:0 sc1\n" \
"v_mfma_f32_16x16x32_bf16 v[68:71], v[22:25], v[136:139], v[68:71]\n" \
"v_cvt_pk_bf16_f32 v200, v200, v201\n" \
"v_cvt_pk_bf16_f32 v201, v202, v203\n" \
"v_mfma_f32_16x16x32_bf16 v[72:75], v[24:27], v[140:143], v[72:75]\n" \
"v_cvt_pk_bf16_f32 v202, v204, v205\n" \
"v_cvt_pk_bf16_f32 v203, v206, v207\n" \
"v_mfma_f32_16x16x32_bf16 v[76:79], v[22:25], v[140:143], v[76:79]\n" \
"v_mfma_f32_16x16x32_bf16 v[64:67], v[24:27], v[160:163], v[64:67]\n" \
"v_mfma_f32_16x16x32_bf16 v[68:71], v[22:25], v[160:163], v[68:71]\n" \
"v_mfma_f32_16x16x32_bf16 v[72:75], v[24:27], v[164:167], v[72:75]\n" \
"s_waitcnt lgkmcnt(4)\n" \
"v_mfma_f32_16x16x32_bf16 v[76:79], v[22:25], v[164:167], v[76:79]\n" \
"v_mfma_f32_16x16x32_bf16 v[240:243], a[160:163], v[96:99], 0\n" \
"ds_read_b128 v[120:123], v29 offset:37888\n" \
"v_mfma_f32_16x16x32_bf16 v[240:243], a[168:171], v[100:103], v[240:243]\n" \
"buffer_store_dwordx4 v[200:203], v4, s[36:39], s77 offen offset:1024 sc1\n" \
"v_mfma_f32_16x16x32_bf16 v[244:247], a[164:167], v[96:99], 0\n" \
"s_waitcnt lgkmcnt(3)\n" \
"ds_read_b128 v[124:127], v29 offset:39936\n" \
"v_fmac_f32 v32, v5, v64\n" \
"v_mfma_f32_16x16x32_bf16 v[244:247], a[172:175], v[100:103], v[244:247]\n" \
"v_fmac_f32 v33, v5, v65\n" \
"v_cvt_pk_bf16_f32 v80, v32, v33\n" \
"v_mfma_f32_16x16x32_bf16 v[248:251], a[160:163], v[104:107], 0\n" \
"ds_read_b128 v[96:99], v29 offset:40960\n" \
"v_fmac_f32 v34, v5, v66\n" \
"v_mfma_f32_16x16x32_bf16 v[248:251], a[168:171], v[108:111], v[248:251]\n" \
"v_fmac_f32 v35, v5, v67\n" \
"v_cvt_pk_bf16_f32 v81, v34, v35\n" \
"v_mfma_f32_16x16x32_bf16 v[252:255], a[164:167], v[104:107], 0\n" \
"s_waitcnt lgkmcnt(3)\n" \
"ds_read_b128 v[100:103], v29 offset:43008\n" \
"v_fmac_f32 v36, v5, v68\n" \
"v_mfma_f32_16x16x32_bf16 v[252:255], a[172:175], v[108:111], v[252:255]\n" \
"v_fmac_f32 v37, v5, v69\n" \
"v_cvt_pk_bf16_f32 v82, v36, v37\n" \
"v_mfma_f32_16x16x32_bf16 v[240:243], a[176:179], v[112:115], v[240:243]\n" \
"ds_read_b128 v[104:107], v29 offset:41984\n" \
"v_fmac_f32 v38, v5, v70\n" \
"v_mfma_f32_16x16x32_bf16 v[240:243], a[184:187], v[116:119], v[240:243]\n" \
"s_add_u32 s92, s82, 0\n" \
"v_fmac_f32 v39, v5, v71\n" \
"v_cvt_pk_bf16_f32 v83, v38, v39\n" \
"v_mfma_f32_16x16x32_bf16 v[244:247], a[180:183], v[112:115], v[244:247]\n" \
"s_waitcnt lgkmcnt(3)\n" \
"ds_read_b128 v[108:111], v29 offset:44032\n" \
"s_nop 0\n" \
"v_mfma_f32_16x16x32_bf16 v[244:247], a[188:191], v[116:119], v[244:247]\n" \
"v_permlane16_swap_b32 v80, v82\n" \
"v_permlane16_swap_b32 v81, v83\n" \
"v_mfma_f32_16x16x32_bf16 v[248:251], a[176:179], v[120:123], v[248:251]\n" \
"ds_read_b128 v[112:115], v29 offset:45056\n" \
"v_fmac_f32 v40, v21, v72\n" \
"v_mfma_f32_16x16x32_bf16 v[248:251], a[184:187], v[124:127], v[248:251]\n" \
"v_fmac_f32 v41, v21, v73\n" \
"v_cvt_pk_bf16_f32 v84, v40, v41\n" \
"v_mfma_f32_16x16x32_bf16 v[252:255], a[180:183], v[120:123], v[252:255]\n" \
"ds_read_b128 v[116:119], v29 offset:47104\n" \
"v_fmac_f32 v42, v21, v74\n" \
"v_mfma_f32_16x16x32_bf16 v[252:255], a[188:191], v[124:127], v[252:255]\n" \
"s_waitcnt vmcnt(19) lgkmcnt(4)\n" \
"s_barrier\n" \
"v_fmac_f32 v43, v21, v75\n" \
"v_mfma_f32_16x16x32_bf16 v[240:243], a[192:195], v[96:99], v[240:243]\n" \
"ds_read_b128 v[120:123], v29 offset:46080\n" \
"v_cvt_pk_bf16_f32 v85, v42, v43\n" \
"v_mfma_f32_16x16x32_bf16 v[240:243], a[200:203], v[100:103], v[240:243]\n" \
"v_fmac_f32 v44, v21, v76\n" \
"v_fmac_f32 v45, v21, v77\n" \
"v_mfma_f32_16x16x32_bf16 v[244:247], a[196:199], v[96:99], v[244:247]\n" \
"s_waitcnt lgkmcnt(3)\n" \
"ds_read_b128 v[124:127], v29 offset:48128\n" \
"v_cvt_pk_bf16_f32 v86, v44, v45\n" \
"v_mfma_f32_16x16x32_bf16 v[244:247], a[204:207], v[100:103], v[244:247]\n" \
"v_fmac_f32 v46, v21, v78\n" \
"v_fmac_f32 v47, v21, v79\n" \
"v_mfma_f32_16x16x32_bf16 v[248:251], a[192:195], v[104:107], v[248:251]\n" \
"s_add_u32 s97, s78, 4096\n" \
"ds_read_b128 v[96:99], v30 offset:16384\n" \
"v_cvt_pk_bf16_f32 v87, v46, v47\n" \
"v_mfma_f32_16x16x32_bf16 v[248:251], a[200:203], v[108:111], v[248:251]\n" \
"buffer_load_dwordx4 v[168:171], v4, s[36:39], s97 offen offset:2048 sc1\n" \
"v_mfma_f32_16x16x32_bf16 v[252:255], a[196:199], v[104:107], v[252:255]\n" \
"s_waitcnt lgkmcnt(3)\n" \
"ds_read_b128 v[100:103], v30 offset:18432\n" \
"s_add_u32 s93, s82, 32768\n" \
"v_mfma_f32_16x16x32_bf16 v[252:255], a[204:207], v[108:111], v[252:255]\n" \
"s_nop 0\n" \
"v_permlane16_swap_b32 v84, v86\n" \
"v_mfma_f32_16x16x32_bf16 v[240:243], a[208:211], v[112:115], v[240:243]\n" \
"ds_read_b128 v[104:107], v30 offset:17408\n" \
"v_permlane16_swap_b32 v85, v87\n" \
"v_mfma_f32_16x16x32_bf16 v[240:243], a[216:219], v[116:119], v[240:243]\n" \
"buffer_load_dwordx4 v[172:175], v4, s[36:39], s97 offen offset:3072 sc1\n" \
"v_mfma_f32_16x16x32_bf16 v[244:247], a[212:215], v[112:115], v[244:247]\n" \
"s_waitcnt lgkmcnt(3)\n" \
"ds_read_b128 v[108:111], v30 offset:19456\n" \
"s_add_u32 s97, s78, 8192\n" \
"v_mfma_f32_16x16x32_bf16 v[244:247], a[220:223], v[116:119], v[244:247]\n" \
"v_mfma_f32_16x16x32_bf16 v[248:251], a[208:211], v[120:123], v[248:251]\n" \
"ds_read_b128 v[112:115], v30 offset:20480\n" \
"v_mfma_f32_16x16x32_bf16 v[248:251], a[216:219], v[124:127], v[248:251]\n" \
"buffer_load_dwordx4 v[224:227], v4, s[36:39], s97 offen offset:0 sc1\n" \
"v_mfma_f32_16x16x32_bf16 v[252:255], a[212:215], v[120:123], v[252:255]\n" \
"s_waitcnt lgkmcnt(3)\n" \
"ds_read_b128 v[116:119], v30 offset:22528\n" \
"v_mfma_f32_16x16x32_bf16 v[252:255], a[220:223], v[124:127], v[252:255]\n" \
"v_mfma_f32_16x16x32_bf16 v[240:243], a[224:227], v[96:99], v[240:243]\n" \
"ds_read_b128 v[120:123], v30 offset:21504\n" \
"v_mfma_f32_16x16x32_bf16 v[240:243], a[232:235], v[100:103], v[240:243]\n" \
"buffer_load_dwordx4 v[228:231], v4, s[36:39], s97 offen offset:1024 sc1\n" \
"v_mfma_f32_16x16x32_bf16 v[244:247], a[228:231], v[96:99], v[244:247]\n" \
"s_waitcnt lgkmcnt(3)\n" \
"ds_read_b128 v[124:127], v30 offset:23552\n" \
"v_mfma_f32_16x16x32_bf16 v[244:247], a[236:239], v[100:103], v[244:247]\n" \
"v_mfma_f32_16x16x32_bf16 v[248:251], a[224:227], v[104:107], v[248:251]\n" \
"ds_read_b128 v[96:99], v30 offset:24576\n" \
"v_mfma_f32_16x16x32_bf16 v[248:251], a[232:235], v[108:111], v[248:251]\n" \
"v_mfma_f32_16x16x32_bf16 v[252:255], a[228:231], v[104:107], v[252:255]\n" \
"s_waitcnt lgkmcnt(3)\n" \
"ds_read_b128 v[100:103], v30 offset:26624\n" \
"v_mfma_f32_16x16x32_bf16 v[252:255], a[236:239], v[108:111], v[252:255]\n" \
"v_mfma_f32_16x16x32_bf16 v[240:243], a[240:243], v[112:115], v[240:243]\n" \
"ds_read_b128 v[104:107], v30 offset:25600\n" \
"v_mfma_f32_16x16x32_bf16 v[240:243], a[248:251], v[116:119], v[240:243]\n" \
"v_mfma_f32_16x16x32_bf16 v[244:247], a[244:247], v[112:115], v[244:247]\n" \
"s_waitcnt lgkmcnt(3)\n" \
"ds_read_b128 v[108:111], v30 offset:27648\n" \
"v_mfma_f32_16x16x32_bf16 v[244:247], a[252:255], v[116:119], v[244:247]\n" \
"v_mfma_f32_16x16x32_bf16 v[248:251], a[240:243], v[120:123], v[248:251]\n" \
"ds_read_b128 v[112:115], v30 offset:28672\n" \
"v_mfma_f32_16x16x32_bf16 v[248:251], a[248:251], v[124:127], v[248:251]\n" \
"v_mfma_f32_16x16x32_bf16 v[252:255], a[244:247], v[120:123], v[252:255]\n" \
"ds_read_b128 v[116:119], v30 offset:30720\n" \
"v_mfma_f32_16x16x32_bf16 v[252:255], a[252:255], v[124:127], v[252:255]\n" \
"s_waitcnt vmcnt(18) lgkmcnt(4)\n" \
"s_barrier\n" \
"v_mfma_f32_16x16x32_bf16 v[240:243], a[64:67], v[96:99], v[240:243]\n" \
"ds_read_b128 v[120:123], v30 offset:29696\n" \
"v_mfma_f32_16x16x32_bf16 v[240:243], a[72:75], v[100:103], v[240:243]\n" \
"v_mfma_f32_16x16x32_bf16 v[244:247], a[68:71], v[96:99], v[244:247]\n" \
"s_waitcnt lgkmcnt(3)\n" \
"ds_read_b128 v[124:127], v30 offset:31744\n" \
"v_mfma_f32_16x16x32_bf16 v[244:247], a[76:79], v[100:103], v[244:247]\n" \
"v_mfma_f32_16x16x32_bf16 v[248:251], a[64:67], v[104:107], v[248:251]\n" \
"ds_read_b128 v[96:99], v30 offset:0\n" \
"v_mfma_f32_16x16x32_bf16 v[248:251], a[72:75], v[108:111], v[248:251]\n" \
"v_mfma_f32_16x16x32_bf16 v[252:255], a[68:71], v[104:107], v[252:255]\n" \
"s_waitcnt lgkmcnt(3)\n" \
"ds_read_b128 v[100:103], v30 offset:2048\n" \
"v_mfma_f32_16x16x32_bf16 v[252:255], a[76:79], v[108:111], v[252:255]\n" \
"v_mfma_f32_16x16x32_bf16 v[240:243], a[80:83], v[112:115], v[240:243]\n" \
"ds_read_b128 v[104:107], v30 offset:1024\n" \
"v_mfma_f32_16x16x32_bf16 v[240:243], a[88:91], v[116:119], v[240:243]\n" \
"v_mfma_f32_16x16x32_bf16 v[244:247], a[84:87], v[112:115], v[244:247]\n" \
"s_waitcnt lgkmcnt(3)\n" \
"ds_read_b128 v[108:111], v30 offset:3072\n" \
"v_mfma_f32_16x16x32_bf16 v[244:247], a[92:95], v[116:119], v[244:247]\n" \
"v_mfma_f32_16x16x32_bf16 v[248:251], a[80:83], v[120:123], v[248:251]\n" \
"ds_read_b128 v[112:115], v30 offset:4096\n" \
"v_mfma_f32_16x16x32_bf16 v[248:251], a[88:91], v[124:127], v[248:251]\n" \
"v_mfma_f32_16x16x32_bf16 v[252:255], a[84:87], v[120:123], v[252:255]\n" \
"s_waitcnt lgkmcnt(3)\n" \
"ds_read_b128 v[116:119], v30 offset:6144\n" \
"v_mfma_f32_16x16x32_bf16 v[252:255], a[92:95], v[124:127], v[252:255]\n" \
"v_mfma_f32_16x16x32_bf16 v[240:243], a[128:131], v[96:99], v[240:243]\n" \
"ds_read_b128 v[120:123], v30 offset:5120\n" \
"v_mfma_f32_16x16x32_bf16 v[240:243], a[136:139], v[100:103], v[240:243]\n" \
"v_mfma_f32_16x16x32_bf16 v[244:247], a[132:135], v[96:99], v[244:247]\n" \
"s_waitcnt lgkmcnt(3)\n" \
"ds_read_b128 v[124:127], v30 offset:7168\n" \
"v_mfma_f32_16x16x32_bf16 v[244:247], a[140:143], v[100:103], v[244:247]\n" \
"v_mfma_f32_16x16x32_bf16 v[248:251], a[128:131], v[104:107], v[248:251]\n" \
"ds_read_b128 v[96:99], v30 offset:8192\n" \
"v_mfma_f32_16x16x32_bf16 v[248:251], a[136:139], v[108:111], v[248:251]\n" \
"v_mfma_f32_16x16x32_bf16 v[252:255], a[132:135], v[104:107], v[252:255]\n" \
"s_waitcnt lgkmcnt(3)\n" \
"ds_read_b128 v[100:103], v30 offset:10240\n" \
"v_mfma_f32_16x16x32_bf16 v[252:255], a[140:143], v[108:111], v[252:255]\n" \
"v_mfma_f32_16x16x32_bf16 v[240:243], a[144:147], v[112:115], v[240:243]\n" \
"ds_read_b128 v[104:107], v30 offset:9216\n" \
"v_mfma_f32_16x16x32_bf16 v[240:243], a[152:155], v[116:119], v[240:243]\n" \
"v_mfma_f32_16x16x32_bf16 v[244:247], a[148:151], v[112:115], v[244:247]\n" \
"s_waitcnt lgkmcnt(3)\n" \
"ds_read_b128 v[108:111], v30 offset:11264\n" \
"v_mfma_f32_16x16x32_bf16 v[244:247], a[156:159], v[116:119], v[244:247]\n" \
"v_mfma_f32_16x16x32_bf16 v[248:251], a[144:147], v[120:123], v[248:251]\n" \
"ds_read_b128 v[112:115], v30 offset:12288\n" \
"v_mfma_f32_16x16x32_bf16 v[248:251], a[152:155], v[124:127], v[248:251]\n" \
"v_mfma_f32_16x16x32_bf16 v[252:255], a[148:151], v[120:123], v[252:255]\n" \
"ds_read_b128 v[116:119], v30 offset:14336\n" \
"v_mfma_f32_16x16x32_bf16 v[252:255], a[156:159], v[124:127], v[252:255]\n" \
"s_waitcnt vmcnt(12) lgkmcnt(4)\n" \
"s_barrier\n" \
"v_mfma_f32_16x16x32_bf16 v[240:243], a[0:3], v[96:99], v[240:243]\n" \
"ds_read_b128 v[120:123], v30 offset:13312\n" \
"v_mfma_f32_16x16x32_bf16 v[240:243], a[8:11], v[100:103], v[240:243]\n" \
"v_mfma_f32_16x16x32_bf16 v[244:247], a[4:7], v[96:99], v[244:247]\n" \
"s_waitcnt lgkmcnt(3)\n" \
"ds_read_b128 v[124:127], v30 offset:15360\n" \
"v_mfma_f32_16x16x32_bf16 v[244:247], a[12:15], v[100:103], v[244:247]\n" \
"v_mfma_f32_16x16x32_bf16 v[248:251], a[0:3], v[104:107], v[248:251]\n" \
"ds_read_b128 v[96:99], v28 offset:16384\n" \
"v_mfma_f32_16x16x32_bf16 v[248:251], a[8:11], v[108:111], v[248:251]\n" \
"v_mfma_f32_16x16x32_bf16 v[252:255], a[4:7], v[104:107], v[252:255]\n" \
"s_waitcnt lgkmcnt(3)\n" \
"ds_read_b128 v[100:103], v28 offset:18432\n" \
"v_mfma_f32_16x16x32_bf16 v[252:255], a[12:15], v[108:111], v[252:255]\n" \
"v_mfma_f32_16x16x32_bf16 v[240:243], a[16:19], v[112:115], v[240:243]\n" \
"ds_read_b128 v[104:107], v28 offset:17408\n" \
"v_mfma_f32_16x16x32_bf16 v[240:243], a[24:27], v[116:119], v[240:243]\n" \
"v_mfma_f32_16x16x32_bf16 v[244:247], a[20:23], v[112:115], v[244:247]\n" \
"s_waitcnt lgkmcnt(3)\n" \
"ds_read_b128 v[108:111], v28 offset:19456\n" \
"s_add_u32 s97, s78, 8192\n" \
"v_mfma_f32_16x16x32_bf16 v[244:247], a[28:31], v[116:119], v[244:247]\n" \
"buffer_load_dwordx4 v[232:235], v4, s[36:39], s97 offen offset:2048 sc1\n" \
"v_mfma_f32_16x16x32_bf16 v[248:251], a[16:19], v[120:123], v[248:251]\n" \
"ds_read_b128 v[112:115], v28 offset:20480\n" \
"s_waitcnt vmcnt(1)\n" \
"v_mfma_f32_16x16x32_bf16 v[248:251], a[24:27], v[124:127], v[248:251]\n" \
"v_max_u32 v18, v168, v169\n" \
"v_max3_u32 v18, v18, v170, v171\n" \
"v_mfma_f32_16x16x32_bf16 v[252:255], a[20:23], v[120:123], v[252:255]\n" \
"s_waitcnt lgkmcnt(3)\n" \
"ds_read_b128 v[116:119], v28 offset:22528\n" \
"v_max3_u32 v18, v18, v172, v173\n" \
"v_mfma_f32_16x16x32_bf16 v[252:255], a[28:31], v[124:127], v[252:255]\n" \
"buffer_load_dwordx4 v[236:239], v4, s[36:39], s97 offen offset:3072 sc1\n" \
"v_mfma_f32_16x16x32_bf16 v[240:243], a[32:35], v[96:99], v[240:243]\n" \
"ds_read_b128 v[120:123], v28 offset:21504\n" \
"v_max3_u32 v18, v18, v174, v175\n" \
"v_mfma_f32_16x16x32_bf16 v[240:243], a[40:43], v[100:103], v[240:243]\n" \
"buffer_store_dwordx4 v[80:83], v184, s[32:35], s92 offen\n" \
"v_mfma_f32_16x16x32_bf16 v[244:247], a[36:39], v[96:99], v[244:247]\n" \
"s_waitcnt lgkmcnt(3)\n" \
"ds_read_b128 v[124:127], v28 offset:23552\n" \
"v_max3_u32 v18, v18, v224, v225\n" \
"v_mfma_f32_16x16x32_bf16 v[244:247], a[44:47], v[100:103], v[244:247]\n" \
"v_max3_u32 v18, v18, v226, v227\n" \
"v_max3_u32 v18, v18, v228, v229\n" \
"v_mfma_f32_16x16x32_bf16 v[248:251], a[32:35], v[104:107], v[248:251]\n" \
"ds_read_b128 v[96:99], v28 offset:24576\n" \
"v_max3_u32 v18, v18, v230, v231\n" \
"v_mfma_f32_16x16x32_bf16 v[248:251], a[40:43], v[108:111], v[248:251]\n" \
"v_cmp_eq_u32 vcc, -1, v18\n" \
"s_cbranch_vccnz LIM_%=\nLIMR_%=:\n" \
"v_mfma_f32_16x16x32_bf16 v[252:255], a[36:39], v[104:107], v[252:255]\n" \
"s_waitcnt lgkmcnt(3)\n" \
"ds_read_b128 v[100:103], v28 offset:26624\n" \
"v_mfma_f32_16x16x32_bf16 v[252:255], a[44:47], v[108:111], v[252:255]\n" \
"v_mfma_f32_16x16x32_bf16 v[240:243], a[48:51], v[112:115], v[240:243]\n" \
"ds_read_b128 v[104:107], v28 offset:25600\n" \
"v_mfma_f32_16x16x32_bf16 v[240:243], a[56:59], v[116:119], v[240:243]\n" \
"v_mfma_f32_16x16x32_bf16 v[244:247], a[52:55], v[112:115], v[244:247]\n" \
"s_waitcnt lgkmcnt(3)\n" \
"ds_read_b128 v[108:111], v28 offset:27648\n" \
"v_mfma_f32_16x16x32_bf16 v[244:247], a[60:63], v[116:119], v[244:247]\n" \
"buffer_store_dwordx4 v[84:87], v184, s[32:35], s93 offen\n" \
"v_mfma_f32_16x16x32_bf16 v[248:251], a[48:51], v[120:123], v[248:251]\n" \
"ds_read_b128 v[112:115], v28 offset:28672\n" \
"v_mfma_f32_16x16x32_bf16 v[248:251], a[56:59], v[124:127], v[248:251]\n" \
"v_mfma_f32_16x16x32_bf16 v[252:255], a[52:55], v[120:123], v[252:255]\n" \
"ds_read_b128 v[116:119], v28 offset:30720\n" \
"v_mfma_f32_16x16x32_bf16 v[252:255], a[60:63], v[124:127], v[252:255]\n" \
"s_waitcnt lgkmcnt(4)\n" \
"s_barrier\n" \
"v_mfma_f32_16x16x32_bf16 v[240:243], a[96:99], v[96:99], v[240:243]\n" \
"ds_read_b128 v[120:123], v28 offset:29696\n" \
"v_mfma_f32_16x16x32_bf16 v[240:243], a[104:107], v[100:103], v[240:243]\n" \
"v_mfma_f32_16x16x32_bf16 v[244:247], a[100:103], v[96:99], v[244:247]\n" \
"s_waitcnt lgkmcnt(3)\n" \
"ds_read_b128 v[124:127], v28 offset:31744\n" \
"v_mfma_f32_16x16x32_bf16 v[244:247], a[108:111], v[100:103], v[244:247]\n" \
"v_mfma_f32_16x16x32_bf16 v[248:251], a[96:99], v[104:107], v[248:251]\n" \
"v_mfma_f32_16x16x32_bf16 v[248:251], a[104:107], v[108:111], v[248:251]\n" \
"v_mfma_f32_16x16x32_bf16 v[252:255], a[100:103], v[104:107], v[252:255]\n" \
"s_waitcnt lgkmcnt(2)\n" \
"v_mfma_f32_16x16x32_bf16 v[252:255], a[108:111], v[108:111], v[252:255]\n" \
"v_mfma_f32_16x16x32_bf16 v[240:243], a[112:115], v[112:115], v[240:243]\n" \
"v_mfma_f32_16x16x32_bf16 v[240:243], a[120:123], v[116:119], v[240:243]\n" \
"v_mfma_f32_16x16x32_bf16 v[244:247], a[116:119], v[112:115], v[244:247]\n" \
"s_waitcnt lgkmcnt(0)\n" \
"v_mfma_f32_16x16x32_bf16 v[244:247], a[124:127], v[116:119], v[244:247]\n" \
"v_mfma_f32_16x16x32_bf16 v[248:251], a[112:115], v[120:123], v[248:251]\n" \
"v_mfma_f32_16x16x32_bf16 v[248:251], a[120:123], v[124:127], v[248:251]\n" \
"v_mfma_f32_16x16x32_bf16 v[252:255], a[116:119], v[120:123], v[252:255]\n" \
"v_mfma_f32_16x16x32_bf16 v[252:255], a[124:127], v[124:127], v[252:255]\n" \
"v_mfma_f32_16x16x32_bf16 v[240:243], v[24:27], v[168:171], v[240:243]\n" \
"v_mfma_f32_16x16x32_bf16 v[244:247], v[22:25], v[168:171], v[244:247]\n" \
"v_mfma_f32_16x16x32_bf16 v[248:251], v[24:27], v[172:175], v[248:251]\n" \
"v_mfma_f32_16x16x32_bf16 v[252:255], v[22:25], v[172:175], v[252:255]\n" \
"v_mfma_f32_16x16x32_bf16 v[240:243], v[24:27], v[224:227], v[240:243]\n" \
"v_mfma_f32_16x16x32_bf16 v[244:247], v[22:25], v[224:227], v[244:247]\n" \
"v_mfma_f32_16x16x32_bf16 v[248:251], v[24:27], v[228:231], v[248:251]\n" \
"v_mfma_f32_16x16x32_bf16 v[252:255], v[22:25], v[228:231], v[252:255]\n" \
"s_mov_b32 s92, s83\n" \
"s_add_u32 s93, s83, 32768\n" \
"s_waitcnt vmcnt(2)\n" \
"v_max_u32 v18, v232, v233\n" \
"v_max3_u32 v18, v18, v234, v235\n" \
"v_max3_u32 v18, v18, v236, v237\n" \
"v_max3_u32 v18, v18, v238, v239\n" \
"v_cmp_eq_u32 vcc, -1, v18\n" \
"s_cbranch_vccnz LIN_%=\nLINR_%=:\n" \
"v_mfma_f32_16x16x32_bf16 v[240:243], v[24:27], v[232:235], v[240:243]\n" \
"v_mfma_f32_16x16x32_bf16 v[244:247], v[22:25], v[232:235], v[244:247]\n" \
"v_mfma_f32_16x16x32_bf16 v[248:251], v[24:27], v[236:239], v[248:251]\n" \
"v_mfma_f32_16x16x32_bf16 v[252:255], v[22:25], v[236:239], v[252:255]\n" \
"s_nop 3\n" \
"v_pk_fma_f32 v[240:241], v[240:241], v[6:7], v[48:49] op_sel_hi:[1,0,1]\n" \
"v_pk_fma_f32 v[242:243], v[242:243], v[6:7], v[50:51] op_sel_hi:[1,0,1]\n" \
"v_pk_fma_f32 v[244:245], v[244:245], v[6:7], v[52:53] op_sel_hi:[1,0,1]\n" \
"v_pk_fma_f32 v[246:247], v[246:247], v[6:7], v[54:55] op_sel_hi:[1,0,1]\n" \
"v_cvt_pk_bf16_f32 v80, v240, v241\n" \
"v_cvt_pk_bf16_f32 v81, v242, v243\n" \
"v_cvt_pk_bf16_f32 v82, v244, v245\n" \
"v_cvt_pk_bf16_f32 v83, v246, v247\n" \
"s_nop 1\n" \
"v_permlane16_swap_b32 v80, v82\n" \
"v_permlane16_swap_b32 v81, v83\n" \
"buffer_store_dwordx4 v[80:83], v184, s[32:35], s92 offen\n" \
"v_pk_fma_f32 v[248:249], v[248:249], v[8:9], v[56:57] op_sel_hi:[1,0,1]\n" \
"v_pk_fma_f32 v[250:251], v[250:251], v[8:9], v[58:59] op_sel_hi:[1,0,1]\n" \
"v_pk_fma_f32 v[252:253], v[252:253], v[8:9], v[60:61] op_sel_hi:[1,0,1]\n" \
"v_pk_fma_f32 v[254:255], v[254:255], v[8:9], v[62:63] op_sel_hi:[1,0,1]\n" \
"v_cvt_pk_bf16_f32 v84, v248, v249\n" \
"v_cvt_pk_bf16_f32 v85, v250, v251\n" \
"v_cvt_pk_bf16_f32 v86, v252, v253\n" \
"v_cvt_pk_bf16_f32 v87, v254, v255\n" \
"s_nop 1\n" \
"v_permlane16_swap_b32 v84, v86\n" \
"v_permlane16_swap_b32 v85, v87\n" \
"buffer_store_dwordx4 v[84:87], v184, s[32:35], s93 offen\n" \
"s_branch LEND_%=\n" \
"LPUK_%=:\n" \
"s_mov_b32 s25, 0\n" \
"LPUKL_%=:\n" \
"s_sleep 2\n" \
"buffer_load_dwordx2 v[58:59], v57, s[36:39], 0 offen sc1\n" \
"s_add_u32 s25, s25, 1\n" \
"s_waitcnt vmcnt(0)\n" \
"v_cmp_ne_u64 vcc, s[14:15], v[58:59]\n" \
"s_cmp_gt_u32 s25, 262144\n" \
"s_cbranch_scc1 LPUKR_%=\n" \
"s_cbranch_vccnz LPUKL_%=\n" \
"s_branch LPUKR_%=\n" \
"LPUV_%=:\n" \
"s_mov_b32 s25, 0\n" \
"LPUVL_%=:\n" \
"s_sleep 2\n" \
"buffer_load_dwordx2 v[58:59], v57, s[36:39], 0 offen sc1\n" \
"s_add_u32 s25, s25, 1\n" \
"s_waitcnt vmcnt(0)\n" \
"v_cmp_ne_u64 vcc, s[14:15], v[58:59]\n" \
"s_cmp_gt_u32 s25, 262144\n" \
"s_cbranch_scc1 LPUVR_%=\n" \
"s_cbranch_vccnz LPUVL_%=\n" \
"s_branch LPUVR_%=\n" \
"LPK_%=:\n" \
"s_mov_b32 s25, 0\n" \
"LPKL_%=:\n" \
"s_sleep 2\n" \
"buffer_load_dwordx2 v[58:59], v57, s[36:39], 0 offen sc1\n" \
"s_add_u32 s25, s25, 1\n" \
"s_waitcnt vmcnt(0)\n" \
"v_cmp_ne_u64 vcc, s[14:15], v[58:59]\n" \
"s_cmp_gt_u32 s25, 262144\n" \
"s_cbranch_scc1 LPKR_%=\n" \
"s_cbranch_vccnz LPKL_%=\n" \
"s_branch LPKR_%=\n" \
"LSW_%=:\n" \
"v_swap_b32 v64, v80\n" \
"v_swap_b32 v65, v81\n" \
"v_swap_b32 v66, v82\n" \
"v_swap_b32 v67, v83\n" \
"v_swap_b32 v68, v84\n" \
"v_swap_b32 v69, v85\n" \
"v_swap_b32 v70, v86\n" \
"v_swap_b32 v71, v87\n" \
"v_swap_b32 v72, v88\n" \
"v_swap_b32 v73, v89\n" \
"v_swap_b32 v74, v90\n" \
"v_swap_b32 v75, v91\n" \
"v_swap_b32 v76, v92\n" \
"v_swap_b32 v77, v93\n" \
"v_swap_b32 v78, v94\n" \
"v_swap_b32 v79, v95\n" \
"s_branch LSWR_%=\n" \
"LIM_%=:\n" \
"s_mov_b32 s25, 0\n" \
"LIML_%=:\n" \
"s_sleep 1\n" \
"s_add_u32 s98, s78, 4096\n" \
"buffer_load_dwordx4 v[168:171], v4, s[36:39], s98 offen offset:2048 sc1\n" \
"buffer_load_dwordx4 v[172:175], v4, s[36:39], s98 offen offset:3072 sc1\n" \
"s_add_u32 s98, s78, 8192\n" \
"buffer_load_dwordx4 v[224:227], v4, s[36:39], s98 offen offset:0 sc1\n" \
"buffer_load_dwordx4 v[228:231], v4, s[36:39], s98 offen offset:1024 sc1\n" \
"s_add_u32 s25, s25, 1\n" \
"s_waitcnt vmcnt(0)\n" \
"v_max_u32 v18, v168, v169\n" \
"v_max3_u32 v18, v18, v170, v171\n" \
"v_max3_u32 v18, v18, v172, v173\n" \
"v_max3_u32 v18, v18, v174, v175\n" \
"v_max3_u32 v18, v18, v224, v225\n" \
"v_max3_u32 v18, v18, v226, v227\n" \
"v_max3_u32 v18, v18, v228, v229\n" \
"v_max3_u32 v18, v18, v230, v231\n" \
"v_cmp_eq_u32 vcc, -1, v18\n" \
"s_cmp_gt_u32 s25, 262144\n" \
"s_cbranch_scc1 LIMR_%=\n" \
"s_cbranch_vccnz LIML_%=\n" \
"s_branch LIMR_%=\n" \
"LIN_%=:\n" \
"s_mov_b32 s25, 0\n" \
"LINL_%=:\n" \
"s_sleep 1\n" \
"s_add_u32 s98, s78, 8192\n" \
"buffer_load_dwordx4 v[232:235], v4, s[36:39], s98 offen offset:2048 sc1\n" \
"buffer_load_dwordx4 v[236:239], v4, s[36:39], s98 offen offset:3072 sc1\n" \
"s_add_u32 s25, s25, 1\n" \
"s_waitcnt vmcnt(0)\n" \
"v_max_u32 v18, v232, v233\n" \
"v_max3_u32 v18, v18, v234, v235\n" \
"v_max3_u32 v18, v18, v236, v237\n" \
"v_max3_u32 v18, v18, v238, v239\n" \
"v_cmp_eq_u32 vcc, -1, v18\n" \
"s_cmp_gt_u32 s25, 262144\n" \
"s_cbranch_scc1 LINR_%=\n" \
"s_cbranch_vccnz LINL_%=\n" \
"s_branch LINR_%=\n" \
"LPZ_%=:\n" \
"s_mov_b32 s25, 0\n" \
"LPZL_%=:\n" \
"s_sleep 2\n" \
"buffer_load_dwordx2 v[18:19], v17, s[36:39], 0 offen sc1\n" \
"s_add_u32 s25, s25, 1\n" \
"s_waitcnt vmcnt(0)\n" \
"v_cmp_ne_u64 vcc, s[14:15], v[18:19]\n" \
"s_cmp_gt_u32 s25, 262144\n" \
"s_cbranch_scc1 LPZR_%=\n" \
"s_cbranch_vccnz LPZL_%=\n" \
"s_branch LPZR_%=\n" \
"LPS_%=:\n" \
"s_mov_b32 s25, 0\n" \
"LPSL_%=:\n" \
"s_sleep 2\n" \
"buffer_load_dwordx2 v[18:19], v17, s[36:39], 0 offen sc1\n" \
"s_add_u32 s25, s25, 1\n" \
"s_waitcnt vmcnt(0)\n" \
"v_cmp_ne_u64 vcc, s[14:15], v[18:19]\n" \
"s_cmp_gt_u32 s25, 262144\n" \
"s_cbranch_scc1 LPSR_%=\n" \
"s_cbranch_vccnz LPSL_%=\n" \
"s_branch LPSR_%=\n" \
"LPL_%=:\n" \
"s_mov_b32 s25, 0\n" \
"LPLL_%=:\n" \
"s_sleep 2\n" \
"buffer_load_dwordx2 v[18:19], v17, s[36:39], 0 offen sc1\n" \
"s_add_u32 s25, s25, 1\n" \
"s_waitcnt vmcnt(0)\n" \
"v_cmp_ne_u64 vcc, s[14:15], v[18:19]\n" \
"s_cmp_gt_u32 s25, 262144\n" \
"s_cbranch_scc1 LPLR_%=\n" \
"s_cbranch_vccnz LPLL_%=\n" \
"s_branch LPLR_%=\n" \
"LPE_%=:\n" \
"s_mov_b32 s25, 0\n" \
"LPEL_%=:\n" \
"s_sleep 2\n" \
"buffer_load_dwordx2 v[18:19], v17, s[36:39], 0 offen sc1\n" \
"s_add_u32 s25, s25, 1\n" \
"s_waitcnt vmcnt(0)\n" \
"v_cmp_ne_u64 vcc, s[14:15], v[18:19]\n" \
"s_cmp_gt_u32 s25, 262144\n" \
"s_cbranch_scc1 LPER_%=\n" \
"s_cbranch_vccnz LPEL_%=\n" \
"s_branch LPER_%=\n" \
"LPE0_%=:\n" \
"s_mov_b32 s25, 0\n" \
"LPE0L_%=:\n" \
"s_sleep 2\n" \
"buffer_load_dwordx2 v[18:19], v17, s[36:39], 0 offen sc1\n" \
"s_add_u32 s25, s25, 1\n" \
"s_waitcnt vmcnt(0)\n" \
"v_cmp_ne_u64 vcc, s[14:15], v[18:19]\n" \
"s_cmp_gt_u32 s25, 262144\n" \
"s_cbranch_scc1 LPE0R_%=\n" \
"s_cbranch_vccnz LPE0L_%=\n" \
"s_branch LPE0R_%=\n" \
"LFBA_%=:\n" \
"s_mov_b32 s99, 0\n" \
"s_branch LFB_%=\n" \
"LFBC_%=:\n" \
"s_mov_b32 s99, 2\n" \
"s_branch LFB_%=\n" \
"LFBB_%=:\n" \
"s_mov_b32 s99, 1\n" \
"LFB_%=:\n" \
"s_waitcnt vmcnt(0) lgkmcnt(0)\n" \
"s_nop 15\n" \
"s_lshl_b32 s25, %1, 2\n" \
"s_add_u32 s25, s25, %2\n" \
"s_lshl_b32 s25, s25, 13\n" \
"s_add_u32 s25, s25, 16777216\n" \
"buffer_store_dwordx4 v[64:67], v4, s[36:39], s25 offen offset:0\n" \
"buffer_store_dwordx4 v[68:71], v4, s[36:39], s25 offen offset:1024\n" \
"buffer_store_dwordx4 v[72:75], v4, s[36:39], s25 offen offset:2048\n" \
"buffer_store_dwordx4 v[76:79], v4, s[36:39], s25 offen offset:3072\n" \
"buffer_store_dwordx4 v[80:83], v7, s[36:39], s25 offen offset:0\n" \
"buffer_store_dwordx4 v[84:87], v7, s[36:39], s25 offen offset:1024\n" \
"buffer_store_dwordx4 v[88:91], v7, s[36:39], s25 offen offset:2048\n" \
"buffer_store_dwordx4 v[92:95], v7, s[36:39], s25 offen offset:3072\n" \
"s_add_u32 s25, s25, 8388608\n" \
"buffer_store_dwordx4 v[96:99], v4, s[36:39], s25 offen offset:0\n" \
"buffer_store_dwordx4 v[100:103], v4, s[36:39], s25 offen offset:1024\n" \
"buffer_store_dwordx4 v[104:107], v4, s[36:39], s25 offen offset:2048\n" \
"buffer_store_dwordx4 v[108:111], v4, s[36:39], s25 offen offset:3072\n" \
"buffer_store_dwordx4 v[112:115], v7, s[36:39], s25 offen offset:0\n" \
"buffer_store_dwordx4 v[116:119], v7, s[36:39], s25 offen offset:1024\n" \
"buffer_store_dwordx4 v[120:123], v7, s[36:39], s25 offen offset:2048\n" \
"buffer_store_dwordx4 v[124:127], v7, s[36:39], s25 offen offset:3072\n" \
"s_add_u32 s25, s25, 58720256\n" \
"buffer_store_dwordx4 v[128:131], v4, s[36:39], s25 offen offset:0\n" \
"buffer_store_dwordx4 v[132:135], v4, s[36:39], s25 offen offset:1024\n" \
"buffer_store_dwordx4 v[136:139], v4, s[36:39], s25 offen offset:2048\n" \
"buffer_store_dwordx4 v[140:143], v4, s[36:39], s25 offen offset:3072\n" \
"buffer_store_dwordx4 v[144:147], v7, s[36:39], s25 offen offset:0\n" \
"buffer_store_dwordx4 v[148:151], v7, s[36:39], s25 offen offset:1024\n" \
"buffer_store_dwordx4 v[152:155], v7, s[36:39], s25 offen offset:2048\n" \
"buffer_store_dwordx4 v[156:159], v7, s[36:39], s25 offen offset:3072\n" \
"s_add_u32 s25, s25, 37748736\n" \
"buffer_store_dwordx4 v[160:163], v4, s[36:39], s25 offen offset:0\n" \
"buffer_store_dwordx4 v[164:167], v4, s[36:39], s25 offen offset:1024\n" \
"buffer_store_dwordx4 v[168:171], v4, s[36:39], s25 offen offset:2048\n" \
"buffer_store_dwordx4 v[172:175], v4, s[36:39], s25 offen offset:3072\n" \
"s_waitcnt vmcnt(0)\n" \
"v_writelane_b32 v172, vcc_lo, 0\n" \
"v_writelane_b32 v172, vcc_hi, 1\n" \
"v_writelane_b32 v172, s64, 2\n" \
"v_writelane_b32 v172, s65, 3\n" \
"v_writelane_b32 v172, s66, 4\n" \
"v_writelane_b32 v172, s67, 5\n" \
"v_writelane_b32 v172, s68, 6\n" \
"v_writelane_b32 v172, s69, 7\n" \
"v_writelane_b32 v172, s70, 8\n" \
"v_writelane_b32 v172, s71, 9\n" \
"v_writelane_b32 v172, s72, 10\n" \
"v_writelane_b32 v172, s73, 11\n" \
"s_cmp_eq_u32 s99, 1\n" \
"s_cselect_b32 s64, s51, s50\n" \
"s_cselect_b32 s65, 2, 4\n" \
"s_cselect_b64 s[54:55], s[54:55], 0\n" \
"s_lshr_b32 s64, s64, 18\n" \
"s_lshl_b32 s64, s64, 5\n" \
"v_mov_b32 v160, 0x3f803f80\n" \
"v_mov_b32 v161, 0x3f803f80\n" \
"v_mov_b32 v162, 0x3f803f80\n" \
"v_mov_b32 v163, 0x3f803f80\n" \
"LFG_%=:\n" \
"s_mov_b32 s69, 0\n" \
"v_bfe_u32 v167, v4, 4, 4\n" \
"v_lshrrev_b32 v168, 8, v4\n" \
"v_add_u32 v165, s64, v167\n" \
"s_lshl_b32 s67, s6, 8\n" \
"v_lshl_add_u32 v128, v167, 1, s67\n" \
"v_lshl_add_u32 v128, v168, 16, v128\n" \
"v_add_u32 v129, 0x2000, v128\n" \
"v_add_u32 v130, 0x2000, v129\n" \
"v_add_u32 v131, 0x2000, v130\n" \
"v_add_u32 v132, 0x2000, v131\n" \
"v_add_u32 v133, 0x2000, v132\n" \
"v_add_u32 v134, 0x2000, v133\n" \
"v_add_u32 v135, 0x2000, v134\n" \
"LFQ_%=:\n" \
"s_load_dwordx2 s[70:71], %0, 0x0\n" \
"s_load_dwordx2 s[72:73], %0, 0x38\n" \
"v_lshlrev_b32 v166, 11, v165\n" \
"v_lshl_add_u32 v166, v168, 4, v166\n" \
"s_mov_b32 s66, 0\n" \
"v_mov_b32 v136, 0\n" \
"v_mov_b32 v137, 0\n" \
"v_mov_b32 v138, 0\n" \
"v_mov_b32 v139, 0\n" \
"v_mov_b32 v140, 0\n" \
"v_mov_b32 v141, 0\n" \
"v_mov_b32 v142, 0\n" \
"v_mov_b32 v143, 0\n" \
"s_waitcnt lgkmcnt(0)\n" \
"LFK_%=:\n" \
"global_load_dwordx4 v[112:115], v166, s[70:71]\n" \
"global_load_ushort v96, v128, s[72:73] offset:0\n" \
"global_load_ushort v97, v129, s[72:73] offset:0\n" \
"global_load_ushort v98, v130, s[72:73] offset:0\n" \
"global_load_ushort v99, v131, s[72:73] offset:0\n" \
"global_load_ushort v100, v132, s[72:73] offset:0\n" \
"global_load_ushort v101, v133, s[72:73] offset:0\n" \
"global_load_ushort v102, v134, s[72:73] offset:0\n" \
"global_load_ushort v103, v135, s[72:73] offset:0\n" \
"global_load_ushort v104, v128, s[72:73] offset:32\n" \
"global_load_ushort v105, v129, s[72:73] offset:32\n" \
"global_load_ushort v106, v130, s[72:73] offset:32\n" \
"global_load_ushort v107, v131, s[72:73] offset:32\n" \
"global_load_ushort v108, v132, s[72:73] offset:32\n" \
"global_load_ushort v109, v133, s[72:73] offset:32\n" \
"global_load_ushort v110, v134, s[72:73] offset:32\n" \
"global_load_ushort v111, v135, s[72:73] offset:32\n" \
"v_add_u32 v166, 64, v166\n" \
"s_add_u32 s72, s72, 262144\ns_addc_u32 s73, s73, 0\n" \
"s_add_u32 s66, s66, 1\n" \
"s_waitcnt vmcnt(0)\n" \
"v_lshl_or_b32 v116, v97, 16, v96\n" \
"v_lshl_or_b32 v117, v99, 16, v98\n" \
"v_lshl_or_b32 v118, v101, 16, v100\n" \
"v_lshl_or_b32 v119, v103, 16, v102\n" \
"v_lshl_or_b32 v120, v105, 16, v104\n" \
"v_lshl_or_b32 v121, v107, 16, v106\n" \
"v_lshl_or_b32 v122, v109, 16, v108\n" \
"v_lshl_or_b32 v123, v111, 16, v110\n" \
"s_nop 1\n" \
"v_mfma_f32_16x16x32_bf16 v[136:139], v[116:119], v[112:115], v[136:139]\n" \
"v_mfma_f32_16x16x32_bf16 v[140:143], v[120:123], v[112:115], v[140:143]\n" \
"s_cmp_lt_u32 s66, 32\n" \
"s_cbranch_scc1 LFK_%=\n" \
"s_nop 7\n" \
"v_mov_b32 v144, v148\n" \
"v_mov_b32 v145, v149\n" \
"v_mov_b32 v146, v150\n" \
"v_mov_b32 v147, v151\n" \
"v_mov_b32 v148, v152\n" \
"v_mov_b32 v149, v153\n" \
"v_mov_b32 v150, v154\n" \
"v_mov_b32 v151, v155\n" \
"v_mov_b32 v152, v156\n" \
"v_mov_b32 v153, v157\n" \
"v_mov_b32 v154, v158\n" \
"v_mov_b32 v155, v159\n" \
"v_cvt_pk_bf16_f32 v156, v136, v137\n" \
"v_cvt_pk_bf16_f32 v157, v138, v139\n" \
"v_cvt_pk_bf16_f32 v158, v140, v141\n" \
"v_cvt_pk_bf16_f32 v159, v142, v143\n" \
"v_add_u32 v128, 64, v128\n" \
"v_add_u32 v129, 64, v129\n" \
"v_add_u32 v130, 64, v130\n" \
"v_add_u32 v131, 64, v131\n" \
"v_add_u32 v132, 64, v132\n" \
"v_add_u32 v133, 64, v133\n" \
"v_add_u32 v134, 64, v134\n" \
"v_add_u32 v135, 64, v135\n" \
"s_add_u32 s69, s69, 1\n" \
"s_cmp_lt_u32 s69, 4\n" \
"s_cbranch_scc1 LFQ_%=\n" \
"v_mov_b32 v64, 0\n" \
"v_mov_b32 v65, 0\n" \
"v_mov_b32 v66, 0\n" \
"v_mov_b32 v67, 0\n" \
"v_mov_b32 v68, 0\n" \
"v_mov_b32 v69, 0\n" \
"v_mov_b32 v70, 0\n" \
"v_mov_b32 v71, 0\n" \
"v_mov_b32 v72, 0\n" \
"v_mov_b32 v73, 0\n" \
"v_mov_b32 v74, 0\n" \
"v_mov_b32 v75, 0\n" \
"v_mov_b32 v76, 0\n" \
"v_mov_b32 v77, 0\n" \
"v_mov_b32 v78, 0\n" \
"v_mov_b32 v79, 0\n" \
"v_mov_b32 v80, 0\n" \
"v_mov_b32 v81, 0\n" \
"v_mov_b32 v82, 0\n" \
"v_mov_b32 v83, 0\n" \
"v_mov_b32 v84, 0\n" \
"v_mov_b32 v85, 0\n" \
"v_mov_b32 v86, 0\n" \
"v_mov_b32 v87, 0\n" \
"v_mov_b32 v88, 0\n" \
"v_mov_b32 v89, 0\n" \
"v_mov_b32 v90, 0\n" \
"v_mov_b32 v91, 0\n" \
"v_mov_b32 v92, 0\n" \
"v_mov_b32 v93, 0\n" \
"v_mov_b32 v94, 0\n" \
"v_mov_b32 v95, 0\n" \
"v_mov_b32 v132, 0\n" \
"v_mov_b32 v133, 0\n" \
"v_mov_b32 v134, 0\n" \
"v_mov_b32 v135, 0\n" \
"v_mov_b32 v164, 0xff800000\n" \
"v_mov_b32 v169, 0xff800000\n" \
"s_mov_b32 s69, 0\n" \
"s_lshr_b32 s68, s64, 5\n" \
"LFP_%=:\n" \
"s_mov_b32 s66, 0\n" \
"s_lshl_b32 s67, s6, 20\n" \
"s_add_u32 s67, s67, 33554432\n" \
"v_lshlrev_b32 v166, 2, v168\n" \
"v_sub_u32 v166, v165, v166\n" \
"LFT_%=:\n" \
"buffer_load_dwordx4 v[96:99], v4, s[36:39], s67 offen offset:0 sc1\n" \
"buffer_load_dwordx4 v[100:103], v4, s[36:39], s67 offen offset:1024 sc1\n" \
"buffer_load_dwordx4 v[104:107], v4, s[36:39], s67 offen offset:2048 sc1\n" \
"buffer_load_dwordx4 v[108:111], v4, s[36:39], s67 offen offset:3072 sc1\n" \
"buffer_load_dwordx4 v[112:115], v7, s[36:39], s67 offen offset:0 sc1\n" \
"buffer_load_dwordx4 v[116:119], v7, s[36:39], s67 offen offset:1024 sc1\n" \
"buffer_load_dwordx4 v[120:123], v7, s[36:39], s67 offen offset:2048 sc1\n" \
"buffer_load_dwordx4 v[124:127], v7, s[36:39], s67 offen offset:3072 sc1\n" \
"s_add_u32 s67, s67, 8192\n" \
"s_waitcnt vmcnt(0)\n" \
"v_mfma_f32_16x16x32_bf16 v[136:139], v[96:99], v[144:147], 0\n" \
"v_mfma_f32_16x16x32_bf16 v[136:139], v[100:103], v[148:151], v[136:139]\n" \
"v_mfma_f32_16x16x32_bf16 v[136:139], v[104:107], v[152:155], v[136:139]\n" \
"v_mfma_f32_16x16x32_bf16 v[136:139], v[108:111], v[156:159], v[136:139]\n" \
"v_mfma_f32_16x16x32_bf16 v[140:143], v[112:115], v[144:147], 0\n" \
"v_mfma_f32_16x16x32_bf16 v[140:143], v[116:119], v[148:151], v[140:143]\n" \
"v_mfma_f32_16x16x32_bf16 v[140:143], v[120:123], v[152:155], v[140:143]\n" \
"v_mfma_f32_16x16x32_bf16 v[140:143], v[124:127], v[156:159], v[140:143]\n" \
"s_nop 7\n" \
"v_cmp_gt_i32 vcc, 0, v166\n" \
"s_nop 1\n" \
"v_cndmask_b32 v136, v136, v169, vcc\n" \
"v_cmp_gt_i32 vcc, 1, v166\n" \
"s_nop 1\n" \
"v_cndmask_b32 v137, v137, v169, vcc\n" \
"v_cmp_gt_i32 vcc, 2, v166\n" \
"s_nop 1\n" \
"v_cndmask_b32 v138, v138, v169, vcc\n" \
"v_cmp_gt_i32 vcc, 3, v166\n" \
"s_nop 1\n" \
"v_cndmask_b32 v139, v139, v169, vcc\n" \
"v_cmp_gt_i32 vcc, 16, v166\n" \
"s_nop 1\n" \
"v_cndmask_b32 v140, v140, v169, vcc\n" \
"v_cmp_gt_i32 vcc, 17, v166\n" \
"s_nop 1\n" \
"v_cndmask_b32 v141, v141, v169, vcc\n" \
"v_cmp_gt_i32 vcc, 18, v166\n" \
"s_nop 1\n" \
"v_cndmask_b32 v142, v142, v169, vcc\n" \
"v_cmp_gt_i32 vcc, 19, v166\n" \
"s_nop 1\n" \
"v_cndmask_b32 v143, v143, v169, vcc\n" \
"s_cmp_lg_u32 s69, 0\n" \
"s_cbranch_scc1 LF2_%=\n" \
"v_max3_f32 v164, v164, v136, v137\n" \
"v_max3_f32 v164, v164, v138, v139\n" \
"v_max3_f32 v164, v164, v140, v141\n" \
"v_max3_f32 v164, v164, v142, v143\n" \
"s_branch LFN_%=\n" \
"LF2_%=:\n" \
"buffer_load_dwordx4 v[96:99], v4, s[36:39], s67 offen offset:0 sc1\n" \
"buffer_load_dwordx4 v[100:103], v4, s[36:39], s67 offen offset:1024 sc1\n" \
"buffer_load_dwordx4 v[104:107], v4, s[36:39], s67 offen offset:2048 sc1\n" \
"buffer_load_dwordx4 v[108:111], v4, s[36:39], s67 offen offset:3072 sc1\n" \
"buffer_load_dwordx4 v[112:115], v7, s[36:39], s67 offen offset:0 sc1\n" \
"buffer_load_dwordx4 v[116:119], v7, s[36:39], s67 offen offset:1024 sc1\n" \
"buffer_load_dwordx4 v[120:123], v7, s[36:39], s67 offen offset:2048 sc1\n" \
"buffer_load_dwordx4 v[124:127], v7, s[36:39], s67 offen offset:3072 sc1\n" \
"v_sub_f32 v136, v136, v164\n" \
"v_sub_f32 v137, v137, v164\n" \
"v_sub_f32 v138, v138, v164\n" \
"v_sub_f32 v139, v139, v164\n" \
"v_sub_f32 v140, v140, v164\n" \
"v_sub_f32 v141, v141, v164\n" \
"v_sub_f32 v142, v142, v164\n" \
"v_sub_f32 v143, v143, v164\n" \
"v_exp_f32 v136, v136\n" \
"v_exp_f32 v137, v137\n" \
"v_exp_f32 v138, v138\n" \
"v_exp_f32 v139, v139\n" \
"v_exp_f32 v140, v140\n" \
"v_exp_f32 v141, v141\n" \
"v_exp_f32 v142, v142\n" \
"v_exp_f32 v143, v143\n" \
"s_nop 0\n" \
"v_cvt_pk_bf16_f32 v128, v136, v137\n" \
"v_cvt_pk_bf16_f32 v129, v138, v139\n" \
"v_cvt_pk_bf16_f32 v130, v140, v141\n" \
"v_cvt_pk_bf16_f32 v131, v142, v143\n" \
"s_waitcnt vmcnt(0)\n" \
"s_nop 1\n" \
"v_mfma_f32_16x16x32_bf16 v[132:135], v[160:163], v[128:131], v[132:135]\n" \
"v_mfma_f32_16x16x32_bf16 v[64:67], v[96:99], v[128:131], v[64:67]\n" \
"v_mfma_f32_16x16x32_bf16 v[68:71], v[100:103], v[128:131], v[68:71]\n" \
"v_mfma_f32_16x16x32_bf16 v[72:75], v[104:107], v[128:131], v[72:75]\n" \
"v_mfma_f32_16x16x32_bf16 v[76:79], v[108:111], v[128:131], v[76:79]\n" \
"v_mfma_f32_16x16x32_bf16 v[80:83], v[112:115], v[128:131], v[80:83]\n" \
"v_mfma_f32_16x16x32_bf16 v[84:87], v[116:119], v[128:131], v[84:87]\n" \
"v_mfma_f32_16x16x32_bf16 v[88:91], v[120:123], v[128:131], v[88:91]\n" \
"v_mfma_f32_16x16x32_bf16 v[92:95], v[124:127], v[128:131], v[92:95]\n" \
"LFN_%=:\n" \
"s_add_u32 s67, s67, 8192\n" \
"v_subrev_u32 v166, 32, v166\n" \
"s_add_u32 s66, s66, 1\n" \
"s_cmp_le_u32 s66, s68\n" \
"s_cbranch_scc1 LFT_%=\n" \
"v_mov_b32 v170, v164\n" \
"s_nop 1\n" \
"v_permlane32_swap_b32 v164, v170\n" \
"s_nop 0\n" \
"v_max_f32 v164, v164, v170\n" \
"v_mov_b32 v170, v164\n" \
"s_nop 1\n" \
"v_permlane16_swap_b32 v164, v170\n" \
"s_nop 0\n" \
"v_max_f32 v164, v164, v170\n" \
"s_add_u32 s69, s69, 1\n" \
"s_cmp_lt_u32 s69, 2\n" \
"s_cbranch_scc1 LFP_%=\n" \
"s_nop 7\n" \
"v_rcp_f32 v170, v132\n" \
"s_lshl_b32 s67, s68, 5\n" \
"s_add_u32 s67, s67, s6\n" \
"s_lshl_b32 s67, s67, 13\n" \
"s_and_b32 s66, s64, 16\n" \
"s_lshl_b32 s66, s66, 6\n" \
"s_add_u32 s67, s67, s66\n" \
"v_mul_f32 v64, v170, v64\n" \
"v_mul_f32 v65, v170, v65\n" \
"v_mul_f32 v66, v170, v66\n" \
"v_mul_f32 v67, v170, v67\n" \
"v_mul_f32 v68, v170, v68\n" \
"v_mul_f32 v69, v170, v69\n" \
"v_mul_f32 v70, v170, v70\n" \
"v_mul_f32 v71, v170, v71\n" \
"v_mul_f32 v72, v170, v72\n" \
"v_mul_f32 v73, v170, v73\n" \
"v_mul_f32 v74, v170, v74\n" \
"v_mul_f32 v75, v170, v75\n" \
"v_mul_f32 v76, v170, v76\n" \
"v_mul_f32 v77, v170, v77\n" \
"v_mul_f32 v78, v170, v78\n" \
"v_mul_f32 v79, v170, v79\n" \
"v_mul_f32 v80, v170, v80\n" \
"v_mul_f32 v81, v170, v81\n" \
"v_mul_f32 v82, v170, v82\n" \
"v_mul_f32 v83, v170, v83\n" \
"v_mul_f32 v84, v170, v84\n" \
"v_mul_f32 v85, v170, v85\n" \
"v_mul_f32 v86, v170, v86\n" \
"v_mul_f32 v87, v170, v87\n" \
"v_mul_f32 v88, v170, v88\n" \
"v_mul_f32 v89, v170, v89\n" \
"v_mul_f32 v90, v170, v90\n" \
"v_mul_f32 v91, v170, v91\n" \
"v_mul_f32 v92, v170, v92\n" \
"v_mul_f32 v93, v170, v93\n" \
"v_mul_f32 v94, v170, v94\n" \
"v_mul_f32 v95, v170, v95\n" \
"v_cvt_pk_bf16_f32 v64, v64, v65\n" \
"v_cvt_pk_bf16_f32 v65, v66, v67\n" \
"v_cvt_pk_bf16_f32 v66, v68, v69\n" \
"v_cvt_pk_bf16_f32 v67, v70, v71\n" \
"v_cvt_pk_bf16_f32 v68, v72, v73\n" \
"v_cvt_pk_bf16_f32 v69, v74, v75\n" \
"v_cvt_pk_bf16_f32 v70, v76, v77\n" \
"v_cvt_pk_bf16_f32 v71, v78, v79\n" \
"v_cvt_pk_bf16_f32 v72, v80, v81\n" \
"v_cvt_pk_bf16_f32 v73, v82, v83\n" \
"v_cvt_pk_bf16_f32 v74, v84, v85\n" \
"v_cvt_pk_bf16_f32 v75, v86, v87\n" \
"v_cvt_pk_bf16_f32 v76, v88, v89\n" \
"v_cvt_pk_bf16_f32 v77, v90, v91\n" \
"v_cvt_pk_bf16_f32 v78, v92, v93\n" \
"v_cvt_pk_bf16_f32 v79, v94, v95\n" \
"buffer_store_dwordx4 v[64:67], v4, s[36:39], s67 offen offset:0 sc1\n" \
"buffer_store_dwordx4 v[68:71], v4, s[36:39], s67 offen offset:2048 sc1\n" \
"buffer_store_dwordx4 v[72:75], v7, s[36:39], s67 offen offset:0 sc1\n" \
"buffer_store_dwordx4 v[76:79], v7, s[36:39], s67 offen offset:2048 sc1\n" \
"s_waitcnt vmcnt(0)\n" \
"s_add_u32 s64, s64, 16\n" \
"s_sub_u32 s65, s65, 1\n" \
"s_cmp_lg_u32 s65, 0\n" \
"s_cbranch_scc1 LFG_%=\n" \
"v_readlane_b32 vcc_lo, v172, 0\n" \
"v_readlane_b32 vcc_hi, v172, 1\n" \
"v_readlane_b32 s64, v172, 2\n" \
"v_readlane_b32 s65, v172, 3\n" \
"v_readlane_b32 s66, v172, 4\n" \
"v_readlane_b32 s67, v172, 5\n" \
"v_readlane_b32 s68, v172, 6\n" \
"v_readlane_b32 s69, v172, 7\n" \
"v_readlane_b32 s70, v172, 8\n" \
"v_readlane_b32 s71, v172, 9\n" \
"v_readlane_b32 s72, v172, 10\n" \
"v_readlane_b32 s73, v172, 11\n" \
"s_lshl_b32 s25, %1, 2\n" \
"s_add_u32 s25, s25, %2\n" \
"s_lshl_b32 s25, s25, 13\n" \
"s_add_u32 s25, s25, 16777216\n" \
"buffer_load_dwordx4 v[64:67], v4, s[36:39], s25 offen offset:0\n" \
"buffer_load_dwordx4 v[68:71], v4, s[36:39], s25 offen offset:1024\n" \
"buffer_load_dwordx4 v[72:75], v4, s[36:39], s25 offen offset:2048\n" \
"buffer_load_dwordx4 v[76:79], v4, s[36:39], s25 offen offset:3072\n" \
"buffer_load_dwordx4 v[80:83], v7, s[36:39], s25 offen offset:0\n" \
"buffer_load_dwordx4 v[84:87], v7, s[36:39], s25 offen offset:1024\n" \
"buffer_load_dwordx4 v[88:91], v7, s[36:39], s25 offen offset:2048\n" \
"buffer_load_dwordx4 v[92:95], v7, s[36:39], s25 offen offset:3072\n" \
"s_add_u32 s25, s25, 8388608\n" \
"buffer_load_dwordx4 v[96:99], v4, s[36:39], s25 offen offset:0\n" \
"buffer_load_dwordx4 v[100:103], v4, s[36:39], s25 offen offset:1024\n" \
"buffer_load_dwordx4 v[104:107], v4, s[36:39], s25 offen offset:2048\n" \
"buffer_load_dwordx4 v[108:111], v4, s[36:39], s25 offen offset:3072\n" \
"buffer_load_dwordx4 v[112:115], v7, s[36:39], s25 offen offset:0\n" \
"buffer_load_dwordx4 v[116:119], v7, s[36:39], s25 offen offset:1024\n" \
"buffer_load_dwordx4 v[120:123], v7, s[36:39], s25 offen offset:2048\n" \
"buffer_load_dwordx4 v[124:127], v7, s[36:39], s25 offen offset:3072\n" \
"s_add_u32 s25, s25, 58720256\n" \
"buffer_load_dwordx4 v[128:131], v4, s[36:39], s25 offen offset:0\n" \
"buffer_load_dwordx4 v[132:135], v4, s[36:39], s25 offen offset:1024\n" \
"buffer_load_dwordx4 v[136:139], v4, s[36:39], s25 offen offset:2048\n" \
"buffer_load_dwordx4 v[140:143], v4, s[36:39], s25 offen offset:3072\n" \
"buffer_load_dwordx4 v[144:147], v7, s[36:39], s25 offen offset:0\n" \
"buffer_load_dwordx4 v[148:151], v7, s[36:39], s25 offen offset:1024\n" \
"buffer_load_dwordx4 v[152:155], v7, s[36:39], s25 offen offset:2048\n" \
"buffer_load_dwordx4 v[156:159], v7, s[36:39], s25 offen offset:3072\n" \
"s_add_u32 s25, s25, 37748736\n" \
"buffer_load_dwordx4 v[160:163], v4, s[36:39], s25 offen offset:0\n" \
"buffer_load_dwordx4 v[164:167], v4, s[36:39], s25 offen offset:1024\n" \
"buffer_load_dwordx4 v[168:171], v4, s[36:39], s25 offen offset:2048\n" \
"buffer_load_dwordx4 v[172:175], v4, s[36:39], s25 offen offset:3072\n" \
"s_waitcnt vmcnt(0)\n" \
"s_cmp_eq_u32 s99, 0\n" \
"s_cbranch_scc1 LFBAR_%=\n" \
"s_cmp_eq_u32 s99, 1\n" \
"s_cbranch_scc1 LFBBR_%=\n" \
"s_branch LFBCR_%=\n" \
"LEND_%=:\n" \
"s_waitcnt vmcnt(0) lgkmcnt(0)\n" \
""
#define QKV_ASM_CLOBBERS "v1", "v2", "v3", "v4", "v5", "v6", "v7", "v8", "v9", "v10", "v11", "v12", "v13", "v14", "v15", "v16", "v17", "v18", "v19", "v20", "v21", "v22", "v23", "v24", "v25", "v26", "v27", "v28", "v29", "v30", "v31", "v32", "v33", "v34", "v35", "v36", "v37", "v38", "v39", "v40", "v41", "v42", "v43", "v44", "v45", "v46", "v47", "v48", "v49", "v50", "v51", "v52", "v53", "v54", "v55", "v56", "v57", "v58", "v59", "v60", "v61", "v62", "v63", "v64", "v65", "v66", "v67", "v68", "v69", "v70", "v71", "v72", "v73", "v74", "v75", "v76", "v77", "v78", "v79", "v80", "v81", "v82", "v83", "v84", "v85", "v86", "v87", "v88", "v89", "v90", "v91", "v92", "v93", "v94", "v95", "v96", "v97", "v98", "v99", "v100", "v101", "v102", "v103", "v104", "v105", "v106", "v107", "v108", "v109", "v110", "v111", "v112", "v113", "v114", "v115", "v116", "v117", "v118", "v119", "v120", "v121", "v122", "v123", "v124", "v125", "v126", "v127", "v128", "v129", "v130", "v131", "v132", "v133", "v134", "v135", "v136", "v137", "v138", "v139", "v140", "v141", "v142", "v143", "v144", "v145", "v146", "v147", "v148", "v149", "v150", "v151", "v152", "v153", "v154", "v155", "v156", "v157", "v158", "v159", "v160", "v161", "v162", "v163", "v164", "v165", "v166", "v167", "v168", "v169", "v170", "v171", "v172", "v173", "v174", "v175", "v176", "v177", "v178", "v179", "v180", "v181", "v182", "v183", "v184", "v185", "v186", "v187", "v188", "v189", "v190", "v191", "v192", "v193", "v194", "v195", "v196", "v197", "v198", "v199", "v200", "v201", "v202", "v203", "v204", "v205", "v206", "v207", "v208", "v209", "v210", "v211", "v212", "v213", "v214", "v215", "v216", "v217", "v218", "v219", "v220", "v221", "v222", "v223", "v224", "v225", "v226", "v227", "v228", "v229", "v230", "v231", "v232", "v233", "v234", "v235", "v236", "v237", "v238", "v239", "v240", "v241", "v242", "v243", "v244", "v245", "v246", "v247", "v248", "v249", "v250", "v251", "v252", "v253", "v254", "v255", "a0", "a1", "a2", "a3", "a4", "a5", "a6", "a7", "a8", "a9", "a10", "a11", "a12", "a13", "a14", "a15", "a16", "a17", "a18", "a19", "a20", "a21", "a22", "a23", "a24", "a25", "a26", "a27", "a28", "a29", "a30", "a31", "a32", "a33", "a34", "a35", "a36", "a37", "a38", "a39", "a40", "a41", "a42", "a43", "a44", "a45", "a46", "a47", "a48", "a49", "a50", "a51", "a52", "a53", "a54", "a55", "a56", "a57", "a58", "a59", "a60", "a61", "a62", "a63", "a64", "a65", "a66", "a67", "a68", "a69", "a70", "a71", "a72", "a73", "a74", "a75", "a76", "a77", "a78", "a79", "a80", "a81", "a82", "a83", "a84", "a85", "a86", "a87", "a88", "a89", "a90", "a91", "a92", "a93", "a94", "a95", "a96", "a97", "a98", "a99", "a100", "a101", "a102", "a103", "a104", "a105", "a106", "a107", "a108", "a109", "a110", "a111", "a112", "a113", "a114", "a115", "a116", "a117", "a118", "a119", "a120", "a121", "a122", "a123", "a124", "a125", "a126", "a127", "a128", "a129", "a130", "a131", "a132", "a133", "a134", "a135", "a136", "a137", "a138", "a139", "a140", "a141", "a142", "a143", "a144", "a145", "a146", "a147", "a148", "a149", "a150", "a151", "a152", "a153", "a154", "a155", "a156", "a157", "a158", "a159", "a160", "a161", "a162", "a163", "a164", "a165", "a166", "a167", "a168", "a169", "a170", "a171", "a172", "a173", "a174", "a175", "a176", "a177", "a178", "a179", "a180", "a181", "a182", "a183", "a184", "a185", "a186", "a187", "a188", "a189", "a190", "a191", "a192", "a193", "a194", "a195", "a196", "a197", "a198", "a199", "a200", "a201", "a202", "a203", "a204", "a205", "a206", "a207", "a208", "a209", "a210", "a211", "a212", "a213", "a214", "a215", "a216", "a217", "a218", "a219", "a220", "a221", "a222", "a223", "a224", "a225", "a226", "a227", "a228", "a229", "a230", "a231", "a232", "a233", "a234", "a235", "a236", "a237", "a238", "a239", "a240", "a241", "a242", "a243", "a244", "a245", "a246", "a247", "a248", "a249", "a250", "a251", "a252", "a253", "a254", "a255", "s4", "s5", "s6", "s7", "s8", "s9", "s10", "s11", "s12", "s13", "s14", "s15", "s16", "s17", "s18", "s19", "s20", "s21", "s22", "s23", "s24", "s25", "s26", "s27", "s28", "s29", "s30", "s31", "s32", "s33", "s34", "s35", "s36", "s37", "s38", "s39", "s40", "s41", "s42", "s43", "s44", "s45", "s46", "s47", "s48", "s49", "s50", "s51", "s52", "s53", "s54", "s55", "s56", "s57", "s58", "s59", "s60", "s61", "s62", "s63", "s64", "s65", "s66", "s67", "s68", "s69", "s70", "s71", "s72", "s73", "s74", "s75", "s76", "s77", "s78", "s79", "s80", "s81", "s82", "s83", "s84", "s85", "s86", "s87", "s88", "s89", "s90", "s91", "s92", "s93", "s94", "s95", "s96", "s97", "s98", "s99", "s100", "s101", "vcc", "scc", "memory"
constexpr int Q_LDS = 163840;
__global__ __launch_bounds__(256, 1) void fused_kernel(const void* x, const void* k, const void* v, const void* shk, const void* ssmk, const void* ssmD, const void* ckv,
                                                       const void* wq, const void* wuk, const void* wuv, const void* wo, const void* gw, void* out, void* ws) {
    const int w = __builtin_amdgcn_readfirstlane(threadIdx.x >> 6);
    const unsigned long long ka = (unsigned long long)__builtin_amdgcn_kernarg_segment_ptr();
    asm volatile(QKV_ASM_TEXT :: "s"(ka), "s"(blockIdx.x), "s"(w) : QKV_ASM_CLOBBERS);
}

extern "C" void kernel_launch(void* const* d_in, const int* in_sizes, int n_in, void* d_out, int out_size, void* d_ws, size_t ws_size,
                              hipStream_t stream) {
    (void)hipFuncSetAttribute((const void*)fused_kernel, hipFuncAttributeMaxDynamicSharedMemorySize, Q_LDS);
    fused_kernel<<<256, 256, Q_LDS, stream>>>(d_in[0], d_in[1], d_in[2], d_in[3], d_in[4], d_in[5], d_in[7], d_in[8], d_in[9], d_in[10], d_in[11], d_in[12], d_out, d_ws);
}
```
